```python
import torch

def _softmax(x, axis=-1):
    return torch.softmax(x, dim=axis)

def _gelu(x):
    return torch.nn.functional.gelu(x, approximate='tanh')

def _silu(x):
    return torch.nn.functional.silu(x)

def _sigmoid(x):
    return torch.sigmoid(x)

def _layernorm(x, axis=-1, eps=1e-05):
    m = x.mean(dim=axis, keepdim=True)
    v = x.var(dim=axis, keepdim=True, unbiased=False)
    return (x - m) / torch.sqrt(v + eps)

def _scatter_add(tgt, idx, src, axis=0):
    return tgt.clone().index_add_(axis, idx, src)

def _segment_sum(src, seg, n):
    out = torch.zeros((n,) + src.shape[1:], dtype=src.dtype)
    return out.index_add_(0, seg, src)

def _pad(x, pw):
    flat = tuple((v for pair in reversed(pw) for v in pair))
    return torch.nn.functional.pad(x, flat)
kernel_name = 'adaLN_cond__gqa_block_cached__moe_arch_mod__prompt'

def setup_inputs():

    _w = lambda a: (lambda t: t.to(torch.bfloat16) if t.is_floating_point() else t)(torch.as_tensor(a))

    def _A_setup():
        B, Sq, D = (1, 2048, 576)
        Hq, Hkv, Sk, Dh = (32, 16, 2048, 128)
        assert Hq % Hkv == 0, (Hq, Hkv)
        return {'x': _w((lambda *s: torch.randn(*s, dtype=torch.bfloat16) * 0.1)(B, Sq, D)), 'k_cache': _w((lambda *s: torch.randn(*s, dtype=torch.bfloat16) * 0.8)(B, Hkv, Sk, Dh)), 'v_cache': _w((lambda *s: torch.randn(*s, dtype=torch.bfloat16) * 0.8)(B, Hkv, Sk, Dh)), 'wq': _w((lambda *s: torch.randn(*s, dtype=torch.bfloat16) * 0.8)(D, Hq * Dh) * D ** (-0.5)), 'wo': _w((lambda *s: torch.randn(*s, dtype=torch.bfloat16) * 0.8)(Hq * Dh, D) * (Hq * Dh) ** (-0.5))}
    _A_in = _A_setup()

    def _B_setup():
        B, S, D, F, C = (1, 2048, 576, 4608, 2)
        return {'x': _w((lambda *s: torch.randn(*s, dtype=torch.bfloat16) * 0.1)(B, S, D)), 'w_r': _w((lambda *s: torch.randn(*s, dtype=torch.bfloat16) * 3.2)(D)), 'w_gate': _w((lambda *s: torch.randn(*s, dtype=torch.bfloat16) * 0.2)(D, F)), 'w_up': _w((lambda *s: torch.randn(*s, dtype=torch.bfloat16) * 0.2)(D, F)), 'w_down': _w((lambda *s: torch.randn(*s, dtype=torch.bfloat16) * 0.2)(F, D))}
    _B_in = _B_setup()
    return {'A_x': _A_in['x'], 'A_k_cache': _A_in['k_cache'], 'A_v_cache': _A_in['v_cache'], 'A_wq': _A_in['wq'], 'A_wo': _A_in['wo'], 'B_x': _B_in['x'], 'B_w_r': _B_in['w_r'], 'B_w_gate': _B_in['w_gate'], 'B_w_up': _B_in['w_up'], 'B_w_down': _B_in['w_down'], 'c': _w((lambda *s: torch.randn(*s, dtype=torch.bfloat16) * 0.4)(1, 576) * 8.0), 'w_mod': _w((lambda *s: torch.randn(*s, dtype=torch.bfloat16) * 0.4)(576, 6 * 576) * 576 ** (-0.5) * 8.0)}

def reference(A_x, A_k_cache, A_v_cache, A_wq, A_wo, B_x, B_w_r, B_w_gate, B_w_up, B_w_down, c, w_mod):

    def _A_ref(x, k_cache, v_cache, wq, wo):
        B, Sq, D = x.shape
        Hq, Dh = (32, 128)
        Hkv = k_cache.shape[1]
        rep = Hq // Hkv
        q = (lambda x, *d: x.permute(*d))((x @ wq).reshape(B, Sq, Hq, Dh), 0, 2, 1, 3)
        k = (lambda x, n, axis: torch.repeat_interleave(x, n, dim=axis))(k_cache, rep, axis=1)
        v = (lambda x, n, axis: torch.repeat_interleave(x, n, dim=axis))(v_cache, rep, axis=1)
        scores = torch.einsum('bhqd,bhkd->bhqk', q, k) * Dh ** (-0.5)
        p = _softmax(scores, axis=-1)
        o = torch.einsum('bhqk,bhkd->bhqd', p, v)
        return (lambda x, *d: x.permute(*d))(o, 0, 2, 1, 3).reshape(B, Sq, Hq * Dh) @ wo

    def _B_ref(x, w_r, w_gate, w_up, w_down):
        B, S, D = x.shape
        C = 2
        r = x @ w_r
        idx = torch.argsort(-r, axis=-1)[:, :C]
        idx_e = (lambda x, n, axis: torch.repeat_interleave(x, n, dim=axis))(idx.reshape(B, C, 1), D, axis=2)
        x_sel = (lambda x, i, axis: torch.take_along_dim(x, i, dim=axis))(x, idx_e, axis=1)
        g = _silu(x_sel @ w_gate)
        u = x_sel @ w_up
        y = g * u @ w_down
        r_sel = _sigmoid((lambda x, i, axis: torch.take_along_dim(x, i, dim=axis))(r, idx, axis=1))
        y = y * r_sel.reshape(B, C, 1)
        b_off = (torch.arange(B) * S).reshape(B, 1)
        flat_idx = (idx + b_off).reshape(B * C)
        zero = (x * 0.0).reshape(B * S, D)
        upd = _scatter_add(zero, flat_idx, y.reshape(B * C, D), axis=0)
        return x + upd.reshape(B, S, D)
    x = A_x
    mod = (_silu(c) @ w_mod).reshape(c.shape[0], 6, 1, -1)
    sa, ca, ga, sb, cb, gb = (mod[:, 0].clone(), mod[:, 1].clone(), mod[:, 2].clone(), mod[:, 3].clone(), mod[:, 4].clone(), mod[:, 5].clone())
    h = _layernorm(x, axis=-1) * (1 + ca) + sa
    x = x + ga * _A_ref(h, A_k_cache, A_v_cache, A_wq, A_wo)
    h = _layernorm(x, axis=-1) * (1 + cb) + sb
    x = x + gb * _B_ref(h, B_w_r, B_w_gate, B_w_up, B_w_down)
    return x
```

```cpp
#include <hip/hip_runtime.h>
#include <stdint.h>


typedef short  bf16x8 __attribute__((ext_vector_type(8)));
typedef float  f32x4  __attribute__((ext_vector_type(4)));
typedef unsigned u32x4 __attribute__((ext_vector_type(4)));
typedef unsigned u32x2 __attribute__((ext_vector_type(2)));

constexpr int SQ = 2048, DM = 576, HKV = 16, FF = 4608;

__device__ __forceinline__ float bf2f(unsigned short u) { return __uint_as_float(((unsigned)u) << 16); }
__device__ __forceinline__ float bflo(unsigned u) { return __uint_as_float(u << 16); }
__device__ __forceinline__ float bfhi(unsigned u) { return __uint_as_float(u & 0xffff0000u); }
__device__ __forceinline__ unsigned cvtpk(float lo, float hi) {
  unsigned r; asm("v_cvt_pk_bf16_f32 %0, %1, %2" : "=v"(r) : "v"(lo), "v"(hi)); return r;
}
__device__ __forceinline__ unsigned short f2bf(float f) { return (unsigned short)(cvtpk(f, f) & 0xffffu); }
__device__ __forceinline__ float rbf(float f) { return bflo(cvtpk(f, f)); }

constexpr size_t WS_MOD  = 0;
constexpr size_t WS_CNT  = 16384;
constexpr size_t WS_R    = WS_CNT + 4096;
constexpr size_t WS_ST   = WS_R + 8192;
constexpr size_t WS_PART = WS_ST + 65536 + 4096;
constexpr size_t WS_H2   = WS_PART + (size_t)HKV * SQ * DM * 2;
constexpr size_t WS_YP   = WS_H2 + (size_t)SQ * DM * 4;
constexpr size_t WS_X1   = WS_YP + (size_t)256 * 2 * DM * 4;
constexpr size_t WS_END  = WS_X1 + (size_t)SQ * DM * 4;

__global__ __launch_bounds__(256) void k_mod(const unsigned short* __restrict__ c, const unsigned short* __restrict__ wmod, float* __restrict__ mod) {
  __shared__ float red[4][32];
  const int tid = threadIdx.x, lane = tid & 63, wid = tid >> 6, cg = tid & 3, ks = tid >> 2;
  const int b = blockIdx.x;
  u32x4 w[9]; unsigned short cv[9];
  const unsigned short* wp = wmod + (size_t)ks * (6 * DM) + b * 32 + cg * 8;
#pragma unroll
  for (int i = 0; i < 9; ++i) { w[i] = *(const u32x4*)(wp + (size_t)(i * 64) * (6 * DM)); cv[i] = c[ks + 64 * i]; }
  float acc[8] = {};
#pragma unroll
  for (int i = 0; i < 9; ++i) {
    const float x = bf2f(cv[i]); const float sv = rbf(x / (1.f + __expf(-x)));
#pragma unroll
    for (int e = 0; e < 4; ++e) { acc[2 * e] += sv * bflo(w[i][e]); acc[2 * e + 1] += sv * bfhi(w[i][e]); }
  }
#pragma unroll
  for (int e = 0; e < 8; ++e) {
#pragma unroll
    for (int o = 4; o < 64; o <<= 1) acc[e] += __shfl_xor(acc[e], o);
  }
  if (lane < 4) {
#pragma unroll
    for (int e = 0; e < 8; ++e) red[wid][lane * 8 + e] = acc[e];
  }
  __syncthreads();
  if (tid < 32) mod[b * 32 + tid] = rbf(red[0][tid] + red[1][tid] + red[2][tid] + red[3][tid]);
}

__global__ __launch_bounds__(256, 1) void k_attn(const void* x, const void* wq, const void* kc, const void* vc, const void* wo, const void* mod, void* part, void* st) {
  extern __shared__ __attribute__((aligned(16))) char lds[];
  const unsigned wid = __builtin_amdgcn_readfirstlane(threadIdx.x >> 6);
  const unsigned ldsb = (unsigned)(size_t)lds;
  asm volatile(
"s_load_dwordx16 s[64:79], %0, 0\n"
"s_mov_b32 s42, %1\n"
"s_mov_b32 s41, %2\n"
"s_mov_b32 s40, %3\n"
"s_mov_b32 s45, 0\n"
"v_mbcnt_lo_u32_b32 v248, -1, 0\n"
"v_mbcnt_hi_u32_b32 v248, -1, v248\n"
"s_and_b32 s56, s42, 7\n"
"s_lshr_b32 s57, s42, 3\n"
"s_and_b32 s58, s57, 1\n"
"s_lshl_b32 s56, s56, 1\n"
"s_add_u32 s43, s56, s58\n"
"s_lshr_b32 s44, s57, 1\n"
"v_lshrrev_b32 v226, 4, v248\n"
"v_bfe_u32 v227, v248, 2, 2\n"
"v_and_b32 v228, 3, v248\n"
"v_and_b32 v229, 15, v248\n"
"v_lshrrev_b32 v230, 3, v248\n"
"v_and_b32 v231, 7, v248\n"
"v_mul_u32_u24 v232, 1152, v230\n"
"v_lshl_add_u32 v216, v231, 4, v232\n"
"s_lshl_b32 s56, s44, 2\n"
"s_add_u32 s56, s56, s41\n"
"s_mul_i32 s46, s56, 36864\n"
"s_add_u32 s47, s46, 9216\n"
"s_add_u32 s48, s46, 18432\n"
"s_add_u32 s49, s46, 27648\n"
"s_lshl_b32 s56, s41, 6\n"
"v_add_u32 v234, s56, v248\n"
"v_lshlrev_b32 v235, 4, v234\n"
"v_lshlrev_b32 v232, 13, v227\n"
"v_lshl_add_u32 v232, v226, 6, v232\n"
"v_lshl_add_u32 v217, v228, 4, v232\n"
"s_lshl_b32 s50, s41, 17\n"
"s_lshl_b32 s56, s43, 9\n"
"s_add_u32 s50, s50, s56\n"
"s_lshl_b32 s56, s41, 11\n"
"s_add_u32 s51, s40, s56\n"
"s_waitcnt lgkmcnt(0)\n"
"s_mov_b32 s36, s74\n"
"s_and_b32 s37, s75, 0xffff\n"
"s_mov_b32 s38, 13824\n"
"s_mov_b32 s39, 0x20000\n"
"s_mov_b32 s20, s64\n"
"s_and_b32 s21, s65, 0xffff\n"
"s_mov_b32 s22, 2359296\n"
"s_mov_b32 s23, 0x20000\n"
"buffer_load_dwordx4 v[144:147], v235, s[36:39], 0 offen\n"
"buffer_load_dwordx4 v[148:151], v235, s[36:39], 0 offen offset:2304\n"
"s_mov_b32 s24, s66\n"
"s_and_b32 s25, s67, 0xffff\n"
"s_mov_b32 s26, 4718592\n"
"s_mov_b32 s27, 0x20000\n"
"buffer_load_dwordx4 v[0:3], v216, s[20:23], s46 offen offset:0\n"
"buffer_load_dwordx4 v[4:7], v216, s[20:23], s47 offen offset:0\n"
"buffer_load_dwordx4 v[8:11], v216, s[20:23], s48 offen offset:0\n"
"buffer_load_dwordx4 v[12:15], v216, s[20:23], s49 offen offset:0\n"
"buffer_load_dwordx4 v[16:19], v216, s[20:23], s46 offen offset:128\n"
"buffer_load_dwordx4 v[20:23], v216, s[20:23], s47 offen offset:128\n"
"buffer_load_dwordx4 v[24:27], v216, s[20:23], s48 offen offset:128\n"
"buffer_load_dwordx4 v[28:31], v216, s[20:23], s49 offen offset:128\n"
"buffer_load_dwordx4 v[32:35], v216, s[20:23], s46 offen offset:256\n"
"buffer_load_dwordx4 v[36:39], v216, s[20:23], s47 offen offset:256\n"
"buffer_load_dwordx4 v[40:43], v216, s[20:23], s48 offen offset:256\n"
"buffer_load_dwordx4 v[44:47], v216, s[20:23], s49 offen offset:256\n"
"buffer_load_dwordx4 v[48:51], v216, s[20:23], s46 offen offset:384\n"
"buffer_load_dwordx4 v[52:55], v216, s[20:23], s47 offen offset:384\n"
"buffer_load_dwordx4 v[56:59], v216, s[20:23], s48 offen offset:384\n"
"buffer_load_dwordx4 v[60:63], v216, s[20:23], s49 offen offset:384\n"
"buffer_load_dwordx4 v[64:67], v216, s[20:23], s46 offen offset:512\n"
"buffer_load_dwordx4 v[68:71], v216, s[20:23], s47 offen offset:512\n"
"buffer_load_dwordx4 v[72:75], v216, s[20:23], s48 offen offset:512\n"
"buffer_load_dwordx4 v[76:79], v216, s[20:23], s49 offen offset:512\n"
"buffer_load_dwordx4 v[80:83], v216, s[20:23], s46 offen offset:640\n"
"buffer_load_dwordx4 v[84:87], v216, s[20:23], s47 offen offset:640\n"
"buffer_load_dwordx4 v[88:91], v216, s[20:23], s48 offen offset:640\n"
"buffer_load_dwordx4 v[92:95], v216, s[20:23], s49 offen offset:640\n"
"buffer_load_dwordx4 v[96:99], v216, s[20:23], s46 offen offset:768\n"
"buffer_load_dwordx4 v[100:103], v216, s[20:23], s47 offen offset:768\n"
"buffer_load_dwordx4 v[104:107], v216, s[20:23], s48 offen offset:768\n"
"buffer_load_dwordx4 v[108:111], v216, s[20:23], s49 offen offset:768\n"
"buffer_load_dwordx4 v[112:115], v216, s[20:23], s46 offen offset:896\n"
"buffer_load_dwordx4 v[116:119], v216, s[20:23], s47 offen offset:896\n"
"buffer_load_dwordx4 v[120:123], v216, s[20:23], s48 offen offset:896\n"
"buffer_load_dwordx4 v[124:127], v216, s[20:23], s49 offen offset:896\n"
"buffer_load_dwordx4 v[128:131], v216, s[20:23], s46 offen offset:1024\n"
"buffer_load_dwordx4 v[132:135], v216, s[20:23], s47 offen offset:1024\n"
"buffer_load_dwordx4 v[136:139], v216, s[20:23], s48 offen offset:1024\n"
"buffer_load_dwordx4 v[140:143], v216, s[20:23], s49 offen offset:1024\n"
"s_mov_b32 m0, s51\n"
"s_nop 0\n"
"buffer_load_dwordx4 v217, s[24:27], s50 offen offset:0 lds\n"
"s_add_u32 m0, s51, 7936\n"
"s_nop 0\n"
"buffer_load_dwordx4 v217, s[24:27], s50 offen offset:256 lds\n"
"s_add_u32 s50, s50, 32768\n"
"s_add_u32 m0, s51, 1024\n"
"s_nop 0\n"
"buffer_load_dwordx4 v217, s[24:27], s50 offen offset:0 lds\n"
"s_add_u32 m0, s51, 8960\n"
"s_nop 0\n"
"buffer_load_dwordx4 v217, s[24:27], s50 offen offset:256 lds\n"
"s_add_u32 s50, s50, 32768\n"
"s_add_u32 m0, s51, 16392\n"
"s_nop 0\n"
"buffer_load_dwordx4 v217, s[24:27], s50 offen offset:0 lds\n"
"s_add_u32 m0, s51, 24328\n"
"s_nop 0\n"
"buffer_load_dwordx4 v217, s[24:27], s50 offen offset:256 lds\n"
"s_add_u32 s50, s50, 32768\n"
"s_add_u32 m0, s51, 17416\n"
"s_nop 0\n"
"buffer_load_dwordx4 v217, s[24:27], s50 offen offset:0 lds\n"
"s_add_u32 m0, s51, 25352\n"
"s_nop 0\n"
"buffer_load_dwordx4 v217, s[24:27], s50 offen offset:256 lds\n"
"s_add_u32 s50, s50, 425984\n"
"s_add_u32 m0, s51, 32784\n"
"s_nop 0\n"
"buffer_load_dwordx4 v217, s[24:27], s50 offen offset:0 lds\n"
"s_add_u32 m0, s51, 40720\n"
"s_nop 0\n"
"buffer_load_dwordx4 v217, s[24:27], s50 offen offset:256 lds\n"
"s_add_u32 s50, s50, 32768\n"
"s_add_u32 m0, s51, 33808\n"
"s_nop 0\n"
"buffer_load_dwordx4 v217, s[24:27], s50 offen offset:0 lds\n"
"s_add_u32 m0, s51, 41744\n"
"s_nop 0\n"
"buffer_load_dwordx4 v217, s[24:27], s50 offen offset:256 lds\n"
"s_add_u32 s50, s50, 32768\n"
"s_add_u32 m0, s51, 49176\n"
"s_nop 0\n"
"buffer_load_dwordx4 v217, s[24:27], s50 offen offset:0 lds\n"
"s_add_u32 m0, s51, 57112\n"
"s_nop 0\n"
"buffer_load_dwordx4 v217, s[24:27], s50 offen offset:256 lds\n"
"s_add_u32 s50, s50, 32768\n"
"s_add_u32 m0, s51, 50200\n"
"s_nop 0\n"
"buffer_load_dwordx4 v217, s[24:27], s50 offen offset:0 lds\n"
"s_add_u32 m0, s51, 58136\n"
"s_nop 0\n"
"buffer_load_dwordx4 v217, s[24:27], s50 offen offset:256 lds\n"
"s_add_u32 s50, s50, 425984\n"
"s_add_u32 m0, s51, 65568\n"
"s_nop 0\n"
"buffer_load_dwordx4 v217, s[24:27], s50 offen offset:0 lds\n"
"s_add_u32 m0, s51, 73504\n"
"s_nop 0\n"
"buffer_load_dwordx4 v217, s[24:27], s50 offen offset:256 lds\n"
"s_add_u32 s50, s50, 32768\n"
"s_add_u32 m0, s51, 66592\n"
"s_nop 0\n"
"buffer_load_dwordx4 v217, s[24:27], s50 offen offset:0 lds\n"
"s_add_u32 m0, s51, 74528\n"
"s_nop 0\n"
"buffer_load_dwordx4 v217, s[24:27], s50 offen offset:256 lds\n"
"s_add_u32 s50, s50, 32768\n"
"s_add_u32 m0, s51, 81960\n"
"s_nop 0\n"
"buffer_load_dwordx4 v217, s[24:27], s50 offen offset:0 lds\n"
"s_add_u32 m0, s51, 89896\n"
"s_nop 0\n"
"buffer_load_dwordx4 v217, s[24:27], s50 offen offset:256 lds\n"
"s_add_u32 s50, s50, 32768\n"
"s_add_u32 m0, s51, 82984\n"
"s_nop 0\n"
"buffer_load_dwordx4 v217, s[24:27], s50 offen offset:0 lds\n"
"s_add_u32 m0, s51, 90920\n"
"s_nop 0\n"
"buffer_load_dwordx4 v217, s[24:27], s50 offen offset:256 lds\n"
"s_add_u32 s50, s50, 425984\n"
"s_lshl_b32 s56, s43, 19\n"
"s_add_u32 s8, s68, s56\n"
"s_addc_u32 s9, s69, 0\n"
"s_and_b32 s9, s9, 0xffff\n"
"s_mov_b32 s10, 524288\n"
"s_mov_b32 s11, 0x20000\n"
"s_add_u32 s12, s70, s56\n"
"s_addc_u32 s13, s71, 0\n"
"s_and_b32 s13, s13, 0xffff\n"
"s_mov_b32 s14, 524288\n"
"s_mov_b32 s15, 0x20000\n"
"s_mul_i32 s56, s43, 294912\n"
"s_add_u32 s28, s72, s56\n"
"s_addc_u32 s29, s73, 0\n"
"s_and_b32 s29, s29, 0xffff\n"
"s_mov_b32 s30, 294912\n"
"s_mov_b32 s31, 0x20000\n"
"s_mul_i32 s56, s43, 2359296\n"
"s_add_u32 s32, s76, s56\n"
"s_addc_u32 s33, s77, 0\n"
"s_and_b32 s33, s33, 0xffff\n"
"s_mov_b32 s34, 2359296\n"
"s_mov_b32 s35, 0x20000\n"
"s_lshl_b32 s16, s41, 12\n"
"s_add_u32 s18, s40, s16\n"
"s_lshl_b32 s56, s41, 13\n"
"s_add_u32 s53, s40, s56\n"
"s_mul_i32 s52, s41, 73728\n"
"s_lshr_b32 s56, s41, 1\n"
"s_lshl_b32 s57, s44, 1\n"
"s_add_u32 s56, s56, s57\n"
"s_mul_i32 s56, s56, 73728\n"
"s_and_b32 s57, s41, 1\n"
"s_lshl_b32 s57, s57, 10\n"
"s_add_u32 s54, s56, s57\n"
"v_and_b32 v232, 2, v227\n"
"v_xor_b32 v232, v232, v226\n"
"v_lshlrev_b32 v233, 10, v227\n"
"v_lshl_add_u32 v233, v228, 6, v233\n"
"v_lshl_add_u32 v233, v232, 4, v233\n"
"v_add_u32 v240, s40, v233\n"
"v_add_u32 v241, 0x10000, v240\n"
"v_lshlrev_b32 v233, 10, v226\n"
"v_lshl_add_u32 v233, v227, 6, v233\n"
"v_lshl_add_u32 v233, v228, 3, v233\n"
"v_add_u32 v233, s40, v233\n"
"v_and_b32 v232, 1, v226\n"
"v_lshl_add_u32 v242, v232, 5, v233\n"
"v_xor_b32 v243, 32, v242\n"
"v_add_u32 v244, 0x10000, v242\n"
"v_add_u32 v245, 0x10000, v243\n"
"v_lshlrev_b32 v233, 8, v227\n"
"v_lshl_add_u32 v233, v226, 6, v233\n"
"v_lshl_add_u32 v246, v228, 4, v233\n"
"v_xor_b32 v247, 32, v246\n"
"v_mul_u32_u24 v233, 16392, v232\n"
"v_lshrrev_b32 v232, 1, v226\n"
"v_lshl_add_u32 v233, v232, 11, v233\n"
"v_lshl_add_u32 v233, v227, 6, v233\n"
"v_lshl_add_u32 v233, v228, 4, v233\n"
"s_and_b32 s56, s41, 1\n"
"s_lshl_b32 s56, s56, 13\n"
"s_add_u32 s56, s56, s40\n"
"v_add_u32 v218, s56, v233\n"
"v_add_u32 v219, 65568, v218\n"
"v_lshrrev_b32 v232, 1, v229\n"
"v_xor_b32 v232, v232, v226\n"
"v_lshlrev_b32 v233, 7, v229\n"
"v_lshl_add_u32 v233, v232, 4, v233\n"
"s_lshr_b32 s56, s41, 1\n"
"s_lshl_b32 s56, s56, 13\n"
"s_add_u32 s56, s56, s40\n"
"s_add_u32 s56, s56, 131072\n"
"v_add_u32 v220, s56, v233\n"
"v_xor_b32 v221, 64, v220\n"
"v_lshrrev_b32 v232, 1, v230\n"
"v_xor_b32 v232, v232, v231\n"
"v_lshlrev_b32 v233, 7, v230\n"
"v_lshl_add_u32 v233, v232, 4, v233\n"
"s_add_u32 s56, s18, 131072\n"
"v_add_u32 v222, s56, v233\n"
"v_xor_b32 v223, 64, v222\n"
"s_add_u32 s56, s40, 99328\n"
"v_lshl_add_u32 v224, v231, 4, s56\n"
"v_lshrrev_b32 v232, 4, v234\n"
"v_lshlrev_b32 v233, 9, v232\n"
"v_and_b32 v232, 1, v234\n"
"v_lshl_add_u32 v233, v232, 7, v233\n"
"v_bfe_u32 v232, v234, 1, 3\n"
"v_lshl_add_u32 v233, v232, 4, v233\n"
"v_add_u32 v235, s56, v233\n"
"v_mov_b32 v236, 0x3e0293ee\n"
"s_waitcnt vmcnt(60)\n"
"v_mul_f32 v144, v236, v144\n"
"v_fma_f32 v148, v148, v236, v236\n"
"v_mul_f32 v145, v236, v145\n"
"v_fma_f32 v149, v149, v236, v236\n"
"v_mul_f32 v146, v236, v146\n"
"v_fma_f32 v150, v150, v236, v236\n"
"v_mul_f32 v147, v236, v147\n"
"v_fma_f32 v151, v151, v236, v236\n"
"ds_write_b128 v235, v[148:151]\n"
"ds_write_b128 v235, v[144:147] offset:256\n"
"v_mov_b32 v237, 0x3f803f80\n"
"v_mov_b32 v200, 0\n"
"v_mov_b32 v202, 0\n"
"v_mov_b32 v204, 0\n"
"v_mov_b32 v206, 0\n"
"v_mov_b32 v208, 0\n"
"v_mov_b32 v210, 0\n"
"v_mov_b32 v212, 0\n"
"v_mov_b32 v214, 0\n"
"s_waitcnt vmcnt(56)\n"
"v_dot2c_f32_bf16 v200, v0, v237\n"
"v_dot2c_f32_bf16 v202, v0, v0\n"
"v_dot2c_f32_bf16 v204, v4, v237\n"
"v_dot2c_f32_bf16 v206, v4, v4\n"
"v_dot2c_f32_bf16 v208, v8, v237\n"
"v_dot2c_f32_bf16 v210, v8, v8\n"
"v_dot2c_f32_bf16 v212, v12, v237\n"
"v_dot2c_f32_bf16 v214, v12, v12\n"
"v_dot2c_f32_bf16 v200, v1, v237\n"
"v_dot2c_f32_bf16 v202, v1, v1\n"
"v_dot2c_f32_bf16 v204, v5, v237\n"
"v_dot2c_f32_bf16 v206, v5, v5\n"
"v_dot2c_f32_bf16 v208, v9, v237\n"
"v_dot2c_f32_bf16 v210, v9, v9\n"
"v_dot2c_f32_bf16 v212, v13, v237\n"
"v_dot2c_f32_bf16 v214, v13, v13\n"
"v_dot2c_f32_bf16 v200, v2, v237\n"
"v_dot2c_f32_bf16 v202, v2, v2\n"
"v_dot2c_f32_bf16 v204, v6, v237\n"
"v_dot2c_f32_bf16 v206, v6, v6\n"
"v_dot2c_f32_bf16 v208, v10, v237\n"
"v_dot2c_f32_bf16 v210, v10, v10\n"
"v_dot2c_f32_bf16 v212, v14, v237\n"
"v_dot2c_f32_bf16 v214, v14, v14\n"
"v_dot2c_f32_bf16 v200, v3, v237\n"
"v_dot2c_f32_bf16 v202, v3, v3\n"
"v_dot2c_f32_bf16 v204, v7, v237\n"
"v_dot2c_f32_bf16 v206, v7, v7\n"
"v_dot2c_f32_bf16 v208, v11, v237\n"
"v_dot2c_f32_bf16 v210, v11, v11\n"
"v_dot2c_f32_bf16 v212, v15, v237\n"
"v_dot2c_f32_bf16 v214, v15, v15\n"
"s_waitcnt vmcnt(52)\n"
"v_dot2c_f32_bf16 v200, v16, v237\n"
"v_dot2c_f32_bf16 v202, v16, v16\n"
"v_dot2c_f32_bf16 v204, v20, v237\n"
"v_dot2c_f32_bf16 v206, v20, v20\n"
"v_dot2c_f32_bf16 v208, v24, v237\n"
"v_dot2c_f32_bf16 v210, v24, v24\n"
"v_dot2c_f32_bf16 v212, v28, v237\n"
"v_dot2c_f32_bf16 v214, v28, v28\n"
"v_dot2c_f32_bf16 v200, v17, v237\n"
"v_dot2c_f32_bf16 v202, v17, v17\n"
"v_dot2c_f32_bf16 v204, v21, v237\n"
"v_dot2c_f32_bf16 v206, v21, v21\n"
"v_dot2c_f32_bf16 v208, v25, v237\n"
"v_dot2c_f32_bf16 v210, v25, v25\n"
"v_dot2c_f32_bf16 v212, v29, v237\n"
"v_dot2c_f32_bf16 v214, v29, v29\n"
"v_dot2c_f32_bf16 v200, v18, v237\n"
"v_dot2c_f32_bf16 v202, v18, v18\n"
"v_dot2c_f32_bf16 v204, v22, v237\n"
"v_dot2c_f32_bf16 v206, v22, v22\n"
"v_dot2c_f32_bf16 v208, v26, v237\n"
"v_dot2c_f32_bf16 v210, v26, v26\n"
"v_dot2c_f32_bf16 v212, v30, v237\n"
"v_dot2c_f32_bf16 v214, v30, v30\n"
"v_dot2c_f32_bf16 v200, v19, v237\n"
"v_dot2c_f32_bf16 v202, v19, v19\n"
"v_dot2c_f32_bf16 v204, v23, v237\n"
"v_dot2c_f32_bf16 v206, v23, v23\n"
"v_dot2c_f32_bf16 v208, v27, v237\n"
"v_dot2c_f32_bf16 v210, v27, v27\n"
"v_dot2c_f32_bf16 v212, v31, v237\n"
"v_dot2c_f32_bf16 v214, v31, v31\n"
"s_waitcnt vmcnt(48)\n"
"v_dot2c_f32_bf16 v200, v32, v237\n"
"v_dot2c_f32_bf16 v202, v32, v32\n"
"v_dot2c_f32_bf16 v204, v36, v237\n"
"v_dot2c_f32_bf16 v206, v36, v36\n"
"v_dot2c_f32_bf16 v208, v40, v237\n"
"v_dot2c_f32_bf16 v210, v40, v40\n"
"v_dot2c_f32_bf16 v212, v44, v237\n"
"v_dot2c_f32_bf16 v214, v44, v44\n"
"v_dot2c_f32_bf16 v200, v33, v237\n"
"v_dot2c_f32_bf16 v202, v33, v33\n"
"v_dot2c_f32_bf16 v204, v37, v237\n"
"v_dot2c_f32_bf16 v206, v37, v37\n"
"v_dot2c_f32_bf16 v208, v41, v237\n"
"v_dot2c_f32_bf16 v210, v41, v41\n"
"v_dot2c_f32_bf16 v212, v45, v237\n"
"v_dot2c_f32_bf16 v214, v45, v45\n"
"v_dot2c_f32_bf16 v200, v34, v237\n"
"v_dot2c_f32_bf16 v202, v34, v34\n"
"v_dot2c_f32_bf16 v204, v38, v237\n"
"v_dot2c_f32_bf16 v206, v38, v38\n"
"v_dot2c_f32_bf16 v208, v42, v237\n"
"v_dot2c_f32_bf16 v210, v42, v42\n"
"v_dot2c_f32_bf16 v212, v46, v237\n"
"v_dot2c_f32_bf16 v214, v46, v46\n"
"v_dot2c_f32_bf16 v200, v35, v237\n"
"v_dot2c_f32_bf16 v202, v35, v35\n"
"v_dot2c_f32_bf16 v204, v39, v237\n"
"v_dot2c_f32_bf16 v206, v39, v39\n"
"v_dot2c_f32_bf16 v208, v43, v237\n"
"v_dot2c_f32_bf16 v210, v43, v43\n"
"v_dot2c_f32_bf16 v212, v47, v237\n"
"v_dot2c_f32_bf16 v214, v47, v47\n"
"s_waitcnt vmcnt(44)\n"
"v_dot2c_f32_bf16 v200, v48, v237\n"
"v_dot2c_f32_bf16 v202, v48, v48\n"
"v_dot2c_f32_bf16 v204, v52, v237\n"
"v_dot2c_f32_bf16 v206, v52, v52\n"
"v_dot2c_f32_bf16 v208, v56, v237\n"
"v_dot2c_f32_bf16 v210, v56, v56\n"
"v_dot2c_f32_bf16 v212, v60, v237\n"
"v_dot2c_f32_bf16 v214, v60, v60\n"
"v_dot2c_f32_bf16 v200, v49, v237\n"
"v_dot2c_f32_bf16 v202, v49, v49\n"
"v_dot2c_f32_bf16 v204, v53, v237\n"
"v_dot2c_f32_bf16 v206, v53, v53\n"
"v_dot2c_f32_bf16 v208, v57, v237\n"
"v_dot2c_f32_bf16 v210, v57, v57\n"
"v_dot2c_f32_bf16 v212, v61, v237\n"
"v_dot2c_f32_bf16 v214, v61, v61\n"
"v_dot2c_f32_bf16 v200, v50, v237\n"
"v_dot2c_f32_bf16 v202, v50, v50\n"
"v_dot2c_f32_bf16 v204, v54, v237\n"
"v_dot2c_f32_bf16 v206, v54, v54\n"
"v_dot2c_f32_bf16 v208, v58, v237\n"
"v_dot2c_f32_bf16 v210, v58, v58\n"
"v_dot2c_f32_bf16 v212, v62, v237\n"
"v_dot2c_f32_bf16 v214, v62, v62\n"
"v_dot2c_f32_bf16 v200, v51, v237\n"
"v_dot2c_f32_bf16 v202, v51, v51\n"
"v_dot2c_f32_bf16 v204, v55, v237\n"
"v_dot2c_f32_bf16 v206, v55, v55\n"
"v_dot2c_f32_bf16 v208, v59, v237\n"
"v_dot2c_f32_bf16 v210, v59, v59\n"
"v_dot2c_f32_bf16 v212, v63, v237\n"
"v_dot2c_f32_bf16 v214, v63, v63\n"
"s_waitcnt vmcnt(40)\n"
"v_dot2c_f32_bf16 v200, v64, v237\n"
"v_dot2c_f32_bf16 v202, v64, v64\n"
"v_dot2c_f32_bf16 v204, v68, v237\n"
"v_dot2c_f32_bf16 v206, v68, v68\n"
"v_dot2c_f32_bf16 v208, v72, v237\n"
"v_dot2c_f32_bf16 v210, v72, v72\n"
"v_dot2c_f32_bf16 v212, v76, v237\n"
"v_dot2c_f32_bf16 v214, v76, v76\n"
"v_dot2c_f32_bf16 v200, v65, v237\n"
"v_dot2c_f32_bf16 v202, v65, v65\n"
"v_dot2c_f32_bf16 v204, v69, v237\n"
"v_dot2c_f32_bf16 v206, v69, v69\n"
"v_dot2c_f32_bf16 v208, v73, v237\n"
"v_dot2c_f32_bf16 v210, v73, v73\n"
"v_dot2c_f32_bf16 v212, v77, v237\n"
"v_dot2c_f32_bf16 v214, v77, v77\n"
"v_dot2c_f32_bf16 v200, v66, v237\n"
"v_dot2c_f32_bf16 v202, v66, v66\n"
"v_dot2c_f32_bf16 v204, v70, v237\n"
"v_dot2c_f32_bf16 v206, v70, v70\n"
"v_dot2c_f32_bf16 v208, v74, v237\n"
"v_dot2c_f32_bf16 v210, v74, v74\n"
"v_dot2c_f32_bf16 v212, v78, v237\n"
"v_dot2c_f32_bf16 v214, v78, v78\n"
"v_dot2c_f32_bf16 v200, v67, v237\n"
"v_dot2c_f32_bf16 v202, v67, v67\n"
"v_dot2c_f32_bf16 v204, v71, v237\n"
"v_dot2c_f32_bf16 v206, v71, v71\n"
"v_dot2c_f32_bf16 v208, v75, v237\n"
"v_dot2c_f32_bf16 v210, v75, v75\n"
"v_dot2c_f32_bf16 v212, v79, v237\n"
"v_dot2c_f32_bf16 v214, v79, v79\n"
"s_waitcnt vmcnt(36)\n"
"v_dot2c_f32_bf16 v200, v80, v237\n"
"v_dot2c_f32_bf16 v202, v80, v80\n"
"v_dot2c_f32_bf16 v204, v84, v237\n"
"v_dot2c_f32_bf16 v206, v84, v84\n"
"v_dot2c_f32_bf16 v208, v88, v237\n"
"v_dot2c_f32_bf16 v210, v88, v88\n"
"v_dot2c_f32_bf16 v212, v92, v237\n"
"v_dot2c_f32_bf16 v214, v92, v92\n"
"v_dot2c_f32_bf16 v200, v81, v237\n"
"v_dot2c_f32_bf16 v202, v81, v81\n"
"v_dot2c_f32_bf16 v204, v85, v237\n"
"v_dot2c_f32_bf16 v206, v85, v85\n"
"v_dot2c_f32_bf16 v208, v89, v237\n"
"v_dot2c_f32_bf16 v210, v89, v89\n"
"v_dot2c_f32_bf16 v212, v93, v237\n"
"v_dot2c_f32_bf16 v214, v93, v93\n"
"v_dot2c_f32_bf16 v200, v82, v237\n"
"v_dot2c_f32_bf16 v202, v82, v82\n"
"v_dot2c_f32_bf16 v204, v86, v237\n"
"v_dot2c_f32_bf16 v206, v86, v86\n"
"v_dot2c_f32_bf16 v208, v90, v237\n"
"v_dot2c_f32_bf16 v210, v90, v90\n"
"v_dot2c_f32_bf16 v212, v94, v237\n"
"v_dot2c_f32_bf16 v214, v94, v94\n"
"v_dot2c_f32_bf16 v200, v83, v237\n"
"v_dot2c_f32_bf16 v202, v83, v83\n"
"v_dot2c_f32_bf16 v204, v87, v237\n"
"v_dot2c_f32_bf16 v206, v87, v87\n"
"v_dot2c_f32_bf16 v208, v91, v237\n"
"v_dot2c_f32_bf16 v210, v91, v91\n"
"v_dot2c_f32_bf16 v212, v95, v237\n"
"v_dot2c_f32_bf16 v214, v95, v95\n"
"s_waitcnt vmcnt(32)\n"
"v_dot2c_f32_bf16 v200, v96, v237\n"
"v_dot2c_f32_bf16 v202, v96, v96\n"
"v_dot2c_f32_bf16 v204, v100, v237\n"
"v_dot2c_f32_bf16 v206, v100, v100\n"
"v_dot2c_f32_bf16 v208, v104, v237\n"
"v_dot2c_f32_bf16 v210, v104, v104\n"
"v_dot2c_f32_bf16 v212, v108, v237\n"
"v_dot2c_f32_bf16 v214, v108, v108\n"
"v_dot2c_f32_bf16 v200, v97, v237\n"
"v_dot2c_f32_bf16 v202, v97, v97\n"
"v_dot2c_f32_bf16 v204, v101, v237\n"
"v_dot2c_f32_bf16 v206, v101, v101\n"
"v_dot2c_f32_bf16 v208, v105, v237\n"
"v_dot2c_f32_bf16 v210, v105, v105\n"
"v_dot2c_f32_bf16 v212, v109, v237\n"
"v_dot2c_f32_bf16 v214, v109, v109\n"
"v_dot2c_f32_bf16 v200, v98, v237\n"
"v_dot2c_f32_bf16 v202, v98, v98\n"
"v_dot2c_f32_bf16 v204, v102, v237\n"
"v_dot2c_f32_bf16 v206, v102, v102\n"
"v_dot2c_f32_bf16 v208, v106, v237\n"
"v_dot2c_f32_bf16 v210, v106, v106\n"
"v_dot2c_f32_bf16 v212, v110, v237\n"
"v_dot2c_f32_bf16 v214, v110, v110\n"
"v_dot2c_f32_bf16 v200, v99, v237\n"
"v_dot2c_f32_bf16 v202, v99, v99\n"
"v_dot2c_f32_bf16 v204, v103, v237\n"
"v_dot2c_f32_bf16 v206, v103, v103\n"
"v_dot2c_f32_bf16 v208, v107, v237\n"
"v_dot2c_f32_bf16 v210, v107, v107\n"
"v_dot2c_f32_bf16 v212, v111, v237\n"
"v_dot2c_f32_bf16 v214, v111, v111\n"
"s_waitcnt vmcnt(28)\n"
"v_dot2c_f32_bf16 v200, v112, v237\n"
"v_dot2c_f32_bf16 v202, v112, v112\n"
"v_dot2c_f32_bf16 v204, v116, v237\n"
"v_dot2c_f32_bf16 v206, v116, v116\n"
"v_dot2c_f32_bf16 v208, v120, v237\n"
"v_dot2c_f32_bf16 v210, v120, v120\n"
"v_dot2c_f32_bf16 v212, v124, v237\n"
"v_dot2c_f32_bf16 v214, v124, v124\n"
"v_dot2c_f32_bf16 v200, v113, v237\n"
"v_dot2c_f32_bf16 v202, v113, v113\n"
"v_dot2c_f32_bf16 v204, v117, v237\n"
"v_dot2c_f32_bf16 v206, v117, v117\n"
"v_dot2c_f32_bf16 v208, v121, v237\n"
"v_dot2c_f32_bf16 v210, v121, v121\n"
"v_dot2c_f32_bf16 v212, v125, v237\n"
"v_dot2c_f32_bf16 v214, v125, v125\n"
"v_dot2c_f32_bf16 v200, v114, v237\n"
"v_dot2c_f32_bf16 v202, v114, v114\n"
"v_dot2c_f32_bf16 v204, v118, v237\n"
"v_dot2c_f32_bf16 v206, v118, v118\n"
"v_dot2c_f32_bf16 v208, v122, v237\n"
"v_dot2c_f32_bf16 v210, v122, v122\n"
"v_dot2c_f32_bf16 v212, v126, v237\n"
"v_dot2c_f32_bf16 v214, v126, v126\n"
"v_dot2c_f32_bf16 v200, v115, v237\n"
"v_dot2c_f32_bf16 v202, v115, v115\n"
"v_dot2c_f32_bf16 v204, v119, v237\n"
"v_dot2c_f32_bf16 v206, v119, v119\n"
"v_dot2c_f32_bf16 v208, v123, v237\n"
"v_dot2c_f32_bf16 v210, v123, v123\n"
"v_dot2c_f32_bf16 v212, v127, v237\n"
"v_dot2c_f32_bf16 v214, v127, v127\n"
"s_waitcnt vmcnt(24)\n"
"v_dot2c_f32_bf16 v200, v128, v237\n"
"v_dot2c_f32_bf16 v202, v128, v128\n"
"v_dot2c_f32_bf16 v204, v132, v237\n"
"v_dot2c_f32_bf16 v206, v132, v132\n"
"v_dot2c_f32_bf16 v208, v136, v237\n"
"v_dot2c_f32_bf16 v210, v136, v136\n"
"v_dot2c_f32_bf16 v212, v140, v237\n"
"v_dot2c_f32_bf16 v214, v140, v140\n"
"v_dot2c_f32_bf16 v200, v129, v237\n"
"v_dot2c_f32_bf16 v202, v129, v129\n"
"v_dot2c_f32_bf16 v204, v133, v237\n"
"v_dot2c_f32_bf16 v206, v133, v133\n"
"v_dot2c_f32_bf16 v208, v137, v237\n"
"v_dot2c_f32_bf16 v210, v137, v137\n"
"v_dot2c_f32_bf16 v212, v141, v237\n"
"v_dot2c_f32_bf16 v214, v141, v141\n"
"v_dot2c_f32_bf16 v200, v130, v237\n"
"v_dot2c_f32_bf16 v202, v130, v130\n"
"v_dot2c_f32_bf16 v204, v134, v237\n"
"v_dot2c_f32_bf16 v206, v134, v134\n"
"v_dot2c_f32_bf16 v208, v138, v237\n"
"v_dot2c_f32_bf16 v210, v138, v138\n"
"v_dot2c_f32_bf16 v212, v142, v237\n"
"v_dot2c_f32_bf16 v214, v142, v142\n"
"v_dot2c_f32_bf16 v200, v131, v237\n"
"v_dot2c_f32_bf16 v202, v131, v131\n"
"v_dot2c_f32_bf16 v204, v135, v237\n"
"v_dot2c_f32_bf16 v206, v135, v135\n"
"v_dot2c_f32_bf16 v208, v139, v237\n"
"v_dot2c_f32_bf16 v210, v139, v139\n"
"v_dot2c_f32_bf16 v212, v143, v237\n"
"v_dot2c_f32_bf16 v214, v143, v143\n"
"s_nop 2\n"
"v_add_f32_dpp v200, v200, v200 quad_perm:[1,0,3,2] row_mask:0xf bank_mask:0xf\n"
"v_add_f32_dpp v202, v202, v202 quad_perm:[1,0,3,2] row_mask:0xf bank_mask:0xf\n"
"v_add_f32_dpp v204, v204, v204 quad_perm:[1,0,3,2] row_mask:0xf bank_mask:0xf\n"
"v_add_f32_dpp v206, v206, v206 quad_perm:[1,0,3,2] row_mask:0xf bank_mask:0xf\n"
"v_add_f32_dpp v208, v208, v208 quad_perm:[1,0,3,2] row_mask:0xf bank_mask:0xf\n"
"v_add_f32_dpp v210, v210, v210 quad_perm:[1,0,3,2] row_mask:0xf bank_mask:0xf\n"
"v_add_f32_dpp v212, v212, v212 quad_perm:[1,0,3,2] row_mask:0xf bank_mask:0xf\n"
"v_add_f32_dpp v214, v214, v214 quad_perm:[1,0,3,2] row_mask:0xf bank_mask:0xf\n"
"v_add_f32_dpp v200, v200, v200 quad_perm:[2,3,0,1] row_mask:0xf bank_mask:0xf\n"
"v_add_f32_dpp v202, v202, v202 quad_perm:[2,3,0,1] row_mask:0xf bank_mask:0xf\n"
"v_add_f32_dpp v204, v204, v204 quad_perm:[2,3,0,1] row_mask:0xf bank_mask:0xf\n"
"v_add_f32_dpp v206, v206, v206 quad_perm:[2,3,0,1] row_mask:0xf bank_mask:0xf\n"
"v_add_f32_dpp v208, v208, v208 quad_perm:[2,3,0,1] row_mask:0xf bank_mask:0xf\n"
"v_add_f32_dpp v210, v210, v210 quad_perm:[2,3,0,1] row_mask:0xf bank_mask:0xf\n"
"v_add_f32_dpp v212, v212, v212 quad_perm:[2,3,0,1] row_mask:0xf bank_mask:0xf\n"
"v_add_f32_dpp v214, v214, v214 quad_perm:[2,3,0,1] row_mask:0xf bank_mask:0xf\n"
"v_add_f32_dpp v200, v200, v200 row_half_mirror row_mask:0xf bank_mask:0xf\n"
"v_add_f32_dpp v202, v202, v202 row_half_mirror row_mask:0xf bank_mask:0xf\n"
"v_add_f32_dpp v204, v204, v204 row_half_mirror row_mask:0xf bank_mask:0xf\n"
"v_add_f32_dpp v206, v206, v206 row_half_mirror row_mask:0xf bank_mask:0xf\n"
"v_add_f32_dpp v208, v208, v208 row_half_mirror row_mask:0xf bank_mask:0xf\n"
"v_add_f32_dpp v210, v210, v210 row_half_mirror row_mask:0xf bank_mask:0xf\n"
"v_add_f32_dpp v212, v212, v212 row_half_mirror row_mask:0xf bank_mask:0xf\n"
"v_add_f32_dpp v214, v214, v214 row_half_mirror row_mask:0xf bank_mask:0xf\n"
"v_mul_f32 v200, 0x3ae38e39, v200\n"
"v_mul_f32 v202, 0x3ae38e39, v202\n"
"v_fma_f32 v202, -v200, v200, v202\n"
"v_max_f32 v202, 0, v202\n"
"v_add_f32 v202, 0x3727c5ac, v202\n"
"v_mul_f32 v204, 0x3ae38e39, v204\n"
"v_mul_f32 v206, 0x3ae38e39, v206\n"
"v_fma_f32 v206, -v204, v204, v206\n"
"v_max_f32 v206, 0, v206\n"
"v_add_f32 v206, 0x3727c5ac, v206\n"
"v_mul_f32 v208, 0x3ae38e39, v208\n"
"v_mul_f32 v210, 0x3ae38e39, v210\n"
"v_fma_f32 v210, -v208, v208, v210\n"
"v_max_f32 v210, 0, v210\n"
"v_add_f32 v210, 0x3727c5ac, v210\n"
"v_mul_f32 v212, 0x3ae38e39, v212\n"
"v_mul_f32 v214, 0x3ae38e39, v214\n"
"v_fma_f32 v214, -v212, v212, v214\n"
"v_max_f32 v214, 0, v214\n"
"v_add_f32 v214, 0x3727c5ac, v214\n"
"v_rsq_f32 v201, v202\n"
"v_rsq_f32 v205, v206\n"
"v_rsq_f32 v209, v210\n"
"v_rsq_f32 v213, v214\n"
"v_mul_f32 v202, v200, v201\n"
"v_sub_f32 v202, 0, v202\n"
"v_mov_b32 v200, v201\n"
"v_mul_f32 v206, v204, v205\n"
"v_sub_f32 v206, 0, v206\n"
"v_mov_b32 v204, v205\n"
"v_mul_f32 v210, v208, v209\n"
"v_sub_f32 v210, 0, v210\n"
"v_mov_b32 v208, v209\n"
"v_mul_f32 v214, v212, v213\n"
"v_sub_f32 v214, 0, v214\n"
"v_mov_b32 v212, v213\n"
"v_mov_b32 v237, 0xffff0000\n"
"v_lshlrev_b32 v176, 16, v0\n"
"v_and_b32 v177, v237, v0\n"
"v_pk_fma_f32 v[176:177], v[176:177], v[200:201], v[202:203] op_sel_hi:[1,0,0]\n"
"v_cvt_pk_bf16_f32 v0, v176, v177\n"
"v_lshlrev_b32 v178, 16, v1\n"
"v_and_b32 v179, v237, v1\n"
"v_pk_fma_f32 v[178:179], v[178:179], v[200:201], v[202:203] op_sel_hi:[1,0,0]\n"
"v_cvt_pk_bf16_f32 v1, v178, v179\n"
"v_lshlrev_b32 v180, 16, v2\n"
"v_and_b32 v181, v237, v2\n"
"v_pk_fma_f32 v[180:181], v[180:181], v[200:201], v[202:203] op_sel_hi:[1,0,0]\n"
"v_cvt_pk_bf16_f32 v2, v180, v181\n"
"v_lshlrev_b32 v182, 16, v3\n"
"v_and_b32 v183, v237, v3\n"
"v_pk_fma_f32 v[182:183], v[182:183], v[200:201], v[202:203] op_sel_hi:[1,0,0]\n"
"v_cvt_pk_bf16_f32 v3, v182, v183\n"
"v_lshlrev_b32 v184, 16, v4\n"
"v_and_b32 v185, v237, v4\n"
"v_pk_fma_f32 v[184:185], v[184:185], v[204:205], v[206:207] op_sel_hi:[1,0,0]\n"
"v_cvt_pk_bf16_f32 v4, v184, v185\n"
"v_lshlrev_b32 v186, 16, v5\n"
"v_and_b32 v187, v237, v5\n"
"v_pk_fma_f32 v[186:187], v[186:187], v[204:205], v[206:207] op_sel_hi:[1,0,0]\n"
"v_cvt_pk_bf16_f32 v5, v186, v187\n"
"v_lshlrev_b32 v188, 16, v6\n"
"v_and_b32 v189, v237, v6\n"
"v_pk_fma_f32 v[188:189], v[188:189], v[204:205], v[206:207] op_sel_hi:[1,0,0]\n"
"v_cvt_pk_bf16_f32 v6, v188, v189\n"
"v_lshlrev_b32 v190, 16, v7\n"
"v_and_b32 v191, v237, v7\n"
"v_pk_fma_f32 v[190:191], v[190:191], v[204:205], v[206:207] op_sel_hi:[1,0,0]\n"
"v_cvt_pk_bf16_f32 v7, v190, v191\n"
"v_lshlrev_b32 v176, 16, v8\n"
"v_and_b32 v177, v237, v8\n"
"v_pk_fma_f32 v[176:177], v[176:177], v[208:209], v[210:211] op_sel_hi:[1,0,0]\n"
"v_cvt_pk_bf16_f32 v8, v176, v177\n"
"v_lshlrev_b32 v178, 16, v9\n"
"v_and_b32 v179, v237, v9\n"
"v_pk_fma_f32 v[178:179], v[178:179], v[208:209], v[210:211] op_sel_hi:[1,0,0]\n"
"v_cvt_pk_bf16_f32 v9, v178, v179\n"
"v_lshlrev_b32 v180, 16, v10\n"
"v_and_b32 v181, v237, v10\n"
"v_pk_fma_f32 v[180:181], v[180:181], v[208:209], v[210:211] op_sel_hi:[1,0,0]\n"
"v_cvt_pk_bf16_f32 v10, v180, v181\n"
"v_lshlrev_b32 v182, 16, v11\n"
"v_and_b32 v183, v237, v11\n"
"v_pk_fma_f32 v[182:183], v[182:183], v[208:209], v[210:211] op_sel_hi:[1,0,0]\n"
"v_cvt_pk_bf16_f32 v11, v182, v183\n"
"v_lshlrev_b32 v184, 16, v12\n"
"v_and_b32 v185, v237, v12\n"
"v_pk_fma_f32 v[184:185], v[184:185], v[212:213], v[214:215] op_sel_hi:[1,0,0]\n"
"v_cvt_pk_bf16_f32 v12, v184, v185\n"
"v_lshlrev_b32 v186, 16, v13\n"
"v_and_b32 v187, v237, v13\n"
"v_pk_fma_f32 v[186:187], v[186:187], v[212:213], v[214:215] op_sel_hi:[1,0,0]\n"
"v_cvt_pk_bf16_f32 v13, v186, v187\n"
"v_lshlrev_b32 v188, 16, v14\n"
"v_and_b32 v189, v237, v14\n"
"v_pk_fma_f32 v[188:189], v[188:189], v[212:213], v[214:215] op_sel_hi:[1,0,0]\n"
"v_cvt_pk_bf16_f32 v14, v188, v189\n"
"v_lshlrev_b32 v190, 16, v15\n"
"v_and_b32 v191, v237, v15\n"
"v_pk_fma_f32 v[190:191], v[190:191], v[212:213], v[214:215] op_sel_hi:[1,0,0]\n"
"v_cvt_pk_bf16_f32 v15, v190, v191\n"
"v_lshlrev_b32 v176, 16, v16\n"
"v_and_b32 v177, v237, v16\n"
"v_pk_fma_f32 v[176:177], v[176:177], v[200:201], v[202:203] op_sel_hi:[1,0,0]\n"
"v_cvt_pk_bf16_f32 v16, v176, v177\n"
"v_lshlrev_b32 v178, 16, v17\n"
"v_and_b32 v179, v237, v17\n"
"v_pk_fma_f32 v[178:179], v[178:179], v[200:201], v[202:203] op_sel_hi:[1,0,0]\n"
"v_cvt_pk_bf16_f32 v17, v178, v179\n"
"v_lshlrev_b32 v180, 16, v18\n"
"v_and_b32 v181, v237, v18\n"
"v_pk_fma_f32 v[180:181], v[180:181], v[200:201], v[202:203] op_sel_hi:[1,0,0]\n"
"v_cvt_pk_bf16_f32 v18, v180, v181\n"
"v_lshlrev_b32 v182, 16, v19\n"
"v_and_b32 v183, v237, v19\n"
"v_pk_fma_f32 v[182:183], v[182:183], v[200:201], v[202:203] op_sel_hi:[1,0,0]\n"
"v_cvt_pk_bf16_f32 v19, v182, v183\n"
"v_lshlrev_b32 v184, 16, v20\n"
"v_and_b32 v185, v237, v20\n"
"v_pk_fma_f32 v[184:185], v[184:185], v[204:205], v[206:207] op_sel_hi:[1,0,0]\n"
"v_cvt_pk_bf16_f32 v20, v184, v185\n"
"v_lshlrev_b32 v186, 16, v21\n"
"v_and_b32 v187, v237, v21\n"
"v_pk_fma_f32 v[186:187], v[186:187], v[204:205], v[206:207] op_sel_hi:[1,0,0]\n"
"v_cvt_pk_bf16_f32 v21, v186, v187\n"
"v_lshlrev_b32 v188, 16, v22\n"
"v_and_b32 v189, v237, v22\n"
"v_pk_fma_f32 v[188:189], v[188:189], v[204:205], v[206:207] op_sel_hi:[1,0,0]\n"
"v_cvt_pk_bf16_f32 v22, v188, v189\n"
"v_lshlrev_b32 v190, 16, v23\n"
"v_and_b32 v191, v237, v23\n"
"v_pk_fma_f32 v[190:191], v[190:191], v[204:205], v[206:207] op_sel_hi:[1,0,0]\n"
"v_cvt_pk_bf16_f32 v23, v190, v191\n"
"v_lshlrev_b32 v176, 16, v24\n"
"v_and_b32 v177, v237, v24\n"
"v_pk_fma_f32 v[176:177], v[176:177], v[208:209], v[210:211] op_sel_hi:[1,0,0]\n"
"v_cvt_pk_bf16_f32 v24, v176, v177\n"
"v_lshlrev_b32 v178, 16, v25\n"
"v_and_b32 v179, v237, v25\n"
"v_pk_fma_f32 v[178:179], v[178:179], v[208:209], v[210:211] op_sel_hi:[1,0,0]\n"
"v_cvt_pk_bf16_f32 v25, v178, v179\n"
"v_lshlrev_b32 v180, 16, v26\n"
"v_and_b32 v181, v237, v26\n"
"v_pk_fma_f32 v[180:181], v[180:181], v[208:209], v[210:211] op_sel_hi:[1,0,0]\n"
"v_cvt_pk_bf16_f32 v26, v180, v181\n"
"v_lshlrev_b32 v182, 16, v27\n"
"v_and_b32 v183, v237, v27\n"
"v_pk_fma_f32 v[182:183], v[182:183], v[208:209], v[210:211] op_sel_hi:[1,0,0]\n"
"v_cvt_pk_bf16_f32 v27, v182, v183\n"
"v_lshlrev_b32 v184, 16, v28\n"
"v_and_b32 v185, v237, v28\n"
"v_pk_fma_f32 v[184:185], v[184:185], v[212:213], v[214:215] op_sel_hi:[1,0,0]\n"
"v_cvt_pk_bf16_f32 v28, v184, v185\n"
"v_lshlrev_b32 v186, 16, v29\n"
"v_and_b32 v187, v237, v29\n"
"v_pk_fma_f32 v[186:187], v[186:187], v[212:213], v[214:215] op_sel_hi:[1,0,0]\n"
"v_cvt_pk_bf16_f32 v29, v186, v187\n"
"v_lshlrev_b32 v188, 16, v30\n"
"v_and_b32 v189, v237, v30\n"
"v_pk_fma_f32 v[188:189], v[188:189], v[212:213], v[214:215] op_sel_hi:[1,0,0]\n"
"v_cvt_pk_bf16_f32 v30, v188, v189\n"
"v_lshlrev_b32 v190, 16, v31\n"
"v_and_b32 v191, v237, v31\n"
"v_pk_fma_f32 v[190:191], v[190:191], v[212:213], v[214:215] op_sel_hi:[1,0,0]\n"
"v_cvt_pk_bf16_f32 v31, v190, v191\n"
"v_lshlrev_b32 v176, 16, v32\n"
"v_and_b32 v177, v237, v32\n"
"v_pk_fma_f32 v[176:177], v[176:177], v[200:201], v[202:203] op_sel_hi:[1,0,0]\n"
"v_cvt_pk_bf16_f32 v32, v176, v177\n"
"v_lshlrev_b32 v178, 16, v33\n"
"v_and_b32 v179, v237, v33\n"
"v_pk_fma_f32 v[178:179], v[178:179], v[200:201], v[202:203] op_sel_hi:[1,0,0]\n"
"v_cvt_pk_bf16_f32 v33, v178, v179\n"
"v_lshlrev_b32 v180, 16, v34\n"
"v_and_b32 v181, v237, v34\n"
"v_pk_fma_f32 v[180:181], v[180:181], v[200:201], v[202:203] op_sel_hi:[1,0,0]\n"
"v_cvt_pk_bf16_f32 v34, v180, v181\n"
"v_lshlrev_b32 v182, 16, v35\n"
"v_and_b32 v183, v237, v35\n"
"v_pk_fma_f32 v[182:183], v[182:183], v[200:201], v[202:203] op_sel_hi:[1,0,0]\n"
"v_cvt_pk_bf16_f32 v35, v182, v183\n"
"v_lshlrev_b32 v184, 16, v36\n"
"v_and_b32 v185, v237, v36\n"
"v_pk_fma_f32 v[184:185], v[184:185], v[204:205], v[206:207] op_sel_hi:[1,0,0]\n"
"v_cvt_pk_bf16_f32 v36, v184, v185\n"
"v_lshlrev_b32 v186, 16, v37\n"
"v_and_b32 v187, v237, v37\n"
"v_pk_fma_f32 v[186:187], v[186:187], v[204:205], v[206:207] op_sel_hi:[1,0,0]\n"
"v_cvt_pk_bf16_f32 v37, v186, v187\n"
"v_lshlrev_b32 v188, 16, v38\n"
"v_and_b32 v189, v237, v38\n"
"v_pk_fma_f32 v[188:189], v[188:189], v[204:205], v[206:207] op_sel_hi:[1,0,0]\n"
"v_cvt_pk_bf16_f32 v38, v188, v189\n"
"v_lshlrev_b32 v190, 16, v39\n"
"v_and_b32 v191, v237, v39\n"
"v_pk_fma_f32 v[190:191], v[190:191], v[204:205], v[206:207] op_sel_hi:[1,0,0]\n"
"v_cvt_pk_bf16_f32 v39, v190, v191\n"
"v_lshlrev_b32 v176, 16, v40\n"
"v_and_b32 v177, v237, v40\n"
"v_pk_fma_f32 v[176:177], v[176:177], v[208:209], v[210:211] op_sel_hi:[1,0,0]\n"
"v_cvt_pk_bf16_f32 v40, v176, v177\n"
"v_lshlrev_b32 v178, 16, v41\n"
"v_and_b32 v179, v237, v41\n"
"v_pk_fma_f32 v[178:179], v[178:179], v[208:209], v[210:211] op_sel_hi:[1,0,0]\n"
"v_cvt_pk_bf16_f32 v41, v178, v179\n"
"v_lshlrev_b32 v180, 16, v42\n"
"v_and_b32 v181, v237, v42\n"
"v_pk_fma_f32 v[180:181], v[180:181], v[208:209], v[210:211] op_sel_hi:[1,0,0]\n"
"v_cvt_pk_bf16_f32 v42, v180, v181\n"
"v_lshlrev_b32 v182, 16, v43\n"
"v_and_b32 v183, v237, v43\n"
"v_pk_fma_f32 v[182:183], v[182:183], v[208:209], v[210:211] op_sel_hi:[1,0,0]\n"
"v_cvt_pk_bf16_f32 v43, v182, v183\n"
"v_lshlrev_b32 v184, 16, v44\n"
"v_and_b32 v185, v237, v44\n"
"v_pk_fma_f32 v[184:185], v[184:185], v[212:213], v[214:215] op_sel_hi:[1,0,0]\n"
"v_cvt_pk_bf16_f32 v44, v184, v185\n"
"v_lshlrev_b32 v186, 16, v45\n"
"v_and_b32 v187, v237, v45\n"
"v_pk_fma_f32 v[186:187], v[186:187], v[212:213], v[214:215] op_sel_hi:[1,0,0]\n"
"v_cvt_pk_bf16_f32 v45, v186, v187\n"
"v_lshlrev_b32 v188, 16, v46\n"
"v_and_b32 v189, v237, v46\n"
"v_pk_fma_f32 v[188:189], v[188:189], v[212:213], v[214:215] op_sel_hi:[1,0,0]\n"
"v_cvt_pk_bf16_f32 v46, v188, v189\n"
"v_lshlrev_b32 v190, 16, v47\n"
"v_and_b32 v191, v237, v47\n"
"v_pk_fma_f32 v[190:191], v[190:191], v[212:213], v[214:215] op_sel_hi:[1,0,0]\n"
"v_cvt_pk_bf16_f32 v47, v190, v191\n"
"v_lshlrev_b32 v176, 16, v48\n"
"v_and_b32 v177, v237, v48\n"
"v_pk_fma_f32 v[176:177], v[176:177], v[200:201], v[202:203] op_sel_hi:[1,0,0]\n"
"v_cvt_pk_bf16_f32 v48, v176, v177\n"
"v_lshlrev_b32 v178, 16, v49\n"
"v_and_b32 v179, v237, v49\n"
"v_pk_fma_f32 v[178:179], v[178:179], v[200:201], v[202:203] op_sel_hi:[1,0,0]\n"
"v_cvt_pk_bf16_f32 v49, v178, v179\n"
"v_lshlrev_b32 v180, 16, v50\n"
"v_and_b32 v181, v237, v50\n"
"v_pk_fma_f32 v[180:181], v[180:181], v[200:201], v[202:203] op_sel_hi:[1,0,0]\n"
"v_cvt_pk_bf16_f32 v50, v180, v181\n"
"v_lshlrev_b32 v182, 16, v51\n"
"v_and_b32 v183, v237, v51\n"
"v_pk_fma_f32 v[182:183], v[182:183], v[200:201], v[202:203] op_sel_hi:[1,0,0]\n"
"v_cvt_pk_bf16_f32 v51, v182, v183\n"
"v_lshlrev_b32 v184, 16, v52\n"
"v_and_b32 v185, v237, v52\n"
"v_pk_fma_f32 v[184:185], v[184:185], v[204:205], v[206:207] op_sel_hi:[1,0,0]\n"
"v_cvt_pk_bf16_f32 v52, v184, v185\n"
"v_lshlrev_b32 v186, 16, v53\n"
"v_and_b32 v187, v237, v53\n"
"v_pk_fma_f32 v[186:187], v[186:187], v[204:205], v[206:207] op_sel_hi:[1,0,0]\n"
"v_cvt_pk_bf16_f32 v53, v186, v187\n"
"v_lshlrev_b32 v188, 16, v54\n"
"v_and_b32 v189, v237, v54\n"
"v_pk_fma_f32 v[188:189], v[188:189], v[204:205], v[206:207] op_sel_hi:[1,0,0]\n"
"v_cvt_pk_bf16_f32 v54, v188, v189\n"
"v_lshlrev_b32 v190, 16, v55\n"
"v_and_b32 v191, v237, v55\n"
"v_pk_fma_f32 v[190:191], v[190:191], v[204:205], v[206:207] op_sel_hi:[1,0,0]\n"
"v_cvt_pk_bf16_f32 v55, v190, v191\n"
"v_lshlrev_b32 v176, 16, v56\n"
"v_and_b32 v177, v237, v56\n"
"v_pk_fma_f32 v[176:177], v[176:177], v[208:209], v[210:211] op_sel_hi:[1,0,0]\n"
"v_cvt_pk_bf16_f32 v56, v176, v177\n"
"v_lshlrev_b32 v178, 16, v57\n"
"v_and_b32 v179, v237, v57\n"
"v_pk_fma_f32 v[178:179], v[178:179], v[208:209], v[210:211] op_sel_hi:[1,0,0]\n"
"v_cvt_pk_bf16_f32 v57, v178, v179\n"
"v_lshlrev_b32 v180, 16, v58\n"
"v_and_b32 v181, v237, v58\n"
"v_pk_fma_f32 v[180:181], v[180:181], v[208:209], v[210:211] op_sel_hi:[1,0,0]\n"
"v_cvt_pk_bf16_f32 v58, v180, v181\n"
"v_lshlrev_b32 v182, 16, v59\n"
"v_and_b32 v183, v237, v59\n"
"v_pk_fma_f32 v[182:183], v[182:183], v[208:209], v[210:211] op_sel_hi:[1,0,0]\n"
"v_cvt_pk_bf16_f32 v59, v182, v183\n"
"v_lshlrev_b32 v184, 16, v60\n"
"v_and_b32 v185, v237, v60\n"
"v_pk_fma_f32 v[184:185], v[184:185], v[212:213], v[214:215] op_sel_hi:[1,0,0]\n"
"v_cvt_pk_bf16_f32 v60, v184, v185\n"
"v_lshlrev_b32 v186, 16, v61\n"
"v_and_b32 v187, v237, v61\n"
"v_pk_fma_f32 v[186:187], v[186:187], v[212:213], v[214:215] op_sel_hi:[1,0,0]\n"
"v_cvt_pk_bf16_f32 v61, v186, v187\n"
"v_lshlrev_b32 v188, 16, v62\n"
"v_and_b32 v189, v237, v62\n"
"v_pk_fma_f32 v[188:189], v[188:189], v[212:213], v[214:215] op_sel_hi:[1,0,0]\n"
"v_cvt_pk_bf16_f32 v62, v188, v189\n"
"v_lshlrev_b32 v190, 16, v63\n"
"v_and_b32 v191, v237, v63\n"
"v_pk_fma_f32 v[190:191], v[190:191], v[212:213], v[214:215] op_sel_hi:[1,0,0]\n"
"v_cvt_pk_bf16_f32 v63, v190, v191\n"
"v_lshlrev_b32 v176, 16, v64\n"
"v_and_b32 v177, v237, v64\n"
"v_pk_fma_f32 v[176:177], v[176:177], v[200:201], v[202:203] op_sel_hi:[1,0,0]\n"
"v_cvt_pk_bf16_f32 v64, v176, v177\n"
"v_lshlrev_b32 v178, 16, v65\n"
"v_and_b32 v179, v237, v65\n"
"v_pk_fma_f32 v[178:179], v[178:179], v[200:201], v[202:203] op_sel_hi:[1,0,0]\n"
"v_cvt_pk_bf16_f32 v65, v178, v179\n"
"v_lshlrev_b32 v180, 16, v66\n"
"v_and_b32 v181, v237, v66\n"
"v_pk_fma_f32 v[180:181], v[180:181], v[200:201], v[202:203] op_sel_hi:[1,0,0]\n"
"v_cvt_pk_bf16_f32 v66, v180, v181\n"
"v_lshlrev_b32 v182, 16, v67\n"
"v_and_b32 v183, v237, v67\n"
"v_pk_fma_f32 v[182:183], v[182:183], v[200:201], v[202:203] op_sel_hi:[1,0,0]\n"
"v_cvt_pk_bf16_f32 v67, v182, v183\n"
"v_lshlrev_b32 v184, 16, v68\n"
"v_and_b32 v185, v237, v68\n"
"v_pk_fma_f32 v[184:185], v[184:185], v[204:205], v[206:207] op_sel_hi:[1,0,0]\n"
"v_cvt_pk_bf16_f32 v68, v184, v185\n"
"v_lshlrev_b32 v186, 16, v69\n"
"v_and_b32 v187, v237, v69\n"
"v_pk_fma_f32 v[186:187], v[186:187], v[204:205], v[206:207] op_sel_hi:[1,0,0]\n"
"v_cvt_pk_bf16_f32 v69, v186, v187\n"
"v_lshlrev_b32 v188, 16, v70\n"
"v_and_b32 v189, v237, v70\n"
"v_pk_fma_f32 v[188:189], v[188:189], v[204:205], v[206:207] op_sel_hi:[1,0,0]\n"
"v_cvt_pk_bf16_f32 v70, v188, v189\n"
"v_lshlrev_b32 v190, 16, v71\n"
"v_and_b32 v191, v237, v71\n"
"v_pk_fma_f32 v[190:191], v[190:191], v[204:205], v[206:207] op_sel_hi:[1,0,0]\n"
"v_cvt_pk_bf16_f32 v71, v190, v191\n"
"v_lshlrev_b32 v176, 16, v72\n"
"v_and_b32 v177, v237, v72\n"
"v_pk_fma_f32 v[176:177], v[176:177], v[208:209], v[210:211] op_sel_hi:[1,0,0]\n"
"v_cvt_pk_bf16_f32 v72, v176, v177\n"
"v_lshlrev_b32 v178, 16, v73\n"
"v_and_b32 v179, v237, v73\n"
"v_pk_fma_f32 v[178:179], v[178:179], v[208:209], v[210:211] op_sel_hi:[1,0,0]\n"
"v_cvt_pk_bf16_f32 v73, v178, v179\n"
"v_lshlrev_b32 v180, 16, v74\n"
"v_and_b32 v181, v237, v74\n"
"v_pk_fma_f32 v[180:181], v[180:181], v[208:209], v[210:211] op_sel_hi:[1,0,0]\n"
"v_cvt_pk_bf16_f32 v74, v180, v181\n"
"v_lshlrev_b32 v182, 16, v75\n"
"v_and_b32 v183, v237, v75\n"
"v_pk_fma_f32 v[182:183], v[182:183], v[208:209], v[210:211] op_sel_hi:[1,0,0]\n"
"v_cvt_pk_bf16_f32 v75, v182, v183\n"
"v_lshlrev_b32 v184, 16, v76\n"
"v_and_b32 v185, v237, v76\n"
"v_pk_fma_f32 v[184:185], v[184:185], v[212:213], v[214:215] op_sel_hi:[1,0,0]\n"
"v_cvt_pk_bf16_f32 v76, v184, v185\n"
"v_lshlrev_b32 v186, 16, v77\n"
"v_and_b32 v187, v237, v77\n"
"v_pk_fma_f32 v[186:187], v[186:187], v[212:213], v[214:215] op_sel_hi:[1,0,0]\n"
"v_cvt_pk_bf16_f32 v77, v186, v187\n"
"v_lshlrev_b32 v188, 16, v78\n"
"v_and_b32 v189, v237, v78\n"
"v_pk_fma_f32 v[188:189], v[188:189], v[212:213], v[214:215] op_sel_hi:[1,0,0]\n"
"v_cvt_pk_bf16_f32 v78, v188, v189\n"
"v_lshlrev_b32 v190, 16, v79\n"
"v_and_b32 v191, v237, v79\n"
"v_pk_fma_f32 v[190:191], v[190:191], v[212:213], v[214:215] op_sel_hi:[1,0,0]\n"
"v_cvt_pk_bf16_f32 v79, v190, v191\n"
"v_lshlrev_b32 v176, 16, v80\n"
"v_and_b32 v177, v237, v80\n"
"v_pk_fma_f32 v[176:177], v[176:177], v[200:201], v[202:203] op_sel_hi:[1,0,0]\n"
"v_cvt_pk_bf16_f32 v80, v176, v177\n"
"v_lshlrev_b32 v178, 16, v81\n"
"v_and_b32 v179, v237, v81\n"
"v_pk_fma_f32 v[178:179], v[178:179], v[200:201], v[202:203] op_sel_hi:[1,0,0]\n"
"v_cvt_pk_bf16_f32 v81, v178, v179\n"
"v_lshlrev_b32 v180, 16, v82\n"
"v_and_b32 v181, v237, v82\n"
"v_pk_fma_f32 v[180:181], v[180:181], v[200:201], v[202:203] op_sel_hi:[1,0,0]\n"
"v_cvt_pk_bf16_f32 v82, v180, v181\n"
"v_lshlrev_b32 v182, 16, v83\n"
"v_and_b32 v183, v237, v83\n"
"v_pk_fma_f32 v[182:183], v[182:183], v[200:201], v[202:203] op_sel_hi:[1,0,0]\n"
"v_cvt_pk_bf16_f32 v83, v182, v183\n"
"v_lshlrev_b32 v184, 16, v84\n"
"v_and_b32 v185, v237, v84\n"
"v_pk_fma_f32 v[184:185], v[184:185], v[204:205], v[206:207] op_sel_hi:[1,0,0]\n"
"v_cvt_pk_bf16_f32 v84, v184, v185\n"
"v_lshlrev_b32 v186, 16, v85\n"
"v_and_b32 v187, v237, v85\n"
"v_pk_fma_f32 v[186:187], v[186:187], v[204:205], v[206:207] op_sel_hi:[1,0,0]\n"
"v_cvt_pk_bf16_f32 v85, v186, v187\n"
"v_lshlrev_b32 v188, 16, v86\n"
"v_and_b32 v189, v237, v86\n"
"v_pk_fma_f32 v[188:189], v[188:189], v[204:205], v[206:207] op_sel_hi:[1,0,0]\n"
"v_cvt_pk_bf16_f32 v86, v188, v189\n"
"v_lshlrev_b32 v190, 16, v87\n"
"v_and_b32 v191, v237, v87\n"
"v_pk_fma_f32 v[190:191], v[190:191], v[204:205], v[206:207] op_sel_hi:[1,0,0]\n"
"v_cvt_pk_bf16_f32 v87, v190, v191\n"
"v_lshlrev_b32 v176, 16, v88\n"
"v_and_b32 v177, v237, v88\n"
"v_pk_fma_f32 v[176:177], v[176:177], v[208:209], v[210:211] op_sel_hi:[1,0,0]\n"
"v_cvt_pk_bf16_f32 v88, v176, v177\n"
"v_lshlrev_b32 v178, 16, v89\n"
"v_and_b32 v179, v237, v89\n"
"v_pk_fma_f32 v[178:179], v[178:179], v[208:209], v[210:211] op_sel_hi:[1,0,0]\n"
"v_cvt_pk_bf16_f32 v89, v178, v179\n"
"v_lshlrev_b32 v180, 16, v90\n"
"v_and_b32 v181, v237, v90\n"
"v_pk_fma_f32 v[180:181], v[180:181], v[208:209], v[210:211] op_sel_hi:[1,0,0]\n"
"v_cvt_pk_bf16_f32 v90, v180, v181\n"
"v_lshlrev_b32 v182, 16, v91\n"
"v_and_b32 v183, v237, v91\n"
"v_pk_fma_f32 v[182:183], v[182:183], v[208:209], v[210:211] op_sel_hi:[1,0,0]\n"
"v_cvt_pk_bf16_f32 v91, v182, v183\n"
"v_lshlrev_b32 v184, 16, v92\n"
"v_and_b32 v185, v237, v92\n"
"v_pk_fma_f32 v[184:185], v[184:185], v[212:213], v[214:215] op_sel_hi:[1,0,0]\n"
"v_cvt_pk_bf16_f32 v92, v184, v185\n"
"v_lshlrev_b32 v186, 16, v93\n"
"v_and_b32 v187, v237, v93\n"
"v_pk_fma_f32 v[186:187], v[186:187], v[212:213], v[214:215] op_sel_hi:[1,0,0]\n"
"v_cvt_pk_bf16_f32 v93, v186, v187\n"
"v_lshlrev_b32 v188, 16, v94\n"
"v_and_b32 v189, v237, v94\n"
"v_pk_fma_f32 v[188:189], v[188:189], v[212:213], v[214:215] op_sel_hi:[1,0,0]\n"
"v_cvt_pk_bf16_f32 v94, v188, v189\n"
"v_lshlrev_b32 v190, 16, v95\n"
"v_and_b32 v191, v237, v95\n"
"v_pk_fma_f32 v[190:191], v[190:191], v[212:213], v[214:215] op_sel_hi:[1,0,0]\n"
"v_cvt_pk_bf16_f32 v95, v190, v191\n"
"v_lshlrev_b32 v176, 16, v96\n"
"v_and_b32 v177, v237, v96\n"
"v_pk_fma_f32 v[176:177], v[176:177], v[200:201], v[202:203] op_sel_hi:[1,0,0]\n"
"v_cvt_pk_bf16_f32 v96, v176, v177\n"
"v_lshlrev_b32 v178, 16, v97\n"
"v_and_b32 v179, v237, v97\n"
"v_pk_fma_f32 v[178:179], v[178:179], v[200:201], v[202:203] op_sel_hi:[1,0,0]\n"
"v_cvt_pk_bf16_f32 v97, v178, v179\n"
"v_lshlrev_b32 v180, 16, v98\n"
"v_and_b32 v181, v237, v98\n"
"v_pk_fma_f32 v[180:181], v[180:181], v[200:201], v[202:203] op_sel_hi:[1,0,0]\n"
"v_cvt_pk_bf16_f32 v98, v180, v181\n"
"v_lshlrev_b32 v182, 16, v99\n"
"v_and_b32 v183, v237, v99\n"
"v_pk_fma_f32 v[182:183], v[182:183], v[200:201], v[202:203] op_sel_hi:[1,0,0]\n"
"v_cvt_pk_bf16_f32 v99, v182, v183\n"
"v_lshlrev_b32 v184, 16, v100\n"
"v_and_b32 v185, v237, v100\n"
"v_pk_fma_f32 v[184:185], v[184:185], v[204:205], v[206:207] op_sel_hi:[1,0,0]\n"
"v_cvt_pk_bf16_f32 v100, v184, v185\n"
"v_lshlrev_b32 v186, 16, v101\n"
"v_and_b32 v187, v237, v101\n"
"v_pk_fma_f32 v[186:187], v[186:187], v[204:205], v[206:207] op_sel_hi:[1,0,0]\n"
"v_cvt_pk_bf16_f32 v101, v186, v187\n"
"v_lshlrev_b32 v188, 16, v102\n"
"v_and_b32 v189, v237, v102\n"
"v_pk_fma_f32 v[188:189], v[188:189], v[204:205], v[206:207] op_sel_hi:[1,0,0]\n"
"v_cvt_pk_bf16_f32 v102, v188, v189\n"
"v_lshlrev_b32 v190, 16, v103\n"
"v_and_b32 v191, v237, v103\n"
"v_pk_fma_f32 v[190:191], v[190:191], v[204:205], v[206:207] op_sel_hi:[1,0,0]\n"
"v_cvt_pk_bf16_f32 v103, v190, v191\n"
"v_lshlrev_b32 v176, 16, v104\n"
"v_and_b32 v177, v237, v104\n"
"v_pk_fma_f32 v[176:177], v[176:177], v[208:209], v[210:211] op_sel_hi:[1,0,0]\n"
"v_cvt_pk_bf16_f32 v104, v176, v177\n"
"v_lshlrev_b32 v178, 16, v105\n"
"v_and_b32 v179, v237, v105\n"
"v_pk_fma_f32 v[178:179], v[178:179], v[208:209], v[210:211] op_sel_hi:[1,0,0]\n"
"v_cvt_pk_bf16_f32 v105, v178, v179\n"
"v_lshlrev_b32 v180, 16, v106\n"
"v_and_b32 v181, v237, v106\n"
"v_pk_fma_f32 v[180:181], v[180:181], v[208:209], v[210:211] op_sel_hi:[1,0,0]\n"
"v_cvt_pk_bf16_f32 v106, v180, v181\n"
"v_lshlrev_b32 v182, 16, v107\n"
"v_and_b32 v183, v237, v107\n"
"v_pk_fma_f32 v[182:183], v[182:183], v[208:209], v[210:211] op_sel_hi:[1,0,0]\n"
"v_cvt_pk_bf16_f32 v107, v182, v183\n"
"v_lshlrev_b32 v184, 16, v108\n"
"v_and_b32 v185, v237, v108\n"
"v_pk_fma_f32 v[184:185], v[184:185], v[212:213], v[214:215] op_sel_hi:[1,0,0]\n"
"v_cvt_pk_bf16_f32 v108, v184, v185\n"
"v_lshlrev_b32 v186, 16, v109\n"
"v_and_b32 v187, v237, v109\n"
"v_pk_fma_f32 v[186:187], v[186:187], v[212:213], v[214:215] op_sel_hi:[1,0,0]\n"
"v_cvt_pk_bf16_f32 v109, v186, v187\n"
"v_lshlrev_b32 v188, 16, v110\n"
"v_and_b32 v189, v237, v110\n"
"v_pk_fma_f32 v[188:189], v[188:189], v[212:213], v[214:215] op_sel_hi:[1,0,0]\n"
"v_cvt_pk_bf16_f32 v110, v188, v189\n"
"v_lshlrev_b32 v190, 16, v111\n"
"v_and_b32 v191, v237, v111\n"
"v_pk_fma_f32 v[190:191], v[190:191], v[212:213], v[214:215] op_sel_hi:[1,0,0]\n"
"v_cvt_pk_bf16_f32 v111, v190, v191\n"
"v_lshlrev_b32 v176, 16, v112\n"
"v_and_b32 v177, v237, v112\n"
"v_pk_fma_f32 v[176:177], v[176:177], v[200:201], v[202:203] op_sel_hi:[1,0,0]\n"
"v_cvt_pk_bf16_f32 v112, v176, v177\n"
"v_lshlrev_b32 v178, 16, v113\n"
"v_and_b32 v179, v237, v113\n"
"v_pk_fma_f32 v[178:179], v[178:179], v[200:201], v[202:203] op_sel_hi:[1,0,0]\n"
"v_cvt_pk_bf16_f32 v113, v178, v179\n"
"v_lshlrev_b32 v180, 16, v114\n"
"v_and_b32 v181, v237, v114\n"
"v_pk_fma_f32 v[180:181], v[180:181], v[200:201], v[202:203] op_sel_hi:[1,0,0]\n"
"v_cvt_pk_bf16_f32 v114, v180, v181\n"
"v_lshlrev_b32 v182, 16, v115\n"
"v_and_b32 v183, v237, v115\n"
"v_pk_fma_f32 v[182:183], v[182:183], v[200:201], v[202:203] op_sel_hi:[1,0,0]\n"
"v_cvt_pk_bf16_f32 v115, v182, v183\n"
"v_lshlrev_b32 v184, 16, v116\n"
"v_and_b32 v185, v237, v116\n"
"v_pk_fma_f32 v[184:185], v[184:185], v[204:205], v[206:207] op_sel_hi:[1,0,0]\n"
"v_cvt_pk_bf16_f32 v116, v184, v185\n"
"v_lshlrev_b32 v186, 16, v117\n"
"v_and_b32 v187, v237, v117\n"
"v_pk_fma_f32 v[186:187], v[186:187], v[204:205], v[206:207] op_sel_hi:[1,0,0]\n"
"v_cvt_pk_bf16_f32 v117, v186, v187\n"
"v_lshlrev_b32 v188, 16, v118\n"
"v_and_b32 v189, v237, v118\n"
"v_pk_fma_f32 v[188:189], v[188:189], v[204:205], v[206:207] op_sel_hi:[1,0,0]\n"
"v_cvt_pk_bf16_f32 v118, v188, v189\n"
"v_lshlrev_b32 v190, 16, v119\n"
"v_and_b32 v191, v237, v119\n"
"v_pk_fma_f32 v[190:191], v[190:191], v[204:205], v[206:207] op_sel_hi:[1,0,0]\n"
"v_cvt_pk_bf16_f32 v119, v190, v191\n"
"v_lshlrev_b32 v176, 16, v120\n"
"v_and_b32 v177, v237, v120\n"
"v_pk_fma_f32 v[176:177], v[176:177], v[208:209], v[210:211] op_sel_hi:[1,0,0]\n"
"v_cvt_pk_bf16_f32 v120, v176, v177\n"
"v_lshlrev_b32 v178, 16, v121\n"
"v_and_b32 v179, v237, v121\n"
"v_pk_fma_f32 v[178:179], v[178:179], v[208:209], v[210:211] op_sel_hi:[1,0,0]\n"
"v_cvt_pk_bf16_f32 v121, v178, v179\n"
"v_lshlrev_b32 v180, 16, v122\n"
"v_and_b32 v181, v237, v122\n"
"v_pk_fma_f32 v[180:181], v[180:181], v[208:209], v[210:211] op_sel_hi:[1,0,0]\n"
"v_cvt_pk_bf16_f32 v122, v180, v181\n"
"v_lshlrev_b32 v182, 16, v123\n"
"v_and_b32 v183, v237, v123\n"
"v_pk_fma_f32 v[182:183], v[182:183], v[208:209], v[210:211] op_sel_hi:[1,0,0]\n"
"v_cvt_pk_bf16_f32 v123, v182, v183\n"
"v_lshlrev_b32 v184, 16, v124\n"
"v_and_b32 v185, v237, v124\n"
"v_pk_fma_f32 v[184:185], v[184:185], v[212:213], v[214:215] op_sel_hi:[1,0,0]\n"
"v_cvt_pk_bf16_f32 v124, v184, v185\n"
"v_lshlrev_b32 v186, 16, v125\n"
"v_and_b32 v187, v237, v125\n"
"v_pk_fma_f32 v[186:187], v[186:187], v[212:213], v[214:215] op_sel_hi:[1,0,0]\n"
"v_cvt_pk_bf16_f32 v125, v186, v187\n"
"v_lshlrev_b32 v188, 16, v126\n"
"v_and_b32 v189, v237, v126\n"
"v_pk_fma_f32 v[188:189], v[188:189], v[212:213], v[214:215] op_sel_hi:[1,0,0]\n"
"v_cvt_pk_bf16_f32 v126, v188, v189\n"
"v_lshlrev_b32 v190, 16, v127\n"
"v_and_b32 v191, v237, v127\n"
"v_pk_fma_f32 v[190:191], v[190:191], v[212:213], v[214:215] op_sel_hi:[1,0,0]\n"
"v_cvt_pk_bf16_f32 v127, v190, v191\n"
"v_lshlrev_b32 v176, 16, v128\n"
"v_and_b32 v177, v237, v128\n"
"v_pk_fma_f32 v[176:177], v[176:177], v[200:201], v[202:203] op_sel_hi:[1,0,0]\n"
"v_cvt_pk_bf16_f32 v128, v176, v177\n"
"v_lshlrev_b32 v178, 16, v129\n"
"v_and_b32 v179, v237, v129\n"
"v_pk_fma_f32 v[178:179], v[178:179], v[200:201], v[202:203] op_sel_hi:[1,0,0]\n"
"v_cvt_pk_bf16_f32 v129, v178, v179\n"
"v_lshlrev_b32 v180, 16, v130\n"
"v_and_b32 v181, v237, v130\n"
"v_pk_fma_f32 v[180:181], v[180:181], v[200:201], v[202:203] op_sel_hi:[1,0,0]\n"
"v_cvt_pk_bf16_f32 v130, v180, v181\n"
"v_lshlrev_b32 v182, 16, v131\n"
"v_and_b32 v183, v237, v131\n"
"v_pk_fma_f32 v[182:183], v[182:183], v[200:201], v[202:203] op_sel_hi:[1,0,0]\n"
"v_cvt_pk_bf16_f32 v131, v182, v183\n"
"v_lshlrev_b32 v184, 16, v132\n"
"v_and_b32 v185, v237, v132\n"
"v_pk_fma_f32 v[184:185], v[184:185], v[204:205], v[206:207] op_sel_hi:[1,0,0]\n"
"v_cvt_pk_bf16_f32 v132, v184, v185\n"
"v_lshlrev_b32 v186, 16, v133\n"
"v_and_b32 v187, v237, v133\n"
"v_pk_fma_f32 v[186:187], v[186:187], v[204:205], v[206:207] op_sel_hi:[1,0,0]\n"
"v_cvt_pk_bf16_f32 v133, v186, v187\n"
"v_lshlrev_b32 v188, 16, v134\n"
"v_and_b32 v189, v237, v134\n"
"v_pk_fma_f32 v[188:189], v[188:189], v[204:205], v[206:207] op_sel_hi:[1,0,0]\n"
"v_cvt_pk_bf16_f32 v134, v188, v189\n"
"v_lshlrev_b32 v190, 16, v135\n"
"v_and_b32 v191, v237, v135\n"
"v_pk_fma_f32 v[190:191], v[190:191], v[204:205], v[206:207] op_sel_hi:[1,0,0]\n"
"v_cvt_pk_bf16_f32 v135, v190, v191\n"
"v_lshlrev_b32 v176, 16, v136\n"
"v_and_b32 v177, v237, v136\n"
"v_pk_fma_f32 v[176:177], v[176:177], v[208:209], v[210:211] op_sel_hi:[1,0,0]\n"
"v_cvt_pk_bf16_f32 v136, v176, v177\n"
"v_lshlrev_b32 v178, 16, v137\n"
"v_and_b32 v179, v237, v137\n"
"v_pk_fma_f32 v[178:179], v[178:179], v[208:209], v[210:211] op_sel_hi:[1,0,0]\n"
"v_cvt_pk_bf16_f32 v137, v178, v179\n"
"v_lshlrev_b32 v180, 16, v138\n"
"v_and_b32 v181, v237, v138\n"
"v_pk_fma_f32 v[180:181], v[180:181], v[208:209], v[210:211] op_sel_hi:[1,0,0]\n"
"v_cvt_pk_bf16_f32 v138, v180, v181\n"
"v_lshlrev_b32 v182, 16, v139\n"
"v_and_b32 v183, v237, v139\n"
"v_pk_fma_f32 v[182:183], v[182:183], v[208:209], v[210:211] op_sel_hi:[1,0,0]\n"
"v_cvt_pk_bf16_f32 v139, v182, v183\n"
"v_lshlrev_b32 v184, 16, v140\n"
"v_and_b32 v185, v237, v140\n"
"v_pk_fma_f32 v[184:185], v[184:185], v[212:213], v[214:215] op_sel_hi:[1,0,0]\n"
"v_cvt_pk_bf16_f32 v140, v184, v185\n"
"v_lshlrev_b32 v186, 16, v141\n"
"v_and_b32 v187, v237, v141\n"
"v_pk_fma_f32 v[186:187], v[186:187], v[212:213], v[214:215] op_sel_hi:[1,0,0]\n"
"v_cvt_pk_bf16_f32 v141, v186, v187\n"
"v_lshlrev_b32 v188, 16, v142\n"
"v_and_b32 v189, v237, v142\n"
"v_pk_fma_f32 v[188:189], v[188:189], v[212:213], v[214:215] op_sel_hi:[1,0,0]\n"
"v_cvt_pk_bf16_f32 v142, v188, v189\n"
"v_lshlrev_b32 v190, 16, v143\n"
"v_and_b32 v191, v237, v143\n"
"v_pk_fma_f32 v[190:191], v[190:191], v[212:213], v[214:215] op_sel_hi:[1,0,0]\n"
"v_cvt_pk_bf16_f32 v143, v190, v191\n"
"s_waitcnt lgkmcnt(0)\n"
"s_barrier\n"
"ds_read_b128 v[144:147], v224 offset:0\n"
"ds_read_b128 v[148:151], v224 offset:128\n"
"ds_read_b128 v[152:155], v224 offset:256\n"
"ds_read_b128 v[156:159], v224 offset:384\n"
"ds_read_b128 v[160:163], v224 offset:512\n"
"ds_read_b128 v[164:167], v224 offset:640\n"
"ds_read_b128 v[168:171], v224 offset:768\n"
"ds_read_b128 v[172:175], v224 offset:896\n"
"v_lshlrev_b32 v176, 16, v0\n"
"v_and_b32 v177, v237, v0\n"
"v_lshlrev_b32 v178, 16, v1\n"
"v_and_b32 v179, v237, v1\n"
"v_lshlrev_b32 v180, 16, v2\n"
"v_and_b32 v181, v237, v2\n"
"v_lshlrev_b32 v182, 16, v3\n"
"v_and_b32 v183, v237, v3\n"
"s_waitcnt lgkmcnt(5)\n"
"v_fma_f32 v176, v176, v144, v152\n"
"v_fma_f32 v177, v177, v145, v153\n"
"v_fma_f32 v178, v178, v146, v154\n"
"v_fma_f32 v179, v179, v147, v155\n"
"s_waitcnt lgkmcnt(4)\n"
"v_fma_f32 v180, v180, v148, v156\n"
"v_fma_f32 v181, v181, v149, v157\n"
"v_fma_f32 v182, v182, v150, v158\n"
"v_fma_f32 v183, v183, v151, v159\n"
"v_cvt_pk_bf16_f32 v192, v176, v177\n"
"v_cvt_pk_bf16_f32 v193, v178, v179\n"
"v_cvt_pk_bf16_f32 v194, v180, v181\n"
"v_cvt_pk_bf16_f32 v195, v182, v183\n"
"ds_write_b128 v222, v[192:195] offset:0\n"
"v_lshlrev_b32 v184, 16, v4\n"
"v_and_b32 v185, v237, v4\n"
"v_lshlrev_b32 v186, 16, v5\n"
"v_and_b32 v187, v237, v5\n"
"v_lshlrev_b32 v188, 16, v6\n"
"v_and_b32 v189, v237, v6\n"
"v_lshlrev_b32 v190, 16, v7\n"
"v_and_b32 v191, v237, v7\n"
"v_fma_f32 v184, v184, v144, v152\n"
"v_fma_f32 v185, v185, v145, v153\n"
"v_fma_f32 v186, v186, v146, v154\n"
"v_fma_f32 v187, v187, v147, v155\n"
"v_fma_f32 v188, v188, v148, v156\n"
"v_fma_f32 v189, v189, v149, v157\n"
"v_fma_f32 v190, v190, v150, v158\n"
"v_fma_f32 v191, v191, v151, v159\n"
"v_cvt_pk_bf16_f32 v196, v184, v185\n"
"v_cvt_pk_bf16_f32 v197, v186, v187\n"
"v_cvt_pk_bf16_f32 v198, v188, v189\n"
"v_cvt_pk_bf16_f32 v199, v190, v191\n"
"ds_write_b128 v223, v[196:199] offset:1024\n"
"v_lshlrev_b32 v176, 16, v8\n"
"v_and_b32 v177, v237, v8\n"
"v_lshlrev_b32 v178, 16, v9\n"
"v_and_b32 v179, v237, v9\n"
"v_lshlrev_b32 v180, 16, v10\n"
"v_and_b32 v181, v237, v10\n"
"v_lshlrev_b32 v182, 16, v11\n"
"v_and_b32 v183, v237, v11\n"
"v_fma_f32 v176, v176, v144, v152\n"
"v_fma_f32 v177, v177, v145, v153\n"
"v_fma_f32 v178, v178, v146, v154\n"
"v_fma_f32 v179, v179, v147, v155\n"
"v_fma_f32 v180, v180, v148, v156\n"
"v_fma_f32 v181, v181, v149, v157\n"
"v_fma_f32 v182, v182, v150, v158\n"
"v_fma_f32 v183, v183, v151, v159\n"
"v_cvt_pk_bf16_f32 v192, v176, v177\n"
"v_cvt_pk_bf16_f32 v193, v178, v179\n"
"v_cvt_pk_bf16_f32 v194, v180, v181\n"
"v_cvt_pk_bf16_f32 v195, v182, v183\n"
"ds_write_b128 v222, v[192:195] offset:2048\n"
"v_lshlrev_b32 v184, 16, v12\n"
"v_and_b32 v185, v237, v12\n"
"v_lshlrev_b32 v186, 16, v13\n"
"v_and_b32 v187, v237, v13\n"
"v_lshlrev_b32 v188, 16, v14\n"
"v_and_b32 v189, v237, v14\n"
"v_lshlrev_b32 v190, 16, v15\n"
"v_and_b32 v191, v237, v15\n"
"v_fma_f32 v184, v184, v144, v152\n"
"v_fma_f32 v185, v185, v145, v153\n"
"v_fma_f32 v186, v186, v146, v154\n"
"v_fma_f32 v187, v187, v147, v155\n"
"v_fma_f32 v188, v188, v148, v156\n"
"v_fma_f32 v189, v189, v149, v157\n"
"v_fma_f32 v190, v190, v150, v158\n"
"v_fma_f32 v191, v191, v151, v159\n"
"v_cvt_pk_bf16_f32 v196, v184, v185\n"
"v_cvt_pk_bf16_f32 v197, v186, v187\n"
"v_cvt_pk_bf16_f32 v198, v188, v189\n"
"v_cvt_pk_bf16_f32 v199, v190, v191\n"
"ds_write_b128 v223, v[196:199] offset:3072\n"
"s_waitcnt vmcnt(16) lgkmcnt(0)\n"
"s_barrier\n"
"ds_read_b64_tr_b16 a[160:161], v218 offset:0\n"
"ds_read_b64_tr_b16 a[162:163], v218 offset:1024\n"
"v_lshlrev_b32 v176, 16, v16\n"
"ds_read_b64_tr_b16 a[164:165], v218 offset:4096\n"
"ds_read_b64_tr_b16 a[166:167], v218 offset:5120\n"
"v_and_b32 v177, v237, v16\n"
"ds_read_b128 a[224:227], v220 offset:0\n"
"ds_read_b128 a[228:231], v221 offset:0\n"
"v_lshlrev_b32 v178, 16, v17\n"
"ds_read_b128 a[232:235], v220 offset:2048\n"
"ds_read_b128 a[236:239], v221 offset:2048\n"
"v_and_b32 v179, v237, v17\n"
"ds_read_b128 a[240:243], v220 offset:4096\n"
"ds_read_b128 a[244:247], v221 offset:4096\n"
"v_lshlrev_b32 v180, 16, v18\n"
"ds_read_b128 a[248:251], v220 offset:6144\n"
"ds_read_b128 a[252:255], v221 offset:6144\n"
"v_and_b32 v181, v237, v18\n"
"ds_read_b64_tr_b16 a[168:169], v218 offset:8\n"
"ds_read_b64_tr_b16 a[170:171], v218 offset:1032\n"
"v_lshlrev_b32 v182, 16, v19\n"
"ds_read_b64_tr_b16 a[172:173], v218 offset:4104\n"
"ds_read_b64_tr_b16 a[174:175], v218 offset:5128\n"
"v_and_b32 v183, v237, v19\n"
"ds_read_b64_tr_b16 a[176:177], v218 offset:256\n"
"v_fma_f32 v176, v176, v160, v168\n"
"v_fma_f32 v177, v177, v161, v169\n"
"ds_read_b64_tr_b16 a[178:179], v218 offset:1280\n"
"v_fma_f32 v178, v178, v162, v170\n"
"v_fma_f32 v179, v179, v163, v171\n"
"ds_read_b64_tr_b16 a[180:181], v218 offset:4352\n"
"v_fma_f32 v180, v180, v164, v172\n"
"v_fma_f32 v181, v181, v165, v173\n"
"ds_read_b64_tr_b16 a[182:183], v218 offset:5376\n"
"v_fma_f32 v182, v182, v166, v174\n"
"v_fma_f32 v183, v183, v167, v175\n"
"v_cvt_pk_bf16_f32 v192, v176, v177\n"
"v_cvt_pk_bf16_f32 v193, v178, v179\n"
"v_cvt_pk_bf16_f32 v194, v180, v181\n"
"v_cvt_pk_bf16_f32 v195, v182, v183\n"
"ds_write_b128 v222, v[192:195] offset:16384\n"
"v_lshlrev_b32 v184, 16, v20\n"
"v_and_b32 v185, v237, v20\n"
"v_lshlrev_b32 v186, 16, v21\n"
"v_and_b32 v187, v237, v21\n"
"v_lshlrev_b32 v188, 16, v22\n"
"s_waitcnt lgkmcnt(14)\n"
"v_mfma_f32_16x16x32_bf16 a[0:3], a[160:163], a[224:227], 0\n"
"ds_read_b64_tr_b16 a[184:185], v218 offset:264\n"
"v_and_b32 v189, v237, v22\n"
"v_mfma_f32_16x16x32_bf16 a[0:3], a[164:167], a[228:231], a[0:3]\n"
"ds_read_b64_tr_b16 a[186:187], v218 offset:1288\n"
"v_lshlrev_b32 v190, 16, v23\n"
"v_and_b32 v191, v237, v23\n"
"v_mfma_f32_16x16x32_bf16 a[4:7], a[160:163], a[232:235], 0\n"
"s_waitcnt lgkmcnt(14)\n"
"ds_read_b64_tr_b16 a[188:189], v218 offset:4360\n"
"v_fma_f32 v184, v184, v160, v168\n"
"v_mfma_f32_16x16x32_bf16 a[4:7], a[164:167], a[236:239], a[4:7]\n"
"ds_read_b64_tr_b16 a[190:191], v218 offset:5384\n"
"v_fma_f32 v185, v185, v161, v169\n"
"v_mfma_f32_16x16x32_bf16 a[8:11], a[160:163], a[240:243], 0\n"
"s_waitcnt lgkmcnt(14)\n"
"v_fma_f32 v186, v186, v162, v170\n"
"v_fma_f32 v187, v187, v163, v171\n"
"v_mfma_f32_16x16x32_bf16 a[8:11], a[164:167], a[244:247], a[8:11]\n"
"v_fma_f32 v188, v188, v164, v172\n"
"v_fma_f32 v189, v189, v165, v173\n"
"v_mfma_f32_16x16x32_bf16 a[12:15], a[160:163], a[248:251], 0\n"
"s_waitcnt lgkmcnt(3)\n"
"v_fma_f32 v190, v190, v166, v174\n"
"v_fma_f32 v191, v191, v167, v175\n"
"v_mfma_f32_16x16x32_bf16 a[12:15], a[164:167], a[252:255], a[12:15]\n"
"v_cvt_pk_bf16_f32 v196, v184, v185\n"
"v_cvt_pk_bf16_f32 v197, v186, v187\n"
"v_cvt_pk_bf16_f32 v198, v188, v189\n"
"v_mfma_f32_16x16x32_bf16 a[16:19], a[168:171], a[224:227], 0\n"
"ds_read_b64_tr_b16 a[160:161], v218 offset:512\n"
"v_cvt_pk_bf16_f32 v199, v190, v191\n"
"v_mfma_f32_16x16x32_bf16 a[16:19], a[172:175], a[228:231], a[16:19]\n"
"ds_read_b64_tr_b16 a[162:163], v218 offset:1536\n"
"ds_write_b128 v223, v[196:199] offset:17408\n"
"v_mfma_f32_16x16x32_bf16 a[20:23], a[168:171], a[232:235], 0\n"
"ds_read_b64_tr_b16 a[164:165], v218 offset:4608\n"
"v_lshlrev_b32 v176, 16, v24\n"
"v_mfma_f32_16x16x32_bf16 a[20:23], a[172:175], a[236:239], a[20:23]\n"
"ds_read_b64_tr_b16 a[166:167], v218 offset:5632\n"
"v_and_b32 v177, v237, v24\n"
"v_mfma_f32_16x16x32_bf16 a[24:27], a[168:171], a[240:243], 0\n"
"v_lshlrev_b32 v178, 16, v25\n"
"v_and_b32 v179, v237, v25\n"
"v_mfma_f32_16x16x32_bf16 a[24:27], a[172:175], a[244:247], a[24:27]\n"
"v_lshlrev_b32 v180, 16, v26\n"
"v_and_b32 v181, v237, v26\n"
"v_lshlrev_b32 v182, 16, v27\n"
"v_mfma_f32_16x16x32_bf16 a[28:31], a[168:171], a[248:251], 0\n"
"v_and_b32 v183, v237, v27\n"
"v_fma_f32 v176, v176, v160, v168\n"
"v_mfma_f32_16x16x32_bf16 a[28:31], a[172:175], a[252:255], a[28:31]\n"
"v_fma_f32 v177, v177, v161, v169\n"
"v_fma_f32 v178, v178, v162, v170\n"
"v_mfma_f32_16x16x32_bf16 a[32:35], a[176:179], a[224:227], 0\n"
"ds_read_b64_tr_b16 a[168:169], v218 offset:520\n"
"v_fma_f32 v179, v179, v163, v171\n"
"v_mfma_f32_16x16x32_bf16 a[32:35], a[180:183], a[228:231], a[32:35]\n"
"ds_read_b64_tr_b16 a[170:171], v218 offset:1544\n"
"v_fma_f32 v180, v180, v164, v172\n"
"v_mfma_f32_16x16x32_bf16 a[36:39], a[176:179], a[232:235], 0\n"
"ds_read_b64_tr_b16 a[172:173], v218 offset:4616\n"
"v_fma_f32 v181, v181, v165, v173\n"
"v_mfma_f32_16x16x32_bf16 a[36:39], a[180:183], a[236:239], a[36:39]\n"
"ds_read_b64_tr_b16 a[174:175], v218 offset:5640\n"
"v_fma_f32 v182, v182, v166, v174\n"
"v_fma_f32 v183, v183, v167, v175\n"
"v_mfma_f32_16x16x32_bf16 a[40:43], a[176:179], a[240:243], 0\n"
"v_cvt_pk_bf16_f32 v192, v176, v177\n"
"v_cvt_pk_bf16_f32 v193, v178, v179\n"
"v_mfma_f32_16x16x32_bf16 a[40:43], a[180:183], a[244:247], a[40:43]\n"
"v_cvt_pk_bf16_f32 v194, v180, v181\n"
"v_cvt_pk_bf16_f32 v195, v182, v183\n"
"v_mfma_f32_16x16x32_bf16 a[44:47], a[176:179], a[248:251], 0\n"
"ds_write_b128 v222, v[192:195] offset:18432\n"
"v_lshlrev_b32 v184, 16, v28\n"
"v_mfma_f32_16x16x32_bf16 a[44:47], a[180:183], a[252:255], a[44:47]\n"
"s_waitcnt lgkmcnt(3)\n"
"v_and_b32 v185, v237, v28\n"
"v_lshlrev_b32 v186, 16, v29\n"
"v_mfma_f32_16x16x32_bf16 a[48:51], a[184:187], a[224:227], 0\n"
"ds_read_b64_tr_b16 a[176:177], v218 offset:768\n"
"v_and_b32 v187, v237, v29\n"
"v_mfma_f32_16x16x32_bf16 a[48:51], a[188:191], a[228:231], a[48:51]\n"
"ds_read_b64_tr_b16 a[178:179], v218 offset:1792\n"
"v_lshlrev_b32 v188, 16, v30\n"
"v_and_b32 v189, v237, v30\n"
"v_mfma_f32_16x16x32_bf16 a[52:55], a[184:187], a[232:235], 0\n"
"ds_read_b64_tr_b16 a[180:181], v218 offset:4864\n"
"v_lshlrev_b32 v190, 16, v31\n"
"v_mfma_f32_16x16x32_bf16 a[52:55], a[188:191], a[236:239], a[52:55]\n"
"ds_read_b64_tr_b16 a[182:183], v218 offset:5888\n"
"v_and_b32 v191, v237, v31\n"
"v_mfma_f32_16x16x32_bf16 a[56:59], a[184:187], a[240:243], 0\n"
"ds_read_b128 v[144:147], v224 offset:1024\n"
"v_fma_f32 v184, v184, v160, v168\n"
"v_mfma_f32_16x16x32_bf16 a[56:59], a[188:191], a[244:247], a[56:59]\n"
"ds_read_b128 v[148:151], v224 offset:1152\n"
"v_fma_f32 v185, v185, v161, v169\n"
"v_mfma_f32_16x16x32_bf16 a[60:63], a[184:187], a[248:251], 0\n"
"ds_read_b128 v[152:155], v224 offset:1280\n"
"v_fma_f32 v186, v186, v162, v170\n"
"v_mfma_f32_16x16x32_bf16 a[60:63], a[188:191], a[252:255], a[60:63]\n"
"ds_read_b128 v[156:159], v224 offset:1408\n"
"v_fma_f32 v187, v187, v163, v171\n"
"v_fma_f32 v188, v188, v164, v172\n"
"v_mfma_f32_16x16x32_bf16 a[64:67], a[160:163], a[224:227], 0\n"
"ds_read_b64_tr_b16 a[184:185], v218 offset:776\n"
"v_fma_f32 v189, v189, v165, v173\n"
"v_mfma_f32_16x16x32_bf16 a[64:67], a[164:167], a[228:231], a[64:67]\n"
"ds_read_b64_tr_b16 a[186:187], v218 offset:1800\n"
"v_fma_f32 v190, v190, v166, v174\n"
"v_mfma_f32_16x16x32_bf16 a[68:71], a[160:163], a[232:235], 0\n"
"ds_read_b64_tr_b16 a[188:189], v218 offset:4872\n"
"v_fma_f32 v191, v191, v167, v175\n"
"v_mfma_f32_16x16x32_bf16 a[68:71], a[164:167], a[236:239], a[68:71]\n"
"ds_read_b64_tr_b16 a[190:191], v218 offset:5896\n"
"v_cvt_pk_bf16_f32 v196, v184, v185\n"
"v_mfma_f32_16x16x32_bf16 a[72:75], a[160:163], a[240:243], 0\n"
"v_cvt_pk_bf16_f32 v197, v186, v187\n"
"v_cvt_pk_bf16_f32 v198, v188, v189\n"
"v_mfma_f32_16x16x32_bf16 a[72:75], a[164:167], a[244:247], a[72:75]\n"
"v_cvt_pk_bf16_f32 v199, v190, v191\n"
"v_mfma_f32_16x16x32_bf16 a[76:79], a[160:163], a[248:251], 0\n"
"ds_write_b128 v223, v[196:199] offset:19456\n"
"v_lshlrev_b32 v176, 16, v32\n"
"v_mfma_f32_16x16x32_bf16 a[76:79], a[164:167], a[252:255], a[76:79]\n"
"v_and_b32 v177, v237, v32\n"
"v_lshlrev_b32 v178, 16, v33\n"
"v_mfma_f32_16x16x32_bf16 a[80:83], a[168:171], a[224:227], 0\n"
"s_waitcnt lgkmcnt(2)\n"
"v_and_b32 v179, v237, v33\n"
"v_lshlrev_b32 v180, 16, v34\n"
"v_mfma_f32_16x16x32_bf16 a[80:83], a[172:175], a[228:231], a[80:83]\n"
"v_and_b32 v181, v237, v34\n"
"v_lshlrev_b32 v182, 16, v35\n"
"v_mfma_f32_16x16x32_bf16 a[84:87], a[168:171], a[232:235], 0\n"
"v_and_b32 v183, v237, v35\n"
"v_fma_f32 v176, v176, v144, v152\n"
"v_mfma_f32_16x16x32_bf16 a[84:87], a[172:175], a[236:239], a[84:87]\n"
"v_fma_f32 v177, v177, v145, v153\n"
"v_fma_f32 v178, v178, v146, v154\n"
"v_fma_f32 v179, v179, v147, v155\n"
"v_mfma_f32_16x16x32_bf16 a[88:91], a[168:171], a[240:243], 0\n"
"v_fma_f32 v180, v180, v148, v156\n"
"v_fma_f32 v181, v181, v149, v157\n"
"v_mfma_f32_16x16x32_bf16 a[88:91], a[172:175], a[244:247], a[88:91]\n"
"v_fma_f32 v182, v182, v150, v158\n"
"v_fma_f32 v183, v183, v151, v159\n"
"v_mfma_f32_16x16x32_bf16 a[92:95], a[168:171], a[248:251], 0\n"
"v_cvt_pk_bf16_f32 v192, v176, v177\n"
"v_cvt_pk_bf16_f32 v193, v178, v179\n"
"v_mfma_f32_16x16x32_bf16 a[92:95], a[172:175], a[252:255], a[92:95]\n"
"s_waitcnt vmcnt(8) lgkmcnt(0)\n"
"s_barrier\n"
"v_cvt_pk_bf16_f32 v194, v180, v181\n"
"v_cvt_pk_bf16_f32 v195, v182, v183\n"
"v_mfma_f32_16x16x32_bf16 a[96:99], a[176:179], a[224:227], 0\n"
"ds_read_b64_tr_b16 a[160:161], v218 offset:32784\n"
"ds_read_b64_tr_b16 a[162:163], v218 offset:33808\n"
"v_mfma_f32_16x16x32_bf16 a[96:99], a[180:183], a[228:231], a[96:99]\n"
"ds_read_b64_tr_b16 a[164:165], v218 offset:36880\n"
"ds_read_b64_tr_b16 a[166:167], v218 offset:37904\n"
"ds_write_b128 v222, v[192:195] offset:0\n"
"v_mfma_f32_16x16x32_bf16 a[100:103], a[176:179], a[232:235], 0\n"
"ds_read_b128 a[192:195], v220 offset:16384\n"
"ds_read_b128 a[196:199], v221 offset:16384\n"
"v_mfma_f32_16x16x32_bf16 a[100:103], a[180:183], a[236:239], a[100:103]\n"
"ds_read_b128 a[200:203], v220 offset:18432\n"
"ds_read_b128 a[204:207], v221 offset:18432\n"
"v_mfma_f32_16x16x32_bf16 a[104:107], a[176:179], a[240:243], 0\n"
"ds_read_b128 a[208:211], v220 offset:20480\n"
"ds_read_b128 a[212:215], v221 offset:20480\n"
"v_mfma_f32_16x16x32_bf16 a[104:107], a[180:183], a[244:247], a[104:107]\n"
"ds_read_b128 a[216:219], v220 offset:22528\n"
"ds_read_b128 a[220:223], v221 offset:22528\n"
"v_mfma_f32_16x16x32_bf16 a[108:111], a[176:179], a[248:251], 0\n"
"ds_read_b64_tr_b16 a[168:169], v218 offset:32792\n"
"ds_read_b64_tr_b16 a[170:171], v218 offset:33816\n"
"v_mfma_f32_16x16x32_bf16 a[108:111], a[180:183], a[252:255], a[108:111]\n"
"ds_read_b64_tr_b16 a[172:173], v218 offset:36888\n"
"ds_read_b64_tr_b16 a[174:175], v218 offset:37912\n"
"v_lshlrev_b32 v184, 16, v36\n"
"v_mfma_f32_16x16x32_bf16 a[112:115], a[184:187], a[224:227], 0\n"
"ds_read_b64_tr_b16 a[176:177], v218 offset:33040\n"
"v_and_b32 v185, v237, v36\n"
"v_mfma_f32_16x16x32_bf16 a[112:115], a[188:191], a[228:231], a[112:115]\n"
"ds_read_b64_tr_b16 a[178:179], v218 offset:34064\n"
"v_lshlrev_b32 v186, 16, v37\n"
"v_mfma_f32_16x16x32_bf16 a[116:119], a[184:187], a[232:235], 0\n"
"ds_read_b64_tr_b16 a[180:181], v218 offset:37136\n"
"v_and_b32 v187, v237, v37\n"
"v_mfma_f32_16x16x32_bf16 a[116:119], a[188:191], a[236:239], a[116:119]\n"
"ds_read_b64_tr_b16 a[182:183], v218 offset:38160\n"
"v_lshlrev_b32 v188, 16, v38\n"
"v_mfma_f32_16x16x32_bf16 a[120:123], a[184:187], a[240:243], 0\n"
"s_mov_b32 m0, s51\n"
"v_and_b32 v189, v237, v38\n"
"v_lshlrev_b32 v190, 16, v39\n"
"v_mfma_f32_16x16x32_bf16 a[120:123], a[188:191], a[244:247], a[120:123]\n"
"buffer_load_dwordx4 v217, s[24:27], s50 offen offset:0 lds\n"
"v_and_b32 v191, v237, v39\n"
"v_fma_f32 v184, v184, v144, v152\n"
"v_mfma_f32_16x16x32_bf16 a[124:127], a[184:187], a[248:251], 0\n"
"s_add_u32 m0, s51, 7936\n"
"v_fma_f32 v185, v185, v145, v153\n"
"v_fma_f32 v186, v186, v146, v154\n"
"v_mfma_f32_16x16x32_bf16 a[124:127], a[188:191], a[252:255], a[124:127]\n"
"s_waitcnt lgkmcnt(2)\n"
"buffer_load_dwordx4 v217, s[24:27], s50 offen offset:256 lds\n"
"v_fma_f32 v187, v187, v147, v155\n"
"v_mfma_f32_16x16x32_bf16 a[0:3], a[160:163], a[192:195], a[0:3]\n"
"s_add_u32 s50, s50, 32768\n"
"ds_read_b64_tr_b16 a[184:185], v218 offset:33048\n"
"v_fma_f32 v188, v188, v148, v156\n"
"v_mfma_f32_16x16x32_bf16 a[0:3], a[164:167], a[196:199], a[0:3]\n"
"ds_read_b64_tr_b16 a[186:187], v218 offset:34072\n"
"v_fma_f32 v189, v189, v149, v157\n"
"v_mfma_f32_16x16x32_bf16 a[4:7], a[160:163], a[200:203], a[4:7]\n"
"ds_read_b64_tr_b16 a[188:189], v218 offset:37144\n"
"v_fma_f32 v190, v190, v150, v158\n"
"v_mfma_f32_16x16x32_bf16 a[4:7], a[164:167], a[204:207], a[4:7]\n"
"ds_read_b64_tr_b16 a[190:191], v218 offset:38168\n"
"v_fma_f32 v191, v191, v151, v159\n"
"v_cvt_pk_bf16_f32 v196, v184, v185\n"
"v_mfma_f32_16x16x32_bf16 a[8:11], a[160:163], a[208:211], a[8:11]\n"
"s_add_u32 m0, s51, 1024\n"
"v_cvt_pk_bf16_f32 v197, v186, v187\n"
"v_cvt_pk_bf16_f32 v198, v188, v189\n"
"v_mfma_f32_16x16x32_bf16 a[8:11], a[164:167], a[212:215], a[8:11]\n"
"buffer_load_dwordx4 v217, s[24:27], s50 offen offset:0 lds\n"
"v_cvt_pk_bf16_f32 v199, v190, v191\n"
"v_mfma_f32_16x16x32_bf16 a[12:15], a[160:163], a[216:219], a[12:15]\n"
"s_add_u32 m0, s51, 8960\n"
"ds_write_b128 v223, v[196:199] offset:1024\n"
"v_lshlrev_b32 v176, 16, v40\n"
"v_mfma_f32_16x16x32_bf16 a[12:15], a[164:167], a[220:223], a[12:15]\n"
"buffer_load_dwordx4 v217, s[24:27], s50 offen offset:256 lds\n"
"v_and_b32 v177, v237, v40\n"
"v_mfma_f32_16x16x32_bf16 a[16:19], a[168:171], a[192:195], a[16:19]\n"
"s_add_u32 s50, s50, 32768\n"
"ds_read_b64_tr_b16 a[160:161], v218 offset:33296\n"
"v_lshlrev_b32 v178, 16, v41\n"
"v_mfma_f32_16x16x32_bf16 a[16:19], a[172:175], a[196:199], a[16:19]\n"
"ds_read_b64_tr_b16 a[162:163], v218 offset:34320\n"
"v_and_b32 v179, v237, v41\n"
"v_lshlrev_b32 v180, 16, v42\n"
"v_mfma_f32_16x16x32_bf16 a[20:23], a[168:171], a[200:203], a[20:23]\n"
"ds_read_b64_tr_b16 a[164:165], v218 offset:37392\n"
"v_and_b32 v181, v237, v42\n"
"v_mfma_f32_16x16x32_bf16 a[20:23], a[172:175], a[204:207], a[20:23]\n"
"ds_read_b64_tr_b16 a[166:167], v218 offset:38416\n"
"v_lshlrev_b32 v182, 16, v43\n"
"v_mfma_f32_16x16x32_bf16 a[24:27], a[168:171], a[208:211], a[24:27]\n"
"s_add_u32 m0, s51, 16392\n"
"v_and_b32 v183, v237, v43\n"
"v_fma_f32 v176, v176, v144, v152\n"
"v_mfma_f32_16x16x32_bf16 a[24:27], a[172:175], a[212:215], a[24:27]\n"
"buffer_load_dwordx4 v217, s[24:27], s50 offen offset:0 lds\n"
"v_fma_f32 v177, v177, v145, v153\n"
"v_mfma_f32_16x16x32_bf16 a[28:31], a[168:171], a[216:219], a[28:31]\n"
"s_add_u32 m0, s51, 24328\n"
"v_fma_f32 v178, v178, v146, v154\n"
"v_fma_f32 v179, v179, v147, v155\n"
"v_mfma_f32_16x16x32_bf16 a[28:31], a[172:175], a[220:223], a[28:31]\n"
"buffer_load_dwordx4 v217, s[24:27], s50 offen offset:256 lds\n"
"v_fma_f32 v180, v180, v148, v156\n"
"v_fma_f32 v181, v181, v149, v157\n"
"v_mfma_f32_16x16x32_bf16 a[32:35], a[176:179], a[192:195], a[32:35]\n"
"s_waitcnt lgkmcnt(1)\n"
"s_add_u32 s50, s50, 32768\n"
"ds_read_b64_tr_b16 a[168:169], v218 offset:33304\n"
"v_fma_f32 v182, v182, v150, v158\n"
"v_mfma_f32_16x16x32_bf16 a[32:35], a[180:183], a[196:199], a[32:35]\n"
"ds_read_b64_tr_b16 a[170:171], v218 offset:34328\n"
"v_fma_f32 v183, v183, v151, v159\n"
"v_mfma_f32_16x16x32_bf16 a[36:39], a[176:179], a[200:203], a[36:39]\n"
"ds_read_b64_tr_b16 a[172:173], v218 offset:37400\n"
"v_cvt_pk_bf16_f32 v192, v176, v177\n"
"v_mfma_f32_16x16x32_bf16 a[36:39], a[180:183], a[204:207], a[36:39]\n"
"ds_read_b64_tr_b16 a[174:175], v218 offset:38424\n"
"v_cvt_pk_bf16_f32 v193, v178, v179\n"
"v_mfma_f32_16x16x32_bf16 a[40:43], a[176:179], a[208:211], a[40:43]\n"
"s_add_u32 m0, s51, 17416\n"
"v_cvt_pk_bf16_f32 v194, v180, v181\n"
"v_cvt_pk_bf16_f32 v195, v182, v183\n"
"v_mfma_f32_16x16x32_bf16 a[40:43], a[180:183], a[212:215], a[40:43]\n"
"buffer_load_dwordx4 v217, s[24:27], s50 offen offset:0 lds\n"
"ds_write_b128 v222, v[192:195] offset:2048\n"
"v_lshlrev_b32 v184, 16, v44\n"
"v_mfma_f32_16x16x32_bf16 a[44:47], a[176:179], a[216:219], a[44:47]\n"
"s_add_u32 m0, s51, 25352\n"
"v_and_b32 v185, v237, v44\n"
"v_lshlrev_b32 v186, 16, v45\n"
"v_mfma_f32_16x16x32_bf16 a[44:47], a[180:183], a[220:223], a[44:47]\n"
"buffer_load_dwordx4 v217, s[24:27], s50 offen offset:256 lds\n"
"v_and_b32 v187, v237, v45\n"
"v_mfma_f32_16x16x32_bf16 a[48:51], a[184:187], a[192:195], a[48:51]\n"
"s_add_u32 s50, s50, 425984\n"
"ds_read_b64_tr_b16 a[176:177], v218 offset:33552\n"
"v_lshlrev_b32 v188, 16, v46\n"
"v_mfma_f32_16x16x32_bf16 a[48:51], a[188:191], a[196:199], a[48:51]\n"
"ds_read_b64_tr_b16 a[178:179], v218 offset:34576\n"
"v_and_b32 v189, v237, v46\n"
"v_mfma_f32_16x16x32_bf16 a[52:55], a[184:187], a[200:203], a[52:55]\n"
"ds_read_b64_tr_b16 a[180:181], v218 offset:37648\n"
"v_lshlrev_b32 v190, 16, v47\n"
"v_mfma_f32_16x16x32_bf16 a[52:55], a[188:191], a[204:207], a[52:55]\n"
"ds_read_b64_tr_b16 a[182:183], v218 offset:38672\n"
"v_and_b32 v191, v237, v47\n"
"v_fma_f32 v184, v184, v144, v152\n"
"v_mfma_f32_16x16x32_bf16 a[56:59], a[184:187], a[208:211], a[56:59]\n"
"ds_read_b128 v[160:163], v224 offset:1536\n"
"v_fma_f32 v185, v185, v145, v153\n"
"v_mfma_f32_16x16x32_bf16 a[56:59], a[188:191], a[212:215], a[56:59]\n"
"ds_read_b128 v[164:167], v224 offset:1664\n"
"v_fma_f32 v186, v186, v146, v154\n"
"v_mfma_f32_16x16x32_bf16 a[60:63], a[184:187], a[216:219], a[60:63]\n"
"ds_read_b128 v[168:171], v224 offset:1792\n"
"v_fma_f32 v187, v187, v147, v155\n"
"v_mfma_f32_16x16x32_bf16 a[60:63], a[188:191], a[220:223], a[60:63]\n"
"ds_read_b128 v[172:175], v224 offset:1920\n"
"v_fma_f32 v188, v188, v148, v156\n"
"v_mfma_f32_16x16x32_bf16 a[64:67], a[160:163], a[192:195], a[64:67]\n"
"s_waitcnt lgkmcnt(5)\n"
"ds_read_b64_tr_b16 a[184:185], v218 offset:33560\n"
"v_fma_f32 v189, v189, v149, v157\n"
"v_mfma_f32_16x16x32_bf16 a[64:67], a[164:167], a[196:199], a[64:67]\n"
"ds_read_b64_tr_b16 a[186:187], v218 offset:34584\n"
"v_fma_f32 v190, v190, v150, v158\n"
"v_fma_f32 v191, v191, v151, v159\n"
"v_mfma_f32_16x16x32_bf16 a[68:71], a[160:163], a[200:203], a[68:71]\n"
"ds_read_b64_tr_b16 a[188:189], v218 offset:37656\n"
"v_cvt_pk_bf16_f32 v196, v184, v185\n"
"v_mfma_f32_16x16x32_bf16 a[68:71], a[164:167], a[204:207], a[68:71]\n"
"ds_read_b64_tr_b16 a[190:191], v218 offset:38680\n"
"v_cvt_pk_bf16_f32 v197, v186, v187\n"
"v_mfma_f32_16x16x32_bf16 a[72:75], a[160:163], a[208:211], a[72:75]\n"
"v_cvt_pk_bf16_f32 v198, v188, v189\n"
"v_cvt_pk_bf16_f32 v199, v190, v191\n"
"v_mfma_f32_16x16x32_bf16 a[72:75], a[164:167], a[212:215], a[72:75]\n"
"ds_write_b128 v223, v[196:199] offset:3072\n"
"v_mfma_f32_16x16x32_bf16 a[76:79], a[160:163], a[216:219], a[76:79]\n"
"v_lshlrev_b32 v176, 16, v48\n"
"v_and_b32 v177, v237, v48\n"
"v_mfma_f32_16x16x32_bf16 a[76:79], a[164:167], a[220:223], a[76:79]\n"
"v_lshlrev_b32 v178, 16, v49\n"
"v_and_b32 v179, v237, v49\n"
"v_lshlrev_b32 v180, 16, v50\n"
"v_mfma_f32_16x16x32_bf16 a[80:83], a[168:171], a[192:195], a[80:83]\n"
"v_and_b32 v181, v237, v50\n"
"v_lshlrev_b32 v182, 16, v51\n"
"v_mfma_f32_16x16x32_bf16 a[80:83], a[172:175], a[196:199], a[80:83]\n"
"s_waitcnt lgkmcnt(1)\n"
"v_and_b32 v183, v237, v51\n"
"v_fma_f32 v176, v176, v160, v168\n"
"v_mfma_f32_16x16x32_bf16 a[84:87], a[168:171], a[200:203], a[84:87]\n"
"v_fma_f32 v177, v177, v161, v169\n"
"v_fma_f32 v178, v178, v162, v170\n"
"v_mfma_f32_16x16x32_bf16 a[84:87], a[172:175], a[204:207], a[84:87]\n"
"v_fma_f32 v179, v179, v163, v171\n"
"v_fma_f32 v180, v180, v164, v172\n"
"v_mfma_f32_16x16x32_bf16 a[88:91], a[168:171], a[208:211], a[88:91]\n"
"v_fma_f32 v181, v181, v165, v173\n"
"v_fma_f32 v182, v182, v166, v174\n"
"v_mfma_f32_16x16x32_bf16 a[88:91], a[172:175], a[212:215], a[88:91]\n"
"v_fma_f32 v183, v183, v167, v175\n"
"v_cvt_pk_bf16_f32 v192, v176, v177\n"
"v_cvt_pk_bf16_f32 v193, v178, v179\n"
"v_mfma_f32_16x16x32_bf16 a[92:95], a[168:171], a[216:219], a[92:95]\n"
"v_cvt_pk_bf16_f32 v194, v180, v181\n"
"v_cvt_pk_bf16_f32 v195, v182, v183\n"
"v_mfma_f32_16x16x32_bf16 a[92:95], a[172:175], a[220:223], a[92:95]\n"
"s_waitcnt vmcnt(8) lgkmcnt(0)\n"
"s_barrier\n"
"v_mfma_f32_16x16x32_bf16 a[96:99], a[176:179], a[192:195], a[96:99]\n"
"ds_read_b64_tr_b16 a[160:161], v219 offset:0\n"
"ds_read_b64_tr_b16 a[162:163], v219 offset:1024\n"
"v_mfma_f32_16x16x32_bf16 a[96:99], a[180:183], a[196:199], a[96:99]\n"
"ds_read_b64_tr_b16 a[164:165], v219 offset:4096\n"
"ds_read_b64_tr_b16 a[166:167], v219 offset:5120\n"
"v_mfma_f32_16x16x32_bf16 a[100:103], a[176:179], a[200:203], a[100:103]\n"
"ds_read_b128 a[224:227], v220 offset:0\n"
"ds_read_b128 a[228:231], v221 offset:0\n"
"v_mfma_f32_16x16x32_bf16 a[100:103], a[180:183], a[204:207], a[100:103]\n"
"ds_read_b128 a[232:235], v220 offset:2048\n"
"ds_read_b128 a[236:239], v221 offset:2048\n"
"ds_write_b128 v222, v[192:195] offset:16384\n"
"v_mfma_f32_16x16x32_bf16 a[104:107], a[176:179], a[208:211], a[104:107]\n"
"ds_read_b128 a[240:243], v220 offset:4096\n"
"ds_read_b128 a[244:247], v221 offset:4096\n"
"v_mfma_f32_16x16x32_bf16 a[104:107], a[180:183], a[212:215], a[104:107]\n"
"ds_read_b128 a[248:251], v220 offset:6144\n"
"ds_read_b128 a[252:255], v221 offset:6144\n"
"v_mfma_f32_16x16x32_bf16 a[108:111], a[176:179], a[216:219], a[108:111]\n"
"ds_read_b64_tr_b16 a[168:169], v219 offset:8\n"
"ds_read_b64_tr_b16 a[170:171], v219 offset:1032\n"
"v_mfma_f32_16x16x32_bf16 a[108:111], a[180:183], a[220:223], a[108:111]\n"
"ds_read_b64_tr_b16 a[172:173], v219 offset:4104\n"
"ds_read_b64_tr_b16 a[174:175], v219 offset:5128\n"
"v_mfma_f32_16x16x32_bf16 a[112:115], a[184:187], a[192:195], a[112:115]\n"
"ds_read_b64_tr_b16 a[176:177], v219 offset:256\n"
"v_lshlrev_b32 v184, 16, v52\n"
"v_mfma_f32_16x16x32_bf16 a[112:115], a[188:191], a[196:199], a[112:115]\n"
"ds_read_b64_tr_b16 a[178:179], v219 offset:1280\n"
"v_and_b32 v185, v237, v52\n"
"v_lshlrev_b32 v186, 16, v53\n"
"v_mfma_f32_16x16x32_bf16 a[116:119], a[184:187], a[200:203], a[116:119]\n"
"ds_read_b64_tr_b16 a[180:181], v219 offset:4352\n"
"v_and_b32 v187, v237, v53\n"
"v_mfma_f32_16x16x32_bf16 a[116:119], a[188:191], a[204:207], a[116:119]\n"
"ds_read_b64_tr_b16 a[182:183], v219 offset:5376\n"
"v_lshlrev_b32 v188, 16, v54\n"
"v_mfma_f32_16x16x32_bf16 a[120:123], a[184:187], a[208:211], a[120:123]\n"
"s_add_u32 m0, s51, 32784\n"
"v_and_b32 v189, v237, v54\n"
"v_lshlrev_b32 v190, 16, v55\n"
"v_mfma_f32_16x16x32_bf16 a[120:123], a[188:191], a[212:215], a[120:123]\n"
"buffer_load_dwordx4 v217, s[24:27], s50 offen offset:0 lds\n"
"v_and_b32 v191, v237, v55\n"
"v_mfma_f32_16x16x32_bf16 a[124:127], a[184:187], a[216:219], a[124:127]\n"
"s_add_u32 m0, s51, 40720\n"
"v_fma_f32 v184, v184, v160, v168\n"
"v_fma_f32 v185, v185, v161, v169\n"
"v_mfma_f32_16x16x32_bf16 a[124:127], a[188:191], a[220:223], a[124:127]\n"
"s_waitcnt lgkmcnt(2)\n"
"buffer_load_dwordx4 v217, s[24:27], s50 offen offset:256 lds\n"
"v_fma_f32 v186, v186, v162, v170\n"
"v_fma_f32 v187, v187, v163, v171\n"
"v_mfma_f32_16x16x32_bf16 a[0:3], a[160:163], a[224:227], a[0:3]\n"
"s_add_u32 s50, s50, 32768\n"
"ds_read_b64_tr_b16 a[184:185], v219 offset:264\n"
"v_fma_f32 v188, v188, v164, v172\n"
"v_mfma_f32_16x16x32_bf16 a[0:3], a[164:167], a[228:231], a[0:3]\n"
"ds_read_b64_tr_b16 a[186:187], v219 offset:1288\n"
"v_fma_f32 v189, v189, v165, v173\n"
"v_mfma_f32_16x16x32_bf16 a[4:7], a[160:163], a[232:235], a[4:7]\n"
"ds_read_b64_tr_b16 a[188:189], v219 offset:4360\n"
"v_fma_f32 v190, v190, v166, v174\n"
"v_mfma_f32_16x16x32_bf16 a[4:7], a[164:167], a[236:239], a[4:7]\n"
"ds_read_b64_tr_b16 a[190:191], v219 offset:5384\n"
"v_fma_f32 v191, v191, v167, v175\n"
"v_mfma_f32_16x16x32_bf16 a[8:11], a[160:163], a[240:243], a[8:11]\n"
"s_add_u32 m0, s51, 33808\n"
"v_cvt_pk_bf16_f32 v196, v184, v185\n"
"v_cvt_pk_bf16_f32 v197, v186, v187\n"
"v_mfma_f32_16x16x32_bf16 a[8:11], a[164:167], a[244:247], a[8:11]\n"
"buffer_load_dwordx4 v217, s[24:27], s50 offen offset:0 lds\n"
"v_cvt_pk_bf16_f32 v198, v188, v189\n"
"v_cvt_pk_bf16_f32 v199, v190, v191\n"
"v_mfma_f32_16x16x32_bf16 a[12:15], a[160:163], a[248:251], a[12:15]\n"
"s_add_u32 m0, s51, 41744\n"
"ds_write_b128 v223, v[196:199] offset:17408\n"
"v_lshlrev_b32 v176, 16, v56\n"
"v_mfma_f32_16x16x32_bf16 a[12:15], a[164:167], a[252:255], a[12:15]\n"
"buffer_load_dwordx4 v217, s[24:27], s50 offen offset:256 lds\n"
"v_and_b32 v177, v237, v56\n"
"v_mfma_f32_16x16x32_bf16 a[16:19], a[168:171], a[224:227], a[16:19]\n"
"s_add_u32 s50, s50, 32768\n"
"ds_read_b64_tr_b16 a[160:161], v219 offset:512\n"
"v_lshlrev_b32 v178, 16, v57\n"
"v_mfma_f32_16x16x32_bf16 a[16:19], a[172:175], a[228:231], a[16:19]\n"
"ds_read_b64_tr_b16 a[162:163], v219 offset:1536\n"
"v_and_b32 v179, v237, v57\n"
"v_mfma_f32_16x16x32_bf16 a[20:23], a[168:171], a[232:235], a[20:23]\n"
"ds_read_b64_tr_b16 a[164:165], v219 offset:4608\n"
"v_lshlrev_b32 v180, 16, v58\n"
"v_mfma_f32_16x16x32_bf16 a[20:23], a[172:175], a[236:239], a[20:23]\n"
"ds_read_b64_tr_b16 a[166:167], v219 offset:5632\n"
"v_and_b32 v181, v237, v58\n"
"v_lshlrev_b32 v182, 16, v59\n"
"v_mfma_f32_16x16x32_bf16 a[24:27], a[168:171], a[240:243], a[24:27]\n"
"s_add_u32 m0, s51, 49176\n"
"v_and_b32 v183, v237, v59\n"
"v_fma_f32 v176, v176, v160, v168\n"
"v_mfma_f32_16x16x32_bf16 a[24:27], a[172:175], a[244:247], a[24:27]\n"
"buffer_load_dwordx4 v217, s[24:27], s50 offen offset:0 lds\n"
"v_fma_f32 v177, v177, v161, v169\n"
"v_mfma_f32_16x16x32_bf16 a[28:31], a[168:171], a[248:251], a[28:31]\n"
"s_add_u32 m0, s51, 57112\n"
"v_fma_f32 v178, v178, v162, v170\n"
"v_fma_f32 v179, v179, v163, v171\n"
"v_mfma_f32_16x16x32_bf16 a[28:31], a[172:175], a[252:255], a[28:31]\n"
"buffer_load_dwordx4 v217, s[24:27], s50 offen offset:256 lds\n"
"v_fma_f32 v180, v180, v164, v172\n"
"v_mfma_f32_16x16x32_bf16 a[32:35], a[176:179], a[224:227], a[32:35]\n"
"s_waitcnt lgkmcnt(1)\n"
"s_add_u32 s50, s50, 32768\n"
"ds_read_b64_tr_b16 a[168:169], v219 offset:520\n"
"v_fma_f32 v181, v181, v165, v173\n"
"v_mfma_f32_16x16x32_bf16 a[32:35], a[180:183], a[228:231], a[32:35]\n"
"ds_read_b64_tr_b16 a[170:171], v219 offset:1544\n"
"v_fma_f32 v182, v182, v166, v174\n"
"v_fma_f32 v183, v183, v167, v175\n"
"v_mfma_f32_16x16x32_bf16 a[36:39], a[176:179], a[232:235], a[36:39]\n"
"ds_read_b64_tr_b16 a[172:173], v219 offset:4616\n"
"v_cvt_pk_bf16_f32 v192, v176, v177\n"
"v_mfma_f32_16x16x32_bf16 a[36:39], a[180:183], a[236:239], a[36:39]\n"
"ds_read_b64_tr_b16 a[174:175], v219 offset:5640\n"
"v_cvt_pk_bf16_f32 v193, v178, v179\n"
"v_mfma_f32_16x16x32_bf16 a[40:43], a[176:179], a[240:243], a[40:43]\n"
"s_add_u32 m0, s51, 50200\n"
"v_cvt_pk_bf16_f32 v194, v180, v181\n"
"v_cvt_pk_bf16_f32 v195, v182, v183\n"
"v_mfma_f32_16x16x32_bf16 a[40:43], a[180:183], a[244:247], a[40:43]\n"
"buffer_load_dwordx4 v217, s[24:27], s50 offen offset:0 lds\n"
"ds_write_b128 v222, v[192:195] offset:18432\n"
"v_mfma_f32_16x16x32_bf16 a[44:47], a[176:179], a[248:251], a[44:47]\n"
"s_add_u32 m0, s51, 58136\n"
"v_lshlrev_b32 v184, 16, v60\n"
"v_and_b32 v185, v237, v60\n"
"v_mfma_f32_16x16x32_bf16 a[44:47], a[180:183], a[252:255], a[44:47]\n"
"buffer_load_dwordx4 v217, s[24:27], s50 offen offset:256 lds\n"
"v_lshlrev_b32 v186, 16, v61\n"
"v_and_b32 v187, v237, v61\n"
"v_mfma_f32_16x16x32_bf16 a[48:51], a[184:187], a[224:227], a[48:51]\n"
"s_add_u32 s50, s50, 425984\n"
"ds_read_b64_tr_b16 a[176:177], v219 offset:768\n"
"v_lshlrev_b32 v188, 16, v62\n"
"v_mfma_f32_16x16x32_bf16 a[48:51], a[188:191], a[228:231], a[48:51]\n"
"ds_read_b64_tr_b16 a[178:179], v219 offset:1792\n"
"v_and_b32 v189, v237, v62\n"
"v_mfma_f32_16x16x32_bf16 a[52:55], a[184:187], a[232:235], a[52:55]\n"
"ds_read_b64_tr_b16 a[180:181], v219 offset:4864\n"
"v_lshlrev_b32 v190, 16, v63\n"
"v_mfma_f32_16x16x32_bf16 a[52:55], a[188:191], a[236:239], a[52:55]\n"
"ds_read_b64_tr_b16 a[182:183], v219 offset:5888\n"
"v_and_b32 v191, v237, v63\n"
"v_mfma_f32_16x16x32_bf16 a[56:59], a[184:187], a[240:243], a[56:59]\n"
"ds_read_b128 v[144:147], v224 offset:2048\n"
"v_fma_f32 v184, v184, v160, v168\n"
"v_mfma_f32_16x16x32_bf16 a[56:59], a[188:191], a[244:247], a[56:59]\n"
"ds_read_b128 v[148:151], v224 offset:2176\n"
"v_fma_f32 v185, v185, v161, v169\n"
"v_fma_f32 v186, v186, v162, v170\n"
"v_mfma_f32_16x16x32_bf16 a[60:63], a[184:187], a[248:251], a[60:63]\n"
"ds_read_b128 v[152:155], v224 offset:2304\n"
"v_fma_f32 v187, v187, v163, v171\n"
"v_mfma_f32_16x16x32_bf16 a[60:63], a[188:191], a[252:255], a[60:63]\n"
"ds_read_b128 v[156:159], v224 offset:2432\n"
"v_fma_f32 v188, v188, v164, v172\n"
"v_mfma_f32_16x16x32_bf16 a[64:67], a[160:163], a[224:227], a[64:67]\n"
"s_waitcnt lgkmcnt(5)\n"
"ds_read_b64_tr_b16 a[184:185], v219 offset:776\n"
"v_fma_f32 v189, v189, v165, v173\n"
"v_mfma_f32_16x16x32_bf16 a[64:67], a[164:167], a[228:231], a[64:67]\n"
"ds_read_b64_tr_b16 a[186:187], v219 offset:1800\n"
"v_fma_f32 v190, v190, v166, v174\n"
"v_mfma_f32_16x16x32_bf16 a[68:71], a[160:163], a[232:235], a[68:71]\n"
"ds_read_b64_tr_b16 a[188:189], v219 offset:4872\n"
"v_fma_f32 v191, v191, v167, v175\n"
"v_mfma_f32_16x16x32_bf16 a[68:71], a[164:167], a[236:239], a[68:71]\n"
"ds_read_b64_tr_b16 a[190:191], v219 offset:5896\n"
"v_cvt_pk_bf16_f32 v196, v184, v185\n"
"v_cvt_pk_bf16_f32 v197, v186, v187\n"
"v_mfma_f32_16x16x32_bf16 a[72:75], a[160:163], a[240:243], a[72:75]\n"
"v_cvt_pk_bf16_f32 v198, v188, v189\n"
"v_cvt_pk_bf16_f32 v199, v190, v191\n"
"v_mfma_f32_16x16x32_bf16 a[72:75], a[164:167], a[244:247], a[72:75]\n"
"ds_write_b128 v223, v[196:199] offset:19456\n"
"v_mfma_f32_16x16x32_bf16 a[76:79], a[160:163], a[248:251], a[76:79]\n"
"v_lshlrev_b32 v176, 16, v64\n"
"v_and_b32 v177, v237, v64\n"
"v_mfma_f32_16x16x32_bf16 a[76:79], a[164:167], a[252:255], a[76:79]\n"
"v_lshlrev_b32 v178, 16, v65\n"
"v_and_b32 v179, v237, v65\n"
"v_mfma_f32_16x16x32_bf16 a[80:83], a[168:171], a[224:227], a[80:83]\n"
"v_lshlrev_b32 v180, 16, v66\n"
"v_and_b32 v181, v237, v66\n"
"v_mfma_f32_16x16x32_bf16 a[80:83], a[172:175], a[228:231], a[80:83]\n"
"s_waitcnt lgkmcnt(1)\n"
"v_lshlrev_b32 v182, 16, v67\n"
"v_and_b32 v183, v237, v67\n"
"v_fma_f32 v176, v176, v144, v152\n"
"v_mfma_f32_16x16x32_bf16 a[84:87], a[168:171], a[232:235], a[84:87]\n"
"v_fma_f32 v177, v177, v145, v153\n"
"v_fma_f32 v178, v178, v146, v154\n"
"v_mfma_f32_16x16x32_bf16 a[84:87], a[172:175], a[236:239], a[84:87]\n"
"v_fma_f32 v179, v179, v147, v155\n"
"v_fma_f32 v180, v180, v148, v156\n"
"v_mfma_f32_16x16x32_bf16 a[88:91], a[168:171], a[240:243], a[88:91]\n"
"v_fma_f32 v181, v181, v149, v157\n"
"v_fma_f32 v182, v182, v150, v158\n"
"v_mfma_f32_16x16x32_bf16 a[88:91], a[172:175], a[244:247], a[88:91]\n"
"v_fma_f32 v183, v183, v151, v159\n"
"v_cvt_pk_bf16_f32 v192, v176, v177\n"
"v_mfma_f32_16x16x32_bf16 a[92:95], a[168:171], a[248:251], a[92:95]\n"
"v_cvt_pk_bf16_f32 v193, v178, v179\n"
"v_cvt_pk_bf16_f32 v194, v180, v181\n"
"v_mfma_f32_16x16x32_bf16 a[92:95], a[172:175], a[252:255], a[92:95]\n"
"s_waitcnt vmcnt(8) lgkmcnt(0)\n"
"s_barrier\n"
"v_cvt_pk_bf16_f32 v195, v182, v183\n"
"v_mfma_f32_16x16x32_bf16 a[96:99], a[176:179], a[224:227], a[96:99]\n"
"ds_read_b64_tr_b16 a[160:161], v218 offset:0\n"
"ds_read_b64_tr_b16 a[162:163], v218 offset:1024\n"
"v_mfma_f32_16x16x32_bf16 a[96:99], a[180:183], a[228:231], a[96:99]\n"
"ds_read_b64_tr_b16 a[164:165], v218 offset:4096\n"
"ds_read_b64_tr_b16 a[166:167], v218 offset:5120\n"
"v_mfma_f32_16x16x32_bf16 a[100:103], a[176:179], a[232:235], a[100:103]\n"
"ds_read_b128 a[192:195], v220 offset:16384\n"
"ds_read_b128 a[196:199], v221 offset:16384\n"
"v_mfma_f32_16x16x32_bf16 a[100:103], a[180:183], a[236:239], a[100:103]\n"
"ds_read_b128 a[200:203], v220 offset:18432\n"
"ds_read_b128 a[204:207], v221 offset:18432\n"
"v_mfma_f32_16x16x32_bf16 a[104:107], a[176:179], a[240:243], a[104:107]\n"
"ds_read_b128 a[208:211], v220 offset:20480\n"
"ds_read_b128 a[212:215], v221 offset:20480\n"
"v_mfma_f32_16x16x32_bf16 a[104:107], a[180:183], a[244:247], a[104:107]\n"
"ds_read_b128 a[216:219], v220 offset:22528\n"
"ds_read_b128 a[220:223], v221 offset:22528\n"
"ds_write_b128 v222, v[192:195] offset:0\n"
"v_mfma_f32_16x16x32_bf16 a[108:111], a[176:179], a[248:251], a[108:111]\n"
"ds_read_b64_tr_b16 a[168:169], v218 offset:8\n"
"ds_read_b64_tr_b16 a[170:171], v218 offset:1032\n"
"v_mfma_f32_16x16x32_bf16 a[108:111], a[180:183], a[252:255], a[108:111]\n"
"ds_read_b64_tr_b16 a[172:173], v218 offset:4104\n"
"ds_read_b64_tr_b16 a[174:175], v218 offset:5128\n"
"v_mfma_f32_16x16x32_bf16 a[112:115], a[184:187], a[224:227], a[112:115]\n"
"ds_read_b64_tr_b16 a[176:177], v218 offset:256\n"
"v_lshlrev_b32 v184, 16, v68\n"
"v_mfma_f32_16x16x32_bf16 a[112:115], a[188:191], a[228:231], a[112:115]\n"
"ds_read_b64_tr_b16 a[178:179], v218 offset:1280\n"
"v_and_b32 v185, v237, v68\n"
"v_mfma_f32_16x16x32_bf16 a[116:119], a[184:187], a[232:235], a[116:119]\n"
"ds_read_b64_tr_b16 a[180:181], v218 offset:4352\n"
"v_lshlrev_b32 v186, 16, v69\n"
"v_mfma_f32_16x16x32_bf16 a[116:119], a[188:191], a[236:239], a[116:119]\n"
"ds_read_b64_tr_b16 a[182:183], v218 offset:5376\n"
"v_and_b32 v187, v237, v69\n"
"v_lshlrev_b32 v188, 16, v70\n"
"v_mfma_f32_16x16x32_bf16 a[120:123], a[184:187], a[240:243], a[120:123]\n"
"s_add_u32 m0, s51, 65568\n"
"v_and_b32 v189, v237, v70\n"
"v_lshlrev_b32 v190, 16, v71\n"
"v_mfma_f32_16x16x32_bf16 a[120:123], a[188:191], a[244:247], a[120:123]\n"
"buffer_load_dwordx4 v217, s[24:27], s50 offen offset:0 lds\n"
"v_and_b32 v191, v237, v71\n"
"v_mfma_f32_16x16x32_bf16 a[124:127], a[184:187], a[248:251], a[124:127]\n"
"s_add_u32 m0, s51, 73504\n"
"v_fma_f32 v184, v184, v144, v152\n"
"v_fma_f32 v185, v185, v145, v153\n"
"v_mfma_f32_16x16x32_bf16 a[124:127], a[188:191], a[252:255], a[124:127]\n"
"s_waitcnt lgkmcnt(2)\n"
"buffer_load_dwordx4 v217, s[24:27], s50 offen offset:256 lds\n"
"v_fma_f32 v186, v186, v146, v154\n"
"v_mfma_f32_16x16x32_bf16 a[0:3], a[160:163], a[192:195], a[0:3]\n"
"s_add_u32 s50, s50, 32768\n"
"ds_read_b64_tr_b16 a[184:185], v218 offset:264\n"
"v_fma_f32 v187, v187, v147, v155\n"
"v_mfma_f32_16x16x32_bf16 a[0:3], a[164:167], a[196:199], a[0:3]\n"
"ds_read_b64_tr_b16 a[186:187], v218 offset:1288\n"
"v_fma_f32 v188, v188, v148, v156\n"
"v_fma_f32 v189, v189, v149, v157\n"
"v_mfma_f32_16x16x32_bf16 a[4:7], a[160:163], a[200:203], a[4:7]\n"
"ds_read_b64_tr_b16 a[188:189], v218 offset:4360\n"
"v_fma_f32 v190, v190, v150, v158\n"
"v_mfma_f32_16x16x32_bf16 a[4:7], a[164:167], a[204:207], a[4:7]\n"
"ds_read_b64_tr_b16 a[190:191], v218 offset:5384\n"
"v_fma_f32 v191, v191, v151, v159\n"
"v_mfma_f32_16x16x32_bf16 a[8:11], a[160:163], a[208:211], a[8:11]\n"
"s_add_u32 m0, s51, 66592\n"
"v_cvt_pk_bf16_f32 v196, v184, v185\n"
"v_cvt_pk_bf16_f32 v197, v186, v187\n"
"v_mfma_f32_16x16x32_bf16 a[8:11], a[164:167], a[212:215], a[8:11]\n"
"buffer_load_dwordx4 v217, s[24:27], s50 offen offset:0 lds\n"
"v_cvt_pk_bf16_f32 v198, v188, v189\n"
"v_mfma_f32_16x16x32_bf16 a[12:15], a[160:163], a[216:219], a[12:15]\n"
"s_add_u32 m0, s51, 74528\n"
"v_cvt_pk_bf16_f32 v199, v190, v191\n"
"v_mfma_f32_16x16x32_bf16 a[12:15], a[164:167], a[220:223], a[12:15]\n"
"buffer_load_dwordx4 v217, s[24:27], s50 offen offset:256 lds\n"
"ds_write_b128 v223, v[196:199] offset:1024\n"
"v_lshlrev_b32 v176, 16, v72\n"
"v_mfma_f32_16x16x32_bf16 a[16:19], a[168:171], a[192:195], a[16:19]\n"
"s_add_u32 s50, s50, 32768\n"
"ds_read_b64_tr_b16 a[160:161], v218 offset:512\n"
"v_and_b32 v177, v237, v72\n"
"v_mfma_f32_16x16x32_bf16 a[16:19], a[172:175], a[196:199], a[16:19]\n"
"ds_read_b64_tr_b16 a[162:163], v218 offset:1536\n"
"v_lshlrev_b32 v178, 16, v73\n"
"v_mfma_f32_16x16x32_bf16 a[20:23], a[168:171], a[200:203], a[20:23]\n"
"ds_read_b64_tr_b16 a[164:165], v218 offset:4608\n"
"v_and_b32 v179, v237, v73\n"
"v_mfma_f32_16x16x32_bf16 a[20:23], a[172:175], a[204:207], a[20:23]\n"
"ds_read_b64_tr_b16 a[166:167], v218 offset:5632\n"
"v_lshlrev_b32 v180, 16, v74\n"
"v_mfma_f32_16x16x32_bf16 a[24:27], a[168:171], a[208:211], a[24:27]\n"
"s_add_u32 m0, s51, 81960\n"
"v_and_b32 v181, v237, v74\n"
"v_lshlrev_b32 v182, 16, v75\n"
"v_mfma_f32_16x16x32_bf16 a[24:27], a[172:175], a[212:215], a[24:27]\n"
"buffer_load_dwordx4 v217, s[24:27], s50 offen offset:0 lds\n"
"v_and_b32 v183, v237, v75\n"
"v_fma_f32 v176, v176, v144, v152\n"
"v_mfma_f32_16x16x32_bf16 a[28:31], a[168:171], a[216:219], a[28:31]\n"
"s_add_u32 m0, s51, 89896\n"
"v_fma_f32 v177, v177, v145, v153\n"
"v_fma_f32 v178, v178, v146, v154\n"
"v_mfma_f32_16x16x32_bf16 a[28:31], a[172:175], a[220:223], a[28:31]\n"
"buffer_load_dwordx4 v217, s[24:27], s50 offen offset:256 lds\n"
"v_fma_f32 v179, v179, v147, v155\n"
"v_mfma_f32_16x16x32_bf16 a[32:35], a[176:179], a[192:195], a[32:35]\n"
"s_waitcnt lgkmcnt(1)\n"
"s_add_u32 s50, s50, 32768\n"
"ds_read_b64_tr_b16 a[168:169], v218 offset:520\n"
"v_fma_f32 v180, v180, v148, v156\n"
"v_mfma_f32_16x16x32_bf16 a[32:35], a[180:183], a[196:199], a[32:35]\n"
"ds_read_b64_tr_b16 a[170:171], v218 offset:1544\n"
"v_fma_f32 v181, v181, v149, v157\n"
"v_mfma_f32_16x16x32_bf16 a[36:39], a[176:179], a[200:203], a[36:39]\n"
"ds_read_b64_tr_b16 a[172:173], v218 offset:4616\n"
"v_fma_f32 v182, v182, v150, v158\n"
"v_mfma_f32_16x16x32_bf16 a[36:39], a[180:183], a[204:207], a[36:39]\n"
"ds_read_b64_tr_b16 a[174:175], v218 offset:5640\n"
"v_fma_f32 v183, v183, v151, v159\n"
"v_cvt_pk_bf16_f32 v192, v176, v177\n"
"v_mfma_f32_16x16x32_bf16 a[40:43], a[176:179], a[208:211], a[40:43]\n"
"s_add_u32 m0, s51, 82984\n"
"v_cvt_pk_bf16_f32 v193, v178, v179\n"
"v_cvt_pk_bf16_f32 v194, v180, v181\n"
"v_mfma_f32_16x16x32_bf16 a[40:43], a[180:183], a[212:215], a[40:43]\n"
"buffer_load_dwordx4 v217, s[24:27], s50 offen offset:0 lds\n"
"v_cvt_pk_bf16_f32 v195, v182, v183\n"
"v_mfma_f32_16x16x32_bf16 a[44:47], a[176:179], a[216:219], a[44:47]\n"
"s_add_u32 m0, s51, 90920\n"
"ds_write_b128 v222, v[192:195] offset:2048\n"
"v_lshlrev_b32 v184, 16, v76\n"
"v_mfma_f32_16x16x32_bf16 a[44:47], a[180:183], a[220:223], a[44:47]\n"
"buffer_load_dwordx4 v217, s[24:27], s50 offen offset:256 lds\n"
"v_and_b32 v185, v237, v76\n"
"v_mfma_f32_16x16x32_bf16 a[48:51], a[184:187], a[192:195], a[48:51]\n"
"s_add_u32 s50, s50, 425984\n"
"ds_read_b64_tr_b16 a[176:177], v218 offset:768\n"
"v_lshlrev_b32 v186, 16, v77\n"
"v_mfma_f32_16x16x32_bf16 a[48:51], a[188:191], a[196:199], a[48:51]\n"
"ds_read_b64_tr_b16 a[178:179], v218 offset:1792\n"
"v_and_b32 v187, v237, v77\n"
"v_lshlrev_b32 v188, 16, v78\n"
"v_mfma_f32_16x16x32_bf16 a[52:55], a[184:187], a[200:203], a[52:55]\n"
"ds_read_b64_tr_b16 a[180:181], v218 offset:4864\n"
"v_and_b32 v189, v237, v78\n"
"v_mfma_f32_16x16x32_bf16 a[52:55], a[188:191], a[204:207], a[52:55]\n"
"ds_read_b64_tr_b16 a[182:183], v218 offset:5888\n"
"v_lshlrev_b32 v190, 16, v79\n"
"v_mfma_f32_16x16x32_bf16 a[56:59], a[184:187], a[208:211], a[56:59]\n"
"ds_read_b128 v[160:163], v224 offset:2560\n"
"v_and_b32 v191, v237, v79\n"
"v_mfma_f32_16x16x32_bf16 a[56:59], a[188:191], a[212:215], a[56:59]\n"
"ds_read_b128 v[164:167], v224 offset:2688\n"
"v_fma_f32 v184, v184, v144, v152\n"
"v_mfma_f32_16x16x32_bf16 a[60:63], a[184:187], a[216:219], a[60:63]\n"
"ds_read_b128 v[168:171], v224 offset:2816\n"
"v_fma_f32 v185, v185, v145, v153\n"
"v_mfma_f32_16x16x32_bf16 a[60:63], a[188:191], a[220:223], a[60:63]\n"
"ds_read_b128 v[172:175], v224 offset:2944\n"
"v_fma_f32 v186, v186, v146, v154\n"
"v_fma_f32 v187, v187, v147, v155\n"
"v_mfma_f32_16x16x32_bf16 a[64:67], a[160:163], a[192:195], a[64:67]\n"
"s_waitcnt lgkmcnt(5)\n"
"ds_read_b64_tr_b16 a[184:185], v218 offset:776\n"
"v_fma_f32 v188, v188, v148, v156\n"
"v_mfma_f32_16x16x32_bf16 a[64:67], a[164:167], a[196:199], a[64:67]\n"
"ds_read_b64_tr_b16 a[186:187], v218 offset:1800\n"
"v_fma_f32 v189, v189, v149, v157\n"
"v_mfma_f32_16x16x32_bf16 a[68:71], a[160:163], a[200:203], a[68:71]\n"
"ds_read_b64_tr_b16 a[188:189], v218 offset:4872\n"
"v_fma_f32 v190, v190, v150, v158\n"
"v_mfma_f32_16x16x32_bf16 a[68:71], a[164:167], a[204:207], a[68:71]\n"
"ds_read_b64_tr_b16 a[190:191], v218 offset:5896\n"
"v_fma_f32 v191, v191, v151, v159\n"
"v_mfma_f32_16x16x32_bf16 a[72:75], a[160:163], a[208:211], a[72:75]\n"
"v_cvt_pk_bf16_f32 v196, v184, v185\n"
"v_cvt_pk_bf16_f32 v197, v186, v187\n"
"v_mfma_f32_16x16x32_bf16 a[72:75], a[164:167], a[212:215], a[72:75]\n"
"v_cvt_pk_bf16_f32 v198, v188, v189\n"
"v_cvt_pk_bf16_f32 v199, v190, v191\n"
"v_mfma_f32_16x16x32_bf16 a[76:79], a[160:163], a[216:219], a[76:79]\n"
"ds_write_b128 v223, v[196:199] offset:3072\n"
"v_lshlrev_b32 v176, 16, v80\n"
"v_mfma_f32_16x16x32_bf16 a[76:79], a[164:167], a[220:223], a[76:79]\n"
"v_and_b32 v177, v237, v80\n"
"v_lshlrev_b32 v178, 16, v81\n"
"v_mfma_f32_16x16x32_bf16 a[80:83], a[168:171], a[192:195], a[80:83]\n"
"v_and_b32 v179, v237, v81\n"
"v_lshlrev_b32 v180, 16, v82\n"
"v_mfma_f32_16x16x32_bf16 a[80:83], a[172:175], a[196:199], a[80:83]\n"
"v_and_b32 v181, v237, v82\n"
"v_lshlrev_b32 v182, 16, v83\n"
"v_mfma_f32_16x16x32_bf16 a[84:87], a[168:171], a[200:203], a[84:87]\n"
"s_waitcnt lgkmcnt(1)\n"
"v_and_b32 v183, v237, v83\n"
"v_fma_f32 v176, v176, v160, v168\n"
"v_mfma_f32_16x16x32_bf16 a[84:87], a[172:175], a[204:207], a[84:87]\n"
"v_fma_f32 v177, v177, v161, v169\n"
"v_fma_f32 v178, v178, v162, v170\n"
"v_fma_f32 v179, v179, v163, v171\n"
"v_mfma_f32_16x16x32_bf16 a[88:91], a[168:171], a[208:211], a[88:91]\n"
"v_fma_f32 v180, v180, v164, v172\n"
"v_fma_f32 v181, v181, v165, v173\n"
"v_mfma_f32_16x16x32_bf16 a[88:91], a[172:175], a[212:215], a[88:91]\n"
"v_fma_f32 v182, v182, v166, v174\n"
"v_fma_f32 v183, v183, v167, v175\n"
"v_mfma_f32_16x16x32_bf16 a[92:95], a[168:171], a[216:219], a[92:95]\n"
"v_cvt_pk_bf16_f32 v192, v176, v177\n"
"v_cvt_pk_bf16_f32 v193, v178, v179\n"
"v_mfma_f32_16x16x32_bf16 a[92:95], a[172:175], a[220:223], a[92:95]\n"
"s_waitcnt vmcnt(8) lgkmcnt(0)\n"
"s_barrier\n"
"v_cvt_pk_bf16_f32 v194, v180, v181\n"
"v_cvt_pk_bf16_f32 v195, v182, v183\n"
"v_mfma_f32_16x16x32_bf16 a[96:99], a[176:179], a[192:195], a[96:99]\n"
"ds_read_b64_tr_b16 a[160:161], v218 offset:32784\n"
"ds_read_b64_tr_b16 a[162:163], v218 offset:33808\n"
"v_mfma_f32_16x16x32_bf16 a[96:99], a[180:183], a[196:199], a[96:99]\n"
"ds_read_b64_tr_b16 a[164:165], v218 offset:36880\n"
"ds_read_b64_tr_b16 a[166:167], v218 offset:37904\n"
"ds_write_b128 v222, v[192:195] offset:16384\n"
"v_mfma_f32_16x16x32_bf16 a[100:103], a[176:179], a[200:203], a[100:103]\n"
"ds_read_b128 a[224:227], v220 offset:0\n"
"ds_read_b128 a[228:231], v221 offset:0\n"
"v_mfma_f32_16x16x32_bf16 a[100:103], a[180:183], a[204:207], a[100:103]\n"
"ds_read_b128 a[232:235], v220 offset:2048\n"
"ds_read_b128 a[236:239], v221 offset:2048\n"
"v_mfma_f32_16x16x32_bf16 a[104:107], a[176:179], a[208:211], a[104:107]\n"
"ds_read_b128 a[240:243], v220 offset:4096\n"
"ds_read_b128 a[244:247], v221 offset:4096\n"
"v_mfma_f32_16x16x32_bf16 a[104:107], a[180:183], a[212:215], a[104:107]\n"
"ds_read_b128 a[248:251], v220 offset:6144\n"
"ds_read_b128 a[252:255], v221 offset:6144\n"
"v_mfma_f32_16x16x32_bf16 a[108:111], a[176:179], a[216:219], a[108:111]\n"
"ds_read_b64_tr_b16 a[168:169], v218 offset:32792\n"
"ds_read_b64_tr_b16 a[170:171], v218 offset:33816\n"
"v_mfma_f32_16x16x32_bf16 a[108:111], a[180:183], a[220:223], a[108:111]\n"
"ds_read_b64_tr_b16 a[172:173], v218 offset:36888\n"
"ds_read_b64_tr_b16 a[174:175], v218 offset:37912\n"
"v_lshlrev_b32 v184, 16, v84\n"
"v_mfma_f32_16x16x32_bf16 a[112:115], a[184:187], a[192:195], a[112:115]\n"
"ds_read_b64_tr_b16 a[176:177], v218 offset:33040\n"
"v_and_b32 v185, v237, v84\n"
"v_mfma_f32_16x16x32_bf16 a[112:115], a[188:191], a[196:199], a[112:115]\n"
"ds_read_b64_tr_b16 a[178:179], v218 offset:34064\n"
"v_lshlrev_b32 v186, 16, v85\n"
"v_mfma_f32_16x16x32_bf16 a[116:119], a[184:187], a[200:203], a[116:119]\n"
"ds_read_b64_tr_b16 a[180:181], v218 offset:37136\n"
"v_and_b32 v187, v237, v85\n"
"v_mfma_f32_16x16x32_bf16 a[116:119], a[188:191], a[204:207], a[116:119]\n"
"ds_read_b64_tr_b16 a[182:183], v218 offset:38160\n"
"v_lshlrev_b32 v188, 16, v86\n"
"v_mfma_f32_16x16x32_bf16 a[120:123], a[184:187], a[208:211], a[120:123]\n"
"s_mov_b32 m0, s51\n"
"v_and_b32 v189, v237, v86\n"
"v_lshlrev_b32 v190, 16, v87\n"
"v_mfma_f32_16x16x32_bf16 a[120:123], a[188:191], a[212:215], a[120:123]\n"
"buffer_load_dwordx4 v217, s[24:27], s50 offen offset:0 lds\n"
"v_and_b32 v191, v237, v87\n"
"v_fma_f32 v184, v184, v160, v168\n"
"v_mfma_f32_16x16x32_bf16 a[124:127], a[184:187], a[216:219], a[124:127]\n"
"s_add_u32 m0, s51, 7936\n"
"v_fma_f32 v185, v185, v161, v169\n"
"v_fma_f32 v186, v186, v162, v170\n"
"v_mfma_f32_16x16x32_bf16 a[124:127], a[188:191], a[220:223], a[124:127]\n"
"s_waitcnt lgkmcnt(2)\n"
"buffer_load_dwordx4 v217, s[24:27], s50 offen offset:256 lds\n"
"v_fma_f32 v187, v187, v163, v171\n"
"v_mfma_f32_16x16x32_bf16 a[0:3], a[160:163], a[224:227], a[0:3]\n"
"s_add_u32 s50, s50, 32768\n"
"ds_read_b64_tr_b16 a[184:185], v218 offset:33048\n"
"v_fma_f32 v188, v188, v164, v172\n"
"v_mfma_f32_16x16x32_bf16 a[0:3], a[164:167], a[228:231], a[0:3]\n"
"ds_read_b64_tr_b16 a[186:187], v218 offset:34072\n"
"v_fma_f32 v189, v189, v165, v173\n"
"v_mfma_f32_16x16x32_bf16 a[4:7], a[160:163], a[232:235], a[4:7]\n"
"ds_read_b64_tr_b16 a[188:189], v218 offset:37144\n"
"v_fma_f32 v190, v190, v166, v174\n"
"v_mfma_f32_16x16x32_bf16 a[4:7], a[164:167], a[236:239], a[4:7]\n"
"ds_read_b64_tr_b16 a[190:191], v218 offset:38168\n"
"v_fma_f32 v191, v191, v167, v175\n"
"v_cvt_pk_bf16_f32 v196, v184, v185\n"
"v_mfma_f32_16x16x32_bf16 a[8:11], a[160:163], a[240:243], a[8:11]\n"
"s_add_u32 m0, s51, 1024\n"
"v_cvt_pk_bf16_f32 v197, v186, v187\n"
"v_cvt_pk_bf16_f32 v198, v188, v189\n"
"v_mfma_f32_16x16x32_bf16 a[8:11], a[164:167], a[244:247], a[8:11]\n"
"buffer_load_dwordx4 v217, s[24:27], s50 offen offset:0 lds\n"
"v_cvt_pk_bf16_f32 v199, v190, v191\n"
"v_mfma_f32_16x16x32_bf16 a[12:15], a[160:163], a[248:251], a[12:15]\n"
"s_add_u32 m0, s51, 8960\n"
"ds_write_b128 v223, v[196:199] offset:17408\n"
"v_lshlrev_b32 v176, 16, v88\n"
"v_mfma_f32_16x16x32_bf16 a[12:15], a[164:167], a[252:255], a[12:15]\n"
"buffer_load_dwordx4 v217, s[24:27], s50 offen offset:256 lds\n"
"v_and_b32 v177, v237, v88\n"
"v_mfma_f32_16x16x32_bf16 a[16:19], a[168:171], a[224:227], a[16:19]\n"
"s_add_u32 s50, s50, 32768\n"
"ds_read_b64_tr_b16 a[160:161], v218 offset:33296\n"
"v_lshlrev_b32 v178, 16, v89\n"
"v_mfma_f32_16x16x32_bf16 a[16:19], a[172:175], a[228:231], a[16:19]\n"
"ds_read_b64_tr_b16 a[162:163], v218 offset:34320\n"
"v_and_b32 v179, v237, v89\n"
"v_lshlrev_b32 v180, 16, v90\n"
"v_mfma_f32_16x16x32_bf16 a[20:23], a[168:171], a[232:235], a[20:23]\n"
"ds_read_b64_tr_b16 a[164:165], v218 offset:37392\n"
"v_and_b32 v181, v237, v90\n"
"v_mfma_f32_16x16x32_bf16 a[20:23], a[172:175], a[236:239], a[20:23]\n"
"ds_read_b64_tr_b16 a[166:167], v218 offset:38416\n"
"v_lshlrev_b32 v182, 16, v91\n"
"v_mfma_f32_16x16x32_bf16 a[24:27], a[168:171], a[240:243], a[24:27]\n"
"s_add_u32 m0, s51, 16392\n"
"v_and_b32 v183, v237, v91\n"
"v_fma_f32 v176, v176, v160, v168\n"
"v_mfma_f32_16x16x32_bf16 a[24:27], a[172:175], a[244:247], a[24:27]\n"
"buffer_load_dwordx4 v217, s[24:27], s50 offen offset:0 lds\n"
"v_fma_f32 v177, v177, v161, v169\n"
"v_mfma_f32_16x16x32_bf16 a[28:31], a[168:171], a[248:251], a[28:31]\n"
"s_add_u32 m0, s51, 24328\n"
"v_fma_f32 v178, v178, v162, v170\n"
"v_fma_f32 v179, v179, v163, v171\n"
"v_mfma_f32_16x16x32_bf16 a[28:31], a[172:175], a[252:255], a[28:31]\n"
"buffer_load_dwordx4 v217, s[24:27], s50 offen offset:256 lds\n"
"v_fma_f32 v180, v180, v164, v172\n"
"v_fma_f32 v181, v181, v165, v173\n"
"v_mfma_f32_16x16x32_bf16 a[32:35], a[176:179], a[224:227], a[32:35]\n"
"s_waitcnt lgkmcnt(1)\n"
"s_add_u32 s50, s50, 32768\n"
"ds_read_b64_tr_b16 a[168:169], v218 offset:33304\n"
"v_fma_f32 v182, v182, v166, v174\n"
"v_mfma_f32_16x16x32_bf16 a[32:35], a[180:183], a[228:231], a[32:35]\n"
"ds_read_b64_tr_b16 a[170:171], v218 offset:34328\n"
"v_fma_f32 v183, v183, v167, v175\n"
"v_mfma_f32_16x16x32_bf16 a[36:39], a[176:179], a[232:235], a[36:39]\n"
"ds_read_b64_tr_b16 a[172:173], v218 offset:37400\n"
"v_cvt_pk_bf16_f32 v192, v176, v177\n"
"v_mfma_f32_16x16x32_bf16 a[36:39], a[180:183], a[236:239], a[36:39]\n"
"ds_read_b64_tr_b16 a[174:175], v218 offset:38424\n"
"v_cvt_pk_bf16_f32 v193, v178, v179\n"
"v_mfma_f32_16x16x32_bf16 a[40:43], a[176:179], a[240:243], a[40:43]\n"
"s_add_u32 m0, s51, 17416\n"
"v_cvt_pk_bf16_f32 v194, v180, v181\n"
"v_cvt_pk_bf16_f32 v195, v182, v183\n"
"v_mfma_f32_16x16x32_bf16 a[40:43], a[180:183], a[244:247], a[40:43]\n"
"buffer_load_dwordx4 v217, s[24:27], s50 offen offset:0 lds\n"
"ds_write_b128 v222, v[192:195] offset:18432\n"
"v_lshlrev_b32 v184, 16, v92\n"
"v_mfma_f32_16x16x32_bf16 a[44:47], a[176:179], a[248:251], a[44:47]\n"
"s_add_u32 m0, s51, 25352\n"
"v_and_b32 v185, v237, v92\n"
"v_lshlrev_b32 v186, 16, v93\n"
"v_mfma_f32_16x16x32_bf16 a[44:47], a[180:183], a[252:255], a[44:47]\n"
"buffer_load_dwordx4 v217, s[24:27], s50 offen offset:256 lds\n"
"v_and_b32 v187, v237, v93\n"
"v_mfma_f32_16x16x32_bf16 a[48:51], a[184:187], a[224:227], a[48:51]\n"
"s_add_u32 s50, s50, 425984\n"
"ds_read_b64_tr_b16 a[176:177], v218 offset:33552\n"
"v_lshlrev_b32 v188, 16, v94\n"
"v_mfma_f32_16x16x32_bf16 a[48:51], a[188:191], a[228:231], a[48:51]\n"
"ds_read_b64_tr_b16 a[178:179], v218 offset:34576\n"
"v_and_b32 v189, v237, v94\n"
"v_mfma_f32_16x16x32_bf16 a[52:55], a[184:187], a[232:235], a[52:55]\n"
"ds_read_b64_tr_b16 a[180:181], v218 offset:37648\n"
"v_lshlrev_b32 v190, 16, v95\n"
"v_mfma_f32_16x16x32_bf16 a[52:55], a[188:191], a[236:239], a[52:55]\n"
"ds_read_b64_tr_b16 a[182:183], v218 offset:38672\n"
"v_and_b32 v191, v237, v95\n"
"v_fma_f32 v184, v184, v160, v168\n"
"v_mfma_f32_16x16x32_bf16 a[56:59], a[184:187], a[240:243], a[56:59]\n"
"ds_read_b128 v[144:147], v224 offset:3072\n"
"v_fma_f32 v185, v185, v161, v169\n"
"v_mfma_f32_16x16x32_bf16 a[56:59], a[188:191], a[244:247], a[56:59]\n"
"ds_read_b128 v[148:151], v224 offset:3200\n"
"v_fma_f32 v186, v186, v162, v170\n"
"v_mfma_f32_16x16x32_bf16 a[60:63], a[184:187], a[248:251], a[60:63]\n"
"ds_read_b128 v[152:155], v224 offset:3328\n"
"v_fma_f32 v187, v187, v163, v171\n"
"v_mfma_f32_16x16x32_bf16 a[60:63], a[188:191], a[252:255], a[60:63]\n"
"ds_read_b128 v[156:159], v224 offset:3456\n"
"v_fma_f32 v188, v188, v164, v172\n"
"v_mfma_f32_16x16x32_bf16 a[64:67], a[160:163], a[224:227], a[64:67]\n"
"s_waitcnt lgkmcnt(5)\n"
"ds_read_b64_tr_b16 a[184:185], v218 offset:33560\n"
"v_fma_f32 v189, v189, v165, v173\n"
"v_mfma_f32_16x16x32_bf16 a[64:67], a[164:167], a[228:231], a[64:67]\n"
"ds_read_b64_tr_b16 a[186:187], v218 offset:34584\n"
"v_fma_f32 v190, v190, v166, v174\n"
"v_fma_f32 v191, v191, v167, v175\n"
"v_mfma_f32_16x16x32_bf16 a[68:71], a[160:163], a[232:235], a[68:71]\n"
"ds_read_b64_tr_b16 a[188:189], v218 offset:37656\n"
"v_cvt_pk_bf16_f32 v196, v184, v185\n"
"v_mfma_f32_16x16x32_bf16 a[68:71], a[164:167], a[236:239], a[68:71]\n"
"ds_read_b64_tr_b16 a[190:191], v218 offset:38680\n"
"v_cvt_pk_bf16_f32 v197, v186, v187\n"
"v_mfma_f32_16x16x32_bf16 a[72:75], a[160:163], a[240:243], a[72:75]\n"
"v_cvt_pk_bf16_f32 v198, v188, v189\n"
"v_cvt_pk_bf16_f32 v199, v190, v191\n"
"v_mfma_f32_16x16x32_bf16 a[72:75], a[164:167], a[244:247], a[72:75]\n"
"ds_write_b128 v223, v[196:199] offset:19456\n"
"v_mfma_f32_16x16x32_bf16 a[76:79], a[160:163], a[248:251], a[76:79]\n"
"v_lshlrev_b32 v176, 16, v96\n"
"v_and_b32 v177, v237, v96\n"
"v_mfma_f32_16x16x32_bf16 a[76:79], a[164:167], a[252:255], a[76:79]\n"
"v_lshlrev_b32 v178, 16, v97\n"
"v_and_b32 v179, v237, v97\n"
"v_lshlrev_b32 v180, 16, v98\n"
"v_mfma_f32_16x16x32_bf16 a[80:83], a[168:171], a[224:227], a[80:83]\n"
"v_and_b32 v181, v237, v98\n"
"v_lshlrev_b32 v182, 16, v99\n"
"v_mfma_f32_16x16x32_bf16 a[80:83], a[172:175], a[228:231], a[80:83]\n"
"s_waitcnt lgkmcnt(1)\n"
"v_and_b32 v183, v237, v99\n"
"v_fma_f32 v176, v176, v144, v152\n"
"v_mfma_f32_16x16x32_bf16 a[84:87], a[168:171], a[232:235], a[84:87]\n"
"v_fma_f32 v177, v177, v145, v153\n"
"v_fma_f32 v178, v178, v146, v154\n"
"v_mfma_f32_16x16x32_bf16 a[84:87], a[172:175], a[236:239], a[84:87]\n"
"v_fma_f32 v179, v179, v147, v155\n"
"v_fma_f32 v180, v180, v148, v156\n"
"v_mfma_f32_16x16x32_bf16 a[88:91], a[168:171], a[240:243], a[88:91]\n"
"v_fma_f32 v181, v181, v149, v157\n"
"v_fma_f32 v182, v182, v150, v158\n"
"v_mfma_f32_16x16x32_bf16 a[88:91], a[172:175], a[244:247], a[88:91]\n"
"v_fma_f32 v183, v183, v151, v159\n"
"v_cvt_pk_bf16_f32 v192, v176, v177\n"
"v_cvt_pk_bf16_f32 v193, v178, v179\n"
"v_mfma_f32_16x16x32_bf16 a[92:95], a[168:171], a[248:251], a[92:95]\n"
"v_cvt_pk_bf16_f32 v194, v180, v181\n"
"v_cvt_pk_bf16_f32 v195, v182, v183\n"
"v_mfma_f32_16x16x32_bf16 a[92:95], a[172:175], a[252:255], a[92:95]\n"
"s_waitcnt vmcnt(8) lgkmcnt(0)\n"
"s_barrier\n"
"v_mfma_f32_16x16x32_bf16 a[96:99], a[176:179], a[224:227], a[96:99]\n"
"ds_read_b64_tr_b16 a[160:161], v219 offset:0\n"
"ds_read_b64_tr_b16 a[162:163], v219 offset:1024\n"
"v_mfma_f32_16x16x32_bf16 a[96:99], a[180:183], a[228:231], a[96:99]\n"
"ds_read_b64_tr_b16 a[164:165], v219 offset:4096\n"
"ds_read_b64_tr_b16 a[166:167], v219 offset:5120\n"
"v_mfma_f32_16x16x32_bf16 a[100:103], a[176:179], a[232:235], a[100:103]\n"
"ds_read_b128 a[192:195], v220 offset:16384\n"
"ds_read_b128 a[196:199], v221 offset:16384\n"
"v_mfma_f32_16x16x32_bf16 a[100:103], a[180:183], a[236:239], a[100:103]\n"
"ds_read_b128 a[200:203], v220 offset:18432\n"
"ds_read_b128 a[204:207], v221 offset:18432\n"
"ds_write_b128 v222, v[192:195] offset:0\n"
"v_mfma_f32_16x16x32_bf16 a[104:107], a[176:179], a[240:243], a[104:107]\n"
"ds_read_b128 a[208:211], v220 offset:20480\n"
"ds_read_b128 a[212:215], v221 offset:20480\n"
"v_mfma_f32_16x16x32_bf16 a[104:107], a[180:183], a[244:247], a[104:107]\n"
"ds_read_b128 a[216:219], v220 offset:22528\n"
"ds_read_b128 a[220:223], v221 offset:22528\n"
"v_mfma_f32_16x16x32_bf16 a[108:111], a[176:179], a[248:251], a[108:111]\n"
"ds_read_b64_tr_b16 a[168:169], v219 offset:8\n"
"ds_read_b64_tr_b16 a[170:171], v219 offset:1032\n"
"v_mfma_f32_16x16x32_bf16 a[108:111], a[180:183], a[252:255], a[108:111]\n"
"ds_read_b64_tr_b16 a[172:173], v219 offset:4104\n"
"ds_read_b64_tr_b16 a[174:175], v219 offset:5128\n"
"v_mfma_f32_16x16x32_bf16 a[112:115], a[184:187], a[224:227], a[112:115]\n"
"ds_read_b64_tr_b16 a[176:177], v219 offset:256\n"
"v_lshlrev_b32 v184, 16, v100\n"
"v_mfma_f32_16x16x32_bf16 a[112:115], a[188:191], a[228:231], a[112:115]\n"
"ds_read_b64_tr_b16 a[178:179], v219 offset:1280\n"
"v_and_b32 v185, v237, v100\n"
"v_lshlrev_b32 v186, 16, v101\n"
"v_mfma_f32_16x16x32_bf16 a[116:119], a[184:187], a[232:235], a[116:119]\n"
"ds_read_b64_tr_b16 a[180:181], v219 offset:4352\n"
"v_and_b32 v187, v237, v101\n"
"v_mfma_f32_16x16x32_bf16 a[116:119], a[188:191], a[236:239], a[116:119]\n"
"ds_read_b64_tr_b16 a[182:183], v219 offset:5376\n"
"v_lshlrev_b32 v188, 16, v102\n"
"v_mfma_f32_16x16x32_bf16 a[120:123], a[184:187], a[240:243], a[120:123]\n"
"s_add_u32 m0, s51, 32784\n"
"v_and_b32 v189, v237, v102\n"
"v_lshlrev_b32 v190, 16, v103\n"
"v_mfma_f32_16x16x32_bf16 a[120:123], a[188:191], a[244:247], a[120:123]\n"
"buffer_load_dwordx4 v217, s[24:27], s50 offen offset:0 lds\n"
"v_and_b32 v191, v237, v103\n"
"v_mfma_f32_16x16x32_bf16 a[124:127], a[184:187], a[248:251], a[124:127]\n"
"s_add_u32 m0, s51, 40720\n"
"v_fma_f32 v184, v184, v144, v152\n"
"v_fma_f32 v185, v185, v145, v153\n"
"v_mfma_f32_16x16x32_bf16 a[124:127], a[188:191], a[252:255], a[124:127]\n"
"s_waitcnt lgkmcnt(2)\n"
"buffer_load_dwordx4 v217, s[24:27], s50 offen offset:256 lds\n"
"v_fma_f32 v186, v186, v146, v154\n"
"v_fma_f32 v187, v187, v147, v155\n"
"v_mfma_f32_16x16x32_bf16 a[0:3], a[160:163], a[192:195], a[0:3]\n"
"s_add_u32 s50, s50, 32768\n"
"ds_read_b64_tr_b16 a[184:185], v219 offset:264\n"
"v_fma_f32 v188, v188, v148, v156\n"
"v_mfma_f32_16x16x32_bf16 a[0:3], a[164:167], a[196:199], a[0:3]\n"
"ds_read_b64_tr_b16 a[186:187], v219 offset:1288\n"
"v_fma_f32 v189, v189, v149, v157\n"
"v_mfma_f32_16x16x32_bf16 a[4:7], a[160:163], a[200:203], a[4:7]\n"
"ds_read_b64_tr_b16 a[188:189], v219 offset:4360\n"
"v_fma_f32 v190, v190, v150, v158\n"
"v_mfma_f32_16x16x32_bf16 a[4:7], a[164:167], a[204:207], a[4:7]\n"
"ds_read_b64_tr_b16 a[190:191], v219 offset:5384\n"
"v_fma_f32 v191, v191, v151, v159\n"
"v_mfma_f32_16x16x32_bf16 a[8:11], a[160:163], a[208:211], a[8:11]\n"
"s_add_u32 m0, s51, 33808\n"
"v_cvt_pk_bf16_f32 v196, v184, v185\n"
"v_cvt_pk_bf16_f32 v197, v186, v187\n"
"v_mfma_f32_16x16x32_bf16 a[8:11], a[164:167], a[212:215], a[8:11]\n"
"buffer_load_dwordx4 v217, s[24:27], s50 offen offset:0 lds\n"
"v_cvt_pk_bf16_f32 v198, v188, v189\n"
"v_cvt_pk_bf16_f32 v199, v190, v191\n"
"v_mfma_f32_16x16x32_bf16 a[12:15], a[160:163], a[216:219], a[12:15]\n"
"s_add_u32 m0, s51, 41744\n"
"ds_write_b128 v223, v[196:199] offset:1024\n"
"v_lshlrev_b32 v176, 16, v104\n"
"v_mfma_f32_16x16x32_bf16 a[12:15], a[164:167], a[220:223], a[12:15]\n"
"buffer_load_dwordx4 v217, s[24:27], s50 offen offset:256 lds\n"
"v_and_b32 v177, v237, v104\n"
"v_mfma_f32_16x16x32_bf16 a[16:19], a[168:171], a[192:195], a[16:19]\n"
"s_add_u32 s50, s50, 32768\n"
"ds_read_b64_tr_b16 a[160:161], v219 offset:512\n"
"v_lshlrev_b32 v178, 16, v105\n"
"v_mfma_f32_16x16x32_bf16 a[16:19], a[172:175], a[196:199], a[16:19]\n"
"ds_read_b64_tr_b16 a[162:163], v219 offset:1536\n"
"v_and_b32 v179, v237, v105\n"
"v_mfma_f32_16x16x32_bf16 a[20:23], a[168:171], a[200:203], a[20:23]\n"
"ds_read_b64_tr_b16 a[164:165], v219 offset:4608\n"
"v_lshlrev_b32 v180, 16, v106\n"
"v_mfma_f32_16x16x32_bf16 a[20:23], a[172:175], a[204:207], a[20:23]\n"
"ds_read_b64_tr_b16 a[166:167], v219 offset:5632\n"
"v_and_b32 v181, v237, v106\n"
"v_lshlrev_b32 v182, 16, v107\n"
"v_mfma_f32_16x16x32_bf16 a[24:27], a[168:171], a[208:211], a[24:27]\n"
"s_add_u32 m0, s51, 49176\n"
"v_and_b32 v183, v237, v107\n"
"v_fma_f32 v176, v176, v144, v152\n"
"v_mfma_f32_16x16x32_bf16 a[24:27], a[172:175], a[212:215], a[24:27]\n"
"buffer_load_dwordx4 v217, s[24:27], s50 offen offset:0 lds\n"
"v_fma_f32 v177, v177, v145, v153\n"
"v_mfma_f32_16x16x32_bf16 a[28:31], a[168:171], a[216:219], a[28:31]\n"
"s_add_u32 m0, s51, 57112\n"
"v_fma_f32 v178, v178, v146, v154\n"
"v_fma_f32 v179, v179, v147, v155\n"
"v_mfma_f32_16x16x32_bf16 a[28:31], a[172:175], a[220:223], a[28:31]\n"
"buffer_load_dwordx4 v217, s[24:27], s50 offen offset:256 lds\n"
"v_fma_f32 v180, v180, v148, v156\n"
"v_mfma_f32_16x16x32_bf16 a[32:35], a[176:179], a[192:195], a[32:35]\n"
"s_waitcnt lgkmcnt(1)\n"
"s_add_u32 s50, s50, 32768\n"
"ds_read_b64_tr_b16 a[168:169], v219 offset:520\n"
"v_fma_f32 v181, v181, v149, v157\n"
"v_mfma_f32_16x16x32_bf16 a[32:35], a[180:183], a[196:199], a[32:35]\n"
"ds_read_b64_tr_b16 a[170:171], v219 offset:1544\n"
"v_fma_f32 v182, v182, v150, v158\n"
"v_fma_f32 v183, v183, v151, v159\n"
"v_mfma_f32_16x16x32_bf16 a[36:39], a[176:179], a[200:203], a[36:39]\n"
"ds_read_b64_tr_b16 a[172:173], v219 offset:4616\n"
"v_cvt_pk_bf16_f32 v192, v176, v177\n"
"v_mfma_f32_16x16x32_bf16 a[36:39], a[180:183], a[204:207], a[36:39]\n"
"ds_read_b64_tr_b16 a[174:175], v219 offset:5640\n"
"v_cvt_pk_bf16_f32 v193, v178, v179\n"
"v_mfma_f32_16x16x32_bf16 a[40:43], a[176:179], a[208:211], a[40:43]\n"
"s_add_u32 m0, s51, 50200\n"
"v_cvt_pk_bf16_f32 v194, v180, v181\n"
"v_cvt_pk_bf16_f32 v195, v182, v183\n"
"v_mfma_f32_16x16x32_bf16 a[40:43], a[180:183], a[212:215], a[40:43]\n"
"buffer_load_dwordx4 v217, s[24:27], s50 offen offset:0 lds\n"
"ds_write_b128 v222, v[192:195] offset:2048\n"
"v_mfma_f32_16x16x32_bf16 a[44:47], a[176:179], a[216:219], a[44:47]\n"
"s_add_u32 m0, s51, 58136\n"
"v_lshlrev_b32 v184, 16, v108\n"
"v_and_b32 v185, v237, v108\n"
"v_mfma_f32_16x16x32_bf16 a[44:47], a[180:183], a[220:223], a[44:47]\n"
"buffer_load_dwordx4 v217, s[24:27], s50 offen offset:256 lds\n"
"v_lshlrev_b32 v186, 16, v109\n"
"v_and_b32 v187, v237, v109\n"
"v_mfma_f32_16x16x32_bf16 a[48:51], a[184:187], a[192:195], a[48:51]\n"
"s_add_u32 s50, s50, 425984\n"
"ds_read_b64_tr_b16 a[176:177], v219 offset:768\n"
"v_lshlrev_b32 v188, 16, v110\n"
"v_mfma_f32_16x16x32_bf16 a[48:51], a[188:191], a[196:199], a[48:51]\n"
"ds_read_b64_tr_b16 a[178:179], v219 offset:1792\n"
"v_and_b32 v189, v237, v110\n"
"v_mfma_f32_16x16x32_bf16 a[52:55], a[184:187], a[200:203], a[52:55]\n"
"ds_read_b64_tr_b16 a[180:181], v219 offset:4864\n"
"v_lshlrev_b32 v190, 16, v111\n"
"v_mfma_f32_16x16x32_bf16 a[52:55], a[188:191], a[204:207], a[52:55]\n"
"ds_read_b64_tr_b16 a[182:183], v219 offset:5888\n"
"v_and_b32 v191, v237, v111\n"
"v_mfma_f32_16x16x32_bf16 a[56:59], a[184:187], a[208:211], a[56:59]\n"
"ds_read_b128 v[160:163], v224 offset:3584\n"
"v_fma_f32 v184, v184, v144, v152\n"
"v_mfma_f32_16x16x32_bf16 a[56:59], a[188:191], a[212:215], a[56:59]\n"
"ds_read_b128 v[164:167], v224 offset:3712\n"
"v_fma_f32 v185, v185, v145, v153\n"
"v_fma_f32 v186, v186, v146, v154\n"
"v_mfma_f32_16x16x32_bf16 a[60:63], a[184:187], a[216:219], a[60:63]\n"
"ds_read_b128 v[168:171], v224 offset:3840\n"
"v_fma_f32 v187, v187, v147, v155\n"
"v_mfma_f32_16x16x32_bf16 a[60:63], a[188:191], a[220:223], a[60:63]\n"
"ds_read_b128 v[172:175], v224 offset:3968\n"
"v_fma_f32 v188, v188, v148, v156\n"
"v_mfma_f32_16x16x32_bf16 a[64:67], a[160:163], a[192:195], a[64:67]\n"
"s_waitcnt lgkmcnt(5)\n"
"ds_read_b64_tr_b16 a[184:185], v219 offset:776\n"
"v_fma_f32 v189, v189, v149, v157\n"
"v_mfma_f32_16x16x32_bf16 a[64:67], a[164:167], a[196:199], a[64:67]\n"
"ds_read_b64_tr_b16 a[186:187], v219 offset:1800\n"
"v_fma_f32 v190, v190, v150, v158\n"
"v_mfma_f32_16x16x32_bf16 a[68:71], a[160:163], a[200:203], a[68:71]\n"
"ds_read_b64_tr_b16 a[188:189], v219 offset:4872\n"
"v_fma_f32 v191, v191, v151, v159\n"
"v_mfma_f32_16x16x32_bf16 a[68:71], a[164:167], a[204:207], a[68:71]\n"
"ds_read_b64_tr_b16 a[190:191], v219 offset:5896\n"
"v_cvt_pk_bf16_f32 v196, v184, v185\n"
"v_cvt_pk_bf16_f32 v197, v186, v187\n"
"v_mfma_f32_16x16x32_bf16 a[72:75], a[160:163], a[208:211], a[72:75]\n"
"v_cvt_pk_bf16_f32 v198, v188, v189\n"
"v_cvt_pk_bf16_f32 v199, v190, v191\n"
"v_mfma_f32_16x16x32_bf16 a[72:75], a[164:167], a[212:215], a[72:75]\n"
"ds_write_b128 v223, v[196:199] offset:3072\n"
"v_mfma_f32_16x16x32_bf16 a[76:79], a[160:163], a[216:219], a[76:79]\n"
"v_lshlrev_b32 v176, 16, v112\n"
"v_and_b32 v177, v237, v112\n"
"v_mfma_f32_16x16x32_bf16 a[76:79], a[164:167], a[220:223], a[76:79]\n"
"v_lshlrev_b32 v178, 16, v113\n"
"v_and_b32 v179, v237, v113\n"
"v_mfma_f32_16x16x32_bf16 a[80:83], a[168:171], a[192:195], a[80:83]\n"
"v_lshlrev_b32 v180, 16, v114\n"
"v_and_b32 v181, v237, v114\n"
"v_mfma_f32_16x16x32_bf16 a[80:83], a[172:175], a[196:199], a[80:83]\n"
"s_waitcnt lgkmcnt(1)\n"
"v_lshlrev_b32 v182, 16, v115\n"
"v_and_b32 v183, v237, v115\n"
"v_fma_f32 v176, v176, v160, v168\n"
"v_mfma_f32_16x16x32_bf16 a[84:87], a[168:171], a[200:203], a[84:87]\n"
"v_fma_f32 v177, v177, v161, v169\n"
"v_fma_f32 v178, v178, v162, v170\n"
"v_mfma_f32_16x16x32_bf16 a[84:87], a[172:175], a[204:207], a[84:87]\n"
"v_fma_f32 v179, v179, v163, v171\n"
"v_fma_f32 v180, v180, v164, v172\n"
"v_mfma_f32_16x16x32_bf16 a[88:91], a[168:171], a[208:211], a[88:91]\n"
"v_fma_f32 v181, v181, v165, v173\n"
"v_fma_f32 v182, v182, v166, v174\n"
"v_mfma_f32_16x16x32_bf16 a[88:91], a[172:175], a[212:215], a[88:91]\n"
"v_fma_f32 v183, v183, v167, v175\n"
"v_cvt_pk_bf16_f32 v192, v176, v177\n"
"v_mfma_f32_16x16x32_bf16 a[92:95], a[168:171], a[216:219], a[92:95]\n"
"v_cvt_pk_bf16_f32 v193, v178, v179\n"
"v_cvt_pk_bf16_f32 v194, v180, v181\n"
"v_mfma_f32_16x16x32_bf16 a[92:95], a[172:175], a[220:223], a[92:95]\n"
"s_waitcnt vmcnt(8) lgkmcnt(0)\n"
"s_barrier\n"
"v_cvt_pk_bf16_f32 v195, v182, v183\n"
"v_mfma_f32_16x16x32_bf16 a[96:99], a[176:179], a[192:195], a[96:99]\n"
"ds_read_b64_tr_b16 a[160:161], v218 offset:0\n"
"ds_read_b64_tr_b16 a[162:163], v218 offset:1024\n"
"v_mfma_f32_16x16x32_bf16 a[96:99], a[180:183], a[196:199], a[96:99]\n"
"ds_read_b64_tr_b16 a[164:165], v218 offset:4096\n"
"ds_read_b64_tr_b16 a[166:167], v218 offset:5120\n"
"v_mfma_f32_16x16x32_bf16 a[100:103], a[176:179], a[200:203], a[100:103]\n"
"ds_read_b128 a[224:227], v220 offset:0\n"
"ds_read_b128 a[228:231], v221 offset:0\n"
"v_mfma_f32_16x16x32_bf16 a[100:103], a[180:183], a[204:207], a[100:103]\n"
"ds_read_b128 a[232:235], v220 offset:2048\n"
"ds_read_b128 a[236:239], v221 offset:2048\n"
"v_mfma_f32_16x16x32_bf16 a[104:107], a[176:179], a[208:211], a[104:107]\n"
"ds_read_b128 a[240:243], v220 offset:4096\n"
"ds_read_b128 a[244:247], v221 offset:4096\n"
"v_mfma_f32_16x16x32_bf16 a[104:107], a[180:183], a[212:215], a[104:107]\n"
"ds_read_b128 a[248:251], v220 offset:6144\n"
"ds_read_b128 a[252:255], v221 offset:6144\n"
"ds_write_b128 v222, v[192:195] offset:16384\n"
"v_mfma_f32_16x16x32_bf16 a[108:111], a[176:179], a[216:219], a[108:111]\n"
"ds_read_b64_tr_b16 a[168:169], v218 offset:8\n"
"ds_read_b64_tr_b16 a[170:171], v218 offset:1032\n"
"v_mfma_f32_16x16x32_bf16 a[108:111], a[180:183], a[220:223], a[108:111]\n"
"ds_read_b64_tr_b16 a[172:173], v218 offset:4104\n"
"ds_read_b64_tr_b16 a[174:175], v218 offset:5128\n"
"v_mfma_f32_16x16x32_bf16 a[112:115], a[184:187], a[192:195], a[112:115]\n"
"ds_read_b64_tr_b16 a[176:177], v218 offset:256\n"
"v_lshlrev_b32 v184, 16, v116\n"
"v_mfma_f32_16x16x32_bf16 a[112:115], a[188:191], a[196:199], a[112:115]\n"
"ds_read_b64_tr_b16 a[178:179], v218 offset:1280\n"
"v_and_b32 v185, v237, v116\n"
"v_mfma_f32_16x16x32_bf16 a[116:119], a[184:187], a[200:203], a[116:119]\n"
"ds_read_b64_tr_b16 a[180:181], v218 offset:4352\n"
"v_lshlrev_b32 v186, 16, v117\n"
"v_mfma_f32_16x16x32_bf16 a[116:119], a[188:191], a[204:207], a[116:119]\n"
"ds_read_b64_tr_b16 a[182:183], v218 offset:5376\n"
"v_and_b32 v187, v237, v117\n"
"v_lshlrev_b32 v188, 16, v118\n"
"v_mfma_f32_16x16x32_bf16 a[120:123], a[184:187], a[208:211], a[120:123]\n"
"s_add_u32 m0, s51, 65568\n"
"v_and_b32 v189, v237, v118\n"
"v_lshlrev_b32 v190, 16, v119\n"
"v_mfma_f32_16x16x32_bf16 a[120:123], a[188:191], a[212:215], a[120:123]\n"
"buffer_load_dwordx4 v217, s[24:27], s50 offen offset:0 lds\n"
"v_and_b32 v191, v237, v119\n"
"v_mfma_f32_16x16x32_bf16 a[124:127], a[184:187], a[216:219], a[124:127]\n"
"s_add_u32 m0, s51, 73504\n"
"v_fma_f32 v184, v184, v160, v168\n"
"v_fma_f32 v185, v185, v161, v169\n"
"v_mfma_f32_16x16x32_bf16 a[124:127], a[188:191], a[220:223], a[124:127]\n"
"s_waitcnt lgkmcnt(2)\n"
"buffer_load_dwordx4 v217, s[24:27], s50 offen offset:256 lds\n"
"v_fma_f32 v186, v186, v162, v170\n"
"v_mfma_f32_16x16x32_bf16 a[0:3], a[160:163], a[224:227], a[0:3]\n"
"s_add_u32 s50, s50, 32768\n"
"ds_read_b64_tr_b16 a[184:185], v218 offset:264\n"
"v_fma_f32 v187, v187, v163, v171\n"
"v_mfma_f32_16x16x32_bf16 a[0:3], a[164:167], a[228:231], a[0:3]\n"
"ds_read_b64_tr_b16 a[186:187], v218 offset:1288\n"
"v_fma_f32 v188, v188, v164, v172\n"
"v_fma_f32 v189, v189, v165, v173\n"
"v_mfma_f32_16x16x32_bf16 a[4:7], a[160:163], a[232:235], a[4:7]\n"
"ds_read_b64_tr_b16 a[188:189], v218 offset:4360\n"
"v_fma_f32 v190, v190, v166, v174\n"
"v_mfma_f32_16x16x32_bf16 a[4:7], a[164:167], a[236:239], a[4:7]\n"
"ds_read_b64_tr_b16 a[190:191], v218 offset:5384\n"
"v_fma_f32 v191, v191, v167, v175\n"
"v_mfma_f32_16x16x32_bf16 a[8:11], a[160:163], a[240:243], a[8:11]\n"
"s_add_u32 m0, s51, 66592\n"
"v_cvt_pk_bf16_f32 v196, v184, v185\n"
"v_cvt_pk_bf16_f32 v197, v186, v187\n"
"v_mfma_f32_16x16x32_bf16 a[8:11], a[164:167], a[244:247], a[8:11]\n"
"buffer_load_dwordx4 v217, s[24:27], s50 offen offset:0 lds\n"
"v_cvt_pk_bf16_f32 v198, v188, v189\n"
"v_mfma_f32_16x16x32_bf16 a[12:15], a[160:163], a[248:251], a[12:15]\n"
"s_add_u32 m0, s51, 74528\n"
"v_cvt_pk_bf16_f32 v199, v190, v191\n"
"v_mfma_f32_16x16x32_bf16 a[12:15], a[164:167], a[252:255], a[12:15]\n"
"buffer_load_dwordx4 v217, s[24:27], s50 offen offset:256 lds\n"
"ds_write_b128 v223, v[196:199] offset:17408\n"
"v_lshlrev_b32 v176, 16, v120\n"
"v_mfma_f32_16x16x32_bf16 a[16:19], a[168:171], a[224:227], a[16:19]\n"
"s_add_u32 s50, s50, 32768\n"
"ds_read_b64_tr_b16 a[160:161], v218 offset:512\n"
"v_and_b32 v177, v237, v120\n"
"v_mfma_f32_16x16x32_bf16 a[16:19], a[172:175], a[228:231], a[16:19]\n"
"ds_read_b64_tr_b16 a[162:163], v218 offset:1536\n"
"v_lshlrev_b32 v178, 16, v121\n"
"v_mfma_f32_16x16x32_bf16 a[20:23], a[168:171], a[232:235], a[20:23]\n"
"ds_read_b64_tr_b16 a[164:165], v218 offset:4608\n"
"v_and_b32 v179, v237, v121\n"
"v_mfma_f32_16x16x32_bf16 a[20:23], a[172:175], a[236:239], a[20:23]\n"
"ds_read_b64_tr_b16 a[166:167], v218 offset:5632\n"
"v_lshlrev_b32 v180, 16, v122\n"
"v_mfma_f32_16x16x32_bf16 a[24:27], a[168:171], a[240:243], a[24:27]\n"
"s_add_u32 m0, s51, 81960\n"
"v_and_b32 v181, v237, v122\n"
"v_lshlrev_b32 v182, 16, v123\n"
"v_mfma_f32_16x16x32_bf16 a[24:27], a[172:175], a[244:247], a[24:27]\n"
"buffer_load_dwordx4 v217, s[24:27], s50 offen offset:0 lds\n"
"v_and_b32 v183, v237, v123\n"
"v_fma_f32 v176, v176, v160, v168\n"
"v_mfma_f32_16x16x32_bf16 a[28:31], a[168:171], a[248:251], a[28:31]\n"
"s_add_u32 m0, s51, 89896\n"
"v_fma_f32 v177, v177, v161, v169\n"
"v_fma_f32 v178, v178, v162, v170\n"
"v_mfma_f32_16x16x32_bf16 a[28:31], a[172:175], a[252:255], a[28:31]\n"
"buffer_load_dwordx4 v217, s[24:27], s50 offen offset:256 lds\n"
"v_fma_f32 v179, v179, v163, v171\n"
"v_mfma_f32_16x16x32_bf16 a[32:35], a[176:179], a[224:227], a[32:35]\n"
"s_waitcnt lgkmcnt(1)\n"
"s_add_u32 s50, s50, 32768\n"
"ds_read_b64_tr_b16 a[168:169], v218 offset:520\n"
"v_fma_f32 v180, v180, v164, v172\n"
"v_mfma_f32_16x16x32_bf16 a[32:35], a[180:183], a[228:231], a[32:35]\n"
"ds_read_b64_tr_b16 a[170:171], v218 offset:1544\n"
"v_fma_f32 v181, v181, v165, v173\n"
"v_mfma_f32_16x16x32_bf16 a[36:39], a[176:179], a[232:235], a[36:39]\n"
"ds_read_b64_tr_b16 a[172:173], v218 offset:4616\n"
"v_fma_f32 v182, v182, v166, v174\n"
"v_mfma_f32_16x16x32_bf16 a[36:39], a[180:183], a[236:239], a[36:39]\n"
"ds_read_b64_tr_b16 a[174:175], v218 offset:5640\n"
"v_fma_f32 v183, v183, v167, v175\n"
"v_cvt_pk_bf16_f32 v192, v176, v177\n"
"v_mfma_f32_16x16x32_bf16 a[40:43], a[176:179], a[240:243], a[40:43]\n"
"s_add_u32 m0, s51, 82984\n"
"v_cvt_pk_bf16_f32 v193, v178, v179\n"
"v_cvt_pk_bf16_f32 v194, v180, v181\n"
"v_mfma_f32_16x16x32_bf16 a[40:43], a[180:183], a[244:247], a[40:43]\n"
"buffer_load_dwordx4 v217, s[24:27], s50 offen offset:0 lds\n"
"v_cvt_pk_bf16_f32 v195, v182, v183\n"
"v_mfma_f32_16x16x32_bf16 a[44:47], a[176:179], a[248:251], a[44:47]\n"
"s_add_u32 m0, s51, 90920\n"
"ds_write_b128 v222, v[192:195] offset:18432\n"
"v_lshlrev_b32 v184, 16, v124\n"
"v_mfma_f32_16x16x32_bf16 a[44:47], a[180:183], a[252:255], a[44:47]\n"
"buffer_load_dwordx4 v217, s[24:27], s50 offen offset:256 lds\n"
"v_and_b32 v185, v237, v124\n"
"v_mfma_f32_16x16x32_bf16 a[48:51], a[184:187], a[224:227], a[48:51]\n"
"s_add_u32 s50, s50, 425984\n"
"ds_read_b64_tr_b16 a[176:177], v218 offset:768\n"
"v_lshlrev_b32 v186, 16, v125\n"
"v_mfma_f32_16x16x32_bf16 a[48:51], a[188:191], a[228:231], a[48:51]\n"
"ds_read_b64_tr_b16 a[178:179], v218 offset:1792\n"
"v_and_b32 v187, v237, v125\n"
"v_lshlrev_b32 v188, 16, v126\n"
"v_mfma_f32_16x16x32_bf16 a[52:55], a[184:187], a[232:235], a[52:55]\n"
"ds_read_b64_tr_b16 a[180:181], v218 offset:4864\n"
"v_and_b32 v189, v237, v126\n"
"v_mfma_f32_16x16x32_bf16 a[52:55], a[188:191], a[236:239], a[52:55]\n"
"ds_read_b64_tr_b16 a[182:183], v218 offset:5888\n"
"v_lshlrev_b32 v190, 16, v127\n"
"v_mfma_f32_16x16x32_bf16 a[56:59], a[184:187], a[240:243], a[56:59]\n"
"ds_read_b128 v[144:147], v224 offset:4096\n"
"v_and_b32 v191, v237, v127\n"
"v_mfma_f32_16x16x32_bf16 a[56:59], a[188:191], a[244:247], a[56:59]\n"
"ds_read_b128 v[148:151], v224 offset:4224\n"
"v_fma_f32 v184, v184, v160, v168\n"
"v_mfma_f32_16x16x32_bf16 a[60:63], a[184:187], a[248:251], a[60:63]\n"
"ds_read_b128 v[152:155], v224 offset:4352\n"
"v_fma_f32 v185, v185, v161, v169\n"
"v_mfma_f32_16x16x32_bf16 a[60:63], a[188:191], a[252:255], a[60:63]\n"
"ds_read_b128 v[156:159], v224 offset:4480\n"
"v_fma_f32 v186, v186, v162, v170\n"
"v_fma_f32 v187, v187, v163, v171\n"
"v_mfma_f32_16x16x32_bf16 a[64:67], a[160:163], a[224:227], a[64:67]\n"
"s_waitcnt lgkmcnt(5)\n"
"ds_read_b64_tr_b16 a[184:185], v218 offset:776\n"
"v_fma_f32 v188, v188, v164, v172\n"
"v_mfma_f32_16x16x32_bf16 a[64:67], a[164:167], a[228:231], a[64:67]\n"
"ds_read_b64_tr_b16 a[186:187], v218 offset:1800\n"
"v_fma_f32 v189, v189, v165, v173\n"
"v_mfma_f32_16x16x32_bf16 a[68:71], a[160:163], a[232:235], a[68:71]\n"
"ds_read_b64_tr_b16 a[188:189], v218 offset:4872\n"
"v_fma_f32 v190, v190, v166, v174\n"
"v_mfma_f32_16x16x32_bf16 a[68:71], a[164:167], a[236:239], a[68:71]\n"
"ds_read_b64_tr_b16 a[190:191], v218 offset:5896\n"
"v_fma_f32 v191, v191, v167, v175\n"
"v_mfma_f32_16x16x32_bf16 a[72:75], a[160:163], a[240:243], a[72:75]\n"
"v_cvt_pk_bf16_f32 v196, v184, v185\n"
"v_cvt_pk_bf16_f32 v197, v186, v187\n"
"v_mfma_f32_16x16x32_bf16 a[72:75], a[164:167], a[244:247], a[72:75]\n"
"v_cvt_pk_bf16_f32 v198, v188, v189\n"
"v_cvt_pk_bf16_f32 v199, v190, v191\n"
"v_mfma_f32_16x16x32_bf16 a[76:79], a[160:163], a[248:251], a[76:79]\n"
"ds_write_b128 v223, v[196:199] offset:19456\n"
"v_lshlrev_b32 v176, 16, v128\n"
"v_mfma_f32_16x16x32_bf16 a[76:79], a[164:167], a[252:255], a[76:79]\n"
"v_and_b32 v177, v237, v128\n"
"v_lshlrev_b32 v178, 16, v129\n"
"v_mfma_f32_16x16x32_bf16 a[80:83], a[168:171], a[224:227], a[80:83]\n"
"v_and_b32 v179, v237, v129\n"
"v_lshlrev_b32 v180, 16, v130\n"
"v_mfma_f32_16x16x32_bf16 a[80:83], a[172:175], a[228:231], a[80:83]\n"
"v_and_b32 v181, v237, v130\n"
"v_lshlrev_b32 v182, 16, v131\n"
"v_mfma_f32_16x16x32_bf16 a[84:87], a[168:171], a[232:235], a[84:87]\n"
"s_waitcnt lgkmcnt(1)\n"
"v_and_b32 v183, v237, v131\n"
"v_fma_f32 v176, v176, v144, v152\n"
"v_mfma_f32_16x16x32_bf16 a[84:87], a[172:175], a[236:239], a[84:87]\n"
"v_fma_f32 v177, v177, v145, v153\n"
"v_fma_f32 v178, v178, v146, v154\n"
"v_fma_f32 v179, v179, v147, v155\n"
"v_mfma_f32_16x16x32_bf16 a[88:91], a[168:171], a[240:243], a[88:91]\n"
"v_fma_f32 v180, v180, v148, v156\n"
"v_fma_f32 v181, v181, v149, v157\n"
"v_mfma_f32_16x16x32_bf16 a[88:91], a[172:175], a[244:247], a[88:91]\n"
"v_fma_f32 v182, v182, v150, v158\n"
"v_fma_f32 v183, v183, v151, v159\n"
"v_mfma_f32_16x16x32_bf16 a[92:95], a[168:171], a[248:251], a[92:95]\n"
"v_cvt_pk_bf16_f32 v192, v176, v177\n"
"v_cvt_pk_bf16_f32 v193, v178, v179\n"
"v_mfma_f32_16x16x32_bf16 a[92:95], a[172:175], a[252:255], a[92:95]\n"
"s_waitcnt vmcnt(8) lgkmcnt(0)\n"
"s_barrier\n"
"v_cvt_pk_bf16_f32 v194, v180, v181\n"
"v_cvt_pk_bf16_f32 v195, v182, v183\n"
"v_mfma_f32_16x16x32_bf16 a[96:99], a[176:179], a[224:227], a[96:99]\n"
"ds_read_b64_tr_b16 a[160:161], v218 offset:32784\n"
"ds_read_b64_tr_b16 a[162:163], v218 offset:33808\n"
"v_mfma_f32_16x16x32_bf16 a[96:99], a[180:183], a[228:231], a[96:99]\n"
"ds_read_b64_tr_b16 a[164:165], v218 offset:36880\n"
"ds_read_b64_tr_b16 a[166:167], v218 offset:37904\n"
"ds_write_b128 v222, v[192:195] offset:0\n"
"v_mfma_f32_16x16x32_bf16 a[100:103], a[176:179], a[232:235], a[100:103]\n"
"ds_read_b128 a[192:195], v220 offset:16384\n"
"ds_read_b128 a[196:199], v221 offset:16384\n"
"v_mfma_f32_16x16x32_bf16 a[100:103], a[180:183], a[236:239], a[100:103]\n"
"ds_read_b128 a[200:203], v220 offset:18432\n"
"ds_read_b128 a[204:207], v221 offset:18432\n"
"v_mfma_f32_16x16x32_bf16 a[104:107], a[176:179], a[240:243], a[104:107]\n"
"ds_read_b128 a[208:211], v220 offset:20480\n"
"ds_read_b128 a[212:215], v221 offset:20480\n"
"v_mfma_f32_16x16x32_bf16 a[104:107], a[180:183], a[244:247], a[104:107]\n"
"ds_read_b128 a[216:219], v220 offset:22528\n"
"ds_read_b128 a[220:223], v221 offset:22528\n"
"v_mfma_f32_16x16x32_bf16 a[108:111], a[176:179], a[248:251], a[108:111]\n"
"ds_read_b64_tr_b16 a[168:169], v218 offset:32792\n"
"ds_read_b64_tr_b16 a[170:171], v218 offset:33816\n"
"v_mfma_f32_16x16x32_bf16 a[108:111], a[180:183], a[252:255], a[108:111]\n"
"ds_read_b64_tr_b16 a[172:173], v218 offset:36888\n"
"ds_read_b64_tr_b16 a[174:175], v218 offset:37912\n"
"v_lshlrev_b32 v184, 16, v132\n"
"v_mfma_f32_16x16x32_bf16 a[112:115], a[184:187], a[224:227], a[112:115]\n"
"ds_read_b64_tr_b16 a[176:177], v218 offset:33040\n"
"v_and_b32 v185, v237, v132\n"
"v_mfma_f32_16x16x32_bf16 a[112:115], a[188:191], a[228:231], a[112:115]\n"
"ds_read_b64_tr_b16 a[178:179], v218 offset:34064\n"
"v_lshlrev_b32 v186, 16, v133\n"
"v_mfma_f32_16x16x32_bf16 a[116:119], a[184:187], a[232:235], a[116:119]\n"
"ds_read_b64_tr_b16 a[180:181], v218 offset:37136\n"
"v_and_b32 v187, v237, v133\n"
"v_mfma_f32_16x16x32_bf16 a[116:119], a[188:191], a[236:239], a[116:119]\n"
"ds_read_b64_tr_b16 a[182:183], v218 offset:38160\n"
"v_lshlrev_b32 v188, 16, v134\n"
"v_mfma_f32_16x16x32_bf16 a[120:123], a[184:187], a[240:243], a[120:123]\n"
"s_mov_b32 m0, s18\n"
"v_and_b32 v189, v237, v134\n"
"v_lshlrev_b32 v190, 16, v135\n"
"v_mfma_f32_16x16x32_bf16 a[120:123], a[188:191], a[244:247], a[120:123]\n"
"buffer_load_dwordx4 v246, s[8:11], s16 offen offset:0 lds\n"
"v_and_b32 v191, v237, v135\n"
"v_fma_f32 v184, v184, v144, v152\n"
"v_mfma_f32_16x16x32_bf16 a[124:127], a[184:187], a[248:251], a[124:127]\n"
"v_fma_f32 v185, v185, v145, v153\n"
"v_fma_f32 v186, v186, v146, v154\n"
"v_mfma_f32_16x16x32_bf16 a[124:127], a[188:191], a[252:255], a[124:127]\n"
"s_waitcnt lgkmcnt(2)\n"
"buffer_load_dwordx4 v246, s[8:11], s16 offen offset:1024 lds\n"
"v_fma_f32 v187, v187, v147, v155\n"
"v_mfma_f32_16x16x32_bf16 a[0:3], a[160:163], a[192:195], a[0:3]\n"
"ds_read_b64_tr_b16 a[184:185], v218 offset:33048\n"
"v_fma_f32 v188, v188, v148, v156\n"
"v_mfma_f32_16x16x32_bf16 a[0:3], a[164:167], a[196:199], a[0:3]\n"
"ds_read_b64_tr_b16 a[186:187], v218 offset:34072\n"
"v_fma_f32 v189, v189, v149, v157\n"
"v_mfma_f32_16x16x32_bf16 a[4:7], a[160:163], a[200:203], a[4:7]\n"
"ds_read_b64_tr_b16 a[188:189], v218 offset:37144\n"
"v_fma_f32 v190, v190, v150, v158\n"
"v_mfma_f32_16x16x32_bf16 a[4:7], a[164:167], a[204:207], a[4:7]\n"
"ds_read_b64_tr_b16 a[190:191], v218 offset:38168\n"
"v_fma_f32 v191, v191, v151, v159\n"
"v_cvt_pk_bf16_f32 v196, v184, v185\n"
"v_mfma_f32_16x16x32_bf16 a[8:11], a[160:163], a[208:211], a[8:11]\n"
"v_cvt_pk_bf16_f32 v197, v186, v187\n"
"v_cvt_pk_bf16_f32 v198, v188, v189\n"
"v_mfma_f32_16x16x32_bf16 a[8:11], a[164:167], a[212:215], a[8:11]\n"
"buffer_load_dwordx4 v247, s[8:11], s16 offen offset:2048 lds\n"
"v_cvt_pk_bf16_f32 v199, v190, v191\n"
"v_mfma_f32_16x16x32_bf16 a[12:15], a[160:163], a[216:219], a[12:15]\n"
"ds_write_b128 v223, v[196:199] offset:1024\n"
"v_lshlrev_b32 v176, 16, v136\n"
"v_mfma_f32_16x16x32_bf16 a[12:15], a[164:167], a[220:223], a[12:15]\n"
"buffer_load_dwordx4 v247, s[8:11], s16 offen offset:3072 lds\n"
"v_and_b32 v177, v237, v136\n"
"v_mfma_f32_16x16x32_bf16 a[16:19], a[168:171], a[192:195], a[16:19]\n"
"ds_read_b64_tr_b16 a[160:161], v218 offset:33296\n"
"v_lshlrev_b32 v178, 16, v137\n"
"v_mfma_f32_16x16x32_bf16 a[16:19], a[172:175], a[196:199], a[16:19]\n"
"ds_read_b64_tr_b16 a[162:163], v218 offset:34320\n"
"v_and_b32 v179, v237, v137\n"
"v_lshlrev_b32 v180, 16, v138\n"
"v_mfma_f32_16x16x32_bf16 a[20:23], a[168:171], a[200:203], a[20:23]\n"
"ds_read_b64_tr_b16 a[164:165], v218 offset:37392\n"
"v_and_b32 v181, v237, v138\n"
"v_mfma_f32_16x16x32_bf16 a[20:23], a[172:175], a[204:207], a[20:23]\n"
"ds_read_b64_tr_b16 a[166:167], v218 offset:38416\n"
"v_lshlrev_b32 v182, 16, v139\n"
"v_mfma_f32_16x16x32_bf16 a[24:27], a[168:171], a[208:211], a[24:27]\n"
"s_add_u32 m0, s18, 16384\n"
"v_and_b32 v183, v237, v139\n"
"v_fma_f32 v176, v176, v144, v152\n"
"v_mfma_f32_16x16x32_bf16 a[24:27], a[172:175], a[212:215], a[24:27]\n"
"buffer_load_dwordx4 v246, s[12:15], s16 offen offset:0 lds\n"
"v_fma_f32 v177, v177, v145, v153\n"
"v_mfma_f32_16x16x32_bf16 a[28:31], a[168:171], a[216:219], a[28:31]\n"
"v_fma_f32 v178, v178, v146, v154\n"
"v_fma_f32 v179, v179, v147, v155\n"
"v_mfma_f32_16x16x32_bf16 a[28:31], a[172:175], a[220:223], a[28:31]\n"
"buffer_load_dwordx4 v247, s[12:15], s16 offen offset:1024 lds\n"
"v_fma_f32 v180, v180, v148, v156\n"
"v_fma_f32 v181, v181, v149, v157\n"
"v_mfma_f32_16x16x32_bf16 a[32:35], a[176:179], a[192:195], a[32:35]\n"
"s_waitcnt lgkmcnt(1)\n"
"ds_read_b64_tr_b16 a[168:169], v218 offset:33304\n"
"v_fma_f32 v182, v182, v150, v158\n"
"v_mfma_f32_16x16x32_bf16 a[32:35], a[180:183], a[196:199], a[32:35]\n"
"ds_read_b64_tr_b16 a[170:171], v218 offset:34328\n"
"v_fma_f32 v183, v183, v151, v159\n"
"v_mfma_f32_16x16x32_bf16 a[36:39], a[176:179], a[200:203], a[36:39]\n"
"ds_read_b64_tr_b16 a[172:173], v218 offset:37400\n"
"v_cvt_pk_bf16_f32 v192, v176, v177\n"
"v_mfma_f32_16x16x32_bf16 a[36:39], a[180:183], a[204:207], a[36:39]\n"
"ds_read_b64_tr_b16 a[174:175], v218 offset:38424\n"
"v_cvt_pk_bf16_f32 v193, v178, v179\n"
"v_mfma_f32_16x16x32_bf16 a[40:43], a[176:179], a[208:211], a[40:43]\n"
"v_cvt_pk_bf16_f32 v194, v180, v181\n"
"v_cvt_pk_bf16_f32 v195, v182, v183\n"
"v_mfma_f32_16x16x32_bf16 a[40:43], a[180:183], a[212:215], a[40:43]\n"
"buffer_load_dwordx4 v246, s[12:15], s16 offen offset:2048 lds\n"
"ds_write_b128 v222, v[192:195] offset:2048\n"
"v_lshlrev_b32 v184, 16, v140\n"
"v_mfma_f32_16x16x32_bf16 a[44:47], a[176:179], a[216:219], a[44:47]\n"
"v_and_b32 v185, v237, v140\n"
"v_lshlrev_b32 v186, 16, v141\n"
"v_mfma_f32_16x16x32_bf16 a[44:47], a[180:183], a[220:223], a[44:47]\n"
"buffer_load_dwordx4 v247, s[12:15], s16 offen offset:3072 lds\n"
"v_and_b32 v187, v237, v141\n"
"v_mfma_f32_16x16x32_bf16 a[48:51], a[184:187], a[192:195], a[48:51]\n"
"s_add_u32 s16, s16, 16384\n"
"ds_read_b64_tr_b16 a[176:177], v218 offset:33552\n"
"v_lshlrev_b32 v188, 16, v142\n"
"v_mfma_f32_16x16x32_bf16 a[48:51], a[188:191], a[196:199], a[48:51]\n"
"ds_read_b64_tr_b16 a[178:179], v218 offset:34576\n"
"v_and_b32 v189, v237, v142\n"
"v_mfma_f32_16x16x32_bf16 a[52:55], a[184:187], a[200:203], a[52:55]\n"
"ds_read_b64_tr_b16 a[180:181], v218 offset:37648\n"
"v_lshlrev_b32 v190, 16, v143\n"
"v_mfma_f32_16x16x32_bf16 a[52:55], a[188:191], a[204:207], a[52:55]\n"
"ds_read_b64_tr_b16 a[182:183], v218 offset:38672\n"
"v_and_b32 v191, v237, v143\n"
"v_fma_f32 v184, v184, v144, v152\n"
"v_mfma_f32_16x16x32_bf16 a[56:59], a[184:187], a[208:211], a[56:59]\n"
"v_fma_f32 v185, v185, v145, v153\n"
"v_fma_f32 v186, v186, v146, v154\n"
"v_mfma_f32_16x16x32_bf16 a[56:59], a[188:191], a[212:215], a[56:59]\n"
"v_fma_f32 v187, v187, v147, v155\n"
"v_fma_f32 v188, v188, v148, v156\n"
"v_mfma_f32_16x16x32_bf16 a[60:63], a[184:187], a[216:219], a[60:63]\n"
"v_fma_f32 v189, v189, v149, v157\n"
"v_fma_f32 v190, v190, v150, v158\n"
"v_mfma_f32_16x16x32_bf16 a[60:63], a[188:191], a[220:223], a[60:63]\n"
"v_fma_f32 v191, v191, v151, v159\n"
"v_cvt_pk_bf16_f32 v196, v184, v185\n"
"v_mfma_f32_16x16x32_bf16 a[64:67], a[160:163], a[192:195], a[64:67]\n"
"s_waitcnt lgkmcnt(1)\n"
"ds_read_b64_tr_b16 a[184:185], v218 offset:33560\n"
"v_cvt_pk_bf16_f32 v197, v186, v187\n"
"v_mfma_f32_16x16x32_bf16 a[64:67], a[164:167], a[196:199], a[64:67]\n"
"ds_read_b64_tr_b16 a[186:187], v218 offset:34584\n"
"v_cvt_pk_bf16_f32 v198, v188, v189\n"
"v_cvt_pk_bf16_f32 v199, v190, v191\n"
"v_mfma_f32_16x16x32_bf16 a[68:71], a[160:163], a[200:203], a[68:71]\n"
"ds_read_b64_tr_b16 a[188:189], v218 offset:37656\n"
"ds_write_b128 v223, v[196:199] offset:3072\n"
"v_mfma_f32_16x16x32_bf16 a[68:71], a[164:167], a[204:207], a[68:71]\n"
"ds_read_b64_tr_b16 a[190:191], v218 offset:38680\n"
"v_mfma_f32_16x16x32_bf16 a[72:75], a[160:163], a[208:211], a[72:75]\n"
"s_nop 0\n"
"v_mfma_f32_16x16x32_bf16 a[72:75], a[164:167], a[212:215], a[72:75]\n"
"s_nop 0\n"
"v_mfma_f32_16x16x32_bf16 a[76:79], a[160:163], a[216:219], a[76:79]\n"
"s_nop 0\n"
"v_mfma_f32_16x16x32_bf16 a[76:79], a[164:167], a[220:223], a[76:79]\n"
"s_nop 0\n"
"v_mfma_f32_16x16x32_bf16 a[80:83], a[168:171], a[192:195], a[80:83]\n"
"s_nop 0\n"
"v_mfma_f32_16x16x32_bf16 a[80:83], a[172:175], a[196:199], a[80:83]\n"
"s_nop 0\n"
"v_mfma_f32_16x16x32_bf16 a[84:87], a[168:171], a[200:203], a[84:87]\n"
"s_nop 0\n"
"v_mfma_f32_16x16x32_bf16 a[84:87], a[172:175], a[204:207], a[84:87]\n"
"s_nop 0\n"
"v_mfma_f32_16x16x32_bf16 a[88:91], a[168:171], a[208:211], a[88:91]\n"
"s_nop 0\n"
"v_mfma_f32_16x16x32_bf16 a[88:91], a[172:175], a[212:215], a[88:91]\n"
"s_nop 0\n"
"v_mfma_f32_16x16x32_bf16 a[92:95], a[168:171], a[216:219], a[92:95]\n"
"s_nop 0\n"
"v_mfma_f32_16x16x32_bf16 a[92:95], a[172:175], a[220:223], a[92:95]\n"
"s_waitcnt vmcnt(8) lgkmcnt(0)\n"
"s_barrier\n"
"v_mfma_f32_16x16x32_bf16 a[96:99], a[176:179], a[192:195], a[96:99]\n"
"ds_read_b64_tr_b16 a[160:161], v219 offset:0\n"
"ds_read_b64_tr_b16 a[162:163], v219 offset:1024\n"
"v_mfma_f32_16x16x32_bf16 a[96:99], a[180:183], a[196:199], a[96:99]\n"
"ds_read_b64_tr_b16 a[164:165], v219 offset:4096\n"
"ds_read_b64_tr_b16 a[166:167], v219 offset:5120\n"
"v_mfma_f32_16x16x32_bf16 a[100:103], a[176:179], a[200:203], a[100:103]\n"
"ds_read_b128 a[224:227], v220 offset:0\n"
"ds_read_b128 a[228:231], v221 offset:0\n"
"v_mfma_f32_16x16x32_bf16 a[100:103], a[180:183], a[204:207], a[100:103]\n"
"ds_read_b128 a[232:235], v220 offset:2048\n"
"ds_read_b128 a[236:239], v221 offset:2048\n"
"v_mfma_f32_16x16x32_bf16 a[104:107], a[176:179], a[208:211], a[104:107]\n"
"ds_read_b128 a[240:243], v220 offset:4096\n"
"ds_read_b128 a[244:247], v221 offset:4096\n"
"v_mfma_f32_16x16x32_bf16 a[104:107], a[180:183], a[212:215], a[104:107]\n"
"ds_read_b128 a[248:251], v220 offset:6144\n"
"ds_read_b128 a[252:255], v221 offset:6144\n"
"v_mfma_f32_16x16x32_bf16 a[108:111], a[176:179], a[216:219], a[108:111]\n"
"ds_read_b64_tr_b16 a[168:169], v219 offset:8\n"
"ds_read_b64_tr_b16 a[170:171], v219 offset:1032\n"
"v_mfma_f32_16x16x32_bf16 a[108:111], a[180:183], a[220:223], a[108:111]\n"
"ds_read_b64_tr_b16 a[172:173], v219 offset:4104\n"
"ds_read_b64_tr_b16 a[174:175], v219 offset:5128\n"
"v_mfma_f32_16x16x32_bf16 a[112:115], a[184:187], a[192:195], a[112:115]\n"
"ds_read_b64_tr_b16 a[176:177], v219 offset:256\n"
"v_mfma_f32_16x16x32_bf16 a[112:115], a[188:191], a[196:199], a[112:115]\n"
"ds_read_b64_tr_b16 a[178:179], v219 offset:1280\n"
"v_mfma_f32_16x16x32_bf16 a[116:119], a[184:187], a[200:203], a[116:119]\n"
"ds_read_b64_tr_b16 a[180:181], v219 offset:4352\n"
"v_mfma_f32_16x16x32_bf16 a[116:119], a[188:191], a[204:207], a[116:119]\n"
"ds_read_b64_tr_b16 a[182:183], v219 offset:5376\n"
"v_mfma_f32_16x16x32_bf16 a[120:123], a[184:187], a[208:211], a[120:123]\n"
"s_add_u32 m0, s18, 32768\n"
"v_mfma_f32_16x16x32_bf16 a[120:123], a[188:191], a[212:215], a[120:123]\n"
"buffer_load_dwordx4 v246, s[8:11], s16 offen offset:0 lds\n"
"v_mfma_f32_16x16x32_bf16 a[124:127], a[184:187], a[216:219], a[124:127]\n"
"s_nop 0\n"
"v_mfma_f32_16x16x32_bf16 a[124:127], a[188:191], a[220:223], a[124:127]\n"
"s_waitcnt lgkmcnt(2)\n"
"buffer_load_dwordx4 v246, s[8:11], s16 offen offset:1024 lds\n"
"v_mfma_f32_16x16x32_bf16 a[0:3], a[160:163], a[224:227], a[0:3]\n"
"ds_read_b64_tr_b16 a[184:185], v219 offset:264\n"
"v_mfma_f32_16x16x32_bf16 a[0:3], a[164:167], a[228:231], a[0:3]\n"
"ds_read_b64_tr_b16 a[186:187], v219 offset:1288\n"
"v_mfma_f32_16x16x32_bf16 a[4:7], a[160:163], a[232:235], a[4:7]\n"
"ds_read_b64_tr_b16 a[188:189], v219 offset:4360\n"
"v_mfma_f32_16x16x32_bf16 a[4:7], a[164:167], a[236:239], a[4:7]\n"
"ds_read_b64_tr_b16 a[190:191], v219 offset:5384\n"
"v_mfma_f32_16x16x32_bf16 a[8:11], a[160:163], a[240:243], a[8:11]\n"
"s_nop 0\n"
"v_mfma_f32_16x16x32_bf16 a[8:11], a[164:167], a[244:247], a[8:11]\n"
"buffer_load_dwordx4 v247, s[8:11], s16 offen offset:2048 lds\n"
"v_mfma_f32_16x16x32_bf16 a[12:15], a[160:163], a[248:251], a[12:15]\n"
"v_accvgpr_read_b32 v176, a0\n"
"v_accvgpr_read_b32 v177, a1\n"
"v_mfma_f32_16x16x32_bf16 a[12:15], a[164:167], a[252:255], a[12:15]\n"
"buffer_load_dwordx4 v247, s[8:11], s16 offen offset:3072 lds\n"
"v_accvgpr_read_b32 v178, a2\n"
"v_mfma_f32_16x16x32_bf16 a[16:19], a[168:171], a[224:227], a[16:19]\n"
"ds_read_b64_tr_b16 a[160:161], v219 offset:512\n"
"v_accvgpr_read_b32 v179, a3\n"
"v_mfma_f32_16x16x32_bf16 a[16:19], a[172:175], a[228:231], a[16:19]\n"
"ds_read_b64_tr_b16 a[162:163], v219 offset:1536\n"
"v_cvt_pk_bf16_f32 v64, v176, v177\n"
"v_mfma_f32_16x16x32_bf16 a[20:23], a[168:171], a[232:235], a[20:23]\n"
"ds_read_b64_tr_b16 a[164:165], v219 offset:4608\n"
"v_cvt_pk_bf16_f32 v65, v178, v179\n"
"v_mfma_f32_16x16x32_bf16 a[20:23], a[172:175], a[236:239], a[20:23]\n"
"ds_read_b64_tr_b16 a[166:167], v219 offset:5632\n"
"v_accvgpr_read_b32 v180, a4\n"
"v_mfma_f32_16x16x32_bf16 a[24:27], a[168:171], a[240:243], a[24:27]\n"
"s_add_u32 m0, s18, 49152\n"
"v_accvgpr_read_b32 v181, a5\n"
"v_accvgpr_read_b32 v182, a6\n"
"v_mfma_f32_16x16x32_bf16 a[24:27], a[172:175], a[244:247], a[24:27]\n"
"buffer_load_dwordx4 v246, s[12:15], s16 offen offset:0 lds\n"
"v_accvgpr_read_b32 v183, a7\n"
"v_mfma_f32_16x16x32_bf16 a[28:31], a[168:171], a[248:251], a[28:31]\n"
"v_cvt_pk_bf16_f32 v80, v180, v181\n"
"v_cvt_pk_bf16_f32 v81, v182, v183\n"
"v_mfma_f32_16x16x32_bf16 a[28:31], a[172:175], a[252:255], a[28:31]\n"
"buffer_load_dwordx4 v247, s[12:15], s16 offen offset:1024 lds\n"
"v_accvgpr_read_b32 v184, a8\n"
"v_mfma_f32_16x16x32_bf16 a[32:35], a[176:179], a[224:227], a[32:35]\n"
"s_waitcnt lgkmcnt(1)\n"
"ds_read_b64_tr_b16 a[168:169], v219 offset:520\n"
"v_accvgpr_read_b32 v185, a9\n"
"v_mfma_f32_16x16x32_bf16 a[32:35], a[180:183], a[228:231], a[32:35]\n"
"ds_read_b64_tr_b16 a[170:171], v219 offset:1544\n"
"v_accvgpr_read_b32 v186, a10\n"
"v_mfma_f32_16x16x32_bf16 a[36:39], a[176:179], a[232:235], a[36:39]\n"
"ds_read_b64_tr_b16 a[172:173], v219 offset:4616\n"
"v_accvgpr_read_b32 v187, a11\n"
"v_mfma_f32_16x16x32_bf16 a[36:39], a[180:183], a[236:239], a[36:39]\n"
"ds_read_b64_tr_b16 a[174:175], v219 offset:5640\n"
"v_cvt_pk_bf16_f32 v96, v184, v185\n"
"v_mfma_f32_16x16x32_bf16 a[40:43], a[176:179], a[240:243], a[40:43]\n"
"v_cvt_pk_bf16_f32 v97, v186, v187\n"
"v_accvgpr_read_b32 v188, a12\n"
"v_mfma_f32_16x16x32_bf16 a[40:43], a[180:183], a[244:247], a[40:43]\n"
"buffer_load_dwordx4 v246, s[12:15], s16 offen offset:2048 lds\n"
"v_accvgpr_read_b32 v189, a13\n"
"v_mfma_f32_16x16x32_bf16 a[44:47], a[176:179], a[248:251], a[44:47]\n"
"v_accvgpr_read_b32 v190, a14\n"
"v_accvgpr_read_b32 v191, a15\n"
"v_mfma_f32_16x16x32_bf16 a[44:47], a[180:183], a[252:255], a[44:47]\n"
"buffer_load_dwordx4 v247, s[12:15], s16 offen offset:3072 lds\n"
"v_cvt_pk_bf16_f32 v112, v188, v189\n"
"v_mfma_f32_16x16x32_bf16 a[48:51], a[184:187], a[224:227], a[48:51]\n"
"s_add_u32 s16, s16, 16384\n"
"ds_read_b64_tr_b16 a[176:177], v219 offset:768\n"
"v_cvt_pk_bf16_f32 v113, v190, v191\n"
"v_mfma_f32_16x16x32_bf16 a[48:51], a[188:191], a[228:231], a[48:51]\n"
"ds_read_b64_tr_b16 a[178:179], v219 offset:1792\n"
"v_accvgpr_read_b32 v176, a16\n"
"v_mfma_f32_16x16x32_bf16 a[52:55], a[184:187], a[232:235], a[52:55]\n"
"ds_read_b64_tr_b16 a[180:181], v219 offset:4864\n"
"v_accvgpr_read_b32 v177, a17\n"
"v_mfma_f32_16x16x32_bf16 a[52:55], a[188:191], a[236:239], a[52:55]\n"
"ds_read_b64_tr_b16 a[182:183], v219 offset:5888\n"
"v_accvgpr_read_b32 v178, a18\n"
"v_mfma_f32_16x16x32_bf16 a[56:59], a[184:187], a[240:243], a[56:59]\n"
"v_accvgpr_read_b32 v179, a19\n"
"v_cvt_pk_bf16_f32 v66, v176, v177\n"
"v_mfma_f32_16x16x32_bf16 a[56:59], a[188:191], a[244:247], a[56:59]\n"
"v_cvt_pk_bf16_f32 v67, v178, v179\n"
"v_accvgpr_read_b32 v180, a20\n"
"v_mfma_f32_16x16x32_bf16 a[60:63], a[184:187], a[248:251], a[60:63]\n"
"v_accvgpr_read_b32 v181, a21\n"
"v_accvgpr_read_b32 v182, a22\n"
"v_mfma_f32_16x16x32_bf16 a[60:63], a[188:191], a[252:255], a[60:63]\n"
"v_accvgpr_read_b32 v183, a23\n"
"v_cvt_pk_bf16_f32 v82, v180, v181\n"
"v_mfma_f32_16x16x32_bf16 a[64:67], a[160:163], a[224:227], a[64:67]\n"
"s_waitcnt lgkmcnt(1)\n"
"ds_read_b64_tr_b16 a[184:185], v219 offset:776\n"
"v_cvt_pk_bf16_f32 v83, v182, v183\n"
"v_mfma_f32_16x16x32_bf16 a[64:67], a[164:167], a[228:231], a[64:67]\n"
"ds_read_b64_tr_b16 a[186:187], v219 offset:1800\n"
"v_accvgpr_read_b32 v184, a24\n"
"v_mfma_f32_16x16x32_bf16 a[68:71], a[160:163], a[232:235], a[68:71]\n"
"ds_read_b64_tr_b16 a[188:189], v219 offset:4872\n"
"v_accvgpr_read_b32 v185, a25\n"
"v_mfma_f32_16x16x32_bf16 a[68:71], a[164:167], a[236:239], a[68:71]\n"
"ds_read_b64_tr_b16 a[190:191], v219 offset:5896\n"
"v_accvgpr_read_b32 v186, a26\n"
"v_mfma_f32_16x16x32_bf16 a[72:75], a[160:163], a[240:243], a[72:75]\n"
"v_accvgpr_read_b32 v187, a27\n"
"v_cvt_pk_bf16_f32 v98, v184, v185\n"
"v_mfma_f32_16x16x32_bf16 a[72:75], a[164:167], a[244:247], a[72:75]\n"
"v_cvt_pk_bf16_f32 v99, v186, v187\n"
"v_accvgpr_read_b32 v188, a28\n"
"v_mfma_f32_16x16x32_bf16 a[76:79], a[160:163], a[248:251], a[76:79]\n"
"v_accvgpr_read_b32 v189, a29\n"
"v_accvgpr_read_b32 v190, a30\n"
"v_mfma_f32_16x16x32_bf16 a[76:79], a[164:167], a[252:255], a[76:79]\n"
"v_accvgpr_read_b32 v191, a31\n"
"v_cvt_pk_bf16_f32 v114, v188, v189\n"
"v_mfma_f32_16x16x32_bf16 a[80:83], a[168:171], a[224:227], a[80:83]\n"
"v_cvt_pk_bf16_f32 v115, v190, v191\n"
"v_accvgpr_read_b32 v176, a32\n"
"v_mfma_f32_16x16x32_bf16 a[80:83], a[172:175], a[228:231], a[80:83]\n"
"v_accvgpr_read_b32 v177, a33\n"
"v_accvgpr_read_b32 v178, a34\n"
"v_mfma_f32_16x16x32_bf16 a[84:87], a[168:171], a[232:235], a[84:87]\n"
"v_accvgpr_read_b32 v179, a35\n"
"v_cvt_pk_bf16_f32 v68, v176, v177\n"
"v_mfma_f32_16x16x32_bf16 a[84:87], a[172:175], a[236:239], a[84:87]\n"
"v_cvt_pk_bf16_f32 v69, v178, v179\n"
"v_accvgpr_read_b32 v180, a36\n"
"v_mfma_f32_16x16x32_bf16 a[88:91], a[168:171], a[240:243], a[88:91]\n"
"v_accvgpr_read_b32 v181, a37\n"
"v_accvgpr_read_b32 v182, a38\n"
"v_mfma_f32_16x16x32_bf16 a[88:91], a[172:175], a[244:247], a[88:91]\n"
"v_accvgpr_read_b32 v183, a39\n"
"v_cvt_pk_bf16_f32 v84, v180, v181\n"
"v_mfma_f32_16x16x32_bf16 a[92:95], a[168:171], a[248:251], a[92:95]\n"
"v_cvt_pk_bf16_f32 v85, v182, v183\n"
"v_accvgpr_read_b32 v184, a40\n"
"v_mfma_f32_16x16x32_bf16 a[92:95], a[172:175], a[252:255], a[92:95]\n"
"s_waitcnt vmcnt(8) lgkmcnt(0)\n"
"s_barrier\n"
"v_accvgpr_read_b32 v185, a41\n"
"v_accvgpr_read_b32 v186, a42\n"
"v_mfma_f32_16x16x32_bf16 a[96:99], a[176:179], a[224:227], a[96:99]\n"
"ds_read_b128 a[128:131], v240 offset:0\n"
"ds_read_b128 a[132:135], v240 offset:256\n"
"v_mfma_f32_16x16x32_bf16 a[96:99], a[180:183], a[228:231], a[96:99]\n"
"ds_read_b128 a[136:139], v240 offset:512\n"
"ds_read_b128 a[140:143], v240 offset:768\n"
"v_mfma_f32_16x16x32_bf16 a[100:103], a[176:179], a[232:235], a[100:103]\n"
"ds_read_b128 a[144:147], v240 offset:4096\n"
"ds_read_b128 a[148:151], v240 offset:4352\n"
"v_mfma_f32_16x16x32_bf16 a[100:103], a[180:183], a[236:239], a[100:103]\n"
"ds_read_b128 a[152:155], v240 offset:4608\n"
"ds_read_b128 a[156:159], v240 offset:4864\n"
"v_mfma_f32_16x16x32_bf16 a[104:107], a[176:179], a[240:243], a[104:107]\n"
"ds_read_b64_tr_b16 a[192:193], v242 offset:16384\n"
"ds_read_b64_tr_b16 a[194:195], v242 offset:20480\n"
"v_mfma_f32_16x16x32_bf16 a[104:107], a[180:183], a[244:247], a[104:107]\n"
"ds_read_b64_tr_b16 a[196:197], v243 offset:16384\n"
"ds_read_b64_tr_b16 a[198:199], v243 offset:20480\n"
"v_mfma_f32_16x16x32_bf16 a[108:111], a[176:179], a[248:251], a[108:111]\n"
"ds_read_b64_tr_b16 a[200:201], v242 offset:16640\n"
"ds_read_b64_tr_b16 a[202:203], v242 offset:20736\n"
"v_mfma_f32_16x16x32_bf16 a[108:111], a[180:183], a[252:255], a[108:111]\n"
"ds_read_b64_tr_b16 a[204:205], v243 offset:16640\n"
"ds_read_b64_tr_b16 a[206:207], v243 offset:20736\n"
"v_mfma_f32_16x16x32_bf16 a[112:115], a[184:187], a[224:227], a[112:115]\n"
"ds_read_b64_tr_b16 a[208:209], v242 offset:16896\n"
"ds_read_b64_tr_b16 a[210:211], v242 offset:20992\n"
"v_mfma_f32_16x16x32_bf16 a[112:115], a[188:191], a[228:231], a[112:115]\n"
"ds_read_b64_tr_b16 a[212:213], v243 offset:16896\n"
"ds_read_b64_tr_b16 a[214:215], v243 offset:20992\n"
"v_mfma_f32_16x16x32_bf16 a[116:119], a[184:187], a[232:235], a[116:119]\n"
"ds_read_b64_tr_b16 a[216:217], v242 offset:17152\n"
"ds_read_b64_tr_b16 a[218:219], v242 offset:21248\n"
"v_mfma_f32_16x16x32_bf16 a[116:119], a[188:191], a[236:239], a[116:119]\n"
"ds_read_b64_tr_b16 a[220:221], v243 offset:17152\n"
"ds_read_b64_tr_b16 a[222:223], v243 offset:21248\n"
"v_mfma_f32_16x16x32_bf16 a[120:123], a[184:187], a[240:243], a[120:123]\n"
"s_add_u32 m0, s18, 65536\n"
"v_accvgpr_read_b32 v187, a43\n"
"v_cvt_pk_bf16_f32 v100, v184, v185\n"
"v_mfma_f32_16x16x32_bf16 a[120:123], a[188:191], a[244:247], a[120:123]\n"
"buffer_load_dwordx4 v246, s[8:11], s16 offen offset:0 lds\n"
"v_cvt_pk_bf16_f32 v101, v186, v187\n"
"v_mfma_f32_16x16x32_bf16 a[124:127], a[184:187], a[248:251], a[124:127]\n"
"v_accvgpr_read_b32 v188, a44\n"
"v_accvgpr_read_b32 v189, a45\n"
"v_mfma_f32_16x16x32_bf16 a[124:127], a[188:191], a[252:255], a[124:127]\n"
"s_waitcnt lgkmcnt(4)\n"
"buffer_load_dwordx4 v246, s[8:11], s16 offen offset:1024 lds\n"
"v_accvgpr_read_b32 v190, a46\n"
"v_accvgpr_read_b32 v191, a47\n"
"v_cvt_pk_bf16_f32 v116, v188, v189\n"
"v_cvt_pk_bf16_f32 v117, v190, v191\n"
"v_accvgpr_read_b32 v176, a48\n"
"buffer_load_dwordx4 v247, s[8:11], s16 offen offset:2048 lds\n"
"v_accvgpr_read_b32 v177, a49\n"
"v_accvgpr_read_b32 v178, a50\n"
"v_accvgpr_read_b32 v179, a51\n"
"v_cvt_pk_bf16_f32 v70, v176, v177\n"
"v_cvt_pk_bf16_f32 v71, v178, v179\n"
"v_accvgpr_read_b32 v180, a52\n"
"v_accvgpr_read_b32 v181, a53\n"
"buffer_load_dwordx4 v247, s[8:11], s16 offen offset:3072 lds\n"
"v_accvgpr_read_b32 v182, a54\n"
"v_accvgpr_read_b32 v183, a55\n"
"v_cvt_pk_bf16_f32 v86, v180, v181\n"
"s_add_u32 m0, s18, 81920\n"
"v_cvt_pk_bf16_f32 v87, v182, v183\n"
"v_accvgpr_read_b32 v184, a56\n"
"v_accvgpr_read_b32 v185, a57\n"
"v_accvgpr_read_b32 v186, a58\n"
"buffer_load_dwordx4 v246, s[12:15], s16 offen offset:0 lds\n"
"v_accvgpr_read_b32 v187, a59\n"
"v_cvt_pk_bf16_f32 v102, v184, v185\n"
"v_cvt_pk_bf16_f32 v103, v186, v187\n"
"v_accvgpr_read_b32 v188, a60\n"
"v_accvgpr_read_b32 v189, a61\n"
"v_accvgpr_read_b32 v190, a62\n"
"v_accvgpr_read_b32 v191, a63\n"
"buffer_load_dwordx4 v247, s[12:15], s16 offen offset:1024 lds\n"
"v_cvt_pk_bf16_f32 v118, v188, v189\n"
"v_cvt_pk_bf16_f32 v119, v190, v191\n"
"v_accvgpr_read_b32 v176, a64\n"
"v_accvgpr_read_b32 v177, a65\n"
"v_accvgpr_read_b32 v178, a66\n"
"v_accvgpr_read_b32 v179, a67\n"
"v_cvt_pk_bf16_f32 v72, v176, v177\n"
"buffer_load_dwordx4 v246, s[12:15], s16 offen offset:2048 lds\n"
"v_cvt_pk_bf16_f32 v73, v178, v179\n"
"v_accvgpr_read_b32 v180, a68\n"
"v_accvgpr_read_b32 v181, a69\n"
"v_accvgpr_read_b32 v182, a70\n"
"v_accvgpr_read_b32 v183, a71\n"
"v_cvt_pk_bf16_f32 v88, v180, v181\n"
"v_cvt_pk_bf16_f32 v89, v182, v183\n"
"buffer_load_dwordx4 v247, s[12:15], s16 offen offset:3072 lds\n"
"v_accvgpr_read_b32 v184, a72\n"
"v_accvgpr_read_b32 v185, a73\n"
"v_accvgpr_read_b32 v186, a74\n"
"s_add_u32 s16, s16, 16384\n"
"s_add_u32 m0, s18, 98304\n"
"v_accvgpr_read_b32 v187, a75\n"
"v_cvt_pk_bf16_f32 v104, v184, v185\n"
"v_cvt_pk_bf16_f32 v105, v186, v187\n"
"v_accvgpr_read_b32 v188, a76\n"
"buffer_load_dwordx4 v246, s[8:11], s16 offen offset:0 lds\n"
"v_accvgpr_read_b32 v189, a77\n"
"v_accvgpr_read_b32 v190, a78\n"
"v_accvgpr_read_b32 v191, a79\n"
"v_cvt_pk_bf16_f32 v120, v188, v189\n"
"v_cvt_pk_bf16_f32 v121, v190, v191\n"
"v_accvgpr_read_b32 v176, a80\n"
"v_accvgpr_read_b32 v177, a81\n"
"buffer_load_dwordx4 v246, s[8:11], s16 offen offset:1024 lds\n"
"v_accvgpr_read_b32 v178, a82\n"
"v_accvgpr_read_b32 v179, a83\n"
"v_cvt_pk_bf16_f32 v74, v176, v177\n"
"v_cvt_pk_bf16_f32 v75, v178, v179\n"
"v_accvgpr_read_b32 v180, a84\n"
"v_accvgpr_read_b32 v181, a85\n"
"v_accvgpr_read_b32 v182, a86\n"
"buffer_load_dwordx4 v247, s[8:11], s16 offen offset:2048 lds\n"
"v_accvgpr_read_b32 v183, a87\n"
"v_cvt_pk_bf16_f32 v90, v180, v181\n"
"v_cvt_pk_bf16_f32 v91, v182, v183\n"
"v_accvgpr_read_b32 v184, a88\n"
"v_accvgpr_read_b32 v185, a89\n"
"v_accvgpr_read_b32 v186, a90\n"
"v_accvgpr_read_b32 v187, a91\n"
"buffer_load_dwordx4 v247, s[8:11], s16 offen offset:3072 lds\n"
"v_cvt_pk_bf16_f32 v106, v184, v185\n"
"v_cvt_pk_bf16_f32 v107, v186, v187\n"
"v_accvgpr_read_b32 v188, a92\n"
"s_add_u32 m0, s18, 114688\n"
"v_accvgpr_read_b32 v189, a93\n"
"v_accvgpr_read_b32 v190, a94\n"
"v_accvgpr_read_b32 v191, a95\n"
"v_cvt_pk_bf16_f32 v122, v188, v189\n"
"buffer_load_dwordx4 v246, s[12:15], s16 offen offset:0 lds\n"
"v_cvt_pk_bf16_f32 v123, v190, v191\n"
"v_accvgpr_read_b32 v176, a96\n"
"v_accvgpr_read_b32 v177, a97\n"
"v_accvgpr_read_b32 v178, a98\n"
"v_accvgpr_read_b32 v179, a99\n"
"v_cvt_pk_bf16_f32 v76, v176, v177\n"
"v_cvt_pk_bf16_f32 v77, v178, v179\n"
"buffer_load_dwordx4 v247, s[12:15], s16 offen offset:1024 lds\n"
"v_accvgpr_read_b32 v180, a100\n"
"v_accvgpr_read_b32 v181, a101\n"
"v_accvgpr_read_b32 v182, a102\n"
"v_accvgpr_read_b32 v183, a103\n"
"v_cvt_pk_bf16_f32 v92, v180, v181\n"
"v_cvt_pk_bf16_f32 v93, v182, v183\n"
"v_accvgpr_read_b32 v184, a104\n"
"buffer_load_dwordx4 v246, s[12:15], s16 offen offset:2048 lds\n"
"v_accvgpr_read_b32 v185, a105\n"
"v_accvgpr_read_b32 v186, a106\n"
"v_accvgpr_read_b32 v187, a107\n"
"v_cvt_pk_bf16_f32 v108, v184, v185\n"
"v_cvt_pk_bf16_f32 v109, v186, v187\n"
"v_accvgpr_read_b32 v188, a108\n"
"v_accvgpr_read_b32 v189, a109\n"
"buffer_load_dwordx4 v247, s[12:15], s16 offen offset:3072 lds\n"
"v_accvgpr_read_b32 v190, a110\n"
"v_accvgpr_read_b32 v191, a111\n"
"v_cvt_pk_bf16_f32 v124, v188, v189\n"
"s_add_u32 s16, s16, 16384\n"
"v_cvt_pk_bf16_f32 v125, v190, v191\n"
"v_accvgpr_read_b32 v176, a112\n"
"v_accvgpr_read_b32 v177, a113\n"
"v_accvgpr_read_b32 v178, a114\n"
"v_accvgpr_read_b32 v179, a115\n"
"v_cvt_pk_bf16_f32 v78, v176, v177\n"
"v_cvt_pk_bf16_f32 v79, v178, v179\n"
"v_accvgpr_read_b32 v180, a116\n"
"v_accvgpr_read_b32 v181, a117\n"
"v_accvgpr_read_b32 v182, a118\n"
"v_accvgpr_read_b32 v183, a119\n"
"v_cvt_pk_bf16_f32 v94, v180, v181\n"
"v_cvt_pk_bf16_f32 v95, v182, v183\n"
"v_accvgpr_read_b32 v184, a120\n"
"v_accvgpr_read_b32 v185, a121\n"
"v_accvgpr_read_b32 v186, a122\n"
"v_accvgpr_read_b32 v187, a123\n"
"v_cvt_pk_bf16_f32 v110, v184, v185\n"
"v_cvt_pk_bf16_f32 v111, v186, v187\n"
"v_accvgpr_read_b32 v188, a124\n"
"v_accvgpr_read_b32 v189, a125\n"
"v_accvgpr_read_b32 v190, a126\n"
"v_accvgpr_read_b32 v191, a127\n"
"v_cvt_pk_bf16_f32 v126, v188, v189\n"
"v_cvt_pk_bf16_f32 v127, v190, v191\n"
"s_and_b32 s56, s41, 1\n"
"s_lshl_b32 s57, s56, 8\n"
"s_add_u32 s57, s57, s40\n"
"s_lshl_b32 s58, s56, 14\n"
"s_add_u32 s59, s57, s58\n"
"s_xor_b32 s58, s58, 16384\n"
"s_add_u32 s60, s57, s58\n"
"s_lshl_b32 s61, s41, 14\n"
"s_add_u32 s62, s40, 98304\n"
"s_add_u32 s63, s62, s61\n"
"s_xor_b32 s61, s61, 16384\n"
"s_add_u32 s61, s62, s61\n"
"s_nop 1\n"
"v_mfma_f32_16x16x32_bf16 v[128:131], a[128:131], v[64:67], 0\n"
"v_lshlrev_b32 v233, 9, v226\n"
"v_lshl_add_u32 v233, v227, 6, v233\n"
"v_mfma_f32_16x16x32_bf16 v[128:131], a[132:135], v[68:71], v[128:131]\n"
"v_lshl_add_u32 v233, v228, 3, v233\n"
"v_and_b32 v232, 1, v226\n"
"v_mfma_f32_16x16x32_bf16 v[128:131], a[136:139], v[72:75], v[128:131]\n"
"v_lshl_add_u32 v233, v232, 5, v233\n"
"v_add_u32 v192, s59, v233\n"
"v_mfma_f32_16x16x32_bf16 v[128:131], a[140:143], v[76:79], v[128:131]\n"
"v_xor_b32 v195, 32, v192\n"
"v_add_u32 v193, 65536, v192\n"
"v_mfma_f32_16x16x32_bf16 v[132:135], a[144:147], v[64:67], 0\n"
"v_add_u32 v196, 65536, v195\n"
"v_add_u32 v194, 131072, v192\n"
"v_mfma_f32_16x16x32_bf16 v[132:135], a[148:151], v[68:71], v[132:135]\n"
"v_add_u32 v197, 131072, v195\n"
"v_add_u32 v198, s60, v233\n"
"v_mfma_f32_16x16x32_bf16 v[132:135], a[152:155], v[72:75], v[132:135]\n"
"v_xor_b32 v201, 32, v198\n"
"v_add_u32 v199, 65536, v198\n"
"v_mfma_f32_16x16x32_bf16 v[132:135], a[156:159], v[76:79], v[132:135]\n"
"v_add_u32 v202, 65536, v201\n"
"v_add_u32 v200, 131072, v198\n"
"v_mfma_f32_16x16x32_bf16 v[136:139], a[128:131], v[80:83], 0\n"
"v_add_u32 v203, 131072, v201\n"
"v_lshlrev_b32 v204, 6, v232\n"
"v_mfma_f32_16x16x32_bf16 v[136:139], a[132:135], v[84:87], v[136:139]\n"
"v_and_b32 v232, 2, v226\n"
"v_xor_b32 v233, v232, v228\n"
"v_mfma_f32_16x16x32_bf16 v[136:139], a[136:139], v[88:91], v[136:139]\n"
"v_lshl_add_u32 v204, v233, 4, v204\n"
"v_lshl_add_u32 v232, v232, 1, v227\n"
"v_mfma_f32_16x16x32_bf16 v[136:139], a[140:143], v[92:95], v[136:139]\n"
"v_mul_u32_u24 v232, 1152, v232\n"
"v_add_u32 v204, v232, v204\n"
"v_mfma_f32_16x16x32_bf16 v[140:143], a[144:147], v[80:83], 0\n"
"v_lshlrev_b32 v205, 4, v248\n"
"v_add_u32 v206, 18432, v205\n"
"v_mfma_f32_16x16x32_bf16 v[140:143], a[148:151], v[84:87], v[140:143]\n"
"v_add_u32 v207, 36864, v205\n"
"v_add_u32 v208, 55296, v205\n"
"v_mfma_f32_16x16x32_bf16 v[140:143], a[152:155], v[88:91], v[140:143]\n"
"v_add_u32 v209, s63, v205\n"
"v_add_u32 v210, s61, v205\n"
"v_mfma_f32_16x16x32_bf16 v[140:143], a[156:159], v[92:95], v[140:143]\n"
"v_lshlrev_b32 v232, 14, v228\n"
"v_add_u32 v211, s62, v232\n"
"v_mfma_f32_16x16x32_bf16 v[144:147], a[128:131], v[96:99], 0\n"
"v_mov_b32 v40, 0x3f803f80\n"
"v_mov_b32 v41, 0x3f803f80\n"
"v_mfma_f32_16x16x32_bf16 v[144:147], a[132:135], v[100:103], v[144:147]\n"
"v_mov_b32 v42, 0x3f803f80\n"
"v_mov_b32 v43, 0x3f803f80\n"
"v_mfma_f32_16x16x32_bf16 v[144:147], a[136:139], v[104:107], v[144:147]\n"
"s_nop 0\n"
"v_mfma_f32_16x16x32_bf16 v[144:147], a[140:143], v[108:111], v[144:147]\n"
"s_nop 0\n"
"v_mfma_f32_16x16x32_bf16 v[148:151], a[144:147], v[96:99], 0\n"
"s_nop 0\n"
"v_mfma_f32_16x16x32_bf16 v[148:151], a[148:151], v[100:103], v[148:151]\n"
"s_nop 0\n"
"v_mfma_f32_16x16x32_bf16 v[148:151], a[152:155], v[104:107], v[148:151]\n"
"s_nop 0\n"
"v_mfma_f32_16x16x32_bf16 v[148:151], a[156:159], v[108:111], v[148:151]\n"
"s_nop 0\n"
"v_mfma_f32_16x16x32_bf16 v[152:155], a[128:131], v[112:115], 0\n"
"s_nop 0\n"
"v_mfma_f32_16x16x32_bf16 v[152:155], a[132:135], v[116:119], v[152:155]\n"
"s_nop 0\n"
"v_mfma_f32_16x16x32_bf16 v[152:155], a[136:139], v[120:123], v[152:155]\n"
"s_nop 0\n"
"v_mfma_f32_16x16x32_bf16 v[152:155], a[140:143], v[124:127], v[152:155]\n"
"s_nop 0\n"
"v_mfma_f32_16x16x32_bf16 v[156:159], a[144:147], v[112:115], 0\n"
"s_nop 0\n"
"v_mfma_f32_16x16x32_bf16 v[156:159], a[148:151], v[116:119], v[156:159]\n"
"s_nop 0\n"
"v_mfma_f32_16x16x32_bf16 v[156:159], a[152:155], v[120:123], v[156:159]\n"
"s_nop 0\n"
"v_mfma_f32_16x16x32_bf16 v[156:159], a[156:159], v[124:127], v[156:159]\n"
"s_nop 0\n"
"v_max3_f32 v160, v128, v129, v130\n"
"v_max3_f32 v160, v160, v131, v132\n"
"v_max3_f32 v160, v160, v133, v134\n"
"v_max_f32 v160, v160, v135\n"
"v_max3_f32 v161, v136, v137, v138\n"
"v_max3_f32 v161, v161, v139, v140\n"
"v_max3_f32 v161, v161, v141, v142\n"
"v_max_f32 v161, v161, v143\n"
"v_max3_f32 v162, v144, v145, v146\n"
"v_max3_f32 v162, v162, v147, v148\n"
"v_max3_f32 v162, v162, v149, v150\n"
"v_max_f32 v162, v162, v151\n"
"v_max3_f32 v163, v152, v153, v154\n"
"v_max3_f32 v163, v163, v155, v156\n"
"v_max3_f32 v163, v163, v157, v158\n"
"v_max_f32 v163, v163, v159\n"
"v_mov_b32 v164, v160\n"
"v_mov_b32 v165, v161\n"
"v_mov_b32 v166, v162\n"
"v_mov_b32 v167, v163\n"
"s_nop 0\n"
"v_permlane32_swap_b32 v160, v164\n"
"v_permlane32_swap_b32 v161, v165\n"
"v_permlane32_swap_b32 v162, v166\n"
"v_permlane32_swap_b32 v163, v167\n"
"v_max_f32 v160, v160, v164\n"
"v_max_f32 v161, v161, v165\n"
"v_max_f32 v162, v162, v166\n"
"v_max_f32 v163, v163, v167\n"
"v_mov_b32 v164, v160\n"
"v_mov_b32 v165, v161\n"
"v_mov_b32 v166, v162\n"
"v_mov_b32 v167, v163\n"
"s_nop 0\n"
"v_permlane16_swap_b32 v160, v164\n"
"v_permlane16_swap_b32 v161, v165\n"
"v_permlane16_swap_b32 v162, v166\n"
"v_permlane16_swap_b32 v163, v167\n"
"v_max_f32 v160, v160, v164\n"
"v_max_f32 v161, v161, v165\n"
"v_max_f32 v162, v162, v166\n"
"v_max_f32 v163, v163, v167\n"
"v_sub_f32 v24, 0xc1f00000, v160\n"
"v_sub_f32 v25, 0xc1f00000, v160\n"
"v_sub_f32 v26, 0xc1f00000, v160\n"
"v_sub_f32 v27, 0xc1f00000, v160\n"
"v_sub_f32 v28, 0xc1f00000, v161\n"
"v_sub_f32 v29, 0xc1f00000, v161\n"
"v_sub_f32 v30, 0xc1f00000, v161\n"
"v_sub_f32 v31, 0xc1f00000, v161\n"
"v_sub_f32 v32, 0xc1f00000, v162\n"
"v_sub_f32 v33, 0xc1f00000, v162\n"
"v_sub_f32 v34, 0xc1f00000, v162\n"
"v_sub_f32 v35, 0xc1f00000, v162\n"
"v_sub_f32 v36, 0xc1f00000, v163\n"
"v_sub_f32 v37, 0xc1f00000, v163\n"
"v_sub_f32 v38, 0xc1f00000, v163\n"
"v_sub_f32 v39, 0xc1f00000, v163\n"
"s_nop 1\n"
"s_waitcnt lgkmcnt(0)\n"
"AT_main:\n"
"v_mfma_f32_16x16x32_bf16 v[0:3], a[128:131], v[64:67], v[24:27]\n"
"ds_read_b128 a[160:163], v240 offset:8192\n"
"v_mfma_f32_16x16x32_bf16 v[0:3], a[132:135], v[68:71], v[0:3]\n"
"s_nop 0\n"
"v_mfma_f32_16x16x32_bf16 v[0:3], a[136:139], v[72:75], v[0:3]\n"
"ds_read_b128 a[164:167], v240 offset:8448\n"
"v_mfma_f32_16x16x32_bf16 v[0:3], a[140:143], v[76:79], v[0:3]\n"
"s_nop 0\n"
"v_mfma_f32_16x16x32_bf16 v[4:7], a[144:147], v[64:67], v[24:27]\n"
"ds_read_b128 a[168:171], v240 offset:8704\n"
"v_mfma_f32_16x16x32_bf16 v[4:7], a[148:151], v[68:71], v[4:7]\n"
"s_nop 0\n"
"v_mfma_f32_16x16x32_bf16 v[4:7], a[152:155], v[72:75], v[4:7]\n"
"ds_read_b128 a[172:175], v240 offset:8960\n"
"v_mfma_f32_16x16x32_bf16 v[4:7], a[156:159], v[76:79], v[4:7]\n"
"s_nop 0\n"
"s_nop 1\n"
"v_exp_f32 v0, v0\n"
"s_nop 1\n"
"ds_read_b128 a[176:179], v240 offset:12288\n"
"s_nop 1\n"
"v_exp_f32 v1, v1\n"
"s_nop 1\n"
"ds_read_b128 a[180:183], v240 offset:12544\n"
"v_cvt_pk_bf16_f32 v16, v0, v1\n"
"s_nop 1\n"
"v_exp_f32 v2, v2\n"
"s_nop 1\n"
"ds_read_b128 a[184:187], v240 offset:12800\n"
"s_nop 1\n"
"v_exp_f32 v3, v3\n"
"s_nop 1\n"
"v_cvt_pk_bf16_f32 v17, v2, v3\n"
"s_nop 1\n"
"v_exp_f32 v4, v4\n"
"v_mfma_f32_16x16x32_bf16 v[8:11], a[128:131], v[80:83], v[28:31]\n"
"ds_read_b128 a[188:191], v240 offset:13056\n"
"v_mfma_f32_16x16x32_bf16 v[8:11], a[132:135], v[84:87], v[8:11]\n"
"v_exp_f32 v5, v5\n"
"v_mfma_f32_16x16x32_bf16 v[8:11], a[136:139], v[88:91], v[8:11]\n"
"ds_read_b64_tr_b16 a[224:225], v242 offset:24576\n"
"v_cvt_pk_bf16_f32 v18, v4, v5\n"
"v_mfma_f32_16x16x32_bf16 v[8:11], a[140:143], v[92:95], v[8:11]\n"
"v_exp_f32 v6, v6\n"
"v_mfma_f32_16x16x32_bf16 v[12:15], a[144:147], v[80:83], v[28:31]\n"
"ds_read_b64_tr_b16 a[226:227], v242 offset:28672\n"
"v_mfma_f32_16x16x32_bf16 v[12:15], a[148:151], v[84:87], v[12:15]\n"
"v_exp_f32 v7, v7\n"
"v_mfma_f32_16x16x32_bf16 v[12:15], a[152:155], v[88:91], v[12:15]\n"
"ds_read_b64_tr_b16 a[228:229], v243 offset:24576\n"
"v_cvt_pk_bf16_f32 v19, v6, v7\n"
"v_mfma_f32_16x16x32_bf16 v[12:15], a[156:159], v[92:95], v[12:15]\n"
"s_nop 0\n"
"v_mfma_f32_16x16x32_bf16 a[0:3], a[192:195], v[16:19], 0\n"
"v_exp_f32 v8, v8\n"
"v_mfma_f32_16x16x32_bf16 a[4:7], a[196:199], v[16:19], 0\n"
"ds_read_b64_tr_b16 a[230:231], v243 offset:28672\n"
"v_mfma_f32_16x16x32_bf16 a[8:11], a[200:203], v[16:19], 0\n"
"v_exp_f32 v9, v9\n"
"v_mfma_f32_16x16x32_bf16 a[12:15], a[204:207], v[16:19], 0\n"
"ds_read_b64_tr_b16 a[232:233], v242 offset:24832\n"
"v_cvt_pk_bf16_f32 v20, v8, v9\n"
"v_mfma_f32_16x16x32_bf16 a[16:19], a[208:211], v[16:19], 0\n"
"v_exp_f32 v10, v10\n"
"v_mfma_f32_16x16x32_bf16 a[20:23], a[212:215], v[16:19], 0\n"
"ds_read_b64_tr_b16 a[234:235], v242 offset:28928\n"
"v_mfma_f32_16x16x32_bf16 a[24:27], a[216:219], v[16:19], 0\n"
"v_exp_f32 v11, v11\n"
"v_mfma_f32_16x16x32_bf16 a[28:31], a[220:223], v[16:19], 0\n"
"v_cvt_pk_bf16_f32 v21, v10, v11\n"
"v_mfma_f32_16x16x32_bf16 v[44:47], v[40:43], v[16:19], 0\n"
"v_exp_f32 v12, v12\n"
"v_mfma_f32_16x16x32_bf16 v[0:3], a[128:131], v[96:99], v[32:35]\n"
"ds_read_b64_tr_b16 a[236:237], v243 offset:24832\n"
"v_mfma_f32_16x16x32_bf16 v[0:3], a[132:135], v[100:103], v[0:3]\n"
"v_exp_f32 v13, v13\n"
"v_mfma_f32_16x16x32_bf16 v[0:3], a[136:139], v[104:107], v[0:3]\n"
"ds_read_b64_tr_b16 a[238:239], v243 offset:28928\n"
"v_cvt_pk_bf16_f32 v22, v12, v13\n"
"v_mfma_f32_16x16x32_bf16 v[0:3], a[140:143], v[108:111], v[0:3]\n"
"v_exp_f32 v14, v14\n"
"v_mfma_f32_16x16x32_bf16 v[4:7], a[144:147], v[96:99], v[32:35]\n"
"ds_read_b64_tr_b16 a[240:241], v242 offset:25088\n"
"v_mfma_f32_16x16x32_bf16 v[4:7], a[148:151], v[100:103], v[4:7]\n"
"v_exp_f32 v15, v15\n"
"v_mfma_f32_16x16x32_bf16 v[4:7], a[152:155], v[104:107], v[4:7]\n"
"ds_read_b64_tr_b16 a[242:243], v242 offset:29184\n"
"v_cvt_pk_bf16_f32 v23, v14, v15\n"
"v_mfma_f32_16x16x32_bf16 v[4:7], a[156:159], v[108:111], v[4:7]\n"
"s_nop 0\n"
"v_mfma_f32_16x16x32_bf16 a[32:35], a[192:195], v[20:23], 0\n"
"v_exp_f32 v0, v0\n"
"v_mfma_f32_16x16x32_bf16 a[36:39], a[196:199], v[20:23], 0\n"
"ds_read_b64_tr_b16 a[244:245], v243 offset:25088\n"
"v_mfma_f32_16x16x32_bf16 a[40:43], a[200:203], v[20:23], 0\n"
"v_exp_f32 v1, v1\n"
"v_mfma_f32_16x16x32_bf16 a[44:47], a[204:207], v[20:23], 0\n"
"ds_read_b64_tr_b16 a[246:247], v243 offset:29184\n"
"v_cvt_pk_bf16_f32 v16, v0, v1\n"
"v_mfma_f32_16x16x32_bf16 a[48:51], a[208:211], v[20:23], 0\n"
"v_exp_f32 v2, v2\n"
"v_mfma_f32_16x16x32_bf16 a[52:55], a[212:215], v[20:23], 0\n"
"ds_read_b64_tr_b16 a[248:249], v242 offset:25344\n"
"v_mfma_f32_16x16x32_bf16 a[56:59], a[216:219], v[20:23], 0\n"
"v_exp_f32 v3, v3\n"
"v_mfma_f32_16x16x32_bf16 a[60:63], a[220:223], v[20:23], 0\n"
"v_cvt_pk_bf16_f32 v17, v2, v3\n"
"v_mfma_f32_16x16x32_bf16 v[48:51], v[40:43], v[20:23], 0\n"
"v_exp_f32 v4, v4\n"
"v_mfma_f32_16x16x32_bf16 v[8:11], a[128:131], v[112:115], v[36:39]\n"
"ds_read_b64_tr_b16 a[250:251], v242 offset:29440\n"
"v_mfma_f32_16x16x32_bf16 v[8:11], a[132:135], v[116:119], v[8:11]\n"
"v_exp_f32 v5, v5\n"
"v_mfma_f32_16x16x32_bf16 v[8:11], a[136:139], v[120:123], v[8:11]\n"
"ds_read_b64_tr_b16 a[252:253], v243 offset:25344\n"
"v_cvt_pk_bf16_f32 v18, v4, v5\n"
"v_mfma_f32_16x16x32_bf16 v[8:11], a[140:143], v[124:127], v[8:11]\n"
"v_exp_f32 v6, v6\n"
"v_mfma_f32_16x16x32_bf16 v[12:15], a[144:147], v[112:115], v[36:39]\n"
"ds_read_b64_tr_b16 a[254:255], v243 offset:29440\n"
"v_mfma_f32_16x16x32_bf16 v[12:15], a[148:151], v[116:119], v[12:15]\n"
"v_exp_f32 v7, v7\n"
"v_mfma_f32_16x16x32_bf16 v[12:15], a[152:155], v[120:123], v[12:15]\n"
"v_cvt_pk_bf16_f32 v19, v6, v7\n"
"v_mfma_f32_16x16x32_bf16 v[12:15], a[156:159], v[124:127], v[12:15]\n"
"s_nop 0\n"
"v_mfma_f32_16x16x32_bf16 a[64:67], a[192:195], v[16:19], 0\n"
"v_exp_f32 v8, v8\n"
"v_mfma_f32_16x16x32_bf16 a[68:71], a[196:199], v[16:19], 0\n"
"s_nop 0\n"
"v_mfma_f32_16x16x32_bf16 a[72:75], a[200:203], v[16:19], 0\n"
"v_exp_f32 v9, v9\n"
"v_mfma_f32_16x16x32_bf16 a[76:79], a[204:207], v[16:19], 0\n"
"v_cvt_pk_bf16_f32 v20, v8, v9\n"
"v_mfma_f32_16x16x32_bf16 a[80:83], a[208:211], v[16:19], 0\n"
"v_exp_f32 v10, v10\n"
"v_mfma_f32_16x16x32_bf16 a[84:87], a[212:215], v[16:19], 0\n"
"s_nop 0\n"
"v_mfma_f32_16x16x32_bf16 a[88:91], a[216:219], v[16:19], 0\n"
"v_exp_f32 v11, v11\n"
"v_mfma_f32_16x16x32_bf16 a[92:95], a[220:223], v[16:19], 0\n"
"s_waitcnt vmcnt(16) lgkmcnt(0)\n"
"s_barrier\n"
"v_cvt_pk_bf16_f32 v21, v10, v11\n"
"v_mfma_f32_16x16x32_bf16 v[52:55], v[40:43], v[16:19], 0\n"
"v_exp_f32 v12, v12\n"
"v_mfma_f32_16x16x32_bf16 v[0:3], a[160:163], v[64:67], v[24:27]\n"
"ds_read_b128 a[128:131], v240 offset:32768\n"
"v_mfma_f32_16x16x32_bf16 v[0:3], a[164:167], v[68:71], v[0:3]\n"
"v_exp_f32 v13, v13\n"
"v_mfma_f32_16x16x32_bf16 v[0:3], a[168:171], v[72:75], v[0:3]\n"
"ds_read_b128 a[132:135], v240 offset:33024\n"
"v_cvt_pk_bf16_f32 v22, v12, v13\n"
"v_mfma_f32_16x16x32_bf16 v[0:3], a[172:175], v[76:79], v[0:3]\n"
"v_exp_f32 v14, v14\n"
"v_mfma_f32_16x16x32_bf16 v[4:7], a[176:179], v[64:67], v[24:27]\n"
"ds_read_b128 a[136:139], v240 offset:33280\n"
"v_mfma_f32_16x16x32_bf16 v[4:7], a[180:183], v[68:71], v[4:7]\n"
"v_exp_f32 v15, v15\n"
"v_mfma_f32_16x16x32_bf16 v[4:7], a[184:187], v[72:75], v[4:7]\n"
"s_mov_b32 m0, s18\n"
"ds_read_b128 a[140:143], v240 offset:33536\n"
"v_cvt_pk_bf16_f32 v23, v14, v15\n"
"v_mfma_f32_16x16x32_bf16 v[4:7], a[188:191], v[76:79], v[4:7]\n"
"buffer_load_dwordx4 v246, s[8:11], s16 offen offset:0 lds\n"
"v_mfma_f32_16x16x32_bf16 a[96:99], a[192:195], v[20:23], 0\n"
"v_exp_f32 v0, v0\n"
"v_mfma_f32_16x16x32_bf16 a[100:103], a[196:199], v[20:23], 0\n"
"ds_read_b128 a[144:147], v240 offset:36864\n"
"v_mfma_f32_16x16x32_bf16 a[104:107], a[200:203], v[20:23], 0\n"
"v_exp_f32 v1, v1\n"
"v_mfma_f32_16x16x32_bf16 a[108:111], a[204:207], v[20:23], 0\n"
"ds_read_b128 a[148:151], v240 offset:37120\n"
"v_cvt_pk_bf16_f32 v16, v0, v1\n"
"v_mfma_f32_16x16x32_bf16 a[112:115], a[208:211], v[20:23], 0\n"
"v_exp_f32 v2, v2\n"
"v_mfma_f32_16x16x32_bf16 a[116:119], a[212:215], v[20:23], 0\n"
"ds_read_b128 a[152:155], v240 offset:37376\n"
"v_mfma_f32_16x16x32_bf16 a[120:123], a[216:219], v[20:23], 0\n"
"v_exp_f32 v3, v3\n"
"v_mfma_f32_16x16x32_bf16 a[124:127], a[220:223], v[20:23], 0\n"
"v_cvt_pk_bf16_f32 v17, v2, v3\n"
"v_mfma_f32_16x16x32_bf16 v[56:59], v[40:43], v[20:23], 0\n"
"v_exp_f32 v4, v4\n"
"v_mfma_f32_16x16x32_bf16 v[8:11], a[160:163], v[80:83], v[28:31]\n"
"ds_read_b128 a[156:159], v240 offset:37632\n"
"v_mfma_f32_16x16x32_bf16 v[8:11], a[164:167], v[84:87], v[8:11]\n"
"v_exp_f32 v5, v5\n"
"v_mfma_f32_16x16x32_bf16 v[8:11], a[168:171], v[88:91], v[8:11]\n"
"ds_read_b64_tr_b16 a[192:193], v242 offset:49152\n"
"v_cvt_pk_bf16_f32 v18, v4, v5\n"
"v_mfma_f32_16x16x32_bf16 v[8:11], a[172:175], v[92:95], v[8:11]\n"
"v_exp_f32 v6, v6\n"
"v_mfma_f32_16x16x32_bf16 v[12:15], a[176:179], v[80:83], v[28:31]\n"
"ds_read_b64_tr_b16 a[194:195], v242 offset:53248\n"
"v_mfma_f32_16x16x32_bf16 v[12:15], a[180:183], v[84:87], v[12:15]\n"
"v_exp_f32 v7, v7\n"
"v_mfma_f32_16x16x32_bf16 v[12:15], a[184:187], v[88:91], v[12:15]\n"
"ds_read_b64_tr_b16 a[196:197], v243 offset:49152\n"
"v_cvt_pk_bf16_f32 v19, v6, v7\n"
"v_mfma_f32_16x16x32_bf16 v[12:15], a[188:191], v[92:95], v[12:15]\n"
"s_waitcnt lgkmcnt(11)\n"
"buffer_load_dwordx4 v246, s[8:11], s16 offen offset:1024 lds\n"
"v_mfma_f32_16x16x32_bf16 a[0:3], a[224:227], v[16:19], a[0:3]\n"
"v_exp_f32 v8, v8\n"
"v_mfma_f32_16x16x32_bf16 a[4:7], a[228:231], v[16:19], a[4:7]\n"
"ds_read_b64_tr_b16 a[198:199], v243 offset:53248\n"
"v_mfma_f32_16x16x32_bf16 a[8:11], a[232:235], v[16:19], a[8:11]\n"
"v_exp_f32 v9, v9\n"
"v_mfma_f32_16x16x32_bf16 a[12:15], a[236:239], v[16:19], a[12:15]\n"
"ds_read_b64_tr_b16 a[200:201], v242 offset:49408\n"
"v_cvt_pk_bf16_f32 v20, v8, v9\n"
"v_mfma_f32_16x16x32_bf16 a[16:19], a[240:243], v[16:19], a[16:19]\n"
"v_exp_f32 v10, v10\n"
"v_mfma_f32_16x16x32_bf16 a[20:23], a[244:247], v[16:19], a[20:23]\n"
"ds_read_b64_tr_b16 a[202:203], v242 offset:53504\n"
"v_mfma_f32_16x16x32_bf16 a[24:27], a[248:251], v[16:19], a[24:27]\n"
"v_exp_f32 v11, v11\n"
"v_mfma_f32_16x16x32_bf16 a[28:31], a[252:255], v[16:19], a[28:31]\n"
"v_cvt_pk_bf16_f32 v21, v10, v11\n"
"v_mfma_f32_16x16x32_bf16 v[44:47], v[40:43], v[16:19], v[44:47]\n"
"v_exp_f32 v12, v12\n"
"v_mfma_f32_16x16x32_bf16 v[0:3], a[160:163], v[96:99], v[32:35]\n"
"ds_read_b64_tr_b16 a[204:205], v243 offset:49408\n"
"v_mfma_f32_16x16x32_bf16 v[0:3], a[164:167], v[100:103], v[0:3]\n"
"v_exp_f32 v13, v13\n"
"v_mfma_f32_16x16x32_bf16 v[0:3], a[168:171], v[104:107], v[0:3]\n"
"ds_read_b64_tr_b16 a[206:207], v243 offset:53504\n"
"v_cvt_pk_bf16_f32 v22, v12, v13\n"
"v_mfma_f32_16x16x32_bf16 v[0:3], a[172:175], v[108:111], v[0:3]\n"
"v_exp_f32 v14, v14\n"
"v_mfma_f32_16x16x32_bf16 v[4:7], a[176:179], v[96:99], v[32:35]\n"
"ds_read_b64_tr_b16 a[208:209], v242 offset:49664\n"
"v_mfma_f32_16x16x32_bf16 v[4:7], a[180:183], v[100:103], v[4:7]\n"
"v_exp_f32 v15, v15\n"
"v_mfma_f32_16x16x32_bf16 v[4:7], a[184:187], v[104:107], v[4:7]\n"
"ds_read_b64_tr_b16 a[210:211], v242 offset:53760\n"
"v_cvt_pk_bf16_f32 v23, v14, v15\n"
"v_mfma_f32_16x16x32_bf16 v[4:7], a[188:191], v[108:111], v[4:7]\n"
"buffer_load_dwordx4 v247, s[8:11], s16 offen offset:2048 lds\n"
"v_mfma_f32_16x16x32_bf16 a[32:35], a[224:227], v[20:23], a[32:35]\n"
"v_exp_f32 v0, v0\n"
"v_mfma_f32_16x16x32_bf16 a[36:39], a[228:231], v[20:23], a[36:39]\n"
"ds_read_b64_tr_b16 a[212:213], v243 offset:49664\n"
"v_mfma_f32_16x16x32_bf16 a[40:43], a[232:235], v[20:23], a[40:43]\n"
"v_exp_f32 v1, v1\n"
"v_mfma_f32_16x16x32_bf16 a[44:47], a[236:239], v[20:23], a[44:47]\n"
"ds_read_b64_tr_b16 a[214:215], v243 offset:53760\n"
"v_cvt_pk_bf16_f32 v16, v0, v1\n"
"v_mfma_f32_16x16x32_bf16 a[48:51], a[240:243], v[20:23], a[48:51]\n"
"v_exp_f32 v2, v2\n"
"v_mfma_f32_16x16x32_bf16 a[52:55], a[244:247], v[20:23], a[52:55]\n"
"ds_read_b64_tr_b16 a[216:217], v242 offset:49920\n"
"v_mfma_f32_16x16x32_bf16 a[56:59], a[248:251], v[20:23], a[56:59]\n"
"v_exp_f32 v3, v3\n"
"v_mfma_f32_16x16x32_bf16 a[60:63], a[252:255], v[20:23], a[60:63]\n"
"v_cvt_pk_bf16_f32 v17, v2, v3\n"
"v_mfma_f32_16x16x32_bf16 v[48:51], v[40:43], v[20:23], v[48:51]\n"
"v_exp_f32 v4, v4\n"
"v_mfma_f32_16x16x32_bf16 v[8:11], a[160:163], v[112:115], v[36:39]\n"
"ds_read_b64_tr_b16 a[218:219], v242 offset:54016\n"
"v_mfma_f32_16x16x32_bf16 v[8:11], a[164:167], v[116:119], v[8:11]\n"
"v_exp_f32 v5, v5\n"
"v_mfma_f32_16x16x32_bf16 v[8:11], a[168:171], v[120:123], v[8:11]\n"
"ds_read_b64_tr_b16 a[220:221], v243 offset:49920\n"
"v_cvt_pk_bf16_f32 v18, v4, v5\n"
"v_mfma_f32_16x16x32_bf16 v[8:11], a[172:175], v[124:127], v[8:11]\n"
"v_exp_f32 v6, v6\n"
"v_mfma_f32_16x16x32_bf16 v[12:15], a[176:179], v[112:115], v[36:39]\n"
"ds_read_b64_tr_b16 a[222:223], v243 offset:54016\n"
"v_mfma_f32_16x16x32_bf16 v[12:15], a[180:183], v[116:119], v[12:15]\n"
"v_exp_f32 v7, v7\n"
"v_mfma_f32_16x16x32_bf16 v[12:15], a[184:187], v[120:123], v[12:15]\n"
"v_cvt_pk_bf16_f32 v19, v6, v7\n"
"v_mfma_f32_16x16x32_bf16 v[12:15], a[188:191], v[124:127], v[12:15]\n"
"buffer_load_dwordx4 v247, s[8:11], s16 offen offset:3072 lds\n"
"v_mfma_f32_16x16x32_bf16 a[64:67], a[224:227], v[16:19], a[64:67]\n"
"v_exp_f32 v8, v8\n"
"v_mfma_f32_16x16x32_bf16 a[68:71], a[228:231], v[16:19], a[68:71]\n"
"s_nop 0\n"
"v_mfma_f32_16x16x32_bf16 a[72:75], a[232:235], v[16:19], a[72:75]\n"
"v_exp_f32 v9, v9\n"
"v_mfma_f32_16x16x32_bf16 a[76:79], a[236:239], v[16:19], a[76:79]\n"
"v_cvt_pk_bf16_f32 v20, v8, v9\n"
"v_mfma_f32_16x16x32_bf16 a[80:83], a[240:243], v[16:19], a[80:83]\n"
"v_exp_f32 v10, v10\n"
"v_mfma_f32_16x16x32_bf16 a[84:87], a[244:247], v[16:19], a[84:87]\n"
"s_nop 0\n"
"v_mfma_f32_16x16x32_bf16 a[88:91], a[248:251], v[16:19], a[88:91]\n"
"v_exp_f32 v11, v11\n"
"v_mfma_f32_16x16x32_bf16 a[92:95], a[252:255], v[16:19], a[92:95]\n"
"s_waitcnt lgkmcnt(8)\n"
"v_cvt_pk_bf16_f32 v21, v10, v11\n"
"v_mfma_f32_16x16x32_bf16 v[52:55], v[40:43], v[16:19], v[52:55]\n"
"v_exp_f32 v12, v12\n"
"v_mfma_f32_16x16x32_bf16 v[0:3], a[128:131], v[64:67], v[24:27]\n"
"ds_read_b128 a[160:163], v240 offset:40960\n"
"v_mfma_f32_16x16x32_bf16 v[0:3], a[132:135], v[68:71], v[0:3]\n"
"v_exp_f32 v13, v13\n"
"v_mfma_f32_16x16x32_bf16 v[0:3], a[136:139], v[72:75], v[0:3]\n"
"ds_read_b128 a[164:167], v240 offset:41216\n"
"v_cvt_pk_bf16_f32 v22, v12, v13\n"
"v_mfma_f32_16x16x32_bf16 v[0:3], a[140:143], v[76:79], v[0:3]\n"
"v_exp_f32 v14, v14\n"
"v_mfma_f32_16x16x32_bf16 v[4:7], a[144:147], v[64:67], v[24:27]\n"
"ds_read_b128 a[168:171], v240 offset:41472\n"
"v_mfma_f32_16x16x32_bf16 v[4:7], a[148:151], v[68:71], v[4:7]\n"
"v_exp_f32 v15, v15\n"
"v_mfma_f32_16x16x32_bf16 v[4:7], a[152:155], v[72:75], v[4:7]\n"
"s_add_u32 m0, s18, 16384\n"
"ds_read_b128 a[172:175], v240 offset:41728\n"
"v_cvt_pk_bf16_f32 v23, v14, v15\n"
"v_mfma_f32_16x16x32_bf16 v[4:7], a[156:159], v[76:79], v[4:7]\n"
"buffer_load_dwordx4 v246, s[12:15], s16 offen offset:0 lds\n"
"v_mfma_f32_16x16x32_bf16 a[96:99], a[224:227], v[20:23], a[96:99]\n"
"v_exp_f32 v0, v0\n"
"v_mfma_f32_16x16x32_bf16 a[100:103], a[228:231], v[20:23], a[100:103]\n"
"ds_read_b128 a[176:179], v240 offset:45056\n"
"v_mfma_f32_16x16x32_bf16 a[104:107], a[232:235], v[20:23], a[104:107]\n"
"v_exp_f32 v1, v1\n"
"v_mfma_f32_16x16x32_bf16 a[108:111], a[236:239], v[20:23], a[108:111]\n"
"ds_read_b128 a[180:183], v240 offset:45312\n"
"v_cvt_pk_bf16_f32 v16, v0, v1\n"
"v_mfma_f32_16x16x32_bf16 a[112:115], a[240:243], v[20:23], a[112:115]\n"
"v_exp_f32 v2, v2\n"
"v_mfma_f32_16x16x32_bf16 a[116:119], a[244:247], v[20:23], a[116:119]\n"
"ds_read_b128 a[184:187], v240 offset:45568\n"
"v_mfma_f32_16x16x32_bf16 a[120:123], a[248:251], v[20:23], a[120:123]\n"
"v_exp_f32 v3, v3\n"
"v_mfma_f32_16x16x32_bf16 a[124:127], a[252:255], v[20:23], a[124:127]\n"
"v_cvt_pk_bf16_f32 v17, v2, v3\n"
"v_mfma_f32_16x16x32_bf16 v[56:59], v[40:43], v[20:23], v[56:59]\n"
"v_exp_f32 v4, v4\n"
"v_mfma_f32_16x16x32_bf16 v[8:11], a[128:131], v[80:83], v[28:31]\n"
"ds_read_b128 a[188:191], v240 offset:45824\n"
"v_mfma_f32_16x16x32_bf16 v[8:11], a[132:135], v[84:87], v[8:11]\n"
"v_exp_f32 v5, v5\n"
"v_mfma_f32_16x16x32_bf16 v[8:11], a[136:139], v[88:91], v[8:11]\n"
"ds_read_b64_tr_b16 a[224:225], v242 offset:57344\n"
"v_cvt_pk_bf16_f32 v18, v4, v5\n"
"v_mfma_f32_16x16x32_bf16 v[8:11], a[140:143], v[92:95], v[8:11]\n"
"v_exp_f32 v6, v6\n"
"v_mfma_f32_16x16x32_bf16 v[12:15], a[144:147], v[80:83], v[28:31]\n"
"ds_read_b64_tr_b16 a[226:227], v242 offset:61440\n"
"v_mfma_f32_16x16x32_bf16 v[12:15], a[148:151], v[84:87], v[12:15]\n"
"v_exp_f32 v7, v7\n"
"v_mfma_f32_16x16x32_bf16 v[12:15], a[152:155], v[88:91], v[12:15]\n"
"ds_read_b64_tr_b16 a[228:229], v243 offset:57344\n"
"v_cvt_pk_bf16_f32 v19, v6, v7\n"
"v_mfma_f32_16x16x32_bf16 v[12:15], a[156:159], v[92:95], v[12:15]\n"
"s_waitcnt lgkmcnt(11)\n"
"buffer_load_dwordx4 v247, s[12:15], s16 offen offset:1024 lds\n"
"v_mfma_f32_16x16x32_bf16 a[0:3], a[192:195], v[16:19], a[0:3]\n"
"v_exp_f32 v8, v8\n"
"v_mfma_f32_16x16x32_bf16 a[4:7], a[196:199], v[16:19], a[4:7]\n"
"ds_read_b64_tr_b16 a[230:231], v243 offset:61440\n"
"v_mfma_f32_16x16x32_bf16 a[8:11], a[200:203], v[16:19], a[8:11]\n"
"v_exp_f32 v9, v9\n"
"v_mfma_f32_16x16x32_bf16 a[12:15], a[204:207], v[16:19], a[12:15]\n"
"ds_read_b64_tr_b16 a[232:233], v242 offset:57600\n"
"v_cvt_pk_bf16_f32 v20, v8, v9\n"
"v_mfma_f32_16x16x32_bf16 a[16:19], a[208:211], v[16:19], a[16:19]\n"
"v_exp_f32 v10, v10\n"
"v_mfma_f32_16x16x32_bf16 a[20:23], a[212:215], v[16:19], a[20:23]\n"
"ds_read_b64_tr_b16 a[234:235], v242 offset:61696\n"
"v_mfma_f32_16x16x32_bf16 a[24:27], a[216:219], v[16:19], a[24:27]\n"
"v_exp_f32 v11, v11\n"
"v_mfma_f32_16x16x32_bf16 a[28:31], a[220:223], v[16:19], a[28:31]\n"
"v_cvt_pk_bf16_f32 v21, v10, v11\n"
"v_mfma_f32_16x16x32_bf16 v[44:47], v[40:43], v[16:19], v[44:47]\n"
"v_exp_f32 v12, v12\n"
"v_mfma_f32_16x16x32_bf16 v[0:3], a[128:131], v[96:99], v[32:35]\n"
"ds_read_b64_tr_b16 a[236:237], v243 offset:57600\n"
"v_mfma_f32_16x16x32_bf16 v[0:3], a[132:135], v[100:103], v[0:3]\n"
"v_exp_f32 v13, v13\n"
"v_mfma_f32_16x16x32_bf16 v[0:3], a[136:139], v[104:107], v[0:3]\n"
"ds_read_b64_tr_b16 a[238:239], v243 offset:61696\n"
"v_cvt_pk_bf16_f32 v22, v12, v13\n"
"v_mfma_f32_16x16x32_bf16 v[0:3], a[140:143], v[108:111], v[0:3]\n"
"v_exp_f32 v14, v14\n"
"v_mfma_f32_16x16x32_bf16 v[4:7], a[144:147], v[96:99], v[32:35]\n"
"ds_read_b64_tr_b16 a[240:241], v242 offset:57856\n"
"v_mfma_f32_16x16x32_bf16 v[4:7], a[148:151], v[100:103], v[4:7]\n"
"v_exp_f32 v15, v15\n"
"v_mfma_f32_16x16x32_bf16 v[4:7], a[152:155], v[104:107], v[4:7]\n"
"ds_read_b64_tr_b16 a[242:243], v242 offset:61952\n"
"v_cvt_pk_bf16_f32 v23, v14, v15\n"
"v_mfma_f32_16x16x32_bf16 v[4:7], a[156:159], v[108:111], v[4:7]\n"
"buffer_load_dwordx4 v246, s[12:15], s16 offen offset:2048 lds\n"
"v_mfma_f32_16x16x32_bf16 a[32:35], a[192:195], v[20:23], a[32:35]\n"
"v_exp_f32 v0, v0\n"
"v_mfma_f32_16x16x32_bf16 a[36:39], a[196:199], v[20:23], a[36:39]\n"
"ds_read_b64_tr_b16 a[244:245], v243 offset:57856\n"
"v_mfma_f32_16x16x32_bf16 a[40:43], a[200:203], v[20:23], a[40:43]\n"
"v_exp_f32 v1, v1\n"
"v_mfma_f32_16x16x32_bf16 a[44:47], a[204:207], v[20:23], a[44:47]\n"
"ds_read_b64_tr_b16 a[246:247], v243 offset:61952\n"
"v_cvt_pk_bf16_f32 v16, v0, v1\n"
"v_mfma_f32_16x16x32_bf16 a[48:51], a[208:211], v[20:23], a[48:51]\n"
"v_exp_f32 v2, v2\n"
"v_mfma_f32_16x16x32_bf16 a[52:55], a[212:215], v[20:23], a[52:55]\n"
"ds_read_b64_tr_b16 a[248:249], v242 offset:58112\n"
"v_mfma_f32_16x16x32_bf16 a[56:59], a[216:219], v[20:23], a[56:59]\n"
"v_exp_f32 v3, v3\n"
"v_mfma_f32_16x16x32_bf16 a[60:63], a[220:223], v[20:23], a[60:63]\n"
"v_cvt_pk_bf16_f32 v17, v2, v3\n"
"v_mfma_f32_16x16x32_bf16 v[48:51], v[40:43], v[20:23], v[48:51]\n"
"v_exp_f32 v4, v4\n"
"v_mfma_f32_16x16x32_bf16 v[8:11], a[128:131], v[112:115], v[36:39]\n"
"ds_read_b64_tr_b16 a[250:251], v242 offset:62208\n"
"v_mfma_f32_16x16x32_bf16 v[8:11], a[132:135], v[116:119], v[8:11]\n"
"v_exp_f32 v5, v5\n"
"v_mfma_f32_16x16x32_bf16 v[8:11], a[136:139], v[120:123], v[8:11]\n"
"ds_read_b64_tr_b16 a[252:253], v243 offset:58112\n"
"v_cvt_pk_bf16_f32 v18, v4, v5\n"
"v_mfma_f32_16x16x32_bf16 v[8:11], a[140:143], v[124:127], v[8:11]\n"
"v_exp_f32 v6, v6\n"
"v_mfma_f32_16x16x32_bf16 v[12:15], a[144:147], v[112:115], v[36:39]\n"
"ds_read_b64_tr_b16 a[254:255], v243 offset:62208\n"
"v_mfma_f32_16x16x32_bf16 v[12:15], a[148:151], v[116:119], v[12:15]\n"
"v_exp_f32 v7, v7\n"
"v_mfma_f32_16x16x32_bf16 v[12:15], a[152:155], v[120:123], v[12:15]\n"
"v_cvt_pk_bf16_f32 v19, v6, v7\n"
"v_mfma_f32_16x16x32_bf16 v[12:15], a[156:159], v[124:127], v[12:15]\n"
"buffer_load_dwordx4 v247, s[12:15], s16 offen offset:3072 lds\n"
"v_mfma_f32_16x16x32_bf16 a[64:67], a[192:195], v[16:19], a[64:67]\n"
"s_add_u32 s16, s16, 16384\n"
"v_exp_f32 v8, v8\n"
"v_mfma_f32_16x16x32_bf16 a[68:71], a[196:199], v[16:19], a[68:71]\n"
"s_nop 0\n"
"v_mfma_f32_16x16x32_bf16 a[72:75], a[200:203], v[16:19], a[72:75]\n"
"v_exp_f32 v9, v9\n"
"v_mfma_f32_16x16x32_bf16 a[76:79], a[204:207], v[16:19], a[76:79]\n"
"v_cvt_pk_bf16_f32 v20, v8, v9\n"
"v_mfma_f32_16x16x32_bf16 a[80:83], a[208:211], v[16:19], a[80:83]\n"
"v_exp_f32 v10, v10\n"
"v_mfma_f32_16x16x32_bf16 a[84:87], a[212:215], v[16:19], a[84:87]\n"
"s_nop 0\n"
"v_mfma_f32_16x16x32_bf16 a[88:91], a[216:219], v[16:19], a[88:91]\n"
"v_exp_f32 v11, v11\n"
"v_mfma_f32_16x16x32_bf16 a[92:95], a[220:223], v[16:19], a[92:95]\n"
"s_waitcnt vmcnt(16) lgkmcnt(0)\n"
"s_barrier\n"
"v_cvt_pk_bf16_f32 v21, v10, v11\n"
"v_mfma_f32_16x16x32_bf16 v[52:55], v[40:43], v[16:19], v[52:55]\n"
"v_exp_f32 v12, v12\n"
"s_mov_b32 s19, 6\n"
".p2align 6\n"
"AT_loop:\n"
"v_mfma_f32_16x16x32_bf16 v[0:3], a[160:163], v[64:67], v[24:27]\n"
"ds_read_b128 a[128:131], v241 offset:0\n"
"v_mfma_f32_16x16x32_bf16 v[0:3], a[164:167], v[68:71], v[0:3]\n"
"v_exp_f32 v13, v13\n"
"v_mfma_f32_16x16x32_bf16 v[0:3], a[168:171], v[72:75], v[0:3]\n"
"ds_read_b128 a[132:135], v241 offset:256\n"
"v_cvt_pk_bf16_f32 v22, v12, v13\n"
"v_mfma_f32_16x16x32_bf16 v[0:3], a[172:175], v[76:79], v[0:3]\n"
"v_exp_f32 v14, v14\n"
"v_mfma_f32_16x16x32_bf16 v[4:7], a[176:179], v[64:67], v[24:27]\n"
"ds_read_b128 a[136:139], v241 offset:512\n"
"v_mfma_f32_16x16x32_bf16 v[4:7], a[180:183], v[68:71], v[4:7]\n"
"v_exp_f32 v15, v15\n"
"v_mfma_f32_16x16x32_bf16 v[4:7], a[184:187], v[72:75], v[4:7]\n"
"s_add_u32 m0, s18, 32768\n"
"ds_read_b128 a[140:143], v241 offset:768\n"
"v_cvt_pk_bf16_f32 v23, v14, v15\n"
"v_mfma_f32_16x16x32_bf16 v[4:7], a[188:191], v[76:79], v[4:7]\n"
"buffer_load_dwordx4 v246, s[8:11], s16 offen offset:0 lds\n"
"v_mfma_f32_16x16x32_bf16 a[96:99], a[192:195], v[20:23], a[96:99]\n"
"v_exp_f32 v0, v0\n"
"v_mfma_f32_16x16x32_bf16 a[100:103], a[196:199], v[20:23], a[100:103]\n"
"ds_read_b128 a[144:147], v241 offset:4096\n"
"v_mfma_f32_16x16x32_bf16 a[104:107], a[200:203], v[20:23], a[104:107]\n"
"v_exp_f32 v1, v1\n"
"v_mfma_f32_16x16x32_bf16 a[108:111], a[204:207], v[20:23], a[108:111]\n"
"ds_read_b128 a[148:151], v241 offset:4352\n"
"v_cvt_pk_bf16_f32 v16, v0, v1\n"
"v_mfma_f32_16x16x32_bf16 a[112:115], a[208:211], v[20:23], a[112:115]\n"
"v_exp_f32 v2, v2\n"
"v_mfma_f32_16x16x32_bf16 a[116:119], a[212:215], v[20:23], a[116:119]\n"
"ds_read_b128 a[152:155], v241 offset:4608\n"
"v_mfma_f32_16x16x32_bf16 a[120:123], a[216:219], v[20:23], a[120:123]\n"
"v_exp_f32 v3, v3\n"
"v_mfma_f32_16x16x32_bf16 a[124:127], a[220:223], v[20:23], a[124:127]\n"
"v_cvt_pk_bf16_f32 v17, v2, v3\n"
"v_mfma_f32_16x16x32_bf16 v[56:59], v[40:43], v[20:23], v[56:59]\n"
"v_exp_f32 v4, v4\n"
"v_mfma_f32_16x16x32_bf16 v[8:11], a[160:163], v[80:83], v[28:31]\n"
"ds_read_b128 a[156:159], v241 offset:4864\n"
"v_mfma_f32_16x16x32_bf16 v[8:11], a[164:167], v[84:87], v[8:11]\n"
"v_exp_f32 v5, v5\n"
"v_mfma_f32_16x16x32_bf16 v[8:11], a[168:171], v[88:91], v[8:11]\n"
"ds_read_b64_tr_b16 a[192:193], v244 offset:16384\n"
"v_cvt_pk_bf16_f32 v18, v4, v5\n"
"v_mfma_f32_16x16x32_bf16 v[8:11], a[172:175], v[92:95], v[8:11]\n"
"v_exp_f32 v6, v6\n"
"v_mfma_f32_16x16x32_bf16 v[12:15], a[176:179], v[80:83], v[28:31]\n"
"ds_read_b64_tr_b16 a[194:195], v244 offset:20480\n"
"v_mfma_f32_16x16x32_bf16 v[12:15], a[180:183], v[84:87], v[12:15]\n"
"v_exp_f32 v7, v7\n"
"v_mfma_f32_16x16x32_bf16 v[12:15], a[184:187], v[88:91], v[12:15]\n"
"ds_read_b64_tr_b16 a[196:197], v245 offset:16384\n"
"v_cvt_pk_bf16_f32 v19, v6, v7\n"
"v_mfma_f32_16x16x32_bf16 v[12:15], a[188:191], v[92:95], v[12:15]\n"
"s_waitcnt lgkmcnt(11)\n"
"buffer_load_dwordx4 v246, s[8:11], s16 offen offset:1024 lds\n"
"v_mfma_f32_16x16x32_bf16 a[0:3], a[224:227], v[16:19], a[0:3]\n"
"v_exp_f32 v8, v8\n"
"v_mfma_f32_16x16x32_bf16 a[4:7], a[228:231], v[16:19], a[4:7]\n"
"ds_read_b64_tr_b16 a[198:199], v245 offset:20480\n"
"v_mfma_f32_16x16x32_bf16 a[8:11], a[232:235], v[16:19], a[8:11]\n"
"v_exp_f32 v9, v9\n"
"v_mfma_f32_16x16x32_bf16 a[12:15], a[236:239], v[16:19], a[12:15]\n"
"ds_read_b64_tr_b16 a[200:201], v244 offset:16640\n"
"v_cvt_pk_bf16_f32 v20, v8, v9\n"
"v_mfma_f32_16x16x32_bf16 a[16:19], a[240:243], v[16:19], a[16:19]\n"
"v_exp_f32 v10, v10\n"
"v_mfma_f32_16x16x32_bf16 a[20:23], a[244:247], v[16:19], a[20:23]\n"
"ds_read_b64_tr_b16 a[202:203], v244 offset:20736\n"
"v_mfma_f32_16x16x32_bf16 a[24:27], a[248:251], v[16:19], a[24:27]\n"
"v_exp_f32 v11, v11\n"
"v_mfma_f32_16x16x32_bf16 a[28:31], a[252:255], v[16:19], a[28:31]\n"
"v_cvt_pk_bf16_f32 v21, v10, v11\n"
"v_mfma_f32_16x16x32_bf16 v[44:47], v[40:43], v[16:19], v[44:47]\n"
"v_exp_f32 v12, v12\n"
"v_mfma_f32_16x16x32_bf16 v[0:3], a[160:163], v[96:99], v[32:35]\n"
"ds_read_b64_tr_b16 a[204:205], v245 offset:16640\n"
"v_mfma_f32_16x16x32_bf16 v[0:3], a[164:167], v[100:103], v[0:3]\n"
"v_exp_f32 v13, v13\n"
"v_mfma_f32_16x16x32_bf16 v[0:3], a[168:171], v[104:107], v[0:3]\n"
"ds_read_b64_tr_b16 a[206:207], v245 offset:20736\n"
"v_cvt_pk_bf16_f32 v22, v12, v13\n"
"v_mfma_f32_16x16x32_bf16 v[0:3], a[172:175], v[108:111], v[0:3]\n"
"v_exp_f32 v14, v14\n"
"v_mfma_f32_16x16x32_bf16 v[4:7], a[176:179], v[96:99], v[32:35]\n"
"ds_read_b64_tr_b16 a[208:209], v244 offset:16896\n"
"v_mfma_f32_16x16x32_bf16 v[4:7], a[180:183], v[100:103], v[4:7]\n"
"v_exp_f32 v15, v15\n"
"v_mfma_f32_16x16x32_bf16 v[4:7], a[184:187], v[104:107], v[4:7]\n"
"ds_read_b64_tr_b16 a[210:211], v244 offset:20992\n"
"v_cvt_pk_bf16_f32 v23, v14, v15\n"
"v_mfma_f32_16x16x32_bf16 v[4:7], a[188:191], v[108:111], v[4:7]\n"
"buffer_load_dwordx4 v247, s[8:11], s16 offen offset:2048 lds\n"
"v_mfma_f32_16x16x32_bf16 a[32:35], a[224:227], v[20:23], a[32:35]\n"
"v_exp_f32 v0, v0\n"
"v_mfma_f32_16x16x32_bf16 a[36:39], a[228:231], v[20:23], a[36:39]\n"
"ds_read_b64_tr_b16 a[212:213], v245 offset:16896\n"
"v_mfma_f32_16x16x32_bf16 a[40:43], a[232:235], v[20:23], a[40:43]\n"
"v_exp_f32 v1, v1\n"
"v_mfma_f32_16x16x32_bf16 a[44:47], a[236:239], v[20:23], a[44:47]\n"
"ds_read_b64_tr_b16 a[214:215], v245 offset:20992\n"
"v_cvt_pk_bf16_f32 v16, v0, v1\n"
"v_mfma_f32_16x16x32_bf16 a[48:51], a[240:243], v[20:23], a[48:51]\n"
"v_exp_f32 v2, v2\n"
"v_mfma_f32_16x16x32_bf16 a[52:55], a[244:247], v[20:23], a[52:55]\n"
"ds_read_b64_tr_b16 a[216:217], v244 offset:17152\n"
"v_mfma_f32_16x16x32_bf16 a[56:59], a[248:251], v[20:23], a[56:59]\n"
"v_exp_f32 v3, v3\n"
"v_mfma_f32_16x16x32_bf16 a[60:63], a[252:255], v[20:23], a[60:63]\n"
"v_cvt_pk_bf16_f32 v17, v2, v3\n"
"v_mfma_f32_16x16x32_bf16 v[48:51], v[40:43], v[20:23], v[48:51]\n"
"v_exp_f32 v4, v4\n"
"v_mfma_f32_16x16x32_bf16 v[8:11], a[160:163], v[112:115], v[36:39]\n"
"ds_read_b64_tr_b16 a[218:219], v244 offset:21248\n"
"v_mfma_f32_16x16x32_bf16 v[8:11], a[164:167], v[116:119], v[8:11]\n"
"v_exp_f32 v5, v5\n"
"v_mfma_f32_16x16x32_bf16 v[8:11], a[168:171], v[120:123], v[8:11]\n"
"ds_read_b64_tr_b16 a[220:221], v245 offset:17152\n"
"v_cvt_pk_bf16_f32 v18, v4, v5\n"
"v_mfma_f32_16x16x32_bf16 v[8:11], a[172:175], v[124:127], v[8:11]\n"
"v_exp_f32 v6, v6\n"
"v_mfma_f32_16x16x32_bf16 v[12:15], a[176:179], v[112:115], v[36:39]\n"
"ds_read_b64_tr_b16 a[222:223], v245 offset:21248\n"
"v_mfma_f32_16x16x32_bf16 v[12:15], a[180:183], v[116:119], v[12:15]\n"
"v_exp_f32 v7, v7\n"
"v_mfma_f32_16x16x32_bf16 v[12:15], a[184:187], v[120:123], v[12:15]\n"
"v_cvt_pk_bf16_f32 v19, v6, v7\n"
"v_mfma_f32_16x16x32_bf16 v[12:15], a[188:191], v[124:127], v[12:15]\n"
"buffer_load_dwordx4 v247, s[8:11], s16 offen offset:3072 lds\n"
"v_mfma_f32_16x16x32_bf16 a[64:67], a[224:227], v[16:19], a[64:67]\n"
"v_exp_f32 v8, v8\n"
"v_mfma_f32_16x16x32_bf16 a[68:71], a[228:231], v[16:19], a[68:71]\n"
"s_nop 0\n"
"v_mfma_f32_16x16x32_bf16 a[72:75], a[232:235], v[16:19], a[72:75]\n"
"v_exp_f32 v9, v9\n"
"v_mfma_f32_16x16x32_bf16 a[76:79], a[236:239], v[16:19], a[76:79]\n"
"v_cvt_pk_bf16_f32 v20, v8, v9\n"
"v_mfma_f32_16x16x32_bf16 a[80:83], a[240:243], v[16:19], a[80:83]\n"
"v_exp_f32 v10, v10\n"
"v_mfma_f32_16x16x32_bf16 a[84:87], a[244:247], v[16:19], a[84:87]\n"
"s_nop 0\n"
"v_mfma_f32_16x16x32_bf16 a[88:91], a[248:251], v[16:19], a[88:91]\n"
"v_exp_f32 v11, v11\n"
"v_mfma_f32_16x16x32_bf16 a[92:95], a[252:255], v[16:19], a[92:95]\n"
"s_waitcnt lgkmcnt(8)\n"
"v_cvt_pk_bf16_f32 v21, v10, v11\n"
"v_mfma_f32_16x16x32_bf16 v[52:55], v[40:43], v[16:19], v[52:55]\n"
"v_exp_f32 v12, v12\n"
"v_mfma_f32_16x16x32_bf16 v[0:3], a[128:131], v[64:67], v[24:27]\n"
"ds_read_b128 a[160:163], v241 offset:8192\n"
"v_mfma_f32_16x16x32_bf16 v[0:3], a[132:135], v[68:71], v[0:3]\n"
"v_exp_f32 v13, v13\n"
"v_mfma_f32_16x16x32_bf16 v[0:3], a[136:139], v[72:75], v[0:3]\n"
"ds_read_b128 a[164:167], v241 offset:8448\n"
"v_cvt_pk_bf16_f32 v22, v12, v13\n"
"v_mfma_f32_16x16x32_bf16 v[0:3], a[140:143], v[76:79], v[0:3]\n"
"v_exp_f32 v14, v14\n"
"v_mfma_f32_16x16x32_bf16 v[4:7], a[144:147], v[64:67], v[24:27]\n"
"ds_read_b128 a[168:171], v241 offset:8704\n"
"v_mfma_f32_16x16x32_bf16 v[4:7], a[148:151], v[68:71], v[4:7]\n"
"v_exp_f32 v15, v15\n"
"v_mfma_f32_16x16x32_bf16 v[4:7], a[152:155], v[72:75], v[4:7]\n"
"s_add_u32 m0, s18, 49152\n"
"ds_read_b128 a[172:175], v241 offset:8960\n"
"v_cvt_pk_bf16_f32 v23, v14, v15\n"
"v_mfma_f32_16x16x32_bf16 v[4:7], a[156:159], v[76:79], v[4:7]\n"
"buffer_load_dwordx4 v246, s[12:15], s16 offen offset:0 lds\n"
"v_mfma_f32_16x16x32_bf16 a[96:99], a[224:227], v[20:23], a[96:99]\n"
"v_exp_f32 v0, v0\n"
"v_mfma_f32_16x16x32_bf16 a[100:103], a[228:231], v[20:23], a[100:103]\n"
"ds_read_b128 a[176:179], v241 offset:12288\n"
"v_mfma_f32_16x16x32_bf16 a[104:107], a[232:235], v[20:23], a[104:107]\n"
"v_exp_f32 v1, v1\n"
"v_mfma_f32_16x16x32_bf16 a[108:111], a[236:239], v[20:23], a[108:111]\n"
"ds_read_b128 a[180:183], v241 offset:12544\n"
"v_cvt_pk_bf16_f32 v16, v0, v1\n"
"v_mfma_f32_16x16x32_bf16 a[112:115], a[240:243], v[20:23], a[112:115]\n"
"v_exp_f32 v2, v2\n"
"v_mfma_f32_16x16x32_bf16 a[116:119], a[244:247], v[20:23], a[116:119]\n"
"ds_read_b128 a[184:187], v241 offset:12800\n"
"v_mfma_f32_16x16x32_bf16 a[120:123], a[248:251], v[20:23], a[120:123]\n"
"v_exp_f32 v3, v3\n"
"v_mfma_f32_16x16x32_bf16 a[124:127], a[252:255], v[20:23], a[124:127]\n"
"v_cvt_pk_bf16_f32 v17, v2, v3\n"
"v_mfma_f32_16x16x32_bf16 v[56:59], v[40:43], v[20:23], v[56:59]\n"
"v_exp_f32 v4, v4\n"
"v_mfma_f32_16x16x32_bf16 v[8:11], a[128:131], v[80:83], v[28:31]\n"
"ds_read_b128 a[188:191], v241 offset:13056\n"
"v_mfma_f32_16x16x32_bf16 v[8:11], a[132:135], v[84:87], v[8:11]\n"
"v_exp_f32 v5, v5\n"
"v_mfma_f32_16x16x32_bf16 v[8:11], a[136:139], v[88:91], v[8:11]\n"
"ds_read_b64_tr_b16 a[224:225], v244 offset:24576\n"
"v_cvt_pk_bf16_f32 v18, v4, v5\n"
"v_mfma_f32_16x16x32_bf16 v[8:11], a[140:143], v[92:95], v[8:11]\n"
"v_exp_f32 v6, v6\n"
"v_mfma_f32_16x16x32_bf16 v[12:15], a[144:147], v[80:83], v[28:31]\n"
"ds_read_b64_tr_b16 a[226:227], v244 offset:28672\n"
"v_mfma_f32_16x16x32_bf16 v[12:15], a[148:151], v[84:87], v[12:15]\n"
"v_exp_f32 v7, v7\n"
"v_mfma_f32_16x16x32_bf16 v[12:15], a[152:155], v[88:91], v[12:15]\n"
"ds_read_b64_tr_b16 a[228:229], v245 offset:24576\n"
"v_cvt_pk_bf16_f32 v19, v6, v7\n"
"v_mfma_f32_16x16x32_bf16 v[12:15], a[156:159], v[92:95], v[12:15]\n"
"s_waitcnt lgkmcnt(11)\n"
"buffer_load_dwordx4 v247, s[12:15], s16 offen offset:1024 lds\n"
"v_mfma_f32_16x16x32_bf16 a[0:3], a[192:195], v[16:19], a[0:3]\n"
"v_exp_f32 v8, v8\n"
"v_mfma_f32_16x16x32_bf16 a[4:7], a[196:199], v[16:19], a[4:7]\n"
"ds_read_b64_tr_b16 a[230:231], v245 offset:28672\n"
"v_mfma_f32_16x16x32_bf16 a[8:11], a[200:203], v[16:19], a[8:11]\n"
"v_exp_f32 v9, v9\n"
"v_mfma_f32_16x16x32_bf16 a[12:15], a[204:207], v[16:19], a[12:15]\n"
"ds_read_b64_tr_b16 a[232:233], v244 offset:24832\n"
"v_cvt_pk_bf16_f32 v20, v8, v9\n"
"v_mfma_f32_16x16x32_bf16 a[16:19], a[208:211], v[16:19], a[16:19]\n"
"v_exp_f32 v10, v10\n"
"v_mfma_f32_16x16x32_bf16 a[20:23], a[212:215], v[16:19], a[20:23]\n"
"ds_read_b64_tr_b16 a[234:235], v244 offset:28928\n"
"v_mfma_f32_16x16x32_bf16 a[24:27], a[216:219], v[16:19], a[24:27]\n"
"v_exp_f32 v11, v11\n"
"v_mfma_f32_16x16x32_bf16 a[28:31], a[220:223], v[16:19], a[28:31]\n"
"v_cvt_pk_bf16_f32 v21, v10, v11\n"
"v_mfma_f32_16x16x32_bf16 v[44:47], v[40:43], v[16:19], v[44:47]\n"
"v_exp_f32 v12, v12\n"
"v_mfma_f32_16x16x32_bf16 v[0:3], a[128:131], v[96:99], v[32:35]\n"
"ds_read_b64_tr_b16 a[236:237], v245 offset:24832\n"
"v_mfma_f32_16x16x32_bf16 v[0:3], a[132:135], v[100:103], v[0:3]\n"
"v_exp_f32 v13, v13\n"
"v_mfma_f32_16x16x32_bf16 v[0:3], a[136:139], v[104:107], v[0:3]\n"
"ds_read_b64_tr_b16 a[238:239], v245 offset:28928\n"
"v_cvt_pk_bf16_f32 v22, v12, v13\n"
"v_mfma_f32_16x16x32_bf16 v[0:3], a[140:143], v[108:111], v[0:3]\n"
"v_exp_f32 v14, v14\n"
"v_mfma_f32_16x16x32_bf16 v[4:7], a[144:147], v[96:99], v[32:35]\n"
"ds_read_b64_tr_b16 a[240:241], v244 offset:25088\n"
"v_mfma_f32_16x16x32_bf16 v[4:7], a[148:151], v[100:103], v[4:7]\n"
"v_exp_f32 v15, v15\n"
"v_mfma_f32_16x16x32_bf16 v[4:7], a[152:155], v[104:107], v[4:7]\n"
"ds_read_b64_tr_b16 a[242:243], v244 offset:29184\n"
"v_cvt_pk_bf16_f32 v23, v14, v15\n"
"v_mfma_f32_16x16x32_bf16 v[4:7], a[156:159], v[108:111], v[4:7]\n"
"buffer_load_dwordx4 v246, s[12:15], s16 offen offset:2048 lds\n"
"v_mfma_f32_16x16x32_bf16 a[32:35], a[192:195], v[20:23], a[32:35]\n"
"v_exp_f32 v0, v0\n"
"v_mfma_f32_16x16x32_bf16 a[36:39], a[196:199], v[20:23], a[36:39]\n"
"ds_read_b64_tr_b16 a[244:245], v245 offset:25088\n"
"v_mfma_f32_16x16x32_bf16 a[40:43], a[200:203], v[20:23], a[40:43]\n"
"v_exp_f32 v1, v1\n"
"v_mfma_f32_16x16x32_bf16 a[44:47], a[204:207], v[20:23], a[44:47]\n"
"ds_read_b64_tr_b16 a[246:247], v245 offset:29184\n"
"v_cvt_pk_bf16_f32 v16, v0, v1\n"
"v_mfma_f32_16x16x32_bf16 a[48:51], a[208:211], v[20:23], a[48:51]\n"
"v_exp_f32 v2, v2\n"
"v_mfma_f32_16x16x32_bf16 a[52:55], a[212:215], v[20:23], a[52:55]\n"
"ds_read_b64_tr_b16 a[248:249], v244 offset:25344\n"
"v_mfma_f32_16x16x32_bf16 a[56:59], a[216:219], v[20:23], a[56:59]\n"
"v_exp_f32 v3, v3\n"
"v_mfma_f32_16x16x32_bf16 a[60:63], a[220:223], v[20:23], a[60:63]\n"
"v_cvt_pk_bf16_f32 v17, v2, v3\n"
"v_mfma_f32_16x16x32_bf16 v[48:51], v[40:43], v[20:23], v[48:51]\n"
"v_exp_f32 v4, v4\n"
"v_mfma_f32_16x16x32_bf16 v[8:11], a[128:131], v[112:115], v[36:39]\n"
"ds_read_b64_tr_b16 a[250:251], v244 offset:29440\n"
"v_mfma_f32_16x16x32_bf16 v[8:11], a[132:135], v[116:119], v[8:11]\n"
"v_exp_f32 v5, v5\n"
"v_mfma_f32_16x16x32_bf16 v[8:11], a[136:139], v[120:123], v[8:11]\n"
"ds_read_b64_tr_b16 a[252:253], v245 offset:25344\n"
"v_cvt_pk_bf16_f32 v18, v4, v5\n"
"v_mfma_f32_16x16x32_bf16 v[8:11], a[140:143], v[124:127], v[8:11]\n"
"v_exp_f32 v6, v6\n"
"v_mfma_f32_16x16x32_bf16 v[12:15], a[144:147], v[112:115], v[36:39]\n"
"ds_read_b64_tr_b16 a[254:255], v245 offset:29440\n"
"v_mfma_f32_16x16x32_bf16 v[12:15], a[148:151], v[116:119], v[12:15]\n"
"v_exp_f32 v7, v7\n"
"v_mfma_f32_16x16x32_bf16 v[12:15], a[152:155], v[120:123], v[12:15]\n"
"v_cvt_pk_bf16_f32 v19, v6, v7\n"
"v_mfma_f32_16x16x32_bf16 v[12:15], a[156:159], v[124:127], v[12:15]\n"
"buffer_load_dwordx4 v247, s[12:15], s16 offen offset:3072 lds\n"
"v_mfma_f32_16x16x32_bf16 a[64:67], a[192:195], v[16:19], a[64:67]\n"
"s_add_u32 s16, s16, 16384\n"
"v_exp_f32 v8, v8\n"
"v_mfma_f32_16x16x32_bf16 a[68:71], a[196:199], v[16:19], a[68:71]\n"
"s_nop 0\n"
"v_mfma_f32_16x16x32_bf16 a[72:75], a[200:203], v[16:19], a[72:75]\n"
"v_exp_f32 v9, v9\n"
"v_mfma_f32_16x16x32_bf16 a[76:79], a[204:207], v[16:19], a[76:79]\n"
"v_cvt_pk_bf16_f32 v20, v8, v9\n"
"v_mfma_f32_16x16x32_bf16 a[80:83], a[208:211], v[16:19], a[80:83]\n"
"v_exp_f32 v10, v10\n"
"v_mfma_f32_16x16x32_bf16 a[84:87], a[212:215], v[16:19], a[84:87]\n"
"s_nop 0\n"
"v_mfma_f32_16x16x32_bf16 a[88:91], a[216:219], v[16:19], a[88:91]\n"
"v_exp_f32 v11, v11\n"
"v_mfma_f32_16x16x32_bf16 a[92:95], a[220:223], v[16:19], a[92:95]\n"
"s_waitcnt vmcnt(16) lgkmcnt(0)\n"
"s_barrier\n"
"v_cvt_pk_bf16_f32 v21, v10, v11\n"
"v_mfma_f32_16x16x32_bf16 v[52:55], v[40:43], v[16:19], v[52:55]\n"
"v_exp_f32 v12, v12\n"
"v_mfma_f32_16x16x32_bf16 v[0:3], a[160:163], v[64:67], v[24:27]\n"
"ds_read_b128 a[128:131], v241 offset:32768\n"
"v_mfma_f32_16x16x32_bf16 v[0:3], a[164:167], v[68:71], v[0:3]\n"
"v_exp_f32 v13, v13\n"
"v_mfma_f32_16x16x32_bf16 v[0:3], a[168:171], v[72:75], v[0:3]\n"
"ds_read_b128 a[132:135], v241 offset:33024\n"
"v_cvt_pk_bf16_f32 v22, v12, v13\n"
"v_mfma_f32_16x16x32_bf16 v[0:3], a[172:175], v[76:79], v[0:3]\n"
"v_exp_f32 v14, v14\n"
"v_mfma_f32_16x16x32_bf16 v[4:7], a[176:179], v[64:67], v[24:27]\n"
"ds_read_b128 a[136:139], v241 offset:33280\n"
"v_mfma_f32_16x16x32_bf16 v[4:7], a[180:183], v[68:71], v[4:7]\n"
"v_exp_f32 v15, v15\n"
"v_mfma_f32_16x16x32_bf16 v[4:7], a[184:187], v[72:75], v[4:7]\n"
"s_add_u32 m0, s18, 65536\n"
"ds_read_b128 a[140:143], v241 offset:33536\n"
"v_cvt_pk_bf16_f32 v23, v14, v15\n"
"v_mfma_f32_16x16x32_bf16 v[4:7], a[188:191], v[76:79], v[4:7]\n"
"buffer_load_dwordx4 v246, s[8:11], s16 offen offset:0 lds\n"
"v_mfma_f32_16x16x32_bf16 a[96:99], a[192:195], v[20:23], a[96:99]\n"
"v_exp_f32 v0, v0\n"
"v_mfma_f32_16x16x32_bf16 a[100:103], a[196:199], v[20:23], a[100:103]\n"
"ds_read_b128 a[144:147], v241 offset:36864\n"
"v_mfma_f32_16x16x32_bf16 a[104:107], a[200:203], v[20:23], a[104:107]\n"
"v_exp_f32 v1, v1\n"
"v_mfma_f32_16x16x32_bf16 a[108:111], a[204:207], v[20:23], a[108:111]\n"
"ds_read_b128 a[148:151], v241 offset:37120\n"
"v_cvt_pk_bf16_f32 v16, v0, v1\n"
"v_mfma_f32_16x16x32_bf16 a[112:115], a[208:211], v[20:23], a[112:115]\n"
"v_exp_f32 v2, v2\n"
"v_mfma_f32_16x16x32_bf16 a[116:119], a[212:215], v[20:23], a[116:119]\n"
"ds_read_b128 a[152:155], v241 offset:37376\n"
"v_mfma_f32_16x16x32_bf16 a[120:123], a[216:219], v[20:23], a[120:123]\n"
"v_exp_f32 v3, v3\n"
"v_mfma_f32_16x16x32_bf16 a[124:127], a[220:223], v[20:23], a[124:127]\n"
"v_cvt_pk_bf16_f32 v17, v2, v3\n"
"v_mfma_f32_16x16x32_bf16 v[56:59], v[40:43], v[20:23], v[56:59]\n"
"v_exp_f32 v4, v4\n"
"v_mfma_f32_16x16x32_bf16 v[8:11], a[160:163], v[80:83], v[28:31]\n"
"ds_read_b128 a[156:159], v241 offset:37632\n"
"v_mfma_f32_16x16x32_bf16 v[8:11], a[164:167], v[84:87], v[8:11]\n"
"v_exp_f32 v5, v5\n"
"v_mfma_f32_16x16x32_bf16 v[8:11], a[168:171], v[88:91], v[8:11]\n"
"ds_read_b64_tr_b16 a[192:193], v244 offset:49152\n"
"v_cvt_pk_bf16_f32 v18, v4, v5\n"
"v_mfma_f32_16x16x32_bf16 v[8:11], a[172:175], v[92:95], v[8:11]\n"
"v_exp_f32 v6, v6\n"
"v_mfma_f32_16x16x32_bf16 v[12:15], a[176:179], v[80:83], v[28:31]\n"
"ds_read_b64_tr_b16 a[194:195], v244 offset:53248\n"
"v_mfma_f32_16x16x32_bf16 v[12:15], a[180:183], v[84:87], v[12:15]\n"
"v_exp_f32 v7, v7\n"
"v_mfma_f32_16x16x32_bf16 v[12:15], a[184:187], v[88:91], v[12:15]\n"
"ds_read_b64_tr_b16 a[196:197], v245 offset:49152\n"
"v_cvt_pk_bf16_f32 v19, v6, v7\n"
"v_mfma_f32_16x16x32_bf16 v[12:15], a[188:191], v[92:95], v[12:15]\n"
"s_waitcnt lgkmcnt(11)\n"
"buffer_load_dwordx4 v246, s[8:11], s16 offen offset:1024 lds\n"
"v_mfma_f32_16x16x32_bf16 a[0:3], a[224:227], v[16:19], a[0:3]\n"
"v_exp_f32 v8, v8\n"
"v_mfma_f32_16x16x32_bf16 a[4:7], a[228:231], v[16:19], a[4:7]\n"
"ds_read_b64_tr_b16 a[198:199], v245 offset:53248\n"
"v_mfma_f32_16x16x32_bf16 a[8:11], a[232:235], v[16:19], a[8:11]\n"
"v_exp_f32 v9, v9\n"
"v_mfma_f32_16x16x32_bf16 a[12:15], a[236:239], v[16:19], a[12:15]\n"
"ds_read_b64_tr_b16 a[200:201], v244 offset:49408\n"
"v_cvt_pk_bf16_f32 v20, v8, v9\n"
"v_mfma_f32_16x16x32_bf16 a[16:19], a[240:243], v[16:19], a[16:19]\n"
"v_exp_f32 v10, v10\n"
"v_mfma_f32_16x16x32_bf16 a[20:23], a[244:247], v[16:19], a[20:23]\n"
"ds_read_b64_tr_b16 a[202:203], v244 offset:53504\n"
"v_mfma_f32_16x16x32_bf16 a[24:27], a[248:251], v[16:19], a[24:27]\n"
"v_exp_f32 v11, v11\n"
"v_mfma_f32_16x16x32_bf16 a[28:31], a[252:255], v[16:19], a[28:31]\n"
"v_cvt_pk_bf16_f32 v21, v10, v11\n"
"v_mfma_f32_16x16x32_bf16 v[44:47], v[40:43], v[16:19], v[44:47]\n"
"v_exp_f32 v12, v12\n"
"v_mfma_f32_16x16x32_bf16 v[0:3], a[160:163], v[96:99], v[32:35]\n"
"ds_read_b64_tr_b16 a[204:205], v245 offset:49408\n"
"v_mfma_f32_16x16x32_bf16 v[0:3], a[164:167], v[100:103], v[0:3]\n"
"v_exp_f32 v13, v13\n"
"v_mfma_f32_16x16x32_bf16 v[0:3], a[168:171], v[104:107], v[0:3]\n"
"ds_read_b64_tr_b16 a[206:207], v245 offset:53504\n"
"v_cvt_pk_bf16_f32 v22, v12, v13\n"
"v_mfma_f32_16x16x32_bf16 v[0:3], a[172:175], v[108:111], v[0:3]\n"
"v_exp_f32 v14, v14\n"
"v_mfma_f32_16x16x32_bf16 v[4:7], a[176:179], v[96:99], v[32:35]\n"
"ds_read_b64_tr_b16 a[208:209], v244 offset:49664\n"
"v_mfma_f32_16x16x32_bf16 v[4:7], a[180:183], v[100:103], v[4:7]\n"
"v_exp_f32 v15, v15\n"
"v_mfma_f32_16x16x32_bf16 v[4:7], a[184:187], v[104:107], v[4:7]\n"
"ds_read_b64_tr_b16 a[210:211], v244 offset:53760\n"
"v_cvt_pk_bf16_f32 v23, v14, v15\n"
"v_mfma_f32_16x16x32_bf16 v[4:7], a[188:191], v[108:111], v[4:7]\n"
"buffer_load_dwordx4 v247, s[8:11], s16 offen offset:2048 lds\n"
"v_mfma_f32_16x16x32_bf16 a[32:35], a[224:227], v[20:23], a[32:35]\n"
"v_exp_f32 v0, v0\n"
"v_mfma_f32_16x16x32_bf16 a[36:39], a[228:231], v[20:23], a[36:39]\n"
"ds_read_b64_tr_b16 a[212:213], v245 offset:49664\n"
"v_mfma_f32_16x16x32_bf16 a[40:43], a[232:235], v[20:23], a[40:43]\n"
"v_exp_f32 v1, v1\n"
"v_mfma_f32_16x16x32_bf16 a[44:47], a[236:239], v[20:23], a[44:47]\n"
"ds_read_b64_tr_b16 a[214:215], v245 offset:53760\n"
"v_cvt_pk_bf16_f32 v16, v0, v1\n"
"v_mfma_f32_16x16x32_bf16 a[48:51], a[240:243], v[20:23], a[48:51]\n"
"v_exp_f32 v2, v2\n"
"v_mfma_f32_16x16x32_bf16 a[52:55], a[244:247], v[20:23], a[52:55]\n"
"ds_read_b64_tr_b16 a[216:217], v244 offset:49920\n"
"v_mfma_f32_16x16x32_bf16 a[56:59], a[248:251], v[20:23], a[56:59]\n"
"v_exp_f32 v3, v3\n"
"v_mfma_f32_16x16x32_bf16 a[60:63], a[252:255], v[20:23], a[60:63]\n"
"v_cvt_pk_bf16_f32 v17, v2, v3\n"
"v_mfma_f32_16x16x32_bf16 v[48:51], v[40:43], v[20:23], v[48:51]\n"
"v_exp_f32 v4, v4\n"
"v_mfma_f32_16x16x32_bf16 v[8:11], a[160:163], v[112:115], v[36:39]\n"
"ds_read_b64_tr_b16 a[218:219], v244 offset:54016\n"
"v_mfma_f32_16x16x32_bf16 v[8:11], a[164:167], v[116:119], v[8:11]\n"
"v_exp_f32 v5, v5\n"
"v_mfma_f32_16x16x32_bf16 v[8:11], a[168:171], v[120:123], v[8:11]\n"
"ds_read_b64_tr_b16 a[220:221], v245 offset:49920\n"
"v_cvt_pk_bf16_f32 v18, v4, v5\n"
"v_mfma_f32_16x16x32_bf16 v[8:11], a[172:175], v[124:127], v[8:11]\n"
"v_exp_f32 v6, v6\n"
"v_mfma_f32_16x16x32_bf16 v[12:15], a[176:179], v[112:115], v[36:39]\n"
"ds_read_b64_tr_b16 a[222:223], v245 offset:54016\n"
"v_mfma_f32_16x16x32_bf16 v[12:15], a[180:183], v[116:119], v[12:15]\n"
"v_exp_f32 v7, v7\n"
"v_mfma_f32_16x16x32_bf16 v[12:15], a[184:187], v[120:123], v[12:15]\n"
"v_cvt_pk_bf16_f32 v19, v6, v7\n"
"v_mfma_f32_16x16x32_bf16 v[12:15], a[188:191], v[124:127], v[12:15]\n"
"buffer_load_dwordx4 v247, s[8:11], s16 offen offset:3072 lds\n"
"v_mfma_f32_16x16x32_bf16 a[64:67], a[224:227], v[16:19], a[64:67]\n"
"v_exp_f32 v8, v8\n"
"v_mfma_f32_16x16x32_bf16 a[68:71], a[228:231], v[16:19], a[68:71]\n"
"s_nop 0\n"
"v_mfma_f32_16x16x32_bf16 a[72:75], a[232:235], v[16:19], a[72:75]\n"
"v_exp_f32 v9, v9\n"
"v_mfma_f32_16x16x32_bf16 a[76:79], a[236:239], v[16:19], a[76:79]\n"
"v_cvt_pk_bf16_f32 v20, v8, v9\n"
"v_mfma_f32_16x16x32_bf16 a[80:83], a[240:243], v[16:19], a[80:83]\n"
"v_exp_f32 v10, v10\n"
"v_mfma_f32_16x16x32_bf16 a[84:87], a[244:247], v[16:19], a[84:87]\n"
"s_nop 0\n"
"v_mfma_f32_16x16x32_bf16 a[88:91], a[248:251], v[16:19], a[88:91]\n"
"v_exp_f32 v11, v11\n"
"v_mfma_f32_16x16x32_bf16 a[92:95], a[252:255], v[16:19], a[92:95]\n"
"s_waitcnt lgkmcnt(8)\n"
"v_cvt_pk_bf16_f32 v21, v10, v11\n"
"v_mfma_f32_16x16x32_bf16 v[52:55], v[40:43], v[16:19], v[52:55]\n"
"v_exp_f32 v12, v12\n"
"v_mfma_f32_16x16x32_bf16 v[0:3], a[128:131], v[64:67], v[24:27]\n"
"ds_read_b128 a[160:163], v241 offset:40960\n"
"v_mfma_f32_16x16x32_bf16 v[0:3], a[132:135], v[68:71], v[0:3]\n"
"v_exp_f32 v13, v13\n"
"v_mfma_f32_16x16x32_bf16 v[0:3], a[136:139], v[72:75], v[0:3]\n"
"ds_read_b128 a[164:167], v241 offset:41216\n"
"v_cvt_pk_bf16_f32 v22, v12, v13\n"
"v_mfma_f32_16x16x32_bf16 v[0:3], a[140:143], v[76:79], v[0:3]\n"
"v_exp_f32 v14, v14\n"
"v_mfma_f32_16x16x32_bf16 v[4:7], a[144:147], v[64:67], v[24:27]\n"
"ds_read_b128 a[168:171], v241 offset:41472\n"
"v_mfma_f32_16x16x32_bf16 v[4:7], a[148:151], v[68:71], v[4:7]\n"
"v_exp_f32 v15, v15\n"
"v_mfma_f32_16x16x32_bf16 v[4:7], a[152:155], v[72:75], v[4:7]\n"
"s_add_u32 m0, s18, 81920\n"
"ds_read_b128 a[172:175], v241 offset:41728\n"
"v_cvt_pk_bf16_f32 v23, v14, v15\n"
"v_mfma_f32_16x16x32_bf16 v[4:7], a[156:159], v[76:79], v[4:7]\n"
"buffer_load_dwordx4 v246, s[12:15], s16 offen offset:0 lds\n"
"v_mfma_f32_16x16x32_bf16 a[96:99], a[224:227], v[20:23], a[96:99]\n"
"v_exp_f32 v0, v0\n"
"v_mfma_f32_16x16x32_bf16 a[100:103], a[228:231], v[20:23], a[100:103]\n"
"ds_read_b128 a[176:179], v241 offset:45056\n"
"v_mfma_f32_16x16x32_bf16 a[104:107], a[232:235], v[20:23], a[104:107]\n"
"v_exp_f32 v1, v1\n"
"v_mfma_f32_16x16x32_bf16 a[108:111], a[236:239], v[20:23], a[108:111]\n"
"ds_read_b128 a[180:183], v241 offset:45312\n"
"v_cvt_pk_bf16_f32 v16, v0, v1\n"
"v_mfma_f32_16x16x32_bf16 a[112:115], a[240:243], v[20:23], a[112:115]\n"
"v_exp_f32 v2, v2\n"
"v_mfma_f32_16x16x32_bf16 a[116:119], a[244:247], v[20:23], a[116:119]\n"
"ds_read_b128 a[184:187], v241 offset:45568\n"
"v_mfma_f32_16x16x32_bf16 a[120:123], a[248:251], v[20:23], a[120:123]\n"
"v_exp_f32 v3, v3\n"
"v_mfma_f32_16x16x32_bf16 a[124:127], a[252:255], v[20:23], a[124:127]\n"
"v_cvt_pk_bf16_f32 v17, v2, v3\n"
"v_mfma_f32_16x16x32_bf16 v[56:59], v[40:43], v[20:23], v[56:59]\n"
"v_exp_f32 v4, v4\n"
"v_mfma_f32_16x16x32_bf16 v[8:11], a[128:131], v[80:83], v[28:31]\n"
"ds_read_b128 a[188:191], v241 offset:45824\n"
"v_mfma_f32_16x16x32_bf16 v[8:11], a[132:135], v[84:87], v[8:11]\n"
"v_exp_f32 v5, v5\n"
"v_mfma_f32_16x16x32_bf16 v[8:11], a[136:139], v[88:91], v[8:11]\n"
"ds_read_b64_tr_b16 a[224:225], v244 offset:57344\n"
"v_cvt_pk_bf16_f32 v18, v4, v5\n"
"v_mfma_f32_16x16x32_bf16 v[8:11], a[140:143], v[92:95], v[8:11]\n"
"v_exp_f32 v6, v6\n"
"v_mfma_f32_16x16x32_bf16 v[12:15], a[144:147], v[80:83], v[28:31]\n"
"ds_read_b64_tr_b16 a[226:227], v244 offset:61440\n"
"v_mfma_f32_16x16x32_bf16 v[12:15], a[148:151], v[84:87], v[12:15]\n"
"v_exp_f32 v7, v7\n"
"v_mfma_f32_16x16x32_bf16 v[12:15], a[152:155], v[88:91], v[12:15]\n"
"ds_read_b64_tr_b16 a[228:229], v245 offset:57344\n"
"v_cvt_pk_bf16_f32 v19, v6, v7\n"
"v_mfma_f32_16x16x32_bf16 v[12:15], a[156:159], v[92:95], v[12:15]\n"
"s_waitcnt lgkmcnt(11)\n"
"buffer_load_dwordx4 v247, s[12:15], s16 offen offset:1024 lds\n"
"v_mfma_f32_16x16x32_bf16 a[0:3], a[192:195], v[16:19], a[0:3]\n"
"v_exp_f32 v8, v8\n"
"v_mfma_f32_16x16x32_bf16 a[4:7], a[196:199], v[16:19], a[4:7]\n"
"ds_read_b64_tr_b16 a[230:231], v245 offset:61440\n"
"v_mfma_f32_16x16x32_bf16 a[8:11], a[200:203], v[16:19], a[8:11]\n"
"v_exp_f32 v9, v9\n"
"v_mfma_f32_16x16x32_bf16 a[12:15], a[204:207], v[16:19], a[12:15]\n"
"ds_read_b64_tr_b16 a[232:233], v244 offset:57600\n"
"v_cvt_pk_bf16_f32 v20, v8, v9\n"
"v_mfma_f32_16x16x32_bf16 a[16:19], a[208:211], v[16:19], a[16:19]\n"
"v_exp_f32 v10, v10\n"
"v_mfma_f32_16x16x32_bf16 a[20:23], a[212:215], v[16:19], a[20:23]\n"
"ds_read_b64_tr_b16 a[234:235], v244 offset:61696\n"
"v_mfma_f32_16x16x32_bf16 a[24:27], a[216:219], v[16:19], a[24:27]\n"
"v_exp_f32 v11, v11\n"
"v_mfma_f32_16x16x32_bf16 a[28:31], a[220:223], v[16:19], a[28:31]\n"
"v_cvt_pk_bf16_f32 v21, v10, v11\n"
"v_mfma_f32_16x16x32_bf16 v[44:47], v[40:43], v[16:19], v[44:47]\n"
"v_exp_f32 v12, v12\n"
"v_mfma_f32_16x16x32_bf16 v[0:3], a[128:131], v[96:99], v[32:35]\n"
"ds_read_b64_tr_b16 a[236:237], v245 offset:57600\n"
"v_mfma_f32_16x16x32_bf16 v[0:3], a[132:135], v[100:103], v[0:3]\n"
"v_exp_f32 v13, v13\n"
"v_mfma_f32_16x16x32_bf16 v[0:3], a[136:139], v[104:107], v[0:3]\n"
"ds_read_b64_tr_b16 a[238:239], v245 offset:61696\n"
"v_cvt_pk_bf16_f32 v22, v12, v13\n"
"v_mfma_f32_16x16x32_bf16 v[0:3], a[140:143], v[108:111], v[0:3]\n"
"v_exp_f32 v14, v14\n"
"v_mfma_f32_16x16x32_bf16 v[4:7], a[144:147], v[96:99], v[32:35]\n"
"ds_read_b64_tr_b16 a[240:241], v244 offset:57856\n"
"v_mfma_f32_16x16x32_bf16 v[4:7], a[148:151], v[100:103], v[4:7]\n"
"v_exp_f32 v15, v15\n"
"v_mfma_f32_16x16x32_bf16 v[4:7], a[152:155], v[104:107], v[4:7]\n"
"ds_read_b64_tr_b16 a[242:243], v244 offset:61952\n"
"v_cvt_pk_bf16_f32 v23, v14, v15\n"
"v_mfma_f32_16x16x32_bf16 v[4:7], a[156:159], v[108:111], v[4:7]\n"
"buffer_load_dwordx4 v246, s[12:15], s16 offen offset:2048 lds\n"
"v_mfma_f32_16x16x32_bf16 a[32:35], a[192:195], v[20:23], a[32:35]\n"
"v_exp_f32 v0, v0\n"
"v_mfma_f32_16x16x32_bf16 a[36:39], a[196:199], v[20:23], a[36:39]\n"
"ds_read_b64_tr_b16 a[244:245], v245 offset:57856\n"
"v_mfma_f32_16x16x32_bf16 a[40:43], a[200:203], v[20:23], a[40:43]\n"
"v_exp_f32 v1, v1\n"
"v_mfma_f32_16x16x32_bf16 a[44:47], a[204:207], v[20:23], a[44:47]\n"
"ds_read_b64_tr_b16 a[246:247], v245 offset:61952\n"
"v_cvt_pk_bf16_f32 v16, v0, v1\n"
"v_mfma_f32_16x16x32_bf16 a[48:51], a[208:211], v[20:23], a[48:51]\n"
"v_exp_f32 v2, v2\n"
"v_mfma_f32_16x16x32_bf16 a[52:55], a[212:215], v[20:23], a[52:55]\n"
"ds_read_b64_tr_b16 a[248:249], v244 offset:58112\n"
"v_mfma_f32_16x16x32_bf16 a[56:59], a[216:219], v[20:23], a[56:59]\n"
"v_exp_f32 v3, v3\n"
"v_mfma_f32_16x16x32_bf16 a[60:63], a[220:223], v[20:23], a[60:63]\n"
"v_cvt_pk_bf16_f32 v17, v2, v3\n"
"v_mfma_f32_16x16x32_bf16 v[48:51], v[40:43], v[20:23], v[48:51]\n"
"v_exp_f32 v4, v4\n"
"v_mfma_f32_16x16x32_bf16 v[8:11], a[128:131], v[112:115], v[36:39]\n"
"ds_read_b64_tr_b16 a[250:251], v244 offset:62208\n"
"v_mfma_f32_16x16x32_bf16 v[8:11], a[132:135], v[116:119], v[8:11]\n"
"v_exp_f32 v5, v5\n"
"v_mfma_f32_16x16x32_bf16 v[8:11], a[136:139], v[120:123], v[8:11]\n"
"ds_read_b64_tr_b16 a[252:253], v245 offset:58112\n"
"v_cvt_pk_bf16_f32 v18, v4, v5\n"
"v_mfma_f32_16x16x32_bf16 v[8:11], a[140:143], v[124:127], v[8:11]\n"
"v_exp_f32 v6, v6\n"
"v_mfma_f32_16x16x32_bf16 v[12:15], a[144:147], v[112:115], v[36:39]\n"
"ds_read_b64_tr_b16 a[254:255], v245 offset:62208\n"
"v_mfma_f32_16x16x32_bf16 v[12:15], a[148:151], v[116:119], v[12:15]\n"
"v_exp_f32 v7, v7\n"
"v_mfma_f32_16x16x32_bf16 v[12:15], a[152:155], v[120:123], v[12:15]\n"
"v_cvt_pk_bf16_f32 v19, v6, v7\n"
"v_mfma_f32_16x16x32_bf16 v[12:15], a[156:159], v[124:127], v[12:15]\n"
"buffer_load_dwordx4 v247, s[12:15], s16 offen offset:3072 lds\n"
"v_mfma_f32_16x16x32_bf16 a[64:67], a[192:195], v[16:19], a[64:67]\n"
"s_add_u32 s16, s16, 16384\n"
"v_exp_f32 v8, v8\n"
"v_mfma_f32_16x16x32_bf16 a[68:71], a[196:199], v[16:19], a[68:71]\n"
"s_nop 0\n"
"v_mfma_f32_16x16x32_bf16 a[72:75], a[200:203], v[16:19], a[72:75]\n"
"v_exp_f32 v9, v9\n"
"v_mfma_f32_16x16x32_bf16 a[76:79], a[204:207], v[16:19], a[76:79]\n"
"v_cvt_pk_bf16_f32 v20, v8, v9\n"
"v_mfma_f32_16x16x32_bf16 a[80:83], a[208:211], v[16:19], a[80:83]\n"
"v_exp_f32 v10, v10\n"
"v_mfma_f32_16x16x32_bf16 a[84:87], a[212:215], v[16:19], a[84:87]\n"
"s_nop 0\n"
"v_mfma_f32_16x16x32_bf16 a[88:91], a[216:219], v[16:19], a[88:91]\n"
"v_exp_f32 v11, v11\n"
"v_mfma_f32_16x16x32_bf16 a[92:95], a[220:223], v[16:19], a[92:95]\n"
"s_waitcnt vmcnt(16) lgkmcnt(0)\n"
"s_barrier\n"
"v_cvt_pk_bf16_f32 v21, v10, v11\n"
"v_mfma_f32_16x16x32_bf16 v[52:55], v[40:43], v[16:19], v[52:55]\n"
"v_exp_f32 v12, v12\n"
"v_mfma_f32_16x16x32_bf16 v[0:3], a[160:163], v[64:67], v[24:27]\n"
"ds_read_b128 a[128:131], v240 offset:0\n"
"v_mfma_f32_16x16x32_bf16 v[0:3], a[164:167], v[68:71], v[0:3]\n"
"v_exp_f32 v13, v13\n"
"v_mfma_f32_16x16x32_bf16 v[0:3], a[168:171], v[72:75], v[0:3]\n"
"ds_read_b128 a[132:135], v240 offset:256\n"
"v_cvt_pk_bf16_f32 v22, v12, v13\n"
"v_mfma_f32_16x16x32_bf16 v[0:3], a[172:175], v[76:79], v[0:3]\n"
"v_exp_f32 v14, v14\n"
"v_mfma_f32_16x16x32_bf16 v[4:7], a[176:179], v[64:67], v[24:27]\n"
"ds_read_b128 a[136:139], v240 offset:512\n"
"v_mfma_f32_16x16x32_bf16 v[4:7], a[180:183], v[68:71], v[4:7]\n"
"v_exp_f32 v15, v15\n"
"v_mfma_f32_16x16x32_bf16 v[4:7], a[184:187], v[72:75], v[4:7]\n"
"s_add_u32 m0, s18, 98304\n"
"ds_read_b128 a[140:143], v240 offset:768\n"
"v_cvt_pk_bf16_f32 v23, v14, v15\n"
"v_mfma_f32_16x16x32_bf16 v[4:7], a[188:191], v[76:79], v[4:7]\n"
"buffer_load_dwordx4 v246, s[8:11], s16 offen offset:0 lds\n"
"v_mfma_f32_16x16x32_bf16 a[96:99], a[192:195], v[20:23], a[96:99]\n"
"v_exp_f32 v0, v0\n"
"v_mfma_f32_16x16x32_bf16 a[100:103], a[196:199], v[20:23], a[100:103]\n"
"ds_read_b128 a[144:147], v240 offset:4096\n"
"v_mfma_f32_16x16x32_bf16 a[104:107], a[200:203], v[20:23], a[104:107]\n"
"v_exp_f32 v1, v1\n"
"v_mfma_f32_16x16x32_bf16 a[108:111], a[204:207], v[20:23], a[108:111]\n"
"ds_read_b128 a[148:151], v240 offset:4352\n"
"v_cvt_pk_bf16_f32 v16, v0, v1\n"
"v_mfma_f32_16x16x32_bf16 a[112:115], a[208:211], v[20:23], a[112:115]\n"
"v_exp_f32 v2, v2\n"
"v_mfma_f32_16x16x32_bf16 a[116:119], a[212:215], v[20:23], a[116:119]\n"
"ds_read_b128 a[152:155], v240 offset:4608\n"
"v_mfma_f32_16x16x32_bf16 a[120:123], a[216:219], v[20:23], a[120:123]\n"
"v_exp_f32 v3, v3\n"
"v_mfma_f32_16x16x32_bf16 a[124:127], a[220:223], v[20:23], a[124:127]\n"
"v_cvt_pk_bf16_f32 v17, v2, v3\n"
"v_mfma_f32_16x16x32_bf16 v[56:59], v[40:43], v[20:23], v[56:59]\n"
"v_exp_f32 v4, v4\n"
"v_mfma_f32_16x16x32_bf16 v[8:11], a[160:163], v[80:83], v[28:31]\n"
"ds_read_b128 a[156:159], v240 offset:4864\n"
"v_mfma_f32_16x16x32_bf16 v[8:11], a[164:167], v[84:87], v[8:11]\n"
"v_exp_f32 v5, v5\n"
"v_mfma_f32_16x16x32_bf16 v[8:11], a[168:171], v[88:91], v[8:11]\n"
"ds_read_b64_tr_b16 a[192:193], v242 offset:16384\n"
"v_cvt_pk_bf16_f32 v18, v4, v5\n"
"v_mfma_f32_16x16x32_bf16 v[8:11], a[172:175], v[92:95], v[8:11]\n"
"v_exp_f32 v6, v6\n"
"v_mfma_f32_16x16x32_bf16 v[12:15], a[176:179], v[80:83], v[28:31]\n"
"ds_read_b64_tr_b16 a[194:195], v242 offset:20480\n"
"v_mfma_f32_16x16x32_bf16 v[12:15], a[180:183], v[84:87], v[12:15]\n"
"v_exp_f32 v7, v7\n"
"v_mfma_f32_16x16x32_bf16 v[12:15], a[184:187], v[88:91], v[12:15]\n"
"ds_read_b64_tr_b16 a[196:197], v243 offset:16384\n"
"v_cvt_pk_bf16_f32 v19, v6, v7\n"
"v_mfma_f32_16x16x32_bf16 v[12:15], a[188:191], v[92:95], v[12:15]\n"
"s_waitcnt lgkmcnt(11)\n"
"buffer_load_dwordx4 v246, s[8:11], s16 offen offset:1024 lds\n"
"v_mfma_f32_16x16x32_bf16 a[0:3], a[224:227], v[16:19], a[0:3]\n"
"v_exp_f32 v8, v8\n"
"v_mfma_f32_16x16x32_bf16 a[4:7], a[228:231], v[16:19], a[4:7]\n"
"ds_read_b64_tr_b16 a[198:199], v243 offset:20480\n"
"v_mfma_f32_16x16x32_bf16 a[8:11], a[232:235], v[16:19], a[8:11]\n"
"v_exp_f32 v9, v9\n"
"v_mfma_f32_16x16x32_bf16 a[12:15], a[236:239], v[16:19], a[12:15]\n"
"ds_read_b64_tr_b16 a[200:201], v242 offset:16640\n"
"v_cvt_pk_bf16_f32 v20, v8, v9\n"
"v_mfma_f32_16x16x32_bf16 a[16:19], a[240:243], v[16:19], a[16:19]\n"
"v_exp_f32 v10, v10\n"
"v_mfma_f32_16x16x32_bf16 a[20:23], a[244:247], v[16:19], a[20:23]\n"
"ds_read_b64_tr_b16 a[202:203], v242 offset:20736\n"
"v_mfma_f32_16x16x32_bf16 a[24:27], a[248:251], v[16:19], a[24:27]\n"
"v_exp_f32 v11, v11\n"
"v_mfma_f32_16x16x32_bf16 a[28:31], a[252:255], v[16:19], a[28:31]\n"
"v_cvt_pk_bf16_f32 v21, v10, v11\n"
"v_mfma_f32_16x16x32_bf16 v[44:47], v[40:43], v[16:19], v[44:47]\n"
"v_exp_f32 v12, v12\n"
"v_mfma_f32_16x16x32_bf16 v[0:3], a[160:163], v[96:99], v[32:35]\n"
"ds_read_b64_tr_b16 a[204:205], v243 offset:16640\n"
"v_mfma_f32_16x16x32_bf16 v[0:3], a[164:167], v[100:103], v[0:3]\n"
"v_exp_f32 v13, v13\n"
"v_mfma_f32_16x16x32_bf16 v[0:3], a[168:171], v[104:107], v[0:3]\n"
"ds_read_b64_tr_b16 a[206:207], v243 offset:20736\n"
"v_cvt_pk_bf16_f32 v22, v12, v13\n"
"v_mfma_f32_16x16x32_bf16 v[0:3], a[172:175], v[108:111], v[0:3]\n"
"v_exp_f32 v14, v14\n"
"v_mfma_f32_16x16x32_bf16 v[4:7], a[176:179], v[96:99], v[32:35]\n"
"ds_read_b64_tr_b16 a[208:209], v242 offset:16896\n"
"v_mfma_f32_16x16x32_bf16 v[4:7], a[180:183], v[100:103], v[4:7]\n"
"v_exp_f32 v15, v15\n"
"v_mfma_f32_16x16x32_bf16 v[4:7], a[184:187], v[104:107], v[4:7]\n"
"ds_read_b64_tr_b16 a[210:211], v242 offset:20992\n"
"v_cvt_pk_bf16_f32 v23, v14, v15\n"
"v_mfma_f32_16x16x32_bf16 v[4:7], a[188:191], v[108:111], v[4:7]\n"
"buffer_load_dwordx4 v247, s[8:11], s16 offen offset:2048 lds\n"
"v_mfma_f32_16x16x32_bf16 a[32:35], a[224:227], v[20:23], a[32:35]\n"
"v_exp_f32 v0, v0\n"
"v_mfma_f32_16x16x32_bf16 a[36:39], a[228:231], v[20:23], a[36:39]\n"
"ds_read_b64_tr_b16 a[212:213], v243 offset:16896\n"
"v_mfma_f32_16x16x32_bf16 a[40:43], a[232:235], v[20:23], a[40:43]\n"
"v_exp_f32 v1, v1\n"
"v_mfma_f32_16x16x32_bf16 a[44:47], a[236:239], v[20:23], a[44:47]\n"
"ds_read_b64_tr_b16 a[214:215], v243 offset:20992\n"
"v_cvt_pk_bf16_f32 v16, v0, v1\n"
"v_mfma_f32_16x16x32_bf16 a[48:51], a[240:243], v[20:23], a[48:51]\n"
"v_exp_f32 v2, v2\n"
"v_mfma_f32_16x16x32_bf16 a[52:55], a[244:247], v[20:23], a[52:55]\n"
"ds_read_b64_tr_b16 a[216:217], v242 offset:17152\n"
"v_mfma_f32_16x16x32_bf16 a[56:59], a[248:251], v[20:23], a[56:59]\n"
"v_exp_f32 v3, v3\n"
"v_mfma_f32_16x16x32_bf16 a[60:63], a[252:255], v[20:23], a[60:63]\n"
"v_cvt_pk_bf16_f32 v17, v2, v3\n"
"v_mfma_f32_16x16x32_bf16 v[48:51], v[40:43], v[20:23], v[48:51]\n"
"v_exp_f32 v4, v4\n"
"v_mfma_f32_16x16x32_bf16 v[8:11], a[160:163], v[112:115], v[36:39]\n"
"ds_read_b64_tr_b16 a[218:219], v242 offset:21248\n"
"v_mfma_f32_16x16x32_bf16 v[8:11], a[164:167], v[116:119], v[8:11]\n"
"v_exp_f32 v5, v5\n"
"v_mfma_f32_16x16x32_bf16 v[8:11], a[168:171], v[120:123], v[8:11]\n"
"ds_read_b64_tr_b16 a[220:221], v243 offset:17152\n"
"v_cvt_pk_bf16_f32 v18, v4, v5\n"
"v_mfma_f32_16x16x32_bf16 v[8:11], a[172:175], v[124:127], v[8:11]\n"
"v_exp_f32 v6, v6\n"
"v_mfma_f32_16x16x32_bf16 v[12:15], a[176:179], v[112:115], v[36:39]\n"
"ds_read_b64_tr_b16 a[222:223], v243 offset:21248\n"
"v_mfma_f32_16x16x32_bf16 v[12:15], a[180:183], v[116:119], v[12:15]\n"
"v_exp_f32 v7, v7\n"
"v_mfma_f32_16x16x32_bf16 v[12:15], a[184:187], v[120:123], v[12:15]\n"
"v_cvt_pk_bf16_f32 v19, v6, v7\n"
"v_mfma_f32_16x16x32_bf16 v[12:15], a[188:191], v[124:127], v[12:15]\n"
"buffer_load_dwordx4 v247, s[8:11], s16 offen offset:3072 lds\n"
"v_mfma_f32_16x16x32_bf16 a[64:67], a[224:227], v[16:19], a[64:67]\n"
"v_exp_f32 v8, v8\n"
"v_mfma_f32_16x16x32_bf16 a[68:71], a[228:231], v[16:19], a[68:71]\n"
"s_nop 0\n"
"v_mfma_f32_16x16x32_bf16 a[72:75], a[232:235], v[16:19], a[72:75]\n"
"v_exp_f32 v9, v9\n"
"v_mfma_f32_16x16x32_bf16 a[76:79], a[236:239], v[16:19], a[76:79]\n"
"v_cvt_pk_bf16_f32 v20, v8, v9\n"
"v_mfma_f32_16x16x32_bf16 a[80:83], a[240:243], v[16:19], a[80:83]\n"
"v_exp_f32 v10, v10\n"
"v_mfma_f32_16x16x32_bf16 a[84:87], a[244:247], v[16:19], a[84:87]\n"
"s_nop 0\n"
"v_mfma_f32_16x16x32_bf16 a[88:91], a[248:251], v[16:19], a[88:91]\n"
"v_exp_f32 v11, v11\n"
"v_mfma_f32_16x16x32_bf16 a[92:95], a[252:255], v[16:19], a[92:95]\n"
"s_waitcnt lgkmcnt(8)\n"
"v_cvt_pk_bf16_f32 v21, v10, v11\n"
"v_mfma_f32_16x16x32_bf16 v[52:55], v[40:43], v[16:19], v[52:55]\n"
"v_exp_f32 v12, v12\n"
"v_mfma_f32_16x16x32_bf16 v[0:3], a[128:131], v[64:67], v[24:27]\n"
"ds_read_b128 a[160:163], v240 offset:8192\n"
"v_mfma_f32_16x16x32_bf16 v[0:3], a[132:135], v[68:71], v[0:3]\n"
"v_exp_f32 v13, v13\n"
"v_mfma_f32_16x16x32_bf16 v[0:3], a[136:139], v[72:75], v[0:3]\n"
"ds_read_b128 a[164:167], v240 offset:8448\n"
"v_cvt_pk_bf16_f32 v22, v12, v13\n"
"v_mfma_f32_16x16x32_bf16 v[0:3], a[140:143], v[76:79], v[0:3]\n"
"v_exp_f32 v14, v14\n"
"v_mfma_f32_16x16x32_bf16 v[4:7], a[144:147], v[64:67], v[24:27]\n"
"ds_read_b128 a[168:171], v240 offset:8704\n"
"v_mfma_f32_16x16x32_bf16 v[4:7], a[148:151], v[68:71], v[4:7]\n"
"v_exp_f32 v15, v15\n"
"v_mfma_f32_16x16x32_bf16 v[4:7], a[152:155], v[72:75], v[4:7]\n"
"s_add_u32 m0, s18, 114688\n"
"ds_read_b128 a[172:175], v240 offset:8960\n"
"v_cvt_pk_bf16_f32 v23, v14, v15\n"
"v_mfma_f32_16x16x32_bf16 v[4:7], a[156:159], v[76:79], v[4:7]\n"
"buffer_load_dwordx4 v246, s[12:15], s16 offen offset:0 lds\n"
"v_mfma_f32_16x16x32_bf16 a[96:99], a[224:227], v[20:23], a[96:99]\n"
"v_exp_f32 v0, v0\n"
"v_mfma_f32_16x16x32_bf16 a[100:103], a[228:231], v[20:23], a[100:103]\n"
"ds_read_b128 a[176:179], v240 offset:12288\n"
"v_mfma_f32_16x16x32_bf16 a[104:107], a[232:235], v[20:23], a[104:107]\n"
"v_exp_f32 v1, v1\n"
"v_mfma_f32_16x16x32_bf16 a[108:111], a[236:239], v[20:23], a[108:111]\n"
"ds_read_b128 a[180:183], v240 offset:12544\n"
"v_cvt_pk_bf16_f32 v16, v0, v1\n"
"v_mfma_f32_16x16x32_bf16 a[112:115], a[240:243], v[20:23], a[112:115]\n"
"v_exp_f32 v2, v2\n"
"v_mfma_f32_16x16x32_bf16 a[116:119], a[244:247], v[20:23], a[116:119]\n"
"ds_read_b128 a[184:187], v240 offset:12800\n"
"v_mfma_f32_16x16x32_bf16 a[120:123], a[248:251], v[20:23], a[120:123]\n"
"v_exp_f32 v3, v3\n"
"v_mfma_f32_16x16x32_bf16 a[124:127], a[252:255], v[20:23], a[124:127]\n"
"v_cvt_pk_bf16_f32 v17, v2, v3\n"
"v_mfma_f32_16x16x32_bf16 v[56:59], v[40:43], v[20:23], v[56:59]\n"
"v_exp_f32 v4, v4\n"
"v_mfma_f32_16x16x32_bf16 v[8:11], a[128:131], v[80:83], v[28:31]\n"
"ds_read_b128 a[188:191], v240 offset:13056\n"
"v_mfma_f32_16x16x32_bf16 v[8:11], a[132:135], v[84:87], v[8:11]\n"
"v_exp_f32 v5, v5\n"
"v_mfma_f32_16x16x32_bf16 v[8:11], a[136:139], v[88:91], v[8:11]\n"
"ds_read_b64_tr_b16 a[224:225], v242 offset:24576\n"
"v_cvt_pk_bf16_f32 v18, v4, v5\n"
"v_mfma_f32_16x16x32_bf16 v[8:11], a[140:143], v[92:95], v[8:11]\n"
"v_exp_f32 v6, v6\n"
"v_mfma_f32_16x16x32_bf16 v[12:15], a[144:147], v[80:83], v[28:31]\n"
"ds_read_b64_tr_b16 a[226:227], v242 offset:28672\n"
"v_mfma_f32_16x16x32_bf16 v[12:15], a[148:151], v[84:87], v[12:15]\n"
"v_exp_f32 v7, v7\n"
"v_mfma_f32_16x16x32_bf16 v[12:15], a[152:155], v[88:91], v[12:15]\n"
"ds_read_b64_tr_b16 a[228:229], v243 offset:24576\n"
"v_cvt_pk_bf16_f32 v19, v6, v7\n"
"v_mfma_f32_16x16x32_bf16 v[12:15], a[156:159], v[92:95], v[12:15]\n"
"s_waitcnt lgkmcnt(11)\n"
"buffer_load_dwordx4 v247, s[12:15], s16 offen offset:1024 lds\n"
"v_mfma_f32_16x16x32_bf16 a[0:3], a[192:195], v[16:19], a[0:3]\n"
"v_exp_f32 v8, v8\n"
"v_mfma_f32_16x16x32_bf16 a[4:7], a[196:199], v[16:19], a[4:7]\n"
"ds_read_b64_tr_b16 a[230:231], v243 offset:28672\n"
"v_mfma_f32_16x16x32_bf16 a[8:11], a[200:203], v[16:19], a[8:11]\n"
"v_exp_f32 v9, v9\n"
"v_mfma_f32_16x16x32_bf16 a[12:15], a[204:207], v[16:19], a[12:15]\n"
"ds_read_b64_tr_b16 a[232:233], v242 offset:24832\n"
"v_cvt_pk_bf16_f32 v20, v8, v9\n"
"v_mfma_f32_16x16x32_bf16 a[16:19], a[208:211], v[16:19], a[16:19]\n"
"v_exp_f32 v10, v10\n"
"v_mfma_f32_16x16x32_bf16 a[20:23], a[212:215], v[16:19], a[20:23]\n"
"ds_read_b64_tr_b16 a[234:235], v242 offset:28928\n"
"v_mfma_f32_16x16x32_bf16 a[24:27], a[216:219], v[16:19], a[24:27]\n"
"v_exp_f32 v11, v11\n"
"v_mfma_f32_16x16x32_bf16 a[28:31], a[220:223], v[16:19], a[28:31]\n"
"v_cvt_pk_bf16_f32 v21, v10, v11\n"
"v_mfma_f32_16x16x32_bf16 v[44:47], v[40:43], v[16:19], v[44:47]\n"
"v_exp_f32 v12, v12\n"
"v_mfma_f32_16x16x32_bf16 v[0:3], a[128:131], v[96:99], v[32:35]\n"
"ds_read_b64_tr_b16 a[236:237], v243 offset:24832\n"
"v_mfma_f32_16x16x32_bf16 v[0:3], a[132:135], v[100:103], v[0:3]\n"
"v_exp_f32 v13, v13\n"
"v_mfma_f32_16x16x32_bf16 v[0:3], a[136:139], v[104:107], v[0:3]\n"
"ds_read_b64_tr_b16 a[238:239], v243 offset:28928\n"
"v_cvt_pk_bf16_f32 v22, v12, v13\n"
"v_mfma_f32_16x16x32_bf16 v[0:3], a[140:143], v[108:111], v[0:3]\n"
"v_exp_f32 v14, v14\n"
"v_mfma_f32_16x16x32_bf16 v[4:7], a[144:147], v[96:99], v[32:35]\n"
"ds_read_b64_tr_b16 a[240:241], v242 offset:25088\n"
"v_mfma_f32_16x16x32_bf16 v[4:7], a[148:151], v[100:103], v[4:7]\n"
"v_exp_f32 v15, v15\n"
"v_mfma_f32_16x16x32_bf16 v[4:7], a[152:155], v[104:107], v[4:7]\n"
"ds_read_b64_tr_b16 a[242:243], v242 offset:29184\n"
"v_cvt_pk_bf16_f32 v23, v14, v15\n"
"v_mfma_f32_16x16x32_bf16 v[4:7], a[156:159], v[108:111], v[4:7]\n"
"buffer_load_dwordx4 v246, s[12:15], s16 offen offset:2048 lds\n"
"v_mfma_f32_16x16x32_bf16 a[32:35], a[192:195], v[20:23], a[32:35]\n"
"v_exp_f32 v0, v0\n"
"v_mfma_f32_16x16x32_bf16 a[36:39], a[196:199], v[20:23], a[36:39]\n"
"ds_read_b64_tr_b16 a[244:245], v243 offset:25088\n"
"v_mfma_f32_16x16x32_bf16 a[40:43], a[200:203], v[20:23], a[40:43]\n"
"v_exp_f32 v1, v1\n"
"v_mfma_f32_16x16x32_bf16 a[44:47], a[204:207], v[20:23], a[44:47]\n"
"ds_read_b64_tr_b16 a[246:247], v243 offset:29184\n"
"v_cvt_pk_bf16_f32 v16, v0, v1\n"
"v_mfma_f32_16x16x32_bf16 a[48:51], a[208:211], v[20:23], a[48:51]\n"
"v_exp_f32 v2, v2\n"
"v_mfma_f32_16x16x32_bf16 a[52:55], a[212:215], v[20:23], a[52:55]\n"
"ds_read_b64_tr_b16 a[248:249], v242 offset:25344\n"
"v_mfma_f32_16x16x32_bf16 a[56:59], a[216:219], v[20:23], a[56:59]\n"
"v_exp_f32 v3, v3\n"
"v_mfma_f32_16x16x32_bf16 a[60:63], a[220:223], v[20:23], a[60:63]\n"
"v_cvt_pk_bf16_f32 v17, v2, v3\n"
"v_mfma_f32_16x16x32_bf16 v[48:51], v[40:43], v[20:23], v[48:51]\n"
"v_exp_f32 v4, v4\n"
"v_mfma_f32_16x16x32_bf16 v[8:11], a[128:131], v[112:115], v[36:39]\n"
"ds_read_b64_tr_b16 a[250:251], v242 offset:29440\n"
"v_mfma_f32_16x16x32_bf16 v[8:11], a[132:135], v[116:119], v[8:11]\n"
"v_exp_f32 v5, v5\n"
"v_mfma_f32_16x16x32_bf16 v[8:11], a[136:139], v[120:123], v[8:11]\n"
"ds_read_b64_tr_b16 a[252:253], v243 offset:25344\n"
"v_cvt_pk_bf16_f32 v18, v4, v5\n"
"v_mfma_f32_16x16x32_bf16 v[8:11], a[140:143], v[124:127], v[8:11]\n"
"v_exp_f32 v6, v6\n"
"v_mfma_f32_16x16x32_bf16 v[12:15], a[144:147], v[112:115], v[36:39]\n"
"ds_read_b64_tr_b16 a[254:255], v243 offset:29440\n"
"v_mfma_f32_16x16x32_bf16 v[12:15], a[148:151], v[116:119], v[12:15]\n"
"v_exp_f32 v7, v7\n"
"v_mfma_f32_16x16x32_bf16 v[12:15], a[152:155], v[120:123], v[12:15]\n"
"v_cvt_pk_bf16_f32 v19, v6, v7\n"
"v_mfma_f32_16x16x32_bf16 v[12:15], a[156:159], v[124:127], v[12:15]\n"
"buffer_load_dwordx4 v247, s[12:15], s16 offen offset:3072 lds\n"
"v_mfma_f32_16x16x32_bf16 a[64:67], a[192:195], v[16:19], a[64:67]\n"
"s_add_u32 s16, s16, 16384\n"
"v_exp_f32 v8, v8\n"
"v_mfma_f32_16x16x32_bf16 a[68:71], a[196:199], v[16:19], a[68:71]\n"
"s_nop 0\n"
"v_mfma_f32_16x16x32_bf16 a[72:75], a[200:203], v[16:19], a[72:75]\n"
"v_exp_f32 v9, v9\n"
"v_mfma_f32_16x16x32_bf16 a[76:79], a[204:207], v[16:19], a[76:79]\n"
"v_cvt_pk_bf16_f32 v20, v8, v9\n"
"v_mfma_f32_16x16x32_bf16 a[80:83], a[208:211], v[16:19], a[80:83]\n"
"v_exp_f32 v10, v10\n"
"v_mfma_f32_16x16x32_bf16 a[84:87], a[212:215], v[16:19], a[84:87]\n"
"s_nop 0\n"
"v_mfma_f32_16x16x32_bf16 a[88:91], a[216:219], v[16:19], a[88:91]\n"
"v_exp_f32 v11, v11\n"
"v_mfma_f32_16x16x32_bf16 a[92:95], a[220:223], v[16:19], a[92:95]\n"
"s_waitcnt vmcnt(16) lgkmcnt(0)\n"
"s_barrier\n"
"v_cvt_pk_bf16_f32 v21, v10, v11\n"
"v_mfma_f32_16x16x32_bf16 v[52:55], v[40:43], v[16:19], v[52:55]\n"
"v_exp_f32 v12, v12\n"
"v_mfma_f32_16x16x32_bf16 v[0:3], a[160:163], v[64:67], v[24:27]\n"
"ds_read_b128 a[128:131], v240 offset:32768\n"
"v_mfma_f32_16x16x32_bf16 v[0:3], a[164:167], v[68:71], v[0:3]\n"
"v_exp_f32 v13, v13\n"
"v_mfma_f32_16x16x32_bf16 v[0:3], a[168:171], v[72:75], v[0:3]\n"
"ds_read_b128 a[132:135], v240 offset:33024\n"
"v_cvt_pk_bf16_f32 v22, v12, v13\n"
"v_mfma_f32_16x16x32_bf16 v[0:3], a[172:175], v[76:79], v[0:3]\n"
"v_exp_f32 v14, v14\n"
"v_mfma_f32_16x16x32_bf16 v[4:7], a[176:179], v[64:67], v[24:27]\n"
"ds_read_b128 a[136:139], v240 offset:33280\n"
"v_mfma_f32_16x16x32_bf16 v[4:7], a[180:183], v[68:71], v[4:7]\n"
"v_exp_f32 v15, v15\n"
"v_mfma_f32_16x16x32_bf16 v[4:7], a[184:187], v[72:75], v[4:7]\n"
"s_mov_b32 m0, s18\n"
"ds_read_b128 a[140:143], v240 offset:33536\n"
"v_cvt_pk_bf16_f32 v23, v14, v15\n"
"v_mfma_f32_16x16x32_bf16 v[4:7], a[188:191], v[76:79], v[4:7]\n"
"buffer_load_dwordx4 v246, s[8:11], s16 offen offset:0 lds\n"
"v_mfma_f32_16x16x32_bf16 a[96:99], a[192:195], v[20:23], a[96:99]\n"
"v_exp_f32 v0, v0\n"
"v_mfma_f32_16x16x32_bf16 a[100:103], a[196:199], v[20:23], a[100:103]\n"
"ds_read_b128 a[144:147], v240 offset:36864\n"
"v_mfma_f32_16x16x32_bf16 a[104:107], a[200:203], v[20:23], a[104:107]\n"
"v_exp_f32 v1, v1\n"
"v_mfma_f32_16x16x32_bf16 a[108:111], a[204:207], v[20:23], a[108:111]\n"
"ds_read_b128 a[148:151], v240 offset:37120\n"
"v_cvt_pk_bf16_f32 v16, v0, v1\n"
"v_mfma_f32_16x16x32_bf16 a[112:115], a[208:211], v[20:23], a[112:115]\n"
"v_exp_f32 v2, v2\n"
"v_mfma_f32_16x16x32_bf16 a[116:119], a[212:215], v[20:23], a[116:119]\n"
"ds_read_b128 a[152:155], v240 offset:37376\n"
"v_mfma_f32_16x16x32_bf16 a[120:123], a[216:219], v[20:23], a[120:123]\n"
"v_exp_f32 v3, v3\n"
"v_mfma_f32_16x16x32_bf16 a[124:127], a[220:223], v[20:23], a[124:127]\n"
"v_cvt_pk_bf16_f32 v17, v2, v3\n"
"v_mfma_f32_16x16x32_bf16 v[56:59], v[40:43], v[20:23], v[56:59]\n"
"v_exp_f32 v4, v4\n"
"v_mfma_f32_16x16x32_bf16 v[8:11], a[160:163], v[80:83], v[28:31]\n"
"ds_read_b128 a[156:159], v240 offset:37632\n"
"v_mfma_f32_16x16x32_bf16 v[8:11], a[164:167], v[84:87], v[8:11]\n"
"v_exp_f32 v5, v5\n"
"v_mfma_f32_16x16x32_bf16 v[8:11], a[168:171], v[88:91], v[8:11]\n"
"ds_read_b64_tr_b16 a[192:193], v242 offset:49152\n"
"v_cvt_pk_bf16_f32 v18, v4, v5\n"
"v_mfma_f32_16x16x32_bf16 v[8:11], a[172:175], v[92:95], v[8:11]\n"
"v_exp_f32 v6, v6\n"
"v_mfma_f32_16x16x32_bf16 v[12:15], a[176:179], v[80:83], v[28:31]\n"
"ds_read_b64_tr_b16 a[194:195], v242 offset:53248\n"
"v_mfma_f32_16x16x32_bf16 v[12:15], a[180:183], v[84:87], v[12:15]\n"
"v_exp_f32 v7, v7\n"
"v_mfma_f32_16x16x32_bf16 v[12:15], a[184:187], v[88:91], v[12:15]\n"
"ds_read_b64_tr_b16 a[196:197], v243 offset:49152\n"
"v_cvt_pk_bf16_f32 v19, v6, v7\n"
"v_mfma_f32_16x16x32_bf16 v[12:15], a[188:191], v[92:95], v[12:15]\n"
"s_waitcnt lgkmcnt(11)\n"
"buffer_load_dwordx4 v246, s[8:11], s16 offen offset:1024 lds\n"
"v_mfma_f32_16x16x32_bf16 a[0:3], a[224:227], v[16:19], a[0:3]\n"
"v_exp_f32 v8, v8\n"
"v_mfma_f32_16x16x32_bf16 a[4:7], a[228:231], v[16:19], a[4:7]\n"
"ds_read_b64_tr_b16 a[198:199], v243 offset:53248\n"
"v_mfma_f32_16x16x32_bf16 a[8:11], a[232:235], v[16:19], a[8:11]\n"
"v_exp_f32 v9, v9\n"
"v_mfma_f32_16x16x32_bf16 a[12:15], a[236:239], v[16:19], a[12:15]\n"
"ds_read_b64_tr_b16 a[200:201], v242 offset:49408\n"
"v_cvt_pk_bf16_f32 v20, v8, v9\n"
"v_mfma_f32_16x16x32_bf16 a[16:19], a[240:243], v[16:19], a[16:19]\n"
"v_exp_f32 v10, v10\n"
"v_mfma_f32_16x16x32_bf16 a[20:23], a[244:247], v[16:19], a[20:23]\n"
"ds_read_b64_tr_b16 a[202:203], v242 offset:53504\n"
"v_mfma_f32_16x16x32_bf16 a[24:27], a[248:251], v[16:19], a[24:27]\n"
"v_exp_f32 v11, v11\n"
"v_mfma_f32_16x16x32_bf16 a[28:31], a[252:255], v[16:19], a[28:31]\n"
"v_cvt_pk_bf16_f32 v21, v10, v11\n"
"v_mfma_f32_16x16x32_bf16 v[44:47], v[40:43], v[16:19], v[44:47]\n"
"v_exp_f32 v12, v12\n"
"v_mfma_f32_16x16x32_bf16 v[0:3], a[160:163], v[96:99], v[32:35]\n"
"ds_read_b64_tr_b16 a[204:205], v243 offset:49408\n"
"v_mfma_f32_16x16x32_bf16 v[0:3], a[164:167], v[100:103], v[0:3]\n"
"v_exp_f32 v13, v13\n"
"v_mfma_f32_16x16x32_bf16 v[0:3], a[168:171], v[104:107], v[0:3]\n"
"ds_read_b64_tr_b16 a[206:207], v243 offset:53504\n"
"v_cvt_pk_bf16_f32 v22, v12, v13\n"
"v_mfma_f32_16x16x32_bf16 v[0:3], a[172:175], v[108:111], v[0:3]\n"
"v_exp_f32 v14, v14\n"
"v_mfma_f32_16x16x32_bf16 v[4:7], a[176:179], v[96:99], v[32:35]\n"
"ds_read_b64_tr_b16 a[208:209], v242 offset:49664\n"
"v_mfma_f32_16x16x32_bf16 v[4:7], a[180:183], v[100:103], v[4:7]\n"
"v_exp_f32 v15, v15\n"
"v_mfma_f32_16x16x32_bf16 v[4:7], a[184:187], v[104:107], v[4:7]\n"
"ds_read_b64_tr_b16 a[210:211], v242 offset:53760\n"
"v_cvt_pk_bf16_f32 v23, v14, v15\n"
"v_mfma_f32_16x16x32_bf16 v[4:7], a[188:191], v[108:111], v[4:7]\n"
"buffer_load_dwordx4 v247, s[8:11], s16 offen offset:2048 lds\n"
"v_mfma_f32_16x16x32_bf16 a[32:35], a[224:227], v[20:23], a[32:35]\n"
"v_exp_f32 v0, v0\n"
"v_mfma_f32_16x16x32_bf16 a[36:39], a[228:231], v[20:23], a[36:39]\n"
"ds_read_b64_tr_b16 a[212:213], v243 offset:49664\n"
"v_mfma_f32_16x16x32_bf16 a[40:43], a[232:235], v[20:23], a[40:43]\n"
"v_exp_f32 v1, v1\n"
"v_mfma_f32_16x16x32_bf16 a[44:47], a[236:239], v[20:23], a[44:47]\n"
"ds_read_b64_tr_b16 a[214:215], v243 offset:53760\n"
"v_cvt_pk_bf16_f32 v16, v0, v1\n"
"v_mfma_f32_16x16x32_bf16 a[48:51], a[240:243], v[20:23], a[48:51]\n"
"v_exp_f32 v2, v2\n"
"v_mfma_f32_16x16x32_bf16 a[52:55], a[244:247], v[20:23], a[52:55]\n"
"ds_read_b64_tr_b16 a[216:217], v242 offset:49920\n"
"v_mfma_f32_16x16x32_bf16 a[56:59], a[248:251], v[20:23], a[56:59]\n"
"v_exp_f32 v3, v3\n"
"v_mfma_f32_16x16x32_bf16 a[60:63], a[252:255], v[20:23], a[60:63]\n"
"v_cvt_pk_bf16_f32 v17, v2, v3\n"
"v_mfma_f32_16x16x32_bf16 v[48:51], v[40:43], v[20:23], v[48:51]\n"
"v_exp_f32 v4, v4\n"
"v_mfma_f32_16x16x32_bf16 v[8:11], a[160:163], v[112:115], v[36:39]\n"
"ds_read_b64_tr_b16 a[218:219], v242 offset:54016\n"
"v_mfma_f32_16x16x32_bf16 v[8:11], a[164:167], v[116:119], v[8:11]\n"
"v_exp_f32 v5, v5\n"
"v_mfma_f32_16x16x32_bf16 v[8:11], a[168:171], v[120:123], v[8:11]\n"
"ds_read_b64_tr_b16 a[220:221], v243 offset:49920\n"
"v_cvt_pk_bf16_f32 v18, v4, v5\n"
"v_mfma_f32_16x16x32_bf16 v[8:11], a[172:175], v[124:127], v[8:11]\n"
"v_exp_f32 v6, v6\n"
"v_mfma_f32_16x16x32_bf16 v[12:15], a[176:179], v[112:115], v[36:39]\n"
"ds_read_b64_tr_b16 a[222:223], v243 offset:54016\n"
"v_mfma_f32_16x16x32_bf16 v[12:15], a[180:183], v[116:119], v[12:15]\n"
"v_exp_f32 v7, v7\n"
"v_mfma_f32_16x16x32_bf16 v[12:15], a[184:187], v[120:123], v[12:15]\n"
"v_cvt_pk_bf16_f32 v19, v6, v7\n"
"v_mfma_f32_16x16x32_bf16 v[12:15], a[188:191], v[124:127], v[12:15]\n"
"buffer_load_dwordx4 v247, s[8:11], s16 offen offset:3072 lds\n"
"v_mfma_f32_16x16x32_bf16 a[64:67], a[224:227], v[16:19], a[64:67]\n"
"v_exp_f32 v8, v8\n"
"v_mfma_f32_16x16x32_bf16 a[68:71], a[228:231], v[16:19], a[68:71]\n"
"s_nop 0\n"
"v_mfma_f32_16x16x32_bf16 a[72:75], a[232:235], v[16:19], a[72:75]\n"
"v_exp_f32 v9, v9\n"
"v_mfma_f32_16x16x32_bf16 a[76:79], a[236:239], v[16:19], a[76:79]\n"
"v_cvt_pk_bf16_f32 v20, v8, v9\n"
"v_mfma_f32_16x16x32_bf16 a[80:83], a[240:243], v[16:19], a[80:83]\n"
"v_exp_f32 v10, v10\n"
"v_mfma_f32_16x16x32_bf16 a[84:87], a[244:247], v[16:19], a[84:87]\n"
"s_nop 0\n"
"v_mfma_f32_16x16x32_bf16 a[88:91], a[248:251], v[16:19], a[88:91]\n"
"v_exp_f32 v11, v11\n"
"v_mfma_f32_16x16x32_bf16 a[92:95], a[252:255], v[16:19], a[92:95]\n"
"s_waitcnt lgkmcnt(8)\n"
"v_cvt_pk_bf16_f32 v21, v10, v11\n"
"v_mfma_f32_16x16x32_bf16 v[52:55], v[40:43], v[16:19], v[52:55]\n"
"v_exp_f32 v12, v12\n"
"v_mfma_f32_16x16x32_bf16 v[0:3], a[128:131], v[64:67], v[24:27]\n"
"ds_read_b128 a[160:163], v240 offset:40960\n"
"v_mfma_f32_16x16x32_bf16 v[0:3], a[132:135], v[68:71], v[0:3]\n"
"v_exp_f32 v13, v13\n"
"v_mfma_f32_16x16x32_bf16 v[0:3], a[136:139], v[72:75], v[0:3]\n"
"ds_read_b128 a[164:167], v240 offset:41216\n"
"v_cvt_pk_bf16_f32 v22, v12, v13\n"
"v_mfma_f32_16x16x32_bf16 v[0:3], a[140:143], v[76:79], v[0:3]\n"
"v_exp_f32 v14, v14\n"
"v_mfma_f32_16x16x32_bf16 v[4:7], a[144:147], v[64:67], v[24:27]\n"
"ds_read_b128 a[168:171], v240 offset:41472\n"
"v_mfma_f32_16x16x32_bf16 v[4:7], a[148:151], v[68:71], v[4:7]\n"
"v_exp_f32 v15, v15\n"
"v_mfma_f32_16x16x32_bf16 v[4:7], a[152:155], v[72:75], v[4:7]\n"
"s_add_u32 m0, s18, 16384\n"
"ds_read_b128 a[172:175], v240 offset:41728\n"
"v_cvt_pk_bf16_f32 v23, v14, v15\n"
"v_mfma_f32_16x16x32_bf16 v[4:7], a[156:159], v[76:79], v[4:7]\n"
"buffer_load_dwordx4 v246, s[12:15], s16 offen offset:0 lds\n"
"v_mfma_f32_16x16x32_bf16 a[96:99], a[224:227], v[20:23], a[96:99]\n"
"v_exp_f32 v0, v0\n"
"v_mfma_f32_16x16x32_bf16 a[100:103], a[228:231], v[20:23], a[100:103]\n"
"ds_read_b128 a[176:179], v240 offset:45056\n"
"v_mfma_f32_16x16x32_bf16 a[104:107], a[232:235], v[20:23], a[104:107]\n"
"v_exp_f32 v1, v1\n"
"v_mfma_f32_16x16x32_bf16 a[108:111], a[236:239], v[20:23], a[108:111]\n"
"ds_read_b128 a[180:183], v240 offset:45312\n"
"v_cvt_pk_bf16_f32 v16, v0, v1\n"
"v_mfma_f32_16x16x32_bf16 a[112:115], a[240:243], v[20:23], a[112:115]\n"
"v_exp_f32 v2, v2\n"
"v_mfma_f32_16x16x32_bf16 a[116:119], a[244:247], v[20:23], a[116:119]\n"
"ds_read_b128 a[184:187], v240 offset:45568\n"
"v_mfma_f32_16x16x32_bf16 a[120:123], a[248:251], v[20:23], a[120:123]\n"
"v_exp_f32 v3, v3\n"
"v_mfma_f32_16x16x32_bf16 a[124:127], a[252:255], v[20:23], a[124:127]\n"
"v_cvt_pk_bf16_f32 v17, v2, v3\n"
"v_mfma_f32_16x16x32_bf16 v[56:59], v[40:43], v[20:23], v[56:59]\n"
"v_exp_f32 v4, v4\n"
"v_mfma_f32_16x16x32_bf16 v[8:11], a[128:131], v[80:83], v[28:31]\n"
"ds_read_b128 a[188:191], v240 offset:45824\n"
"v_mfma_f32_16x16x32_bf16 v[8:11], a[132:135], v[84:87], v[8:11]\n"
"v_exp_f32 v5, v5\n"
"v_mfma_f32_16x16x32_bf16 v[8:11], a[136:139], v[88:91], v[8:11]\n"
"ds_read_b64_tr_b16 a[224:225], v242 offset:57344\n"
"v_cvt_pk_bf16_f32 v18, v4, v5\n"
"v_mfma_f32_16x16x32_bf16 v[8:11], a[140:143], v[92:95], v[8:11]\n"
"v_exp_f32 v6, v6\n"
"v_mfma_f32_16x16x32_bf16 v[12:15], a[144:147], v[80:83], v[28:31]\n"
"ds_read_b64_tr_b16 a[226:227], v242 offset:61440\n"
"v_mfma_f32_16x16x32_bf16 v[12:15], a[148:151], v[84:87], v[12:15]\n"
"v_exp_f32 v7, v7\n"
"v_mfma_f32_16x16x32_bf16 v[12:15], a[152:155], v[88:91], v[12:15]\n"
"ds_read_b64_tr_b16 a[228:229], v243 offset:57344\n"
"v_cvt_pk_bf16_f32 v19, v6, v7\n"
"v_mfma_f32_16x16x32_bf16 v[12:15], a[156:159], v[92:95], v[12:15]\n"
"s_waitcnt lgkmcnt(11)\n"
"buffer_load_dwordx4 v247, s[12:15], s16 offen offset:1024 lds\n"
"v_mfma_f32_16x16x32_bf16 a[0:3], a[192:195], v[16:19], a[0:3]\n"
"v_exp_f32 v8, v8\n"
"v_mfma_f32_16x16x32_bf16 a[4:7], a[196:199], v[16:19], a[4:7]\n"
"ds_read_b64_tr_b16 a[230:231], v243 offset:61440\n"
"v_mfma_f32_16x16x32_bf16 a[8:11], a[200:203], v[16:19], a[8:11]\n"
"v_exp_f32 v9, v9\n"
"v_mfma_f32_16x16x32_bf16 a[12:15], a[204:207], v[16:19], a[12:15]\n"
"ds_read_b64_tr_b16 a[232:233], v242 offset:57600\n"
"v_cvt_pk_bf16_f32 v20, v8, v9\n"
"v_mfma_f32_16x16x32_bf16 a[16:19], a[208:211], v[16:19], a[16:19]\n"
"v_exp_f32 v10, v10\n"
"v_mfma_f32_16x16x32_bf16 a[20:23], a[212:215], v[16:19], a[20:23]\n"
"ds_read_b64_tr_b16 a[234:235], v242 offset:61696\n"
"v_mfma_f32_16x16x32_bf16 a[24:27], a[216:219], v[16:19], a[24:27]\n"
"v_exp_f32 v11, v11\n"
"v_mfma_f32_16x16x32_bf16 a[28:31], a[220:223], v[16:19], a[28:31]\n"
"v_cvt_pk_bf16_f32 v21, v10, v11\n"
"v_mfma_f32_16x16x32_bf16 v[44:47], v[40:43], v[16:19], v[44:47]\n"
"v_exp_f32 v12, v12\n"
"v_mfma_f32_16x16x32_bf16 v[0:3], a[128:131], v[96:99], v[32:35]\n"
"ds_read_b64_tr_b16 a[236:237], v243 offset:57600\n"
"v_mfma_f32_16x16x32_bf16 v[0:3], a[132:135], v[100:103], v[0:3]\n"
"v_exp_f32 v13, v13\n"
"v_mfma_f32_16x16x32_bf16 v[0:3], a[136:139], v[104:107], v[0:3]\n"
"ds_read_b64_tr_b16 a[238:239], v243 offset:61696\n"
"v_cvt_pk_bf16_f32 v22, v12, v13\n"
"v_mfma_f32_16x16x32_bf16 v[0:3], a[140:143], v[108:111], v[0:3]\n"
"v_exp_f32 v14, v14\n"
"v_mfma_f32_16x16x32_bf16 v[4:7], a[144:147], v[96:99], v[32:35]\n"
"ds_read_b64_tr_b16 a[240:241], v242 offset:57856\n"
"v_mfma_f32_16x16x32_bf16 v[4:7], a[148:151], v[100:103], v[4:7]\n"
"v_exp_f32 v15, v15\n"
"v_mfma_f32_16x16x32_bf16 v[4:7], a[152:155], v[104:107], v[4:7]\n"
"ds_read_b64_tr_b16 a[242:243], v242 offset:61952\n"
"v_cvt_pk_bf16_f32 v23, v14, v15\n"
"v_mfma_f32_16x16x32_bf16 v[4:7], a[156:159], v[108:111], v[4:7]\n"
"buffer_load_dwordx4 v246, s[12:15], s16 offen offset:2048 lds\n"
"v_mfma_f32_16x16x32_bf16 a[32:35], a[192:195], v[20:23], a[32:35]\n"
"v_exp_f32 v0, v0\n"
"v_mfma_f32_16x16x32_bf16 a[36:39], a[196:199], v[20:23], a[36:39]\n"
"ds_read_b64_tr_b16 a[244:245], v243 offset:57856\n"
"v_mfma_f32_16x16x32_bf16 a[40:43], a[200:203], v[20:23], a[40:43]\n"
"v_exp_f32 v1, v1\n"
"v_mfma_f32_16x16x32_bf16 a[44:47], a[204:207], v[20:23], a[44:47]\n"
"ds_read_b64_tr_b16 a[246:247], v243 offset:61952\n"
"v_cvt_pk_bf16_f32 v16, v0, v1\n"
"v_mfma_f32_16x16x32_bf16 a[48:51], a[208:211], v[20:23], a[48:51]\n"
"v_exp_f32 v2, v2\n"
"v_mfma_f32_16x16x32_bf16 a[52:55], a[212:215], v[20:23], a[52:55]\n"
"ds_read_b64_tr_b16 a[248:249], v242 offset:58112\n"
"v_mfma_f32_16x16x32_bf16 a[56:59], a[216:219], v[20:23], a[56:59]\n"
"v_exp_f32 v3, v3\n"
"v_mfma_f32_16x16x32_bf16 a[60:63], a[220:223], v[20:23], a[60:63]\n"
"v_cvt_pk_bf16_f32 v17, v2, v3\n"
"v_mfma_f32_16x16x32_bf16 v[48:51], v[40:43], v[20:23], v[48:51]\n"
"v_exp_f32 v4, v4\n"
"v_mfma_f32_16x16x32_bf16 v[8:11], a[128:131], v[112:115], v[36:39]\n"
"ds_read_b64_tr_b16 a[250:251], v242 offset:62208\n"
"v_mfma_f32_16x16x32_bf16 v[8:11], a[132:135], v[116:119], v[8:11]\n"
"v_exp_f32 v5, v5\n"
"v_mfma_f32_16x16x32_bf16 v[8:11], a[136:139], v[120:123], v[8:11]\n"
"ds_read_b64_tr_b16 a[252:253], v243 offset:58112\n"
"v_cvt_pk_bf16_f32 v18, v4, v5\n"
"v_mfma_f32_16x16x32_bf16 v[8:11], a[140:143], v[124:127], v[8:11]\n"
"v_exp_f32 v6, v6\n"
"v_mfma_f32_16x16x32_bf16 v[12:15], a[144:147], v[112:115], v[36:39]\n"
"ds_read_b64_tr_b16 a[254:255], v243 offset:62208\n"
"v_mfma_f32_16x16x32_bf16 v[12:15], a[148:151], v[116:119], v[12:15]\n"
"v_exp_f32 v7, v7\n"
"v_mfma_f32_16x16x32_bf16 v[12:15], a[152:155], v[120:123], v[12:15]\n"
"v_cvt_pk_bf16_f32 v19, v6, v7\n"
"v_mfma_f32_16x16x32_bf16 v[12:15], a[156:159], v[124:127], v[12:15]\n"
"buffer_load_dwordx4 v247, s[12:15], s16 offen offset:3072 lds\n"
"v_mfma_f32_16x16x32_bf16 a[64:67], a[192:195], v[16:19], a[64:67]\n"
"s_add_u32 s16, s16, 16384\n"
"v_exp_f32 v8, v8\n"
"v_mfma_f32_16x16x32_bf16 a[68:71], a[196:199], v[16:19], a[68:71]\n"
"s_nop 0\n"
"v_mfma_f32_16x16x32_bf16 a[72:75], a[200:203], v[16:19], a[72:75]\n"
"v_exp_f32 v9, v9\n"
"v_mfma_f32_16x16x32_bf16 a[76:79], a[204:207], v[16:19], a[76:79]\n"
"v_cvt_pk_bf16_f32 v20, v8, v9\n"
"v_mfma_f32_16x16x32_bf16 a[80:83], a[208:211], v[16:19], a[80:83]\n"
"s_sub_u32 s19, s19, 1\n"
"v_exp_f32 v10, v10\n"
"v_mfma_f32_16x16x32_bf16 a[84:87], a[212:215], v[16:19], a[84:87]\n"
"s_cmp_lg_u32 s19, 0\n"
"v_mfma_f32_16x16x32_bf16 a[88:91], a[216:219], v[16:19], a[88:91]\n"
"v_exp_f32 v11, v11\n"
"v_mfma_f32_16x16x32_bf16 a[92:95], a[220:223], v[16:19], a[92:95]\n"
"s_waitcnt vmcnt(16) lgkmcnt(0)\n"
"s_barrier\n"
"v_cvt_pk_bf16_f32 v21, v10, v11\n"
"v_mfma_f32_16x16x32_bf16 v[52:55], v[40:43], v[16:19], v[52:55]\n"
"v_exp_f32 v12, v12\n"
"s_cbranch_scc1 AT_loop\n"
"v_mfma_f32_16x16x32_bf16 v[0:3], a[160:163], v[64:67], v[24:27]\n"
"ds_read_b128 a[128:131], v241 offset:0\n"
"v_mfma_f32_16x16x32_bf16 v[0:3], a[164:167], v[68:71], v[0:3]\n"
"v_exp_f32 v13, v13\n"
"v_mfma_f32_16x16x32_bf16 v[0:3], a[168:171], v[72:75], v[0:3]\n"
"ds_read_b128 a[132:135], v241 offset:256\n"
"v_cvt_pk_bf16_f32 v22, v12, v13\n"
"v_mfma_f32_16x16x32_bf16 v[0:3], a[172:175], v[76:79], v[0:3]\n"
"v_exp_f32 v14, v14\n"
"v_mfma_f32_16x16x32_bf16 v[4:7], a[176:179], v[64:67], v[24:27]\n"
"ds_read_b128 a[136:139], v241 offset:512\n"
"v_mfma_f32_16x16x32_bf16 v[4:7], a[180:183], v[68:71], v[4:7]\n"
"v_exp_f32 v15, v15\n"
"v_mfma_f32_16x16x32_bf16 v[4:7], a[184:187], v[72:75], v[4:7]\n"
"s_add_u32 m0, s18, 32768\n"
"ds_read_b128 a[140:143], v241 offset:768\n"
"v_cvt_pk_bf16_f32 v23, v14, v15\n"
"v_mfma_f32_16x16x32_bf16 v[4:7], a[188:191], v[76:79], v[4:7]\n"
"buffer_load_dwordx4 v246, s[8:11], s16 offen offset:0 lds\n"
"v_mfma_f32_16x16x32_bf16 a[96:99], a[192:195], v[20:23], a[96:99]\n"
"v_exp_f32 v0, v0\n"
"v_mfma_f32_16x16x32_bf16 a[100:103], a[196:199], v[20:23], a[100:103]\n"
"ds_read_b128 a[144:147], v241 offset:4096\n"
"v_mfma_f32_16x16x32_bf16 a[104:107], a[200:203], v[20:23], a[104:107]\n"
"v_exp_f32 v1, v1\n"
"v_mfma_f32_16x16x32_bf16 a[108:111], a[204:207], v[20:23], a[108:111]\n"
"ds_read_b128 a[148:151], v241 offset:4352\n"
"v_cvt_pk_bf16_f32 v16, v0, v1\n"
"v_mfma_f32_16x16x32_bf16 a[112:115], a[208:211], v[20:23], a[112:115]\n"
"v_exp_f32 v2, v2\n"
"v_mfma_f32_16x16x32_bf16 a[116:119], a[212:215], v[20:23], a[116:119]\n"
"ds_read_b128 a[152:155], v241 offset:4608\n"
"v_mfma_f32_16x16x32_bf16 a[120:123], a[216:219], v[20:23], a[120:123]\n"
"v_exp_f32 v3, v3\n"
"v_mfma_f32_16x16x32_bf16 a[124:127], a[220:223], v[20:23], a[124:127]\n"
"v_cvt_pk_bf16_f32 v17, v2, v3\n"
"v_mfma_f32_16x16x32_bf16 v[56:59], v[40:43], v[20:23], v[56:59]\n"
"v_exp_f32 v4, v4\n"
"v_mfma_f32_16x16x32_bf16 v[8:11], a[160:163], v[80:83], v[28:31]\n"
"ds_read_b128 a[156:159], v241 offset:4864\n"
"v_mfma_f32_16x16x32_bf16 v[8:11], a[164:167], v[84:87], v[8:11]\n"
"v_exp_f32 v5, v5\n"
"v_mfma_f32_16x16x32_bf16 v[8:11], a[168:171], v[88:91], v[8:11]\n"
"ds_read_b64_tr_b16 a[192:193], v244 offset:16384\n"
"v_cvt_pk_bf16_f32 v18, v4, v5\n"
"v_mfma_f32_16x16x32_bf16 v[8:11], a[172:175], v[92:95], v[8:11]\n"
"v_exp_f32 v6, v6\n"
"v_mfma_f32_16x16x32_bf16 v[12:15], a[176:179], v[80:83], v[28:31]\n"
"ds_read_b64_tr_b16 a[194:195], v244 offset:20480\n"
"v_mfma_f32_16x16x32_bf16 v[12:15], a[180:183], v[84:87], v[12:15]\n"
"v_exp_f32 v7, v7\n"
"v_mfma_f32_16x16x32_bf16 v[12:15], a[184:187], v[88:91], v[12:15]\n"
"ds_read_b64_tr_b16 a[196:197], v245 offset:16384\n"
"v_cvt_pk_bf16_f32 v19, v6, v7\n"
"v_mfma_f32_16x16x32_bf16 v[12:15], a[188:191], v[92:95], v[12:15]\n"
"s_waitcnt lgkmcnt(11)\n"
"buffer_load_dwordx4 v246, s[8:11], s16 offen offset:1024 lds\n"
"v_mfma_f32_16x16x32_bf16 a[0:3], a[224:227], v[16:19], a[0:3]\n"
"v_exp_f32 v8, v8\n"
"v_mfma_f32_16x16x32_bf16 a[4:7], a[228:231], v[16:19], a[4:7]\n"
"ds_read_b64_tr_b16 a[198:199], v245 offset:20480\n"
"v_mfma_f32_16x16x32_bf16 a[8:11], a[232:235], v[16:19], a[8:11]\n"
"v_exp_f32 v9, v9\n"
"v_mfma_f32_16x16x32_bf16 a[12:15], a[236:239], v[16:19], a[12:15]\n"
"ds_read_b64_tr_b16 a[200:201], v244 offset:16640\n"
"v_cvt_pk_bf16_f32 v20, v8, v9\n"
"v_mfma_f32_16x16x32_bf16 a[16:19], a[240:243], v[16:19], a[16:19]\n"
"v_exp_f32 v10, v10\n"
"v_mfma_f32_16x16x32_bf16 a[20:23], a[244:247], v[16:19], a[20:23]\n"
"ds_read_b64_tr_b16 a[202:203], v244 offset:20736\n"
"v_mfma_f32_16x16x32_bf16 a[24:27], a[248:251], v[16:19], a[24:27]\n"
"v_exp_f32 v11, v11\n"
"v_mfma_f32_16x16x32_bf16 a[28:31], a[252:255], v[16:19], a[28:31]\n"
"v_cvt_pk_bf16_f32 v21, v10, v11\n"
"v_mfma_f32_16x16x32_bf16 v[44:47], v[40:43], v[16:19], v[44:47]\n"
"v_exp_f32 v12, v12\n"
"v_mfma_f32_16x16x32_bf16 v[0:3], a[160:163], v[96:99], v[32:35]\n"
"ds_read_b64_tr_b16 a[204:205], v245 offset:16640\n"
"v_mfma_f32_16x16x32_bf16 v[0:3], a[164:167], v[100:103], v[0:3]\n"
"v_exp_f32 v13, v13\n"
"v_mfma_f32_16x16x32_bf16 v[0:3], a[168:171], v[104:107], v[0:3]\n"
"ds_read_b64_tr_b16 a[206:207], v245 offset:20736\n"
"v_cvt_pk_bf16_f32 v22, v12, v13\n"
"v_mfma_f32_16x16x32_bf16 v[0:3], a[172:175], v[108:111], v[0:3]\n"
"v_exp_f32 v14, v14\n"
"v_mfma_f32_16x16x32_bf16 v[4:7], a[176:179], v[96:99], v[32:35]\n"
"ds_read_b64_tr_b16 a[208:209], v244 offset:16896\n"
"v_mfma_f32_16x16x32_bf16 v[4:7], a[180:183], v[100:103], v[4:7]\n"
"v_exp_f32 v15, v15\n"
"v_mfma_f32_16x16x32_bf16 v[4:7], a[184:187], v[104:107], v[4:7]\n"
"ds_read_b64_tr_b16 a[210:211], v244 offset:20992\n"
"v_cvt_pk_bf16_f32 v23, v14, v15\n"
"v_mfma_f32_16x16x32_bf16 v[4:7], a[188:191], v[108:111], v[4:7]\n"
"buffer_load_dwordx4 v247, s[8:11], s16 offen offset:2048 lds\n"
"v_mfma_f32_16x16x32_bf16 a[32:35], a[224:227], v[20:23], a[32:35]\n"
"v_exp_f32 v0, v0\n"
"v_mfma_f32_16x16x32_bf16 a[36:39], a[228:231], v[20:23], a[36:39]\n"
"ds_read_b64_tr_b16 a[212:213], v245 offset:16896\n"
"v_mfma_f32_16x16x32_bf16 a[40:43], a[232:235], v[20:23], a[40:43]\n"
"v_exp_f32 v1, v1\n"
"v_mfma_f32_16x16x32_bf16 a[44:47], a[236:239], v[20:23], a[44:47]\n"
"ds_read_b64_tr_b16 a[214:215], v245 offset:20992\n"
"v_cvt_pk_bf16_f32 v16, v0, v1\n"
"v_mfma_f32_16x16x32_bf16 a[48:51], a[240:243], v[20:23], a[48:51]\n"
"v_exp_f32 v2, v2\n"
"v_mfma_f32_16x16x32_bf16 a[52:55], a[244:247], v[20:23], a[52:55]\n"
"ds_read_b64_tr_b16 a[216:217], v244 offset:17152\n"
"v_mfma_f32_16x16x32_bf16 a[56:59], a[248:251], v[20:23], a[56:59]\n"
"v_exp_f32 v3, v3\n"
"v_mfma_f32_16x16x32_bf16 a[60:63], a[252:255], v[20:23], a[60:63]\n"
"v_cvt_pk_bf16_f32 v17, v2, v3\n"
"v_mfma_f32_16x16x32_bf16 v[48:51], v[40:43], v[20:23], v[48:51]\n"
"v_exp_f32 v4, v4\n"
"v_mfma_f32_16x16x32_bf16 v[8:11], a[160:163], v[112:115], v[36:39]\n"
"ds_read_b64_tr_b16 a[218:219], v244 offset:21248\n"
"v_mfma_f32_16x16x32_bf16 v[8:11], a[164:167], v[116:119], v[8:11]\n"
"v_exp_f32 v5, v5\n"
"v_mfma_f32_16x16x32_bf16 v[8:11], a[168:171], v[120:123], v[8:11]\n"
"ds_read_b64_tr_b16 a[220:221], v245 offset:17152\n"
"v_cvt_pk_bf16_f32 v18, v4, v5\n"
"v_mfma_f32_16x16x32_bf16 v[8:11], a[172:175], v[124:127], v[8:11]\n"
"v_exp_f32 v6, v6\n"
"v_mfma_f32_16x16x32_bf16 v[12:15], a[176:179], v[112:115], v[36:39]\n"
"ds_read_b64_tr_b16 a[222:223], v245 offset:21248\n"
"v_mfma_f32_16x16x32_bf16 v[12:15], a[180:183], v[116:119], v[12:15]\n"
"v_exp_f32 v7, v7\n"
"v_mfma_f32_16x16x32_bf16 v[12:15], a[184:187], v[120:123], v[12:15]\n"
"v_cvt_pk_bf16_f32 v19, v6, v7\n"
"v_mfma_f32_16x16x32_bf16 v[12:15], a[188:191], v[124:127], v[12:15]\n"
"buffer_load_dwordx4 v247, s[8:11], s16 offen offset:3072 lds\n"
"v_mfma_f32_16x16x32_bf16 a[64:67], a[224:227], v[16:19], a[64:67]\n"
"v_exp_f32 v8, v8\n"
"v_mfma_f32_16x16x32_bf16 a[68:71], a[228:231], v[16:19], a[68:71]\n"
"s_nop 0\n"
"v_mfma_f32_16x16x32_bf16 a[72:75], a[232:235], v[16:19], a[72:75]\n"
"v_exp_f32 v9, v9\n"
"v_mfma_f32_16x16x32_bf16 a[76:79], a[236:239], v[16:19], a[76:79]\n"
"v_cvt_pk_bf16_f32 v20, v8, v9\n"
"v_mfma_f32_16x16x32_bf16 a[80:83], a[240:243], v[16:19], a[80:83]\n"
"v_exp_f32 v10, v10\n"
"v_mfma_f32_16x16x32_bf16 a[84:87], a[244:247], v[16:19], a[84:87]\n"
"s_nop 0\n"
"v_mfma_f32_16x16x32_bf16 a[88:91], a[248:251], v[16:19], a[88:91]\n"
"v_exp_f32 v11, v11\n"
"v_mfma_f32_16x16x32_bf16 a[92:95], a[252:255], v[16:19], a[92:95]\n"
"s_waitcnt lgkmcnt(8)\n"
"v_cvt_pk_bf16_f32 v21, v10, v11\n"
"v_mfma_f32_16x16x32_bf16 v[52:55], v[40:43], v[16:19], v[52:55]\n"
"v_exp_f32 v12, v12\n"
"v_mfma_f32_16x16x32_bf16 v[0:3], a[128:131], v[64:67], v[24:27]\n"
"ds_read_b128 a[160:163], v241 offset:8192\n"
"v_mfma_f32_16x16x32_bf16 v[0:3], a[132:135], v[68:71], v[0:3]\n"
"v_exp_f32 v13, v13\n"
"v_mfma_f32_16x16x32_bf16 v[0:3], a[136:139], v[72:75], v[0:3]\n"
"ds_read_b128 a[164:167], v241 offset:8448\n"
"v_cvt_pk_bf16_f32 v22, v12, v13\n"
"v_mfma_f32_16x16x32_bf16 v[0:3], a[140:143], v[76:79], v[0:3]\n"
"v_exp_f32 v14, v14\n"
"v_mfma_f32_16x16x32_bf16 v[4:7], a[144:147], v[64:67], v[24:27]\n"
"ds_read_b128 a[168:171], v241 offset:8704\n"
"v_mfma_f32_16x16x32_bf16 v[4:7], a[148:151], v[68:71], v[4:7]\n"
"v_exp_f32 v15, v15\n"
"v_mfma_f32_16x16x32_bf16 v[4:7], a[152:155], v[72:75], v[4:7]\n"
"s_add_u32 m0, s18, 49152\n"
"ds_read_b128 a[172:175], v241 offset:8960\n"
"v_cvt_pk_bf16_f32 v23, v14, v15\n"
"v_mfma_f32_16x16x32_bf16 v[4:7], a[156:159], v[76:79], v[4:7]\n"
"buffer_load_dwordx4 v246, s[12:15], s16 offen offset:0 lds\n"
"v_mfma_f32_16x16x32_bf16 a[96:99], a[224:227], v[20:23], a[96:99]\n"
"v_exp_f32 v0, v0\n"
"v_mfma_f32_16x16x32_bf16 a[100:103], a[228:231], v[20:23], a[100:103]\n"
"ds_read_b128 a[176:179], v241 offset:12288\n"
"v_mfma_f32_16x16x32_bf16 a[104:107], a[232:235], v[20:23], a[104:107]\n"
"v_exp_f32 v1, v1\n"
"v_mfma_f32_16x16x32_bf16 a[108:111], a[236:239], v[20:23], a[108:111]\n"
"ds_read_b128 a[180:183], v241 offset:12544\n"
"v_cvt_pk_bf16_f32 v16, v0, v1\n"
"v_mfma_f32_16x16x32_bf16 a[112:115], a[240:243], v[20:23], a[112:115]\n"
"v_exp_f32 v2, v2\n"
"v_mfma_f32_16x16x32_bf16 a[116:119], a[244:247], v[20:23], a[116:119]\n"
"ds_read_b128 a[184:187], v241 offset:12800\n"
"v_mfma_f32_16x16x32_bf16 a[120:123], a[248:251], v[20:23], a[120:123]\n"
"v_exp_f32 v3, v3\n"
"v_mfma_f32_16x16x32_bf16 a[124:127], a[252:255], v[20:23], a[124:127]\n"
"v_cvt_pk_bf16_f32 v17, v2, v3\n"
"v_mfma_f32_16x16x32_bf16 v[56:59], v[40:43], v[20:23], v[56:59]\n"
"v_exp_f32 v4, v4\n"
"v_mfma_f32_16x16x32_bf16 v[8:11], a[128:131], v[80:83], v[28:31]\n"
"ds_read_b128 a[188:191], v241 offset:13056\n"
"v_mfma_f32_16x16x32_bf16 v[8:11], a[132:135], v[84:87], v[8:11]\n"
"v_exp_f32 v5, v5\n"
"v_mfma_f32_16x16x32_bf16 v[8:11], a[136:139], v[88:91], v[8:11]\n"
"ds_read_b64_tr_b16 a[224:225], v244 offset:24576\n"
"v_cvt_pk_bf16_f32 v18, v4, v5\n"
"v_mfma_f32_16x16x32_bf16 v[8:11], a[140:143], v[92:95], v[8:11]\n"
"v_exp_f32 v6, v6\n"
"v_mfma_f32_16x16x32_bf16 v[12:15], a[144:147], v[80:83], v[28:31]\n"
"ds_read_b64_tr_b16 a[226:227], v244 offset:28672\n"
"v_mfma_f32_16x16x32_bf16 v[12:15], a[148:151], v[84:87], v[12:15]\n"
"v_exp_f32 v7, v7\n"
"v_mfma_f32_16x16x32_bf16 v[12:15], a[152:155], v[88:91], v[12:15]\n"
"ds_read_b64_tr_b16 a[228:229], v245 offset:24576\n"
"v_cvt_pk_bf16_f32 v19, v6, v7\n"
"v_mfma_f32_16x16x32_bf16 v[12:15], a[156:159], v[92:95], v[12:15]\n"
"s_waitcnt lgkmcnt(11)\n"
"buffer_load_dwordx4 v247, s[12:15], s16 offen offset:1024 lds\n"
"v_mfma_f32_16x16x32_bf16 a[0:3], a[192:195], v[16:19], a[0:3]\n"
"v_exp_f32 v8, v8\n"
"v_mfma_f32_16x16x32_bf16 a[4:7], a[196:199], v[16:19], a[4:7]\n"
"ds_read_b64_tr_b16 a[230:231], v245 offset:28672\n"
"v_mfma_f32_16x16x32_bf16 a[8:11], a[200:203], v[16:19], a[8:11]\n"
"v_exp_f32 v9, v9\n"
"v_mfma_f32_16x16x32_bf16 a[12:15], a[204:207], v[16:19], a[12:15]\n"
"ds_read_b64_tr_b16 a[232:233], v244 offset:24832\n"
"v_cvt_pk_bf16_f32 v20, v8, v9\n"
"v_mfma_f32_16x16x32_bf16 a[16:19], a[208:211], v[16:19], a[16:19]\n"
"v_exp_f32 v10, v10\n"
"v_mfma_f32_16x16x32_bf16 a[20:23], a[212:215], v[16:19], a[20:23]\n"
"ds_read_b64_tr_b16 a[234:235], v244 offset:28928\n"
"v_mfma_f32_16x16x32_bf16 a[24:27], a[216:219], v[16:19], a[24:27]\n"
"v_exp_f32 v11, v11\n"
"v_mfma_f32_16x16x32_bf16 a[28:31], a[220:223], v[16:19], a[28:31]\n"
"v_cvt_pk_bf16_f32 v21, v10, v11\n"
"v_mfma_f32_16x16x32_bf16 v[44:47], v[40:43], v[16:19], v[44:47]\n"
"v_exp_f32 v12, v12\n"
"v_mfma_f32_16x16x32_bf16 v[0:3], a[128:131], v[96:99], v[32:35]\n"
"ds_read_b64_tr_b16 a[236:237], v245 offset:24832\n"
"v_mfma_f32_16x16x32_bf16 v[0:3], a[132:135], v[100:103], v[0:3]\n"
"v_exp_f32 v13, v13\n"
"v_mfma_f32_16x16x32_bf16 v[0:3], a[136:139], v[104:107], v[0:3]\n"
"ds_read_b64_tr_b16 a[238:239], v245 offset:28928\n"
"v_cvt_pk_bf16_f32 v22, v12, v13\n"
"v_mfma_f32_16x16x32_bf16 v[0:3], a[140:143], v[108:111], v[0:3]\n"
"v_exp_f32 v14, v14\n"
"v_mfma_f32_16x16x32_bf16 v[4:7], a[144:147], v[96:99], v[32:35]\n"
"ds_read_b64_tr_b16 a[240:241], v244 offset:25088\n"
"v_mfma_f32_16x16x32_bf16 v[4:7], a[148:151], v[100:103], v[4:7]\n"
"v_exp_f32 v15, v15\n"
"v_mfma_f32_16x16x32_bf16 v[4:7], a[152:155], v[104:107], v[4:7]\n"
"ds_read_b64_tr_b16 a[242:243], v244 offset:29184\n"
"v_cvt_pk_bf16_f32 v23, v14, v15\n"
"v_mfma_f32_16x16x32_bf16 v[4:7], a[156:159], v[108:111], v[4:7]\n"
"buffer_load_dwordx4 v246, s[12:15], s16 offen offset:2048 lds\n"
"v_mfma_f32_16x16x32_bf16 a[32:35], a[192:195], v[20:23], a[32:35]\n"
"v_exp_f32 v0, v0\n"
"v_mfma_f32_16x16x32_bf16 a[36:39], a[196:199], v[20:23], a[36:39]\n"
"ds_read_b64_tr_b16 a[244:245], v245 offset:25088\n"
"v_mfma_f32_16x16x32_bf16 a[40:43], a[200:203], v[20:23], a[40:43]\n"
"v_exp_f32 v1, v1\n"
"v_mfma_f32_16x16x32_bf16 a[44:47], a[204:207], v[20:23], a[44:47]\n"
"ds_read_b64_tr_b16 a[246:247], v245 offset:29184\n"
"v_cvt_pk_bf16_f32 v16, v0, v1\n"
"v_mfma_f32_16x16x32_bf16 a[48:51], a[208:211], v[20:23], a[48:51]\n"
"v_exp_f32 v2, v2\n"
"v_mfma_f32_16x16x32_bf16 a[52:55], a[212:215], v[20:23], a[52:55]\n"
"ds_read_b64_tr_b16 a[248:249], v244 offset:25344\n"
"v_mfma_f32_16x16x32_bf16 a[56:59], a[216:219], v[20:23], a[56:59]\n"
"v_exp_f32 v3, v3\n"
"v_mfma_f32_16x16x32_bf16 a[60:63], a[220:223], v[20:23], a[60:63]\n"
"v_cvt_pk_bf16_f32 v17, v2, v3\n"
"v_mfma_f32_16x16x32_bf16 v[48:51], v[40:43], v[20:23], v[48:51]\n"
"v_exp_f32 v4, v4\n"
"v_mfma_f32_16x16x32_bf16 v[8:11], a[128:131], v[112:115], v[36:39]\n"
"ds_read_b64_tr_b16 a[250:251], v244 offset:29440\n"
"v_mfma_f32_16x16x32_bf16 v[8:11], a[132:135], v[116:119], v[8:11]\n"
"v_exp_f32 v5, v5\n"
"v_mfma_f32_16x16x32_bf16 v[8:11], a[136:139], v[120:123], v[8:11]\n"
"ds_read_b64_tr_b16 a[252:253], v245 offset:25344\n"
"v_cvt_pk_bf16_f32 v18, v4, v5\n"
"v_mfma_f32_16x16x32_bf16 v[8:11], a[140:143], v[124:127], v[8:11]\n"
"v_exp_f32 v6, v6\n"
"v_mfma_f32_16x16x32_bf16 v[12:15], a[144:147], v[112:115], v[36:39]\n"
"ds_read_b64_tr_b16 a[254:255], v245 offset:29440\n"
"v_mfma_f32_16x16x32_bf16 v[12:15], a[148:151], v[116:119], v[12:15]\n"
"v_exp_f32 v7, v7\n"
"v_mfma_f32_16x16x32_bf16 v[12:15], a[152:155], v[120:123], v[12:15]\n"
"v_cvt_pk_bf16_f32 v19, v6, v7\n"
"v_mfma_f32_16x16x32_bf16 v[12:15], a[156:159], v[124:127], v[12:15]\n"
"buffer_load_dwordx4 v247, s[12:15], s16 offen offset:3072 lds\n"
"v_mfma_f32_16x16x32_bf16 a[64:67], a[192:195], v[16:19], a[64:67]\n"
"s_add_u32 s16, s16, 16384\n"
"v_exp_f32 v8, v8\n"
"v_mfma_f32_16x16x32_bf16 a[68:71], a[196:199], v[16:19], a[68:71]\n"
"s_nop 0\n"
"v_mfma_f32_16x16x32_bf16 a[72:75], a[200:203], v[16:19], a[72:75]\n"
"v_exp_f32 v9, v9\n"
"v_mfma_f32_16x16x32_bf16 a[76:79], a[204:207], v[16:19], a[76:79]\n"
"v_cvt_pk_bf16_f32 v20, v8, v9\n"
"v_mfma_f32_16x16x32_bf16 a[80:83], a[208:211], v[16:19], a[80:83]\n"
"v_exp_f32 v10, v10\n"
"v_mfma_f32_16x16x32_bf16 a[84:87], a[212:215], v[16:19], a[84:87]\n"
"s_nop 0\n"
"v_mfma_f32_16x16x32_bf16 a[88:91], a[216:219], v[16:19], a[88:91]\n"
"v_exp_f32 v11, v11\n"
"v_mfma_f32_16x16x32_bf16 a[92:95], a[220:223], v[16:19], a[92:95]\n"
"s_waitcnt vmcnt(16) lgkmcnt(0)\n"
"s_barrier\n"
"v_cvt_pk_bf16_f32 v21, v10, v11\n"
"v_mfma_f32_16x16x32_bf16 v[52:55], v[40:43], v[16:19], v[52:55]\n"
"v_exp_f32 v12, v12\n"
"v_mfma_f32_16x16x32_bf16 v[0:3], a[160:163], v[64:67], v[24:27]\n"
"ds_read_b128 a[128:131], v241 offset:32768\n"
"v_mfma_f32_16x16x32_bf16 v[0:3], a[164:167], v[68:71], v[0:3]\n"
"v_exp_f32 v13, v13\n"
"v_mfma_f32_16x16x32_bf16 v[0:3], a[168:171], v[72:75], v[0:3]\n"
"ds_read_b128 a[132:135], v241 offset:33024\n"
"v_cvt_pk_bf16_f32 v22, v12, v13\n"
"v_mfma_f32_16x16x32_bf16 v[0:3], a[172:175], v[76:79], v[0:3]\n"
"v_exp_f32 v14, v14\n"
"v_mfma_f32_16x16x32_bf16 v[4:7], a[176:179], v[64:67], v[24:27]\n"
"ds_read_b128 a[136:139], v241 offset:33280\n"
"v_mfma_f32_16x16x32_bf16 v[4:7], a[180:183], v[68:71], v[4:7]\n"
"v_exp_f32 v15, v15\n"
"v_mfma_f32_16x16x32_bf16 v[4:7], a[184:187], v[72:75], v[4:7]\n"
"s_add_u32 m0, s18, 65536\n"
"ds_read_b128 a[140:143], v241 offset:33536\n"
"v_cvt_pk_bf16_f32 v23, v14, v15\n"
"v_mfma_f32_16x16x32_bf16 v[4:7], a[188:191], v[76:79], v[4:7]\n"
"buffer_load_dwordx4 v246, s[8:11], s16 offen offset:0 lds\n"
"v_mfma_f32_16x16x32_bf16 a[96:99], a[192:195], v[20:23], a[96:99]\n"
"v_exp_f32 v0, v0\n"
"v_mfma_f32_16x16x32_bf16 a[100:103], a[196:199], v[20:23], a[100:103]\n"
"ds_read_b128 a[144:147], v241 offset:36864\n"
"v_mfma_f32_16x16x32_bf16 a[104:107], a[200:203], v[20:23], a[104:107]\n"
"v_exp_f32 v1, v1\n"
"v_mfma_f32_16x16x32_bf16 a[108:111], a[204:207], v[20:23], a[108:111]\n"
"ds_read_b128 a[148:151], v241 offset:37120\n"
"v_cvt_pk_bf16_f32 v16, v0, v1\n"
"v_mfma_f32_16x16x32_bf16 a[112:115], a[208:211], v[20:23], a[112:115]\n"
"v_exp_f32 v2, v2\n"
"v_mfma_f32_16x16x32_bf16 a[116:119], a[212:215], v[20:23], a[116:119]\n"
"ds_read_b128 a[152:155], v241 offset:37376\n"
"v_mfma_f32_16x16x32_bf16 a[120:123], a[216:219], v[20:23], a[120:123]\n"
"v_exp_f32 v3, v3\n"
"v_mfma_f32_16x16x32_bf16 a[124:127], a[220:223], v[20:23], a[124:127]\n"
"v_cvt_pk_bf16_f32 v17, v2, v3\n"
"v_mfma_f32_16x16x32_bf16 v[56:59], v[40:43], v[20:23], v[56:59]\n"
"v_exp_f32 v4, v4\n"
"v_mfma_f32_16x16x32_bf16 v[8:11], a[160:163], v[80:83], v[28:31]\n"
"ds_read_b128 a[156:159], v241 offset:37632\n"
"v_mfma_f32_16x16x32_bf16 v[8:11], a[164:167], v[84:87], v[8:11]\n"
"v_exp_f32 v5, v5\n"
"v_mfma_f32_16x16x32_bf16 v[8:11], a[168:171], v[88:91], v[8:11]\n"
"ds_read_b64_tr_b16 a[192:193], v244 offset:49152\n"
"v_cvt_pk_bf16_f32 v18, v4, v5\n"
"v_mfma_f32_16x16x32_bf16 v[8:11], a[172:175], v[92:95], v[8:11]\n"
"v_exp_f32 v6, v6\n"
"v_mfma_f32_16x16x32_bf16 v[12:15], a[176:179], v[80:83], v[28:31]\n"
"ds_read_b64_tr_b16 a[194:195], v244 offset:53248\n"
"v_mfma_f32_16x16x32_bf16 v[12:15], a[180:183], v[84:87], v[12:15]\n"
"v_exp_f32 v7, v7\n"
"v_mfma_f32_16x16x32_bf16 v[12:15], a[184:187], v[88:91], v[12:15]\n"
"ds_read_b64_tr_b16 a[196:197], v245 offset:49152\n"
"v_cvt_pk_bf16_f32 v19, v6, v7\n"
"v_mfma_f32_16x16x32_bf16 v[12:15], a[188:191], v[92:95], v[12:15]\n"
"s_waitcnt lgkmcnt(11)\n"
"buffer_load_dwordx4 v246, s[8:11], s16 offen offset:1024 lds\n"
"v_mfma_f32_16x16x32_bf16 a[0:3], a[224:227], v[16:19], a[0:3]\n"
"v_exp_f32 v8, v8\n"
"v_mfma_f32_16x16x32_bf16 a[4:7], a[228:231], v[16:19], a[4:7]\n"
"ds_read_b64_tr_b16 a[198:199], v245 offset:53248\n"
"v_mfma_f32_16x16x32_bf16 a[8:11], a[232:235], v[16:19], a[8:11]\n"
"v_exp_f32 v9, v9\n"
"v_mfma_f32_16x16x32_bf16 a[12:15], a[236:239], v[16:19], a[12:15]\n"
"ds_read_b64_tr_b16 a[200:201], v244 offset:49408\n"
"v_cvt_pk_bf16_f32 v20, v8, v9\n"
"v_mfma_f32_16x16x32_bf16 a[16:19], a[240:243], v[16:19], a[16:19]\n"
"v_exp_f32 v10, v10\n"
"v_mfma_f32_16x16x32_bf16 a[20:23], a[244:247], v[16:19], a[20:23]\n"
"ds_read_b64_tr_b16 a[202:203], v244 offset:53504\n"
"v_mfma_f32_16x16x32_bf16 a[24:27], a[248:251], v[16:19], a[24:27]\n"
"v_exp_f32 v11, v11\n"
"v_mfma_f32_16x16x32_bf16 a[28:31], a[252:255], v[16:19], a[28:31]\n"
"v_cvt_pk_bf16_f32 v21, v10, v11\n"
"v_mfma_f32_16x16x32_bf16 v[44:47], v[40:43], v[16:19], v[44:47]\n"
"v_exp_f32 v12, v12\n"
"v_mfma_f32_16x16x32_bf16 v[0:3], a[160:163], v[96:99], v[32:35]\n"
"ds_read_b64_tr_b16 a[204:205], v245 offset:49408\n"
"v_mfma_f32_16x16x32_bf16 v[0:3], a[164:167], v[100:103], v[0:3]\n"
"v_exp_f32 v13, v13\n"
"v_mfma_f32_16x16x32_bf16 v[0:3], a[168:171], v[104:107], v[0:3]\n"
"ds_read_b64_tr_b16 a[206:207], v245 offset:53504\n"
"v_cvt_pk_bf16_f32 v22, v12, v13\n"
"v_mfma_f32_16x16x32_bf16 v[0:3], a[172:175], v[108:111], v[0:3]\n"
"v_exp_f32 v14, v14\n"
"v_mfma_f32_16x16x32_bf16 v[4:7], a[176:179], v[96:99], v[32:35]\n"
"ds_read_b64_tr_b16 a[208:209], v244 offset:49664\n"
"v_mfma_f32_16x16x32_bf16 v[4:7], a[180:183], v[100:103], v[4:7]\n"
"v_exp_f32 v15, v15\n"
"v_mfma_f32_16x16x32_bf16 v[4:7], a[184:187], v[104:107], v[4:7]\n"
"ds_read_b64_tr_b16 a[210:211], v244 offset:53760\n"
"v_cvt_pk_bf16_f32 v23, v14, v15\n"
"v_mfma_f32_16x16x32_bf16 v[4:7], a[188:191], v[108:111], v[4:7]\n"
"buffer_load_dwordx4 v247, s[8:11], s16 offen offset:2048 lds\n"
"v_mfma_f32_16x16x32_bf16 a[32:35], a[224:227], v[20:23], a[32:35]\n"
"v_exp_f32 v0, v0\n"
"v_mfma_f32_16x16x32_bf16 a[36:39], a[228:231], v[20:23], a[36:39]\n"
"ds_read_b64_tr_b16 a[212:213], v245 offset:49664\n"
"v_mfma_f32_16x16x32_bf16 a[40:43], a[232:235], v[20:23], a[40:43]\n"
"v_exp_f32 v1, v1\n"
"v_mfma_f32_16x16x32_bf16 a[44:47], a[236:239], v[20:23], a[44:47]\n"
"ds_read_b64_tr_b16 a[214:215], v245 offset:53760\n"
"v_cvt_pk_bf16_f32 v16, v0, v1\n"
"v_mfma_f32_16x16x32_bf16 a[48:51], a[240:243], v[20:23], a[48:51]\n"
"v_exp_f32 v2, v2\n"
"v_mfma_f32_16x16x32_bf16 a[52:55], a[244:247], v[20:23], a[52:55]\n"
"ds_read_b64_tr_b16 a[216:217], v244 offset:49920\n"
"v_mfma_f32_16x16x32_bf16 a[56:59], a[248:251], v[20:23], a[56:59]\n"
"v_exp_f32 v3, v3\n"
"v_mfma_f32_16x16x32_bf16 a[60:63], a[252:255], v[20:23], a[60:63]\n"
"v_cvt_pk_bf16_f32 v17, v2, v3\n"
"v_mfma_f32_16x16x32_bf16 v[48:51], v[40:43], v[20:23], v[48:51]\n"
"v_exp_f32 v4, v4\n"
"v_mfma_f32_16x16x32_bf16 v[8:11], a[160:163], v[112:115], v[36:39]\n"
"ds_read_b64_tr_b16 a[218:219], v244 offset:54016\n"
"v_mfma_f32_16x16x32_bf16 v[8:11], a[164:167], v[116:119], v[8:11]\n"
"v_exp_f32 v5, v5\n"
"v_mfma_f32_16x16x32_bf16 v[8:11], a[168:171], v[120:123], v[8:11]\n"
"ds_read_b64_tr_b16 a[220:221], v245 offset:49920\n"
"v_cvt_pk_bf16_f32 v18, v4, v5\n"
"v_mfma_f32_16x16x32_bf16 v[8:11], a[172:175], v[124:127], v[8:11]\n"
"v_exp_f32 v6, v6\n"
"v_mfma_f32_16x16x32_bf16 v[12:15], a[176:179], v[112:115], v[36:39]\n"
"ds_read_b64_tr_b16 a[222:223], v245 offset:54016\n"
"v_mfma_f32_16x16x32_bf16 v[12:15], a[180:183], v[116:119], v[12:15]\n"
"v_exp_f32 v7, v7\n"
"v_mfma_f32_16x16x32_bf16 v[12:15], a[184:187], v[120:123], v[12:15]\n"
"v_cvt_pk_bf16_f32 v19, v6, v7\n"
"v_mfma_f32_16x16x32_bf16 v[12:15], a[188:191], v[124:127], v[12:15]\n"
"buffer_load_dwordx4 v247, s[8:11], s16 offen offset:3072 lds\n"
"v_mfma_f32_16x16x32_bf16 a[64:67], a[224:227], v[16:19], a[64:67]\n"
"v_exp_f32 v8, v8\n"
"v_mfma_f32_16x16x32_bf16 a[68:71], a[228:231], v[16:19], a[68:71]\n"
"s_nop 0\n"
"v_mfma_f32_16x16x32_bf16 a[72:75], a[232:235], v[16:19], a[72:75]\n"
"v_exp_f32 v9, v9\n"
"v_mfma_f32_16x16x32_bf16 a[76:79], a[236:239], v[16:19], a[76:79]\n"
"v_cvt_pk_bf16_f32 v20, v8, v9\n"
"v_mfma_f32_16x16x32_bf16 a[80:83], a[240:243], v[16:19], a[80:83]\n"
"v_exp_f32 v10, v10\n"
"v_mfma_f32_16x16x32_bf16 a[84:87], a[244:247], v[16:19], a[84:87]\n"
"s_nop 0\n"
"v_mfma_f32_16x16x32_bf16 a[88:91], a[248:251], v[16:19], a[88:91]\n"
"v_exp_f32 v11, v11\n"
"v_mfma_f32_16x16x32_bf16 a[92:95], a[252:255], v[16:19], a[92:95]\n"
"s_waitcnt lgkmcnt(8)\n"
"v_cvt_pk_bf16_f32 v21, v10, v11\n"
"v_mfma_f32_16x16x32_bf16 v[52:55], v[40:43], v[16:19], v[52:55]\n"
"v_exp_f32 v12, v12\n"
"v_mfma_f32_16x16x32_bf16 v[0:3], a[128:131], v[64:67], v[24:27]\n"
"ds_read_b128 a[160:163], v241 offset:40960\n"
"v_mfma_f32_16x16x32_bf16 v[0:3], a[132:135], v[68:71], v[0:3]\n"
"v_exp_f32 v13, v13\n"
"v_mfma_f32_16x16x32_bf16 v[0:3], a[136:139], v[72:75], v[0:3]\n"
"ds_read_b128 a[164:167], v241 offset:41216\n"
"v_cvt_pk_bf16_f32 v22, v12, v13\n"
"v_mfma_f32_16x16x32_bf16 v[0:3], a[140:143], v[76:79], v[0:3]\n"
"v_exp_f32 v14, v14\n"
"v_mfma_f32_16x16x32_bf16 v[4:7], a[144:147], v[64:67], v[24:27]\n"
"ds_read_b128 a[168:171], v241 offset:41472\n"
"v_mfma_f32_16x16x32_bf16 v[4:7], a[148:151], v[68:71], v[4:7]\n"
"v_exp_f32 v15, v15\n"
"v_mfma_f32_16x16x32_bf16 v[4:7], a[152:155], v[72:75], v[4:7]\n"
"s_add_u32 m0, s18, 81920\n"
"ds_read_b128 a[172:175], v241 offset:41728\n"
"v_cvt_pk_bf16_f32 v23, v14, v15\n"
"v_mfma_f32_16x16x32_bf16 v[4:7], a[156:159], v[76:79], v[4:7]\n"
"buffer_load_dwordx4 v246, s[12:15], s16 offen offset:0 lds\n"
"v_mfma_f32_16x16x32_bf16 a[96:99], a[224:227], v[20:23], a[96:99]\n"
"v_exp_f32 v0, v0\n"
"v_mfma_f32_16x16x32_bf16 a[100:103], a[228:231], v[20:23], a[100:103]\n"
"ds_read_b128 a[176:179], v241 offset:45056\n"
"v_mfma_f32_16x16x32_bf16 a[104:107], a[232:235], v[20:23], a[104:107]\n"
"v_exp_f32 v1, v1\n"
"v_mfma_f32_16x16x32_bf16 a[108:111], a[236:239], v[20:23], a[108:111]\n"
"ds_read_b128 a[180:183], v241 offset:45312\n"
"v_cvt_pk_bf16_f32 v16, v0, v1\n"
"v_mfma_f32_16x16x32_bf16 a[112:115], a[240:243], v[20:23], a[112:115]\n"
"v_exp_f32 v2, v2\n"
"v_mfma_f32_16x16x32_bf16 a[116:119], a[244:247], v[20:23], a[116:119]\n"
"ds_read_b128 a[184:187], v241 offset:45568\n"
"v_mfma_f32_16x16x32_bf16 a[120:123], a[248:251], v[20:23], a[120:123]\n"
"v_exp_f32 v3, v3\n"
"v_mfma_f32_16x16x32_bf16 a[124:127], a[252:255], v[20:23], a[124:127]\n"
"v_cvt_pk_bf16_f32 v17, v2, v3\n"
"v_mfma_f32_16x16x32_bf16 v[56:59], v[40:43], v[20:23], v[56:59]\n"
"v_exp_f32 v4, v4\n"
"v_mfma_f32_16x16x32_bf16 v[8:11], a[128:131], v[80:83], v[28:31]\n"
"ds_read_b128 a[188:191], v241 offset:45824\n"
"v_mfma_f32_16x16x32_bf16 v[8:11], a[132:135], v[84:87], v[8:11]\n"
"v_exp_f32 v5, v5\n"
"v_mfma_f32_16x16x32_bf16 v[8:11], a[136:139], v[88:91], v[8:11]\n"
"ds_read_b64_tr_b16 a[224:225], v244 offset:57344\n"
"v_cvt_pk_bf16_f32 v18, v4, v5\n"
"v_mfma_f32_16x16x32_bf16 v[8:11], a[140:143], v[92:95], v[8:11]\n"
"v_exp_f32 v6, v6\n"
"v_mfma_f32_16x16x32_bf16 v[12:15], a[144:147], v[80:83], v[28:31]\n"
"ds_read_b64_tr_b16 a[226:227], v244 offset:61440\n"
"v_mfma_f32_16x16x32_bf16 v[12:15], a[148:151], v[84:87], v[12:15]\n"
"v_exp_f32 v7, v7\n"
"v_mfma_f32_16x16x32_bf16 v[12:15], a[152:155], v[88:91], v[12:15]\n"
"ds_read_b64_tr_b16 a[228:229], v245 offset:57344\n"
"v_cvt_pk_bf16_f32 v19, v6, v7\n"
"v_mfma_f32_16x16x32_bf16 v[12:15], a[156:159], v[92:95], v[12:15]\n"
"s_waitcnt lgkmcnt(11)\n"
"buffer_load_dwordx4 v247, s[12:15], s16 offen offset:1024 lds\n"
"v_mfma_f32_16x16x32_bf16 a[0:3], a[192:195], v[16:19], a[0:3]\n"
"v_exp_f32 v8, v8\n"
"v_mfma_f32_16x16x32_bf16 a[4:7], a[196:199], v[16:19], a[4:7]\n"
"ds_read_b64_tr_b16 a[230:231], v245 offset:61440\n"
"v_mfma_f32_16x16x32_bf16 a[8:11], a[200:203], v[16:19], a[8:11]\n"
"v_exp_f32 v9, v9\n"
"v_mfma_f32_16x16x32_bf16 a[12:15], a[204:207], v[16:19], a[12:15]\n"
"ds_read_b64_tr_b16 a[232:233], v244 offset:57600\n"
"v_cvt_pk_bf16_f32 v20, v8, v9\n"
"v_mfma_f32_16x16x32_bf16 a[16:19], a[208:211], v[16:19], a[16:19]\n"
"v_exp_f32 v10, v10\n"
"v_mfma_f32_16x16x32_bf16 a[20:23], a[212:215], v[16:19], a[20:23]\n"
"ds_read_b64_tr_b16 a[234:235], v244 offset:61696\n"
"v_mfma_f32_16x16x32_bf16 a[24:27], a[216:219], v[16:19], a[24:27]\n"
"v_exp_f32 v11, v11\n"
"v_mfma_f32_16x16x32_bf16 a[28:31], a[220:223], v[16:19], a[28:31]\n"
"v_cvt_pk_bf16_f32 v21, v10, v11\n"
"v_mfma_f32_16x16x32_bf16 v[44:47], v[40:43], v[16:19], v[44:47]\n"
"v_exp_f32 v12, v12\n"
"v_mfma_f32_16x16x32_bf16 v[0:3], a[128:131], v[96:99], v[32:35]\n"
"ds_read_b64_tr_b16 a[236:237], v245 offset:57600\n"
"v_mfma_f32_16x16x32_bf16 v[0:3], a[132:135], v[100:103], v[0:3]\n"
"v_exp_f32 v13, v13\n"
"v_mfma_f32_16x16x32_bf16 v[0:3], a[136:139], v[104:107], v[0:3]\n"
"ds_read_b64_tr_b16 a[238:239], v245 offset:61696\n"
"v_cvt_pk_bf16_f32 v22, v12, v13\n"
"v_mfma_f32_16x16x32_bf16 v[0:3], a[140:143], v[108:111], v[0:3]\n"
"v_exp_f32 v14, v14\n"
"v_mfma_f32_16x16x32_bf16 v[4:7], a[144:147], v[96:99], v[32:35]\n"
"ds_read_b64_tr_b16 a[240:241], v244 offset:57856\n"
"v_mfma_f32_16x16x32_bf16 v[4:7], a[148:151], v[100:103], v[4:7]\n"
"v_exp_f32 v15, v15\n"
"v_mfma_f32_16x16x32_bf16 v[4:7], a[152:155], v[104:107], v[4:7]\n"
"ds_read_b64_tr_b16 a[242:243], v244 offset:61952\n"
"v_cvt_pk_bf16_f32 v23, v14, v15\n"
"v_mfma_f32_16x16x32_bf16 v[4:7], a[156:159], v[108:111], v[4:7]\n"
"buffer_load_dwordx4 v246, s[12:15], s16 offen offset:2048 lds\n"
"v_mfma_f32_16x16x32_bf16 a[32:35], a[192:195], v[20:23], a[32:35]\n"
"v_exp_f32 v0, v0\n"
"v_mfma_f32_16x16x32_bf16 a[36:39], a[196:199], v[20:23], a[36:39]\n"
"ds_read_b64_tr_b16 a[244:245], v245 offset:57856\n"
"v_mfma_f32_16x16x32_bf16 a[40:43], a[200:203], v[20:23], a[40:43]\n"
"v_exp_f32 v1, v1\n"
"v_mfma_f32_16x16x32_bf16 a[44:47], a[204:207], v[20:23], a[44:47]\n"
"ds_read_b64_tr_b16 a[246:247], v245 offset:61952\n"
"v_cvt_pk_bf16_f32 v16, v0, v1\n"
"v_mfma_f32_16x16x32_bf16 a[48:51], a[208:211], v[20:23], a[48:51]\n"
"v_exp_f32 v2, v2\n"
"v_mfma_f32_16x16x32_bf16 a[52:55], a[212:215], v[20:23], a[52:55]\n"
"ds_read_b64_tr_b16 a[248:249], v244 offset:58112\n"
"v_mfma_f32_16x16x32_bf16 a[56:59], a[216:219], v[20:23], a[56:59]\n"
"v_exp_f32 v3, v3\n"
"v_mfma_f32_16x16x32_bf16 a[60:63], a[220:223], v[20:23], a[60:63]\n"
"v_cvt_pk_bf16_f32 v17, v2, v3\n"
"v_mfma_f32_16x16x32_bf16 v[48:51], v[40:43], v[20:23], v[48:51]\n"
"v_exp_f32 v4, v4\n"
"v_mfma_f32_16x16x32_bf16 v[8:11], a[128:131], v[112:115], v[36:39]\n"
"ds_read_b64_tr_b16 a[250:251], v244 offset:62208\n"
"v_mfma_f32_16x16x32_bf16 v[8:11], a[132:135], v[116:119], v[8:11]\n"
"v_exp_f32 v5, v5\n"
"v_mfma_f32_16x16x32_bf16 v[8:11], a[136:139], v[120:123], v[8:11]\n"
"ds_read_b64_tr_b16 a[252:253], v245 offset:58112\n"
"v_cvt_pk_bf16_f32 v18, v4, v5\n"
"v_mfma_f32_16x16x32_bf16 v[8:11], a[140:143], v[124:127], v[8:11]\n"
"v_exp_f32 v6, v6\n"
"v_mfma_f32_16x16x32_bf16 v[12:15], a[144:147], v[112:115], v[36:39]\n"
"ds_read_b64_tr_b16 a[254:255], v245 offset:62208\n"
"v_mfma_f32_16x16x32_bf16 v[12:15], a[148:151], v[116:119], v[12:15]\n"
"v_exp_f32 v7, v7\n"
"v_mfma_f32_16x16x32_bf16 v[12:15], a[152:155], v[120:123], v[12:15]\n"
"v_cvt_pk_bf16_f32 v19, v6, v7\n"
"v_mfma_f32_16x16x32_bf16 v[12:15], a[156:159], v[124:127], v[12:15]\n"
"buffer_load_dwordx4 v247, s[12:15], s16 offen offset:3072 lds\n"
"v_mfma_f32_16x16x32_bf16 a[64:67], a[192:195], v[16:19], a[64:67]\n"
"s_add_u32 s16, s16, 16384\n"
"v_exp_f32 v8, v8\n"
"v_mfma_f32_16x16x32_bf16 a[68:71], a[196:199], v[16:19], a[68:71]\n"
"s_nop 0\n"
"v_mfma_f32_16x16x32_bf16 a[72:75], a[200:203], v[16:19], a[72:75]\n"
"v_exp_f32 v9, v9\n"
"v_mfma_f32_16x16x32_bf16 a[76:79], a[204:207], v[16:19], a[76:79]\n"
"v_cvt_pk_bf16_f32 v20, v8, v9\n"
"v_mfma_f32_16x16x32_bf16 a[80:83], a[208:211], v[16:19], a[80:83]\n"
"v_exp_f32 v10, v10\n"
"v_mfma_f32_16x16x32_bf16 a[84:87], a[212:215], v[16:19], a[84:87]\n"
"s_nop 0\n"
"v_mfma_f32_16x16x32_bf16 a[88:91], a[216:219], v[16:19], a[88:91]\n"
"v_exp_f32 v11, v11\n"
"v_mfma_f32_16x16x32_bf16 a[92:95], a[220:223], v[16:19], a[92:95]\n"
"s_waitcnt vmcnt(16) lgkmcnt(0)\n"
"s_barrier\n"
"v_cvt_pk_bf16_f32 v21, v10, v11\n"
"v_mfma_f32_16x16x32_bf16 v[52:55], v[40:43], v[16:19], v[52:55]\n"
"v_exp_f32 v12, v12\n"
"v_mfma_f32_16x16x32_bf16 v[0:3], a[160:163], v[64:67], v[24:27]\n"
"ds_read_b128 a[128:131], v240 offset:0\n"
"v_mfma_f32_16x16x32_bf16 v[0:3], a[164:167], v[68:71], v[0:3]\n"
"v_exp_f32 v13, v13\n"
"v_mfma_f32_16x16x32_bf16 v[0:3], a[168:171], v[72:75], v[0:3]\n"
"ds_read_b128 a[132:135], v240 offset:256\n"
"v_cvt_pk_bf16_f32 v22, v12, v13\n"
"v_mfma_f32_16x16x32_bf16 v[0:3], a[172:175], v[76:79], v[0:3]\n"
"v_exp_f32 v14, v14\n"
"v_mfma_f32_16x16x32_bf16 v[4:7], a[176:179], v[64:67], v[24:27]\n"
"ds_read_b128 a[136:139], v240 offset:512\n"
"v_mfma_f32_16x16x32_bf16 v[4:7], a[180:183], v[68:71], v[4:7]\n"
"v_exp_f32 v15, v15\n"
"v_mfma_f32_16x16x32_bf16 v[4:7], a[184:187], v[72:75], v[4:7]\n"
"s_add_u32 m0, s18, 98304\n"
"ds_read_b128 a[140:143], v240 offset:768\n"
"v_cvt_pk_bf16_f32 v23, v14, v15\n"
"v_mfma_f32_16x16x32_bf16 v[4:7], a[188:191], v[76:79], v[4:7]\n"
"buffer_load_dwordx4 v246, s[8:11], s16 offen offset:0 lds\n"
"v_mfma_f32_16x16x32_bf16 a[96:99], a[192:195], v[20:23], a[96:99]\n"
"v_exp_f32 v0, v0\n"
"v_mfma_f32_16x16x32_bf16 a[100:103], a[196:199], v[20:23], a[100:103]\n"
"ds_read_b128 a[144:147], v240 offset:4096\n"
"v_mfma_f32_16x16x32_bf16 a[104:107], a[200:203], v[20:23], a[104:107]\n"
"v_exp_f32 v1, v1\n"
"v_mfma_f32_16x16x32_bf16 a[108:111], a[204:207], v[20:23], a[108:111]\n"
"ds_read_b128 a[148:151], v240 offset:4352\n"
"v_cvt_pk_bf16_f32 v16, v0, v1\n"
"v_mfma_f32_16x16x32_bf16 a[112:115], a[208:211], v[20:23], a[112:115]\n"
"v_exp_f32 v2, v2\n"
"v_mfma_f32_16x16x32_bf16 a[116:119], a[212:215], v[20:23], a[116:119]\n"
"ds_read_b128 a[152:155], v240 offset:4608\n"
"v_mfma_f32_16x16x32_bf16 a[120:123], a[216:219], v[20:23], a[120:123]\n"
"v_exp_f32 v3, v3\n"
"v_mfma_f32_16x16x32_bf16 a[124:127], a[220:223], v[20:23], a[124:127]\n"
"v_cvt_pk_bf16_f32 v17, v2, v3\n"
"v_mfma_f32_16x16x32_bf16 v[56:59], v[40:43], v[20:23], v[56:59]\n"
"v_exp_f32 v4, v4\n"
"v_mfma_f32_16x16x32_bf16 v[8:11], a[160:163], v[80:83], v[28:31]\n"
"ds_read_b128 a[156:159], v240 offset:4864\n"
"v_mfma_f32_16x16x32_bf16 v[8:11], a[164:167], v[84:87], v[8:11]\n"
"v_exp_f32 v5, v5\n"
"v_mfma_f32_16x16x32_bf16 v[8:11], a[168:171], v[88:91], v[8:11]\n"
"ds_read_b64_tr_b16 a[192:193], v242 offset:16384\n"
"v_cvt_pk_bf16_f32 v18, v4, v5\n"
"v_mfma_f32_16x16x32_bf16 v[8:11], a[172:175], v[92:95], v[8:11]\n"
"v_exp_f32 v6, v6\n"
"v_mfma_f32_16x16x32_bf16 v[12:15], a[176:179], v[80:83], v[28:31]\n"
"ds_read_b64_tr_b16 a[194:195], v242 offset:20480\n"
"v_mfma_f32_16x16x32_bf16 v[12:15], a[180:183], v[84:87], v[12:15]\n"
"v_exp_f32 v7, v7\n"
"v_mfma_f32_16x16x32_bf16 v[12:15], a[184:187], v[88:91], v[12:15]\n"
"ds_read_b64_tr_b16 a[196:197], v243 offset:16384\n"
"v_cvt_pk_bf16_f32 v19, v6, v7\n"
"v_mfma_f32_16x16x32_bf16 v[12:15], a[188:191], v[92:95], v[12:15]\n"
"s_waitcnt lgkmcnt(11)\n"
"buffer_load_dwordx4 v246, s[8:11], s16 offen offset:1024 lds\n"
"v_mfma_f32_16x16x32_bf16 a[0:3], a[224:227], v[16:19], a[0:3]\n"
"v_exp_f32 v8, v8\n"
"v_mfma_f32_16x16x32_bf16 a[4:7], a[228:231], v[16:19], a[4:7]\n"
"ds_read_b64_tr_b16 a[198:199], v243 offset:20480\n"
"v_mfma_f32_16x16x32_bf16 a[8:11], a[232:235], v[16:19], a[8:11]\n"
"v_exp_f32 v9, v9\n"
"v_mfma_f32_16x16x32_bf16 a[12:15], a[236:239], v[16:19], a[12:15]\n"
"ds_read_b64_tr_b16 a[200:201], v242 offset:16640\n"
"v_cvt_pk_bf16_f32 v20, v8, v9\n"
"v_mfma_f32_16x16x32_bf16 a[16:19], a[240:243], v[16:19], a[16:19]\n"
"v_exp_f32 v10, v10\n"
"v_mfma_f32_16x16x32_bf16 a[20:23], a[244:247], v[16:19], a[20:23]\n"
"ds_read_b64_tr_b16 a[202:203], v242 offset:20736\n"
"v_mfma_f32_16x16x32_bf16 a[24:27], a[248:251], v[16:19], a[24:27]\n"
"v_exp_f32 v11, v11\n"
"v_mfma_f32_16x16x32_bf16 a[28:31], a[252:255], v[16:19], a[28:31]\n"
"v_cvt_pk_bf16_f32 v21, v10, v11\n"
"v_mfma_f32_16x16x32_bf16 v[44:47], v[40:43], v[16:19], v[44:47]\n"
"v_exp_f32 v12, v12\n"
"v_mfma_f32_16x16x32_bf16 v[0:3], a[160:163], v[96:99], v[32:35]\n"
"ds_read_b64_tr_b16 a[204:205], v243 offset:16640\n"
"v_mfma_f32_16x16x32_bf16 v[0:3], a[164:167], v[100:103], v[0:3]\n"
"v_exp_f32 v13, v13\n"
"v_mfma_f32_16x16x32_bf16 v[0:3], a[168:171], v[104:107], v[0:3]\n"
"ds_read_b64_tr_b16 a[206:207], v243 offset:20736\n"
"v_cvt_pk_bf16_f32 v22, v12, v13\n"
"v_mfma_f32_16x16x32_bf16 v[0:3], a[172:175], v[108:111], v[0:3]\n"
"v_exp_f32 v14, v14\n"
"v_mfma_f32_16x16x32_bf16 v[4:7], a[176:179], v[96:99], v[32:35]\n"
"ds_read_b64_tr_b16 a[208:209], v242 offset:16896\n"
"v_mfma_f32_16x16x32_bf16 v[4:7], a[180:183], v[100:103], v[4:7]\n"
"v_exp_f32 v15, v15\n"
"v_mfma_f32_16x16x32_bf16 v[4:7], a[184:187], v[104:107], v[4:7]\n"
"ds_read_b64_tr_b16 a[210:211], v242 offset:20992\n"
"v_cvt_pk_bf16_f32 v23, v14, v15\n"
"v_mfma_f32_16x16x32_bf16 v[4:7], a[188:191], v[108:111], v[4:7]\n"
"buffer_load_dwordx4 v247, s[8:11], s16 offen offset:2048 lds\n"
"v_mfma_f32_16x16x32_bf16 a[32:35], a[224:227], v[20:23], a[32:35]\n"
"v_exp_f32 v0, v0\n"
"v_mfma_f32_16x16x32_bf16 a[36:39], a[228:231], v[20:23], a[36:39]\n"
"ds_read_b64_tr_b16 a[212:213], v243 offset:16896\n"
"v_mfma_f32_16x16x32_bf16 a[40:43], a[232:235], v[20:23], a[40:43]\n"
"v_exp_f32 v1, v1\n"
"v_mfma_f32_16x16x32_bf16 a[44:47], a[236:239], v[20:23], a[44:47]\n"
"ds_read_b64_tr_b16 a[214:215], v243 offset:20992\n"
"v_cvt_pk_bf16_f32 v16, v0, v1\n"
"v_mfma_f32_16x16x32_bf16 a[48:51], a[240:243], v[20:23], a[48:51]\n"
"v_exp_f32 v2, v2\n"
"v_mfma_f32_16x16x32_bf16 a[52:55], a[244:247], v[20:23], a[52:55]\n"
"ds_read_b64_tr_b16 a[216:217], v242 offset:17152\n"
"v_mfma_f32_16x16x32_bf16 a[56:59], a[248:251], v[20:23], a[56:59]\n"
"v_exp_f32 v3, v3\n"
"v_mfma_f32_16x16x32_bf16 a[60:63], a[252:255], v[20:23], a[60:63]\n"
"v_cvt_pk_bf16_f32 v17, v2, v3\n"
"v_mfma_f32_16x16x32_bf16 v[48:51], v[40:43], v[20:23], v[48:51]\n"
"v_exp_f32 v4, v4\n"
"v_mfma_f32_16x16x32_bf16 v[8:11], a[160:163], v[112:115], v[36:39]\n"
"ds_read_b64_tr_b16 a[218:219], v242 offset:21248\n"
"v_mfma_f32_16x16x32_bf16 v[8:11], a[164:167], v[116:119], v[8:11]\n"
"v_exp_f32 v5, v5\n"
"v_mfma_f32_16x16x32_bf16 v[8:11], a[168:171], v[120:123], v[8:11]\n"
"ds_read_b64_tr_b16 a[220:221], v243 offset:17152\n"
"v_cvt_pk_bf16_f32 v18, v4, v5\n"
"v_mfma_f32_16x16x32_bf16 v[8:11], a[172:175], v[124:127], v[8:11]\n"
"v_exp_f32 v6, v6\n"
"v_mfma_f32_16x16x32_bf16 v[12:15], a[176:179], v[112:115], v[36:39]\n"
"ds_read_b64_tr_b16 a[222:223], v243 offset:21248\n"
"v_mfma_f32_16x16x32_bf16 v[12:15], a[180:183], v[116:119], v[12:15]\n"
"v_exp_f32 v7, v7\n"
"v_mfma_f32_16x16x32_bf16 v[12:15], a[184:187], v[120:123], v[12:15]\n"
"v_cvt_pk_bf16_f32 v19, v6, v7\n"
"v_mfma_f32_16x16x32_bf16 v[12:15], a[188:191], v[124:127], v[12:15]\n"
"buffer_load_dwordx4 v247, s[8:11], s16 offen offset:3072 lds\n"
"v_mfma_f32_16x16x32_bf16 a[64:67], a[224:227], v[16:19], a[64:67]\n"
"v_exp_f32 v8, v8\n"
"v_mfma_f32_16x16x32_bf16 a[68:71], a[228:231], v[16:19], a[68:71]\n"
"s_nop 0\n"
"v_mfma_f32_16x16x32_bf16 a[72:75], a[232:235], v[16:19], a[72:75]\n"
"v_exp_f32 v9, v9\n"
"v_mfma_f32_16x16x32_bf16 a[76:79], a[236:239], v[16:19], a[76:79]\n"
"v_cvt_pk_bf16_f32 v20, v8, v9\n"
"v_mfma_f32_16x16x32_bf16 a[80:83], a[240:243], v[16:19], a[80:83]\n"
"v_exp_f32 v10, v10\n"
"v_mfma_f32_16x16x32_bf16 a[84:87], a[244:247], v[16:19], a[84:87]\n"
"s_nop 0\n"
"v_mfma_f32_16x16x32_bf16 a[88:91], a[248:251], v[16:19], a[88:91]\n"
"v_exp_f32 v11, v11\n"
"v_mfma_f32_16x16x32_bf16 a[92:95], a[252:255], v[16:19], a[92:95]\n"
"s_waitcnt lgkmcnt(8)\n"
"v_cvt_pk_bf16_f32 v21, v10, v11\n"
"v_mfma_f32_16x16x32_bf16 v[52:55], v[40:43], v[16:19], v[52:55]\n"
"v_exp_f32 v12, v12\n"
"v_mfma_f32_16x16x32_bf16 v[0:3], a[128:131], v[64:67], v[24:27]\n"
"ds_read_b128 a[160:163], v240 offset:8192\n"
"v_mfma_f32_16x16x32_bf16 v[0:3], a[132:135], v[68:71], v[0:3]\n"
"v_exp_f32 v13, v13\n"
"v_mfma_f32_16x16x32_bf16 v[0:3], a[136:139], v[72:75], v[0:3]\n"
"ds_read_b128 a[164:167], v240 offset:8448\n"
"v_cvt_pk_bf16_f32 v22, v12, v13\n"
"v_mfma_f32_16x16x32_bf16 v[0:3], a[140:143], v[76:79], v[0:3]\n"
"v_exp_f32 v14, v14\n"
"v_mfma_f32_16x16x32_bf16 v[4:7], a[144:147], v[64:67], v[24:27]\n"
"ds_read_b128 a[168:171], v240 offset:8704\n"
"v_mfma_f32_16x16x32_bf16 v[4:7], a[148:151], v[68:71], v[4:7]\n"
"v_exp_f32 v15, v15\n"
"v_mfma_f32_16x16x32_bf16 v[4:7], a[152:155], v[72:75], v[4:7]\n"
"s_add_u32 m0, s18, 114688\n"
"ds_read_b128 a[172:175], v240 offset:8960\n"
"v_cvt_pk_bf16_f32 v23, v14, v15\n"
"v_mfma_f32_16x16x32_bf16 v[4:7], a[156:159], v[76:79], v[4:7]\n"
"buffer_load_dwordx4 v246, s[12:15], s16 offen offset:0 lds\n"
"v_mfma_f32_16x16x32_bf16 a[96:99], a[224:227], v[20:23], a[96:99]\n"
"v_exp_f32 v0, v0\n"
"v_mfma_f32_16x16x32_bf16 a[100:103], a[228:231], v[20:23], a[100:103]\n"
"ds_read_b128 a[176:179], v240 offset:12288\n"
"v_mfma_f32_16x16x32_bf16 a[104:107], a[232:235], v[20:23], a[104:107]\n"
"v_exp_f32 v1, v1\n"
"v_mfma_f32_16x16x32_bf16 a[108:111], a[236:239], v[20:23], a[108:111]\n"
"ds_read_b128 a[180:183], v240 offset:12544\n"
"v_cvt_pk_bf16_f32 v16, v0, v1\n"
"v_mfma_f32_16x16x32_bf16 a[112:115], a[240:243], v[20:23], a[112:115]\n"
"v_exp_f32 v2, v2\n"
"v_mfma_f32_16x16x32_bf16 a[116:119], a[244:247], v[20:23], a[116:119]\n"
"ds_read_b128 a[184:187], v240 offset:12800\n"
"v_mfma_f32_16x16x32_bf16 a[120:123], a[248:251], v[20:23], a[120:123]\n"
"v_exp_f32 v3, v3\n"
"v_mfma_f32_16x16x32_bf16 a[124:127], a[252:255], v[20:23], a[124:127]\n"
"v_cvt_pk_bf16_f32 v17, v2, v3\n"
"v_mfma_f32_16x16x32_bf16 v[56:59], v[40:43], v[20:23], v[56:59]\n"
"v_exp_f32 v4, v4\n"
"v_mfma_f32_16x16x32_bf16 v[8:11], a[128:131], v[80:83], v[28:31]\n"
"ds_read_b128 a[188:191], v240 offset:13056\n"
"v_mfma_f32_16x16x32_bf16 v[8:11], a[132:135], v[84:87], v[8:11]\n"
"v_exp_f32 v5, v5\n"
"v_mfma_f32_16x16x32_bf16 v[8:11], a[136:139], v[88:91], v[8:11]\n"
"ds_read_b64_tr_b16 a[224:225], v242 offset:24576\n"
"v_cvt_pk_bf16_f32 v18, v4, v5\n"
"v_mfma_f32_16x16x32_bf16 v[8:11], a[140:143], v[92:95], v[8:11]\n"
"v_exp_f32 v6, v6\n"
"v_mfma_f32_16x16x32_bf16 v[12:15], a[144:147], v[80:83], v[28:31]\n"
"ds_read_b64_tr_b16 a[226:227], v242 offset:28672\n"
"v_mfma_f32_16x16x32_bf16 v[12:15], a[148:151], v[84:87], v[12:15]\n"
"v_exp_f32 v7, v7\n"
"v_mfma_f32_16x16x32_bf16 v[12:15], a[152:155], v[88:91], v[12:15]\n"
"ds_read_b64_tr_b16 a[228:229], v243 offset:24576\n"
"v_cvt_pk_bf16_f32 v19, v6, v7\n"
"v_mfma_f32_16x16x32_bf16 v[12:15], a[156:159], v[92:95], v[12:15]\n"
"s_waitcnt lgkmcnt(11)\n"
"buffer_load_dwordx4 v247, s[12:15], s16 offen offset:1024 lds\n"
"v_mfma_f32_16x16x32_bf16 a[0:3], a[192:195], v[16:19], a[0:3]\n"
"v_exp_f32 v8, v8\n"
"v_mfma_f32_16x16x32_bf16 a[4:7], a[196:199], v[16:19], a[4:7]\n"
"ds_read_b64_tr_b16 a[230:231], v243 offset:28672\n"
"v_mfma_f32_16x16x32_bf16 a[8:11], a[200:203], v[16:19], a[8:11]\n"
"v_exp_f32 v9, v9\n"
"v_mfma_f32_16x16x32_bf16 a[12:15], a[204:207], v[16:19], a[12:15]\n"
"ds_read_b64_tr_b16 a[232:233], v242 offset:24832\n"
"v_cvt_pk_bf16_f32 v20, v8, v9\n"
"v_mfma_f32_16x16x32_bf16 a[16:19], a[208:211], v[16:19], a[16:19]\n"
"v_exp_f32 v10, v10\n"
"v_mfma_f32_16x16x32_bf16 a[20:23], a[212:215], v[16:19], a[20:23]\n"
"ds_read_b64_tr_b16 a[234:235], v242 offset:28928\n"
"v_mfma_f32_16x16x32_bf16 a[24:27], a[216:219], v[16:19], a[24:27]\n"
"v_exp_f32 v11, v11\n"
"v_mfma_f32_16x16x32_bf16 a[28:31], a[220:223], v[16:19], a[28:31]\n"
"v_cvt_pk_bf16_f32 v21, v10, v11\n"
"v_mfma_f32_16x16x32_bf16 v[44:47], v[40:43], v[16:19], v[44:47]\n"
"v_exp_f32 v12, v12\n"
"v_mfma_f32_16x16x32_bf16 v[0:3], a[128:131], v[96:99], v[32:35]\n"
"ds_read_b64_tr_b16 a[236:237], v243 offset:24832\n"
"v_mfma_f32_16x16x32_bf16 v[0:3], a[132:135], v[100:103], v[0:3]\n"
"v_exp_f32 v13, v13\n"
"v_mfma_f32_16x16x32_bf16 v[0:3], a[136:139], v[104:107], v[0:3]\n"
"ds_read_b64_tr_b16 a[238:239], v243 offset:28928\n"
"v_cvt_pk_bf16_f32 v22, v12, v13\n"
"v_mfma_f32_16x16x32_bf16 v[0:3], a[140:143], v[108:111], v[0:3]\n"
"v_exp_f32 v14, v14\n"
"v_mfma_f32_16x16x32_bf16 v[4:7], a[144:147], v[96:99], v[32:35]\n"
"ds_read_b64_tr_b16 a[240:241], v242 offset:25088\n"
"v_mfma_f32_16x16x32_bf16 v[4:7], a[148:151], v[100:103], v[4:7]\n"
"v_exp_f32 v15, v15\n"
"v_mfma_f32_16x16x32_bf16 v[4:7], a[152:155], v[104:107], v[4:7]\n"
"ds_read_b64_tr_b16 a[242:243], v242 offset:29184\n"
"v_cvt_pk_bf16_f32 v23, v14, v15\n"
"v_mfma_f32_16x16x32_bf16 v[4:7], a[156:159], v[108:111], v[4:7]\n"
"buffer_load_dwordx4 v246, s[12:15], s16 offen offset:2048 lds\n"
"v_mfma_f32_16x16x32_bf16 a[32:35], a[192:195], v[20:23], a[32:35]\n"
"v_exp_f32 v0, v0\n"
"v_mfma_f32_16x16x32_bf16 a[36:39], a[196:199], v[20:23], a[36:39]\n"
"ds_read_b64_tr_b16 a[244:245], v243 offset:25088\n"
"v_mfma_f32_16x16x32_bf16 a[40:43], a[200:203], v[20:23], a[40:43]\n"
"v_exp_f32 v1, v1\n"
"v_mfma_f32_16x16x32_bf16 a[44:47], a[204:207], v[20:23], a[44:47]\n"
"ds_read_b64_tr_b16 a[246:247], v243 offset:29184\n"
"v_cvt_pk_bf16_f32 v16, v0, v1\n"
"v_mfma_f32_16x16x32_bf16 a[48:51], a[208:211], v[20:23], a[48:51]\n"
"v_exp_f32 v2, v2\n"
"v_mfma_f32_16x16x32_bf16 a[52:55], a[212:215], v[20:23], a[52:55]\n"
"ds_read_b64_tr_b16 a[248:249], v242 offset:25344\n"
"v_mfma_f32_16x16x32_bf16 a[56:59], a[216:219], v[20:23], a[56:59]\n"
"v_exp_f32 v3, v3\n"
"v_mfma_f32_16x16x32_bf16 a[60:63], a[220:223], v[20:23], a[60:63]\n"
"v_cvt_pk_bf16_f32 v17, v2, v3\n"
"v_mfma_f32_16x16x32_bf16 v[48:51], v[40:43], v[20:23], v[48:51]\n"
"v_exp_f32 v4, v4\n"
"v_mfma_f32_16x16x32_bf16 v[8:11], a[128:131], v[112:115], v[36:39]\n"
"ds_read_b64_tr_b16 a[250:251], v242 offset:29440\n"
"v_mfma_f32_16x16x32_bf16 v[8:11], a[132:135], v[116:119], v[8:11]\n"
"v_exp_f32 v5, v5\n"
"v_mfma_f32_16x16x32_bf16 v[8:11], a[136:139], v[120:123], v[8:11]\n"
"ds_read_b64_tr_b16 a[252:253], v243 offset:25344\n"
"v_cvt_pk_bf16_f32 v18, v4, v5\n"
"v_mfma_f32_16x16x32_bf16 v[8:11], a[140:143], v[124:127], v[8:11]\n"
"v_exp_f32 v6, v6\n"
"v_mfma_f32_16x16x32_bf16 v[12:15], a[144:147], v[112:115], v[36:39]\n"
"ds_read_b64_tr_b16 a[254:255], v243 offset:29440\n"
"v_mfma_f32_16x16x32_bf16 v[12:15], a[148:151], v[116:119], v[12:15]\n"
"v_exp_f32 v7, v7\n"
"v_mfma_f32_16x16x32_bf16 v[12:15], a[152:155], v[120:123], v[12:15]\n"
"v_cvt_pk_bf16_f32 v19, v6, v7\n"
"v_mfma_f32_16x16x32_bf16 v[12:15], a[156:159], v[124:127], v[12:15]\n"
"buffer_load_dwordx4 v247, s[12:15], s16 offen offset:3072 lds\n"
"v_mfma_f32_16x16x32_bf16 a[64:67], a[192:195], v[16:19], a[64:67]\n"
"s_add_u32 s16, s16, 16384\n"
"v_exp_f32 v8, v8\n"
"v_mfma_f32_16x16x32_bf16 a[68:71], a[196:199], v[16:19], a[68:71]\n"
"s_nop 0\n"
"v_mfma_f32_16x16x32_bf16 a[72:75], a[200:203], v[16:19], a[72:75]\n"
"v_exp_f32 v9, v9\n"
"v_mfma_f32_16x16x32_bf16 a[76:79], a[204:207], v[16:19], a[76:79]\n"
"v_cvt_pk_bf16_f32 v20, v8, v9\n"
"v_mfma_f32_16x16x32_bf16 a[80:83], a[208:211], v[16:19], a[80:83]\n"
"v_exp_f32 v10, v10\n"
"v_mfma_f32_16x16x32_bf16 a[84:87], a[212:215], v[16:19], a[84:87]\n"
"s_nop 0\n"
"v_mfma_f32_16x16x32_bf16 a[88:91], a[216:219], v[16:19], a[88:91]\n"
"v_exp_f32 v11, v11\n"
"v_mfma_f32_16x16x32_bf16 a[92:95], a[220:223], v[16:19], a[92:95]\n"
"s_waitcnt vmcnt(16) lgkmcnt(0)\n"
"s_barrier\n"
"v_cvt_pk_bf16_f32 v21, v10, v11\n"
"v_mfma_f32_16x16x32_bf16 v[52:55], v[40:43], v[16:19], v[52:55]\n"
"v_exp_f32 v12, v12\n"
"v_mfma_f32_16x16x32_bf16 v[0:3], a[160:163], v[64:67], v[24:27]\n"
"ds_read_b128 a[128:131], v240 offset:32768\n"
"v_mfma_f32_16x16x32_bf16 v[0:3], a[164:167], v[68:71], v[0:3]\n"
"v_exp_f32 v13, v13\n"
"v_mfma_f32_16x16x32_bf16 v[0:3], a[168:171], v[72:75], v[0:3]\n"
"ds_read_b128 a[132:135], v240 offset:33024\n"
"v_cvt_pk_bf16_f32 v22, v12, v13\n"
"v_mfma_f32_16x16x32_bf16 v[0:3], a[172:175], v[76:79], v[0:3]\n"
"v_exp_f32 v14, v14\n"
"v_mfma_f32_16x16x32_bf16 v[4:7], a[176:179], v[64:67], v[24:27]\n"
"ds_read_b128 a[136:139], v240 offset:33280\n"
"v_mfma_f32_16x16x32_bf16 v[4:7], a[180:183], v[68:71], v[4:7]\n"
"v_exp_f32 v15, v15\n"
"v_mfma_f32_16x16x32_bf16 v[4:7], a[184:187], v[72:75], v[4:7]\n"
"s_mov_b32 m0, s53\n"
"ds_read_b128 a[140:143], v240 offset:33536\n"
"v_cvt_pk_bf16_f32 v23, v14, v15\n"
"v_mfma_f32_16x16x32_bf16 v[4:7], a[188:191], v[76:79], v[4:7]\n"
"buffer_load_dwordx4 v204, s[28:31], s52 offen offset:0 lds\n"
"v_mfma_f32_16x16x32_bf16 a[96:99], a[192:195], v[20:23], a[96:99]\n"
"s_add_u32 s52, s52, 8192\n"
"v_exp_f32 v0, v0\n"
"v_mfma_f32_16x16x32_bf16 a[100:103], a[196:199], v[20:23], a[100:103]\n"
"ds_read_b128 a[144:147], v240 offset:36864\n"
"v_mfma_f32_16x16x32_bf16 a[104:107], a[200:203], v[20:23], a[104:107]\n"
"v_exp_f32 v1, v1\n"
"v_mfma_f32_16x16x32_bf16 a[108:111], a[204:207], v[20:23], a[108:111]\n"
"ds_read_b128 a[148:151], v240 offset:37120\n"
"v_cvt_pk_bf16_f32 v16, v0, v1\n"
"v_mfma_f32_16x16x32_bf16 a[112:115], a[208:211], v[20:23], a[112:115]\n"
"v_exp_f32 v2, v2\n"
"v_mfma_f32_16x16x32_bf16 a[116:119], a[212:215], v[20:23], a[116:119]\n"
"ds_read_b128 a[152:155], v240 offset:37376\n"
"v_mfma_f32_16x16x32_bf16 a[120:123], a[216:219], v[20:23], a[120:123]\n"
"v_exp_f32 v3, v3\n"
"v_mfma_f32_16x16x32_bf16 a[124:127], a[220:223], v[20:23], a[124:127]\n"
"v_cvt_pk_bf16_f32 v17, v2, v3\n"
"v_mfma_f32_16x16x32_bf16 v[56:59], v[40:43], v[20:23], v[56:59]\n"
"v_exp_f32 v4, v4\n"
"v_mfma_f32_16x16x32_bf16 v[8:11], a[160:163], v[80:83], v[28:31]\n"
"ds_read_b128 a[156:159], v240 offset:37632\n"
"v_mfma_f32_16x16x32_bf16 v[8:11], a[164:167], v[84:87], v[8:11]\n"
"v_exp_f32 v5, v5\n"
"v_mfma_f32_16x16x32_bf16 v[8:11], a[168:171], v[88:91], v[8:11]\n"
"ds_read_b64_tr_b16 a[192:193], v242 offset:49152\n"
"v_cvt_pk_bf16_f32 v18, v4, v5\n"
"v_mfma_f32_16x16x32_bf16 v[8:11], a[172:175], v[92:95], v[8:11]\n"
"v_exp_f32 v6, v6\n"
"v_mfma_f32_16x16x32_bf16 v[12:15], a[176:179], v[80:83], v[28:31]\n"
"ds_read_b64_tr_b16 a[194:195], v242 offset:53248\n"
"v_mfma_f32_16x16x32_bf16 v[12:15], a[180:183], v[84:87], v[12:15]\n"
"v_exp_f32 v7, v7\n"
"v_mfma_f32_16x16x32_bf16 v[12:15], a[184:187], v[88:91], v[12:15]\n"
"ds_read_b64_tr_b16 a[196:197], v243 offset:49152\n"
"v_cvt_pk_bf16_f32 v19, v6, v7\n"
"v_mfma_f32_16x16x32_bf16 v[12:15], a[188:191], v[92:95], v[12:15]\n"
"s_waitcnt lgkmcnt(11)\n"
"buffer_load_dwordx4 v204, s[28:31], s52 offen offset:1024 lds\n"
"v_mfma_f32_16x16x32_bf16 a[0:3], a[224:227], v[16:19], a[0:3]\n"
"s_add_u32 s52, s52, 8192\n"
"v_exp_f32 v8, v8\n"
"v_mfma_f32_16x16x32_bf16 a[4:7], a[228:231], v[16:19], a[4:7]\n"
"ds_read_b64_tr_b16 a[198:199], v243 offset:53248\n"
"v_mfma_f32_16x16x32_bf16 a[8:11], a[232:235], v[16:19], a[8:11]\n"
"v_exp_f32 v9, v9\n"
"v_mfma_f32_16x16x32_bf16 a[12:15], a[236:239], v[16:19], a[12:15]\n"
"ds_read_b64_tr_b16 a[200:201], v242 offset:49408\n"
"v_cvt_pk_bf16_f32 v20, v8, v9\n"
"v_mfma_f32_16x16x32_bf16 a[16:19], a[240:243], v[16:19], a[16:19]\n"
"v_exp_f32 v10, v10\n"
"v_mfma_f32_16x16x32_bf16 a[20:23], a[244:247], v[16:19], a[20:23]\n"
"ds_read_b64_tr_b16 a[202:203], v242 offset:53504\n"
"v_mfma_f32_16x16x32_bf16 a[24:27], a[248:251], v[16:19], a[24:27]\n"
"v_exp_f32 v11, v11\n"
"v_mfma_f32_16x16x32_bf16 a[28:31], a[252:255], v[16:19], a[28:31]\n"
"v_cvt_pk_bf16_f32 v21, v10, v11\n"
"v_mfma_f32_16x16x32_bf16 v[44:47], v[40:43], v[16:19], v[44:47]\n"
"v_exp_f32 v12, v12\n"
"v_mfma_f32_16x16x32_bf16 v[0:3], a[160:163], v[96:99], v[32:35]\n"
"ds_read_b64_tr_b16 a[204:205], v243 offset:49408\n"
"v_mfma_f32_16x16x32_bf16 v[0:3], a[164:167], v[100:103], v[0:3]\n"
"v_exp_f32 v13, v13\n"
"v_mfma_f32_16x16x32_bf16 v[0:3], a[168:171], v[104:107], v[0:3]\n"
"ds_read_b64_tr_b16 a[206:207], v243 offset:53504\n"
"v_cvt_pk_bf16_f32 v22, v12, v13\n"
"v_mfma_f32_16x16x32_bf16 v[0:3], a[172:175], v[108:111], v[0:3]\n"
"v_exp_f32 v14, v14\n"
"v_mfma_f32_16x16x32_bf16 v[4:7], a[176:179], v[96:99], v[32:35]\n"
"ds_read_b64_tr_b16 a[208:209], v242 offset:49664\n"
"v_mfma_f32_16x16x32_bf16 v[4:7], a[180:183], v[100:103], v[4:7]\n"
"v_exp_f32 v15, v15\n"
"v_mfma_f32_16x16x32_bf16 v[4:7], a[184:187], v[104:107], v[4:7]\n"
"ds_read_b64_tr_b16 a[210:211], v242 offset:53760\n"
"v_cvt_pk_bf16_f32 v23, v14, v15\n"
"v_mfma_f32_16x16x32_bf16 v[4:7], a[188:191], v[108:111], v[4:7]\n"
"buffer_load_dwordx4 v204, s[28:31], s52 offen offset:2048 lds\n"
"v_mfma_f32_16x16x32_bf16 a[32:35], a[224:227], v[20:23], a[32:35]\n"
"s_add_u32 s52, s52, 8192\n"
"v_exp_f32 v0, v0\n"
"v_mfma_f32_16x16x32_bf16 a[36:39], a[228:231], v[20:23], a[36:39]\n"
"ds_read_b64_tr_b16 a[212:213], v243 offset:49664\n"
"v_mfma_f32_16x16x32_bf16 a[40:43], a[232:235], v[20:23], a[40:43]\n"
"v_exp_f32 v1, v1\n"
"v_mfma_f32_16x16x32_bf16 a[44:47], a[236:239], v[20:23], a[44:47]\n"
"ds_read_b64_tr_b16 a[214:215], v243 offset:53760\n"
"v_cvt_pk_bf16_f32 v16, v0, v1\n"
"v_mfma_f32_16x16x32_bf16 a[48:51], a[240:243], v[20:23], a[48:51]\n"
"v_exp_f32 v2, v2\n"
"v_mfma_f32_16x16x32_bf16 a[52:55], a[244:247], v[20:23], a[52:55]\n"
"ds_read_b64_tr_b16 a[216:217], v242 offset:49920\n"
"v_mfma_f32_16x16x32_bf16 a[56:59], a[248:251], v[20:23], a[56:59]\n"
"v_exp_f32 v3, v3\n"
"v_mfma_f32_16x16x32_bf16 a[60:63], a[252:255], v[20:23], a[60:63]\n"
"v_cvt_pk_bf16_f32 v17, v2, v3\n"
"v_mfma_f32_16x16x32_bf16 v[48:51], v[40:43], v[20:23], v[48:51]\n"
"v_exp_f32 v4, v4\n"
"v_mfma_f32_16x16x32_bf16 v[8:11], a[160:163], v[112:115], v[36:39]\n"
"ds_read_b64_tr_b16 a[218:219], v242 offset:54016\n"
"v_mfma_f32_16x16x32_bf16 v[8:11], a[164:167], v[116:119], v[8:11]\n"
"v_exp_f32 v5, v5\n"
"v_mfma_f32_16x16x32_bf16 v[8:11], a[168:171], v[120:123], v[8:11]\n"
"ds_read_b64_tr_b16 a[220:221], v243 offset:49920\n"
"v_cvt_pk_bf16_f32 v18, v4, v5\n"
"v_mfma_f32_16x16x32_bf16 v[8:11], a[172:175], v[124:127], v[8:11]\n"
"v_exp_f32 v6, v6\n"
"v_mfma_f32_16x16x32_bf16 v[12:15], a[176:179], v[112:115], v[36:39]\n"
"ds_read_b64_tr_b16 a[222:223], v243 offset:54016\n"
"v_mfma_f32_16x16x32_bf16 v[12:15], a[180:183], v[116:119], v[12:15]\n"
"v_exp_f32 v7, v7\n"
"v_mfma_f32_16x16x32_bf16 v[12:15], a[184:187], v[120:123], v[12:15]\n"
"v_cvt_pk_bf16_f32 v19, v6, v7\n"
"v_mfma_f32_16x16x32_bf16 v[12:15], a[188:191], v[124:127], v[12:15]\n"
"buffer_load_dwordx4 v204, s[28:31], s52 offen offset:3072 lds\n"
"v_mfma_f32_16x16x32_bf16 a[64:67], a[224:227], v[16:19], a[64:67]\n"
"s_add_u32 s52, s52, 12288\n"
"v_exp_f32 v8, v8\n"
"v_mfma_f32_16x16x32_bf16 a[68:71], a[228:231], v[16:19], a[68:71]\n"
"s_nop 0\n"
"v_mfma_f32_16x16x32_bf16 a[72:75], a[232:235], v[16:19], a[72:75]\n"
"v_exp_f32 v9, v9\n"
"v_mfma_f32_16x16x32_bf16 a[76:79], a[236:239], v[16:19], a[76:79]\n"
"v_cvt_pk_bf16_f32 v20, v8, v9\n"
"v_mfma_f32_16x16x32_bf16 a[80:83], a[240:243], v[16:19], a[80:83]\n"
"v_exp_f32 v10, v10\n"
"v_mfma_f32_16x16x32_bf16 a[84:87], a[244:247], v[16:19], a[84:87]\n"
"s_nop 0\n"
"v_mfma_f32_16x16x32_bf16 a[88:91], a[248:251], v[16:19], a[88:91]\n"
"v_exp_f32 v11, v11\n"
"v_mfma_f32_16x16x32_bf16 a[92:95], a[252:255], v[16:19], a[92:95]\n"
"s_waitcnt lgkmcnt(8)\n"
"v_cvt_pk_bf16_f32 v21, v10, v11\n"
"v_mfma_f32_16x16x32_bf16 v[52:55], v[40:43], v[16:19], v[52:55]\n"
"v_exp_f32 v12, v12\n"
"v_mfma_f32_16x16x32_bf16 v[0:3], a[128:131], v[64:67], v[24:27]\n"
"ds_read_b128 a[160:163], v240 offset:40960\n"
"v_mfma_f32_16x16x32_bf16 v[0:3], a[132:135], v[68:71], v[0:3]\n"
"v_exp_f32 v13, v13\n"
"v_mfma_f32_16x16x32_bf16 v[0:3], a[136:139], v[72:75], v[0:3]\n"
"ds_read_b128 a[164:167], v240 offset:41216\n"
"v_cvt_pk_bf16_f32 v22, v12, v13\n"
"v_mfma_f32_16x16x32_bf16 v[0:3], a[140:143], v[76:79], v[0:3]\n"
"v_exp_f32 v14, v14\n"
"v_mfma_f32_16x16x32_bf16 v[4:7], a[144:147], v[64:67], v[24:27]\n"
"ds_read_b128 a[168:171], v240 offset:41472\n"
"v_mfma_f32_16x16x32_bf16 v[4:7], a[148:151], v[68:71], v[4:7]\n"
"v_exp_f32 v15, v15\n"
"v_mfma_f32_16x16x32_bf16 v[4:7], a[152:155], v[72:75], v[4:7]\n"
"s_add_u32 m0, s53, 4096\n"
"ds_read_b128 a[172:175], v240 offset:41728\n"
"v_cvt_pk_bf16_f32 v23, v14, v15\n"
"v_mfma_f32_16x16x32_bf16 v[4:7], a[156:159], v[76:79], v[4:7]\n"
"buffer_load_dwordx4 v204, s[28:31], s52 offen offset:0 lds\n"
"v_mfma_f32_16x16x32_bf16 a[96:99], a[224:227], v[20:23], a[96:99]\n"
"s_add_u32 s52, s52, 8192\n"
"v_exp_f32 v0, v0\n"
"v_mfma_f32_16x16x32_bf16 a[100:103], a[228:231], v[20:23], a[100:103]\n"
"ds_read_b128 a[176:179], v240 offset:45056\n"
"v_mfma_f32_16x16x32_bf16 a[104:107], a[232:235], v[20:23], a[104:107]\n"
"v_exp_f32 v1, v1\n"
"v_mfma_f32_16x16x32_bf16 a[108:111], a[236:239], v[20:23], a[108:111]\n"
"ds_read_b128 a[180:183], v240 offset:45312\n"
"v_cvt_pk_bf16_f32 v16, v0, v1\n"
"v_mfma_f32_16x16x32_bf16 a[112:115], a[240:243], v[20:23], a[112:115]\n"
"v_exp_f32 v2, v2\n"
"v_mfma_f32_16x16x32_bf16 a[116:119], a[244:247], v[20:23], a[116:119]\n"
"ds_read_b128 a[184:187], v240 offset:45568\n"
"v_mfma_f32_16x16x32_bf16 a[120:123], a[248:251], v[20:23], a[120:123]\n"
"v_exp_f32 v3, v3\n"
"v_mfma_f32_16x16x32_bf16 a[124:127], a[252:255], v[20:23], a[124:127]\n"
"v_cvt_pk_bf16_f32 v17, v2, v3\n"
"v_mfma_f32_16x16x32_bf16 v[56:59], v[40:43], v[20:23], v[56:59]\n"
"v_exp_f32 v4, v4\n"
"v_mfma_f32_16x16x32_bf16 v[8:11], a[128:131], v[80:83], v[28:31]\n"
"ds_read_b128 a[188:191], v240 offset:45824\n"
"v_mfma_f32_16x16x32_bf16 v[8:11], a[132:135], v[84:87], v[8:11]\n"
"v_exp_f32 v5, v5\n"
"v_mfma_f32_16x16x32_bf16 v[8:11], a[136:139], v[88:91], v[8:11]\n"
"ds_read_b64_tr_b16 a[224:225], v242 offset:57344\n"
"v_cvt_pk_bf16_f32 v18, v4, v5\n"
"v_mfma_f32_16x16x32_bf16 v[8:11], a[140:143], v[92:95], v[8:11]\n"
"v_exp_f32 v6, v6\n"
"v_mfma_f32_16x16x32_bf16 v[12:15], a[144:147], v[80:83], v[28:31]\n"
"ds_read_b64_tr_b16 a[226:227], v242 offset:61440\n"
"v_mfma_f32_16x16x32_bf16 v[12:15], a[148:151], v[84:87], v[12:15]\n"
"v_exp_f32 v7, v7\n"
"v_mfma_f32_16x16x32_bf16 v[12:15], a[152:155], v[88:91], v[12:15]\n"
"ds_read_b64_tr_b16 a[228:229], v243 offset:57344\n"
"v_cvt_pk_bf16_f32 v19, v6, v7\n"
"v_mfma_f32_16x16x32_bf16 v[12:15], a[156:159], v[92:95], v[12:15]\n"
"s_waitcnt lgkmcnt(11)\n"
"buffer_load_dwordx4 v204, s[28:31], s52 offen offset:1024 lds\n"
"v_mfma_f32_16x16x32_bf16 a[0:3], a[192:195], v[16:19], a[0:3]\n"
"s_add_u32 s52, s52, 8192\n"
"v_exp_f32 v8, v8\n"
"v_mfma_f32_16x16x32_bf16 a[4:7], a[196:199], v[16:19], a[4:7]\n"
"ds_read_b64_tr_b16 a[230:231], v243 offset:61440\n"
"v_mfma_f32_16x16x32_bf16 a[8:11], a[200:203], v[16:19], a[8:11]\n"
"v_exp_f32 v9, v9\n"
"v_mfma_f32_16x16x32_bf16 a[12:15], a[204:207], v[16:19], a[12:15]\n"
"ds_read_b64_tr_b16 a[232:233], v242 offset:57600\n"
"v_cvt_pk_bf16_f32 v20, v8, v9\n"
"v_mfma_f32_16x16x32_bf16 a[16:19], a[208:211], v[16:19], a[16:19]\n"
"v_exp_f32 v10, v10\n"
"v_mfma_f32_16x16x32_bf16 a[20:23], a[212:215], v[16:19], a[20:23]\n"
"ds_read_b64_tr_b16 a[234:235], v242 offset:61696\n"
"v_mfma_f32_16x16x32_bf16 a[24:27], a[216:219], v[16:19], a[24:27]\n"
"v_exp_f32 v11, v11\n"
"v_mfma_f32_16x16x32_bf16 a[28:31], a[220:223], v[16:19], a[28:31]\n"
"v_cvt_pk_bf16_f32 v21, v10, v11\n"
"v_mfma_f32_16x16x32_bf16 v[44:47], v[40:43], v[16:19], v[44:47]\n"
"v_exp_f32 v12, v12\n"
"v_mfma_f32_16x16x32_bf16 v[0:3], a[128:131], v[96:99], v[32:35]\n"
"ds_read_b64_tr_b16 a[236:237], v243 offset:57600\n"
"v_mfma_f32_16x16x32_bf16 v[0:3], a[132:135], v[100:103], v[0:3]\n"
"v_exp_f32 v13, v13\n"
"v_mfma_f32_16x16x32_bf16 v[0:3], a[136:139], v[104:107], v[0:3]\n"
"ds_read_b64_tr_b16 a[238:239], v243 offset:61696\n"
"v_cvt_pk_bf16_f32 v22, v12, v13\n"
"v_mfma_f32_16x16x32_bf16 v[0:3], a[140:143], v[108:111], v[0:3]\n"
"v_exp_f32 v14, v14\n"
"v_mfma_f32_16x16x32_bf16 v[4:7], a[144:147], v[96:99], v[32:35]\n"
"ds_read_b64_tr_b16 a[240:241], v242 offset:57856\n"
"v_mfma_f32_16x16x32_bf16 v[4:7], a[148:151], v[100:103], v[4:7]\n"
"v_exp_f32 v15, v15\n"
"v_mfma_f32_16x16x32_bf16 v[4:7], a[152:155], v[104:107], v[4:7]\n"
"ds_read_b64_tr_b16 a[242:243], v242 offset:61952\n"
"v_cvt_pk_bf16_f32 v23, v14, v15\n"
"v_mfma_f32_16x16x32_bf16 v[4:7], a[156:159], v[108:111], v[4:7]\n"
"buffer_load_dwordx4 v204, s[28:31], s52 offen offset:2048 lds\n"
"v_mfma_f32_16x16x32_bf16 a[32:35], a[192:195], v[20:23], a[32:35]\n"
"s_add_u32 s52, s52, 8192\n"
"v_exp_f32 v0, v0\n"
"v_mfma_f32_16x16x32_bf16 a[36:39], a[196:199], v[20:23], a[36:39]\n"
"ds_read_b64_tr_b16 a[244:245], v243 offset:57856\n"
"v_mfma_f32_16x16x32_bf16 a[40:43], a[200:203], v[20:23], a[40:43]\n"
"v_exp_f32 v1, v1\n"
"v_mfma_f32_16x16x32_bf16 a[44:47], a[204:207], v[20:23], a[44:47]\n"
"ds_read_b64_tr_b16 a[246:247], v243 offset:61952\n"
"v_cvt_pk_bf16_f32 v16, v0, v1\n"
"v_mfma_f32_16x16x32_bf16 a[48:51], a[208:211], v[20:23], a[48:51]\n"
"v_exp_f32 v2, v2\n"
"v_mfma_f32_16x16x32_bf16 a[52:55], a[212:215], v[20:23], a[52:55]\n"
"ds_read_b64_tr_b16 a[248:249], v242 offset:58112\n"
"v_mfma_f32_16x16x32_bf16 a[56:59], a[216:219], v[20:23], a[56:59]\n"
"v_exp_f32 v3, v3\n"
"v_mfma_f32_16x16x32_bf16 a[60:63], a[220:223], v[20:23], a[60:63]\n"
"v_cvt_pk_bf16_f32 v17, v2, v3\n"
"v_mfma_f32_16x16x32_bf16 v[48:51], v[40:43], v[20:23], v[48:51]\n"
"v_exp_f32 v4, v4\n"
"v_mfma_f32_16x16x32_bf16 v[8:11], a[128:131], v[112:115], v[36:39]\n"
"ds_read_b64_tr_b16 a[250:251], v242 offset:62208\n"
"v_mfma_f32_16x16x32_bf16 v[8:11], a[132:135], v[116:119], v[8:11]\n"
"v_exp_f32 v5, v5\n"
"v_mfma_f32_16x16x32_bf16 v[8:11], a[136:139], v[120:123], v[8:11]\n"
"ds_read_b64_tr_b16 a[252:253], v243 offset:58112\n"
"v_cvt_pk_bf16_f32 v18, v4, v5\n"
"v_mfma_f32_16x16x32_bf16 v[8:11], a[140:143], v[124:127], v[8:11]\n"
"v_exp_f32 v6, v6\n"
"v_mfma_f32_16x16x32_bf16 v[12:15], a[144:147], v[112:115], v[36:39]\n"
"ds_read_b64_tr_b16 a[254:255], v243 offset:62208\n"
"v_mfma_f32_16x16x32_bf16 v[12:15], a[148:151], v[116:119], v[12:15]\n"
"v_exp_f32 v7, v7\n"
"v_mfma_f32_16x16x32_bf16 v[12:15], a[152:155], v[120:123], v[12:15]\n"
"v_cvt_pk_bf16_f32 v19, v6, v7\n"
"v_mfma_f32_16x16x32_bf16 v[12:15], a[156:159], v[124:127], v[12:15]\n"
"buffer_load_dwordx4 v204, s[28:31], s52 offen offset:3072 lds\n"
"v_mfma_f32_16x16x32_bf16 a[64:67], a[192:195], v[16:19], a[64:67]\n"
"s_sub_u32 s52, s52, 61312\n"
"v_exp_f32 v8, v8\n"
"v_mfma_f32_16x16x32_bf16 a[68:71], a[196:199], v[16:19], a[68:71]\n"
"s_nop 0\n"
"v_mfma_f32_16x16x32_bf16 a[72:75], a[200:203], v[16:19], a[72:75]\n"
"v_exp_f32 v9, v9\n"
"v_mfma_f32_16x16x32_bf16 a[76:79], a[204:207], v[16:19], a[76:79]\n"
"v_cvt_pk_bf16_f32 v20, v8, v9\n"
"v_mfma_f32_16x16x32_bf16 a[80:83], a[208:211], v[16:19], a[80:83]\n"
"v_exp_f32 v10, v10\n"
"v_mfma_f32_16x16x32_bf16 a[84:87], a[212:215], v[16:19], a[84:87]\n"
"s_nop 0\n"
"v_mfma_f32_16x16x32_bf16 a[88:91], a[216:219], v[16:19], a[88:91]\n"
"v_exp_f32 v11, v11\n"
"v_mfma_f32_16x16x32_bf16 a[92:95], a[220:223], v[16:19], a[92:95]\n"
"s_waitcnt vmcnt(16) lgkmcnt(0)\n"
"s_barrier\n"
"v_cvt_pk_bf16_f32 v21, v10, v11\n"
"v_mfma_f32_16x16x32_bf16 v[52:55], v[40:43], v[16:19], v[52:55]\n"
"v_exp_f32 v12, v12\n"
"v_mfma_f32_16x16x32_bf16 v[0:3], a[160:163], v[64:67], v[24:27]\n"
"ds_read_b128 a[128:131], v241 offset:0\n"
"v_mfma_f32_16x16x32_bf16 v[0:3], a[164:167], v[68:71], v[0:3]\n"
"v_exp_f32 v13, v13\n"
"v_mfma_f32_16x16x32_bf16 v[0:3], a[168:171], v[72:75], v[0:3]\n"
"ds_read_b128 a[132:135], v241 offset:256\n"
"v_cvt_pk_bf16_f32 v22, v12, v13\n"
"v_mfma_f32_16x16x32_bf16 v[0:3], a[172:175], v[76:79], v[0:3]\n"
"v_exp_f32 v14, v14\n"
"v_mfma_f32_16x16x32_bf16 v[4:7], a[176:179], v[64:67], v[24:27]\n"
"ds_read_b128 a[136:139], v241 offset:512\n"
"v_mfma_f32_16x16x32_bf16 v[4:7], a[180:183], v[68:71], v[4:7]\n"
"v_exp_f32 v15, v15\n"
"v_mfma_f32_16x16x32_bf16 v[4:7], a[184:187], v[72:75], v[4:7]\n"
"s_add_u32 m0, s53, 32768\n"
"ds_read_b128 a[140:143], v241 offset:768\n"
"v_cvt_pk_bf16_f32 v23, v14, v15\n"
"v_mfma_f32_16x16x32_bf16 v[4:7], a[188:191], v[76:79], v[4:7]\n"
"buffer_load_dwordx4 v204, s[28:31], s52 offen offset:0 lds\n"
"v_mfma_f32_16x16x32_bf16 a[96:99], a[192:195], v[20:23], a[96:99]\n"
"s_add_u32 s52, s52, 8192\n"
"v_exp_f32 v0, v0\n"
"v_mfma_f32_16x16x32_bf16 a[100:103], a[196:199], v[20:23], a[100:103]\n"
"ds_read_b128 a[144:147], v241 offset:4096\n"
"v_mfma_f32_16x16x32_bf16 a[104:107], a[200:203], v[20:23], a[104:107]\n"
"v_exp_f32 v1, v1\n"
"v_mfma_f32_16x16x32_bf16 a[108:111], a[204:207], v[20:23], a[108:111]\n"
"ds_read_b128 a[148:151], v241 offset:4352\n"
"v_cvt_pk_bf16_f32 v16, v0, v1\n"
"v_mfma_f32_16x16x32_bf16 a[112:115], a[208:211], v[20:23], a[112:115]\n"
"v_exp_f32 v2, v2\n"
"v_mfma_f32_16x16x32_bf16 a[116:119], a[212:215], v[20:23], a[116:119]\n"
"ds_read_b128 a[152:155], v241 offset:4608\n"
"v_mfma_f32_16x16x32_bf16 a[120:123], a[216:219], v[20:23], a[120:123]\n"
"v_exp_f32 v3, v3\n"
"v_mfma_f32_16x16x32_bf16 a[124:127], a[220:223], v[20:23], a[124:127]\n"
"v_cvt_pk_bf16_f32 v17, v2, v3\n"
"v_mfma_f32_16x16x32_bf16 v[56:59], v[40:43], v[20:23], v[56:59]\n"
"v_exp_f32 v4, v4\n"
"v_mfma_f32_16x16x32_bf16 v[8:11], a[160:163], v[80:83], v[28:31]\n"
"ds_read_b128 a[156:159], v241 offset:4864\n"
"v_mfma_f32_16x16x32_bf16 v[8:11], a[164:167], v[84:87], v[8:11]\n"
"v_exp_f32 v5, v5\n"
"v_mfma_f32_16x16x32_bf16 v[8:11], a[168:171], v[88:91], v[8:11]\n"
"ds_read_b64_tr_b16 a[192:193], v244 offset:16384\n"
"v_cvt_pk_bf16_f32 v18, v4, v5\n"
"v_mfma_f32_16x16x32_bf16 v[8:11], a[172:175], v[92:95], v[8:11]\n"
"v_exp_f32 v6, v6\n"
"v_mfma_f32_16x16x32_bf16 v[12:15], a[176:179], v[80:83], v[28:31]\n"
"ds_read_b64_tr_b16 a[194:195], v244 offset:20480\n"
"v_mfma_f32_16x16x32_bf16 v[12:15], a[180:183], v[84:87], v[12:15]\n"
"v_exp_f32 v7, v7\n"
"v_mfma_f32_16x16x32_bf16 v[12:15], a[184:187], v[88:91], v[12:15]\n"
"ds_read_b64_tr_b16 a[196:197], v245 offset:16384\n"
"v_cvt_pk_bf16_f32 v19, v6, v7\n"
"v_mfma_f32_16x16x32_bf16 v[12:15], a[188:191], v[92:95], v[12:15]\n"
"s_waitcnt lgkmcnt(11)\n"
"buffer_load_dwordx4 v204, s[28:31], s52 offen offset:1024 lds\n"
"v_mfma_f32_16x16x32_bf16 a[0:3], a[224:227], v[16:19], a[0:3]\n"
"s_add_u32 s52, s52, 8192\n"
"v_exp_f32 v8, v8\n"
"v_mfma_f32_16x16x32_bf16 a[4:7], a[228:231], v[16:19], a[4:7]\n"
"ds_read_b64_tr_b16 a[198:199], v245 offset:20480\n"
"v_mfma_f32_16x16x32_bf16 a[8:11], a[232:235], v[16:19], a[8:11]\n"
"v_exp_f32 v9, v9\n"
"v_mfma_f32_16x16x32_bf16 a[12:15], a[236:239], v[16:19], a[12:15]\n"
"ds_read_b64_tr_b16 a[200:201], v244 offset:16640\n"
"v_cvt_pk_bf16_f32 v20, v8, v9\n"
"v_mfma_f32_16x16x32_bf16 a[16:19], a[240:243], v[16:19], a[16:19]\n"
"v_exp_f32 v10, v10\n"
"v_mfma_f32_16x16x32_bf16 a[20:23], a[244:247], v[16:19], a[20:23]\n"
"ds_read_b64_tr_b16 a[202:203], v244 offset:20736\n"
"v_mfma_f32_16x16x32_bf16 a[24:27], a[248:251], v[16:19], a[24:27]\n"
"v_exp_f32 v11, v11\n"
"v_mfma_f32_16x16x32_bf16 a[28:31], a[252:255], v[16:19], a[28:31]\n"
"v_cvt_pk_bf16_f32 v21, v10, v11\n"
"v_mfma_f32_16x16x32_bf16 v[44:47], v[40:43], v[16:19], v[44:47]\n"
"v_exp_f32 v12, v12\n"
"v_mfma_f32_16x16x32_bf16 v[0:3], a[160:163], v[96:99], v[32:35]\n"
"ds_read_b64_tr_b16 a[204:205], v245 offset:16640\n"
"v_mfma_f32_16x16x32_bf16 v[0:3], a[164:167], v[100:103], v[0:3]\n"
"v_exp_f32 v13, v13\n"
"v_mfma_f32_16x16x32_bf16 v[0:3], a[168:171], v[104:107], v[0:3]\n"
"ds_read_b64_tr_b16 a[206:207], v245 offset:20736\n"
"v_cvt_pk_bf16_f32 v22, v12, v13\n"
"v_mfma_f32_16x16x32_bf16 v[0:3], a[172:175], v[108:111], v[0:3]\n"
"v_exp_f32 v14, v14\n"
"v_mfma_f32_16x16x32_bf16 v[4:7], a[176:179], v[96:99], v[32:35]\n"
"ds_read_b64_tr_b16 a[208:209], v244 offset:16896\n"
"v_mfma_f32_16x16x32_bf16 v[4:7], a[180:183], v[100:103], v[4:7]\n"
"v_exp_f32 v15, v15\n"
"v_mfma_f32_16x16x32_bf16 v[4:7], a[184:187], v[104:107], v[4:7]\n"
"ds_read_b64_tr_b16 a[210:211], v244 offset:20992\n"
"v_cvt_pk_bf16_f32 v23, v14, v15\n"
"v_mfma_f32_16x16x32_bf16 v[4:7], a[188:191], v[108:111], v[4:7]\n"
"buffer_load_dwordx4 v204, s[28:31], s52 offen offset:2048 lds\n"
"v_mfma_f32_16x16x32_bf16 a[32:35], a[224:227], v[20:23], a[32:35]\n"
"s_add_u32 s52, s52, 8192\n"
"v_exp_f32 v0, v0\n"
"v_mfma_f32_16x16x32_bf16 a[36:39], a[228:231], v[20:23], a[36:39]\n"
"ds_read_b64_tr_b16 a[212:213], v245 offset:16896\n"
"v_mfma_f32_16x16x32_bf16 a[40:43], a[232:235], v[20:23], a[40:43]\n"
"v_exp_f32 v1, v1\n"
"v_mfma_f32_16x16x32_bf16 a[44:47], a[236:239], v[20:23], a[44:47]\n"
"ds_read_b64_tr_b16 a[214:215], v245 offset:20992\n"
"v_cvt_pk_bf16_f32 v16, v0, v1\n"
"v_mfma_f32_16x16x32_bf16 a[48:51], a[240:243], v[20:23], a[48:51]\n"
"v_exp_f32 v2, v2\n"
"v_mfma_f32_16x16x32_bf16 a[52:55], a[244:247], v[20:23], a[52:55]\n"
"ds_read_b64_tr_b16 a[216:217], v244 offset:17152\n"
"v_mfma_f32_16x16x32_bf16 a[56:59], a[248:251], v[20:23], a[56:59]\n"
"v_exp_f32 v3, v3\n"
"v_mfma_f32_16x16x32_bf16 a[60:63], a[252:255], v[20:23], a[60:63]\n"
"v_cvt_pk_bf16_f32 v17, v2, v3\n"
"v_mfma_f32_16x16x32_bf16 v[48:51], v[40:43], v[20:23], v[48:51]\n"
"v_exp_f32 v4, v4\n"
"v_mfma_f32_16x16x32_bf16 v[8:11], a[160:163], v[112:115], v[36:39]\n"
"ds_read_b64_tr_b16 a[218:219], v244 offset:21248\n"
"v_mfma_f32_16x16x32_bf16 v[8:11], a[164:167], v[116:119], v[8:11]\n"
"v_exp_f32 v5, v5\n"
"v_mfma_f32_16x16x32_bf16 v[8:11], a[168:171], v[120:123], v[8:11]\n"
"ds_read_b64_tr_b16 a[220:221], v245 offset:17152\n"
"v_cvt_pk_bf16_f32 v18, v4, v5\n"
"v_mfma_f32_16x16x32_bf16 v[8:11], a[172:175], v[124:127], v[8:11]\n"
"v_exp_f32 v6, v6\n"
"v_mfma_f32_16x16x32_bf16 v[12:15], a[176:179], v[112:115], v[36:39]\n"
"ds_read_b64_tr_b16 a[222:223], v245 offset:21248\n"
"v_mfma_f32_16x16x32_bf16 v[12:15], a[180:183], v[116:119], v[12:15]\n"
"v_exp_f32 v7, v7\n"
"v_mfma_f32_16x16x32_bf16 v[12:15], a[184:187], v[120:123], v[12:15]\n"
"v_cvt_pk_bf16_f32 v19, v6, v7\n"
"v_mfma_f32_16x16x32_bf16 v[12:15], a[188:191], v[124:127], v[12:15]\n"
"buffer_load_dwordx4 v204, s[28:31], s52 offen offset:3072 lds\n"
"v_mfma_f32_16x16x32_bf16 a[64:67], a[224:227], v[16:19], a[64:67]\n"
"s_add_u32 s52, s52, 12288\n"
"v_exp_f32 v8, v8\n"
"v_mfma_f32_16x16x32_bf16 a[68:71], a[228:231], v[16:19], a[68:71]\n"
"s_nop 0\n"
"v_mfma_f32_16x16x32_bf16 a[72:75], a[232:235], v[16:19], a[72:75]\n"
"v_exp_f32 v9, v9\n"
"v_mfma_f32_16x16x32_bf16 a[76:79], a[236:239], v[16:19], a[76:79]\n"
"v_cvt_pk_bf16_f32 v20, v8, v9\n"
"v_mfma_f32_16x16x32_bf16 a[80:83], a[240:243], v[16:19], a[80:83]\n"
"v_exp_f32 v10, v10\n"
"v_mfma_f32_16x16x32_bf16 a[84:87], a[244:247], v[16:19], a[84:87]\n"
"s_nop 0\n"
"v_mfma_f32_16x16x32_bf16 a[88:91], a[248:251], v[16:19], a[88:91]\n"
"v_exp_f32 v11, v11\n"
"v_mfma_f32_16x16x32_bf16 a[92:95], a[252:255], v[16:19], a[92:95]\n"
"s_waitcnt lgkmcnt(8)\n"
"v_cvt_pk_bf16_f32 v21, v10, v11\n"
"v_mfma_f32_16x16x32_bf16 v[52:55], v[40:43], v[16:19], v[52:55]\n"
"v_exp_f32 v12, v12\n"
"v_mfma_f32_16x16x32_bf16 v[0:3], a[128:131], v[64:67], v[24:27]\n"
"ds_read_b128 a[160:163], v241 offset:8192\n"
"v_mfma_f32_16x16x32_bf16 v[0:3], a[132:135], v[68:71], v[0:3]\n"
"v_exp_f32 v13, v13\n"
"v_mfma_f32_16x16x32_bf16 v[0:3], a[136:139], v[72:75], v[0:3]\n"
"ds_read_b128 a[164:167], v241 offset:8448\n"
"v_cvt_pk_bf16_f32 v22, v12, v13\n"
"v_mfma_f32_16x16x32_bf16 v[0:3], a[140:143], v[76:79], v[0:3]\n"
"v_exp_f32 v14, v14\n"
"v_mfma_f32_16x16x32_bf16 v[4:7], a[144:147], v[64:67], v[24:27]\n"
"ds_read_b128 a[168:171], v241 offset:8704\n"
"v_mfma_f32_16x16x32_bf16 v[4:7], a[148:151], v[68:71], v[4:7]\n"
"v_exp_f32 v15, v15\n"
"v_mfma_f32_16x16x32_bf16 v[4:7], a[152:155], v[72:75], v[4:7]\n"
"s_add_u32 m0, s53, 36864\n"
"ds_read_b128 a[172:175], v241 offset:8960\n"
"v_cvt_pk_bf16_f32 v23, v14, v15\n"
"v_mfma_f32_16x16x32_bf16 v[4:7], a[156:159], v[76:79], v[4:7]\n"
"buffer_load_dwordx4 v204, s[28:31], s52 offen offset:0 lds\n"
"v_mfma_f32_16x16x32_bf16 a[96:99], a[224:227], v[20:23], a[96:99]\n"
"s_add_u32 s52, s52, 8192\n"
"v_exp_f32 v0, v0\n"
"v_mfma_f32_16x16x32_bf16 a[100:103], a[228:231], v[20:23], a[100:103]\n"
"ds_read_b128 a[176:179], v241 offset:12288\n"
"v_mfma_f32_16x16x32_bf16 a[104:107], a[232:235], v[20:23], a[104:107]\n"
"v_exp_f32 v1, v1\n"
"v_mfma_f32_16x16x32_bf16 a[108:111], a[236:239], v[20:23], a[108:111]\n"
"ds_read_b128 a[180:183], v241 offset:12544\n"
"v_cvt_pk_bf16_f32 v16, v0, v1\n"
"v_mfma_f32_16x16x32_bf16 a[112:115], a[240:243], v[20:23], a[112:115]\n"
"v_exp_f32 v2, v2\n"
"v_mfma_f32_16x16x32_bf16 a[116:119], a[244:247], v[20:23], a[116:119]\n"
"ds_read_b128 a[184:187], v241 offset:12800\n"
"v_mfma_f32_16x16x32_bf16 a[120:123], a[248:251], v[20:23], a[120:123]\n"
"v_exp_f32 v3, v3\n"
"v_mfma_f32_16x16x32_bf16 a[124:127], a[252:255], v[20:23], a[124:127]\n"
"v_cvt_pk_bf16_f32 v17, v2, v3\n"
"v_mfma_f32_16x16x32_bf16 v[56:59], v[40:43], v[20:23], v[56:59]\n"
"v_exp_f32 v4, v4\n"
"v_mfma_f32_16x16x32_bf16 v[8:11], a[128:131], v[80:83], v[28:31]\n"
"ds_read_b128 a[188:191], v241 offset:13056\n"
"v_mfma_f32_16x16x32_bf16 v[8:11], a[132:135], v[84:87], v[8:11]\n"
"v_exp_f32 v5, v5\n"
"v_mfma_f32_16x16x32_bf16 v[8:11], a[136:139], v[88:91], v[8:11]\n"
"ds_read_b64_tr_b16 a[224:225], v244 offset:24576\n"
"v_cvt_pk_bf16_f32 v18, v4, v5\n"
"v_mfma_f32_16x16x32_bf16 v[8:11], a[140:143], v[92:95], v[8:11]\n"
"v_exp_f32 v6, v6\n"
"v_mfma_f32_16x16x32_bf16 v[12:15], a[144:147], v[80:83], v[28:31]\n"
"ds_read_b64_tr_b16 a[226:227], v244 offset:28672\n"
"v_mfma_f32_16x16x32_bf16 v[12:15], a[148:151], v[84:87], v[12:15]\n"
"v_exp_f32 v7, v7\n"
"v_mfma_f32_16x16x32_bf16 v[12:15], a[152:155], v[88:91], v[12:15]\n"
"ds_read_b64_tr_b16 a[228:229], v245 offset:24576\n"
"v_cvt_pk_bf16_f32 v19, v6, v7\n"
"v_mfma_f32_16x16x32_bf16 v[12:15], a[156:159], v[92:95], v[12:15]\n"
"s_waitcnt lgkmcnt(11)\n"
"buffer_load_dwordx4 v204, s[28:31], s52 offen offset:1024 lds\n"
"v_mfma_f32_16x16x32_bf16 a[0:3], a[192:195], v[16:19], a[0:3]\n"
"s_add_u32 s52, s52, 8192\n"
"v_exp_f32 v8, v8\n"
"v_mfma_f32_16x16x32_bf16 a[4:7], a[196:199], v[16:19], a[4:7]\n"
"ds_read_b64_tr_b16 a[230:231], v245 offset:28672\n"
"v_mfma_f32_16x16x32_bf16 a[8:11], a[200:203], v[16:19], a[8:11]\n"
"v_exp_f32 v9, v9\n"
"v_mfma_f32_16x16x32_bf16 a[12:15], a[204:207], v[16:19], a[12:15]\n"
"ds_read_b64_tr_b16 a[232:233], v244 offset:24832\n"
"v_cvt_pk_bf16_f32 v20, v8, v9\n"
"v_mfma_f32_16x16x32_bf16 a[16:19], a[208:211], v[16:19], a[16:19]\n"
"v_exp_f32 v10, v10\n"
"v_mfma_f32_16x16x32_bf16 a[20:23], a[212:215], v[16:19], a[20:23]\n"
"ds_read_b64_tr_b16 a[234:235], v244 offset:28928\n"
"v_mfma_f32_16x16x32_bf16 a[24:27], a[216:219], v[16:19], a[24:27]\n"
"v_exp_f32 v11, v11\n"
"v_mfma_f32_16x16x32_bf16 a[28:31], a[220:223], v[16:19], a[28:31]\n"
"v_cvt_pk_bf16_f32 v21, v10, v11\n"
"v_mfma_f32_16x16x32_bf16 v[44:47], v[40:43], v[16:19], v[44:47]\n"
"v_exp_f32 v12, v12\n"
"v_mfma_f32_16x16x32_bf16 v[0:3], a[128:131], v[96:99], v[32:35]\n"
"ds_read_b64_tr_b16 a[236:237], v245 offset:24832\n"
"v_mfma_f32_16x16x32_bf16 v[0:3], a[132:135], v[100:103], v[0:3]\n"
"v_exp_f32 v13, v13\n"
"v_mfma_f32_16x16x32_bf16 v[0:3], a[136:139], v[104:107], v[0:3]\n"
"ds_read_b64_tr_b16 a[238:239], v245 offset:28928\n"
"v_cvt_pk_bf16_f32 v22, v12, v13\n"
"v_mfma_f32_16x16x32_bf16 v[0:3], a[140:143], v[108:111], v[0:3]\n"
"v_exp_f32 v14, v14\n"
"v_mfma_f32_16x16x32_bf16 v[4:7], a[144:147], v[96:99], v[32:35]\n"
"ds_read_b64_tr_b16 a[240:241], v244 offset:25088\n"
"v_mfma_f32_16x16x32_bf16 v[4:7], a[148:151], v[100:103], v[4:7]\n"
"v_exp_f32 v15, v15\n"
"v_mfma_f32_16x16x32_bf16 v[4:7], a[152:155], v[104:107], v[4:7]\n"
"ds_read_b64_tr_b16 a[242:243], v244 offset:29184\n"
"v_cvt_pk_bf16_f32 v23, v14, v15\n"
"v_mfma_f32_16x16x32_bf16 v[4:7], a[156:159], v[108:111], v[4:7]\n"
"buffer_load_dwordx4 v204, s[28:31], s52 offen offset:2048 lds\n"
"v_mfma_f32_16x16x32_bf16 a[32:35], a[192:195], v[20:23], a[32:35]\n"
"s_add_u32 s52, s52, 8192\n"
"v_exp_f32 v0, v0\n"
"v_mfma_f32_16x16x32_bf16 a[36:39], a[196:199], v[20:23], a[36:39]\n"
"ds_read_b64_tr_b16 a[244:245], v245 offset:25088\n"
"v_mfma_f32_16x16x32_bf16 a[40:43], a[200:203], v[20:23], a[40:43]\n"
"v_exp_f32 v1, v1\n"
"v_mfma_f32_16x16x32_bf16 a[44:47], a[204:207], v[20:23], a[44:47]\n"
"ds_read_b64_tr_b16 a[246:247], v245 offset:29184\n"
"v_cvt_pk_bf16_f32 v16, v0, v1\n"
"v_mfma_f32_16x16x32_bf16 a[48:51], a[208:211], v[20:23], a[48:51]\n"
"v_exp_f32 v2, v2\n"
"v_mfma_f32_16x16x32_bf16 a[52:55], a[212:215], v[20:23], a[52:55]\n"
"ds_read_b64_tr_b16 a[248:249], v244 offset:25344\n"
"v_mfma_f32_16x16x32_bf16 a[56:59], a[216:219], v[20:23], a[56:59]\n"
"v_exp_f32 v3, v3\n"
"v_mfma_f32_16x16x32_bf16 a[60:63], a[220:223], v[20:23], a[60:63]\n"
"v_cvt_pk_bf16_f32 v17, v2, v3\n"
"v_mfma_f32_16x16x32_bf16 v[48:51], v[40:43], v[20:23], v[48:51]\n"
"v_exp_f32 v4, v4\n"
"v_mfma_f32_16x16x32_bf16 v[8:11], a[128:131], v[112:115], v[36:39]\n"
"ds_read_b64_tr_b16 a[250:251], v244 offset:29440\n"
"v_mfma_f32_16x16x32_bf16 v[8:11], a[132:135], v[116:119], v[8:11]\n"
"v_exp_f32 v5, v5\n"
"v_mfma_f32_16x16x32_bf16 v[8:11], a[136:139], v[120:123], v[8:11]\n"
"ds_read_b64_tr_b16 a[252:253], v245 offset:25344\n"
"v_cvt_pk_bf16_f32 v18, v4, v5\n"
"v_mfma_f32_16x16x32_bf16 v[8:11], a[140:143], v[124:127], v[8:11]\n"
"v_exp_f32 v6, v6\n"
"v_mfma_f32_16x16x32_bf16 v[12:15], a[144:147], v[112:115], v[36:39]\n"
"ds_read_b64_tr_b16 a[254:255], v245 offset:29440\n"
"v_mfma_f32_16x16x32_bf16 v[12:15], a[148:151], v[116:119], v[12:15]\n"
"v_exp_f32 v7, v7\n"
"v_mfma_f32_16x16x32_bf16 v[12:15], a[152:155], v[120:123], v[12:15]\n"
"v_cvt_pk_bf16_f32 v19, v6, v7\n"
"v_mfma_f32_16x16x32_bf16 v[12:15], a[156:159], v[124:127], v[12:15]\n"
"buffer_load_dwordx4 v204, s[28:31], s52 offen offset:3072 lds\n"
"v_mfma_f32_16x16x32_bf16 a[64:67], a[192:195], v[16:19], a[64:67]\n"
"s_sub_u32 s52, s52, 61312\n"
"v_exp_f32 v8, v8\n"
"v_mfma_f32_16x16x32_bf16 a[68:71], a[196:199], v[16:19], a[68:71]\n"
"s_nop 0\n"
"v_mfma_f32_16x16x32_bf16 a[72:75], a[200:203], v[16:19], a[72:75]\n"
"v_exp_f32 v9, v9\n"
"v_mfma_f32_16x16x32_bf16 a[76:79], a[204:207], v[16:19], a[76:79]\n"
"v_cvt_pk_bf16_f32 v20, v8, v9\n"
"v_mfma_f32_16x16x32_bf16 a[80:83], a[208:211], v[16:19], a[80:83]\n"
"v_exp_f32 v10, v10\n"
"v_mfma_f32_16x16x32_bf16 a[84:87], a[212:215], v[16:19], a[84:87]\n"
"s_nop 0\n"
"v_mfma_f32_16x16x32_bf16 a[88:91], a[216:219], v[16:19], a[88:91]\n"
"v_exp_f32 v11, v11\n"
"v_mfma_f32_16x16x32_bf16 a[92:95], a[220:223], v[16:19], a[92:95]\n"
"s_waitcnt vmcnt(16) lgkmcnt(0)\n"
"s_barrier\n"
"v_cvt_pk_bf16_f32 v21, v10, v11\n"
"v_mfma_f32_16x16x32_bf16 v[52:55], v[40:43], v[16:19], v[52:55]\n"
"v_exp_f32 v12, v12\n"
"v_mfma_f32_16x16x32_bf16 v[0:3], a[160:163], v[64:67], v[24:27]\n"
"ds_read_b128 a[128:131], v241 offset:32768\n"
"v_mfma_f32_16x16x32_bf16 v[0:3], a[164:167], v[68:71], v[0:3]\n"
"v_exp_f32 v13, v13\n"
"v_mfma_f32_16x16x32_bf16 v[0:3], a[168:171], v[72:75], v[0:3]\n"
"ds_read_b128 a[132:135], v241 offset:33024\n"
"v_cvt_pk_bf16_f32 v22, v12, v13\n"
"v_mfma_f32_16x16x32_bf16 v[0:3], a[172:175], v[76:79], v[0:3]\n"
"v_exp_f32 v14, v14\n"
"v_mfma_f32_16x16x32_bf16 v[4:7], a[176:179], v[64:67], v[24:27]\n"
"ds_read_b128 a[136:139], v241 offset:33280\n"
"v_mfma_f32_16x16x32_bf16 v[4:7], a[180:183], v[68:71], v[4:7]\n"
"v_exp_f32 v15, v15\n"
"v_mfma_f32_16x16x32_bf16 v[4:7], a[184:187], v[72:75], v[4:7]\n"
"s_add_u32 m0, s53, 65536\n"
"ds_read_b128 a[140:143], v241 offset:33536\n"
"v_cvt_pk_bf16_f32 v23, v14, v15\n"
"v_mfma_f32_16x16x32_bf16 v[4:7], a[188:191], v[76:79], v[4:7]\n"
"buffer_load_dwordx4 v204, s[28:31], s52 offen offset:0 lds\n"
"v_mfma_f32_16x16x32_bf16 a[96:99], a[192:195], v[20:23], a[96:99]\n"
"s_add_u32 s52, s52, 8192\n"
"v_exp_f32 v0, v0\n"
"v_mfma_f32_16x16x32_bf16 a[100:103], a[196:199], v[20:23], a[100:103]\n"
"ds_read_b128 a[144:147], v241 offset:36864\n"
"v_mfma_f32_16x16x32_bf16 a[104:107], a[200:203], v[20:23], a[104:107]\n"
"v_exp_f32 v1, v1\n"
"v_mfma_f32_16x16x32_bf16 a[108:111], a[204:207], v[20:23], a[108:111]\n"
"ds_read_b128 a[148:151], v241 offset:37120\n"
"v_cvt_pk_bf16_f32 v16, v0, v1\n"
"v_mfma_f32_16x16x32_bf16 a[112:115], a[208:211], v[20:23], a[112:115]\n"
"v_exp_f32 v2, v2\n"
"v_mfma_f32_16x16x32_bf16 a[116:119], a[212:215], v[20:23], a[116:119]\n"
"ds_read_b128 a[152:155], v241 offset:37376\n"
"v_mfma_f32_16x16x32_bf16 a[120:123], a[216:219], v[20:23], a[120:123]\n"
"v_exp_f32 v3, v3\n"
"v_mfma_f32_16x16x32_bf16 a[124:127], a[220:223], v[20:23], a[124:127]\n"
"v_cvt_pk_bf16_f32 v17, v2, v3\n"
"v_mfma_f32_16x16x32_bf16 v[56:59], v[40:43], v[20:23], v[56:59]\n"
"v_exp_f32 v4, v4\n"
"v_mfma_f32_16x16x32_bf16 v[8:11], a[160:163], v[80:83], v[28:31]\n"
"ds_read_b128 a[156:159], v241 offset:37632\n"
"v_mfma_f32_16x16x32_bf16 v[8:11], a[164:167], v[84:87], v[8:11]\n"
"v_exp_f32 v5, v5\n"
"v_mfma_f32_16x16x32_bf16 v[8:11], a[168:171], v[88:91], v[8:11]\n"
"ds_read_b64_tr_b16 a[192:193], v244 offset:49152\n"
"v_cvt_pk_bf16_f32 v18, v4, v5\n"
"v_mfma_f32_16x16x32_bf16 v[8:11], a[172:175], v[92:95], v[8:11]\n"
"v_exp_f32 v6, v6\n"
"v_mfma_f32_16x16x32_bf16 v[12:15], a[176:179], v[80:83], v[28:31]\n"
"ds_read_b64_tr_b16 a[194:195], v244 offset:53248\n"
"v_mfma_f32_16x16x32_bf16 v[12:15], a[180:183], v[84:87], v[12:15]\n"
"v_exp_f32 v7, v7\n"
"v_mfma_f32_16x16x32_bf16 v[12:15], a[184:187], v[88:91], v[12:15]\n"
"ds_read_b64_tr_b16 a[196:197], v245 offset:49152\n"
"v_cvt_pk_bf16_f32 v19, v6, v7\n"
"v_mfma_f32_16x16x32_bf16 v[12:15], a[188:191], v[92:95], v[12:15]\n"
"s_waitcnt lgkmcnt(11)\n"
"buffer_load_dwordx4 v204, s[28:31], s52 offen offset:1024 lds\n"
"v_mfma_f32_16x16x32_bf16 a[0:3], a[224:227], v[16:19], a[0:3]\n"
"s_add_u32 s52, s52, 8192\n"
"v_exp_f32 v8, v8\n"
"v_mfma_f32_16x16x32_bf16 a[4:7], a[228:231], v[16:19], a[4:7]\n"
"ds_read_b64_tr_b16 a[198:199], v245 offset:53248\n"
"v_mfma_f32_16x16x32_bf16 a[8:11], a[232:235], v[16:19], a[8:11]\n"
"v_exp_f32 v9, v9\n"
"v_mfma_f32_16x16x32_bf16 a[12:15], a[236:239], v[16:19], a[12:15]\n"
"ds_read_b64_tr_b16 a[200:201], v244 offset:49408\n"
"v_cvt_pk_bf16_f32 v20, v8, v9\n"
"v_mfma_f32_16x16x32_bf16 a[16:19], a[240:243], v[16:19], a[16:19]\n"
"v_exp_f32 v10, v10\n"
"v_mfma_f32_16x16x32_bf16 a[20:23], a[244:247], v[16:19], a[20:23]\n"
"ds_read_b64_tr_b16 a[202:203], v244 offset:53504\n"
"v_mfma_f32_16x16x32_bf16 a[24:27], a[248:251], v[16:19], a[24:27]\n"
"v_exp_f32 v11, v11\n"
"v_mfma_f32_16x16x32_bf16 a[28:31], a[252:255], v[16:19], a[28:31]\n"
"v_cvt_pk_bf16_f32 v21, v10, v11\n"
"v_mfma_f32_16x16x32_bf16 v[44:47], v[40:43], v[16:19], v[44:47]\n"
"v_exp_f32 v12, v12\n"
"v_mfma_f32_16x16x32_bf16 v[0:3], a[160:163], v[96:99], v[32:35]\n"
"ds_read_b64_tr_b16 a[204:205], v245 offset:49408\n"
"v_mfma_f32_16x16x32_bf16 v[0:3], a[164:167], v[100:103], v[0:3]\n"
"v_exp_f32 v13, v13\n"
"v_mfma_f32_16x16x32_bf16 v[0:3], a[168:171], v[104:107], v[0:3]\n"
"ds_read_b64_tr_b16 a[206:207], v245 offset:53504\n"
"v_cvt_pk_bf16_f32 v22, v12, v13\n"
"v_mfma_f32_16x16x32_bf16 v[0:3], a[172:175], v[108:111], v[0:3]\n"
"v_exp_f32 v14, v14\n"
"v_mfma_f32_16x16x32_bf16 v[4:7], a[176:179], v[96:99], v[32:35]\n"
"ds_read_b64_tr_b16 a[208:209], v244 offset:49664\n"
"v_mfma_f32_16x16x32_bf16 v[4:7], a[180:183], v[100:103], v[4:7]\n"
"v_exp_f32 v15, v15\n"
"v_mfma_f32_16x16x32_bf16 v[4:7], a[184:187], v[104:107], v[4:7]\n"
"ds_read_b64_tr_b16 a[210:211], v244 offset:53760\n"
"v_cvt_pk_bf16_f32 v23, v14, v15\n"
"v_mfma_f32_16x16x32_bf16 v[4:7], a[188:191], v[108:111], v[4:7]\n"
"buffer_load_dwordx4 v204, s[28:31], s52 offen offset:2048 lds\n"
"v_mfma_f32_16x16x32_bf16 a[32:35], a[224:227], v[20:23], a[32:35]\n"
"s_add_u32 s52, s52, 8192\n"
"v_exp_f32 v0, v0\n"
"v_mfma_f32_16x16x32_bf16 a[36:39], a[228:231], v[20:23], a[36:39]\n"
"ds_read_b64_tr_b16 a[212:213], v245 offset:49664\n"
"v_mfma_f32_16x16x32_bf16 a[40:43], a[232:235], v[20:23], a[40:43]\n"
"v_exp_f32 v1, v1\n"
"v_mfma_f32_16x16x32_bf16 a[44:47], a[236:239], v[20:23], a[44:47]\n"
"ds_read_b64_tr_b16 a[214:215], v245 offset:53760\n"
"v_cvt_pk_bf16_f32 v16, v0, v1\n"
"v_mfma_f32_16x16x32_bf16 a[48:51], a[240:243], v[20:23], a[48:51]\n"
"v_exp_f32 v2, v2\n"
"v_mfma_f32_16x16x32_bf16 a[52:55], a[244:247], v[20:23], a[52:55]\n"
"ds_read_b64_tr_b16 a[216:217], v244 offset:49920\n"
"v_mfma_f32_16x16x32_bf16 a[56:59], a[248:251], v[20:23], a[56:59]\n"
"v_exp_f32 v3, v3\n"
"v_mfma_f32_16x16x32_bf16 a[60:63], a[252:255], v[20:23], a[60:63]\n"
"v_cvt_pk_bf16_f32 v17, v2, v3\n"
"v_mfma_f32_16x16x32_bf16 v[48:51], v[40:43], v[20:23], v[48:51]\n"
"v_exp_f32 v4, v4\n"
"v_mfma_f32_16x16x32_bf16 v[8:11], a[160:163], v[112:115], v[36:39]\n"
"ds_read_b64_tr_b16 a[218:219], v244 offset:54016\n"
"v_mfma_f32_16x16x32_bf16 v[8:11], a[164:167], v[116:119], v[8:11]\n"
"v_exp_f32 v5, v5\n"
"v_mfma_f32_16x16x32_bf16 v[8:11], a[168:171], v[120:123], v[8:11]\n"
"ds_read_b64_tr_b16 a[220:221], v245 offset:49920\n"
"v_cvt_pk_bf16_f32 v18, v4, v5\n"
"v_mfma_f32_16x16x32_bf16 v[8:11], a[172:175], v[124:127], v[8:11]\n"
"v_exp_f32 v6, v6\n"
"v_mfma_f32_16x16x32_bf16 v[12:15], a[176:179], v[112:115], v[36:39]\n"
"ds_read_b64_tr_b16 a[222:223], v245 offset:54016\n"
"v_mfma_f32_16x16x32_bf16 v[12:15], a[180:183], v[116:119], v[12:15]\n"
"v_exp_f32 v7, v7\n"
"v_mfma_f32_16x16x32_bf16 v[12:15], a[184:187], v[120:123], v[12:15]\n"
"v_cvt_pk_bf16_f32 v19, v6, v7\n"
"v_mfma_f32_16x16x32_bf16 v[12:15], a[188:191], v[124:127], v[12:15]\n"
"buffer_load_dwordx4 v204, s[28:31], s52 offen offset:3072 lds\n"
"v_mfma_f32_16x16x32_bf16 a[64:67], a[224:227], v[16:19], a[64:67]\n"
"s_add_u32 s52, s52, 12288\n"
"v_exp_f32 v8, v8\n"
"v_mfma_f32_16x16x32_bf16 a[68:71], a[228:231], v[16:19], a[68:71]\n"
"s_nop 0\n"
"v_mfma_f32_16x16x32_bf16 a[72:75], a[232:235], v[16:19], a[72:75]\n"
"v_exp_f32 v9, v9\n"
"v_mfma_f32_16x16x32_bf16 a[76:79], a[236:239], v[16:19], a[76:79]\n"
"v_cvt_pk_bf16_f32 v20, v8, v9\n"
"v_mfma_f32_16x16x32_bf16 a[80:83], a[240:243], v[16:19], a[80:83]\n"
"v_exp_f32 v10, v10\n"
"v_mfma_f32_16x16x32_bf16 a[84:87], a[244:247], v[16:19], a[84:87]\n"
"s_nop 0\n"
"v_mfma_f32_16x16x32_bf16 a[88:91], a[248:251], v[16:19], a[88:91]\n"
"v_exp_f32 v11, v11\n"
"v_mfma_f32_16x16x32_bf16 a[92:95], a[252:255], v[16:19], a[92:95]\n"
"s_waitcnt lgkmcnt(8)\n"
"v_cvt_pk_bf16_f32 v21, v10, v11\n"
"v_mfma_f32_16x16x32_bf16 v[52:55], v[40:43], v[16:19], v[52:55]\n"
"v_exp_f32 v12, v12\n"
"v_mfma_f32_16x16x32_bf16 v[0:3], a[128:131], v[64:67], v[24:27]\n"
"ds_read_b128 a[160:163], v241 offset:40960\n"
"v_mfma_f32_16x16x32_bf16 v[0:3], a[132:135], v[68:71], v[0:3]\n"
"v_exp_f32 v13, v13\n"
"v_mfma_f32_16x16x32_bf16 v[0:3], a[136:139], v[72:75], v[0:3]\n"
"ds_read_b128 a[164:167], v241 offset:41216\n"
"v_cvt_pk_bf16_f32 v22, v12, v13\n"
"v_mfma_f32_16x16x32_bf16 v[0:3], a[140:143], v[76:79], v[0:3]\n"
"v_exp_f32 v14, v14\n"
"v_mfma_f32_16x16x32_bf16 v[4:7], a[144:147], v[64:67], v[24:27]\n"
"ds_read_b128 a[168:171], v241 offset:41472\n"
"v_mfma_f32_16x16x32_bf16 v[4:7], a[148:151], v[68:71], v[4:7]\n"
"v_exp_f32 v15, v15\n"
"v_mfma_f32_16x16x32_bf16 v[4:7], a[152:155], v[72:75], v[4:7]\n"
"s_add_u32 m0, s53, 69632\n"
"ds_read_b128 a[172:175], v241 offset:41728\n"
"v_cvt_pk_bf16_f32 v23, v14, v15\n"
"v_mfma_f32_16x16x32_bf16 v[4:7], a[156:159], v[76:79], v[4:7]\n"
"buffer_load_dwordx4 v204, s[28:31], s52 offen offset:0 lds\n"
"v_mfma_f32_16x16x32_bf16 a[96:99], a[224:227], v[20:23], a[96:99]\n"
"s_add_u32 s52, s52, 8192\n"
"v_exp_f32 v0, v0\n"
"v_mfma_f32_16x16x32_bf16 a[100:103], a[228:231], v[20:23], a[100:103]\n"
"ds_read_b128 a[176:179], v241 offset:45056\n"
"v_mfma_f32_16x16x32_bf16 a[104:107], a[232:235], v[20:23], a[104:107]\n"
"v_exp_f32 v1, v1\n"
"v_mfma_f32_16x16x32_bf16 a[108:111], a[236:239], v[20:23], a[108:111]\n"
"ds_read_b128 a[180:183], v241 offset:45312\n"
"v_cvt_pk_bf16_f32 v16, v0, v1\n"
"v_mfma_f32_16x16x32_bf16 a[112:115], a[240:243], v[20:23], a[112:115]\n"
"v_exp_f32 v2, v2\n"
"v_mfma_f32_16x16x32_bf16 a[116:119], a[244:247], v[20:23], a[116:119]\n"
"ds_read_b128 a[184:187], v241 offset:45568\n"
"v_mfma_f32_16x16x32_bf16 a[120:123], a[248:251], v[20:23], a[120:123]\n"
"v_exp_f32 v3, v3\n"
"v_mfma_f32_16x16x32_bf16 a[124:127], a[252:255], v[20:23], a[124:127]\n"
"v_cvt_pk_bf16_f32 v17, v2, v3\n"
"v_mfma_f32_16x16x32_bf16 v[56:59], v[40:43], v[20:23], v[56:59]\n"
"v_exp_f32 v4, v4\n"
"v_mfma_f32_16x16x32_bf16 v[8:11], a[128:131], v[80:83], v[28:31]\n"
"ds_read_b128 a[188:191], v241 offset:45824\n"
"v_mfma_f32_16x16x32_bf16 v[8:11], a[132:135], v[84:87], v[8:11]\n"
"v_exp_f32 v5, v5\n"
"v_mfma_f32_16x16x32_bf16 v[8:11], a[136:139], v[88:91], v[8:11]\n"
"ds_read_b64_tr_b16 a[224:225], v244 offset:57344\n"
"v_cvt_pk_bf16_f32 v18, v4, v5\n"
"v_mfma_f32_16x16x32_bf16 v[8:11], a[140:143], v[92:95], v[8:11]\n"
"v_exp_f32 v6, v6\n"
"v_mfma_f32_16x16x32_bf16 v[12:15], a[144:147], v[80:83], v[28:31]\n"
"ds_read_b64_tr_b16 a[226:227], v244 offset:61440\n"
"v_mfma_f32_16x16x32_bf16 v[12:15], a[148:151], v[84:87], v[12:15]\n"
"v_exp_f32 v7, v7\n"
"v_mfma_f32_16x16x32_bf16 v[12:15], a[152:155], v[88:91], v[12:15]\n"
"ds_read_b64_tr_b16 a[228:229], v245 offset:57344\n"
"v_cvt_pk_bf16_f32 v19, v6, v7\n"
"v_mfma_f32_16x16x32_bf16 v[12:15], a[156:159], v[92:95], v[12:15]\n"
"s_waitcnt lgkmcnt(11)\n"
"buffer_load_dwordx4 v204, s[28:31], s52 offen offset:1024 lds\n"
"v_mfma_f32_16x16x32_bf16 a[0:3], a[192:195], v[16:19], a[0:3]\n"
"s_add_u32 s52, s52, 8192\n"
"v_exp_f32 v8, v8\n"
"v_mfma_f32_16x16x32_bf16 a[4:7], a[196:199], v[16:19], a[4:7]\n"
"ds_read_b64_tr_b16 a[230:231], v245 offset:61440\n"
"v_mfma_f32_16x16x32_bf16 a[8:11], a[200:203], v[16:19], a[8:11]\n"
"v_exp_f32 v9, v9\n"
"v_mfma_f32_16x16x32_bf16 a[12:15], a[204:207], v[16:19], a[12:15]\n"
"ds_read_b64_tr_b16 a[232:233], v244 offset:57600\n"
"v_cvt_pk_bf16_f32 v20, v8, v9\n"
"v_mfma_f32_16x16x32_bf16 a[16:19], a[208:211], v[16:19], a[16:19]\n"
"v_exp_f32 v10, v10\n"
"v_mfma_f32_16x16x32_bf16 a[20:23], a[212:215], v[16:19], a[20:23]\n"
"ds_read_b64_tr_b16 a[234:235], v244 offset:61696\n"
"v_mfma_f32_16x16x32_bf16 a[24:27], a[216:219], v[16:19], a[24:27]\n"
"v_exp_f32 v11, v11\n"
"v_mfma_f32_16x16x32_bf16 a[28:31], a[220:223], v[16:19], a[28:31]\n"
"v_cvt_pk_bf16_f32 v21, v10, v11\n"
"v_mfma_f32_16x16x32_bf16 v[44:47], v[40:43], v[16:19], v[44:47]\n"
"v_exp_f32 v12, v12\n"
"v_mfma_f32_16x16x32_bf16 v[0:3], a[128:131], v[96:99], v[32:35]\n"
"ds_read_b64_tr_b16 a[236:237], v245 offset:57600\n"
"v_mfma_f32_16x16x32_bf16 v[0:3], a[132:135], v[100:103], v[0:3]\n"
"v_exp_f32 v13, v13\n"
"v_mfma_f32_16x16x32_bf16 v[0:3], a[136:139], v[104:107], v[0:3]\n"
"ds_read_b64_tr_b16 a[238:239], v245 offset:61696\n"
"v_cvt_pk_bf16_f32 v22, v12, v13\n"
"v_mfma_f32_16x16x32_bf16 v[0:3], a[140:143], v[108:111], v[0:3]\n"
"v_exp_f32 v14, v14\n"
"v_mfma_f32_16x16x32_bf16 v[4:7], a[144:147], v[96:99], v[32:35]\n"
"ds_read_b64_tr_b16 a[240:241], v244 offset:57856\n"
"v_mfma_f32_16x16x32_bf16 v[4:7], a[148:151], v[100:103], v[4:7]\n"
"v_exp_f32 v15, v15\n"
"v_mfma_f32_16x16x32_bf16 v[4:7], a[152:155], v[104:107], v[4:7]\n"
"ds_read_b64_tr_b16 a[242:243], v244 offset:61952\n"
"v_cvt_pk_bf16_f32 v23, v14, v15\n"
"v_mfma_f32_16x16x32_bf16 v[4:7], a[156:159], v[108:111], v[4:7]\n"
"buffer_load_dwordx4 v204, s[28:31], s52 offen offset:2048 lds\n"
"v_mfma_f32_16x16x32_bf16 a[32:35], a[192:195], v[20:23], a[32:35]\n"
"s_add_u32 s52, s52, 8192\n"
"v_exp_f32 v0, v0\n"
"v_mfma_f32_16x16x32_bf16 a[36:39], a[196:199], v[20:23], a[36:39]\n"
"ds_read_b64_tr_b16 a[244:245], v245 offset:57856\n"
"v_mfma_f32_16x16x32_bf16 a[40:43], a[200:203], v[20:23], a[40:43]\n"
"v_exp_f32 v1, v1\n"
"v_mfma_f32_16x16x32_bf16 a[44:47], a[204:207], v[20:23], a[44:47]\n"
"ds_read_b64_tr_b16 a[246:247], v245 offset:61952\n"
"v_cvt_pk_bf16_f32 v16, v0, v1\n"
"v_mfma_f32_16x16x32_bf16 a[48:51], a[208:211], v[20:23], a[48:51]\n"
"v_exp_f32 v2, v2\n"
"v_mfma_f32_16x16x32_bf16 a[52:55], a[212:215], v[20:23], a[52:55]\n"
"ds_read_b64_tr_b16 a[248:249], v244 offset:58112\n"
"v_mfma_f32_16x16x32_bf16 a[56:59], a[216:219], v[20:23], a[56:59]\n"
"v_exp_f32 v3, v3\n"
"v_mfma_f32_16x16x32_bf16 a[60:63], a[220:223], v[20:23], a[60:63]\n"
"v_cvt_pk_bf16_f32 v17, v2, v3\n"
"v_mfma_f32_16x16x32_bf16 v[48:51], v[40:43], v[20:23], v[48:51]\n"
"v_exp_f32 v4, v4\n"
"v_mfma_f32_16x16x32_bf16 v[8:11], a[128:131], v[112:115], v[36:39]\n"
"ds_read_b64_tr_b16 a[250:251], v244 offset:62208\n"
"v_mfma_f32_16x16x32_bf16 v[8:11], a[132:135], v[116:119], v[8:11]\n"
"v_exp_f32 v5, v5\n"
"v_mfma_f32_16x16x32_bf16 v[8:11], a[136:139], v[120:123], v[8:11]\n"
"ds_read_b64_tr_b16 a[252:253], v245 offset:58112\n"
"v_cvt_pk_bf16_f32 v18, v4, v5\n"
"v_mfma_f32_16x16x32_bf16 v[8:11], a[140:143], v[124:127], v[8:11]\n"
"v_exp_f32 v6, v6\n"
"v_mfma_f32_16x16x32_bf16 v[12:15], a[144:147], v[112:115], v[36:39]\n"
"ds_read_b64_tr_b16 a[254:255], v245 offset:62208\n"
"v_mfma_f32_16x16x32_bf16 v[12:15], a[148:151], v[116:119], v[12:15]\n"
"v_exp_f32 v7, v7\n"
"v_mfma_f32_16x16x32_bf16 v[12:15], a[152:155], v[120:123], v[12:15]\n"
"v_cvt_pk_bf16_f32 v19, v6, v7\n"
"v_mfma_f32_16x16x32_bf16 v[12:15], a[156:159], v[124:127], v[12:15]\n"
"buffer_load_dwordx4 v204, s[28:31], s52 offen offset:3072 lds\n"
"v_mfma_f32_16x16x32_bf16 a[64:67], a[192:195], v[16:19], a[64:67]\n"
"s_sub_u32 s52, s52, 61312\n"
"v_exp_f32 v8, v8\n"
"v_mfma_f32_16x16x32_bf16 a[68:71], a[196:199], v[16:19], a[68:71]\n"
"s_nop 0\n"
"v_mfma_f32_16x16x32_bf16 a[72:75], a[200:203], v[16:19], a[72:75]\n"
"v_exp_f32 v9, v9\n"
"v_mfma_f32_16x16x32_bf16 a[76:79], a[204:207], v[16:19], a[76:79]\n"
"v_cvt_pk_bf16_f32 v20, v8, v9\n"
"v_mfma_f32_16x16x32_bf16 a[80:83], a[208:211], v[16:19], a[80:83]\n"
"v_exp_f32 v10, v10\n"
"v_mfma_f32_16x16x32_bf16 a[84:87], a[212:215], v[16:19], a[84:87]\n"
"s_nop 0\n"
"v_mfma_f32_16x16x32_bf16 a[88:91], a[216:219], v[16:19], a[88:91]\n"
"v_exp_f32 v11, v11\n"
"v_mfma_f32_16x16x32_bf16 a[92:95], a[220:223], v[16:19], a[92:95]\n"
"s_waitcnt vmcnt(16) lgkmcnt(0)\n"
"s_barrier\n"
"v_cvt_pk_bf16_f32 v21, v10, v11\n"
"v_mfma_f32_16x16x32_bf16 v[52:55], v[40:43], v[16:19], v[52:55]\n"
"v_exp_f32 v12, v12\n"
"v_mfma_f32_16x16x32_bf16 v[0:3], a[160:163], v[64:67], v[24:27]\n"
"ds_read_b64_tr_b16 a[128:129], v192 offset:0\n"
"v_mfma_f32_16x16x32_bf16 v[0:3], a[164:167], v[68:71], v[0:3]\n"
"v_exp_f32 v13, v13\n"
"v_mfma_f32_16x16x32_bf16 v[0:3], a[168:171], v[72:75], v[0:3]\n"
"v_cvt_pk_bf16_f32 v22, v12, v13\n"
"v_mfma_f32_16x16x32_bf16 v[0:3], a[172:175], v[76:79], v[0:3]\n"
"v_exp_f32 v14, v14\n"
"v_mfma_f32_16x16x32_bf16 v[4:7], a[176:179], v[64:67], v[24:27]\n"
"ds_read_b64_tr_b16 a[130:131], v192 offset:2048\n"
"v_mfma_f32_16x16x32_bf16 v[4:7], a[180:183], v[68:71], v[4:7]\n"
"v_exp_f32 v15, v15\n"
"v_mfma_f32_16x16x32_bf16 v[4:7], a[184:187], v[72:75], v[4:7]\n"
"v_cvt_pk_bf16_f32 v23, v14, v15\n"
"v_mfma_f32_16x16x32_bf16 v[4:7], a[188:191], v[76:79], v[4:7]\n"
"ds_read_b64_tr_b16 a[132:133], v192 offset:4096\n"
"v_mfma_f32_16x16x32_bf16 a[96:99], a[192:195], v[20:23], a[96:99]\n"
"v_exp_f32 v0, v0\n"
"v_mfma_f32_16x16x32_bf16 a[100:103], a[196:199], v[20:23], a[100:103]\n"
"ds_read_b64_tr_b16 a[134:135], v192 offset:6144\n"
"v_mfma_f32_16x16x32_bf16 a[104:107], a[200:203], v[20:23], a[104:107]\n"
"v_exp_f32 v1, v1\n"
"v_mfma_f32_16x16x32_bf16 a[108:111], a[204:207], v[20:23], a[108:111]\n"
"v_cvt_pk_bf16_f32 v16, v0, v1\n"
"v_mfma_f32_16x16x32_bf16 a[112:115], a[208:211], v[20:23], a[112:115]\n"
"v_exp_f32 v2, v2\n"
"v_mfma_f32_16x16x32_bf16 a[116:119], a[212:215], v[20:23], a[116:119]\n"
"ds_read_b64_tr_b16 a[136:137], v192 offset:8192\n"
"v_mfma_f32_16x16x32_bf16 a[120:123], a[216:219], v[20:23], a[120:123]\n"
"v_exp_f32 v3, v3\n"
"v_mfma_f32_16x16x32_bf16 a[124:127], a[220:223], v[20:23], a[124:127]\n"
"v_cvt_pk_bf16_f32 v17, v2, v3\n"
"v_mfma_f32_16x16x32_bf16 v[56:59], v[40:43], v[20:23], v[56:59]\n"
"v_exp_f32 v4, v4\n"
"v_mfma_f32_16x16x32_bf16 v[8:11], a[160:163], v[80:83], v[28:31]\n"
"ds_read_b64_tr_b16 a[138:139], v192 offset:10240\n"
"v_mfma_f32_16x16x32_bf16 v[8:11], a[164:167], v[84:87], v[8:11]\n"
"v_exp_f32 v5, v5\n"
"v_mfma_f32_16x16x32_bf16 v[8:11], a[168:171], v[88:91], v[8:11]\n"
"v_cvt_pk_bf16_f32 v18, v4, v5\n"
"v_mfma_f32_16x16x32_bf16 v[8:11], a[172:175], v[92:95], v[8:11]\n"
"v_exp_f32 v6, v6\n"
"v_mfma_f32_16x16x32_bf16 v[12:15], a[176:179], v[80:83], v[28:31]\n"
"ds_read_b64_tr_b16 a[140:141], v192 offset:12288\n"
"v_mfma_f32_16x16x32_bf16 v[12:15], a[180:183], v[84:87], v[12:15]\n"
"v_exp_f32 v7, v7\n"
"v_mfma_f32_16x16x32_bf16 v[12:15], a[184:187], v[88:91], v[12:15]\n"
"v_cvt_pk_bf16_f32 v19, v6, v7\n"
"v_mfma_f32_16x16x32_bf16 v[12:15], a[188:191], v[92:95], v[12:15]\n"
"s_waitcnt lgkmcnt(0)\n"
"ds_read_b64_tr_b16 a[142:143], v192 offset:14336\n"
"v_mfma_f32_16x16x32_bf16 a[0:3], a[224:227], v[16:19], a[0:3]\n"
"v_exp_f32 v8, v8\n"
"v_mfma_f32_16x16x32_bf16 a[4:7], a[228:231], v[16:19], a[4:7]\n"
"ds_read_b64_tr_b16 a[144:145], v198 offset:0\n"
"v_mfma_f32_16x16x32_bf16 a[8:11], a[232:235], v[16:19], a[8:11]\n"
"v_exp_f32 v9, v9\n"
"v_mfma_f32_16x16x32_bf16 a[12:15], a[236:239], v[16:19], a[12:15]\n"
"v_cvt_pk_bf16_f32 v20, v8, v9\n"
"v_mfma_f32_16x16x32_bf16 a[16:19], a[240:243], v[16:19], a[16:19]\n"
"v_exp_f32 v10, v10\n"
"v_mfma_f32_16x16x32_bf16 a[20:23], a[244:247], v[16:19], a[20:23]\n"
"ds_read_b64_tr_b16 a[146:147], v198 offset:2048\n"
"v_mfma_f32_16x16x32_bf16 a[24:27], a[248:251], v[16:19], a[24:27]\n"
"v_exp_f32 v11, v11\n"
"v_mfma_f32_16x16x32_bf16 a[28:31], a[252:255], v[16:19], a[28:31]\n"
"v_cvt_pk_bf16_f32 v21, v10, v11\n"
"v_mfma_f32_16x16x32_bf16 v[44:47], v[40:43], v[16:19], v[44:47]\n"
"v_exp_f32 v12, v12\n"
"v_mfma_f32_16x16x32_bf16 v[0:3], a[160:163], v[96:99], v[32:35]\n"
"ds_read_b64_tr_b16 a[148:149], v198 offset:4096\n"
"v_mfma_f32_16x16x32_bf16 v[0:3], a[164:167], v[100:103], v[0:3]\n"
"v_exp_f32 v13, v13\n"
"v_mfma_f32_16x16x32_bf16 v[0:3], a[168:171], v[104:107], v[0:3]\n"
"v_cvt_pk_bf16_f32 v22, v12, v13\n"
"v_mfma_f32_16x16x32_bf16 v[0:3], a[172:175], v[108:111], v[0:3]\n"
"v_exp_f32 v14, v14\n"
"v_mfma_f32_16x16x32_bf16 v[4:7], a[176:179], v[96:99], v[32:35]\n"
"ds_read_b64_tr_b16 a[150:151], v198 offset:6144\n"
"v_mfma_f32_16x16x32_bf16 v[4:7], a[180:183], v[100:103], v[4:7]\n"
"v_exp_f32 v15, v15\n"
"v_mfma_f32_16x16x32_bf16 v[4:7], a[184:187], v[104:107], v[4:7]\n"
"v_cvt_pk_bf16_f32 v23, v14, v15\n"
"v_mfma_f32_16x16x32_bf16 v[4:7], a[188:191], v[108:111], v[4:7]\n"
"ds_read_b64_tr_b16 a[152:153], v198 offset:8192\n"
"v_mfma_f32_16x16x32_bf16 a[32:35], a[224:227], v[20:23], a[32:35]\n"
"v_exp_f32 v0, v0\n"
"v_mfma_f32_16x16x32_bf16 a[36:39], a[228:231], v[20:23], a[36:39]\n"
"ds_read_b64_tr_b16 a[154:155], v198 offset:10240\n"
"v_mfma_f32_16x16x32_bf16 a[40:43], a[232:235], v[20:23], a[40:43]\n"
"v_exp_f32 v1, v1\n"
"v_mfma_f32_16x16x32_bf16 a[44:47], a[236:239], v[20:23], a[44:47]\n"
"v_cvt_pk_bf16_f32 v16, v0, v1\n"
"v_mfma_f32_16x16x32_bf16 a[48:51], a[240:243], v[20:23], a[48:51]\n"
"v_exp_f32 v2, v2\n"
"v_mfma_f32_16x16x32_bf16 a[52:55], a[244:247], v[20:23], a[52:55]\n"
"ds_read_b64_tr_b16 a[156:157], v198 offset:12288\n"
"v_mfma_f32_16x16x32_bf16 a[56:59], a[248:251], v[20:23], a[56:59]\n"
"v_exp_f32 v3, v3\n"
"v_mfma_f32_16x16x32_bf16 a[60:63], a[252:255], v[20:23], a[60:63]\n"
"v_cvt_pk_bf16_f32 v17, v2, v3\n"
"v_mfma_f32_16x16x32_bf16 v[48:51], v[40:43], v[20:23], v[48:51]\n"
"v_exp_f32 v4, v4\n"
"v_mfma_f32_16x16x32_bf16 v[8:11], a[160:163], v[112:115], v[36:39]\n"
"ds_read_b64_tr_b16 a[158:159], v198 offset:14336\n"
"v_mfma_f32_16x16x32_bf16 v[8:11], a[164:167], v[116:119], v[8:11]\n"
"v_exp_f32 v5, v5\n"
"v_mfma_f32_16x16x32_bf16 v[8:11], a[168:171], v[120:123], v[8:11]\n"
"v_cvt_pk_bf16_f32 v18, v4, v5\n"
"v_mfma_f32_16x16x32_bf16 v[8:11], a[172:175], v[124:127], v[8:11]\n"
"v_exp_f32 v6, v6\n"
"v_mfma_f32_16x16x32_bf16 v[12:15], a[176:179], v[112:115], v[36:39]\n"
"s_nop 0\n"
"v_mfma_f32_16x16x32_bf16 v[12:15], a[180:183], v[116:119], v[12:15]\n"
"v_exp_f32 v7, v7\n"
"v_mfma_f32_16x16x32_bf16 v[12:15], a[184:187], v[120:123], v[12:15]\n"
"v_cvt_pk_bf16_f32 v19, v6, v7\n"
"v_mfma_f32_16x16x32_bf16 v[12:15], a[188:191], v[124:127], v[12:15]\n"
"s_nop 0\n"
"v_mfma_f32_16x16x32_bf16 a[64:67], a[224:227], v[16:19], a[64:67]\n"
"v_exp_f32 v8, v8\n"
"v_mfma_f32_16x16x32_bf16 a[68:71], a[228:231], v[16:19], a[68:71]\n"
"s_nop 0\n"
"v_mfma_f32_16x16x32_bf16 a[72:75], a[232:235], v[16:19], a[72:75]\n"
"v_exp_f32 v9, v9\n"
"v_mfma_f32_16x16x32_bf16 a[76:79], a[236:239], v[16:19], a[76:79]\n"
"v_cvt_pk_bf16_f32 v20, v8, v9\n"
"v_mfma_f32_16x16x32_bf16 a[80:83], a[240:243], v[16:19], a[80:83]\n"
"v_exp_f32 v10, v10\n"
"v_mfma_f32_16x16x32_bf16 a[84:87], a[244:247], v[16:19], a[84:87]\n"
"s_nop 0\n"
"v_mfma_f32_16x16x32_bf16 a[88:91], a[248:251], v[16:19], a[88:91]\n"
"v_exp_f32 v11, v11\n"
"v_mfma_f32_16x16x32_bf16 a[92:95], a[252:255], v[16:19], a[92:95]\n"
"s_waitcnt lgkmcnt(8)\n"
"v_cvt_pk_bf16_f32 v21, v10, v11\n"
"v_mfma_f32_16x16x32_bf16 v[52:55], v[40:43], v[16:19], v[52:55]\n"
"v_exp_f32 v12, v12\n"
"s_nop 0\n"
"v_exp_f32 v13, v13\n"
"s_nop 0\n"
"v_cvt_pk_bf16_f32 v22, v12, v13\n"
"s_nop 0\n"
"v_exp_f32 v14, v14\n"
"s_nop 0\n"
"s_nop 0\n"
"v_exp_f32 v15, v15\n"
"s_nop 0\n"
"v_cvt_pk_bf16_f32 v23, v14, v15\n"
"s_nop 0\n"
"s_nop 0\n"
"s_nop 1\n"
"v_mfma_f32_16x16x32_bf16 a[96:99], a[224:227], v[20:23], a[96:99]\n"
"v_mfma_f32_16x16x32_bf16 a[100:103], a[228:231], v[20:23], a[100:103]\n"
"v_mfma_f32_16x16x32_bf16 a[104:107], a[232:235], v[20:23], a[104:107]\n"
"v_mfma_f32_16x16x32_bf16 a[108:111], a[236:239], v[20:23], a[108:111]\n"
"v_mfma_f32_16x16x32_bf16 a[112:115], a[240:243], v[20:23], a[112:115]\n"
"v_mfma_f32_16x16x32_bf16 a[116:119], a[244:247], v[20:23], a[116:119]\n"
"v_mfma_f32_16x16x32_bf16 a[120:123], a[248:251], v[20:23], a[120:123]\n"
"v_mfma_f32_16x16x32_bf16 a[124:127], a[252:255], v[20:23], a[124:127]\n"
"v_mfma_f32_16x16x32_bf16 v[56:59], v[40:43], v[20:23], v[56:59]\n"
"s_nop 7\n"
"v_mov_b32 v213, 0x5f800000\n"
"v_cmp_nle_f32 s[56:57], v44, v213\n"
"v_cmp_nle_f32 s[58:59], v48, v213\n"
"v_cmp_nle_f32 s[60:61], v52, v213\n"
"v_cmp_nle_f32 s[62:63], v56, v213\n"
"v_rcp_f32 v212, v44\n"
"v_rcp_f32 v214, v48\n"
"v_rcp_f32 v216, v52\n"
"v_rcp_f32 v218, v56\n"
"s_or_b64 s[56:57], s[56:57], s[58:59]\n"
"s_or_b64 s[60:61], s[60:61], s[62:63]\n"
"s_or_b64 s[56:57], s[56:57], s[60:61]\n"
"s_cselect_b32 s56, -1, 0\n"
"s_cmp_eq_u32 s45, 0\n"
"s_cselect_b32 s56, s56, 0\n"
"v_accvgpr_read_b32 v220, a0\n"
"v_accvgpr_read_b32 v221, a1\n"
"v_accvgpr_read_b32 v222, a2\n"
"v_accvgpr_read_b32 v223, a3\n"
"v_pk_mul_f32 v[220:221], v[220:221], v[212:213] op_sel_hi:[1,0]\n"
"v_pk_mul_f32 v[222:223], v[222:223], v[212:213] op_sel_hi:[1,0]\n"
"v_cvt_pk_bf16_f32 v128, v220, v221\n"
"v_cvt_pk_bf16_f32 v129, v222, v223\n"
"v_accvgpr_read_b32 v224, a4\n"
"v_accvgpr_read_b32 v225, a5\n"
"v_accvgpr_read_b32 v226, a6\n"
"v_accvgpr_read_b32 v227, a7\n"
"v_pk_mul_f32 v[224:225], v[224:225], v[212:213] op_sel_hi:[1,0]\n"
"v_pk_mul_f32 v[226:227], v[226:227], v[212:213] op_sel_hi:[1,0]\n"
"v_cvt_pk_bf16_f32 v130, v224, v225\n"
"v_cvt_pk_bf16_f32 v131, v226, v227\n"
"v_or_b32 v128, s56, v128\n"
"ds_write_b128 v209, v[128:131] offset:0\n"
"v_accvgpr_read_b32 v228, a8\n"
"v_accvgpr_read_b32 v229, a9\n"
"v_accvgpr_read_b32 v230, a10\n"
"v_accvgpr_read_b32 v231, a11\n"
"v_pk_mul_f32 v[228:229], v[228:229], v[212:213] op_sel_hi:[1,0]\n"
"v_pk_mul_f32 v[230:231], v[230:231], v[212:213] op_sel_hi:[1,0]\n"
"v_cvt_pk_bf16_f32 v132, v228, v229\n"
"v_cvt_pk_bf16_f32 v133, v230, v231\n"
"v_accvgpr_read_b32 v232, a12\n"
"v_accvgpr_read_b32 v233, a13\n"
"v_accvgpr_read_b32 v234, a14\n"
"v_accvgpr_read_b32 v235, a15\n"
"v_pk_mul_f32 v[232:233], v[232:233], v[212:213] op_sel_hi:[1,0]\n"
"v_pk_mul_f32 v[234:235], v[234:235], v[212:213] op_sel_hi:[1,0]\n"
"v_cvt_pk_bf16_f32 v134, v232, v233\n"
"v_cvt_pk_bf16_f32 v135, v234, v235\n"
"ds_write_b128 v209, v[132:135] offset:1024\n"
"v_accvgpr_read_b32 v220, a16\n"
"v_accvgpr_read_b32 v221, a17\n"
"v_accvgpr_read_b32 v222, a18\n"
"v_accvgpr_read_b32 v223, a19\n"
"v_pk_mul_f32 v[220:221], v[220:221], v[212:213] op_sel_hi:[1,0]\n"
"v_pk_mul_f32 v[222:223], v[222:223], v[212:213] op_sel_hi:[1,0]\n"
"v_cvt_pk_bf16_f32 v136, v220, v221\n"
"v_cvt_pk_bf16_f32 v137, v222, v223\n"
"v_accvgpr_read_b32 v224, a20\n"
"v_accvgpr_read_b32 v225, a21\n"
"v_accvgpr_read_b32 v226, a22\n"
"v_accvgpr_read_b32 v227, a23\n"
"v_pk_mul_f32 v[224:225], v[224:225], v[212:213] op_sel_hi:[1,0]\n"
"v_pk_mul_f32 v[226:227], v[226:227], v[212:213] op_sel_hi:[1,0]\n"
"v_cvt_pk_bf16_f32 v138, v224, v225\n"
"v_cvt_pk_bf16_f32 v139, v226, v227\n"
"ds_write_b128 v209, v[136:139] offset:2048\n"
"v_accvgpr_read_b32 v228, a24\n"
"v_accvgpr_read_b32 v229, a25\n"
"v_accvgpr_read_b32 v230, a26\n"
"v_accvgpr_read_b32 v231, a27\n"
"v_pk_mul_f32 v[228:229], v[228:229], v[212:213] op_sel_hi:[1,0]\n"
"v_pk_mul_f32 v[230:231], v[230:231], v[212:213] op_sel_hi:[1,0]\n"
"v_cvt_pk_bf16_f32 v140, v228, v229\n"
"v_cvt_pk_bf16_f32 v141, v230, v231\n"
"v_accvgpr_read_b32 v232, a28\n"
"v_accvgpr_read_b32 v233, a29\n"
"v_accvgpr_read_b32 v234, a30\n"
"v_accvgpr_read_b32 v235, a31\n"
"v_pk_mul_f32 v[232:233], v[232:233], v[212:213] op_sel_hi:[1,0]\n"
"v_pk_mul_f32 v[234:235], v[234:235], v[212:213] op_sel_hi:[1,0]\n"
"v_cvt_pk_bf16_f32 v142, v232, v233\n"
"v_cvt_pk_bf16_f32 v143, v234, v235\n"
"ds_write_b128 v209, v[140:143] offset:3072\n"
"v_accvgpr_read_b32 v220, a32\n"
"v_accvgpr_read_b32 v221, a33\n"
"v_accvgpr_read_b32 v222, a34\n"
"v_accvgpr_read_b32 v223, a35\n"
"v_pk_mul_f32 v[220:221], v[220:221], v[214:215] op_sel_hi:[1,0]\n"
"v_pk_mul_f32 v[222:223], v[222:223], v[214:215] op_sel_hi:[1,0]\n"
"v_cvt_pk_bf16_f32 v144, v220, v221\n"
"v_cvt_pk_bf16_f32 v145, v222, v223\n"
"v_accvgpr_read_b32 v224, a36\n"
"v_accvgpr_read_b32 v225, a37\n"
"v_accvgpr_read_b32 v226, a38\n"
"v_accvgpr_read_b32 v227, a39\n"
"v_pk_mul_f32 v[224:225], v[224:225], v[214:215] op_sel_hi:[1,0]\n"
"v_pk_mul_f32 v[226:227], v[226:227], v[214:215] op_sel_hi:[1,0]\n"
"v_cvt_pk_bf16_f32 v146, v224, v225\n"
"v_cvt_pk_bf16_f32 v147, v226, v227\n"
"ds_write_b128 v209, v[144:147] offset:4096\n"
"v_accvgpr_read_b32 v228, a40\n"
"v_accvgpr_read_b32 v229, a41\n"
"v_accvgpr_read_b32 v230, a42\n"
"v_accvgpr_read_b32 v231, a43\n"
"v_pk_mul_f32 v[228:229], v[228:229], v[214:215] op_sel_hi:[1,0]\n"
"v_pk_mul_f32 v[230:231], v[230:231], v[214:215] op_sel_hi:[1,0]\n"
"v_cvt_pk_bf16_f32 v148, v228, v229\n"
"v_cvt_pk_bf16_f32 v149, v230, v231\n"
"v_accvgpr_read_b32 v232, a44\n"
"v_accvgpr_read_b32 v233, a45\n"
"v_accvgpr_read_b32 v234, a46\n"
"v_accvgpr_read_b32 v235, a47\n"
"v_pk_mul_f32 v[232:233], v[232:233], v[214:215] op_sel_hi:[1,0]\n"
"v_pk_mul_f32 v[234:235], v[234:235], v[214:215] op_sel_hi:[1,0]\n"
"v_cvt_pk_bf16_f32 v150, v232, v233\n"
"v_cvt_pk_bf16_f32 v151, v234, v235\n"
"ds_write_b128 v209, v[148:151] offset:5120\n"
"v_accvgpr_read_b32 v220, a48\n"
"v_accvgpr_read_b32 v221, a49\n"
"v_accvgpr_read_b32 v222, a50\n"
"v_accvgpr_read_b32 v223, a51\n"
"v_pk_mul_f32 v[220:221], v[220:221], v[214:215] op_sel_hi:[1,0]\n"
"v_pk_mul_f32 v[222:223], v[222:223], v[214:215] op_sel_hi:[1,0]\n"
"v_cvt_pk_bf16_f32 v152, v220, v221\n"
"v_cvt_pk_bf16_f32 v153, v222, v223\n"
"v_accvgpr_read_b32 v224, a52\n"
"v_accvgpr_read_b32 v225, a53\n"
"v_accvgpr_read_b32 v226, a54\n"
"v_accvgpr_read_b32 v227, a55\n"
"v_pk_mul_f32 v[224:225], v[224:225], v[214:215] op_sel_hi:[1,0]\n"
"v_pk_mul_f32 v[226:227], v[226:227], v[214:215] op_sel_hi:[1,0]\n"
"v_cvt_pk_bf16_f32 v154, v224, v225\n"
"v_cvt_pk_bf16_f32 v155, v226, v227\n"
"ds_write_b128 v209, v[152:155] offset:6144\n"
"v_accvgpr_read_b32 v228, a56\n"
"v_accvgpr_read_b32 v229, a57\n"
"v_accvgpr_read_b32 v230, a58\n"
"v_accvgpr_read_b32 v231, a59\n"
"v_pk_mul_f32 v[228:229], v[228:229], v[214:215] op_sel_hi:[1,0]\n"
"v_pk_mul_f32 v[230:231], v[230:231], v[214:215] op_sel_hi:[1,0]\n"
"v_cvt_pk_bf16_f32 v156, v228, v229\n"
"v_cvt_pk_bf16_f32 v157, v230, v231\n"
"v_accvgpr_read_b32 v232, a60\n"
"v_accvgpr_read_b32 v233, a61\n"
"v_accvgpr_read_b32 v234, a62\n"
"v_accvgpr_read_b32 v235, a63\n"
"v_pk_mul_f32 v[232:233], v[232:233], v[214:215] op_sel_hi:[1,0]\n"
"v_pk_mul_f32 v[234:235], v[234:235], v[214:215] op_sel_hi:[1,0]\n"
"v_cvt_pk_bf16_f32 v158, v232, v233\n"
"v_cvt_pk_bf16_f32 v159, v234, v235\n"
"ds_write_b128 v209, v[156:159] offset:7168\n"
"v_accvgpr_read_b32 v220, a64\n"
"v_accvgpr_read_b32 v221, a65\n"
"v_accvgpr_read_b32 v222, a66\n"
"v_accvgpr_read_b32 v223, a67\n"
"v_pk_mul_f32 v[220:221], v[220:221], v[216:217] op_sel_hi:[1,0]\n"
"v_pk_mul_f32 v[222:223], v[222:223], v[216:217] op_sel_hi:[1,0]\n"
"v_cvt_pk_bf16_f32 v160, v220, v221\n"
"v_cvt_pk_bf16_f32 v161, v222, v223\n"
"v_accvgpr_read_b32 v224, a68\n"
"v_accvgpr_read_b32 v225, a69\n"
"v_accvgpr_read_b32 v226, a70\n"
"v_accvgpr_read_b32 v227, a71\n"
"v_pk_mul_f32 v[224:225], v[224:225], v[216:217] op_sel_hi:[1,0]\n"
"v_pk_mul_f32 v[226:227], v[226:227], v[216:217] op_sel_hi:[1,0]\n"
"v_cvt_pk_bf16_f32 v162, v224, v225\n"
"v_cvt_pk_bf16_f32 v163, v226, v227\n"
"ds_write_b128 v209, v[160:163] offset:8192\n"
"v_accvgpr_read_b32 v228, a72\n"
"v_accvgpr_read_b32 v229, a73\n"
"v_accvgpr_read_b32 v230, a74\n"
"v_accvgpr_read_b32 v231, a75\n"
"v_pk_mul_f32 v[228:229], v[228:229], v[216:217] op_sel_hi:[1,0]\n"
"v_pk_mul_f32 v[230:231], v[230:231], v[216:217] op_sel_hi:[1,0]\n"
"v_cvt_pk_bf16_f32 v164, v228, v229\n"
"v_cvt_pk_bf16_f32 v165, v230, v231\n"
"v_accvgpr_read_b32 v232, a76\n"
"v_accvgpr_read_b32 v233, a77\n"
"v_accvgpr_read_b32 v234, a78\n"
"v_accvgpr_read_b32 v235, a79\n"
"v_pk_mul_f32 v[232:233], v[232:233], v[216:217] op_sel_hi:[1,0]\n"
"v_pk_mul_f32 v[234:235], v[234:235], v[216:217] op_sel_hi:[1,0]\n"
"v_cvt_pk_bf16_f32 v166, v232, v233\n"
"v_cvt_pk_bf16_f32 v167, v234, v235\n"
"ds_write_b128 v209, v[164:167] offset:9216\n"
"v_accvgpr_read_b32 v220, a80\n"
"v_accvgpr_read_b32 v221, a81\n"
"v_accvgpr_read_b32 v222, a82\n"
"v_accvgpr_read_b32 v223, a83\n"
"v_pk_mul_f32 v[220:221], v[220:221], v[216:217] op_sel_hi:[1,0]\n"
"v_pk_mul_f32 v[222:223], v[222:223], v[216:217] op_sel_hi:[1,0]\n"
"v_cvt_pk_bf16_f32 v168, v220, v221\n"
"v_cvt_pk_bf16_f32 v169, v222, v223\n"
"v_accvgpr_read_b32 v224, a84\n"
"v_accvgpr_read_b32 v225, a85\n"
"v_accvgpr_read_b32 v226, a86\n"
"v_accvgpr_read_b32 v227, a87\n"
"v_pk_mul_f32 v[224:225], v[224:225], v[216:217] op_sel_hi:[1,0]\n"
"v_pk_mul_f32 v[226:227], v[226:227], v[216:217] op_sel_hi:[1,0]\n"
"v_cvt_pk_bf16_f32 v170, v224, v225\n"
"v_cvt_pk_bf16_f32 v171, v226, v227\n"
"ds_write_b128 v209, v[168:171] offset:10240\n"
"v_accvgpr_read_b32 v228, a88\n"
"v_accvgpr_read_b32 v229, a89\n"
"v_accvgpr_read_b32 v230, a90\n"
"v_accvgpr_read_b32 v231, a91\n"
"v_pk_mul_f32 v[228:229], v[228:229], v[216:217] op_sel_hi:[1,0]\n"
"v_pk_mul_f32 v[230:231], v[230:231], v[216:217] op_sel_hi:[1,0]\n"
"v_cvt_pk_bf16_f32 v172, v228, v229\n"
"v_cvt_pk_bf16_f32 v173, v230, v231\n"
"v_accvgpr_read_b32 v232, a92\n"
"v_accvgpr_read_b32 v233, a93\n"
"v_accvgpr_read_b32 v234, a94\n"
"v_accvgpr_read_b32 v235, a95\n"
"v_pk_mul_f32 v[232:233], v[232:233], v[216:217] op_sel_hi:[1,0]\n"
"v_pk_mul_f32 v[234:235], v[234:235], v[216:217] op_sel_hi:[1,0]\n"
"v_cvt_pk_bf16_f32 v174, v232, v233\n"
"v_cvt_pk_bf16_f32 v175, v234, v235\n"
"ds_write_b128 v209, v[172:175] offset:11264\n"
"v_accvgpr_read_b32 v220, a96\n"
"v_accvgpr_read_b32 v221, a97\n"
"v_accvgpr_read_b32 v222, a98\n"
"v_accvgpr_read_b32 v223, a99\n"
"v_pk_mul_f32 v[220:221], v[220:221], v[218:219] op_sel_hi:[1,0]\n"
"v_pk_mul_f32 v[222:223], v[222:223], v[218:219] op_sel_hi:[1,0]\n"
"v_cvt_pk_bf16_f32 v176, v220, v221\n"
"v_cvt_pk_bf16_f32 v177, v222, v223\n"
"v_accvgpr_read_b32 v224, a100\n"
"v_accvgpr_read_b32 v225, a101\n"
"v_accvgpr_read_b32 v226, a102\n"
"v_accvgpr_read_b32 v227, a103\n"
"v_pk_mul_f32 v[224:225], v[224:225], v[218:219] op_sel_hi:[1,0]\n"
"v_pk_mul_f32 v[226:227], v[226:227], v[218:219] op_sel_hi:[1,0]\n"
"v_cvt_pk_bf16_f32 v178, v224, v225\n"
"v_cvt_pk_bf16_f32 v179, v226, v227\n"
"ds_write_b128 v209, v[176:179] offset:12288\n"
"v_accvgpr_read_b32 v228, a104\n"
"v_accvgpr_read_b32 v229, a105\n"
"v_accvgpr_read_b32 v230, a106\n"
"v_accvgpr_read_b32 v231, a107\n"
"v_pk_mul_f32 v[228:229], v[228:229], v[218:219] op_sel_hi:[1,0]\n"
"v_pk_mul_f32 v[230:231], v[230:231], v[218:219] op_sel_hi:[1,0]\n"
"v_cvt_pk_bf16_f32 v180, v228, v229\n"
"v_cvt_pk_bf16_f32 v181, v230, v231\n"
"v_accvgpr_read_b32 v232, a108\n"
"v_accvgpr_read_b32 v233, a109\n"
"v_accvgpr_read_b32 v234, a110\n"
"v_accvgpr_read_b32 v235, a111\n"
"v_pk_mul_f32 v[232:233], v[232:233], v[218:219] op_sel_hi:[1,0]\n"
"v_pk_mul_f32 v[234:235], v[234:235], v[218:219] op_sel_hi:[1,0]\n"
"v_cvt_pk_bf16_f32 v182, v232, v233\n"
"v_cvt_pk_bf16_f32 v183, v234, v235\n"
"ds_write_b128 v209, v[180:183] offset:13312\n"
"v_accvgpr_read_b32 v220, a112\n"
"v_accvgpr_read_b32 v221, a113\n"
"v_accvgpr_read_b32 v222, a114\n"
"v_accvgpr_read_b32 v223, a115\n"
"v_pk_mul_f32 v[220:221], v[220:221], v[218:219] op_sel_hi:[1,0]\n"
"v_pk_mul_f32 v[222:223], v[222:223], v[218:219] op_sel_hi:[1,0]\n"
"v_cvt_pk_bf16_f32 v184, v220, v221\n"
"v_cvt_pk_bf16_f32 v185, v222, v223\n"
"v_accvgpr_read_b32 v224, a116\n"
"v_accvgpr_read_b32 v225, a117\n"
"v_accvgpr_read_b32 v226, a118\n"
"v_accvgpr_read_b32 v227, a119\n"
"v_pk_mul_f32 v[224:225], v[224:225], v[218:219] op_sel_hi:[1,0]\n"
"v_pk_mul_f32 v[226:227], v[226:227], v[218:219] op_sel_hi:[1,0]\n"
"v_cvt_pk_bf16_f32 v186, v224, v225\n"
"v_cvt_pk_bf16_f32 v187, v226, v227\n"
"ds_write_b128 v209, v[184:187] offset:14336\n"
"v_accvgpr_read_b32 v228, a120\n"
"v_accvgpr_read_b32 v229, a121\n"
"v_accvgpr_read_b32 v230, a122\n"
"v_accvgpr_read_b32 v231, a123\n"
"v_pk_mul_f32 v[228:229], v[228:229], v[218:219] op_sel_hi:[1,0]\n"
"v_pk_mul_f32 v[230:231], v[230:231], v[218:219] op_sel_hi:[1,0]\n"
"v_cvt_pk_bf16_f32 v188, v228, v229\n"
"v_cvt_pk_bf16_f32 v189, v230, v231\n"
"v_accvgpr_read_b32 v232, a124\n"
"v_accvgpr_read_b32 v233, a125\n"
"v_accvgpr_read_b32 v234, a126\n"
"v_accvgpr_read_b32 v235, a127\n"
"v_pk_mul_f32 v[232:233], v[232:233], v[218:219] op_sel_hi:[1,0]\n"
"v_pk_mul_f32 v[234:235], v[234:235], v[218:219] op_sel_hi:[1,0]\n"
"v_cvt_pk_bf16_f32 v190, v232, v233\n"
"v_cvt_pk_bf16_f32 v191, v234, v235\n"
"ds_write_b128 v209, v[188:191] offset:15360\n"
"s_waitcnt lgkmcnt(0)\n"
"s_barrier\n"
"ds_read_b32 v220, v211\n"
"ds_read_b128 a[192:195], v210 offset:0\n"
"ds_read_b128 a[196:199], v210 offset:1024\n"
"ds_read_b128 a[200:203], v210 offset:2048\n"
"ds_read_b128 a[204:207], v210 offset:3072\n"
"ds_read_b128 a[208:211], v210 offset:4096\n"
"ds_read_b128 a[212:215], v210 offset:5120\n"
"ds_read_b128 a[216:219], v210 offset:6144\n"
"ds_read_b128 a[220:223], v210 offset:7168\n"
"ds_read_b128 a[224:227], v210 offset:8192\n"
"ds_read_b128 a[228:231], v210 offset:9216\n"
"ds_read_b128 a[232:235], v210 offset:10240\n"
"ds_read_b128 a[236:239], v210 offset:11264\n"
"ds_read_b128 a[240:243], v210 offset:12288\n"
"ds_read_b128 a[244:247], v210 offset:13312\n"
"ds_read_b128 a[248:251], v210 offset:14336\n"
"ds_read_b128 a[252:255], v210 offset:15360\n"
"s_waitcnt lgkmcnt(14)\n"
"v_cmp_eq_u32 vcc, -1, v220\n"
"s_cmp_eq_u32 s45, 0\n"
"s_cselect_b64 vcc, vcc, 0\n"
"s_cbranch_vccnz AT_fb\n"
"v_mfma_f32_16x16x32_bf16 v[0:3], a[128:131], v[128:131], 0\n"
"ds_read_b64_tr_b16 a[160:161], v195 offset:0\n"
"v_mfma_f32_16x16x32_bf16 v[0:3], a[132:135], v[132:135], v[0:3]\n"
"ds_read_b64_tr_b16 a[162:163], v195 offset:2048\n"
"v_mfma_f32_16x16x32_bf16 v[0:3], a[136:139], v[136:139], v[0:3]\n"
"ds_read_b64_tr_b16 a[164:165], v195 offset:4096\n"
"v_mfma_f32_16x16x32_bf16 v[0:3], a[140:143], v[140:143], v[0:3]\n"
"ds_read_b64_tr_b16 a[166:167], v195 offset:6144\n"
"v_mfma_f32_16x16x32_bf16 v[0:3], a[144:147], a[192:195], v[0:3]\n"
"ds_read_b64_tr_b16 a[168:169], v195 offset:8192\n"
"v_mfma_f32_16x16x32_bf16 v[0:3], a[148:151], a[196:199], v[0:3]\n"
"s_waitcnt lgkmcnt(5)\n"
"ds_read_b64_tr_b16 a[170:171], v195 offset:10240\n"
"v_mfma_f32_16x16x32_bf16 v[0:3], a[152:155], a[200:203], v[0:3]\n"
"ds_read_b64_tr_b16 a[172:173], v195 offset:12288\n"
"v_mfma_f32_16x16x32_bf16 v[0:3], a[156:159], a[204:207], v[0:3]\n"
"ds_read_b64_tr_b16 a[174:175], v195 offset:14336\n"
"v_mfma_f32_16x16x32_bf16 v[4:7], a[128:131], v[144:147], 0\n"
"ds_read_b64_tr_b16 a[176:177], v201 offset:0\n"
"v_mfma_f32_16x16x32_bf16 v[4:7], a[132:135], v[148:151], v[4:7]\n"
"ds_read_b64_tr_b16 a[178:179], v201 offset:2048\n"
"v_mfma_f32_16x16x32_bf16 v[4:7], a[136:139], v[152:155], v[4:7]\n"
"ds_read_b64_tr_b16 a[180:181], v201 offset:4096\n"
"v_mfma_f32_16x16x32_bf16 v[4:7], a[140:143], v[156:159], v[4:7]\n"
"ds_read_b64_tr_b16 a[182:183], v201 offset:6144\n"
"v_mfma_f32_16x16x32_bf16 v[4:7], a[144:147], a[208:211], v[4:7]\n"
"ds_read_b64_tr_b16 a[184:185], v201 offset:8192\n"
"v_cvt_pk_bf16_f32 v32, v0, v1\n"
"v_mfma_f32_16x16x32_bf16 v[4:7], a[148:151], a[212:215], v[4:7]\n"
"ds_read_b64_tr_b16 a[186:187], v201 offset:10240\n"
"v_cvt_pk_bf16_f32 v33, v2, v3\n"
"v_mfma_f32_16x16x32_bf16 v[4:7], a[152:155], a[216:219], v[4:7]\n"
"ds_read_b64_tr_b16 a[188:189], v201 offset:12288\n"
"v_mfma_f32_16x16x32_bf16 v[4:7], a[156:159], a[220:223], v[4:7]\n"
"ds_read_b64_tr_b16 a[190:191], v201 offset:14336\n"
"v_mfma_f32_16x16x32_bf16 v[8:11], a[128:131], v[160:163], 0\n"
"s_nop 0\n"
"v_mfma_f32_16x16x32_bf16 v[8:11], a[132:135], v[164:167], v[8:11]\n"
"s_nop 0\n"
"v_mfma_f32_16x16x32_bf16 v[8:11], a[136:139], v[168:171], v[8:11]\n"
"s_nop 0\n"
"v_mfma_f32_16x16x32_bf16 v[8:11], a[140:143], v[172:175], v[8:11]\n"
"s_nop 0\n"
"v_mfma_f32_16x16x32_bf16 v[8:11], a[144:147], a[224:227], v[8:11]\n"
"v_cvt_pk_bf16_f32 v36, v4, v5\n"
"v_mfma_f32_16x16x32_bf16 v[8:11], a[148:151], a[228:231], v[8:11]\n"
"v_cvt_pk_bf16_f32 v37, v6, v7\n"
"v_mfma_f32_16x16x32_bf16 v[8:11], a[152:155], a[232:235], v[8:11]\n"
"s_nop 0\n"
"v_mfma_f32_16x16x32_bf16 v[8:11], a[156:159], a[236:239], v[8:11]\n"
"s_nop 0\n"
"v_mfma_f32_16x16x32_bf16 v[12:15], a[128:131], v[176:179], 0\n"
"s_nop 0\n"
"v_mfma_f32_16x16x32_bf16 v[12:15], a[132:135], v[180:183], v[12:15]\n"
"s_nop 0\n"
"v_mfma_f32_16x16x32_bf16 v[12:15], a[136:139], v[184:187], v[12:15]\n"
"s_nop 0\n"
"v_mfma_f32_16x16x32_bf16 v[12:15], a[140:143], v[188:191], v[12:15]\n"
"s_nop 0\n"
"v_mfma_f32_16x16x32_bf16 v[12:15], a[144:147], a[240:243], v[12:15]\n"
"v_cvt_pk_bf16_f32 v40, v8, v9\n"
"v_mfma_f32_16x16x32_bf16 v[12:15], a[148:151], a[244:247], v[12:15]\n"
"v_cvt_pk_bf16_f32 v41, v10, v11\n"
"v_mfma_f32_16x16x32_bf16 v[12:15], a[152:155], a[248:251], v[12:15]\n"
"s_waitcnt vmcnt(8) lgkmcnt(0)\n"
"s_barrier\n"
"v_mfma_f32_16x16x32_bf16 v[12:15], a[156:159], a[252:255], v[12:15]\n"
"s_nop 0\n"
"v_mfma_f32_16x16x32_bf16 v[16:19], a[160:163], v[128:131], 0\n"
"ds_read_b64_tr_b16 a[128:129], v192 offset:32768\n"
"v_mfma_f32_16x16x32_bf16 v[16:19], a[164:167], v[132:135], v[16:19]\n"
"ds_read_b64_tr_b16 a[130:131], v192 offset:34816\n"
"v_mfma_f32_16x16x32_bf16 v[16:19], a[168:171], v[136:139], v[16:19]\n"
"ds_read_b64_tr_b16 a[132:133], v192 offset:36864\n"
"v_mfma_f32_16x16x32_bf16 v[16:19], a[172:175], v[140:143], v[16:19]\n"
"ds_read_b64_tr_b16 a[134:135], v192 offset:38912\n"
"v_mfma_f32_16x16x32_bf16 v[16:19], a[176:179], a[192:195], v[16:19]\n"
"ds_read_b64_tr_b16 a[136:137], v192 offset:40960\n"
"v_cvt_pk_bf16_f32 v44, v12, v13\n"
"v_mfma_f32_16x16x32_bf16 v[16:19], a[180:183], a[196:199], v[16:19]\n"
"ds_read_b64_tr_b16 a[138:139], v192 offset:43008\n"
"v_cvt_pk_bf16_f32 v45, v14, v15\n"
"v_mfma_f32_16x16x32_bf16 v[16:19], a[184:187], a[200:203], v[16:19]\n"
"ds_read_b64_tr_b16 a[140:141], v192 offset:45056\n"
"v_mfma_f32_16x16x32_bf16 v[16:19], a[188:191], a[204:207], v[16:19]\n"
"ds_read_b64_tr_b16 a[142:143], v192 offset:47104\n"
"v_mfma_f32_16x16x32_bf16 v[20:23], a[160:163], v[144:147], 0\n"
"ds_read_b64_tr_b16 a[144:145], v198 offset:32768\n"
"v_mfma_f32_16x16x32_bf16 v[20:23], a[164:167], v[148:151], v[20:23]\n"
"ds_read_b64_tr_b16 a[146:147], v198 offset:34816\n"
"v_mfma_f32_16x16x32_bf16 v[20:23], a[168:171], v[152:155], v[20:23]\n"
"ds_read_b64_tr_b16 a[148:149], v198 offset:36864\n"
"v_mfma_f32_16x16x32_bf16 v[20:23], a[172:175], v[156:159], v[20:23]\n"
"ds_read_b64_tr_b16 a[150:151], v198 offset:38912\n"
"v_mfma_f32_16x16x32_bf16 v[20:23], a[176:179], a[208:211], v[20:23]\n"
"ds_read_b64_tr_b16 a[152:153], v198 offset:40960\n"
"v_cvt_pk_bf16_f32 v34, v16, v17\n"
"v_mfma_f32_16x16x32_bf16 v[20:23], a[180:183], a[212:215], v[20:23]\n"
"ds_read_b64_tr_b16 a[154:155], v198 offset:43008\n"
"v_cvt_pk_bf16_f32 v35, v18, v19\n"
"v_mfma_f32_16x16x32_bf16 v[20:23], a[184:187], a[216:219], v[20:23]\n"
"ds_read_b64_tr_b16 a[156:157], v198 offset:45056\n"
"v_mfma_f32_16x16x32_bf16 v[20:23], a[188:191], a[220:223], v[20:23]\n"
"s_add_u32 m0, s53, 98304\n"
"ds_read_b64_tr_b16 a[158:159], v198 offset:47104\n"
"buffer_store_dwordx4 v[32:35], v205, s[32:35], s54 offen offset:0\n"
"v_mfma_f32_16x16x32_bf16 v[24:27], a[160:163], v[160:163], 0\n"
"buffer_load_dwordx4 v204, s[28:31], s52 offen offset:0 lds\n"
"v_mfma_f32_16x16x32_bf16 v[24:27], a[164:167], v[164:167], v[24:27]\n"
"s_add_u32 s52, s52, 8192\n"
"v_mfma_f32_16x16x32_bf16 v[24:27], a[168:171], v[168:171], v[24:27]\n"
"s_nop 0\n"
"v_mfma_f32_16x16x32_bf16 v[24:27], a[172:175], v[172:175], v[24:27]\n"
"s_nop 0\n"
"v_mfma_f32_16x16x32_bf16 v[24:27], a[176:179], a[224:227], v[24:27]\n"
"v_cvt_pk_bf16_f32 v38, v20, v21\n"
"v_mfma_f32_16x16x32_bf16 v[24:27], a[180:183], a[228:231], v[24:27]\n"
"v_cvt_pk_bf16_f32 v39, v22, v23\n"
"v_mfma_f32_16x16x32_bf16 v[24:27], a[184:187], a[232:235], v[24:27]\n"
"buffer_load_dwordx4 v204, s[28:31], s52 offen offset:1024 lds\n"
"v_mfma_f32_16x16x32_bf16 v[24:27], a[188:191], a[236:239], v[24:27]\n"
"s_add_u32 s52, s52, 8192\n"
"buffer_store_dwordx4 v[36:39], v206, s[32:35], s54 offen offset:0\n"
"v_mfma_f32_16x16x32_bf16 v[28:31], a[160:163], v[176:179], 0\n"
"s_nop 0\n"
"v_mfma_f32_16x16x32_bf16 v[28:31], a[164:167], v[180:183], v[28:31]\n"
"s_nop 0\n"
"v_mfma_f32_16x16x32_bf16 v[28:31], a[168:171], v[184:187], v[28:31]\n"
"buffer_load_dwordx4 v204, s[28:31], s52 offen offset:2048 lds\n"
"v_mfma_f32_16x16x32_bf16 v[28:31], a[172:175], v[188:191], v[28:31]\n"
"s_add_u32 s52, s52, 8192\n"
"v_mfma_f32_16x16x32_bf16 v[28:31], a[176:179], a[240:243], v[28:31]\n"
"v_cvt_pk_bf16_f32 v42, v24, v25\n"
"v_mfma_f32_16x16x32_bf16 v[28:31], a[180:183], a[244:247], v[28:31]\n"
"v_cvt_pk_bf16_f32 v43, v26, v27\n"
"v_mfma_f32_16x16x32_bf16 v[28:31], a[184:187], a[248:251], v[28:31]\n"
"buffer_load_dwordx4 v204, s[28:31], s52 offen offset:3072 lds\n"
"v_mfma_f32_16x16x32_bf16 v[28:31], a[188:191], a[252:255], v[28:31]\n"
"s_waitcnt lgkmcnt(0)\n"
"s_add_u32 s52, s52, 12288\n"
"buffer_store_dwordx4 v[40:43], v207, s[32:35], s54 offen offset:0\n"
"v_mfma_f32_16x16x32_bf16 v[0:3], a[128:131], v[128:131], 0\n"
"ds_read_b64_tr_b16 a[160:161], v195 offset:32768\n"
"v_mfma_f32_16x16x32_bf16 v[0:3], a[132:135], v[132:135], v[0:3]\n"
"ds_read_b64_tr_b16 a[162:163], v195 offset:34816\n"
"v_mfma_f32_16x16x32_bf16 v[0:3], a[136:139], v[136:139], v[0:3]\n"
"ds_read_b64_tr_b16 a[164:165], v195 offset:36864\n"
"v_mfma_f32_16x16x32_bf16 v[0:3], a[140:143], v[140:143], v[0:3]\n"
"ds_read_b64_tr_b16 a[166:167], v195 offset:38912\n"
"v_mfma_f32_16x16x32_bf16 v[0:3], a[144:147], a[192:195], v[0:3]\n"
"ds_read_b64_tr_b16 a[168:169], v195 offset:40960\n"
"v_cvt_pk_bf16_f32 v46, v28, v29\n"
"v_mfma_f32_16x16x32_bf16 v[0:3], a[148:151], a[196:199], v[0:3]\n"
"ds_read_b64_tr_b16 a[170:171], v195 offset:43008\n"
"v_cvt_pk_bf16_f32 v47, v30, v31\n"
"v_mfma_f32_16x16x32_bf16 v[0:3], a[152:155], a[200:203], v[0:3]\n"
"ds_read_b64_tr_b16 a[172:173], v195 offset:45056\n"
"v_mfma_f32_16x16x32_bf16 v[0:3], a[156:159], a[204:207], v[0:3]\n"
"buffer_store_dwordx4 v[44:47], v208, s[32:35], s54 offen offset:0\n"
"ds_read_b64_tr_b16 a[174:175], v195 offset:47104\n"
"v_mfma_f32_16x16x32_bf16 v[4:7], a[128:131], v[144:147], 0\n"
"ds_read_b64_tr_b16 a[176:177], v201 offset:32768\n"
"v_mfma_f32_16x16x32_bf16 v[4:7], a[132:135], v[148:151], v[4:7]\n"
"ds_read_b64_tr_b16 a[178:179], v201 offset:34816\n"
"v_mfma_f32_16x16x32_bf16 v[4:7], a[136:139], v[152:155], v[4:7]\n"
"ds_read_b64_tr_b16 a[180:181], v201 offset:36864\n"
"v_mfma_f32_16x16x32_bf16 v[4:7], a[140:143], v[156:159], v[4:7]\n"
"ds_read_b64_tr_b16 a[182:183], v201 offset:38912\n"
"v_mfma_f32_16x16x32_bf16 v[4:7], a[144:147], a[208:211], v[4:7]\n"
"ds_read_b64_tr_b16 a[184:185], v201 offset:40960\n"
"v_cvt_pk_bf16_f32 v32, v0, v1\n"
"v_mfma_f32_16x16x32_bf16 v[4:7], a[148:151], a[212:215], v[4:7]\n"
"ds_read_b64_tr_b16 a[186:187], v201 offset:43008\n"
"v_cvt_pk_bf16_f32 v33, v2, v3\n"
"v_mfma_f32_16x16x32_bf16 v[4:7], a[152:155], a[216:219], v[4:7]\n"
"ds_read_b64_tr_b16 a[188:189], v201 offset:45056\n"
"v_mfma_f32_16x16x32_bf16 v[4:7], a[156:159], a[220:223], v[4:7]\n"
"s_add_u32 m0, s53, 102400\n"
"ds_read_b64_tr_b16 a[190:191], v201 offset:47104\n"
"v_mfma_f32_16x16x32_bf16 v[8:11], a[128:131], v[160:163], 0\n"
"buffer_load_dwordx4 v204, s[28:31], s52 offen offset:0 lds\n"
"v_mfma_f32_16x16x32_bf16 v[8:11], a[132:135], v[164:167], v[8:11]\n"
"s_add_u32 s52, s52, 8192\n"
"v_mfma_f32_16x16x32_bf16 v[8:11], a[136:139], v[168:171], v[8:11]\n"
"s_nop 0\n"
"v_mfma_f32_16x16x32_bf16 v[8:11], a[140:143], v[172:175], v[8:11]\n"
"s_nop 0\n"
"v_mfma_f32_16x16x32_bf16 v[8:11], a[144:147], a[224:227], v[8:11]\n"
"v_cvt_pk_bf16_f32 v36, v4, v5\n"
"v_mfma_f32_16x16x32_bf16 v[8:11], a[148:151], a[228:231], v[8:11]\n"
"v_cvt_pk_bf16_f32 v37, v6, v7\n"
"v_mfma_f32_16x16x32_bf16 v[8:11], a[152:155], a[232:235], v[8:11]\n"
"buffer_load_dwordx4 v204, s[28:31], s52 offen offset:1024 lds\n"
"v_mfma_f32_16x16x32_bf16 v[8:11], a[156:159], a[236:239], v[8:11]\n"
"s_add_u32 s52, s52, 8192\n"
"v_mfma_f32_16x16x32_bf16 v[12:15], a[128:131], v[176:179], 0\n"
"s_nop 0\n"
"v_mfma_f32_16x16x32_bf16 v[12:15], a[132:135], v[180:183], v[12:15]\n"
"s_nop 0\n"
"v_mfma_f32_16x16x32_bf16 v[12:15], a[136:139], v[184:187], v[12:15]\n"
"buffer_load_dwordx4 v204, s[28:31], s52 offen offset:2048 lds\n"
"v_mfma_f32_16x16x32_bf16 v[12:15], a[140:143], v[188:191], v[12:15]\n"
"s_add_u32 s52, s52, 8192\n"
"v_mfma_f32_16x16x32_bf16 v[12:15], a[144:147], a[240:243], v[12:15]\n"
"v_cvt_pk_bf16_f32 v40, v8, v9\n"
"v_mfma_f32_16x16x32_bf16 v[12:15], a[148:151], a[244:247], v[12:15]\n"
"v_cvt_pk_bf16_f32 v41, v10, v11\n"
"v_mfma_f32_16x16x32_bf16 v[12:15], a[152:155], a[248:251], v[12:15]\n"
"s_waitcnt vmcnt(11) lgkmcnt(0)\n"
"s_barrier\n"
"v_mfma_f32_16x16x32_bf16 v[12:15], a[156:159], a[252:255], v[12:15]\n"
"s_nop 0\n"
"v_mfma_f32_16x16x32_bf16 v[16:19], a[160:163], v[128:131], 0\n"
"ds_read_b64_tr_b16 a[128:129], v193 offset:0\n"
"v_mfma_f32_16x16x32_bf16 v[16:19], a[164:167], v[132:135], v[16:19]\n"
"ds_read_b64_tr_b16 a[130:131], v193 offset:2048\n"
"v_mfma_f32_16x16x32_bf16 v[16:19], a[168:171], v[136:139], v[16:19]\n"
"ds_read_b64_tr_b16 a[132:133], v193 offset:4096\n"
"v_mfma_f32_16x16x32_bf16 v[16:19], a[172:175], v[140:143], v[16:19]\n"
"ds_read_b64_tr_b16 a[134:135], v193 offset:6144\n"
"v_mfma_f32_16x16x32_bf16 v[16:19], a[176:179], a[192:195], v[16:19]\n"
"ds_read_b64_tr_b16 a[136:137], v193 offset:8192\n"
"v_cvt_pk_bf16_f32 v44, v12, v13\n"
"v_mfma_f32_16x16x32_bf16 v[16:19], a[180:183], a[196:199], v[16:19]\n"
"ds_read_b64_tr_b16 a[138:139], v193 offset:10240\n"
"v_cvt_pk_bf16_f32 v45, v14, v15\n"
"v_mfma_f32_16x16x32_bf16 v[16:19], a[184:187], a[200:203], v[16:19]\n"
"ds_read_b64_tr_b16 a[140:141], v193 offset:12288\n"
"v_mfma_f32_16x16x32_bf16 v[16:19], a[188:191], a[204:207], v[16:19]\n"
"ds_read_b64_tr_b16 a[142:143], v193 offset:14336\n"
"v_mfma_f32_16x16x32_bf16 v[20:23], a[160:163], v[144:147], 0\n"
"ds_read_b64_tr_b16 a[144:145], v199 offset:0\n"
"v_mfma_f32_16x16x32_bf16 v[20:23], a[164:167], v[148:151], v[20:23]\n"
"ds_read_b64_tr_b16 a[146:147], v199 offset:2048\n"
"v_mfma_f32_16x16x32_bf16 v[20:23], a[168:171], v[152:155], v[20:23]\n"
"ds_read_b64_tr_b16 a[148:149], v199 offset:4096\n"
"v_mfma_f32_16x16x32_bf16 v[20:23], a[172:175], v[156:159], v[20:23]\n"
"ds_read_b64_tr_b16 a[150:151], v199 offset:6144\n"
"v_mfma_f32_16x16x32_bf16 v[20:23], a[176:179], a[208:211], v[20:23]\n"
"ds_read_b64_tr_b16 a[152:153], v199 offset:8192\n"
"v_cvt_pk_bf16_f32 v34, v16, v17\n"
"v_mfma_f32_16x16x32_bf16 v[20:23], a[180:183], a[212:215], v[20:23]\n"
"ds_read_b64_tr_b16 a[154:155], v199 offset:10240\n"
"v_cvt_pk_bf16_f32 v35, v18, v19\n"
"v_mfma_f32_16x16x32_bf16 v[20:23], a[184:187], a[216:219], v[20:23]\n"
"ds_read_b64_tr_b16 a[156:157], v199 offset:12288\n"
"v_mfma_f32_16x16x32_bf16 v[20:23], a[188:191], a[220:223], v[20:23]\n"
"ds_read_b64_tr_b16 a[158:159], v199 offset:14336\n"
"buffer_store_dwordx4 v[32:35], v205, s[32:35], s54 offen offset:2048\n"
"v_mfma_f32_16x16x32_bf16 v[24:27], a[160:163], v[160:163], 0\n"
"buffer_load_dwordx4 v204, s[28:31], s52 offen offset:3072 lds\n"
"v_mfma_f32_16x16x32_bf16 v[24:27], a[164:167], v[164:167], v[24:27]\n"
"s_sub_u32 s52, s52, 61312\n"
"v_mfma_f32_16x16x32_bf16 v[24:27], a[168:171], v[168:171], v[24:27]\n"
"s_nop 0\n"
"v_mfma_f32_16x16x32_bf16 v[24:27], a[172:175], v[172:175], v[24:27]\n"
"s_nop 0\n"
"v_mfma_f32_16x16x32_bf16 v[24:27], a[176:179], a[224:227], v[24:27]\n"
"v_cvt_pk_bf16_f32 v38, v20, v21\n"
"v_mfma_f32_16x16x32_bf16 v[24:27], a[180:183], a[228:231], v[24:27]\n"
"s_add_u32 m0, s53, 131072\n"
"v_cvt_pk_bf16_f32 v39, v22, v23\n"
"v_mfma_f32_16x16x32_bf16 v[24:27], a[184:187], a[232:235], v[24:27]\n"
"buffer_load_dwordx4 v204, s[28:31], s52 offen offset:0 lds\n"
"v_mfma_f32_16x16x32_bf16 v[24:27], a[188:191], a[236:239], v[24:27]\n"
"s_add_u32 s52, s52, 8192\n"
"buffer_store_dwordx4 v[36:39], v206, s[32:35], s54 offen offset:2048\n"
"v_mfma_f32_16x16x32_bf16 v[28:31], a[160:163], v[176:179], 0\n"
"s_nop 0\n"
"v_mfma_f32_16x16x32_bf16 v[28:31], a[164:167], v[180:183], v[28:31]\n"
"s_nop 0\n"
"v_mfma_f32_16x16x32_bf16 v[28:31], a[168:171], v[184:187], v[28:31]\n"
"buffer_load_dwordx4 v204, s[28:31], s52 offen offset:1024 lds\n"
"v_mfma_f32_16x16x32_bf16 v[28:31], a[172:175], v[188:191], v[28:31]\n"
"s_add_u32 s52, s52, 8192\n"
"v_mfma_f32_16x16x32_bf16 v[28:31], a[176:179], a[240:243], v[28:31]\n"
"v_cvt_pk_bf16_f32 v42, v24, v25\n"
"v_mfma_f32_16x16x32_bf16 v[28:31], a[180:183], a[244:247], v[28:31]\n"
"v_cvt_pk_bf16_f32 v43, v26, v27\n"
"v_mfma_f32_16x16x32_bf16 v[28:31], a[184:187], a[248:251], v[28:31]\n"
"buffer_load_dwordx4 v204, s[28:31], s52 offen offset:2048 lds\n"
"v_mfma_f32_16x16x32_bf16 v[28:31], a[188:191], a[252:255], v[28:31]\n"
"s_waitcnt lgkmcnt(0)\n"
"s_add_u32 s52, s52, 8192\n"
"buffer_store_dwordx4 v[40:43], v207, s[32:35], s54 offen offset:2048\n"
"v_mfma_f32_16x16x32_bf16 v[0:3], a[128:131], v[128:131], 0\n"
"ds_read_b64_tr_b16 a[160:161], v196 offset:0\n"
"v_mfma_f32_16x16x32_bf16 v[0:3], a[132:135], v[132:135], v[0:3]\n"
"ds_read_b64_tr_b16 a[162:163], v196 offset:2048\n"
"v_mfma_f32_16x16x32_bf16 v[0:3], a[136:139], v[136:139], v[0:3]\n"
"ds_read_b64_tr_b16 a[164:165], v196 offset:4096\n"
"v_mfma_f32_16x16x32_bf16 v[0:3], a[140:143], v[140:143], v[0:3]\n"
"ds_read_b64_tr_b16 a[166:167], v196 offset:6144\n"
"v_mfma_f32_16x16x32_bf16 v[0:3], a[144:147], a[192:195], v[0:3]\n"
"ds_read_b64_tr_b16 a[168:169], v196 offset:8192\n"
"v_cvt_pk_bf16_f32 v46, v28, v29\n"
"v_mfma_f32_16x16x32_bf16 v[0:3], a[148:151], a[196:199], v[0:3]\n"
"ds_read_b64_tr_b16 a[170:171], v196 offset:10240\n"
"v_cvt_pk_bf16_f32 v47, v30, v31\n"
"v_mfma_f32_16x16x32_bf16 v[0:3], a[152:155], a[200:203], v[0:3]\n"
"ds_read_b64_tr_b16 a[172:173], v196 offset:12288\n"
"v_mfma_f32_16x16x32_bf16 v[0:3], a[156:159], a[204:207], v[0:3]\n"
"buffer_store_dwordx4 v[44:47], v208, s[32:35], s54 offen offset:2048\n"
"ds_read_b64_tr_b16 a[174:175], v196 offset:14336\n"
"v_mfma_f32_16x16x32_bf16 v[4:7], a[128:131], v[144:147], 0\n"
"s_add_u32 s54, s54, 4096\n"
"ds_read_b64_tr_b16 a[176:177], v202 offset:0\n"
"v_mfma_f32_16x16x32_bf16 v[4:7], a[132:135], v[148:151], v[4:7]\n"
"ds_read_b64_tr_b16 a[178:179], v202 offset:2048\n"
"v_mfma_f32_16x16x32_bf16 v[4:7], a[136:139], v[152:155], v[4:7]\n"
"ds_read_b64_tr_b16 a[180:181], v202 offset:4096\n"
"v_mfma_f32_16x16x32_bf16 v[4:7], a[140:143], v[156:159], v[4:7]\n"
"ds_read_b64_tr_b16 a[182:183], v202 offset:6144\n"
"v_mfma_f32_16x16x32_bf16 v[4:7], a[144:147], a[208:211], v[4:7]\n"
"ds_read_b64_tr_b16 a[184:185], v202 offset:8192\n"
"v_cvt_pk_bf16_f32 v32, v0, v1\n"
"v_mfma_f32_16x16x32_bf16 v[4:7], a[148:151], a[212:215], v[4:7]\n"
"ds_read_b64_tr_b16 a[186:187], v202 offset:10240\n"
"v_cvt_pk_bf16_f32 v33, v2, v3\n"
"v_mfma_f32_16x16x32_bf16 v[4:7], a[152:155], a[216:219], v[4:7]\n"
"ds_read_b64_tr_b16 a[188:189], v202 offset:12288\n"
"v_mfma_f32_16x16x32_bf16 v[4:7], a[156:159], a[220:223], v[4:7]\n"
"ds_read_b64_tr_b16 a[190:191], v202 offset:14336\n"
"v_mfma_f32_16x16x32_bf16 v[8:11], a[128:131], v[160:163], 0\n"
"buffer_load_dwordx4 v204, s[28:31], s52 offen offset:3072 lds\n"
"v_mfma_f32_16x16x32_bf16 v[8:11], a[132:135], v[164:167], v[8:11]\n"
"s_add_u32 s52, s52, 12288\n"
"v_mfma_f32_16x16x32_bf16 v[8:11], a[136:139], v[168:171], v[8:11]\n"
"s_nop 0\n"
"v_mfma_f32_16x16x32_bf16 v[8:11], a[140:143], v[172:175], v[8:11]\n"
"s_nop 0\n"
"v_mfma_f32_16x16x32_bf16 v[8:11], a[144:147], a[224:227], v[8:11]\n"
"v_cvt_pk_bf16_f32 v36, v4, v5\n"
"v_mfma_f32_16x16x32_bf16 v[8:11], a[148:151], a[228:231], v[8:11]\n"
"s_add_u32 m0, s53, 135168\n"
"v_cvt_pk_bf16_f32 v37, v6, v7\n"
"v_mfma_f32_16x16x32_bf16 v[8:11], a[152:155], a[232:235], v[8:11]\n"
"buffer_load_dwordx4 v204, s[28:31], s52 offen offset:0 lds\n"
"v_mfma_f32_16x16x32_bf16 v[8:11], a[156:159], a[236:239], v[8:11]\n"
"s_add_u32 s52, s52, 8192\n"
"v_mfma_f32_16x16x32_bf16 v[12:15], a[128:131], v[176:179], 0\n"
"s_nop 0\n"
"v_mfma_f32_16x16x32_bf16 v[12:15], a[132:135], v[180:183], v[12:15]\n"
"s_nop 0\n"
"v_mfma_f32_16x16x32_bf16 v[12:15], a[136:139], v[184:187], v[12:15]\n"
"buffer_load_dwordx4 v204, s[28:31], s52 offen offset:1024 lds\n"
"v_mfma_f32_16x16x32_bf16 v[12:15], a[140:143], v[188:191], v[12:15]\n"
"s_add_u32 s52, s52, 8192\n"
"v_mfma_f32_16x16x32_bf16 v[12:15], a[144:147], a[240:243], v[12:15]\n"
"v_cvt_pk_bf16_f32 v40, v8, v9\n"
"v_mfma_f32_16x16x32_bf16 v[12:15], a[148:151], a[244:247], v[12:15]\n"
"v_cvt_pk_bf16_f32 v41, v10, v11\n"
"v_mfma_f32_16x16x32_bf16 v[12:15], a[152:155], a[248:251], v[12:15]\n"
"s_waitcnt vmcnt(9) lgkmcnt(0)\n"
"s_barrier\n"
"v_mfma_f32_16x16x32_bf16 v[12:15], a[156:159], a[252:255], v[12:15]\n"
"s_nop 0\n"
"v_mfma_f32_16x16x32_bf16 v[16:19], a[160:163], v[128:131], 0\n"
"ds_read_b64_tr_b16 a[128:129], v193 offset:32768\n"
"v_mfma_f32_16x16x32_bf16 v[16:19], a[164:167], v[132:135], v[16:19]\n"
"ds_read_b64_tr_b16 a[130:131], v193 offset:34816\n"
"v_mfma_f32_16x16x32_bf16 v[16:19], a[168:171], v[136:139], v[16:19]\n"
"ds_read_b64_tr_b16 a[132:133], v193 offset:36864\n"
"v_mfma_f32_16x16x32_bf16 v[16:19], a[172:175], v[140:143], v[16:19]\n"
"ds_read_b64_tr_b16 a[134:135], v193 offset:38912\n"
"v_mfma_f32_16x16x32_bf16 v[16:19], a[176:179], a[192:195], v[16:19]\n"
"ds_read_b64_tr_b16 a[136:137], v193 offset:40960\n"
"v_cvt_pk_bf16_f32 v44, v12, v13\n"
"v_mfma_f32_16x16x32_bf16 v[16:19], a[180:183], a[196:199], v[16:19]\n"
"ds_read_b64_tr_b16 a[138:139], v193 offset:43008\n"
"v_cvt_pk_bf16_f32 v45, v14, v15\n"
"v_mfma_f32_16x16x32_bf16 v[16:19], a[184:187], a[200:203], v[16:19]\n"
"ds_read_b64_tr_b16 a[140:141], v193 offset:45056\n"
"v_mfma_f32_16x16x32_bf16 v[16:19], a[188:191], a[204:207], v[16:19]\n"
"ds_read_b64_tr_b16 a[142:143], v193 offset:47104\n"
"v_mfma_f32_16x16x32_bf16 v[20:23], a[160:163], v[144:147], 0\n"
"ds_read_b64_tr_b16 a[144:145], v199 offset:32768\n"
"v_mfma_f32_16x16x32_bf16 v[20:23], a[164:167], v[148:151], v[20:23]\n"
"ds_read_b64_tr_b16 a[146:147], v199 offset:34816\n"
"v_mfma_f32_16x16x32_bf16 v[20:23], a[168:171], v[152:155], v[20:23]\n"
"ds_read_b64_tr_b16 a[148:149], v199 offset:36864\n"
"v_mfma_f32_16x16x32_bf16 v[20:23], a[172:175], v[156:159], v[20:23]\n"
"ds_read_b64_tr_b16 a[150:151], v199 offset:38912\n"
"v_mfma_f32_16x16x32_bf16 v[20:23], a[176:179], a[208:211], v[20:23]\n"
"ds_read_b64_tr_b16 a[152:153], v199 offset:40960\n"
"v_cvt_pk_bf16_f32 v34, v16, v17\n"
"v_mfma_f32_16x16x32_bf16 v[20:23], a[180:183], a[212:215], v[20:23]\n"
"ds_read_b64_tr_b16 a[154:155], v199 offset:43008\n"
"v_cvt_pk_bf16_f32 v35, v18, v19\n"
"v_mfma_f32_16x16x32_bf16 v[20:23], a[184:187], a[216:219], v[20:23]\n"
"ds_read_b64_tr_b16 a[156:157], v199 offset:45056\n"
"v_mfma_f32_16x16x32_bf16 v[20:23], a[188:191], a[220:223], v[20:23]\n"
"ds_read_b64_tr_b16 a[158:159], v199 offset:47104\n"
"buffer_store_dwordx4 v[32:35], v205, s[32:35], s54 offen offset:0\n"
"v_mfma_f32_16x16x32_bf16 v[24:27], a[160:163], v[160:163], 0\n"
"buffer_load_dwordx4 v204, s[28:31], s52 offen offset:2048 lds\n"
"v_mfma_f32_16x16x32_bf16 v[24:27], a[164:167], v[164:167], v[24:27]\n"
"s_add_u32 s52, s52, 8192\n"
"v_mfma_f32_16x16x32_bf16 v[24:27], a[168:171], v[168:171], v[24:27]\n"
"s_nop 0\n"
"v_mfma_f32_16x16x32_bf16 v[24:27], a[172:175], v[172:175], v[24:27]\n"
"s_nop 0\n"
"v_mfma_f32_16x16x32_bf16 v[24:27], a[176:179], a[224:227], v[24:27]\n"
"v_cvt_pk_bf16_f32 v38, v20, v21\n"
"v_mfma_f32_16x16x32_bf16 v[24:27], a[180:183], a[228:231], v[24:27]\n"
"v_cvt_pk_bf16_f32 v39, v22, v23\n"
"v_mfma_f32_16x16x32_bf16 v[24:27], a[184:187], a[232:235], v[24:27]\n"
"buffer_load_dwordx4 v204, s[28:31], s52 offen offset:3072 lds\n"
"v_mfma_f32_16x16x32_bf16 v[24:27], a[188:191], a[236:239], v[24:27]\n"
"s_sub_u32 s52, s52, 61312\n"
"buffer_store_dwordx4 v[36:39], v206, s[32:35], s54 offen offset:0\n"
"v_mfma_f32_16x16x32_bf16 v[28:31], a[160:163], v[176:179], 0\n"
"s_nop 0\n"
"v_mfma_f32_16x16x32_bf16 v[28:31], a[164:167], v[180:183], v[28:31]\n"
"s_mov_b32 m0, s53\n"
"v_mfma_f32_16x16x32_bf16 v[28:31], a[168:171], v[184:187], v[28:31]\n"
"buffer_load_dwordx4 v204, s[28:31], s52 offen offset:0 lds\n"
"v_mfma_f32_16x16x32_bf16 v[28:31], a[172:175], v[188:191], v[28:31]\n"
"s_add_u32 s52, s52, 8192\n"
"v_mfma_f32_16x16x32_bf16 v[28:31], a[176:179], a[240:243], v[28:31]\n"
"v_cvt_pk_bf16_f32 v42, v24, v25\n"
"v_mfma_f32_16x16x32_bf16 v[28:31], a[180:183], a[244:247], v[28:31]\n"
"v_cvt_pk_bf16_f32 v43, v26, v27\n"
"v_mfma_f32_16x16x32_bf16 v[28:31], a[184:187], a[248:251], v[28:31]\n"
"buffer_load_dwordx4 v204, s[28:31], s52 offen offset:1024 lds\n"
"v_mfma_f32_16x16x32_bf16 v[28:31], a[188:191], a[252:255], v[28:31]\n"
"s_waitcnt lgkmcnt(0)\n"
"s_add_u32 s52, s52, 8192\n"
"buffer_store_dwordx4 v[40:43], v207, s[32:35], s54 offen offset:0\n"
"v_mfma_f32_16x16x32_bf16 v[0:3], a[128:131], v[128:131], 0\n"
"ds_read_b64_tr_b16 a[160:161], v196 offset:32768\n"
"v_mfma_f32_16x16x32_bf16 v[0:3], a[132:135], v[132:135], v[0:3]\n"
"ds_read_b64_tr_b16 a[162:163], v196 offset:34816\n"
"v_mfma_f32_16x16x32_bf16 v[0:3], a[136:139], v[136:139], v[0:3]\n"
"ds_read_b64_tr_b16 a[164:165], v196 offset:36864\n"
"v_mfma_f32_16x16x32_bf16 v[0:3], a[140:143], v[140:143], v[0:3]\n"
"ds_read_b64_tr_b16 a[166:167], v196 offset:38912\n"
"v_mfma_f32_16x16x32_bf16 v[0:3], a[144:147], a[192:195], v[0:3]\n"
"ds_read_b64_tr_b16 a[168:169], v196 offset:40960\n"
"v_cvt_pk_bf16_f32 v46, v28, v29\n"
"v_mfma_f32_16x16x32_bf16 v[0:3], a[148:151], a[196:199], v[0:3]\n"
"ds_read_b64_tr_b16 a[170:171], v196 offset:43008\n"
"v_cvt_pk_bf16_f32 v47, v30, v31\n"
"v_mfma_f32_16x16x32_bf16 v[0:3], a[152:155], a[200:203], v[0:3]\n"
"ds_read_b64_tr_b16 a[172:173], v196 offset:45056\n"
"v_mfma_f32_16x16x32_bf16 v[0:3], a[156:159], a[204:207], v[0:3]\n"
"buffer_store_dwordx4 v[44:47], v208, s[32:35], s54 offen offset:0\n"
"ds_read_b64_tr_b16 a[174:175], v196 offset:47104\n"
"v_mfma_f32_16x16x32_bf16 v[4:7], a[128:131], v[144:147], 0\n"
"ds_read_b64_tr_b16 a[176:177], v202 offset:32768\n"
"v_mfma_f32_16x16x32_bf16 v[4:7], a[132:135], v[148:151], v[4:7]\n"
"ds_read_b64_tr_b16 a[178:179], v202 offset:34816\n"
"v_mfma_f32_16x16x32_bf16 v[4:7], a[136:139], v[152:155], v[4:7]\n"
"ds_read_b64_tr_b16 a[180:181], v202 offset:36864\n"
"v_mfma_f32_16x16x32_bf16 v[4:7], a[140:143], v[156:159], v[4:7]\n"
"ds_read_b64_tr_b16 a[182:183], v202 offset:38912\n"
"v_mfma_f32_16x16x32_bf16 v[4:7], a[144:147], a[208:211], v[4:7]\n"
"ds_read_b64_tr_b16 a[184:185], v202 offset:40960\n"
"v_cvt_pk_bf16_f32 v32, v0, v1\n"
"v_mfma_f32_16x16x32_bf16 v[4:7], a[148:151], a[212:215], v[4:7]\n"
"ds_read_b64_tr_b16 a[186:187], v202 offset:43008\n"
"v_cvt_pk_bf16_f32 v33, v2, v3\n"
"v_mfma_f32_16x16x32_bf16 v[4:7], a[152:155], a[216:219], v[4:7]\n"
"ds_read_b64_tr_b16 a[188:189], v202 offset:45056\n"
"v_mfma_f32_16x16x32_bf16 v[4:7], a[156:159], a[220:223], v[4:7]\n"
"ds_read_b64_tr_b16 a[190:191], v202 offset:47104\n"
"v_mfma_f32_16x16x32_bf16 v[8:11], a[128:131], v[160:163], 0\n"
"buffer_load_dwordx4 v204, s[28:31], s52 offen offset:2048 lds\n"
"v_mfma_f32_16x16x32_bf16 v[8:11], a[132:135], v[164:167], v[8:11]\n"
"s_add_u32 s52, s52, 8192\n"
"v_mfma_f32_16x16x32_bf16 v[8:11], a[136:139], v[168:171], v[8:11]\n"
"s_nop 0\n"
"v_mfma_f32_16x16x32_bf16 v[8:11], a[140:143], v[172:175], v[8:11]\n"
"s_nop 0\n"
"v_mfma_f32_16x16x32_bf16 v[8:11], a[144:147], a[224:227], v[8:11]\n"
"v_cvt_pk_bf16_f32 v36, v4, v5\n"
"v_mfma_f32_16x16x32_bf16 v[8:11], a[148:151], a[228:231], v[8:11]\n"
"v_cvt_pk_bf16_f32 v37, v6, v7\n"
"v_mfma_f32_16x16x32_bf16 v[8:11], a[152:155], a[232:235], v[8:11]\n"
"buffer_load_dwordx4 v204, s[28:31], s52 offen offset:3072 lds\n"
"v_mfma_f32_16x16x32_bf16 v[8:11], a[156:159], a[236:239], v[8:11]\n"
"s_add_u32 s52, s52, 12288\n"
"v_mfma_f32_16x16x32_bf16 v[12:15], a[128:131], v[176:179], 0\n"
"s_nop 0\n"
"v_mfma_f32_16x16x32_bf16 v[12:15], a[132:135], v[180:183], v[12:15]\n"
"s_add_u32 m0, s53, 4096\n"
"v_mfma_f32_16x16x32_bf16 v[12:15], a[136:139], v[184:187], v[12:15]\n"
"buffer_load_dwordx4 v204, s[28:31], s52 offen offset:0 lds\n"
"v_mfma_f32_16x16x32_bf16 v[12:15], a[140:143], v[188:191], v[12:15]\n"
"s_add_u32 s52, s52, 8192\n"
"v_mfma_f32_16x16x32_bf16 v[12:15], a[144:147], a[240:243], v[12:15]\n"
"v_cvt_pk_bf16_f32 v40, v8, v9\n"
"v_mfma_f32_16x16x32_bf16 v[12:15], a[148:151], a[244:247], v[12:15]\n"
"v_cvt_pk_bf16_f32 v41, v10, v11\n"
"v_mfma_f32_16x16x32_bf16 v[12:15], a[152:155], a[248:251], v[12:15]\n"
"s_waitcnt vmcnt(8) lgkmcnt(0)\n"
"s_barrier\n"
"v_mfma_f32_16x16x32_bf16 v[12:15], a[156:159], a[252:255], v[12:15]\n"
"s_nop 0\n"
"v_mfma_f32_16x16x32_bf16 v[16:19], a[160:163], v[128:131], 0\n"
"ds_read_b64_tr_b16 a[128:129], v194 offset:0\n"
"v_mfma_f32_16x16x32_bf16 v[16:19], a[164:167], v[132:135], v[16:19]\n"
"ds_read_b64_tr_b16 a[130:131], v194 offset:2048\n"
"v_mfma_f32_16x16x32_bf16 v[16:19], a[168:171], v[136:139], v[16:19]\n"
"ds_read_b64_tr_b16 a[132:133], v194 offset:4096\n"
"v_mfma_f32_16x16x32_bf16 v[16:19], a[172:175], v[140:143], v[16:19]\n"
"ds_read_b64_tr_b16 a[134:135], v194 offset:6144\n"
"v_mfma_f32_16x16x32_bf16 v[16:19], a[176:179], a[192:195], v[16:19]\n"
"ds_read_b64_tr_b16 a[136:137], v194 offset:8192\n"
"v_cvt_pk_bf16_f32 v44, v12, v13\n"
"v_mfma_f32_16x16x32_bf16 v[16:19], a[180:183], a[196:199], v[16:19]\n"
"ds_read_b64_tr_b16 a[138:139], v194 offset:10240\n"
"v_cvt_pk_bf16_f32 v45, v14, v15\n"
"v_mfma_f32_16x16x32_bf16 v[16:19], a[184:187], a[200:203], v[16:19]\n"
"ds_read_b64_tr_b16 a[140:141], v194 offset:12288\n"
"v_mfma_f32_16x16x32_bf16 v[16:19], a[188:191], a[204:207], v[16:19]\n"
"ds_read_b64_tr_b16 a[142:143], v194 offset:14336\n"
"v_mfma_f32_16x16x32_bf16 v[20:23], a[160:163], v[144:147], 0\n"
"ds_read_b64_tr_b16 a[144:145], v200 offset:0\n"
"v_mfma_f32_16x16x32_bf16 v[20:23], a[164:167], v[148:151], v[20:23]\n"
"ds_read_b64_tr_b16 a[146:147], v200 offset:2048\n"
"v_mfma_f32_16x16x32_bf16 v[20:23], a[168:171], v[152:155], v[20:23]\n"
"ds_read_b64_tr_b16 a[148:149], v200 offset:4096\n"
"v_mfma_f32_16x16x32_bf16 v[20:23], a[172:175], v[156:159], v[20:23]\n"
"ds_read_b64_tr_b16 a[150:151], v200 offset:6144\n"
"v_mfma_f32_16x16x32_bf16 v[20:23], a[176:179], a[208:211], v[20:23]\n"
"ds_read_b64_tr_b16 a[152:153], v200 offset:8192\n"
"v_cvt_pk_bf16_f32 v34, v16, v17\n"
"v_mfma_f32_16x16x32_bf16 v[20:23], a[180:183], a[212:215], v[20:23]\n"
"ds_read_b64_tr_b16 a[154:155], v200 offset:10240\n"
"v_cvt_pk_bf16_f32 v35, v18, v19\n"
"v_mfma_f32_16x16x32_bf16 v[20:23], a[184:187], a[216:219], v[20:23]\n"
"ds_read_b64_tr_b16 a[156:157], v200 offset:12288\n"
"v_mfma_f32_16x16x32_bf16 v[20:23], a[188:191], a[220:223], v[20:23]\n"
"ds_read_b64_tr_b16 a[158:159], v200 offset:14336\n"
"buffer_store_dwordx4 v[32:35], v205, s[32:35], s54 offen offset:2048\n"
"v_mfma_f32_16x16x32_bf16 v[24:27], a[160:163], v[160:163], 0\n"
"buffer_load_dwordx4 v204, s[28:31], s52 offen offset:1024 lds\n"
"v_mfma_f32_16x16x32_bf16 v[24:27], a[164:167], v[164:167], v[24:27]\n"
"s_add_u32 s52, s52, 8192\n"
"v_mfma_f32_16x16x32_bf16 v[24:27], a[168:171], v[168:171], v[24:27]\n"
"s_nop 0\n"
"v_mfma_f32_16x16x32_bf16 v[24:27], a[172:175], v[172:175], v[24:27]\n"
"s_nop 0\n"
"v_mfma_f32_16x16x32_bf16 v[24:27], a[176:179], a[224:227], v[24:27]\n"
"v_cvt_pk_bf16_f32 v38, v20, v21\n"
"v_mfma_f32_16x16x32_bf16 v[24:27], a[180:183], a[228:231], v[24:27]\n"
"v_cvt_pk_bf16_f32 v39, v22, v23\n"
"v_mfma_f32_16x16x32_bf16 v[24:27], a[184:187], a[232:235], v[24:27]\n"
"buffer_load_dwordx4 v204, s[28:31], s52 offen offset:2048 lds\n"
"v_mfma_f32_16x16x32_bf16 v[24:27], a[188:191], a[236:239], v[24:27]\n"
"s_add_u32 s52, s52, 8192\n"
"buffer_store_dwordx4 v[36:39], v206, s[32:35], s54 offen offset:2048\n"
"v_mfma_f32_16x16x32_bf16 v[28:31], a[160:163], v[176:179], 0\n"
"s_nop 0\n"
"v_mfma_f32_16x16x32_bf16 v[28:31], a[164:167], v[180:183], v[28:31]\n"
"s_nop 0\n"
"v_mfma_f32_16x16x32_bf16 v[28:31], a[168:171], v[184:187], v[28:31]\n"
"buffer_load_dwordx4 v204, s[28:31], s52 offen offset:3072 lds\n"
"v_mfma_f32_16x16x32_bf16 v[28:31], a[172:175], v[188:191], v[28:31]\n"
"s_sub_u32 s52, s52, 61312\n"
"v_mfma_f32_16x16x32_bf16 v[28:31], a[176:179], a[240:243], v[28:31]\n"
"v_cvt_pk_bf16_f32 v42, v24, v25\n"
"v_mfma_f32_16x16x32_bf16 v[28:31], a[180:183], a[244:247], v[28:31]\n"
"s_add_u32 m0, s53, 32768\n"
"v_cvt_pk_bf16_f32 v43, v26, v27\n"
"v_mfma_f32_16x16x32_bf16 v[28:31], a[184:187], a[248:251], v[28:31]\n"
"buffer_load_dwordx4 v204, s[28:31], s52 offen offset:0 lds\n"
"v_mfma_f32_16x16x32_bf16 v[28:31], a[188:191], a[252:255], v[28:31]\n"
"s_waitcnt lgkmcnt(0)\n"
"s_add_u32 s52, s52, 8192\n"
"buffer_store_dwordx4 v[40:43], v207, s[32:35], s54 offen offset:2048\n"
"v_mfma_f32_16x16x32_bf16 v[0:3], a[128:131], v[128:131], 0\n"
"ds_read_b64_tr_b16 a[160:161], v197 offset:0\n"
"v_mfma_f32_16x16x32_bf16 v[0:3], a[132:135], v[132:135], v[0:3]\n"
"ds_read_b64_tr_b16 a[162:163], v197 offset:2048\n"
"v_mfma_f32_16x16x32_bf16 v[0:3], a[136:139], v[136:139], v[0:3]\n"
"ds_read_b64_tr_b16 a[164:165], v197 offset:4096\n"
"v_mfma_f32_16x16x32_bf16 v[0:3], a[140:143], v[140:143], v[0:3]\n"
"ds_read_b64_tr_b16 a[166:167], v197 offset:6144\n"
"v_mfma_f32_16x16x32_bf16 v[0:3], a[144:147], a[192:195], v[0:3]\n"
"ds_read_b64_tr_b16 a[168:169], v197 offset:8192\n"
"v_cvt_pk_bf16_f32 v46, v28, v29\n"
"v_mfma_f32_16x16x32_bf16 v[0:3], a[148:151], a[196:199], v[0:3]\n"
"ds_read_b64_tr_b16 a[170:171], v197 offset:10240\n"
"v_cvt_pk_bf16_f32 v47, v30, v31\n"
"v_mfma_f32_16x16x32_bf16 v[0:3], a[152:155], a[200:203], v[0:3]\n"
"ds_read_b64_tr_b16 a[172:173], v197 offset:12288\n"
"v_mfma_f32_16x16x32_bf16 v[0:3], a[156:159], a[204:207], v[0:3]\n"
"buffer_store_dwordx4 v[44:47], v208, s[32:35], s54 offen offset:2048\n"
"ds_read_b64_tr_b16 a[174:175], v197 offset:14336\n"
"v_mfma_f32_16x16x32_bf16 v[4:7], a[128:131], v[144:147], 0\n"
"s_add_u32 s54, s54, 4096\n"
"ds_read_b64_tr_b16 a[176:177], v203 offset:0\n"
"v_mfma_f32_16x16x32_bf16 v[4:7], a[132:135], v[148:151], v[4:7]\n"
"ds_read_b64_tr_b16 a[178:179], v203 offset:2048\n"
"v_mfma_f32_16x16x32_bf16 v[4:7], a[136:139], v[152:155], v[4:7]\n"
"ds_read_b64_tr_b16 a[180:181], v203 offset:4096\n"
"v_mfma_f32_16x16x32_bf16 v[4:7], a[140:143], v[156:159], v[4:7]\n"
"ds_read_b64_tr_b16 a[182:183], v203 offset:6144\n"
"v_mfma_f32_16x16x32_bf16 v[4:7], a[144:147], a[208:211], v[4:7]\n"
"ds_read_b64_tr_b16 a[184:185], v203 offset:8192\n"
"v_cvt_pk_bf16_f32 v32, v0, v1\n"
"v_mfma_f32_16x16x32_bf16 v[4:7], a[148:151], a[212:215], v[4:7]\n"
"ds_read_b64_tr_b16 a[186:187], v203 offset:10240\n"
"v_cvt_pk_bf16_f32 v33, v2, v3\n"
"v_mfma_f32_16x16x32_bf16 v[4:7], a[152:155], a[216:219], v[4:7]\n"
"ds_read_b64_tr_b16 a[188:189], v203 offset:12288\n"
"v_mfma_f32_16x16x32_bf16 v[4:7], a[156:159], a[220:223], v[4:7]\n"
"ds_read_b64_tr_b16 a[190:191], v203 offset:14336\n"
"v_mfma_f32_16x16x32_bf16 v[8:11], a[128:131], v[160:163], 0\n"
"buffer_load_dwordx4 v204, s[28:31], s52 offen offset:1024 lds\n"
"v_mfma_f32_16x16x32_bf16 v[8:11], a[132:135], v[164:167], v[8:11]\n"
"s_add_u32 s52, s52, 8192\n"
"v_mfma_f32_16x16x32_bf16 v[8:11], a[136:139], v[168:171], v[8:11]\n"
"s_nop 0\n"
"v_mfma_f32_16x16x32_bf16 v[8:11], a[140:143], v[172:175], v[8:11]\n"
"s_nop 0\n"
"v_mfma_f32_16x16x32_bf16 v[8:11], a[144:147], a[224:227], v[8:11]\n"
"v_cvt_pk_bf16_f32 v36, v4, v5\n"
"v_mfma_f32_16x16x32_bf16 v[8:11], a[148:151], a[228:231], v[8:11]\n"
"v_cvt_pk_bf16_f32 v37, v6, v7\n"
"v_mfma_f32_16x16x32_bf16 v[8:11], a[152:155], a[232:235], v[8:11]\n"
"buffer_load_dwordx4 v204, s[28:31], s52 offen offset:2048 lds\n"
"v_mfma_f32_16x16x32_bf16 v[8:11], a[156:159], a[236:239], v[8:11]\n"
"s_add_u32 s52, s52, 8192\n"
"v_mfma_f32_16x16x32_bf16 v[12:15], a[128:131], v[176:179], 0\n"
"s_nop 0\n"
"v_mfma_f32_16x16x32_bf16 v[12:15], a[132:135], v[180:183], v[12:15]\n"
"s_nop 0\n"
"v_mfma_f32_16x16x32_bf16 v[12:15], a[136:139], v[184:187], v[12:15]\n"
"buffer_load_dwordx4 v204, s[28:31], s52 offen offset:3072 lds\n"
"v_mfma_f32_16x16x32_bf16 v[12:15], a[140:143], v[188:191], v[12:15]\n"
"s_add_u32 s52, s52, 12288\n"
"v_mfma_f32_16x16x32_bf16 v[12:15], a[144:147], a[240:243], v[12:15]\n"
"v_cvt_pk_bf16_f32 v40, v8, v9\n"
"v_mfma_f32_16x16x32_bf16 v[12:15], a[148:151], a[244:247], v[12:15]\n"
"v_cvt_pk_bf16_f32 v41, v10, v11\n"
"v_mfma_f32_16x16x32_bf16 v[12:15], a[152:155], a[248:251], v[12:15]\n"
"s_waitcnt vmcnt(6) lgkmcnt(0)\n"
"s_barrier\n"
"v_mfma_f32_16x16x32_bf16 v[12:15], a[156:159], a[252:255], v[12:15]\n"
"s_nop 0\n"
"v_mfma_f32_16x16x32_bf16 v[16:19], a[160:163], v[128:131], 0\n"
"ds_read_b64_tr_b16 a[128:129], v192 offset:0\n"
"v_mfma_f32_16x16x32_bf16 v[16:19], a[164:167], v[132:135], v[16:19]\n"
"ds_read_b64_tr_b16 a[130:131], v192 offset:2048\n"
"v_mfma_f32_16x16x32_bf16 v[16:19], a[168:171], v[136:139], v[16:19]\n"
"ds_read_b64_tr_b16 a[132:133], v192 offset:4096\n"
"v_mfma_f32_16x16x32_bf16 v[16:19], a[172:175], v[140:143], v[16:19]\n"
"ds_read_b64_tr_b16 a[134:135], v192 offset:6144\n"
"v_mfma_f32_16x16x32_bf16 v[16:19], a[176:179], a[192:195], v[16:19]\n"
"ds_read_b64_tr_b16 a[136:137], v192 offset:8192\n"
"v_cvt_pk_bf16_f32 v44, v12, v13\n"
"v_mfma_f32_16x16x32_bf16 v[16:19], a[180:183], a[196:199], v[16:19]\n"
"ds_read_b64_tr_b16 a[138:139], v192 offset:10240\n"
"v_cvt_pk_bf16_f32 v45, v14, v15\n"
"v_mfma_f32_16x16x32_bf16 v[16:19], a[184:187], a[200:203], v[16:19]\n"
"ds_read_b64_tr_b16 a[140:141], v192 offset:12288\n"
"v_mfma_f32_16x16x32_bf16 v[16:19], a[188:191], a[204:207], v[16:19]\n"
"ds_read_b64_tr_b16 a[142:143], v192 offset:14336\n"
"v_mfma_f32_16x16x32_bf16 v[20:23], a[160:163], v[144:147], 0\n"
"ds_read_b64_tr_b16 a[144:145], v198 offset:0\n"
"v_mfma_f32_16x16x32_bf16 v[20:23], a[164:167], v[148:151], v[20:23]\n"
"ds_read_b64_tr_b16 a[146:147], v198 offset:2048\n"
"v_mfma_f32_16x16x32_bf16 v[20:23], a[168:171], v[152:155], v[20:23]\n"
"ds_read_b64_tr_b16 a[148:149], v198 offset:4096\n"
"v_mfma_f32_16x16x32_bf16 v[20:23], a[172:175], v[156:159], v[20:23]\n"
"ds_read_b64_tr_b16 a[150:151], v198 offset:6144\n"
"v_mfma_f32_16x16x32_bf16 v[20:23], a[176:179], a[208:211], v[20:23]\n"
"ds_read_b64_tr_b16 a[152:153], v198 offset:8192\n"
"v_cvt_pk_bf16_f32 v34, v16, v17\n"
"v_mfma_f32_16x16x32_bf16 v[20:23], a[180:183], a[212:215], v[20:23]\n"
"ds_read_b64_tr_b16 a[154:155], v198 offset:10240\n"
"v_cvt_pk_bf16_f32 v35, v18, v19\n"
"v_mfma_f32_16x16x32_bf16 v[20:23], a[184:187], a[216:219], v[20:23]\n"
"ds_read_b64_tr_b16 a[156:157], v198 offset:12288\n"
"v_mfma_f32_16x16x32_bf16 v[20:23], a[188:191], a[220:223], v[20:23]\n"
"s_add_u32 m0, s53, 36864\n"
"ds_read_b64_tr_b16 a[158:159], v198 offset:14336\n"
"buffer_store_dwordx4 v[32:35], v205, s[32:35], s54 offen offset:0\n"
"v_mfma_f32_16x16x32_bf16 v[24:27], a[160:163], v[160:163], 0\n"
"buffer_load_dwordx4 v204, s[28:31], s52 offen offset:0 lds\n"
"v_mfma_f32_16x16x32_bf16 v[24:27], a[164:167], v[164:167], v[24:27]\n"
"s_add_u32 s52, s52, 8192\n"
"v_mfma_f32_16x16x32_bf16 v[24:27], a[168:171], v[168:171], v[24:27]\n"
"s_nop 0\n"
"v_mfma_f32_16x16x32_bf16 v[24:27], a[172:175], v[172:175], v[24:27]\n"
"s_nop 0\n"
"v_mfma_f32_16x16x32_bf16 v[24:27], a[176:179], a[224:227], v[24:27]\n"
"v_cvt_pk_bf16_f32 v38, v20, v21\n"
"v_mfma_f32_16x16x32_bf16 v[24:27], a[180:183], a[228:231], v[24:27]\n"
"v_cvt_pk_bf16_f32 v39, v22, v23\n"
"v_mfma_f32_16x16x32_bf16 v[24:27], a[184:187], a[232:235], v[24:27]\n"
"buffer_load_dwordx4 v204, s[28:31], s52 offen offset:1024 lds\n"
"v_mfma_f32_16x16x32_bf16 v[24:27], a[188:191], a[236:239], v[24:27]\n"
"s_add_u32 s52, s52, 8192\n"
"buffer_store_dwordx4 v[36:39], v206, s[32:35], s54 offen offset:0\n"
"v_mfma_f32_16x16x32_bf16 v[28:31], a[160:163], v[176:179], 0\n"
"s_nop 0\n"
"v_mfma_f32_16x16x32_bf16 v[28:31], a[164:167], v[180:183], v[28:31]\n"
"s_nop 0\n"
"v_mfma_f32_16x16x32_bf16 v[28:31], a[168:171], v[184:187], v[28:31]\n"
"buffer_load_dwordx4 v204, s[28:31], s52 offen offset:2048 lds\n"
"v_mfma_f32_16x16x32_bf16 v[28:31], a[172:175], v[188:191], v[28:31]\n"
"s_add_u32 s52, s52, 8192\n"
"v_mfma_f32_16x16x32_bf16 v[28:31], a[176:179], a[240:243], v[28:31]\n"
"v_cvt_pk_bf16_f32 v42, v24, v25\n"
"v_mfma_f32_16x16x32_bf16 v[28:31], a[180:183], a[244:247], v[28:31]\n"
"v_cvt_pk_bf16_f32 v43, v26, v27\n"
"v_mfma_f32_16x16x32_bf16 v[28:31], a[184:187], a[248:251], v[28:31]\n"
"buffer_load_dwordx4 v204, s[28:31], s52 offen offset:3072 lds\n"
"v_mfma_f32_16x16x32_bf16 v[28:31], a[188:191], a[252:255], v[28:31]\n"
"s_waitcnt lgkmcnt(0)\n"
"s_sub_u32 s52, s52, 61312\n"
"buffer_store_dwordx4 v[40:43], v207, s[32:35], s54 offen offset:0\n"
"v_mfma_f32_16x16x32_bf16 v[0:3], a[128:131], v[128:131], 0\n"
"ds_read_b64_tr_b16 a[160:161], v195 offset:0\n"
"v_mfma_f32_16x16x32_bf16 v[0:3], a[132:135], v[132:135], v[0:3]\n"
"ds_read_b64_tr_b16 a[162:163], v195 offset:2048\n"
"v_mfma_f32_16x16x32_bf16 v[0:3], a[136:139], v[136:139], v[0:3]\n"
"ds_read_b64_tr_b16 a[164:165], v195 offset:4096\n"
"v_mfma_f32_16x16x32_bf16 v[0:3], a[140:143], v[140:143], v[0:3]\n"
"ds_read_b64_tr_b16 a[166:167], v195 offset:6144\n"
"v_mfma_f32_16x16x32_bf16 v[0:3], a[144:147], a[192:195], v[0:3]\n"
"ds_read_b64_tr_b16 a[168:169], v195 offset:8192\n"
"v_cvt_pk_bf16_f32 v46, v28, v29\n"
"v_mfma_f32_16x16x32_bf16 v[0:3], a[148:151], a[196:199], v[0:3]\n"
"ds_read_b64_tr_b16 a[170:171], v195 offset:10240\n"
"v_cvt_pk_bf16_f32 v47, v30, v31\n"
"v_mfma_f32_16x16x32_bf16 v[0:3], a[152:155], a[200:203], v[0:3]\n"
"ds_read_b64_tr_b16 a[172:173], v195 offset:12288\n"
"v_mfma_f32_16x16x32_bf16 v[0:3], a[156:159], a[204:207], v[0:3]\n"
"buffer_store_dwordx4 v[44:47], v208, s[32:35], s54 offen offset:0\n"
"ds_read_b64_tr_b16 a[174:175], v195 offset:14336\n"
"v_mfma_f32_16x16x32_bf16 v[4:7], a[128:131], v[144:147], 0\n"
"ds_read_b64_tr_b16 a[176:177], v201 offset:0\n"
"v_mfma_f32_16x16x32_bf16 v[4:7], a[132:135], v[148:151], v[4:7]\n"
"ds_read_b64_tr_b16 a[178:179], v201 offset:2048\n"
"v_mfma_f32_16x16x32_bf16 v[4:7], a[136:139], v[152:155], v[4:7]\n"
"ds_read_b64_tr_b16 a[180:181], v201 offset:4096\n"
"v_mfma_f32_16x16x32_bf16 v[4:7], a[140:143], v[156:159], v[4:7]\n"
"ds_read_b64_tr_b16 a[182:183], v201 offset:6144\n"
"v_mfma_f32_16x16x32_bf16 v[4:7], a[144:147], a[208:211], v[4:7]\n"
"ds_read_b64_tr_b16 a[184:185], v201 offset:8192\n"
"v_cvt_pk_bf16_f32 v32, v0, v1\n"
"v_mfma_f32_16x16x32_bf16 v[4:7], a[148:151], a[212:215], v[4:7]\n"
"ds_read_b64_tr_b16 a[186:187], v201 offset:10240\n"
"v_cvt_pk_bf16_f32 v33, v2, v3\n"
"v_mfma_f32_16x16x32_bf16 v[4:7], a[152:155], a[216:219], v[4:7]\n"
"ds_read_b64_tr_b16 a[188:189], v201 offset:12288\n"
"v_mfma_f32_16x16x32_bf16 v[4:7], a[156:159], a[220:223], v[4:7]\n"
"s_add_u32 m0, s53, 65536\n"
"ds_read_b64_tr_b16 a[190:191], v201 offset:14336\n"
"v_mfma_f32_16x16x32_bf16 v[8:11], a[128:131], v[160:163], 0\n"
"buffer_load_dwordx4 v204, s[28:31], s52 offen offset:0 lds\n"
"v_mfma_f32_16x16x32_bf16 v[8:11], a[132:135], v[164:167], v[8:11]\n"
"s_add_u32 s52, s52, 8192\n"
"v_mfma_f32_16x16x32_bf16 v[8:11], a[136:139], v[168:171], v[8:11]\n"
"s_nop 0\n"
"v_mfma_f32_16x16x32_bf16 v[8:11], a[140:143], v[172:175], v[8:11]\n"
"s_nop 0\n"
"v_mfma_f32_16x16x32_bf16 v[8:11], a[144:147], a[224:227], v[8:11]\n"
"v_cvt_pk_bf16_f32 v36, v4, v5\n"
"v_mfma_f32_16x16x32_bf16 v[8:11], a[148:151], a[228:231], v[8:11]\n"
"v_cvt_pk_bf16_f32 v37, v6, v7\n"
"v_mfma_f32_16x16x32_bf16 v[8:11], a[152:155], a[232:235], v[8:11]\n"
"buffer_load_dwordx4 v204, s[28:31], s52 offen offset:1024 lds\n"
"v_mfma_f32_16x16x32_bf16 v[8:11], a[156:159], a[236:239], v[8:11]\n"
"s_add_u32 s52, s52, 8192\n"
"v_mfma_f32_16x16x32_bf16 v[12:15], a[128:131], v[176:179], 0\n"
"s_nop 0\n"
"v_mfma_f32_16x16x32_bf16 v[12:15], a[132:135], v[180:183], v[12:15]\n"
"s_nop 0\n"
"v_mfma_f32_16x16x32_bf16 v[12:15], a[136:139], v[184:187], v[12:15]\n"
"buffer_load_dwordx4 v204, s[28:31], s52 offen offset:2048 lds\n"
"v_mfma_f32_16x16x32_bf16 v[12:15], a[140:143], v[188:191], v[12:15]\n"
"s_add_u32 s52, s52, 8192\n"
"v_mfma_f32_16x16x32_bf16 v[12:15], a[144:147], a[240:243], v[12:15]\n"
"v_cvt_pk_bf16_f32 v40, v8, v9\n"
"v_mfma_f32_16x16x32_bf16 v[12:15], a[148:151], a[244:247], v[12:15]\n"
"v_cvt_pk_bf16_f32 v41, v10, v11\n"
"v_mfma_f32_16x16x32_bf16 v[12:15], a[152:155], a[248:251], v[12:15]\n"
"s_waitcnt vmcnt(5) lgkmcnt(0)\n"
"s_barrier\n"
"v_mfma_f32_16x16x32_bf16 v[12:15], a[156:159], a[252:255], v[12:15]\n"
"s_nop 0\n"
"v_mfma_f32_16x16x32_bf16 v[16:19], a[160:163], v[128:131], 0\n"
"ds_read_b64_tr_b16 a[128:129], v192 offset:32768\n"
"v_mfma_f32_16x16x32_bf16 v[16:19], a[164:167], v[132:135], v[16:19]\n"
"ds_read_b64_tr_b16 a[130:131], v192 offset:34816\n"
"v_mfma_f32_16x16x32_bf16 v[16:19], a[168:171], v[136:139], v[16:19]\n"
"ds_read_b64_tr_b16 a[132:133], v192 offset:36864\n"
"v_mfma_f32_16x16x32_bf16 v[16:19], a[172:175], v[140:143], v[16:19]\n"
"ds_read_b64_tr_b16 a[134:135], v192 offset:38912\n"
"v_mfma_f32_16x16x32_bf16 v[16:19], a[176:179], a[192:195], v[16:19]\n"
"ds_read_b64_tr_b16 a[136:137], v192 offset:40960\n"
"v_cvt_pk_bf16_f32 v44, v12, v13\n"
"v_mfma_f32_16x16x32_bf16 v[16:19], a[180:183], a[196:199], v[16:19]\n"
"ds_read_b64_tr_b16 a[138:139], v192 offset:43008\n"
"v_cvt_pk_bf16_f32 v45, v14, v15\n"
"v_mfma_f32_16x16x32_bf16 v[16:19], a[184:187], a[200:203], v[16:19]\n"
"ds_read_b64_tr_b16 a[140:141], v192 offset:45056\n"
"v_mfma_f32_16x16x32_bf16 v[16:19], a[188:191], a[204:207], v[16:19]\n"
"ds_read_b64_tr_b16 a[142:143], v192 offset:47104\n"
"v_mfma_f32_16x16x32_bf16 v[20:23], a[160:163], v[144:147], 0\n"
"ds_read_b64_tr_b16 a[144:145], v198 offset:32768\n"
"v_mfma_f32_16x16x32_bf16 v[20:23], a[164:167], v[148:151], v[20:23]\n"
"ds_read_b64_tr_b16 a[146:147], v198 offset:34816\n"
"v_mfma_f32_16x16x32_bf16 v[20:23], a[168:171], v[152:155], v[20:23]\n"
"ds_read_b64_tr_b16 a[148:149], v198 offset:36864\n"
"v_mfma_f32_16x16x32_bf16 v[20:23], a[172:175], v[156:159], v[20:23]\n"
"ds_read_b64_tr_b16 a[150:151], v198 offset:38912\n"
"v_mfma_f32_16x16x32_bf16 v[20:23], a[176:179], a[208:211], v[20:23]\n"
"ds_read_b64_tr_b16 a[152:153], v198 offset:40960\n"
"v_cvt_pk_bf16_f32 v34, v16, v17\n"
"v_mfma_f32_16x16x32_bf16 v[20:23], a[180:183], a[212:215], v[20:23]\n"
"ds_read_b64_tr_b16 a[154:155], v198 offset:43008\n"
"v_cvt_pk_bf16_f32 v35, v18, v19\n"
"v_mfma_f32_16x16x32_bf16 v[20:23], a[184:187], a[216:219], v[20:23]\n"
"ds_read_b64_tr_b16 a[156:157], v198 offset:45056\n"
"v_mfma_f32_16x16x32_bf16 v[20:23], a[188:191], a[220:223], v[20:23]\n"
"ds_read_b64_tr_b16 a[158:159], v198 offset:47104\n"
"buffer_store_dwordx4 v[32:35], v205, s[32:35], s54 offen offset:2048\n"
"v_mfma_f32_16x16x32_bf16 v[24:27], a[160:163], v[160:163], 0\n"
"buffer_load_dwordx4 v204, s[28:31], s52 offen offset:3072 lds\n"
"v_mfma_f32_16x16x32_bf16 v[24:27], a[164:167], v[164:167], v[24:27]\n"
"s_add_u32 s52, s52, 12288\n"
"v_mfma_f32_16x16x32_bf16 v[24:27], a[168:171], v[168:171], v[24:27]\n"
"s_nop 0\n"
"v_mfma_f32_16x16x32_bf16 v[24:27], a[172:175], v[172:175], v[24:27]\n"
"s_nop 0\n"
"v_mfma_f32_16x16x32_bf16 v[24:27], a[176:179], a[224:227], v[24:27]\n"
"v_cvt_pk_bf16_f32 v38, v20, v21\n"
"v_mfma_f32_16x16x32_bf16 v[24:27], a[180:183], a[228:231], v[24:27]\n"
"s_add_u32 m0, s53, 69632\n"
"v_cvt_pk_bf16_f32 v39, v22, v23\n"
"v_mfma_f32_16x16x32_bf16 v[24:27], a[184:187], a[232:235], v[24:27]\n"
"buffer_load_dwordx4 v204, s[28:31], s52 offen offset:0 lds\n"
"v_mfma_f32_16x16x32_bf16 v[24:27], a[188:191], a[236:239], v[24:27]\n"
"s_add_u32 s52, s52, 8192\n"
"buffer_store_dwordx4 v[36:39], v206, s[32:35], s54 offen offset:2048\n"
"v_mfma_f32_16x16x32_bf16 v[28:31], a[160:163], v[176:179], 0\n"
"s_nop 0\n"
"v_mfma_f32_16x16x32_bf16 v[28:31], a[164:167], v[180:183], v[28:31]\n"
"s_nop 0\n"
"v_mfma_f32_16x16x32_bf16 v[28:31], a[168:171], v[184:187], v[28:31]\n"
"buffer_load_dwordx4 v204, s[28:31], s52 offen offset:1024 lds\n"
"v_mfma_f32_16x16x32_bf16 v[28:31], a[172:175], v[188:191], v[28:31]\n"
"s_add_u32 s52, s52, 8192\n"
"v_mfma_f32_16x16x32_bf16 v[28:31], a[176:179], a[240:243], v[28:31]\n"
"v_cvt_pk_bf16_f32 v42, v24, v25\n"
"v_mfma_f32_16x16x32_bf16 v[28:31], a[180:183], a[244:247], v[28:31]\n"
"v_cvt_pk_bf16_f32 v43, v26, v27\n"
"v_mfma_f32_16x16x32_bf16 v[28:31], a[184:187], a[248:251], v[28:31]\n"
"buffer_load_dwordx4 v204, s[28:31], s52 offen offset:2048 lds\n"
"v_mfma_f32_16x16x32_bf16 v[28:31], a[188:191], a[252:255], v[28:31]\n"
"s_waitcnt lgkmcnt(0)\n"
"s_add_u32 s52, s52, 8192\n"
"buffer_store_dwordx4 v[40:43], v207, s[32:35], s54 offen offset:2048\n"
"v_mfma_f32_16x16x32_bf16 v[0:3], a[128:131], v[128:131], 0\n"
"ds_read_b64_tr_b16 a[160:161], v195 offset:32768\n"
"v_mfma_f32_16x16x32_bf16 v[0:3], a[132:135], v[132:135], v[0:3]\n"
"ds_read_b64_tr_b16 a[162:163], v195 offset:34816\n"
"v_mfma_f32_16x16x32_bf16 v[0:3], a[136:139], v[136:139], v[0:3]\n"
"ds_read_b64_tr_b16 a[164:165], v195 offset:36864\n"
"v_mfma_f32_16x16x32_bf16 v[0:3], a[140:143], v[140:143], v[0:3]\n"
"ds_read_b64_tr_b16 a[166:167], v195 offset:38912\n"
"v_mfma_f32_16x16x32_bf16 v[0:3], a[144:147], a[192:195], v[0:3]\n"
"ds_read_b64_tr_b16 a[168:169], v195 offset:40960\n"
"v_cvt_pk_bf16_f32 v46, v28, v29\n"
"v_mfma_f32_16x16x32_bf16 v[0:3], a[148:151], a[196:199], v[0:3]\n"
"ds_read_b64_tr_b16 a[170:171], v195 offset:43008\n"
"v_cvt_pk_bf16_f32 v47, v30, v31\n"
"v_mfma_f32_16x16x32_bf16 v[0:3], a[152:155], a[200:203], v[0:3]\n"
"ds_read_b64_tr_b16 a[172:173], v195 offset:45056\n"
"v_mfma_f32_16x16x32_bf16 v[0:3], a[156:159], a[204:207], v[0:3]\n"
"buffer_store_dwordx4 v[44:47], v208, s[32:35], s54 offen offset:2048\n"
"ds_read_b64_tr_b16 a[174:175], v195 offset:47104\n"
"v_mfma_f32_16x16x32_bf16 v[4:7], a[128:131], v[144:147], 0\n"
"s_add_u32 s54, s54, 4096\n"
"ds_read_b64_tr_b16 a[176:177], v201 offset:32768\n"
"v_mfma_f32_16x16x32_bf16 v[4:7], a[132:135], v[148:151], v[4:7]\n"
"ds_read_b64_tr_b16 a[178:179], v201 offset:34816\n"
"v_mfma_f32_16x16x32_bf16 v[4:7], a[136:139], v[152:155], v[4:7]\n"
"ds_read_b64_tr_b16 a[180:181], v201 offset:36864\n"
"v_mfma_f32_16x16x32_bf16 v[4:7], a[140:143], v[156:159], v[4:7]\n"
"ds_read_b64_tr_b16 a[182:183], v201 offset:38912\n"
"v_mfma_f32_16x16x32_bf16 v[4:7], a[144:147], a[208:211], v[4:7]\n"
"ds_read_b64_tr_b16 a[184:185], v201 offset:40960\n"
"v_cvt_pk_bf16_f32 v32, v0, v1\n"
"v_mfma_f32_16x16x32_bf16 v[4:7], a[148:151], a[212:215], v[4:7]\n"
"ds_read_b64_tr_b16 a[186:187], v201 offset:43008\n"
"v_cvt_pk_bf16_f32 v33, v2, v3\n"
"v_mfma_f32_16x16x32_bf16 v[4:7], a[152:155], a[216:219], v[4:7]\n"
"ds_read_b64_tr_b16 a[188:189], v201 offset:45056\n"
"v_mfma_f32_16x16x32_bf16 v[4:7], a[156:159], a[220:223], v[4:7]\n"
"ds_read_b64_tr_b16 a[190:191], v201 offset:47104\n"
"v_mfma_f32_16x16x32_bf16 v[8:11], a[128:131], v[160:163], 0\n"
"buffer_load_dwordx4 v204, s[28:31], s52 offen offset:3072 lds\n"
"v_mfma_f32_16x16x32_bf16 v[8:11], a[132:135], v[164:167], v[8:11]\n"
"s_sub_u32 s52, s52, 61312\n"
"v_mfma_f32_16x16x32_bf16 v[8:11], a[136:139], v[168:171], v[8:11]\n"
"s_nop 0\n"
"v_mfma_f32_16x16x32_bf16 v[8:11], a[140:143], v[172:175], v[8:11]\n"
"s_nop 0\n"
"v_mfma_f32_16x16x32_bf16 v[8:11], a[144:147], a[224:227], v[8:11]\n"
"v_cvt_pk_bf16_f32 v36, v4, v5\n"
"v_mfma_f32_16x16x32_bf16 v[8:11], a[148:151], a[228:231], v[8:11]\n"
"s_add_u32 m0, s53, 98304\n"
"v_cvt_pk_bf16_f32 v37, v6, v7\n"
"v_mfma_f32_16x16x32_bf16 v[8:11], a[152:155], a[232:235], v[8:11]\n"
"buffer_load_dwordx4 v204, s[28:31], s52 offen offset:0 lds\n"
"v_mfma_f32_16x16x32_bf16 v[8:11], a[156:159], a[236:239], v[8:11]\n"
"s_add_u32 s52, s52, 8192\n"
"v_mfma_f32_16x16x32_bf16 v[12:15], a[128:131], v[176:179], 0\n"
"s_nop 0\n"
"v_mfma_f32_16x16x32_bf16 v[12:15], a[132:135], v[180:183], v[12:15]\n"
"s_nop 0\n"
"v_mfma_f32_16x16x32_bf16 v[12:15], a[136:139], v[184:187], v[12:15]\n"
"buffer_load_dwordx4 v204, s[28:31], s52 offen offset:1024 lds\n"
"v_mfma_f32_16x16x32_bf16 v[12:15], a[140:143], v[188:191], v[12:15]\n"
"s_add_u32 s52, s52, 8192\n"
"v_mfma_f32_16x16x32_bf16 v[12:15], a[144:147], a[240:243], v[12:15]\n"
"v_cvt_pk_bf16_f32 v40, v8, v9\n"
"v_mfma_f32_16x16x32_bf16 v[12:15], a[148:151], a[244:247], v[12:15]\n"
"v_cvt_pk_bf16_f32 v41, v10, v11\n"
"v_mfma_f32_16x16x32_bf16 v[12:15], a[152:155], a[248:251], v[12:15]\n"
"s_waitcnt vmcnt(2) lgkmcnt(0)\n"
"s_barrier\n"
"v_mfma_f32_16x16x32_bf16 v[12:15], a[156:159], a[252:255], v[12:15]\n"
"s_nop 0\n"
"v_mfma_f32_16x16x32_bf16 v[16:19], a[160:163], v[128:131], 0\n"
"ds_read_b64_tr_b16 a[128:129], v193 offset:0\n"
"v_mfma_f32_16x16x32_bf16 v[16:19], a[164:167], v[132:135], v[16:19]\n"
"ds_read_b64_tr_b16 a[130:131], v193 offset:2048\n"
"v_mfma_f32_16x16x32_bf16 v[16:19], a[168:171], v[136:139], v[16:19]\n"
"ds_read_b64_tr_b16 a[132:133], v193 offset:4096\n"
"v_mfma_f32_16x16x32_bf16 v[16:19], a[172:175], v[140:143], v[16:19]\n"
"ds_read_b64_tr_b16 a[134:135], v193 offset:6144\n"
"v_mfma_f32_16x16x32_bf16 v[16:19], a[176:179], a[192:195], v[16:19]\n"
"ds_read_b64_tr_b16 a[136:137], v193 offset:8192\n"
"v_cvt_pk_bf16_f32 v44, v12, v13\n"
"v_mfma_f32_16x16x32_bf16 v[16:19], a[180:183], a[196:199], v[16:19]\n"
"ds_read_b64_tr_b16 a[138:139], v193 offset:10240\n"
"v_cvt_pk_bf16_f32 v45, v14, v15\n"
"v_mfma_f32_16x16x32_bf16 v[16:19], a[184:187], a[200:203], v[16:19]\n"
"ds_read_b64_tr_b16 a[140:141], v193 offset:12288\n"
"v_mfma_f32_16x16x32_bf16 v[16:19], a[188:191], a[204:207], v[16:19]\n"
"ds_read_b64_tr_b16 a[142:143], v193 offset:14336\n"
"v_mfma_f32_16x16x32_bf16 v[20:23], a[160:163], v[144:147], 0\n"
"ds_read_b64_tr_b16 a[144:145], v199 offset:0\n"
"v_mfma_f32_16x16x32_bf16 v[20:23], a[164:167], v[148:151], v[20:23]\n"
"ds_read_b64_tr_b16 a[146:147], v199 offset:2048\n"
"v_mfma_f32_16x16x32_bf16 v[20:23], a[168:171], v[152:155], v[20:23]\n"
"ds_read_b64_tr_b16 a[148:149], v199 offset:4096\n"
"v_mfma_f32_16x16x32_bf16 v[20:23], a[172:175], v[156:159], v[20:23]\n"
"ds_read_b64_tr_b16 a[150:151], v199 offset:6144\n"
"v_mfma_f32_16x16x32_bf16 v[20:23], a[176:179], a[208:211], v[20:23]\n"
"ds_read_b64_tr_b16 a[152:153], v199 offset:8192\n"
"v_cvt_pk_bf16_f32 v34, v16, v17\n"
"v_mfma_f32_16x16x32_bf16 v[20:23], a[180:183], a[212:215], v[20:23]\n"
"ds_read_b64_tr_b16 a[154:155], v199 offset:10240\n"
"v_cvt_pk_bf16_f32 v35, v18, v19\n"
"v_mfma_f32_16x16x32_bf16 v[20:23], a[184:187], a[216:219], v[20:23]\n"
"ds_read_b64_tr_b16 a[156:157], v199 offset:12288\n"
"v_mfma_f32_16x16x32_bf16 v[20:23], a[188:191], a[220:223], v[20:23]\n"
"ds_read_b64_tr_b16 a[158:159], v199 offset:14336\n"
"buffer_store_dwordx4 v[32:35], v205, s[32:35], s54 offen offset:0\n"
"v_mfma_f32_16x16x32_bf16 v[24:27], a[160:163], v[160:163], 0\n"
"buffer_load_dwordx4 v204, s[28:31], s52 offen offset:2048 lds\n"
"v_mfma_f32_16x16x32_bf16 v[24:27], a[164:167], v[164:167], v[24:27]\n"
"s_add_u32 s52, s52, 8192\n"
"v_mfma_f32_16x16x32_bf16 v[24:27], a[168:171], v[168:171], v[24:27]\n"
"s_nop 0\n"
"v_mfma_f32_16x16x32_bf16 v[24:27], a[172:175], v[172:175], v[24:27]\n"
"s_nop 0\n"
"v_mfma_f32_16x16x32_bf16 v[24:27], a[176:179], a[224:227], v[24:27]\n"
"v_cvt_pk_bf16_f32 v38, v20, v21\n"
"v_mfma_f32_16x16x32_bf16 v[24:27], a[180:183], a[228:231], v[24:27]\n"
"v_cvt_pk_bf16_f32 v39, v22, v23\n"
"v_mfma_f32_16x16x32_bf16 v[24:27], a[184:187], a[232:235], v[24:27]\n"
"buffer_load_dwordx4 v204, s[28:31], s52 offen offset:3072 lds\n"
"v_mfma_f32_16x16x32_bf16 v[24:27], a[188:191], a[236:239], v[24:27]\n"
"s_add_u32 s52, s52, 12288\n"
"buffer_store_dwordx4 v[36:39], v206, s[32:35], s54 offen offset:0\n"
"v_mfma_f32_16x16x32_bf16 v[28:31], a[160:163], v[176:179], 0\n"
"s_nop 0\n"
"v_mfma_f32_16x16x32_bf16 v[28:31], a[164:167], v[180:183], v[28:31]\n"
"s_add_u32 m0, s53, 102400\n"
"v_mfma_f32_16x16x32_bf16 v[28:31], a[168:171], v[184:187], v[28:31]\n"
"buffer_load_dwordx4 v204, s[28:31], s52 offen offset:0 lds\n"
"v_mfma_f32_16x16x32_bf16 v[28:31], a[172:175], v[188:191], v[28:31]\n"
"s_add_u32 s52, s52, 8192\n"
"v_mfma_f32_16x16x32_bf16 v[28:31], a[176:179], a[240:243], v[28:31]\n"
"v_cvt_pk_bf16_f32 v42, v24, v25\n"
"v_mfma_f32_16x16x32_bf16 v[28:31], a[180:183], a[244:247], v[28:31]\n"
"v_cvt_pk_bf16_f32 v43, v26, v27\n"
"v_mfma_f32_16x16x32_bf16 v[28:31], a[184:187], a[248:251], v[28:31]\n"
"buffer_load_dwordx4 v204, s[28:31], s52 offen offset:1024 lds\n"
"v_mfma_f32_16x16x32_bf16 v[28:31], a[188:191], a[252:255], v[28:31]\n"
"s_waitcnt lgkmcnt(0)\n"
"s_add_u32 s52, s52, 8192\n"
"buffer_store_dwordx4 v[40:43], v207, s[32:35], s54 offen offset:0\n"
"v_mfma_f32_16x16x32_bf16 v[0:3], a[128:131], v[128:131], 0\n"
"ds_read_b64_tr_b16 a[160:161], v196 offset:0\n"
"v_mfma_f32_16x16x32_bf16 v[0:3], a[132:135], v[132:135], v[0:3]\n"
"ds_read_b64_tr_b16 a[162:163], v196 offset:2048\n"
"v_mfma_f32_16x16x32_bf16 v[0:3], a[136:139], v[136:139], v[0:3]\n"
"ds_read_b64_tr_b16 a[164:165], v196 offset:4096\n"
"v_mfma_f32_16x16x32_bf16 v[0:3], a[140:143], v[140:143], v[0:3]\n"
"ds_read_b64_tr_b16 a[166:167], v196 offset:6144\n"
"v_mfma_f32_16x16x32_bf16 v[0:3], a[144:147], a[192:195], v[0:3]\n"
"ds_read_b64_tr_b16 a[168:169], v196 offset:8192\n"
"v_cvt_pk_bf16_f32 v46, v28, v29\n"
"v_mfma_f32_16x16x32_bf16 v[0:3], a[148:151], a[196:199], v[0:3]\n"
"ds_read_b64_tr_b16 a[170:171], v196 offset:10240\n"
"v_cvt_pk_bf16_f32 v47, v30, v31\n"
"v_mfma_f32_16x16x32_bf16 v[0:3], a[152:155], a[200:203], v[0:3]\n"
"ds_read_b64_tr_b16 a[172:173], v196 offset:12288\n"
"v_mfma_f32_16x16x32_bf16 v[0:3], a[156:159], a[204:207], v[0:3]\n"
"buffer_store_dwordx4 v[44:47], v208, s[32:35], s54 offen offset:0\n"
"ds_read_b64_tr_b16 a[174:175], v196 offset:14336\n"
"v_mfma_f32_16x16x32_bf16 v[4:7], a[128:131], v[144:147], 0\n"
"ds_read_b64_tr_b16 a[176:177], v202 offset:0\n"
"v_mfma_f32_16x16x32_bf16 v[4:7], a[132:135], v[148:151], v[4:7]\n"
"ds_read_b64_tr_b16 a[178:179], v202 offset:2048\n"
"v_mfma_f32_16x16x32_bf16 v[4:7], a[136:139], v[152:155], v[4:7]\n"
"ds_read_b64_tr_b16 a[180:181], v202 offset:4096\n"
"v_mfma_f32_16x16x32_bf16 v[4:7], a[140:143], v[156:159], v[4:7]\n"
"ds_read_b64_tr_b16 a[182:183], v202 offset:6144\n"
"v_mfma_f32_16x16x32_bf16 v[4:7], a[144:147], a[208:211], v[4:7]\n"
"ds_read_b64_tr_b16 a[184:185], v202 offset:8192\n"
"v_cvt_pk_bf16_f32 v32, v0, v1\n"
"v_mfma_f32_16x16x32_bf16 v[4:7], a[148:151], a[212:215], v[4:7]\n"
"ds_read_b64_tr_b16 a[186:187], v202 offset:10240\n"
"v_cvt_pk_bf16_f32 v33, v2, v3\n"
"v_mfma_f32_16x16x32_bf16 v[4:7], a[152:155], a[216:219], v[4:7]\n"
"ds_read_b64_tr_b16 a[188:189], v202 offset:12288\n"
"v_mfma_f32_16x16x32_bf16 v[4:7], a[156:159], a[220:223], v[4:7]\n"
"ds_read_b64_tr_b16 a[190:191], v202 offset:14336\n"
"v_mfma_f32_16x16x32_bf16 v[8:11], a[128:131], v[160:163], 0\n"
"buffer_load_dwordx4 v204, s[28:31], s52 offen offset:2048 lds\n"
"v_mfma_f32_16x16x32_bf16 v[8:11], a[132:135], v[164:167], v[8:11]\n"
"s_add_u32 s52, s52, 8192\n"
"v_mfma_f32_16x16x32_bf16 v[8:11], a[136:139], v[168:171], v[8:11]\n"
"s_nop 0\n"
"v_mfma_f32_16x16x32_bf16 v[8:11], a[140:143], v[172:175], v[8:11]\n"
"s_nop 0\n"
"v_mfma_f32_16x16x32_bf16 v[8:11], a[144:147], a[224:227], v[8:11]\n"
"v_cvt_pk_bf16_f32 v36, v4, v5\n"
"v_mfma_f32_16x16x32_bf16 v[8:11], a[148:151], a[228:231], v[8:11]\n"
"v_cvt_pk_bf16_f32 v37, v6, v7\n"
"v_mfma_f32_16x16x32_bf16 v[8:11], a[152:155], a[232:235], v[8:11]\n"
"buffer_load_dwordx4 v204, s[28:31], s52 offen offset:3072 lds\n"
"v_mfma_f32_16x16x32_bf16 v[8:11], a[156:159], a[236:239], v[8:11]\n"
"s_sub_u32 s52, s52, 61312\n"
"v_mfma_f32_16x16x32_bf16 v[12:15], a[128:131], v[176:179], 0\n"
"s_nop 0\n"
"v_mfma_f32_16x16x32_bf16 v[12:15], a[132:135], v[180:183], v[12:15]\n"
"s_nop 0\n"
"v_mfma_f32_16x16x32_bf16 v[12:15], a[136:139], v[184:187], v[12:15]\n"
"s_nop 0\n"
"v_mfma_f32_16x16x32_bf16 v[12:15], a[140:143], v[188:191], v[12:15]\n"
"s_nop 0\n"
"v_mfma_f32_16x16x32_bf16 v[12:15], a[144:147], a[240:243], v[12:15]\n"
"v_cvt_pk_bf16_f32 v40, v8, v9\n"
"v_mfma_f32_16x16x32_bf16 v[12:15], a[148:151], a[244:247], v[12:15]\n"
"v_cvt_pk_bf16_f32 v41, v10, v11\n"
"v_mfma_f32_16x16x32_bf16 v[12:15], a[152:155], a[248:251], v[12:15]\n"
"s_waitcnt vmcnt(0) lgkmcnt(0)\n"
"s_barrier\n"
"v_mfma_f32_16x16x32_bf16 v[12:15], a[156:159], a[252:255], v[12:15]\n"
"s_nop 0\n"
"v_mfma_f32_16x16x32_bf16 v[16:19], a[160:163], v[128:131], 0\n"
"ds_read_b64_tr_b16 a[128:129], v193 offset:32768\n"
"v_mfma_f32_16x16x32_bf16 v[16:19], a[164:167], v[132:135], v[16:19]\n"
"ds_read_b64_tr_b16 a[130:131], v193 offset:34816\n"
"v_mfma_f32_16x16x32_bf16 v[16:19], a[168:171], v[136:139], v[16:19]\n"
"ds_read_b64_tr_b16 a[132:133], v193 offset:36864\n"
"v_mfma_f32_16x16x32_bf16 v[16:19], a[172:175], v[140:143], v[16:19]\n"
"ds_read_b64_tr_b16 a[134:135], v193 offset:38912\n"
"v_mfma_f32_16x16x32_bf16 v[16:19], a[176:179], a[192:195], v[16:19]\n"
"ds_read_b64_tr_b16 a[136:137], v193 offset:40960\n"
"v_cvt_pk_bf16_f32 v44, v12, v13\n"
"v_mfma_f32_16x16x32_bf16 v[16:19], a[180:183], a[196:199], v[16:19]\n"
"ds_read_b64_tr_b16 a[138:139], v193 offset:43008\n"
"v_cvt_pk_bf16_f32 v45, v14, v15\n"
"v_mfma_f32_16x16x32_bf16 v[16:19], a[184:187], a[200:203], v[16:19]\n"
"ds_read_b64_tr_b16 a[140:141], v193 offset:45056\n"
"v_mfma_f32_16x16x32_bf16 v[16:19], a[188:191], a[204:207], v[16:19]\n"
"ds_read_b64_tr_b16 a[142:143], v193 offset:47104\n"
"v_mfma_f32_16x16x32_bf16 v[20:23], a[160:163], v[144:147], 0\n"
"ds_read_b64_tr_b16 a[144:145], v199 offset:32768\n"
"v_mfma_f32_16x16x32_bf16 v[20:23], a[164:167], v[148:151], v[20:23]\n"
"ds_read_b64_tr_b16 a[146:147], v199 offset:34816\n"
"v_mfma_f32_16x16x32_bf16 v[20:23], a[168:171], v[152:155], v[20:23]\n"
"ds_read_b64_tr_b16 a[148:149], v199 offset:36864\n"
"v_mfma_f32_16x16x32_bf16 v[20:23], a[172:175], v[156:159], v[20:23]\n"
"ds_read_b64_tr_b16 a[150:151], v199 offset:38912\n"
"v_mfma_f32_16x16x32_bf16 v[20:23], a[176:179], a[208:211], v[20:23]\n"
"ds_read_b64_tr_b16 a[152:153], v199 offset:40960\n"
"v_cvt_pk_bf16_f32 v34, v16, v17\n"
"v_mfma_f32_16x16x32_bf16 v[20:23], a[180:183], a[212:215], v[20:23]\n"
"ds_read_b64_tr_b16 a[154:155], v199 offset:43008\n"
"v_cvt_pk_bf16_f32 v35, v18, v19\n"
"v_mfma_f32_16x16x32_bf16 v[20:23], a[184:187], a[216:219], v[20:23]\n"
"ds_read_b64_tr_b16 a[156:157], v199 offset:45056\n"
"v_mfma_f32_16x16x32_bf16 v[20:23], a[188:191], a[220:223], v[20:23]\n"
"ds_read_b64_tr_b16 a[158:159], v199 offset:47104\n"
"buffer_store_dwordx4 v[32:35], v205, s[32:35], s54 offen offset:2048\n"
"v_mfma_f32_16x16x32_bf16 v[24:27], a[160:163], v[160:163], 0\n"
"s_nop 0\n"
"v_mfma_f32_16x16x32_bf16 v[24:27], a[164:167], v[164:167], v[24:27]\n"
"s_nop 0\n"
"v_mfma_f32_16x16x32_bf16 v[24:27], a[168:171], v[168:171], v[24:27]\n"
"s_nop 0\n"
"v_mfma_f32_16x16x32_bf16 v[24:27], a[172:175], v[172:175], v[24:27]\n"
"s_nop 0\n"
"v_mfma_f32_16x16x32_bf16 v[24:27], a[176:179], a[224:227], v[24:27]\n"
"v_cvt_pk_bf16_f32 v38, v20, v21\n"
"v_mfma_f32_16x16x32_bf16 v[24:27], a[180:183], a[228:231], v[24:27]\n"
"v_cvt_pk_bf16_f32 v39, v22, v23\n"
"v_mfma_f32_16x16x32_bf16 v[24:27], a[184:187], a[232:235], v[24:27]\n"
"s_nop 0\n"
"v_mfma_f32_16x16x32_bf16 v[24:27], a[188:191], a[236:239], v[24:27]\n"
"buffer_store_dwordx4 v[36:39], v206, s[32:35], s54 offen offset:2048\n"
"v_mfma_f32_16x16x32_bf16 v[28:31], a[160:163], v[176:179], 0\n"
"s_nop 0\n"
"v_mfma_f32_16x16x32_bf16 v[28:31], a[164:167], v[180:183], v[28:31]\n"
"s_nop 0\n"
"v_mfma_f32_16x16x32_bf16 v[28:31], a[168:171], v[184:187], v[28:31]\n"
"s_nop 0\n"
"v_mfma_f32_16x16x32_bf16 v[28:31], a[172:175], v[188:191], v[28:31]\n"
"s_nop 0\n"
"v_mfma_f32_16x16x32_bf16 v[28:31], a[176:179], a[240:243], v[28:31]\n"
"v_cvt_pk_bf16_f32 v42, v24, v25\n"
"v_mfma_f32_16x16x32_bf16 v[28:31], a[180:183], a[244:247], v[28:31]\n"
"v_cvt_pk_bf16_f32 v43, v26, v27\n"
"v_mfma_f32_16x16x32_bf16 v[28:31], a[184:187], a[248:251], v[28:31]\n"
"s_nop 0\n"
"v_mfma_f32_16x16x32_bf16 v[28:31], a[188:191], a[252:255], v[28:31]\n"
"s_waitcnt lgkmcnt(0)\n"
"buffer_store_dwordx4 v[40:43], v207, s[32:35], s54 offen offset:2048\n"
"v_mfma_f32_16x16x32_bf16 v[0:3], a[128:131], v[128:131], 0\n"
"ds_read_b64_tr_b16 a[160:161], v196 offset:32768\n"
"v_mfma_f32_16x16x32_bf16 v[0:3], a[132:135], v[132:135], v[0:3]\n"
"ds_read_b64_tr_b16 a[162:163], v196 offset:34816\n"
"v_mfma_f32_16x16x32_bf16 v[0:3], a[136:139], v[136:139], v[0:3]\n"
"ds_read_b64_tr_b16 a[164:165], v196 offset:36864\n"
"v_mfma_f32_16x16x32_bf16 v[0:3], a[140:143], v[140:143], v[0:3]\n"
"ds_read_b64_tr_b16 a[166:167], v196 offset:38912\n"
"v_mfma_f32_16x16x32_bf16 v[0:3], a[144:147], a[192:195], v[0:3]\n"
"ds_read_b64_tr_b16 a[168:169], v196 offset:40960\n"
"v_cvt_pk_bf16_f32 v46, v28, v29\n"
"v_mfma_f32_16x16x32_bf16 v[0:3], a[148:151], a[196:199], v[0:3]\n"
"ds_read_b64_tr_b16 a[170:171], v196 offset:43008\n"
"v_cvt_pk_bf16_f32 v47, v30, v31\n"
"v_mfma_f32_16x16x32_bf16 v[0:3], a[152:155], a[200:203], v[0:3]\n"
"ds_read_b64_tr_b16 a[172:173], v196 offset:45056\n"
"v_mfma_f32_16x16x32_bf16 v[0:3], a[156:159], a[204:207], v[0:3]\n"
"buffer_store_dwordx4 v[44:47], v208, s[32:35], s54 offen offset:2048\n"
"ds_read_b64_tr_b16 a[174:175], v196 offset:47104\n"
"v_mfma_f32_16x16x32_bf16 v[4:7], a[128:131], v[144:147], 0\n"
"s_add_u32 s54, s54, 4096\n"
"ds_read_b64_tr_b16 a[176:177], v202 offset:32768\n"
"v_mfma_f32_16x16x32_bf16 v[4:7], a[132:135], v[148:151], v[4:7]\n"
"ds_read_b64_tr_b16 a[178:179], v202 offset:34816\n"
"v_mfma_f32_16x16x32_bf16 v[4:7], a[136:139], v[152:155], v[4:7]\n"
"ds_read_b64_tr_b16 a[180:181], v202 offset:36864\n"
"v_mfma_f32_16x16x32_bf16 v[4:7], a[140:143], v[156:159], v[4:7]\n"
"ds_read_b64_tr_b16 a[182:183], v202 offset:38912\n"
"v_mfma_f32_16x16x32_bf16 v[4:7], a[144:147], a[208:211], v[4:7]\n"
"ds_read_b64_tr_b16 a[184:185], v202 offset:40960\n"
"v_cvt_pk_bf16_f32 v32, v0, v1\n"
"v_mfma_f32_16x16x32_bf16 v[4:7], a[148:151], a[212:215], v[4:7]\n"
"ds_read_b64_tr_b16 a[186:187], v202 offset:43008\n"
"v_cvt_pk_bf16_f32 v33, v2, v3\n"
"v_mfma_f32_16x16x32_bf16 v[4:7], a[152:155], a[216:219], v[4:7]\n"
"ds_read_b64_tr_b16 a[188:189], v202 offset:45056\n"
"v_mfma_f32_16x16x32_bf16 v[4:7], a[156:159], a[220:223], v[4:7]\n"
"ds_read_b64_tr_b16 a[190:191], v202 offset:47104\n"
"v_mfma_f32_16x16x32_bf16 v[8:11], a[128:131], v[160:163], 0\n"
"s_nop 0\n"
"v_mfma_f32_16x16x32_bf16 v[8:11], a[132:135], v[164:167], v[8:11]\n"
"s_nop 0\n"
"v_mfma_f32_16x16x32_bf16 v[8:11], a[136:139], v[168:171], v[8:11]\n"
"s_nop 0\n"
"v_mfma_f32_16x16x32_bf16 v[8:11], a[140:143], v[172:175], v[8:11]\n"
"s_nop 0\n"
"v_mfma_f32_16x16x32_bf16 v[8:11], a[144:147], a[224:227], v[8:11]\n"
"v_cvt_pk_bf16_f32 v36, v4, v5\n"
"v_mfma_f32_16x16x32_bf16 v[8:11], a[148:151], a[228:231], v[8:11]\n"
"v_cvt_pk_bf16_f32 v37, v6, v7\n"
"v_mfma_f32_16x16x32_bf16 v[8:11], a[152:155], a[232:235], v[8:11]\n"
"s_nop 0\n"
"v_mfma_f32_16x16x32_bf16 v[8:11], a[156:159], a[236:239], v[8:11]\n"
"s_nop 0\n"
"v_mfma_f32_16x16x32_bf16 v[12:15], a[128:131], v[176:179], 0\n"
"s_nop 0\n"
"v_mfma_f32_16x16x32_bf16 v[12:15], a[132:135], v[180:183], v[12:15]\n"
"s_nop 0\n"
"v_mfma_f32_16x16x32_bf16 v[12:15], a[136:139], v[184:187], v[12:15]\n"
"s_nop 0\n"
"v_mfma_f32_16x16x32_bf16 v[12:15], a[140:143], v[188:191], v[12:15]\n"
"s_nop 0\n"
"v_mfma_f32_16x16x32_bf16 v[12:15], a[144:147], a[240:243], v[12:15]\n"
"v_cvt_pk_bf16_f32 v40, v8, v9\n"
"v_mfma_f32_16x16x32_bf16 v[12:15], a[148:151], a[244:247], v[12:15]\n"
"v_cvt_pk_bf16_f32 v41, v10, v11\n"
"v_mfma_f32_16x16x32_bf16 v[12:15], a[152:155], a[248:251], v[12:15]\n"
"s_nop 0\n"
"v_mfma_f32_16x16x32_bf16 v[12:15], a[156:159], a[252:255], v[12:15]\n"
"s_waitcnt lgkmcnt(0)\n"
"v_mfma_f32_16x16x32_bf16 v[16:19], a[160:163], v[128:131], 0\n"
"s_nop 0\n"
"v_mfma_f32_16x16x32_bf16 v[16:19], a[164:167], v[132:135], v[16:19]\n"
"s_nop 0\n"
"v_mfma_f32_16x16x32_bf16 v[16:19], a[168:171], v[136:139], v[16:19]\n"
"s_nop 0\n"
"v_mfma_f32_16x16x32_bf16 v[16:19], a[172:175], v[140:143], v[16:19]\n"
"s_nop 0\n"
"v_mfma_f32_16x16x32_bf16 v[16:19], a[176:179], a[192:195], v[16:19]\n"
"v_cvt_pk_bf16_f32 v44, v12, v13\n"
"v_mfma_f32_16x16x32_bf16 v[16:19], a[180:183], a[196:199], v[16:19]\n"
"v_cvt_pk_bf16_f32 v45, v14, v15\n"
"v_mfma_f32_16x16x32_bf16 v[16:19], a[184:187], a[200:203], v[16:19]\n"
"s_nop 0\n"
"v_mfma_f32_16x16x32_bf16 v[16:19], a[188:191], a[204:207], v[16:19]\n"
"s_nop 0\n"
"v_mfma_f32_16x16x32_bf16 v[20:23], a[160:163], v[144:147], 0\n"
"s_nop 0\n"
"v_mfma_f32_16x16x32_bf16 v[20:23], a[164:167], v[148:151], v[20:23]\n"
"s_nop 0\n"
"v_mfma_f32_16x16x32_bf16 v[20:23], a[168:171], v[152:155], v[20:23]\n"
"s_nop 0\n"
"v_mfma_f32_16x16x32_bf16 v[20:23], a[172:175], v[156:159], v[20:23]\n"
"s_nop 0\n"
"v_mfma_f32_16x16x32_bf16 v[20:23], a[176:179], a[208:211], v[20:23]\n"
"v_cvt_pk_bf16_f32 v34, v16, v17\n"
"v_mfma_f32_16x16x32_bf16 v[20:23], a[180:183], a[212:215], v[20:23]\n"
"v_cvt_pk_bf16_f32 v35, v18, v19\n"
"v_mfma_f32_16x16x32_bf16 v[20:23], a[184:187], a[216:219], v[20:23]\n"
"s_nop 0\n"
"v_mfma_f32_16x16x32_bf16 v[20:23], a[188:191], a[220:223], v[20:23]\n"
"buffer_store_dwordx4 v[32:35], v205, s[32:35], s54 offen offset:0\n"
"v_mfma_f32_16x16x32_bf16 v[24:27], a[160:163], v[160:163], 0\n"
"s_nop 0\n"
"v_mfma_f32_16x16x32_bf16 v[24:27], a[164:167], v[164:167], v[24:27]\n"
"s_nop 0\n"
"v_mfma_f32_16x16x32_bf16 v[24:27], a[168:171], v[168:171], v[24:27]\n"
"s_nop 0\n"
"v_mfma_f32_16x16x32_bf16 v[24:27], a[172:175], v[172:175], v[24:27]\n"
"s_nop 0\n"
"v_mfma_f32_16x16x32_bf16 v[24:27], a[176:179], a[224:227], v[24:27]\n"
"v_cvt_pk_bf16_f32 v38, v20, v21\n"
"v_mfma_f32_16x16x32_bf16 v[24:27], a[180:183], a[228:231], v[24:27]\n"
"v_cvt_pk_bf16_f32 v39, v22, v23\n"
"v_mfma_f32_16x16x32_bf16 v[24:27], a[184:187], a[232:235], v[24:27]\n"
"s_nop 0\n"
"v_mfma_f32_16x16x32_bf16 v[24:27], a[188:191], a[236:239], v[24:27]\n"
"buffer_store_dwordx4 v[36:39], v206, s[32:35], s54 offen offset:0\n"
"v_mfma_f32_16x16x32_bf16 v[28:31], a[160:163], v[176:179], 0\n"
"s_nop 0\n"
"v_mfma_f32_16x16x32_bf16 v[28:31], a[164:167], v[180:183], v[28:31]\n"
"s_nop 0\n"
"v_mfma_f32_16x16x32_bf16 v[28:31], a[168:171], v[184:187], v[28:31]\n"
"s_nop 0\n"
"v_mfma_f32_16x16x32_bf16 v[28:31], a[172:175], v[188:191], v[28:31]\n"
"s_nop 0\n"
"v_mfma_f32_16x16x32_bf16 v[28:31], a[176:179], a[240:243], v[28:31]\n"
"v_cvt_pk_bf16_f32 v42, v24, v25\n"
"v_mfma_f32_16x16x32_bf16 v[28:31], a[180:183], a[244:247], v[28:31]\n"
"v_cvt_pk_bf16_f32 v43, v26, v27\n"
"v_mfma_f32_16x16x32_bf16 v[28:31], a[184:187], a[248:251], v[28:31]\n"
"s_nop 0\n"
"v_mfma_f32_16x16x32_bf16 v[28:31], a[188:191], a[252:255], v[28:31]\n"
"buffer_store_dwordx4 v[40:43], v207, s[32:35], s54 offen offset:0\n"
"s_nop 1\n"
"s_nop 1\n"
"s_nop 1\n"
"s_nop 1\n"
"s_nop 1\n"
"v_cvt_pk_bf16_f32 v46, v28, v29\n"
"s_nop 1\n"
"v_cvt_pk_bf16_f32 v47, v30, v31\n"
"s_nop 1\n"
"s_nop 1\n"
"buffer_store_dwordx4 v[44:47], v208, s[32:35], s54 offen offset:0\n"
"s_nop 1\n"
"s_nop 1\n"
"s_nop 1\n"
"s_nop 1\n"
"s_nop 1\n"
"s_nop 1\n"
"s_nop 1\n"
"s_nop 1\n"
"s_branch AT_end\n"
"AT_fb:\n"
"s_waitcnt vmcnt(0) lgkmcnt(0)\n"
"s_barrier\n"
"s_mov_b32 s45, 1\n"
"s_lshl_b32 s16, s41, 12\n"
"s_mov_b32 s19, 32\n"
"v_mov_b32 v160, 0xff800000\n"
"v_mov_b32 v161, 0xff800000\n"
"v_mov_b32 v162, 0xff800000\n"
"v_mov_b32 v163, 0xff800000\n"
"AT_fbl:\n"
"s_mov_b32 m0, s18\n"
"s_nop 0\n"
"buffer_load_dwordx4 v246, s[8:11], s16 offen offset:0 lds\n"
"buffer_load_dwordx4 v246, s[8:11], s16 offen offset:1024 lds\n"
"buffer_load_dwordx4 v247, s[8:11], s16 offen offset:2048 lds\n"
"buffer_load_dwordx4 v247, s[8:11], s16 offen offset:3072 lds\n"
"s_add_u32 s16, s16, 16384\n"
"s_waitcnt vmcnt(0)\n"
"s_barrier\n"
"ds_read_b128 a[128:131], v240 offset:0\n"
"ds_read_b128 a[132:135], v240 offset:256\n"
"ds_read_b128 a[136:139], v240 offset:512\n"
"ds_read_b128 a[140:143], v240 offset:768\n"
"ds_read_b128 a[144:147], v240 offset:4096\n"
"ds_read_b128 a[148:151], v240 offset:4352\n"
"ds_read_b128 a[152:155], v240 offset:4608\n"
"ds_read_b128 a[156:159], v240 offset:4864\n"
"s_waitcnt lgkmcnt(0)\n"
"v_mfma_f32_16x16x32_bf16 v[128:131], a[128:131], v[64:67], 0\n"
"v_mfma_f32_16x16x32_bf16 v[128:131], a[132:135], v[68:71], v[128:131]\n"
"v_mfma_f32_16x16x32_bf16 v[128:131], a[136:139], v[72:75], v[128:131]\n"
"v_mfma_f32_16x16x32_bf16 v[128:131], a[140:143], v[76:79], v[128:131]\n"
"v_mfma_f32_16x16x32_bf16 v[132:135], a[144:147], v[64:67], 0\n"
"v_mfma_f32_16x16x32_bf16 v[132:135], a[148:151], v[68:71], v[132:135]\n"
"v_mfma_f32_16x16x32_bf16 v[132:135], a[152:155], v[72:75], v[132:135]\n"
"v_mfma_f32_16x16x32_bf16 v[132:135], a[156:159], v[76:79], v[132:135]\n"
"v_mfma_f32_16x16x32_bf16 v[136:139], a[128:131], v[80:83], 0\n"
"v_mfma_f32_16x16x32_bf16 v[136:139], a[132:135], v[84:87], v[136:139]\n"
"v_mfma_f32_16x16x32_bf16 v[136:139], a[136:139], v[88:91], v[136:139]\n"
"v_mfma_f32_16x16x32_bf16 v[136:139], a[140:143], v[92:95], v[136:139]\n"
"v_mfma_f32_16x16x32_bf16 v[140:143], a[144:147], v[80:83], 0\n"
"v_mfma_f32_16x16x32_bf16 v[140:143], a[148:151], v[84:87], v[140:143]\n"
"v_mfma_f32_16x16x32_bf16 v[140:143], a[152:155], v[88:91], v[140:143]\n"
"v_mfma_f32_16x16x32_bf16 v[140:143], a[156:159], v[92:95], v[140:143]\n"
"v_mfma_f32_16x16x32_bf16 v[144:147], a[128:131], v[96:99], 0\n"
"v_mfma_f32_16x16x32_bf16 v[144:147], a[132:135], v[100:103], v[144:147]\n"
"v_mfma_f32_16x16x32_bf16 v[144:147], a[136:139], v[104:107], v[144:147]\n"
"v_mfma_f32_16x16x32_bf16 v[144:147], a[140:143], v[108:111], v[144:147]\n"
"v_mfma_f32_16x16x32_bf16 v[148:151], a[144:147], v[96:99], 0\n"
"v_mfma_f32_16x16x32_bf16 v[148:151], a[148:151], v[100:103], v[148:151]\n"
"v_mfma_f32_16x16x32_bf16 v[148:151], a[152:155], v[104:107], v[148:151]\n"
"v_mfma_f32_16x16x32_bf16 v[148:151], a[156:159], v[108:111], v[148:151]\n"
"v_mfma_f32_16x16x32_bf16 v[152:155], a[128:131], v[112:115], 0\n"
"v_mfma_f32_16x16x32_bf16 v[152:155], a[132:135], v[116:119], v[152:155]\n"
"v_mfma_f32_16x16x32_bf16 v[152:155], a[136:139], v[120:123], v[152:155]\n"
"v_mfma_f32_16x16x32_bf16 v[152:155], a[140:143], v[124:127], v[152:155]\n"
"v_mfma_f32_16x16x32_bf16 v[156:159], a[144:147], v[112:115], 0\n"
"v_mfma_f32_16x16x32_bf16 v[156:159], a[148:151], v[116:119], v[156:159]\n"
"v_mfma_f32_16x16x32_bf16 v[156:159], a[152:155], v[120:123], v[156:159]\n"
"v_mfma_f32_16x16x32_bf16 v[156:159], a[156:159], v[124:127], v[156:159]\n"
"s_nop 7\n"
"v_max3_f32 v160, v160, v128, v129\n"
"v_max3_f32 v160, v160, v130, v131\n"
"v_max3_f32 v160, v160, v132, v133\n"
"v_max3_f32 v160, v160, v134, v135\n"
"v_max3_f32 v161, v161, v136, v137\n"
"v_max3_f32 v161, v161, v138, v139\n"
"v_max3_f32 v161, v161, v140, v141\n"
"v_max3_f32 v161, v161, v142, v143\n"
"v_max3_f32 v162, v162, v144, v145\n"
"v_max3_f32 v162, v162, v146, v147\n"
"v_max3_f32 v162, v162, v148, v149\n"
"v_max3_f32 v162, v162, v150, v151\n"
"v_max3_f32 v163, v163, v152, v153\n"
"v_max3_f32 v163, v163, v154, v155\n"
"v_max3_f32 v163, v163, v156, v157\n"
"v_max3_f32 v163, v163, v158, v159\n"
"ds_read_b128 a[128:131], v240 offset:8192\n"
"ds_read_b128 a[132:135], v240 offset:8448\n"
"ds_read_b128 a[136:139], v240 offset:8704\n"
"ds_read_b128 a[140:143], v240 offset:8960\n"
"ds_read_b128 a[144:147], v240 offset:12288\n"
"ds_read_b128 a[148:151], v240 offset:12544\n"
"ds_read_b128 a[152:155], v240 offset:12800\n"
"ds_read_b128 a[156:159], v240 offset:13056\n"
"s_waitcnt lgkmcnt(0)\n"
"v_mfma_f32_16x16x32_bf16 v[128:131], a[128:131], v[64:67], 0\n"
"v_mfma_f32_16x16x32_bf16 v[128:131], a[132:135], v[68:71], v[128:131]\n"
"v_mfma_f32_16x16x32_bf16 v[128:131], a[136:139], v[72:75], v[128:131]\n"
"v_mfma_f32_16x16x32_bf16 v[128:131], a[140:143], v[76:79], v[128:131]\n"
"v_mfma_f32_16x16x32_bf16 v[132:135], a[144:147], v[64:67], 0\n"
"v_mfma_f32_16x16x32_bf16 v[132:135], a[148:151], v[68:71], v[132:135]\n"
"v_mfma_f32_16x16x32_bf16 v[132:135], a[152:155], v[72:75], v[132:135]\n"
"v_mfma_f32_16x16x32_bf16 v[132:135], a[156:159], v[76:79], v[132:135]\n"
"v_mfma_f32_16x16x32_bf16 v[136:139], a[128:131], v[80:83], 0\n"
"v_mfma_f32_16x16x32_bf16 v[136:139], a[132:135], v[84:87], v[136:139]\n"
"v_mfma_f32_16x16x32_bf16 v[136:139], a[136:139], v[88:91], v[136:139]\n"
"v_mfma_f32_16x16x32_bf16 v[136:139], a[140:143], v[92:95], v[136:139]\n"
"v_mfma_f32_16x16x32_bf16 v[140:143], a[144:147], v[80:83], 0\n"
"v_mfma_f32_16x16x32_bf16 v[140:143], a[148:151], v[84:87], v[140:143]\n"
"v_mfma_f32_16x16x32_bf16 v[140:143], a[152:155], v[88:91], v[140:143]\n"
"v_mfma_f32_16x16x32_bf16 v[140:143], a[156:159], v[92:95], v[140:143]\n"
"v_mfma_f32_16x16x32_bf16 v[144:147], a[128:131], v[96:99], 0\n"
"v_mfma_f32_16x16x32_bf16 v[144:147], a[132:135], v[100:103], v[144:147]\n"
"v_mfma_f32_16x16x32_bf16 v[144:147], a[136:139], v[104:107], v[144:147]\n"
"v_mfma_f32_16x16x32_bf16 v[144:147], a[140:143], v[108:111], v[144:147]\n"
"v_mfma_f32_16x16x32_bf16 v[148:151], a[144:147], v[96:99], 0\n"
"v_mfma_f32_16x16x32_bf16 v[148:151], a[148:151], v[100:103], v[148:151]\n"
"v_mfma_f32_16x16x32_bf16 v[148:151], a[152:155], v[104:107], v[148:151]\n"
"v_mfma_f32_16x16x32_bf16 v[148:151], a[156:159], v[108:111], v[148:151]\n"
"v_mfma_f32_16x16x32_bf16 v[152:155], a[128:131], v[112:115], 0\n"
"v_mfma_f32_16x16x32_bf16 v[152:155], a[132:135], v[116:119], v[152:155]\n"
"v_mfma_f32_16x16x32_bf16 v[152:155], a[136:139], v[120:123], v[152:155]\n"
"v_mfma_f32_16x16x32_bf16 v[152:155], a[140:143], v[124:127], v[152:155]\n"
"v_mfma_f32_16x16x32_bf16 v[156:159], a[144:147], v[112:115], 0\n"
"v_mfma_f32_16x16x32_bf16 v[156:159], a[148:151], v[116:119], v[156:159]\n"
"v_mfma_f32_16x16x32_bf16 v[156:159], a[152:155], v[120:123], v[156:159]\n"
"v_mfma_f32_16x16x32_bf16 v[156:159], a[156:159], v[124:127], v[156:159]\n"
"s_nop 7\n"
"v_max3_f32 v160, v160, v128, v129\n"
"v_max3_f32 v160, v160, v130, v131\n"
"v_max3_f32 v160, v160, v132, v133\n"
"v_max3_f32 v160, v160, v134, v135\n"
"v_max3_f32 v161, v161, v136, v137\n"
"v_max3_f32 v161, v161, v138, v139\n"
"v_max3_f32 v161, v161, v140, v141\n"
"v_max3_f32 v161, v161, v142, v143\n"
"v_max3_f32 v162, v162, v144, v145\n"
"v_max3_f32 v162, v162, v146, v147\n"
"v_max3_f32 v162, v162, v148, v149\n"
"v_max3_f32 v162, v162, v150, v151\n"
"v_max3_f32 v163, v163, v152, v153\n"
"v_max3_f32 v163, v163, v154, v155\n"
"v_max3_f32 v163, v163, v156, v157\n"
"v_max3_f32 v163, v163, v158, v159\n"
"s_barrier\n"
"s_sub_u32 s19, s19, 1\n"
"s_cmp_lg_u32 s19, 0\n"
"s_cbranch_scc1 AT_fbl\n"
"v_mov_b32 v164, v160\n"
"s_nop 1\n"
"v_permlane32_swap_b32 v160, v164\n"
"v_max_f32 v160, v160, v164\n"
"v_mov_b32 v164, v160\n"
"s_nop 1\n"
"v_permlane16_swap_b32 v160, v164\n"
"v_max_f32 v160, v160, v164\n"
"v_sub_f32 v24, 0, v160\n"
"v_sub_f32 v25, 0, v160\n"
"v_sub_f32 v26, 0, v160\n"
"v_sub_f32 v27, 0, v160\n"
"v_mov_b32 v164, v161\n"
"s_nop 1\n"
"v_permlane32_swap_b32 v161, v164\n"
"v_max_f32 v161, v161, v164\n"
"v_mov_b32 v164, v161\n"
"s_nop 1\n"
"v_permlane16_swap_b32 v161, v164\n"
"v_max_f32 v161, v161, v164\n"
"v_sub_f32 v28, 0, v161\n"
"v_sub_f32 v29, 0, v161\n"
"v_sub_f32 v30, 0, v161\n"
"v_sub_f32 v31, 0, v161\n"
"v_mov_b32 v164, v162\n"
"s_nop 1\n"
"v_permlane32_swap_b32 v162, v164\n"
"v_max_f32 v162, v162, v164\n"
"v_mov_b32 v164, v162\n"
"s_nop 1\n"
"v_permlane16_swap_b32 v162, v164\n"
"v_max_f32 v162, v162, v164\n"
"v_sub_f32 v32, 0, v162\n"
"v_sub_f32 v33, 0, v162\n"
"v_sub_f32 v34, 0, v162\n"
"v_sub_f32 v35, 0, v162\n"
"v_mov_b32 v164, v163\n"
"s_nop 1\n"
"v_permlane32_swap_b32 v163, v164\n"
"v_max_f32 v163, v163, v164\n"
"v_mov_b32 v164, v163\n"
"s_nop 1\n"
"v_permlane16_swap_b32 v163, v164\n"
"v_max_f32 v163, v163, v164\n"
"v_sub_f32 v36, 0, v163\n"
"v_sub_f32 v37, 0, v163\n"
"v_sub_f32 v38, 0, v163\n"
"v_sub_f32 v39, 0, v163\n"
"v_mov_b32 v40, 0x3f803f80\n"
"v_mov_b32 v41, 0x3f803f80\n"
"v_mov_b32 v42, 0x3f803f80\n"
"v_mov_b32 v43, 0x3f803f80\n"
"s_lshl_b32 s16, s41, 12\n"
"s_mul_i32 s52, s41, 73728\n"
"s_mov_b32 m0, s18\n"
"s_nop 0\n"
"buffer_load_dwordx4 v246, s[8:11], s16 offen offset:0 lds\n"
"buffer_load_dwordx4 v246, s[8:11], s16 offen offset:1024 lds\n"
"buffer_load_dwordx4 v247, s[8:11], s16 offen offset:2048 lds\n"
"buffer_load_dwordx4 v247, s[8:11], s16 offen offset:3072 lds\n"
"s_add_u32 m0, s18, 16384\n"
"s_nop 0\n"
"buffer_load_dwordx4 v246, s[12:15], s16 offen offset:0 lds\n"
"buffer_load_dwordx4 v247, s[12:15], s16 offen offset:1024 lds\n"
"buffer_load_dwordx4 v246, s[12:15], s16 offen offset:2048 lds\n"
"buffer_load_dwordx4 v247, s[12:15], s16 offen offset:3072 lds\n"
"s_add_u32 s16, s16, 16384\n"
"s_add_u32 m0, s18, 32768\n"
"s_nop 0\n"
"buffer_load_dwordx4 v246, s[8:11], s16 offen offset:0 lds\n"
"buffer_load_dwordx4 v246, s[8:11], s16 offen offset:1024 lds\n"
"buffer_load_dwordx4 v247, s[8:11], s16 offen offset:2048 lds\n"
"buffer_load_dwordx4 v247, s[8:11], s16 offen offset:3072 lds\n"
"s_add_u32 m0, s18, 49152\n"
"s_nop 0\n"
"buffer_load_dwordx4 v246, s[12:15], s16 offen offset:0 lds\n"
"buffer_load_dwordx4 v247, s[12:15], s16 offen offset:1024 lds\n"
"buffer_load_dwordx4 v246, s[12:15], s16 offen offset:2048 lds\n"
"buffer_load_dwordx4 v247, s[12:15], s16 offen offset:3072 lds\n"
"s_add_u32 s16, s16, 16384\n"
"s_add_u32 m0, s18, 65536\n"
"s_nop 0\n"
"buffer_load_dwordx4 v246, s[8:11], s16 offen offset:0 lds\n"
"buffer_load_dwordx4 v246, s[8:11], s16 offen offset:1024 lds\n"
"buffer_load_dwordx4 v247, s[8:11], s16 offen offset:2048 lds\n"
"buffer_load_dwordx4 v247, s[8:11], s16 offen offset:3072 lds\n"
"s_add_u32 m0, s18, 81920\n"
"s_nop 0\n"
"buffer_load_dwordx4 v246, s[12:15], s16 offen offset:0 lds\n"
"buffer_load_dwordx4 v247, s[12:15], s16 offen offset:1024 lds\n"
"buffer_load_dwordx4 v246, s[12:15], s16 offen offset:2048 lds\n"
"buffer_load_dwordx4 v247, s[12:15], s16 offen offset:3072 lds\n"
"s_add_u32 s16, s16, 16384\n"
"s_add_u32 m0, s18, 98304\n"
"s_nop 0\n"
"buffer_load_dwordx4 v246, s[8:11], s16 offen offset:0 lds\n"
"buffer_load_dwordx4 v246, s[8:11], s16 offen offset:1024 lds\n"
"buffer_load_dwordx4 v247, s[8:11], s16 offen offset:2048 lds\n"
"buffer_load_dwordx4 v247, s[8:11], s16 offen offset:3072 lds\n"
"s_add_u32 m0, s18, 114688\n"
"s_nop 0\n"
"buffer_load_dwordx4 v246, s[12:15], s16 offen offset:0 lds\n"
"buffer_load_dwordx4 v247, s[12:15], s16 offen offset:1024 lds\n"
"buffer_load_dwordx4 v246, s[12:15], s16 offen offset:2048 lds\n"
"buffer_load_dwordx4 v247, s[12:15], s16 offen offset:3072 lds\n"
"s_add_u32 s16, s16, 16384\n"
"s_waitcnt vmcnt(24)\n"
"s_barrier\n"
"ds_read_b128 a[128:131], v240 offset:0\n"
"ds_read_b128 a[132:135], v240 offset:256\n"
"ds_read_b128 a[136:139], v240 offset:512\n"
"ds_read_b128 a[140:143], v240 offset:768\n"
"ds_read_b128 a[144:147], v240 offset:4096\n"
"ds_read_b128 a[148:151], v240 offset:4352\n"
"ds_read_b128 a[152:155], v240 offset:4608\n"
"ds_read_b128 a[156:159], v240 offset:4864\n"
"ds_read_b64_tr_b16 a[192:193], v242 offset:16384\n"
"ds_read_b64_tr_b16 a[194:195], v242 offset:20480\n"
"ds_read_b64_tr_b16 a[196:197], v243 offset:16384\n"
"ds_read_b64_tr_b16 a[198:199], v243 offset:20480\n"
"ds_read_b64_tr_b16 a[200:201], v242 offset:16640\n"
"ds_read_b64_tr_b16 a[202:203], v242 offset:20736\n"
"ds_read_b64_tr_b16 a[204:205], v243 offset:16640\n"
"ds_read_b64_tr_b16 a[206:207], v243 offset:20736\n"
"ds_read_b64_tr_b16 a[208:209], v242 offset:16896\n"
"ds_read_b64_tr_b16 a[210:211], v242 offset:20992\n"
"ds_read_b64_tr_b16 a[212:213], v243 offset:16896\n"
"ds_read_b64_tr_b16 a[214:215], v243 offset:20992\n"
"ds_read_b64_tr_b16 a[216:217], v242 offset:17152\n"
"ds_read_b64_tr_b16 a[218:219], v242 offset:21248\n"
"ds_read_b64_tr_b16 a[220:221], v243 offset:17152\n"
"ds_read_b64_tr_b16 a[222:223], v243 offset:21248\n"
"s_waitcnt lgkmcnt(0)\n"
"s_branch AT_main\n"
"AT_end:\n"
  :: "s"(__builtin_amdgcn_kernarg_segment_ptr()), "s"(blockIdx.x), "s"(wid), "s"(ldsb) : "v0", "v1", "v2", "v3", "v4", "v5", "v6", "v7", "v8", "v9", "v10", "v11", "v12", "v13", "v14", "v15", "v16", "v17", "v18", "v19", "v20", "v21", "v22", "v23", "v24", "v25", "v26", "v27", "v28", "v29", "v30", "v31", "v32", "v33", "v34", "v35", "v36", "v37", "v38", "v39", "v40", "v41", "v42", "v43", "v44", "v45", "v46", "v47", "v48", "v49", "v50", "v51", "v52", "v53", "v54", "v55", "v56", "v57", "v58", "v59", "v60", "v61", "v62", "v63", "v64", "v65", "v66", "v67", "v68", "v69", "v70", "v71", "v72", "v73", "v74", "v75", "v76", "v77", "v78", "v79", "v80", "v81", "v82", "v83", "v84", "v85", "v86", "v87", "v88", "v89", "v90", "v91", "v92", "v93", "v94", "v95", "v96", "v97", "v98", "v99", "v100", "v101", "v102", "v103", "v104", "v105", "v106", "v107", "v108", "v109", "v110", "v111", "v112", "v113", "v114", "v115", "v116", "v117", "v118", "v119", "v120", "v121", "v122", "v123", "v124", "v125", "v126", "v127", "v128", "v129", "v130", "v131", "v132", "v133", "v134", "v135", "v136", "v137", "v138", "v139", "v140", "v141", "v142", "v143", "v144", "v145", "v146", "v147", "v148", "v149", "v150", "v151", "v152", "v153", "v154", "v155", "v156", "v157", "v158", "v159", "v160", "v161", "v162", "v163", "v164", "v165", "v166", "v167", "v168", "v169", "v170", "v171", "v172", "v173", "v174", "v175", "v176", "v177", "v178", "v179", "v180", "v181", "v182", "v183", "v184", "v185", "v186", "v187", "v188", "v189", "v190", "v191", "v192", "v193", "v194", "v195", "v196", "v197", "v198", "v199", "v200", "v201", "v202", "v203", "v204", "v205", "v206", "v207", "v208", "v209", "v210", "v211", "v212", "v213", "v214", "v215", "v216", "v217", "v218", "v219", "v220", "v221", "v222", "v223", "v224", "v225", "v226", "v227", "v228", "v229", "v230", "v231", "v232", "v233", "v234", "v235", "v236", "v237", "v238", "v239", "v240", "v241", "v242", "v243", "v244", "v245", "v246", "v247", "v248", "v249", "v250", "v251", "v252", "v253", "v254", "v255", "a0", "a1", "a2", "a3", "a4", "a5", "a6", "a7", "a8", "a9", "a10", "a11", "a12", "a13", "a14", "a15", "a16", "a17", "a18", "a19", "a20", "a21", "a22", "a23", "a24", "a25", "a26", "a27", "a28", "a29", "a30", "a31", "a32", "a33", "a34", "a35", "a36", "a37", "a38", "a39", "a40", "a41", "a42", "a43", "a44", "a45", "a46", "a47", "a48", "a49", "a50", "a51", "a52", "a53", "a54", "a55", "a56", "a57", "a58", "a59", "a60", "a61", "a62", "a63", "a64", "a65", "a66", "a67", "a68", "a69", "a70", "a71", "a72", "a73", "a74", "a75", "a76", "a77", "a78", "a79", "a80", "a81", "a82", "a83", "a84", "a85", "a86", "a87", "a88", "a89", "a90", "a91", "a92", "a93", "a94", "a95", "a96", "a97", "a98", "a99", "a100", "a101", "a102", "a103", "a104", "a105", "a106", "a107", "a108", "a109", "a110", "a111", "a112", "a113", "a114", "a115", "a116", "a117", "a118", "a119", "a120", "a121", "a122", "a123", "a124", "a125", "a126", "a127", "a128", "a129", "a130", "a131", "a132", "a133", "a134", "a135", "a136", "a137", "a138", "a139", "a140", "a141", "a142", "a143", "a144", "a145", "a146", "a147", "a148", "a149", "a150", "a151", "a152", "a153", "a154", "a155", "a156", "a157", "a158", "a159", "a160", "a161", "a162", "a163", "a164", "a165", "a166", "a167", "a168", "a169", "a170", "a171", "a172", "a173", "a174", "a175", "a176", "a177", "a178", "a179", "a180", "a181", "a182", "a183", "a184", "a185", "a186", "a187", "a188", "a189", "a190", "a191", "a192", "a193", "a194", "a195", "a196", "a197", "a198", "a199", "a200", "a201", "a202", "a203", "a204", "a205", "a206", "a207", "a208", "a209", "a210", "a211", "a212", "a213", "a214", "a215", "a216", "a217", "a218", "a219", "a220", "a221", "a222", "a223", "a224", "a225", "a226", "a227", "a228", "a229", "a230", "a231", "a232", "a233", "a234", "a235", "a236", "a237", "a238", "a239", "a240", "a241", "a242", "a243", "a244", "a245", "a246", "a247", "a248", "a249", "a250", "a251", "a252", "a253", "a254", "a255", "s8", "s9", "s10", "s11", "s12", "s13", "s14", "s15", "s16", "s17", "s18", "s19", "s20", "s21", "s22", "s23", "s24", "s25", "s26", "s27", "s28", "s29", "s30", "s31", "s32", "s33", "s34", "s35", "s36", "s37", "s38", "s39", "s40", "s41", "s42", "s43", "s44", "s45", "s46", "s47", "s48", "s49", "s50", "s51", "s52", "s53", "s54", "s55", "s56", "s57", "s58", "s59", "s60", "s61", "s62", "s63", "s64", "s65", "s66", "s67", "s68", "s69", "s70", "s71", "s72", "s73", "s74", "s75", "s76", "s77", "s78", "s79", "s80", "s81", "s82", "s83", "s84", "s85", "s86", "s87", "s88", "s89", "s90", "s91", "s92", "s93", "s94", "s95", "s96", "s97", "s98", "s99", "vcc", "scc", "memory");
}

__global__ __launch_bounds__(576) void k_post(const char* __restrict__ part, const unsigned short* __restrict__ x, const float* __restrict__ mod,
                                              const unsigned short* __restrict__ wr, unsigned short* __restrict__ out,
                                              float* __restrict__ h2, float* __restrict__ rout, float* __restrict__ x1) {
  __shared__ float red[3][9][8];
  const int tid = threadIdx.x, a = tid & 7, seg = tid >> 3, u = seg >> 2, g = seg & 3, wid = tid >> 6, lane = tid & 63;
  const int blk = blockIdx.x, row = blk * 8 + a;
  const char* pp = part + ((size_t)((blk >> 1) * 18 + u) * 1024 + (16 * g + 8 * (blk & 1) + a) * 16);
  u32x4 pv[16];
#pragma unroll
  for (int p = 0; p < 16; ++p) pv[p] = __builtin_nontemporal_load((const u32x4*)(pp + (size_t)p * (SQ * DM * 2)));
  const int n0 = 32 * u + 4 * g, n1 = n0 + 16;
  const u32x2 xa = *(const u32x2*)(x + (size_t)row * DM + n0), xb = *(const u32x2*)(x + (size_t)row * DM + n1);
  const u32x2 wa = *(const u32x2*)(wr + n0), wb = *(const u32x2*)(wr + n1);
  f32x4 m[4][2];
#pragma unroll
  for (int k = 0; k < 4; ++k) { m[k][0] = *(const f32x4*)(mod + (2 + k) * DM + n0); m[k][1] = *(const f32x4*)(mod + (2 + k) * DM + n1); }
  float acc[8] = {};
#pragma unroll
  for (int p = 0; p < 16; ++p)
#pragma unroll
    for (int e = 0; e < 4; ++e) { acc[2 * e] += bflo(pv[p][e]); acc[2 * e + 1] += bfhi(pv[p][e]); }
  const float xv[8] = {bflo(xa[0]), bfhi(xa[0]), bflo(xa[1]), bfhi(xa[1]), bflo(xb[0]), bfhi(xb[0]), bflo(xb[1]), bfhi(xb[1])};
  const float wv[8] = {bflo(wa[0]), bfhi(wa[0]), bflo(wa[1]), bfhi(wa[1]), bflo(wb[0]), bfhi(wb[0]), bflo(wb[1]), bfhi(wb[1])};
  float v[8]; float s = 0.f;
#pragma unroll
  for (int e = 0; e < 8; ++e) { v[e] = xv[e] + m[0][e >> 2][e & 3] * acc[e]; s += v[e]; }
  s += __shfl_xor(s, 8); s += __shfl_xor(s, 16); s += __shfl_xor(s, 32);
  if (lane < 8) red[0][wid][lane] = s;
  __syncthreads();
  float mean = 0.f;
#pragma unroll
  for (int w = 0; w < 9; ++w) mean += red[0][w][a];
  mean *= (1.f / DM);
  float q = 0.f;
#pragma unroll
  for (int e = 0; e < 8; ++e) { const float d = v[e] - mean; q += d * d; }
  q += __shfl_xor(q, 8); q += __shfl_xor(q, 16); q += __shfl_xor(q, 32);
  if (lane < 8) red[1][wid][lane] = q;
  __syncthreads();
  float var = 0.f;
#pragma unroll
  for (int w = 0; w < 9; ++w) var += red[1][w][a];
  const float rstd = 1.f / sqrtf(var * (1.f / DM) + 1e-5f);
  float r = 0.f, t[8], o[8];
#pragma unroll
  for (int e = 0; e < 8; ++e) {
    t[e] = (v[e] - mean) * rstd * (1.f + m[2][e >> 2][e & 3]) + m[1][e >> 2][e & 3];
    r += t[e] * wv[e];
    o[e] = v[e] + m[3][e >> 2][e & 3] * t[e];
  }
  r += __shfl_xor(r, 8); r += __shfl_xor(r, 16); r += __shfl_xor(r, 32);
  if (lane < 8) red[2][wid][lane] = r;
  *(u32x2*)(out + (size_t)row * DM + n0) = u32x2{cvtpk(o[0], o[1]), cvtpk(o[2], o[3])};
  *(u32x2*)(out + (size_t)row * DM + n1) = u32x2{cvtpk(o[4], o[5]), cvtpk(o[6], o[7])};
  *(f32x4*)(h2 + (size_t)row * DM + n0) = f32x4{t[0], t[1], t[2], t[3]}; *(f32x4*)(h2 + (size_t)row * DM + n1) = f32x4{t[4], t[5], t[6], t[7]};
  *(f32x4*)(x1 + (size_t)row * DM + n0) = f32x4{v[0], v[1], v[2], v[3]}; *(f32x4*)(x1 + (size_t)row * DM + n1) = f32x4{v[4], v[5], v[6], v[7]};
  __syncthreads();
  if (tid < 8) { float rr = 0.f;
#pragma unroll
    for (int w = 0; w < 9; ++w) rr += red[2][w][tid];
    rout[blk * 8 + tid] = rr; }
}

constexpr int MOE_BLK = FF / 32;
__global__ __launch_bounds__(256) void k_moe1(const float* __restrict__ rin, const float* __restrict__ h2, const unsigned short* __restrict__ wg,
                                              const unsigned short* __restrict__ wu, const unsigned short* __restrict__ wd, float* __restrict__ yp, int* __restrict__ selg) {
  __shared__ float xs[2][DM];
  __shared__ float red[64][4][33];
  __shared__ float act[2][32];
  __shared__ float dred[3][2][DM];
  __shared__ int sidx[2];
  __shared__ float wv[2][4]; __shared__ int wi[2][4];
  const int tid = threadIdx.x, lane = tid & 63, wid = tid >> 6;
  const int bb = blockIdx.x, slice = ((bb >> 4) << 4) | ((bb & 7) << 1) | ((bb >> 3) & 1);
  const int f0 = slice * 32;
  const int cg = tid & 3, ks = tid >> 2;
  bf16x8 wgr[9], wur[9];
#pragma unroll
  for (int i = 0; i < 9; ++i) { const size_t off = (size_t)(ks * 9 + i) * FF + f0 + cg * 8; wgr[i] = *(const bf16x8*)(wg + off); wur[i] = *(const bf16x8*)(wu + off); }
  const int dcg = tid % 72, drs = tid / 72;
  const int dr0 = drs * 11, dr1 = drs == 2 ? 32 : dr0 + 11;
  bf16x8 wdr[11];
  if (tid < 216) {
#pragma unroll
    for (int i = 0; i < 11; ++i) { int r = dr0 + i; if (r < dr1) wdr[i] = *(const bf16x8*)(wd + (size_t)(f0 + r) * DM + dcg * 8); }
  }
  float b0 = -INFINITY, b1 = -INFINITY; int i0 = 0x7fffffff, i1 = 0x7fffffff;
#pragma unroll
  for (int j = 0; j < SQ / 256; ++j) { const int i = tid + j * 256; float v = rin[i];
    if (v > b0) { b1 = b0; i1 = i0; b0 = v; i0 = i; } else if (v > b1) { b1 = v; i1 = i; } }
#pragma unroll
  for (int o = 32; o > 0; o >>= 1) {
    float c0 = __shfl_xor(b0, o), c1 = __shfl_xor(b1, o); int j0 = __shfl_xor(i0, o), j1 = __shfl_xor(i1, o);
    bool a_first = (b0 > c0) || (b0 == c0 && i0 < j0);
    float n0 = a_first ? b0 : c0; int m0 = a_first ? i0 : j0;
    float sa = a_first ? b1 : b0; int ia = a_first ? i1 : i0;
    float sb = a_first ? c0 : c1; int ib = a_first ? j0 : j1;
    bool s_first = (sa > sb) || (sa == sb && ia < ib);
    b0 = n0; i0 = m0; b1 = s_first ? sa : sb; i1 = s_first ? ia : ib;
  }
  if (lane == 0) { wv[0][wid] = b0; wi[0][wid] = i0; wv[1][wid] = b1; wi[1][wid] = i1; }
  __syncthreads();
  if (tid == 0) {
    float t0 = -INFINITY, t1 = -INFINITY; int k0 = 0x7fffffff, k1 = 0x7fffffff;
    for (int w = 0; w < 4; ++w) for (int s = 0; s < 2; ++s) { float v = wv[s][w]; int id = wi[s][w];
      if (v > t0 || (v == t0 && id < k0)) { t1 = t0; k1 = k0; t0 = v; k0 = id; } else if (v > t1 || (v == t1 && id < k1)) { t1 = v; k1 = id; } }
    sidx[0] = k0; sidx[1] = k1;
    if (blockIdx.x == 0) { selg[0] = k0; selg[1] = k1; }
  }
  __syncthreads();
  for (int i = tid; i < 2 * DM; i += 256) { int t = i / DM, k = i % DM; xs[t][k] = h2[(size_t)sidx[t] * DM + k]; }
  __syncthreads();
  {
    float g0[8] = {}, g1[8] = {}, u0[8] = {}, u1[8] = {};
#pragma unroll
    for (int i = 0; i < 9; ++i) { const float x0 = xs[0][ks * 9 + i], x1v = xs[1][ks * 9 + i];
#pragma unroll
      for (int e = 0; e < 8; ++e) { float a = bf2f((unsigned short)wgr[i][e]), b = bf2f((unsigned short)wur[i][e]);
        g0[e] += x0 * a; g1[e] += x1v * a; u0[e] += x0 * b; u1[e] += x1v * b; } }
#pragma unroll
    for (int e = 0; e < 8; ++e) { red[ks][0][cg * 8 + e] = g0[e]; red[ks][1][cg * 8 + e] = g1[e]; red[ks][2][cg * 8 + e] = u0[e]; red[ks][3][cg * 8 + e] = u1[e]; }
  }
  __syncthreads();
  if (tid < 64) { int t = tid >> 5, cc = tid & 31; float g = 0, u = 0;
#pragma unroll 16
    for (int s = 0; s < 64; ++s) { g += red[s][t][cc]; u += red[s][2 + t][cc]; }
    float sg = g / (1.f + __expf(-g));
    act[t][cc] = sg * u; }
  __syncthreads();
  if (tid < 216) {
    float y0[8] = {}, y1[8] = {};
#pragma unroll
    for (int i = 0; i < 11; ++i) { int r = dr0 + i; if (r < dr1) { const float a0 = act[0][r], a1 = act[1][r];
#pragma unroll
        for (int e = 0; e < 8; ++e) { float w = bf2f((unsigned short)wdr[i][e]); y0[e] += a0 * w; y1[e] += a1 * w; } } }
#pragma unroll
    for (int e = 0; e < 8; ++e) { dred[drs][0][dcg * 8 + e] = y0[e]; dred[drs][1][dcg * 8 + e] = y1[e]; }
  }
  __syncthreads();
  for (int i = tid; i < 2 * DM; i += 256) { int t = i / DM, n = i % DM; yp[(size_t)blockIdx.x * 2 * DM + i] = dred[0][t][n] + dred[1][t][n] + dred[2][t][n]; }
}
__global__ __launch_bounds__(256) void k_moe2(const float* __restrict__ yp, const int* __restrict__ selg, const float* __restrict__ rin,
                                              const float* __restrict__ h2, const float* __restrict__ mod,
                                              unsigned short* __restrict__ out, const float* __restrict__ x1) {
  __shared__ float red[8][33];
  const int tid = threadIdx.x, o = tid & 31, g = tid >> 5, i = blockIdx.x * 32 + o;
  float v[18];
#pragma unroll
  for (int j = 0; j < 18; ++j) v[j] = yp[(size_t)(g * 18 + j) * 2 * DM + i];
  const int t = i / DM, n = i % DM, row = selg[t];
  float rr = 0.f, hv = 0.f, xv = 0.f, gb = 0.f;
  if (g == 0) { rr = rin[row]; hv = h2[(size_t)row * DM + n]; xv = x1[(size_t)row * DM + n]; gb = mod[5 * DM + n]; }
  float a = 0.f;
#pragma unroll
  for (int j = 0; j < 18; ++j) a += v[j];
  red[g][o] = a;
  __syncthreads();
  if (g == 0) {
    float y = 0.f;
#pragma unroll
    for (int k = 0; k < 8; ++k) y += red[k][o];
    const float sg = 1.f / (1.f + __expf(-rr));
    out[(size_t)row * DM + n] = f2bf(xv + gb * (hv + y * sg));
  }
}


extern "C" void kernel_launch(void* const* d_in, const int* in_sizes, int n_in, void* d_out, int out_size, void* d_ws, size_t ws_size,
                              hipStream_t stream) {
  const unsigned short* x    = (const unsigned short*)d_in[0];
  const unsigned short* wr   = (const unsigned short*)d_in[6];
  const unsigned short* wg   = (const unsigned short*)d_in[7];
  const unsigned short* wu   = (const unsigned short*)d_in[8];
  const unsigned short* wd   = (const unsigned short*)d_in[9];
  const unsigned short* c    = (const unsigned short*)d_in[10];
  const unsigned short* wmod = (const unsigned short*)d_in[11];
  char* ws = (char*)d_ws;
  float* mod = (float*)(ws + WS_MOD); int* sel = (int*)(ws + WS_CNT); float* rbuf = (float*)(ws + WS_R);
  float* h2 = (float*)(ws + WS_H2); float* yp = (float*)(ws + WS_YP); float* x1 = (float*)(ws + WS_X1);
  unsigned short* out = (unsigned short*)d_out;

  hipFuncSetAttribute((const void*)k_attn, hipFuncAttributeMaxDynamicSharedMemorySize, 163840);
  k_mod<<<108, 256, 0, stream>>>(c, wmod, mod);
  k_attn<<<256, 256, 163840, stream>>>(d_in[0], d_in[3], d_in[1], d_in[2], d_in[4], mod, ws + WS_PART, ws + WS_ST);
  k_post<<<256, 576, 0, stream>>>(ws + WS_PART, x, mod, wr, out, h2, rbuf, x1);
  k_moe1<<<MOE_BLK, 256, 0, stream>>>(rbuf, h2, wg, wu, wd, yp, sel);
  k_moe2<<<36, 256, 0, stream>>>(yp, sel, rbuf, h2, mod, out, x1);
}
```

```cpp
#include <hip/hip_runtime.h>
#include <stdint.h>

typedef short  bf16x8 __attribute__((ext_vector_type(8)));
typedef float  f32x4  __attribute__((ext_vector_type(4)));
typedef unsigned u32x4 __attribute__((ext_vector_type(4)));
typedef unsigned u32x2 __attribute__((ext_vector_type(2)));

constexpr int SQ = 2048, DM = 576, HKV = 16, FF = 4608;

__device__ __forceinline__ float bf2f(unsigned short u) { return __uint_as_float(((unsigned)u) << 16); }
__device__ __forceinline__ float bflo(unsigned u) { return __uint_as_float(u << 16); }
__device__ __forceinline__ float bfhi(unsigned u) { return __uint_as_float(u & 0xffff0000u); }
__device__ __forceinline__ unsigned cvtpk(float lo, float hi) {
  unsigned r; asm("v_cvt_pk_bf16_f32 %0, %1, %2" : "=v"(r) : "v"(lo), "v"(hi)); return r;
}
__device__ __forceinline__ unsigned short f2bf(float f) { return (unsigned short)(cvtpk(f, f) & 0xffffu); }
__device__ __forceinline__ float rbf(float f) { return bflo(cvtpk(f, f)); }

constexpr size_t WS_MOD  = 0;
constexpr size_t WS_CNT  = 16384;
constexpr size_t WS_R    = WS_CNT + 4096;
constexpr size_t WS_GRAN = 32768;
constexpr size_t WS_EPOCH = 65536;
constexpr size_t WS_ST   = 69632;
constexpr size_t WS_PART = 139264;
constexpr size_t WS_H2   = WS_PART + (size_t)HKV * SQ * DM * 2;
constexpr size_t WS_YP   = WS_H2 + (size_t)SQ * DM * 4;
constexpr size_t WS_X1   = WS_YP + (size_t)256 * 2 * DM * 4;
constexpr size_t WS_END  = WS_X1 + (size_t)SQ * DM * 4;

#define KST0(k)
#define KST1(k)
__global__ __launch_bounds__(256, 1) void k_attn(const void* x, const void* wq, const void* kc, const void* vc, const void* wo, const void* c, const void* wmod, void* ws,
                                                 const void* wg, const void* wu, const void* wd, void* out) {
  extern __shared__ __attribute__((aligned(16))) char lds[];
  const unsigned wid = __builtin_amdgcn_readfirstlane(threadIdx.x >> 6);
  const unsigned ldsb = (unsigned)(size_t)lds;
  asm volatile(
"AT_start:\n"
"s_load_dwordx16 s[64:79], %0, 0\n"
"s_load_dwordx8 s[80:87], %0, 64\n"
"s_mov_b32 s42, %1\n"
"s_mov_b32 s41, %2\n"
"s_mov_b32 s40, %3\n"
"s_mov_b32 s45, 0\n"
"v_mbcnt_lo_u32_b32 v248, -1, 0\n"
"v_mbcnt_hi_u32_b32 v248, -1, v248\n"
"s_and_b32 s56, s42, 3\n"
"s_lshr_b32 s57, s42, 3\n"
"s_and_b32 s58, s57, 3\n"
"s_lshl_b32 s56, s56, 2\n"
"s_add_u32 s43, s56, s58\n"
"s_lshr_b32 s44, s57, 2\n"
"s_and_b32 s56, s42, 7\n"
"s_lshr_b32 s56, s56, 2\n"
"s_lshl_b32 s56, s56, 3\n"
"s_add_u32 s44, s44, s56\n"
"s_lshr_b32 s56, s42, 5\n"
"s_lshl_b32 s56, s56, 3\n"
"s_and_b32 s58, s42, 7\n"
"s_add_u32 s56, s56, s58\n"
"s_lshl_b32 s56, s56, 2\n"
"s_and_b32 s57, s57, 3\n"
"s_add_u32 s55, s56, s57\n"
"v_lshrrev_b32 v226, 4, v248\n"
"v_bfe_u32 v227, v248, 2, 2\n"
"v_and_b32 v228, 3, v248\n"
"v_and_b32 v229, 15, v248\n"
"v_lshrrev_b32 v230, 3, v248\n"
"v_and_b32 v231, 7, v248\n"
"s_lshl_b32 s56, s41, 6\n"
"v_add_u32 v234, s56, v248\n"
"v_lshrrev_b32 v232, 1, v234\n"
"v_and_b32 v233, 1, v234\n"
"v_lshlrev_b32 v236, 1, v232\n"
"v_mul_u32_u24 v232, 6912, v232\n"
"v_lshl_add_u32 v235, v233, 4, v232\n"
"s_lshl_b32 s56, s55, 5\n"
"s_add_u32 s57, s56, 884736\n"
"s_add_u32 s58, s56, 1769472\n"
"s_add_u32 s59, s56, 2654208\n"
"s_add_u32 s60, s56, 3538944\n"
"v_lshrrev_b32 v232, 1, v230\n"
"v_xor_b32 v232, v232, v231\n"
"v_mul_u32_u24 v233, 1152, v230\n"
"v_lshl_add_u32 v222, v232, 4, v233\n"
"v_xor_b32 v232, 4, v232\n"
"v_lshl_add_u32 v223, v232, 4, v233\n"
"s_lshl_b32 s61, s44, 2\n"
"s_add_u32 s61, s61, s41\n"
"s_mul_i32 s46, s61, 36864\n"
"s_add_u32 s47, s46, 9216\n"
"s_add_u32 s48, s46, 18432\n"
"s_add_u32 s49, s46, 27648\n"
"s_mul_i32 s54, s61, 36864\n"
"s_mul_i32 s63, s41, 36864\n"
"s_add_u32 s63, s63, s40\n"
"s_cmp_lt_u32 s55, 216\n"
"s_cselect_b32 s10, 3981312, 0\n"
"s_getpc_b64 s[28:29]\n"
"AT_pc0:\n"
"s_sub_u32 s28, s28, AT_pc0-AT_start\n"
"s_subb_u32 s29, s29, 0\n"
"s_and_b32 s29, s29, 0xffff\n"
"s_mov_b32 s30, AT_end-AT_start\n"
"s_mov_b32 s31, 0x20000\n"
"s_cmp_ge_u32 s42, 224\n"
"s_cselect_b32 s30, s30, 0\n"
"s_lshr_b32 s52, s42, 3\n"
"s_lshl_b32 s52, s52, 2\n"
"s_add_u32 s52, s52, s41\n"
"s_and_b32 s52, s52, 15\n"
"s_lshl_b32 s52, s52, 12\n"
"v_lshlrev_b32 v214, 6, v248\n"
"buffer_load_dword v215, v214, s[28:31], s52 offen\n"
"s_add_u32 s52, s52, 65536\n"
"buffer_load_dword v215, v214, s[28:31], s52 offen\n"
"s_waitcnt lgkmcnt(0)\n"
"s_load_dword s17, s[78:79], 65536\n"
"s_mov_b32 s8, s76\n"
"s_and_b32 s9, s77, 0xffff\n"
"s_mov_b32 s11, 0x20000\n"
"s_mov_b32 s12, s74\n"
"s_and_b32 s13, s75, 0xffff\n"
"s_mov_b32 s14, 1152\n"
"s_mov_b32 s15, 0x20000\n"
"buffer_load_ushort v164, v236, s[12:15], 0 offen offset:0\n"
"buffer_load_ushort v165, v236, s[12:15], 0 offen offset:256\n"
"buffer_load_ushort v166, v236, s[12:15], 0 offen offset:512\n"
"buffer_load_ushort v167, v236, s[12:15], 0 offen offset:768\n"
"buffer_load_ushort v168, v236, s[12:15], 0 offen offset:1024\n"
"buffer_load_dwordx4 v[144:147], v235, s[8:11], s56 offen\n"
"buffer_load_dwordx4 v[148:151], v235, s[8:11], s57 offen\n"
"buffer_load_dwordx4 v[152:155], v235, s[8:11], s58 offen\n"
"buffer_load_dwordx4 v[156:159], v235, s[8:11], s59 offen\n"
"buffer_load_dwordx4 v[160:163], v235, s[8:11], s60 offen\n"
"s_mov_b32 s20, s64\n"
"s_and_b32 s21, s65, 0xffff\n"
"s_mov_b32 s22, 2359296\n"
"s_mov_b32 s23, 0x20000\n"
"s_mov_b32 m0, s63\n"
"s_nop 0\n"
"buffer_load_dwordx4 v222, s[20:23], s46 offen offset:0 lds\n"
"s_add_u32 m0, s63, 1024\n"
"s_nop 0\n"
"buffer_load_dwordx4 v223, s[20:23], s47 offen offset:0 lds\n"
"s_add_u32 m0, s63, 2048\n"
"s_nop 0\n"
"buffer_load_dwordx4 v222, s[20:23], s48 offen offset:0 lds\n"
"s_add_u32 m0, s63, 3072\n"
"s_nop 0\n"
"buffer_load_dwordx4 v223, s[20:23], s49 offen offset:0 lds\n"
"s_add_u32 m0, s63, 3968\n"
"s_nop 0\n"
"buffer_load_dwordx4 v222, s[20:23], s46 offen offset:128 lds\n"
"s_add_u32 m0, s63, 4992\n"
"s_nop 0\n"
"buffer_load_dwordx4 v223, s[20:23], s47 offen offset:128 lds\n"
"s_add_u32 m0, s63, 6016\n"
"s_nop 0\n"
"buffer_load_dwordx4 v222, s[20:23], s48 offen offset:128 lds\n"
"s_add_u32 m0, s63, 7040\n"
"s_nop 0\n"
"buffer_load_dwordx4 v223, s[20:23], s49 offen offset:128 lds\n"
"s_mov_b32 s24, s66\n"
"s_and_b32 s25, s67, 0xffff\n"
"s_mov_b32 s26, 4718592\n"
"s_mov_b32 s27, 0x20000\n"
"s_mov_b32 s36, s78\n"
"s_and_b32 s37, s79, 0xffff\n"
"s_mov_b32 s38, 65536\n"
"s_mov_b32 s39, 0x20000\n"
"s_mul_i32 s56, s43, 294912\n"
"s_add_u32 s28, s72, s56\n"
"s_addc_u32 s29, s73, 0\n"
"s_and_b32 s29, s29, 0xffff\n"
"s_mov_b32 s30, 294912\n"
"s_mov_b32 s31, 0x20000\n"
"s_mul_i32 s56, s43, 2359296\n"
"s_add_u32 s56, s56, 139264\n"
"s_add_u32 s32, s78, s56\n"
"s_addc_u32 s33, s79, 0\n"
"s_and_b32 s33, s33, 0xffff\n"
"s_mov_b32 s34, 2359296\n"
"s_mov_b32 s35, 0x20000\n"
"s_lshl_b32 s16, s41, 12\n"
"s_add_u32 s18, s40, s16\n"
"s_lshl_b32 s56, s41, 13\n"
"s_add_u32 s53, s40, s56\n"
"s_mul_i32 s52, s41, 73728\n"
"s_lshl_b32 s50, s41, 17\n"
"s_lshl_b32 s56, s43, 9\n"
"s_add_u32 s50, s50, s56\n"
"s_lshl_b32 s56, s41, 11\n"
"s_add_u32 s51, s40, s56\n"
"s_mov_b32 s61, 32768\n"
"s_mov_b32 s62, 37376\n"
"v_lshlrev_b32 v232, 13, v227\n"
"v_lshl_add_u32 v232, v226, 6, v232\n"
"v_lshl_add_u32 v217, v228, 4, v232\n"
"v_and_b32 v232, 2, v227\n"
"v_xor_b32 v232, v232, v226\n"
"v_lshlrev_b32 v233, 10, v227\n"
"v_lshl_add_u32 v233, v228, 6, v233\n"
"v_lshl_add_u32 v233, v232, 4, v233\n"
"v_add_u32 v240, s40, v233\n"
"v_add_u32 v241, 0x10000, v240\n"
"v_lshlrev_b32 v233, 10, v226\n"
"v_lshl_add_u32 v233, v227, 6, v233\n"
"v_lshl_add_u32 v233, v228, 3, v233\n"
"v_add_u32 v233, s40, v233\n"
"v_and_b32 v232, 1, v226\n"
"v_lshl_add_u32 v242, v232, 5, v233\n"
"v_xor_b32 v243, 32, v242\n"
"v_add_u32 v244, 0x10000, v242\n"
"v_add_u32 v245, 0x10000, v243\n"
"v_lshlrev_b32 v233, 8, v227\n"
"v_lshl_add_u32 v233, v226, 6, v233\n"
"v_lshl_add_u32 v246, v228, 4, v233\n"
"v_xor_b32 v247, 32, v246\n"
"v_mul_u32_u24 v233, 16392, v232\n"
"v_lshrrev_b32 v232, 1, v226\n"
"v_lshl_add_u32 v233, v232, 11, v233\n"
"v_lshl_add_u32 v233, v227, 6, v233\n"
"v_lshl_add_u32 v233, v228, 4, v233\n"
"v_add_u32 v218, s40, v233\n"
"v_add_u32 v219, 65568, v218\n"
"v_lshrrev_b32 v232, 1, v229\n"
"v_xor_b32 v232, v232, v226\n"
"v_lshlrev_b32 v233, 7, v229\n"
"v_lshl_add_u32 v233, v232, 4, v233\n"
"v_add_u32 v220, s63, v233\n"
"v_xor_b32 v221, 64, v220\n"
"s_add_u32 s56, s40, 148480\n"
"v_lshl_add_u32 v224, v226, 4, s56\n"
"v_lshrrev_b32 v232, 3, v234\n"
"v_lshlrev_b32 v233, 8, v232\n"
"v_and_b32 v232, 1, v234\n"
"v_lshl_add_u32 v233, v232, 6, v233\n"
"v_bfe_u32 v232, v234, 1, 2\n"
"v_lshl_add_u32 v233, v232, 4, v233\n"
"v_add_u32 v216, s56, v233\n"
"v_lshlrev_b32 v225, 5, v234\n"
"v_mov_b32 v232, 0x40000000\n"
"v_cmp_gt_u32 vcc, 144, v234\n"
"s_nop 1\n"
"v_cndmask_b32 v225, v232, v225, vcc\n"
"s_lshl_b32 s56, s41, 8\n"
"s_add_u32 s57, s40, 147456\n"
"s_add_u32 s56, s56, s57\n"
"v_and_b32 v232, 1, v248\n"
"v_lshlrev_b32 v232, 5, v232\n"
"v_lshl_add_u32 v232, v226, 6, v232\n"
"v_add_u32 v238, s56, v232\n"
"v_lshlrev_b32 v232, 8, v226\n"
"v_lshl_add_u32 v232, v229, 2, v232\n"
"v_add_u32 v239, s57, v232\n"
"v_mov_b32 v237, 0xffff0000\n"
"v_mov_b32 v176, 0\n"
"v_mov_b32 v177, 0\n"
"v_mov_b32 v178, 0\n"
"v_mov_b32 v179, 0\n"
"v_mov_b32 v180, 0\n"
"v_mov_b32 v181, 0\n"
"v_mov_b32 v182, 0\n"
"v_mov_b32 v183, 0\n"
"s_waitcnt vmcnt(13)\n"
"v_lshlrev_b32 v164, 16, v164\n"
"v_mul_f32 v184, 0xbfb8aa3b, v164\n"
"v_lshlrev_b32 v165, 16, v165\n"
"v_mul_f32 v185, 0xbfb8aa3b, v165\n"
"v_lshlrev_b32 v166, 16, v166\n"
"v_mul_f32 v186, 0xbfb8aa3b, v166\n"
"v_lshlrev_b32 v167, 16, v167\n"
"v_mul_f32 v187, 0xbfb8aa3b, v167\n"
"v_lshlrev_b32 v168, 16, v168\n"
"v_mul_f32 v188, 0xbfb8aa3b, v168\n"
"v_exp_f32 v184, v184\n"
"s_add_u32 m0, s63, 7936\n"
"s_nop 0\n"
"buffer_load_dwordx4 v222, s[20:23], s46 offen offset:256 lds\n"
"v_exp_f32 v185, v185\n"
"v_exp_f32 v186, v186\n"
"v_exp_f32 v187, v187\n"
"v_exp_f32 v188, v188\n"
"v_add_f32 v184, 1.0, v184\n"
"v_add_f32 v185, 1.0, v185\n"
"v_add_f32 v186, 1.0, v186\n"
"v_add_f32 v187, 1.0, v187\n"
"v_add_f32 v188, 1.0, v188\n"
"v_rcp_f32 v184, v184\n"
"v_rcp_f32 v185, v185\n"
"v_rcp_f32 v186, v186\n"
"s_add_u32 m0, s63, 8960\n"
"s_nop 0\n"
"buffer_load_dwordx4 v223, s[20:23], s47 offen offset:256 lds\n"
"v_rcp_f32 v187, v187\n"
"v_rcp_f32 v188, v188\n"
"v_mul_f32 v164, v164, v184\n"
"v_mul_f32 v165, v165, v185\n"
"v_mul_f32 v166, v166, v186\n"
"v_mul_f32 v167, v167, v187\n"
"v_mul_f32 v168, v168, v188\n"
"v_cvt_pk_bf16_f32 v164, v164, v164\n"
"v_and_b32 v164, v237, v164\n"
"v_cvt_pk_bf16_f32 v165, v165, v165\n"
"v_and_b32 v165, v237, v165\n"
"v_cvt_pk_bf16_f32 v166, v166, v166\n"
"s_add_u32 m0, s63, 9984\n"
"s_nop 0\n"
"buffer_load_dwordx4 v222, s[20:23], s48 offen offset:256 lds\n"
"v_and_b32 v166, v237, v166\n"
"v_cvt_pk_bf16_f32 v167, v167, v167\n"
"v_and_b32 v167, v237, v167\n"
"v_cvt_pk_bf16_f32 v168, v168, v168\n"
"v_and_b32 v168, v237, v168\n"
"s_waitcnt vmcnt(15)\n"
"v_lshlrev_b32 v184, 16, v144\n"
"v_and_b32 v185, v237, v144\n"
"v_lshlrev_b32 v186, 16, v145\n"
"v_and_b32 v187, v237, v145\n"
"v_lshlrev_b32 v188, 16, v146\n"
"v_and_b32 v189, v237, v146\n"
"s_add_u32 m0, s63, 11008\n"
"s_nop 0\n"
"buffer_load_dwordx4 v223, s[20:23], s49 offen offset:256 lds\n"
"v_lshlrev_b32 v190, 16, v147\n"
"v_and_b32 v191, v237, v147\n"
"v_fmac_f32 v176, v164, v184\n"
"v_fmac_f32 v177, v164, v185\n"
"v_fmac_f32 v178, v164, v186\n"
"v_fmac_f32 v179, v164, v187\n"
"v_fmac_f32 v180, v164, v188\n"
"v_fmac_f32 v181, v164, v189\n"
"v_fmac_f32 v182, v164, v190\n"
"v_fmac_f32 v183, v164, v191\n"
"s_waitcnt vmcnt(15)\n"
"v_lshlrev_b32 v184, 16, v148\n"
"s_add_u32 m0, s63, 11904\n"
"s_nop 0\n"
"buffer_load_dwordx4 v222, s[20:23], s46 offen offset:384 lds\n"
"v_and_b32 v185, v237, v148\n"
"v_lshlrev_b32 v186, 16, v149\n"
"v_and_b32 v187, v237, v149\n"
"v_lshlrev_b32 v188, 16, v150\n"
"v_and_b32 v189, v237, v150\n"
"v_lshlrev_b32 v190, 16, v151\n"
"v_and_b32 v191, v237, v151\n"
"v_fmac_f32 v176, v165, v184\n"
"v_fmac_f32 v177, v165, v185\n"
"v_fmac_f32 v178, v165, v186\n"
"v_fmac_f32 v179, v165, v187\n"
"v_fmac_f32 v180, v165, v188\n"
"s_add_u32 m0, s63, 12928\n"
"s_nop 0\n"
"buffer_load_dwordx4 v223, s[20:23], s47 offen offset:384 lds\n"
"v_fmac_f32 v181, v165, v189\n"
"v_fmac_f32 v182, v165, v190\n"
"v_fmac_f32 v183, v165, v191\n"
"s_waitcnt vmcnt(16)\n"
"v_lshlrev_b32 v184, 16, v152\n"
"v_and_b32 v185, v237, v152\n"
"v_lshlrev_b32 v186, 16, v153\n"
"v_and_b32 v187, v237, v153\n"
"v_lshlrev_b32 v188, 16, v154\n"
"v_and_b32 v189, v237, v154\n"
"v_lshlrev_b32 v190, 16, v155\n"
"v_and_b32 v191, v237, v155\n"
"s_add_u32 m0, s63, 13952\n"
"s_nop 0\n"
"buffer_load_dwordx4 v222, s[20:23], s48 offen offset:384 lds\n"
"v_fmac_f32 v176, v166, v184\n"
"v_fmac_f32 v177, v166, v185\n"
"v_fmac_f32 v178, v166, v186\n"
"v_fmac_f32 v179, v166, v187\n"
"v_fmac_f32 v180, v166, v188\n"
"v_fmac_f32 v181, v166, v189\n"
"v_fmac_f32 v182, v166, v190\n"
"v_fmac_f32 v183, v166, v191\n"
"s_waitcnt vmcnt(16)\n"
"v_lshlrev_b32 v184, 16, v156\n"
"v_and_b32 v185, v237, v156\n"
"v_lshlrev_b32 v186, 16, v157\n"
"s_add_u32 m0, s63, 14976\n"
"s_nop 0\n"
"buffer_load_dwordx4 v223, s[20:23], s49 offen offset:384 lds\n"
"v_and_b32 v187, v237, v157\n"
"v_lshlrev_b32 v188, 16, v158\n"
"v_and_b32 v189, v237, v158\n"
"v_lshlrev_b32 v190, 16, v159\n"
"v_and_b32 v191, v237, v159\n"
"v_fmac_f32 v176, v167, v184\n"
"v_fmac_f32 v177, v167, v185\n"
"v_fmac_f32 v178, v167, v186\n"
"v_fmac_f32 v179, v167, v187\n"
"v_fmac_f32 v180, v167, v188\n"
"v_fmac_f32 v181, v167, v189\n"
"v_fmac_f32 v182, v167, v190\n"
"s_add_u32 m0, s63, 15872\n"
"s_nop 0\n"
"buffer_load_dwordx4 v222, s[20:23], s46 offen offset:512 lds\n"
"v_fmac_f32 v183, v167, v191\n"
"s_waitcnt vmcnt(17)\n"
"v_lshlrev_b32 v184, 16, v160\n"
"v_and_b32 v185, v237, v160\n"
"v_lshlrev_b32 v186, 16, v161\n"
"v_and_b32 v187, v237, v161\n"
"v_lshlrev_b32 v188, 16, v162\n"
"v_and_b32 v189, v237, v162\n"
"v_lshlrev_b32 v190, 16, v163\n"
"v_and_b32 v191, v237, v163\n"
"v_fmac_f32 v176, v168, v184\n"
"v_fmac_f32 v177, v168, v185\n"
"s_add_u32 m0, s63, 16896\n"
"s_nop 0\n"
"buffer_load_dwordx4 v223, s[20:23], s47 offen offset:512 lds\n"
"v_fmac_f32 v178, v168, v186\n"
"v_fmac_f32 v179, v168, v187\n"
"v_fmac_f32 v180, v168, v188\n"
"v_fmac_f32 v181, v168, v189\n"
"v_fmac_f32 v182, v168, v190\n"
"v_fmac_f32 v183, v168, v191\n"
"v_add_f32_dpp v176, v176, v176 row_ror:2 row_mask:0xf bank_mask:0xf\n"
"v_add_f32_dpp v177, v177, v177 row_ror:2 row_mask:0xf bank_mask:0xf\n"
"v_add_f32_dpp v178, v178, v178 row_ror:2 row_mask:0xf bank_mask:0xf\n"
"v_add_f32_dpp v179, v179, v179 row_ror:2 row_mask:0xf bank_mask:0xf\n"
"v_add_f32_dpp v180, v180, v180 row_ror:2 row_mask:0xf bank_mask:0xf\n"
"v_add_f32_dpp v181, v181, v181 row_ror:2 row_mask:0xf bank_mask:0xf\n"
"s_add_u32 m0, s63, 17920\n"
"s_nop 0\n"
"buffer_load_dwordx4 v222, s[20:23], s48 offen offset:512 lds\n"
"v_add_f32_dpp v182, v182, v182 row_ror:2 row_mask:0xf bank_mask:0xf\n"
"v_add_f32_dpp v183, v183, v183 row_ror:2 row_mask:0xf bank_mask:0xf\n"
"v_add_f32_dpp v176, v176, v176 row_ror:4 row_mask:0xf bank_mask:0xf\n"
"v_add_f32_dpp v177, v177, v177 row_ror:4 row_mask:0xf bank_mask:0xf\n"
"v_add_f32_dpp v178, v178, v178 row_ror:4 row_mask:0xf bank_mask:0xf\n"
"v_add_f32_dpp v179, v179, v179 row_ror:4 row_mask:0xf bank_mask:0xf\n"
"v_add_f32_dpp v180, v180, v180 row_ror:4 row_mask:0xf bank_mask:0xf\n"
"v_add_f32_dpp v181, v181, v181 row_ror:4 row_mask:0xf bank_mask:0xf\n"
"v_add_f32_dpp v182, v182, v182 row_ror:4 row_mask:0xf bank_mask:0xf\n"
"v_add_f32_dpp v183, v183, v183 row_ror:4 row_mask:0xf bank_mask:0xf\n"
"v_add_f32_dpp v176, v176, v176 row_ror:8 row_mask:0xf bank_mask:0xf\n"
"v_add_f32_dpp v177, v177, v177 row_ror:8 row_mask:0xf bank_mask:0xf\n"
"s_add_u32 m0, s63, 18944\n"
"s_nop 0\n"
"buffer_load_dwordx4 v223, s[20:23], s49 offen offset:512 lds\n"
"v_add_f32_dpp v178, v178, v178 row_ror:8 row_mask:0xf bank_mask:0xf\n"
"v_add_f32_dpp v179, v179, v179 row_ror:8 row_mask:0xf bank_mask:0xf\n"
"v_add_f32_dpp v180, v180, v180 row_ror:8 row_mask:0xf bank_mask:0xf\n"
"v_add_f32_dpp v181, v181, v181 row_ror:8 row_mask:0xf bank_mask:0xf\n"
"v_add_f32_dpp v182, v182, v182 row_ror:8 row_mask:0xf bank_mask:0xf\n"
"v_add_f32_dpp v183, v183, v183 row_ror:8 row_mask:0xf bank_mask:0xf\n"
"s_mov_b32 exec_lo, 0x00030003\n"
"s_mov_b32 exec_hi, 0x00030003\n"
"ds_write_b128 v238, v[176:179]\n"
"ds_write_b128 v238, v[180:183] offset:16\n"
"s_mov_b64 exec, -1\n"
"s_waitcnt lgkmcnt(0)\n"
"s_barrier\n"
"ds_read_b32 v184, v239 offset:0\n"
"ds_read_b32 v185, v239 offset:64\n"
"ds_read_b32 v186, v239 offset:128\n"
"ds_read_b32 v187, v239 offset:192\n"
"s_add_u32 s17, s17, 1\n"
"s_lshl_b32 s56, s55, 7\n"
"s_add_u32 s56, s56, 32768\n"
"s_lshl_b32 s57, s55, 6\n"
"s_lshl_b32 s58, s41, 2\n"
"s_lshl_b32 s58, 15, s58\n"
"s_cmp_lt_u32 s55, 216\n"
"s_cselect_b32 s57, s57, 0x40000000\n"
"v_lshlrev_b32 v190, 3, v248\n"
"v_lshlrev_b32 v191, 2, v248\n"
"v_mov_b32 v189, s17\n"
"s_waitcnt lgkmcnt(2)\n"
"v_add_f32 v184, v184, v185\n"
"s_waitcnt lgkmcnt(0)\n"
"v_add_f32 v186, v186, v187\n"
"v_add_f32 v184, v184, v186\n"
"v_mov_b32 v185, v184\n"
"s_nop 1\n"
"v_permlane32_swap_b32 v184, v185\n"
"v_add_f32 v184, v184, v185\n"
"v_mov_b32 v185, v184\n"
"s_nop 1\n"
"v_permlane16_swap_b32 v184, v185\n"
"v_add_f32 v184, v184, v185\n"
"v_cvt_pk_bf16_f32 v188, v184, v184\n"
"v_and_b32 v188, v237, v188\n"
"s_mov_b32 exec_lo, s58\n"
"s_mov_b32 exec_hi, 0\n"
"buffer_store_dwordx2 v[188:189], v190, s[36:39], s56 offen sc1\n"
"buffer_store_dword v188, v191, s[36:39], s57 offen\n"
"s_mov_b64 exec, -1\n"
"s_add_u32 m0, s63, 19840\n"
"s_nop 0\n"
"buffer_load_dwordx4 v222, s[20:23], s46 offen offset:640 lds\n"
"s_add_u32 m0, s63, 20864\n"
"s_nop 0\n"
"buffer_load_dwordx4 v223, s[20:23], s47 offen offset:640 lds\n"
"s_add_u32 m0, s63, 21888\n"
"s_nop 0\n"
"buffer_load_dwordx4 v222, s[20:23], s48 offen offset:640 lds\n"
"s_add_u32 m0, s63, 22912\n"
"s_nop 0\n"
"buffer_load_dwordx4 v223, s[20:23], s49 offen offset:640 lds\n"
"s_add_u32 m0, s63, 23808\n"
"s_nop 0\n"
"buffer_load_dwordx4 v222, s[20:23], s46 offen offset:768 lds\n"
"s_add_u32 m0, s63, 24832\n"
"s_nop 0\n"
"buffer_load_dwordx4 v223, s[20:23], s47 offen offset:768 lds\n"
"s_add_u32 m0, s63, 25856\n"
"s_nop 0\n"
"buffer_load_dwordx4 v222, s[20:23], s48 offen offset:768 lds\n"
"s_add_u32 m0, s63, 26880\n"
"s_nop 0\n"
"buffer_load_dwordx4 v223, s[20:23], s49 offen offset:768 lds\n"
"s_add_u32 m0, s63, 27776\n"
"s_nop 0\n"
"buffer_load_dwordx4 v222, s[20:23], s46 offen offset:896 lds\n"
"s_add_u32 m0, s63, 28800\n"
"s_nop 0\n"
"buffer_load_dwordx4 v223, s[20:23], s47 offen offset:896 lds\n"
"s_add_u32 m0, s63, 29824\n"
"s_nop 0\n"
"buffer_load_dwordx4 v222, s[20:23], s48 offen offset:896 lds\n"
"s_add_u32 m0, s63, 30848\n"
"s_nop 0\n"
"buffer_load_dwordx4 v223, s[20:23], s49 offen offset:896 lds\n"
"s_add_u32 m0, s63, 31744\n"
"s_nop 0\n"
"buffer_load_dwordx4 v222, s[20:23], s46 offen offset:1024 lds\n"
"s_add_u32 m0, s63, 32768\n"
"s_nop 0\n"
"buffer_load_dwordx4 v223, s[20:23], s47 offen offset:1024 lds\n"
"s_add_u32 m0, s63, 33792\n"
"s_nop 0\n"
"buffer_load_dwordx4 v222, s[20:23], s48 offen offset:1024 lds\n"
"s_add_u32 m0, s63, 34816\n"
"s_nop 0\n"
"buffer_load_dwordx4 v223, s[20:23], s49 offen offset:1024 lds\n"
"s_lshl_b32 s56, s43, 19\n"
"s_add_u32 s8, s68, s56\n"
"s_addc_u32 s9, s69, 0\n"
"s_and_b32 s9, s9, 0xffff\n"
"s_mov_b32 s10, 524288\n"
"s_mov_b32 s11, 0x20000\n"
"s_add_u32 s12, s70, s56\n"
"s_addc_u32 s13, s71, 0\n"
"s_and_b32 s13, s13, 0xffff\n"
"s_mov_b32 s14, 524288\n"
"s_mov_b32 s15, 0x20000\n"
"v_mov_b32 v236, 0x3f803f80\n"
"v_mov_b32 v200, 0\n"
"v_mov_b32 v202, 0\n"
"v_mov_b32 v204, 0\n"
"v_mov_b32 v206, 0\n"
"v_mov_b32 v208, 0\n"
"v_mov_b32 v210, 0\n"
"v_mov_b32 v212, 0\n"
"v_mov_b32 v214, 0\n"
"s_waitcnt vmcnt(34)\n"
"ds_read_b128 v[0:3], v220 offset:0\n"
"ds_read_b128 v[4:7], v220 offset:2048\n"
"ds_read_b128 v[8:11], v221 offset:0\n"
"ds_read_b128 v[12:15], v221 offset:2048\n"
"s_waitcnt vmcnt(30)\n"
"ds_read_b128 v[16:19], v220 offset:4096\n"
"ds_read_b128 v[20:23], v220 offset:6144\n"
"ds_read_b128 v[24:27], v221 offset:4096\n"
"ds_read_b128 v[28:31], v221 offset:6144\n"
"s_waitcnt lgkmcnt(7)\n"
"v_dot2c_f32_bf16 v200, v0, v236\n"
"v_dot2c_f32_bf16 v202, v0, v0\n"
"s_waitcnt lgkmcnt(6)\n"
"v_dot2c_f32_bf16 v204, v4, v236\n"
"v_dot2c_f32_bf16 v206, v4, v4\n"
"s_waitcnt lgkmcnt(5)\n"
"v_dot2c_f32_bf16 v208, v8, v236\n"
"v_dot2c_f32_bf16 v210, v8, v8\n"
"s_waitcnt lgkmcnt(4)\n"
"v_dot2c_f32_bf16 v212, v12, v236\n"
"v_dot2c_f32_bf16 v214, v12, v12\n"
"v_dot2c_f32_bf16 v200, v1, v236\n"
"v_dot2c_f32_bf16 v202, v1, v1\n"
"v_dot2c_f32_bf16 v204, v5, v236\n"
"v_dot2c_f32_bf16 v206, v5, v5\n"
"v_dot2c_f32_bf16 v208, v9, v236\n"
"v_dot2c_f32_bf16 v210, v9, v9\n"
"v_dot2c_f32_bf16 v212, v13, v236\n"
"v_dot2c_f32_bf16 v214, v13, v13\n"
"v_dot2c_f32_bf16 v200, v2, v236\n"
"v_dot2c_f32_bf16 v202, v2, v2\n"
"v_dot2c_f32_bf16 v204, v6, v236\n"
"v_dot2c_f32_bf16 v206, v6, v6\n"
"v_dot2c_f32_bf16 v208, v10, v236\n"
"v_dot2c_f32_bf16 v210, v10, v10\n"
"v_dot2c_f32_bf16 v212, v14, v236\n"
"v_dot2c_f32_bf16 v214, v14, v14\n"
"v_dot2c_f32_bf16 v200, v3, v236\n"
"v_dot2c_f32_bf16 v202, v3, v3\n"
"v_dot2c_f32_bf16 v204, v7, v236\n"
"v_dot2c_f32_bf16 v206, v7, v7\n"
"v_dot2c_f32_bf16 v208, v11, v236\n"
"v_dot2c_f32_bf16 v210, v11, v11\n"
"v_dot2c_f32_bf16 v212, v15, v236\n"
"v_dot2c_f32_bf16 v214, v15, v15\n"
"s_waitcnt vmcnt(26)\n"
"ds_read_b128 v[32:35], v220 offset:8192\n"
"ds_read_b128 v[36:39], v220 offset:10240\n"
"ds_read_b128 v[40:43], v221 offset:8192\n"
"ds_read_b128 v[44:47], v221 offset:10240\n"
"s_waitcnt lgkmcnt(7)\n"
"v_dot2c_f32_bf16 v200, v16, v236\n"
"v_dot2c_f32_bf16 v202, v16, v16\n"
"s_waitcnt lgkmcnt(6)\n"
"v_dot2c_f32_bf16 v204, v20, v236\n"
"v_dot2c_f32_bf16 v206, v20, v20\n"
"s_waitcnt lgkmcnt(5)\n"
"v_dot2c_f32_bf16 v208, v24, v236\n"
"v_dot2c_f32_bf16 v210, v24, v24\n"
"s_waitcnt lgkmcnt(4)\n"
"v_dot2c_f32_bf16 v212, v28, v236\n"
"v_dot2c_f32_bf16 v214, v28, v28\n"
"v_dot2c_f32_bf16 v200, v17, v236\n"
"v_dot2c_f32_bf16 v202, v17, v17\n"
"v_dot2c_f32_bf16 v204, v21, v236\n"
"v_dot2c_f32_bf16 v206, v21, v21\n"
"v_dot2c_f32_bf16 v208, v25, v236\n"
"v_dot2c_f32_bf16 v210, v25, v25\n"
"v_dot2c_f32_bf16 v212, v29, v236\n"
"v_dot2c_f32_bf16 v214, v29, v29\n"
"v_dot2c_f32_bf16 v200, v18, v236\n"
"v_dot2c_f32_bf16 v202, v18, v18\n"
"v_dot2c_f32_bf16 v204, v22, v236\n"
"v_dot2c_f32_bf16 v206, v22, v22\n"
"v_dot2c_f32_bf16 v208, v26, v236\n"
"v_dot2c_f32_bf16 v210, v26, v26\n"
"v_dot2c_f32_bf16 v212, v30, v236\n"
"v_dot2c_f32_bf16 v214, v30, v30\n"
"v_dot2c_f32_bf16 v200, v19, v236\n"
"v_dot2c_f32_bf16 v202, v19, v19\n"
"v_dot2c_f32_bf16 v204, v23, v236\n"
"v_dot2c_f32_bf16 v206, v23, v23\n"
"v_dot2c_f32_bf16 v208, v27, v236\n"
"v_dot2c_f32_bf16 v210, v27, v27\n"
"v_dot2c_f32_bf16 v212, v31, v236\n"
"v_dot2c_f32_bf16 v214, v31, v31\n"
"s_waitcnt vmcnt(22)\n"
"ds_read_b128 v[48:51], v220 offset:12288\n"
"ds_read_b128 v[52:55], v220 offset:14336\n"
"ds_read_b128 v[56:59], v221 offset:12288\n"
"ds_read_b128 v[60:63], v221 offset:14336\n"
"s_waitcnt lgkmcnt(7)\n"
"v_dot2c_f32_bf16 v200, v32, v236\n"
"v_dot2c_f32_bf16 v202, v32, v32\n"
"s_waitcnt lgkmcnt(6)\n"
"v_dot2c_f32_bf16 v204, v36, v236\n"
"v_dot2c_f32_bf16 v206, v36, v36\n"
"s_waitcnt lgkmcnt(5)\n"
"v_dot2c_f32_bf16 v208, v40, v236\n"
"v_dot2c_f32_bf16 v210, v40, v40\n"
"s_waitcnt lgkmcnt(4)\n"
"v_dot2c_f32_bf16 v212, v44, v236\n"
"v_dot2c_f32_bf16 v214, v44, v44\n"
"v_dot2c_f32_bf16 v200, v33, v236\n"
"v_dot2c_f32_bf16 v202, v33, v33\n"
"v_dot2c_f32_bf16 v204, v37, v236\n"
"v_dot2c_f32_bf16 v206, v37, v37\n"
"v_dot2c_f32_bf16 v208, v41, v236\n"
"v_dot2c_f32_bf16 v210, v41, v41\n"
"v_dot2c_f32_bf16 v212, v45, v236\n"
"v_dot2c_f32_bf16 v214, v45, v45\n"
"v_dot2c_f32_bf16 v200, v34, v236\n"
"v_dot2c_f32_bf16 v202, v34, v34\n"
"v_dot2c_f32_bf16 v204, v38, v236\n"
"v_dot2c_f32_bf16 v206, v38, v38\n"
"v_dot2c_f32_bf16 v208, v42, v236\n"
"v_dot2c_f32_bf16 v210, v42, v42\n"
"v_dot2c_f32_bf16 v212, v46, v236\n"
"v_dot2c_f32_bf16 v214, v46, v46\n"
"v_dot2c_f32_bf16 v200, v35, v236\n"
"v_dot2c_f32_bf16 v202, v35, v35\n"
"v_dot2c_f32_bf16 v204, v39, v236\n"
"v_dot2c_f32_bf16 v206, v39, v39\n"
"v_dot2c_f32_bf16 v208, v43, v236\n"
"v_dot2c_f32_bf16 v210, v43, v43\n"
"v_dot2c_f32_bf16 v212, v47, v236\n"
"v_dot2c_f32_bf16 v214, v47, v47\n"
"s_waitcnt vmcnt(18)\n"
"ds_read_b128 v[64:67], v220 offset:16384\n"
"ds_read_b128 v[68:71], v220 offset:18432\n"
"ds_read_b128 v[72:75], v221 offset:16384\n"
"ds_read_b128 v[76:79], v221 offset:18432\n"
"s_waitcnt lgkmcnt(7)\n"
"v_dot2c_f32_bf16 v200, v48, v236\n"
"v_dot2c_f32_bf16 v202, v48, v48\n"
"s_waitcnt lgkmcnt(6)\n"
"v_dot2c_f32_bf16 v204, v52, v236\n"
"v_dot2c_f32_bf16 v206, v52, v52\n"
"s_waitcnt lgkmcnt(5)\n"
"v_dot2c_f32_bf16 v208, v56, v236\n"
"v_dot2c_f32_bf16 v210, v56, v56\n"
"s_waitcnt lgkmcnt(4)\n"
"v_dot2c_f32_bf16 v212, v60, v236\n"
"v_dot2c_f32_bf16 v214, v60, v60\n"
"v_dot2c_f32_bf16 v200, v49, v236\n"
"v_dot2c_f32_bf16 v202, v49, v49\n"
"v_dot2c_f32_bf16 v204, v53, v236\n"
"v_dot2c_f32_bf16 v206, v53, v53\n"
"v_dot2c_f32_bf16 v208, v57, v236\n"
"v_dot2c_f32_bf16 v210, v57, v57\n"
"v_dot2c_f32_bf16 v212, v61, v236\n"
"v_dot2c_f32_bf16 v214, v61, v61\n"
"v_dot2c_f32_bf16 v200, v50, v236\n"
"v_dot2c_f32_bf16 v202, v50, v50\n"
"v_dot2c_f32_bf16 v204, v54, v236\n"
"v_dot2c_f32_bf16 v206, v54, v54\n"
"v_dot2c_f32_bf16 v208, v58, v236\n"
"v_dot2c_f32_bf16 v210, v58, v58\n"
"v_dot2c_f32_bf16 v212, v62, v236\n"
"v_dot2c_f32_bf16 v214, v62, v62\n"
"v_dot2c_f32_bf16 v200, v51, v236\n"
"v_dot2c_f32_bf16 v202, v51, v51\n"
"v_dot2c_f32_bf16 v204, v55, v236\n"
"v_dot2c_f32_bf16 v206, v55, v55\n"
"v_dot2c_f32_bf16 v208, v59, v236\n"
"v_dot2c_f32_bf16 v210, v59, v59\n"
"v_dot2c_f32_bf16 v212, v63, v236\n"
"v_dot2c_f32_bf16 v214, v63, v63\n"
"s_waitcnt vmcnt(12)\n"
"ds_read_b128 v[80:83], v220 offset:20480\n"
"ds_read_b128 v[84:87], v220 offset:22528\n"
"ds_read_b128 v[88:91], v221 offset:20480\n"
"ds_read_b128 v[92:95], v221 offset:22528\n"
"s_waitcnt lgkmcnt(7)\n"
"v_dot2c_f32_bf16 v200, v64, v236\n"
"v_dot2c_f32_bf16 v202, v64, v64\n"
"s_waitcnt lgkmcnt(6)\n"
"v_dot2c_f32_bf16 v204, v68, v236\n"
"v_dot2c_f32_bf16 v206, v68, v68\n"
"s_waitcnt lgkmcnt(5)\n"
"v_dot2c_f32_bf16 v208, v72, v236\n"
"v_dot2c_f32_bf16 v210, v72, v72\n"
"s_waitcnt lgkmcnt(4)\n"
"v_dot2c_f32_bf16 v212, v76, v236\n"
"v_dot2c_f32_bf16 v214, v76, v76\n"
"v_dot2c_f32_bf16 v200, v65, v236\n"
"v_dot2c_f32_bf16 v202, v65, v65\n"
"v_dot2c_f32_bf16 v204, v69, v236\n"
"v_dot2c_f32_bf16 v206, v69, v69\n"
"v_dot2c_f32_bf16 v208, v73, v236\n"
"v_dot2c_f32_bf16 v210, v73, v73\n"
"v_dot2c_f32_bf16 v212, v77, v236\n"
"v_dot2c_f32_bf16 v214, v77, v77\n"
"v_dot2c_f32_bf16 v200, v66, v236\n"
"v_dot2c_f32_bf16 v202, v66, v66\n"
"v_dot2c_f32_bf16 v204, v70, v236\n"
"v_dot2c_f32_bf16 v206, v70, v70\n"
"v_dot2c_f32_bf16 v208, v74, v236\n"
"v_dot2c_f32_bf16 v210, v74, v74\n"
"v_dot2c_f32_bf16 v212, v78, v236\n"
"v_dot2c_f32_bf16 v214, v78, v78\n"
"v_dot2c_f32_bf16 v200, v67, v236\n"
"v_dot2c_f32_bf16 v202, v67, v67\n"
"v_dot2c_f32_bf16 v204, v71, v236\n"
"v_dot2c_f32_bf16 v206, v71, v71\n"
"v_dot2c_f32_bf16 v208, v75, v236\n"
"v_dot2c_f32_bf16 v210, v75, v75\n"
"v_dot2c_f32_bf16 v212, v79, v236\n"
"v_dot2c_f32_bf16 v214, v79, v79\n"
"s_waitcnt vmcnt(8)\n"
"ds_read_b128 v[96:99], v220 offset:24576\n"
"ds_read_b128 v[100:103], v220 offset:26624\n"
"ds_read_b128 v[104:107], v221 offset:24576\n"
"ds_read_b128 v[108:111], v221 offset:26624\n"
"s_waitcnt lgkmcnt(7)\n"
"v_dot2c_f32_bf16 v200, v80, v236\n"
"v_dot2c_f32_bf16 v202, v80, v80\n"
"s_waitcnt lgkmcnt(6)\n"
"v_dot2c_f32_bf16 v204, v84, v236\n"
"v_dot2c_f32_bf16 v206, v84, v84\n"
"s_waitcnt lgkmcnt(5)\n"
"v_dot2c_f32_bf16 v208, v88, v236\n"
"v_dot2c_f32_bf16 v210, v88, v88\n"
"s_waitcnt lgkmcnt(4)\n"
"v_dot2c_f32_bf16 v212, v92, v236\n"
"v_dot2c_f32_bf16 v214, v92, v92\n"
"v_dot2c_f32_bf16 v200, v81, v236\n"
"v_dot2c_f32_bf16 v202, v81, v81\n"
"v_dot2c_f32_bf16 v204, v85, v236\n"
"v_dot2c_f32_bf16 v206, v85, v85\n"
"v_dot2c_f32_bf16 v208, v89, v236\n"
"v_dot2c_f32_bf16 v210, v89, v89\n"
"v_dot2c_f32_bf16 v212, v93, v236\n"
"v_dot2c_f32_bf16 v214, v93, v93\n"
"v_dot2c_f32_bf16 v200, v82, v236\n"
"v_dot2c_f32_bf16 v202, v82, v82\n"
"v_dot2c_f32_bf16 v204, v86, v236\n"
"v_dot2c_f32_bf16 v206, v86, v86\n"
"v_dot2c_f32_bf16 v208, v90, v236\n"
"v_dot2c_f32_bf16 v210, v90, v90\n"
"v_dot2c_f32_bf16 v212, v94, v236\n"
"v_dot2c_f32_bf16 v214, v94, v94\n"
"v_dot2c_f32_bf16 v200, v83, v236\n"
"v_dot2c_f32_bf16 v202, v83, v83\n"
"v_dot2c_f32_bf16 v204, v87, v236\n"
"v_dot2c_f32_bf16 v206, v87, v87\n"
"v_dot2c_f32_bf16 v208, v91, v236\n"
"v_dot2c_f32_bf16 v210, v91, v91\n"
"v_dot2c_f32_bf16 v212, v95, v236\n"
"v_dot2c_f32_bf16 v214, v95, v95\n"
"s_waitcnt vmcnt(4)\n"
"ds_read_b128 v[112:115], v220 offset:28672\n"
"ds_read_b128 v[116:119], v220 offset:30720\n"
"ds_read_b128 v[120:123], v221 offset:28672\n"
"ds_read_b128 v[124:127], v221 offset:30720\n"
"s_waitcnt lgkmcnt(7)\n"
"v_dot2c_f32_bf16 v200, v96, v236\n"
"v_dot2c_f32_bf16 v202, v96, v96\n"
"s_waitcnt lgkmcnt(6)\n"
"v_dot2c_f32_bf16 v204, v100, v236\n"
"v_dot2c_f32_bf16 v206, v100, v100\n"
"s_waitcnt lgkmcnt(5)\n"
"v_dot2c_f32_bf16 v208, v104, v236\n"
"v_dot2c_f32_bf16 v210, v104, v104\n"
"s_waitcnt lgkmcnt(4)\n"
"v_dot2c_f32_bf16 v212, v108, v236\n"
"v_dot2c_f32_bf16 v214, v108, v108\n"
"v_dot2c_f32_bf16 v200, v97, v236\n"
"v_dot2c_f32_bf16 v202, v97, v97\n"
"v_dot2c_f32_bf16 v204, v101, v236\n"
"v_dot2c_f32_bf16 v206, v101, v101\n"
"v_dot2c_f32_bf16 v208, v105, v236\n"
"v_dot2c_f32_bf16 v210, v105, v105\n"
"v_dot2c_f32_bf16 v212, v109, v236\n"
"v_dot2c_f32_bf16 v214, v109, v109\n"
"v_dot2c_f32_bf16 v200, v98, v236\n"
"v_dot2c_f32_bf16 v202, v98, v98\n"
"v_dot2c_f32_bf16 v204, v102, v236\n"
"v_dot2c_f32_bf16 v206, v102, v102\n"
"v_dot2c_f32_bf16 v208, v106, v236\n"
"v_dot2c_f32_bf16 v210, v106, v106\n"
"v_dot2c_f32_bf16 v212, v110, v236\n"
"v_dot2c_f32_bf16 v214, v110, v110\n"
"v_dot2c_f32_bf16 v200, v99, v236\n"
"v_dot2c_f32_bf16 v202, v99, v99\n"
"v_dot2c_f32_bf16 v204, v103, v236\n"
"v_dot2c_f32_bf16 v206, v103, v103\n"
"v_dot2c_f32_bf16 v208, v107, v236\n"
"v_dot2c_f32_bf16 v210, v107, v107\n"
"v_dot2c_f32_bf16 v212, v111, v236\n"
"v_dot2c_f32_bf16 v214, v111, v111\n"
"s_waitcnt vmcnt(0)\n"
"ds_read_b128 v[128:131], v220 offset:32768\n"
"ds_read_b128 v[132:135], v220 offset:34816\n"
"ds_read_b128 v[136:139], v221 offset:32768\n"
"ds_read_b128 v[140:143], v221 offset:34816\n"
"s_waitcnt lgkmcnt(7)\n"
"v_dot2c_f32_bf16 v200, v112, v236\n"
"v_dot2c_f32_bf16 v202, v112, v112\n"
"s_waitcnt lgkmcnt(6)\n"
"v_dot2c_f32_bf16 v204, v116, v236\n"
"v_dot2c_f32_bf16 v206, v116, v116\n"
"s_waitcnt lgkmcnt(5)\n"
"v_dot2c_f32_bf16 v208, v120, v236\n"
"v_dot2c_f32_bf16 v210, v120, v120\n"
"s_waitcnt lgkmcnt(4)\n"
"v_dot2c_f32_bf16 v212, v124, v236\n"
"v_dot2c_f32_bf16 v214, v124, v124\n"
"v_dot2c_f32_bf16 v200, v113, v236\n"
"v_dot2c_f32_bf16 v202, v113, v113\n"
"v_dot2c_f32_bf16 v204, v117, v236\n"
"v_dot2c_f32_bf16 v206, v117, v117\n"
"v_dot2c_f32_bf16 v208, v121, v236\n"
"v_dot2c_f32_bf16 v210, v121, v121\n"
"v_dot2c_f32_bf16 v212, v125, v236\n"
"v_dot2c_f32_bf16 v214, v125, v125\n"
"v_dot2c_f32_bf16 v200, v114, v236\n"
"v_dot2c_f32_bf16 v202, v114, v114\n"
"v_dot2c_f32_bf16 v204, v118, v236\n"
"v_dot2c_f32_bf16 v206, v118, v118\n"
"v_dot2c_f32_bf16 v208, v122, v236\n"
"v_dot2c_f32_bf16 v210, v122, v122\n"
"v_dot2c_f32_bf16 v212, v126, v236\n"
"v_dot2c_f32_bf16 v214, v126, v126\n"
"v_dot2c_f32_bf16 v200, v115, v236\n"
"v_dot2c_f32_bf16 v202, v115, v115\n"
"v_dot2c_f32_bf16 v204, v119, v236\n"
"v_dot2c_f32_bf16 v206, v119, v119\n"
"v_dot2c_f32_bf16 v208, v123, v236\n"
"v_dot2c_f32_bf16 v210, v123, v123\n"
"v_dot2c_f32_bf16 v212, v127, v236\n"
"v_dot2c_f32_bf16 v214, v127, v127\n"
"s_waitcnt lgkmcnt(0)\n"
"s_barrier\n"
"s_add_u32 m0, s51, 98352\n"
"s_nop 0\n"
"buffer_load_dwordx4 v217, s[24:27], s50 offen offset:0 lds\n"
"s_add_u32 m0, s51, 106288\n"
"s_nop 0\n"
"buffer_load_dwordx4 v217, s[24:27], s50 offen offset:256 lds\n"
"s_add_u32 s50, s50, 32768\n"
"v_dot2c_f32_bf16 v200, v128, v236\n"
"v_dot2c_f32_bf16 v202, v128, v128\n"
"v_dot2c_f32_bf16 v204, v132, v236\n"
"v_dot2c_f32_bf16 v206, v132, v132\n"
"v_dot2c_f32_bf16 v208, v136, v236\n"
"v_dot2c_f32_bf16 v210, v136, v136\n"
"v_dot2c_f32_bf16 v212, v140, v236\n"
"v_dot2c_f32_bf16 v214, v140, v140\n"
"v_dot2c_f32_bf16 v200, v129, v236\n"
"v_dot2c_f32_bf16 v202, v129, v129\n"
"v_dot2c_f32_bf16 v204, v133, v236\n"
"v_dot2c_f32_bf16 v206, v133, v133\n"
"v_dot2c_f32_bf16 v208, v137, v236\n"
"v_dot2c_f32_bf16 v210, v137, v137\n"
"v_dot2c_f32_bf16 v212, v141, v236\n"
"v_dot2c_f32_bf16 v214, v141, v141\n"
"v_dot2c_f32_bf16 v200, v130, v236\n"
"v_dot2c_f32_bf16 v202, v130, v130\n"
"v_dot2c_f32_bf16 v204, v134, v236\n"
"v_dot2c_f32_bf16 v206, v134, v134\n"
"v_dot2c_f32_bf16 v208, v138, v236\n"
"v_dot2c_f32_bf16 v210, v138, v138\n"
"v_dot2c_f32_bf16 v212, v142, v236\n"
"v_dot2c_f32_bf16 v214, v142, v142\n"
"v_dot2c_f32_bf16 v200, v131, v236\n"
"v_dot2c_f32_bf16 v202, v131, v131\n"
"v_dot2c_f32_bf16 v204, v135, v236\n"
"v_dot2c_f32_bf16 v206, v135, v135\n"
"v_dot2c_f32_bf16 v208, v139, v236\n"
"v_dot2c_f32_bf16 v210, v139, v139\n"
"v_dot2c_f32_bf16 v212, v143, v236\n"
"v_dot2c_f32_bf16 v214, v143, v143\n"
"s_nop 2\n"
"v_add_f32 v200, v200, v208\n"
"v_add_f32 v202, v202, v210\n"
"v_add_f32 v204, v204, v212\n"
"v_add_f32 v206, v206, v214\n"
"v_mov_b32 v208, v200\n"
"v_mov_b32 v209, v202\n"
"v_mov_b32 v210, v204\n"
"v_mov_b32 v211, v206\n"
"s_nop 0\n"
"v_permlane32_swap_b32 v200, v208\n"
"v_permlane32_swap_b32 v202, v209\n"
"v_permlane32_swap_b32 v204, v210\n"
"v_permlane32_swap_b32 v206, v211\n"
"v_add_f32 v200, v200, v208\n"
"v_add_f32 v202, v202, v209\n"
"v_add_f32 v204, v204, v210\n"
"v_add_f32 v206, v206, v211\n"
"v_mov_b32 v208, v200\n"
"v_mov_b32 v209, v202\n"
"v_mov_b32 v210, v204\n"
"v_mov_b32 v211, v206\n"
"s_nop 0\n"
"v_permlane16_swap_b32 v200, v208\n"
"v_permlane16_swap_b32 v202, v209\n"
"v_permlane16_swap_b32 v204, v210\n"
"v_permlane16_swap_b32 v206, v211\n"
"v_add_f32 v200, v200, v208\n"
"v_add_f32 v202, v202, v209\n"
"v_add_f32 v204, v204, v210\n"
"v_add_f32 v206, v206, v211\n"
"v_mul_f32 v200, 0x3ae38e39, v200\n"
"v_mul_f32 v202, 0x3ae38e39, v202\n"
"v_fma_f32 v202, -v200, v200, v202\n"
"v_max_f32 v202, 0, v202\n"
"v_add_f32 v202, 0x3727c5ac, v202\n"
"v_mul_f32 v204, 0x3ae38e39, v204\n"
"v_mul_f32 v206, 0x3ae38e39, v206\n"
"v_fma_f32 v206, -v204, v204, v206\n"
"v_max_f32 v206, 0, v206\n"
"v_add_f32 v206, 0x3727c5ac, v206\n"
"v_rsq_f32 v201, v202\n"
"v_rsq_f32 v205, v206\n"
"s_nop 0\n"
"v_mul_f32 v202, v200, v201\n"
"v_sub_f32 v202, 0, v202\n"
"v_mov_b32 v200, v201\n"
"v_mul_f32 v206, v204, v205\n"
"v_sub_f32 v206, 0, v206\n"
"v_mov_b32 v204, v205\n"
"v_lshlrev_b32 v176, 16, v0\n"
"v_and_b32 v177, v237, v0\n"
"v_pk_fma_f32 v[176:177], v[176:177], v[200:201], v[202:203] op_sel_hi:[1,0,0]\n"
"v_cvt_pk_f16_f32 v0, v176, v177\n"
"v_lshlrev_b32 v178, 16, v1\n"
"v_and_b32 v179, v237, v1\n"
"v_pk_fma_f32 v[178:179], v[178:179], v[200:201], v[202:203] op_sel_hi:[1,0,0]\n"
"v_cvt_pk_f16_f32 v1, v178, v179\n"
"v_lshlrev_b32 v180, 16, v2\n"
"v_and_b32 v181, v237, v2\n"
"v_pk_fma_f32 v[180:181], v[180:181], v[200:201], v[202:203] op_sel_hi:[1,0,0]\n"
"v_cvt_pk_f16_f32 v2, v180, v181\n"
"v_lshlrev_b32 v182, 16, v3\n"
"v_and_b32 v183, v237, v3\n"
"v_pk_fma_f32 v[182:183], v[182:183], v[200:201], v[202:203] op_sel_hi:[1,0,0]\n"
"v_cvt_pk_f16_f32 v3, v182, v183\n"
"v_lshlrev_b32 v184, 16, v4\n"
"v_and_b32 v185, v237, v4\n"
"v_pk_fma_f32 v[184:185], v[184:185], v[204:205], v[206:207] op_sel_hi:[1,0,0]\n"
"v_cvt_pk_f16_f32 v4, v184, v185\n"
"v_lshlrev_b32 v186, 16, v5\n"
"s_add_u32 m0, s51, 99376\n"
"s_nop 0\n"
"buffer_load_dwordx4 v217, s[24:27], s50 offen offset:0 lds\n"
"v_and_b32 v187, v237, v5\n"
"v_pk_fma_f32 v[186:187], v[186:187], v[204:205], v[206:207] op_sel_hi:[1,0,0]\n"
"v_cvt_pk_f16_f32 v5, v186, v187\n"
"v_lshlrev_b32 v188, 16, v6\n"
"v_and_b32 v189, v237, v6\n"
"v_pk_fma_f32 v[188:189], v[188:189], v[204:205], v[206:207] op_sel_hi:[1,0,0]\n"
"v_cvt_pk_f16_f32 v6, v188, v189\n"
"v_lshlrev_b32 v190, 16, v7\n"
"v_and_b32 v191, v237, v7\n"
"v_pk_fma_f32 v[190:191], v[190:191], v[204:205], v[206:207] op_sel_hi:[1,0,0]\n"
"v_cvt_pk_f16_f32 v7, v190, v191\n"
"v_lshlrev_b32 v176, 16, v8\n"
"v_and_b32 v177, v237, v8\n"
"v_pk_fma_f32 v[176:177], v[176:177], v[200:201], v[202:203] op_sel_hi:[1,0,0]\n"
"v_cvt_pk_f16_f32 v8, v176, v177\n"
"v_lshlrev_b32 v178, 16, v9\n"
"v_and_b32 v179, v237, v9\n"
"v_pk_fma_f32 v[178:179], v[178:179], v[200:201], v[202:203] op_sel_hi:[1,0,0]\n"
"v_cvt_pk_f16_f32 v9, v178, v179\n"
"v_lshlrev_b32 v180, 16, v10\n"
"v_and_b32 v181, v237, v10\n"
"s_add_u32 m0, s51, 107312\n"
"s_nop 0\n"
"buffer_load_dwordx4 v217, s[24:27], s50 offen offset:256 lds\n"
"s_add_u32 s50, s50, 32768\n"
"v_pk_fma_f32 v[180:181], v[180:181], v[200:201], v[202:203] op_sel_hi:[1,0,0]\n"
"v_cvt_pk_f16_f32 v10, v180, v181\n"
"v_lshlrev_b32 v182, 16, v11\n"
"v_and_b32 v183, v237, v11\n"
"v_pk_fma_f32 v[182:183], v[182:183], v[200:201], v[202:203] op_sel_hi:[1,0,0]\n"
"v_cvt_pk_f16_f32 v11, v182, v183\n"
"v_lshlrev_b32 v184, 16, v12\n"
"v_and_b32 v185, v237, v12\n"
"v_pk_fma_f32 v[184:185], v[184:185], v[204:205], v[206:207] op_sel_hi:[1,0,0]\n"
"v_cvt_pk_f16_f32 v12, v184, v185\n"
"v_lshlrev_b32 v186, 16, v13\n"
"v_and_b32 v187, v237, v13\n"
"v_pk_fma_f32 v[186:187], v[186:187], v[204:205], v[206:207] op_sel_hi:[1,0,0]\n"
"v_cvt_pk_f16_f32 v13, v186, v187\n"
"v_lshlrev_b32 v188, 16, v14\n"
"v_and_b32 v189, v237, v14\n"
"v_pk_fma_f32 v[188:189], v[188:189], v[204:205], v[206:207] op_sel_hi:[1,0,0]\n"
"v_cvt_pk_f16_f32 v14, v188, v189\n"
"v_lshlrev_b32 v190, 16, v15\n"
"v_and_b32 v191, v237, v15\n"
"v_pk_fma_f32 v[190:191], v[190:191], v[204:205], v[206:207] op_sel_hi:[1,0,0]\n"
"s_add_u32 m0, s51, 114744\n"
"s_nop 0\n"
"buffer_load_dwordx4 v217, s[24:27], s50 offen offset:0 lds\n"
"v_cvt_pk_f16_f32 v15, v190, v191\n"
"v_lshlrev_b32 v176, 16, v16\n"
"v_and_b32 v177, v237, v16\n"
"v_pk_fma_f32 v[176:177], v[176:177], v[200:201], v[202:203] op_sel_hi:[1,0,0]\n"
"v_cvt_pk_f16_f32 v16, v176, v177\n"
"v_lshlrev_b32 v178, 16, v17\n"
"v_and_b32 v179, v237, v17\n"
"v_pk_fma_f32 v[178:179], v[178:179], v[200:201], v[202:203] op_sel_hi:[1,0,0]\n"
"v_cvt_pk_f16_f32 v17, v178, v179\n"
"v_lshlrev_b32 v180, 16, v18\n"
"v_and_b32 v181, v237, v18\n"
"v_pk_fma_f32 v[180:181], v[180:181], v[200:201], v[202:203] op_sel_hi:[1,0,0]\n"
"v_cvt_pk_f16_f32 v18, v180, v181\n"
"v_lshlrev_b32 v182, 16, v19\n"
"v_and_b32 v183, v237, v19\n"
"v_pk_fma_f32 v[182:183], v[182:183], v[200:201], v[202:203] op_sel_hi:[1,0,0]\n"
"v_cvt_pk_f16_f32 v19, v182, v183\n"
"v_lshlrev_b32 v184, 16, v20\n"
"v_and_b32 v185, v237, v20\n"
"v_pk_fma_f32 v[184:185], v[184:185], v[204:205], v[206:207] op_sel_hi:[1,0,0]\n"
"v_cvt_pk_f16_f32 v20, v184, v185\n"
"v_lshlrev_b32 v186, 16, v21\n"
"s_add_u32 m0, s51, 122680\n"
"s_nop 0\n"
"buffer_load_dwordx4 v217, s[24:27], s50 offen offset:256 lds\n"
"s_add_u32 s50, s50, 32768\n"
"v_and_b32 v187, v237, v21\n"
"v_pk_fma_f32 v[186:187], v[186:187], v[204:205], v[206:207] op_sel_hi:[1,0,0]\n"
"v_cvt_pk_f16_f32 v21, v186, v187\n"
"v_lshlrev_b32 v188, 16, v22\n"
"v_and_b32 v189, v237, v22\n"
"v_pk_fma_f32 v[188:189], v[188:189], v[204:205], v[206:207] op_sel_hi:[1,0,0]\n"
"v_cvt_pk_f16_f32 v22, v188, v189\n"
"v_lshlrev_b32 v190, 16, v23\n"
"v_and_b32 v191, v237, v23\n"
"v_pk_fma_f32 v[190:191], v[190:191], v[204:205], v[206:207] op_sel_hi:[1,0,0]\n"
"v_cvt_pk_f16_f32 v23, v190, v191\n"
"v_lshlrev_b32 v176, 16, v24\n"
"v_and_b32 v177, v237, v24\n"
"v_pk_fma_f32 v[176:177], v[176:177], v[200:201], v[202:203] op_sel_hi:[1,0,0]\n"
"v_cvt_pk_f16_f32 v24, v176, v177\n"
"v_lshlrev_b32 v178, 16, v25\n"
"v_and_b32 v179, v237, v25\n"
"v_pk_fma_f32 v[178:179], v[178:179], v[200:201], v[202:203] op_sel_hi:[1,0,0]\n"
"v_cvt_pk_f16_f32 v25, v178, v179\n"
"v_lshlrev_b32 v180, 16, v26\n"
"v_and_b32 v181, v237, v26\n"
"s_add_u32 m0, s51, 115768\n"
"s_nop 0\n"
"buffer_load_dwordx4 v217, s[24:27], s50 offen offset:0 lds\n"
"v_pk_fma_f32 v[180:181], v[180:181], v[200:201], v[202:203] op_sel_hi:[1,0,0]\n"
"v_cvt_pk_f16_f32 v26, v180, v181\n"
"v_lshlrev_b32 v182, 16, v27\n"
"v_and_b32 v183, v237, v27\n"
"v_pk_fma_f32 v[182:183], v[182:183], v[200:201], v[202:203] op_sel_hi:[1,0,0]\n"
"v_cvt_pk_f16_f32 v27, v182, v183\n"
"v_lshlrev_b32 v184, 16, v28\n"
"v_and_b32 v185, v237, v28\n"
"v_pk_fma_f32 v[184:185], v[184:185], v[204:205], v[206:207] op_sel_hi:[1,0,0]\n"
"v_cvt_pk_f16_f32 v28, v184, v185\n"
"v_lshlrev_b32 v186, 16, v29\n"
"v_and_b32 v187, v237, v29\n"
"v_pk_fma_f32 v[186:187], v[186:187], v[204:205], v[206:207] op_sel_hi:[1,0,0]\n"
"v_cvt_pk_f16_f32 v29, v186, v187\n"
"v_lshlrev_b32 v188, 16, v30\n"
"v_and_b32 v189, v237, v30\n"
"v_pk_fma_f32 v[188:189], v[188:189], v[204:205], v[206:207] op_sel_hi:[1,0,0]\n"
"v_cvt_pk_f16_f32 v30, v188, v189\n"
"v_lshlrev_b32 v190, 16, v31\n"
"v_and_b32 v191, v237, v31\n"
"v_pk_fma_f32 v[190:191], v[190:191], v[204:205], v[206:207] op_sel_hi:[1,0,0]\n"
"s_add_u32 m0, s51, 123704\n"
"s_nop 0\n"
"buffer_load_dwordx4 v217, s[24:27], s50 offen offset:256 lds\n"
"s_add_u32 s50, s50, 425984\n"
"v_cvt_pk_f16_f32 v31, v190, v191\n"
"buffer_load_dwordx4 v[144:147], v225, s[36:39], s61 offen offset:0 sc1\n"
"buffer_load_dwordx4 v[148:151], v225, s[36:39], s61 offen offset:16 sc1\n"
"buffer_load_dwordx4 v[152:155], v225, s[36:39], s62 offen offset:0 sc1\n"
"buffer_load_dwordx4 v[156:159], v225, s[36:39], s62 offen offset:16 sc1\n"
"v_lshlrev_b32 v176, 16, v32\n"
"v_and_b32 v177, v237, v32\n"
"v_pk_fma_f32 v[176:177], v[176:177], v[200:201], v[202:203] op_sel_hi:[1,0,0]\n"
"v_cvt_pk_f16_f32 v32, v176, v177\n"
"v_lshlrev_b32 v178, 16, v33\n"
"v_and_b32 v179, v237, v33\n"
"v_pk_fma_f32 v[178:179], v[178:179], v[200:201], v[202:203] op_sel_hi:[1,0,0]\n"
"v_cvt_pk_f16_f32 v33, v178, v179\n"
"v_lshlrev_b32 v180, 16, v34\n"
"v_and_b32 v181, v237, v34\n"
"v_pk_fma_f32 v[180:181], v[180:181], v[200:201], v[202:203] op_sel_hi:[1,0,0]\n"
"v_cvt_pk_f16_f32 v34, v180, v181\n"
"v_lshlrev_b32 v182, 16, v35\n"
"v_and_b32 v183, v237, v35\n"
"v_pk_fma_f32 v[182:183], v[182:183], v[200:201], v[202:203] op_sel_hi:[1,0,0]\n"
"v_cvt_pk_f16_f32 v35, v182, v183\n"
"v_lshlrev_b32 v184, 16, v36\n"
"v_and_b32 v185, v237, v36\n"
"v_pk_fma_f32 v[184:185], v[184:185], v[204:205], v[206:207] op_sel_hi:[1,0,0]\n"
"v_cvt_pk_f16_f32 v36, v184, v185\n"
"v_lshlrev_b32 v186, 16, v37\n"
"s_mov_b32 m0, s51\n"
"s_nop 0\n"
"buffer_load_dwordx4 v217, s[24:27], s50 offen offset:0 lds\n"
"v_and_b32 v187, v237, v37\n"
"v_pk_fma_f32 v[186:187], v[186:187], v[204:205], v[206:207] op_sel_hi:[1,0,0]\n"
"v_cvt_pk_f16_f32 v37, v186, v187\n"
"v_lshlrev_b32 v188, 16, v38\n"
"v_and_b32 v189, v237, v38\n"
"v_pk_fma_f32 v[188:189], v[188:189], v[204:205], v[206:207] op_sel_hi:[1,0,0]\n"
"v_cvt_pk_f16_f32 v38, v188, v189\n"
"v_lshlrev_b32 v190, 16, v39\n"
"v_and_b32 v191, v237, v39\n"
"v_pk_fma_f32 v[190:191], v[190:191], v[204:205], v[206:207] op_sel_hi:[1,0,0]\n"
"v_cvt_pk_f16_f32 v39, v190, v191\n"
"v_lshlrev_b32 v176, 16, v40\n"
"v_and_b32 v177, v237, v40\n"
"v_pk_fma_f32 v[176:177], v[176:177], v[200:201], v[202:203] op_sel_hi:[1,0,0]\n"
"v_cvt_pk_f16_f32 v40, v176, v177\n"
"v_lshlrev_b32 v178, 16, v41\n"
"v_and_b32 v179, v237, v41\n"
"v_pk_fma_f32 v[178:179], v[178:179], v[200:201], v[202:203] op_sel_hi:[1,0,0]\n"
"v_cvt_pk_f16_f32 v41, v178, v179\n"
"v_lshlrev_b32 v180, 16, v42\n"
"v_and_b32 v181, v237, v42\n"
"s_add_u32 m0, s51, 7936\n"
"s_nop 0\n"
"buffer_load_dwordx4 v217, s[24:27], s50 offen offset:256 lds\n"
"s_add_u32 s50, s50, 32768\n"
"v_pk_fma_f32 v[180:181], v[180:181], v[200:201], v[202:203] op_sel_hi:[1,0,0]\n"
"v_cvt_pk_f16_f32 v42, v180, v181\n"
"v_lshlrev_b32 v182, 16, v43\n"
"v_and_b32 v183, v237, v43\n"
"v_pk_fma_f32 v[182:183], v[182:183], v[200:201], v[202:203] op_sel_hi:[1,0,0]\n"
"v_cvt_pk_f16_f32 v43, v182, v183\n"
"v_lshlrev_b32 v184, 16, v44\n"
"v_and_b32 v185, v237, v44\n"
"v_pk_fma_f32 v[184:185], v[184:185], v[204:205], v[206:207] op_sel_hi:[1,0,0]\n"
"v_cvt_pk_f16_f32 v44, v184, v185\n"
"v_lshlrev_b32 v186, 16, v45\n"
"v_and_b32 v187, v237, v45\n"
"v_pk_fma_f32 v[186:187], v[186:187], v[204:205], v[206:207] op_sel_hi:[1,0,0]\n"
"v_cvt_pk_f16_f32 v45, v186, v187\n"
"v_lshlrev_b32 v188, 16, v46\n"
"v_and_b32 v189, v237, v46\n"
"v_pk_fma_f32 v[188:189], v[188:189], v[204:205], v[206:207] op_sel_hi:[1,0,0]\n"
"v_cvt_pk_f16_f32 v46, v188, v189\n"
"v_lshlrev_b32 v190, 16, v47\n"
"v_and_b32 v191, v237, v47\n"
"v_pk_fma_f32 v[190:191], v[190:191], v[204:205], v[206:207] op_sel_hi:[1,0,0]\n"
"s_add_u32 m0, s51, 1024\n"
"s_nop 0\n"
"buffer_load_dwordx4 v217, s[24:27], s50 offen offset:0 lds\n"
"v_cvt_pk_f16_f32 v47, v190, v191\n"
"v_lshlrev_b32 v176, 16, v48\n"
"v_and_b32 v177, v237, v48\n"
"v_pk_fma_f32 v[176:177], v[176:177], v[200:201], v[202:203] op_sel_hi:[1,0,0]\n"
"v_cvt_pk_f16_f32 v48, v176, v177\n"
"v_lshlrev_b32 v178, 16, v49\n"
"v_and_b32 v179, v237, v49\n"
"v_pk_fma_f32 v[178:179], v[178:179], v[200:201], v[202:203] op_sel_hi:[1,0,0]\n"
"v_cvt_pk_f16_f32 v49, v178, v179\n"
"v_lshlrev_b32 v180, 16, v50\n"
"v_and_b32 v181, v237, v50\n"
"v_pk_fma_f32 v[180:181], v[180:181], v[200:201], v[202:203] op_sel_hi:[1,0,0]\n"
"v_cvt_pk_f16_f32 v50, v180, v181\n"
"v_lshlrev_b32 v182, 16, v51\n"
"v_and_b32 v183, v237, v51\n"
"v_pk_fma_f32 v[182:183], v[182:183], v[200:201], v[202:203] op_sel_hi:[1,0,0]\n"
"v_cvt_pk_f16_f32 v51, v182, v183\n"
"v_lshlrev_b32 v184, 16, v52\n"
"v_and_b32 v185, v237, v52\n"
"v_pk_fma_f32 v[184:185], v[184:185], v[204:205], v[206:207] op_sel_hi:[1,0,0]\n"
"v_cvt_pk_f16_f32 v52, v184, v185\n"
"v_lshlrev_b32 v186, 16, v53\n"
"s_add_u32 m0, s51, 8960\n"
"s_nop 0\n"
"buffer_load_dwordx4 v217, s[24:27], s50 offen offset:256 lds\n"
"s_add_u32 s50, s50, 32768\n"
"v_and_b32 v187, v237, v53\n"
"v_pk_fma_f32 v[186:187], v[186:187], v[204:205], v[206:207] op_sel_hi:[1,0,0]\n"
"v_cvt_pk_f16_f32 v53, v186, v187\n"
"v_lshlrev_b32 v188, 16, v54\n"
"v_and_b32 v189, v237, v54\n"
"v_pk_fma_f32 v[188:189], v[188:189], v[204:205], v[206:207] op_sel_hi:[1,0,0]\n"
"v_cvt_pk_f16_f32 v54, v188, v189\n"
"v_lshlrev_b32 v190, 16, v55\n"
"v_and_b32 v191, v237, v55\n"
"v_pk_fma_f32 v[190:191], v[190:191], v[204:205], v[206:207] op_sel_hi:[1,0,0]\n"
"v_cvt_pk_f16_f32 v55, v190, v191\n"
"v_lshlrev_b32 v176, 16, v56\n"
"v_and_b32 v177, v237, v56\n"
"v_pk_fma_f32 v[176:177], v[176:177], v[200:201], v[202:203] op_sel_hi:[1,0,0]\n"
"v_cvt_pk_f16_f32 v56, v176, v177\n"
"v_lshlrev_b32 v178, 16, v57\n"
"v_and_b32 v179, v237, v57\n"
"v_pk_fma_f32 v[178:179], v[178:179], v[200:201], v[202:203] op_sel_hi:[1,0,0]\n"
"v_cvt_pk_f16_f32 v57, v178, v179\n"
"v_lshlrev_b32 v180, 16, v58\n"
"v_and_b32 v181, v237, v58\n"
"s_add_u32 m0, s51, 16392\n"
"s_nop 0\n"
"buffer_load_dwordx4 v217, s[24:27], s50 offen offset:0 lds\n"
"v_pk_fma_f32 v[180:181], v[180:181], v[200:201], v[202:203] op_sel_hi:[1,0,0]\n"
"v_cvt_pk_f16_f32 v58, v180, v181\n"
"v_lshlrev_b32 v182, 16, v59\n"
"v_and_b32 v183, v237, v59\n"
"v_pk_fma_f32 v[182:183], v[182:183], v[200:201], v[202:203] op_sel_hi:[1,0,0]\n"
"v_cvt_pk_f16_f32 v59, v182, v183\n"
"v_lshlrev_b32 v184, 16, v60\n"
"v_and_b32 v185, v237, v60\n"
"v_pk_fma_f32 v[184:185], v[184:185], v[204:205], v[206:207] op_sel_hi:[1,0,0]\n"
"v_cvt_pk_f16_f32 v60, v184, v185\n"
"v_lshlrev_b32 v186, 16, v61\n"
"v_and_b32 v187, v237, v61\n"
"v_pk_fma_f32 v[186:187], v[186:187], v[204:205], v[206:207] op_sel_hi:[1,0,0]\n"
"v_cvt_pk_f16_f32 v61, v186, v187\n"
"v_lshlrev_b32 v188, 16, v62\n"
"v_and_b32 v189, v237, v62\n"
"v_pk_fma_f32 v[188:189], v[188:189], v[204:205], v[206:207] op_sel_hi:[1,0,0]\n"
"v_cvt_pk_f16_f32 v62, v188, v189\n"
"v_lshlrev_b32 v190, 16, v63\n"
"v_and_b32 v191, v237, v63\n"
"v_pk_fma_f32 v[190:191], v[190:191], v[204:205], v[206:207] op_sel_hi:[1,0,0]\n"
"s_add_u32 m0, s51, 24328\n"
"s_nop 0\n"
"buffer_load_dwordx4 v217, s[24:27], s50 offen offset:256 lds\n"
"s_add_u32 s50, s50, 32768\n"
"v_cvt_pk_f16_f32 v63, v190, v191\n"
"v_lshlrev_b32 v176, 16, v64\n"
"v_and_b32 v177, v237, v64\n"
"v_pk_fma_f32 v[176:177], v[176:177], v[200:201], v[202:203] op_sel_hi:[1,0,0]\n"
"v_cvt_pk_f16_f32 v64, v176, v177\n"
"v_lshlrev_b32 v178, 16, v65\n"
"v_and_b32 v179, v237, v65\n"
"v_pk_fma_f32 v[178:179], v[178:179], v[200:201], v[202:203] op_sel_hi:[1,0,0]\n"
"v_cvt_pk_f16_f32 v65, v178, v179\n"
"v_lshlrev_b32 v180, 16, v66\n"
"v_and_b32 v181, v237, v66\n"
"v_pk_fma_f32 v[180:181], v[180:181], v[200:201], v[202:203] op_sel_hi:[1,0,0]\n"
"v_cvt_pk_f16_f32 v66, v180, v181\n"
"v_lshlrev_b32 v182, 16, v67\n"
"v_and_b32 v183, v237, v67\n"
"v_pk_fma_f32 v[182:183], v[182:183], v[200:201], v[202:203] op_sel_hi:[1,0,0]\n"
"v_cvt_pk_f16_f32 v67, v182, v183\n"
"v_lshlrev_b32 v184, 16, v68\n"
"v_and_b32 v185, v237, v68\n"
"v_pk_fma_f32 v[184:185], v[184:185], v[204:205], v[206:207] op_sel_hi:[1,0,0]\n"
"v_cvt_pk_f16_f32 v68, v184, v185\n"
"v_lshlrev_b32 v186, 16, v69\n"
"s_add_u32 m0, s51, 17416\n"
"s_nop 0\n"
"buffer_load_dwordx4 v217, s[24:27], s50 offen offset:0 lds\n"
"v_and_b32 v187, v237, v69\n"
"v_pk_fma_f32 v[186:187], v[186:187], v[204:205], v[206:207] op_sel_hi:[1,0,0]\n"
"v_cvt_pk_f16_f32 v69, v186, v187\n"
"v_lshlrev_b32 v188, 16, v70\n"
"v_and_b32 v189, v237, v70\n"
"v_pk_fma_f32 v[188:189], v[188:189], v[204:205], v[206:207] op_sel_hi:[1,0,0]\n"
"v_cvt_pk_f16_f32 v70, v188, v189\n"
"v_lshlrev_b32 v190, 16, v71\n"
"v_and_b32 v191, v237, v71\n"
"v_pk_fma_f32 v[190:191], v[190:191], v[204:205], v[206:207] op_sel_hi:[1,0,0]\n"
"v_cvt_pk_f16_f32 v71, v190, v191\n"
"v_lshlrev_b32 v176, 16, v72\n"
"v_and_b32 v177, v237, v72\n"
"v_pk_fma_f32 v[176:177], v[176:177], v[200:201], v[202:203] op_sel_hi:[1,0,0]\n"
"v_cvt_pk_f16_f32 v72, v176, v177\n"
"v_lshlrev_b32 v178, 16, v73\n"
"v_and_b32 v179, v237, v73\n"
"v_pk_fma_f32 v[178:179], v[178:179], v[200:201], v[202:203] op_sel_hi:[1,0,0]\n"
"v_cvt_pk_f16_f32 v73, v178, v179\n"
"v_lshlrev_b32 v180, 16, v74\n"
"v_and_b32 v181, v237, v74\n"
"s_add_u32 m0, s51, 25352\n"
"s_nop 0\n"
"buffer_load_dwordx4 v217, s[24:27], s50 offen offset:256 lds\n"
"s_add_u32 s50, s50, 425984\n"
"v_pk_fma_f32 v[180:181], v[180:181], v[200:201], v[202:203] op_sel_hi:[1,0,0]\n"
"v_cvt_pk_f16_f32 v74, v180, v181\n"
"v_lshlrev_b32 v182, 16, v75\n"
"v_and_b32 v183, v237, v75\n"
"v_pk_fma_f32 v[182:183], v[182:183], v[200:201], v[202:203] op_sel_hi:[1,0,0]\n"
"v_cvt_pk_f16_f32 v75, v182, v183\n"
"v_lshlrev_b32 v184, 16, v76\n"
"v_and_b32 v185, v237, v76\n"
"v_pk_fma_f32 v[184:185], v[184:185], v[204:205], v[206:207] op_sel_hi:[1,0,0]\n"
"v_cvt_pk_f16_f32 v76, v184, v185\n"
"v_lshlrev_b32 v186, 16, v77\n"
"v_and_b32 v187, v237, v77\n"
"v_pk_fma_f32 v[186:187], v[186:187], v[204:205], v[206:207] op_sel_hi:[1,0,0]\n"
"v_cvt_pk_f16_f32 v77, v186, v187\n"
"v_lshlrev_b32 v188, 16, v78\n"
"v_and_b32 v189, v237, v78\n"
"v_pk_fma_f32 v[188:189], v[188:189], v[204:205], v[206:207] op_sel_hi:[1,0,0]\n"
"v_cvt_pk_f16_f32 v78, v188, v189\n"
"v_lshlrev_b32 v190, 16, v79\n"
"v_and_b32 v191, v237, v79\n"
"v_pk_fma_f32 v[190:191], v[190:191], v[204:205], v[206:207] op_sel_hi:[1,0,0]\n"
"s_add_u32 m0, s51, 32784\n"
"s_nop 0\n"
"buffer_load_dwordx4 v217, s[24:27], s50 offen offset:0 lds\n"
"v_cvt_pk_f16_f32 v79, v190, v191\n"
"v_lshlrev_b32 v176, 16, v80\n"
"v_and_b32 v177, v237, v80\n"
"v_pk_fma_f32 v[176:177], v[176:177], v[200:201], v[202:203] op_sel_hi:[1,0,0]\n"
"v_cvt_pk_f16_f32 v80, v176, v177\n"
"v_lshlrev_b32 v178, 16, v81\n"
"v_and_b32 v179, v237, v81\n"
"v_pk_fma_f32 v[178:179], v[178:179], v[200:201], v[202:203] op_sel_hi:[1,0,0]\n"
"v_cvt_pk_f16_f32 v81, v178, v179\n"
"v_lshlrev_b32 v180, 16, v82\n"
"v_and_b32 v181, v237, v82\n"
"v_pk_fma_f32 v[180:181], v[180:181], v[200:201], v[202:203] op_sel_hi:[1,0,0]\n"
"v_cvt_pk_f16_f32 v82, v180, v181\n"
"v_lshlrev_b32 v182, 16, v83\n"
"v_and_b32 v183, v237, v83\n"
"v_pk_fma_f32 v[182:183], v[182:183], v[200:201], v[202:203] op_sel_hi:[1,0,0]\n"
"v_cvt_pk_f16_f32 v83, v182, v183\n"
"v_lshlrev_b32 v184, 16, v84\n"
"v_and_b32 v185, v237, v84\n"
"v_pk_fma_f32 v[184:185], v[184:185], v[204:205], v[206:207] op_sel_hi:[1,0,0]\n"
"v_cvt_pk_f16_f32 v84, v184, v185\n"
"v_lshlrev_b32 v186, 16, v85\n"
"s_add_u32 m0, s51, 40720\n"
"s_nop 0\n"
"buffer_load_dwordx4 v217, s[24:27], s50 offen offset:256 lds\n"
"s_add_u32 s50, s50, 32768\n"
"v_and_b32 v187, v237, v85\n"
"v_pk_fma_f32 v[186:187], v[186:187], v[204:205], v[206:207] op_sel_hi:[1,0,0]\n"
"v_cvt_pk_f16_f32 v85, v186, v187\n"
"v_lshlrev_b32 v188, 16, v86\n"
"v_and_b32 v189, v237, v86\n"
"v_pk_fma_f32 v[188:189], v[188:189], v[204:205], v[206:207] op_sel_hi:[1,0,0]\n"
"v_cvt_pk_f16_f32 v86, v188, v189\n"
"v_lshlrev_b32 v190, 16, v87\n"
"v_and_b32 v191, v237, v87\n"
"v_pk_fma_f32 v[190:191], v[190:191], v[204:205], v[206:207] op_sel_hi:[1,0,0]\n"
"v_cvt_pk_f16_f32 v87, v190, v191\n"
"v_lshlrev_b32 v176, 16, v88\n"
"v_and_b32 v177, v237, v88\n"
"v_pk_fma_f32 v[176:177], v[176:177], v[200:201], v[202:203] op_sel_hi:[1,0,0]\n"
"v_cvt_pk_f16_f32 v88, v176, v177\n"
"v_lshlrev_b32 v178, 16, v89\n"
"v_and_b32 v179, v237, v89\n"
"v_pk_fma_f32 v[178:179], v[178:179], v[200:201], v[202:203] op_sel_hi:[1,0,0]\n"
"v_cvt_pk_f16_f32 v89, v178, v179\n"
"v_lshlrev_b32 v180, 16, v90\n"
"v_and_b32 v181, v237, v90\n"
"s_add_u32 m0, s51, 33808\n"
"s_nop 0\n"
"buffer_load_dwordx4 v217, s[24:27], s50 offen offset:0 lds\n"
"v_pk_fma_f32 v[180:181], v[180:181], v[200:201], v[202:203] op_sel_hi:[1,0,0]\n"
"v_cvt_pk_f16_f32 v90, v180, v181\n"
"v_lshlrev_b32 v182, 16, v91\n"
"v_and_b32 v183, v237, v91\n"
"v_pk_fma_f32 v[182:183], v[182:183], v[200:201], v[202:203] op_sel_hi:[1,0,0]\n"
"v_cvt_pk_f16_f32 v91, v182, v183\n"
"v_lshlrev_b32 v184, 16, v92\n"
"v_and_b32 v185, v237, v92\n"
"v_pk_fma_f32 v[184:185], v[184:185], v[204:205], v[206:207] op_sel_hi:[1,0,0]\n"
"v_cvt_pk_f16_f32 v92, v184, v185\n"
"v_lshlrev_b32 v186, 16, v93\n"
"v_and_b32 v187, v237, v93\n"
"v_pk_fma_f32 v[186:187], v[186:187], v[204:205], v[206:207] op_sel_hi:[1,0,0]\n"
"v_cvt_pk_f16_f32 v93, v186, v187\n"
"v_lshlrev_b32 v188, 16, v94\n"
"v_and_b32 v189, v237, v94\n"
"v_pk_fma_f32 v[188:189], v[188:189], v[204:205], v[206:207] op_sel_hi:[1,0,0]\n"
"v_cvt_pk_f16_f32 v94, v188, v189\n"
"v_lshlrev_b32 v190, 16, v95\n"
"v_and_b32 v191, v237, v95\n"
"v_pk_fma_f32 v[190:191], v[190:191], v[204:205], v[206:207] op_sel_hi:[1,0,0]\n"
"s_add_u32 m0, s51, 41744\n"
"s_nop 0\n"
"buffer_load_dwordx4 v217, s[24:27], s50 offen offset:256 lds\n"
"s_add_u32 s50, s50, 32768\n"
"v_cvt_pk_f16_f32 v95, v190, v191\n"
"v_lshlrev_b32 v176, 16, v96\n"
"v_and_b32 v177, v237, v96\n"
"v_pk_fma_f32 v[176:177], v[176:177], v[200:201], v[202:203] op_sel_hi:[1,0,0]\n"
"v_cvt_pk_f16_f32 v96, v176, v177\n"
"v_lshlrev_b32 v178, 16, v97\n"
"v_and_b32 v179, v237, v97\n"
"v_pk_fma_f32 v[178:179], v[178:179], v[200:201], v[202:203] op_sel_hi:[1,0,0]\n"
"v_cvt_pk_f16_f32 v97, v178, v179\n"
"v_lshlrev_b32 v180, 16, v98\n"
"v_and_b32 v181, v237, v98\n"
"v_pk_fma_f32 v[180:181], v[180:181], v[200:201], v[202:203] op_sel_hi:[1,0,0]\n"
"v_cvt_pk_f16_f32 v98, v180, v181\n"
"v_lshlrev_b32 v182, 16, v99\n"
"v_and_b32 v183, v237, v99\n"
"v_pk_fma_f32 v[182:183], v[182:183], v[200:201], v[202:203] op_sel_hi:[1,0,0]\n"
"v_cvt_pk_f16_f32 v99, v182, v183\n"
"v_lshlrev_b32 v184, 16, v100\n"
"v_and_b32 v185, v237, v100\n"
"v_pk_fma_f32 v[184:185], v[184:185], v[204:205], v[206:207] op_sel_hi:[1,0,0]\n"
"v_cvt_pk_f16_f32 v100, v184, v185\n"
"v_lshlrev_b32 v186, 16, v101\n"
"s_add_u32 m0, s51, 49176\n"
"s_nop 0\n"
"buffer_load_dwordx4 v217, s[24:27], s50 offen offset:0 lds\n"
"v_and_b32 v187, v237, v101\n"
"v_pk_fma_f32 v[186:187], v[186:187], v[204:205], v[206:207] op_sel_hi:[1,0,0]\n"
"v_cvt_pk_f16_f32 v101, v186, v187\n"
"v_lshlrev_b32 v188, 16, v102\n"
"v_and_b32 v189, v237, v102\n"
"v_pk_fma_f32 v[188:189], v[188:189], v[204:205], v[206:207] op_sel_hi:[1,0,0]\n"
"v_cvt_pk_f16_f32 v102, v188, v189\n"
"v_lshlrev_b32 v190, 16, v103\n"
"v_and_b32 v191, v237, v103\n"
"v_pk_fma_f32 v[190:191], v[190:191], v[204:205], v[206:207] op_sel_hi:[1,0,0]\n"
"v_cvt_pk_f16_f32 v103, v190, v191\n"
"v_lshlrev_b32 v176, 16, v104\n"
"v_and_b32 v177, v237, v104\n"
"v_pk_fma_f32 v[176:177], v[176:177], v[200:201], v[202:203] op_sel_hi:[1,0,0]\n"
"v_cvt_pk_f16_f32 v104, v176, v177\n"
"v_lshlrev_b32 v178, 16, v105\n"
"v_and_b32 v179, v237, v105\n"
"v_pk_fma_f32 v[178:179], v[178:179], v[200:201], v[202:203] op_sel_hi:[1,0,0]\n"
"v_cvt_pk_f16_f32 v105, v178, v179\n"
"v_lshlrev_b32 v180, 16, v106\n"
"v_and_b32 v181, v237, v106\n"
"s_add_u32 m0, s51, 57112\n"
"s_nop 0\n"
"buffer_load_dwordx4 v217, s[24:27], s50 offen offset:256 lds\n"
"s_add_u32 s50, s50, 32768\n"
"v_pk_fma_f32 v[180:181], v[180:181], v[200:201], v[202:203] op_sel_hi:[1,0,0]\n"
"v_cvt_pk_f16_f32 v106, v180, v181\n"
"v_lshlrev_b32 v182, 16, v107\n"
"v_and_b32 v183, v237, v107\n"
"v_pk_fma_f32 v[182:183], v[182:183], v[200:201], v[202:203] op_sel_hi:[1,0,0]\n"
"v_cvt_pk_f16_f32 v107, v182, v183\n"
"v_lshlrev_b32 v184, 16, v108\n"
"v_and_b32 v185, v237, v108\n"
"v_pk_fma_f32 v[184:185], v[184:185], v[204:205], v[206:207] op_sel_hi:[1,0,0]\n"
"v_cvt_pk_f16_f32 v108, v184, v185\n"
"v_lshlrev_b32 v186, 16, v109\n"
"v_and_b32 v187, v237, v109\n"
"v_pk_fma_f32 v[186:187], v[186:187], v[204:205], v[206:207] op_sel_hi:[1,0,0]\n"
"v_cvt_pk_f16_f32 v109, v186, v187\n"
"v_lshlrev_b32 v188, 16, v110\n"
"v_and_b32 v189, v237, v110\n"
"v_pk_fma_f32 v[188:189], v[188:189], v[204:205], v[206:207] op_sel_hi:[1,0,0]\n"
"v_cvt_pk_f16_f32 v110, v188, v189\n"
"v_lshlrev_b32 v190, 16, v111\n"
"v_and_b32 v191, v237, v111\n"
"v_pk_fma_f32 v[190:191], v[190:191], v[204:205], v[206:207] op_sel_hi:[1,0,0]\n"
"s_add_u32 m0, s51, 50200\n"
"s_nop 0\n"
"buffer_load_dwordx4 v217, s[24:27], s50 offen offset:0 lds\n"
"v_cvt_pk_f16_f32 v111, v190, v191\n"
"v_lshlrev_b32 v176, 16, v112\n"
"v_and_b32 v177, v237, v112\n"
"v_pk_fma_f32 v[176:177], v[176:177], v[200:201], v[202:203] op_sel_hi:[1,0,0]\n"
"v_cvt_pk_f16_f32 v112, v176, v177\n"
"v_lshlrev_b32 v178, 16, v113\n"
"v_and_b32 v179, v237, v113\n"
"v_pk_fma_f32 v[178:179], v[178:179], v[200:201], v[202:203] op_sel_hi:[1,0,0]\n"
"v_cvt_pk_f16_f32 v113, v178, v179\n"
"v_lshlrev_b32 v180, 16, v114\n"
"v_and_b32 v181, v237, v114\n"
"v_pk_fma_f32 v[180:181], v[180:181], v[200:201], v[202:203] op_sel_hi:[1,0,0]\n"
"v_cvt_pk_f16_f32 v114, v180, v181\n"
"v_lshlrev_b32 v182, 16, v115\n"
"v_and_b32 v183, v237, v115\n"
"v_pk_fma_f32 v[182:183], v[182:183], v[200:201], v[202:203] op_sel_hi:[1,0,0]\n"
"v_cvt_pk_f16_f32 v115, v182, v183\n"
"v_lshlrev_b32 v184, 16, v116\n"
"v_and_b32 v185, v237, v116\n"
"v_pk_fma_f32 v[184:185], v[184:185], v[204:205], v[206:207] op_sel_hi:[1,0,0]\n"
"v_cvt_pk_f16_f32 v116, v184, v185\n"
"v_lshlrev_b32 v186, 16, v117\n"
"s_add_u32 m0, s51, 58136\n"
"s_nop 0\n"
"buffer_load_dwordx4 v217, s[24:27], s50 offen offset:256 lds\n"
"s_add_u32 s50, s50, 425984\n"
"v_and_b32 v187, v237, v117\n"
"v_pk_fma_f32 v[186:187], v[186:187], v[204:205], v[206:207] op_sel_hi:[1,0,0]\n"
"v_cvt_pk_f16_f32 v117, v186, v187\n"
"v_lshlrev_b32 v188, 16, v118\n"
"v_and_b32 v189, v237, v118\n"
"v_pk_fma_f32 v[188:189], v[188:189], v[204:205], v[206:207] op_sel_hi:[1,0,0]\n"
"v_cvt_pk_f16_f32 v118, v188, v189\n"
"v_lshlrev_b32 v190, 16, v119\n"
"v_and_b32 v191, v237, v119\n"
"v_pk_fma_f32 v[190:191], v[190:191], v[204:205], v[206:207] op_sel_hi:[1,0,0]\n"
"v_cvt_pk_f16_f32 v119, v190, v191\n"
"v_lshlrev_b32 v176, 16, v120\n"
"v_and_b32 v177, v237, v120\n"
"v_pk_fma_f32 v[176:177], v[176:177], v[200:201], v[202:203] op_sel_hi:[1,0,0]\n"
"v_cvt_pk_f16_f32 v120, v176, v177\n"
"v_lshlrev_b32 v178, 16, v121\n"
"v_and_b32 v179, v237, v121\n"
"v_pk_fma_f32 v[178:179], v[178:179], v[200:201], v[202:203] op_sel_hi:[1,0,0]\n"
"v_cvt_pk_f16_f32 v121, v178, v179\n"
"v_lshlrev_b32 v180, 16, v122\n"
"v_and_b32 v181, v237, v122\n"
"v_pk_fma_f32 v[180:181], v[180:181], v[200:201], v[202:203] op_sel_hi:[1,0,0]\n"
"v_cvt_pk_f16_f32 v122, v180, v181\n"
"v_lshlrev_b32 v182, 16, v123\n"
"v_and_b32 v183, v237, v123\n"
"v_pk_fma_f32 v[182:183], v[182:183], v[200:201], v[202:203] op_sel_hi:[1,0,0]\n"
"v_cvt_pk_f16_f32 v123, v182, v183\n"
"v_lshlrev_b32 v184, 16, v124\n"
"v_and_b32 v185, v237, v124\n"
"v_pk_fma_f32 v[184:185], v[184:185], v[204:205], v[206:207] op_sel_hi:[1,0,0]\n"
"v_cvt_pk_f16_f32 v124, v184, v185\n"
"v_lshlrev_b32 v186, 16, v125\n"
"v_and_b32 v187, v237, v125\n"
"v_pk_fma_f32 v[186:187], v[186:187], v[204:205], v[206:207] op_sel_hi:[1,0,0]\n"
"v_cvt_pk_f16_f32 v125, v186, v187\n"
"v_lshlrev_b32 v188, 16, v126\n"
"v_and_b32 v189, v237, v126\n"
"v_pk_fma_f32 v[188:189], v[188:189], v[204:205], v[206:207] op_sel_hi:[1,0,0]\n"
"v_cvt_pk_f16_f32 v126, v188, v189\n"
"v_lshlrev_b32 v190, 16, v127\n"
"v_and_b32 v191, v237, v127\n"
"v_pk_fma_f32 v[190:191], v[190:191], v[204:205], v[206:207] op_sel_hi:[1,0,0]\n"
"v_cvt_pk_f16_f32 v127, v190, v191\n"
"v_lshlrev_b32 v176, 16, v128\n"
"v_and_b32 v177, v237, v128\n"
"v_pk_fma_f32 v[176:177], v[176:177], v[200:201], v[202:203] op_sel_hi:[1,0,0]\n"
"v_cvt_pk_f16_f32 v128, v176, v177\n"
"v_lshlrev_b32 v178, 16, v129\n"
"v_and_b32 v179, v237, v129\n"
"v_pk_fma_f32 v[178:179], v[178:179], v[200:201], v[202:203] op_sel_hi:[1,0,0]\n"
"v_cvt_pk_f16_f32 v129, v178, v179\n"
"v_lshlrev_b32 v180, 16, v130\n"
"v_and_b32 v181, v237, v130\n"
"v_pk_fma_f32 v[180:181], v[180:181], v[200:201], v[202:203] op_sel_hi:[1,0,0]\n"
"v_cvt_pk_f16_f32 v130, v180, v181\n"
"v_lshlrev_b32 v182, 16, v131\n"
"v_and_b32 v183, v237, v131\n"
"v_pk_fma_f32 v[182:183], v[182:183], v[200:201], v[202:203] op_sel_hi:[1,0,0]\n"
"v_cvt_pk_f16_f32 v131, v182, v183\n"
"v_lshlrev_b32 v184, 16, v132\n"
"v_and_b32 v185, v237, v132\n"
"v_pk_fma_f32 v[184:185], v[184:185], v[204:205], v[206:207] op_sel_hi:[1,0,0]\n"
"v_cvt_pk_f16_f32 v132, v184, v185\n"
"v_lshlrev_b32 v186, 16, v133\n"
"v_and_b32 v187, v237, v133\n"
"v_pk_fma_f32 v[186:187], v[186:187], v[204:205], v[206:207] op_sel_hi:[1,0,0]\n"
"v_cvt_pk_f16_f32 v133, v186, v187\n"
"v_lshlrev_b32 v188, 16, v134\n"
"v_and_b32 v189, v237, v134\n"
"v_pk_fma_f32 v[188:189], v[188:189], v[204:205], v[206:207] op_sel_hi:[1,0,0]\n"
"v_cvt_pk_f16_f32 v134, v188, v189\n"
"v_lshlrev_b32 v190, 16, v135\n"
"v_and_b32 v191, v237, v135\n"
"v_pk_fma_f32 v[190:191], v[190:191], v[204:205], v[206:207] op_sel_hi:[1,0,0]\n"
"v_cvt_pk_f16_f32 v135, v190, v191\n"
"v_lshlrev_b32 v176, 16, v136\n"
"v_and_b32 v177, v237, v136\n"
"v_pk_fma_f32 v[176:177], v[176:177], v[200:201], v[202:203] op_sel_hi:[1,0,0]\n"
"v_cvt_pk_f16_f32 v136, v176, v177\n"
"v_lshlrev_b32 v178, 16, v137\n"
"v_and_b32 v179, v237, v137\n"
"v_pk_fma_f32 v[178:179], v[178:179], v[200:201], v[202:203] op_sel_hi:[1,0,0]\n"
"v_cvt_pk_f16_f32 v137, v178, v179\n"
"v_lshlrev_b32 v180, 16, v138\n"
"v_and_b32 v181, v237, v138\n"
"v_pk_fma_f32 v[180:181], v[180:181], v[200:201], v[202:203] op_sel_hi:[1,0,0]\n"
"v_cvt_pk_f16_f32 v138, v180, v181\n"
"v_lshlrev_b32 v182, 16, v139\n"
"v_and_b32 v183, v237, v139\n"
"v_pk_fma_f32 v[182:183], v[182:183], v[200:201], v[202:203] op_sel_hi:[1,0,0]\n"
"v_cvt_pk_f16_f32 v139, v182, v183\n"
"v_lshlrev_b32 v184, 16, v140\n"
"v_and_b32 v185, v237, v140\n"
"v_pk_fma_f32 v[184:185], v[184:185], v[204:205], v[206:207] op_sel_hi:[1,0,0]\n"
"v_cvt_pk_f16_f32 v140, v184, v185\n"
"v_lshlrev_b32 v186, 16, v141\n"
"v_and_b32 v187, v237, v141\n"
"v_pk_fma_f32 v[186:187], v[186:187], v[204:205], v[206:207] op_sel_hi:[1,0,0]\n"
"v_cvt_pk_f16_f32 v141, v186, v187\n"
"v_lshlrev_b32 v188, 16, v142\n"
"v_and_b32 v189, v237, v142\n"
"v_pk_fma_f32 v[188:189], v[188:189], v[204:205], v[206:207] op_sel_hi:[1,0,0]\n"
"v_cvt_pk_f16_f32 v142, v188, v189\n"
"v_lshlrev_b32 v190, 16, v143\n"
"v_and_b32 v191, v237, v143\n"
"v_pk_fma_f32 v[190:191], v[190:191], v[204:205], v[206:207] op_sel_hi:[1,0,0]\n"
"v_cvt_pk_f16_f32 v143, v190, v191\n"
"s_mov_b32 s19, 0x80000\n"
"s_waitcnt vmcnt(16)\n"
"AT_gchk:\n"
"v_xor_b32 v176, s17, v145\n"
"v_xor_b32 v177, s17, v147\n"
"v_xor_b32 v178, s17, v149\n"
"v_xor_b32 v179, s17, v151\n"
"v_xor_b32 v180, s17, v153\n"
"v_xor_b32 v181, s17, v155\n"
"v_xor_b32 v182, s17, v157\n"
"v_xor_b32 v183, s17, v159\n"
"v_or3_b32 v176, v176, v177, v178\n"
"v_or3_b32 v179, v179, v180, v181\n"
"v_or3_b32 v176, v176, v182, v183\n"
"v_or_b32 v176, v176, v179\n"
"v_cmp_ne_u32 s[56:57], 0, v176\n"
"v_cmp_gt_u32 vcc, 144, v234\n"
"s_nop 1\n"
"s_and_b64 vcc, vcc, s[56:57]\n"
"s_cbranch_vccz AT_gok\n"
"s_sub_u32 s19, s19, 1\n"
"s_cmp_eq_u32 s19, 0\n"
"s_cbranch_scc1 AT_gok\n"
"s_sleep 2\n"
"buffer_load_dwordx4 v[144:147], v225, s[36:39], s61 offen offset:0 sc1\n"
"buffer_load_dwordx4 v[148:151], v225, s[36:39], s61 offen offset:16 sc1\n"
"buffer_load_dwordx4 v[152:155], v225, s[36:39], s62 offen offset:0 sc1\n"
"buffer_load_dwordx4 v[156:159], v225, s[36:39], s62 offen offset:16 sc1\n"
"s_waitcnt vmcnt(0)\n"
"s_branch AT_gchk\n"
"AT_gok:\n"
"v_mov_b32 v236, 0x3e0293ee\n"
"v_mul_f32 v176, v236, v144\n"
"v_fma_f32 v180, v152, v236, v236\n"
"v_mul_f32 v177, v236, v146\n"
"v_fma_f32 v181, v154, v236, v236\n"
"v_mul_f32 v178, v236, v148\n"
"v_fma_f32 v182, v156, v236, v236\n"
"v_mul_f32 v179, v236, v150\n"
"v_fma_f32 v183, v158, v236, v236\n"
"ds_write_b128 v216, v[180:183]\n"
"ds_write_b128 v216, v[176:179] offset:128\n"
"s_waitcnt vmcnt(20) lgkmcnt(0)\n"
"s_barrier\n"
"ds_read_b128 v[144:147], v224 offset:0\n"
"ds_read_b128 v[148:151], v224 offset:64\n"
"ds_read_b128 v[152:155], v224 offset:128\n"
"ds_read_b128 v[156:159], v224 offset:192\n"
"ds_read_b128 v[160:163], v224 offset:256\n"
"ds_read_b128 v[164:167], v224 offset:320\n"
"ds_read_b128 v[168:171], v224 offset:384\n"
"ds_read_b128 v[172:175], v224 offset:448\n"
"ds_read_b64_tr_b16 a[160:161], v219 offset:32784\n"
"s_waitcnt lgkmcnt(6)\n"
"v_fma_mix_f32 v176, v0, v144, v152 op_sel:[0,0,0] op_sel_hi:[1,0,0]\n"
"v_fma_mix_f32 v177, v0, v145, v153 op_sel:[1,0,0] op_sel_hi:[1,0,0]\n"
"v_fma_mix_f32 v178, v1, v146, v154 op_sel:[0,0,0] op_sel_hi:[1,0,0]\n"
"v_fma_mix_f32 v179, v1, v147, v155 op_sel:[1,0,0] op_sel_hi:[1,0,0]\n"
"ds_read_b64_tr_b16 a[162:163], v219 offset:33808\n"
"s_waitcnt lgkmcnt(6)\n"
"v_fma_mix_f32 v180, v2, v148, v156 op_sel:[0,0,0] op_sel_hi:[1,0,0]\n"
"v_fma_mix_f32 v181, v2, v149, v157 op_sel:[1,0,0] op_sel_hi:[1,0,0]\n"
"v_fma_mix_f32 v182, v3, v150, v158 op_sel:[0,0,0] op_sel_hi:[1,0,0]\n"
"v_fma_mix_f32 v183, v3, v151, v159 op_sel:[1,0,0] op_sel_hi:[1,0,0]\n"
"ds_read_b64_tr_b16 a[164:165], v219 offset:36880\n"
"v_cvt_pk_bf16_f32 v0, v176, v177\n"
"v_cvt_pk_bf16_f32 v1, v178, v179\n"
"v_cvt_pk_bf16_f32 v2, v180, v181\n"
"v_cvt_pk_bf16_f32 v3, v182, v183\n"
"ds_read_b64_tr_b16 a[166:167], v219 offset:37904\n"
"v_fma_mix_f32 v184, v4, v144, v152 op_sel:[0,0,0] op_sel_hi:[1,0,0]\n"
"v_fma_mix_f32 v185, v4, v145, v153 op_sel:[1,0,0] op_sel_hi:[1,0,0]\n"
"v_fma_mix_f32 v186, v5, v146, v154 op_sel:[0,0,0] op_sel_hi:[1,0,0]\n"
"v_fma_mix_f32 v187, v5, v147, v155 op_sel:[1,0,0] op_sel_hi:[1,0,0]\n"
"ds_read_b64_tr_b16 a[168:169], v219 offset:32792\n"
"v_fma_mix_f32 v188, v6, v148, v156 op_sel:[0,0,0] op_sel_hi:[1,0,0]\n"
"v_fma_mix_f32 v189, v6, v149, v157 op_sel:[1,0,0] op_sel_hi:[1,0,0]\n"
"v_fma_mix_f32 v190, v7, v150, v158 op_sel:[0,0,0] op_sel_hi:[1,0,0]\n"
"v_fma_mix_f32 v191, v7, v151, v159 op_sel:[1,0,0] op_sel_hi:[1,0,0]\n"
"ds_read_b64_tr_b16 a[170:171], v219 offset:33816\n"
"ds_read_b128 v[144:147], v224 offset:512\n"
"ds_read_b128 v[148:151], v224 offset:576\n"
"ds_read_b128 v[152:155], v224 offset:640\n"
"ds_read_b128 v[156:159], v224 offset:704\n"
"ds_read_b64_tr_b16 a[172:173], v219 offset:36888\n"
"v_cvt_pk_bf16_f32 v4, v184, v185\n"
"v_cvt_pk_bf16_f32 v5, v186, v187\n"
"v_cvt_pk_bf16_f32 v6, v188, v189\n"
"v_cvt_pk_bf16_f32 v7, v190, v191\n"
"ds_read_b64_tr_b16 a[174:175], v219 offset:37912\n"
"s_waitcnt lgkmcnt(13)\n"
"v_fma_mix_f32 v176, v8, v160, v168 op_sel:[0,0,0] op_sel_hi:[1,0,0]\n"
"v_fma_mix_f32 v177, v8, v161, v169 op_sel:[1,0,0] op_sel_hi:[1,0,0]\n"
"v_fma_mix_f32 v178, v9, v162, v170 op_sel:[0,0,0] op_sel_hi:[1,0,0]\n"
"v_fma_mix_f32 v179, v9, v163, v171 op_sel:[1,0,0] op_sel_hi:[1,0,0]\n"
"ds_read_b64_tr_b16 a[176:177], v219 offset:33040\n"
"s_waitcnt lgkmcnt(13)\n"
"v_fma_mix_f32 v180, v10, v164, v172 op_sel:[0,0,0] op_sel_hi:[1,0,0]\n"
"v_fma_mix_f32 v181, v10, v165, v173 op_sel:[1,0,0] op_sel_hi:[1,0,0]\n"
"v_fma_mix_f32 v182, v11, v166, v174 op_sel:[0,0,0] op_sel_hi:[1,0,0]\n"
"v_fma_mix_f32 v183, v11, v167, v175 op_sel:[1,0,0] op_sel_hi:[1,0,0]\n"
"ds_read_b64_tr_b16 a[178:179], v219 offset:34064\n"
"v_cvt_pk_bf16_f32 v8, v176, v177\n"
"v_cvt_pk_bf16_f32 v9, v178, v179\n"
"v_cvt_pk_bf16_f32 v10, v180, v181\n"
"v_cvt_pk_bf16_f32 v11, v182, v183\n"
"ds_read_b64_tr_b16 a[180:181], v219 offset:37136\n"
"v_fma_mix_f32 v184, v12, v160, v168 op_sel:[0,0,0] op_sel_hi:[1,0,0]\n"
"v_fma_mix_f32 v185, v12, v161, v169 op_sel:[1,0,0] op_sel_hi:[1,0,0]\n"
"v_fma_mix_f32 v186, v13, v162, v170 op_sel:[0,0,0] op_sel_hi:[1,0,0]\n"
"v_fma_mix_f32 v187, v13, v163, v171 op_sel:[1,0,0] op_sel_hi:[1,0,0]\n"
"ds_read_b64_tr_b16 a[182:183], v219 offset:38160\n"
"v_fma_mix_f32 v188, v14, v164, v172 op_sel:[0,0,0] op_sel_hi:[1,0,0]\n"
"v_fma_mix_f32 v189, v14, v165, v173 op_sel:[1,0,0] op_sel_hi:[1,0,0]\n"
"v_fma_mix_f32 v190, v15, v166, v174 op_sel:[0,0,0] op_sel_hi:[1,0,0]\n"
"v_fma_mix_f32 v191, v15, v167, v175 op_sel:[1,0,0] op_sel_hi:[1,0,0]\n"
"ds_read_b64_tr_b16 a[184:185], v219 offset:33048\n"
"ds_read_b128 v[160:163], v224 offset:768\n"
"ds_read_b128 v[164:167], v224 offset:832\n"
"ds_read_b128 v[168:171], v224 offset:896\n"
"ds_read_b128 v[172:175], v224 offset:960\n"
"ds_read_b64_tr_b16 a[186:187], v219 offset:34072\n"
"v_cvt_pk_bf16_f32 v12, v184, v185\n"
"v_cvt_pk_bf16_f32 v13, v186, v187\n"
"v_cvt_pk_bf16_f32 v14, v188, v189\n"
"v_cvt_pk_bf16_f32 v15, v190, v191\n"
"ds_read_b64_tr_b16 a[188:189], v219 offset:37144\n"
"s_waitcnt lgkmcnt(14)\n"
"v_fma_mix_f32 v176, v16, v144, v152 op_sel:[0,0,0] op_sel_hi:[1,0,0]\n"
"v_fma_mix_f32 v177, v16, v145, v153 op_sel:[1,0,0] op_sel_hi:[1,0,0]\n"
"v_fma_mix_f32 v178, v17, v146, v154 op_sel:[0,0,0] op_sel_hi:[1,0,0]\n"
"v_fma_mix_f32 v179, v17, v147, v155 op_sel:[1,0,0] op_sel_hi:[1,0,0]\n"
"ds_read_b64_tr_b16 a[190:191], v219 offset:38168\n"
"s_waitcnt lgkmcnt(14)\n"
"v_fma_mix_f32 v180, v18, v148, v156 op_sel:[0,0,0] op_sel_hi:[1,0,0]\n"
"v_fma_mix_f32 v181, v18, v149, v157 op_sel:[1,0,0] op_sel_hi:[1,0,0]\n"
"v_fma_mix_f32 v182, v19, v150, v158 op_sel:[0,0,0] op_sel_hi:[1,0,0]\n"
"v_fma_mix_f32 v183, v19, v151, v159 op_sel:[1,0,0] op_sel_hi:[1,0,0]\n"
"ds_read_b64_tr_b16 a[224:225], v219 offset:33296\n"
"v_cvt_pk_bf16_f32 v16, v176, v177\n"
"v_cvt_pk_bf16_f32 v17, v178, v179\n"
"v_cvt_pk_bf16_f32 v18, v180, v181\n"
"v_cvt_pk_bf16_f32 v19, v182, v183\n"
"ds_read_b64_tr_b16 a[226:227], v219 offset:34320\n"
"v_fma_mix_f32 v184, v20, v144, v152 op_sel:[0,0,0] op_sel_hi:[1,0,0]\n"
"v_fma_mix_f32 v185, v20, v145, v153 op_sel:[1,0,0] op_sel_hi:[1,0,0]\n"
"v_fma_mix_f32 v186, v21, v146, v154 op_sel:[0,0,0] op_sel_hi:[1,0,0]\n"
"v_fma_mix_f32 v187, v21, v147, v155 op_sel:[1,0,0] op_sel_hi:[1,0,0]\n"
"ds_read_b64_tr_b16 a[228:229], v219 offset:37392\n"
"v_fma_mix_f32 v188, v22, v148, v156 op_sel:[0,0,0] op_sel_hi:[1,0,0]\n"
"v_fma_mix_f32 v189, v22, v149, v157 op_sel:[1,0,0] op_sel_hi:[1,0,0]\n"
"v_fma_mix_f32 v190, v23, v150, v158 op_sel:[0,0,0] op_sel_hi:[1,0,0]\n"
"v_fma_mix_f32 v191, v23, v151, v159 op_sel:[1,0,0] op_sel_hi:[1,0,0]\n"
"ds_read_b64_tr_b16 a[230:231], v219 offset:38416\n"
"ds_read_b128 v[144:147], v224 offset:1024\n"
"ds_read_b128 v[148:151], v224 offset:1088\n"
"ds_read_b128 v[152:155], v224 offset:1152\n"
"ds_read_b128 v[156:159], v224 offset:1216\n"
"v_mfma_f32_16x16x32_bf16 a[0:3], a[160:163], v[0:3], 0\n"
"ds_read_b64_tr_b16 a[232:233], v219 offset:33304\n"
"v_cvt_pk_bf16_f32 v20, v184, v185\n"
"v_mfma_f32_16x16x32_bf16 a[0:3], a[164:167], v[8:11], a[0:3]\n"
"ds_read_b64_tr_b16 a[234:235], v219 offset:34328\n"
"v_cvt_pk_bf16_f32 v21, v186, v187\n"
"v_mfma_f32_16x16x32_bf16 a[4:7], a[160:163], v[4:7], 0\n"
"s_waitcnt vmcnt(8)\n"
"s_barrier\n"
"ds_read_b64_tr_b16 a[236:237], v219 offset:37400\n"
"v_cvt_pk_bf16_f32 v22, v188, v189\n"
"v_mfma_f32_16x16x32_bf16 a[4:7], a[164:167], v[12:15], a[4:7]\n"
"ds_read_b64_tr_b16 a[238:239], v219 offset:38424\n"
"v_cvt_pk_bf16_f32 v23, v190, v191\n"
"v_mfma_f32_16x16x32_bf16 a[8:11], a[168:171], v[0:3], 0\n"
"s_waitcnt lgkmcnt(14)\n"
"s_add_u32 m0, s51, 65568\n"
"ds_read_b64_tr_b16 a[240:241], v219 offset:33552\n"
"v_fma_mix_f32 v176, v24, v160, v168 op_sel:[0,0,0] op_sel_hi:[1,0,0]\n"
"v_mfma_f32_16x16x32_bf16 a[8:11], a[172:175], v[8:11], a[8:11]\n"
"ds_read_b64_tr_b16 a[242:243], v219 offset:34576\n"
"buffer_load_dwordx4 v217, s[24:27], s50 offen offset:0 lds\n"
"v_mfma_f32_16x16x32_bf16 a[12:15], a[168:171], v[4:7], 0\n"
"ds_read_b64_tr_b16 a[244:245], v219 offset:37648\n"
"v_fma_mix_f32 v177, v24, v161, v169 op_sel:[1,0,0] op_sel_hi:[1,0,0]\n"
"v_mfma_f32_16x16x32_bf16 a[12:15], a[172:175], v[12:15], a[12:15]\n"
"ds_read_b64_tr_b16 a[246:247], v219 offset:38672\n"
"v_fma_mix_f32 v178, v25, v162, v170 op_sel:[0,0,0] op_sel_hi:[1,0,0]\n"
"v_mfma_f32_16x16x32_bf16 a[16:19], a[176:179], v[0:3], 0\n"
"ds_read_b64_tr_b16 a[248:249], v219 offset:33560\n"
"v_fma_mix_f32 v179, v25, v163, v171 op_sel:[1,0,0] op_sel_hi:[1,0,0]\n"
"v_mfma_f32_16x16x32_bf16 a[16:19], a[180:183], v[8:11], a[16:19]\n"
"ds_read_b64_tr_b16 a[250:251], v219 offset:34584\n"
"v_fma_mix_f32 v180, v26, v164, v172 op_sel:[0,0,0] op_sel_hi:[1,0,0]\n"
"v_mfma_f32_16x16x32_bf16 a[20:23], a[176:179], v[4:7], 0\n"
"s_add_u32 m0, s51, 73504\n"
"ds_read_b64_tr_b16 a[252:253], v219 offset:37656\n"
"v_fma_mix_f32 v181, v26, v165, v173 op_sel:[1,0,0] op_sel_hi:[1,0,0]\n"
"v_mfma_f32_16x16x32_bf16 a[20:23], a[180:183], v[12:15], a[20:23]\n"
"ds_read_b64_tr_b16 a[254:255], v219 offset:38680\n"
"buffer_load_dwordx4 v217, s[24:27], s50 offen offset:256 lds\n"
"v_mfma_f32_16x16x32_bf16 a[24:27], a[184:187], v[0:3], 0\n"
"s_waitcnt lgkmcnt(11)\n"
"s_add_u32 s50, s50, 32768\n"
"ds_read_b64_tr_b16 a[160:161], v219 offset:40976\n"
"v_fma_mix_f32 v182, v27, v166, v174 op_sel:[0,0,0] op_sel_hi:[1,0,0]\n"
"v_mfma_f32_16x16x32_bf16 a[24:27], a[188:191], v[8:11], a[24:27]\n"
"ds_read_b64_tr_b16 a[162:163], v219 offset:42000\n"
"v_fma_mix_f32 v183, v27, v167, v175 op_sel:[1,0,0] op_sel_hi:[1,0,0]\n"
"v_mfma_f32_16x16x32_bf16 a[28:31], a[184:187], v[4:7], 0\n"
"ds_read_b64_tr_b16 a[164:165], v219 offset:45072\n"
"v_cvt_pk_bf16_f32 v24, v176, v177\n"
"v_mfma_f32_16x16x32_bf16 a[28:31], a[188:191], v[12:15], a[28:31]\n"
"ds_read_b64_tr_b16 a[166:167], v219 offset:46096\n"
"v_cvt_pk_bf16_f32 v25, v178, v179\n"
"v_mfma_f32_16x16x32_bf16 a[32:35], a[224:227], v[0:3], 0\n"
"s_add_u32 m0, s51, 66592\n"
"ds_read_b64_tr_b16 a[168:169], v219 offset:40984\n"
"v_cvt_pk_bf16_f32 v26, v180, v181\n"
"v_mfma_f32_16x16x32_bf16 a[32:35], a[228:231], v[8:11], a[32:35]\n"
"ds_read_b64_tr_b16 a[170:171], v219 offset:42008\n"
"buffer_load_dwordx4 v217, s[24:27], s50 offen offset:0 lds\n"
"v_mfma_f32_16x16x32_bf16 a[36:39], a[224:227], v[4:7], 0\n"
"ds_read_b64_tr_b16 a[172:173], v219 offset:45080\n"
"v_cvt_pk_bf16_f32 v27, v182, v183\n"
"v_mfma_f32_16x16x32_bf16 a[36:39], a[228:231], v[12:15], a[36:39]\n"
"s_waitcnt lgkmcnt(11)\n"
"ds_read_b64_tr_b16 a[174:175], v219 offset:46104\n"
"v_fma_mix_f32 v184, v28, v160, v168 op_sel:[0,0,0] op_sel_hi:[1,0,0]\n"
"v_mfma_f32_16x16x32_bf16 a[40:43], a[232:235], v[0:3], 0\n"
"ds_read_b64_tr_b16 a[176:177], v219 offset:41232\n"
"v_fma_mix_f32 v185, v28, v161, v169 op_sel:[1,0,0] op_sel_hi:[1,0,0]\n"
"v_mfma_f32_16x16x32_bf16 a[40:43], a[236:239], v[8:11], a[40:43]\n"
"ds_read_b64_tr_b16 a[178:179], v219 offset:42256\n"
"v_fma_mix_f32 v186, v29, v162, v170 op_sel:[0,0,0] op_sel_hi:[1,0,0]\n"
"v_mfma_f32_16x16x32_bf16 a[44:47], a[232:235], v[4:7], 0\n"
"s_add_u32 m0, s51, 74528\n"
"ds_read_b64_tr_b16 a[180:181], v219 offset:45328\n"
"v_fma_mix_f32 v187, v29, v163, v171 op_sel:[1,0,0] op_sel_hi:[1,0,0]\n"
"v_mfma_f32_16x16x32_bf16 a[44:47], a[236:239], v[12:15], a[44:47]\n"
"ds_read_b64_tr_b16 a[182:183], v219 offset:46352\n"
"buffer_load_dwordx4 v217, s[24:27], s50 offen offset:256 lds\n"
"v_mfma_f32_16x16x32_bf16 a[48:51], a[240:243], v[0:3], 0\n"
"s_add_u32 s50, s50, 32768\n"
"ds_read_b64_tr_b16 a[184:185], v219 offset:41240\n"
"v_fma_mix_f32 v188, v30, v164, v172 op_sel:[0,0,0] op_sel_hi:[1,0,0]\n"
"v_mfma_f32_16x16x32_bf16 a[48:51], a[244:247], v[8:11], a[48:51]\n"
"ds_read_b64_tr_b16 a[186:187], v219 offset:42264\n"
"v_fma_mix_f32 v189, v30, v165, v173 op_sel:[1,0,0] op_sel_hi:[1,0,0]\n"
"v_mfma_f32_16x16x32_bf16 a[52:55], a[240:243], v[4:7], 0\n"
"ds_read_b64_tr_b16 a[188:189], v219 offset:45336\n"
"v_fma_mix_f32 v190, v31, v166, v174 op_sel:[0,0,0] op_sel_hi:[1,0,0]\n"
"v_mfma_f32_16x16x32_bf16 a[52:55], a[244:247], v[12:15], a[52:55]\n"
"s_waitcnt lgkmcnt(11)\n"
"ds_read_b64_tr_b16 a[190:191], v219 offset:46360\n"
"v_fma_mix_f32 v191, v31, v167, v175 op_sel:[1,0,0] op_sel_hi:[1,0,0]\n"
"v_mfma_f32_16x16x32_bf16 a[56:59], a[248:251], v[0:3], 0\n"
"s_add_u32 m0, s51, 81960\n"
"ds_read_b64_tr_b16 a[224:225], v219 offset:41488\n"
"ds_read_b128 v[160:163], v224 offset:1280\n"
"v_mfma_f32_16x16x32_bf16 a[56:59], a[252:255], v[8:11], a[56:59]\n"
"ds_read_b64_tr_b16 a[226:227], v219 offset:42512\n"
"buffer_load_dwordx4 v217, s[24:27], s50 offen offset:0 lds\n"
"v_mfma_f32_16x16x32_bf16 a[60:63], a[248:251], v[4:7], 0\n"
"ds_read_b64_tr_b16 a[228:229], v219 offset:45584\n"
"ds_read_b128 v[164:167], v224 offset:1344\n"
"v_mfma_f32_16x16x32_bf16 a[60:63], a[252:255], v[12:15], a[60:63]\n"
"ds_read_b64_tr_b16 a[230:231], v219 offset:46608\n"
"ds_read_b128 v[168:171], v224 offset:1408\n"
"v_mfma_f32_16x16x32_bf16 a[64:67], a[160:163], v[0:3], 0\n"
"ds_read_b64_tr_b16 a[232:233], v219 offset:41496\n"
"ds_read_b128 v[172:175], v224 offset:1472\n"
"v_mfma_f32_16x16x32_bf16 a[64:67], a[164:167], v[8:11], a[64:67]\n"
"ds_read_b64_tr_b16 a[234:235], v219 offset:42520\n"
"v_cvt_pk_bf16_f32 v28, v184, v185\n"
"v_mfma_f32_16x16x32_bf16 a[68:71], a[160:163], v[4:7], 0\n"
"s_add_u32 m0, s51, 89896\n"
"ds_read_b64_tr_b16 a[236:237], v219 offset:45592\n"
"v_cvt_pk_bf16_f32 v29, v186, v187\n"
"v_mfma_f32_16x16x32_bf16 a[68:71], a[164:167], v[12:15], a[68:71]\n"
"s_waitcnt lgkmcnt(14)\n"
"ds_read_b64_tr_b16 a[238:239], v219 offset:46616\n"
"buffer_load_dwordx4 v217, s[24:27], s50 offen offset:256 lds\n"
"v_mfma_f32_16x16x32_bf16 a[72:75], a[168:171], v[0:3], 0\n"
"s_add_u32 s50, s50, 32768\n"
"ds_read_b64_tr_b16 a[240:241], v219 offset:41744\n"
"v_cvt_pk_bf16_f32 v30, v188, v189\n"
"v_mfma_f32_16x16x32_bf16 a[72:75], a[172:175], v[8:11], a[72:75]\n"
"ds_read_b64_tr_b16 a[242:243], v219 offset:42768\n"
"v_cvt_pk_bf16_f32 v31, v190, v191\n"
"v_mfma_f32_16x16x32_bf16 a[76:79], a[168:171], v[4:7], 0\n"
"ds_read_b64_tr_b16 a[244:245], v219 offset:45840\n"
"v_fma_mix_f32 v176, v32, v144, v152 op_sel:[0,0,0] op_sel_hi:[1,0,0]\n"
"v_mfma_f32_16x16x32_bf16 a[76:79], a[172:175], v[12:15], a[76:79]\n"
"ds_read_b64_tr_b16 a[246:247], v219 offset:46864\n"
"v_fma_mix_f32 v177, v32, v145, v153 op_sel:[1,0,0] op_sel_hi:[1,0,0]\n"
"v_mfma_f32_16x16x32_bf16 a[80:83], a[176:179], v[0:3], 0\n"
"s_add_u32 m0, s51, 82984\n"
"ds_read_b64_tr_b16 a[248:249], v219 offset:41752\n"
"v_fma_mix_f32 v178, v33, v146, v154 op_sel:[0,0,0] op_sel_hi:[1,0,0]\n"
"v_mfma_f32_16x16x32_bf16 a[80:83], a[180:183], v[8:11], a[80:83]\n"
"ds_read_b64_tr_b16 a[250:251], v219 offset:42776\n"
"buffer_load_dwordx4 v217, s[24:27], s50 offen offset:0 lds\n"
"v_mfma_f32_16x16x32_bf16 a[84:87], a[176:179], v[4:7], 0\n"
"ds_read_b64_tr_b16 a[252:253], v219 offset:45848\n"
"v_fma_mix_f32 v179, v33, v147, v155 op_sel:[1,0,0] op_sel_hi:[1,0,0]\n"
"v_mfma_f32_16x16x32_bf16 a[84:87], a[180:183], v[12:15], a[84:87]\n"
"s_waitcnt lgkmcnt(12)\n"
"ds_read_b64_tr_b16 a[254:255], v219 offset:46872\n"
"v_fma_mix_f32 v180, v34, v148, v156 op_sel:[0,0,0] op_sel_hi:[1,0,0]\n"
"v_mfma_f32_16x16x32_bf16 a[88:91], a[184:187], v[0:3], 0\n"
"ds_read_b64_tr_b16 a[160:161], v218 offset:0\n"
"v_fma_mix_f32 v181, v34, v149, v157 op_sel:[1,0,0] op_sel_hi:[1,0,0]\n"
"v_mfma_f32_16x16x32_bf16 a[88:91], a[188:191], v[8:11], a[88:91]\n"
"ds_read_b64_tr_b16 a[162:163], v218 offset:1024\n"
"v_fma_mix_f32 v182, v35, v150, v158 op_sel:[0,0,0] op_sel_hi:[1,0,0]\n"
"v_mfma_f32_16x16x32_bf16 a[92:95], a[184:187], v[4:7], 0\n"
"s_add_u32 m0, s51, 90920\n"
"ds_read_b64_tr_b16 a[164:165], v218 offset:4096\n"
"v_fma_mix_f32 v183, v35, v151, v159 op_sel:[1,0,0] op_sel_hi:[1,0,0]\n"
"v_mfma_f32_16x16x32_bf16 a[92:95], a[188:191], v[12:15], a[92:95]\n"
"buffer_load_dwordx4 v217, s[24:27], s50 offen offset:256 lds\n"
"ds_read_b64_tr_b16 a[166:167], v218 offset:5120\n"
"v_mfma_f32_16x16x32_bf16 a[96:99], a[224:227], v[0:3], 0\n"
"s_add_u32 s50, s50, 425984\n"
"ds_read_b64_tr_b16 a[168:169], v218 offset:8\n"
"v_cvt_pk_bf16_f32 v32, v176, v177\n"
"v_mfma_f32_16x16x32_bf16 a[96:99], a[228:231], v[8:11], a[96:99]\n"
"ds_read_b64_tr_b16 a[170:171], v218 offset:1032\n"
"v_cvt_pk_bf16_f32 v33, v178, v179\n"
"v_mfma_f32_16x16x32_bf16 a[100:103], a[224:227], v[4:7], 0\n"
"ds_read_b64_tr_b16 a[172:173], v218 offset:4104\n"
"v_cvt_pk_bf16_f32 v34, v180, v181\n"
"v_mfma_f32_16x16x32_bf16 a[100:103], a[228:231], v[12:15], a[100:103]\n"
"s_waitcnt lgkmcnt(11)\n"
"ds_read_b64_tr_b16 a[174:175], v218 offset:5128\n"
"v_cvt_pk_bf16_f32 v35, v182, v183\n"
"v_mfma_f32_16x16x32_bf16 a[104:107], a[232:235], v[0:3], 0\n"
"ds_read_b64_tr_b16 a[176:177], v218 offset:256\n"
"v_fma_mix_f32 v184, v36, v144, v152 op_sel:[0,0,0] op_sel_hi:[1,0,0]\n"
"v_mfma_f32_16x16x32_bf16 a[104:107], a[236:239], v[8:11], a[104:107]\n"
"ds_read_b64_tr_b16 a[178:179], v218 offset:1280\n"
"v_fma_mix_f32 v185, v36, v145, v153 op_sel:[1,0,0] op_sel_hi:[1,0,0]\n"
"v_mfma_f32_16x16x32_bf16 a[108:111], a[232:235], v[4:7], 0\n"
"ds_read_b64_tr_b16 a[180:181], v218 offset:4352\n"
"v_fma_mix_f32 v186, v37, v146, v154 op_sel:[0,0,0] op_sel_hi:[1,0,0]\n"
"v_mfma_f32_16x16x32_bf16 a[108:111], a[236:239], v[12:15], a[108:111]\n"
"ds_read_b64_tr_b16 a[182:183], v218 offset:5376\n"
"v_fma_mix_f32 v187, v37, v147, v155 op_sel:[1,0,0] op_sel_hi:[1,0,0]\n"
"v_mfma_f32_16x16x32_bf16 a[112:115], a[240:243], v[0:3], 0\n"
"ds_read_b64_tr_b16 a[184:185], v218 offset:264\n"
"v_fma_mix_f32 v188, v38, v148, v156 op_sel:[0,0,0] op_sel_hi:[1,0,0]\n"
"v_mfma_f32_16x16x32_bf16 a[112:115], a[244:247], v[8:11], a[112:115]\n"
"ds_read_b64_tr_b16 a[186:187], v218 offset:1288\n"
"v_fma_mix_f32 v189, v38, v149, v157 op_sel:[1,0,0] op_sel_hi:[1,0,0]\n"
"v_mfma_f32_16x16x32_bf16 a[116:119], a[240:243], v[4:7], 0\n"
"ds_read_b64_tr_b16 a[188:189], v218 offset:4360\n"
"v_fma_mix_f32 v190, v39, v150, v158 op_sel:[0,0,0] op_sel_hi:[1,0,0]\n"
"v_mfma_f32_16x16x32_bf16 a[116:119], a[244:247], v[12:15], a[116:119]\n"
"s_waitcnt lgkmcnt(11)\n"
"ds_read_b64_tr_b16 a[190:191], v218 offset:5384\n"
"v_fma_mix_f32 v191, v39, v151, v159 op_sel:[1,0,0] op_sel_hi:[1,0,0]\n"
"v_mfma_f32_16x16x32_bf16 a[120:123], a[248:251], v[0:3], 0\n"
"ds_read_b64_tr_b16 a[224:225], v218 offset:512\n"
"ds_read_b128 v[144:147], v224 offset:1536\n"
"v_mfma_f32_16x16x32_bf16 a[120:123], a[252:255], v[8:11], a[120:123]\n"
"ds_read_b64_tr_b16 a[226:227], v218 offset:1536\n"
"ds_read_b128 v[148:151], v224 offset:1600\n"
"v_mfma_f32_16x16x32_bf16 a[124:127], a[248:251], v[4:7], 0\n"
"ds_read_b64_tr_b16 a[228:229], v218 offset:4608\n"
"ds_read_b128 v[152:155], v224 offset:1664\n"
"v_mfma_f32_16x16x32_bf16 a[124:127], a[252:255], v[12:15], a[124:127]\n"
"ds_read_b64_tr_b16 a[230:231], v218 offset:5632\n"
"ds_read_b128 v[156:159], v224 offset:1728\n"
"v_mfma_f32_16x16x32_bf16 a[0:3], a[160:163], v[16:19], a[0:3]\n"
"ds_read_b64_tr_b16 a[232:233], v218 offset:520\n"
"v_cvt_pk_bf16_f32 v36, v184, v185\n"
"v_mfma_f32_16x16x32_bf16 a[0:3], a[164:167], v[24:27], a[0:3]\n"
"ds_read_b64_tr_b16 a[234:235], v218 offset:1544\n"
"v_cvt_pk_bf16_f32 v37, v186, v187\n"
"v_mfma_f32_16x16x32_bf16 a[4:7], a[160:163], v[20:23], a[4:7]\n"
"s_waitcnt vmcnt(8)\n"
"s_barrier\n"
"ds_read_b64_tr_b16 a[236:237], v218 offset:4616\n"
"v_cvt_pk_bf16_f32 v38, v188, v189\n"
"v_mfma_f32_16x16x32_bf16 a[4:7], a[164:167], v[28:31], a[4:7]\n"
"s_waitcnt lgkmcnt(14)\n"
"ds_read_b64_tr_b16 a[238:239], v218 offset:5640\n"
"v_cvt_pk_bf16_f32 v39, v190, v191\n"
"v_mfma_f32_16x16x32_bf16 a[8:11], a[168:171], v[16:19], a[8:11]\n"
"s_add_u32 m0, s51, 98352\n"
"ds_read_b64_tr_b16 a[240:241], v218 offset:768\n"
"v_fma_mix_f32 v176, v40, v160, v168 op_sel:[0,0,0] op_sel_hi:[1,0,0]\n"
"v_mfma_f32_16x16x32_bf16 a[8:11], a[172:175], v[24:27], a[8:11]\n"
"ds_read_b64_tr_b16 a[242:243], v218 offset:1792\n"
"buffer_load_dwordx4 v217, s[24:27], s50 offen offset:0 lds\n"
"v_mfma_f32_16x16x32_bf16 a[12:15], a[168:171], v[20:23], a[12:15]\n"
"ds_read_b64_tr_b16 a[244:245], v218 offset:4864\n"
"v_fma_mix_f32 v177, v40, v161, v169 op_sel:[1,0,0] op_sel_hi:[1,0,0]\n"
"v_mfma_f32_16x16x32_bf16 a[12:15], a[172:175], v[28:31], a[12:15]\n"
"ds_read_b64_tr_b16 a[246:247], v218 offset:5888\n"
"v_fma_mix_f32 v178, v41, v162, v170 op_sel:[0,0,0] op_sel_hi:[1,0,0]\n"
"v_mfma_f32_16x16x32_bf16 a[16:19], a[176:179], v[16:19], a[16:19]\n"
"ds_read_b64_tr_b16 a[248:249], v218 offset:776\n"
"v_fma_mix_f32 v179, v41, v163, v171 op_sel:[1,0,0] op_sel_hi:[1,0,0]\n"
"v_mfma_f32_16x16x32_bf16 a[16:19], a[180:183], v[24:27], a[16:19]\n"
"ds_read_b64_tr_b16 a[250:251], v218 offset:1800\n"
"v_fma_mix_f32 v180, v42, v164, v172 op_sel:[0,0,0] op_sel_hi:[1,0,0]\n"
"v_mfma_f32_16x16x32_bf16 a[20:23], a[176:179], v[20:23], a[20:23]\n"
"s_add_u32 m0, s51, 106288\n"
"ds_read_b64_tr_b16 a[252:253], v218 offset:4872\n"
"v_fma_mix_f32 v181, v42, v165, v173 op_sel:[1,0,0] op_sel_hi:[1,0,0]\n"
"v_mfma_f32_16x16x32_bf16 a[20:23], a[180:183], v[28:31], a[20:23]\n"
"s_waitcnt lgkmcnt(11)\n"
"ds_read_b64_tr_b16 a[254:255], v218 offset:5896\n"
"buffer_load_dwordx4 v217, s[24:27], s50 offen offset:256 lds\n"
"v_mfma_f32_16x16x32_bf16 a[24:27], a[184:187], v[16:19], a[24:27]\n"
"s_add_u32 s50, s50, 32768\n"
"ds_read_b64_tr_b16 a[160:161], v218 offset:8192\n"
"v_fma_mix_f32 v182, v43, v166, v174 op_sel:[0,0,0] op_sel_hi:[1,0,0]\n"
"v_mfma_f32_16x16x32_bf16 a[24:27], a[188:191], v[24:27], a[24:27]\n"
"ds_read_b64_tr_b16 a[162:163], v218 offset:9216\n"
"v_fma_mix_f32 v183, v43, v167, v175 op_sel:[1,0,0] op_sel_hi:[1,0,0]\n"
"v_mfma_f32_16x16x32_bf16 a[28:31], a[184:187], v[20:23], a[28:31]\n"
"ds_read_b64_tr_b16 a[164:165], v218 offset:12288\n"
"v_cvt_pk_bf16_f32 v40, v176, v177\n"
"v_mfma_f32_16x16x32_bf16 a[28:31], a[188:191], v[28:31], a[28:31]\n"
"ds_read_b64_tr_b16 a[166:167], v218 offset:13312\n"
"v_cvt_pk_bf16_f32 v41, v178, v179\n"
"v_mfma_f32_16x16x32_bf16 a[32:35], a[224:227], v[16:19], a[32:35]\n"
"s_add_u32 m0, s51, 99376\n"
"ds_read_b64_tr_b16 a[168:169], v218 offset:8200\n"
"v_cvt_pk_bf16_f32 v42, v180, v181\n"
"v_mfma_f32_16x16x32_bf16 a[32:35], a[228:231], v[24:27], a[32:35]\n"
"ds_read_b64_tr_b16 a[170:171], v218 offset:9224\n"
"buffer_load_dwordx4 v217, s[24:27], s50 offen offset:0 lds\n"
"v_mfma_f32_16x16x32_bf16 a[36:39], a[224:227], v[20:23], a[36:39]\n"
"ds_read_b64_tr_b16 a[172:173], v218 offset:12296\n"
"v_cvt_pk_bf16_f32 v43, v182, v183\n"
"v_mfma_f32_16x16x32_bf16 a[36:39], a[228:231], v[28:31], a[36:39]\n"
"s_waitcnt lgkmcnt(11)\n"
"ds_read_b64_tr_b16 a[174:175], v218 offset:13320\n"
"v_fma_mix_f32 v184, v44, v160, v168 op_sel:[0,0,0] op_sel_hi:[1,0,0]\n"
"v_mfma_f32_16x16x32_bf16 a[40:43], a[232:235], v[16:19], a[40:43]\n"
"ds_read_b64_tr_b16 a[176:177], v218 offset:8448\n"
"v_fma_mix_f32 v185, v44, v161, v169 op_sel:[1,0,0] op_sel_hi:[1,0,0]\n"
"v_mfma_f32_16x16x32_bf16 a[40:43], a[236:239], v[24:27], a[40:43]\n"
"ds_read_b64_tr_b16 a[178:179], v218 offset:9472\n"
"v_fma_mix_f32 v186, v45, v162, v170 op_sel:[0,0,0] op_sel_hi:[1,0,0]\n"
"v_mfma_f32_16x16x32_bf16 a[44:47], a[232:235], v[20:23], a[44:47]\n"
"s_add_u32 m0, s51, 107312\n"
"ds_read_b64_tr_b16 a[180:181], v218 offset:12544\n"
"v_fma_mix_f32 v187, v45, v163, v171 op_sel:[1,0,0] op_sel_hi:[1,0,0]\n"
"v_mfma_f32_16x16x32_bf16 a[44:47], a[236:239], v[28:31], a[44:47]\n"
"ds_read_b64_tr_b16 a[182:183], v218 offset:13568\n"
"buffer_load_dwordx4 v217, s[24:27], s50 offen offset:256 lds\n"
"v_mfma_f32_16x16x32_bf16 a[48:51], a[240:243], v[16:19], a[48:51]\n"
"s_add_u32 s50, s50, 32768\n"
"ds_read_b64_tr_b16 a[184:185], v218 offset:8456\n"
"v_fma_mix_f32 v188, v46, v164, v172 op_sel:[0,0,0] op_sel_hi:[1,0,0]\n"
"v_mfma_f32_16x16x32_bf16 a[48:51], a[244:247], v[24:27], a[48:51]\n"
"ds_read_b64_tr_b16 a[186:187], v218 offset:9480\n"
"v_fma_mix_f32 v189, v46, v165, v173 op_sel:[1,0,0] op_sel_hi:[1,0,0]\n"
"v_mfma_f32_16x16x32_bf16 a[52:55], a[240:243], v[20:23], a[52:55]\n"
"ds_read_b64_tr_b16 a[188:189], v218 offset:12552\n"
"v_fma_mix_f32 v190, v47, v166, v174 op_sel:[0,0,0] op_sel_hi:[1,0,0]\n"
"v_mfma_f32_16x16x32_bf16 a[52:55], a[244:247], v[28:31], a[52:55]\n"
"s_waitcnt lgkmcnt(11)\n"
"ds_read_b64_tr_b16 a[190:191], v218 offset:13576\n"
"v_fma_mix_f32 v191, v47, v167, v175 op_sel:[1,0,0] op_sel_hi:[1,0,0]\n"
"v_mfma_f32_16x16x32_bf16 a[56:59], a[248:251], v[16:19], a[56:59]\n"
"s_add_u32 m0, s51, 114744\n"
"ds_read_b64_tr_b16 a[224:225], v218 offset:8704\n"
"ds_read_b128 v[160:163], v224 offset:1792\n"
"v_mfma_f32_16x16x32_bf16 a[56:59], a[252:255], v[24:27], a[56:59]\n"
"ds_read_b64_tr_b16 a[226:227], v218 offset:9728\n"
"buffer_load_dwordx4 v217, s[24:27], s50 offen offset:0 lds\n"
"v_mfma_f32_16x16x32_bf16 a[60:63], a[248:251], v[20:23], a[60:63]\n"
"ds_read_b64_tr_b16 a[228:229], v218 offset:12800\n"
"ds_read_b128 v[164:167], v224 offset:1856\n"
"v_mfma_f32_16x16x32_bf16 a[60:63], a[252:255], v[28:31], a[60:63]\n"
"ds_read_b64_tr_b16 a[230:231], v218 offset:13824\n"
"ds_read_b128 v[168:171], v224 offset:1920\n"
"v_mfma_f32_16x16x32_bf16 a[64:67], a[160:163], v[16:19], a[64:67]\n"
"ds_read_b64_tr_b16 a[232:233], v218 offset:8712\n"
"ds_read_b128 v[172:175], v224 offset:1984\n"
"v_mfma_f32_16x16x32_bf16 a[64:67], a[164:167], v[24:27], a[64:67]\n"
"ds_read_b64_tr_b16 a[234:235], v218 offset:9736\n"
"v_cvt_pk_bf16_f32 v44, v184, v185\n"
"v_mfma_f32_16x16x32_bf16 a[68:71], a[160:163], v[20:23], a[68:71]\n"
"s_add_u32 m0, s51, 122680\n"
"ds_read_b64_tr_b16 a[236:237], v218 offset:12808\n"
"v_cvt_pk_bf16_f32 v45, v186, v187\n"
"v_mfma_f32_16x16x32_bf16 a[68:71], a[164:167], v[28:31], a[68:71]\n"
"s_waitcnt lgkmcnt(14)\n"
"ds_read_b64_tr_b16 a[238:239], v218 offset:13832\n"
"buffer_load_dwordx4 v217, s[24:27], s50 offen offset:256 lds\n"
"v_mfma_f32_16x16x32_bf16 a[72:75], a[168:171], v[16:19], a[72:75]\n"
"s_add_u32 s50, s50, 32768\n"
"ds_read_b64_tr_b16 a[240:241], v218 offset:8960\n"
"v_cvt_pk_bf16_f32 v46, v188, v189\n"
"v_mfma_f32_16x16x32_bf16 a[72:75], a[172:175], v[24:27], a[72:75]\n"
"ds_read_b64_tr_b16 a[242:243], v218 offset:9984\n"
"v_cvt_pk_bf16_f32 v47, v190, v191\n"
"v_mfma_f32_16x16x32_bf16 a[76:79], a[168:171], v[20:23], a[76:79]\n"
"ds_read_b64_tr_b16 a[244:245], v218 offset:13056\n"
"v_fma_mix_f32 v176, v48, v144, v152 op_sel:[0,0,0] op_sel_hi:[1,0,0]\n"
"v_mfma_f32_16x16x32_bf16 a[76:79], a[172:175], v[28:31], a[76:79]\n"
"ds_read_b64_tr_b16 a[246:247], v218 offset:14080\n"
"v_fma_mix_f32 v177, v48, v145, v153 op_sel:[1,0,0] op_sel_hi:[1,0,0]\n"
"v_mfma_f32_16x16x32_bf16 a[80:83], a[176:179], v[16:19], a[80:83]\n"
"s_add_u32 m0, s51, 115768\n"
"ds_read_b64_tr_b16 a[248:249], v218 offset:8968\n"
"v_fma_mix_f32 v178, v49, v146, v154 op_sel:[0,0,0] op_sel_hi:[1,0,0]\n"
"v_mfma_f32_16x16x32_bf16 a[80:83], a[180:183], v[24:27], a[80:83]\n"
"ds_read_b64_tr_b16 a[250:251], v218 offset:9992\n"
"buffer_load_dwordx4 v217, s[24:27], s50 offen offset:0 lds\n"
"v_mfma_f32_16x16x32_bf16 a[84:87], a[176:179], v[20:23], a[84:87]\n"
"ds_read_b64_tr_b16 a[252:253], v218 offset:13064\n"
"v_fma_mix_f32 v179, v49, v147, v155 op_sel:[1,0,0] op_sel_hi:[1,0,0]\n"
"v_mfma_f32_16x16x32_bf16 a[84:87], a[180:183], v[28:31], a[84:87]\n"
"s_waitcnt lgkmcnt(12)\n"
"ds_read_b64_tr_b16 a[254:255], v218 offset:14088\n"
"v_fma_mix_f32 v180, v50, v148, v156 op_sel:[0,0,0] op_sel_hi:[1,0,0]\n"
"v_mfma_f32_16x16x32_bf16 a[88:91], a[184:187], v[16:19], a[88:91]\n"
"ds_read_b64_tr_b16 a[160:161], v218 offset:32784\n"
"v_fma_mix_f32 v181, v50, v149, v157 op_sel:[1,0,0] op_sel_hi:[1,0,0]\n"
"v_mfma_f32_16x16x32_bf16 a[88:91], a[188:191], v[24:27], a[88:91]\n"
"ds_read_b64_tr_b16 a[162:163], v218 offset:33808\n"
"v_fma_mix_f32 v182, v51, v150, v158 op_sel:[0,0,0] op_sel_hi:[1,0,0]\n"
"v_mfma_f32_16x16x32_bf16 a[92:95], a[184:187], v[20:23], a[92:95]\n"
"s_add_u32 m0, s51, 123704\n"
"ds_read_b64_tr_b16 a[164:165], v218 offset:36880\n"
"v_fma_mix_f32 v183, v51, v151, v159 op_sel:[1,0,0] op_sel_hi:[1,0,0]\n"
"v_mfma_f32_16x16x32_bf16 a[92:95], a[188:191], v[28:31], a[92:95]\n"
"buffer_load_dwordx4 v217, s[24:27], s50 offen offset:256 lds\n"
"ds_read_b64_tr_b16 a[166:167], v218 offset:37904\n"
"v_mfma_f32_16x16x32_bf16 a[96:99], a[224:227], v[16:19], a[96:99]\n"
"s_add_u32 s50, s50, 425984\n"
"ds_read_b64_tr_b16 a[168:169], v218 offset:32792\n"
"v_cvt_pk_bf16_f32 v48, v176, v177\n"
"v_mfma_f32_16x16x32_bf16 a[96:99], a[228:231], v[24:27], a[96:99]\n"
"ds_read_b64_tr_b16 a[170:171], v218 offset:33816\n"
"v_cvt_pk_bf16_f32 v49, v178, v179\n"
"v_mfma_f32_16x16x32_bf16 a[100:103], a[224:227], v[20:23], a[100:103]\n"
"ds_read_b64_tr_b16 a[172:173], v218 offset:36888\n"
"v_cvt_pk_bf16_f32 v50, v180, v181\n"
"v_mfma_f32_16x16x32_bf16 a[100:103], a[228:231], v[28:31], a[100:103]\n"
"s_waitcnt lgkmcnt(11)\n"
"ds_read_b64_tr_b16 a[174:175], v218 offset:37912\n"
"v_cvt_pk_bf16_f32 v51, v182, v183\n"
"v_mfma_f32_16x16x32_bf16 a[104:107], a[232:235], v[16:19], a[104:107]\n"
"ds_read_b64_tr_b16 a[176:177], v218 offset:33040\n"
"v_fma_mix_f32 v184, v52, v144, v152 op_sel:[0,0,0] op_sel_hi:[1,0,0]\n"
"v_mfma_f32_16x16x32_bf16 a[104:107], a[236:239], v[24:27], a[104:107]\n"
"ds_read_b64_tr_b16 a[178:179], v218 offset:34064\n"
"v_fma_mix_f32 v185, v52, v145, v153 op_sel:[1,0,0] op_sel_hi:[1,0,0]\n"
"v_mfma_f32_16x16x32_bf16 a[108:111], a[232:235], v[20:23], a[108:111]\n"
"ds_read_b64_tr_b16 a[180:181], v218 offset:37136\n"
"v_fma_mix_f32 v186, v53, v146, v154 op_sel:[0,0,0] op_sel_hi:[1,0,0]\n"
"v_mfma_f32_16x16x32_bf16 a[108:111], a[236:239], v[28:31], a[108:111]\n"
"ds_read_b64_tr_b16 a[182:183], v218 offset:38160\n"
"v_fma_mix_f32 v187, v53, v147, v155 op_sel:[1,0,0] op_sel_hi:[1,0,0]\n"
"v_mfma_f32_16x16x32_bf16 a[112:115], a[240:243], v[16:19], a[112:115]\n"
"ds_read_b64_tr_b16 a[184:185], v218 offset:33048\n"
"v_fma_mix_f32 v188, v54, v148, v156 op_sel:[0,0,0] op_sel_hi:[1,0,0]\n"
"v_mfma_f32_16x16x32_bf16 a[112:115], a[244:247], v[24:27], a[112:115]\n"
"ds_read_b64_tr_b16 a[186:187], v218 offset:34072\n"
"v_fma_mix_f32 v189, v54, v149, v157 op_sel:[1,0,0] op_sel_hi:[1,0,0]\n"
"v_mfma_f32_16x16x32_bf16 a[116:119], a[240:243], v[20:23], a[116:119]\n"
"ds_read_b64_tr_b16 a[188:189], v218 offset:37144\n"
"v_fma_mix_f32 v190, v55, v150, v158 op_sel:[0,0,0] op_sel_hi:[1,0,0]\n"
"v_mfma_f32_16x16x32_bf16 a[116:119], a[244:247], v[28:31], a[116:119]\n"
"s_waitcnt lgkmcnt(11)\n"
"ds_read_b64_tr_b16 a[190:191], v218 offset:38168\n"
"v_fma_mix_f32 v191, v55, v151, v159 op_sel:[1,0,0] op_sel_hi:[1,0,0]\n"
"v_mfma_f32_16x16x32_bf16 a[120:123], a[248:251], v[16:19], a[120:123]\n"
"ds_read_b64_tr_b16 a[224:225], v218 offset:33296\n"
"ds_read_b128 v[144:147], v224 offset:2048\n"
"v_mfma_f32_16x16x32_bf16 a[120:123], a[252:255], v[24:27], a[120:123]\n"
"ds_read_b64_tr_b16 a[226:227], v218 offset:34320\n"
"ds_read_b128 v[148:151], v224 offset:2112\n"
"v_mfma_f32_16x16x32_bf16 a[124:127], a[248:251], v[20:23], a[124:127]\n"
"ds_read_b64_tr_b16 a[228:229], v218 offset:37392\n"
"ds_read_b128 v[152:155], v224 offset:2176\n"
"v_mfma_f32_16x16x32_bf16 a[124:127], a[252:255], v[28:31], a[124:127]\n"
"ds_read_b64_tr_b16 a[230:231], v218 offset:38416\n"
"ds_read_b128 v[156:159], v224 offset:2240\n"
"v_mfma_f32_16x16x32_bf16 a[0:3], a[160:163], v[32:35], a[0:3]\n"
"ds_read_b64_tr_b16 a[232:233], v218 offset:33304\n"
"v_cvt_pk_bf16_f32 v52, v184, v185\n"
"v_mfma_f32_16x16x32_bf16 a[0:3], a[164:167], v[40:43], a[0:3]\n"
"ds_read_b64_tr_b16 a[234:235], v218 offset:34328\n"
"v_cvt_pk_bf16_f32 v53, v186, v187\n"
"v_mfma_f32_16x16x32_bf16 a[4:7], a[160:163], v[36:39], a[4:7]\n"
"s_waitcnt vmcnt(8)\n"
"s_barrier\n"
"ds_read_b64_tr_b16 a[236:237], v218 offset:37400\n"
"v_cvt_pk_bf16_f32 v54, v188, v189\n"
"v_mfma_f32_16x16x32_bf16 a[4:7], a[164:167], v[44:47], a[4:7]\n"
"s_waitcnt lgkmcnt(14)\n"
"ds_read_b64_tr_b16 a[238:239], v218 offset:38424\n"
"v_cvt_pk_bf16_f32 v55, v190, v191\n"
"v_mfma_f32_16x16x32_bf16 a[8:11], a[168:171], v[32:35], a[8:11]\n"
"s_mov_b32 m0, s51\n"
"ds_read_b64_tr_b16 a[240:241], v218 offset:33552\n"
"v_fma_mix_f32 v176, v56, v160, v168 op_sel:[0,0,0] op_sel_hi:[1,0,0]\n"
"v_mfma_f32_16x16x32_bf16 a[8:11], a[172:175], v[40:43], a[8:11]\n"
"ds_read_b64_tr_b16 a[242:243], v218 offset:34576\n"
"buffer_load_dwordx4 v217, s[24:27], s50 offen offset:0 lds\n"
"v_mfma_f32_16x16x32_bf16 a[12:15], a[168:171], v[36:39], a[12:15]\n"
"ds_read_b64_tr_b16 a[244:245], v218 offset:37648\n"
"v_fma_mix_f32 v177, v56, v161, v169 op_sel:[1,0,0] op_sel_hi:[1,0,0]\n"
"v_mfma_f32_16x16x32_bf16 a[12:15], a[172:175], v[44:47], a[12:15]\n"
"ds_read_b64_tr_b16 a[246:247], v218 offset:38672\n"
"v_fma_mix_f32 v178, v57, v162, v170 op_sel:[0,0,0] op_sel_hi:[1,0,0]\n"
"v_mfma_f32_16x16x32_bf16 a[16:19], a[176:179], v[32:35], a[16:19]\n"
"ds_read_b64_tr_b16 a[248:249], v218 offset:33560\n"
"v_fma_mix_f32 v179, v57, v163, v171 op_sel:[1,0,0] op_sel_hi:[1,0,0]\n"
"v_mfma_f32_16x16x32_bf16 a[16:19], a[180:183], v[40:43], a[16:19]\n"
"ds_read_b64_tr_b16 a[250:251], v218 offset:34584\n"
"v_fma_mix_f32 v180, v58, v164, v172 op_sel:[0,0,0] op_sel_hi:[1,0,0]\n"
"v_mfma_f32_16x16x32_bf16 a[20:23], a[176:179], v[36:39], a[20:23]\n"
"s_add_u32 m0, s51, 7936\n"
"ds_read_b64_tr_b16 a[252:253], v218 offset:37656\n"
"v_fma_mix_f32 v181, v58, v165, v173 op_sel:[1,0,0] op_sel_hi:[1,0,0]\n"
"v_mfma_f32_16x16x32_bf16 a[20:23], a[180:183], v[44:47], a[20:23]\n"
"s_waitcnt lgkmcnt(11)\n"
"ds_read_b64_tr_b16 a[254:255], v218 offset:38680\n"
"buffer_load_dwordx4 v217, s[24:27], s50 offen offset:256 lds\n"
"v_mfma_f32_16x16x32_bf16 a[24:27], a[184:187], v[32:35], a[24:27]\n"
"s_add_u32 s50, s50, 32768\n"
"ds_read_b64_tr_b16 a[160:161], v218 offset:40976\n"
"v_fma_mix_f32 v182, v59, v166, v174 op_sel:[0,0,0] op_sel_hi:[1,0,0]\n"
"v_mfma_f32_16x16x32_bf16 a[24:27], a[188:191], v[40:43], a[24:27]\n"
"ds_read_b64_tr_b16 a[162:163], v218 offset:42000\n"
"v_fma_mix_f32 v183, v59, v167, v175 op_sel:[1,0,0] op_sel_hi:[1,0,0]\n"
"v_mfma_f32_16x16x32_bf16 a[28:31], a[184:187], v[36:39], a[28:31]\n"
"ds_read_b64_tr_b16 a[164:165], v218 offset:45072\n"
"v_cvt_pk_bf16_f32 v56, v176, v177\n"
"v_mfma_f32_16x16x32_bf16 a[28:31], a[188:191], v[44:47], a[28:31]\n"
"ds_read_b64_tr_b16 a[166:167], v218 offset:46096\n"
"v_cvt_pk_bf16_f32 v57, v178, v179\n"
"v_mfma_f32_16x16x32_bf16 a[32:35], a[224:227], v[32:35], a[32:35]\n"
"s_add_u32 m0, s51, 1024\n"
"ds_read_b64_tr_b16 a[168:169], v218 offset:40984\n"
"v_cvt_pk_bf16_f32 v58, v180, v181\n"
"v_mfma_f32_16x16x32_bf16 a[32:35], a[228:231], v[40:43], a[32:35]\n"
"ds_read_b64_tr_b16 a[170:171], v218 offset:42008\n"
"buffer_load_dwordx4 v217, s[24:27], s50 offen offset:0 lds\n"
"v_mfma_f32_16x16x32_bf16 a[36:39], a[224:227], v[36:39], a[36:39]\n"
"ds_read_b64_tr_b16 a[172:173], v218 offset:45080\n"
"v_cvt_pk_bf16_f32 v59, v182, v183\n"
"v_mfma_f32_16x16x32_bf16 a[36:39], a[228:231], v[44:47], a[36:39]\n"
"s_waitcnt lgkmcnt(11)\n"
"ds_read_b64_tr_b16 a[174:175], v218 offset:46104\n"
"v_fma_mix_f32 v184, v60, v160, v168 op_sel:[0,0,0] op_sel_hi:[1,0,0]\n"
"v_mfma_f32_16x16x32_bf16 a[40:43], a[232:235], v[32:35], a[40:43]\n"
"ds_read_b64_tr_b16 a[176:177], v218 offset:41232\n"
"v_fma_mix_f32 v185, v60, v161, v169 op_sel:[1,0,0] op_sel_hi:[1,0,0]\n"
"v_mfma_f32_16x16x32_bf16 a[40:43], a[236:239], v[40:43], a[40:43]\n"
"ds_read_b64_tr_b16 a[178:179], v218 offset:42256\n"
"v_fma_mix_f32 v186, v61, v162, v170 op_sel:[0,0,0] op_sel_hi:[1,0,0]\n"
"v_mfma_f32_16x16x32_bf16 a[44:47], a[232:235], v[36:39], a[44:47]\n"
"s_add_u32 m0, s51, 8960\n"
"ds_read_b64_tr_b16 a[180:181], v218 offset:45328\n"
"v_fma_mix_f32 v187, v61, v163, v171 op_sel:[1,0,0] op_sel_hi:[1,0,0]\n"
"v_mfma_f32_16x16x32_bf16 a[44:47], a[236:239], v[44:47], a[44:47]\n"
"ds_read_b64_tr_b16 a[182:183], v218 offset:46352\n"
"buffer_load_dwordx4 v217, s[24:27], s50 offen offset:256 lds\n"
"v_mfma_f32_16x16x32_bf16 a[48:51], a[240:243], v[32:35], a[48:51]\n"
"s_add_u32 s50, s50, 32768\n"
"ds_read_b64_tr_b16 a[184:185], v218 offset:41240\n"
"v_fma_mix_f32 v188, v62, v164, v172 op_sel:[0,0,0] op_sel_hi:[1,0,0]\n"
"v_mfma_f32_16x16x32_bf16 a[48:51], a[244:247], v[40:43], a[48:51]\n"
"ds_read_b64_tr_b16 a[186:187], v218 offset:42264\n"
"v_fma_mix_f32 v189, v62, v165, v173 op_sel:[1,0,0] op_sel_hi:[1,0,0]\n"
"v_mfma_f32_16x16x32_bf16 a[52:55], a[240:243], v[36:39], a[52:55]\n"
"ds_read_b64_tr_b16 a[188:189], v218 offset:45336\n"
"v_fma_mix_f32 v190, v63, v166, v174 op_sel:[0,0,0] op_sel_hi:[1,0,0]\n"
"v_mfma_f32_16x16x32_bf16 a[52:55], a[244:247], v[44:47], a[52:55]\n"
"s_waitcnt lgkmcnt(11)\n"
"ds_read_b64_tr_b16 a[190:191], v218 offset:46360\n"
"v_fma_mix_f32 v191, v63, v167, v175 op_sel:[1,0,0] op_sel_hi:[1,0,0]\n"
"v_mfma_f32_16x16x32_bf16 a[56:59], a[248:251], v[32:35], a[56:59]\n"
"s_add_u32 m0, s51, 16392\n"
"ds_read_b64_tr_b16 a[224:225], v218 offset:41488\n"
"ds_read_b128 v[160:163], v224 offset:2304\n"
"v_mfma_f32_16x16x32_bf16 a[56:59], a[252:255], v[40:43], a[56:59]\n"
"ds_read_b64_tr_b16 a[226:227], v218 offset:42512\n"
"buffer_load_dwordx4 v217, s[24:27], s50 offen offset:0 lds\n"
"v_mfma_f32_16x16x32_bf16 a[60:63], a[248:251], v[36:39], a[60:63]\n"
"ds_read_b64_tr_b16 a[228:229], v218 offset:45584\n"
"ds_read_b128 v[164:167], v224 offset:2368\n"
"v_mfma_f32_16x16x32_bf16 a[60:63], a[252:255], v[44:47], a[60:63]\n"
"ds_read_b64_tr_b16 a[230:231], v218 offset:46608\n"
"ds_read_b128 v[168:171], v224 offset:2432\n"
"v_mfma_f32_16x16x32_bf16 a[64:67], a[160:163], v[32:35], a[64:67]\n"
"ds_read_b64_tr_b16 a[232:233], v218 offset:41496\n"
"ds_read_b128 v[172:175], v224 offset:2496\n"
"v_mfma_f32_16x16x32_bf16 a[64:67], a[164:167], v[40:43], a[64:67]\n"
"ds_read_b64_tr_b16 a[234:235], v218 offset:42520\n"
"v_cvt_pk_bf16_f32 v60, v184, v185\n"
"v_mfma_f32_16x16x32_bf16 a[68:71], a[160:163], v[36:39], a[68:71]\n"
"s_add_u32 m0, s51, 24328\n"
"ds_read_b64_tr_b16 a[236:237], v218 offset:45592\n"
"v_cvt_pk_bf16_f32 v61, v186, v187\n"
"v_mfma_f32_16x16x32_bf16 a[68:71], a[164:167], v[44:47], a[68:71]\n"
"s_waitcnt lgkmcnt(14)\n"
"ds_read_b64_tr_b16 a[238:239], v218 offset:46616\n"
"buffer_load_dwordx4 v217, s[24:27], s50 offen offset:256 lds\n"
"v_mfma_f32_16x16x32_bf16 a[72:75], a[168:171], v[32:35], a[72:75]\n"
"s_add_u32 s50, s50, 32768\n"
"ds_read_b64_tr_b16 a[240:241], v218 offset:41744\n"
"v_cvt_pk_bf16_f32 v62, v188, v189\n"
"v_mfma_f32_16x16x32_bf16 a[72:75], a[172:175], v[40:43], a[72:75]\n"
"ds_read_b64_tr_b16 a[242:243], v218 offset:42768\n"
"v_cvt_pk_bf16_f32 v63, v190, v191\n"
"v_mfma_f32_16x16x32_bf16 a[76:79], a[168:171], v[36:39], a[76:79]\n"
"ds_read_b64_tr_b16 a[244:245], v218 offset:45840\n"
"v_fma_mix_f32 v176, v64, v144, v152 op_sel:[0,0,0] op_sel_hi:[1,0,0]\n"
"v_mfma_f32_16x16x32_bf16 a[76:79], a[172:175], v[44:47], a[76:79]\n"
"ds_read_b64_tr_b16 a[246:247], v218 offset:46864\n"
"v_fma_mix_f32 v177, v64, v145, v153 op_sel:[1,0,0] op_sel_hi:[1,0,0]\n"
"v_mfma_f32_16x16x32_bf16 a[80:83], a[176:179], v[32:35], a[80:83]\n"
"s_add_u32 m0, s51, 17416\n"
"ds_read_b64_tr_b16 a[248:249], v218 offset:41752\n"
"v_fma_mix_f32 v178, v65, v146, v154 op_sel:[0,0,0] op_sel_hi:[1,0,0]\n"
"v_mfma_f32_16x16x32_bf16 a[80:83], a[180:183], v[40:43], a[80:83]\n"
"ds_read_b64_tr_b16 a[250:251], v218 offset:42776\n"
"buffer_load_dwordx4 v217, s[24:27], s50 offen offset:0 lds\n"
"v_mfma_f32_16x16x32_bf16 a[84:87], a[176:179], v[36:39], a[84:87]\n"
"ds_read_b64_tr_b16 a[252:253], v218 offset:45848\n"
"v_fma_mix_f32 v179, v65, v147, v155 op_sel:[1,0,0] op_sel_hi:[1,0,0]\n"
"v_mfma_f32_16x16x32_bf16 a[84:87], a[180:183], v[44:47], a[84:87]\n"
"s_waitcnt lgkmcnt(12)\n"
"ds_read_b64_tr_b16 a[254:255], v218 offset:46872\n"
"v_fma_mix_f32 v180, v66, v148, v156 op_sel:[0,0,0] op_sel_hi:[1,0,0]\n"
"v_mfma_f32_16x16x32_bf16 a[88:91], a[184:187], v[32:35], a[88:91]\n"
"ds_read_b64_tr_b16 a[160:161], v219 offset:0\n"
"v_fma_mix_f32 v181, v66, v149, v157 op_sel:[1,0,0] op_sel_hi:[1,0,0]\n"
"v_mfma_f32_16x16x32_bf16 a[88:91], a[188:191], v[40:43], a[88:91]\n"
"ds_read_b64_tr_b16 a[162:163], v219 offset:1024\n"
"v_fma_mix_f32 v182, v67, v150, v158 op_sel:[0,0,0] op_sel_hi:[1,0,0]\n"
"v_mfma_f32_16x16x32_bf16 a[92:95], a[184:187], v[36:39], a[92:95]\n"
"s_add_u32 m0, s51, 25352\n"
"ds_read_b64_tr_b16 a[164:165], v219 offset:4096\n"
"v_fma_mix_f32 v183, v67, v151, v159 op_sel:[1,0,0] op_sel_hi:[1,0,0]\n"
"v_mfma_f32_16x16x32_bf16 a[92:95], a[188:191], v[44:47], a[92:95]\n"
"buffer_load_dwordx4 v217, s[24:27], s50 offen offset:256 lds\n"
"ds_read_b64_tr_b16 a[166:167], v219 offset:5120\n"
"v_mfma_f32_16x16x32_bf16 a[96:99], a[224:227], v[32:35], a[96:99]\n"
"s_add_u32 s50, s50, 425984\n"
"ds_read_b64_tr_b16 a[168:169], v219 offset:8\n"
"v_cvt_pk_bf16_f32 v64, v176, v177\n"
"v_mfma_f32_16x16x32_bf16 a[96:99], a[228:231], v[40:43], a[96:99]\n"
"ds_read_b64_tr_b16 a[170:171], v219 offset:1032\n"
"v_cvt_pk_bf16_f32 v65, v178, v179\n"
"v_mfma_f32_16x16x32_bf16 a[100:103], a[224:227], v[36:39], a[100:103]\n"
"ds_read_b64_tr_b16 a[172:173], v219 offset:4104\n"
"v_cvt_pk_bf16_f32 v66, v180, v181\n"
"v_mfma_f32_16x16x32_bf16 a[100:103], a[228:231], v[44:47], a[100:103]\n"
"s_waitcnt lgkmcnt(11)\n"
"ds_read_b64_tr_b16 a[174:175], v219 offset:5128\n"
"v_cvt_pk_bf16_f32 v67, v182, v183\n"
"v_mfma_f32_16x16x32_bf16 a[104:107], a[232:235], v[32:35], a[104:107]\n"
"ds_read_b64_tr_b16 a[176:177], v219 offset:256\n"
"v_fma_mix_f32 v184, v68, v144, v152 op_sel:[0,0,0] op_sel_hi:[1,0,0]\n"
"v_mfma_f32_16x16x32_bf16 a[104:107], a[236:239], v[40:43], a[104:107]\n"
"ds_read_b64_tr_b16 a[178:179], v219 offset:1280\n"
"v_fma_mix_f32 v185, v68, v145, v153 op_sel:[1,0,0] op_sel_hi:[1,0,0]\n"
"v_mfma_f32_16x16x32_bf16 a[108:111], a[232:235], v[36:39], a[108:111]\n"
"ds_read_b64_tr_b16 a[180:181], v219 offset:4352\n"
"v_fma_mix_f32 v186, v69, v146, v154 op_sel:[0,0,0] op_sel_hi:[1,0,0]\n"
"v_mfma_f32_16x16x32_bf16 a[108:111], a[236:239], v[44:47], a[108:111]\n"
"ds_read_b64_tr_b16 a[182:183], v219 offset:5376\n"
"v_fma_mix_f32 v187, v69, v147, v155 op_sel:[1,0,0] op_sel_hi:[1,0,0]\n"
"v_mfma_f32_16x16x32_bf16 a[112:115], a[240:243], v[32:35], a[112:115]\n"
"ds_read_b64_tr_b16 a[184:185], v219 offset:264\n"
"v_fma_mix_f32 v188, v70, v148, v156 op_sel:[0,0,0] op_sel_hi:[1,0,0]\n"
"v_mfma_f32_16x16x32_bf16 a[112:115], a[244:247], v[40:43], a[112:115]\n"
"ds_read_b64_tr_b16 a[186:187], v219 offset:1288\n"
"v_fma_mix_f32 v189, v70, v149, v157 op_sel:[1,0,0] op_sel_hi:[1,0,0]\n"
"v_mfma_f32_16x16x32_bf16 a[116:119], a[240:243], v[36:39], a[116:119]\n"
"ds_read_b64_tr_b16 a[188:189], v219 offset:4360\n"
"v_fma_mix_f32 v190, v71, v150, v158 op_sel:[0,0,0] op_sel_hi:[1,0,0]\n"
"v_mfma_f32_16x16x32_bf16 a[116:119], a[244:247], v[44:47], a[116:119]\n"
"s_waitcnt lgkmcnt(11)\n"
"ds_read_b64_tr_b16 a[190:191], v219 offset:5384\n"
"v_fma_mix_f32 v191, v71, v151, v159 op_sel:[1,0,0] op_sel_hi:[1,0,0]\n"
"v_mfma_f32_16x16x32_bf16 a[120:123], a[248:251], v[32:35], a[120:123]\n"
"ds_read_b64_tr_b16 a[224:225], v219 offset:512\n"
"ds_read_b128 v[144:147], v224 offset:2560\n"
"v_mfma_f32_16x16x32_bf16 a[120:123], a[252:255], v[40:43], a[120:123]\n"
"ds_read_b64_tr_b16 a[226:227], v219 offset:1536\n"
"ds_read_b128 v[148:151], v224 offset:2624\n"
"v_mfma_f32_16x16x32_bf16 a[124:127], a[248:251], v[36:39], a[124:127]\n"
"ds_read_b64_tr_b16 a[228:229], v219 offset:4608\n"
"ds_read_b128 v[152:155], v224 offset:2688\n"
"v_mfma_f32_16x16x32_bf16 a[124:127], a[252:255], v[44:47], a[124:127]\n"
"ds_read_b64_tr_b16 a[230:231], v219 offset:5632\n"
"ds_read_b128 v[156:159], v224 offset:2752\n"
"v_mfma_f32_16x16x32_bf16 a[0:3], a[160:163], v[48:51], a[0:3]\n"
"ds_read_b64_tr_b16 a[232:233], v219 offset:520\n"
"v_cvt_pk_bf16_f32 v68, v184, v185\n"
"v_mfma_f32_16x16x32_bf16 a[0:3], a[164:167], v[56:59], a[0:3]\n"
"ds_read_b64_tr_b16 a[234:235], v219 offset:1544\n"
"v_cvt_pk_bf16_f32 v69, v186, v187\n"
"v_mfma_f32_16x16x32_bf16 a[4:7], a[160:163], v[52:55], a[4:7]\n"
"s_waitcnt vmcnt(8)\n"
"s_barrier\n"
"ds_read_b64_tr_b16 a[236:237], v219 offset:4616\n"
"v_cvt_pk_bf16_f32 v70, v188, v189\n"
"v_mfma_f32_16x16x32_bf16 a[4:7], a[164:167], v[60:63], a[4:7]\n"
"s_waitcnt lgkmcnt(14)\n"
"ds_read_b64_tr_b16 a[238:239], v219 offset:5640\n"
"v_cvt_pk_bf16_f32 v71, v190, v191\n"
"v_mfma_f32_16x16x32_bf16 a[8:11], a[168:171], v[48:51], a[8:11]\n"
"s_add_u32 m0, s51, 32784\n"
"ds_read_b64_tr_b16 a[240:241], v219 offset:768\n"
"v_fma_mix_f32 v176, v72, v160, v168 op_sel:[0,0,0] op_sel_hi:[1,0,0]\n"
"v_mfma_f32_16x16x32_bf16 a[8:11], a[172:175], v[56:59], a[8:11]\n"
"ds_read_b64_tr_b16 a[242:243], v219 offset:1792\n"
"buffer_load_dwordx4 v217, s[24:27], s50 offen offset:0 lds\n"
"v_mfma_f32_16x16x32_bf16 a[12:15], a[168:171], v[52:55], a[12:15]\n"
"ds_read_b64_tr_b16 a[244:245], v219 offset:4864\n"
"v_fma_mix_f32 v177, v72, v161, v169 op_sel:[1,0,0] op_sel_hi:[1,0,0]\n"
"v_mfma_f32_16x16x32_bf16 a[12:15], a[172:175], v[60:63], a[12:15]\n"
"ds_read_b64_tr_b16 a[246:247], v219 offset:5888\n"
"v_fma_mix_f32 v178, v73, v162, v170 op_sel:[0,0,0] op_sel_hi:[1,0,0]\n"
"v_mfma_f32_16x16x32_bf16 a[16:19], a[176:179], v[48:51], a[16:19]\n"
"ds_read_b64_tr_b16 a[248:249], v219 offset:776\n"
"v_fma_mix_f32 v179, v73, v163, v171 op_sel:[1,0,0] op_sel_hi:[1,0,0]\n"
"v_mfma_f32_16x16x32_bf16 a[16:19], a[180:183], v[56:59], a[16:19]\n"
"ds_read_b64_tr_b16 a[250:251], v219 offset:1800\n"
"v_fma_mix_f32 v180, v74, v164, v172 op_sel:[0,0,0] op_sel_hi:[1,0,0]\n"
"v_mfma_f32_16x16x32_bf16 a[20:23], a[176:179], v[52:55], a[20:23]\n"
"s_add_u32 m0, s51, 40720\n"
"ds_read_b64_tr_b16 a[252:253], v219 offset:4872\n"
"v_fma_mix_f32 v181, v74, v165, v173 op_sel:[1,0,0] op_sel_hi:[1,0,0]\n"
"v_mfma_f32_16x16x32_bf16 a[20:23], a[180:183], v[60:63], a[20:23]\n"
"s_waitcnt lgkmcnt(11)\n"
"ds_read_b64_tr_b16 a[254:255], v219 offset:5896\n"
"buffer_load_dwordx4 v217, s[24:27], s50 offen offset:256 lds\n"
"v_mfma_f32_16x16x32_bf16 a[24:27], a[184:187], v[48:51], a[24:27]\n"
"s_add_u32 s50, s50, 32768\n"
"ds_read_b64_tr_b16 a[160:161], v219 offset:8192\n"
"v_fma_mix_f32 v182, v75, v166, v174 op_sel:[0,0,0] op_sel_hi:[1,0,0]\n"
"v_mfma_f32_16x16x32_bf16 a[24:27], a[188:191], v[56:59], a[24:27]\n"
"ds_read_b64_tr_b16 a[162:163], v219 offset:9216\n"
"v_fma_mix_f32 v183, v75, v167, v175 op_sel:[1,0,0] op_sel_hi:[1,0,0]\n"
"v_mfma_f32_16x16x32_bf16 a[28:31], a[184:187], v[52:55], a[28:31]\n"
"ds_read_b64_tr_b16 a[164:165], v219 offset:12288\n"
"v_cvt_pk_bf16_f32 v72, v176, v177\n"
"v_mfma_f32_16x16x32_bf16 a[28:31], a[188:191], v[60:63], a[28:31]\n"
"ds_read_b64_tr_b16 a[166:167], v219 offset:13312\n"
"v_cvt_pk_bf16_f32 v73, v178, v179\n"
"v_mfma_f32_16x16x32_bf16 a[32:35], a[224:227], v[48:51], a[32:35]\n"
"s_add_u32 m0, s51, 33808\n"
"ds_read_b64_tr_b16 a[168:169], v219 offset:8200\n"
"v_cvt_pk_bf16_f32 v74, v180, v181\n"
"v_mfma_f32_16x16x32_bf16 a[32:35], a[228:231], v[56:59], a[32:35]\n"
"ds_read_b64_tr_b16 a[170:171], v219 offset:9224\n"
"buffer_load_dwordx4 v217, s[24:27], s50 offen offset:0 lds\n"
"v_mfma_f32_16x16x32_bf16 a[36:39], a[224:227], v[52:55], a[36:39]\n"
"ds_read_b64_tr_b16 a[172:173], v219 offset:12296\n"
"v_cvt_pk_bf16_f32 v75, v182, v183\n"
"v_mfma_f32_16x16x32_bf16 a[36:39], a[228:231], v[60:63], a[36:39]\n"
"s_waitcnt lgkmcnt(11)\n"
"ds_read_b64_tr_b16 a[174:175], v219 offset:13320\n"
"v_fma_mix_f32 v184, v76, v160, v168 op_sel:[0,0,0] op_sel_hi:[1,0,0]\n"
"v_mfma_f32_16x16x32_bf16 a[40:43], a[232:235], v[48:51], a[40:43]\n"
"ds_read_b64_tr_b16 a[176:177], v219 offset:8448\n"
"v_fma_mix_f32 v185, v76, v161, v169 op_sel:[1,0,0] op_sel_hi:[1,0,0]\n"
"v_mfma_f32_16x16x32_bf16 a[40:43], a[236:239], v[56:59], a[40:43]\n"
"ds_read_b64_tr_b16 a[178:179], v219 offset:9472\n"
"v_fma_mix_f32 v186, v77, v162, v170 op_sel:[0,0,0] op_sel_hi:[1,0,0]\n"
"v_mfma_f32_16x16x32_bf16 a[44:47], a[232:235], v[52:55], a[44:47]\n"
"s_add_u32 m0, s51, 41744\n"
"ds_read_b64_tr_b16 a[180:181], v219 offset:12544\n"
"v_fma_mix_f32 v187, v77, v163, v171 op_sel:[1,0,0] op_sel_hi:[1,0,0]\n"
"v_mfma_f32_16x16x32_bf16 a[44:47], a[236:239], v[60:63], a[44:47]\n"
"ds_read_b64_tr_b16 a[182:183], v219 offset:13568\n"
"buffer_load_dwordx4 v217, s[24:27], s50 offen offset:256 lds\n"
"v_mfma_f32_16x16x32_bf16 a[48:51], a[240:243], v[48:51], a[48:51]\n"
"s_add_u32 s50, s50, 32768\n"
"ds_read_b64_tr_b16 a[184:185], v219 offset:8456\n"
"v_fma_mix_f32 v188, v78, v164, v172 op_sel:[0,0,0] op_sel_hi:[1,0,0]\n"
"v_mfma_f32_16x16x32_bf16 a[48:51], a[244:247], v[56:59], a[48:51]\n"
"ds_read_b64_tr_b16 a[186:187], v219 offset:9480\n"
"v_fma_mix_f32 v189, v78, v165, v173 op_sel:[1,0,0] op_sel_hi:[1,0,0]\n"
"v_mfma_f32_16x16x32_bf16 a[52:55], a[240:243], v[52:55], a[52:55]\n"
"ds_read_b64_tr_b16 a[188:189], v219 offset:12552\n"
"v_fma_mix_f32 v190, v79, v166, v174 op_sel:[0,0,0] op_sel_hi:[1,0,0]\n"
"v_mfma_f32_16x16x32_bf16 a[52:55], a[244:247], v[60:63], a[52:55]\n"
"s_waitcnt lgkmcnt(11)\n"
"ds_read_b64_tr_b16 a[190:191], v219 offset:13576\n"
"v_fma_mix_f32 v191, v79, v167, v175 op_sel:[1,0,0] op_sel_hi:[1,0,0]\n"
"v_mfma_f32_16x16x32_bf16 a[56:59], a[248:251], v[48:51], a[56:59]\n"
"s_add_u32 m0, s51, 49176\n"
"ds_read_b64_tr_b16 a[224:225], v219 offset:8704\n"
"ds_read_b128 v[160:163], v224 offset:2816\n"
"v_mfma_f32_16x16x32_bf16 a[56:59], a[252:255], v[56:59], a[56:59]\n"
"ds_read_b64_tr_b16 a[226:227], v219 offset:9728\n"
"buffer_load_dwordx4 v217, s[24:27], s50 offen offset:0 lds\n"
"v_mfma_f32_16x16x32_bf16 a[60:63], a[248:251], v[52:55], a[60:63]\n"
"ds_read_b64_tr_b16 a[228:229], v219 offset:12800\n"
"ds_read_b128 v[164:167], v224 offset:2880\n"
"v_mfma_f32_16x16x32_bf16 a[60:63], a[252:255], v[60:63], a[60:63]\n"
"ds_read_b64_tr_b16 a[230:231], v219 offset:13824\n"
"ds_read_b128 v[168:171], v224 offset:2944\n"
"v_mfma_f32_16x16x32_bf16 a[64:67], a[160:163], v[48:51], a[64:67]\n"
"ds_read_b64_tr_b16 a[232:233], v219 offset:8712\n"
"ds_read_b128 v[172:175], v224 offset:3008\n"
"v_mfma_f32_16x16x32_bf16 a[64:67], a[164:167], v[56:59], a[64:67]\n"
"ds_read_b64_tr_b16 a[234:235], v219 offset:9736\n"
"v_cvt_pk_bf16_f32 v76, v184, v185\n"
"v_mfma_f32_16x16x32_bf16 a[68:71], a[160:163], v[52:55], a[68:71]\n"
"s_add_u32 m0, s51, 57112\n"
"ds_read_b64_tr_b16 a[236:237], v219 offset:12808\n"
"v_cvt_pk_bf16_f32 v77, v186, v187\n"
"v_mfma_f32_16x16x32_bf16 a[68:71], a[164:167], v[60:63], a[68:71]\n"
"s_waitcnt lgkmcnt(14)\n"
"ds_read_b64_tr_b16 a[238:239], v219 offset:13832\n"
"buffer_load_dwordx4 v217, s[24:27], s50 offen offset:256 lds\n"
"v_mfma_f32_16x16x32_bf16 a[72:75], a[168:171], v[48:51], a[72:75]\n"
"s_add_u32 s50, s50, 32768\n"
"ds_read_b64_tr_b16 a[240:241], v219 offset:8960\n"
"v_cvt_pk_bf16_f32 v78, v188, v189\n"
"v_mfma_f32_16x16x32_bf16 a[72:75], a[172:175], v[56:59], a[72:75]\n"
"ds_read_b64_tr_b16 a[242:243], v219 offset:9984\n"
"v_cvt_pk_bf16_f32 v79, v190, v191\n"
"v_mfma_f32_16x16x32_bf16 a[76:79], a[168:171], v[52:55], a[76:79]\n"
"ds_read_b64_tr_b16 a[244:245], v219 offset:13056\n"
"v_fma_mix_f32 v176, v80, v144, v152 op_sel:[0,0,0] op_sel_hi:[1,0,0]\n"
"v_mfma_f32_16x16x32_bf16 a[76:79], a[172:175], v[60:63], a[76:79]\n"
"ds_read_b64_tr_b16 a[246:247], v219 offset:14080\n"
"v_fma_mix_f32 v177, v80, v145, v153 op_sel:[1,0,0] op_sel_hi:[1,0,0]\n"
"v_mfma_f32_16x16x32_bf16 a[80:83], a[176:179], v[48:51], a[80:83]\n"
"s_add_u32 m0, s51, 50200\n"
"ds_read_b64_tr_b16 a[248:249], v219 offset:8968\n"
"v_fma_mix_f32 v178, v81, v146, v154 op_sel:[0,0,0] op_sel_hi:[1,0,0]\n"
"v_mfma_f32_16x16x32_bf16 a[80:83], a[180:183], v[56:59], a[80:83]\n"
"ds_read_b64_tr_b16 a[250:251], v219 offset:9992\n"
"buffer_load_dwordx4 v217, s[24:27], s50 offen offset:0 lds\n"
"v_mfma_f32_16x16x32_bf16 a[84:87], a[176:179], v[52:55], a[84:87]\n"
"ds_read_b64_tr_b16 a[252:253], v219 offset:13064\n"
"v_fma_mix_f32 v179, v81, v147, v155 op_sel:[1,0,0] op_sel_hi:[1,0,0]\n"
"v_mfma_f32_16x16x32_bf16 a[84:87], a[180:183], v[60:63], a[84:87]\n"
"s_waitcnt lgkmcnt(12)\n"
"ds_read_b64_tr_b16 a[254:255], v219 offset:14088\n"
"v_fma_mix_f32 v180, v82, v148, v156 op_sel:[0,0,0] op_sel_hi:[1,0,0]\n"
"v_mfma_f32_16x16x32_bf16 a[88:91], a[184:187], v[48:51], a[88:91]\n"
"ds_read_b64_tr_b16 a[160:161], v219 offset:32784\n"
"v_fma_mix_f32 v181, v82, v149, v157 op_sel:[1,0,0] op_sel_hi:[1,0,0]\n"
"v_mfma_f32_16x16x32_bf16 a[88:91], a[188:191], v[56:59], a[88:91]\n"
"ds_read_b64_tr_b16 a[162:163], v219 offset:33808\n"
"v_fma_mix_f32 v182, v83, v150, v158 op_sel:[0,0,0] op_sel_hi:[1,0,0]\n"
"v_mfma_f32_16x16x32_bf16 a[92:95], a[184:187], v[52:55], a[92:95]\n"
"s_add_u32 m0, s51, 58136\n"
"ds_read_b64_tr_b16 a[164:165], v219 offset:36880\n"
"v_fma_mix_f32 v183, v83, v151, v159 op_sel:[1,0,0] op_sel_hi:[1,0,0]\n"
"v_mfma_f32_16x16x32_bf16 a[92:95], a[188:191], v[60:63], a[92:95]\n"
"buffer_load_dwordx4 v217, s[24:27], s50 offen offset:256 lds\n"
"ds_read_b64_tr_b16 a[166:167], v219 offset:37904\n"
"v_mfma_f32_16x16x32_bf16 a[96:99], a[224:227], v[48:51], a[96:99]\n"
"s_add_u32 s50, s50, 425984\n"
"ds_read_b64_tr_b16 a[168:169], v219 offset:32792\n"
"v_cvt_pk_bf16_f32 v80, v176, v177\n"
"v_mfma_f32_16x16x32_bf16 a[96:99], a[228:231], v[56:59], a[96:99]\n"
"ds_read_b64_tr_b16 a[170:171], v219 offset:33816\n"
"v_cvt_pk_bf16_f32 v81, v178, v179\n"
"v_mfma_f32_16x16x32_bf16 a[100:103], a[224:227], v[52:55], a[100:103]\n"
"ds_read_b64_tr_b16 a[172:173], v219 offset:36888\n"
"v_cvt_pk_bf16_f32 v82, v180, v181\n"
"v_mfma_f32_16x16x32_bf16 a[100:103], a[228:231], v[60:63], a[100:103]\n"
"s_waitcnt lgkmcnt(11)\n"
"ds_read_b64_tr_b16 a[174:175], v219 offset:37912\n"
"v_cvt_pk_bf16_f32 v83, v182, v183\n"
"v_mfma_f32_16x16x32_bf16 a[104:107], a[232:235], v[48:51], a[104:107]\n"
"ds_read_b64_tr_b16 a[176:177], v219 offset:33040\n"
"v_fma_mix_f32 v184, v84, v144, v152 op_sel:[0,0,0] op_sel_hi:[1,0,0]\n"
"v_mfma_f32_16x16x32_bf16 a[104:107], a[236:239], v[56:59], a[104:107]\n"
"ds_read_b64_tr_b16 a[178:179], v219 offset:34064\n"
"v_fma_mix_f32 v185, v84, v145, v153 op_sel:[1,0,0] op_sel_hi:[1,0,0]\n"
"v_mfma_f32_16x16x32_bf16 a[108:111], a[232:235], v[52:55], a[108:111]\n"
"ds_read_b64_tr_b16 a[180:181], v219 offset:37136\n"
"v_fma_mix_f32 v186, v85, v146, v154 op_sel:[0,0,0] op_sel_hi:[1,0,0]\n"
"v_mfma_f32_16x16x32_bf16 a[108:111], a[236:239], v[60:63], a[108:111]\n"
"ds_read_b64_tr_b16 a[182:183], v219 offset:38160\n"
"v_fma_mix_f32 v187, v85, v147, v155 op_sel:[1,0,0] op_sel_hi:[1,0,0]\n"
"v_mfma_f32_16x16x32_bf16 a[112:115], a[240:243], v[48:51], a[112:115]\n"
"ds_read_b64_tr_b16 a[184:185], v219 offset:33048\n"
"v_fma_mix_f32 v188, v86, v148, v156 op_sel:[0,0,0] op_sel_hi:[1,0,0]\n"
"v_mfma_f32_16x16x32_bf16 a[112:115], a[244:247], v[56:59], a[112:115]\n"
"ds_read_b64_tr_b16 a[186:187], v219 offset:34072\n"
"v_fma_mix_f32 v189, v86, v149, v157 op_sel:[1,0,0] op_sel_hi:[1,0,0]\n"
"v_mfma_f32_16x16x32_bf16 a[116:119], a[240:243], v[52:55], a[116:119]\n"
"ds_read_b64_tr_b16 a[188:189], v219 offset:37144\n"
"v_fma_mix_f32 v190, v87, v150, v158 op_sel:[0,0,0] op_sel_hi:[1,0,0]\n"
"v_mfma_f32_16x16x32_bf16 a[116:119], a[244:247], v[60:63], a[116:119]\n"
"s_waitcnt lgkmcnt(11)\n"
"ds_read_b64_tr_b16 a[190:191], v219 offset:38168\n"
"v_fma_mix_f32 v191, v87, v151, v159 op_sel:[1,0,0] op_sel_hi:[1,0,0]\n"
"v_mfma_f32_16x16x32_bf16 a[120:123], a[248:251], v[48:51], a[120:123]\n"
"ds_read_b64_tr_b16 a[224:225], v219 offset:33296\n"
"ds_read_b128 v[144:147], v224 offset:3072\n"
"v_mfma_f32_16x16x32_bf16 a[120:123], a[252:255], v[56:59], a[120:123]\n"
"ds_read_b64_tr_b16 a[226:227], v219 offset:34320\n"
"ds_read_b128 v[148:151], v224 offset:3136\n"
"v_mfma_f32_16x16x32_bf16 a[124:127], a[248:251], v[52:55], a[124:127]\n"
"ds_read_b64_tr_b16 a[228:229], v219 offset:37392\n"
"ds_read_b128 v[152:155], v224 offset:3200\n"
"v_mfma_f32_16x16x32_bf16 a[124:127], a[252:255], v[60:63], a[124:127]\n"
"ds_read_b64_tr_b16 a[230:231], v219 offset:38416\n"
"ds_read_b128 v[156:159], v224 offset:3264\n"
"v_mfma_f32_16x16x32_bf16 a[0:3], a[160:163], v[64:67], a[0:3]\n"
"ds_read_b64_tr_b16 a[232:233], v219 offset:33304\n"
"v_cvt_pk_bf16_f32 v84, v184, v185\n"
"v_mfma_f32_16x16x32_bf16 a[0:3], a[164:167], v[72:75], a[0:3]\n"
"ds_read_b64_tr_b16 a[234:235], v219 offset:34328\n"
"v_cvt_pk_bf16_f32 v85, v186, v187\n"
"v_mfma_f32_16x16x32_bf16 a[4:7], a[160:163], v[68:71], a[4:7]\n"
"s_waitcnt vmcnt(8)\n"
"s_barrier\n"
"ds_read_b64_tr_b16 a[236:237], v219 offset:37400\n"
"v_cvt_pk_bf16_f32 v86, v188, v189\n"
"v_mfma_f32_16x16x32_bf16 a[4:7], a[164:167], v[76:79], a[4:7]\n"
"s_waitcnt lgkmcnt(14)\n"
"ds_read_b64_tr_b16 a[238:239], v219 offset:38424\n"
"v_cvt_pk_bf16_f32 v87, v190, v191\n"
"v_mfma_f32_16x16x32_bf16 a[8:11], a[168:171], v[64:67], a[8:11]\n"
"s_add_u32 m0, s51, 65568\n"
"ds_read_b64_tr_b16 a[240:241], v219 offset:33552\n"
"v_fma_mix_f32 v176, v88, v160, v168 op_sel:[0,0,0] op_sel_hi:[1,0,0]\n"
"v_mfma_f32_16x16x32_bf16 a[8:11], a[172:175], v[72:75], a[8:11]\n"
"ds_read_b64_tr_b16 a[242:243], v219 offset:34576\n"
"buffer_load_dwordx4 v217, s[24:27], s50 offen offset:0 lds\n"
"v_mfma_f32_16x16x32_bf16 a[12:15], a[168:171], v[68:71], a[12:15]\n"
"ds_read_b64_tr_b16 a[244:245], v219 offset:37648\n"
"v_fma_mix_f32 v177, v88, v161, v169 op_sel:[1,0,0] op_sel_hi:[1,0,0]\n"
"v_mfma_f32_16x16x32_bf16 a[12:15], a[172:175], v[76:79], a[12:15]\n"
"ds_read_b64_tr_b16 a[246:247], v219 offset:38672\n"
"v_fma_mix_f32 v178, v89, v162, v170 op_sel:[0,0,0] op_sel_hi:[1,0,0]\n"
"v_mfma_f32_16x16x32_bf16 a[16:19], a[176:179], v[64:67], a[16:19]\n"
"ds_read_b64_tr_b16 a[248:249], v219 offset:33560\n"
"v_fma_mix_f32 v179, v89, v163, v171 op_sel:[1,0,0] op_sel_hi:[1,0,0]\n"
"v_mfma_f32_16x16x32_bf16 a[16:19], a[180:183], v[72:75], a[16:19]\n"
"ds_read_b64_tr_b16 a[250:251], v219 offset:34584\n"
"v_fma_mix_f32 v180, v90, v164, v172 op_sel:[0,0,0] op_sel_hi:[1,0,0]\n"
"v_mfma_f32_16x16x32_bf16 a[20:23], a[176:179], v[68:71], a[20:23]\n"
"s_add_u32 m0, s51, 73504\n"
"ds_read_b64_tr_b16 a[252:253], v219 offset:37656\n"
"v_fma_mix_f32 v181, v90, v165, v173 op_sel:[1,0,0] op_sel_hi:[1,0,0]\n"
"v_mfma_f32_16x16x32_bf16 a[20:23], a[180:183], v[76:79], a[20:23]\n"
"s_waitcnt lgkmcnt(11)\n"
"ds_read_b64_tr_b16 a[254:255], v219 offset:38680\n"
"buffer_load_dwordx4 v217, s[24:27], s50 offen offset:256 lds\n"
"v_mfma_f32_16x16x32_bf16 a[24:27], a[184:187], v[64:67], a[24:27]\n"
"s_add_u32 s50, s50, 32768\n"
"ds_read_b64_tr_b16 a[160:161], v219 offset:40976\n"
"v_fma_mix_f32 v182, v91, v166, v174 op_sel:[0,0,0] op_sel_hi:[1,0,0]\n"
"v_mfma_f32_16x16x32_bf16 a[24:27], a[188:191], v[72:75], a[24:27]\n"
"ds_read_b64_tr_b16 a[162:163], v219 offset:42000\n"
"v_fma_mix_f32 v183, v91, v167, v175 op_sel:[1,0,0] op_sel_hi:[1,0,0]\n"
"v_mfma_f32_16x16x32_bf16 a[28:31], a[184:187], v[68:71], a[28:31]\n"
"ds_read_b64_tr_b16 a[164:165], v219 offset:45072\n"
"v_cvt_pk_bf16_f32 v88, v176, v177\n"
"v_mfma_f32_16x16x32_bf16 a[28:31], a[188:191], v[76:79], a[28:31]\n"
"ds_read_b64_tr_b16 a[166:167], v219 offset:46096\n"
"v_cvt_pk_bf16_f32 v89, v178, v179\n"
"v_mfma_f32_16x16x32_bf16 a[32:35], a[224:227], v[64:67], a[32:35]\n"
"s_add_u32 m0, s51, 66592\n"
"ds_read_b64_tr_b16 a[168:169], v219 offset:40984\n"
"v_cvt_pk_bf16_f32 v90, v180, v181\n"
"v_mfma_f32_16x16x32_bf16 a[32:35], a[228:231], v[72:75], a[32:35]\n"
"ds_read_b64_tr_b16 a[170:171], v219 offset:42008\n"
"buffer_load_dwordx4 v217, s[24:27], s50 offen offset:0 lds\n"
"v_mfma_f32_16x16x32_bf16 a[36:39], a[224:227], v[68:71], a[36:39]\n"
"ds_read_b64_tr_b16 a[172:173], v219 offset:45080\n"
"v_cvt_pk_bf16_f32 v91, v182, v183\n"
"v_mfma_f32_16x16x32_bf16 a[36:39], a[228:231], v[76:79], a[36:39]\n"
"s_waitcnt lgkmcnt(11)\n"
"ds_read_b64_tr_b16 a[174:175], v219 offset:46104\n"
"v_fma_mix_f32 v184, v92, v160, v168 op_sel:[0,0,0] op_sel_hi:[1,0,0]\n"
"v_mfma_f32_16x16x32_bf16 a[40:43], a[232:235], v[64:67], a[40:43]\n"
"ds_read_b64_tr_b16 a[176:177], v219 offset:41232\n"
"v_fma_mix_f32 v185, v92, v161, v169 op_sel:[1,0,0] op_sel_hi:[1,0,0]\n"
"v_mfma_f32_16x16x32_bf16 a[40:43], a[236:239], v[72:75], a[40:43]\n"
"ds_read_b64_tr_b16 a[178:179], v219 offset:42256\n"
"v_fma_mix_f32 v186, v93, v162, v170 op_sel:[0,0,0] op_sel_hi:[1,0,0]\n"
"v_mfma_f32_16x16x32_bf16 a[44:47], a[232:235], v[68:71], a[44:47]\n"
"s_add_u32 m0, s51, 74528\n"
"ds_read_b64_tr_b16 a[180:181], v219 offset:45328\n"
"v_fma_mix_f32 v187, v93, v163, v171 op_sel:[1,0,0] op_sel_hi:[1,0,0]\n"
"v_mfma_f32_16x16x32_bf16 a[44:47], a[236:239], v[76:79], a[44:47]\n"
"ds_read_b64_tr_b16 a[182:183], v219 offset:46352\n"
"buffer_load_dwordx4 v217, s[24:27], s50 offen offset:256 lds\n"
"v_mfma_f32_16x16x32_bf16 a[48:51], a[240:243], v[64:67], a[48:51]\n"
"s_add_u32 s50, s50, 32768\n"
"ds_read_b64_tr_b16 a[184:185], v219 offset:41240\n"
"v_fma_mix_f32 v188, v94, v164, v172 op_sel:[0,0,0] op_sel_hi:[1,0,0]\n"
"v_mfma_f32_16x16x32_bf16 a[48:51], a[244:247], v[72:75], a[48:51]\n"
"ds_read_b64_tr_b16 a[186:187], v219 offset:42264\n"
"v_fma_mix_f32 v189, v94, v165, v173 op_sel:[1,0,0] op_sel_hi:[1,0,0]\n"
"v_mfma_f32_16x16x32_bf16 a[52:55], a[240:243], v[68:71], a[52:55]\n"
"ds_read_b64_tr_b16 a[188:189], v219 offset:45336\n"
"v_fma_mix_f32 v190, v95, v166, v174 op_sel:[0,0,0] op_sel_hi:[1,0,0]\n"
"v_mfma_f32_16x16x32_bf16 a[52:55], a[244:247], v[76:79], a[52:55]\n"
"s_waitcnt lgkmcnt(11)\n"
"ds_read_b64_tr_b16 a[190:191], v219 offset:46360\n"
"v_fma_mix_f32 v191, v95, v167, v175 op_sel:[1,0,0] op_sel_hi:[1,0,0]\n"
"v_mfma_f32_16x16x32_bf16 a[56:59], a[248:251], v[64:67], a[56:59]\n"
"s_add_u32 m0, s51, 81960\n"
"ds_read_b64_tr_b16 a[224:225], v219 offset:41488\n"
"ds_read_b128 v[160:163], v224 offset:3328\n"
"v_mfma_f32_16x16x32_bf16 a[56:59], a[252:255], v[72:75], a[56:59]\n"
"ds_read_b64_tr_b16 a[226:227], v219 offset:42512\n"
"buffer_load_dwordx4 v217, s[24:27], s50 offen offset:0 lds\n"
"v_mfma_f32_16x16x32_bf16 a[60:63], a[248:251], v[68:71], a[60:63]\n"
"ds_read_b64_tr_b16 a[228:229], v219 offset:45584\n"
"ds_read_b128 v[164:167], v224 offset:3392\n"
"v_mfma_f32_16x16x32_bf16 a[60:63], a[252:255], v[76:79], a[60:63]\n"
"ds_read_b64_tr_b16 a[230:231], v219 offset:46608\n"
"ds_read_b128 v[168:171], v224 offset:3456\n"
"v_mfma_f32_16x16x32_bf16 a[64:67], a[160:163], v[64:67], a[64:67]\n"
"ds_read_b64_tr_b16 a[232:233], v219 offset:41496\n"
"ds_read_b128 v[172:175], v224 offset:3520\n"
"v_mfma_f32_16x16x32_bf16 a[64:67], a[164:167], v[72:75], a[64:67]\n"
"ds_read_b64_tr_b16 a[234:235], v219 offset:42520\n"
"v_cvt_pk_bf16_f32 v92, v184, v185\n"
"v_mfma_f32_16x16x32_bf16 a[68:71], a[160:163], v[68:71], a[68:71]\n"
"s_add_u32 m0, s51, 89896\n"
"ds_read_b64_tr_b16 a[236:237], v219 offset:45592\n"
"v_cvt_pk_bf16_f32 v93, v186, v187\n"
"v_mfma_f32_16x16x32_bf16 a[68:71], a[164:167], v[76:79], a[68:71]\n"
"s_waitcnt lgkmcnt(14)\n"
"ds_read_b64_tr_b16 a[238:239], v219 offset:46616\n"
"buffer_load_dwordx4 v217, s[24:27], s50 offen offset:256 lds\n"
"v_mfma_f32_16x16x32_bf16 a[72:75], a[168:171], v[64:67], a[72:75]\n"
"s_add_u32 s50, s50, 32768\n"
"ds_read_b64_tr_b16 a[240:241], v219 offset:41744\n"
"v_cvt_pk_bf16_f32 v94, v188, v189\n"
"v_mfma_f32_16x16x32_bf16 a[72:75], a[172:175], v[72:75], a[72:75]\n"
"ds_read_b64_tr_b16 a[242:243], v219 offset:42768\n"
"v_cvt_pk_bf16_f32 v95, v190, v191\n"
"v_mfma_f32_16x16x32_bf16 a[76:79], a[168:171], v[68:71], a[76:79]\n"
"ds_read_b64_tr_b16 a[244:245], v219 offset:45840\n"
"v_fma_mix_f32 v176, v96, v144, v152 op_sel:[0,0,0] op_sel_hi:[1,0,0]\n"
"v_mfma_f32_16x16x32_bf16 a[76:79], a[172:175], v[76:79], a[76:79]\n"
"ds_read_b64_tr_b16 a[246:247], v219 offset:46864\n"
"v_fma_mix_f32 v177, v96, v145, v153 op_sel:[1,0,0] op_sel_hi:[1,0,0]\n"
"v_mfma_f32_16x16x32_bf16 a[80:83], a[176:179], v[64:67], a[80:83]\n"
"s_add_u32 m0, s51, 82984\n"
"ds_read_b64_tr_b16 a[248:249], v219 offset:41752\n"
"v_fma_mix_f32 v178, v97, v146, v154 op_sel:[0,0,0] op_sel_hi:[1,0,0]\n"
"v_mfma_f32_16x16x32_bf16 a[80:83], a[180:183], v[72:75], a[80:83]\n"
"ds_read_b64_tr_b16 a[250:251], v219 offset:42776\n"
"buffer_load_dwordx4 v217, s[24:27], s50 offen offset:0 lds\n"
"v_mfma_f32_16x16x32_bf16 a[84:87], a[176:179], v[68:71], a[84:87]\n"
"ds_read_b64_tr_b16 a[252:253], v219 offset:45848\n"
"v_fma_mix_f32 v179, v97, v147, v155 op_sel:[1,0,0] op_sel_hi:[1,0,0]\n"
"v_mfma_f32_16x16x32_bf16 a[84:87], a[180:183], v[76:79], a[84:87]\n"
"s_waitcnt lgkmcnt(12)\n"
"ds_read_b64_tr_b16 a[254:255], v219 offset:46872\n"
"v_fma_mix_f32 v180, v98, v148, v156 op_sel:[0,0,0] op_sel_hi:[1,0,0]\n"
"v_mfma_f32_16x16x32_bf16 a[88:91], a[184:187], v[64:67], a[88:91]\n"
"ds_read_b64_tr_b16 a[160:161], v218 offset:0\n"
"v_fma_mix_f32 v181, v98, v149, v157 op_sel:[1,0,0] op_sel_hi:[1,0,0]\n"
"v_mfma_f32_16x16x32_bf16 a[88:91], a[188:191], v[72:75], a[88:91]\n"
"ds_read_b64_tr_b16 a[162:163], v218 offset:1024\n"
"v_fma_mix_f32 v182, v99, v150, v158 op_sel:[0,0,0] op_sel_hi:[1,0,0]\n"
"v_mfma_f32_16x16x32_bf16 a[92:95], a[184:187], v[68:71], a[92:95]\n"
"s_add_u32 m0, s51, 90920\n"
"ds_read_b64_tr_b16 a[164:165], v218 offset:4096\n"
"v_fma_mix_f32 v183, v99, v151, v159 op_sel:[1,0,0] op_sel_hi:[1,0,0]\n"
"v_mfma_f32_16x16x32_bf16 a[92:95], a[188:191], v[76:79], a[92:95]\n"
"buffer_load_dwordx4 v217, s[24:27], s50 offen offset:256 lds\n"
"ds_read_b64_tr_b16 a[166:167], v218 offset:5120\n"
"v_mfma_f32_16x16x32_bf16 a[96:99], a[224:227], v[64:67], a[96:99]\n"
"s_add_u32 s50, s50, 425984\n"
"ds_read_b64_tr_b16 a[168:169], v218 offset:8\n"
"v_cvt_pk_bf16_f32 v96, v176, v177\n"
"v_mfma_f32_16x16x32_bf16 a[96:99], a[228:231], v[72:75], a[96:99]\n"
"ds_read_b64_tr_b16 a[170:171], v218 offset:1032\n"
"v_cvt_pk_bf16_f32 v97, v178, v179\n"
"v_mfma_f32_16x16x32_bf16 a[100:103], a[224:227], v[68:71], a[100:103]\n"
"ds_read_b64_tr_b16 a[172:173], v218 offset:4104\n"
"v_cvt_pk_bf16_f32 v98, v180, v181\n"
"v_mfma_f32_16x16x32_bf16 a[100:103], a[228:231], v[76:79], a[100:103]\n"
"s_waitcnt lgkmcnt(11)\n"
"ds_read_b64_tr_b16 a[174:175], v218 offset:5128\n"
"v_cvt_pk_bf16_f32 v99, v182, v183\n"
"v_mfma_f32_16x16x32_bf16 a[104:107], a[232:235], v[64:67], a[104:107]\n"
"ds_read_b64_tr_b16 a[176:177], v218 offset:256\n"
"v_fma_mix_f32 v184, v100, v144, v152 op_sel:[0,0,0] op_sel_hi:[1,0,0]\n"
"v_mfma_f32_16x16x32_bf16 a[104:107], a[236:239], v[72:75], a[104:107]\n"
"ds_read_b64_tr_b16 a[178:179], v218 offset:1280\n"
"v_fma_mix_f32 v185, v100, v145, v153 op_sel:[1,0,0] op_sel_hi:[1,0,0]\n"
"v_mfma_f32_16x16x32_bf16 a[108:111], a[232:235], v[68:71], a[108:111]\n"
"ds_read_b64_tr_b16 a[180:181], v218 offset:4352\n"
"v_fma_mix_f32 v186, v101, v146, v154 op_sel:[0,0,0] op_sel_hi:[1,0,0]\n"
"v_mfma_f32_16x16x32_bf16 a[108:111], a[236:239], v[76:79], a[108:111]\n"
"ds_read_b64_tr_b16 a[182:183], v218 offset:5376\n"
"v_fma_mix_f32 v187, v101, v147, v155 op_sel:[1,0,0] op_sel_hi:[1,0,0]\n"
"v_mfma_f32_16x16x32_bf16 a[112:115], a[240:243], v[64:67], a[112:115]\n"
"ds_read_b64_tr_b16 a[184:185], v218 offset:264\n"
"v_fma_mix_f32 v188, v102, v148, v156 op_sel:[0,0,0] op_sel_hi:[1,0,0]\n"
"v_mfma_f32_16x16x32_bf16 a[112:115], a[244:247], v[72:75], a[112:115]\n"
"ds_read_b64_tr_b16 a[186:187], v218 offset:1288\n"
"v_fma_mix_f32 v189, v102, v149, v157 op_sel:[1,0,0] op_sel_hi:[1,0,0]\n"
"v_mfma_f32_16x16x32_bf16 a[116:119], a[240:243], v[68:71], a[116:119]\n"
"ds_read_b64_tr_b16 a[188:189], v218 offset:4360\n"
"v_fma_mix_f32 v190, v103, v150, v158 op_sel:[0,0,0] op_sel_hi:[1,0,0]\n"
"v_mfma_f32_16x16x32_bf16 a[116:119], a[244:247], v[76:79], a[116:119]\n"
"s_waitcnt lgkmcnt(11)\n"
"ds_read_b64_tr_b16 a[190:191], v218 offset:5384\n"
"v_fma_mix_f32 v191, v103, v151, v159 op_sel:[1,0,0] op_sel_hi:[1,0,0]\n"
"v_mfma_f32_16x16x32_bf16 a[120:123], a[248:251], v[64:67], a[120:123]\n"
"ds_read_b64_tr_b16 a[224:225], v218 offset:512\n"
"ds_read_b128 v[144:147], v224 offset:3584\n"
"v_mfma_f32_16x16x32_bf16 a[120:123], a[252:255], v[72:75], a[120:123]\n"
"ds_read_b64_tr_b16 a[226:227], v218 offset:1536\n"
"ds_read_b128 v[148:151], v224 offset:3648\n"
"v_mfma_f32_16x16x32_bf16 a[124:127], a[248:251], v[68:71], a[124:127]\n"
"ds_read_b64_tr_b16 a[228:229], v218 offset:4608\n"
"ds_read_b128 v[152:155], v224 offset:3712\n"
"v_mfma_f32_16x16x32_bf16 a[124:127], a[252:255], v[76:79], a[124:127]\n"
"ds_read_b64_tr_b16 a[230:231], v218 offset:5632\n"
"ds_read_b128 v[156:159], v224 offset:3776\n"
"v_mfma_f32_16x16x32_bf16 a[0:3], a[160:163], v[80:83], a[0:3]\n"
"ds_read_b64_tr_b16 a[232:233], v218 offset:520\n"
"v_cvt_pk_bf16_f32 v100, v184, v185\n"
"v_mfma_f32_16x16x32_bf16 a[0:3], a[164:167], v[88:91], a[0:3]\n"
"ds_read_b64_tr_b16 a[234:235], v218 offset:1544\n"
"v_cvt_pk_bf16_f32 v101, v186, v187\n"
"v_mfma_f32_16x16x32_bf16 a[4:7], a[160:163], v[84:87], a[4:7]\n"
"s_waitcnt vmcnt(8)\n"
"s_barrier\n"
"ds_read_b64_tr_b16 a[236:237], v218 offset:4616\n"
"v_cvt_pk_bf16_f32 v102, v188, v189\n"
"v_mfma_f32_16x16x32_bf16 a[4:7], a[164:167], v[92:95], a[4:7]\n"
"s_waitcnt lgkmcnt(14)\n"
"ds_read_b64_tr_b16 a[238:239], v218 offset:5640\n"
"v_cvt_pk_bf16_f32 v103, v190, v191\n"
"v_mfma_f32_16x16x32_bf16 a[8:11], a[168:171], v[80:83], a[8:11]\n"
"s_add_u32 m0, s51, 98352\n"
"ds_read_b64_tr_b16 a[240:241], v218 offset:768\n"
"v_fma_mix_f32 v176, v104, v160, v168 op_sel:[0,0,0] op_sel_hi:[1,0,0]\n"
"v_mfma_f32_16x16x32_bf16 a[8:11], a[172:175], v[88:91], a[8:11]\n"
"ds_read_b64_tr_b16 a[242:243], v218 offset:1792\n"
"buffer_load_dwordx4 v217, s[24:27], s50 offen offset:0 lds\n"
"v_mfma_f32_16x16x32_bf16 a[12:15], a[168:171], v[84:87], a[12:15]\n"
"ds_read_b64_tr_b16 a[244:245], v218 offset:4864\n"
"v_fma_mix_f32 v177, v104, v161, v169 op_sel:[1,0,0] op_sel_hi:[1,0,0]\n"
"v_mfma_f32_16x16x32_bf16 a[12:15], a[172:175], v[92:95], a[12:15]\n"
"ds_read_b64_tr_b16 a[246:247], v218 offset:5888\n"
"v_fma_mix_f32 v178, v105, v162, v170 op_sel:[0,0,0] op_sel_hi:[1,0,0]\n"
"v_mfma_f32_16x16x32_bf16 a[16:19], a[176:179], v[80:83], a[16:19]\n"
"ds_read_b64_tr_b16 a[248:249], v218 offset:776\n"
"v_fma_mix_f32 v179, v105, v163, v171 op_sel:[1,0,0] op_sel_hi:[1,0,0]\n"
"v_mfma_f32_16x16x32_bf16 a[16:19], a[180:183], v[88:91], a[16:19]\n"
"ds_read_b64_tr_b16 a[250:251], v218 offset:1800\n"
"v_fma_mix_f32 v180, v106, v164, v172 op_sel:[0,0,0] op_sel_hi:[1,0,0]\n"
"v_mfma_f32_16x16x32_bf16 a[20:23], a[176:179], v[84:87], a[20:23]\n"
"s_add_u32 m0, s51, 106288\n"
"ds_read_b64_tr_b16 a[252:253], v218 offset:4872\n"
"v_fma_mix_f32 v181, v106, v165, v173 op_sel:[1,0,0] op_sel_hi:[1,0,0]\n"
"v_mfma_f32_16x16x32_bf16 a[20:23], a[180:183], v[92:95], a[20:23]\n"
"s_waitcnt lgkmcnt(11)\n"
"ds_read_b64_tr_b16 a[254:255], v218 offset:5896\n"
"buffer_load_dwordx4 v217, s[24:27], s50 offen offset:256 lds\n"
"v_mfma_f32_16x16x32_bf16 a[24:27], a[184:187], v[80:83], a[24:27]\n"
"s_add_u32 s50, s50, 32768\n"
"ds_read_b64_tr_b16 a[160:161], v218 offset:8192\n"
"v_fma_mix_f32 v182, v107, v166, v174 op_sel:[0,0,0] op_sel_hi:[1,0,0]\n"
"v_mfma_f32_16x16x32_bf16 a[24:27], a[188:191], v[88:91], a[24:27]\n"
"ds_read_b64_tr_b16 a[162:163], v218 offset:9216\n"
"v_fma_mix_f32 v183, v107, v167, v175 op_sel:[1,0,0] op_sel_hi:[1,0,0]\n"
"v_mfma_f32_16x16x32_bf16 a[28:31], a[184:187], v[84:87], a[28:31]\n"
"ds_read_b64_tr_b16 a[164:165], v218 offset:12288\n"
"v_cvt_pk_bf16_f32 v104, v176, v177\n"
"v_mfma_f32_16x16x32_bf16 a[28:31], a[188:191], v[92:95], a[28:31]\n"
"ds_read_b64_tr_b16 a[166:167], v218 offset:13312\n"
"v_cvt_pk_bf16_f32 v105, v178, v179\n"
"v_mfma_f32_16x16x32_bf16 a[32:35], a[224:227], v[80:83], a[32:35]\n"
"s_add_u32 m0, s51, 99376\n"
"ds_read_b64_tr_b16 a[168:169], v218 offset:8200\n"
"v_cvt_pk_bf16_f32 v106, v180, v181\n"
"v_mfma_f32_16x16x32_bf16 a[32:35], a[228:231], v[88:91], a[32:35]\n"
"ds_read_b64_tr_b16 a[170:171], v218 offset:9224\n"
"buffer_load_dwordx4 v217, s[24:27], s50 offen offset:0 lds\n"
"v_mfma_f32_16x16x32_bf16 a[36:39], a[224:227], v[84:87], a[36:39]\n"
"ds_read_b64_tr_b16 a[172:173], v218 offset:12296\n"
"v_cvt_pk_bf16_f32 v107, v182, v183\n"
"v_mfma_f32_16x16x32_bf16 a[36:39], a[228:231], v[92:95], a[36:39]\n"
"s_waitcnt lgkmcnt(11)\n"
"ds_read_b64_tr_b16 a[174:175], v218 offset:13320\n"
"v_fma_mix_f32 v184, v108, v160, v168 op_sel:[0,0,0] op_sel_hi:[1,0,0]\n"
"v_mfma_f32_16x16x32_bf16 a[40:43], a[232:235], v[80:83], a[40:43]\n"
"ds_read_b64_tr_b16 a[176:177], v218 offset:8448\n"
"v_fma_mix_f32 v185, v108, v161, v169 op_sel:[1,0,0] op_sel_hi:[1,0,0]\n"
"v_mfma_f32_16x16x32_bf16 a[40:43], a[236:239], v[88:91], a[40:43]\n"
"ds_read_b64_tr_b16 a[178:179], v218 offset:9472\n"
"v_fma_mix_f32 v186, v109, v162, v170 op_sel:[0,0,0] op_sel_hi:[1,0,0]\n"
"v_mfma_f32_16x16x32_bf16 a[44:47], a[232:235], v[84:87], a[44:47]\n"
"s_add_u32 m0, s51, 107312\n"
"ds_read_b64_tr_b16 a[180:181], v218 offset:12544\n"
"v_fma_mix_f32 v187, v109, v163, v171 op_sel:[1,0,0] op_sel_hi:[1,0,0]\n"
"v_mfma_f32_16x16x32_bf16 a[44:47], a[236:239], v[92:95], a[44:47]\n"
"ds_read_b64_tr_b16 a[182:183], v218 offset:13568\n"
"buffer_load_dwordx4 v217, s[24:27], s50 offen offset:256 lds\n"
"v_mfma_f32_16x16x32_bf16 a[48:51], a[240:243], v[80:83], a[48:51]\n"
"s_add_u32 s50, s50, 32768\n"
"ds_read_b64_tr_b16 a[184:185], v218 offset:8456\n"
"v_fma_mix_f32 v188, v110, v164, v172 op_sel:[0,0,0] op_sel_hi:[1,0,0]\n"
"v_mfma_f32_16x16x32_bf16 a[48:51], a[244:247], v[88:91], a[48:51]\n"
"ds_read_b64_tr_b16 a[186:187], v218 offset:9480\n"
"v_fma_mix_f32 v189, v110, v165, v173 op_sel:[1,0,0] op_sel_hi:[1,0,0]\n"
"v_mfma_f32_16x16x32_bf16 a[52:55], a[240:243], v[84:87], a[52:55]\n"
"ds_read_b64_tr_b16 a[188:189], v218 offset:12552\n"
"v_fma_mix_f32 v190, v111, v166, v174 op_sel:[0,0,0] op_sel_hi:[1,0,0]\n"
"v_mfma_f32_16x16x32_bf16 a[52:55], a[244:247], v[92:95], a[52:55]\n"
"s_waitcnt lgkmcnt(11)\n"
"ds_read_b64_tr_b16 a[190:191], v218 offset:13576\n"
"v_fma_mix_f32 v191, v111, v167, v175 op_sel:[1,0,0] op_sel_hi:[1,0,0]\n"
"v_mfma_f32_16x16x32_bf16 a[56:59], a[248:251], v[80:83], a[56:59]\n"
"s_add_u32 m0, s51, 114744\n"
"ds_read_b64_tr_b16 a[224:225], v218 offset:8704\n"
"ds_read_b128 v[160:163], v224 offset:3840\n"
"v_mfma_f32_16x16x32_bf16 a[56:59], a[252:255], v[88:91], a[56:59]\n"
"ds_read_b64_tr_b16 a[226:227], v218 offset:9728\n"
"buffer_load_dwordx4 v217, s[24:27], s50 offen offset:0 lds\n"
"v_mfma_f32_16x16x32_bf16 a[60:63], a[248:251], v[84:87], a[60:63]\n"
"ds_read_b64_tr_b16 a[228:229], v218 offset:12800\n"
"ds_read_b128 v[164:167], v224 offset:3904\n"
"v_mfma_f32_16x16x32_bf16 a[60:63], a[252:255], v[92:95], a[60:63]\n"
"ds_read_b64_tr_b16 a[230:231], v218 offset:13824\n"
"ds_read_b128 v[168:171], v224 offset:3968\n"
"v_mfma_f32_16x16x32_bf16 a[64:67], a[160:163], v[80:83], a[64:67]\n"
"ds_read_b64_tr_b16 a[232:233], v218 offset:8712\n"
"ds_read_b128 v[172:175], v224 offset:4032\n"
"v_mfma_f32_16x16x32_bf16 a[64:67], a[164:167], v[88:91], a[64:67]\n"
"ds_read_b64_tr_b16 a[234:235], v218 offset:9736\n"
"v_cvt_pk_bf16_f32 v108, v184, v185\n"
"v_mfma_f32_16x16x32_bf16 a[68:71], a[160:163], v[84:87], a[68:71]\n"
"s_add_u32 m0, s51, 122680\n"
"ds_read_b64_tr_b16 a[236:237], v218 offset:12808\n"
"v_cvt_pk_bf16_f32 v109, v186, v187\n"
"v_mfma_f32_16x16x32_bf16 a[68:71], a[164:167], v[92:95], a[68:71]\n"
"s_waitcnt lgkmcnt(14)\n"
"ds_read_b64_tr_b16 a[238:239], v218 offset:13832\n"
"buffer_load_dwordx4 v217, s[24:27], s50 offen offset:256 lds\n"
"v_mfma_f32_16x16x32_bf16 a[72:75], a[168:171], v[80:83], a[72:75]\n"
"s_add_u32 s50, s50, 32768\n"
"ds_read_b64_tr_b16 a[240:241], v218 offset:8960\n"
"v_cvt_pk_bf16_f32 v110, v188, v189\n"
"v_mfma_f32_16x16x32_bf16 a[72:75], a[172:175], v[88:91], a[72:75]\n"
"ds_read_b64_tr_b16 a[242:243], v218 offset:9984\n"
"v_cvt_pk_bf16_f32 v111, v190, v191\n"
"v_mfma_f32_16x16x32_bf16 a[76:79], a[168:171], v[84:87], a[76:79]\n"
"ds_read_b64_tr_b16 a[244:245], v218 offset:13056\n"
"v_fma_mix_f32 v176, v112, v144, v152 op_sel:[0,0,0] op_sel_hi:[1,0,0]\n"
"v_mfma_f32_16x16x32_bf16 a[76:79], a[172:175], v[92:95], a[76:79]\n"
"ds_read_b64_tr_b16 a[246:247], v218 offset:14080\n"
"v_fma_mix_f32 v177, v112, v145, v153 op_sel:[1,0,0] op_sel_hi:[1,0,0]\n"
"v_mfma_f32_16x16x32_bf16 a[80:83], a[176:179], v[80:83], a[80:83]\n"
"s_add_u32 m0, s51, 115768\n"
"ds_read_b64_tr_b16 a[248:249], v218 offset:8968\n"
"v_fma_mix_f32 v178, v113, v146, v154 op_sel:[0,0,0] op_sel_hi:[1,0,0]\n"
"v_mfma_f32_16x16x32_bf16 a[80:83], a[180:183], v[88:91], a[80:83]\n"
"ds_read_b64_tr_b16 a[250:251], v218 offset:9992\n"
"buffer_load_dwordx4 v217, s[24:27], s50 offen offset:0 lds\n"
"v_mfma_f32_16x16x32_bf16 a[84:87], a[176:179], v[84:87], a[84:87]\n"
"ds_read_b64_tr_b16 a[252:253], v218 offset:13064\n"
"v_fma_mix_f32 v179, v113, v147, v155 op_sel:[1,0,0] op_sel_hi:[1,0,0]\n"
"v_mfma_f32_16x16x32_bf16 a[84:87], a[180:183], v[92:95], a[84:87]\n"
"s_waitcnt lgkmcnt(12)\n"
"ds_read_b64_tr_b16 a[254:255], v218 offset:14088\n"
"v_fma_mix_f32 v180, v114, v148, v156 op_sel:[0,0,0] op_sel_hi:[1,0,0]\n"
"v_mfma_f32_16x16x32_bf16 a[88:91], a[184:187], v[80:83], a[88:91]\n"
"ds_read_b64_tr_b16 a[160:161], v218 offset:32784\n"
"v_fma_mix_f32 v181, v114, v149, v157 op_sel:[1,0,0] op_sel_hi:[1,0,0]\n"
"v_mfma_f32_16x16x32_bf16 a[88:91], a[188:191], v[88:91], a[88:91]\n"
"ds_read_b64_tr_b16 a[162:163], v218 offset:33808\n"
"v_fma_mix_f32 v182, v115, v150, v158 op_sel:[0,0,0] op_sel_hi:[1,0,0]\n"
"v_mfma_f32_16x16x32_bf16 a[92:95], a[184:187], v[84:87], a[92:95]\n"
"s_add_u32 m0, s51, 123704\n"
"ds_read_b64_tr_b16 a[164:165], v218 offset:36880\n"
"v_fma_mix_f32 v183, v115, v151, v159 op_sel:[1,0,0] op_sel_hi:[1,0,0]\n"
"v_mfma_f32_16x16x32_bf16 a[92:95], a[188:191], v[92:95], a[92:95]\n"
"buffer_load_dwordx4 v217, s[24:27], s50 offen offset:256 lds\n"
"ds_read_b64_tr_b16 a[166:167], v218 offset:37904\n"
"v_mfma_f32_16x16x32_bf16 a[96:99], a[224:227], v[80:83], a[96:99]\n"
"s_add_u32 s50, s50, 425984\n"
"ds_read_b64_tr_b16 a[168:169], v218 offset:32792\n"
"v_cvt_pk_bf16_f32 v112, v176, v177\n"
"v_mfma_f32_16x16x32_bf16 a[96:99], a[228:231], v[88:91], a[96:99]\n"
"ds_read_b64_tr_b16 a[170:171], v218 offset:33816\n"
"v_cvt_pk_bf16_f32 v113, v178, v179\n"
"v_mfma_f32_16x16x32_bf16 a[100:103], a[224:227], v[84:87], a[100:103]\n"
"ds_read_b64_tr_b16 a[172:173], v218 offset:36888\n"
"v_cvt_pk_bf16_f32 v114, v180, v181\n"
"v_mfma_f32_16x16x32_bf16 a[100:103], a[228:231], v[92:95], a[100:103]\n"
"s_waitcnt lgkmcnt(11)\n"
"ds_read_b64_tr_b16 a[174:175], v218 offset:37912\n"
"v_cvt_pk_bf16_f32 v115, v182, v183\n"
"v_mfma_f32_16x16x32_bf16 a[104:107], a[232:235], v[80:83], a[104:107]\n"
"ds_read_b64_tr_b16 a[176:177], v218 offset:33040\n"
"v_fma_mix_f32 v184, v116, v144, v152 op_sel:[0,0,0] op_sel_hi:[1,0,0]\n"
"v_mfma_f32_16x16x32_bf16 a[104:107], a[236:239], v[88:91], a[104:107]\n"
"ds_read_b64_tr_b16 a[178:179], v218 offset:34064\n"
"v_fma_mix_f32 v185, v116, v145, v153 op_sel:[1,0,0] op_sel_hi:[1,0,0]\n"
"v_mfma_f32_16x16x32_bf16 a[108:111], a[232:235], v[84:87], a[108:111]\n"
"ds_read_b64_tr_b16 a[180:181], v218 offset:37136\n"
"v_fma_mix_f32 v186, v117, v146, v154 op_sel:[0,0,0] op_sel_hi:[1,0,0]\n"
"v_mfma_f32_16x16x32_bf16 a[108:111], a[236:239], v[92:95], a[108:111]\n"
"ds_read_b64_tr_b16 a[182:183], v218 offset:38160\n"
"v_fma_mix_f32 v187, v117, v147, v155 op_sel:[1,0,0] op_sel_hi:[1,0,0]\n"
"v_mfma_f32_16x16x32_bf16 a[112:115], a[240:243], v[80:83], a[112:115]\n"
"ds_read_b64_tr_b16 a[184:185], v218 offset:33048\n"
"v_fma_mix_f32 v188, v118, v148, v156 op_sel:[0,0,0] op_sel_hi:[1,0,0]\n"
"v_mfma_f32_16x16x32_bf16 a[112:115], a[244:247], v[88:91], a[112:115]\n"
"ds_read_b64_tr_b16 a[186:187], v218 offset:34072\n"
"v_fma_mix_f32 v189, v118, v149, v157 op_sel:[1,0,0] op_sel_hi:[1,0,0]\n"
"v_mfma_f32_16x16x32_bf16 a[116:119], a[240:243], v[84:87], a[116:119]\n"
"ds_read_b64_tr_b16 a[188:189], v218 offset:37144\n"
"v_fma_mix_f32 v190, v119, v150, v158 op_sel:[0,0,0] op_sel_hi:[1,0,0]\n"
"v_mfma_f32_16x16x32_bf16 a[116:119], a[244:247], v[92:95], a[116:119]\n"
"s_waitcnt lgkmcnt(11)\n"
"ds_read_b64_tr_b16 a[190:191], v218 offset:38168\n"
"v_fma_mix_f32 v191, v119, v151, v159 op_sel:[1,0,0] op_sel_hi:[1,0,0]\n"
"v_mfma_f32_16x16x32_bf16 a[120:123], a[248:251], v[80:83], a[120:123]\n"
"ds_read_b64_tr_b16 a[224:225], v218 offset:33296\n"
"ds_read_b128 v[144:147], v224 offset:4096\n"
"v_mfma_f32_16x16x32_bf16 a[120:123], a[252:255], v[88:91], a[120:123]\n"
"ds_read_b64_tr_b16 a[226:227], v218 offset:34320\n"
"ds_read_b128 v[148:151], v224 offset:4160\n"
"v_mfma_f32_16x16x32_bf16 a[124:127], a[248:251], v[84:87], a[124:127]\n"
"ds_read_b64_tr_b16 a[228:229], v218 offset:37392\n"
"ds_read_b128 v[152:155], v224 offset:4224\n"
"v_mfma_f32_16x16x32_bf16 a[124:127], a[252:255], v[92:95], a[124:127]\n"
"ds_read_b64_tr_b16 a[230:231], v218 offset:38416\n"
"ds_read_b128 v[156:159], v224 offset:4288\n"
"v_mfma_f32_16x16x32_bf16 a[0:3], a[160:163], v[96:99], a[0:3]\n"
"ds_read_b64_tr_b16 a[232:233], v218 offset:33304\n"
"v_cvt_pk_bf16_f32 v116, v184, v185\n"
"v_mfma_f32_16x16x32_bf16 a[0:3], a[164:167], v[104:107], a[0:3]\n"
"ds_read_b64_tr_b16 a[234:235], v218 offset:34328\n"
"v_cvt_pk_bf16_f32 v117, v186, v187\n"
"v_mfma_f32_16x16x32_bf16 a[4:7], a[160:163], v[100:103], a[4:7]\n"
"s_waitcnt vmcnt(8)\n"
"s_barrier\n"
"ds_read_b64_tr_b16 a[236:237], v218 offset:37400\n"
"v_cvt_pk_bf16_f32 v118, v188, v189\n"
"v_mfma_f32_16x16x32_bf16 a[4:7], a[164:167], v[108:111], a[4:7]\n"
"s_waitcnt lgkmcnt(14)\n"
"ds_read_b64_tr_b16 a[238:239], v218 offset:38424\n"
"v_cvt_pk_bf16_f32 v119, v190, v191\n"
"v_mfma_f32_16x16x32_bf16 a[8:11], a[168:171], v[96:99], a[8:11]\n"
"s_mov_b32 m0, s18\n"
"ds_read_b64_tr_b16 a[240:241], v218 offset:33552\n"
"v_fma_mix_f32 v176, v120, v160, v168 op_sel:[0,0,0] op_sel_hi:[1,0,0]\n"
"v_mfma_f32_16x16x32_bf16 a[8:11], a[172:175], v[104:107], a[8:11]\n"
"ds_read_b64_tr_b16 a[242:243], v218 offset:34576\n"
"buffer_load_dwordx4 v246, s[8:11], s16 offen offset:0 lds\n"
"v_mfma_f32_16x16x32_bf16 a[12:15], a[168:171], v[100:103], a[12:15]\n"
"ds_read_b64_tr_b16 a[244:245], v218 offset:37648\n"
"v_fma_mix_f32 v177, v120, v161, v169 op_sel:[1,0,0] op_sel_hi:[1,0,0]\n"
"v_mfma_f32_16x16x32_bf16 a[12:15], a[172:175], v[108:111], a[12:15]\n"
"ds_read_b64_tr_b16 a[246:247], v218 offset:38672\n"
"v_fma_mix_f32 v178, v121, v162, v170 op_sel:[0,0,0] op_sel_hi:[1,0,0]\n"
"v_mfma_f32_16x16x32_bf16 a[16:19], a[176:179], v[96:99], a[16:19]\n"
"ds_read_b64_tr_b16 a[248:249], v218 offset:33560\n"
"v_fma_mix_f32 v179, v121, v163, v171 op_sel:[1,0,0] op_sel_hi:[1,0,0]\n"
"v_mfma_f32_16x16x32_bf16 a[16:19], a[180:183], v[104:107], a[16:19]\n"
"ds_read_b64_tr_b16 a[250:251], v218 offset:34584\n"
"v_fma_mix_f32 v180, v122, v164, v172 op_sel:[0,0,0] op_sel_hi:[1,0,0]\n"
"v_mfma_f32_16x16x32_bf16 a[20:23], a[176:179], v[100:103], a[20:23]\n"
"ds_read_b64_tr_b16 a[252:253], v218 offset:37656\n"
"v_fma_mix_f32 v181, v122, v165, v173 op_sel:[1,0,0] op_sel_hi:[1,0,0]\n"
"v_mfma_f32_16x16x32_bf16 a[20:23], a[180:183], v[108:111], a[20:23]\n"
"s_waitcnt lgkmcnt(11)\n"
"ds_read_b64_tr_b16 a[254:255], v218 offset:38680\n"
"buffer_load_dwordx4 v246, s[8:11], s16 offen offset:1024 lds\n"
"v_mfma_f32_16x16x32_bf16 a[24:27], a[184:187], v[96:99], a[24:27]\n"
"ds_read_b64_tr_b16 a[160:161], v218 offset:40976\n"
"v_fma_mix_f32 v182, v123, v166, v174 op_sel:[0,0,0] op_sel_hi:[1,0,0]\n"
"v_mfma_f32_16x16x32_bf16 a[24:27], a[188:191], v[104:107], a[24:27]\n"
"ds_read_b64_tr_b16 a[162:163], v218 offset:42000\n"
"v_fma_mix_f32 v183, v123, v167, v175 op_sel:[1,0,0] op_sel_hi:[1,0,0]\n"
"v_mfma_f32_16x16x32_bf16 a[28:31], a[184:187], v[100:103], a[28:31]\n"
"ds_read_b64_tr_b16 a[164:165], v218 offset:45072\n"
"v_cvt_pk_bf16_f32 v120, v176, v177\n"
"v_mfma_f32_16x16x32_bf16 a[28:31], a[188:191], v[108:111], a[28:31]\n"
"ds_read_b64_tr_b16 a[166:167], v218 offset:46096\n"
"v_cvt_pk_bf16_f32 v121, v178, v179\n"
"v_mfma_f32_16x16x32_bf16 a[32:35], a[224:227], v[96:99], a[32:35]\n"
"ds_read_b64_tr_b16 a[168:169], v218 offset:40984\n"
"v_cvt_pk_bf16_f32 v122, v180, v181\n"
"v_mfma_f32_16x16x32_bf16 a[32:35], a[228:231], v[104:107], a[32:35]\n"
"ds_read_b64_tr_b16 a[170:171], v218 offset:42008\n"
"buffer_load_dwordx4 v247, s[8:11], s16 offen offset:2048 lds\n"
"v_mfma_f32_16x16x32_bf16 a[36:39], a[224:227], v[100:103], a[36:39]\n"
"ds_read_b64_tr_b16 a[172:173], v218 offset:45080\n"
"v_cvt_pk_bf16_f32 v123, v182, v183\n"
"v_mfma_f32_16x16x32_bf16 a[36:39], a[228:231], v[108:111], a[36:39]\n"
"s_waitcnt lgkmcnt(11)\n"
"ds_read_b64_tr_b16 a[174:175], v218 offset:46104\n"
"v_fma_mix_f32 v184, v124, v160, v168 op_sel:[0,0,0] op_sel_hi:[1,0,0]\n"
"v_mfma_f32_16x16x32_bf16 a[40:43], a[232:235], v[96:99], a[40:43]\n"
"ds_read_b64_tr_b16 a[176:177], v218 offset:41232\n"
"v_fma_mix_f32 v185, v124, v161, v169 op_sel:[1,0,0] op_sel_hi:[1,0,0]\n"
"v_mfma_f32_16x16x32_bf16 a[40:43], a[236:239], v[104:107], a[40:43]\n"
"ds_read_b64_tr_b16 a[178:179], v218 offset:42256\n"
"v_fma_mix_f32 v186, v125, v162, v170 op_sel:[0,0,0] op_sel_hi:[1,0,0]\n"
"v_mfma_f32_16x16x32_bf16 a[44:47], a[232:235], v[100:103], a[44:47]\n"
"ds_read_b64_tr_b16 a[180:181], v218 offset:45328\n"
"v_fma_mix_f32 v187, v125, v163, v171 op_sel:[1,0,0] op_sel_hi:[1,0,0]\n"
"v_mfma_f32_16x16x32_bf16 a[44:47], a[236:239], v[108:111], a[44:47]\n"
"ds_read_b64_tr_b16 a[182:183], v218 offset:46352\n"
"buffer_load_dwordx4 v247, s[8:11], s16 offen offset:3072 lds\n"
"v_mfma_f32_16x16x32_bf16 a[48:51], a[240:243], v[96:99], a[48:51]\n"
"ds_read_b64_tr_b16 a[184:185], v218 offset:41240\n"
"v_fma_mix_f32 v188, v126, v164, v172 op_sel:[0,0,0] op_sel_hi:[1,0,0]\n"
"v_mfma_f32_16x16x32_bf16 a[48:51], a[244:247], v[104:107], a[48:51]\n"
"ds_read_b64_tr_b16 a[186:187], v218 offset:42264\n"
"v_fma_mix_f32 v189, v126, v165, v173 op_sel:[1,0,0] op_sel_hi:[1,0,0]\n"
"v_mfma_f32_16x16x32_bf16 a[52:55], a[240:243], v[100:103], a[52:55]\n"
"ds_read_b64_tr_b16 a[188:189], v218 offset:45336\n"
"v_fma_mix_f32 v190, v127, v166, v174 op_sel:[0,0,0] op_sel_hi:[1,0,0]\n"
"v_mfma_f32_16x16x32_bf16 a[52:55], a[244:247], v[108:111], a[52:55]\n"
"s_waitcnt lgkmcnt(11)\n"
"ds_read_b64_tr_b16 a[190:191], v218 offset:46360\n"
"v_fma_mix_f32 v191, v127, v167, v175 op_sel:[1,0,0] op_sel_hi:[1,0,0]\n"
"v_mfma_f32_16x16x32_bf16 a[56:59], a[248:251], v[96:99], a[56:59]\n"
"s_add_u32 m0, s18, 16384\n"
"ds_read_b64_tr_b16 a[224:225], v218 offset:41488\n"
"ds_read_b128 v[160:163], v224 offset:4352\n"
"v_mfma_f32_16x16x32_bf16 a[56:59], a[252:255], v[104:107], a[56:59]\n"
"ds_read_b64_tr_b16 a[226:227], v218 offset:42512\n"
"buffer_load_dwordx4 v246, s[12:15], s16 offen offset:0 lds\n"
"v_mfma_f32_16x16x32_bf16 a[60:63], a[248:251], v[100:103], a[60:63]\n"
"ds_read_b64_tr_b16 a[228:229], v218 offset:45584\n"
"ds_read_b128 v[164:167], v224 offset:4416\n"
"v_mfma_f32_16x16x32_bf16 a[60:63], a[252:255], v[108:111], a[60:63]\n"
"ds_read_b64_tr_b16 a[230:231], v218 offset:46608\n"
"ds_read_b128 v[168:171], v224 offset:4480\n"
"v_mfma_f32_16x16x32_bf16 a[64:67], a[160:163], v[96:99], a[64:67]\n"
"ds_read_b64_tr_b16 a[232:233], v218 offset:41496\n"
"ds_read_b128 v[172:175], v224 offset:4544\n"
"v_mfma_f32_16x16x32_bf16 a[64:67], a[164:167], v[104:107], a[64:67]\n"
"ds_read_b64_tr_b16 a[234:235], v218 offset:42520\n"
"v_cvt_pk_bf16_f32 v124, v184, v185\n"
"v_mfma_f32_16x16x32_bf16 a[68:71], a[160:163], v[100:103], a[68:71]\n"
"ds_read_b64_tr_b16 a[236:237], v218 offset:45592\n"
"v_cvt_pk_bf16_f32 v125, v186, v187\n"
"v_mfma_f32_16x16x32_bf16 a[68:71], a[164:167], v[108:111], a[68:71]\n"
"s_waitcnt lgkmcnt(14)\n"
"ds_read_b64_tr_b16 a[238:239], v218 offset:46616\n"
"buffer_load_dwordx4 v247, s[12:15], s16 offen offset:1024 lds\n"
"v_mfma_f32_16x16x32_bf16 a[72:75], a[168:171], v[96:99], a[72:75]\n"
"ds_read_b64_tr_b16 a[240:241], v218 offset:41744\n"
"v_cvt_pk_bf16_f32 v126, v188, v189\n"
"v_mfma_f32_16x16x32_bf16 a[72:75], a[172:175], v[104:107], a[72:75]\n"
"ds_read_b64_tr_b16 a[242:243], v218 offset:42768\n"
"v_cvt_pk_bf16_f32 v127, v190, v191\n"
"v_mfma_f32_16x16x32_bf16 a[76:79], a[168:171], v[100:103], a[76:79]\n"
"ds_read_b64_tr_b16 a[244:245], v218 offset:45840\n"
"v_fma_mix_f32 v176, v128, v144, v152 op_sel:[0,0,0] op_sel_hi:[1,0,0]\n"
"v_mfma_f32_16x16x32_bf16 a[76:79], a[172:175], v[108:111], a[76:79]\n"
"ds_read_b64_tr_b16 a[246:247], v218 offset:46864\n"
"v_fma_mix_f32 v177, v128, v145, v153 op_sel:[1,0,0] op_sel_hi:[1,0,0]\n"
"v_mfma_f32_16x16x32_bf16 a[80:83], a[176:179], v[96:99], a[80:83]\n"
"ds_read_b64_tr_b16 a[248:249], v218 offset:41752\n"
"v_fma_mix_f32 v178, v129, v146, v154 op_sel:[0,0,0] op_sel_hi:[1,0,0]\n"
"v_mfma_f32_16x16x32_bf16 a[80:83], a[180:183], v[104:107], a[80:83]\n"
"ds_read_b64_tr_b16 a[250:251], v218 offset:42776\n"
"buffer_load_dwordx4 v246, s[12:15], s16 offen offset:2048 lds\n"
"v_mfma_f32_16x16x32_bf16 a[84:87], a[176:179], v[100:103], a[84:87]\n"
"ds_read_b64_tr_b16 a[252:253], v218 offset:45848\n"
"v_fma_mix_f32 v179, v129, v147, v155 op_sel:[1,0,0] op_sel_hi:[1,0,0]\n"
"v_mfma_f32_16x16x32_bf16 a[84:87], a[180:183], v[108:111], a[84:87]\n"
"s_waitcnt lgkmcnt(12)\n"
"ds_read_b64_tr_b16 a[254:255], v218 offset:46872\n"
"v_fma_mix_f32 v180, v130, v148, v156 op_sel:[0,0,0] op_sel_hi:[1,0,0]\n"
"v_mfma_f32_16x16x32_bf16 a[88:91], a[184:187], v[96:99], a[88:91]\n"
"ds_read_b64_tr_b16 a[160:161], v219 offset:0\n"
"v_fma_mix_f32 v181, v130, v149, v157 op_sel:[1,0,0] op_sel_hi:[1,0,0]\n"
"v_mfma_f32_16x16x32_bf16 a[88:91], a[188:191], v[104:107], a[88:91]\n"
"ds_read_b64_tr_b16 a[162:163], v219 offset:1024\n"
"v_fma_mix_f32 v182, v131, v150, v158 op_sel:[0,0,0] op_sel_hi:[1,0,0]\n"
"v_mfma_f32_16x16x32_bf16 a[92:95], a[184:187], v[100:103], a[92:95]\n"
"ds_read_b64_tr_b16 a[164:165], v219 offset:4096\n"
"v_fma_mix_f32 v183, v131, v151, v159 op_sel:[1,0,0] op_sel_hi:[1,0,0]\n"
"v_mfma_f32_16x16x32_bf16 a[92:95], a[188:191], v[108:111], a[92:95]\n"
"buffer_load_dwordx4 v247, s[12:15], s16 offen offset:3072 lds\n"
"ds_read_b64_tr_b16 a[166:167], v219 offset:5120\n"
"v_mfma_f32_16x16x32_bf16 a[96:99], a[224:227], v[96:99], a[96:99]\n"
"s_add_u32 s16, s16, 16384\n"
"ds_read_b64_tr_b16 a[168:169], v219 offset:8\n"
"v_cvt_pk_bf16_f32 v128, v176, v177\n"
"v_mfma_f32_16x16x32_bf16 a[96:99], a[228:231], v[104:107], a[96:99]\n"
"ds_read_b64_tr_b16 a[170:171], v219 offset:1032\n"
"v_cvt_pk_bf16_f32 v129, v178, v179\n"
"v_mfma_f32_16x16x32_bf16 a[100:103], a[224:227], v[100:103], a[100:103]\n"
"ds_read_b64_tr_b16 a[172:173], v219 offset:4104\n"
"v_cvt_pk_bf16_f32 v130, v180, v181\n"
"v_mfma_f32_16x16x32_bf16 a[100:103], a[228:231], v[108:111], a[100:103]\n"
"s_waitcnt lgkmcnt(11)\n"
"ds_read_b64_tr_b16 a[174:175], v219 offset:5128\n"
"v_cvt_pk_bf16_f32 v131, v182, v183\n"
"v_mfma_f32_16x16x32_bf16 a[104:107], a[232:235], v[96:99], a[104:107]\n"
"ds_read_b64_tr_b16 a[176:177], v219 offset:256\n"
"v_fma_mix_f32 v184, v132, v144, v152 op_sel:[0,0,0] op_sel_hi:[1,0,0]\n"
"v_mfma_f32_16x16x32_bf16 a[104:107], a[236:239], v[104:107], a[104:107]\n"
"ds_read_b64_tr_b16 a[178:179], v219 offset:1280\n"
"v_fma_mix_f32 v185, v132, v145, v153 op_sel:[1,0,0] op_sel_hi:[1,0,0]\n"
"v_mfma_f32_16x16x32_bf16 a[108:111], a[232:235], v[100:103], a[108:111]\n"
"ds_read_b64_tr_b16 a[180:181], v219 offset:4352\n"
"v_fma_mix_f32 v186, v133, v146, v154 op_sel:[0,0,0] op_sel_hi:[1,0,0]\n"
"v_mfma_f32_16x16x32_bf16 a[108:111], a[236:239], v[108:111], a[108:111]\n"
"ds_read_b64_tr_b16 a[182:183], v219 offset:5376\n"
"v_fma_mix_f32 v187, v133, v147, v155 op_sel:[1,0,0] op_sel_hi:[1,0,0]\n"
"v_mfma_f32_16x16x32_bf16 a[112:115], a[240:243], v[96:99], a[112:115]\n"
"ds_read_b64_tr_b16 a[184:185], v219 offset:264\n"
"v_fma_mix_f32 v188, v134, v148, v156 op_sel:[0,0,0] op_sel_hi:[1,0,0]\n"
"v_mfma_f32_16x16x32_bf16 a[112:115], a[244:247], v[104:107], a[112:115]\n"
"ds_read_b64_tr_b16 a[186:187], v219 offset:1288\n"
"v_fma_mix_f32 v189, v134, v149, v157 op_sel:[1,0,0] op_sel_hi:[1,0,0]\n"
"v_mfma_f32_16x16x32_bf16 a[116:119], a[240:243], v[100:103], a[116:119]\n"
"ds_read_b64_tr_b16 a[188:189], v219 offset:4360\n"
"v_fma_mix_f32 v190, v135, v150, v158 op_sel:[0,0,0] op_sel_hi:[1,0,0]\n"
"v_mfma_f32_16x16x32_bf16 a[116:119], a[244:247], v[108:111], a[116:119]\n"
"s_waitcnt lgkmcnt(11)\n"
"ds_read_b64_tr_b16 a[190:191], v219 offset:5384\n"
"v_fma_mix_f32 v191, v135, v151, v159 op_sel:[1,0,0] op_sel_hi:[1,0,0]\n"
"v_mfma_f32_16x16x32_bf16 a[120:123], a[248:251], v[96:99], a[120:123]\n"
"ds_read_b64_tr_b16 a[224:225], v219 offset:512\n"
"v_cvt_pk_bf16_f32 v132, v184, v185\n"
"v_mfma_f32_16x16x32_bf16 a[120:123], a[252:255], v[104:107], a[120:123]\n"
"ds_read_b64_tr_b16 a[226:227], v219 offset:1536\n"
"v_cvt_pk_bf16_f32 v133, v186, v187\n"
"v_mfma_f32_16x16x32_bf16 a[124:127], a[248:251], v[100:103], a[124:127]\n"
"ds_read_b64_tr_b16 a[228:229], v219 offset:4608\n"
"v_cvt_pk_bf16_f32 v134, v188, v189\n"
"v_mfma_f32_16x16x32_bf16 a[124:127], a[252:255], v[108:111], a[124:127]\n"
"ds_read_b64_tr_b16 a[230:231], v219 offset:5632\n"
"v_cvt_pk_bf16_f32 v135, v190, v191\n"
"v_mfma_f32_16x16x32_bf16 a[0:3], a[160:163], v[112:115], a[0:3]\n"
"ds_read_b64_tr_b16 a[232:233], v219 offset:520\n"
"v_fma_mix_f32 v176, v136, v160, v168 op_sel:[0,0,0] op_sel_hi:[1,0,0]\n"
"v_mfma_f32_16x16x32_bf16 a[0:3], a[164:167], v[120:123], a[0:3]\n"
"ds_read_b64_tr_b16 a[234:235], v219 offset:1544\n"
"v_fma_mix_f32 v177, v136, v161, v169 op_sel:[1,0,0] op_sel_hi:[1,0,0]\n"
"v_mfma_f32_16x16x32_bf16 a[4:7], a[160:163], v[116:119], a[4:7]\n"
"s_waitcnt vmcnt(8)\n"
"s_barrier\n"
"ds_read_b64_tr_b16 a[236:237], v219 offset:4616\n"
"v_fma_mix_f32 v178, v137, v162, v170 op_sel:[0,0,0] op_sel_hi:[1,0,0]\n"
"v_mfma_f32_16x16x32_bf16 a[4:7], a[164:167], v[124:127], a[4:7]\n"
"s_waitcnt lgkmcnt(11)\n"
"ds_read_b64_tr_b16 a[238:239], v219 offset:5640\n"
"v_fma_mix_f32 v179, v137, v163, v171 op_sel:[1,0,0] op_sel_hi:[1,0,0]\n"
"v_mfma_f32_16x16x32_bf16 a[8:11], a[168:171], v[112:115], a[8:11]\n"
"s_add_u32 m0, s18, 32768\n"
"ds_read_b64_tr_b16 a[240:241], v219 offset:768\n"
"v_fma_mix_f32 v180, v138, v164, v172 op_sel:[0,0,0] op_sel_hi:[1,0,0]\n"
"v_mfma_f32_16x16x32_bf16 a[8:11], a[172:175], v[120:123], a[8:11]\n"
"ds_read_b64_tr_b16 a[242:243], v219 offset:1792\n"
"buffer_load_dwordx4 v246, s[8:11], s16 offen offset:0 lds\n"
"v_mfma_f32_16x16x32_bf16 a[12:15], a[168:171], v[116:119], a[12:15]\n"
"ds_read_b64_tr_b16 a[244:245], v219 offset:4864\n"
"v_fma_mix_f32 v181, v138, v165, v173 op_sel:[1,0,0] op_sel_hi:[1,0,0]\n"
"v_mfma_f32_16x16x32_bf16 a[12:15], a[172:175], v[124:127], a[12:15]\n"
"ds_read_b64_tr_b16 a[246:247], v219 offset:5888\n"
"v_fma_mix_f32 v182, v139, v166, v174 op_sel:[0,0,0] op_sel_hi:[1,0,0]\n"
"v_mfma_f32_16x16x32_bf16 a[16:19], a[176:179], v[112:115], a[16:19]\n"
"ds_read_b64_tr_b16 a[248:249], v219 offset:776\n"
"v_fma_mix_f32 v183, v139, v167, v175 op_sel:[1,0,0] op_sel_hi:[1,0,0]\n"
"v_mfma_f32_16x16x32_bf16 a[16:19], a[180:183], v[120:123], a[16:19]\n"
"ds_read_b64_tr_b16 a[250:251], v219 offset:1800\n"
"v_cvt_pk_bf16_f32 v136, v176, v177\n"
"v_mfma_f32_16x16x32_bf16 a[20:23], a[176:179], v[116:119], a[20:23]\n"
"ds_read_b64_tr_b16 a[252:253], v219 offset:4872\n"
"v_cvt_pk_bf16_f32 v137, v178, v179\n"
"v_mfma_f32_16x16x32_bf16 a[20:23], a[180:183], v[124:127], a[20:23]\n"
"s_waitcnt lgkmcnt(11)\n"
"ds_read_b64_tr_b16 a[254:255], v219 offset:5896\n"
"buffer_load_dwordx4 v246, s[8:11], s16 offen offset:1024 lds\n"
"v_mfma_f32_16x16x32_bf16 a[24:27], a[184:187], v[112:115], a[24:27]\n"
"ds_read_b64_tr_b16 a[160:161], v219 offset:8192\n"
"v_cvt_pk_bf16_f32 v138, v180, v181\n"
"v_mfma_f32_16x16x32_bf16 a[24:27], a[188:191], v[120:123], a[24:27]\n"
"ds_read_b64_tr_b16 a[162:163], v219 offset:9216\n"
"v_cvt_pk_bf16_f32 v139, v182, v183\n"
"v_mfma_f32_16x16x32_bf16 a[28:31], a[184:187], v[116:119], a[28:31]\n"
"ds_read_b64_tr_b16 a[164:165], v219 offset:12288\n"
"v_fma_mix_f32 v184, v140, v160, v168 op_sel:[0,0,0] op_sel_hi:[1,0,0]\n"
"v_mfma_f32_16x16x32_bf16 a[28:31], a[188:191], v[124:127], a[28:31]\n"
"ds_read_b64_tr_b16 a[166:167], v219 offset:13312\n"
"v_fma_mix_f32 v185, v140, v161, v169 op_sel:[1,0,0] op_sel_hi:[1,0,0]\n"
"v_mfma_f32_16x16x32_bf16 a[32:35], a[224:227], v[112:115], a[32:35]\n"
"ds_read_b64_tr_b16 a[168:169], v219 offset:8200\n"
"v_fma_mix_f32 v186, v141, v162, v170 op_sel:[0,0,0] op_sel_hi:[1,0,0]\n"
"v_mfma_f32_16x16x32_bf16 a[32:35], a[228:231], v[120:123], a[32:35]\n"
"ds_read_b64_tr_b16 a[170:171], v219 offset:9224\n"
"buffer_load_dwordx4 v247, s[8:11], s16 offen offset:2048 lds\n"
"v_mfma_f32_16x16x32_bf16 a[36:39], a[224:227], v[116:119], a[36:39]\n"
"ds_read_b64_tr_b16 a[172:173], v219 offset:12296\n"
"v_fma_mix_f32 v187, v141, v163, v171 op_sel:[1,0,0] op_sel_hi:[1,0,0]\n"
"v_mfma_f32_16x16x32_bf16 a[36:39], a[228:231], v[124:127], a[36:39]\n"
"s_waitcnt lgkmcnt(11)\n"
"ds_read_b64_tr_b16 a[174:175], v219 offset:13320\n"
"v_fma_mix_f32 v188, v142, v164, v172 op_sel:[0,0,0] op_sel_hi:[1,0,0]\n"
"v_mfma_f32_16x16x32_bf16 a[40:43], a[232:235], v[112:115], a[40:43]\n"
"ds_read_b64_tr_b16 a[176:177], v219 offset:8448\n"
"v_fma_mix_f32 v189, v142, v165, v173 op_sel:[1,0,0] op_sel_hi:[1,0,0]\n"
"v_mfma_f32_16x16x32_bf16 a[40:43], a[236:239], v[120:123], a[40:43]\n"
"ds_read_b64_tr_b16 a[178:179], v219 offset:9472\n"
"v_fma_mix_f32 v190, v143, v166, v174 op_sel:[0,0,0] op_sel_hi:[1,0,0]\n"
"v_mfma_f32_16x16x32_bf16 a[44:47], a[232:235], v[116:119], a[44:47]\n"
"ds_read_b64_tr_b16 a[180:181], v219 offset:12544\n"
"v_fma_mix_f32 v191, v143, v167, v175 op_sel:[1,0,0] op_sel_hi:[1,0,0]\n"
"v_mfma_f32_16x16x32_bf16 a[44:47], a[236:239], v[124:127], a[44:47]\n"
"ds_read_b64_tr_b16 a[182:183], v219 offset:13568\n"
"buffer_load_dwordx4 v247, s[8:11], s16 offen offset:3072 lds\n"
"v_mfma_f32_16x16x32_bf16 a[48:51], a[240:243], v[112:115], a[48:51]\n"
"ds_read_b64_tr_b16 a[184:185], v219 offset:8456\n"
"v_cvt_pk_bf16_f32 v140, v184, v185\n"
"v_mfma_f32_16x16x32_bf16 a[48:51], a[244:247], v[120:123], a[48:51]\n"
"ds_read_b64_tr_b16 a[186:187], v219 offset:9480\n"
"v_cvt_pk_bf16_f32 v141, v186, v187\n"
"v_mfma_f32_16x16x32_bf16 a[52:55], a[240:243], v[116:119], a[52:55]\n"
"ds_read_b64_tr_b16 a[188:189], v219 offset:12552\n"
"v_cvt_pk_bf16_f32 v142, v188, v189\n"
"v_mfma_f32_16x16x32_bf16 a[52:55], a[244:247], v[124:127], a[52:55]\n"
"s_waitcnt lgkmcnt(11)\n"
"ds_read_b64_tr_b16 a[190:191], v219 offset:13576\n"
"v_cvt_pk_bf16_f32 v143, v190, v191\n"
"v_mfma_f32_16x16x32_bf16 a[56:59], a[248:251], v[112:115], a[56:59]\n"
"s_add_u32 m0, s18, 49152\n"
"ds_read_b64_tr_b16 a[224:225], v219 offset:8704\n"
"v_mfma_f32_16x16x32_bf16 a[56:59], a[252:255], v[120:123], a[56:59]\n"
"ds_read_b64_tr_b16 a[226:227], v219 offset:9728\n"
"buffer_load_dwordx4 v246, s[12:15], s16 offen offset:0 lds\n"
"v_mfma_f32_16x16x32_bf16 a[60:63], a[248:251], v[116:119], a[60:63]\n"
"ds_read_b64_tr_b16 a[228:229], v219 offset:12800\n"
"v_mfma_f32_16x16x32_bf16 a[60:63], a[252:255], v[124:127], a[60:63]\n"
"ds_read_b64_tr_b16 a[230:231], v219 offset:13824\n"
"v_mfma_f32_16x16x32_bf16 a[64:67], a[160:163], v[112:115], a[64:67]\n"
"ds_read_b64_tr_b16 a[232:233], v219 offset:8712\n"
"v_mfma_f32_16x16x32_bf16 a[64:67], a[164:167], v[120:123], a[64:67]\n"
"ds_read_b64_tr_b16 a[234:235], v219 offset:9736\n"
"v_mfma_f32_16x16x32_bf16 a[68:71], a[160:163], v[116:119], a[68:71]\n"
"ds_read_b64_tr_b16 a[236:237], v219 offset:12808\n"
"v_mfma_f32_16x16x32_bf16 a[68:71], a[164:167], v[124:127], a[68:71]\n"
"s_waitcnt lgkmcnt(11)\n"
"ds_read_b64_tr_b16 a[238:239], v219 offset:13832\n"
"buffer_load_dwordx4 v247, s[12:15], s16 offen offset:1024 lds\n"
"v_mfma_f32_16x16x32_bf16 a[72:75], a[168:171], v[112:115], a[72:75]\n"
"ds_read_b64_tr_b16 a[240:241], v219 offset:8960\n"
"v_mfma_f32_16x16x32_bf16 a[72:75], a[172:175], v[120:123], a[72:75]\n"
"ds_read_b64_tr_b16 a[242:243], v219 offset:9984\n"
"v_mfma_f32_16x16x32_bf16 a[76:79], a[168:171], v[116:119], a[76:79]\n"
"ds_read_b64_tr_b16 a[244:245], v219 offset:13056\n"
"v_mfma_f32_16x16x32_bf16 a[76:79], a[172:175], v[124:127], a[76:79]\n"
"ds_read_b64_tr_b16 a[246:247], v219 offset:14080\n"
"v_mfma_f32_16x16x32_bf16 a[80:83], a[176:179], v[112:115], a[80:83]\n"
"ds_read_b64_tr_b16 a[248:249], v219 offset:8968\n"
"v_mfma_f32_16x16x32_bf16 a[80:83], a[180:183], v[120:123], a[80:83]\n"
"ds_read_b64_tr_b16 a[250:251], v219 offset:9992\n"
"buffer_load_dwordx4 v246, s[12:15], s16 offen offset:2048 lds\n"
"v_mfma_f32_16x16x32_bf16 a[84:87], a[176:179], v[116:119], a[84:87]\n"
"ds_read_b64_tr_b16 a[252:253], v219 offset:13064\n"
"v_mfma_f32_16x16x32_bf16 a[84:87], a[180:183], v[124:127], a[84:87]\n"
"s_waitcnt lgkmcnt(11)\n"
"ds_read_b64_tr_b16 a[254:255], v219 offset:14088\n"
"v_mfma_f32_16x16x32_bf16 a[88:91], a[184:187], v[112:115], a[88:91]\n"
"ds_read_b64_tr_b16 a[160:161], v219 offset:32784\n"
"v_mfma_f32_16x16x32_bf16 a[88:91], a[188:191], v[120:123], a[88:91]\n"
"ds_read_b64_tr_b16 a[162:163], v219 offset:33808\n"
"v_mfma_f32_16x16x32_bf16 a[92:95], a[184:187], v[116:119], a[92:95]\n"
"ds_read_b64_tr_b16 a[164:165], v219 offset:36880\n"
"v_mfma_f32_16x16x32_bf16 a[92:95], a[188:191], v[124:127], a[92:95]\n"
"buffer_load_dwordx4 v247, s[12:15], s16 offen offset:3072 lds\n"
"ds_read_b64_tr_b16 a[166:167], v219 offset:37904\n"
"v_mfma_f32_16x16x32_bf16 a[96:99], a[224:227], v[112:115], a[96:99]\n"
"s_add_u32 s16, s16, 16384\n"
"ds_read_b64_tr_b16 a[168:169], v219 offset:32792\n"
"v_mfma_f32_16x16x32_bf16 a[96:99], a[228:231], v[120:123], a[96:99]\n"
"ds_read_b64_tr_b16 a[170:171], v219 offset:33816\n"
"v_mfma_f32_16x16x32_bf16 a[100:103], a[224:227], v[116:119], a[100:103]\n"
"ds_read_b64_tr_b16 a[172:173], v219 offset:36888\n"
"v_mfma_f32_16x16x32_bf16 a[100:103], a[228:231], v[124:127], a[100:103]\n"
"s_waitcnt lgkmcnt(11)\n"
"ds_read_b64_tr_b16 a[174:175], v219 offset:37912\n"
"v_mfma_f32_16x16x32_bf16 a[104:107], a[232:235], v[112:115], a[104:107]\n"
"ds_read_b64_tr_b16 a[176:177], v219 offset:33040\n"
"v_mfma_f32_16x16x32_bf16 a[104:107], a[236:239], v[120:123], a[104:107]\n"
"ds_read_b64_tr_b16 a[178:179], v219 offset:34064\n"
"v_mfma_f32_16x16x32_bf16 a[108:111], a[232:235], v[116:119], a[108:111]\n"
"ds_read_b64_tr_b16 a[180:181], v219 offset:37136\n"
"v_mfma_f32_16x16x32_bf16 a[108:111], a[236:239], v[124:127], a[108:111]\n"
"ds_read_b64_tr_b16 a[182:183], v219 offset:38160\n"
"v_mfma_f32_16x16x32_bf16 a[112:115], a[240:243], v[112:115], a[112:115]\n"
"ds_read_b64_tr_b16 a[184:185], v219 offset:33048\n"
"v_mfma_f32_16x16x32_bf16 a[112:115], a[244:247], v[120:123], a[112:115]\n"
"ds_read_b64_tr_b16 a[186:187], v219 offset:34072\n"
"v_mfma_f32_16x16x32_bf16 a[116:119], a[240:243], v[116:119], a[116:119]\n"
"ds_read_b64_tr_b16 a[188:189], v219 offset:37144\n"
"v_mfma_f32_16x16x32_bf16 a[116:119], a[244:247], v[124:127], a[116:119]\n"
"s_waitcnt lgkmcnt(11)\n"
"ds_read_b64_tr_b16 a[190:191], v219 offset:38168\n"
"v_mfma_f32_16x16x32_bf16 a[120:123], a[248:251], v[112:115], a[120:123]\n"
"ds_read_b64_tr_b16 a[224:225], v219 offset:33296\n"
"v_mfma_f32_16x16x32_bf16 a[120:123], a[252:255], v[120:123], a[120:123]\n"
"ds_read_b64_tr_b16 a[226:227], v219 offset:34320\n"
"v_mfma_f32_16x16x32_bf16 a[124:127], a[248:251], v[116:119], a[124:127]\n"
"ds_read_b64_tr_b16 a[228:229], v219 offset:37392\n"
"v_mfma_f32_16x16x32_bf16 a[124:127], a[252:255], v[124:127], a[124:127]\n"
"ds_read_b64_tr_b16 a[230:231], v219 offset:38416\n"
"v_mfma_f32_16x16x32_bf16 a[0:3], a[160:163], v[128:131], a[0:3]\n"
"ds_read_b64_tr_b16 a[232:233], v219 offset:33304\n"
"v_mfma_f32_16x16x32_bf16 a[0:3], a[164:167], v[136:139], a[0:3]\n"
"ds_read_b64_tr_b16 a[234:235], v219 offset:34328\n"
"v_mfma_f32_16x16x32_bf16 a[4:7], a[160:163], v[132:135], a[4:7]\n"
"s_waitcnt vmcnt(8)\n"
"s_barrier\n"
"ds_read_b64_tr_b16 a[236:237], v219 offset:37400\n"
"v_mfma_f32_16x16x32_bf16 a[4:7], a[164:167], v[140:143], a[4:7]\n"
"s_waitcnt lgkmcnt(11)\n"
"ds_read_b64_tr_b16 a[238:239], v219 offset:38424\n"
"v_mfma_f32_16x16x32_bf16 a[8:11], a[168:171], v[128:131], a[8:11]\n"
"s_add_u32 m0, s18, 65536\n"
"ds_read_b64_tr_b16 a[240:241], v219 offset:33552\n"
"v_mfma_f32_16x16x32_bf16 a[8:11], a[172:175], v[136:139], a[8:11]\n"
"ds_read_b64_tr_b16 a[242:243], v219 offset:34576\n"
"buffer_load_dwordx4 v246, s[8:11], s16 offen offset:0 lds\n"
"v_mfma_f32_16x16x32_bf16 a[12:15], a[168:171], v[132:135], a[12:15]\n"
"ds_read_b64_tr_b16 a[244:245], v219 offset:37648\n"
"v_accvgpr_read_b32 v176, a0\n"
"v_mfma_f32_16x16x32_bf16 a[12:15], a[172:175], v[140:143], a[12:15]\n"
"ds_read_b64_tr_b16 a[246:247], v219 offset:38672\n"
"v_accvgpr_read_b32 v177, a1\n"
"v_mfma_f32_16x16x32_bf16 a[16:19], a[176:179], v[128:131], a[16:19]\n"
"ds_read_b64_tr_b16 a[248:249], v219 offset:33560\n"
"v_accvgpr_read_b32 v178, a2\n"
"v_mfma_f32_16x16x32_bf16 a[16:19], a[180:183], v[136:139], a[16:19]\n"
"ds_read_b64_tr_b16 a[250:251], v219 offset:34584\n"
"v_accvgpr_read_b32 v179, a3\n"
"v_mfma_f32_16x16x32_bf16 a[20:23], a[176:179], v[132:135], a[20:23]\n"
"ds_read_b64_tr_b16 a[252:253], v219 offset:37656\n"
"v_cvt_pk_bf16_f32 v64, v176, v177\n"
"v_mfma_f32_16x16x32_bf16 a[20:23], a[180:183], v[140:143], a[20:23]\n"
"s_waitcnt lgkmcnt(11)\n"
"ds_read_b64_tr_b16 a[254:255], v219 offset:38680\n"
"buffer_load_dwordx4 v246, s[8:11], s16 offen offset:1024 lds\n"
"v_mfma_f32_16x16x32_bf16 a[24:27], a[184:187], v[128:131], a[24:27]\n"
"ds_read_b64_tr_b16 a[160:161], v219 offset:40976\n"
"v_cvt_pk_bf16_f32 v65, v178, v179\n"
"v_mfma_f32_16x16x32_bf16 a[24:27], a[188:191], v[136:139], a[24:27]\n"
"ds_read_b64_tr_b16 a[162:163], v219 offset:42000\n"
"v_accvgpr_read_b32 v180, a4\n"
"v_mfma_f32_16x16x32_bf16 a[28:31], a[184:187], v[132:135], a[28:31]\n"
"ds_read_b64_tr_b16 a[164:165], v219 offset:45072\n"
"v_accvgpr_read_b32 v181, a5\n"
"v_mfma_f32_16x16x32_bf16 a[28:31], a[188:191], v[140:143], a[28:31]\n"
"ds_read_b64_tr_b16 a[166:167], v219 offset:46096\n"
"v_accvgpr_read_b32 v182, a6\n"
"v_mfma_f32_16x16x32_bf16 a[32:35], a[224:227], v[128:131], a[32:35]\n"
"ds_read_b64_tr_b16 a[168:169], v219 offset:40984\n"
"v_accvgpr_read_b32 v183, a7\n"
"v_mfma_f32_16x16x32_bf16 a[32:35], a[228:231], v[136:139], a[32:35]\n"
"ds_read_b64_tr_b16 a[170:171], v219 offset:42008\n"
"buffer_load_dwordx4 v247, s[8:11], s16 offen offset:2048 lds\n"
"v_mfma_f32_16x16x32_bf16 a[36:39], a[224:227], v[132:135], a[36:39]\n"
"ds_read_b64_tr_b16 a[172:173], v219 offset:45080\n"
"v_cvt_pk_bf16_f32 v96, v180, v181\n"
"v_mfma_f32_16x16x32_bf16 a[36:39], a[228:231], v[140:143], a[36:39]\n"
"s_waitcnt lgkmcnt(11)\n"
"ds_read_b64_tr_b16 a[174:175], v219 offset:46104\n"
"v_cvt_pk_bf16_f32 v97, v182, v183\n"
"v_mfma_f32_16x16x32_bf16 a[40:43], a[232:235], v[128:131], a[40:43]\n"
"ds_read_b64_tr_b16 a[176:177], v219 offset:41232\n"
"v_accvgpr_read_b32 v184, a8\n"
"v_mfma_f32_16x16x32_bf16 a[40:43], a[236:239], v[136:139], a[40:43]\n"
"ds_read_b64_tr_b16 a[178:179], v219 offset:42256\n"
"v_accvgpr_read_b32 v185, a9\n"
"v_mfma_f32_16x16x32_bf16 a[44:47], a[232:235], v[132:135], a[44:47]\n"
"ds_read_b64_tr_b16 a[180:181], v219 offset:45328\n"
"v_accvgpr_read_b32 v186, a10\n"
"v_mfma_f32_16x16x32_bf16 a[44:47], a[236:239], v[140:143], a[44:47]\n"
"ds_read_b64_tr_b16 a[182:183], v219 offset:46352\n"
"buffer_load_dwordx4 v247, s[8:11], s16 offen offset:3072 lds\n"
"v_mfma_f32_16x16x32_bf16 a[48:51], a[240:243], v[128:131], a[48:51]\n"
"ds_read_b64_tr_b16 a[184:185], v219 offset:41240\n"
"v_accvgpr_read_b32 v187, a11\n"
"v_mfma_f32_16x16x32_bf16 a[48:51], a[244:247], v[136:139], a[48:51]\n"
"ds_read_b64_tr_b16 a[186:187], v219 offset:42264\n"
"v_cvt_pk_bf16_f32 v66, v184, v185\n"
"v_mfma_f32_16x16x32_bf16 a[52:55], a[240:243], v[132:135], a[52:55]\n"
"ds_read_b64_tr_b16 a[188:189], v219 offset:45336\n"
"v_cvt_pk_bf16_f32 v67, v186, v187\n"
"v_mfma_f32_16x16x32_bf16 a[52:55], a[244:247], v[140:143], a[52:55]\n"
"s_waitcnt lgkmcnt(11)\n"
"ds_read_b64_tr_b16 a[190:191], v219 offset:46360\n"
"v_accvgpr_read_b32 v188, a12\n"
"v_mfma_f32_16x16x32_bf16 a[56:59], a[248:251], v[128:131], a[56:59]\n"
"s_add_u32 m0, s18, 81920\n"
"ds_read_b64_tr_b16 a[224:225], v219 offset:41488\n"
"v_accvgpr_read_b32 v189, a13\n"
"v_mfma_f32_16x16x32_bf16 a[56:59], a[252:255], v[136:139], a[56:59]\n"
"ds_read_b64_tr_b16 a[226:227], v219 offset:42512\n"
"buffer_load_dwordx4 v246, s[12:15], s16 offen offset:0 lds\n"
"v_mfma_f32_16x16x32_bf16 a[60:63], a[248:251], v[132:135], a[60:63]\n"
"ds_read_b64_tr_b16 a[228:229], v219 offset:45584\n"
"v_accvgpr_read_b32 v190, a14\n"
"v_mfma_f32_16x16x32_bf16 a[60:63], a[252:255], v[140:143], a[60:63]\n"
"ds_read_b64_tr_b16 a[230:231], v219 offset:46608\n"
"v_accvgpr_read_b32 v191, a15\n"
"v_mfma_f32_16x16x32_bf16 a[64:67], a[160:163], v[128:131], a[64:67]\n"
"ds_read_b64_tr_b16 a[232:233], v219 offset:41496\n"
"v_cvt_pk_bf16_f32 v98, v188, v189\n"
"v_mfma_f32_16x16x32_bf16 a[64:67], a[164:167], v[136:139], a[64:67]\n"
"ds_read_b64_tr_b16 a[234:235], v219 offset:42520\n"
"v_cvt_pk_bf16_f32 v99, v190, v191\n"
"v_mfma_f32_16x16x32_bf16 a[68:71], a[160:163], v[132:135], a[68:71]\n"
"ds_read_b64_tr_b16 a[236:237], v219 offset:45592\n"
"v_accvgpr_read_b32 v176, a16\n"
"v_mfma_f32_16x16x32_bf16 a[68:71], a[164:167], v[140:143], a[68:71]\n"
"s_waitcnt lgkmcnt(11)\n"
"ds_read_b64_tr_b16 a[238:239], v219 offset:46616\n"
"buffer_load_dwordx4 v247, s[12:15], s16 offen offset:1024 lds\n"
"v_mfma_f32_16x16x32_bf16 a[72:75], a[168:171], v[128:131], a[72:75]\n"
"ds_read_b64_tr_b16 a[240:241], v219 offset:41744\n"
"v_accvgpr_read_b32 v177, a17\n"
"v_mfma_f32_16x16x32_bf16 a[72:75], a[172:175], v[136:139], a[72:75]\n"
"ds_read_b64_tr_b16 a[242:243], v219 offset:42768\n"
"v_accvgpr_read_b32 v178, a18\n"
"v_mfma_f32_16x16x32_bf16 a[76:79], a[168:171], v[132:135], a[76:79]\n"
"ds_read_b64_tr_b16 a[244:245], v219 offset:45840\n"
"v_accvgpr_read_b32 v179, a19\n"
"v_mfma_f32_16x16x32_bf16 a[76:79], a[172:175], v[140:143], a[76:79]\n"
"ds_read_b64_tr_b16 a[246:247], v219 offset:46864\n"
"v_cvt_pk_bf16_f32 v68, v176, v177\n"
"v_mfma_f32_16x16x32_bf16 a[80:83], a[176:179], v[128:131], a[80:83]\n"
"ds_read_b64_tr_b16 a[248:249], v219 offset:41752\n"
"v_cvt_pk_bf16_f32 v69, v178, v179\n"
"v_mfma_f32_16x16x32_bf16 a[80:83], a[180:183], v[136:139], a[80:83]\n"
"ds_read_b64_tr_b16 a[250:251], v219 offset:42776\n"
"buffer_load_dwordx4 v246, s[12:15], s16 offen offset:2048 lds\n"
"v_mfma_f32_16x16x32_bf16 a[84:87], a[176:179], v[132:135], a[84:87]\n"
"ds_read_b64_tr_b16 a[252:253], v219 offset:45848\n"
"v_accvgpr_read_b32 v180, a20\n"
"v_mfma_f32_16x16x32_bf16 a[84:87], a[180:183], v[140:143], a[84:87]\n"
"s_waitcnt lgkmcnt(11)\n"
"ds_read_b64_tr_b16 a[254:255], v219 offset:46872\n"
"v_accvgpr_read_b32 v181, a21\n"
"v_mfma_f32_16x16x32_bf16 a[88:91], a[184:187], v[128:131], a[88:91]\n"
"ds_read_b128 a[128:131], v240 offset:0\n"
"v_accvgpr_read_b32 v182, a22\n"
"v_mfma_f32_16x16x32_bf16 a[88:91], a[188:191], v[136:139], a[88:91]\n"
"ds_read_b128 a[132:135], v240 offset:256\n"
"v_accvgpr_read_b32 v183, a23\n"
"v_mfma_f32_16x16x32_bf16 a[92:95], a[184:187], v[132:135], a[92:95]\n"
"ds_read_b128 a[136:139], v240 offset:512\n"
"v_cvt_pk_bf16_f32 v100, v180, v181\n"
"v_mfma_f32_16x16x32_bf16 a[92:95], a[188:191], v[140:143], a[92:95]\n"
"buffer_load_dwordx4 v247, s[12:15], s16 offen offset:3072 lds\n"
"ds_read_b128 a[140:143], v240 offset:768\n"
"v_mfma_f32_16x16x32_bf16 a[96:99], a[224:227], v[128:131], a[96:99]\n"
"s_add_u32 s16, s16, 16384\n"
"ds_read_b128 a[144:147], v240 offset:4096\n"
"v_cvt_pk_bf16_f32 v101, v182, v183\n"
"v_mfma_f32_16x16x32_bf16 a[96:99], a[228:231], v[136:139], a[96:99]\n"
"ds_read_b128 a[148:151], v240 offset:4352\n"
"v_accvgpr_read_b32 v184, a24\n"
"v_mfma_f32_16x16x32_bf16 a[100:103], a[224:227], v[132:135], a[100:103]\n"
"ds_read_b128 a[152:155], v240 offset:4608\n"
"v_accvgpr_read_b32 v185, a25\n"
"v_mfma_f32_16x16x32_bf16 a[100:103], a[228:231], v[140:143], a[100:103]\n"
"s_waitcnt lgkmcnt(11)\n"
"ds_read_b128 a[156:159], v240 offset:4864\n"
"v_accvgpr_read_b32 v186, a26\n"
"v_mfma_f32_16x16x32_bf16 a[104:107], a[232:235], v[128:131], a[104:107]\n"
"ds_read_b64_tr_b16 a[192:193], v242 offset:16384\n"
"v_accvgpr_read_b32 v187, a27\n"
"v_mfma_f32_16x16x32_bf16 a[104:107], a[236:239], v[136:139], a[104:107]\n"
"ds_read_b64_tr_b16 a[194:195], v242 offset:20480\n"
"v_cvt_pk_bf16_f32 v70, v184, v185\n"
"v_mfma_f32_16x16x32_bf16 a[108:111], a[232:235], v[132:135], a[108:111]\n"
"ds_read_b64_tr_b16 a[196:197], v243 offset:16384\n"
"v_cvt_pk_bf16_f32 v71, v186, v187\n"
"v_mfma_f32_16x16x32_bf16 a[108:111], a[236:239], v[140:143], a[108:111]\n"
"ds_read_b64_tr_b16 a[198:199], v243 offset:20480\n"
"v_accvgpr_read_b32 v188, a28\n"
"v_mfma_f32_16x16x32_bf16 a[112:115], a[240:243], v[128:131], a[112:115]\n"
"ds_read_b64_tr_b16 a[200:201], v242 offset:16640\n"
"v_accvgpr_read_b32 v189, a29\n"
"v_mfma_f32_16x16x32_bf16 a[112:115], a[244:247], v[136:139], a[112:115]\n"
"ds_read_b64_tr_b16 a[202:203], v242 offset:20736\n"
"v_accvgpr_read_b32 v190, a30\n"
"v_mfma_f32_16x16x32_bf16 a[116:119], a[240:243], v[132:135], a[116:119]\n"
"ds_read_b64_tr_b16 a[204:205], v243 offset:16640\n"
"v_accvgpr_read_b32 v191, a31\n"
"v_mfma_f32_16x16x32_bf16 a[116:119], a[244:247], v[140:143], a[116:119]\n"
"s_waitcnt lgkmcnt(11)\n"
"ds_read_b64_tr_b16 a[206:207], v243 offset:20736\n"
"v_cvt_pk_bf16_f32 v102, v188, v189\n"
"v_mfma_f32_16x16x32_bf16 a[120:123], a[248:251], v[128:131], a[120:123]\n"
"ds_read_b64_tr_b16 a[208:209], v242 offset:16896\n"
"v_cvt_pk_bf16_f32 v103, v190, v191\n"
"v_mfma_f32_16x16x32_bf16 a[120:123], a[252:255], v[136:139], a[120:123]\n"
"ds_read_b64_tr_b16 a[210:211], v242 offset:20992\n"
"v_accvgpr_read_b32 v176, a32\n"
"v_mfma_f32_16x16x32_bf16 a[124:127], a[248:251], v[132:135], a[124:127]\n"
"ds_read_b64_tr_b16 a[212:213], v243 offset:16896\n"
"v_accvgpr_read_b32 v177, a33\n"
"v_mfma_f32_16x16x32_bf16 a[124:127], a[252:255], v[140:143], a[124:127]\n"
"ds_read_b64_tr_b16 a[214:215], v243 offset:20992\n"
"v_accvgpr_read_b32 v178, a34\n"
"ds_read_b64_tr_b16 a[216:217], v242 offset:17152\n"
"v_accvgpr_read_b32 v179, a35\n"
"v_cvt_pk_bf16_f32 v72, v176, v177\n"
"v_cvt_pk_bf16_f32 v73, v178, v179\n"
"ds_read_b64_tr_b16 a[218:219], v242 offset:21248\n"
"v_accvgpr_read_b32 v180, a36\n"
"v_accvgpr_read_b32 v181, a37\n"
"v_accvgpr_read_b32 v182, a38\n"
"ds_read_b64_tr_b16 a[220:221], v243 offset:17152\n"
"v_accvgpr_read_b32 v183, a39\n"
"v_cvt_pk_bf16_f32 v104, v180, v181\n"
"v_cvt_pk_bf16_f32 v105, v182, v183\n"
"ds_read_b64_tr_b16 a[222:223], v243 offset:21248\n"
"v_accvgpr_read_b32 v184, a40\n"
"v_accvgpr_read_b32 v185, a41\n"
"v_accvgpr_read_b32 v186, a42\n"
"v_accvgpr_read_b32 v187, a43\n"
"v_cvt_pk_bf16_f32 v74, v184, v185\n"
"v_cvt_pk_bf16_f32 v75, v186, v187\n"
"v_accvgpr_read_b32 v188, a44\n"
"v_accvgpr_read_b32 v189, a45\n"
"v_accvgpr_read_b32 v190, a46\n"
"v_accvgpr_read_b32 v191, a47\n"
"v_cvt_pk_bf16_f32 v106, v188, v189\n"
"v_cvt_pk_bf16_f32 v107, v190, v191\n"
"v_accvgpr_read_b32 v176, a48\n"
"v_accvgpr_read_b32 v177, a49\n"
"v_accvgpr_read_b32 v178, a50\n"
"v_accvgpr_read_b32 v179, a51\n"
"v_cvt_pk_bf16_f32 v76, v176, v177\n"
"v_cvt_pk_bf16_f32 v77, v178, v179\n"
"v_accvgpr_read_b32 v180, a52\n"
"v_accvgpr_read_b32 v181, a53\n"
"v_accvgpr_read_b32 v182, a54\n"
"v_accvgpr_read_b32 v183, a55\n"
"v_cvt_pk_bf16_f32 v108, v180, v181\n"
"v_cvt_pk_bf16_f32 v109, v182, v183\n"
"v_accvgpr_read_b32 v184, a56\n"
"v_accvgpr_read_b32 v185, a57\n"
"v_accvgpr_read_b32 v186, a58\n"
"v_accvgpr_read_b32 v187, a59\n"
"v_cvt_pk_bf16_f32 v78, v184, v185\n"
"v_cvt_pk_bf16_f32 v79, v186, v187\n"
"v_accvgpr_read_b32 v188, a60\n"
"v_accvgpr_read_b32 v189, a61\n"
"v_accvgpr_read_b32 v190, a62\n"
"v_accvgpr_read_b32 v191, a63\n"
"v_cvt_pk_bf16_f32 v110, v188, v189\n"
"v_cvt_pk_bf16_f32 v111, v190, v191\n"
"v_accvgpr_read_b32 v176, a64\n"
"v_accvgpr_read_b32 v177, a65\n"
"v_accvgpr_read_b32 v178, a66\n"
"v_accvgpr_read_b32 v179, a67\n"
"v_cvt_pk_bf16_f32 v80, v176, v177\n"
"v_cvt_pk_bf16_f32 v81, v178, v179\n"
"v_accvgpr_read_b32 v180, a68\n"
"v_accvgpr_read_b32 v181, a69\n"
"v_accvgpr_read_b32 v182, a70\n"
"v_accvgpr_read_b32 v183, a71\n"
"v_cvt_pk_bf16_f32 v112, v180, v181\n"
"v_cvt_pk_bf16_f32 v113, v182, v183\n"
"v_accvgpr_read_b32 v184, a72\n"
"v_accvgpr_read_b32 v185, a73\n"
"v_accvgpr_read_b32 v186, a74\n"
"v_accvgpr_read_b32 v187, a75\n"
"v_cvt_pk_bf16_f32 v82, v184, v185\n"
"v_cvt_pk_bf16_f32 v83, v186, v187\n"
"v_accvgpr_read_b32 v188, a76\n"
"v_accvgpr_read_b32 v189, a77\n"
"v_accvgpr_read_b32 v190, a78\n"
"v_accvgpr_read_b32 v191, a79\n"
"v_cvt_pk_bf16_f32 v114, v188, v189\n"
"v_cvt_pk_bf16_f32 v115, v190, v191\n"
"v_accvgpr_read_b32 v176, a80\n"
"v_accvgpr_read_b32 v177, a81\n"
"v_accvgpr_read_b32 v178, a82\n"
"v_accvgpr_read_b32 v179, a83\n"
"v_cvt_pk_bf16_f32 v84, v176, v177\n"
"v_cvt_pk_bf16_f32 v85, v178, v179\n"
"v_accvgpr_read_b32 v180, a84\n"
"v_accvgpr_read_b32 v181, a85\n"
"v_accvgpr_read_b32 v182, a86\n"
"v_accvgpr_read_b32 v183, a87\n"
"v_cvt_pk_bf16_f32 v116, v180, v181\n"
"v_cvt_pk_bf16_f32 v117, v182, v183\n"
"v_accvgpr_read_b32 v184, a88\n"
"v_accvgpr_read_b32 v185, a89\n"
"v_accvgpr_read_b32 v186, a90\n"
"v_accvgpr_read_b32 v187, a91\n"
"v_cvt_pk_bf16_f32 v86, v184, v185\n"
"v_cvt_pk_bf16_f32 v87, v186, v187\n"
"v_accvgpr_read_b32 v188, a92\n"
"v_accvgpr_read_b32 v189, a93\n"
"v_accvgpr_read_b32 v190, a94\n"
"v_accvgpr_read_b32 v191, a95\n"
"v_cvt_pk_bf16_f32 v118, v188, v189\n"
"v_cvt_pk_bf16_f32 v119, v190, v191\n"
"v_accvgpr_read_b32 v176, a96\n"
"v_accvgpr_read_b32 v177, a97\n"
"v_accvgpr_read_b32 v178, a98\n"
"v_accvgpr_read_b32 v179, a99\n"
"v_cvt_pk_bf16_f32 v88, v176, v177\n"
"v_cvt_pk_bf16_f32 v89, v178, v179\n"
"v_accvgpr_read_b32 v180, a100\n"
"v_accvgpr_read_b32 v181, a101\n"
"v_accvgpr_read_b32 v182, a102\n"
"v_accvgpr_read_b32 v183, a103\n"
"v_cvt_pk_bf16_f32 v120, v180, v181\n"
"v_cvt_pk_bf16_f32 v121, v182, v183\n"
"v_accvgpr_read_b32 v184, a104\n"
"v_accvgpr_read_b32 v185, a105\n"
"v_accvgpr_read_b32 v186, a106\n"
"v_accvgpr_read_b32 v187, a107\n"
"v_cvt_pk_bf16_f32 v90, v184, v185\n"
"v_cvt_pk_bf16_f32 v91, v186, v187\n"
"v_accvgpr_read_b32 v188, a108\n"
"v_accvgpr_read_b32 v189, a109\n"
"v_accvgpr_read_b32 v190, a110\n"
"v_accvgpr_read_b32 v191, a111\n"
"v_cvt_pk_bf16_f32 v122, v188, v189\n"
"v_cvt_pk_bf16_f32 v123, v190, v191\n"
"v_accvgpr_read_b32 v176, a112\n"
"v_accvgpr_read_b32 v177, a113\n"
"v_accvgpr_read_b32 v178, a114\n"
"v_accvgpr_read_b32 v179, a115\n"
"v_cvt_pk_bf16_f32 v92, v176, v177\n"
"v_cvt_pk_bf16_f32 v93, v178, v179\n"
"v_accvgpr_read_b32 v180, a116\n"
"v_accvgpr_read_b32 v181, a117\n"
"v_accvgpr_read_b32 v182, a118\n"
"v_accvgpr_read_b32 v183, a119\n"
"v_cvt_pk_bf16_f32 v124, v180, v181\n"
"v_cvt_pk_bf16_f32 v125, v182, v183\n"
"v_accvgpr_read_b32 v184, a120\n"
"v_accvgpr_read_b32 v185, a121\n"
"v_accvgpr_read_b32 v186, a122\n"
"v_accvgpr_read_b32 v187, a123\n"
"v_cvt_pk_bf16_f32 v94, v184, v185\n"
"v_cvt_pk_bf16_f32 v95, v186, v187\n"
"v_accvgpr_read_b32 v188, a124\n"
"v_accvgpr_read_b32 v189, a125\n"
"v_accvgpr_read_b32 v190, a126\n"
"v_accvgpr_read_b32 v191, a127\n"
"v_cvt_pk_bf16_f32 v126, v188, v189\n"
"v_cvt_pk_bf16_f32 v127, v190, v191\n"
"s_barrier\n"
"s_add_u32 s56, s40, 98304\n"
"s_nop 1\n"
"v_mfma_f32_16x16x32_bf16 v[128:131], a[128:131], v[64:67], 0\n"
"s_add_u32 m0, s18, 98304\n"
"v_and_b32 v232, 1, v226\n"
"v_lshlrev_b32 v204, 6, v232\n"
"v_mfma_f32_16x16x32_bf16 v[128:131], a[132:135], v[68:71], v[128:131]\n"
"buffer_load_dwordx4 v246, s[8:11], s16 offen offset:0 lds\n"
"v_lshlrev_b32 v192, 5, v232\n"
"v_mfma_f32_16x16x32_bf16 v[128:131], a[136:139], v[72:75], v[128:131]\n"
"v_and_b32 v232, 2, v226\n"
"v_xor_b32 v233, v232, v228\n"
"v_mfma_f32_16x16x32_bf16 v[128:131], a[140:143], v[76:79], v[128:131]\n"
"s_waitcnt lgkmcnt(12)\n"
"buffer_load_dwordx4 v246, s[8:11], s16 offen offset:1024 lds\n"
"v_lshl_add_u32 v204, v233, 4, v204\n"
"v_mfma_f32_16x16x32_bf16 v[132:135], a[144:147], v[64:67], 0\n"
"v_lshl_add_u32 v232, v232, 1, v227\n"
"v_mul_u32_u24 v232, 1152, v232\n"
"v_mfma_f32_16x16x32_bf16 v[132:135], a[148:151], v[68:71], v[132:135]\n"
"buffer_load_dwordx4 v247, s[8:11], s16 offen offset:2048 lds\n"
"v_add_u32 v204, v232, v204\n"
"v_mfma_f32_16x16x32_bf16 v[132:135], a[152:155], v[72:75], v[132:135]\n"
"v_lshl_add_u32 v192, v226, 9, v192\n"
"v_lshl_add_u32 v192, v227, 6, v192\n"
"v_mfma_f32_16x16x32_bf16 v[132:135], a[156:159], v[76:79], v[132:135]\n"
"buffer_load_dwordx4 v247, s[8:11], s16 offen offset:3072 lds\n"
"v_lshl_add_u32 v192, v228, 3, v192\n"
"v_mfma_f32_16x16x32_bf16 v[136:139], a[128:131], v[80:83], 0\n"
"s_add_u32 m0, s18, 114688\n"
"v_add_u32 v192, s40, v192\n"
"v_xor_b32 v195, 32, v192\n"
"v_mfma_f32_16x16x32_bf16 v[136:139], a[132:135], v[84:87], v[136:139]\n"
"buffer_load_dwordx4 v246, s[12:15], s16 offen offset:0 lds\n"
"v_add_u32 v193, 65536, v192\n"
"v_mfma_f32_16x16x32_bf16 v[136:139], a[136:139], v[88:91], v[136:139]\n"
"v_add_u32 v196, 65536, v195\n"
"v_add_u32 v194, 131072, v192\n"
"v_mfma_f32_16x16x32_bf16 v[136:139], a[140:143], v[92:95], v[136:139]\n"
"buffer_load_dwordx4 v247, s[12:15], s16 offen offset:1024 lds\n"
"v_add_u32 v197, 131072, v195\n"
"v_mfma_f32_16x16x32_bf16 v[140:143], a[144:147], v[80:83], 0\n"
"v_lshlrev_b32 v205, 4, v248\n"
"v_add_u32 v206, 18432, v205\n"
"v_mfma_f32_16x16x32_bf16 v[140:143], a[148:151], v[84:87], v[140:143]\n"
"buffer_load_dwordx4 v246, s[12:15], s16 offen offset:2048 lds\n"
"v_lshl_add_u32 v207, v228, 2, s56\n"
"v_mfma_f32_16x16x32_bf16 v[140:143], a[152:155], v[88:91], v[140:143]\n"
"v_mov_b32 v40, 0x3f803f80\n"
"v_mov_b32 v41, 0x3f803f80\n"
"v_mfma_f32_16x16x32_bf16 v[140:143], a[156:159], v[92:95], v[140:143]\n"
"buffer_load_dwordx4 v247, s[12:15], s16 offen offset:3072 lds\n"
"v_mov_b32 v42, 0x3f803f80\n"
"v_mfma_f32_16x16x32_bf16 v[144:147], a[128:131], v[96:99], 0\n"
"s_add_u32 s16, s16, 16384\n"
"s_add_u32 m0, s53, 131072\n"
"v_mov_b32 v43, 0x3f803f80\n"
"v_mfma_f32_16x16x32_bf16 v[144:147], a[132:135], v[100:103], v[144:147]\n"
"buffer_load_dwordx4 v204, s[28:31], s52 offen offset:0 lds\n"
"v_mfma_f32_16x16x32_bf16 v[144:147], a[136:139], v[104:107], v[144:147]\n"
"s_add_u32 s52, s52, 8192\n"
"v_mfma_f32_16x16x32_bf16 v[144:147], a[140:143], v[108:111], v[144:147]\n"
"buffer_load_dwordx4 v204, s[28:31], s52 offen offset:1024 lds\n"
"v_mfma_f32_16x16x32_bf16 v[148:151], a[144:147], v[96:99], 0\n"
"s_add_u32 s52, s52, 8192\n"
"v_mfma_f32_16x16x32_bf16 v[148:151], a[148:151], v[100:103], v[148:151]\n"
"buffer_load_dwordx4 v204, s[28:31], s52 offen offset:2048 lds\n"
"v_mfma_f32_16x16x32_bf16 v[148:151], a[152:155], v[104:107], v[148:151]\n"
"s_add_u32 s52, s52, 8192\n"
"v_mfma_f32_16x16x32_bf16 v[148:151], a[156:159], v[108:111], v[148:151]\n"
"buffer_load_dwordx4 v204, s[28:31], s52 offen offset:3072 lds\n"
"v_mfma_f32_16x16x32_bf16 v[152:155], a[128:131], v[112:115], 0\n"
"s_add_u32 s52, s52, 12288\n"
"s_add_u32 m0, s53, 135168\n"
"v_mfma_f32_16x16x32_bf16 v[152:155], a[132:135], v[116:119], v[152:155]\n"
"buffer_load_dwordx4 v204, s[28:31], s52 offen offset:0 lds\n"
"v_mfma_f32_16x16x32_bf16 v[152:155], a[136:139], v[120:123], v[152:155]\n"
"s_add_u32 s52, s52, 8192\n"
"v_mfma_f32_16x16x32_bf16 v[152:155], a[140:143], v[124:127], v[152:155]\n"
"buffer_load_dwordx4 v204, s[28:31], s52 offen offset:1024 lds\n"
"v_mfma_f32_16x16x32_bf16 v[156:159], a[144:147], v[112:115], 0\n"
"s_add_u32 s52, s52, 8192\n"
"v_mfma_f32_16x16x32_bf16 v[156:159], a[148:151], v[116:119], v[156:159]\n"
"buffer_load_dwordx4 v204, s[28:31], s52 offen offset:2048 lds\n"
"v_mfma_f32_16x16x32_bf16 v[156:159], a[152:155], v[120:123], v[156:159]\n"
"s_add_u32 s52, s52, 8192\n"
"v_mfma_f32_16x16x32_bf16 v[156:159], a[156:159], v[124:127], v[156:159]\n"
"buffer_load_dwordx4 v204, s[28:31], s52 offen offset:3072 lds\n"
"s_sub_u32 s52, s52, 61312\n"
"v_max3_f32 v160, v128, v129, v130\n"
"v_max3_f32 v160, v160, v131, v132\n"
"v_max3_f32 v160, v160, v133, v134\n"
"v_max_f32 v160, v160, v135\n"
"v_max3_f32 v161, v136, v137, v138\n"
"v_max3_f32 v161, v161, v139, v140\n"
"v_max3_f32 v161, v161, v141, v142\n"
"v_max_f32 v161, v161, v143\n"
"v_max3_f32 v162, v144, v145, v146\n"
"v_max3_f32 v162, v162, v147, v148\n"
"v_max3_f32 v162, v162, v149, v150\n"
"v_max_f32 v162, v162, v151\n"
"v_max3_f32 v163, v152, v153, v154\n"
"v_max3_f32 v163, v163, v155, v156\n"
"v_max3_f32 v163, v163, v157, v158\n"
"v_max_f32 v163, v163, v159\n"
"v_mov_b32 v164, v160\n"
"v_mov_b32 v165, v161\n"
"v_mov_b32 v166, v162\n"
"v_mov_b32 v167, v163\n"
"s_nop 0\n"
"v_permlane32_swap_b32 v160, v164\n"
"v_permlane32_swap_b32 v161, v165\n"
"v_permlane32_swap_b32 v162, v166\n"
"v_permlane32_swap_b32 v163, v167\n"
"v_max_f32 v160, v160, v164\n"
"v_max_f32 v161, v161, v165\n"
"v_max_f32 v162, v162, v166\n"
"v_max_f32 v163, v163, v167\n"
"v_mov_b32 v164, v160\n"
"v_mov_b32 v165, v161\n"
"v_mov_b32 v166, v162\n"
"v_mov_b32 v167, v163\n"
"s_nop 0\n"
"v_permlane16_swap_b32 v160, v164\n"
"v_permlane16_swap_b32 v161, v165\n"
"v_permlane16_swap_b32 v162, v166\n"
"v_permlane16_swap_b32 v163, v167\n"
"v_max_f32 v160, v160, v164\n"
"v_max_f32 v161, v161, v165\n"
"v_max_f32 v162, v162, v166\n"
"v_max_f32 v163, v163, v167\n"
"v_sub_f32 v24, 0xc1f00000, v160\n"
"v_sub_f32 v25, 0xc1f00000, v160\n"
"v_sub_f32 v26, 0xc1f00000, v160\n"
"v_sub_f32 v27, 0xc1f00000, v160\n"
"v_sub_f32 v28, 0xc1f00000, v161\n"
"v_sub_f32 v29, 0xc1f00000, v161\n"
"v_sub_f32 v30, 0xc1f00000, v161\n"
"v_sub_f32 v31, 0xc1f00000, v161\n"
"v_sub_f32 v32, 0xc1f00000, v162\n"
"v_sub_f32 v33, 0xc1f00000, v162\n"
"v_sub_f32 v34, 0xc1f00000, v162\n"
"v_sub_f32 v35, 0xc1f00000, v162\n"
"v_sub_f32 v36, 0xc1f00000, v163\n"
"v_sub_f32 v37, 0xc1f00000, v163\n"
"v_sub_f32 v38, 0xc1f00000, v163\n"
"v_sub_f32 v39, 0xc1f00000, v163\n"
"s_nop 1\n"
"s_waitcnt lgkmcnt(0)\n"
"AT_main:\n"
"v_mfma_f32_16x16x32_bf16 v[0:3], a[128:131], v[64:67], v[24:27]\n"
"ds_read_b128 a[160:163], v240 offset:8192\n"
"v_mfma_f32_16x16x32_bf16 v[0:3], a[132:135], v[68:71], v[0:3]\n"
"s_nop 0\n"
"v_mfma_f32_16x16x32_bf16 v[0:3], a[136:139], v[72:75], v[0:3]\n"
"ds_read_b128 a[164:167], v240 offset:8448\n"
"v_mfma_f32_16x16x32_bf16 v[0:3], a[140:143], v[76:79], v[0:3]\n"
"s_nop 0\n"
"v_mfma_f32_16x16x32_bf16 v[4:7], a[144:147], v[64:67], v[24:27]\n"
"ds_read_b128 a[168:171], v240 offset:8704\n"
"v_mfma_f32_16x16x32_bf16 v[4:7], a[148:151], v[68:71], v[4:7]\n"
"s_nop 0\n"
"v_mfma_f32_16x16x32_bf16 v[4:7], a[152:155], v[72:75], v[4:7]\n"
"ds_read_b128 a[172:175], v240 offset:8960\n"
"v_mfma_f32_16x16x32_bf16 v[4:7], a[156:159], v[76:79], v[4:7]\n"
"s_nop 0\n"
"s_nop 1\n"
"v_exp_f32 v0, v0\n"
"s_nop 1\n"
"ds_read_b128 a[176:179], v240 offset:12288\n"
"s_nop 1\n"
"v_exp_f32 v1, v1\n"
"s_nop 1\n"
"ds_read_b128 a[180:183], v240 offset:12544\n"
"v_cvt_pk_bf16_f32 v16, v0, v1\n"
"s_nop 1\n"
"v_exp_f32 v2, v2\n"
"s_nop 1\n"
"ds_read_b128 a[184:187], v240 offset:12800\n"
"s_nop 1\n"
"v_exp_f32 v3, v3\n"
"s_nop 1\n"
"v_cvt_pk_bf16_f32 v17, v2, v3\n"
"s_nop 1\n"
"v_exp_f32 v4, v4\n"
"v_mfma_f32_16x16x32_bf16 v[8:11], a[128:131], v[80:83], v[28:31]\n"
"ds_read_b128 a[188:191], v240 offset:13056\n"
"v_mfma_f32_16x16x32_bf16 v[8:11], a[132:135], v[84:87], v[8:11]\n"
"v_exp_f32 v5, v5\n"
"v_mfma_f32_16x16x32_bf16 v[8:11], a[136:139], v[88:91], v[8:11]\n"
"ds_read_b64_tr_b16 a[224:225], v242 offset:24576\n"
"v_cvt_pk_bf16_f32 v18, v4, v5\n"
"v_mfma_f32_16x16x32_bf16 v[8:11], a[140:143], v[92:95], v[8:11]\n"
"v_exp_f32 v6, v6\n"
"v_mfma_f32_16x16x32_bf16 v[12:15], a[144:147], v[80:83], v[28:31]\n"
"ds_read_b64_tr_b16 a[226:227], v242 offset:28672\n"
"v_mfma_f32_16x16x32_bf16 v[12:15], a[148:151], v[84:87], v[12:15]\n"
"v_exp_f32 v7, v7\n"
"v_mfma_f32_16x16x32_bf16 v[12:15], a[152:155], v[88:91], v[12:15]\n"
"ds_read_b64_tr_b16 a[228:229], v243 offset:24576\n"
"v_cvt_pk_bf16_f32 v19, v6, v7\n"
"v_mfma_f32_16x16x32_bf16 v[12:15], a[156:159], v[92:95], v[12:15]\n"
"s_nop 0\n"
"v_mfma_f32_16x16x32_bf16 a[0:3], a[192:195], v[16:19], 0\n"
"v_exp_f32 v8, v8\n"
"v_mfma_f32_16x16x32_bf16 a[4:7], a[196:199], v[16:19], 0\n"
"ds_read_b64_tr_b16 a[230:231], v243 offset:28672\n"
"v_mfma_f32_16x16x32_bf16 a[8:11], a[200:203], v[16:19], 0\n"
"v_exp_f32 v9, v9\n"
"v_mfma_f32_16x16x32_bf16 a[12:15], a[204:207], v[16:19], 0\n"
"ds_read_b64_tr_b16 a[232:233], v242 offset:24832\n"
"v_cvt_pk_bf16_f32 v20, v8, v9\n"
"v_mfma_f32_16x16x32_bf16 a[16:19], a[208:211], v[16:19], 0\n"
"v_exp_f32 v10, v10\n"
"v_mfma_f32_16x16x32_bf16 a[20:23], a[212:215], v[16:19], 0\n"
"ds_read_b64_tr_b16 a[234:235], v242 offset:28928\n"
"v_mfma_f32_16x16x32_bf16 a[24:27], a[216:219], v[16:19], 0\n"
"v_exp_f32 v11, v11\n"
"v_mfma_f32_16x16x32_bf16 a[28:31], a[220:223], v[16:19], 0\n"
"v_cvt_pk_bf16_f32 v21, v10, v11\n"
"v_mfma_f32_16x16x32_bf16 v[44:47], v[40:43], v[16:19], 0\n"
"v_exp_f32 v12, v12\n"
"v_mfma_f32_16x16x32_bf16 v[0:3], a[128:131], v[96:99], v[32:35]\n"
"ds_read_b64_tr_b16 a[236:237], v243 offset:24832\n"
"v_mfma_f32_16x16x32_bf16 v[0:3], a[132:135], v[100:103], v[0:3]\n"
"v_exp_f32 v13, v13\n"
"v_mfma_f32_16x16x32_bf16 v[0:3], a[136:139], v[104:107], v[0:3]\n"
"ds_read_b64_tr_b16 a[238:239], v243 offset:28928\n"
"v_cvt_pk_bf16_f32 v22, v12, v13\n"
"v_mfma_f32_16x16x32_bf16 v[0:3], a[140:143], v[108:111], v[0:3]\n"
"v_exp_f32 v14, v14\n"
"v_mfma_f32_16x16x32_bf16 v[4:7], a[144:147], v[96:99], v[32:35]\n"
"ds_read_b64_tr_b16 a[240:241], v242 offset:25088\n"
"v_mfma_f32_16x16x32_bf16 v[4:7], a[148:151], v[100:103], v[4:7]\n"
"v_exp_f32 v15, v15\n"
"v_mfma_f32_16x16x32_bf16 v[4:7], a[152:155], v[104:107], v[4:7]\n"
"ds_read_b64_tr_b16 a[242:243], v242 offset:29184\n"
"v_cvt_pk_bf16_f32 v23, v14, v15\n"
"v_mfma_f32_16x16x32_bf16 v[4:7], a[156:159], v[108:111], v[4:7]\n"
"s_nop 0\n"
"v_mfma_f32_16x16x32_bf16 a[32:35], a[192:195], v[20:23], 0\n"
"v_exp_f32 v0, v0\n"
"v_mfma_f32_16x16x32_bf16 a[36:39], a[196:199], v[20:23], 0\n"
"ds_read_b64_tr_b16 a[244:245], v243 offset:25088\n"
"v_mfma_f32_16x16x32_bf16 a[40:43], a[200:203], v[20:23], 0\n"
"v_exp_f32 v1, v1\n"
"v_mfma_f32_16x16x32_bf16 a[44:47], a[204:207], v[20:23], 0\n"
"ds_read_b64_tr_b16 a[246:247], v243 offset:29184\n"
"v_cvt_pk_bf16_f32 v16, v0, v1\n"
"v_mfma_f32_16x16x32_bf16 a[48:51], a[208:211], v[20:23], 0\n"
"v_exp_f32 v2, v2\n"
"v_mfma_f32_16x16x32_bf16 a[52:55], a[212:215], v[20:23], 0\n"
"ds_read_b64_tr_b16 a[248:249], v242 offset:25344\n"
"v_mfma_f32_16x16x32_bf16 a[56:59], a[216:219], v[20:23], 0\n"
"v_exp_f32 v3, v3\n"
"v_mfma_f32_16x16x32_bf16 a[60:63], a[220:223], v[20:23], 0\n"
"v_cvt_pk_bf16_f32 v17, v2, v3\n"
"v_mfma_f32_16x16x32_bf16 v[48:51], v[40:43], v[20:23], 0\n"
"v_exp_f32 v4, v4\n"
"v_mfma_f32_16x16x32_bf16 v[8:11], a[128:131], v[112:115], v[36:39]\n"
"ds_read_b64_tr_b16 a[250:251], v242 offset:29440\n"
"v_mfma_f32_16x16x32_bf16 v[8:11], a[132:135], v[116:119], v[8:11]\n"
"v_exp_f32 v5, v5\n"
"v_mfma_f32_16x16x32_bf16 v[8:11], a[136:139], v[120:123], v[8:11]\n"
"ds_read_b64_tr_b16 a[252:253], v243 offset:25344\n"
"v_cvt_pk_bf16_f32 v18, v4, v5\n"
"v_mfma_f32_16x16x32_bf16 v[8:11], a[140:143], v[124:127], v[8:11]\n"
"v_exp_f32 v6, v6\n"
"v_mfma_f32_16x16x32_bf16 v[12:15], a[144:147], v[112:115], v[36:39]\n"
"ds_read_b64_tr_b16 a[254:255], v243 offset:29440\n"
"v_mfma_f32_16x16x32_bf16 v[12:15], a[148:151], v[116:119], v[12:15]\n"
"v_exp_f32 v7, v7\n"
"v_mfma_f32_16x16x32_bf16 v[12:15], a[152:155], v[120:123], v[12:15]\n"
"v_cvt_pk_bf16_f32 v19, v6, v7\n"
"v_mfma_f32_16x16x32_bf16 v[12:15], a[156:159], v[124:127], v[12:15]\n"
"s_nop 0\n"
"v_mfma_f32_16x16x32_bf16 a[64:67], a[192:195], v[16:19], 0\n"
"v_exp_f32 v8, v8\n"
"v_mfma_f32_16x16x32_bf16 a[68:71], a[196:199], v[16:19], 0\n"
"s_nop 0\n"
"v_mfma_f32_16x16x32_bf16 a[72:75], a[200:203], v[16:19], 0\n"
"v_exp_f32 v9, v9\n"
"v_mfma_f32_16x16x32_bf16 a[76:79], a[204:207], v[16:19], 0\n"
"v_cvt_pk_bf16_f32 v20, v8, v9\n"
"v_mfma_f32_16x16x32_bf16 a[80:83], a[208:211], v[16:19], 0\n"
"v_exp_f32 v10, v10\n"
"v_mfma_f32_16x16x32_bf16 a[84:87], a[212:215], v[16:19], 0\n"
"s_nop 0\n"
"v_mfma_f32_16x16x32_bf16 a[88:91], a[216:219], v[16:19], 0\n"
"v_exp_f32 v11, v11\n"
"v_mfma_f32_16x16x32_bf16 a[92:95], a[220:223], v[16:19], 0\n"
"s_waitcnt vmcnt(24) lgkmcnt(0)\n"
"s_barrier\n"
"v_cvt_pk_bf16_f32 v21, v10, v11\n"
"v_mfma_f32_16x16x32_bf16 v[52:55], v[40:43], v[16:19], 0\n"
"v_exp_f32 v12, v12\n"
"v_mfma_f32_16x16x32_bf16 v[0:3], a[160:163], v[64:67], v[24:27]\n"
"ds_read_b128 a[128:131], v240 offset:32768\n"
"v_mfma_f32_16x16x32_bf16 v[0:3], a[164:167], v[68:71], v[0:3]\n"
"v_exp_f32 v13, v13\n"
"v_mfma_f32_16x16x32_bf16 v[0:3], a[168:171], v[72:75], v[0:3]\n"
"ds_read_b128 a[132:135], v240 offset:33024\n"
"v_cvt_pk_bf16_f32 v22, v12, v13\n"
"v_mfma_f32_16x16x32_bf16 v[0:3], a[172:175], v[76:79], v[0:3]\n"
"v_exp_f32 v14, v14\n"
"v_mfma_f32_16x16x32_bf16 v[4:7], a[176:179], v[64:67], v[24:27]\n"
"ds_read_b128 a[136:139], v240 offset:33280\n"
"v_mfma_f32_16x16x32_bf16 v[4:7], a[180:183], v[68:71], v[4:7]\n"
"v_exp_f32 v15, v15\n"
"v_mfma_f32_16x16x32_bf16 v[4:7], a[184:187], v[72:75], v[4:7]\n"
"s_mov_b32 m0, s18\n"
"ds_read_b128 a[140:143], v240 offset:33536\n"
"v_cvt_pk_bf16_f32 v23, v14, v15\n"
"v_mfma_f32_16x16x32_bf16 v[4:7], a[188:191], v[76:79], v[4:7]\n"
"buffer_load_dwordx4 v246, s[8:11], s16 offen offset:0 lds\n"
"v_mfma_f32_16x16x32_bf16 a[96:99], a[192:195], v[20:23], 0\n"
"v_exp_f32 v0, v0\n"
"v_mfma_f32_16x16x32_bf16 a[100:103], a[196:199], v[20:23], 0\n"
"ds_read_b128 a[144:147], v240 offset:36864\n"
"v_mfma_f32_16x16x32_bf16 a[104:107], a[200:203], v[20:23], 0\n"
"v_exp_f32 v1, v1\n"
"v_mfma_f32_16x16x32_bf16 a[108:111], a[204:207], v[20:23], 0\n"
"ds_read_b128 a[148:151], v240 offset:37120\n"
"v_cvt_pk_bf16_f32 v16, v0, v1\n"
"v_mfma_f32_16x16x32_bf16 a[112:115], a[208:211], v[20:23], 0\n"
"v_exp_f32 v2, v2\n"
"v_mfma_f32_16x16x32_bf16 a[116:119], a[212:215], v[20:23], 0\n"
"ds_read_b128 a[152:155], v240 offset:37376\n"
"v_mfma_f32_16x16x32_bf16 a[120:123], a[216:219], v[20:23], 0\n"
"v_exp_f32 v3, v3\n"
"v_mfma_f32_16x16x32_bf16 a[124:127], a[220:223], v[20:23], 0\n"
"v_cvt_pk_bf16_f32 v17, v2, v3\n"
"v_mfma_f32_16x16x32_bf16 v[56:59], v[40:43], v[20:23], 0\n"
"v_exp_f32 v4, v4\n"
"v_mfma_f32_16x16x32_bf16 v[8:11], a[160:163], v[80:83], v[28:31]\n"
"ds_read_b128 a[156:159], v240 offset:37632\n"
"v_mfma_f32_16x16x32_bf16 v[8:11], a[164:167], v[84:87], v[8:11]\n"
"v_exp_f32 v5, v5\n"
"v_mfma_f32_16x16x32_bf16 v[8:11], a[168:171], v[88:91], v[8:11]\n"
"ds_read_b64_tr_b16 a[192:193], v242 offset:49152\n"
"v_cvt_pk_bf16_f32 v18, v4, v5\n"
"v_mfma_f32_16x16x32_bf16 v[8:11], a[172:175], v[92:95], v[8:11]\n"
"v_exp_f32 v6, v6\n"
"v_mfma_f32_16x16x32_bf16 v[12:15], a[176:179], v[80:83], v[28:31]\n"
"ds_read_b64_tr_b16 a[194:195], v242 offset:53248\n"
"v_mfma_f32_16x16x32_bf16 v[12:15], a[180:183], v[84:87], v[12:15]\n"
"v_exp_f32 v7, v7\n"
"v_mfma_f32_16x16x32_bf16 v[12:15], a[184:187], v[88:91], v[12:15]\n"
"ds_read_b64_tr_b16 a[196:197], v243 offset:49152\n"
"v_cvt_pk_bf16_f32 v19, v6, v7\n"
"v_mfma_f32_16x16x32_bf16 v[12:15], a[188:191], v[92:95], v[12:15]\n"
"s_waitcnt lgkmcnt(11)\n"
"buffer_load_dwordx4 v246, s[8:11], s16 offen offset:1024 lds\n"
"v_mfma_f32_16x16x32_bf16 a[0:3], a[224:227], v[16:19], a[0:3]\n"
"v_exp_f32 v8, v8\n"
"v_mfma_f32_16x16x32_bf16 a[4:7], a[228:231], v[16:19], a[4:7]\n"
"ds_read_b64_tr_b16 a[198:199], v243 offset:53248\n"
"v_mfma_f32_16x16x32_bf16 a[8:11], a[232:235], v[16:19], a[8:11]\n"
"v_exp_f32 v9, v9\n"
"v_mfma_f32_16x16x32_bf16 a[12:15], a[236:239], v[16:19], a[12:15]\n"
"ds_read_b64_tr_b16 a[200:201], v242 offset:49408\n"
"v_cvt_pk_bf16_f32 v20, v8, v9\n"
"v_mfma_f32_16x16x32_bf16 a[16:19], a[240:243], v[16:19], a[16:19]\n"
"v_exp_f32 v10, v10\n"
"v_mfma_f32_16x16x32_bf16 a[20:23], a[244:247], v[16:19], a[20:23]\n"
"ds_read_b64_tr_b16 a[202:203], v242 offset:53504\n"
"v_mfma_f32_16x16x32_bf16 a[24:27], a[248:251], v[16:19], a[24:27]\n"
"v_exp_f32 v11, v11\n"
"v_mfma_f32_16x16x32_bf16 a[28:31], a[252:255], v[16:19], a[28:31]\n"
"v_cvt_pk_bf16_f32 v21, v10, v11\n"
"v_mfma_f32_16x16x32_bf16 v[44:47], v[40:43], v[16:19], v[44:47]\n"
"v_exp_f32 v12, v12\n"
"v_mfma_f32_16x16x32_bf16 v[0:3], a[160:163], v[96:99], v[32:35]\n"
"ds_read_b64_tr_b16 a[204:205], v243 offset:49408\n"
"v_mfma_f32_16x16x32_bf16 v[0:3], a[164:167], v[100:103], v[0:3]\n"
"v_exp_f32 v13, v13\n"
"v_mfma_f32_16x16x32_bf16 v[0:3], a[168:171], v[104:107], v[0:3]\n"
"ds_read_b64_tr_b16 a[206:207], v243 offset:53504\n"
"v_cvt_pk_bf16_f32 v22, v12, v13\n"
"v_mfma_f32_16x16x32_bf16 v[0:3], a[172:175], v[108:111], v[0:3]\n"
"v_exp_f32 v14, v14\n"
"v_mfma_f32_16x16x32_bf16 v[4:7], a[176:179], v[96:99], v[32:35]\n"
"ds_read_b64_tr_b16 a[208:209], v242 offset:49664\n"
"v_mfma_f32_16x16x32_bf16 v[4:7], a[180:183], v[100:103], v[4:7]\n"
"v_exp_f32 v15, v15\n"
"v_mfma_f32_16x16x32_bf16 v[4:7], a[184:187], v[104:107], v[4:7]\n"
"ds_read_b64_tr_b16 a[210:211], v242 offset:53760\n"
"v_cvt_pk_bf16_f32 v23, v14, v15\n"
"v_mfma_f32_16x16x32_bf16 v[4:7], a[188:191], v[108:111], v[4:7]\n"
"buffer_load_dwordx4 v247, s[8:11], s16 offen offset:2048 lds\n"
"v_mfma_f32_16x16x32_bf16 a[32:35], a[224:227], v[20:23], a[32:35]\n"
"v_exp_f32 v0, v0\n"
"v_mfma_f32_16x16x32_bf16 a[36:39], a[228:231], v[20:23], a[36:39]\n"
"ds_read_b64_tr_b16 a[212:213], v243 offset:49664\n"
"v_mfma_f32_16x16x32_bf16 a[40:43], a[232:235], v[20:23], a[40:43]\n"
"v_exp_f32 v1, v1\n"
"v_mfma_f32_16x16x32_bf16 a[44:47], a[236:239], v[20:23], a[44:47]\n"
"ds_read_b64_tr_b16 a[214:215], v243 offset:53760\n"
"v_cvt_pk_bf16_f32 v16, v0, v1\n"
"v_mfma_f32_16x16x32_bf16 a[48:51], a[240:243], v[20:23], a[48:51]\n"
"v_exp_f32 v2, v2\n"
"v_mfma_f32_16x16x32_bf16 a[52:55], a[244:247], v[20:23], a[52:55]\n"
"ds_read_b64_tr_b16 a[216:217], v242 offset:49920\n"
"v_mfma_f32_16x16x32_bf16 a[56:59], a[248:251], v[20:23], a[56:59]\n"
"v_exp_f32 v3, v3\n"
"v_mfma_f32_16x16x32_bf16 a[60:63], a[252:255], v[20:23], a[60:63]\n"
"v_cvt_pk_bf16_f32 v17, v2, v3\n"
"v_mfma_f32_16x16x32_bf16 v[48:51], v[40:43], v[20:23], v[48:51]\n"
"v_exp_f32 v4, v4\n"
"v_mfma_f32_16x16x32_bf16 v[8:11], a[160:163], v[112:115], v[36:39]\n"
"ds_read_b64_tr_b16 a[218:219], v242 offset:54016\n"
"v_mfma_f32_16x16x32_bf16 v[8:11], a[164:167], v[116:119], v[8:11]\n"
"v_exp_f32 v5, v5\n"
"v_mfma_f32_16x16x32_bf16 v[8:11], a[168:171], v[120:123], v[8:11]\n"
"ds_read_b64_tr_b16 a[220:221], v243 offset:49920\n"
"v_cvt_pk_bf16_f32 v18, v4, v5\n"
"v_mfma_f32_16x16x32_bf16 v[8:11], a[172:175], v[124:127], v[8:11]\n"
"v_exp_f32 v6, v6\n"
"v_mfma_f32_16x16x32_bf16 v[12:15], a[176:179], v[112:115], v[36:39]\n"
"ds_read_b64_tr_b16 a[222:223], v243 offset:54016\n"
"v_mfma_f32_16x16x32_bf16 v[12:15], a[180:183], v[116:119], v[12:15]\n"
"v_exp_f32 v7, v7\n"
"v_mfma_f32_16x16x32_bf16 v[12:15], a[184:187], v[120:123], v[12:15]\n"
"v_cvt_pk_bf16_f32 v19, v6, v7\n"
"v_mfma_f32_16x16x32_bf16 v[12:15], a[188:191], v[124:127], v[12:15]\n"
"buffer_load_dwordx4 v247, s[8:11], s16 offen offset:3072 lds\n"
"v_mfma_f32_16x16x32_bf16 a[64:67], a[224:227], v[16:19], a[64:67]\n"
"v_exp_f32 v8, v8\n"
"v_mfma_f32_16x16x32_bf16 a[68:71], a[228:231], v[16:19], a[68:71]\n"
"s_nop 0\n"
"v_mfma_f32_16x16x32_bf16 a[72:75], a[232:235], v[16:19], a[72:75]\n"
"v_exp_f32 v9, v9\n"
"v_mfma_f32_16x16x32_bf16 a[76:79], a[236:239], v[16:19], a[76:79]\n"
"v_cvt_pk_bf16_f32 v20, v8, v9\n"
"v_mfma_f32_16x16x32_bf16 a[80:83], a[240:243], v[16:19], a[80:83]\n"
"v_exp_f32 v10, v10\n"
"v_mfma_f32_16x16x32_bf16 a[84:87], a[244:247], v[16:19], a[84:87]\n"
"s_nop 0\n"
"v_mfma_f32_16x16x32_bf16 a[88:91], a[248:251], v[16:19], a[88:91]\n"
"v_exp_f32 v11, v11\n"
"v_mfma_f32_16x16x32_bf16 a[92:95], a[252:255], v[16:19], a[92:95]\n"
"s_waitcnt lgkmcnt(8)\n"
"v_cvt_pk_bf16_f32 v21, v10, v11\n"
"v_mfma_f32_16x16x32_bf16 v[52:55], v[40:43], v[16:19], v[52:55]\n"
"v_exp_f32 v12, v12\n"
"v_mfma_f32_16x16x32_bf16 v[0:3], a[128:131], v[64:67], v[24:27]\n"
"ds_read_b128 a[160:163], v240 offset:40960\n"
"v_mfma_f32_16x16x32_bf16 v[0:3], a[132:135], v[68:71], v[0:3]\n"
"v_exp_f32 v13, v13\n"
"v_mfma_f32_16x16x32_bf16 v[0:3], a[136:139], v[72:75], v[0:3]\n"
"ds_read_b128 a[164:167], v240 offset:41216\n"
"v_cvt_pk_bf16_f32 v22, v12, v13\n"
"v_mfma_f32_16x16x32_bf16 v[0:3], a[140:143], v[76:79], v[0:3]\n"
"v_exp_f32 v14, v14\n"
"v_mfma_f32_16x16x32_bf16 v[4:7], a[144:147], v[64:67], v[24:27]\n"
"ds_read_b128 a[168:171], v240 offset:41472\n"
"v_mfma_f32_16x16x32_bf16 v[4:7], a[148:151], v[68:71], v[4:7]\n"
"v_exp_f32 v15, v15\n"
"v_mfma_f32_16x16x32_bf16 v[4:7], a[152:155], v[72:75], v[4:7]\n"
"s_add_u32 m0, s18, 16384\n"
"ds_read_b128 a[172:175], v240 offset:41728\n"
"v_cvt_pk_bf16_f32 v23, v14, v15\n"
"v_mfma_f32_16x16x32_bf16 v[4:7], a[156:159], v[76:79], v[4:7]\n"
"buffer_load_dwordx4 v246, s[12:15], s16 offen offset:0 lds\n"
"v_mfma_f32_16x16x32_bf16 a[96:99], a[224:227], v[20:23], a[96:99]\n"
"v_exp_f32 v0, v0\n"
"v_mfma_f32_16x16x32_bf16 a[100:103], a[228:231], v[20:23], a[100:103]\n"
"ds_read_b128 a[176:179], v240 offset:45056\n"
"v_mfma_f32_16x16x32_bf16 a[104:107], a[232:235], v[20:23], a[104:107]\n"
"v_exp_f32 v1, v1\n"
"v_mfma_f32_16x16x32_bf16 a[108:111], a[236:239], v[20:23], a[108:111]\n"
"ds_read_b128 a[180:183], v240 offset:45312\n"
"v_cvt_pk_bf16_f32 v16, v0, v1\n"
"v_mfma_f32_16x16x32_bf16 a[112:115], a[240:243], v[20:23], a[112:115]\n"
"v_exp_f32 v2, v2\n"
"v_mfma_f32_16x16x32_bf16 a[116:119], a[244:247], v[20:23], a[116:119]\n"
"ds_read_b128 a[184:187], v240 offset:45568\n"
"v_mfma_f32_16x16x32_bf16 a[120:123], a[248:251], v[20:23], a[120:123]\n"
"v_exp_f32 v3, v3\n"
"v_mfma_f32_16x16x32_bf16 a[124:127], a[252:255], v[20:23], a[124:127]\n"
"v_cvt_pk_bf16_f32 v17, v2, v3\n"
"v_mfma_f32_16x16x32_bf16 v[56:59], v[40:43], v[20:23], v[56:59]\n"
"v_exp_f32 v4, v4\n"
"v_mfma_f32_16x16x32_bf16 v[8:11], a[128:131], v[80:83], v[28:31]\n"
"ds_read_b128 a[188:191], v240 offset:45824\n"
"v_mfma_f32_16x16x32_bf16 v[8:11], a[132:135], v[84:87], v[8:11]\n"
"v_exp_f32 v5, v5\n"
"v_mfma_f32_16x16x32_bf16 v[8:11], a[136:139], v[88:91], v[8:11]\n"
"ds_read_b64_tr_b16 a[224:225], v242 offset:57344\n"
"v_cvt_pk_bf16_f32 v18, v4, v5\n"
"v_mfma_f32_16x16x32_bf16 v[8:11], a[140:143], v[92:95], v[8:11]\n"
"v_exp_f32 v6, v6\n"
"v_mfma_f32_16x16x32_bf16 v[12:15], a[144:147], v[80:83], v[28:31]\n"
"ds_read_b64_tr_b16 a[226:227], v242 offset:61440\n"
"v_mfma_f32_16x16x32_bf16 v[12:15], a[148:151], v[84:87], v[12:15]\n"
"v_exp_f32 v7, v7\n"
"v_mfma_f32_16x16x32_bf16 v[12:15], a[152:155], v[88:91], v[12:15]\n"
"ds_read_b64_tr_b16 a[228:229], v243 offset:57344\n"
"v_cvt_pk_bf16_f32 v19, v6, v7\n"
"v_mfma_f32_16x16x32_bf16 v[12:15], a[156:159], v[92:95], v[12:15]\n"
"s_waitcnt lgkmcnt(11)\n"
"buffer_load_dwordx4 v247, s[12:15], s16 offen offset:1024 lds\n"
"v_mfma_f32_16x16x32_bf16 a[0:3], a[192:195], v[16:19], a[0:3]\n"
"v_exp_f32 v8, v8\n"
"v_mfma_f32_16x16x32_bf16 a[4:7], a[196:199], v[16:19], a[4:7]\n"
"ds_read_b64_tr_b16 a[230:231], v243 offset:61440\n"
"v_mfma_f32_16x16x32_bf16 a[8:11], a[200:203], v[16:19], a[8:11]\n"
"v_exp_f32 v9, v9\n"
"v_mfma_f32_16x16x32_bf16 a[12:15], a[204:207], v[16:19], a[12:15]\n"
"ds_read_b64_tr_b16 a[232:233], v242 offset:57600\n"
"v_cvt_pk_bf16_f32 v20, v8, v9\n"
"v_mfma_f32_16x16x32_bf16 a[16:19], a[208:211], v[16:19], a[16:19]\n"
"v_exp_f32 v10, v10\n"
"v_mfma_f32_16x16x32_bf16 a[20:23], a[212:215], v[16:19], a[20:23]\n"
"ds_read_b64_tr_b16 a[234:235], v242 offset:61696\n"
"v_mfma_f32_16x16x32_bf16 a[24:27], a[216:219], v[16:19], a[24:27]\n"
"v_exp_f32 v11, v11\n"
"v_mfma_f32_16x16x32_bf16 a[28:31], a[220:223], v[16:19], a[28:31]\n"
"v_cvt_pk_bf16_f32 v21, v10, v11\n"
"v_mfma_f32_16x16x32_bf16 v[44:47], v[40:43], v[16:19], v[44:47]\n"
"v_exp_f32 v12, v12\n"
"v_mfma_f32_16x16x32_bf16 v[0:3], a[128:131], v[96:99], v[32:35]\n"
"ds_read_b64_tr_b16 a[236:237], v243 offset:57600\n"
"v_mfma_f32_16x16x32_bf16 v[0:3], a[132:135], v[100:103], v[0:3]\n"
"v_exp_f32 v13, v13\n"
"v_mfma_f32_16x16x32_bf16 v[0:3], a[136:139], v[104:107], v[0:3]\n"
"ds_read_b64_tr_b16 a[238:239], v243 offset:61696\n"
"v_cvt_pk_bf16_f32 v22, v12, v13\n"
"v_mfma_f32_16x16x32_bf16 v[0:3], a[140:143], v[108:111], v[0:3]\n"
"v_exp_f32 v14, v14\n"
"v_mfma_f32_16x16x32_bf16 v[4:7], a[144:147], v[96:99], v[32:35]\n"
"ds_read_b64_tr_b16 a[240:241], v242 offset:57856\n"
"v_mfma_f32_16x16x32_bf16 v[4:7], a[148:151], v[100:103], v[4:7]\n"
"v_exp_f32 v15, v15\n"
"v_mfma_f32_16x16x32_bf16 v[4:7], a[152:155], v[104:107], v[4:7]\n"
"ds_read_b64_tr_b16 a[242:243], v242 offset:61952\n"
"v_cvt_pk_bf16_f32 v23, v14, v15\n"
"v_mfma_f32_16x16x32_bf16 v[4:7], a[156:159], v[108:111], v[4:7]\n"
"buffer_load_dwordx4 v246, s[12:15], s16 offen offset:2048 lds\n"
"v_mfma_f32_16x16x32_bf16 a[32:35], a[192:195], v[20:23], a[32:35]\n"
"v_exp_f32 v0, v0\n"
"v_mfma_f32_16x16x32_bf16 a[36:39], a[196:199], v[20:23], a[36:39]\n"
"ds_read_b64_tr_b16 a[244:245], v243 offset:57856\n"
"v_mfma_f32_16x16x32_bf16 a[40:43], a[200:203], v[20:23], a[40:43]\n"
"v_exp_f32 v1, v1\n"
"v_mfma_f32_16x16x32_bf16 a[44:47], a[204:207], v[20:23], a[44:47]\n"
"ds_read_b64_tr_b16 a[246:247], v243 offset:61952\n"
"v_cvt_pk_bf16_f32 v16, v0, v1\n"
"v_mfma_f32_16x16x32_bf16 a[48:51], a[208:211], v[20:23], a[48:51]\n"
"v_exp_f32 v2, v2\n"
"v_mfma_f32_16x16x32_bf16 a[52:55], a[212:215], v[20:23], a[52:55]\n"
"ds_read_b64_tr_b16 a[248:249], v242 offset:58112\n"
"v_mfma_f32_16x16x32_bf16 a[56:59], a[216:219], v[20:23], a[56:59]\n"
"v_exp_f32 v3, v3\n"
"v_mfma_f32_16x16x32_bf16 a[60:63], a[220:223], v[20:23], a[60:63]\n"
"v_cvt_pk_bf16_f32 v17, v2, v3\n"
"v_mfma_f32_16x16x32_bf16 v[48:51], v[40:43], v[20:23], v[48:51]\n"
"v_exp_f32 v4, v4\n"
"v_mfma_f32_16x16x32_bf16 v[8:11], a[128:131], v[112:115], v[36:39]\n"
"ds_read_b64_tr_b16 a[250:251], v242 offset:62208\n"
"v_mfma_f32_16x16x32_bf16 v[8:11], a[132:135], v[116:119], v[8:11]\n"
"v_exp_f32 v5, v5\n"
"v_mfma_f32_16x16x32_bf16 v[8:11], a[136:139], v[120:123], v[8:11]\n"
"ds_read_b64_tr_b16 a[252:253], v243 offset:58112\n"
"v_cvt_pk_bf16_f32 v18, v4, v5\n"
"v_mfma_f32_16x16x32_bf16 v[8:11], a[140:143], v[124:127], v[8:11]\n"
"v_exp_f32 v6, v6\n"
"v_mfma_f32_16x16x32_bf16 v[12:15], a[144:147], v[112:115], v[36:39]\n"
"ds_read_b64_tr_b16 a[254:255], v243 offset:62208\n"
"v_mfma_f32_16x16x32_bf16 v[12:15], a[148:151], v[116:119], v[12:15]\n"
"v_exp_f32 v7, v7\n"
"v_mfma_f32_16x16x32_bf16 v[12:15], a[152:155], v[120:123], v[12:15]\n"
"v_cvt_pk_bf16_f32 v19, v6, v7\n"
"v_mfma_f32_16x16x32_bf16 v[12:15], a[156:159], v[124:127], v[12:15]\n"
"buffer_load_dwordx4 v247, s[12:15], s16 offen offset:3072 lds\n"
"v_mfma_f32_16x16x32_bf16 a[64:67], a[192:195], v[16:19], a[64:67]\n"
"s_add_u32 s16, s16, 16384\n"
"v_exp_f32 v8, v8\n"
"v_mfma_f32_16x16x32_bf16 a[68:71], a[196:199], v[16:19], a[68:71]\n"
"s_nop 0\n"
"v_mfma_f32_16x16x32_bf16 a[72:75], a[200:203], v[16:19], a[72:75]\n"
"v_exp_f32 v9, v9\n"
"v_mfma_f32_16x16x32_bf16 a[76:79], a[204:207], v[16:19], a[76:79]\n"
"v_cvt_pk_bf16_f32 v20, v8, v9\n"
"v_mfma_f32_16x16x32_bf16 a[80:83], a[208:211], v[16:19], a[80:83]\n"
"v_exp_f32 v10, v10\n"
"v_mfma_f32_16x16x32_bf16 a[84:87], a[212:215], v[16:19], a[84:87]\n"
"s_nop 0\n"
"v_mfma_f32_16x16x32_bf16 a[88:91], a[216:219], v[16:19], a[88:91]\n"
"v_exp_f32 v11, v11\n"
"v_mfma_f32_16x16x32_bf16 a[92:95], a[220:223], v[16:19], a[92:95]\n"
"s_waitcnt vmcnt(24) lgkmcnt(0)\n"
"s_barrier\n"
"v_cvt_pk_bf16_f32 v21, v10, v11\n"
"v_mfma_f32_16x16x32_bf16 v[52:55], v[40:43], v[16:19], v[52:55]\n"
"v_exp_f32 v12, v12\n"
"s_mov_b32 s19, 6\n"
".p2align 6\n"
"AT_loop:\n"
"v_mfma_f32_16x16x32_bf16 v[0:3], a[160:163], v[64:67], v[24:27]\n"
"ds_read_b128 a[128:131], v241 offset:0\n"
"v_mfma_f32_16x16x32_bf16 v[0:3], a[164:167], v[68:71], v[0:3]\n"
"v_exp_f32 v13, v13\n"
"v_mfma_f32_16x16x32_bf16 v[0:3], a[168:171], v[72:75], v[0:3]\n"
"ds_read_b128 a[132:135], v241 offset:256\n"
"v_cvt_pk_bf16_f32 v22, v12, v13\n"
"v_mfma_f32_16x16x32_bf16 v[0:3], a[172:175], v[76:79], v[0:3]\n"
"v_exp_f32 v14, v14\n"
"v_mfma_f32_16x16x32_bf16 v[4:7], a[176:179], v[64:67], v[24:27]\n"
"ds_read_b128 a[136:139], v241 offset:512\n"
"v_mfma_f32_16x16x32_bf16 v[4:7], a[180:183], v[68:71], v[4:7]\n"
"v_exp_f32 v15, v15\n"
"v_mfma_f32_16x16x32_bf16 v[4:7], a[184:187], v[72:75], v[4:7]\n"
"s_add_u32 m0, s18, 32768\n"
"ds_read_b128 a[140:143], v241 offset:768\n"
"v_cvt_pk_bf16_f32 v23, v14, v15\n"
"v_mfma_f32_16x16x32_bf16 v[4:7], a[188:191], v[76:79], v[4:7]\n"
"buffer_load_dwordx4 v246, s[8:11], s16 offen offset:0 lds\n"
"v_mfma_f32_16x16x32_bf16 a[96:99], a[192:195], v[20:23], a[96:99]\n"
"v_exp_f32 v0, v0\n"
"v_mfma_f32_16x16x32_bf16 a[100:103], a[196:199], v[20:23], a[100:103]\n"
"ds_read_b128 a[144:147], v241 offset:4096\n"
"v_mfma_f32_16x16x32_bf16 a[104:107], a[200:203], v[20:23], a[104:107]\n"
"v_exp_f32 v1, v1\n"
"v_mfma_f32_16x16x32_bf16 a[108:111], a[204:207], v[20:23], a[108:111]\n"
"ds_read_b128 a[148:151], v241 offset:4352\n"
"v_cvt_pk_bf16_f32 v16, v0, v1\n"
"v_mfma_f32_16x16x32_bf16 a[112:115], a[208:211], v[20:23], a[112:115]\n"
"v_exp_f32 v2, v2\n"
"v_mfma_f32_16x16x32_bf16 a[116:119], a[212:215], v[20:23], a[116:119]\n"
"ds_read_b128 a[152:155], v241 offset:4608\n"
"v_mfma_f32_16x16x32_bf16 a[120:123], a[216:219], v[20:23], a[120:123]\n"
"v_exp_f32 v3, v3\n"
"v_mfma_f32_16x16x32_bf16 a[124:127], a[220:223], v[20:23], a[124:127]\n"
"v_cvt_pk_bf16_f32 v17, v2, v3\n"
"v_mfma_f32_16x16x32_bf16 v[56:59], v[40:43], v[20:23], v[56:59]\n"
"v_exp_f32 v4, v4\n"
"v_mfma_f32_16x16x32_bf16 v[8:11], a[160:163], v[80:83], v[28:31]\n"
"ds_read_b128 a[156:159], v241 offset:4864\n"
"v_mfma_f32_16x16x32_bf16 v[8:11], a[164:167], v[84:87], v[8:11]\n"
"v_exp_f32 v5, v5\n"
"v_mfma_f32_16x16x32_bf16 v[8:11], a[168:171], v[88:91], v[8:11]\n"
"ds_read_b64_tr_b16 a[192:193], v244 offset:16384\n"
"v_cvt_pk_bf16_f32 v18, v4, v5\n"
"v_mfma_f32_16x16x32_bf16 v[8:11], a[172:175], v[92:95], v[8:11]\n"
"v_exp_f32 v6, v6\n"
"v_mfma_f32_16x16x32_bf16 v[12:15], a[176:179], v[80:83], v[28:31]\n"
"ds_read_b64_tr_b16 a[194:195], v244 offset:20480\n"
"v_mfma_f32_16x16x32_bf16 v[12:15], a[180:183], v[84:87], v[12:15]\n"
"v_exp_f32 v7, v7\n"
"v_mfma_f32_16x16x32_bf16 v[12:15], a[184:187], v[88:91], v[12:15]\n"
"ds_read_b64_tr_b16 a[196:197], v245 offset:16384\n"
"v_cvt_pk_bf16_f32 v19, v6, v7\n"
"v_mfma_f32_16x16x32_bf16 v[12:15], a[188:191], v[92:95], v[12:15]\n"
"s_waitcnt lgkmcnt(11)\n"
"buffer_load_dwordx4 v246, s[8:11], s16 offen offset:1024 lds\n"
"v_mfma_f32_16x16x32_bf16 a[0:3], a[224:227], v[16:19], a[0:3]\n"
"v_exp_f32 v8, v8\n"
"v_mfma_f32_16x16x32_bf16 a[4:7], a[228:231], v[16:19], a[4:7]\n"
"ds_read_b64_tr_b16 a[198:199], v245 offset:20480\n"
"v_mfma_f32_16x16x32_bf16 a[8:11], a[232:235], v[16:19], a[8:11]\n"
"v_exp_f32 v9, v9\n"
"v_mfma_f32_16x16x32_bf16 a[12:15], a[236:239], v[16:19], a[12:15]\n"
"ds_read_b64_tr_b16 a[200:201], v244 offset:16640\n"
"v_cvt_pk_bf16_f32 v20, v8, v9\n"
"v_mfma_f32_16x16x32_bf16 a[16:19], a[240:243], v[16:19], a[16:19]\n"
"v_exp_f32 v10, v10\n"
"v_mfma_f32_16x16x32_bf16 a[20:23], a[244:247], v[16:19], a[20:23]\n"
"ds_read_b64_tr_b16 a[202:203], v244 offset:20736\n"
"v_mfma_f32_16x16x32_bf16 a[24:27], a[248:251], v[16:19], a[24:27]\n"
"v_exp_f32 v11, v11\n"
"v_mfma_f32_16x16x32_bf16 a[28:31], a[252:255], v[16:19], a[28:31]\n"
"v_cvt_pk_bf16_f32 v21, v10, v11\n"
"v_mfma_f32_16x16x32_bf16 v[44:47], v[40:43], v[16:19], v[44:47]\n"
"v_exp_f32 v12, v12\n"
"v_mfma_f32_16x16x32_bf16 v[0:3], a[160:163], v[96:99], v[32:35]\n"
"ds_read_b64_tr_b16 a[204:205], v245 offset:16640\n"
"v_mfma_f32_16x16x32_bf16 v[0:3], a[164:167], v[100:103], v[0:3]\n"
"v_exp_f32 v13, v13\n"
"v_mfma_f32_16x16x32_bf16 v[0:3], a[168:171], v[104:107], v[0:3]\n"
"ds_read_b64_tr_b16 a[206:207], v245 offset:20736\n"
"v_cvt_pk_bf16_f32 v22, v12, v13\n"
"v_mfma_f32_16x16x32_bf16 v[0:3], a[172:175], v[108:111], v[0:3]\n"
"v_exp_f32 v14, v14\n"
"v_mfma_f32_16x16x32_bf16 v[4:7], a[176:179], v[96:99], v[32:35]\n"
"ds_read_b64_tr_b16 a[208:209], v244 offset:16896\n"
"v_mfma_f32_16x16x32_bf16 v[4:7], a[180:183], v[100:103], v[4:7]\n"
"v_exp_f32 v15, v15\n"
"v_mfma_f32_16x16x32_bf16 v[4:7], a[184:187], v[104:107], v[4:7]\n"
"ds_read_b64_tr_b16 a[210:211], v244 offset:20992\n"
"v_cvt_pk_bf16_f32 v23, v14, v15\n"
"v_mfma_f32_16x16x32_bf16 v[4:7], a[188:191], v[108:111], v[4:7]\n"
"buffer_load_dwordx4 v247, s[8:11], s16 offen offset:2048 lds\n"
"v_mfma_f32_16x16x32_bf16 a[32:35], a[224:227], v[20:23], a[32:35]\n"
"v_exp_f32 v0, v0\n"
"v_mfma_f32_16x16x32_bf16 a[36:39], a[228:231], v[20:23], a[36:39]\n"
"ds_read_b64_tr_b16 a[212:213], v245 offset:16896\n"
"v_mfma_f32_16x16x32_bf16 a[40:43], a[232:235], v[20:23], a[40:43]\n"
"v_exp_f32 v1, v1\n"
"v_mfma_f32_16x16x32_bf16 a[44:47], a[236:239], v[20:23], a[44:47]\n"
"ds_read_b64_tr_b16 a[214:215], v245 offset:20992\n"
"v_cvt_pk_bf16_f32 v16, v0, v1\n"
"v_mfma_f32_16x16x32_bf16 a[48:51], a[240:243], v[20:23], a[48:51]\n"
"v_exp_f32 v2, v2\n"
"v_mfma_f32_16x16x32_bf16 a[52:55], a[244:247], v[20:23], a[52:55]\n"
"ds_read_b64_tr_b16 a[216:217], v244 offset:17152\n"
"v_mfma_f32_16x16x32_bf16 a[56:59], a[248:251], v[20:23], a[56:59]\n"
"v_exp_f32 v3, v3\n"
"v_mfma_f32_16x16x32_bf16 a[60:63], a[252:255], v[20:23], a[60:63]\n"
"v_cvt_pk_bf16_f32 v17, v2, v3\n"
"v_mfma_f32_16x16x32_bf16 v[48:51], v[40:43], v[20:23], v[48:51]\n"
"v_exp_f32 v4, v4\n"
"v_mfma_f32_16x16x32_bf16 v[8:11], a[160:163], v[112:115], v[36:39]\n"
"ds_read_b64_tr_b16 a[218:219], v244 offset:21248\n"
"v_mfma_f32_16x16x32_bf16 v[8:11], a[164:167], v[116:119], v[8:11]\n"
"v_exp_f32 v5, v5\n"
"v_mfma_f32_16x16x32_bf16 v[8:11], a[168:171], v[120:123], v[8:11]\n"
"ds_read_b64_tr_b16 a[220:221], v245 offset:17152\n"
"v_cvt_pk_bf16_f32 v18, v4, v5\n"
"v_mfma_f32_16x16x32_bf16 v[8:11], a[172:175], v[124:127], v[8:11]\n"
"v_exp_f32 v6, v6\n"
"v_mfma_f32_16x16x32_bf16 v[12:15], a[176:179], v[112:115], v[36:39]\n"
"ds_read_b64_tr_b16 a[222:223], v245 offset:21248\n"
"v_mfma_f32_16x16x32_bf16 v[12:15], a[180:183], v[116:119], v[12:15]\n"
"v_exp_f32 v7, v7\n"
"v_mfma_f32_16x16x32_bf16 v[12:15], a[184:187], v[120:123], v[12:15]\n"
"v_cvt_pk_bf16_f32 v19, v6, v7\n"
"v_mfma_f32_16x16x32_bf16 v[12:15], a[188:191], v[124:127], v[12:15]\n"
"buffer_load_dwordx4 v247, s[8:11], s16 offen offset:3072 lds\n"
"v_mfma_f32_16x16x32_bf16 a[64:67], a[224:227], v[16:19], a[64:67]\n"
"v_exp_f32 v8, v8\n"
"v_mfma_f32_16x16x32_bf16 a[68:71], a[228:231], v[16:19], a[68:71]\n"
"s_nop 0\n"
"v_mfma_f32_16x16x32_bf16 a[72:75], a[232:235], v[16:19], a[72:75]\n"
"v_exp_f32 v9, v9\n"
"v_mfma_f32_16x16x32_bf16 a[76:79], a[236:239], v[16:19], a[76:79]\n"
"v_cvt_pk_bf16_f32 v20, v8, v9\n"
"v_mfma_f32_16x16x32_bf16 a[80:83], a[240:243], v[16:19], a[80:83]\n"
"v_exp_f32 v10, v10\n"
"v_mfma_f32_16x16x32_bf16 a[84:87], a[244:247], v[16:19], a[84:87]\n"
"s_nop 0\n"
"v_mfma_f32_16x16x32_bf16 a[88:91], a[248:251], v[16:19], a[88:91]\n"
"v_exp_f32 v11, v11\n"
"v_mfma_f32_16x16x32_bf16 a[92:95], a[252:255], v[16:19], a[92:95]\n"
"s_waitcnt lgkmcnt(8)\n"
"v_cvt_pk_bf16_f32 v21, v10, v11\n"
"v_mfma_f32_16x16x32_bf16 v[52:55], v[40:43], v[16:19], v[52:55]\n"
"v_exp_f32 v12, v12\n"
"v_mfma_f32_16x16x32_bf16 v[0:3], a[128:131], v[64:67], v[24:27]\n"
"ds_read_b128 a[160:163], v241 offset:8192\n"
"v_mfma_f32_16x16x32_bf16 v[0:3], a[132:135], v[68:71], v[0:3]\n"
"v_exp_f32 v13, v13\n"
"v_mfma_f32_16x16x32_bf16 v[0:3], a[136:139], v[72:75], v[0:3]\n"
"ds_read_b128 a[164:167], v241 offset:8448\n"
"v_cvt_pk_bf16_f32 v22, v12, v13\n"
"v_mfma_f32_16x16x32_bf16 v[0:3], a[140:143], v[76:79], v[0:3]\n"
"v_exp_f32 v14, v14\n"
"v_mfma_f32_16x16x32_bf16 v[4:7], a[144:147], v[64:67], v[24:27]\n"
"ds_read_b128 a[168:171], v241 offset:8704\n"
"v_mfma_f32_16x16x32_bf16 v[4:7], a[148:151], v[68:71], v[4:7]\n"
"v_exp_f32 v15, v15\n"
"v_mfma_f32_16x16x32_bf16 v[4:7], a[152:155], v[72:75], v[4:7]\n"
"s_add_u32 m0, s18, 49152\n"
"ds_read_b128 a[172:175], v241 offset:8960\n"
"v_cvt_pk_bf16_f32 v23, v14, v15\n"
"v_mfma_f32_16x16x32_bf16 v[4:7], a[156:159], v[76:79], v[4:7]\n"
"buffer_load_dwordx4 v246, s[12:15], s16 offen offset:0 lds\n"
"v_mfma_f32_16x16x32_bf16 a[96:99], a[224:227], v[20:23], a[96:99]\n"
"v_exp_f32 v0, v0\n"
"v_mfma_f32_16x16x32_bf16 a[100:103], a[228:231], v[20:23], a[100:103]\n"
"ds_read_b128 a[176:179], v241 offset:12288\n"
"v_mfma_f32_16x16x32_bf16 a[104:107], a[232:235], v[20:23], a[104:107]\n"
"v_exp_f32 v1, v1\n"
"v_mfma_f32_16x16x32_bf16 a[108:111], a[236:239], v[20:23], a[108:111]\n"
"ds_read_b128 a[180:183], v241 offset:12544\n"
"v_cvt_pk_bf16_f32 v16, v0, v1\n"
"v_mfma_f32_16x16x32_bf16 a[112:115], a[240:243], v[20:23], a[112:115]\n"
"v_exp_f32 v2, v2\n"
"v_mfma_f32_16x16x32_bf16 a[116:119], a[244:247], v[20:23], a[116:119]\n"
"ds_read_b128 a[184:187], v241 offset:12800\n"
"v_mfma_f32_16x16x32_bf16 a[120:123], a[248:251], v[20:23], a[120:123]\n"
"v_exp_f32 v3, v3\n"
"v_mfma_f32_16x16x32_bf16 a[124:127], a[252:255], v[20:23], a[124:127]\n"
"v_cvt_pk_bf16_f32 v17, v2, v3\n"
"v_mfma_f32_16x16x32_bf16 v[56:59], v[40:43], v[20:23], v[56:59]\n"
"v_exp_f32 v4, v4\n"
"v_mfma_f32_16x16x32_bf16 v[8:11], a[128:131], v[80:83], v[28:31]\n"
"ds_read_b128 a[188:191], v241 offset:13056\n"
"v_mfma_f32_16x16x32_bf16 v[8:11], a[132:135], v[84:87], v[8:11]\n"
"v_exp_f32 v5, v5\n"
"v_mfma_f32_16x16x32_bf16 v[8:11], a[136:139], v[88:91], v[8:11]\n"
"ds_read_b64_tr_b16 a[224:225], v244 offset:24576\n"
"v_cvt_pk_bf16_f32 v18, v4, v5\n"
"v_mfma_f32_16x16x32_bf16 v[8:11], a[140:143], v[92:95], v[8:11]\n"
"v_exp_f32 v6, v6\n"
"v_mfma_f32_16x16x32_bf16 v[12:15], a[144:147], v[80:83], v[28:31]\n"
"ds_read_b64_tr_b16 a[226:227], v244 offset:28672\n"
"v_mfma_f32_16x16x32_bf16 v[12:15], a[148:151], v[84:87], v[12:15]\n"
"v_exp_f32 v7, v7\n"
"v_mfma_f32_16x16x32_bf16 v[12:15], a[152:155], v[88:91], v[12:15]\n"
"ds_read_b64_tr_b16 a[228:229], v245 offset:24576\n"
"v_cvt_pk_bf16_f32 v19, v6, v7\n"
"v_mfma_f32_16x16x32_bf16 v[12:15], a[156:159], v[92:95], v[12:15]\n"
"s_waitcnt lgkmcnt(11)\n"
"buffer_load_dwordx4 v247, s[12:15], s16 offen offset:1024 lds\n"
"v_mfma_f32_16x16x32_bf16 a[0:3], a[192:195], v[16:19], a[0:3]\n"
"v_exp_f32 v8, v8\n"
"v_mfma_f32_16x16x32_bf16 a[4:7], a[196:199], v[16:19], a[4:7]\n"
"ds_read_b64_tr_b16 a[230:231], v245 offset:28672\n"
"v_mfma_f32_16x16x32_bf16 a[8:11], a[200:203], v[16:19], a[8:11]\n"
"v_exp_f32 v9, v9\n"
"v_mfma_f32_16x16x32_bf16 a[12:15], a[204:207], v[16:19], a[12:15]\n"
"ds_read_b64_tr_b16 a[232:233], v244 offset:24832\n"
"v_cvt_pk_bf16_f32 v20, v8, v9\n"
"v_mfma_f32_16x16x32_bf16 a[16:19], a[208:211], v[16:19], a[16:19]\n"
"v_exp_f32 v10, v10\n"
"v_mfma_f32_16x16x32_bf16 a[20:23], a[212:215], v[16:19], a[20:23]\n"
"ds_read_b64_tr_b16 a[234:235], v244 offset:28928\n"
"v_mfma_f32_16x16x32_bf16 a[24:27], a[216:219], v[16:19], a[24:27]\n"
"v_exp_f32 v11, v11\n"
"v_mfma_f32_16x16x32_bf16 a[28:31], a[220:223], v[16:19], a[28:31]\n"
"v_cvt_pk_bf16_f32 v21, v10, v11\n"
"v_mfma_f32_16x16x32_bf16 v[44:47], v[40:43], v[16:19], v[44:47]\n"
"v_exp_f32 v12, v12\n"
"v_mfma_f32_16x16x32_bf16 v[0:3], a[128:131], v[96:99], v[32:35]\n"
"ds_read_b64_tr_b16 a[236:237], v245 offset:24832\n"
"v_mfma_f32_16x16x32_bf16 v[0:3], a[132:135], v[100:103], v[0:3]\n"
"v_exp_f32 v13, v13\n"
"v_mfma_f32_16x16x32_bf16 v[0:3], a[136:139], v[104:107], v[0:3]\n"
"ds_read_b64_tr_b16 a[238:239], v245 offset:28928\n"
"v_cvt_pk_bf16_f32 v22, v12, v13\n"
"v_mfma_f32_16x16x32_bf16 v[0:3], a[140:143], v[108:111], v[0:3]\n"
"v_exp_f32 v14, v14\n"
"v_mfma_f32_16x16x32_bf16 v[4:7], a[144:147], v[96:99], v[32:35]\n"
"ds_read_b64_tr_b16 a[240:241], v244 offset:25088\n"
"v_mfma_f32_16x16x32_bf16 v[4:7], a[148:151], v[100:103], v[4:7]\n"
"v_exp_f32 v15, v15\n"
"v_mfma_f32_16x16x32_bf16 v[4:7], a[152:155], v[104:107], v[4:7]\n"
"ds_read_b64_tr_b16 a[242:243], v244 offset:29184\n"
"v_cvt_pk_bf16_f32 v23, v14, v15\n"
"v_mfma_f32_16x16x32_bf16 v[4:7], a[156:159], v[108:111], v[4:7]\n"
"buffer_load_dwordx4 v246, s[12:15], s16 offen offset:2048 lds\n"
"v_mfma_f32_16x16x32_bf16 a[32:35], a[192:195], v[20:23], a[32:35]\n"
"v_exp_f32 v0, v0\n"
"v_mfma_f32_16x16x32_bf16 a[36:39], a[196:199], v[20:23], a[36:39]\n"
"ds_read_b64_tr_b16 a[244:245], v245 offset:25088\n"
"v_mfma_f32_16x16x32_bf16 a[40:43], a[200:203], v[20:23], a[40:43]\n"
"v_exp_f32 v1, v1\n"
"v_mfma_f32_16x16x32_bf16 a[44:47], a[204:207], v[20:23], a[44:47]\n"
"ds_read_b64_tr_b16 a[246:247], v245 offset:29184\n"
"v_cvt_pk_bf16_f32 v16, v0, v1\n"
"v_mfma_f32_16x16x32_bf16 a[48:51], a[208:211], v[20:23], a[48:51]\n"
"v_exp_f32 v2, v2\n"
"v_mfma_f32_16x16x32_bf16 a[52:55], a[212:215], v[20:23], a[52:55]\n"
"ds_read_b64_tr_b16 a[248:249], v244 offset:25344\n"
"v_mfma_f32_16x16x32_bf16 a[56:59], a[216:219], v[20:23], a[56:59]\n"
"v_exp_f32 v3, v3\n"
"v_mfma_f32_16x16x32_bf16 a[60:63], a[220:223], v[20:23], a[60:63]\n"
"v_cvt_pk_bf16_f32 v17, v2, v3\n"
"v_mfma_f32_16x16x32_bf16 v[48:51], v[40:43], v[20:23], v[48:51]\n"
"v_exp_f32 v4, v4\n"
"v_mfma_f32_16x16x32_bf16 v[8:11], a[128:131], v[112:115], v[36:39]\n"
"ds_read_b64_tr_b16 a[250:251], v244 offset:29440\n"
"v_mfma_f32_16x16x32_bf16 v[8:11], a[132:135], v[116:119], v[8:11]\n"
"v_exp_f32 v5, v5\n"
"v_mfma_f32_16x16x32_bf16 v[8:11], a[136:139], v[120:123], v[8:11]\n"
"ds_read_b64_tr_b16 a[252:253], v245 offset:25344\n"
"v_cvt_pk_bf16_f32 v18, v4, v5\n"
"v_mfma_f32_16x16x32_bf16 v[8:11], a[140:143], v[124:127], v[8:11]\n"
"v_exp_f32 v6, v6\n"
"v_mfma_f32_16x16x32_bf16 v[12:15], a[144:147], v[112:115], v[36:39]\n"
"ds_read_b64_tr_b16 a[254:255], v245 offset:29440\n"
"v_mfma_f32_16x16x32_bf16 v[12:15], a[148:151], v[116:119], v[12:15]\n"
"v_exp_f32 v7, v7\n"
"v_mfma_f32_16x16x32_bf16 v[12:15], a[152:155], v[120:123], v[12:15]\n"
"v_cvt_pk_bf16_f32 v19, v6, v7\n"
"v_mfma_f32_16x16x32_bf16 v[12:15], a[156:159], v[124:127], v[12:15]\n"
"buffer_load_dwordx4 v247, s[12:15], s16 offen offset:3072 lds\n"
"v_mfma_f32_16x16x32_bf16 a[64:67], a[192:195], v[16:19], a[64:67]\n"
"s_add_u32 s16, s16, 16384\n"
"v_exp_f32 v8, v8\n"
"v_mfma_f32_16x16x32_bf16 a[68:71], a[196:199], v[16:19], a[68:71]\n"
"s_nop 0\n"
"v_mfma_f32_16x16x32_bf16 a[72:75], a[200:203], v[16:19], a[72:75]\n"
"v_exp_f32 v9, v9\n"
"v_mfma_f32_16x16x32_bf16 a[76:79], a[204:207], v[16:19], a[76:79]\n"
"v_cvt_pk_bf16_f32 v20, v8, v9\n"
"v_mfma_f32_16x16x32_bf16 a[80:83], a[208:211], v[16:19], a[80:83]\n"
"v_exp_f32 v10, v10\n"
"v_mfma_f32_16x16x32_bf16 a[84:87], a[212:215], v[16:19], a[84:87]\n"
"s_nop 0\n"
"v_mfma_f32_16x16x32_bf16 a[88:91], a[216:219], v[16:19], a[88:91]\n"
"v_exp_f32 v11, v11\n"
"v_mfma_f32_16x16x32_bf16 a[92:95], a[220:223], v[16:19], a[92:95]\n"
"s_waitcnt vmcnt(16) lgkmcnt(0)\n"
"s_barrier\n"
"v_cvt_pk_bf16_f32 v21, v10, v11\n"
"v_mfma_f32_16x16x32_bf16 v[52:55], v[40:43], v[16:19], v[52:55]\n"
"v_exp_f32 v12, v12\n"
"v_mfma_f32_16x16x32_bf16 v[0:3], a[160:163], v[64:67], v[24:27]\n"
"ds_read_b128 a[128:131], v241 offset:32768\n"
"v_mfma_f32_16x16x32_bf16 v[0:3], a[164:167], v[68:71], v[0:3]\n"
"v_exp_f32 v13, v13\n"
"v_mfma_f32_16x16x32_bf16 v[0:3], a[168:171], v[72:75], v[0:3]\n"
"ds_read_b128 a[132:135], v241 offset:33024\n"
"v_cvt_pk_bf16_f32 v22, v12, v13\n"
"v_mfma_f32_16x16x32_bf16 v[0:3], a[172:175], v[76:79], v[0:3]\n"
"v_exp_f32 v14, v14\n"
"v_mfma_f32_16x16x32_bf16 v[4:7], a[176:179], v[64:67], v[24:27]\n"
"ds_read_b128 a[136:139], v241 offset:33280\n"
"v_mfma_f32_16x16x32_bf16 v[4:7], a[180:183], v[68:71], v[4:7]\n"
"v_exp_f32 v15, v15\n"
"v_mfma_f32_16x16x32_bf16 v[4:7], a[184:187], v[72:75], v[4:7]\n"
"s_add_u32 m0, s18, 65536\n"
"ds_read_b128 a[140:143], v241 offset:33536\n"
"v_cvt_pk_bf16_f32 v23, v14, v15\n"
"v_mfma_f32_16x16x32_bf16 v[4:7], a[188:191], v[76:79], v[4:7]\n"
"buffer_load_dwordx4 v246, s[8:11], s16 offen offset:0 lds\n"
"v_mfma_f32_16x16x32_bf16 a[96:99], a[192:195], v[20:23], a[96:99]\n"
"v_exp_f32 v0, v0\n"
"v_mfma_f32_16x16x32_bf16 a[100:103], a[196:199], v[20:23], a[100:103]\n"
"ds_read_b128 a[144:147], v241 offset:36864\n"
"v_mfma_f32_16x16x32_bf16 a[104:107], a[200:203], v[20:23], a[104:107]\n"
"v_exp_f32 v1, v1\n"
"v_mfma_f32_16x16x32_bf16 a[108:111], a[204:207], v[20:23], a[108:111]\n"
"ds_read_b128 a[148:151], v241 offset:37120\n"
"v_cvt_pk_bf16_f32 v16, v0, v1\n"
"v_mfma_f32_16x16x32_bf16 a[112:115], a[208:211], v[20:23], a[112:115]\n"
"v_exp_f32 v2, v2\n"
"v_mfma_f32_16x16x32_bf16 a[116:119], a[212:215], v[20:23], a[116:119]\n"
"ds_read_b128 a[152:155], v241 offset:37376\n"
"v_mfma_f32_16x16x32_bf16 a[120:123], a[216:219], v[20:23], a[120:123]\n"
"v_exp_f32 v3, v3\n"
"v_mfma_f32_16x16x32_bf16 a[124:127], a[220:223], v[20:23], a[124:127]\n"
"v_cvt_pk_bf16_f32 v17, v2, v3\n"
"v_mfma_f32_16x16x32_bf16 v[56:59], v[40:43], v[20:23], v[56:59]\n"
"v_exp_f32 v4, v4\n"
"v_mfma_f32_16x16x32_bf16 v[8:11], a[160:163], v[80:83], v[28:31]\n"
"ds_read_b128 a[156:159], v241 offset:37632\n"
"v_mfma_f32_16x16x32_bf16 v[8:11], a[164:167], v[84:87], v[8:11]\n"
"v_exp_f32 v5, v5\n"
"v_mfma_f32_16x16x32_bf16 v[8:11], a[168:171], v[88:91], v[8:11]\n"
"ds_read_b64_tr_b16 a[192:193], v244 offset:49152\n"
"v_cvt_pk_bf16_f32 v18, v4, v5\n"
"v_mfma_f32_16x16x32_bf16 v[8:11], a[172:175], v[92:95], v[8:11]\n"
"v_exp_f32 v6, v6\n"
"v_mfma_f32_16x16x32_bf16 v[12:15], a[176:179], v[80:83], v[28:31]\n"
"ds_read_b64_tr_b16 a[194:195], v244 offset:53248\n"
"v_mfma_f32_16x16x32_bf16 v[12:15], a[180:183], v[84:87], v[12:15]\n"
"v_exp_f32 v7, v7\n"
"v_mfma_f32_16x16x32_bf16 v[12:15], a[184:187], v[88:91], v[12:15]\n"
"ds_read_b64_tr_b16 a[196:197], v245 offset:49152\n"
"v_cvt_pk_bf16_f32 v19, v6, v7\n"
"v_mfma_f32_16x16x32_bf16 v[12:15], a[188:191], v[92:95], v[12:15]\n"
"s_waitcnt lgkmcnt(11)\n"
"buffer_load_dwordx4 v246, s[8:11], s16 offen offset:1024 lds\n"
"v_mfma_f32_16x16x32_bf16 a[0:3], a[224:227], v[16:19], a[0:3]\n"
"v_exp_f32 v8, v8\n"
"v_mfma_f32_16x16x32_bf16 a[4:7], a[228:231], v[16:19], a[4:7]\n"
"ds_read_b64_tr_b16 a[198:199], v245 offset:53248\n"
"v_mfma_f32_16x16x32_bf16 a[8:11], a[232:235], v[16:19], a[8:11]\n"
"v_exp_f32 v9, v9\n"
"v_mfma_f32_16x16x32_bf16 a[12:15], a[236:239], v[16:19], a[12:15]\n"
"ds_read_b64_tr_b16 a[200:201], v244 offset:49408\n"
"v_cvt_pk_bf16_f32 v20, v8, v9\n"
"v_mfma_f32_16x16x32_bf16 a[16:19], a[240:243], v[16:19], a[16:19]\n"
"v_exp_f32 v10, v10\n"
"v_mfma_f32_16x16x32_bf16 a[20:23], a[244:247], v[16:19], a[20:23]\n"
"ds_read_b64_tr_b16 a[202:203], v244 offset:53504\n"
"v_mfma_f32_16x16x32_bf16 a[24:27], a[248:251], v[16:19], a[24:27]\n"
"v_exp_f32 v11, v11\n"
"v_mfma_f32_16x16x32_bf16 a[28:31], a[252:255], v[16:19], a[28:31]\n"
"v_cvt_pk_bf16_f32 v21, v10, v11\n"
"v_mfma_f32_16x16x32_bf16 v[44:47], v[40:43], v[16:19], v[44:47]\n"
"v_exp_f32 v12, v12\n"
"v_mfma_f32_16x16x32_bf16 v[0:3], a[160:163], v[96:99], v[32:35]\n"
"ds_read_b64_tr_b16 a[204:205], v245 offset:49408\n"
"v_mfma_f32_16x16x32_bf16 v[0:3], a[164:167], v[100:103], v[0:3]\n"
"v_exp_f32 v13, v13\n"
"v_mfma_f32_16x16x32_bf16 v[0:3], a[168:171], v[104:107], v[0:3]\n"
"ds_read_b64_tr_b16 a[206:207], v245 offset:53504\n"
"v_cvt_pk_bf16_f32 v22, v12, v13\n"
"v_mfma_f32_16x16x32_bf16 v[0:3], a[172:175], v[108:111], v[0:3]\n"
"v_exp_f32 v14, v14\n"
"v_mfma_f32_16x16x32_bf16 v[4:7], a[176:179], v[96:99], v[32:35]\n"
"ds_read_b64_tr_b16 a[208:209], v244 offset:49664\n"
"v_mfma_f32_16x16x32_bf16 v[4:7], a[180:183], v[100:103], v[4:7]\n"
"v_exp_f32 v15, v15\n"
"v_mfma_f32_16x16x32_bf16 v[4:7], a[184:187], v[104:107], v[4:7]\n"
"ds_read_b64_tr_b16 a[210:211], v244 offset:53760\n"
"v_cvt_pk_bf16_f32 v23, v14, v15\n"
"v_mfma_f32_16x16x32_bf16 v[4:7], a[188:191], v[108:111], v[4:7]\n"
"buffer_load_dwordx4 v247, s[8:11], s16 offen offset:2048 lds\n"
"v_mfma_f32_16x16x32_bf16 a[32:35], a[224:227], v[20:23], a[32:35]\n"
"v_exp_f32 v0, v0\n"
"v_mfma_f32_16x16x32_bf16 a[36:39], a[228:231], v[20:23], a[36:39]\n"
"ds_read_b64_tr_b16 a[212:213], v245 offset:49664\n"
"v_mfma_f32_16x16x32_bf16 a[40:43], a[232:235], v[20:23], a[40:43]\n"
"v_exp_f32 v1, v1\n"
"v_mfma_f32_16x16x32_bf16 a[44:47], a[236:239], v[20:23], a[44:47]\n"
"ds_read_b64_tr_b16 a[214:215], v245 offset:53760\n"
"v_cvt_pk_bf16_f32 v16, v0, v1\n"
"v_mfma_f32_16x16x32_bf16 a[48:51], a[240:243], v[20:23], a[48:51]\n"
"v_exp_f32 v2, v2\n"
"v_mfma_f32_16x16x32_bf16 a[52:55], a[244:247], v[20:23], a[52:55]\n"
"ds_read_b64_tr_b16 a[216:217], v244 offset:49920\n"
"v_mfma_f32_16x16x32_bf16 a[56:59], a[248:251], v[20:23], a[56:59]\n"
"v_exp_f32 v3, v3\n"
"v_mfma_f32_16x16x32_bf16 a[60:63], a[252:255], v[20:23], a[60:63]\n"
"v_cvt_pk_bf16_f32 v17, v2, v3\n"
"v_mfma_f32_16x16x32_bf16 v[48:51], v[40:43], v[20:23], v[48:51]\n"
"v_exp_f32 v4, v4\n"
"v_mfma_f32_16x16x32_bf16 v[8:11], a[160:163], v[112:115], v[36:39]\n"
"ds_read_b64_tr_b16 a[218:219], v244 offset:54016\n"
"v_mfma_f32_16x16x32_bf16 v[8:11], a[164:167], v[116:119], v[8:11]\n"
"v_exp_f32 v5, v5\n"
"v_mfma_f32_16x16x32_bf16 v[8:11], a[168:171], v[120:123], v[8:11]\n"
"ds_read_b64_tr_b16 a[220:221], v245 offset:49920\n"
"v_cvt_pk_bf16_f32 v18, v4, v5\n"
"v_mfma_f32_16x16x32_bf16 v[8:11], a[172:175], v[124:127], v[8:11]\n"
"v_exp_f32 v6, v6\n"
"v_mfma_f32_16x16x32_bf16 v[12:15], a[176:179], v[112:115], v[36:39]\n"
"ds_read_b64_tr_b16 a[222:223], v245 offset:54016\n"
"v_mfma_f32_16x16x32_bf16 v[12:15], a[180:183], v[116:119], v[12:15]\n"
"v_exp_f32 v7, v7\n"
"v_mfma_f32_16x16x32_bf16 v[12:15], a[184:187], v[120:123], v[12:15]\n"
"v_cvt_pk_bf16_f32 v19, v6, v7\n"
"v_mfma_f32_16x16x32_bf16 v[12:15], a[188:191], v[124:127], v[12:15]\n"
"buffer_load_dwordx4 v247, s[8:11], s16 offen offset:3072 lds\n"
"v_mfma_f32_16x16x32_bf16 a[64:67], a[224:227], v[16:19], a[64:67]\n"
"v_exp_f32 v8, v8\n"
"v_mfma_f32_16x16x32_bf16 a[68:71], a[228:231], v[16:19], a[68:71]\n"
"s_nop 0\n"
"v_mfma_f32_16x16x32_bf16 a[72:75], a[232:235], v[16:19], a[72:75]\n"
"v_exp_f32 v9, v9\n"
"v_mfma_f32_16x16x32_bf16 a[76:79], a[236:239], v[16:19], a[76:79]\n"
"v_cvt_pk_bf16_f32 v20, v8, v9\n"
"v_mfma_f32_16x16x32_bf16 a[80:83], a[240:243], v[16:19], a[80:83]\n"
"v_exp_f32 v10, v10\n"
"v_mfma_f32_16x16x32_bf16 a[84:87], a[244:247], v[16:19], a[84:87]\n"
"s_nop 0\n"
"v_mfma_f32_16x16x32_bf16 a[88:91], a[248:251], v[16:19], a[88:91]\n"
"v_exp_f32 v11, v11\n"
"v_mfma_f32_16x16x32_bf16 a[92:95], a[252:255], v[16:19], a[92:95]\n"
"s_waitcnt lgkmcnt(8)\n"
"v_cvt_pk_bf16_f32 v21, v10, v11\n"
"v_mfma_f32_16x16x32_bf16 v[52:55], v[40:43], v[16:19], v[52:55]\n"
"v_exp_f32 v12, v12\n"
"v_mfma_f32_16x16x32_bf16 v[0:3], a[128:131], v[64:67], v[24:27]\n"
"ds_read_b128 a[160:163], v241 offset:40960\n"
"v_mfma_f32_16x16x32_bf16 v[0:3], a[132:135], v[68:71], v[0:3]\n"
"v_exp_f32 v13, v13\n"
"v_mfma_f32_16x16x32_bf16 v[0:3], a[136:139], v[72:75], v[0:3]\n"
"ds_read_b128 a[164:167], v241 offset:41216\n"
"v_cvt_pk_bf16_f32 v22, v12, v13\n"
"v_mfma_f32_16x16x32_bf16 v[0:3], a[140:143], v[76:79], v[0:3]\n"
"v_exp_f32 v14, v14\n"
"v_mfma_f32_16x16x32_bf16 v[4:7], a[144:147], v[64:67], v[24:27]\n"
"ds_read_b128 a[168:171], v241 offset:41472\n"
"v_mfma_f32_16x16x32_bf16 v[4:7], a[148:151], v[68:71], v[4:7]\n"
"v_exp_f32 v15, v15\n"
"v_mfma_f32_16x16x32_bf16 v[4:7], a[152:155], v[72:75], v[4:7]\n"
"s_add_u32 m0, s18, 81920\n"
"ds_read_b128 a[172:175], v241 offset:41728\n"
"v_cvt_pk_bf16_f32 v23, v14, v15\n"
"v_mfma_f32_16x16x32_bf16 v[4:7], a[156:159], v[76:79], v[4:7]\n"
"buffer_load_dwordx4 v246, s[12:15], s16 offen offset:0 lds\n"
"v_mfma_f32_16x16x32_bf16 a[96:99], a[224:227], v[20:23], a[96:99]\n"
"v_exp_f32 v0, v0\n"
"v_mfma_f32_16x16x32_bf16 a[100:103], a[228:231], v[20:23], a[100:103]\n"
"ds_read_b128 a[176:179], v241 offset:45056\n"
"v_mfma_f32_16x16x32_bf16 a[104:107], a[232:235], v[20:23], a[104:107]\n"
"v_exp_f32 v1, v1\n"
"v_mfma_f32_16x16x32_bf16 a[108:111], a[236:239], v[20:23], a[108:111]\n"
"ds_read_b128 a[180:183], v241 offset:45312\n"
"v_cvt_pk_bf16_f32 v16, v0, v1\n"
"v_mfma_f32_16x16x32_bf16 a[112:115], a[240:243], v[20:23], a[112:115]\n"
"v_exp_f32 v2, v2\n"
"v_mfma_f32_16x16x32_bf16 a[116:119], a[244:247], v[20:23], a[116:119]\n"
"ds_read_b128 a[184:187], v241 offset:45568\n"
"v_mfma_f32_16x16x32_bf16 a[120:123], a[248:251], v[20:23], a[120:123]\n"
"v_exp_f32 v3, v3\n"
"v_mfma_f32_16x16x32_bf16 a[124:127], a[252:255], v[20:23], a[124:127]\n"
"v_cvt_pk_bf16_f32 v17, v2, v3\n"
"v_mfma_f32_16x16x32_bf16 v[56:59], v[40:43], v[20:23], v[56:59]\n"
"v_exp_f32 v4, v4\n"
"v_mfma_f32_16x16x32_bf16 v[8:11], a[128:131], v[80:83], v[28:31]\n"
"ds_read_b128 a[188:191], v241 offset:45824\n"
"v_mfma_f32_16x16x32_bf16 v[8:11], a[132:135], v[84:87], v[8:11]\n"
"v_exp_f32 v5, v5\n"
"v_mfma_f32_16x16x32_bf16 v[8:11], a[136:139], v[88:91], v[8:11]\n"
"ds_read_b64_tr_b16 a[224:225], v244 offset:57344\n"
"v_cvt_pk_bf16_f32 v18, v4, v5\n"
"v_mfma_f32_16x16x32_bf16 v[8:11], a[140:143], v[92:95], v[8:11]\n"
"v_exp_f32 v6, v6\n"
"v_mfma_f32_16x16x32_bf16 v[12:15], a[144:147], v[80:83], v[28:31]\n"
"ds_read_b64_tr_b16 a[226:227], v244 offset:61440\n"
"v_mfma_f32_16x16x32_bf16 v[12:15], a[148:151], v[84:87], v[12:15]\n"
"v_exp_f32 v7, v7\n"
"v_mfma_f32_16x16x32_bf16 v[12:15], a[152:155], v[88:91], v[12:15]\n"
"ds_read_b64_tr_b16 a[228:229], v245 offset:57344\n"
"v_cvt_pk_bf16_f32 v19, v6, v7\n"
"v_mfma_f32_16x16x32_bf16 v[12:15], a[156:159], v[92:95], v[12:15]\n"
"s_waitcnt lgkmcnt(11)\n"
"buffer_load_dwordx4 v247, s[12:15], s16 offen offset:1024 lds\n"
"v_mfma_f32_16x16x32_bf16 a[0:3], a[192:195], v[16:19], a[0:3]\n"
"v_exp_f32 v8, v8\n"
"v_mfma_f32_16x16x32_bf16 a[4:7], a[196:199], v[16:19], a[4:7]\n"
"ds_read_b64_tr_b16 a[230:231], v245 offset:61440\n"
"v_mfma_f32_16x16x32_bf16 a[8:11], a[200:203], v[16:19], a[8:11]\n"
"v_exp_f32 v9, v9\n"
"v_mfma_f32_16x16x32_bf16 a[12:15], a[204:207], v[16:19], a[12:15]\n"
"ds_read_b64_tr_b16 a[232:233], v244 offset:57600\n"
"v_cvt_pk_bf16_f32 v20, v8, v9\n"
"v_mfma_f32_16x16x32_bf16 a[16:19], a[208:211], v[16:19], a[16:19]\n"
"v_exp_f32 v10, v10\n"
"v_mfma_f32_16x16x32_bf16 a[20:23], a[212:215], v[16:19], a[20:23]\n"
"ds_read_b64_tr_b16 a[234:235], v244 offset:61696\n"
"v_mfma_f32_16x16x32_bf16 a[24:27], a[216:219], v[16:19], a[24:27]\n"
"v_exp_f32 v11, v11\n"
"v_mfma_f32_16x16x32_bf16 a[28:31], a[220:223], v[16:19], a[28:31]\n"
"v_cvt_pk_bf16_f32 v21, v10, v11\n"
"v_mfma_f32_16x16x32_bf16 v[44:47], v[40:43], v[16:19], v[44:47]\n"
"v_exp_f32 v12, v12\n"
"v_mfma_f32_16x16x32_bf16 v[0:3], a[128:131], v[96:99], v[32:35]\n"
"ds_read_b64_tr_b16 a[236:237], v245 offset:57600\n"
"v_mfma_f32_16x16x32_bf16 v[0:3], a[132:135], v[100:103], v[0:3]\n"
"v_exp_f32 v13, v13\n"
"v_mfma_f32_16x16x32_bf16 v[0:3], a[136:139], v[104:107], v[0:3]\n"
"ds_read_b64_tr_b16 a[238:239], v245 offset:61696\n"
"v_cvt_pk_bf16_f32 v22, v12, v13\n"
"v_mfma_f32_16x16x32_bf16 v[0:3], a[140:143], v[108:111], v[0:3]\n"
"v_exp_f32 v14, v14\n"
"v_mfma_f32_16x16x32_bf16 v[4:7], a[144:147], v[96:99], v[32:35]\n"
"ds_read_b64_tr_b16 a[240:241], v244 offset:57856\n"
"v_mfma_f32_16x16x32_bf16 v[4:7], a[148:151], v[100:103], v[4:7]\n"
"v_exp_f32 v15, v15\n"
"v_mfma_f32_16x16x32_bf16 v[4:7], a[152:155], v[104:107], v[4:7]\n"
"ds_read_b64_tr_b16 a[242:243], v244 offset:61952\n"
"v_cvt_pk_bf16_f32 v23, v14, v15\n"
"v_mfma_f32_16x16x32_bf16 v[4:7], a[156:159], v[108:111], v[4:7]\n"
"buffer_load_dwordx4 v246, s[12:15], s16 offen offset:2048 lds\n"
"v_mfma_f32_16x16x32_bf16 a[32:35], a[192:195], v[20:23], a[32:35]\n"
"v_exp_f32 v0, v0\n"
"v_mfma_f32_16x16x32_bf16 a[36:39], a[196:199], v[20:23], a[36:39]\n"
"ds_read_b64_tr_b16 a[244:245], v245 offset:57856\n"
"v_mfma_f32_16x16x32_bf16 a[40:43], a[200:203], v[20:23], a[40:43]\n"
"v_exp_f32 v1, v1\n"
"v_mfma_f32_16x16x32_bf16 a[44:47], a[204:207], v[20:23], a[44:47]\n"
"ds_read_b64_tr_b16 a[246:247], v245 offset:61952\n"
"v_cvt_pk_bf16_f32 v16, v0, v1\n"
"v_mfma_f32_16x16x32_bf16 a[48:51], a[208:211], v[20:23], a[48:51]\n"
"v_exp_f32 v2, v2\n"
"v_mfma_f32_16x16x32_bf16 a[52:55], a[212:215], v[20:23], a[52:55]\n"
"ds_read_b64_tr_b16 a[248:249], v244 offset:58112\n"
"v_mfma_f32_16x16x32_bf16 a[56:59], a[216:219], v[20:23], a[56:59]\n"
"v_exp_f32 v3, v3\n"
"v_mfma_f32_16x16x32_bf16 a[60:63], a[220:223], v[20:23], a[60:63]\n"
"v_cvt_pk_bf16_f32 v17, v2, v3\n"
"v_mfma_f32_16x16x32_bf16 v[48:51], v[40:43], v[20:23], v[48:51]\n"
"v_exp_f32 v4, v4\n"
"v_mfma_f32_16x16x32_bf16 v[8:11], a[128:131], v[112:115], v[36:39]\n"
"ds_read_b64_tr_b16 a[250:251], v244 offset:62208\n"
"v_mfma_f32_16x16x32_bf16 v[8:11], a[132:135], v[116:119], v[8:11]\n"
"v_exp_f32 v5, v5\n"
"v_mfma_f32_16x16x32_bf16 v[8:11], a[136:139], v[120:123], v[8:11]\n"
"ds_read_b64_tr_b16 a[252:253], v245 offset:58112\n"
"v_cvt_pk_bf16_f32 v18, v4, v5\n"
"v_mfma_f32_16x16x32_bf16 v[8:11], a[140:143], v[124:127], v[8:11]\n"
"v_exp_f32 v6, v6\n"
"v_mfma_f32_16x16x32_bf16 v[12:15], a[144:147], v[112:115], v[36:39]\n"
"ds_read_b64_tr_b16 a[254:255], v245 offset:62208\n"
"v_mfma_f32_16x16x32_bf16 v[12:15], a[148:151], v[116:119], v[12:15]\n"
"v_exp_f32 v7, v7\n"
"v_mfma_f32_16x16x32_bf16 v[12:15], a[152:155], v[120:123], v[12:15]\n"
"v_cvt_pk_bf16_f32 v19, v6, v7\n"
"v_mfma_f32_16x16x32_bf16 v[12:15], a[156:159], v[124:127], v[12:15]\n"
"buffer_load_dwordx4 v247, s[12:15], s16 offen offset:3072 lds\n"
"v_mfma_f32_16x16x32_bf16 a[64:67], a[192:195], v[16:19], a[64:67]\n"
"s_add_u32 s16, s16, 16384\n"
"v_exp_f32 v8, v8\n"
"v_mfma_f32_16x16x32_bf16 a[68:71], a[196:199], v[16:19], a[68:71]\n"
"s_nop 0\n"
"v_mfma_f32_16x16x32_bf16 a[72:75], a[200:203], v[16:19], a[72:75]\n"
"v_exp_f32 v9, v9\n"
"v_mfma_f32_16x16x32_bf16 a[76:79], a[204:207], v[16:19], a[76:79]\n"
"v_cvt_pk_bf16_f32 v20, v8, v9\n"
"v_mfma_f32_16x16x32_bf16 a[80:83], a[208:211], v[16:19], a[80:83]\n"
"v_exp_f32 v10, v10\n"
"v_mfma_f32_16x16x32_bf16 a[84:87], a[212:215], v[16:19], a[84:87]\n"
"s_nop 0\n"
"v_mfma_f32_16x16x32_bf16 a[88:91], a[216:219], v[16:19], a[88:91]\n"
"v_exp_f32 v11, v11\n"
"v_mfma_f32_16x16x32_bf16 a[92:95], a[220:223], v[16:19], a[92:95]\n"
"s_waitcnt vmcnt(16) lgkmcnt(0)\n"
"s_barrier\n"
"v_cvt_pk_bf16_f32 v21, v10, v11\n"
"v_mfma_f32_16x16x32_bf16 v[52:55], v[40:43], v[16:19], v[52:55]\n"
"v_exp_f32 v12, v12\n"
"v_mfma_f32_16x16x32_bf16 v[0:3], a[160:163], v[64:67], v[24:27]\n"
"ds_read_b128 a[128:131], v240 offset:0\n"
"v_mfma_f32_16x16x32_bf16 v[0:3], a[164:167], v[68:71], v[0:3]\n"
"v_exp_f32 v13, v13\n"
"v_mfma_f32_16x16x32_bf16 v[0:3], a[168:171], v[72:75], v[0:3]\n"
"ds_read_b128 a[132:135], v240 offset:256\n"
"v_cvt_pk_bf16_f32 v22, v12, v13\n"
"v_mfma_f32_16x16x32_bf16 v[0:3], a[172:175], v[76:79], v[0:3]\n"
"v_exp_f32 v14, v14\n"
"v_mfma_f32_16x16x32_bf16 v[4:7], a[176:179], v[64:67], v[24:27]\n"
"ds_read_b128 a[136:139], v240 offset:512\n"
"v_mfma_f32_16x16x32_bf16 v[4:7], a[180:183], v[68:71], v[4:7]\n"
"v_exp_f32 v15, v15\n"
"v_mfma_f32_16x16x32_bf16 v[4:7], a[184:187], v[72:75], v[4:7]\n"
"s_add_u32 m0, s18, 98304\n"
"ds_read_b128 a[140:143], v240 offset:768\n"
"v_cvt_pk_bf16_f32 v23, v14, v15\n"
"v_mfma_f32_16x16x32_bf16 v[4:7], a[188:191], v[76:79], v[4:7]\n"
"buffer_load_dwordx4 v246, s[8:11], s16 offen offset:0 lds\n"
"v_mfma_f32_16x16x32_bf16 a[96:99], a[192:195], v[20:23], a[96:99]\n"
"v_exp_f32 v0, v0\n"
"v_mfma_f32_16x16x32_bf16 a[100:103], a[196:199], v[20:23], a[100:103]\n"
"ds_read_b128 a[144:147], v240 offset:4096\n"
"v_mfma_f32_16x16x32_bf16 a[104:107], a[200:203], v[20:23], a[104:107]\n"
"v_exp_f32 v1, v1\n"
"v_mfma_f32_16x16x32_bf16 a[108:111], a[204:207], v[20:23], a[108:111]\n"
"ds_read_b128 a[148:151], v240 offset:4352\n"
"v_cvt_pk_bf16_f32 v16, v0, v1\n"
"v_mfma_f32_16x16x32_bf16 a[112:115], a[208:211], v[20:23], a[112:115]\n"
"v_exp_f32 v2, v2\n"
"v_mfma_f32_16x16x32_bf16 a[116:119], a[212:215], v[20:23], a[116:119]\n"
"ds_read_b128 a[152:155], v240 offset:4608\n"
"v_mfma_f32_16x16x32_bf16 a[120:123], a[216:219], v[20:23], a[120:123]\n"
"v_exp_f32 v3, v3\n"
"v_mfma_f32_16x16x32_bf16 a[124:127], a[220:223], v[20:23], a[124:127]\n"
"v_cvt_pk_bf16_f32 v17, v2, v3\n"
"v_mfma_f32_16x16x32_bf16 v[56:59], v[40:43], v[20:23], v[56:59]\n"
"v_exp_f32 v4, v4\n"
"v_mfma_f32_16x16x32_bf16 v[8:11], a[160:163], v[80:83], v[28:31]\n"
"ds_read_b128 a[156:159], v240 offset:4864\n"
"v_mfma_f32_16x16x32_bf16 v[8:11], a[164:167], v[84:87], v[8:11]\n"
"v_exp_f32 v5, v5\n"
"v_mfma_f32_16x16x32_bf16 v[8:11], a[168:171], v[88:91], v[8:11]\n"
"ds_read_b64_tr_b16 a[192:193], v242 offset:16384\n"
"v_cvt_pk_bf16_f32 v18, v4, v5\n"
"v_mfma_f32_16x16x32_bf16 v[8:11], a[172:175], v[92:95], v[8:11]\n"
"v_exp_f32 v6, v6\n"
"v_mfma_f32_16x16x32_bf16 v[12:15], a[176:179], v[80:83], v[28:31]\n"
"ds_read_b64_tr_b16 a[194:195], v242 offset:20480\n"
"v_mfma_f32_16x16x32_bf16 v[12:15], a[180:183], v[84:87], v[12:15]\n"
"v_exp_f32 v7, v7\n"
"v_mfma_f32_16x16x32_bf16 v[12:15], a[184:187], v[88:91], v[12:15]\n"
"ds_read_b64_tr_b16 a[196:197], v243 offset:16384\n"
"v_cvt_pk_bf16_f32 v19, v6, v7\n"
"v_mfma_f32_16x16x32_bf16 v[12:15], a[188:191], v[92:95], v[12:15]\n"
"s_waitcnt lgkmcnt(11)\n"
"buffer_load_dwordx4 v246, s[8:11], s16 offen offset:1024 lds\n"
"v_mfma_f32_16x16x32_bf16 a[0:3], a[224:227], v[16:19], a[0:3]\n"
"v_exp_f32 v8, v8\n"
"v_mfma_f32_16x16x32_bf16 a[4:7], a[228:231], v[16:19], a[4:7]\n"
"ds_read_b64_tr_b16 a[198:199], v243 offset:20480\n"
"v_mfma_f32_16x16x32_bf16 a[8:11], a[232:235], v[16:19], a[8:11]\n"
"v_exp_f32 v9, v9\n"
"v_mfma_f32_16x16x32_bf16 a[12:15], a[236:239], v[16:19], a[12:15]\n"
"ds_read_b64_tr_b16 a[200:201], v242 offset:16640\n"
"v_cvt_pk_bf16_f32 v20, v8, v9\n"
"v_mfma_f32_16x16x32_bf16 a[16:19], a[240:243], v[16:19], a[16:19]\n"
"v_exp_f32 v10, v10\n"
"v_mfma_f32_16x16x32_bf16 a[20:23], a[244:247], v[16:19], a[20:23]\n"
"ds_read_b64_tr_b16 a[202:203], v242 offset:20736\n"
"v_mfma_f32_16x16x32_bf16 a[24:27], a[248:251], v[16:19], a[24:27]\n"
"v_exp_f32 v11, v11\n"
"v_mfma_f32_16x16x32_bf16 a[28:31], a[252:255], v[16:19], a[28:31]\n"
"v_cvt_pk_bf16_f32 v21, v10, v11\n"
"v_mfma_f32_16x16x32_bf16 v[44:47], v[40:43], v[16:19], v[44:47]\n"
"v_exp_f32 v12, v12\n"
"v_mfma_f32_16x16x32_bf16 v[0:3], a[160:163], v[96:99], v[32:35]\n"
"ds_read_b64_tr_b16 a[204:205], v243 offset:16640\n"
"v_mfma_f32_16x16x32_bf16 v[0:3], a[164:167], v[100:103], v[0:3]\n"
"v_exp_f32 v13, v13\n"
"v_mfma_f32_16x16x32_bf16 v[0:3], a[168:171], v[104:107], v[0:3]\n"
"ds_read_b64_tr_b16 a[206:207], v243 offset:20736\n"
"v_cvt_pk_bf16_f32 v22, v12, v13\n"
"v_mfma_f32_16x16x32_bf16 v[0:3], a[172:175], v[108:111], v[0:3]\n"
"v_exp_f32 v14, v14\n"
"v_mfma_f32_16x16x32_bf16 v[4:7], a[176:179], v[96:99], v[32:35]\n"
"ds_read_b64_tr_b16 a[208:209], v242 offset:16896\n"
"v_mfma_f32_16x16x32_bf16 v[4:7], a[180:183], v[100:103], v[4:7]\n"
"v_exp_f32 v15, v15\n"
"v_mfma_f32_16x16x32_bf16 v[4:7], a[184:187], v[104:107], v[4:7]\n"
"ds_read_b64_tr_b16 a[210:211], v242 offset:20992\n"
"v_cvt_pk_bf16_f32 v23, v14, v15\n"
"v_mfma_f32_16x16x32_bf16 v[4:7], a[188:191], v[108:111], v[4:7]\n"
"buffer_load_dwordx4 v247, s[8:11], s16 offen offset:2048 lds\n"
"v_mfma_f32_16x16x32_bf16 a[32:35], a[224:227], v[20:23], a[32:35]\n"
"v_exp_f32 v0, v0\n"
"v_mfma_f32_16x16x32_bf16 a[36:39], a[228:231], v[20:23], a[36:39]\n"
"ds_read_b64_tr_b16 a[212:213], v243 offset:16896\n"
"v_mfma_f32_16x16x32_bf16 a[40:43], a[232:235], v[20:23], a[40:43]\n"
"v_exp_f32 v1, v1\n"
"v_mfma_f32_16x16x32_bf16 a[44:47], a[236:239], v[20:23], a[44:47]\n"
"ds_read_b64_tr_b16 a[214:215], v243 offset:20992\n"
"v_cvt_pk_bf16_f32 v16, v0, v1\n"
"v_mfma_f32_16x16x32_bf16 a[48:51], a[240:243], v[20:23], a[48:51]\n"
"v_exp_f32 v2, v2\n"
"v_mfma_f32_16x16x32_bf16 a[52:55], a[244:247], v[20:23], a[52:55]\n"
"ds_read_b64_tr_b16 a[216:217], v242 offset:17152\n"
"v_mfma_f32_16x16x32_bf16 a[56:59], a[248:251], v[20:23], a[56:59]\n"
"v_exp_f32 v3, v3\n"
"v_mfma_f32_16x16x32_bf16 a[60:63], a[252:255], v[20:23], a[60:63]\n"
"v_cvt_pk_bf16_f32 v17, v2, v3\n"
"v_mfma_f32_16x16x32_bf16 v[48:51], v[40:43], v[20:23], v[48:51]\n"
"v_exp_f32 v4, v4\n"
"v_mfma_f32_16x16x32_bf16 v[8:11], a[160:163], v[112:115], v[36:39]\n"
"ds_read_b64_tr_b16 a[218:219], v242 offset:21248\n"
"v_mfma_f32_16x16x32_bf16 v[8:11], a[164:167], v[116:119], v[8:11]\n"
"v_exp_f32 v5, v5\n"
"v_mfma_f32_16x16x32_bf16 v[8:11], a[168:171], v[120:123], v[8:11]\n"
"ds_read_b64_tr_b16 a[220:221], v243 offset:17152\n"
"v_cvt_pk_bf16_f32 v18, v4, v5\n"
"v_mfma_f32_16x16x32_bf16 v[8:11], a[172:175], v[124:127], v[8:11]\n"
"v_exp_f32 v6, v6\n"
"v_mfma_f32_16x16x32_bf16 v[12:15], a[176:179], v[112:115], v[36:39]\n"
"ds_read_b64_tr_b16 a[222:223], v243 offset:21248\n"
"v_mfma_f32_16x16x32_bf16 v[12:15], a[180:183], v[116:119], v[12:15]\n"
"v_exp_f32 v7, v7\n"
"v_mfma_f32_16x16x32_bf16 v[12:15], a[184:187], v[120:123], v[12:15]\n"
"v_cvt_pk_bf16_f32 v19, v6, v7\n"
"v_mfma_f32_16x16x32_bf16 v[12:15], a[188:191], v[124:127], v[12:15]\n"
"buffer_load_dwordx4 v247, s[8:11], s16 offen offset:3072 lds\n"
"v_mfma_f32_16x16x32_bf16 a[64:67], a[224:227], v[16:19], a[64:67]\n"
"v_exp_f32 v8, v8\n"
"v_mfma_f32_16x16x32_bf16 a[68:71], a[228:231], v[16:19], a[68:71]\n"
"s_nop 0\n"
"v_mfma_f32_16x16x32_bf16 a[72:75], a[232:235], v[16:19], a[72:75]\n"
"v_exp_f32 v9, v9\n"
"v_mfma_f32_16x16x32_bf16 a[76:79], a[236:239], v[16:19], a[76:79]\n"
"v_cvt_pk_bf16_f32 v20, v8, v9\n"
"v_mfma_f32_16x16x32_bf16 a[80:83], a[240:243], v[16:19], a[80:83]\n"
"v_exp_f32 v10, v10\n"
"v_mfma_f32_16x16x32_bf16 a[84:87], a[244:247], v[16:19], a[84:87]\n"
"s_nop 0\n"
"v_mfma_f32_16x16x32_bf16 a[88:91], a[248:251], v[16:19], a[88:91]\n"
"v_exp_f32 v11, v11\n"
"v_mfma_f32_16x16x32_bf16 a[92:95], a[252:255], v[16:19], a[92:95]\n"
"s_waitcnt lgkmcnt(8)\n"
"v_cvt_pk_bf16_f32 v21, v10, v11\n"
"v_mfma_f32_16x16x32_bf16 v[52:55], v[40:43], v[16:19], v[52:55]\n"
"v_exp_f32 v12, v12\n"
"v_mfma_f32_16x16x32_bf16 v[0:3], a[128:131], v[64:67], v[24:27]\n"
"ds_read_b128 a[160:163], v240 offset:8192\n"
"v_mfma_f32_16x16x32_bf16 v[0:3], a[132:135], v[68:71], v[0:3]\n"
"v_exp_f32 v13, v13\n"
"v_mfma_f32_16x16x32_bf16 v[0:3], a[136:139], v[72:75], v[0:3]\n"
"ds_read_b128 a[164:167], v240 offset:8448\n"
"v_cvt_pk_bf16_f32 v22, v12, v13\n"
"v_mfma_f32_16x16x32_bf16 v[0:3], a[140:143], v[76:79], v[0:3]\n"
"v_exp_f32 v14, v14\n"
"v_mfma_f32_16x16x32_bf16 v[4:7], a[144:147], v[64:67], v[24:27]\n"
"ds_read_b128 a[168:171], v240 offset:8704\n"
"v_mfma_f32_16x16x32_bf16 v[4:7], a[148:151], v[68:71], v[4:7]\n"
"v_exp_f32 v15, v15\n"
"v_mfma_f32_16x16x32_bf16 v[4:7], a[152:155], v[72:75], v[4:7]\n"
"s_add_u32 m0, s18, 114688\n"
"ds_read_b128 a[172:175], v240 offset:8960\n"
"v_cvt_pk_bf16_f32 v23, v14, v15\n"
"v_mfma_f32_16x16x32_bf16 v[4:7], a[156:159], v[76:79], v[4:7]\n"
"buffer_load_dwordx4 v246, s[12:15], s16 offen offset:0 lds\n"
"v_mfma_f32_16x16x32_bf16 a[96:99], a[224:227], v[20:23], a[96:99]\n"
"v_exp_f32 v0, v0\n"
"v_mfma_f32_16x16x32_bf16 a[100:103], a[228:231], v[20:23], a[100:103]\n"
"ds_read_b128 a[176:179], v240 offset:12288\n"
"v_mfma_f32_16x16x32_bf16 a[104:107], a[232:235], v[20:23], a[104:107]\n"
"v_exp_f32 v1, v1\n"
"v_mfma_f32_16x16x32_bf16 a[108:111], a[236:239], v[20:23], a[108:111]\n"
"ds_read_b128 a[180:183], v240 offset:12544\n"
"v_cvt_pk_bf16_f32 v16, v0, v1\n"
"v_mfma_f32_16x16x32_bf16 a[112:115], a[240:243], v[20:23], a[112:115]\n"
"v_exp_f32 v2, v2\n"
"v_mfma_f32_16x16x32_bf16 a[116:119], a[244:247], v[20:23], a[116:119]\n"
"ds_read_b128 a[184:187], v240 offset:12800\n"
"v_mfma_f32_16x16x32_bf16 a[120:123], a[248:251], v[20:23], a[120:123]\n"
"v_exp_f32 v3, v3\n"
"v_mfma_f32_16x16x32_bf16 a[124:127], a[252:255], v[20:23], a[124:127]\n"
"v_cvt_pk_bf16_f32 v17, v2, v3\n"
"v_mfma_f32_16x16x32_bf16 v[56:59], v[40:43], v[20:23], v[56:59]\n"
"v_exp_f32 v4, v4\n"
"v_mfma_f32_16x16x32_bf16 v[8:11], a[128:131], v[80:83], v[28:31]\n"
"ds_read_b128 a[188:191], v240 offset:13056\n"
"v_mfma_f32_16x16x32_bf16 v[8:11], a[132:135], v[84:87], v[8:11]\n"
"v_exp_f32 v5, v5\n"
"v_mfma_f32_16x16x32_bf16 v[8:11], a[136:139], v[88:91], v[8:11]\n"
"ds_read_b64_tr_b16 a[224:225], v242 offset:24576\n"
"v_cvt_pk_bf16_f32 v18, v4, v5\n"
"v_mfma_f32_16x16x32_bf16 v[8:11], a[140:143], v[92:95], v[8:11]\n"
"v_exp_f32 v6, v6\n"
"v_mfma_f32_16x16x32_bf16 v[12:15], a[144:147], v[80:83], v[28:31]\n"
"ds_read_b64_tr_b16 a[226:227], v242 offset:28672\n"
"v_mfma_f32_16x16x32_bf16 v[12:15], a[148:151], v[84:87], v[12:15]\n"
"v_exp_f32 v7, v7\n"
"v_mfma_f32_16x16x32_bf16 v[12:15], a[152:155], v[88:91], v[12:15]\n"
"ds_read_b64_tr_b16 a[228:229], v243 offset:24576\n"
"v_cvt_pk_bf16_f32 v19, v6, v7\n"
"v_mfma_f32_16x16x32_bf16 v[12:15], a[156:159], v[92:95], v[12:15]\n"
"s_waitcnt lgkmcnt(11)\n"
"buffer_load_dwordx4 v247, s[12:15], s16 offen offset:1024 lds\n"
"v_mfma_f32_16x16x32_bf16 a[0:3], a[192:195], v[16:19], a[0:3]\n"
"v_exp_f32 v8, v8\n"
"v_mfma_f32_16x16x32_bf16 a[4:7], a[196:199], v[16:19], a[4:7]\n"
"ds_read_b64_tr_b16 a[230:231], v243 offset:28672\n"
"v_mfma_f32_16x16x32_bf16 a[8:11], a[200:203], v[16:19], a[8:11]\n"
"v_exp_f32 v9, v9\n"
"v_mfma_f32_16x16x32_bf16 a[12:15], a[204:207], v[16:19], a[12:15]\n"
"ds_read_b64_tr_b16 a[232:233], v242 offset:24832\n"
"v_cvt_pk_bf16_f32 v20, v8, v9\n"
"v_mfma_f32_16x16x32_bf16 a[16:19], a[208:211], v[16:19], a[16:19]\n"
"v_exp_f32 v10, v10\n"
"v_mfma_f32_16x16x32_bf16 a[20:23], a[212:215], v[16:19], a[20:23]\n"
"ds_read_b64_tr_b16 a[234:235], v242 offset:28928\n"
"v_mfma_f32_16x16x32_bf16 a[24:27], a[216:219], v[16:19], a[24:27]\n"
"v_exp_f32 v11, v11\n"
"v_mfma_f32_16x16x32_bf16 a[28:31], a[220:223], v[16:19], a[28:31]\n"
"v_cvt_pk_bf16_f32 v21, v10, v11\n"
"v_mfma_f32_16x16x32_bf16 v[44:47], v[40:43], v[16:19], v[44:47]\n"
"v_exp_f32 v12, v12\n"
"v_mfma_f32_16x16x32_bf16 v[0:3], a[128:131], v[96:99], v[32:35]\n"
"ds_read_b64_tr_b16 a[236:237], v243 offset:24832\n"
"v_mfma_f32_16x16x32_bf16 v[0:3], a[132:135], v[100:103], v[0:3]\n"
"v_exp_f32 v13, v13\n"
"v_mfma_f32_16x16x32_bf16 v[0:3], a[136:139], v[104:107], v[0:3]\n"
"ds_read_b64_tr_b16 a[238:239], v243 offset:28928\n"
"v_cvt_pk_bf16_f32 v22, v12, v13\n"
"v_mfma_f32_16x16x32_bf16 v[0:3], a[140:143], v[108:111], v[0:3]\n"
"v_exp_f32 v14, v14\n"
"v_mfma_f32_16x16x32_bf16 v[4:7], a[144:147], v[96:99], v[32:35]\n"
"ds_read_b64_tr_b16 a[240:241], v242 offset:25088\n"
"v_mfma_f32_16x16x32_bf16 v[4:7], a[148:151], v[100:103], v[4:7]\n"
"v_exp_f32 v15, v15\n"
"v_mfma_f32_16x16x32_bf16 v[4:7], a[152:155], v[104:107], v[4:7]\n"
"ds_read_b64_tr_b16 a[242:243], v242 offset:29184\n"
"v_cvt_pk_bf16_f32 v23, v14, v15\n"
"v_mfma_f32_16x16x32_bf16 v[4:7], a[156:159], v[108:111], v[4:7]\n"
"buffer_load_dwordx4 v246, s[12:15], s16 offen offset:2048 lds\n"
"v_mfma_f32_16x16x32_bf16 a[32:35], a[192:195], v[20:23], a[32:35]\n"
"v_exp_f32 v0, v0\n"
"v_mfma_f32_16x16x32_bf16 a[36:39], a[196:199], v[20:23], a[36:39]\n"
"ds_read_b64_tr_b16 a[244:245], v243 offset:25088\n"
"v_mfma_f32_16x16x32_bf16 a[40:43], a[200:203], v[20:23], a[40:43]\n"
"v_exp_f32 v1, v1\n"
"v_mfma_f32_16x16x32_bf16 a[44:47], a[204:207], v[20:23], a[44:47]\n"
"ds_read_b64_tr_b16 a[246:247], v243 offset:29184\n"
"v_cvt_pk_bf16_f32 v16, v0, v1\n"
"v_mfma_f32_16x16x32_bf16 a[48:51], a[208:211], v[20:23], a[48:51]\n"
"v_exp_f32 v2, v2\n"
"v_mfma_f32_16x16x32_bf16 a[52:55], a[212:215], v[20:23], a[52:55]\n"
"ds_read_b64_tr_b16 a[248:249], v242 offset:25344\n"
"v_mfma_f32_16x16x32_bf16 a[56:59], a[216:219], v[20:23], a[56:59]\n"
"v_exp_f32 v3, v3\n"
"v_mfma_f32_16x16x32_bf16 a[60:63], a[220:223], v[20:23], a[60:63]\n"
"v_cvt_pk_bf16_f32 v17, v2, v3\n"
"v_mfma_f32_16x16x32_bf16 v[48:51], v[40:43], v[20:23], v[48:51]\n"
"v_exp_f32 v4, v4\n"
"v_mfma_f32_16x16x32_bf16 v[8:11], a[128:131], v[112:115], v[36:39]\n"
"ds_read_b64_tr_b16 a[250:251], v242 offset:29440\n"
"v_mfma_f32_16x16x32_bf16 v[8:11], a[132:135], v[116:119], v[8:11]\n"
"v_exp_f32 v5, v5\n"
"v_mfma_f32_16x16x32_bf16 v[8:11], a[136:139], v[120:123], v[8:11]\n"
"ds_read_b64_tr_b16 a[252:253], v243 offset:25344\n"
"v_cvt_pk_bf16_f32 v18, v4, v5\n"
"v_mfma_f32_16x16x32_bf16 v[8:11], a[140:143], v[124:127], v[8:11]\n"
"v_exp_f32 v6, v6\n"
"v_mfma_f32_16x16x32_bf16 v[12:15], a[144:147], v[112:115], v[36:39]\n"
"ds_read_b64_tr_b16 a[254:255], v243 offset:29440\n"
"v_mfma_f32_16x16x32_bf16 v[12:15], a[148:151], v[116:119], v[12:15]\n"
"v_exp_f32 v7, v7\n"
"v_mfma_f32_16x16x32_bf16 v[12:15], a[152:155], v[120:123], v[12:15]\n"
"v_cvt_pk_bf16_f32 v19, v6, v7\n"
"v_mfma_f32_16x16x32_bf16 v[12:15], a[156:159], v[124:127], v[12:15]\n"
"buffer_load_dwordx4 v247, s[12:15], s16 offen offset:3072 lds\n"
"v_mfma_f32_16x16x32_bf16 a[64:67], a[192:195], v[16:19], a[64:67]\n"
"s_add_u32 s16, s16, 16384\n"
"v_exp_f32 v8, v8\n"
"v_mfma_f32_16x16x32_bf16 a[68:71], a[196:199], v[16:19], a[68:71]\n"
"s_nop 0\n"
"v_mfma_f32_16x16x32_bf16 a[72:75], a[200:203], v[16:19], a[72:75]\n"
"v_exp_f32 v9, v9\n"
"v_mfma_f32_16x16x32_bf16 a[76:79], a[204:207], v[16:19], a[76:79]\n"
"v_cvt_pk_bf16_f32 v20, v8, v9\n"
"v_mfma_f32_16x16x32_bf16 a[80:83], a[208:211], v[16:19], a[80:83]\n"
"v_exp_f32 v10, v10\n"
"v_mfma_f32_16x16x32_bf16 a[84:87], a[212:215], v[16:19], a[84:87]\n"
"s_nop 0\n"
"v_mfma_f32_16x16x32_bf16 a[88:91], a[216:219], v[16:19], a[88:91]\n"
"v_exp_f32 v11, v11\n"
"v_mfma_f32_16x16x32_bf16 a[92:95], a[220:223], v[16:19], a[92:95]\n"
"s_waitcnt vmcnt(16) lgkmcnt(0)\n"
"s_barrier\n"
"v_cvt_pk_bf16_f32 v21, v10, v11\n"
"v_mfma_f32_16x16x32_bf16 v[52:55], v[40:43], v[16:19], v[52:55]\n"
"v_exp_f32 v12, v12\n"
"v_mfma_f32_16x16x32_bf16 v[0:3], a[160:163], v[64:67], v[24:27]\n"
"ds_read_b128 a[128:131], v240 offset:32768\n"
"v_mfma_f32_16x16x32_bf16 v[0:3], a[164:167], v[68:71], v[0:3]\n"
"v_exp_f32 v13, v13\n"
"v_mfma_f32_16x16x32_bf16 v[0:3], a[168:171], v[72:75], v[0:3]\n"
"ds_read_b128 a[132:135], v240 offset:33024\n"
"v_cvt_pk_bf16_f32 v22, v12, v13\n"
"v_mfma_f32_16x16x32_bf16 v[0:3], a[172:175], v[76:79], v[0:3]\n"
"v_exp_f32 v14, v14\n"
"v_mfma_f32_16x16x32_bf16 v[4:7], a[176:179], v[64:67], v[24:27]\n"
"ds_read_b128 a[136:139], v240 offset:33280\n"
"v_mfma_f32_16x16x32_bf16 v[4:7], a[180:183], v[68:71], v[4:7]\n"
"v_exp_f32 v15, v15\n"
"v_mfma_f32_16x16x32_bf16 v[4:7], a[184:187], v[72:75], v[4:7]\n"
"s_mov_b32 m0, s18\n"
"ds_read_b128 a[140:143], v240 offset:33536\n"
"v_cvt_pk_bf16_f32 v23, v14, v15\n"
"v_mfma_f32_16x16x32_bf16 v[4:7], a[188:191], v[76:79], v[4:7]\n"
"buffer_load_dwordx4 v246, s[8:11], s16 offen offset:0 lds\n"
"v_mfma_f32_16x16x32_bf16 a[96:99], a[192:195], v[20:23], a[96:99]\n"
"v_exp_f32 v0, v0\n"
"v_mfma_f32_16x16x32_bf16 a[100:103], a[196:199], v[20:23], a[100:103]\n"
"ds_read_b128 a[144:147], v240 offset:36864\n"
"v_mfma_f32_16x16x32_bf16 a[104:107], a[200:203], v[20:23], a[104:107]\n"
"v_exp_f32 v1, v1\n"
"v_mfma_f32_16x16x32_bf16 a[108:111], a[204:207], v[20:23], a[108:111]\n"
"ds_read_b128 a[148:151], v240 offset:37120\n"
"v_cvt_pk_bf16_f32 v16, v0, v1\n"
"v_mfma_f32_16x16x32_bf16 a[112:115], a[208:211], v[20:23], a[112:115]\n"
"v_exp_f32 v2, v2\n"
"v_mfma_f32_16x16x32_bf16 a[116:119], a[212:215], v[20:23], a[116:119]\n"
"ds_read_b128 a[152:155], v240 offset:37376\n"
"v_mfma_f32_16x16x32_bf16 a[120:123], a[216:219], v[20:23], a[120:123]\n"
"v_exp_f32 v3, v3\n"
"v_mfma_f32_16x16x32_bf16 a[124:127], a[220:223], v[20:23], a[124:127]\n"
"v_cvt_pk_bf16_f32 v17, v2, v3\n"
"v_mfma_f32_16x16x32_bf16 v[56:59], v[40:43], v[20:23], v[56:59]\n"
"v_exp_f32 v4, v4\n"
"v_mfma_f32_16x16x32_bf16 v[8:11], a[160:163], v[80:83], v[28:31]\n"
"ds_read_b128 a[156:159], v240 offset:37632\n"
"v_mfma_f32_16x16x32_bf16 v[8:11], a[164:167], v[84:87], v[8:11]\n"
"v_exp_f32 v5, v5\n"
"v_mfma_f32_16x16x32_bf16 v[8:11], a[168:171], v[88:91], v[8:11]\n"
"ds_read_b64_tr_b16 a[192:193], v242 offset:49152\n"
"v_cvt_pk_bf16_f32 v18, v4, v5\n"
"v_mfma_f32_16x16x32_bf16 v[8:11], a[172:175], v[92:95], v[8:11]\n"
"v_exp_f32 v6, v6\n"
"v_mfma_f32_16x16x32_bf16 v[12:15], a[176:179], v[80:83], v[28:31]\n"
"ds_read_b64_tr_b16 a[194:195], v242 offset:53248\n"
"v_mfma_f32_16x16x32_bf16 v[12:15], a[180:183], v[84:87], v[12:15]\n"
"v_exp_f32 v7, v7\n"
"v_mfma_f32_16x16x32_bf16 v[12:15], a[184:187], v[88:91], v[12:15]\n"
"ds_read_b64_tr_b16 a[196:197], v243 offset:49152\n"
"v_cvt_pk_bf16_f32 v19, v6, v7\n"
"v_mfma_f32_16x16x32_bf16 v[12:15], a[188:191], v[92:95], v[12:15]\n"
"s_waitcnt lgkmcnt(11)\n"
"buffer_load_dwordx4 v246, s[8:11], s16 offen offset:1024 lds\n"
"v_mfma_f32_16x16x32_bf16 a[0:3], a[224:227], v[16:19], a[0:3]\n"
"v_exp_f32 v8, v8\n"
"v_mfma_f32_16x16x32_bf16 a[4:7], a[228:231], v[16:19], a[4:7]\n"
"ds_read_b64_tr_b16 a[198:199], v243 offset:53248\n"
"v_mfma_f32_16x16x32_bf16 a[8:11], a[232:235], v[16:19], a[8:11]\n"
"v_exp_f32 v9, v9\n"
"v_mfma_f32_16x16x32_bf16 a[12:15], a[236:239], v[16:19], a[12:15]\n"
"ds_read_b64_tr_b16 a[200:201], v242 offset:49408\n"
"v_cvt_pk_bf16_f32 v20, v8, v9\n"
"v_mfma_f32_16x16x32_bf16 a[16:19], a[240:243], v[16:19], a[16:19]\n"
"v_exp_f32 v10, v10\n"
"v_mfma_f32_16x16x32_bf16 a[20:23], a[244:247], v[16:19], a[20:23]\n"
"ds_read_b64_tr_b16 a[202:203], v242 offset:53504\n"
"v_mfma_f32_16x16x32_bf16 a[24:27], a[248:251], v[16:19], a[24:27]\n"
"v_exp_f32 v11, v11\n"
"v_mfma_f32_16x16x32_bf16 a[28:31], a[252:255], v[16:19], a[28:31]\n"
"v_cvt_pk_bf16_f32 v21, v10, v11\n"
"v_mfma_f32_16x16x32_bf16 v[44:47], v[40:43], v[16:19], v[44:47]\n"
"v_exp_f32 v12, v12\n"
"v_mfma_f32_16x16x32_bf16 v[0:3], a[160:163], v[96:99], v[32:35]\n"
"ds_read_b64_tr_b16 a[204:205], v243 offset:49408\n"
"v_mfma_f32_16x16x32_bf16 v[0:3], a[164:167], v[100:103], v[0:3]\n"
"v_exp_f32 v13, v13\n"
"v_mfma_f32_16x16x32_bf16 v[0:3], a[168:171], v[104:107], v[0:3]\n"
"ds_read_b64_tr_b16 a[206:207], v243 offset:53504\n"
"v_cvt_pk_bf16_f32 v22, v12, v13\n"
"v_mfma_f32_16x16x32_bf16 v[0:3], a[172:175], v[108:111], v[0:3]\n"
"v_exp_f32 v14, v14\n"
"v_mfma_f32_16x16x32_bf16 v[4:7], a[176:179], v[96:99], v[32:35]\n"
"ds_read_b64_tr_b16 a[208:209], v242 offset:49664\n"
"v_mfma_f32_16x16x32_bf16 v[4:7], a[180:183], v[100:103], v[4:7]\n"
"v_exp_f32 v15, v15\n"
"v_mfma_f32_16x16x32_bf16 v[4:7], a[184:187], v[104:107], v[4:7]\n"
"ds_read_b64_tr_b16 a[210:211], v242 offset:53760\n"
"v_cvt_pk_bf16_f32 v23, v14, v15\n"
"v_mfma_f32_16x16x32_bf16 v[4:7], a[188:191], v[108:111], v[4:7]\n"
"buffer_load_dwordx4 v247, s[8:11], s16 offen offset:2048 lds\n"
"v_mfma_f32_16x16x32_bf16 a[32:35], a[224:227], v[20:23], a[32:35]\n"
"v_exp_f32 v0, v0\n"
"v_mfma_f32_16x16x32_bf16 a[36:39], a[228:231], v[20:23], a[36:39]\n"
"ds_read_b64_tr_b16 a[212:213], v243 offset:49664\n"
"v_mfma_f32_16x16x32_bf16 a[40:43], a[232:235], v[20:23], a[40:43]\n"
"v_exp_f32 v1, v1\n"
"v_mfma_f32_16x16x32_bf16 a[44:47], a[236:239], v[20:23], a[44:47]\n"
"ds_read_b64_tr_b16 a[214:215], v243 offset:53760\n"
"v_cvt_pk_bf16_f32 v16, v0, v1\n"
"v_mfma_f32_16x16x32_bf16 a[48:51], a[240:243], v[20:23], a[48:51]\n"
"v_exp_f32 v2, v2\n"
"v_mfma_f32_16x16x32_bf16 a[52:55], a[244:247], v[20:23], a[52:55]\n"
"ds_read_b64_tr_b16 a[216:217], v242 offset:49920\n"
"v_mfma_f32_16x16x32_bf16 a[56:59], a[248:251], v[20:23], a[56:59]\n"
"v_exp_f32 v3, v3\n"
"v_mfma_f32_16x16x32_bf16 a[60:63], a[252:255], v[20:23], a[60:63]\n"
"v_cvt_pk_bf16_f32 v17, v2, v3\n"
"v_mfma_f32_16x16x32_bf16 v[48:51], v[40:43], v[20:23], v[48:51]\n"
"v_exp_f32 v4, v4\n"
"v_mfma_f32_16x16x32_bf16 v[8:11], a[160:163], v[112:115], v[36:39]\n"
"ds_read_b64_tr_b16 a[218:219], v242 offset:54016\n"
"v_mfma_f32_16x16x32_bf16 v[8:11], a[164:167], v[116:119], v[8:11]\n"
"v_exp_f32 v5, v5\n"
"v_mfma_f32_16x16x32_bf16 v[8:11], a[168:171], v[120:123], v[8:11]\n"
"ds_read_b64_tr_b16 a[220:221], v243 offset:49920\n"
"v_cvt_pk_bf16_f32 v18, v4, v5\n"
"v_mfma_f32_16x16x32_bf16 v[8:11], a[172:175], v[124:127], v[8:11]\n"
"v_exp_f32 v6, v6\n"
"v_mfma_f32_16x16x32_bf16 v[12:15], a[176:179], v[112:115], v[36:39]\n"
"ds_read_b64_tr_b16 a[222:223], v243 offset:54016\n"
"v_mfma_f32_16x16x32_bf16 v[12:15], a[180:183], v[116:119], v[12:15]\n"
"v_exp_f32 v7, v7\n"
"v_mfma_f32_16x16x32_bf16 v[12:15], a[184:187], v[120:123], v[12:15]\n"
"v_cvt_pk_bf16_f32 v19, v6, v7\n"
"v_mfma_f32_16x16x32_bf16 v[12:15], a[188:191], v[124:127], v[12:15]\n"
"buffer_load_dwordx4 v247, s[8:11], s16 offen offset:3072 lds\n"
"v_mfma_f32_16x16x32_bf16 a[64:67], a[224:227], v[16:19], a[64:67]\n"
"v_exp_f32 v8, v8\n"
"v_mfma_f32_16x16x32_bf16 a[68:71], a[228:231], v[16:19], a[68:71]\n"
"s_nop 0\n"
"v_mfma_f32_16x16x32_bf16 a[72:75], a[232:235], v[16:19], a[72:75]\n"
"v_exp_f32 v9, v9\n"
"v_mfma_f32_16x16x32_bf16 a[76:79], a[236:239], v[16:19], a[76:79]\n"
"v_cvt_pk_bf16_f32 v20, v8, v9\n"
"v_mfma_f32_16x16x32_bf16 a[80:83], a[240:243], v[16:19], a[80:83]\n"
"v_exp_f32 v10, v10\n"
"v_mfma_f32_16x16x32_bf16 a[84:87], a[244:247], v[16:19], a[84:87]\n"
"s_nop 0\n"
"v_mfma_f32_16x16x32_bf16 a[88:91], a[248:251], v[16:19], a[88:91]\n"
"v_exp_f32 v11, v11\n"
"v_mfma_f32_16x16x32_bf16 a[92:95], a[252:255], v[16:19], a[92:95]\n"
"s_waitcnt lgkmcnt(8)\n"
"v_cvt_pk_bf16_f32 v21, v10, v11\n"
"v_mfma_f32_16x16x32_bf16 v[52:55], v[40:43], v[16:19], v[52:55]\n"
"v_exp_f32 v12, v12\n"
"v_mfma_f32_16x16x32_bf16 v[0:3], a[128:131], v[64:67], v[24:27]\n"
"ds_read_b128 a[160:163], v240 offset:40960\n"
"v_mfma_f32_16x16x32_bf16 v[0:3], a[132:135], v[68:71], v[0:3]\n"
"v_exp_f32 v13, v13\n"
"v_mfma_f32_16x16x32_bf16 v[0:3], a[136:139], v[72:75], v[0:3]\n"
"ds_read_b128 a[164:167], v240 offset:41216\n"
"v_cvt_pk_bf16_f32 v22, v12, v13\n"
"v_mfma_f32_16x16x32_bf16 v[0:3], a[140:143], v[76:79], v[0:3]\n"
"v_exp_f32 v14, v14\n"
"v_mfma_f32_16x16x32_bf16 v[4:7], a[144:147], v[64:67], v[24:27]\n"
"ds_read_b128 a[168:171], v240 offset:41472\n"
"v_mfma_f32_16x16x32_bf16 v[4:7], a[148:151], v[68:71], v[4:7]\n"
"v_exp_f32 v15, v15\n"
"v_mfma_f32_16x16x32_bf16 v[4:7], a[152:155], v[72:75], v[4:7]\n"
"s_add_u32 m0, s18, 16384\n"
"ds_read_b128 a[172:175], v240 offset:41728\n"
"v_cvt_pk_bf16_f32 v23, v14, v15\n"
"v_mfma_f32_16x16x32_bf16 v[4:7], a[156:159], v[76:79], v[4:7]\n"
"buffer_load_dwordx4 v246, s[12:15], s16 offen offset:0 lds\n"
"v_mfma_f32_16x16x32_bf16 a[96:99], a[224:227], v[20:23], a[96:99]\n"
"v_exp_f32 v0, v0\n"
"v_mfma_f32_16x16x32_bf16 a[100:103], a[228:231], v[20:23], a[100:103]\n"
"ds_read_b128 a[176:179], v240 offset:45056\n"
"v_mfma_f32_16x16x32_bf16 a[104:107], a[232:235], v[20:23], a[104:107]\n"
"v_exp_f32 v1, v1\n"
"v_mfma_f32_16x16x32_bf16 a[108:111], a[236:239], v[20:23], a[108:111]\n"
"ds_read_b128 a[180:183], v240 offset:45312\n"
"v_cvt_pk_bf16_f32 v16, v0, v1\n"
"v_mfma_f32_16x16x32_bf16 a[112:115], a[240:243], v[20:23], a[112:115]\n"
"v_exp_f32 v2, v2\n"
"v_mfma_f32_16x16x32_bf16 a[116:119], a[244:247], v[20:23], a[116:119]\n"
"ds_read_b128 a[184:187], v240 offset:45568\n"
"v_mfma_f32_16x16x32_bf16 a[120:123], a[248:251], v[20:23], a[120:123]\n"
"v_exp_f32 v3, v3\n"
"v_mfma_f32_16x16x32_bf16 a[124:127], a[252:255], v[20:23], a[124:127]\n"
"v_cvt_pk_bf16_f32 v17, v2, v3\n"
"v_mfma_f32_16x16x32_bf16 v[56:59], v[40:43], v[20:23], v[56:59]\n"
"v_exp_f32 v4, v4\n"
"v_mfma_f32_16x16x32_bf16 v[8:11], a[128:131], v[80:83], v[28:31]\n"
"ds_read_b128 a[188:191], v240 offset:45824\n"
"v_mfma_f32_16x16x32_bf16 v[8:11], a[132:135], v[84:87], v[8:11]\n"
"v_exp_f32 v5, v5\n"
"v_mfma_f32_16x16x32_bf16 v[8:11], a[136:139], v[88:91], v[8:11]\n"
"ds_read_b64_tr_b16 a[224:225], v242 offset:57344\n"
"v_cvt_pk_bf16_f32 v18, v4, v5\n"
"v_mfma_f32_16x16x32_bf16 v[8:11], a[140:143], v[92:95], v[8:11]\n"
"v_exp_f32 v6, v6\n"
"v_mfma_f32_16x16x32_bf16 v[12:15], a[144:147], v[80:83], v[28:31]\n"
"ds_read_b64_tr_b16 a[226:227], v242 offset:61440\n"
"v_mfma_f32_16x16x32_bf16 v[12:15], a[148:151], v[84:87], v[12:15]\n"
"v_exp_f32 v7, v7\n"
"v_mfma_f32_16x16x32_bf16 v[12:15], a[152:155], v[88:91], v[12:15]\n"
"ds_read_b64_tr_b16 a[228:229], v243 offset:57344\n"
"v_cvt_pk_bf16_f32 v19, v6, v7\n"
"v_mfma_f32_16x16x32_bf16 v[12:15], a[156:159], v[92:95], v[12:15]\n"
"s_waitcnt lgkmcnt(11)\n"
"buffer_load_dwordx4 v247, s[12:15], s16 offen offset:1024 lds\n"
"v_mfma_f32_16x16x32_bf16 a[0:3], a[192:195], v[16:19], a[0:3]\n"
"v_exp_f32 v8, v8\n"
"v_mfma_f32_16x16x32_bf16 a[4:7], a[196:199], v[16:19], a[4:7]\n"
"ds_read_b64_tr_b16 a[230:231], v243 offset:61440\n"
"v_mfma_f32_16x16x32_bf16 a[8:11], a[200:203], v[16:19], a[8:11]\n"
"v_exp_f32 v9, v9\n"
"v_mfma_f32_16x16x32_bf16 a[12:15], a[204:207], v[16:19], a[12:15]\n"
"ds_read_b64_tr_b16 a[232:233], v242 offset:57600\n"
"v_cvt_pk_bf16_f32 v20, v8, v9\n"
"v_mfma_f32_16x16x32_bf16 a[16:19], a[208:211], v[16:19], a[16:19]\n"
"v_exp_f32 v10, v10\n"
"v_mfma_f32_16x16x32_bf16 a[20:23], a[212:215], v[16:19], a[20:23]\n"
"ds_read_b64_tr_b16 a[234:235], v242 offset:61696\n"
"v_mfma_f32_16x16x32_bf16 a[24:27], a[216:219], v[16:19], a[24:27]\n"
"v_exp_f32 v11, v11\n"
"v_mfma_f32_16x16x32_bf16 a[28:31], a[220:223], v[16:19], a[28:31]\n"
"v_cvt_pk_bf16_f32 v21, v10, v11\n"
"v_mfma_f32_16x16x32_bf16 v[44:47], v[40:43], v[16:19], v[44:47]\n"
"v_exp_f32 v12, v12\n"
"v_mfma_f32_16x16x32_bf16 v[0:3], a[128:131], v[96:99], v[32:35]\n"
"ds_read_b64_tr_b16 a[236:237], v243 offset:57600\n"
"v_mfma_f32_16x16x32_bf16 v[0:3], a[132:135], v[100:103], v[0:3]\n"
"v_exp_f32 v13, v13\n"
"v_mfma_f32_16x16x32_bf16 v[0:3], a[136:139], v[104:107], v[0:3]\n"
"ds_read_b64_tr_b16 a[238:239], v243 offset:61696\n"
"v_cvt_pk_bf16_f32 v22, v12, v13\n"
"v_mfma_f32_16x16x32_bf16 v[0:3], a[140:143], v[108:111], v[0:3]\n"
"v_exp_f32 v14, v14\n"
"v_mfma_f32_16x16x32_bf16 v[4:7], a[144:147], v[96:99], v[32:35]\n"
"ds_read_b64_tr_b16 a[240:241], v242 offset:57856\n"
"v_mfma_f32_16x16x32_bf16 v[4:7], a[148:151], v[100:103], v[4:7]\n"
"v_exp_f32 v15, v15\n"
"v_mfma_f32_16x16x32_bf16 v[4:7], a[152:155], v[104:107], v[4:7]\n"
"ds_read_b64_tr_b16 a[242:243], v242 offset:61952\n"
"v_cvt_pk_bf16_f32 v23, v14, v15\n"
"v_mfma_f32_16x16x32_bf16 v[4:7], a[156:159], v[108:111], v[4:7]\n"
"buffer_load_dwordx4 v246, s[12:15], s16 offen offset:2048 lds\n"
"v_mfma_f32_16x16x32_bf16 a[32:35], a[192:195], v[20:23], a[32:35]\n"
"v_exp_f32 v0, v0\n"
"v_mfma_f32_16x16x32_bf16 a[36:39], a[196:199], v[20:23], a[36:39]\n"
"ds_read_b64_tr_b16 a[244:245], v243 offset:57856\n"
"v_mfma_f32_16x16x32_bf16 a[40:43], a[200:203], v[20:23], a[40:43]\n"
"v_exp_f32 v1, v1\n"
"v_mfma_f32_16x16x32_bf16 a[44:47], a[204:207], v[20:23], a[44:47]\n"
"ds_read_b64_tr_b16 a[246:247], v243 offset:61952\n"
"v_cvt_pk_bf16_f32 v16, v0, v1\n"
"v_mfma_f32_16x16x32_bf16 a[48:51], a[208:211], v[20:23], a[48:51]\n"
"v_exp_f32 v2, v2\n"
"v_mfma_f32_16x16x32_bf16 a[52:55], a[212:215], v[20:23], a[52:55]\n"
"ds_read_b64_tr_b16 a[248:249], v242 offset:58112\n"
"v_mfma_f32_16x16x32_bf16 a[56:59], a[216:219], v[20:23], a[56:59]\n"
"v_exp_f32 v3, v3\n"
"v_mfma_f32_16x16x32_bf16 a[60:63], a[220:223], v[20:23], a[60:63]\n"
"v_cvt_pk_bf16_f32 v17, v2, v3\n"
"v_mfma_f32_16x16x32_bf16 v[48:51], v[40:43], v[20:23], v[48:51]\n"
"v_exp_f32 v4, v4\n"
"v_mfma_f32_16x16x32_bf16 v[8:11], a[128:131], v[112:115], v[36:39]\n"
"ds_read_b64_tr_b16 a[250:251], v242 offset:62208\n"
"v_mfma_f32_16x16x32_bf16 v[8:11], a[132:135], v[116:119], v[8:11]\n"
"v_exp_f32 v5, v5\n"
"v_mfma_f32_16x16x32_bf16 v[8:11], a[136:139], v[120:123], v[8:11]\n"
"ds_read_b64_tr_b16 a[252:253], v243 offset:58112\n"
"v_cvt_pk_bf16_f32 v18, v4, v5\n"
"v_mfma_f32_16x16x32_bf16 v[8:11], a[140:143], v[124:127], v[8:11]\n"
"v_exp_f32 v6, v6\n"
"v_mfma_f32_16x16x32_bf16 v[12:15], a[144:147], v[112:115], v[36:39]\n"
"ds_read_b64_tr_b16 a[254:255], v243 offset:62208\n"
"v_mfma_f32_16x16x32_bf16 v[12:15], a[148:151], v[116:119], v[12:15]\n"
"v_exp_f32 v7, v7\n"
"v_mfma_f32_16x16x32_bf16 v[12:15], a[152:155], v[120:123], v[12:15]\n"
"v_cvt_pk_bf16_f32 v19, v6, v7\n"
"v_mfma_f32_16x16x32_bf16 v[12:15], a[156:159], v[124:127], v[12:15]\n"
"buffer_load_dwordx4 v247, s[12:15], s16 offen offset:3072 lds\n"
"v_mfma_f32_16x16x32_bf16 a[64:67], a[192:195], v[16:19], a[64:67]\n"
"s_add_u32 s16, s16, 16384\n"
"v_exp_f32 v8, v8\n"
"v_mfma_f32_16x16x32_bf16 a[68:71], a[196:199], v[16:19], a[68:71]\n"
"s_nop 0\n"
"v_mfma_f32_16x16x32_bf16 a[72:75], a[200:203], v[16:19], a[72:75]\n"
"v_exp_f32 v9, v9\n"
"v_mfma_f32_16x16x32_bf16 a[76:79], a[204:207], v[16:19], a[76:79]\n"
"v_cvt_pk_bf16_f32 v20, v8, v9\n"
"v_mfma_f32_16x16x32_bf16 a[80:83], a[208:211], v[16:19], a[80:83]\n"
"s_sub_u32 s19, s19, 1\n"
"v_exp_f32 v10, v10\n"
"v_mfma_f32_16x16x32_bf16 a[84:87], a[212:215], v[16:19], a[84:87]\n"
"s_cmp_lg_u32 s19, 0\n"
"v_mfma_f32_16x16x32_bf16 a[88:91], a[216:219], v[16:19], a[88:91]\n"
"v_exp_f32 v11, v11\n"
"v_mfma_f32_16x16x32_bf16 a[92:95], a[220:223], v[16:19], a[92:95]\n"
"s_waitcnt vmcnt(16) lgkmcnt(0)\n"
"s_barrier\n"
"v_cvt_pk_bf16_f32 v21, v10, v11\n"
"v_mfma_f32_16x16x32_bf16 v[52:55], v[40:43], v[16:19], v[52:55]\n"
"v_exp_f32 v12, v12\n"
"s_cbranch_scc1 AT_loop\n"
"v_lshlrev_b32 v214, 6, v248\n"
"v_lshrrev_b32 v215, 3, v248\n"
"v_mul_u32_u24 v215, 1152, v215\n"
"v_and_b32 v213, 7, v248\n"
"v_lshl_add_u32 v215, v213, 7, v215\n"
"s_and_b32 s46, s44, 7\n"
"s_lshl_b32 s46, s46, 2\n"
"s_add_u32 s46, s46, s41\n"
"s_mul_i32 s46, s46, 9216\n"
"s_getpc_b64 s[20:21]\n"
"AT_pc1:\n"
"s_sub_u32 s20, s20, AT_pc1-AT_start\n"
"s_subb_u32 s21, s21, 0\n"
"s_and_b32 s21, s21, 0xffff\n"
"s_mov_b32 s22, AT_end-AT_start\n"
"s_mov_b32 s23, 0x20000\n"
"s_lshr_b32 s47, s42, 3\n"
"s_lshl_b32 s47, s47, 2\n"
"s_add_u32 s47, s47, s41\n"
"s_and_b32 s47, s47, 31\n"
"s_lshl_b32 s47, s47, 12\n"
"s_lshl_b32 s48, s42, 2\n"
"s_add_u32 s48, s48, s41\n"
"s_mul_i32 s48, s48, 5184\n"
"s_add_u32 s49, s48, 1088\n"
"s_mov_b32 s24, s80\n"
"s_and_b32 s25, s81, 0xffff\n"
"s_mov_b32 s26, 5308416\n"
"s_mov_b32 s27, 0x20000\n"
"s_mov_b32 s64, s82\n"
"s_and_b32 s65, s83, 0xffff\n"
"s_mov_b32 s66, 5308416\n"
"s_mov_b32 s67, 0x20000\n"
"s_mov_b32 s68, s84\n"
"s_and_b32 s69, s85, 0xffff\n"
"s_mov_b32 s70, 5308416\n"
"s_mov_b32 s71, 0x20000\n"
"s_or_b32 s56, s42, s41\n"
"s_cmp_lg_u32 s56, 0\n"
"s_cbranch_scc1 AT_h1\n"
"v_lshlrev_b32 v212, 7, v248\n"
"s_mov_b32 s57, 32768\n"
"s_mov_b32 s58, 40960\n"
"s_mov_b32 s59, 49152\n"
"s_mov_b32 s60, 57344\n"
"buffer_load_dword v208, v212, s[36:39], s57 offen offset:4 sc1\n"
"buffer_load_dword v209, v212, s[36:39], s58 offen offset:4 sc1\n"
"buffer_load_dword v210, v212, s[36:39], s59 offen offset:4 sc1\n"
"buffer_load_dword v211, v212, s[36:39], s60 offen offset:4 sc1\n"
"AT_h1:\n"
"v_mfma_f32_16x16x32_bf16 v[0:3], a[160:163], v[64:67], v[24:27]\n"
"ds_read_b128 a[128:131], v241 offset:0\n"
"v_mfma_f32_16x16x32_bf16 v[0:3], a[164:167], v[68:71], v[0:3]\n"
"v_exp_f32 v13, v13\n"
"v_mfma_f32_16x16x32_bf16 v[0:3], a[168:171], v[72:75], v[0:3]\n"
"ds_read_b128 a[132:135], v241 offset:256\n"
"v_cvt_pk_bf16_f32 v22, v12, v13\n"
"v_mfma_f32_16x16x32_bf16 v[0:3], a[172:175], v[76:79], v[0:3]\n"
"v_exp_f32 v14, v14\n"
"v_mfma_f32_16x16x32_bf16 v[4:7], a[176:179], v[64:67], v[24:27]\n"
"ds_read_b128 a[136:139], v241 offset:512\n"
"v_mfma_f32_16x16x32_bf16 v[4:7], a[180:183], v[68:71], v[4:7]\n"
"v_exp_f32 v15, v15\n"
"v_mfma_f32_16x16x32_bf16 v[4:7], a[184:187], v[72:75], v[4:7]\n"
"s_add_u32 m0, s18, 32768\n"
"ds_read_b128 a[140:143], v241 offset:768\n"
"v_cvt_pk_bf16_f32 v23, v14, v15\n"
"v_mfma_f32_16x16x32_bf16 v[4:7], a[188:191], v[76:79], v[4:7]\n"
"buffer_load_dwordx4 v246, s[8:11], s16 offen offset:0 lds\n"
"v_mfma_f32_16x16x32_bf16 a[96:99], a[192:195], v[20:23], a[96:99]\n"
"v_exp_f32 v0, v0\n"
"v_mfma_f32_16x16x32_bf16 a[100:103], a[196:199], v[20:23], a[100:103]\n"
"ds_read_b128 a[144:147], v241 offset:4096\n"
"v_mfma_f32_16x16x32_bf16 a[104:107], a[200:203], v[20:23], a[104:107]\n"
"v_exp_f32 v1, v1\n"
"v_mfma_f32_16x16x32_bf16 a[108:111], a[204:207], v[20:23], a[108:111]\n"
"ds_read_b128 a[148:151], v241 offset:4352\n"
"v_cvt_pk_bf16_f32 v16, v0, v1\n"
"v_mfma_f32_16x16x32_bf16 a[112:115], a[208:211], v[20:23], a[112:115]\n"
"v_exp_f32 v2, v2\n"
"v_mfma_f32_16x16x32_bf16 a[116:119], a[212:215], v[20:23], a[116:119]\n"
"ds_read_b128 a[152:155], v241 offset:4608\n"
"v_mfma_f32_16x16x32_bf16 a[120:123], a[216:219], v[20:23], a[120:123]\n"
"v_exp_f32 v3, v3\n"
"v_mfma_f32_16x16x32_bf16 a[124:127], a[220:223], v[20:23], a[124:127]\n"
"v_cvt_pk_bf16_f32 v17, v2, v3\n"
"v_mfma_f32_16x16x32_bf16 v[56:59], v[40:43], v[20:23], v[56:59]\n"
"v_exp_f32 v4, v4\n"
"v_mfma_f32_16x16x32_bf16 v[8:11], a[160:163], v[80:83], v[28:31]\n"
"ds_read_b128 a[156:159], v241 offset:4864\n"
"v_mfma_f32_16x16x32_bf16 v[8:11], a[164:167], v[84:87], v[8:11]\n"
"v_exp_f32 v5, v5\n"
"v_mfma_f32_16x16x32_bf16 v[8:11], a[168:171], v[88:91], v[8:11]\n"
"ds_read_b64_tr_b16 a[192:193], v244 offset:16384\n"
"v_cvt_pk_bf16_f32 v18, v4, v5\n"
"v_mfma_f32_16x16x32_bf16 v[8:11], a[172:175], v[92:95], v[8:11]\n"
"v_exp_f32 v6, v6\n"
"v_mfma_f32_16x16x32_bf16 v[12:15], a[176:179], v[80:83], v[28:31]\n"
"ds_read_b64_tr_b16 a[194:195], v244 offset:20480\n"
"v_mfma_f32_16x16x32_bf16 v[12:15], a[180:183], v[84:87], v[12:15]\n"
"v_exp_f32 v7, v7\n"
"v_mfma_f32_16x16x32_bf16 v[12:15], a[184:187], v[88:91], v[12:15]\n"
"ds_read_b64_tr_b16 a[196:197], v245 offset:16384\n"
"v_cvt_pk_bf16_f32 v19, v6, v7\n"
"v_mfma_f32_16x16x32_bf16 v[12:15], a[188:191], v[92:95], v[12:15]\n"
"s_waitcnt lgkmcnt(11)\n"
"buffer_load_dwordx4 v246, s[8:11], s16 offen offset:1024 lds\n"
"v_mfma_f32_16x16x32_bf16 a[0:3], a[224:227], v[16:19], a[0:3]\n"
"v_exp_f32 v8, v8\n"
"v_mfma_f32_16x16x32_bf16 a[4:7], a[228:231], v[16:19], a[4:7]\n"
"ds_read_b64_tr_b16 a[198:199], v245 offset:20480\n"
"v_mfma_f32_16x16x32_bf16 a[8:11], a[232:235], v[16:19], a[8:11]\n"
"v_exp_f32 v9, v9\n"
"v_mfma_f32_16x16x32_bf16 a[12:15], a[236:239], v[16:19], a[12:15]\n"
"ds_read_b64_tr_b16 a[200:201], v244 offset:16640\n"
"v_cvt_pk_bf16_f32 v20, v8, v9\n"
"v_mfma_f32_16x16x32_bf16 a[16:19], a[240:243], v[16:19], a[16:19]\n"
"v_exp_f32 v10, v10\n"
"v_mfma_f32_16x16x32_bf16 a[20:23], a[244:247], v[16:19], a[20:23]\n"
"ds_read_b64_tr_b16 a[202:203], v244 offset:20736\n"
"v_mfma_f32_16x16x32_bf16 a[24:27], a[248:251], v[16:19], a[24:27]\n"
"v_exp_f32 v11, v11\n"
"v_mfma_f32_16x16x32_bf16 a[28:31], a[252:255], v[16:19], a[28:31]\n"
"v_cvt_pk_bf16_f32 v21, v10, v11\n"
"v_mfma_f32_16x16x32_bf16 v[44:47], v[40:43], v[16:19], v[44:47]\n"
"v_exp_f32 v12, v12\n"
"v_mfma_f32_16x16x32_bf16 v[0:3], a[160:163], v[96:99], v[32:35]\n"
"ds_read_b64_tr_b16 a[204:205], v245 offset:16640\n"
"v_mfma_f32_16x16x32_bf16 v[0:3], a[164:167], v[100:103], v[0:3]\n"
"v_exp_f32 v13, v13\n"
"v_mfma_f32_16x16x32_bf16 v[0:3], a[168:171], v[104:107], v[0:3]\n"
"ds_read_b64_tr_b16 a[206:207], v245 offset:20736\n"
"v_cvt_pk_bf16_f32 v22, v12, v13\n"
"v_mfma_f32_16x16x32_bf16 v[0:3], a[172:175], v[108:111], v[0:3]\n"
"v_exp_f32 v14, v14\n"
"v_mfma_f32_16x16x32_bf16 v[4:7], a[176:179], v[96:99], v[32:35]\n"
"ds_read_b64_tr_b16 a[208:209], v244 offset:16896\n"
"v_mfma_f32_16x16x32_bf16 v[4:7], a[180:183], v[100:103], v[4:7]\n"
"v_exp_f32 v15, v15\n"
"v_mfma_f32_16x16x32_bf16 v[4:7], a[184:187], v[104:107], v[4:7]\n"
"ds_read_b64_tr_b16 a[210:211], v244 offset:20992\n"
"v_cvt_pk_bf16_f32 v23, v14, v15\n"
"v_mfma_f32_16x16x32_bf16 v[4:7], a[188:191], v[108:111], v[4:7]\n"
"buffer_load_dwordx4 v247, s[8:11], s16 offen offset:2048 lds\n"
"v_mfma_f32_16x16x32_bf16 a[32:35], a[224:227], v[20:23], a[32:35]\n"
"v_exp_f32 v0, v0\n"
"v_mfma_f32_16x16x32_bf16 a[36:39], a[228:231], v[20:23], a[36:39]\n"
"ds_read_b64_tr_b16 a[212:213], v245 offset:16896\n"
"v_mfma_f32_16x16x32_bf16 a[40:43], a[232:235], v[20:23], a[40:43]\n"
"v_exp_f32 v1, v1\n"
"v_mfma_f32_16x16x32_bf16 a[44:47], a[236:239], v[20:23], a[44:47]\n"
"ds_read_b64_tr_b16 a[214:215], v245 offset:20992\n"
"v_cvt_pk_bf16_f32 v16, v0, v1\n"
"v_mfma_f32_16x16x32_bf16 a[48:51], a[240:243], v[20:23], a[48:51]\n"
"v_exp_f32 v2, v2\n"
"v_mfma_f32_16x16x32_bf16 a[52:55], a[244:247], v[20:23], a[52:55]\n"
"ds_read_b64_tr_b16 a[216:217], v244 offset:17152\n"
"v_mfma_f32_16x16x32_bf16 a[56:59], a[248:251], v[20:23], a[56:59]\n"
"v_exp_f32 v3, v3\n"
"v_mfma_f32_16x16x32_bf16 a[60:63], a[252:255], v[20:23], a[60:63]\n"
"v_cvt_pk_bf16_f32 v17, v2, v3\n"
"v_mfma_f32_16x16x32_bf16 v[48:51], v[40:43], v[20:23], v[48:51]\n"
"v_exp_f32 v4, v4\n"
"v_mfma_f32_16x16x32_bf16 v[8:11], a[160:163], v[112:115], v[36:39]\n"
"ds_read_b64_tr_b16 a[218:219], v244 offset:21248\n"
"v_mfma_f32_16x16x32_bf16 v[8:11], a[164:167], v[116:119], v[8:11]\n"
"v_exp_f32 v5, v5\n"
"v_mfma_f32_16x16x32_bf16 v[8:11], a[168:171], v[120:123], v[8:11]\n"
"ds_read_b64_tr_b16 a[220:221], v245 offset:17152\n"
"v_cvt_pk_bf16_f32 v18, v4, v5\n"
"v_mfma_f32_16x16x32_bf16 v[8:11], a[172:175], v[124:127], v[8:11]\n"
"v_exp_f32 v6, v6\n"
"v_mfma_f32_16x16x32_bf16 v[12:15], a[176:179], v[112:115], v[36:39]\n"
"ds_read_b64_tr_b16 a[222:223], v245 offset:21248\n"
"v_mfma_f32_16x16x32_bf16 v[12:15], a[180:183], v[116:119], v[12:15]\n"
"v_exp_f32 v7, v7\n"
"v_mfma_f32_16x16x32_bf16 v[12:15], a[184:187], v[120:123], v[12:15]\n"
"v_cvt_pk_bf16_f32 v19, v6, v7\n"
"v_mfma_f32_16x16x32_bf16 v[12:15], a[188:191], v[124:127], v[12:15]\n"
"buffer_load_dwordx4 v247, s[8:11], s16 offen offset:3072 lds\n"
"v_mfma_f32_16x16x32_bf16 a[64:67], a[224:227], v[16:19], a[64:67]\n"
"v_exp_f32 v8, v8\n"
"v_mfma_f32_16x16x32_bf16 a[68:71], a[228:231], v[16:19], a[68:71]\n"
"buffer_load_dword v250, v215, s[28:31], s46 offen offset:128\n"
"v_mfma_f32_16x16x32_bf16 a[72:75], a[232:235], v[16:19], a[72:75]\n"
"v_exp_f32 v9, v9\n"
"v_mfma_f32_16x16x32_bf16 a[76:79], a[236:239], v[16:19], a[76:79]\n"
"v_cvt_pk_bf16_f32 v20, v8, v9\n"
"v_mfma_f32_16x16x32_bf16 a[80:83], a[240:243], v[16:19], a[80:83]\n"
"v_exp_f32 v10, v10\n"
"v_mfma_f32_16x16x32_bf16 a[84:87], a[244:247], v[16:19], a[84:87]\n"
"s_nop 0\n"
"v_mfma_f32_16x16x32_bf16 a[88:91], a[248:251], v[16:19], a[88:91]\n"
"v_exp_f32 v11, v11\n"
"v_mfma_f32_16x16x32_bf16 a[92:95], a[252:255], v[16:19], a[92:95]\n"
"s_waitcnt lgkmcnt(8)\n"
"v_cvt_pk_bf16_f32 v21, v10, v11\n"
"v_mfma_f32_16x16x32_bf16 v[52:55], v[40:43], v[16:19], v[52:55]\n"
"v_exp_f32 v12, v12\n"
"v_mfma_f32_16x16x32_bf16 v[0:3], a[128:131], v[64:67], v[24:27]\n"
"ds_read_b128 a[160:163], v241 offset:8192\n"
"v_mfma_f32_16x16x32_bf16 v[0:3], a[132:135], v[68:71], v[0:3]\n"
"v_exp_f32 v13, v13\n"
"v_mfma_f32_16x16x32_bf16 v[0:3], a[136:139], v[72:75], v[0:3]\n"
"ds_read_b128 a[164:167], v241 offset:8448\n"
"v_cvt_pk_bf16_f32 v22, v12, v13\n"
"v_mfma_f32_16x16x32_bf16 v[0:3], a[140:143], v[76:79], v[0:3]\n"
"v_exp_f32 v14, v14\n"
"v_mfma_f32_16x16x32_bf16 v[4:7], a[144:147], v[64:67], v[24:27]\n"
"ds_read_b128 a[168:171], v241 offset:8704\n"
"v_mfma_f32_16x16x32_bf16 v[4:7], a[148:151], v[68:71], v[4:7]\n"
"v_exp_f32 v15, v15\n"
"v_mfma_f32_16x16x32_bf16 v[4:7], a[152:155], v[72:75], v[4:7]\n"
"s_add_u32 m0, s18, 49152\n"
"ds_read_b128 a[172:175], v241 offset:8960\n"
"v_cvt_pk_bf16_f32 v23, v14, v15\n"
"v_mfma_f32_16x16x32_bf16 v[4:7], a[156:159], v[76:79], v[4:7]\n"
"buffer_load_dwordx4 v246, s[12:15], s16 offen offset:0 lds\n"
"v_mfma_f32_16x16x32_bf16 a[96:99], a[224:227], v[20:23], a[96:99]\n"
"v_exp_f32 v0, v0\n"
"v_mfma_f32_16x16x32_bf16 a[100:103], a[228:231], v[20:23], a[100:103]\n"
"ds_read_b128 a[176:179], v241 offset:12288\n"
"v_mfma_f32_16x16x32_bf16 a[104:107], a[232:235], v[20:23], a[104:107]\n"
"v_exp_f32 v1, v1\n"
"v_mfma_f32_16x16x32_bf16 a[108:111], a[236:239], v[20:23], a[108:111]\n"
"ds_read_b128 a[180:183], v241 offset:12544\n"
"v_cvt_pk_bf16_f32 v16, v0, v1\n"
"v_mfma_f32_16x16x32_bf16 a[112:115], a[240:243], v[20:23], a[112:115]\n"
"v_exp_f32 v2, v2\n"
"v_mfma_f32_16x16x32_bf16 a[116:119], a[244:247], v[20:23], a[116:119]\n"
"ds_read_b128 a[184:187], v241 offset:12800\n"
"v_mfma_f32_16x16x32_bf16 a[120:123], a[248:251], v[20:23], a[120:123]\n"
"v_exp_f32 v3, v3\n"
"v_mfma_f32_16x16x32_bf16 a[124:127], a[252:255], v[20:23], a[124:127]\n"
"v_cvt_pk_bf16_f32 v17, v2, v3\n"
"v_mfma_f32_16x16x32_bf16 v[56:59], v[40:43], v[20:23], v[56:59]\n"
"v_exp_f32 v4, v4\n"
"v_mfma_f32_16x16x32_bf16 v[8:11], a[128:131], v[80:83], v[28:31]\n"
"ds_read_b128 a[188:191], v241 offset:13056\n"
"v_mfma_f32_16x16x32_bf16 v[8:11], a[132:135], v[84:87], v[8:11]\n"
"v_exp_f32 v5, v5\n"
"v_mfma_f32_16x16x32_bf16 v[8:11], a[136:139], v[88:91], v[8:11]\n"
"ds_read_b64_tr_b16 a[224:225], v244 offset:24576\n"
"v_cvt_pk_bf16_f32 v18, v4, v5\n"
"v_mfma_f32_16x16x32_bf16 v[8:11], a[140:143], v[92:95], v[8:11]\n"
"v_exp_f32 v6, v6\n"
"v_mfma_f32_16x16x32_bf16 v[12:15], a[144:147], v[80:83], v[28:31]\n"
"ds_read_b64_tr_b16 a[226:227], v244 offset:28672\n"
"v_mfma_f32_16x16x32_bf16 v[12:15], a[148:151], v[84:87], v[12:15]\n"
"v_exp_f32 v7, v7\n"
"v_mfma_f32_16x16x32_bf16 v[12:15], a[152:155], v[88:91], v[12:15]\n"
"ds_read_b64_tr_b16 a[228:229], v245 offset:24576\n"
"v_cvt_pk_bf16_f32 v19, v6, v7\n"
"v_mfma_f32_16x16x32_bf16 v[12:15], a[156:159], v[92:95], v[12:15]\n"
"s_waitcnt lgkmcnt(11)\n"
"buffer_load_dwordx4 v247, s[12:15], s16 offen offset:1024 lds\n"
"v_mfma_f32_16x16x32_bf16 a[0:3], a[192:195], v[16:19], a[0:3]\n"
"v_exp_f32 v8, v8\n"
"v_mfma_f32_16x16x32_bf16 a[4:7], a[196:199], v[16:19], a[4:7]\n"
"ds_read_b64_tr_b16 a[230:231], v245 offset:28672\n"
"v_mfma_f32_16x16x32_bf16 a[8:11], a[200:203], v[16:19], a[8:11]\n"
"v_exp_f32 v9, v9\n"
"v_mfma_f32_16x16x32_bf16 a[12:15], a[204:207], v[16:19], a[12:15]\n"
"ds_read_b64_tr_b16 a[232:233], v244 offset:24832\n"
"v_cvt_pk_bf16_f32 v20, v8, v9\n"
"v_mfma_f32_16x16x32_bf16 a[16:19], a[208:211], v[16:19], a[16:19]\n"
"v_exp_f32 v10, v10\n"
"v_mfma_f32_16x16x32_bf16 a[20:23], a[212:215], v[16:19], a[20:23]\n"
"ds_read_b64_tr_b16 a[234:235], v244 offset:28928\n"
"v_mfma_f32_16x16x32_bf16 a[24:27], a[216:219], v[16:19], a[24:27]\n"
"v_exp_f32 v11, v11\n"
"v_mfma_f32_16x16x32_bf16 a[28:31], a[220:223], v[16:19], a[28:31]\n"
"v_cvt_pk_bf16_f32 v21, v10, v11\n"
"v_mfma_f32_16x16x32_bf16 v[44:47], v[40:43], v[16:19], v[44:47]\n"
"v_exp_f32 v12, v12\n"
"v_mfma_f32_16x16x32_bf16 v[0:3], a[128:131], v[96:99], v[32:35]\n"
"ds_read_b64_tr_b16 a[236:237], v245 offset:24832\n"
"v_mfma_f32_16x16x32_bf16 v[0:3], a[132:135], v[100:103], v[0:3]\n"
"v_exp_f32 v13, v13\n"
"v_mfma_f32_16x16x32_bf16 v[0:3], a[136:139], v[104:107], v[0:3]\n"
"ds_read_b64_tr_b16 a[238:239], v245 offset:28928\n"
"v_cvt_pk_bf16_f32 v22, v12, v13\n"
"v_mfma_f32_16x16x32_bf16 v[0:3], a[140:143], v[108:111], v[0:3]\n"
"v_exp_f32 v14, v14\n"
"v_mfma_f32_16x16x32_bf16 v[4:7], a[144:147], v[96:99], v[32:35]\n"
"ds_read_b64_tr_b16 a[240:241], v244 offset:25088\n"
"v_mfma_f32_16x16x32_bf16 v[4:7], a[148:151], v[100:103], v[4:7]\n"
"v_exp_f32 v15, v15\n"
"v_mfma_f32_16x16x32_bf16 v[4:7], a[152:155], v[104:107], v[4:7]\n"
"ds_read_b64_tr_b16 a[242:243], v244 offset:29184\n"
"v_cvt_pk_bf16_f32 v23, v14, v15\n"
"v_mfma_f32_16x16x32_bf16 v[4:7], a[156:159], v[108:111], v[4:7]\n"
"buffer_load_dwordx4 v246, s[12:15], s16 offen offset:2048 lds\n"
"v_mfma_f32_16x16x32_bf16 a[32:35], a[192:195], v[20:23], a[32:35]\n"
"v_exp_f32 v0, v0\n"
"v_mfma_f32_16x16x32_bf16 a[36:39], a[196:199], v[20:23], a[36:39]\n"
"ds_read_b64_tr_b16 a[244:245], v245 offset:25088\n"
"v_mfma_f32_16x16x32_bf16 a[40:43], a[200:203], v[20:23], a[40:43]\n"
"v_exp_f32 v1, v1\n"
"v_mfma_f32_16x16x32_bf16 a[44:47], a[204:207], v[20:23], a[44:47]\n"
"ds_read_b64_tr_b16 a[246:247], v245 offset:29184\n"
"v_cvt_pk_bf16_f32 v16, v0, v1\n"
"v_mfma_f32_16x16x32_bf16 a[48:51], a[208:211], v[20:23], a[48:51]\n"
"v_exp_f32 v2, v2\n"
"v_mfma_f32_16x16x32_bf16 a[52:55], a[212:215], v[20:23], a[52:55]\n"
"ds_read_b64_tr_b16 a[248:249], v244 offset:25344\n"
"v_mfma_f32_16x16x32_bf16 a[56:59], a[216:219], v[20:23], a[56:59]\n"
"v_exp_f32 v3, v3\n"
"v_mfma_f32_16x16x32_bf16 a[60:63], a[220:223], v[20:23], a[60:63]\n"
"v_cvt_pk_bf16_f32 v17, v2, v3\n"
"v_mfma_f32_16x16x32_bf16 v[48:51], v[40:43], v[20:23], v[48:51]\n"
"v_exp_f32 v4, v4\n"
"v_mfma_f32_16x16x32_bf16 v[8:11], a[128:131], v[112:115], v[36:39]\n"
"ds_read_b64_tr_b16 a[250:251], v244 offset:29440\n"
"v_mfma_f32_16x16x32_bf16 v[8:11], a[132:135], v[116:119], v[8:11]\n"
"v_exp_f32 v5, v5\n"
"v_mfma_f32_16x16x32_bf16 v[8:11], a[136:139], v[120:123], v[8:11]\n"
"ds_read_b64_tr_b16 a[252:253], v245 offset:25344\n"
"v_cvt_pk_bf16_f32 v18, v4, v5\n"
"v_mfma_f32_16x16x32_bf16 v[8:11], a[140:143], v[124:127], v[8:11]\n"
"v_exp_f32 v6, v6\n"
"v_mfma_f32_16x16x32_bf16 v[12:15], a[144:147], v[112:115], v[36:39]\n"
"ds_read_b64_tr_b16 a[254:255], v245 offset:29440\n"
"v_mfma_f32_16x16x32_bf16 v[12:15], a[148:151], v[116:119], v[12:15]\n"
"v_exp_f32 v7, v7\n"
"v_mfma_f32_16x16x32_bf16 v[12:15], a[152:155], v[120:123], v[12:15]\n"
"v_cvt_pk_bf16_f32 v19, v6, v7\n"
"v_mfma_f32_16x16x32_bf16 v[12:15], a[156:159], v[124:127], v[12:15]\n"
"buffer_load_dwordx4 v247, s[12:15], s16 offen offset:3072 lds\n"
"v_mfma_f32_16x16x32_bf16 a[64:67], a[192:195], v[16:19], a[64:67]\n"
"s_add_u32 s16, s16, 16384\n"
"v_exp_f32 v8, v8\n"
"v_mfma_f32_16x16x32_bf16 a[68:71], a[196:199], v[16:19], a[68:71]\n"
"buffer_load_dword v250, v215, s[28:31], s46 offen offset:192\n"
"v_mfma_f32_16x16x32_bf16 a[72:75], a[200:203], v[16:19], a[72:75]\n"
"v_exp_f32 v9, v9\n"
"v_mfma_f32_16x16x32_bf16 a[76:79], a[204:207], v[16:19], a[76:79]\n"
"v_cvt_pk_bf16_f32 v20, v8, v9\n"
"v_mfma_f32_16x16x32_bf16 a[80:83], a[208:211], v[16:19], a[80:83]\n"
"v_exp_f32 v10, v10\n"
"v_mfma_f32_16x16x32_bf16 a[84:87], a[212:215], v[16:19], a[84:87]\n"
"s_nop 0\n"
"v_mfma_f32_16x16x32_bf16 a[88:91], a[216:219], v[16:19], a[88:91]\n"
"v_exp_f32 v11, v11\n"
"v_mfma_f32_16x16x32_bf16 a[92:95], a[220:223], v[16:19], a[92:95]\n"
"s_waitcnt vmcnt(18) lgkmcnt(0)\n"
"s_barrier\n"
"v_cvt_pk_bf16_f32 v21, v10, v11\n"
"v_mfma_f32_16x16x32_bf16 v[52:55], v[40:43], v[16:19], v[52:55]\n"
"v_exp_f32 v12, v12\n"
"v_mfma_f32_16x16x32_bf16 v[0:3], a[160:163], v[64:67], v[24:27]\n"
"ds_read_b128 a[128:131], v241 offset:32768\n"
"v_mfma_f32_16x16x32_bf16 v[0:3], a[164:167], v[68:71], v[0:3]\n"
"v_exp_f32 v13, v13\n"
"v_mfma_f32_16x16x32_bf16 v[0:3], a[168:171], v[72:75], v[0:3]\n"
"ds_read_b128 a[132:135], v241 offset:33024\n"
"v_cvt_pk_bf16_f32 v22, v12, v13\n"
"v_mfma_f32_16x16x32_bf16 v[0:3], a[172:175], v[76:79], v[0:3]\n"
"v_exp_f32 v14, v14\n"
"v_mfma_f32_16x16x32_bf16 v[4:7], a[176:179], v[64:67], v[24:27]\n"
"ds_read_b128 a[136:139], v241 offset:33280\n"
"v_mfma_f32_16x16x32_bf16 v[4:7], a[180:183], v[68:71], v[4:7]\n"
"v_exp_f32 v15, v15\n"
"v_mfma_f32_16x16x32_bf16 v[4:7], a[184:187], v[72:75], v[4:7]\n"
"s_add_u32 m0, s18, 65536\n"
"ds_read_b128 a[140:143], v241 offset:33536\n"
"v_cvt_pk_bf16_f32 v23, v14, v15\n"
"v_mfma_f32_16x16x32_bf16 v[4:7], a[188:191], v[76:79], v[4:7]\n"
"buffer_load_dwordx4 v246, s[8:11], s16 offen offset:0 lds\n"
"v_mfma_f32_16x16x32_bf16 a[96:99], a[192:195], v[20:23], a[96:99]\n"
"v_exp_f32 v0, v0\n"
"v_mfma_f32_16x16x32_bf16 a[100:103], a[196:199], v[20:23], a[100:103]\n"
"ds_read_b128 a[144:147], v241 offset:36864\n"
"v_mfma_f32_16x16x32_bf16 a[104:107], a[200:203], v[20:23], a[104:107]\n"
"v_exp_f32 v1, v1\n"
"v_mfma_f32_16x16x32_bf16 a[108:111], a[204:207], v[20:23], a[108:111]\n"
"ds_read_b128 a[148:151], v241 offset:37120\n"
"v_cvt_pk_bf16_f32 v16, v0, v1\n"
"v_mfma_f32_16x16x32_bf16 a[112:115], a[208:211], v[20:23], a[112:115]\n"
"v_exp_f32 v2, v2\n"
"v_mfma_f32_16x16x32_bf16 a[116:119], a[212:215], v[20:23], a[116:119]\n"
"ds_read_b128 a[152:155], v241 offset:37376\n"
"v_mfma_f32_16x16x32_bf16 a[120:123], a[216:219], v[20:23], a[120:123]\n"
"v_exp_f32 v3, v3\n"
"v_mfma_f32_16x16x32_bf16 a[124:127], a[220:223], v[20:23], a[124:127]\n"
"v_cvt_pk_bf16_f32 v17, v2, v3\n"
"v_mfma_f32_16x16x32_bf16 v[56:59], v[40:43], v[20:23], v[56:59]\n"
"v_exp_f32 v4, v4\n"
"v_mfma_f32_16x16x32_bf16 v[8:11], a[160:163], v[80:83], v[28:31]\n"
"ds_read_b128 a[156:159], v241 offset:37632\n"
"v_mfma_f32_16x16x32_bf16 v[8:11], a[164:167], v[84:87], v[8:11]\n"
"v_exp_f32 v5, v5\n"
"v_mfma_f32_16x16x32_bf16 v[8:11], a[168:171], v[88:91], v[8:11]\n"
"ds_read_b64_tr_b16 a[192:193], v244 offset:49152\n"
"v_cvt_pk_bf16_f32 v18, v4, v5\n"
"v_mfma_f32_16x16x32_bf16 v[8:11], a[172:175], v[92:95], v[8:11]\n"
"v_exp_f32 v6, v6\n"
"v_mfma_f32_16x16x32_bf16 v[12:15], a[176:179], v[80:83], v[28:31]\n"
"ds_read_b64_tr_b16 a[194:195], v244 offset:53248\n"
"v_mfma_f32_16x16x32_bf16 v[12:15], a[180:183], v[84:87], v[12:15]\n"
"v_exp_f32 v7, v7\n"
"v_mfma_f32_16x16x32_bf16 v[12:15], a[184:187], v[88:91], v[12:15]\n"
"ds_read_b64_tr_b16 a[196:197], v245 offset:49152\n"
"v_cvt_pk_bf16_f32 v19, v6, v7\n"
"v_mfma_f32_16x16x32_bf16 v[12:15], a[188:191], v[92:95], v[12:15]\n"
"s_waitcnt lgkmcnt(11)\n"
"buffer_load_dwordx4 v246, s[8:11], s16 offen offset:1024 lds\n"
"v_mfma_f32_16x16x32_bf16 a[0:3], a[224:227], v[16:19], a[0:3]\n"
"v_exp_f32 v8, v8\n"
"v_mfma_f32_16x16x32_bf16 a[4:7], a[228:231], v[16:19], a[4:7]\n"
"ds_read_b64_tr_b16 a[198:199], v245 offset:53248\n"
"v_mfma_f32_16x16x32_bf16 a[8:11], a[232:235], v[16:19], a[8:11]\n"
"v_exp_f32 v9, v9\n"
"v_mfma_f32_16x16x32_bf16 a[12:15], a[236:239], v[16:19], a[12:15]\n"
"ds_read_b64_tr_b16 a[200:201], v244 offset:49408\n"
"v_cvt_pk_bf16_f32 v20, v8, v9\n"
"v_mfma_f32_16x16x32_bf16 a[16:19], a[240:243], v[16:19], a[16:19]\n"
"v_exp_f32 v10, v10\n"
"v_mfma_f32_16x16x32_bf16 a[20:23], a[244:247], v[16:19], a[20:23]\n"
"ds_read_b64_tr_b16 a[202:203], v244 offset:53504\n"
"v_mfma_f32_16x16x32_bf16 a[24:27], a[248:251], v[16:19], a[24:27]\n"
"v_exp_f32 v11, v11\n"
"v_mfma_f32_16x16x32_bf16 a[28:31], a[252:255], v[16:19], a[28:31]\n"
"v_cvt_pk_bf16_f32 v21, v10, v11\n"
"v_mfma_f32_16x16x32_bf16 v[44:47], v[40:43], v[16:19], v[44:47]\n"
"v_exp_f32 v12, v12\n"
"v_mfma_f32_16x16x32_bf16 v[0:3], a[160:163], v[96:99], v[32:35]\n"
"ds_read_b64_tr_b16 a[204:205], v245 offset:49408\n"
"v_mfma_f32_16x16x32_bf16 v[0:3], a[164:167], v[100:103], v[0:3]\n"
"v_exp_f32 v13, v13\n"
"v_mfma_f32_16x16x32_bf16 v[0:3], a[168:171], v[104:107], v[0:3]\n"
"ds_read_b64_tr_b16 a[206:207], v245 offset:53504\n"
"v_cvt_pk_bf16_f32 v22, v12, v13\n"
"v_mfma_f32_16x16x32_bf16 v[0:3], a[172:175], v[108:111], v[0:3]\n"
"v_exp_f32 v14, v14\n"
"v_mfma_f32_16x16x32_bf16 v[4:7], a[176:179], v[96:99], v[32:35]\n"
"ds_read_b64_tr_b16 a[208:209], v244 offset:49664\n"
"v_mfma_f32_16x16x32_bf16 v[4:7], a[180:183], v[100:103], v[4:7]\n"
"v_exp_f32 v15, v15\n"
"v_mfma_f32_16x16x32_bf16 v[4:7], a[184:187], v[104:107], v[4:7]\n"
"ds_read_b64_tr_b16 a[210:211], v244 offset:53760\n"
"v_cvt_pk_bf16_f32 v23, v14, v15\n"
"v_mfma_f32_16x16x32_bf16 v[4:7], a[188:191], v[108:111], v[4:7]\n"
"buffer_load_dwordx4 v247, s[8:11], s16 offen offset:2048 lds\n"
"v_mfma_f32_16x16x32_bf16 a[32:35], a[224:227], v[20:23], a[32:35]\n"
"v_exp_f32 v0, v0\n"
"v_mfma_f32_16x16x32_bf16 a[36:39], a[228:231], v[20:23], a[36:39]\n"
"ds_read_b64_tr_b16 a[212:213], v245 offset:49664\n"
"v_mfma_f32_16x16x32_bf16 a[40:43], a[232:235], v[20:23], a[40:43]\n"
"v_exp_f32 v1, v1\n"
"v_mfma_f32_16x16x32_bf16 a[44:47], a[236:239], v[20:23], a[44:47]\n"
"ds_read_b64_tr_b16 a[214:215], v245 offset:53760\n"
"v_cvt_pk_bf16_f32 v16, v0, v1\n"
"v_mfma_f32_16x16x32_bf16 a[48:51], a[240:243], v[20:23], a[48:51]\n"
"v_exp_f32 v2, v2\n"
"v_mfma_f32_16x16x32_bf16 a[52:55], a[244:247], v[20:23], a[52:55]\n"
"ds_read_b64_tr_b16 a[216:217], v244 offset:49920\n"
"v_mfma_f32_16x16x32_bf16 a[56:59], a[248:251], v[20:23], a[56:59]\n"
"v_exp_f32 v3, v3\n"
"v_mfma_f32_16x16x32_bf16 a[60:63], a[252:255], v[20:23], a[60:63]\n"
"v_cvt_pk_bf16_f32 v17, v2, v3\n"
"v_mfma_f32_16x16x32_bf16 v[48:51], v[40:43], v[20:23], v[48:51]\n"
"v_exp_f32 v4, v4\n"
"v_mfma_f32_16x16x32_bf16 v[8:11], a[160:163], v[112:115], v[36:39]\n"
"ds_read_b64_tr_b16 a[218:219], v244 offset:54016\n"
"v_mfma_f32_16x16x32_bf16 v[8:11], a[164:167], v[116:119], v[8:11]\n"
"v_exp_f32 v5, v5\n"
"v_mfma_f32_16x16x32_bf16 v[8:11], a[168:171], v[120:123], v[8:11]\n"
"ds_read_b64_tr_b16 a[220:221], v245 offset:49920\n"
"v_cvt_pk_bf16_f32 v18, v4, v5\n"
"v_mfma_f32_16x16x32_bf16 v[8:11], a[172:175], v[124:127], v[8:11]\n"
"v_exp_f32 v6, v6\n"
"v_mfma_f32_16x16x32_bf16 v[12:15], a[176:179], v[112:115], v[36:39]\n"
"ds_read_b64_tr_b16 a[222:223], v245 offset:54016\n"
"v_mfma_f32_16x16x32_bf16 v[12:15], a[180:183], v[116:119], v[12:15]\n"
"v_exp_f32 v7, v7\n"
"v_mfma_f32_16x16x32_bf16 v[12:15], a[184:187], v[120:123], v[12:15]\n"
"v_cvt_pk_bf16_f32 v19, v6, v7\n"
"v_mfma_f32_16x16x32_bf16 v[12:15], a[188:191], v[124:127], v[12:15]\n"
"buffer_load_dwordx4 v247, s[8:11], s16 offen offset:3072 lds\n"
"v_mfma_f32_16x16x32_bf16 a[64:67], a[224:227], v[16:19], a[64:67]\n"
"v_exp_f32 v8, v8\n"
"v_mfma_f32_16x16x32_bf16 a[68:71], a[228:231], v[16:19], a[68:71]\n"
"buffer_load_dword v250, v214, s[20:23], s47 offen\n"
"v_mfma_f32_16x16x32_bf16 a[72:75], a[232:235], v[16:19], a[72:75]\n"
"v_exp_f32 v9, v9\n"
"v_mfma_f32_16x16x32_bf16 a[76:79], a[236:239], v[16:19], a[76:79]\n"
"v_cvt_pk_bf16_f32 v20, v8, v9\n"
"v_mfma_f32_16x16x32_bf16 a[80:83], a[240:243], v[16:19], a[80:83]\n"
"v_exp_f32 v10, v10\n"
"v_mfma_f32_16x16x32_bf16 a[84:87], a[244:247], v[16:19], a[84:87]\n"
"s_nop 0\n"
"v_mfma_f32_16x16x32_bf16 a[88:91], a[248:251], v[16:19], a[88:91]\n"
"v_exp_f32 v11, v11\n"
"v_mfma_f32_16x16x32_bf16 a[92:95], a[252:255], v[16:19], a[92:95]\n"
"s_waitcnt lgkmcnt(8)\n"
"v_cvt_pk_bf16_f32 v21, v10, v11\n"
"v_mfma_f32_16x16x32_bf16 v[52:55], v[40:43], v[16:19], v[52:55]\n"
"v_exp_f32 v12, v12\n"
"v_mfma_f32_16x16x32_bf16 v[0:3], a[128:131], v[64:67], v[24:27]\n"
"ds_read_b128 a[160:163], v241 offset:40960\n"
"v_mfma_f32_16x16x32_bf16 v[0:3], a[132:135], v[68:71], v[0:3]\n"
"v_exp_f32 v13, v13\n"
"v_mfma_f32_16x16x32_bf16 v[0:3], a[136:139], v[72:75], v[0:3]\n"
"ds_read_b128 a[164:167], v241 offset:41216\n"
"v_cvt_pk_bf16_f32 v22, v12, v13\n"
"v_mfma_f32_16x16x32_bf16 v[0:3], a[140:143], v[76:79], v[0:3]\n"
"v_exp_f32 v14, v14\n"
"v_mfma_f32_16x16x32_bf16 v[4:7], a[144:147], v[64:67], v[24:27]\n"
"ds_read_b128 a[168:171], v241 offset:41472\n"
"v_mfma_f32_16x16x32_bf16 v[4:7], a[148:151], v[68:71], v[4:7]\n"
"v_exp_f32 v15, v15\n"
"v_mfma_f32_16x16x32_bf16 v[4:7], a[152:155], v[72:75], v[4:7]\n"
"s_add_u32 m0, s18, 81920\n"
"ds_read_b128 a[172:175], v241 offset:41728\n"
"v_cvt_pk_bf16_f32 v23, v14, v15\n"
"v_mfma_f32_16x16x32_bf16 v[4:7], a[156:159], v[76:79], v[4:7]\n"
"buffer_load_dwordx4 v246, s[12:15], s16 offen offset:0 lds\n"
"v_mfma_f32_16x16x32_bf16 a[96:99], a[224:227], v[20:23], a[96:99]\n"
"v_exp_f32 v0, v0\n"
"v_mfma_f32_16x16x32_bf16 a[100:103], a[228:231], v[20:23], a[100:103]\n"
"ds_read_b128 a[176:179], v241 offset:45056\n"
"v_mfma_f32_16x16x32_bf16 a[104:107], a[232:235], v[20:23], a[104:107]\n"
"v_exp_f32 v1, v1\n"
"v_mfma_f32_16x16x32_bf16 a[108:111], a[236:239], v[20:23], a[108:111]\n"
"ds_read_b128 a[180:183], v241 offset:45312\n"
"v_cvt_pk_bf16_f32 v16, v0, v1\n"
"v_mfma_f32_16x16x32_bf16 a[112:115], a[240:243], v[20:23], a[112:115]\n"
"v_exp_f32 v2, v2\n"
"v_mfma_f32_16x16x32_bf16 a[116:119], a[244:247], v[20:23], a[116:119]\n"
"ds_read_b128 a[184:187], v241 offset:45568\n"
"v_mfma_f32_16x16x32_bf16 a[120:123], a[248:251], v[20:23], a[120:123]\n"
"v_exp_f32 v3, v3\n"
"v_mfma_f32_16x16x32_bf16 a[124:127], a[252:255], v[20:23], a[124:127]\n"
"v_cvt_pk_bf16_f32 v17, v2, v3\n"
"v_mfma_f32_16x16x32_bf16 v[56:59], v[40:43], v[20:23], v[56:59]\n"
"v_exp_f32 v4, v4\n"
"v_mfma_f32_16x16x32_bf16 v[8:11], a[128:131], v[80:83], v[28:31]\n"
"ds_read_b128 a[188:191], v241 offset:45824\n"
"v_mfma_f32_16x16x32_bf16 v[8:11], a[132:135], v[84:87], v[8:11]\n"
"v_exp_f32 v5, v5\n"
"v_mfma_f32_16x16x32_bf16 v[8:11], a[136:139], v[88:91], v[8:11]\n"
"ds_read_b64_tr_b16 a[224:225], v244 offset:57344\n"
"v_cvt_pk_bf16_f32 v18, v4, v5\n"
"v_mfma_f32_16x16x32_bf16 v[8:11], a[140:143], v[92:95], v[8:11]\n"
"v_exp_f32 v6, v6\n"
"v_mfma_f32_16x16x32_bf16 v[12:15], a[144:147], v[80:83], v[28:31]\n"
"ds_read_b64_tr_b16 a[226:227], v244 offset:61440\n"
"v_mfma_f32_16x16x32_bf16 v[12:15], a[148:151], v[84:87], v[12:15]\n"
"v_exp_f32 v7, v7\n"
"v_mfma_f32_16x16x32_bf16 v[12:15], a[152:155], v[88:91], v[12:15]\n"
"ds_read_b64_tr_b16 a[228:229], v245 offset:57344\n"
"v_cvt_pk_bf16_f32 v19, v6, v7\n"
"v_mfma_f32_16x16x32_bf16 v[12:15], a[156:159], v[92:95], v[12:15]\n"
"s_waitcnt lgkmcnt(11)\n"
"buffer_load_dwordx4 v247, s[12:15], s16 offen offset:1024 lds\n"
"v_mfma_f32_16x16x32_bf16 a[0:3], a[192:195], v[16:19], a[0:3]\n"
"v_exp_f32 v8, v8\n"
"v_mfma_f32_16x16x32_bf16 a[4:7], a[196:199], v[16:19], a[4:7]\n"
"ds_read_b64_tr_b16 a[230:231], v245 offset:61440\n"
"v_mfma_f32_16x16x32_bf16 a[8:11], a[200:203], v[16:19], a[8:11]\n"
"v_exp_f32 v9, v9\n"
"v_mfma_f32_16x16x32_bf16 a[12:15], a[204:207], v[16:19], a[12:15]\n"
"ds_read_b64_tr_b16 a[232:233], v244 offset:57600\n"
"v_cvt_pk_bf16_f32 v20, v8, v9\n"
"v_mfma_f32_16x16x32_bf16 a[16:19], a[208:211], v[16:19], a[16:19]\n"
"v_exp_f32 v10, v10\n"
"v_mfma_f32_16x16x32_bf16 a[20:23], a[212:215], v[16:19], a[20:23]\n"
"ds_read_b64_tr_b16 a[234:235], v244 offset:61696\n"
"v_mfma_f32_16x16x32_bf16 a[24:27], a[216:219], v[16:19], a[24:27]\n"
"v_exp_f32 v11, v11\n"
"v_mfma_f32_16x16x32_bf16 a[28:31], a[220:223], v[16:19], a[28:31]\n"
"v_cvt_pk_bf16_f32 v21, v10, v11\n"
"v_mfma_f32_16x16x32_bf16 v[44:47], v[40:43], v[16:19], v[44:47]\n"
"v_exp_f32 v12, v12\n"
"v_mfma_f32_16x16x32_bf16 v[0:3], a[128:131], v[96:99], v[32:35]\n"
"ds_read_b64_tr_b16 a[236:237], v245 offset:57600\n"
"v_mfma_f32_16x16x32_bf16 v[0:3], a[132:135], v[100:103], v[0:3]\n"
"v_exp_f32 v13, v13\n"
"v_mfma_f32_16x16x32_bf16 v[0:3], a[136:139], v[104:107], v[0:3]\n"
"ds_read_b64_tr_b16 a[238:239], v245 offset:61696\n"
"v_cvt_pk_bf16_f32 v22, v12, v13\n"
"v_mfma_f32_16x16x32_bf16 v[0:3], a[140:143], v[108:111], v[0:3]\n"
"v_exp_f32 v14, v14\n"
"v_mfma_f32_16x16x32_bf16 v[4:7], a[144:147], v[96:99], v[32:35]\n"
"ds_read_b64_tr_b16 a[240:241], v244 offset:57856\n"
"v_mfma_f32_16x16x32_bf16 v[4:7], a[148:151], v[100:103], v[4:7]\n"
"v_exp_f32 v15, v15\n"
"v_mfma_f32_16x16x32_bf16 v[4:7], a[152:155], v[104:107], v[4:7]\n"
"ds_read_b64_tr_b16 a[242:243], v244 offset:61952\n"
"v_cvt_pk_bf16_f32 v23, v14, v15\n"
"v_mfma_f32_16x16x32_bf16 v[4:7], a[156:159], v[108:111], v[4:7]\n"
"buffer_load_dwordx4 v246, s[12:15], s16 offen offset:2048 lds\n"
"v_mfma_f32_16x16x32_bf16 a[32:35], a[192:195], v[20:23], a[32:35]\n"
"v_exp_f32 v0, v0\n"
"v_mfma_f32_16x16x32_bf16 a[36:39], a[196:199], v[20:23], a[36:39]\n"
"ds_read_b64_tr_b16 a[244:245], v245 offset:57856\n"
"v_mfma_f32_16x16x32_bf16 a[40:43], a[200:203], v[20:23], a[40:43]\n"
"v_exp_f32 v1, v1\n"
"v_mfma_f32_16x16x32_bf16 a[44:47], a[204:207], v[20:23], a[44:47]\n"
"ds_read_b64_tr_b16 a[246:247], v245 offset:61952\n"
"v_cvt_pk_bf16_f32 v16, v0, v1\n"
"v_mfma_f32_16x16x32_bf16 a[48:51], a[208:211], v[20:23], a[48:51]\n"
"v_exp_f32 v2, v2\n"
"v_mfma_f32_16x16x32_bf16 a[52:55], a[212:215], v[20:23], a[52:55]\n"
"ds_read_b64_tr_b16 a[248:249], v244 offset:58112\n"
"v_mfma_f32_16x16x32_bf16 a[56:59], a[216:219], v[20:23], a[56:59]\n"
"v_exp_f32 v3, v3\n"
"v_mfma_f32_16x16x32_bf16 a[60:63], a[220:223], v[20:23], a[60:63]\n"
"v_cvt_pk_bf16_f32 v17, v2, v3\n"
"v_mfma_f32_16x16x32_bf16 v[48:51], v[40:43], v[20:23], v[48:51]\n"
"v_exp_f32 v4, v4\n"
"v_mfma_f32_16x16x32_bf16 v[8:11], a[128:131], v[112:115], v[36:39]\n"
"ds_read_b64_tr_b16 a[250:251], v244 offset:62208\n"
"v_mfma_f32_16x16x32_bf16 v[8:11], a[132:135], v[116:119], v[8:11]\n"
"v_exp_f32 v5, v5\n"
"v_mfma_f32_16x16x32_bf16 v[8:11], a[136:139], v[120:123], v[8:11]\n"
"ds_read_b64_tr_b16 a[252:253], v245 offset:58112\n"
"v_cvt_pk_bf16_f32 v18, v4, v5\n"
"v_mfma_f32_16x16x32_bf16 v[8:11], a[140:143], v[124:127], v[8:11]\n"
"v_exp_f32 v6, v6\n"
"v_mfma_f32_16x16x32_bf16 v[12:15], a[144:147], v[112:115], v[36:39]\n"
"ds_read_b64_tr_b16 a[254:255], v245 offset:62208\n"
"v_mfma_f32_16x16x32_bf16 v[12:15], a[148:151], v[116:119], v[12:15]\n"
"v_exp_f32 v7, v7\n"
"v_mfma_f32_16x16x32_bf16 v[12:15], a[152:155], v[120:123], v[12:15]\n"
"v_cvt_pk_bf16_f32 v19, v6, v7\n"
"v_mfma_f32_16x16x32_bf16 v[12:15], a[156:159], v[124:127], v[12:15]\n"
"buffer_load_dwordx4 v247, s[12:15], s16 offen offset:3072 lds\n"
"v_mfma_f32_16x16x32_bf16 a[64:67], a[192:195], v[16:19], a[64:67]\n"
"s_add_u32 s16, s16, 16384\n"
"v_exp_f32 v8, v8\n"
"v_mfma_f32_16x16x32_bf16 a[68:71], a[196:199], v[16:19], a[68:71]\n"
"buffer_load_dword v250, v214, s[24:27], s48 offen\n"
"v_mfma_f32_16x16x32_bf16 a[72:75], a[200:203], v[16:19], a[72:75]\n"
"v_exp_f32 v9, v9\n"
"v_mfma_f32_16x16x32_bf16 a[76:79], a[204:207], v[16:19], a[76:79]\n"
"v_cvt_pk_bf16_f32 v20, v8, v9\n"
"v_mfma_f32_16x16x32_bf16 a[80:83], a[208:211], v[16:19], a[80:83]\n"
"v_exp_f32 v10, v10\n"
"v_mfma_f32_16x16x32_bf16 a[84:87], a[212:215], v[16:19], a[84:87]\n"
"s_nop 0\n"
"v_mfma_f32_16x16x32_bf16 a[88:91], a[216:219], v[16:19], a[88:91]\n"
"v_exp_f32 v11, v11\n"
"v_mfma_f32_16x16x32_bf16 a[92:95], a[220:223], v[16:19], a[92:95]\n"
"s_waitcnt vmcnt(20) lgkmcnt(0)\n"
"s_barrier\n"
"v_cvt_pk_bf16_f32 v21, v10, v11\n"
"v_mfma_f32_16x16x32_bf16 v[52:55], v[40:43], v[16:19], v[52:55]\n"
"v_exp_f32 v12, v12\n"
"s_cmp_lg_u32 s56, 0\n"
"s_cbranch_scc1 AT_h3\n"
"s_mov_b32 s61, 0x80000\n"
"AT_h2:\n"
"v_xor_b32 v208, s17, v208\n"
"v_xor_b32 v209, s17, v209\n"
"v_xor_b32 v210, s17, v210\n"
"v_xor_b32 v211, s17, v211\n"
"v_or3_b32 v208, v208, v209, v210\n"
"v_or_b32 v208, v208, v211\n"
"v_cmp_ne_u32 vcc, 0, v208\n"
"s_sub_u32 s61, s61, 1\n"
"s_cbranch_vccz AT_h4\n"
"s_cmp_eq_u32 s61, 0\n"
"s_cbranch_scc1 AT_h4\n"
"s_sleep 2\n"
"buffer_load_dword v208, v212, s[36:39], s57 offen offset:4 sc1\n"
"buffer_load_dword v209, v212, s[36:39], s58 offen offset:4 sc1\n"
"buffer_load_dword v210, v212, s[36:39], s59 offen offset:4 sc1\n"
"buffer_load_dword v211, v212, s[36:39], s60 offen offset:4 sc1\n"
"s_waitcnt vmcnt(0)\n"
"s_branch AT_h2\n"
"AT_h4:\n"
"v_mov_b32 v208, s17\n"
"v_mov_b32 v209, 65536\n"
"s_mov_b64 exec, 1\n"
"global_store_dword v209, v208, s[78:79] sc1\n"
"s_mov_b64 exec, -1\n"
"AT_h3:\n"
"v_mfma_f32_16x16x32_bf16 v[0:3], a[160:163], v[64:67], v[24:27]\n"
"ds_read_b128 a[128:131], v240 offset:0\n"
"v_mfma_f32_16x16x32_bf16 v[0:3], a[164:167], v[68:71], v[0:3]\n"
"v_exp_f32 v13, v13\n"
"v_mfma_f32_16x16x32_bf16 v[0:3], a[168:171], v[72:75], v[0:3]\n"
"ds_read_b128 a[132:135], v240 offset:256\n"
"v_cvt_pk_bf16_f32 v22, v12, v13\n"
"v_mfma_f32_16x16x32_bf16 v[0:3], a[172:175], v[76:79], v[0:3]\n"
"v_exp_f32 v14, v14\n"
"v_mfma_f32_16x16x32_bf16 v[4:7], a[176:179], v[64:67], v[24:27]\n"
"ds_read_b128 a[136:139], v240 offset:512\n"
"v_mfma_f32_16x16x32_bf16 v[4:7], a[180:183], v[68:71], v[4:7]\n"
"v_exp_f32 v15, v15\n"
"v_mfma_f32_16x16x32_bf16 v[4:7], a[184:187], v[72:75], v[4:7]\n"
"s_add_u32 m0, s18, 98304\n"
"ds_read_b128 a[140:143], v240 offset:768\n"
"v_cvt_pk_bf16_f32 v23, v14, v15\n"
"v_mfma_f32_16x16x32_bf16 v[4:7], a[188:191], v[76:79], v[4:7]\n"
"buffer_load_dwordx4 v246, s[8:11], s16 offen offset:0 lds\n"
"v_mfma_f32_16x16x32_bf16 a[96:99], a[192:195], v[20:23], a[96:99]\n"
"v_exp_f32 v0, v0\n"
"v_mfma_f32_16x16x32_bf16 a[100:103], a[196:199], v[20:23], a[100:103]\n"
"ds_read_b128 a[144:147], v240 offset:4096\n"
"v_mfma_f32_16x16x32_bf16 a[104:107], a[200:203], v[20:23], a[104:107]\n"
"v_exp_f32 v1, v1\n"
"v_mfma_f32_16x16x32_bf16 a[108:111], a[204:207], v[20:23], a[108:111]\n"
"ds_read_b128 a[148:151], v240 offset:4352\n"
"v_cvt_pk_bf16_f32 v16, v0, v1\n"
"v_mfma_f32_16x16x32_bf16 a[112:115], a[208:211], v[20:23], a[112:115]\n"
"v_exp_f32 v2, v2\n"
"v_mfma_f32_16x16x32_bf16 a[116:119], a[212:215], v[20:23], a[116:119]\n"
"ds_read_b128 a[152:155], v240 offset:4608\n"
"v_mfma_f32_16x16x32_bf16 a[120:123], a[216:219], v[20:23], a[120:123]\n"
"v_exp_f32 v3, v3\n"
"v_mfma_f32_16x16x32_bf16 a[124:127], a[220:223], v[20:23], a[124:127]\n"
"v_cvt_pk_bf16_f32 v17, v2, v3\n"
"v_mfma_f32_16x16x32_bf16 v[56:59], v[40:43], v[20:23], v[56:59]\n"
"v_exp_f32 v4, v4\n"
"v_mfma_f32_16x16x32_bf16 v[8:11], a[160:163], v[80:83], v[28:31]\n"
"ds_read_b128 a[156:159], v240 offset:4864\n"
"v_mfma_f32_16x16x32_bf16 v[8:11], a[164:167], v[84:87], v[8:11]\n"
"v_exp_f32 v5, v5\n"
"v_mfma_f32_16x16x32_bf16 v[8:11], a[168:171], v[88:91], v[8:11]\n"
"ds_read_b64_tr_b16 a[192:193], v242 offset:16384\n"
"v_cvt_pk_bf16_f32 v18, v4, v5\n"
"v_mfma_f32_16x16x32_bf16 v[8:11], a[172:175], v[92:95], v[8:11]\n"
"v_exp_f32 v6, v6\n"
"v_mfma_f32_16x16x32_bf16 v[12:15], a[176:179], v[80:83], v[28:31]\n"
"ds_read_b64_tr_b16 a[194:195], v242 offset:20480\n"
"v_mfma_f32_16x16x32_bf16 v[12:15], a[180:183], v[84:87], v[12:15]\n"
"v_exp_f32 v7, v7\n"
"v_mfma_f32_16x16x32_bf16 v[12:15], a[184:187], v[88:91], v[12:15]\n"
"ds_read_b64_tr_b16 a[196:197], v243 offset:16384\n"
"v_cvt_pk_bf16_f32 v19, v6, v7\n"
"v_mfma_f32_16x16x32_bf16 v[12:15], a[188:191], v[92:95], v[12:15]\n"
"s_waitcnt lgkmcnt(11)\n"
"buffer_load_dwordx4 v246, s[8:11], s16 offen offset:1024 lds\n"
"v_mfma_f32_16x16x32_bf16 a[0:3], a[224:227], v[16:19], a[0:3]\n"
"v_exp_f32 v8, v8\n"
"v_mfma_f32_16x16x32_bf16 a[4:7], a[228:231], v[16:19], a[4:7]\n"
"ds_read_b64_tr_b16 a[198:199], v243 offset:20480\n"
"v_mfma_f32_16x16x32_bf16 a[8:11], a[232:235], v[16:19], a[8:11]\n"
"v_exp_f32 v9, v9\n"
"v_mfma_f32_16x16x32_bf16 a[12:15], a[236:239], v[16:19], a[12:15]\n"
"ds_read_b64_tr_b16 a[200:201], v242 offset:16640\n"
"v_cvt_pk_bf16_f32 v20, v8, v9\n"
"v_mfma_f32_16x16x32_bf16 a[16:19], a[240:243], v[16:19], a[16:19]\n"
"v_exp_f32 v10, v10\n"
"v_mfma_f32_16x16x32_bf16 a[20:23], a[244:247], v[16:19], a[20:23]\n"
"ds_read_b64_tr_b16 a[202:203], v242 offset:20736\n"
"v_mfma_f32_16x16x32_bf16 a[24:27], a[248:251], v[16:19], a[24:27]\n"
"v_exp_f32 v11, v11\n"
"v_mfma_f32_16x16x32_bf16 a[28:31], a[252:255], v[16:19], a[28:31]\n"
"v_cvt_pk_bf16_f32 v21, v10, v11\n"
"v_mfma_f32_16x16x32_bf16 v[44:47], v[40:43], v[16:19], v[44:47]\n"
"v_exp_f32 v12, v12\n"
"v_mfma_f32_16x16x32_bf16 v[0:3], a[160:163], v[96:99], v[32:35]\n"
"ds_read_b64_tr_b16 a[204:205], v243 offset:16640\n"
"v_mfma_f32_16x16x32_bf16 v[0:3], a[164:167], v[100:103], v[0:3]\n"
"v_exp_f32 v13, v13\n"
"v_mfma_f32_16x16x32_bf16 v[0:3], a[168:171], v[104:107], v[0:3]\n"
"ds_read_b64_tr_b16 a[206:207], v243 offset:20736\n"
"v_cvt_pk_bf16_f32 v22, v12, v13\n"
"v_mfma_f32_16x16x32_bf16 v[0:3], a[172:175], v[108:111], v[0:3]\n"
"v_exp_f32 v14, v14\n"
"v_mfma_f32_16x16x32_bf16 v[4:7], a[176:179], v[96:99], v[32:35]\n"
"ds_read_b64_tr_b16 a[208:209], v242 offset:16896\n"
"v_mfma_f32_16x16x32_bf16 v[4:7], a[180:183], v[100:103], v[4:7]\n"
"v_exp_f32 v15, v15\n"
"v_mfma_f32_16x16x32_bf16 v[4:7], a[184:187], v[104:107], v[4:7]\n"
"ds_read_b64_tr_b16 a[210:211], v242 offset:20992\n"
"v_cvt_pk_bf16_f32 v23, v14, v15\n"
"v_mfma_f32_16x16x32_bf16 v[4:7], a[188:191], v[108:111], v[4:7]\n"
"buffer_load_dwordx4 v247, s[8:11], s16 offen offset:2048 lds\n"
"v_mfma_f32_16x16x32_bf16 a[32:35], a[224:227], v[20:23], a[32:35]\n"
"v_exp_f32 v0, v0\n"
"v_mfma_f32_16x16x32_bf16 a[36:39], a[228:231], v[20:23], a[36:39]\n"
"ds_read_b64_tr_b16 a[212:213], v243 offset:16896\n"
"v_mfma_f32_16x16x32_bf16 a[40:43], a[232:235], v[20:23], a[40:43]\n"
"v_exp_f32 v1, v1\n"
"v_mfma_f32_16x16x32_bf16 a[44:47], a[236:239], v[20:23], a[44:47]\n"
"ds_read_b64_tr_b16 a[214:215], v243 offset:20992\n"
"v_cvt_pk_bf16_f32 v16, v0, v1\n"
"v_mfma_f32_16x16x32_bf16 a[48:51], a[240:243], v[20:23], a[48:51]\n"
"v_exp_f32 v2, v2\n"
"v_mfma_f32_16x16x32_bf16 a[52:55], a[244:247], v[20:23], a[52:55]\n"
"ds_read_b64_tr_b16 a[216:217], v242 offset:17152\n"
"v_mfma_f32_16x16x32_bf16 a[56:59], a[248:251], v[20:23], a[56:59]\n"
"v_exp_f32 v3, v3\n"
"v_mfma_f32_16x16x32_bf16 a[60:63], a[252:255], v[20:23], a[60:63]\n"
"v_cvt_pk_bf16_f32 v17, v2, v3\n"
"v_mfma_f32_16x16x32_bf16 v[48:51], v[40:43], v[20:23], v[48:51]\n"
"v_exp_f32 v4, v4\n"
"v_mfma_f32_16x16x32_bf16 v[8:11], a[160:163], v[112:115], v[36:39]\n"
"ds_read_b64_tr_b16 a[218:219], v242 offset:21248\n"
"v_mfma_f32_16x16x32_bf16 v[8:11], a[164:167], v[116:119], v[8:11]\n"
"v_exp_f32 v5, v5\n"
"v_mfma_f32_16x16x32_bf16 v[8:11], a[168:171], v[120:123], v[8:11]\n"
"ds_read_b64_tr_b16 a[220:221], v243 offset:17152\n"
"v_cvt_pk_bf16_f32 v18, v4, v5\n"
"v_mfma_f32_16x16x32_bf16 v[8:11], a[172:175], v[124:127], v[8:11]\n"
"v_exp_f32 v6, v6\n"
"v_mfma_f32_16x16x32_bf16 v[12:15], a[176:179], v[112:115], v[36:39]\n"
"ds_read_b64_tr_b16 a[222:223], v243 offset:21248\n"
"v_mfma_f32_16x16x32_bf16 v[12:15], a[180:183], v[116:119], v[12:15]\n"
"v_exp_f32 v7, v7\n"
"v_mfma_f32_16x16x32_bf16 v[12:15], a[184:187], v[120:123], v[12:15]\n"
"v_cvt_pk_bf16_f32 v19, v6, v7\n"
"v_mfma_f32_16x16x32_bf16 v[12:15], a[188:191], v[124:127], v[12:15]\n"
"buffer_load_dwordx4 v247, s[8:11], s16 offen offset:3072 lds\n"
"v_mfma_f32_16x16x32_bf16 a[64:67], a[224:227], v[16:19], a[64:67]\n"
"v_exp_f32 v8, v8\n"
"v_mfma_f32_16x16x32_bf16 a[68:71], a[228:231], v[16:19], a[68:71]\n"
"buffer_load_dword v250, v214, s[24:27], s49 offen\n"
"v_mfma_f32_16x16x32_bf16 a[72:75], a[232:235], v[16:19], a[72:75]\n"
"v_exp_f32 v9, v9\n"
"v_mfma_f32_16x16x32_bf16 a[76:79], a[236:239], v[16:19], a[76:79]\n"
"v_cvt_pk_bf16_f32 v20, v8, v9\n"
"v_mfma_f32_16x16x32_bf16 a[80:83], a[240:243], v[16:19], a[80:83]\n"
"v_exp_f32 v10, v10\n"
"v_mfma_f32_16x16x32_bf16 a[84:87], a[244:247], v[16:19], a[84:87]\n"
"s_nop 0\n"
"v_mfma_f32_16x16x32_bf16 a[88:91], a[248:251], v[16:19], a[88:91]\n"
"v_exp_f32 v11, v11\n"
"v_mfma_f32_16x16x32_bf16 a[92:95], a[252:255], v[16:19], a[92:95]\n"
"s_waitcnt lgkmcnt(8)\n"
"v_cvt_pk_bf16_f32 v21, v10, v11\n"
"v_mfma_f32_16x16x32_bf16 v[52:55], v[40:43], v[16:19], v[52:55]\n"
"v_exp_f32 v12, v12\n"
"v_mfma_f32_16x16x32_bf16 v[0:3], a[128:131], v[64:67], v[24:27]\n"
"ds_read_b128 a[160:163], v240 offset:8192\n"
"v_mfma_f32_16x16x32_bf16 v[0:3], a[132:135], v[68:71], v[0:3]\n"
"v_exp_f32 v13, v13\n"
"v_mfma_f32_16x16x32_bf16 v[0:3], a[136:139], v[72:75], v[0:3]\n"
"ds_read_b128 a[164:167], v240 offset:8448\n"
"v_cvt_pk_bf16_f32 v22, v12, v13\n"
"v_mfma_f32_16x16x32_bf16 v[0:3], a[140:143], v[76:79], v[0:3]\n"
"v_exp_f32 v14, v14\n"
"v_mfma_f32_16x16x32_bf16 v[4:7], a[144:147], v[64:67], v[24:27]\n"
"ds_read_b128 a[168:171], v240 offset:8704\n"
"v_mfma_f32_16x16x32_bf16 v[4:7], a[148:151], v[68:71], v[4:7]\n"
"v_exp_f32 v15, v15\n"
"v_mfma_f32_16x16x32_bf16 v[4:7], a[152:155], v[72:75], v[4:7]\n"
"s_add_u32 m0, s18, 114688\n"
"ds_read_b128 a[172:175], v240 offset:8960\n"
"v_cvt_pk_bf16_f32 v23, v14, v15\n"
"v_mfma_f32_16x16x32_bf16 v[4:7], a[156:159], v[76:79], v[4:7]\n"
"buffer_load_dwordx4 v246, s[12:15], s16 offen offset:0 lds\n"
"v_mfma_f32_16x16x32_bf16 a[96:99], a[224:227], v[20:23], a[96:99]\n"
"v_exp_f32 v0, v0\n"
"v_mfma_f32_16x16x32_bf16 a[100:103], a[228:231], v[20:23], a[100:103]\n"
"ds_read_b128 a[176:179], v240 offset:12288\n"
"v_mfma_f32_16x16x32_bf16 a[104:107], a[232:235], v[20:23], a[104:107]\n"
"v_exp_f32 v1, v1\n"
"v_mfma_f32_16x16x32_bf16 a[108:111], a[236:239], v[20:23], a[108:111]\n"
"ds_read_b128 a[180:183], v240 offset:12544\n"
"v_cvt_pk_bf16_f32 v16, v0, v1\n"
"v_mfma_f32_16x16x32_bf16 a[112:115], a[240:243], v[20:23], a[112:115]\n"
"v_exp_f32 v2, v2\n"
"v_mfma_f32_16x16x32_bf16 a[116:119], a[244:247], v[20:23], a[116:119]\n"
"ds_read_b128 a[184:187], v240 offset:12800\n"
"v_mfma_f32_16x16x32_bf16 a[120:123], a[248:251], v[20:23], a[120:123]\n"
"v_exp_f32 v3, v3\n"
"v_mfma_f32_16x16x32_bf16 a[124:127], a[252:255], v[20:23], a[124:127]\n"
"v_cvt_pk_bf16_f32 v17, v2, v3\n"
"v_mfma_f32_16x16x32_bf16 v[56:59], v[40:43], v[20:23], v[56:59]\n"
"v_exp_f32 v4, v4\n"
"v_mfma_f32_16x16x32_bf16 v[8:11], a[128:131], v[80:83], v[28:31]\n"
"ds_read_b128 a[188:191], v240 offset:13056\n"
"v_mfma_f32_16x16x32_bf16 v[8:11], a[132:135], v[84:87], v[8:11]\n"
"v_exp_f32 v5, v5\n"
"v_mfma_f32_16x16x32_bf16 v[8:11], a[136:139], v[88:91], v[8:11]\n"
"ds_read_b64_tr_b16 a[224:225], v242 offset:24576\n"
"v_cvt_pk_bf16_f32 v18, v4, v5\n"
"v_mfma_f32_16x16x32_bf16 v[8:11], a[140:143], v[92:95], v[8:11]\n"
"v_exp_f32 v6, v6\n"
"v_mfma_f32_16x16x32_bf16 v[12:15], a[144:147], v[80:83], v[28:31]\n"
"ds_read_b64_tr_b16 a[226:227], v242 offset:28672\n"
"v_mfma_f32_16x16x32_bf16 v[12:15], a[148:151], v[84:87], v[12:15]\n"
"v_exp_f32 v7, v7\n"
"v_mfma_f32_16x16x32_bf16 v[12:15], a[152:155], v[88:91], v[12:15]\n"
"ds_read_b64_tr_b16 a[228:229], v243 offset:24576\n"
"v_cvt_pk_bf16_f32 v19, v6, v7\n"
"v_mfma_f32_16x16x32_bf16 v[12:15], a[156:159], v[92:95], v[12:15]\n"
"s_waitcnt lgkmcnt(11)\n"
"buffer_load_dwordx4 v247, s[12:15], s16 offen offset:1024 lds\n"
"v_mfma_f32_16x16x32_bf16 a[0:3], a[192:195], v[16:19], a[0:3]\n"
"v_exp_f32 v8, v8\n"
"v_mfma_f32_16x16x32_bf16 a[4:7], a[196:199], v[16:19], a[4:7]\n"
"ds_read_b64_tr_b16 a[230:231], v243 offset:28672\n"
"v_mfma_f32_16x16x32_bf16 a[8:11], a[200:203], v[16:19], a[8:11]\n"
"v_exp_f32 v9, v9\n"
"v_mfma_f32_16x16x32_bf16 a[12:15], a[204:207], v[16:19], a[12:15]\n"
"ds_read_b64_tr_b16 a[232:233], v242 offset:24832\n"
"v_cvt_pk_bf16_f32 v20, v8, v9\n"
"v_mfma_f32_16x16x32_bf16 a[16:19], a[208:211], v[16:19], a[16:19]\n"
"v_exp_f32 v10, v10\n"
"v_mfma_f32_16x16x32_bf16 a[20:23], a[212:215], v[16:19], a[20:23]\n"
"ds_read_b64_tr_b16 a[234:235], v242 offset:28928\n"
"v_mfma_f32_16x16x32_bf16 a[24:27], a[216:219], v[16:19], a[24:27]\n"
"v_exp_f32 v11, v11\n"
"v_mfma_f32_16x16x32_bf16 a[28:31], a[220:223], v[16:19], a[28:31]\n"
"v_cvt_pk_bf16_f32 v21, v10, v11\n"
"v_mfma_f32_16x16x32_bf16 v[44:47], v[40:43], v[16:19], v[44:47]\n"
"v_exp_f32 v12, v12\n"
"v_mfma_f32_16x16x32_bf16 v[0:3], a[128:131], v[96:99], v[32:35]\n"
"ds_read_b64_tr_b16 a[236:237], v243 offset:24832\n"
"v_mfma_f32_16x16x32_bf16 v[0:3], a[132:135], v[100:103], v[0:3]\n"
"v_exp_f32 v13, v13\n"
"v_mfma_f32_16x16x32_bf16 v[0:3], a[136:139], v[104:107], v[0:3]\n"
"ds_read_b64_tr_b16 a[238:239], v243 offset:28928\n"
"v_cvt_pk_bf16_f32 v22, v12, v13\n"
"v_mfma_f32_16x16x32_bf16 v[0:3], a[140:143], v[108:111], v[0:3]\n"
"v_exp_f32 v14, v14\n"
"v_mfma_f32_16x16x32_bf16 v[4:7], a[144:147], v[96:99], v[32:35]\n"
"ds_read_b64_tr_b16 a[240:241], v242 offset:25088\n"
"v_mfma_f32_16x16x32_bf16 v[4:7], a[148:151], v[100:103], v[4:7]\n"
"v_exp_f32 v15, v15\n"
"v_mfma_f32_16x16x32_bf16 v[4:7], a[152:155], v[104:107], v[4:7]\n"
"ds_read_b64_tr_b16 a[242:243], v242 offset:29184\n"
"v_cvt_pk_bf16_f32 v23, v14, v15\n"
"v_mfma_f32_16x16x32_bf16 v[4:7], a[156:159], v[108:111], v[4:7]\n"
"buffer_load_dwordx4 v246, s[12:15], s16 offen offset:2048 lds\n"
"v_mfma_f32_16x16x32_bf16 a[32:35], a[192:195], v[20:23], a[32:35]\n"
"v_exp_f32 v0, v0\n"
"v_mfma_f32_16x16x32_bf16 a[36:39], a[196:199], v[20:23], a[36:39]\n"
"ds_read_b64_tr_b16 a[244:245], v243 offset:25088\n"
"v_mfma_f32_16x16x32_bf16 a[40:43], a[200:203], v[20:23], a[40:43]\n"
"v_exp_f32 v1, v1\n"
"v_mfma_f32_16x16x32_bf16 a[44:47], a[204:207], v[20:23], a[44:47]\n"
"ds_read_b64_tr_b16 a[246:247], v243 offset:29184\n"
"v_cvt_pk_bf16_f32 v16, v0, v1\n"
"v_mfma_f32_16x16x32_bf16 a[48:51], a[208:211], v[20:23], a[48:51]\n"
"v_exp_f32 v2, v2\n"
"v_mfma_f32_16x16x32_bf16 a[52:55], a[212:215], v[20:23], a[52:55]\n"
"ds_read_b64_tr_b16 a[248:249], v242 offset:25344\n"
"v_mfma_f32_16x16x32_bf16 a[56:59], a[216:219], v[20:23], a[56:59]\n"
"v_exp_f32 v3, v3\n"
"v_mfma_f32_16x16x32_bf16 a[60:63], a[220:223], v[20:23], a[60:63]\n"
"v_cvt_pk_bf16_f32 v17, v2, v3\n"
"v_mfma_f32_16x16x32_bf16 v[48:51], v[40:43], v[20:23], v[48:51]\n"
"v_exp_f32 v4, v4\n"
"v_mfma_f32_16x16x32_bf16 v[8:11], a[128:131], v[112:115], v[36:39]\n"
"ds_read_b64_tr_b16 a[250:251], v242 offset:29440\n"
"v_mfma_f32_16x16x32_bf16 v[8:11], a[132:135], v[116:119], v[8:11]\n"
"v_exp_f32 v5, v5\n"
"v_mfma_f32_16x16x32_bf16 v[8:11], a[136:139], v[120:123], v[8:11]\n"
"ds_read_b64_tr_b16 a[252:253], v243 offset:25344\n"
"v_cvt_pk_bf16_f32 v18, v4, v5\n"
"v_mfma_f32_16x16x32_bf16 v[8:11], a[140:143], v[124:127], v[8:11]\n"
"v_exp_f32 v6, v6\n"
"v_mfma_f32_16x16x32_bf16 v[12:15], a[144:147], v[112:115], v[36:39]\n"
"ds_read_b64_tr_b16 a[254:255], v243 offset:29440\n"
"v_mfma_f32_16x16x32_bf16 v[12:15], a[148:151], v[116:119], v[12:15]\n"
"v_exp_f32 v7, v7\n"
"v_mfma_f32_16x16x32_bf16 v[12:15], a[152:155], v[120:123], v[12:15]\n"
"v_cvt_pk_bf16_f32 v19, v6, v7\n"
"v_mfma_f32_16x16x32_bf16 v[12:15], a[156:159], v[124:127], v[12:15]\n"
"buffer_load_dwordx4 v247, s[12:15], s16 offen offset:3072 lds\n"
"v_mfma_f32_16x16x32_bf16 a[64:67], a[192:195], v[16:19], a[64:67]\n"
"s_add_u32 s16, s16, 16384\n"
"v_exp_f32 v8, v8\n"
"v_mfma_f32_16x16x32_bf16 a[68:71], a[196:199], v[16:19], a[68:71]\n"
"buffer_load_dword v250, v214, s[64:67], s48 offen\n"
"v_mfma_f32_16x16x32_bf16 a[72:75], a[200:203], v[16:19], a[72:75]\n"
"v_exp_f32 v9, v9\n"
"v_mfma_f32_16x16x32_bf16 a[76:79], a[204:207], v[16:19], a[76:79]\n"
"v_cvt_pk_bf16_f32 v20, v8, v9\n"
"v_mfma_f32_16x16x32_bf16 a[80:83], a[208:211], v[16:19], a[80:83]\n"
"v_exp_f32 v10, v10\n"
"v_mfma_f32_16x16x32_bf16 a[84:87], a[212:215], v[16:19], a[84:87]\n"
"s_nop 0\n"
"v_mfma_f32_16x16x32_bf16 a[88:91], a[216:219], v[16:19], a[88:91]\n"
"v_exp_f32 v11, v11\n"
"v_mfma_f32_16x16x32_bf16 a[92:95], a[220:223], v[16:19], a[92:95]\n"
"s_waitcnt vmcnt(21) lgkmcnt(0)\n"
"s_barrier\n"
"v_cvt_pk_bf16_f32 v21, v10, v11\n"
"v_mfma_f32_16x16x32_bf16 v[52:55], v[40:43], v[16:19], v[52:55]\n"
"v_exp_f32 v12, v12\n"
"v_mfma_f32_16x16x32_bf16 v[0:3], a[160:163], v[64:67], v[24:27]\n"
"ds_read_b128 a[128:131], v240 offset:32768\n"
"v_mfma_f32_16x16x32_bf16 v[0:3], a[164:167], v[68:71], v[0:3]\n"
"v_exp_f32 v13, v13\n"
"v_mfma_f32_16x16x32_bf16 v[0:3], a[168:171], v[72:75], v[0:3]\n"
"ds_read_b128 a[132:135], v240 offset:33024\n"
"v_cvt_pk_bf16_f32 v22, v12, v13\n"
"v_mfma_f32_16x16x32_bf16 v[0:3], a[172:175], v[76:79], v[0:3]\n"
"v_exp_f32 v14, v14\n"
"v_mfma_f32_16x16x32_bf16 v[4:7], a[176:179], v[64:67], v[24:27]\n"
"ds_read_b128 a[136:139], v240 offset:33280\n"
"v_mfma_f32_16x16x32_bf16 v[4:7], a[180:183], v[68:71], v[4:7]\n"
"v_exp_f32 v15, v15\n"
"v_mfma_f32_16x16x32_bf16 v[4:7], a[184:187], v[72:75], v[4:7]\n"
"s_mov_b32 m0, s53\n"
"ds_read_b128 a[140:143], v240 offset:33536\n"
"v_cvt_pk_bf16_f32 v23, v14, v15\n"
"v_mfma_f32_16x16x32_bf16 v[4:7], a[188:191], v[76:79], v[4:7]\n"
"buffer_load_dwordx4 v204, s[28:31], s52 offen offset:0 lds\n"
"v_mfma_f32_16x16x32_bf16 a[96:99], a[192:195], v[20:23], a[96:99]\n"
"s_add_u32 s52, s52, 8192\n"
"v_exp_f32 v0, v0\n"
"v_mfma_f32_16x16x32_bf16 a[100:103], a[196:199], v[20:23], a[100:103]\n"
"ds_read_b128 a[144:147], v240 offset:36864\n"
"v_mfma_f32_16x16x32_bf16 a[104:107], a[200:203], v[20:23], a[104:107]\n"
"v_exp_f32 v1, v1\n"
"v_mfma_f32_16x16x32_bf16 a[108:111], a[204:207], v[20:23], a[108:111]\n"
"ds_read_b128 a[148:151], v240 offset:37120\n"
"v_cvt_pk_bf16_f32 v16, v0, v1\n"
"v_mfma_f32_16x16x32_bf16 a[112:115], a[208:211], v[20:23], a[112:115]\n"
"v_exp_f32 v2, v2\n"
"v_mfma_f32_16x16x32_bf16 a[116:119], a[212:215], v[20:23], a[116:119]\n"
"ds_read_b128 a[152:155], v240 offset:37376\n"
"v_mfma_f32_16x16x32_bf16 a[120:123], a[216:219], v[20:23], a[120:123]\n"
"v_exp_f32 v3, v3\n"
"v_mfma_f32_16x16x32_bf16 a[124:127], a[220:223], v[20:23], a[124:127]\n"
"v_cvt_pk_bf16_f32 v17, v2, v3\n"
"v_mfma_f32_16x16x32_bf16 v[56:59], v[40:43], v[20:23], v[56:59]\n"
"v_exp_f32 v4, v4\n"
"v_mfma_f32_16x16x32_bf16 v[8:11], a[160:163], v[80:83], v[28:31]\n"
"ds_read_b128 a[156:159], v240 offset:37632\n"
"v_mfma_f32_16x16x32_bf16 v[8:11], a[164:167], v[84:87], v[8:11]\n"
"v_exp_f32 v5, v5\n"
"v_mfma_f32_16x16x32_bf16 v[8:11], a[168:171], v[88:91], v[8:11]\n"
"ds_read_b64_tr_b16 a[192:193], v242 offset:49152\n"
"v_cvt_pk_bf16_f32 v18, v4, v5\n"
"v_mfma_f32_16x16x32_bf16 v[8:11], a[172:175], v[92:95], v[8:11]\n"
"v_exp_f32 v6, v6\n"
"v_mfma_f32_16x16x32_bf16 v[12:15], a[176:179], v[80:83], v[28:31]\n"
"ds_read_b64_tr_b16 a[194:195], v242 offset:53248\n"
"v_mfma_f32_16x16x32_bf16 v[12:15], a[180:183], v[84:87], v[12:15]\n"
"v_exp_f32 v7, v7\n"
"v_mfma_f32_16x16x32_bf16 v[12:15], a[184:187], v[88:91], v[12:15]\n"
"ds_read_b64_tr_b16 a[196:197], v243 offset:49152\n"
"v_cvt_pk_bf16_f32 v19, v6, v7\n"
"v_mfma_f32_16x16x32_bf16 v[12:15], a[188:191], v[92:95], v[12:15]\n"
"s_waitcnt lgkmcnt(11)\n"
"buffer_load_dwordx4 v204, s[28:31], s52 offen offset:1024 lds\n"
"v_mfma_f32_16x16x32_bf16 a[0:3], a[224:227], v[16:19], a[0:3]\n"
"s_add_u32 s52, s52, 8192\n"
"v_exp_f32 v8, v8\n"
"v_mfma_f32_16x16x32_bf16 a[4:7], a[228:231], v[16:19], a[4:7]\n"
"ds_read_b64_tr_b16 a[198:199], v243 offset:53248\n"
"v_mfma_f32_16x16x32_bf16 a[8:11], a[232:235], v[16:19], a[8:11]\n"
"v_exp_f32 v9, v9\n"
"v_mfma_f32_16x16x32_bf16 a[12:15], a[236:239], v[16:19], a[12:15]\n"
"ds_read_b64_tr_b16 a[200:201], v242 offset:49408\n"
"v_cvt_pk_bf16_f32 v20, v8, v9\n"
"v_mfma_f32_16x16x32_bf16 a[16:19], a[240:243], v[16:19], a[16:19]\n"
"v_exp_f32 v10, v10\n"
"v_mfma_f32_16x16x32_bf16 a[20:23], a[244:247], v[16:19], a[20:23]\n"
"ds_read_b64_tr_b16 a[202:203], v242 offset:53504\n"
"v_mfma_f32_16x16x32_bf16 a[24:27], a[248:251], v[16:19], a[24:27]\n"
"v_exp_f32 v11, v11\n"
"v_mfma_f32_16x16x32_bf16 a[28:31], a[252:255], v[16:19], a[28:31]\n"
"v_cvt_pk_bf16_f32 v21, v10, v11\n"
"v_mfma_f32_16x16x32_bf16 v[44:47], v[40:43], v[16:19], v[44:47]\n"
"v_exp_f32 v12, v12\n"
"v_mfma_f32_16x16x32_bf16 v[0:3], a[160:163], v[96:99], v[32:35]\n"
"ds_read_b64_tr_b16 a[204:205], v243 offset:49408\n"
"v_mfma_f32_16x16x32_bf16 v[0:3], a[164:167], v[100:103], v[0:3]\n"
"v_exp_f32 v13, v13\n"
"v_mfma_f32_16x16x32_bf16 v[0:3], a[168:171], v[104:107], v[0:3]\n"
"ds_read_b64_tr_b16 a[206:207], v243 offset:53504\n"
"v_cvt_pk_bf16_f32 v22, v12, v13\n"
"v_mfma_f32_16x16x32_bf16 v[0:3], a[172:175], v[108:111], v[0:3]\n"
"v_exp_f32 v14, v14\n"
"v_mfma_f32_16x16x32_bf16 v[4:7], a[176:179], v[96:99], v[32:35]\n"
"ds_read_b64_tr_b16 a[208:209], v242 offset:49664\n"
"v_mfma_f32_16x16x32_bf16 v[4:7], a[180:183], v[100:103], v[4:7]\n"
"v_exp_f32 v15, v15\n"
"v_mfma_f32_16x16x32_bf16 v[4:7], a[184:187], v[104:107], v[4:7]\n"
"ds_read_b64_tr_b16 a[210:211], v242 offset:53760\n"
"v_cvt_pk_bf16_f32 v23, v14, v15\n"
"v_mfma_f32_16x16x32_bf16 v[4:7], a[188:191], v[108:111], v[4:7]\n"
"buffer_load_dwordx4 v204, s[28:31], s52 offen offset:2048 lds\n"
"v_mfma_f32_16x16x32_bf16 a[32:35], a[224:227], v[20:23], a[32:35]\n"
"s_add_u32 s52, s52, 8192\n"
"v_exp_f32 v0, v0\n"
"v_mfma_f32_16x16x32_bf16 a[36:39], a[228:231], v[20:23], a[36:39]\n"
"ds_read_b64_tr_b16 a[212:213], v243 offset:49664\n"
"v_mfma_f32_16x16x32_bf16 a[40:43], a[232:235], v[20:23], a[40:43]\n"
"v_exp_f32 v1, v1\n"
"v_mfma_f32_16x16x32_bf16 a[44:47], a[236:239], v[20:23], a[44:47]\n"
"ds_read_b64_tr_b16 a[214:215], v243 offset:53760\n"
"v_cvt_pk_bf16_f32 v16, v0, v1\n"
"v_mfma_f32_16x16x32_bf16 a[48:51], a[240:243], v[20:23], a[48:51]\n"
"v_exp_f32 v2, v2\n"
"v_mfma_f32_16x16x32_bf16 a[52:55], a[244:247], v[20:23], a[52:55]\n"
"ds_read_b64_tr_b16 a[216:217], v242 offset:49920\n"
"v_mfma_f32_16x16x32_bf16 a[56:59], a[248:251], v[20:23], a[56:59]\n"
"v_exp_f32 v3, v3\n"
"v_mfma_f32_16x16x32_bf16 a[60:63], a[252:255], v[20:23], a[60:63]\n"
"v_cvt_pk_bf16_f32 v17, v2, v3\n"
"v_mfma_f32_16x16x32_bf16 v[48:51], v[40:43], v[20:23], v[48:51]\n"
"v_exp_f32 v4, v4\n"
"v_mfma_f32_16x16x32_bf16 v[8:11], a[160:163], v[112:115], v[36:39]\n"
"ds_read_b64_tr_b16 a[218:219], v242 offset:54016\n"
"v_mfma_f32_16x16x32_bf16 v[8:11], a[164:167], v[116:119], v[8:11]\n"
"v_exp_f32 v5, v5\n"
"v_mfma_f32_16x16x32_bf16 v[8:11], a[168:171], v[120:123], v[8:11]\n"
"ds_read_b64_tr_b16 a[220:221], v243 offset:49920\n"
"v_cvt_pk_bf16_f32 v18, v4, v5\n"
"v_mfma_f32_16x16x32_bf16 v[8:11], a[172:175], v[124:127], v[8:11]\n"
"v_exp_f32 v6, v6\n"
"v_mfma_f32_16x16x32_bf16 v[12:15], a[176:179], v[112:115], v[36:39]\n"
"ds_read_b64_tr_b16 a[222:223], v243 offset:54016\n"
"v_mfma_f32_16x16x32_bf16 v[12:15], a[180:183], v[116:119], v[12:15]\n"
"v_exp_f32 v7, v7\n"
"v_mfma_f32_16x16x32_bf16 v[12:15], a[184:187], v[120:123], v[12:15]\n"
"v_cvt_pk_bf16_f32 v19, v6, v7\n"
"v_mfma_f32_16x16x32_bf16 v[12:15], a[188:191], v[124:127], v[12:15]\n"
"buffer_load_dwordx4 v204, s[28:31], s52 offen offset:3072 lds\n"
"v_mfma_f32_16x16x32_bf16 a[64:67], a[224:227], v[16:19], a[64:67]\n"
"s_add_u32 s52, s52, 12288\n"
"v_exp_f32 v8, v8\n"
"v_mfma_f32_16x16x32_bf16 a[68:71], a[228:231], v[16:19], a[68:71]\n"
"buffer_load_dword v250, v214, s[64:67], s49 offen\n"
"v_mfma_f32_16x16x32_bf16 a[72:75], a[232:235], v[16:19], a[72:75]\n"
"v_exp_f32 v9, v9\n"
"v_mfma_f32_16x16x32_bf16 a[76:79], a[236:239], v[16:19], a[76:79]\n"
"v_cvt_pk_bf16_f32 v20, v8, v9\n"
"v_mfma_f32_16x16x32_bf16 a[80:83], a[240:243], v[16:19], a[80:83]\n"
"v_exp_f32 v10, v10\n"
"v_mfma_f32_16x16x32_bf16 a[84:87], a[244:247], v[16:19], a[84:87]\n"
"s_nop 0\n"
"v_mfma_f32_16x16x32_bf16 a[88:91], a[248:251], v[16:19], a[88:91]\n"
"v_exp_f32 v11, v11\n"
"v_mfma_f32_16x16x32_bf16 a[92:95], a[252:255], v[16:19], a[92:95]\n"
"s_waitcnt lgkmcnt(8)\n"
"v_cvt_pk_bf16_f32 v21, v10, v11\n"
"v_mfma_f32_16x16x32_bf16 v[52:55], v[40:43], v[16:19], v[52:55]\n"
"v_exp_f32 v12, v12\n"
"v_mfma_f32_16x16x32_bf16 v[0:3], a[128:131], v[64:67], v[24:27]\n"
"ds_read_b128 a[160:163], v240 offset:40960\n"
"v_mfma_f32_16x16x32_bf16 v[0:3], a[132:135], v[68:71], v[0:3]\n"
"v_exp_f32 v13, v13\n"
"v_mfma_f32_16x16x32_bf16 v[0:3], a[136:139], v[72:75], v[0:3]\n"
"ds_read_b128 a[164:167], v240 offset:41216\n"
"v_cvt_pk_bf16_f32 v22, v12, v13\n"
"v_mfma_f32_16x16x32_bf16 v[0:3], a[140:143], v[76:79], v[0:3]\n"
"v_exp_f32 v14, v14\n"
"v_mfma_f32_16x16x32_bf16 v[4:7], a[144:147], v[64:67], v[24:27]\n"
"ds_read_b128 a[168:171], v240 offset:41472\n"
"v_mfma_f32_16x16x32_bf16 v[4:7], a[148:151], v[68:71], v[4:7]\n"
"v_exp_f32 v15, v15\n"
"v_mfma_f32_16x16x32_bf16 v[4:7], a[152:155], v[72:75], v[4:7]\n"
"s_add_u32 m0, s53, 4096\n"
"ds_read_b128 a[172:175], v240 offset:41728\n"
"v_cvt_pk_bf16_f32 v23, v14, v15\n"
"v_mfma_f32_16x16x32_bf16 v[4:7], a[156:159], v[76:79], v[4:7]\n"
"buffer_load_dwordx4 v204, s[28:31], s52 offen offset:0 lds\n"
"v_mfma_f32_16x16x32_bf16 a[96:99], a[224:227], v[20:23], a[96:99]\n"
"s_add_u32 s52, s52, 8192\n"
"v_exp_f32 v0, v0\n"
"v_mfma_f32_16x16x32_bf16 a[100:103], a[228:231], v[20:23], a[100:103]\n"
"ds_read_b128 a[176:179], v240 offset:45056\n"
"v_mfma_f32_16x16x32_bf16 a[104:107], a[232:235], v[20:23], a[104:107]\n"
"v_exp_f32 v1, v1\n"
"v_mfma_f32_16x16x32_bf16 a[108:111], a[236:239], v[20:23], a[108:111]\n"
"ds_read_b128 a[180:183], v240 offset:45312\n"
"v_cvt_pk_bf16_f32 v16, v0, v1\n"
"v_mfma_f32_16x16x32_bf16 a[112:115], a[240:243], v[20:23], a[112:115]\n"
"v_exp_f32 v2, v2\n"
"v_mfma_f32_16x16x32_bf16 a[116:119], a[244:247], v[20:23], a[116:119]\n"
"ds_read_b128 a[184:187], v240 offset:45568\n"
"v_mfma_f32_16x16x32_bf16 a[120:123], a[248:251], v[20:23], a[120:123]\n"
"v_exp_f32 v3, v3\n"
"v_mfma_f32_16x16x32_bf16 a[124:127], a[252:255], v[20:23], a[124:127]\n"
"v_cvt_pk_bf16_f32 v17, v2, v3\n"
"v_mfma_f32_16x16x32_bf16 v[56:59], v[40:43], v[20:23], v[56:59]\n"
"v_exp_f32 v4, v4\n"
"v_mfma_f32_16x16x32_bf16 v[8:11], a[128:131], v[80:83], v[28:31]\n"
"ds_read_b128 a[188:191], v240 offset:45824\n"
"v_mfma_f32_16x16x32_bf16 v[8:11], a[132:135], v[84:87], v[8:11]\n"
"v_exp_f32 v5, v5\n"
"v_mfma_f32_16x16x32_bf16 v[8:11], a[136:139], v[88:91], v[8:11]\n"
"ds_read_b64_tr_b16 a[224:225], v242 offset:57344\n"
"v_cvt_pk_bf16_f32 v18, v4, v5\n"
"v_mfma_f32_16x16x32_bf16 v[8:11], a[140:143], v[92:95], v[8:11]\n"
"v_exp_f32 v6, v6\n"
"v_mfma_f32_16x16x32_bf16 v[12:15], a[144:147], v[80:83], v[28:31]\n"
"ds_read_b64_tr_b16 a[226:227], v242 offset:61440\n"
"v_mfma_f32_16x16x32_bf16 v[12:15], a[148:151], v[84:87], v[12:15]\n"
"v_exp_f32 v7, v7\n"
"v_mfma_f32_16x16x32_bf16 v[12:15], a[152:155], v[88:91], v[12:15]\n"
"ds_read_b64_tr_b16 a[228:229], v243 offset:57344\n"
"v_cvt_pk_bf16_f32 v19, v6, v7\n"
"v_mfma_f32_16x16x32_bf16 v[12:15], a[156:159], v[92:95], v[12:15]\n"
"s_waitcnt lgkmcnt(11)\n"
"buffer_load_dwordx4 v204, s[28:31], s52 offen offset:1024 lds\n"
"v_mfma_f32_16x16x32_bf16 a[0:3], a[192:195], v[16:19], a[0:3]\n"
"s_add_u32 s52, s52, 8192\n"
"v_exp_f32 v8, v8\n"
"v_mfma_f32_16x16x32_bf16 a[4:7], a[196:199], v[16:19], a[4:7]\n"
"ds_read_b64_tr_b16 a[230:231], v243 offset:61440\n"
"v_mfma_f32_16x16x32_bf16 a[8:11], a[200:203], v[16:19], a[8:11]\n"
"v_exp_f32 v9, v9\n"
"v_mfma_f32_16x16x32_bf16 a[12:15], a[204:207], v[16:19], a[12:15]\n"
"ds_read_b64_tr_b16 a[232:233], v242 offset:57600\n"
"v_cvt_pk_bf16_f32 v20, v8, v9\n"
"v_mfma_f32_16x16x32_bf16 a[16:19], a[208:211], v[16:19], a[16:19]\n"
"v_exp_f32 v10, v10\n"
"v_mfma_f32_16x16x32_bf16 a[20:23], a[212:215], v[16:19], a[20:23]\n"
"ds_read_b64_tr_b16 a[234:235], v242 offset:61696\n"
"v_mfma_f32_16x16x32_bf16 a[24:27], a[216:219], v[16:19], a[24:27]\n"
"v_exp_f32 v11, v11\n"
"v_mfma_f32_16x16x32_bf16 a[28:31], a[220:223], v[16:19], a[28:31]\n"
"v_cvt_pk_bf16_f32 v21, v10, v11\n"
"v_mfma_f32_16x16x32_bf16 v[44:47], v[40:43], v[16:19], v[44:47]\n"
"v_exp_f32 v12, v12\n"
"v_mfma_f32_16x16x32_bf16 v[0:3], a[128:131], v[96:99], v[32:35]\n"
"ds_read_b64_tr_b16 a[236:237], v243 offset:57600\n"
"v_mfma_f32_16x16x32_bf16 v[0:3], a[132:135], v[100:103], v[0:3]\n"
"v_exp_f32 v13, v13\n"
"v_mfma_f32_16x16x32_bf16 v[0:3], a[136:139], v[104:107], v[0:3]\n"
"ds_read_b64_tr_b16 a[238:239], v243 offset:61696\n"
"v_cvt_pk_bf16_f32 v22, v12, v13\n"
"v_mfma_f32_16x16x32_bf16 v[0:3], a[140:143], v[108:111], v[0:3]\n"
"v_exp_f32 v14, v14\n"
"v_mfma_f32_16x16x32_bf16 v[4:7], a[144:147], v[96:99], v[32:35]\n"
"ds_read_b64_tr_b16 a[240:241], v242 offset:57856\n"
"v_mfma_f32_16x16x32_bf16 v[4:7], a[148:151], v[100:103], v[4:7]\n"
"v_exp_f32 v15, v15\n"
"v_mfma_f32_16x16x32_bf16 v[4:7], a[152:155], v[104:107], v[4:7]\n"
"ds_read_b64_tr_b16 a[242:243], v242 offset:61952\n"
"v_cvt_pk_bf16_f32 v23, v14, v15\n"
"v_mfma_f32_16x16x32_bf16 v[4:7], a[156:159], v[108:111], v[4:7]\n"
"buffer_load_dwordx4 v204, s[28:31], s52 offen offset:2048 lds\n"
"v_mfma_f32_16x16x32_bf16 a[32:35], a[192:195], v[20:23], a[32:35]\n"
"s_add_u32 s52, s52, 8192\n"
"v_exp_f32 v0, v0\n"
"v_mfma_f32_16x16x32_bf16 a[36:39], a[196:199], v[20:23], a[36:39]\n"
"ds_read_b64_tr_b16 a[244:245], v243 offset:57856\n"
"v_mfma_f32_16x16x32_bf16 a[40:43], a[200:203], v[20:23], a[40:43]\n"
"v_exp_f32 v1, v1\n"
"v_mfma_f32_16x16x32_bf16 a[44:47], a[204:207], v[20:23], a[44:47]\n"
"ds_read_b64_tr_b16 a[246:247], v243 offset:61952\n"
"v_cvt_pk_bf16_f32 v16, v0, v1\n"
"v_mfma_f32_16x16x32_bf16 a[48:51], a[208:211], v[20:23], a[48:51]\n"
"v_exp_f32 v2, v2\n"
"v_mfma_f32_16x16x32_bf16 a[52:55], a[212:215], v[20:23], a[52:55]\n"
"ds_read_b64_tr_b16 a[248:249], v242 offset:58112\n"
"v_mfma_f32_16x16x32_bf16 a[56:59], a[216:219], v[20:23], a[56:59]\n"
"v_exp_f32 v3, v3\n"
"v_mfma_f32_16x16x32_bf16 a[60:63], a[220:223], v[20:23], a[60:63]\n"
"v_cvt_pk_bf16_f32 v17, v2, v3\n"
"v_mfma_f32_16x16x32_bf16 v[48:51], v[40:43], v[20:23], v[48:51]\n"
"v_exp_f32 v4, v4\n"
"v_mfma_f32_16x16x32_bf16 v[8:11], a[128:131], v[112:115], v[36:39]\n"
"ds_read_b64_tr_b16 a[250:251], v242 offset:62208\n"
"v_mfma_f32_16x16x32_bf16 v[8:11], a[132:135], v[116:119], v[8:11]\n"
"v_exp_f32 v5, v5\n"
"v_mfma_f32_16x16x32_bf16 v[8:11], a[136:139], v[120:123], v[8:11]\n"
"ds_read_b64_tr_b16 a[252:253], v243 offset:58112\n"
"v_cvt_pk_bf16_f32 v18, v4, v5\n"
"v_mfma_f32_16x16x32_bf16 v[8:11], a[140:143], v[124:127], v[8:11]\n"
"v_exp_f32 v6, v6\n"
"v_mfma_f32_16x16x32_bf16 v[12:15], a[144:147], v[112:115], v[36:39]\n"
"ds_read_b64_tr_b16 a[254:255], v243 offset:62208\n"
"v_mfma_f32_16x16x32_bf16 v[12:15], a[148:151], v[116:119], v[12:15]\n"
"v_exp_f32 v7, v7\n"
"v_mfma_f32_16x16x32_bf16 v[12:15], a[152:155], v[120:123], v[12:15]\n"
"v_cvt_pk_bf16_f32 v19, v6, v7\n"
"v_mfma_f32_16x16x32_bf16 v[12:15], a[156:159], v[124:127], v[12:15]\n"
"buffer_load_dwordx4 v204, s[28:31], s52 offen offset:3072 lds\n"
"v_mfma_f32_16x16x32_bf16 a[64:67], a[192:195], v[16:19], a[64:67]\n"
"s_sub_u32 s52, s52, 61312\n"
"v_exp_f32 v8, v8\n"
"v_mfma_f32_16x16x32_bf16 a[68:71], a[196:199], v[16:19], a[68:71]\n"
"buffer_load_dword v250, v214, s[68:71], s48 offen\n"
"v_mfma_f32_16x16x32_bf16 a[72:75], a[200:203], v[16:19], a[72:75]\n"
"v_exp_f32 v9, v9\n"
"v_mfma_f32_16x16x32_bf16 a[76:79], a[204:207], v[16:19], a[76:79]\n"
"v_cvt_pk_bf16_f32 v20, v8, v9\n"
"v_mfma_f32_16x16x32_bf16 a[80:83], a[208:211], v[16:19], a[80:83]\n"
"v_exp_f32 v10, v10\n"
"v_mfma_f32_16x16x32_bf16 a[84:87], a[212:215], v[16:19], a[84:87]\n"
"s_nop 0\n"
"v_mfma_f32_16x16x32_bf16 a[88:91], a[216:219], v[16:19], a[88:91]\n"
"v_exp_f32 v11, v11\n"
"v_mfma_f32_16x16x32_bf16 a[92:95], a[220:223], v[16:19], a[92:95]\n"
"s_waitcnt vmcnt(21) lgkmcnt(0)\n"
"s_barrier\n"
"v_cvt_pk_bf16_f32 v21, v10, v11\n"
"v_mfma_f32_16x16x32_bf16 v[52:55], v[40:43], v[16:19], v[52:55]\n"
"v_exp_f32 v12, v12\n"
"v_mfma_f32_16x16x32_bf16 v[0:3], a[160:163], v[64:67], v[24:27]\n"
"ds_read_b128 a[128:131], v241 offset:0\n"
"v_mfma_f32_16x16x32_bf16 v[0:3], a[164:167], v[68:71], v[0:3]\n"
"v_exp_f32 v13, v13\n"
"v_mfma_f32_16x16x32_bf16 v[0:3], a[168:171], v[72:75], v[0:3]\n"
"ds_read_b128 a[132:135], v241 offset:256\n"
"v_cvt_pk_bf16_f32 v22, v12, v13\n"
"v_mfma_f32_16x16x32_bf16 v[0:3], a[172:175], v[76:79], v[0:3]\n"
"v_exp_f32 v14, v14\n"
"v_mfma_f32_16x16x32_bf16 v[4:7], a[176:179], v[64:67], v[24:27]\n"
"ds_read_b128 a[136:139], v241 offset:512\n"
"v_mfma_f32_16x16x32_bf16 v[4:7], a[180:183], v[68:71], v[4:7]\n"
"v_exp_f32 v15, v15\n"
"v_mfma_f32_16x16x32_bf16 v[4:7], a[184:187], v[72:75], v[4:7]\n"
"s_add_u32 m0, s53, 32768\n"
"ds_read_b128 a[140:143], v241 offset:768\n"
"v_cvt_pk_bf16_f32 v23, v14, v15\n"
"v_mfma_f32_16x16x32_bf16 v[4:7], a[188:191], v[76:79], v[4:7]\n"
"buffer_load_dwordx4 v204, s[28:31], s52 offen offset:0 lds\n"
"v_mfma_f32_16x16x32_bf16 a[96:99], a[192:195], v[20:23], a[96:99]\n"
"s_add_u32 s52, s52, 8192\n"
"v_exp_f32 v0, v0\n"
"v_mfma_f32_16x16x32_bf16 a[100:103], a[196:199], v[20:23], a[100:103]\n"
"ds_read_b128 a[144:147], v241 offset:4096\n"
"v_mfma_f32_16x16x32_bf16 a[104:107], a[200:203], v[20:23], a[104:107]\n"
"v_exp_f32 v1, v1\n"
"v_mfma_f32_16x16x32_bf16 a[108:111], a[204:207], v[20:23], a[108:111]\n"
"ds_read_b128 a[148:151], v241 offset:4352\n"
"v_cvt_pk_bf16_f32 v16, v0, v1\n"
"v_mfma_f32_16x16x32_bf16 a[112:115], a[208:211], v[20:23], a[112:115]\n"
"v_exp_f32 v2, v2\n"
"v_mfma_f32_16x16x32_bf16 a[116:119], a[212:215], v[20:23], a[116:119]\n"
"ds_read_b128 a[152:155], v241 offset:4608\n"
"v_mfma_f32_16x16x32_bf16 a[120:123], a[216:219], v[20:23], a[120:123]\n"
"v_exp_f32 v3, v3\n"
"v_mfma_f32_16x16x32_bf16 a[124:127], a[220:223], v[20:23], a[124:127]\n"
"v_cvt_pk_bf16_f32 v17, v2, v3\n"
"v_mfma_f32_16x16x32_bf16 v[56:59], v[40:43], v[20:23], v[56:59]\n"
"v_exp_f32 v4, v4\n"
"v_mfma_f32_16x16x32_bf16 v[8:11], a[160:163], v[80:83], v[28:31]\n"
"ds_read_b128 a[156:159], v241 offset:4864\n"
"v_mfma_f32_16x16x32_bf16 v[8:11], a[164:167], v[84:87], v[8:11]\n"
"v_exp_f32 v5, v5\n"
"v_mfma_f32_16x16x32_bf16 v[8:11], a[168:171], v[88:91], v[8:11]\n"
"ds_read_b64_tr_b16 a[192:193], v244 offset:16384\n"
"v_cvt_pk_bf16_f32 v18, v4, v5\n"
"v_mfma_f32_16x16x32_bf16 v[8:11], a[172:175], v[92:95], v[8:11]\n"
"v_exp_f32 v6, v6\n"
"v_mfma_f32_16x16x32_bf16 v[12:15], a[176:179], v[80:83], v[28:31]\n"
"ds_read_b64_tr_b16 a[194:195], v244 offset:20480\n"
"v_mfma_f32_16x16x32_bf16 v[12:15], a[180:183], v[84:87], v[12:15]\n"
"v_exp_f32 v7, v7\n"
"v_mfma_f32_16x16x32_bf16 v[12:15], a[184:187], v[88:91], v[12:15]\n"
"ds_read_b64_tr_b16 a[196:197], v245 offset:16384\n"
"v_cvt_pk_bf16_f32 v19, v6, v7\n"
"v_mfma_f32_16x16x32_bf16 v[12:15], a[188:191], v[92:95], v[12:15]\n"
"s_waitcnt lgkmcnt(11)\n"
"buffer_load_dwordx4 v204, s[28:31], s52 offen offset:1024 lds\n"
"v_mfma_f32_16x16x32_bf16 a[0:3], a[224:227], v[16:19], a[0:3]\n"
"s_add_u32 s52, s52, 8192\n"
"v_exp_f32 v8, v8\n"
"v_mfma_f32_16x16x32_bf16 a[4:7], a[228:231], v[16:19], a[4:7]\n"
"ds_read_b64_tr_b16 a[198:199], v245 offset:20480\n"
"v_mfma_f32_16x16x32_bf16 a[8:11], a[232:235], v[16:19], a[8:11]\n"
"v_exp_f32 v9, v9\n"
"v_mfma_f32_16x16x32_bf16 a[12:15], a[236:239], v[16:19], a[12:15]\n"
"ds_read_b64_tr_b16 a[200:201], v244 offset:16640\n"
"v_cvt_pk_bf16_f32 v20, v8, v9\n"
"v_mfma_f32_16x16x32_bf16 a[16:19], a[240:243], v[16:19], a[16:19]\n"
"v_exp_f32 v10, v10\n"
"v_mfma_f32_16x16x32_bf16 a[20:23], a[244:247], v[16:19], a[20:23]\n"
"ds_read_b64_tr_b16 a[202:203], v244 offset:20736\n"
"v_mfma_f32_16x16x32_bf16 a[24:27], a[248:251], v[16:19], a[24:27]\n"
"v_exp_f32 v11, v11\n"
"v_mfma_f32_16x16x32_bf16 a[28:31], a[252:255], v[16:19], a[28:31]\n"
"v_cvt_pk_bf16_f32 v21, v10, v11\n"
"v_mfma_f32_16x16x32_bf16 v[44:47], v[40:43], v[16:19], v[44:47]\n"
"v_exp_f32 v12, v12\n"
"v_mfma_f32_16x16x32_bf16 v[0:3], a[160:163], v[96:99], v[32:35]\n"
"ds_read_b64_tr_b16 a[204:205], v245 offset:16640\n"
"v_mfma_f32_16x16x32_bf16 v[0:3], a[164:167], v[100:103], v[0:3]\n"
"v_exp_f32 v13, v13\n"
"v_mfma_f32_16x16x32_bf16 v[0:3], a[168:171], v[104:107], v[0:3]\n"
"ds_read_b64_tr_b16 a[206:207], v245 offset:20736\n"
"v_cvt_pk_bf16_f32 v22, v12, v13\n"
"v_mfma_f32_16x16x32_bf16 v[0:3], a[172:175], v[108:111], v[0:3]\n"
"v_exp_f32 v14, v14\n"
"v_mfma_f32_16x16x32_bf16 v[4:7], a[176:179], v[96:99], v[32:35]\n"
"ds_read_b64_tr_b16 a[208:209], v244 offset:16896\n"
"v_mfma_f32_16x16x32_bf16 v[4:7], a[180:183], v[100:103], v[4:7]\n"
"v_exp_f32 v15, v15\n"
"v_mfma_f32_16x16x32_bf16 v[4:7], a[184:187], v[104:107], v[4:7]\n"
"ds_read_b64_tr_b16 a[210:211], v244 offset:20992\n"
"v_cvt_pk_bf16_f32 v23, v14, v15\n"
"v_mfma_f32_16x16x32_bf16 v[4:7], a[188:191], v[108:111], v[4:7]\n"
"buffer_load_dwordx4 v204, s[28:31], s52 offen offset:2048 lds\n"
"v_mfma_f32_16x16x32_bf16 a[32:35], a[224:227], v[20:23], a[32:35]\n"
"s_add_u32 s52, s52, 8192\n"
"v_exp_f32 v0, v0\n"
"v_mfma_f32_16x16x32_bf16 a[36:39], a[228:231], v[20:23], a[36:39]\n"
"ds_read_b64_tr_b16 a[212:213], v245 offset:16896\n"
"v_mfma_f32_16x16x32_bf16 a[40:43], a[232:235], v[20:23], a[40:43]\n"
"v_exp_f32 v1, v1\n"
"v_mfma_f32_16x16x32_bf16 a[44:47], a[236:239], v[20:23], a[44:47]\n"
"ds_read_b64_tr_b16 a[214:215], v245 offset:20992\n"
"v_cvt_pk_bf16_f32 v16, v0, v1\n"
"v_mfma_f32_16x16x32_bf16 a[48:51], a[240:243], v[20:23], a[48:51]\n"
"v_exp_f32 v2, v2\n"
"v_mfma_f32_16x16x32_bf16 a[52:55], a[244:247], v[20:23], a[52:55]\n"
"ds_read_b64_tr_b16 a[216:217], v244 offset:17152\n"
"v_mfma_f32_16x16x32_bf16 a[56:59], a[248:251], v[20:23], a[56:59]\n"
"v_exp_f32 v3, v3\n"
"v_mfma_f32_16x16x32_bf16 a[60:63], a[252:255], v[20:23], a[60:63]\n"
"v_cvt_pk_bf16_f32 v17, v2, v3\n"
"v_mfma_f32_16x16x32_bf16 v[48:51], v[40:43], v[20:23], v[48:51]\n"
"v_exp_f32 v4, v4\n"
"v_mfma_f32_16x16x32_bf16 v[8:11], a[160:163], v[112:115], v[36:39]\n"
"ds_read_b64_tr_b16 a[218:219], v244 offset:21248\n"
"v_mfma_f32_16x16x32_bf16 v[8:11], a[164:167], v[116:119], v[8:11]\n"
"v_exp_f32 v5, v5\n"
"v_mfma_f32_16x16x32_bf16 v[8:11], a[168:171], v[120:123], v[8:11]\n"
"ds_read_b64_tr_b16 a[220:221], v245 offset:17152\n"
"v_cvt_pk_bf16_f32 v18, v4, v5\n"
"v_mfma_f32_16x16x32_bf16 v[8:11], a[172:175], v[124:127], v[8:11]\n"
"v_exp_f32 v6, v6\n"
"v_mfma_f32_16x16x32_bf16 v[12:15], a[176:179], v[112:115], v[36:39]\n"
"ds_read_b64_tr_b16 a[222:223], v245 offset:21248\n"
"v_mfma_f32_16x16x32_bf16 v[12:15], a[180:183], v[116:119], v[12:15]\n"
"v_exp_f32 v7, v7\n"
"v_mfma_f32_16x16x32_bf16 v[12:15], a[184:187], v[120:123], v[12:15]\n"
"v_cvt_pk_bf16_f32 v19, v6, v7\n"
"v_mfma_f32_16x16x32_bf16 v[12:15], a[188:191], v[124:127], v[12:15]\n"
"buffer_load_dwordx4 v204, s[28:31], s52 offen offset:3072 lds\n"
"v_mfma_f32_16x16x32_bf16 a[64:67], a[224:227], v[16:19], a[64:67]\n"
"s_add_u32 s52, s52, 12288\n"
"v_exp_f32 v8, v8\n"
"v_mfma_f32_16x16x32_bf16 a[68:71], a[228:231], v[16:19], a[68:71]\n"
"buffer_load_dword v250, v214, s[68:71], s49 offen\n"
"v_mfma_f32_16x16x32_bf16 a[72:75], a[232:235], v[16:19], a[72:75]\n"
"v_exp_f32 v9, v9\n"
"v_mfma_f32_16x16x32_bf16 a[76:79], a[236:239], v[16:19], a[76:79]\n"
"v_cvt_pk_bf16_f32 v20, v8, v9\n"
"v_mfma_f32_16x16x32_bf16 a[80:83], a[240:243], v[16:19], a[80:83]\n"
"v_exp_f32 v10, v10\n"
"v_mfma_f32_16x16x32_bf16 a[84:87], a[244:247], v[16:19], a[84:87]\n"
"s_nop 0\n"
"v_mfma_f32_16x16x32_bf16 a[88:91], a[248:251], v[16:19], a[88:91]\n"
"v_exp_f32 v11, v11\n"
"v_mfma_f32_16x16x32_bf16 a[92:95], a[252:255], v[16:19], a[92:95]\n"
"s_waitcnt lgkmcnt(8)\n"
"v_cvt_pk_bf16_f32 v21, v10, v11\n"
"v_mfma_f32_16x16x32_bf16 v[52:55], v[40:43], v[16:19], v[52:55]\n"
"v_exp_f32 v12, v12\n"
"v_mfma_f32_16x16x32_bf16 v[0:3], a[128:131], v[64:67], v[24:27]\n"
"ds_read_b128 a[160:163], v241 offset:8192\n"
"v_mfma_f32_16x16x32_bf16 v[0:3], a[132:135], v[68:71], v[0:3]\n"
"v_exp_f32 v13, v13\n"
"v_mfma_f32_16x16x32_bf16 v[0:3], a[136:139], v[72:75], v[0:3]\n"
"ds_read_b128 a[164:167], v241 offset:8448\n"
"v_cvt_pk_bf16_f32 v22, v12, v13\n"
"v_mfma_f32_16x16x32_bf16 v[0:3], a[140:143], v[76:79], v[0:3]\n"
"v_exp_f32 v14, v14\n"
"v_mfma_f32_16x16x32_bf16 v[4:7], a[144:147], v[64:67], v[24:27]\n"
"ds_read_b128 a[168:171], v241 offset:8704\n"
"v_mfma_f32_16x16x32_bf16 v[4:7], a[148:151], v[68:71], v[4:7]\n"
"v_exp_f32 v15, v15\n"
"v_mfma_f32_16x16x32_bf16 v[4:7], a[152:155], v[72:75], v[4:7]\n"
"s_add_u32 m0, s53, 36864\n"
"ds_read_b128 a[172:175], v241 offset:8960\n"
"v_cvt_pk_bf16_f32 v23, v14, v15\n"
"v_mfma_f32_16x16x32_bf16 v[4:7], a[156:159], v[76:79], v[4:7]\n"
"buffer_load_dwordx4 v204, s[28:31], s52 offen offset:0 lds\n"
"v_mfma_f32_16x16x32_bf16 a[96:99], a[224:227], v[20:23], a[96:99]\n"
"s_add_u32 s52, s52, 8192\n"
"v_exp_f32 v0, v0\n"
"v_mfma_f32_16x16x32_bf16 a[100:103], a[228:231], v[20:23], a[100:103]\n"
"ds_read_b128 a[176:179], v241 offset:12288\n"
"v_mfma_f32_16x16x32_bf16 a[104:107], a[232:235], v[20:23], a[104:107]\n"
"v_exp_f32 v1, v1\n"
"v_mfma_f32_16x16x32_bf16 a[108:111], a[236:239], v[20:23], a[108:111]\n"
"ds_read_b128 a[180:183], v241 offset:12544\n"
"v_cvt_pk_bf16_f32 v16, v0, v1\n"
"v_mfma_f32_16x16x32_bf16 a[112:115], a[240:243], v[20:23], a[112:115]\n"
"v_exp_f32 v2, v2\n"
"v_mfma_f32_16x16x32_bf16 a[116:119], a[244:247], v[20:23], a[116:119]\n"
"ds_read_b128 a[184:187], v241 offset:12800\n"
"v_mfma_f32_16x16x32_bf16 a[120:123], a[248:251], v[20:23], a[120:123]\n"
"v_exp_f32 v3, v3\n"
"v_mfma_f32_16x16x32_bf16 a[124:127], a[252:255], v[20:23], a[124:127]\n"
"v_cvt_pk_bf16_f32 v17, v2, v3\n"
"v_mfma_f32_16x16x32_bf16 v[56:59], v[40:43], v[20:23], v[56:59]\n"
"v_exp_f32 v4, v4\n"
"v_mfma_f32_16x16x32_bf16 v[8:11], a[128:131], v[80:83], v[28:31]\n"
"ds_read_b128 a[188:191], v241 offset:13056\n"
"v_mfma_f32_16x16x32_bf16 v[8:11], a[132:135], v[84:87], v[8:11]\n"
"v_exp_f32 v5, v5\n"
"v_mfma_f32_16x16x32_bf16 v[8:11], a[136:139], v[88:91], v[8:11]\n"
"ds_read_b64_tr_b16 a[224:225], v244 offset:24576\n"
"v_cvt_pk_bf16_f32 v18, v4, v5\n"
"v_mfma_f32_16x16x32_bf16 v[8:11], a[140:143], v[92:95], v[8:11]\n"
"v_exp_f32 v6, v6\n"
"v_mfma_f32_16x16x32_bf16 v[12:15], a[144:147], v[80:83], v[28:31]\n"
"ds_read_b64_tr_b16 a[226:227], v244 offset:28672\n"
"v_mfma_f32_16x16x32_bf16 v[12:15], a[148:151], v[84:87], v[12:15]\n"
"v_exp_f32 v7, v7\n"
"v_mfma_f32_16x16x32_bf16 v[12:15], a[152:155], v[88:91], v[12:15]\n"
"ds_read_b64_tr_b16 a[228:229], v245 offset:24576\n"
"v_cvt_pk_bf16_f32 v19, v6, v7\n"
"v_mfma_f32_16x16x32_bf16 v[12:15], a[156:159], v[92:95], v[12:15]\n"
"s_waitcnt lgkmcnt(11)\n"
"buffer_load_dwordx4 v204, s[28:31], s52 offen offset:1024 lds\n"
"v_mfma_f32_16x16x32_bf16 a[0:3], a[192:195], v[16:19], a[0:3]\n"
"s_add_u32 s52, s52, 8192\n"
"v_exp_f32 v8, v8\n"
"v_mfma_f32_16x16x32_bf16 a[4:7], a[196:199], v[16:19], a[4:7]\n"
"ds_read_b64_tr_b16 a[230:231], v245 offset:28672\n"
"v_mfma_f32_16x16x32_bf16 a[8:11], a[200:203], v[16:19], a[8:11]\n"
"v_exp_f32 v9, v9\n"
"v_mfma_f32_16x16x32_bf16 a[12:15], a[204:207], v[16:19], a[12:15]\n"
"ds_read_b64_tr_b16 a[232:233], v244 offset:24832\n"
"v_cvt_pk_bf16_f32 v20, v8, v9\n"
"v_mfma_f32_16x16x32_bf16 a[16:19], a[208:211], v[16:19], a[16:19]\n"
"v_exp_f32 v10, v10\n"
"v_mfma_f32_16x16x32_bf16 a[20:23], a[212:215], v[16:19], a[20:23]\n"
"ds_read_b64_tr_b16 a[234:235], v244 offset:28928\n"
"v_mfma_f32_16x16x32_bf16 a[24:27], a[216:219], v[16:19], a[24:27]\n"
"v_exp_f32 v11, v11\n"
"v_mfma_f32_16x16x32_bf16 a[28:31], a[220:223], v[16:19], a[28:31]\n"
"v_cvt_pk_bf16_f32 v21, v10, v11\n"
"v_mfma_f32_16x16x32_bf16 v[44:47], v[40:43], v[16:19], v[44:47]\n"
"v_exp_f32 v12, v12\n"
"v_mfma_f32_16x16x32_bf16 v[0:3], a[128:131], v[96:99], v[32:35]\n"
"ds_read_b64_tr_b16 a[236:237], v245 offset:24832\n"
"v_mfma_f32_16x16x32_bf16 v[0:3], a[132:135], v[100:103], v[0:3]\n"
"v_exp_f32 v13, v13\n"
"v_mfma_f32_16x16x32_bf16 v[0:3], a[136:139], v[104:107], v[0:3]\n"
"ds_read_b64_tr_b16 a[238:239], v245 offset:28928\n"
"v_cvt_pk_bf16_f32 v22, v12, v13\n"
"v_mfma_f32_16x16x32_bf16 v[0:3], a[140:143], v[108:111], v[0:3]\n"
"v_exp_f32 v14, v14\n"
"v_mfma_f32_16x16x32_bf16 v[4:7], a[144:147], v[96:99], v[32:35]\n"
"ds_read_b64_tr_b16 a[240:241], v244 offset:25088\n"
"v_mfma_f32_16x16x32_bf16 v[4:7], a[148:151], v[100:103], v[4:7]\n"
"v_exp_f32 v15, v15\n"
"v_mfma_f32_16x16x32_bf16 v[4:7], a[152:155], v[104:107], v[4:7]\n"
"ds_read_b64_tr_b16 a[242:243], v244 offset:29184\n"
"v_cvt_pk_bf16_f32 v23, v14, v15\n"
"v_mfma_f32_16x16x32_bf16 v[4:7], a[156:159], v[108:111], v[4:7]\n"
"buffer_load_dwordx4 v204, s[28:31], s52 offen offset:2048 lds\n"
"v_mfma_f32_16x16x32_bf16 a[32:35], a[192:195], v[20:23], a[32:35]\n"
"s_add_u32 s52, s52, 8192\n"
"v_exp_f32 v0, v0\n"
"v_mfma_f32_16x16x32_bf16 a[36:39], a[196:199], v[20:23], a[36:39]\n"
"ds_read_b64_tr_b16 a[244:245], v245 offset:25088\n"
"v_mfma_f32_16x16x32_bf16 a[40:43], a[200:203], v[20:23], a[40:43]\n"
"v_exp_f32 v1, v1\n"
"v_mfma_f32_16x16x32_bf16 a[44:47], a[204:207], v[20:23], a[44:47]\n"
"ds_read_b64_tr_b16 a[246:247], v245 offset:29184\n"
"v_cvt_pk_bf16_f32 v16, v0, v1\n"
"v_mfma_f32_16x16x32_bf16 a[48:51], a[208:211], v[20:23], a[48:51]\n"
"v_exp_f32 v2, v2\n"
"v_mfma_f32_16x16x32_bf16 a[52:55], a[212:215], v[20:23], a[52:55]\n"
"ds_read_b64_tr_b16 a[248:249], v244 offset:25344\n"
"v_mfma_f32_16x16x32_bf16 a[56:59], a[216:219], v[20:23], a[56:59]\n"
"v_exp_f32 v3, v3\n"
"v_mfma_f32_16x16x32_bf16 a[60:63], a[220:223], v[20:23], a[60:63]\n"
"v_cvt_pk_bf16_f32 v17, v2, v3\n"
"v_mfma_f32_16x16x32_bf16 v[48:51], v[40:43], v[20:23], v[48:51]\n"
"v_exp_f32 v4, v4\n"
"v_mfma_f32_16x16x32_bf16 v[8:11], a[128:131], v[112:115], v[36:39]\n"
"ds_read_b64_tr_b16 a[250:251], v244 offset:29440\n"
"v_mfma_f32_16x16x32_bf16 v[8:11], a[132:135], v[116:119], v[8:11]\n"
"v_exp_f32 v5, v5\n"
"v_mfma_f32_16x16x32_bf16 v[8:11], a[136:139], v[120:123], v[8:11]\n"
"ds_read_b64_tr_b16 a[252:253], v245 offset:25344\n"
"v_cvt_pk_bf16_f32 v18, v4, v5\n"
"v_mfma_f32_16x16x32_bf16 v[8:11], a[140:143], v[124:127], v[8:11]\n"
"v_exp_f32 v6, v6\n"
"v_mfma_f32_16x16x32_bf16 v[12:15], a[144:147], v[112:115], v[36:39]\n"
"ds_read_b64_tr_b16 a[254:255], v245 offset:29440\n"
"v_mfma_f32_16x16x32_bf16 v[12:15], a[148:151], v[116:119], v[12:15]\n"
"v_exp_f32 v7, v7\n"
"v_mfma_f32_16x16x32_bf16 v[12:15], a[152:155], v[120:123], v[12:15]\n"
"v_cvt_pk_bf16_f32 v19, v6, v7\n"
"v_mfma_f32_16x16x32_bf16 v[12:15], a[156:159], v[124:127], v[12:15]\n"
"buffer_load_dwordx4 v204, s[28:31], s52 offen offset:3072 lds\n"
"v_mfma_f32_16x16x32_bf16 a[64:67], a[192:195], v[16:19], a[64:67]\n"
"s_sub_u32 s52, s52, 61312\n"
"v_exp_f32 v8, v8\n"
"v_mfma_f32_16x16x32_bf16 a[68:71], a[196:199], v[16:19], a[68:71]\n"
"s_nop 0\n"
"v_mfma_f32_16x16x32_bf16 a[72:75], a[200:203], v[16:19], a[72:75]\n"
"v_exp_f32 v9, v9\n"
"v_mfma_f32_16x16x32_bf16 a[76:79], a[204:207], v[16:19], a[76:79]\n"
"v_cvt_pk_bf16_f32 v20, v8, v9\n"
"v_mfma_f32_16x16x32_bf16 a[80:83], a[208:211], v[16:19], a[80:83]\n"
"v_exp_f32 v10, v10\n"
"v_mfma_f32_16x16x32_bf16 a[84:87], a[212:215], v[16:19], a[84:87]\n"
"s_nop 0\n"
"v_mfma_f32_16x16x32_bf16 a[88:91], a[216:219], v[16:19], a[88:91]\n"
"v_exp_f32 v11, v11\n"
"v_mfma_f32_16x16x32_bf16 a[92:95], a[220:223], v[16:19], a[92:95]\n"
"s_waitcnt vmcnt(20) lgkmcnt(0)\n"
"s_barrier\n"
"v_cvt_pk_bf16_f32 v21, v10, v11\n"
"v_mfma_f32_16x16x32_bf16 v[52:55], v[40:43], v[16:19], v[52:55]\n"
"v_exp_f32 v12, v12\n"
"v_mfma_f32_16x16x32_bf16 v[0:3], a[160:163], v[64:67], v[24:27]\n"
"ds_read_b128 a[128:131], v241 offset:32768\n"
"v_mfma_f32_16x16x32_bf16 v[0:3], a[164:167], v[68:71], v[0:3]\n"
"v_exp_f32 v13, v13\n"
"v_mfma_f32_16x16x32_bf16 v[0:3], a[168:171], v[72:75], v[0:3]\n"
"ds_read_b128 a[132:135], v241 offset:33024\n"
"v_cvt_pk_bf16_f32 v22, v12, v13\n"
"v_mfma_f32_16x16x32_bf16 v[0:3], a[172:175], v[76:79], v[0:3]\n"
"v_exp_f32 v14, v14\n"
"v_mfma_f32_16x16x32_bf16 v[4:7], a[176:179], v[64:67], v[24:27]\n"
"ds_read_b128 a[136:139], v241 offset:33280\n"
"v_mfma_f32_16x16x32_bf16 v[4:7], a[180:183], v[68:71], v[4:7]\n"
"v_exp_f32 v15, v15\n"
"v_mfma_f32_16x16x32_bf16 v[4:7], a[184:187], v[72:75], v[4:7]\n"
"s_add_u32 m0, s53, 65536\n"
"ds_read_b128 a[140:143], v241 offset:33536\n"
"v_cvt_pk_bf16_f32 v23, v14, v15\n"
"v_mfma_f32_16x16x32_bf16 v[4:7], a[188:191], v[76:79], v[4:7]\n"
"buffer_load_dwordx4 v204, s[28:31], s52 offen offset:0 lds\n"
"v_mfma_f32_16x16x32_bf16 a[96:99], a[192:195], v[20:23], a[96:99]\n"
"s_add_u32 s52, s52, 8192\n"
"v_exp_f32 v0, v0\n"
"v_mfma_f32_16x16x32_bf16 a[100:103], a[196:199], v[20:23], a[100:103]\n"
"ds_read_b128 a[144:147], v241 offset:36864\n"
"v_mfma_f32_16x16x32_bf16 a[104:107], a[200:203], v[20:23], a[104:107]\n"
"v_exp_f32 v1, v1\n"
"v_mfma_f32_16x16x32_bf16 a[108:111], a[204:207], v[20:23], a[108:111]\n"
"ds_read_b128 a[148:151], v241 offset:37120\n"
"v_cvt_pk_bf16_f32 v16, v0, v1\n"
"v_mfma_f32_16x16x32_bf16 a[112:115], a[208:211], v[20:23], a[112:115]\n"
"v_exp_f32 v2, v2\n"
"v_mfma_f32_16x16x32_bf16 a[116:119], a[212:215], v[20:23], a[116:119]\n"
"ds_read_b128 a[152:155], v241 offset:37376\n"
"v_mfma_f32_16x16x32_bf16 a[120:123], a[216:219], v[20:23], a[120:123]\n"
"v_exp_f32 v3, v3\n"
"v_mfma_f32_16x16x32_bf16 a[124:127], a[220:223], v[20:23], a[124:127]\n"
"v_cvt_pk_bf16_f32 v17, v2, v3\n"
"v_mfma_f32_16x16x32_bf16 v[56:59], v[40:43], v[20:23], v[56:59]\n"
"v_exp_f32 v4, v4\n"
"v_mfma_f32_16x16x32_bf16 v[8:11], a[160:163], v[80:83], v[28:31]\n"
"ds_read_b128 a[156:159], v241 offset:37632\n"
"v_mfma_f32_16x16x32_bf16 v[8:11], a[164:167], v[84:87], v[8:11]\n"
"v_exp_f32 v5, v5\n"
"v_mfma_f32_16x16x32_bf16 v[8:11], a[168:171], v[88:91], v[8:11]\n"
"ds_read_b64_tr_b16 a[192:193], v244 offset:49152\n"
"v_cvt_pk_bf16_f32 v18, v4, v5\n"
"v_mfma_f32_16x16x32_bf16 v[8:11], a[172:175], v[92:95], v[8:11]\n"
"v_exp_f32 v6, v6\n"
"v_mfma_f32_16x16x32_bf16 v[12:15], a[176:179], v[80:83], v[28:31]\n"
"ds_read_b64_tr_b16 a[194:195], v244 offset:53248\n"
"v_mfma_f32_16x16x32_bf16 v[12:15], a[180:183], v[84:87], v[12:15]\n"
"v_exp_f32 v7, v7\n"
"v_mfma_f32_16x16x32_bf16 v[12:15], a[184:187], v[88:91], v[12:15]\n"
"ds_read_b64_tr_b16 a[196:197], v245 offset:49152\n"
"v_cvt_pk_bf16_f32 v19, v6, v7\n"
"v_mfma_f32_16x16x32_bf16 v[12:15], a[188:191], v[92:95], v[12:15]\n"
"s_waitcnt lgkmcnt(11)\n"
"buffer_load_dwordx4 v204, s[28:31], s52 offen offset:1024 lds\n"
"v_mfma_f32_16x16x32_bf16 a[0:3], a[224:227], v[16:19], a[0:3]\n"
"s_add_u32 s52, s52, 8192\n"
"v_exp_f32 v8, v8\n"
"v_mfma_f32_16x16x32_bf16 a[4:7], a[228:231], v[16:19], a[4:7]\n"
"ds_read_b64_tr_b16 a[198:199], v245 offset:53248\n"
"v_mfma_f32_16x16x32_bf16 a[8:11], a[232:235], v[16:19], a[8:11]\n"
"v_exp_f32 v9, v9\n"
"v_mfma_f32_16x16x32_bf16 a[12:15], a[236:239], v[16:19], a[12:15]\n"
"ds_read_b64_tr_b16 a[200:201], v244 offset:49408\n"
"v_cvt_pk_bf16_f32 v20, v8, v9\n"
"v_mfma_f32_16x16x32_bf16 a[16:19], a[240:243], v[16:19], a[16:19]\n"
"v_exp_f32 v10, v10\n"
"v_mfma_f32_16x16x32_bf16 a[20:23], a[244:247], v[16:19], a[20:23]\n"
"ds_read_b64_tr_b16 a[202:203], v244 offset:53504\n"
"v_mfma_f32_16x16x32_bf16 a[24:27], a[248:251], v[16:19], a[24:27]\n"
"v_exp_f32 v11, v11\n"
"v_mfma_f32_16x16x32_bf16 a[28:31], a[252:255], v[16:19], a[28:31]\n"
"v_cvt_pk_bf16_f32 v21, v10, v11\n"
"v_mfma_f32_16x16x32_bf16 v[44:47], v[40:43], v[16:19], v[44:47]\n"
"v_exp_f32 v12, v12\n"
"v_mfma_f32_16x16x32_bf16 v[0:3], a[160:163], v[96:99], v[32:35]\n"
"ds_read_b64_tr_b16 a[204:205], v245 offset:49408\n"
"v_mfma_f32_16x16x32_bf16 v[0:3], a[164:167], v[100:103], v[0:3]\n"
"v_exp_f32 v13, v13\n"
"v_mfma_f32_16x16x32_bf16 v[0:3], a[168:171], v[104:107], v[0:3]\n"
"ds_read_b64_tr_b16 a[206:207], v245 offset:53504\n"
"v_cvt_pk_bf16_f32 v22, v12, v13\n"
"v_mfma_f32_16x16x32_bf16 v[0:3], a[172:175], v[108:111], v[0:3]\n"
"v_exp_f32 v14, v14\n"
"v_mfma_f32_16x16x32_bf16 v[4:7], a[176:179], v[96:99], v[32:35]\n"
"ds_read_b64_tr_b16 a[208:209], v244 offset:49664\n"
"v_mfma_f32_16x16x32_bf16 v[4:7], a[180:183], v[100:103], v[4:7]\n"
"v_exp_f32 v15, v15\n"
"v_mfma_f32_16x16x32_bf16 v[4:7], a[184:187], v[104:107], v[4:7]\n"
"ds_read_b64_tr_b16 a[210:211], v244 offset:53760\n"
"v_cvt_pk_bf16_f32 v23, v14, v15\n"
"v_mfma_f32_16x16x32_bf16 v[4:7], a[188:191], v[108:111], v[4:7]\n"
"buffer_load_dwordx4 v204, s[28:31], s52 offen offset:2048 lds\n"
"v_mfma_f32_16x16x32_bf16 a[32:35], a[224:227], v[20:23], a[32:35]\n"
"s_add_u32 s52, s52, 8192\n"
"v_exp_f32 v0, v0\n"
"v_mfma_f32_16x16x32_bf16 a[36:39], a[228:231], v[20:23], a[36:39]\n"
"ds_read_b64_tr_b16 a[212:213], v245 offset:49664\n"
"v_mfma_f32_16x16x32_bf16 a[40:43], a[232:235], v[20:23], a[40:43]\n"
"v_exp_f32 v1, v1\n"
"v_mfma_f32_16x16x32_bf16 a[44:47], a[236:239], v[20:23], a[44:47]\n"
"ds_read_b64_tr_b16 a[214:215], v245 offset:53760\n"
"v_cvt_pk_bf16_f32 v16, v0, v1\n"
"v_mfma_f32_16x16x32_bf16 a[48:51], a[240:243], v[20:23], a[48:51]\n"
"v_exp_f32 v2, v2\n"
"v_mfma_f32_16x16x32_bf16 a[52:55], a[244:247], v[20:23], a[52:55]\n"
"ds_read_b64_tr_b16 a[216:217], v244 offset:49920\n"
"v_mfma_f32_16x16x32_bf16 a[56:59], a[248:251], v[20:23], a[56:59]\n"
"v_exp_f32 v3, v3\n"
"v_mfma_f32_16x16x32_bf16 a[60:63], a[252:255], v[20:23], a[60:63]\n"
"v_cvt_pk_bf16_f32 v17, v2, v3\n"
"v_mfma_f32_16x16x32_bf16 v[48:51], v[40:43], v[20:23], v[48:51]\n"
"v_exp_f32 v4, v4\n"
"v_mfma_f32_16x16x32_bf16 v[8:11], a[160:163], v[112:115], v[36:39]\n"
"ds_read_b64_tr_b16 a[218:219], v244 offset:54016\n"
"v_mfma_f32_16x16x32_bf16 v[8:11], a[164:167], v[116:119], v[8:11]\n"
"v_exp_f32 v5, v5\n"
"v_mfma_f32_16x16x32_bf16 v[8:11], a[168:171], v[120:123], v[8:11]\n"
"ds_read_b64_tr_b16 a[220:221], v245 offset:49920\n"
"v_cvt_pk_bf16_f32 v18, v4, v5\n"
"v_mfma_f32_16x16x32_bf16 v[8:11], a[172:175], v[124:127], v[8:11]\n"
"v_exp_f32 v6, v6\n"
"v_mfma_f32_16x16x32_bf16 v[12:15], a[176:179], v[112:115], v[36:39]\n"
"ds_read_b64_tr_b16 a[222:223], v245 offset:54016\n"
"v_mfma_f32_16x16x32_bf16 v[12:15], a[180:183], v[116:119], v[12:15]\n"
"v_exp_f32 v7, v7\n"
"v_mfma_f32_16x16x32_bf16 v[12:15], a[184:187], v[120:123], v[12:15]\n"
"v_cvt_pk_bf16_f32 v19, v6, v7\n"
"v_mfma_f32_16x16x32_bf16 v[12:15], a[188:191], v[124:127], v[12:15]\n"
"buffer_load_dwordx4 v204, s[28:31], s52 offen offset:3072 lds\n"
"v_mfma_f32_16x16x32_bf16 a[64:67], a[224:227], v[16:19], a[64:67]\n"
"s_add_u32 s52, s52, 12288\n"
"v_exp_f32 v8, v8\n"
"v_mfma_f32_16x16x32_bf16 a[68:71], a[228:231], v[16:19], a[68:71]\n"
"s_nop 0\n"
"v_mfma_f32_16x16x32_bf16 a[72:75], a[232:235], v[16:19], a[72:75]\n"
"v_exp_f32 v9, v9\n"
"v_mfma_f32_16x16x32_bf16 a[76:79], a[236:239], v[16:19], a[76:79]\n"
"v_cvt_pk_bf16_f32 v20, v8, v9\n"
"v_mfma_f32_16x16x32_bf16 a[80:83], a[240:243], v[16:19], a[80:83]\n"
"v_exp_f32 v10, v10\n"
"v_mfma_f32_16x16x32_bf16 a[84:87], a[244:247], v[16:19], a[84:87]\n"
"s_nop 0\n"
"v_mfma_f32_16x16x32_bf16 a[88:91], a[248:251], v[16:19], a[88:91]\n"
"v_exp_f32 v11, v11\n"
"v_mfma_f32_16x16x32_bf16 a[92:95], a[252:255], v[16:19], a[92:95]\n"
"s_waitcnt lgkmcnt(8)\n"
"v_cvt_pk_bf16_f32 v21, v10, v11\n"
"v_mfma_f32_16x16x32_bf16 v[52:55], v[40:43], v[16:19], v[52:55]\n"
"v_exp_f32 v12, v12\n"
"v_mfma_f32_16x16x32_bf16 v[0:3], a[128:131], v[64:67], v[24:27]\n"
"ds_read_b128 a[160:163], v241 offset:40960\n"
"v_mfma_f32_16x16x32_bf16 v[0:3], a[132:135], v[68:71], v[0:3]\n"
"v_exp_f32 v13, v13\n"
"v_mfma_f32_16x16x32_bf16 v[0:3], a[136:139], v[72:75], v[0:3]\n"
"ds_read_b128 a[164:167], v241 offset:41216\n"
"v_cvt_pk_bf16_f32 v22, v12, v13\n"
"v_mfma_f32_16x16x32_bf16 v[0:3], a[140:143], v[76:79], v[0:3]\n"
"v_exp_f32 v14, v14\n"
"v_mfma_f32_16x16x32_bf16 v[4:7], a[144:147], v[64:67], v[24:27]\n"
"ds_read_b128 a[168:171], v241 offset:41472\n"
"v_mfma_f32_16x16x32_bf16 v[4:7], a[148:151], v[68:71], v[4:7]\n"
"v_exp_f32 v15, v15\n"
"v_mfma_f32_16x16x32_bf16 v[4:7], a[152:155], v[72:75], v[4:7]\n"
"s_add_u32 m0, s53, 69632\n"
"ds_read_b128 a[172:175], v241 offset:41728\n"
"v_cvt_pk_bf16_f32 v23, v14, v15\n"
"v_mfma_f32_16x16x32_bf16 v[4:7], a[156:159], v[76:79], v[4:7]\n"
"buffer_load_dwordx4 v204, s[28:31], s52 offen offset:0 lds\n"
"v_mfma_f32_16x16x32_bf16 a[96:99], a[224:227], v[20:23], a[96:99]\n"
"s_add_u32 s52, s52, 8192\n"
"v_exp_f32 v0, v0\n"
"v_mfma_f32_16x16x32_bf16 a[100:103], a[228:231], v[20:23], a[100:103]\n"
"ds_read_b128 a[176:179], v241 offset:45056\n"
"v_mfma_f32_16x16x32_bf16 a[104:107], a[232:235], v[20:23], a[104:107]\n"
"v_exp_f32 v1, v1\n"
"v_mfma_f32_16x16x32_bf16 a[108:111], a[236:239], v[20:23], a[108:111]\n"
"ds_read_b128 a[180:183], v241 offset:45312\n"
"v_cvt_pk_bf16_f32 v16, v0, v1\n"
"v_mfma_f32_16x16x32_bf16 a[112:115], a[240:243], v[20:23], a[112:115]\n"
"v_exp_f32 v2, v2\n"
"v_mfma_f32_16x16x32_bf16 a[116:119], a[244:247], v[20:23], a[116:119]\n"
"ds_read_b128 a[184:187], v241 offset:45568\n"
"v_mfma_f32_16x16x32_bf16 a[120:123], a[248:251], v[20:23], a[120:123]\n"
"v_exp_f32 v3, v3\n"
"v_mfma_f32_16x16x32_bf16 a[124:127], a[252:255], v[20:23], a[124:127]\n"
"v_cvt_pk_bf16_f32 v17, v2, v3\n"
"v_mfma_f32_16x16x32_bf16 v[56:59], v[40:43], v[20:23], v[56:59]\n"
"v_exp_f32 v4, v4\n"
"v_mfma_f32_16x16x32_bf16 v[8:11], a[128:131], v[80:83], v[28:31]\n"
"ds_read_b128 a[188:191], v241 offset:45824\n"
"v_mfma_f32_16x16x32_bf16 v[8:11], a[132:135], v[84:87], v[8:11]\n"
"v_exp_f32 v5, v5\n"
"v_mfma_f32_16x16x32_bf16 v[8:11], a[136:139], v[88:91], v[8:11]\n"
"ds_read_b64_tr_b16 a[224:225], v244 offset:57344\n"
"v_cvt_pk_bf16_f32 v18, v4, v5\n"
"v_mfma_f32_16x16x32_bf16 v[8:11], a[140:143], v[92:95], v[8:11]\n"
"v_exp_f32 v6, v6\n"
"v_mfma_f32_16x16x32_bf16 v[12:15], a[144:147], v[80:83], v[28:31]\n"
"ds_read_b64_tr_b16 a[226:227], v244 offset:61440\n"
"v_mfma_f32_16x16x32_bf16 v[12:15], a[148:151], v[84:87], v[12:15]\n"
"v_exp_f32 v7, v7\n"
"v_mfma_f32_16x16x32_bf16 v[12:15], a[152:155], v[88:91], v[12:15]\n"
"ds_read_b64_tr_b16 a[228:229], v245 offset:57344\n"
"v_cvt_pk_bf16_f32 v19, v6, v7\n"
"v_mfma_f32_16x16x32_bf16 v[12:15], a[156:159], v[92:95], v[12:15]\n"
"s_waitcnt lgkmcnt(11)\n"
"buffer_load_dwordx4 v204, s[28:31], s52 offen offset:1024 lds\n"
"v_mfma_f32_16x16x32_bf16 a[0:3], a[192:195], v[16:19], a[0:3]\n"
"s_add_u32 s52, s52, 8192\n"
"v_exp_f32 v8, v8\n"
"v_mfma_f32_16x16x32_bf16 a[4:7], a[196:199], v[16:19], a[4:7]\n"
"ds_read_b64_tr_b16 a[230:231], v245 offset:61440\n"
"v_mfma_f32_16x16x32_bf16 a[8:11], a[200:203], v[16:19], a[8:11]\n"
"v_exp_f32 v9, v9\n"
"v_mfma_f32_16x16x32_bf16 a[12:15], a[204:207], v[16:19], a[12:15]\n"
"ds_read_b64_tr_b16 a[232:233], v244 offset:57600\n"
"v_cvt_pk_bf16_f32 v20, v8, v9\n"
"v_mfma_f32_16x16x32_bf16 a[16:19], a[208:211], v[16:19], a[16:19]\n"
"v_exp_f32 v10, v10\n"
"v_mfma_f32_16x16x32_bf16 a[20:23], a[212:215], v[16:19], a[20:23]\n"
"ds_read_b64_tr_b16 a[234:235], v244 offset:61696\n"
"v_mfma_f32_16x16x32_bf16 a[24:27], a[216:219], v[16:19], a[24:27]\n"
"v_exp_f32 v11, v11\n"
"v_mfma_f32_16x16x32_bf16 a[28:31], a[220:223], v[16:19], a[28:31]\n"
"v_cvt_pk_bf16_f32 v21, v10, v11\n"
"v_mfma_f32_16x16x32_bf16 v[44:47], v[40:43], v[16:19], v[44:47]\n"
"v_exp_f32 v12, v12\n"
"v_mfma_f32_16x16x32_bf16 v[0:3], a[128:131], v[96:99], v[32:35]\n"
"ds_read_b64_tr_b16 a[236:237], v245 offset:57600\n"
"v_mfma_f32_16x16x32_bf16 v[0:3], a[132:135], v[100:103], v[0:3]\n"
"v_exp_f32 v13, v13\n"
"v_mfma_f32_16x16x32_bf16 v[0:3], a[136:139], v[104:107], v[0:3]\n"
"ds_read_b64_tr_b16 a[238:239], v245 offset:61696\n"
"v_cvt_pk_bf16_f32 v22, v12, v13\n"
"v_mfma_f32_16x16x32_bf16 v[0:3], a[140:143], v[108:111], v[0:3]\n"
"v_exp_f32 v14, v14\n"
"v_mfma_f32_16x16x32_bf16 v[4:7], a[144:147], v[96:99], v[32:35]\n"
"ds_read_b64_tr_b16 a[240:241], v244 offset:57856\n"
"v_mfma_f32_16x16x32_bf16 v[4:7], a[148:151], v[100:103], v[4:7]\n"
"v_exp_f32 v15, v15\n"
"v_mfma_f32_16x16x32_bf16 v[4:7], a[152:155], v[104:107], v[4:7]\n"
"ds_read_b64_tr_b16 a[242:243], v244 offset:61952\n"
"v_cvt_pk_bf16_f32 v23, v14, v15\n"
"v_mfma_f32_16x16x32_bf16 v[4:7], a[156:159], v[108:111], v[4:7]\n"
"buffer_load_dwordx4 v204, s[28:31], s52 offen offset:2048 lds\n"
"v_mfma_f32_16x16x32_bf16 a[32:35], a[192:195], v[20:23], a[32:35]\n"
"s_add_u32 s52, s52, 8192\n"
"v_exp_f32 v0, v0\n"
"v_mfma_f32_16x16x32_bf16 a[36:39], a[196:199], v[20:23], a[36:39]\n"
"ds_read_b64_tr_b16 a[244:245], v245 offset:57856\n"
"v_mfma_f32_16x16x32_bf16 a[40:43], a[200:203], v[20:23], a[40:43]\n"
"v_exp_f32 v1, v1\n"
"v_mfma_f32_16x16x32_bf16 a[44:47], a[204:207], v[20:23], a[44:47]\n"
"ds_read_b64_tr_b16 a[246:247], v245 offset:61952\n"
"v_cvt_pk_bf16_f32 v16, v0, v1\n"
"v_mfma_f32_16x16x32_bf16 a[48:51], a[208:211], v[20:23], a[48:51]\n"
"v_exp_f32 v2, v2\n"
"v_mfma_f32_16x16x32_bf16 a[52:55], a[212:215], v[20:23], a[52:55]\n"
"ds_read_b64_tr_b16 a[248:249], v244 offset:58112\n"
"v_mfma_f32_16x16x32_bf16 a[56:59], a[216:219], v[20:23], a[56:59]\n"
"v_exp_f32 v3, v3\n"
"v_mfma_f32_16x16x32_bf16 a[60:63], a[220:223], v[20:23], a[60:63]\n"
"v_cvt_pk_bf16_f32 v17, v2, v3\n"
"v_mfma_f32_16x16x32_bf16 v[48:51], v[40:43], v[20:23], v[48:51]\n"
"v_exp_f32 v4, v4\n"
"v_mfma_f32_16x16x32_bf16 v[8:11], a[128:131], v[112:115], v[36:39]\n"
"ds_read_b64_tr_b16 a[250:251], v244 offset:62208\n"
"v_mfma_f32_16x16x32_bf16 v[8:11], a[132:135], v[116:119], v[8:11]\n"
"v_exp_f32 v5, v5\n"
"v_mfma_f32_16x16x32_bf16 v[8:11], a[136:139], v[120:123], v[8:11]\n"
"ds_read_b64_tr_b16 a[252:253], v245 offset:58112\n"
"v_cvt_pk_bf16_f32 v18, v4, v5\n"
"v_mfma_f32_16x16x32_bf16 v[8:11], a[140:143], v[124:127], v[8:11]\n"
"v_exp_f32 v6, v6\n"
"v_mfma_f32_16x16x32_bf16 v[12:15], a[144:147], v[112:115], v[36:39]\n"
"ds_read_b64_tr_b16 a[254:255], v245 offset:62208\n"
"v_mfma_f32_16x16x32_bf16 v[12:15], a[148:151], v[116:119], v[12:15]\n"
"v_exp_f32 v7, v7\n"
"v_mfma_f32_16x16x32_bf16 v[12:15], a[152:155], v[120:123], v[12:15]\n"
"v_cvt_pk_bf16_f32 v19, v6, v7\n"
"v_mfma_f32_16x16x32_bf16 v[12:15], a[156:159], v[124:127], v[12:15]\n"
"buffer_load_dwordx4 v204, s[28:31], s52 offen offset:3072 lds\n"
"v_mfma_f32_16x16x32_bf16 a[64:67], a[192:195], v[16:19], a[64:67]\n"
"s_sub_u32 s52, s52, 61312\n"
"v_exp_f32 v8, v8\n"
"v_mfma_f32_16x16x32_bf16 a[68:71], a[196:199], v[16:19], a[68:71]\n"
"s_nop 0\n"
"v_mfma_f32_16x16x32_bf16 a[72:75], a[200:203], v[16:19], a[72:75]\n"
"v_exp_f32 v9, v9\n"
"v_mfma_f32_16x16x32_bf16 a[76:79], a[204:207], v[16:19], a[76:79]\n"
"v_cvt_pk_bf16_f32 v20, v8, v9\n"
"v_mfma_f32_16x16x32_bf16 a[80:83], a[208:211], v[16:19], a[80:83]\n"
"v_exp_f32 v10, v10\n"
"v_mfma_f32_16x16x32_bf16 a[84:87], a[212:215], v[16:19], a[84:87]\n"
"s_nop 0\n"
"v_mfma_f32_16x16x32_bf16 a[88:91], a[216:219], v[16:19], a[88:91]\n"
"v_exp_f32 v11, v11\n"
"v_mfma_f32_16x16x32_bf16 a[92:95], a[220:223], v[16:19], a[92:95]\n"
"s_waitcnt vmcnt(18) lgkmcnt(0)\n"
"s_barrier\n"
"v_cvt_pk_bf16_f32 v21, v10, v11\n"
"v_mfma_f32_16x16x32_bf16 v[52:55], v[40:43], v[16:19], v[52:55]\n"
"v_exp_f32 v12, v12\n"
"v_mfma_f32_16x16x32_bf16 v[0:3], a[160:163], v[64:67], v[24:27]\n"
"ds_read_b64_tr_b16 a[128:129], v194 offset:0\n"
"v_mfma_f32_16x16x32_bf16 v[0:3], a[164:167], v[68:71], v[0:3]\n"
"v_exp_f32 v13, v13\n"
"v_mfma_f32_16x16x32_bf16 v[0:3], a[168:171], v[72:75], v[0:3]\n"
"ds_read_b64_tr_b16 a[130:131], v194 offset:2048\n"
"v_cvt_pk_bf16_f32 v22, v12, v13\n"
"v_mfma_f32_16x16x32_bf16 v[0:3], a[172:175], v[76:79], v[0:3]\n"
"v_exp_f32 v14, v14\n"
"v_mfma_f32_16x16x32_bf16 v[4:7], a[176:179], v[64:67], v[24:27]\n"
"ds_read_b64_tr_b16 a[132:133], v194 offset:4096\n"
"v_mfma_f32_16x16x32_bf16 v[4:7], a[180:183], v[68:71], v[4:7]\n"
"v_exp_f32 v15, v15\n"
"v_mfma_f32_16x16x32_bf16 v[4:7], a[184:187], v[72:75], v[4:7]\n"
"ds_read_b64_tr_b16 a[134:135], v194 offset:6144\n"
"v_cvt_pk_bf16_f32 v23, v14, v15\n"
"v_mfma_f32_16x16x32_bf16 v[4:7], a[188:191], v[76:79], v[4:7]\n"
"ds_read_b64_tr_b16 a[136:137], v194 offset:8192\n"
"v_mfma_f32_16x16x32_bf16 a[96:99], a[192:195], v[20:23], a[96:99]\n"
"v_exp_f32 v0, v0\n"
"v_mfma_f32_16x16x32_bf16 a[100:103], a[196:199], v[20:23], a[100:103]\n"
"ds_read_b64_tr_b16 a[138:139], v194 offset:10240\n"
"v_mfma_f32_16x16x32_bf16 a[104:107], a[200:203], v[20:23], a[104:107]\n"
"v_exp_f32 v1, v1\n"
"v_mfma_f32_16x16x32_bf16 a[108:111], a[204:207], v[20:23], a[108:111]\n"
"ds_read_b64_tr_b16 a[140:141], v194 offset:12288\n"
"v_cvt_pk_bf16_f32 v16, v0, v1\n"
"v_mfma_f32_16x16x32_bf16 a[112:115], a[208:211], v[20:23], a[112:115]\n"
"v_exp_f32 v2, v2\n"
"v_mfma_f32_16x16x32_bf16 a[116:119], a[212:215], v[20:23], a[116:119]\n"
"ds_read_b64_tr_b16 a[142:143], v194 offset:14336\n"
"v_mfma_f32_16x16x32_bf16 a[120:123], a[216:219], v[20:23], a[120:123]\n"
"v_exp_f32 v3, v3\n"
"v_mfma_f32_16x16x32_bf16 a[124:127], a[220:223], v[20:23], a[124:127]\n"
"v_cvt_pk_bf16_f32 v17, v2, v3\n"
"v_mfma_f32_16x16x32_bf16 v[56:59], v[40:43], v[20:23], v[56:59]\n"
"v_exp_f32 v4, v4\n"
"v_mfma_f32_16x16x32_bf16 v[8:11], a[160:163], v[80:83], v[28:31]\n"
"ds_read_b64_tr_b16 a[144:145], v194 offset:16384\n"
"v_mfma_f32_16x16x32_bf16 v[8:11], a[164:167], v[84:87], v[8:11]\n"
"v_exp_f32 v5, v5\n"
"v_mfma_f32_16x16x32_bf16 v[8:11], a[168:171], v[88:91], v[8:11]\n"
"ds_read_b64_tr_b16 a[146:147], v194 offset:18432\n"
"v_cvt_pk_bf16_f32 v18, v4, v5\n"
"v_mfma_f32_16x16x32_bf16 v[8:11], a[172:175], v[92:95], v[8:11]\n"
"v_exp_f32 v6, v6\n"
"v_mfma_f32_16x16x32_bf16 v[12:15], a[176:179], v[80:83], v[28:31]\n"
"ds_read_b64_tr_b16 a[148:149], v194 offset:20480\n"
"v_mfma_f32_16x16x32_bf16 v[12:15], a[180:183], v[84:87], v[12:15]\n"
"v_exp_f32 v7, v7\n"
"v_mfma_f32_16x16x32_bf16 v[12:15], a[184:187], v[88:91], v[12:15]\n"
"ds_read_b64_tr_b16 a[150:151], v194 offset:22528\n"
"v_cvt_pk_bf16_f32 v19, v6, v7\n"
"v_mfma_f32_16x16x32_bf16 v[12:15], a[188:191], v[92:95], v[12:15]\n"
"ds_read_b64_tr_b16 a[152:153], v194 offset:24576\n"
"v_mfma_f32_16x16x32_bf16 a[0:3], a[224:227], v[16:19], a[0:3]\n"
"v_exp_f32 v8, v8\n"
"v_mfma_f32_16x16x32_bf16 a[4:7], a[228:231], v[16:19], a[4:7]\n"
"ds_read_b64_tr_b16 a[154:155], v194 offset:26624\n"
"v_mfma_f32_16x16x32_bf16 a[8:11], a[232:235], v[16:19], a[8:11]\n"
"v_exp_f32 v9, v9\n"
"v_mfma_f32_16x16x32_bf16 a[12:15], a[236:239], v[16:19], a[12:15]\n"
"ds_read_b64_tr_b16 a[156:157], v194 offset:28672\n"
"v_cvt_pk_bf16_f32 v20, v8, v9\n"
"v_mfma_f32_16x16x32_bf16 a[16:19], a[240:243], v[16:19], a[16:19]\n"
"v_exp_f32 v10, v10\n"
"v_mfma_f32_16x16x32_bf16 a[20:23], a[244:247], v[16:19], a[20:23]\n"
"ds_read_b64_tr_b16 a[158:159], v194 offset:30720\n"
"v_mfma_f32_16x16x32_bf16 a[24:27], a[248:251], v[16:19], a[24:27]\n"
"v_exp_f32 v11, v11\n"
"v_mfma_f32_16x16x32_bf16 a[28:31], a[252:255], v[16:19], a[28:31]\n"
"v_cvt_pk_bf16_f32 v21, v10, v11\n"
"v_mfma_f32_16x16x32_bf16 v[44:47], v[40:43], v[16:19], v[44:47]\n"
"v_exp_f32 v12, v12\n"
"v_mfma_f32_16x16x32_bf16 v[0:3], a[160:163], v[96:99], v[32:35]\n"
"ds_read_b64_tr_b16 a[192:193], v197 offset:0\n"
"v_mfma_f32_16x16x32_bf16 v[0:3], a[164:167], v[100:103], v[0:3]\n"
"v_exp_f32 v13, v13\n"
"v_mfma_f32_16x16x32_bf16 v[0:3], a[168:171], v[104:107], v[0:3]\n"
"ds_read_b64_tr_b16 a[194:195], v197 offset:2048\n"
"v_cvt_pk_bf16_f32 v22, v12, v13\n"
"v_mfma_f32_16x16x32_bf16 v[0:3], a[172:175], v[108:111], v[0:3]\n"
"v_exp_f32 v14, v14\n"
"v_mfma_f32_16x16x32_bf16 v[4:7], a[176:179], v[96:99], v[32:35]\n"
"ds_read_b64_tr_b16 a[196:197], v197 offset:4096\n"
"v_mfma_f32_16x16x32_bf16 v[4:7], a[180:183], v[100:103], v[4:7]\n"
"v_exp_f32 v15, v15\n"
"v_mfma_f32_16x16x32_bf16 v[4:7], a[184:187], v[104:107], v[4:7]\n"
"ds_read_b64_tr_b16 a[198:199], v197 offset:6144\n"
"v_cvt_pk_bf16_f32 v23, v14, v15\n"
"v_mfma_f32_16x16x32_bf16 v[4:7], a[188:191], v[108:111], v[4:7]\n"
"ds_read_b64_tr_b16 a[200:201], v197 offset:8192\n"
"v_mfma_f32_16x16x32_bf16 a[32:35], a[224:227], v[20:23], a[32:35]\n"
"v_exp_f32 v0, v0\n"
"v_mfma_f32_16x16x32_bf16 a[36:39], a[228:231], v[20:23], a[36:39]\n"
"ds_read_b64_tr_b16 a[202:203], v197 offset:10240\n"
"v_mfma_f32_16x16x32_bf16 a[40:43], a[232:235], v[20:23], a[40:43]\n"
"v_exp_f32 v1, v1\n"
"v_mfma_f32_16x16x32_bf16 a[44:47], a[236:239], v[20:23], a[44:47]\n"
"ds_read_b64_tr_b16 a[204:205], v197 offset:12288\n"
"v_cvt_pk_bf16_f32 v16, v0, v1\n"
"v_mfma_f32_16x16x32_bf16 a[48:51], a[240:243], v[20:23], a[48:51]\n"
"v_exp_f32 v2, v2\n"
"v_mfma_f32_16x16x32_bf16 a[52:55], a[244:247], v[20:23], a[52:55]\n"
"ds_read_b64_tr_b16 a[206:207], v197 offset:14336\n"
"v_mfma_f32_16x16x32_bf16 a[56:59], a[248:251], v[20:23], a[56:59]\n"
"v_exp_f32 v3, v3\n"
"v_mfma_f32_16x16x32_bf16 a[60:63], a[252:255], v[20:23], a[60:63]\n"
"v_cvt_pk_bf16_f32 v17, v2, v3\n"
"v_mfma_f32_16x16x32_bf16 v[48:51], v[40:43], v[20:23], v[48:51]\n"
"v_exp_f32 v4, v4\n"
"v_mfma_f32_16x16x32_bf16 v[8:11], a[160:163], v[112:115], v[36:39]\n"
"s_nop 0\n"
"v_mfma_f32_16x16x32_bf16 v[8:11], a[164:167], v[116:119], v[8:11]\n"
"v_exp_f32 v5, v5\n"
"v_mfma_f32_16x16x32_bf16 v[8:11], a[168:171], v[120:123], v[8:11]\n"
"v_cvt_pk_bf16_f32 v18, v4, v5\n"
"v_mfma_f32_16x16x32_bf16 v[8:11], a[172:175], v[124:127], v[8:11]\n"
"v_exp_f32 v6, v6\n"
"v_mfma_f32_16x16x32_bf16 v[12:15], a[176:179], v[112:115], v[36:39]\n"
"s_nop 0\n"
"v_mfma_f32_16x16x32_bf16 v[12:15], a[180:183], v[116:119], v[12:15]\n"
"v_exp_f32 v7, v7\n"
"v_mfma_f32_16x16x32_bf16 v[12:15], a[184:187], v[120:123], v[12:15]\n"
"v_cvt_pk_bf16_f32 v19, v6, v7\n"
"v_mfma_f32_16x16x32_bf16 v[12:15], a[188:191], v[124:127], v[12:15]\n"
"s_nop 0\n"
"v_mfma_f32_16x16x32_bf16 a[64:67], a[224:227], v[16:19], a[64:67]\n"
"v_exp_f32 v8, v8\n"
"v_mfma_f32_16x16x32_bf16 a[68:71], a[228:231], v[16:19], a[68:71]\n"
"s_nop 0\n"
"v_mfma_f32_16x16x32_bf16 a[72:75], a[232:235], v[16:19], a[72:75]\n"
"v_exp_f32 v9, v9\n"
"v_mfma_f32_16x16x32_bf16 a[76:79], a[236:239], v[16:19], a[76:79]\n"
"v_cvt_pk_bf16_f32 v20, v8, v9\n"
"v_mfma_f32_16x16x32_bf16 a[80:83], a[240:243], v[16:19], a[80:83]\n"
"v_exp_f32 v10, v10\n"
"v_mfma_f32_16x16x32_bf16 a[84:87], a[244:247], v[16:19], a[84:87]\n"
"s_nop 0\n"
"v_mfma_f32_16x16x32_bf16 a[88:91], a[248:251], v[16:19], a[88:91]\n"
"v_exp_f32 v11, v11\n"
"v_mfma_f32_16x16x32_bf16 a[92:95], a[252:255], v[16:19], a[92:95]\n"
"v_cvt_pk_bf16_f32 v21, v10, v11\n"
"v_mfma_f32_16x16x32_bf16 v[52:55], v[40:43], v[16:19], v[52:55]\n"
"v_exp_f32 v12, v12\n"
"s_nop 0\n"
"v_exp_f32 v13, v13\n"
"s_nop 0\n"
"v_cvt_pk_bf16_f32 v22, v12, v13\n"
"s_nop 0\n"
"v_exp_f32 v14, v14\n"
"s_nop 0\n"
"s_nop 0\n"
"v_exp_f32 v15, v15\n"
"s_nop 0\n"
"v_cvt_pk_bf16_f32 v23, v14, v15\n"
"s_nop 0\n"
"s_nop 0\n"
"s_nop 1\n"
"v_mfma_f32_16x16x32_bf16 a[96:99], a[224:227], v[20:23], a[96:99]\n"
"v_mfma_f32_16x16x32_bf16 a[100:103], a[228:231], v[20:23], a[100:103]\n"
"v_mfma_f32_16x16x32_bf16 a[104:107], a[232:235], v[20:23], a[104:107]\n"
"v_mfma_f32_16x16x32_bf16 a[108:111], a[236:239], v[20:23], a[108:111]\n"
"v_mfma_f32_16x16x32_bf16 a[112:115], a[240:243], v[20:23], a[112:115]\n"
"v_mfma_f32_16x16x32_bf16 a[116:119], a[244:247], v[20:23], a[116:119]\n"
"v_mfma_f32_16x16x32_bf16 a[120:123], a[248:251], v[20:23], a[120:123]\n"
"v_mfma_f32_16x16x32_bf16 a[124:127], a[252:255], v[20:23], a[124:127]\n"
"v_mfma_f32_16x16x32_bf16 v[56:59], v[40:43], v[20:23], v[56:59]\n"
"s_nop 7\n"
"v_mov_b32 v213, 0x5f800000\n"
"v_cmp_nle_f32 s[56:57], v44, v213\n"
"v_cmp_nle_f32 s[58:59], v48, v213\n"
"v_cmp_nle_f32 s[60:61], v52, v213\n"
"v_cmp_nle_f32 s[62:63], v56, v213\n"
"v_rcp_f32 v212, v44\n"
"v_rcp_f32 v214, v48\n"
"v_rcp_f32 v216, v52\n"
"v_rcp_f32 v218, v56\n"
"s_or_b64 s[56:57], s[56:57], s[58:59]\n"
"s_or_b64 s[60:61], s[60:61], s[62:63]\n"
"s_or_b64 s[56:57], s[56:57], s[60:61]\n"
"s_cselect_b32 s56, 1, 0\n"
"s_cmp_eq_u32 s45, 0\n"
"s_cselect_b32 s56, s56, 0\n"
"s_lshl_b32 s57, s41, 2\n"
"s_add_u32 s57, s57, s40\n"
"s_add_u32 s57, s57, 98304\n"
"v_mov_b32 v236, s56\n"
"v_mov_b32 v237, s57\n"
"ds_write_b32 v237, v236\n"
"v_accvgpr_read_b32 v220, a0\n"
"v_accvgpr_read_b32 v221, a1\n"
"v_accvgpr_read_b32 v222, a2\n"
"v_accvgpr_read_b32 v223, a3\n"
"v_pk_mul_f32 v[220:221], v[220:221], v[212:213] op_sel_hi:[1,0]\n"
"v_pk_mul_f32 v[222:223], v[222:223], v[212:213] op_sel_hi:[1,0]\n"
"v_cvt_pk_bf16_f32 v128, v220, v221\n"
"v_cvt_pk_bf16_f32 v129, v222, v223\n"
"v_accvgpr_read_b32 v224, a4\n"
"v_accvgpr_read_b32 v225, a5\n"
"v_accvgpr_read_b32 v226, a6\n"
"v_accvgpr_read_b32 v227, a7\n"
"v_pk_mul_f32 v[224:225], v[224:225], v[212:213] op_sel_hi:[1,0]\n"
"v_pk_mul_f32 v[226:227], v[226:227], v[212:213] op_sel_hi:[1,0]\n"
"v_cvt_pk_bf16_f32 v130, v224, v225\n"
"v_cvt_pk_bf16_f32 v131, v226, v227\n"
"v_accvgpr_read_b32 v228, a8\n"
"v_accvgpr_read_b32 v229, a9\n"
"v_accvgpr_read_b32 v230, a10\n"
"v_accvgpr_read_b32 v231, a11\n"
"v_pk_mul_f32 v[228:229], v[228:229], v[212:213] op_sel_hi:[1,0]\n"
"v_pk_mul_f32 v[230:231], v[230:231], v[212:213] op_sel_hi:[1,0]\n"
"v_cvt_pk_bf16_f32 v132, v228, v229\n"
"v_cvt_pk_bf16_f32 v133, v230, v231\n"
"v_accvgpr_read_b32 v232, a12\n"
"v_accvgpr_read_b32 v233, a13\n"
"v_accvgpr_read_b32 v234, a14\n"
"v_accvgpr_read_b32 v235, a15\n"
"v_pk_mul_f32 v[232:233], v[232:233], v[212:213] op_sel_hi:[1,0]\n"
"v_pk_mul_f32 v[234:235], v[234:235], v[212:213] op_sel_hi:[1,0]\n"
"v_cvt_pk_bf16_f32 v134, v232, v233\n"
"v_cvt_pk_bf16_f32 v135, v234, v235\n"
"v_accvgpr_read_b32 v220, a16\n"
"v_accvgpr_read_b32 v221, a17\n"
"v_accvgpr_read_b32 v222, a18\n"
"v_accvgpr_read_b32 v223, a19\n"
"v_pk_mul_f32 v[220:221], v[220:221], v[212:213] op_sel_hi:[1,0]\n"
"v_pk_mul_f32 v[222:223], v[222:223], v[212:213] op_sel_hi:[1,0]\n"
"v_cvt_pk_bf16_f32 v136, v220, v221\n"
"v_cvt_pk_bf16_f32 v137, v222, v223\n"
"v_accvgpr_read_b32 v224, a20\n"
"v_accvgpr_read_b32 v225, a21\n"
"v_accvgpr_read_b32 v226, a22\n"
"v_accvgpr_read_b32 v227, a23\n"
"v_pk_mul_f32 v[224:225], v[224:225], v[212:213] op_sel_hi:[1,0]\n"
"v_pk_mul_f32 v[226:227], v[226:227], v[212:213] op_sel_hi:[1,0]\n"
"v_cvt_pk_bf16_f32 v138, v224, v225\n"
"v_cvt_pk_bf16_f32 v139, v226, v227\n"
"v_accvgpr_read_b32 v228, a24\n"
"v_accvgpr_read_b32 v229, a25\n"
"v_accvgpr_read_b32 v230, a26\n"
"v_accvgpr_read_b32 v231, a27\n"
"v_pk_mul_f32 v[228:229], v[228:229], v[212:213] op_sel_hi:[1,0]\n"
"v_pk_mul_f32 v[230:231], v[230:231], v[212:213] op_sel_hi:[1,0]\n"
"v_cvt_pk_bf16_f32 v140, v228, v229\n"
"v_cvt_pk_bf16_f32 v141, v230, v231\n"
"v_accvgpr_read_b32 v232, a28\n"
"v_accvgpr_read_b32 v233, a29\n"
"v_accvgpr_read_b32 v234, a30\n"
"v_accvgpr_read_b32 v235, a31\n"
"v_pk_mul_f32 v[232:233], v[232:233], v[212:213] op_sel_hi:[1,0]\n"
"v_pk_mul_f32 v[234:235], v[234:235], v[212:213] op_sel_hi:[1,0]\n"
"v_cvt_pk_bf16_f32 v142, v232, v233\n"
"v_cvt_pk_bf16_f32 v143, v234, v235\n"
"v_accvgpr_read_b32 v220, a32\n"
"v_accvgpr_read_b32 v221, a33\n"
"v_accvgpr_read_b32 v222, a34\n"
"v_accvgpr_read_b32 v223, a35\n"
"v_pk_mul_f32 v[220:221], v[220:221], v[214:215] op_sel_hi:[1,0]\n"
"v_pk_mul_f32 v[222:223], v[222:223], v[214:215] op_sel_hi:[1,0]\n"
"v_cvt_pk_bf16_f32 v144, v220, v221\n"
"v_cvt_pk_bf16_f32 v145, v222, v223\n"
"v_accvgpr_read_b32 v224, a36\n"
"v_accvgpr_read_b32 v225, a37\n"
"v_accvgpr_read_b32 v226, a38\n"
"v_accvgpr_read_b32 v227, a39\n"
"v_pk_mul_f32 v[224:225], v[224:225], v[214:215] op_sel_hi:[1,0]\n"
"v_pk_mul_f32 v[226:227], v[226:227], v[214:215] op_sel_hi:[1,0]\n"
"v_cvt_pk_bf16_f32 v146, v224, v225\n"
"v_cvt_pk_bf16_f32 v147, v226, v227\n"
"v_accvgpr_read_b32 v228, a40\n"
"v_accvgpr_read_b32 v229, a41\n"
"v_accvgpr_read_b32 v230, a42\n"
"v_accvgpr_read_b32 v231, a43\n"
"v_pk_mul_f32 v[228:229], v[228:229], v[214:215] op_sel_hi:[1,0]\n"
"v_pk_mul_f32 v[230:231], v[230:231], v[214:215] op_sel_hi:[1,0]\n"
"v_cvt_pk_bf16_f32 v148, v228, v229\n"
"v_cvt_pk_bf16_f32 v149, v230, v231\n"
"v_accvgpr_read_b32 v232, a44\n"
"v_accvgpr_read_b32 v233, a45\n"
"v_accvgpr_read_b32 v234, a46\n"
"v_accvgpr_read_b32 v235, a47\n"
"v_pk_mul_f32 v[232:233], v[232:233], v[214:215] op_sel_hi:[1,0]\n"
"v_pk_mul_f32 v[234:235], v[234:235], v[214:215] op_sel_hi:[1,0]\n"
"v_cvt_pk_bf16_f32 v150, v232, v233\n"
"v_cvt_pk_bf16_f32 v151, v234, v235\n"
"v_accvgpr_read_b32 v220, a48\n"
"v_accvgpr_read_b32 v221, a49\n"
"v_accvgpr_read_b32 v222, a50\n"
"v_accvgpr_read_b32 v223, a51\n"
"v_pk_mul_f32 v[220:221], v[220:221], v[214:215] op_sel_hi:[1,0]\n"
"v_pk_mul_f32 v[222:223], v[222:223], v[214:215] op_sel_hi:[1,0]\n"
"v_cvt_pk_bf16_f32 v152, v220, v221\n"
"v_cvt_pk_bf16_f32 v153, v222, v223\n"
"v_accvgpr_read_b32 v224, a52\n"
"v_accvgpr_read_b32 v225, a53\n"
"v_accvgpr_read_b32 v226, a54\n"
"v_accvgpr_read_b32 v227, a55\n"
"v_pk_mul_f32 v[224:225], v[224:225], v[214:215] op_sel_hi:[1,0]\n"
"v_pk_mul_f32 v[226:227], v[226:227], v[214:215] op_sel_hi:[1,0]\n"
"v_cvt_pk_bf16_f32 v154, v224, v225\n"
"v_cvt_pk_bf16_f32 v155, v226, v227\n"
"v_accvgpr_read_b32 v228, a56\n"
"v_accvgpr_read_b32 v229, a57\n"
"v_accvgpr_read_b32 v230, a58\n"
"v_accvgpr_read_b32 v231, a59\n"
"v_pk_mul_f32 v[228:229], v[228:229], v[214:215] op_sel_hi:[1,0]\n"
"v_pk_mul_f32 v[230:231], v[230:231], v[214:215] op_sel_hi:[1,0]\n"
"v_cvt_pk_bf16_f32 v156, v228, v229\n"
"v_cvt_pk_bf16_f32 v157, v230, v231\n"
"v_accvgpr_read_b32 v232, a60\n"
"v_accvgpr_read_b32 v233, a61\n"
"v_accvgpr_read_b32 v234, a62\n"
"v_accvgpr_read_b32 v235, a63\n"
"v_pk_mul_f32 v[232:233], v[232:233], v[214:215] op_sel_hi:[1,0]\n"
"v_pk_mul_f32 v[234:235], v[234:235], v[214:215] op_sel_hi:[1,0]\n"
"v_cvt_pk_bf16_f32 v158, v232, v233\n"
"v_cvt_pk_bf16_f32 v159, v234, v235\n"
"v_accvgpr_read_b32 v220, a64\n"
"v_accvgpr_read_b32 v221, a65\n"
"v_accvgpr_read_b32 v222, a66\n"
"v_accvgpr_read_b32 v223, a67\n"
"v_pk_mul_f32 v[220:221], v[220:221], v[216:217] op_sel_hi:[1,0]\n"
"v_pk_mul_f32 v[222:223], v[222:223], v[216:217] op_sel_hi:[1,0]\n"
"v_cvt_pk_bf16_f32 v160, v220, v221\n"
"v_cvt_pk_bf16_f32 v161, v222, v223\n"
"v_accvgpr_read_b32 v224, a68\n"
"v_accvgpr_read_b32 v225, a69\n"
"v_accvgpr_read_b32 v226, a70\n"
"v_accvgpr_read_b32 v227, a71\n"
"v_pk_mul_f32 v[224:225], v[224:225], v[216:217] op_sel_hi:[1,0]\n"
"v_pk_mul_f32 v[226:227], v[226:227], v[216:217] op_sel_hi:[1,0]\n"
"v_cvt_pk_bf16_f32 v162, v224, v225\n"
"v_cvt_pk_bf16_f32 v163, v226, v227\n"
"v_accvgpr_read_b32 v228, a72\n"
"v_accvgpr_read_b32 v229, a73\n"
"v_accvgpr_read_b32 v230, a74\n"
"v_accvgpr_read_b32 v231, a75\n"
"v_pk_mul_f32 v[228:229], v[228:229], v[216:217] op_sel_hi:[1,0]\n"
"v_pk_mul_f32 v[230:231], v[230:231], v[216:217] op_sel_hi:[1,0]\n"
"v_cvt_pk_bf16_f32 v164, v228, v229\n"
"v_cvt_pk_bf16_f32 v165, v230, v231\n"
"v_accvgpr_read_b32 v232, a76\n"
"v_accvgpr_read_b32 v233, a77\n"
"v_accvgpr_read_b32 v234, a78\n"
"v_accvgpr_read_b32 v235, a79\n"
"v_pk_mul_f32 v[232:233], v[232:233], v[216:217] op_sel_hi:[1,0]\n"
"v_pk_mul_f32 v[234:235], v[234:235], v[216:217] op_sel_hi:[1,0]\n"
"v_cvt_pk_bf16_f32 v166, v232, v233\n"
"v_cvt_pk_bf16_f32 v167, v234, v235\n"
"v_accvgpr_read_b32 v220, a80\n"
"v_accvgpr_read_b32 v221, a81\n"
"v_accvgpr_read_b32 v222, a82\n"
"v_accvgpr_read_b32 v223, a83\n"
"v_pk_mul_f32 v[220:221], v[220:221], v[216:217] op_sel_hi:[1,0]\n"
"v_pk_mul_f32 v[222:223], v[222:223], v[216:217] op_sel_hi:[1,0]\n"
"v_cvt_pk_bf16_f32 v168, v220, v221\n"
"v_cvt_pk_bf16_f32 v169, v222, v223\n"
"v_accvgpr_read_b32 v224, a84\n"
"v_accvgpr_read_b32 v225, a85\n"
"v_accvgpr_read_b32 v226, a86\n"
"v_accvgpr_read_b32 v227, a87\n"
"v_pk_mul_f32 v[224:225], v[224:225], v[216:217] op_sel_hi:[1,0]\n"
"v_pk_mul_f32 v[226:227], v[226:227], v[216:217] op_sel_hi:[1,0]\n"
"v_cvt_pk_bf16_f32 v170, v224, v225\n"
"v_cvt_pk_bf16_f32 v171, v226, v227\n"
"v_accvgpr_read_b32 v228, a88\n"
"v_accvgpr_read_b32 v229, a89\n"
"v_accvgpr_read_b32 v230, a90\n"
"v_accvgpr_read_b32 v231, a91\n"
"v_pk_mul_f32 v[228:229], v[228:229], v[216:217] op_sel_hi:[1,0]\n"
"v_pk_mul_f32 v[230:231], v[230:231], v[216:217] op_sel_hi:[1,0]\n"
"v_cvt_pk_bf16_f32 v172, v228, v229\n"
"v_cvt_pk_bf16_f32 v173, v230, v231\n"
"v_accvgpr_read_b32 v232, a92\n"
"v_accvgpr_read_b32 v233, a93\n"
"v_accvgpr_read_b32 v234, a94\n"
"v_accvgpr_read_b32 v235, a95\n"
"v_pk_mul_f32 v[232:233], v[232:233], v[216:217] op_sel_hi:[1,0]\n"
"v_pk_mul_f32 v[234:235], v[234:235], v[216:217] op_sel_hi:[1,0]\n"
"v_cvt_pk_bf16_f32 v174, v232, v233\n"
"v_cvt_pk_bf16_f32 v175, v234, v235\n"
"v_accvgpr_read_b32 v220, a96\n"
"v_accvgpr_read_b32 v221, a97\n"
"v_accvgpr_read_b32 v222, a98\n"
"v_accvgpr_read_b32 v223, a99\n"
"v_pk_mul_f32 v[220:221], v[220:221], v[218:219] op_sel_hi:[1,0]\n"
"v_pk_mul_f32 v[222:223], v[222:223], v[218:219] op_sel_hi:[1,0]\n"
"v_cvt_pk_bf16_f32 v176, v220, v221\n"
"v_cvt_pk_bf16_f32 v177, v222, v223\n"
"v_accvgpr_read_b32 v224, a100\n"
"v_accvgpr_read_b32 v225, a101\n"
"v_accvgpr_read_b32 v226, a102\n"
"v_accvgpr_read_b32 v227, a103\n"
"v_pk_mul_f32 v[224:225], v[224:225], v[218:219] op_sel_hi:[1,0]\n"
"v_pk_mul_f32 v[226:227], v[226:227], v[218:219] op_sel_hi:[1,0]\n"
"v_cvt_pk_bf16_f32 v178, v224, v225\n"
"v_cvt_pk_bf16_f32 v179, v226, v227\n"
"v_accvgpr_read_b32 v228, a104\n"
"v_accvgpr_read_b32 v229, a105\n"
"v_accvgpr_read_b32 v230, a106\n"
"v_accvgpr_read_b32 v231, a107\n"
"v_pk_mul_f32 v[228:229], v[228:229], v[218:219] op_sel_hi:[1,0]\n"
"v_pk_mul_f32 v[230:231], v[230:231], v[218:219] op_sel_hi:[1,0]\n"
"v_cvt_pk_bf16_f32 v180, v228, v229\n"
"v_cvt_pk_bf16_f32 v181, v230, v231\n"
"v_accvgpr_read_b32 v232, a108\n"
"v_accvgpr_read_b32 v233, a109\n"
"v_accvgpr_read_b32 v234, a110\n"
"v_accvgpr_read_b32 v235, a111\n"
"v_pk_mul_f32 v[232:233], v[232:233], v[218:219] op_sel_hi:[1,0]\n"
"v_pk_mul_f32 v[234:235], v[234:235], v[218:219] op_sel_hi:[1,0]\n"
"v_cvt_pk_bf16_f32 v182, v232, v233\n"
"v_cvt_pk_bf16_f32 v183, v234, v235\n"
"v_accvgpr_read_b32 v220, a112\n"
"v_accvgpr_read_b32 v221, a113\n"
"v_accvgpr_read_b32 v222, a114\n"
"v_accvgpr_read_b32 v223, a115\n"
"v_pk_mul_f32 v[220:221], v[220:221], v[218:219] op_sel_hi:[1,0]\n"
"v_pk_mul_f32 v[222:223], v[222:223], v[218:219] op_sel_hi:[1,0]\n"
"v_cvt_pk_bf16_f32 v184, v220, v221\n"
"v_cvt_pk_bf16_f32 v185, v222, v223\n"
"v_accvgpr_read_b32 v224, a116\n"
"v_accvgpr_read_b32 v225, a117\n"
"v_accvgpr_read_b32 v226, a118\n"
"v_accvgpr_read_b32 v227, a119\n"
"v_pk_mul_f32 v[224:225], v[224:225], v[218:219] op_sel_hi:[1,0]\n"
"v_pk_mul_f32 v[226:227], v[226:227], v[218:219] op_sel_hi:[1,0]\n"
"v_cvt_pk_bf16_f32 v186, v224, v225\n"
"v_cvt_pk_bf16_f32 v187, v226, v227\n"
"v_accvgpr_read_b32 v228, a120\n"
"v_accvgpr_read_b32 v229, a121\n"
"v_accvgpr_read_b32 v230, a122\n"
"v_accvgpr_read_b32 v231, a123\n"
"v_pk_mul_f32 v[228:229], v[228:229], v[218:219] op_sel_hi:[1,0]\n"
"v_pk_mul_f32 v[230:231], v[230:231], v[218:219] op_sel_hi:[1,0]\n"
"v_cvt_pk_bf16_f32 v188, v228, v229\n"
"v_cvt_pk_bf16_f32 v189, v230, v231\n"
"v_accvgpr_read_b32 v232, a124\n"
"v_accvgpr_read_b32 v233, a125\n"
"v_accvgpr_read_b32 v234, a126\n"
"v_accvgpr_read_b32 v235, a127\n"
"v_pk_mul_f32 v[232:233], v[232:233], v[218:219] op_sel_hi:[1,0]\n"
"v_pk_mul_f32 v[234:235], v[234:235], v[218:219] op_sel_hi:[1,0]\n"
"v_cvt_pk_bf16_f32 v190, v232, v233\n"
"v_cvt_pk_bf16_f32 v191, v234, v235\n"
"s_waitcnt lgkmcnt(0)\n"
"s_nop 0\n"
"v_mfma_f32_16x16x32_bf16 v[0:3], a[128:131], v[128:131], 0\n"
"ds_read_b64_tr_b16 a[208:209], v197 offset:16384\n"
"v_mfma_f32_16x16x32_bf16 v[0:3], a[132:135], v[132:135], v[0:3]\n"
"ds_read_b64_tr_b16 a[210:211], v197 offset:18432\n"
"v_mfma_f32_16x16x32_bf16 v[0:3], a[136:139], v[136:139], v[0:3]\n"
"ds_read_b64_tr_b16 a[212:213], v197 offset:20480\n"
"v_mfma_f32_16x16x32_bf16 v[0:3], a[140:143], v[140:143], v[0:3]\n"
"ds_read_b64_tr_b16 a[214:215], v197 offset:22528\n"
"v_mfma_f32_16x16x32_bf16 v[0:3], a[144:147], v[144:147], v[0:3]\n"
"s_waitcnt vmcnt(18)\n"
"s_barrier\n"
"ds_read_b64_tr_b16 a[216:217], v197 offset:24576\n"
"v_mfma_f32_16x16x32_bf16 v[0:3], a[148:151], v[148:151], v[0:3]\n"
"ds_read_b64_tr_b16 a[218:219], v197 offset:26624\n"
"ds_read_b32 v236, v207\n"
"v_mfma_f32_16x16x32_bf16 v[0:3], a[152:155], v[152:155], v[0:3]\n"
"ds_read_b64_tr_b16 a[220:221], v197 offset:28672\n"
"v_mfma_f32_16x16x32_bf16 v[0:3], a[156:159], v[156:159], v[0:3]\n"
"ds_read_b64_tr_b16 a[222:223], v197 offset:30720\n"
"v_mfma_f32_16x16x32_bf16 v[4:7], a[128:131], v[160:163], 0\n"
"ds_read_b64_tr_b16 a[160:161], v194 offset:256\n"
"v_mfma_f32_16x16x32_bf16 v[4:7], a[132:135], v[164:167], v[4:7]\n"
"ds_read_b64_tr_b16 a[162:163], v194 offset:2304\n"
"v_mfma_f32_16x16x32_bf16 v[4:7], a[136:139], v[168:171], v[4:7]\n"
"ds_read_b64_tr_b16 a[164:165], v194 offset:4352\n"
"v_mfma_f32_16x16x32_bf16 v[4:7], a[140:143], v[172:175], v[4:7]\n"
"ds_read_b64_tr_b16 a[166:167], v194 offset:6400\n"
"v_mfma_f32_16x16x32_bf16 v[4:7], a[144:147], v[176:179], v[4:7]\n"
"ds_read_b64_tr_b16 a[168:169], v194 offset:8448\n"
"v_cvt_pk_bf16_f32 v32, v0, v1\n"
"v_mfma_f32_16x16x32_bf16 v[4:7], a[148:151], v[180:183], v[4:7]\n"
"ds_read_b64_tr_b16 a[170:171], v194 offset:10496\n"
"v_cvt_pk_bf16_f32 v33, v2, v3\n"
"v_mfma_f32_16x16x32_bf16 v[4:7], a[152:155], v[184:187], v[4:7]\n"
"ds_read_b64_tr_b16 a[172:173], v194 offset:12544\n"
"v_mfma_f32_16x16x32_bf16 v[4:7], a[156:159], v[188:191], v[4:7]\n"
"ds_read_b64_tr_b16 a[174:175], v194 offset:14592\n"
"v_mfma_f32_16x16x32_bf16 v[8:11], a[192:195], v[128:131], 0\n"
"s_waitcnt lgkmcnt(10)\n"
"ds_read_b64_tr_b16 a[176:177], v194 offset:16640\n"
"v_cmp_ne_u32 vcc, 0, v236\n"
"v_mfma_f32_16x16x32_bf16 v[8:11], a[196:199], v[132:135], v[8:11]\n"
"ds_read_b64_tr_b16 a[178:179], v194 offset:18688\n"
"v_mfma_f32_16x16x32_bf16 v[8:11], a[200:203], v[136:139], v[8:11]\n"
"s_cbranch_vccnz AT_fb\n"
"ds_read_b64_tr_b16 a[180:181], v194 offset:20736\n"
"v_mfma_f32_16x16x32_bf16 v[8:11], a[204:207], v[140:143], v[8:11]\n"
"ds_read_b64_tr_b16 a[182:183], v194 offset:22784\n"
"v_mfma_f32_16x16x32_bf16 v[8:11], a[208:211], v[144:147], v[8:11]\n"
"ds_read_b64_tr_b16 a[184:185], v194 offset:24832\n"
"v_cvt_pk_bf16_f32 v36, v4, v5\n"
"v_mfma_f32_16x16x32_bf16 v[8:11], a[212:215], v[148:151], v[8:11]\n"
"ds_read_b64_tr_b16 a[186:187], v194 offset:26880\n"
"v_cvt_pk_bf16_f32 v37, v6, v7\n"
"v_mfma_f32_16x16x32_bf16 v[8:11], a[216:219], v[152:155], v[8:11]\n"
"s_waitcnt lgkmcnt(11)\n"
"ds_read_b64_tr_b16 a[188:189], v194 offset:28928\n"
"v_mfma_f32_16x16x32_bf16 v[8:11], a[220:223], v[156:159], v[8:11]\n"
"ds_read_b64_tr_b16 a[190:191], v194 offset:30976\n"
"v_mfma_f32_16x16x32_bf16 v[12:15], a[192:195], v[160:163], 0\n"
"ds_read_b64_tr_b16 a[224:225], v197 offset:256\n"
"v_mfma_f32_16x16x32_bf16 v[12:15], a[196:199], v[164:167], v[12:15]\n"
"ds_read_b64_tr_b16 a[226:227], v197 offset:2304\n"
"v_mfma_f32_16x16x32_bf16 v[12:15], a[200:203], v[168:171], v[12:15]\n"
"ds_read_b64_tr_b16 a[228:229], v197 offset:4352\n"
"v_mfma_f32_16x16x32_bf16 v[12:15], a[204:207], v[172:175], v[12:15]\n"
"ds_read_b64_tr_b16 a[230:231], v197 offset:6400\n"
"v_mfma_f32_16x16x32_bf16 v[12:15], a[208:211], v[176:179], v[12:15]\n"
"ds_read_b64_tr_b16 a[232:233], v197 offset:8448\n"
"v_cvt_pk_bf16_f32 v34, v8, v9\n"
"v_mfma_f32_16x16x32_bf16 v[12:15], a[212:215], v[180:183], v[12:15]\n"
"ds_read_b64_tr_b16 a[234:235], v197 offset:10496\n"
"v_cvt_pk_bf16_f32 v35, v10, v11\n"
"v_mfma_f32_16x16x32_bf16 v[12:15], a[216:219], v[184:187], v[12:15]\n"
"ds_read_b64_tr_b16 a[236:237], v197 offset:12544\n"
"v_mfma_f32_16x16x32_bf16 v[12:15], a[220:223], v[188:191], v[12:15]\n"
"ds_read_b64_tr_b16 a[238:239], v197 offset:14592\n"
"buffer_store_dwordx4 v[32:35], v205, s[32:35], s54 offen offset:0 sc1\n"
"v_mfma_f32_16x16x32_bf16 v[16:19], a[160:163], v[128:131], 0\n"
"s_waitcnt lgkmcnt(11)\n"
"ds_read_b64_tr_b16 a[240:241], v197 offset:16640\n"
"v_mfma_f32_16x16x32_bf16 v[16:19], a[164:167], v[132:135], v[16:19]\n"
"ds_read_b64_tr_b16 a[242:243], v197 offset:18688\n"
"v_mfma_f32_16x16x32_bf16 v[16:19], a[168:171], v[136:139], v[16:19]\n"
"ds_read_b64_tr_b16 a[244:245], v197 offset:20736\n"
"v_mfma_f32_16x16x32_bf16 v[16:19], a[172:175], v[140:143], v[16:19]\n"
"ds_read_b64_tr_b16 a[246:247], v197 offset:22784\n"
"v_mfma_f32_16x16x32_bf16 v[16:19], a[176:179], v[144:147], v[16:19]\n"
"ds_read_b64_tr_b16 a[248:249], v197 offset:24832\n"
"v_cvt_pk_bf16_f32 v38, v12, v13\n"
"v_mfma_f32_16x16x32_bf16 v[16:19], a[180:183], v[148:151], v[16:19]\n"
"s_waitcnt lgkmcnt(11)\n"
"ds_read_b64_tr_b16 a[250:251], v197 offset:26880\n"
"v_cvt_pk_bf16_f32 v39, v14, v15\n"
"v_mfma_f32_16x16x32_bf16 v[16:19], a[184:187], v[152:155], v[16:19]\n"
"ds_read_b64_tr_b16 a[252:253], v197 offset:28928\n"
"v_mfma_f32_16x16x32_bf16 v[16:19], a[188:191], v[156:159], v[16:19]\n"
"ds_read_b64_tr_b16 a[254:255], v197 offset:30976\n"
"buffer_store_dwordx4 v[36:39], v206, s[32:35], s54 offen offset:0 sc1\n"
"v_mfma_f32_16x16x32_bf16 v[20:23], a[160:163], v[160:163], 0\n"
"ds_read_b64_tr_b16 a[128:129], v192 offset:0\n"
"v_mfma_f32_16x16x32_bf16 v[20:23], a[164:167], v[164:167], v[20:23]\n"
"ds_read_b64_tr_b16 a[130:131], v192 offset:2048\n"
"v_mfma_f32_16x16x32_bf16 v[20:23], a[168:171], v[168:171], v[20:23]\n"
"ds_read_b64_tr_b16 a[132:133], v192 offset:4096\n"
"v_mfma_f32_16x16x32_bf16 v[20:23], a[172:175], v[172:175], v[20:23]\n"
"ds_read_b64_tr_b16 a[134:135], v192 offset:6144\n"
"v_mfma_f32_16x16x32_bf16 v[20:23], a[176:179], v[176:179], v[20:23]\n"
"ds_read_b64_tr_b16 a[136:137], v192 offset:8192\n"
"v_cvt_pk_bf16_f32 v40, v16, v17\n"
"v_mfma_f32_16x16x32_bf16 v[20:23], a[180:183], v[180:183], v[20:23]\n"
"ds_read_b64_tr_b16 a[138:139], v192 offset:10240\n"
"v_cvt_pk_bf16_f32 v41, v18, v19\n"
"v_mfma_f32_16x16x32_bf16 v[20:23], a[184:187], v[184:187], v[20:23]\n"
"ds_read_b64_tr_b16 a[140:141], v192 offset:12288\n"
"v_mfma_f32_16x16x32_bf16 v[20:23], a[188:191], v[188:191], v[20:23]\n"
"ds_read_b64_tr_b16 a[142:143], v192 offset:14336\n"
"v_mfma_f32_16x16x32_bf16 v[24:27], a[224:227], v[128:131], 0\n"
"s_waitcnt lgkmcnt(11)\n"
"ds_read_b64_tr_b16 a[144:145], v192 offset:16384\n"
"v_mfma_f32_16x16x32_bf16 v[24:27], a[228:231], v[132:135], v[24:27]\n"
"ds_read_b64_tr_b16 a[146:147], v192 offset:18432\n"
"v_mfma_f32_16x16x32_bf16 v[24:27], a[232:235], v[136:139], v[24:27]\n"
"ds_read_b64_tr_b16 a[148:149], v192 offset:20480\n"
"v_mfma_f32_16x16x32_bf16 v[24:27], a[236:239], v[140:143], v[24:27]\n"
"ds_read_b64_tr_b16 a[150:151], v192 offset:22528\n"
"v_mfma_f32_16x16x32_bf16 v[24:27], a[240:243], v[144:147], v[24:27]\n"
"ds_read_b64_tr_b16 a[152:153], v192 offset:24576\n"
"v_cvt_pk_bf16_f32 v44, v20, v21\n"
"v_mfma_f32_16x16x32_bf16 v[24:27], a[244:247], v[148:151], v[24:27]\n"
"s_waitcnt lgkmcnt(11)\n"
"ds_read_b64_tr_b16 a[154:155], v192 offset:26624\n"
"v_cvt_pk_bf16_f32 v45, v22, v23\n"
"v_mfma_f32_16x16x32_bf16 v[24:27], a[248:251], v[152:155], v[24:27]\n"
"ds_read_b64_tr_b16 a[156:157], v192 offset:28672\n"
"v_mfma_f32_16x16x32_bf16 v[24:27], a[252:255], v[156:159], v[24:27]\n"
"ds_read_b64_tr_b16 a[158:159], v192 offset:30720\n"
"v_mfma_f32_16x16x32_bf16 v[28:31], a[224:227], v[160:163], 0\n"
"ds_read_b64_tr_b16 a[192:193], v195 offset:0\n"
"v_mfma_f32_16x16x32_bf16 v[28:31], a[228:231], v[164:167], v[28:31]\n"
"ds_read_b64_tr_b16 a[194:195], v195 offset:2048\n"
"v_mfma_f32_16x16x32_bf16 v[28:31], a[232:235], v[168:171], v[28:31]\n"
"ds_read_b64_tr_b16 a[196:197], v195 offset:4096\n"
"v_mfma_f32_16x16x32_bf16 v[28:31], a[236:239], v[172:175], v[28:31]\n"
"ds_read_b64_tr_b16 a[198:199], v195 offset:6144\n"
"v_mfma_f32_16x16x32_bf16 v[28:31], a[240:243], v[176:179], v[28:31]\n"
"ds_read_b64_tr_b16 a[200:201], v195 offset:8192\n"
"v_cvt_pk_bf16_f32 v42, v24, v25\n"
"v_mfma_f32_16x16x32_bf16 v[28:31], a[244:247], v[180:183], v[28:31]\n"
"ds_read_b64_tr_b16 a[202:203], v195 offset:10240\n"
"v_cvt_pk_bf16_f32 v43, v26, v27\n"
"v_mfma_f32_16x16x32_bf16 v[28:31], a[248:251], v[184:187], v[28:31]\n"
"ds_read_b64_tr_b16 a[204:205], v195 offset:12288\n"
"v_mfma_f32_16x16x32_bf16 v[28:31], a[252:255], v[188:191], v[28:31]\n"
"buffer_store_dwordx4 v[40:43], v205, s[32:35], s54 offen offset:1024 sc1\n"
"ds_read_b64_tr_b16 a[206:207], v195 offset:14336\n"
"v_mfma_f32_16x16x32_bf16 v[0:3], a[128:131], v[128:131], 0\n"
"s_waitcnt lgkmcnt(11)\n"
"ds_read_b64_tr_b16 a[208:209], v195 offset:16384\n"
"v_mfma_f32_16x16x32_bf16 v[0:3], a[132:135], v[132:135], v[0:3]\n"
"ds_read_b64_tr_b16 a[210:211], v195 offset:18432\n"
"v_mfma_f32_16x16x32_bf16 v[0:3], a[136:139], v[136:139], v[0:3]\n"
"ds_read_b64_tr_b16 a[212:213], v195 offset:20480\n"
"v_mfma_f32_16x16x32_bf16 v[0:3], a[140:143], v[140:143], v[0:3]\n"
"ds_read_b64_tr_b16 a[214:215], v195 offset:22528\n"
"v_mfma_f32_16x16x32_bf16 v[0:3], a[144:147], v[144:147], v[0:3]\n"
"s_waitcnt vmcnt(11)\n"
"s_barrier\n"
"ds_read_b64_tr_b16 a[216:217], v195 offset:24576\n"
"v_cvt_pk_bf16_f32 v46, v28, v29\n"
"v_mfma_f32_16x16x32_bf16 v[0:3], a[148:151], v[148:151], v[0:3]\n"
"s_waitcnt lgkmcnt(11)\n"
"s_add_u32 m0, s53, 98304\n"
"ds_read_b64_tr_b16 a[218:219], v195 offset:26624\n"
"v_cvt_pk_bf16_f32 v47, v30, v31\n"
"v_mfma_f32_16x16x32_bf16 v[0:3], a[152:155], v[152:155], v[0:3]\n"
"ds_read_b64_tr_b16 a[220:221], v195 offset:28672\n"
"buffer_load_dwordx4 v204, s[28:31], s52 offen offset:0 lds\n"
"v_mfma_f32_16x16x32_bf16 v[0:3], a[156:159], v[156:159], v[0:3]\n"
"s_add_u32 s52, s52, 8192\n"
"buffer_store_dwordx4 v[44:47], v206, s[32:35], s54 offen offset:1024 sc1\n"
"ds_read_b64_tr_b16 a[222:223], v195 offset:30720\n"
"v_mfma_f32_16x16x32_bf16 v[4:7], a[128:131], v[160:163], 0\n"
"ds_read_b64_tr_b16 a[160:161], v192 offset:256\n"
"v_mfma_f32_16x16x32_bf16 v[4:7], a[132:135], v[164:167], v[4:7]\n"
"ds_read_b64_tr_b16 a[162:163], v192 offset:2304\n"
"v_mfma_f32_16x16x32_bf16 v[4:7], a[136:139], v[168:171], v[4:7]\n"
"ds_read_b64_tr_b16 a[164:165], v192 offset:4352\n"
"buffer_load_dwordx4 v204, s[28:31], s52 offen offset:1024 lds\n"
"v_mfma_f32_16x16x32_bf16 v[4:7], a[140:143], v[172:175], v[4:7]\n"
"s_add_u32 s52, s52, 8192\n"
"ds_read_b64_tr_b16 a[166:167], v192 offset:6400\n"
"v_mfma_f32_16x16x32_bf16 v[4:7], a[144:147], v[176:179], v[4:7]\n"
"ds_read_b64_tr_b16 a[168:169], v192 offset:8448\n"
"v_cvt_pk_bf16_f32 v32, v0, v1\n"
"v_mfma_f32_16x16x32_bf16 v[4:7], a[148:151], v[180:183], v[4:7]\n"
"ds_read_b64_tr_b16 a[170:171], v192 offset:10496\n"
"v_cvt_pk_bf16_f32 v33, v2, v3\n"
"v_mfma_f32_16x16x32_bf16 v[4:7], a[152:155], v[184:187], v[4:7]\n"
"ds_read_b64_tr_b16 a[172:173], v192 offset:12544\n"
"buffer_load_dwordx4 v204, s[28:31], s52 offen offset:2048 lds\n"
"v_mfma_f32_16x16x32_bf16 v[4:7], a[156:159], v[188:191], v[4:7]\n"
"s_add_u32 s52, s52, 8192\n"
"ds_read_b64_tr_b16 a[174:175], v192 offset:14592\n"
"v_mfma_f32_16x16x32_bf16 v[8:11], a[192:195], v[128:131], 0\n"
"s_waitcnt lgkmcnt(11)\n"
"ds_read_b64_tr_b16 a[176:177], v192 offset:16640\n"
"v_mfma_f32_16x16x32_bf16 v[8:11], a[196:199], v[132:135], v[8:11]\n"
"ds_read_b64_tr_b16 a[178:179], v192 offset:18688\n"
"v_mfma_f32_16x16x32_bf16 v[8:11], a[200:203], v[136:139], v[8:11]\n"
"ds_read_b64_tr_b16 a[180:181], v192 offset:20736\n"
"buffer_load_dwordx4 v204, s[28:31], s52 offen offset:3072 lds\n"
"v_mfma_f32_16x16x32_bf16 v[8:11], a[204:207], v[140:143], v[8:11]\n"
"s_add_u32 s52, s52, 12288\n"
"ds_read_b64_tr_b16 a[182:183], v192 offset:22784\n"
"v_mfma_f32_16x16x32_bf16 v[8:11], a[208:211], v[144:147], v[8:11]\n"
"ds_read_b64_tr_b16 a[184:185], v192 offset:24832\n"
"v_cvt_pk_bf16_f32 v36, v4, v5\n"
"v_mfma_f32_16x16x32_bf16 v[8:11], a[212:215], v[148:151], v[8:11]\n"
"s_waitcnt lgkmcnt(11)\n"
"s_add_u32 m0, s53, 102400\n"
"ds_read_b64_tr_b16 a[186:187], v192 offset:26880\n"
"v_cvt_pk_bf16_f32 v37, v6, v7\n"
"v_mfma_f32_16x16x32_bf16 v[8:11], a[216:219], v[152:155], v[8:11]\n"
"ds_read_b64_tr_b16 a[188:189], v192 offset:28928\n"
"buffer_load_dwordx4 v204, s[28:31], s52 offen offset:0 lds\n"
"v_mfma_f32_16x16x32_bf16 v[8:11], a[220:223], v[156:159], v[8:11]\n"
"s_add_u32 s52, s52, 8192\n"
"ds_read_b64_tr_b16 a[190:191], v192 offset:30976\n"
"v_mfma_f32_16x16x32_bf16 v[12:15], a[192:195], v[160:163], 0\n"
"ds_read_b64_tr_b16 a[224:225], v195 offset:256\n"
"v_mfma_f32_16x16x32_bf16 v[12:15], a[196:199], v[164:167], v[12:15]\n"
"ds_read_b64_tr_b16 a[226:227], v195 offset:2304\n"
"v_mfma_f32_16x16x32_bf16 v[12:15], a[200:203], v[168:171], v[12:15]\n"
"ds_read_b64_tr_b16 a[228:229], v195 offset:4352\n"
"buffer_load_dwordx4 v204, s[28:31], s52 offen offset:1024 lds\n"
"v_mfma_f32_16x16x32_bf16 v[12:15], a[204:207], v[172:175], v[12:15]\n"
"s_add_u32 s52, s52, 8192\n"
"ds_read_b64_tr_b16 a[230:231], v195 offset:6400\n"
"v_mfma_f32_16x16x32_bf16 v[12:15], a[208:211], v[176:179], v[12:15]\n"
"ds_read_b64_tr_b16 a[232:233], v195 offset:8448\n"
"v_cvt_pk_bf16_f32 v34, v8, v9\n"
"v_mfma_f32_16x16x32_bf16 v[12:15], a[212:215], v[180:183], v[12:15]\n"
"ds_read_b64_tr_b16 a[234:235], v195 offset:10496\n"
"v_cvt_pk_bf16_f32 v35, v10, v11\n"
"v_mfma_f32_16x16x32_bf16 v[12:15], a[216:219], v[184:187], v[12:15]\n"
"ds_read_b64_tr_b16 a[236:237], v195 offset:12544\n"
"buffer_load_dwordx4 v204, s[28:31], s52 offen offset:2048 lds\n"
"v_mfma_f32_16x16x32_bf16 v[12:15], a[220:223], v[188:191], v[12:15]\n"
"s_add_u32 s52, s52, 8192\n"
"ds_read_b64_tr_b16 a[238:239], v195 offset:14592\n"
"buffer_store_dwordx4 v[32:35], v205, s[32:35], s54 offen offset:2048 sc1\n"
"v_mfma_f32_16x16x32_bf16 v[16:19], a[160:163], v[128:131], 0\n"
"s_waitcnt lgkmcnt(11)\n"
"ds_read_b64_tr_b16 a[240:241], v195 offset:16640\n"
"v_mfma_f32_16x16x32_bf16 v[16:19], a[164:167], v[132:135], v[16:19]\n"
"ds_read_b64_tr_b16 a[242:243], v195 offset:18688\n"
"v_mfma_f32_16x16x32_bf16 v[16:19], a[168:171], v[136:139], v[16:19]\n"
"ds_read_b64_tr_b16 a[244:245], v195 offset:20736\n"
"buffer_load_dwordx4 v204, s[28:31], s52 offen offset:3072 lds\n"
"v_mfma_f32_16x16x32_bf16 v[16:19], a[172:175], v[140:143], v[16:19]\n"
"s_sub_u32 s52, s52, 61312\n"
"ds_read_b64_tr_b16 a[246:247], v195 offset:22784\n"
"v_mfma_f32_16x16x32_bf16 v[16:19], a[176:179], v[144:147], v[16:19]\n"
"ds_read_b64_tr_b16 a[248:249], v195 offset:24832\n"
"v_cvt_pk_bf16_f32 v38, v12, v13\n"
"v_mfma_f32_16x16x32_bf16 v[16:19], a[180:183], v[148:151], v[16:19]\n"
"s_waitcnt lgkmcnt(11)\n"
"s_add_u32 m0, s53, 131072\n"
"ds_read_b64_tr_b16 a[250:251], v195 offset:26880\n"
"v_cvt_pk_bf16_f32 v39, v14, v15\n"
"v_mfma_f32_16x16x32_bf16 v[16:19], a[184:187], v[152:155], v[16:19]\n"
"ds_read_b64_tr_b16 a[252:253], v195 offset:28928\n"
"buffer_load_dwordx4 v204, s[28:31], s52 offen offset:0 lds\n"
"v_mfma_f32_16x16x32_bf16 v[16:19], a[188:191], v[156:159], v[16:19]\n"
"s_add_u32 s52, s52, 8192\n"
"ds_read_b64_tr_b16 a[254:255], v195 offset:30976\n"
"buffer_store_dwordx4 v[36:39], v206, s[32:35], s54 offen offset:2048 sc1\n"
"v_mfma_f32_16x16x32_bf16 v[20:23], a[160:163], v[160:163], 0\n"
"ds_read_b64_tr_b16 a[128:129], v192 offset:32768\n"
"v_mfma_f32_16x16x32_bf16 v[20:23], a[164:167], v[164:167], v[20:23]\n"
"ds_read_b64_tr_b16 a[130:131], v192 offset:34816\n"
"v_mfma_f32_16x16x32_bf16 v[20:23], a[168:171], v[168:171], v[20:23]\n"
"ds_read_b64_tr_b16 a[132:133], v192 offset:36864\n"
"buffer_load_dwordx4 v204, s[28:31], s52 offen offset:1024 lds\n"
"v_mfma_f32_16x16x32_bf16 v[20:23], a[172:175], v[172:175], v[20:23]\n"
"s_add_u32 s52, s52, 8192\n"
"ds_read_b64_tr_b16 a[134:135], v192 offset:38912\n"
"v_mfma_f32_16x16x32_bf16 v[20:23], a[176:179], v[176:179], v[20:23]\n"
"ds_read_b64_tr_b16 a[136:137], v192 offset:40960\n"
"v_cvt_pk_bf16_f32 v40, v16, v17\n"
"v_mfma_f32_16x16x32_bf16 v[20:23], a[180:183], v[180:183], v[20:23]\n"
"ds_read_b64_tr_b16 a[138:139], v192 offset:43008\n"
"v_cvt_pk_bf16_f32 v41, v18, v19\n"
"v_mfma_f32_16x16x32_bf16 v[20:23], a[184:187], v[184:187], v[20:23]\n"
"ds_read_b64_tr_b16 a[140:141], v192 offset:45056\n"
"buffer_load_dwordx4 v204, s[28:31], s52 offen offset:2048 lds\n"
"v_mfma_f32_16x16x32_bf16 v[20:23], a[188:191], v[188:191], v[20:23]\n"
"s_add_u32 s52, s52, 8192\n"
"ds_read_b64_tr_b16 a[142:143], v192 offset:47104\n"
"v_mfma_f32_16x16x32_bf16 v[24:27], a[224:227], v[128:131], 0\n"
"s_waitcnt lgkmcnt(11)\n"
"ds_read_b64_tr_b16 a[144:145], v192 offset:49152\n"
"v_mfma_f32_16x16x32_bf16 v[24:27], a[228:231], v[132:135], v[24:27]\n"
"ds_read_b64_tr_b16 a[146:147], v192 offset:51200\n"
"v_mfma_f32_16x16x32_bf16 v[24:27], a[232:235], v[136:139], v[24:27]\n"
"ds_read_b64_tr_b16 a[148:149], v192 offset:53248\n"
"buffer_load_dwordx4 v204, s[28:31], s52 offen offset:3072 lds\n"
"v_mfma_f32_16x16x32_bf16 v[24:27], a[236:239], v[140:143], v[24:27]\n"
"s_add_u32 s52, s52, 12288\n"
"ds_read_b64_tr_b16 a[150:151], v192 offset:55296\n"
"v_mfma_f32_16x16x32_bf16 v[24:27], a[240:243], v[144:147], v[24:27]\n"
"ds_read_b64_tr_b16 a[152:153], v192 offset:57344\n"
"v_cvt_pk_bf16_f32 v44, v20, v21\n"
"v_mfma_f32_16x16x32_bf16 v[24:27], a[244:247], v[148:151], v[24:27]\n"
"s_waitcnt lgkmcnt(11)\n"
"s_add_u32 m0, s53, 135168\n"
"ds_read_b64_tr_b16 a[154:155], v192 offset:59392\n"
"v_cvt_pk_bf16_f32 v45, v22, v23\n"
"v_mfma_f32_16x16x32_bf16 v[24:27], a[248:251], v[152:155], v[24:27]\n"
"ds_read_b64_tr_b16 a[156:157], v192 offset:61440\n"
"buffer_load_dwordx4 v204, s[28:31], s52 offen offset:0 lds\n"
"v_mfma_f32_16x16x32_bf16 v[24:27], a[252:255], v[156:159], v[24:27]\n"
"s_add_u32 s52, s52, 8192\n"
"ds_read_b64_tr_b16 a[158:159], v192 offset:63488\n"
"v_mfma_f32_16x16x32_bf16 v[28:31], a[224:227], v[160:163], 0\n"
"ds_read_b64_tr_b16 a[192:193], v195 offset:32768\n"
"v_mfma_f32_16x16x32_bf16 v[28:31], a[228:231], v[164:167], v[28:31]\n"
"ds_read_b64_tr_b16 a[194:195], v195 offset:34816\n"
"v_mfma_f32_16x16x32_bf16 v[28:31], a[232:235], v[168:171], v[28:31]\n"
"ds_read_b64_tr_b16 a[196:197], v195 offset:36864\n"
"buffer_load_dwordx4 v204, s[28:31], s52 offen offset:1024 lds\n"
"v_mfma_f32_16x16x32_bf16 v[28:31], a[236:239], v[172:175], v[28:31]\n"
"s_add_u32 s52, s52, 8192\n"
"ds_read_b64_tr_b16 a[198:199], v195 offset:38912\n"
"v_mfma_f32_16x16x32_bf16 v[28:31], a[240:243], v[176:179], v[28:31]\n"
"ds_read_b64_tr_b16 a[200:201], v195 offset:40960\n"
"v_cvt_pk_bf16_f32 v42, v24, v25\n"
"v_mfma_f32_16x16x32_bf16 v[28:31], a[244:247], v[180:183], v[28:31]\n"
"ds_read_b64_tr_b16 a[202:203], v195 offset:43008\n"
"v_cvt_pk_bf16_f32 v43, v26, v27\n"
"v_mfma_f32_16x16x32_bf16 v[28:31], a[248:251], v[184:187], v[28:31]\n"
"ds_read_b64_tr_b16 a[204:205], v195 offset:45056\n"
"buffer_load_dwordx4 v204, s[28:31], s52 offen offset:2048 lds\n"
"v_mfma_f32_16x16x32_bf16 v[28:31], a[252:255], v[188:191], v[28:31]\n"
"s_add_u32 s52, s52, 8192\n"
"buffer_store_dwordx4 v[40:43], v205, s[32:35], s54 offen offset:3072 sc1\n"
"ds_read_b64_tr_b16 a[206:207], v195 offset:47104\n"
"v_mfma_f32_16x16x32_bf16 v[0:3], a[128:131], v[128:131], 0\n"
"s_waitcnt lgkmcnt(11)\n"
"ds_read_b64_tr_b16 a[208:209], v195 offset:49152\n"
"v_mfma_f32_16x16x32_bf16 v[0:3], a[132:135], v[132:135], v[0:3]\n"
"ds_read_b64_tr_b16 a[210:211], v195 offset:51200\n"
"v_mfma_f32_16x16x32_bf16 v[0:3], a[136:139], v[136:139], v[0:3]\n"
"ds_read_b64_tr_b16 a[212:213], v195 offset:53248\n"
"buffer_load_dwordx4 v204, s[28:31], s52 offen offset:3072 lds\n"
"v_mfma_f32_16x16x32_bf16 v[0:3], a[140:143], v[140:143], v[0:3]\n"
"s_sub_u32 s52, s52, 61312\n"
"ds_read_b64_tr_b16 a[214:215], v195 offset:55296\n"
"v_mfma_f32_16x16x32_bf16 v[0:3], a[144:147], v[144:147], v[0:3]\n"
"s_waitcnt vmcnt(23)\n"
"s_barrier\n"
"ds_read_b64_tr_b16 a[216:217], v195 offset:57344\n"
"v_cvt_pk_bf16_f32 v46, v28, v29\n"
"v_mfma_f32_16x16x32_bf16 v[0:3], a[148:151], v[148:151], v[0:3]\n"
"s_waitcnt lgkmcnt(11)\n"
"s_mov_b32 m0, s53\n"
"ds_read_b64_tr_b16 a[218:219], v195 offset:59392\n"
"v_cvt_pk_bf16_f32 v47, v30, v31\n"
"v_mfma_f32_16x16x32_bf16 v[0:3], a[152:155], v[152:155], v[0:3]\n"
"ds_read_b64_tr_b16 a[220:221], v195 offset:61440\n"
"buffer_load_dwordx4 v204, s[28:31], s52 offen offset:0 lds\n"
"v_mfma_f32_16x16x32_bf16 v[0:3], a[156:159], v[156:159], v[0:3]\n"
"s_add_u32 s52, s52, 8192\n"
"buffer_store_dwordx4 v[44:47], v206, s[32:35], s54 offen offset:3072 sc1\n"
"ds_read_b64_tr_b16 a[222:223], v195 offset:63488\n"
"v_mfma_f32_16x16x32_bf16 v[4:7], a[128:131], v[160:163], 0\n"
"s_add_u32 s54, s54, 4096\n"
"ds_read_b64_tr_b16 a[160:161], v192 offset:33024\n"
"v_mfma_f32_16x16x32_bf16 v[4:7], a[132:135], v[164:167], v[4:7]\n"
"ds_read_b64_tr_b16 a[162:163], v192 offset:35072\n"
"v_mfma_f32_16x16x32_bf16 v[4:7], a[136:139], v[168:171], v[4:7]\n"
"ds_read_b64_tr_b16 a[164:165], v192 offset:37120\n"
"buffer_load_dwordx4 v204, s[28:31], s52 offen offset:1024 lds\n"
"v_mfma_f32_16x16x32_bf16 v[4:7], a[140:143], v[172:175], v[4:7]\n"
"s_add_u32 s52, s52, 8192\n"
"ds_read_b64_tr_b16 a[166:167], v192 offset:39168\n"
"v_mfma_f32_16x16x32_bf16 v[4:7], a[144:147], v[176:179], v[4:7]\n"
"ds_read_b64_tr_b16 a[168:169], v192 offset:41216\n"
"v_cvt_pk_bf16_f32 v32, v0, v1\n"
"v_mfma_f32_16x16x32_bf16 v[4:7], a[148:151], v[180:183], v[4:7]\n"
"ds_read_b64_tr_b16 a[170:171], v192 offset:43264\n"
"v_cvt_pk_bf16_f32 v33, v2, v3\n"
"v_mfma_f32_16x16x32_bf16 v[4:7], a[152:155], v[184:187], v[4:7]\n"
"ds_read_b64_tr_b16 a[172:173], v192 offset:45312\n"
"buffer_load_dwordx4 v204, s[28:31], s52 offen offset:2048 lds\n"
"v_mfma_f32_16x16x32_bf16 v[4:7], a[156:159], v[188:191], v[4:7]\n"
"s_add_u32 s52, s52, 8192\n"
"ds_read_b64_tr_b16 a[174:175], v192 offset:47360\n"
"v_mfma_f32_16x16x32_bf16 v[8:11], a[192:195], v[128:131], 0\n"
"s_waitcnt lgkmcnt(11)\n"
"ds_read_b64_tr_b16 a[176:177], v192 offset:49408\n"
"v_mfma_f32_16x16x32_bf16 v[8:11], a[196:199], v[132:135], v[8:11]\n"
"ds_read_b64_tr_b16 a[178:179], v192 offset:51456\n"
"v_mfma_f32_16x16x32_bf16 v[8:11], a[200:203], v[136:139], v[8:11]\n"
"ds_read_b64_tr_b16 a[180:181], v192 offset:53504\n"
"buffer_load_dwordx4 v204, s[28:31], s52 offen offset:3072 lds\n"
"v_mfma_f32_16x16x32_bf16 v[8:11], a[204:207], v[140:143], v[8:11]\n"
"s_add_u32 s52, s52, 12288\n"
"ds_read_b64_tr_b16 a[182:183], v192 offset:55552\n"
"v_mfma_f32_16x16x32_bf16 v[8:11], a[208:211], v[144:147], v[8:11]\n"
"ds_read_b64_tr_b16 a[184:185], v192 offset:57600\n"
"v_cvt_pk_bf16_f32 v36, v4, v5\n"
"v_mfma_f32_16x16x32_bf16 v[8:11], a[212:215], v[148:151], v[8:11]\n"
"s_waitcnt lgkmcnt(11)\n"
"s_add_u32 m0, s53, 4096\n"
"ds_read_b64_tr_b16 a[186:187], v192 offset:59648\n"
"v_cvt_pk_bf16_f32 v37, v6, v7\n"
"v_mfma_f32_16x16x32_bf16 v[8:11], a[216:219], v[152:155], v[8:11]\n"
"ds_read_b64_tr_b16 a[188:189], v192 offset:61696\n"
"buffer_load_dwordx4 v204, s[28:31], s52 offen offset:0 lds\n"
"v_mfma_f32_16x16x32_bf16 v[8:11], a[220:223], v[156:159], v[8:11]\n"
"s_add_u32 s52, s52, 8192\n"
"ds_read_b64_tr_b16 a[190:191], v192 offset:63744\n"
"v_mfma_f32_16x16x32_bf16 v[12:15], a[192:195], v[160:163], 0\n"
"ds_read_b64_tr_b16 a[224:225], v195 offset:33024\n"
"v_mfma_f32_16x16x32_bf16 v[12:15], a[196:199], v[164:167], v[12:15]\n"
"ds_read_b64_tr_b16 a[226:227], v195 offset:35072\n"
"v_mfma_f32_16x16x32_bf16 v[12:15], a[200:203], v[168:171], v[12:15]\n"
"ds_read_b64_tr_b16 a[228:229], v195 offset:37120\n"
"buffer_load_dwordx4 v204, s[28:31], s52 offen offset:1024 lds\n"
"v_mfma_f32_16x16x32_bf16 v[12:15], a[204:207], v[172:175], v[12:15]\n"
"s_add_u32 s52, s52, 8192\n"
"ds_read_b64_tr_b16 a[230:231], v195 offset:39168\n"
"v_mfma_f32_16x16x32_bf16 v[12:15], a[208:211], v[176:179], v[12:15]\n"
"ds_read_b64_tr_b16 a[232:233], v195 offset:41216\n"
"v_cvt_pk_bf16_f32 v34, v8, v9\n"
"v_mfma_f32_16x16x32_bf16 v[12:15], a[212:215], v[180:183], v[12:15]\n"
"ds_read_b64_tr_b16 a[234:235], v195 offset:43264\n"
"v_cvt_pk_bf16_f32 v35, v10, v11\n"
"v_mfma_f32_16x16x32_bf16 v[12:15], a[216:219], v[184:187], v[12:15]\n"
"ds_read_b64_tr_b16 a[236:237], v195 offset:45312\n"
"buffer_load_dwordx4 v204, s[28:31], s52 offen offset:2048 lds\n"
"v_mfma_f32_16x16x32_bf16 v[12:15], a[220:223], v[188:191], v[12:15]\n"
"s_add_u32 s52, s52, 8192\n"
"ds_read_b64_tr_b16 a[238:239], v195 offset:47360\n"
"buffer_store_dwordx4 v[32:35], v205, s[32:35], s54 offen offset:0 sc1\n"
"v_mfma_f32_16x16x32_bf16 v[16:19], a[160:163], v[128:131], 0\n"
"s_waitcnt lgkmcnt(11)\n"
"ds_read_b64_tr_b16 a[240:241], v195 offset:49408\n"
"v_mfma_f32_16x16x32_bf16 v[16:19], a[164:167], v[132:135], v[16:19]\n"
"ds_read_b64_tr_b16 a[242:243], v195 offset:51456\n"
"v_mfma_f32_16x16x32_bf16 v[16:19], a[168:171], v[136:139], v[16:19]\n"
"ds_read_b64_tr_b16 a[244:245], v195 offset:53504\n"
"buffer_load_dwordx4 v204, s[28:31], s52 offen offset:3072 lds\n"
"v_mfma_f32_16x16x32_bf16 v[16:19], a[172:175], v[140:143], v[16:19]\n"
"s_sub_u32 s52, s52, 61312\n"
"ds_read_b64_tr_b16 a[246:247], v195 offset:55552\n"
"v_mfma_f32_16x16x32_bf16 v[16:19], a[176:179], v[144:147], v[16:19]\n"
"ds_read_b64_tr_b16 a[248:249], v195 offset:57600\n"
"v_cvt_pk_bf16_f32 v38, v12, v13\n"
"v_mfma_f32_16x16x32_bf16 v[16:19], a[180:183], v[148:151], v[16:19]\n"
"s_waitcnt lgkmcnt(11)\n"
"ds_read_b64_tr_b16 a[250:251], v195 offset:59648\n"
"v_cvt_pk_bf16_f32 v39, v14, v15\n"
"v_mfma_f32_16x16x32_bf16 v[16:19], a[184:187], v[152:155], v[16:19]\n"
"ds_read_b64_tr_b16 a[252:253], v195 offset:61696\n"
"v_mfma_f32_16x16x32_bf16 v[16:19], a[188:191], v[156:159], v[16:19]\n"
"ds_read_b64_tr_b16 a[254:255], v195 offset:63744\n"
"buffer_store_dwordx4 v[36:39], v206, s[32:35], s54 offen offset:0 sc1\n"
"v_mfma_f32_16x16x32_bf16 v[20:23], a[160:163], v[160:163], 0\n"
"ds_read_b64_tr_b16 a[128:129], v193 offset:0\n"
"v_mfma_f32_16x16x32_bf16 v[20:23], a[164:167], v[164:167], v[20:23]\n"
"ds_read_b64_tr_b16 a[130:131], v193 offset:2048\n"
"v_mfma_f32_16x16x32_bf16 v[20:23], a[168:171], v[168:171], v[20:23]\n"
"ds_read_b64_tr_b16 a[132:133], v193 offset:4096\n"
"v_mfma_f32_16x16x32_bf16 v[20:23], a[172:175], v[172:175], v[20:23]\n"
"ds_read_b64_tr_b16 a[134:135], v193 offset:6144\n"
"v_mfma_f32_16x16x32_bf16 v[20:23], a[176:179], v[176:179], v[20:23]\n"
"ds_read_b64_tr_b16 a[136:137], v193 offset:8192\n"
"v_cvt_pk_bf16_f32 v40, v16, v17\n"
"v_mfma_f32_16x16x32_bf16 v[20:23], a[180:183], v[180:183], v[20:23]\n"
"ds_read_b64_tr_b16 a[138:139], v193 offset:10240\n"
"v_cvt_pk_bf16_f32 v41, v18, v19\n"
"v_mfma_f32_16x16x32_bf16 v[20:23], a[184:187], v[184:187], v[20:23]\n"
"ds_read_b64_tr_b16 a[140:141], v193 offset:12288\n"
"v_mfma_f32_16x16x32_bf16 v[20:23], a[188:191], v[188:191], v[20:23]\n"
"ds_read_b64_tr_b16 a[142:143], v193 offset:14336\n"
"v_mfma_f32_16x16x32_bf16 v[24:27], a[224:227], v[128:131], 0\n"
"s_waitcnt lgkmcnt(11)\n"
"ds_read_b64_tr_b16 a[144:145], v193 offset:16384\n"
"v_mfma_f32_16x16x32_bf16 v[24:27], a[228:231], v[132:135], v[24:27]\n"
"ds_read_b64_tr_b16 a[146:147], v193 offset:18432\n"
"v_mfma_f32_16x16x32_bf16 v[24:27], a[232:235], v[136:139], v[24:27]\n"
"ds_read_b64_tr_b16 a[148:149], v193 offset:20480\n"
"v_mfma_f32_16x16x32_bf16 v[24:27], a[236:239], v[140:143], v[24:27]\n"
"ds_read_b64_tr_b16 a[150:151], v193 offset:22528\n"
"v_mfma_f32_16x16x32_bf16 v[24:27], a[240:243], v[144:147], v[24:27]\n"
"ds_read_b64_tr_b16 a[152:153], v193 offset:24576\n"
"v_cvt_pk_bf16_f32 v44, v20, v21\n"
"v_mfma_f32_16x16x32_bf16 v[24:27], a[244:247], v[148:151], v[24:27]\n"
"s_waitcnt lgkmcnt(11)\n"
"ds_read_b64_tr_b16 a[154:155], v193 offset:26624\n"
"v_cvt_pk_bf16_f32 v45, v22, v23\n"
"v_mfma_f32_16x16x32_bf16 v[24:27], a[248:251], v[152:155], v[24:27]\n"
"ds_read_b64_tr_b16 a[156:157], v193 offset:28672\n"
"v_mfma_f32_16x16x32_bf16 v[24:27], a[252:255], v[156:159], v[24:27]\n"
"ds_read_b64_tr_b16 a[158:159], v193 offset:30720\n"
"v_mfma_f32_16x16x32_bf16 v[28:31], a[224:227], v[160:163], 0\n"
"ds_read_b64_tr_b16 a[192:193], v196 offset:0\n"
"v_mfma_f32_16x16x32_bf16 v[28:31], a[228:231], v[164:167], v[28:31]\n"
"ds_read_b64_tr_b16 a[194:195], v196 offset:2048\n"
"v_mfma_f32_16x16x32_bf16 v[28:31], a[232:235], v[168:171], v[28:31]\n"
"ds_read_b64_tr_b16 a[196:197], v196 offset:4096\n"
"v_mfma_f32_16x16x32_bf16 v[28:31], a[236:239], v[172:175], v[28:31]\n"
"ds_read_b64_tr_b16 a[198:199], v196 offset:6144\n"
"v_mfma_f32_16x16x32_bf16 v[28:31], a[240:243], v[176:179], v[28:31]\n"
"ds_read_b64_tr_b16 a[200:201], v196 offset:8192\n"
"v_cvt_pk_bf16_f32 v42, v24, v25\n"
"v_mfma_f32_16x16x32_bf16 v[28:31], a[244:247], v[180:183], v[28:31]\n"
"ds_read_b64_tr_b16 a[202:203], v196 offset:10240\n"
"v_cvt_pk_bf16_f32 v43, v26, v27\n"
"v_mfma_f32_16x16x32_bf16 v[28:31], a[248:251], v[184:187], v[28:31]\n"
"ds_read_b64_tr_b16 a[204:205], v196 offset:12288\n"
"v_mfma_f32_16x16x32_bf16 v[28:31], a[252:255], v[188:191], v[28:31]\n"
"buffer_store_dwordx4 v[40:43], v205, s[32:35], s54 offen offset:1024 sc1\n"
"ds_read_b64_tr_b16 a[206:207], v196 offset:14336\n"
"v_mfma_f32_16x16x32_bf16 v[0:3], a[128:131], v[128:131], 0\n"
"s_waitcnt lgkmcnt(11)\n"
"ds_read_b64_tr_b16 a[208:209], v196 offset:16384\n"
"v_mfma_f32_16x16x32_bf16 v[0:3], a[132:135], v[132:135], v[0:3]\n"
"ds_read_b64_tr_b16 a[210:211], v196 offset:18432\n"
"v_mfma_f32_16x16x32_bf16 v[0:3], a[136:139], v[136:139], v[0:3]\n"
"ds_read_b64_tr_b16 a[212:213], v196 offset:20480\n"
"v_mfma_f32_16x16x32_bf16 v[0:3], a[140:143], v[140:143], v[0:3]\n"
"ds_read_b64_tr_b16 a[214:215], v196 offset:22528\n"
"v_mfma_f32_16x16x32_bf16 v[0:3], a[144:147], v[144:147], v[0:3]\n"
"s_waitcnt vmcnt(22)\n"
"s_barrier\n"
"ds_read_b64_tr_b16 a[216:217], v196 offset:24576\n"
"v_cvt_pk_bf16_f32 v46, v28, v29\n"
"v_mfma_f32_16x16x32_bf16 v[0:3], a[148:151], v[148:151], v[0:3]\n"
"s_waitcnt lgkmcnt(11)\n"
"s_add_u32 m0, s53, 32768\n"
"ds_read_b64_tr_b16 a[218:219], v196 offset:26624\n"
"v_cvt_pk_bf16_f32 v47, v30, v31\n"
"v_mfma_f32_16x16x32_bf16 v[0:3], a[152:155], v[152:155], v[0:3]\n"
"ds_read_b64_tr_b16 a[220:221], v196 offset:28672\n"
"buffer_load_dwordx4 v204, s[28:31], s52 offen offset:0 lds\n"
"v_mfma_f32_16x16x32_bf16 v[0:3], a[156:159], v[156:159], v[0:3]\n"
"s_add_u32 s52, s52, 8192\n"
"buffer_store_dwordx4 v[44:47], v206, s[32:35], s54 offen offset:1024 sc1\n"
"ds_read_b64_tr_b16 a[222:223], v196 offset:30720\n"
"v_mfma_f32_16x16x32_bf16 v[4:7], a[128:131], v[160:163], 0\n"
"ds_read_b64_tr_b16 a[160:161], v193 offset:256\n"
"v_mfma_f32_16x16x32_bf16 v[4:7], a[132:135], v[164:167], v[4:7]\n"
"ds_read_b64_tr_b16 a[162:163], v193 offset:2304\n"
"v_mfma_f32_16x16x32_bf16 v[4:7], a[136:139], v[168:171], v[4:7]\n"
"ds_read_b64_tr_b16 a[164:165], v193 offset:4352\n"
"buffer_load_dwordx4 v204, s[28:31], s52 offen offset:1024 lds\n"
"v_mfma_f32_16x16x32_bf16 v[4:7], a[140:143], v[172:175], v[4:7]\n"
"s_add_u32 s52, s52, 8192\n"
"ds_read_b64_tr_b16 a[166:167], v193 offset:6400\n"
"v_mfma_f32_16x16x32_bf16 v[4:7], a[144:147], v[176:179], v[4:7]\n"
"ds_read_b64_tr_b16 a[168:169], v193 offset:8448\n"
"v_cvt_pk_bf16_f32 v32, v0, v1\n"
"v_mfma_f32_16x16x32_bf16 v[4:7], a[148:151], v[180:183], v[4:7]\n"
"ds_read_b64_tr_b16 a[170:171], v193 offset:10496\n"
"v_cvt_pk_bf16_f32 v33, v2, v3\n"
"v_mfma_f32_16x16x32_bf16 v[4:7], a[152:155], v[184:187], v[4:7]\n"
"ds_read_b64_tr_b16 a[172:173], v193 offset:12544\n"
"buffer_load_dwordx4 v204, s[28:31], s52 offen offset:2048 lds\n"
"v_mfma_f32_16x16x32_bf16 v[4:7], a[156:159], v[188:191], v[4:7]\n"
"s_add_u32 s52, s52, 8192\n"
"ds_read_b64_tr_b16 a[174:175], v193 offset:14592\n"
"v_mfma_f32_16x16x32_bf16 v[8:11], a[192:195], v[128:131], 0\n"
"s_waitcnt lgkmcnt(11)\n"
"ds_read_b64_tr_b16 a[176:177], v193 offset:16640\n"
"v_mfma_f32_16x16x32_bf16 v[8:11], a[196:199], v[132:135], v[8:11]\n"
"ds_read_b64_tr_b16 a[178:179], v193 offset:18688\n"
"v_mfma_f32_16x16x32_bf16 v[8:11], a[200:203], v[136:139], v[8:11]\n"
"ds_read_b64_tr_b16 a[180:181], v193 offset:20736\n"
"buffer_load_dwordx4 v204, s[28:31], s52 offen offset:3072 lds\n"
"v_mfma_f32_16x16x32_bf16 v[8:11], a[204:207], v[140:143], v[8:11]\n"
"s_add_u32 s52, s52, 12288\n"
"ds_read_b64_tr_b16 a[182:183], v193 offset:22784\n"
"v_mfma_f32_16x16x32_bf16 v[8:11], a[208:211], v[144:147], v[8:11]\n"
"ds_read_b64_tr_b16 a[184:185], v193 offset:24832\n"
"v_cvt_pk_bf16_f32 v36, v4, v5\n"
"v_mfma_f32_16x16x32_bf16 v[8:11], a[212:215], v[148:151], v[8:11]\n"
"s_waitcnt lgkmcnt(11)\n"
"s_add_u32 m0, s53, 36864\n"
"ds_read_b64_tr_b16 a[186:187], v193 offset:26880\n"
"v_cvt_pk_bf16_f32 v37, v6, v7\n"
"v_mfma_f32_16x16x32_bf16 v[8:11], a[216:219], v[152:155], v[8:11]\n"
"ds_read_b64_tr_b16 a[188:189], v193 offset:28928\n"
"buffer_load_dwordx4 v204, s[28:31], s52 offen offset:0 lds\n"
"v_mfma_f32_16x16x32_bf16 v[8:11], a[220:223], v[156:159], v[8:11]\n"
"s_add_u32 s52, s52, 8192\n"
"ds_read_b64_tr_b16 a[190:191], v193 offset:30976\n"
"v_mfma_f32_16x16x32_bf16 v[12:15], a[192:195], v[160:163], 0\n"
"ds_read_b64_tr_b16 a[224:225], v196 offset:256\n"
"v_mfma_f32_16x16x32_bf16 v[12:15], a[196:199], v[164:167], v[12:15]\n"
"ds_read_b64_tr_b16 a[226:227], v196 offset:2304\n"
"v_mfma_f32_16x16x32_bf16 v[12:15], a[200:203], v[168:171], v[12:15]\n"
"ds_read_b64_tr_b16 a[228:229], v196 offset:4352\n"
"buffer_load_dwordx4 v204, s[28:31], s52 offen offset:1024 lds\n"
"v_mfma_f32_16x16x32_bf16 v[12:15], a[204:207], v[172:175], v[12:15]\n"
"s_add_u32 s52, s52, 8192\n"
"ds_read_b64_tr_b16 a[230:231], v196 offset:6400\n"
"v_mfma_f32_16x16x32_bf16 v[12:15], a[208:211], v[176:179], v[12:15]\n"
"ds_read_b64_tr_b16 a[232:233], v196 offset:8448\n"
"v_cvt_pk_bf16_f32 v34, v8, v9\n"
"v_mfma_f32_16x16x32_bf16 v[12:15], a[212:215], v[180:183], v[12:15]\n"
"ds_read_b64_tr_b16 a[234:235], v196 offset:10496\n"
"v_cvt_pk_bf16_f32 v35, v10, v11\n"
"v_mfma_f32_16x16x32_bf16 v[12:15], a[216:219], v[184:187], v[12:15]\n"
"ds_read_b64_tr_b16 a[236:237], v196 offset:12544\n"
"buffer_load_dwordx4 v204, s[28:31], s52 offen offset:2048 lds\n"
"v_mfma_f32_16x16x32_bf16 v[12:15], a[220:223], v[188:191], v[12:15]\n"
"s_add_u32 s52, s52, 8192\n"
"ds_read_b64_tr_b16 a[238:239], v196 offset:14592\n"
"buffer_store_dwordx4 v[32:35], v205, s[32:35], s54 offen offset:2048 sc1\n"
"v_mfma_f32_16x16x32_bf16 v[16:19], a[160:163], v[128:131], 0\n"
"s_waitcnt lgkmcnt(11)\n"
"ds_read_b64_tr_b16 a[240:241], v196 offset:16640\n"
"v_mfma_f32_16x16x32_bf16 v[16:19], a[164:167], v[132:135], v[16:19]\n"
"ds_read_b64_tr_b16 a[242:243], v196 offset:18688\n"
"v_mfma_f32_16x16x32_bf16 v[16:19], a[168:171], v[136:139], v[16:19]\n"
"ds_read_b64_tr_b16 a[244:245], v196 offset:20736\n"
"buffer_load_dwordx4 v204, s[28:31], s52 offen offset:3072 lds\n"
"v_mfma_f32_16x16x32_bf16 v[16:19], a[172:175], v[140:143], v[16:19]\n"
"s_sub_u32 s52, s52, 61312\n"
"ds_read_b64_tr_b16 a[246:247], v196 offset:22784\n"
"v_mfma_f32_16x16x32_bf16 v[16:19], a[176:179], v[144:147], v[16:19]\n"
"ds_read_b64_tr_b16 a[248:249], v196 offset:24832\n"
"v_cvt_pk_bf16_f32 v38, v12, v13\n"
"v_mfma_f32_16x16x32_bf16 v[16:19], a[180:183], v[148:151], v[16:19]\n"
"s_waitcnt lgkmcnt(11)\n"
"ds_read_b64_tr_b16 a[250:251], v196 offset:26880\n"
"v_cvt_pk_bf16_f32 v39, v14, v15\n"
"v_mfma_f32_16x16x32_bf16 v[16:19], a[184:187], v[152:155], v[16:19]\n"
"ds_read_b64_tr_b16 a[252:253], v196 offset:28928\n"
"v_mfma_f32_16x16x32_bf16 v[16:19], a[188:191], v[156:159], v[16:19]\n"
"ds_read_b64_tr_b16 a[254:255], v196 offset:30976\n"
"buffer_store_dwordx4 v[36:39], v206, s[32:35], s54 offen offset:2048 sc1\n"
"v_mfma_f32_16x16x32_bf16 v[20:23], a[160:163], v[160:163], 0\n"
"ds_read_b64_tr_b16 a[128:129], v193 offset:32768\n"
"v_mfma_f32_16x16x32_bf16 v[20:23], a[164:167], v[164:167], v[20:23]\n"
"ds_read_b64_tr_b16 a[130:131], v193 offset:34816\n"
"v_mfma_f32_16x16x32_bf16 v[20:23], a[168:171], v[168:171], v[20:23]\n"
"ds_read_b64_tr_b16 a[132:133], v193 offset:36864\n"
"v_mfma_f32_16x16x32_bf16 v[20:23], a[172:175], v[172:175], v[20:23]\n"
"ds_read_b64_tr_b16 a[134:135], v193 offset:38912\n"
"v_mfma_f32_16x16x32_bf16 v[20:23], a[176:179], v[176:179], v[20:23]\n"
"ds_read_b64_tr_b16 a[136:137], v193 offset:40960\n"
"v_cvt_pk_bf16_f32 v40, v16, v17\n"
"v_mfma_f32_16x16x32_bf16 v[20:23], a[180:183], v[180:183], v[20:23]\n"
"ds_read_b64_tr_b16 a[138:139], v193 offset:43008\n"
"v_cvt_pk_bf16_f32 v41, v18, v19\n"
"v_mfma_f32_16x16x32_bf16 v[20:23], a[184:187], v[184:187], v[20:23]\n"
"ds_read_b64_tr_b16 a[140:141], v193 offset:45056\n"
"v_mfma_f32_16x16x32_bf16 v[20:23], a[188:191], v[188:191], v[20:23]\n"
"ds_read_b64_tr_b16 a[142:143], v193 offset:47104\n"
"v_mfma_f32_16x16x32_bf16 v[24:27], a[224:227], v[128:131], 0\n"
"s_waitcnt lgkmcnt(11)\n"
"ds_read_b64_tr_b16 a[144:145], v193 offset:49152\n"
"v_mfma_f32_16x16x32_bf16 v[24:27], a[228:231], v[132:135], v[24:27]\n"
"ds_read_b64_tr_b16 a[146:147], v193 offset:51200\n"
"v_mfma_f32_16x16x32_bf16 v[24:27], a[232:235], v[136:139], v[24:27]\n"
"ds_read_b64_tr_b16 a[148:149], v193 offset:53248\n"
"v_mfma_f32_16x16x32_bf16 v[24:27], a[236:239], v[140:143], v[24:27]\n"
"ds_read_b64_tr_b16 a[150:151], v193 offset:55296\n"
"v_mfma_f32_16x16x32_bf16 v[24:27], a[240:243], v[144:147], v[24:27]\n"
"ds_read_b64_tr_b16 a[152:153], v193 offset:57344\n"
"v_cvt_pk_bf16_f32 v44, v20, v21\n"
"v_mfma_f32_16x16x32_bf16 v[24:27], a[244:247], v[148:151], v[24:27]\n"
"s_waitcnt lgkmcnt(11)\n"
"ds_read_b64_tr_b16 a[154:155], v193 offset:59392\n"
"v_cvt_pk_bf16_f32 v45, v22, v23\n"
"v_mfma_f32_16x16x32_bf16 v[24:27], a[248:251], v[152:155], v[24:27]\n"
"ds_read_b64_tr_b16 a[156:157], v193 offset:61440\n"
"v_mfma_f32_16x16x32_bf16 v[24:27], a[252:255], v[156:159], v[24:27]\n"
"ds_read_b64_tr_b16 a[158:159], v193 offset:63488\n"
"v_mfma_f32_16x16x32_bf16 v[28:31], a[224:227], v[160:163], 0\n"
"ds_read_b64_tr_b16 a[192:193], v196 offset:32768\n"
"v_mfma_f32_16x16x32_bf16 v[28:31], a[228:231], v[164:167], v[28:31]\n"
"ds_read_b64_tr_b16 a[194:195], v196 offset:34816\n"
"v_mfma_f32_16x16x32_bf16 v[28:31], a[232:235], v[168:171], v[28:31]\n"
"ds_read_b64_tr_b16 a[196:197], v196 offset:36864\n"
"v_mfma_f32_16x16x32_bf16 v[28:31], a[236:239], v[172:175], v[28:31]\n"
"ds_read_b64_tr_b16 a[198:199], v196 offset:38912\n"
"v_mfma_f32_16x16x32_bf16 v[28:31], a[240:243], v[176:179], v[28:31]\n"
"ds_read_b64_tr_b16 a[200:201], v196 offset:40960\n"
"v_cvt_pk_bf16_f32 v42, v24, v25\n"
"v_mfma_f32_16x16x32_bf16 v[28:31], a[244:247], v[180:183], v[28:31]\n"
"ds_read_b64_tr_b16 a[202:203], v196 offset:43008\n"
"v_cvt_pk_bf16_f32 v43, v26, v27\n"
"v_mfma_f32_16x16x32_bf16 v[28:31], a[248:251], v[184:187], v[28:31]\n"
"ds_read_b64_tr_b16 a[204:205], v196 offset:45056\n"
"v_mfma_f32_16x16x32_bf16 v[28:31], a[252:255], v[188:191], v[28:31]\n"
"buffer_store_dwordx4 v[40:43], v205, s[32:35], s54 offen offset:3072 sc1\n"
"ds_read_b64_tr_b16 a[206:207], v196 offset:47104\n"
"v_mfma_f32_16x16x32_bf16 v[0:3], a[128:131], v[128:131], 0\n"
"s_waitcnt lgkmcnt(11)\n"
"ds_read_b64_tr_b16 a[208:209], v196 offset:49152\n"
"v_mfma_f32_16x16x32_bf16 v[0:3], a[132:135], v[132:135], v[0:3]\n"
"ds_read_b64_tr_b16 a[210:211], v196 offset:51200\n"
"v_mfma_f32_16x16x32_bf16 v[0:3], a[136:139], v[136:139], v[0:3]\n"
"ds_read_b64_tr_b16 a[212:213], v196 offset:53248\n"
"v_mfma_f32_16x16x32_bf16 v[0:3], a[140:143], v[140:143], v[0:3]\n"
"ds_read_b64_tr_b16 a[214:215], v196 offset:55296\n"
"v_mfma_f32_16x16x32_bf16 v[0:3], a[144:147], v[144:147], v[0:3]\n"
"s_waitcnt vmcnt(24)\n"
"s_barrier\n"
"ds_read_b64_tr_b16 a[216:217], v196 offset:57344\n"
"v_cvt_pk_bf16_f32 v46, v28, v29\n"
"v_mfma_f32_16x16x32_bf16 v[0:3], a[148:151], v[148:151], v[0:3]\n"
"s_waitcnt lgkmcnt(11)\n"
"s_add_u32 m0, s53, 65536\n"
"ds_read_b64_tr_b16 a[218:219], v196 offset:59392\n"
"v_cvt_pk_bf16_f32 v47, v30, v31\n"
"v_mfma_f32_16x16x32_bf16 v[0:3], a[152:155], v[152:155], v[0:3]\n"
"ds_read_b64_tr_b16 a[220:221], v196 offset:61440\n"
"buffer_load_dwordx4 v204, s[28:31], s52 offen offset:0 lds\n"
"v_mfma_f32_16x16x32_bf16 v[0:3], a[156:159], v[156:159], v[0:3]\n"
"s_add_u32 s52, s52, 8192\n"
"buffer_store_dwordx4 v[44:47], v206, s[32:35], s54 offen offset:3072 sc1\n"
"ds_read_b64_tr_b16 a[222:223], v196 offset:63488\n"
"v_mfma_f32_16x16x32_bf16 v[4:7], a[128:131], v[160:163], 0\n"
"s_add_u32 s54, s54, 4096\n"
"ds_read_b64_tr_b16 a[160:161], v193 offset:33024\n"
"v_mfma_f32_16x16x32_bf16 v[4:7], a[132:135], v[164:167], v[4:7]\n"
"ds_read_b64_tr_b16 a[162:163], v193 offset:35072\n"
"v_mfma_f32_16x16x32_bf16 v[4:7], a[136:139], v[168:171], v[4:7]\n"
"ds_read_b64_tr_b16 a[164:165], v193 offset:37120\n"
"buffer_load_dwordx4 v204, s[28:31], s52 offen offset:1024 lds\n"
"v_mfma_f32_16x16x32_bf16 v[4:7], a[140:143], v[172:175], v[4:7]\n"
"s_add_u32 s52, s52, 8192\n"
"ds_read_b64_tr_b16 a[166:167], v193 offset:39168\n"
"v_mfma_f32_16x16x32_bf16 v[4:7], a[144:147], v[176:179], v[4:7]\n"
"ds_read_b64_tr_b16 a[168:169], v193 offset:41216\n"
"v_cvt_pk_bf16_f32 v32, v0, v1\n"
"v_mfma_f32_16x16x32_bf16 v[4:7], a[148:151], v[180:183], v[4:7]\n"
"ds_read_b64_tr_b16 a[170:171], v193 offset:43264\n"
"v_cvt_pk_bf16_f32 v33, v2, v3\n"
"v_mfma_f32_16x16x32_bf16 v[4:7], a[152:155], v[184:187], v[4:7]\n"
"ds_read_b64_tr_b16 a[172:173], v193 offset:45312\n"
"buffer_load_dwordx4 v204, s[28:31], s52 offen offset:2048 lds\n"
"v_mfma_f32_16x16x32_bf16 v[4:7], a[156:159], v[188:191], v[4:7]\n"
"s_add_u32 s52, s52, 8192\n"
"ds_read_b64_tr_b16 a[174:175], v193 offset:47360\n"
"v_mfma_f32_16x16x32_bf16 v[8:11], a[192:195], v[128:131], 0\n"
"s_waitcnt lgkmcnt(11)\n"
"ds_read_b64_tr_b16 a[176:177], v193 offset:49408\n"
"v_mfma_f32_16x16x32_bf16 v[8:11], a[196:199], v[132:135], v[8:11]\n"
"ds_read_b64_tr_b16 a[178:179], v193 offset:51456\n"
"v_mfma_f32_16x16x32_bf16 v[8:11], a[200:203], v[136:139], v[8:11]\n"
"ds_read_b64_tr_b16 a[180:181], v193 offset:53504\n"
"buffer_load_dwordx4 v204, s[28:31], s52 offen offset:3072 lds\n"
"v_mfma_f32_16x16x32_bf16 v[8:11], a[204:207], v[140:143], v[8:11]\n"
"s_add_u32 s52, s52, 12288\n"
"ds_read_b64_tr_b16 a[182:183], v193 offset:55552\n"
"v_mfma_f32_16x16x32_bf16 v[8:11], a[208:211], v[144:147], v[8:11]\n"
"ds_read_b64_tr_b16 a[184:185], v193 offset:57600\n"
"v_cvt_pk_bf16_f32 v36, v4, v5\n"
"v_mfma_f32_16x16x32_bf16 v[8:11], a[212:215], v[148:151], v[8:11]\n"
"s_waitcnt lgkmcnt(11)\n"
"s_add_u32 m0, s53, 69632\n"
"ds_read_b64_tr_b16 a[186:187], v193 offset:59648\n"
"v_cvt_pk_bf16_f32 v37, v6, v7\n"
"v_mfma_f32_16x16x32_bf16 v[8:11], a[216:219], v[152:155], v[8:11]\n"
"ds_read_b64_tr_b16 a[188:189], v193 offset:61696\n"
"buffer_load_dwordx4 v204, s[28:31], s52 offen offset:0 lds\n"
"v_mfma_f32_16x16x32_bf16 v[8:11], a[220:223], v[156:159], v[8:11]\n"
"s_add_u32 s52, s52, 8192\n"
"ds_read_b64_tr_b16 a[190:191], v193 offset:63744\n"
"v_mfma_f32_16x16x32_bf16 v[12:15], a[192:195], v[160:163], 0\n"
"ds_read_b64_tr_b16 a[224:225], v196 offset:33024\n"
"v_mfma_f32_16x16x32_bf16 v[12:15], a[196:199], v[164:167], v[12:15]\n"
"ds_read_b64_tr_b16 a[226:227], v196 offset:35072\n"
"v_mfma_f32_16x16x32_bf16 v[12:15], a[200:203], v[168:171], v[12:15]\n"
"ds_read_b64_tr_b16 a[228:229], v196 offset:37120\n"
"buffer_load_dwordx4 v204, s[28:31], s52 offen offset:1024 lds\n"
"v_mfma_f32_16x16x32_bf16 v[12:15], a[204:207], v[172:175], v[12:15]\n"
"s_add_u32 s52, s52, 8192\n"
"ds_read_b64_tr_b16 a[230:231], v196 offset:39168\n"
"v_mfma_f32_16x16x32_bf16 v[12:15], a[208:211], v[176:179], v[12:15]\n"
"ds_read_b64_tr_b16 a[232:233], v196 offset:41216\n"
"v_cvt_pk_bf16_f32 v34, v8, v9\n"
"v_mfma_f32_16x16x32_bf16 v[12:15], a[212:215], v[180:183], v[12:15]\n"
"ds_read_b64_tr_b16 a[234:235], v196 offset:43264\n"
"v_cvt_pk_bf16_f32 v35, v10, v11\n"
"v_mfma_f32_16x16x32_bf16 v[12:15], a[216:219], v[184:187], v[12:15]\n"
"ds_read_b64_tr_b16 a[236:237], v196 offset:45312\n"
"buffer_load_dwordx4 v204, s[28:31], s52 offen offset:2048 lds\n"
"v_mfma_f32_16x16x32_bf16 v[12:15], a[220:223], v[188:191], v[12:15]\n"
"s_add_u32 s52, s52, 8192\n"
"ds_read_b64_tr_b16 a[238:239], v196 offset:47360\n"
"buffer_store_dwordx4 v[32:35], v205, s[32:35], s54 offen offset:0 sc1\n"
"v_mfma_f32_16x16x32_bf16 v[16:19], a[160:163], v[128:131], 0\n"
"s_waitcnt lgkmcnt(11)\n"
"ds_read_b64_tr_b16 a[240:241], v196 offset:49408\n"
"v_mfma_f32_16x16x32_bf16 v[16:19], a[164:167], v[132:135], v[16:19]\n"
"ds_read_b64_tr_b16 a[242:243], v196 offset:51456\n"
"v_mfma_f32_16x16x32_bf16 v[16:19], a[168:171], v[136:139], v[16:19]\n"
"ds_read_b64_tr_b16 a[244:245], v196 offset:53504\n"
"buffer_load_dwordx4 v204, s[28:31], s52 offen offset:3072 lds\n"
"v_mfma_f32_16x16x32_bf16 v[16:19], a[172:175], v[140:143], v[16:19]\n"
"s_sub_u32 s52, s52, 61312\n"
"ds_read_b64_tr_b16 a[246:247], v196 offset:55552\n"
"v_mfma_f32_16x16x32_bf16 v[16:19], a[176:179], v[144:147], v[16:19]\n"
"ds_read_b64_tr_b16 a[248:249], v196 offset:57600\n"
"v_cvt_pk_bf16_f32 v38, v12, v13\n"
"v_mfma_f32_16x16x32_bf16 v[16:19], a[180:183], v[148:151], v[16:19]\n"
"s_waitcnt lgkmcnt(11)\n"
"ds_read_b64_tr_b16 a[250:251], v196 offset:59648\n"
"v_cvt_pk_bf16_f32 v39, v14, v15\n"
"v_mfma_f32_16x16x32_bf16 v[16:19], a[184:187], v[152:155], v[16:19]\n"
"ds_read_b64_tr_b16 a[252:253], v196 offset:61696\n"
"v_mfma_f32_16x16x32_bf16 v[16:19], a[188:191], v[156:159], v[16:19]\n"
"ds_read_b64_tr_b16 a[254:255], v196 offset:63744\n"
"buffer_store_dwordx4 v[36:39], v206, s[32:35], s54 offen offset:0 sc1\n"
"v_mfma_f32_16x16x32_bf16 v[20:23], a[160:163], v[160:163], 0\n"
"ds_read_b64_tr_b16 a[128:129], v194 offset:0\n"
"v_mfma_f32_16x16x32_bf16 v[20:23], a[164:167], v[164:167], v[20:23]\n"
"ds_read_b64_tr_b16 a[130:131], v194 offset:2048\n"
"v_mfma_f32_16x16x32_bf16 v[20:23], a[168:171], v[168:171], v[20:23]\n"
"ds_read_b64_tr_b16 a[132:133], v194 offset:4096\n"
"v_mfma_f32_16x16x32_bf16 v[20:23], a[172:175], v[172:175], v[20:23]\n"
"ds_read_b64_tr_b16 a[134:135], v194 offset:6144\n"
"v_mfma_f32_16x16x32_bf16 v[20:23], a[176:179], v[176:179], v[20:23]\n"
"ds_read_b64_tr_b16 a[136:137], v194 offset:8192\n"
"v_cvt_pk_bf16_f32 v40, v16, v17\n"
"v_mfma_f32_16x16x32_bf16 v[20:23], a[180:183], v[180:183], v[20:23]\n"
"ds_read_b64_tr_b16 a[138:139], v194 offset:10240\n"
"v_cvt_pk_bf16_f32 v41, v18, v19\n"
"v_mfma_f32_16x16x32_bf16 v[20:23], a[184:187], v[184:187], v[20:23]\n"
"ds_read_b64_tr_b16 a[140:141], v194 offset:12288\n"
"v_mfma_f32_16x16x32_bf16 v[20:23], a[188:191], v[188:191], v[20:23]\n"
"ds_read_b64_tr_b16 a[142:143], v194 offset:14336\n"
"v_mfma_f32_16x16x32_bf16 v[24:27], a[224:227], v[128:131], 0\n"
"s_waitcnt lgkmcnt(11)\n"
"ds_read_b64_tr_b16 a[144:145], v194 offset:16384\n"
"v_mfma_f32_16x16x32_bf16 v[24:27], a[228:231], v[132:135], v[24:27]\n"
"ds_read_b64_tr_b16 a[146:147], v194 offset:18432\n"
"v_mfma_f32_16x16x32_bf16 v[24:27], a[232:235], v[136:139], v[24:27]\n"
"ds_read_b64_tr_b16 a[148:149], v194 offset:20480\n"
"v_mfma_f32_16x16x32_bf16 v[24:27], a[236:239], v[140:143], v[24:27]\n"
"ds_read_b64_tr_b16 a[150:151], v194 offset:22528\n"
"v_mfma_f32_16x16x32_bf16 v[24:27], a[240:243], v[144:147], v[24:27]\n"
"ds_read_b64_tr_b16 a[152:153], v194 offset:24576\n"
"v_cvt_pk_bf16_f32 v44, v20, v21\n"
"v_mfma_f32_16x16x32_bf16 v[24:27], a[244:247], v[148:151], v[24:27]\n"
"s_waitcnt lgkmcnt(11)\n"
"ds_read_b64_tr_b16 a[154:155], v194 offset:26624\n"
"v_cvt_pk_bf16_f32 v45, v22, v23\n"
"v_mfma_f32_16x16x32_bf16 v[24:27], a[248:251], v[152:155], v[24:27]\n"
"ds_read_b64_tr_b16 a[156:157], v194 offset:28672\n"
"v_mfma_f32_16x16x32_bf16 v[24:27], a[252:255], v[156:159], v[24:27]\n"
"ds_read_b64_tr_b16 a[158:159], v194 offset:30720\n"
"v_mfma_f32_16x16x32_bf16 v[28:31], a[224:227], v[160:163], 0\n"
"ds_read_b64_tr_b16 a[192:193], v197 offset:0\n"
"v_mfma_f32_16x16x32_bf16 v[28:31], a[228:231], v[164:167], v[28:31]\n"
"ds_read_b64_tr_b16 a[194:195], v197 offset:2048\n"
"v_mfma_f32_16x16x32_bf16 v[28:31], a[232:235], v[168:171], v[28:31]\n"
"ds_read_b64_tr_b16 a[196:197], v197 offset:4096\n"
"v_mfma_f32_16x16x32_bf16 v[28:31], a[236:239], v[172:175], v[28:31]\n"
"ds_read_b64_tr_b16 a[198:199], v197 offset:6144\n"
"v_mfma_f32_16x16x32_bf16 v[28:31], a[240:243], v[176:179], v[28:31]\n"
"ds_read_b64_tr_b16 a[200:201], v197 offset:8192\n"
"v_cvt_pk_bf16_f32 v42, v24, v25\n"
"v_mfma_f32_16x16x32_bf16 v[28:31], a[244:247], v[180:183], v[28:31]\n"
"ds_read_b64_tr_b16 a[202:203], v197 offset:10240\n"
"v_cvt_pk_bf16_f32 v43, v26, v27\n"
"v_mfma_f32_16x16x32_bf16 v[28:31], a[248:251], v[184:187], v[28:31]\n"
"ds_read_b64_tr_b16 a[204:205], v197 offset:12288\n"
"v_mfma_f32_16x16x32_bf16 v[28:31], a[252:255], v[188:191], v[28:31]\n"
"buffer_store_dwordx4 v[40:43], v205, s[32:35], s54 offen offset:1024 sc1\n"
"ds_read_b64_tr_b16 a[206:207], v197 offset:14336\n"
"v_mfma_f32_16x16x32_bf16 v[0:3], a[128:131], v[128:131], 0\n"
"s_waitcnt lgkmcnt(11)\n"
"ds_read_b64_tr_b16 a[208:209], v197 offset:16384\n"
"v_mfma_f32_16x16x32_bf16 v[0:3], a[132:135], v[132:135], v[0:3]\n"
"ds_read_b64_tr_b16 a[210:211], v197 offset:18432\n"
"v_mfma_f32_16x16x32_bf16 v[0:3], a[136:139], v[136:139], v[0:3]\n"
"ds_read_b64_tr_b16 a[212:213], v197 offset:20480\n"
"v_mfma_f32_16x16x32_bf16 v[0:3], a[140:143], v[140:143], v[0:3]\n"
"ds_read_b64_tr_b16 a[214:215], v197 offset:22528\n"
"v_mfma_f32_16x16x32_bf16 v[0:3], a[144:147], v[144:147], v[0:3]\n"
"s_waitcnt vmcnt(26)\n"
"s_barrier\n"
"ds_read_b64_tr_b16 a[216:217], v197 offset:24576\n"
"v_cvt_pk_bf16_f32 v46, v28, v29\n"
"v_mfma_f32_16x16x32_bf16 v[0:3], a[148:151], v[148:151], v[0:3]\n"
"s_waitcnt lgkmcnt(11)\n"
"ds_read_b64_tr_b16 a[218:219], v197 offset:26624\n"
"v_cvt_pk_bf16_f32 v47, v30, v31\n"
"v_mfma_f32_16x16x32_bf16 v[0:3], a[152:155], v[152:155], v[0:3]\n"
"ds_read_b64_tr_b16 a[220:221], v197 offset:28672\n"
"v_mfma_f32_16x16x32_bf16 v[0:3], a[156:159], v[156:159], v[0:3]\n"
"buffer_store_dwordx4 v[44:47], v206, s[32:35], s54 offen offset:1024 sc1\n"
"ds_read_b64_tr_b16 a[222:223], v197 offset:30720\n"
"v_mfma_f32_16x16x32_bf16 v[4:7], a[128:131], v[160:163], 0\n"
"ds_read_b64_tr_b16 a[160:161], v194 offset:256\n"
"v_mfma_f32_16x16x32_bf16 v[4:7], a[132:135], v[164:167], v[4:7]\n"
"ds_read_b64_tr_b16 a[162:163], v194 offset:2304\n"
"v_mfma_f32_16x16x32_bf16 v[4:7], a[136:139], v[168:171], v[4:7]\n"
"ds_read_b64_tr_b16 a[164:165], v194 offset:4352\n"
"v_mfma_f32_16x16x32_bf16 v[4:7], a[140:143], v[172:175], v[4:7]\n"
"ds_read_b64_tr_b16 a[166:167], v194 offset:6400\n"
"v_mfma_f32_16x16x32_bf16 v[4:7], a[144:147], v[176:179], v[4:7]\n"
"ds_read_b64_tr_b16 a[168:169], v194 offset:8448\n"
"v_cvt_pk_bf16_f32 v32, v0, v1\n"
"v_mfma_f32_16x16x32_bf16 v[4:7], a[148:151], v[180:183], v[4:7]\n"
"ds_read_b64_tr_b16 a[170:171], v194 offset:10496\n"
"v_cvt_pk_bf16_f32 v33, v2, v3\n"
"v_mfma_f32_16x16x32_bf16 v[4:7], a[152:155], v[184:187], v[4:7]\n"
"ds_read_b64_tr_b16 a[172:173], v194 offset:12544\n"
"v_mfma_f32_16x16x32_bf16 v[4:7], a[156:159], v[188:191], v[4:7]\n"
"ds_read_b64_tr_b16 a[174:175], v194 offset:14592\n"
"v_mfma_f32_16x16x32_bf16 v[8:11], a[192:195], v[128:131], 0\n"
"s_waitcnt lgkmcnt(11)\n"
"ds_read_b64_tr_b16 a[176:177], v194 offset:16640\n"
"v_mfma_f32_16x16x32_bf16 v[8:11], a[196:199], v[132:135], v[8:11]\n"
"ds_read_b64_tr_b16 a[178:179], v194 offset:18688\n"
"v_mfma_f32_16x16x32_bf16 v[8:11], a[200:203], v[136:139], v[8:11]\n"
"ds_read_b64_tr_b16 a[180:181], v194 offset:20736\n"
"v_mfma_f32_16x16x32_bf16 v[8:11], a[204:207], v[140:143], v[8:11]\n"
"ds_read_b64_tr_b16 a[182:183], v194 offset:22784\n"
"v_mfma_f32_16x16x32_bf16 v[8:11], a[208:211], v[144:147], v[8:11]\n"
"ds_read_b64_tr_b16 a[184:185], v194 offset:24832\n"
"v_cvt_pk_bf16_f32 v36, v4, v5\n"
"v_mfma_f32_16x16x32_bf16 v[8:11], a[212:215], v[148:151], v[8:11]\n"
"s_waitcnt lgkmcnt(11)\n"
"ds_read_b64_tr_b16 a[186:187], v194 offset:26880\n"
"v_cvt_pk_bf16_f32 v37, v6, v7\n"
"v_mfma_f32_16x16x32_bf16 v[8:11], a[216:219], v[152:155], v[8:11]\n"
"ds_read_b64_tr_b16 a[188:189], v194 offset:28928\n"
"v_mfma_f32_16x16x32_bf16 v[8:11], a[220:223], v[156:159], v[8:11]\n"
"ds_read_b64_tr_b16 a[190:191], v194 offset:30976\n"
"v_mfma_f32_16x16x32_bf16 v[12:15], a[192:195], v[160:163], 0\n"
"ds_read_b64_tr_b16 a[224:225], v197 offset:256\n"
"v_mfma_f32_16x16x32_bf16 v[12:15], a[196:199], v[164:167], v[12:15]\n"
"ds_read_b64_tr_b16 a[226:227], v197 offset:2304\n"
"v_mfma_f32_16x16x32_bf16 v[12:15], a[200:203], v[168:171], v[12:15]\n"
"ds_read_b64_tr_b16 a[228:229], v197 offset:4352\n"
"v_mfma_f32_16x16x32_bf16 v[12:15], a[204:207], v[172:175], v[12:15]\n"
"ds_read_b64_tr_b16 a[230:231], v197 offset:6400\n"
"v_mfma_f32_16x16x32_bf16 v[12:15], a[208:211], v[176:179], v[12:15]\n"
"ds_read_b64_tr_b16 a[232:233], v197 offset:8448\n"
"v_cvt_pk_bf16_f32 v34, v8, v9\n"
"v_mfma_f32_16x16x32_bf16 v[12:15], a[212:215], v[180:183], v[12:15]\n"
"ds_read_b64_tr_b16 a[234:235], v197 offset:10496\n"
"v_cvt_pk_bf16_f32 v35, v10, v11\n"
"v_mfma_f32_16x16x32_bf16 v[12:15], a[216:219], v[184:187], v[12:15]\n"
"ds_read_b64_tr_b16 a[236:237], v197 offset:12544\n"
"v_mfma_f32_16x16x32_bf16 v[12:15], a[220:223], v[188:191], v[12:15]\n"
"ds_read_b64_tr_b16 a[238:239], v197 offset:14592\n"
"buffer_store_dwordx4 v[32:35], v205, s[32:35], s54 offen offset:2048 sc1\n"
"v_mfma_f32_16x16x32_bf16 v[16:19], a[160:163], v[128:131], 0\n"
"s_waitcnt lgkmcnt(11)\n"
"ds_read_b64_tr_b16 a[240:241], v197 offset:16640\n"
"v_mfma_f32_16x16x32_bf16 v[16:19], a[164:167], v[132:135], v[16:19]\n"
"ds_read_b64_tr_b16 a[242:243], v197 offset:18688\n"
"v_mfma_f32_16x16x32_bf16 v[16:19], a[168:171], v[136:139], v[16:19]\n"
"ds_read_b64_tr_b16 a[244:245], v197 offset:20736\n"
"v_mfma_f32_16x16x32_bf16 v[16:19], a[172:175], v[140:143], v[16:19]\n"
"ds_read_b64_tr_b16 a[246:247], v197 offset:22784\n"
"v_mfma_f32_16x16x32_bf16 v[16:19], a[176:179], v[144:147], v[16:19]\n"
"ds_read_b64_tr_b16 a[248:249], v197 offset:24832\n"
"v_cvt_pk_bf16_f32 v38, v12, v13\n"
"v_mfma_f32_16x16x32_bf16 v[16:19], a[180:183], v[148:151], v[16:19]\n"
"s_waitcnt lgkmcnt(11)\n"
"ds_read_b64_tr_b16 a[250:251], v197 offset:26880\n"
"v_cvt_pk_bf16_f32 v39, v14, v15\n"
"v_mfma_f32_16x16x32_bf16 v[16:19], a[184:187], v[152:155], v[16:19]\n"
"ds_read_b64_tr_b16 a[252:253], v197 offset:28928\n"
"v_mfma_f32_16x16x32_bf16 v[16:19], a[188:191], v[156:159], v[16:19]\n"
"ds_read_b64_tr_b16 a[254:255], v197 offset:30976\n"
"buffer_store_dwordx4 v[36:39], v206, s[32:35], s54 offen offset:2048 sc1\n"
"v_mfma_f32_16x16x32_bf16 v[20:23], a[160:163], v[160:163], 0\n"
"ds_read_b64_tr_b16 a[128:129], v192 offset:0\n"
"v_mfma_f32_16x16x32_bf16 v[20:23], a[164:167], v[164:167], v[20:23]\n"
"ds_read_b64_tr_b16 a[130:131], v192 offset:2048\n"
"v_mfma_f32_16x16x32_bf16 v[20:23], a[168:171], v[168:171], v[20:23]\n"
"ds_read_b64_tr_b16 a[132:133], v192 offset:4096\n"
"v_mfma_f32_16x16x32_bf16 v[20:23], a[172:175], v[172:175], v[20:23]\n"
"ds_read_b64_tr_b16 a[134:135], v192 offset:6144\n"
"v_mfma_f32_16x16x32_bf16 v[20:23], a[176:179], v[176:179], v[20:23]\n"
"ds_read_b64_tr_b16 a[136:137], v192 offset:8192\n"
"v_cvt_pk_bf16_f32 v40, v16, v17\n"
"v_mfma_f32_16x16x32_bf16 v[20:23], a[180:183], v[180:183], v[20:23]\n"
"ds_read_b64_tr_b16 a[138:139], v192 offset:10240\n"
"v_cvt_pk_bf16_f32 v41, v18, v19\n"
"v_mfma_f32_16x16x32_bf16 v[20:23], a[184:187], v[184:187], v[20:23]\n"
"ds_read_b64_tr_b16 a[140:141], v192 offset:12288\n"
"v_mfma_f32_16x16x32_bf16 v[20:23], a[188:191], v[188:191], v[20:23]\n"
"ds_read_b64_tr_b16 a[142:143], v192 offset:14336\n"
"v_mfma_f32_16x16x32_bf16 v[24:27], a[224:227], v[128:131], 0\n"
"s_waitcnt lgkmcnt(11)\n"
"ds_read_b64_tr_b16 a[144:145], v192 offset:16384\n"
"v_mfma_f32_16x16x32_bf16 v[24:27], a[228:231], v[132:135], v[24:27]\n"
"ds_read_b64_tr_b16 a[146:147], v192 offset:18432\n"
"v_mfma_f32_16x16x32_bf16 v[24:27], a[232:235], v[136:139], v[24:27]\n"
"ds_read_b64_tr_b16 a[148:149], v192 offset:20480\n"
"v_mfma_f32_16x16x32_bf16 v[24:27], a[236:239], v[140:143], v[24:27]\n"
"ds_read_b64_tr_b16 a[150:151], v192 offset:22528\n"
"v_mfma_f32_16x16x32_bf16 v[24:27], a[240:243], v[144:147], v[24:27]\n"
"ds_read_b64_tr_b16 a[152:153], v192 offset:24576\n"
"v_cvt_pk_bf16_f32 v44, v20, v21\n"
"v_mfma_f32_16x16x32_bf16 v[24:27], a[244:247], v[148:151], v[24:27]\n"
"s_waitcnt lgkmcnt(11)\n"
"ds_read_b64_tr_b16 a[154:155], v192 offset:26624\n"
"v_cvt_pk_bf16_f32 v45, v22, v23\n"
"v_mfma_f32_16x16x32_bf16 v[24:27], a[248:251], v[152:155], v[24:27]\n"
"ds_read_b64_tr_b16 a[156:157], v192 offset:28672\n"
"v_mfma_f32_16x16x32_bf16 v[24:27], a[252:255], v[156:159], v[24:27]\n"
"ds_read_b64_tr_b16 a[158:159], v192 offset:30720\n"
"v_mfma_f32_16x16x32_bf16 v[28:31], a[224:227], v[160:163], 0\n"
"ds_read_b64_tr_b16 a[192:193], v195 offset:0\n"
"v_mfma_f32_16x16x32_bf16 v[28:31], a[228:231], v[164:167], v[28:31]\n"
"ds_read_b64_tr_b16 a[194:195], v195 offset:2048\n"
"v_mfma_f32_16x16x32_bf16 v[28:31], a[232:235], v[168:171], v[28:31]\n"
"ds_read_b64_tr_b16 a[196:197], v195 offset:4096\n"
"v_mfma_f32_16x16x32_bf16 v[28:31], a[236:239], v[172:175], v[28:31]\n"
"ds_read_b64_tr_b16 a[198:199], v195 offset:6144\n"
"v_mfma_f32_16x16x32_bf16 v[28:31], a[240:243], v[176:179], v[28:31]\n"
"ds_read_b64_tr_b16 a[200:201], v195 offset:8192\n"
"v_cvt_pk_bf16_f32 v42, v24, v25\n"
"v_mfma_f32_16x16x32_bf16 v[28:31], a[244:247], v[180:183], v[28:31]\n"
"ds_read_b64_tr_b16 a[202:203], v195 offset:10240\n"
"v_cvt_pk_bf16_f32 v43, v26, v27\n"
"v_mfma_f32_16x16x32_bf16 v[28:31], a[248:251], v[184:187], v[28:31]\n"
"ds_read_b64_tr_b16 a[204:205], v195 offset:12288\n"
"v_mfma_f32_16x16x32_bf16 v[28:31], a[252:255], v[188:191], v[28:31]\n"
"buffer_store_dwordx4 v[40:43], v205, s[32:35], s54 offen offset:3072 sc1\n"
"ds_read_b64_tr_b16 a[206:207], v195 offset:14336\n"
"v_mfma_f32_16x16x32_bf16 v[0:3], a[128:131], v[128:131], 0\n"
"s_waitcnt lgkmcnt(11)\n"
"ds_read_b64_tr_b16 a[208:209], v195 offset:16384\n"
"v_mfma_f32_16x16x32_bf16 v[0:3], a[132:135], v[132:135], v[0:3]\n"
"ds_read_b64_tr_b16 a[210:211], v195 offset:18432\n"
"v_mfma_f32_16x16x32_bf16 v[0:3], a[136:139], v[136:139], v[0:3]\n"
"ds_read_b64_tr_b16 a[212:213], v195 offset:20480\n"
"v_mfma_f32_16x16x32_bf16 v[0:3], a[140:143], v[140:143], v[0:3]\n"
"ds_read_b64_tr_b16 a[214:215], v195 offset:22528\n"
"v_mfma_f32_16x16x32_bf16 v[0:3], a[144:147], v[144:147], v[0:3]\n"
"s_waitcnt vmcnt(18)\n"
"s_barrier\n"
"ds_read_b64_tr_b16 a[216:217], v195 offset:24576\n"
"v_cvt_pk_bf16_f32 v46, v28, v29\n"
"v_mfma_f32_16x16x32_bf16 v[0:3], a[148:151], v[148:151], v[0:3]\n"
"s_waitcnt lgkmcnt(11)\n"
"ds_read_b64_tr_b16 a[218:219], v195 offset:26624\n"
"v_cvt_pk_bf16_f32 v47, v30, v31\n"
"v_mfma_f32_16x16x32_bf16 v[0:3], a[152:155], v[152:155], v[0:3]\n"
"ds_read_b64_tr_b16 a[220:221], v195 offset:28672\n"
"v_mfma_f32_16x16x32_bf16 v[0:3], a[156:159], v[156:159], v[0:3]\n"
"buffer_store_dwordx4 v[44:47], v206, s[32:35], s54 offen offset:3072 sc1\n"
"ds_read_b64_tr_b16 a[222:223], v195 offset:30720\n"
"v_mfma_f32_16x16x32_bf16 v[4:7], a[128:131], v[160:163], 0\n"
"s_add_u32 s54, s54, 4096\n"
"ds_read_b64_tr_b16 a[160:161], v192 offset:256\n"
"v_mfma_f32_16x16x32_bf16 v[4:7], a[132:135], v[164:167], v[4:7]\n"
"ds_read_b64_tr_b16 a[162:163], v192 offset:2304\n"
"v_mfma_f32_16x16x32_bf16 v[4:7], a[136:139], v[168:171], v[4:7]\n"
"ds_read_b64_tr_b16 a[164:165], v192 offset:4352\n"
"v_mfma_f32_16x16x32_bf16 v[4:7], a[140:143], v[172:175], v[4:7]\n"
"ds_read_b64_tr_b16 a[166:167], v192 offset:6400\n"
"v_mfma_f32_16x16x32_bf16 v[4:7], a[144:147], v[176:179], v[4:7]\n"
"ds_read_b64_tr_b16 a[168:169], v192 offset:8448\n"
"v_cvt_pk_bf16_f32 v32, v0, v1\n"
"v_mfma_f32_16x16x32_bf16 v[4:7], a[148:151], v[180:183], v[4:7]\n"
"ds_read_b64_tr_b16 a[170:171], v192 offset:10496\n"
"v_cvt_pk_bf16_f32 v33, v2, v3\n"
"v_mfma_f32_16x16x32_bf16 v[4:7], a[152:155], v[184:187], v[4:7]\n"
"ds_read_b64_tr_b16 a[172:173], v192 offset:12544\n"
"v_mfma_f32_16x16x32_bf16 v[4:7], a[156:159], v[188:191], v[4:7]\n"
"ds_read_b64_tr_b16 a[174:175], v192 offset:14592\n"
"v_mfma_f32_16x16x32_bf16 v[8:11], a[192:195], v[128:131], 0\n"
"s_waitcnt lgkmcnt(11)\n"
"ds_read_b64_tr_b16 a[176:177], v192 offset:16640\n"
"v_mfma_f32_16x16x32_bf16 v[8:11], a[196:199], v[132:135], v[8:11]\n"
"ds_read_b64_tr_b16 a[178:179], v192 offset:18688\n"
"v_mfma_f32_16x16x32_bf16 v[8:11], a[200:203], v[136:139], v[8:11]\n"
"ds_read_b64_tr_b16 a[180:181], v192 offset:20736\n"
"v_mfma_f32_16x16x32_bf16 v[8:11], a[204:207], v[140:143], v[8:11]\n"
"ds_read_b64_tr_b16 a[182:183], v192 offset:22784\n"
"v_mfma_f32_16x16x32_bf16 v[8:11], a[208:211], v[144:147], v[8:11]\n"
"ds_read_b64_tr_b16 a[184:185], v192 offset:24832\n"
"v_cvt_pk_bf16_f32 v36, v4, v5\n"
"v_mfma_f32_16x16x32_bf16 v[8:11], a[212:215], v[148:151], v[8:11]\n"
"s_waitcnt lgkmcnt(11)\n"
"ds_read_b64_tr_b16 a[186:187], v192 offset:26880\n"
"v_cvt_pk_bf16_f32 v37, v6, v7\n"
"v_mfma_f32_16x16x32_bf16 v[8:11], a[216:219], v[152:155], v[8:11]\n"
"ds_read_b64_tr_b16 a[188:189], v192 offset:28928\n"
"v_mfma_f32_16x16x32_bf16 v[8:11], a[220:223], v[156:159], v[8:11]\n"
"ds_read_b64_tr_b16 a[190:191], v192 offset:30976\n"
"v_mfma_f32_16x16x32_bf16 v[12:15], a[192:195], v[160:163], 0\n"
"ds_read_b64_tr_b16 a[224:225], v195 offset:256\n"
"v_mfma_f32_16x16x32_bf16 v[12:15], a[196:199], v[164:167], v[12:15]\n"
"ds_read_b64_tr_b16 a[226:227], v195 offset:2304\n"
"v_mfma_f32_16x16x32_bf16 v[12:15], a[200:203], v[168:171], v[12:15]\n"
"ds_read_b64_tr_b16 a[228:229], v195 offset:4352\n"
"v_mfma_f32_16x16x32_bf16 v[12:15], a[204:207], v[172:175], v[12:15]\n"
"ds_read_b64_tr_b16 a[230:231], v195 offset:6400\n"
"v_mfma_f32_16x16x32_bf16 v[12:15], a[208:211], v[176:179], v[12:15]\n"
"ds_read_b64_tr_b16 a[232:233], v195 offset:8448\n"
"v_cvt_pk_bf16_f32 v34, v8, v9\n"
"v_mfma_f32_16x16x32_bf16 v[12:15], a[212:215], v[180:183], v[12:15]\n"
"ds_read_b64_tr_b16 a[234:235], v195 offset:10496\n"
"v_cvt_pk_bf16_f32 v35, v10, v11\n"
"v_mfma_f32_16x16x32_bf16 v[12:15], a[216:219], v[184:187], v[12:15]\n"
"ds_read_b64_tr_b16 a[236:237], v195 offset:12544\n"
"v_mfma_f32_16x16x32_bf16 v[12:15], a[220:223], v[188:191], v[12:15]\n"
"ds_read_b64_tr_b16 a[238:239], v195 offset:14592\n"
"buffer_store_dwordx4 v[32:35], v205, s[32:35], s54 offen offset:0 sc1\n"
"v_mfma_f32_16x16x32_bf16 v[16:19], a[160:163], v[128:131], 0\n"
"s_waitcnt lgkmcnt(11)\n"
"ds_read_b64_tr_b16 a[240:241], v195 offset:16640\n"
"v_mfma_f32_16x16x32_bf16 v[16:19], a[164:167], v[132:135], v[16:19]\n"
"ds_read_b64_tr_b16 a[242:243], v195 offset:18688\n"
"v_mfma_f32_16x16x32_bf16 v[16:19], a[168:171], v[136:139], v[16:19]\n"
"ds_read_b64_tr_b16 a[244:245], v195 offset:20736\n"
"v_mfma_f32_16x16x32_bf16 v[16:19], a[172:175], v[140:143], v[16:19]\n"
"ds_read_b64_tr_b16 a[246:247], v195 offset:22784\n"
"v_mfma_f32_16x16x32_bf16 v[16:19], a[176:179], v[144:147], v[16:19]\n"
"ds_read_b64_tr_b16 a[248:249], v195 offset:24832\n"
"v_cvt_pk_bf16_f32 v38, v12, v13\n"
"v_mfma_f32_16x16x32_bf16 v[16:19], a[180:183], v[148:151], v[16:19]\n"
"s_waitcnt lgkmcnt(11)\n"
"ds_read_b64_tr_b16 a[250:251], v195 offset:26880\n"
"v_cvt_pk_bf16_f32 v39, v14, v15\n"
"v_mfma_f32_16x16x32_bf16 v[16:19], a[184:187], v[152:155], v[16:19]\n"
"ds_read_b64_tr_b16 a[252:253], v195 offset:28928\n"
"v_mfma_f32_16x16x32_bf16 v[16:19], a[188:191], v[156:159], v[16:19]\n"
"ds_read_b64_tr_b16 a[254:255], v195 offset:30976\n"
"buffer_store_dwordx4 v[36:39], v206, s[32:35], s54 offen offset:0 sc1\n"
"v_mfma_f32_16x16x32_bf16 v[20:23], a[160:163], v[160:163], 0\n"
"ds_read_b64_tr_b16 a[128:129], v192 offset:32768\n"
"v_mfma_f32_16x16x32_bf16 v[20:23], a[164:167], v[164:167], v[20:23]\n"
"ds_read_b64_tr_b16 a[130:131], v192 offset:34816\n"
"v_mfma_f32_16x16x32_bf16 v[20:23], a[168:171], v[168:171], v[20:23]\n"
"ds_read_b64_tr_b16 a[132:133], v192 offset:36864\n"
"v_mfma_f32_16x16x32_bf16 v[20:23], a[172:175], v[172:175], v[20:23]\n"
"ds_read_b64_tr_b16 a[134:135], v192 offset:38912\n"
"v_mfma_f32_16x16x32_bf16 v[20:23], a[176:179], v[176:179], v[20:23]\n"
"ds_read_b64_tr_b16 a[136:137], v192 offset:40960\n"
"v_cvt_pk_bf16_f32 v40, v16, v17\n"
"v_mfma_f32_16x16x32_bf16 v[20:23], a[180:183], v[180:183], v[20:23]\n"
"ds_read_b64_tr_b16 a[138:139], v192 offset:43008\n"
"v_cvt_pk_bf16_f32 v41, v18, v19\n"
"v_mfma_f32_16x16x32_bf16 v[20:23], a[184:187], v[184:187], v[20:23]\n"
"ds_read_b64_tr_b16 a[140:141], v192 offset:45056\n"
"v_mfma_f32_16x16x32_bf16 v[20:23], a[188:191], v[188:191], v[20:23]\n"
"ds_read_b64_tr_b16 a[142:143], v192 offset:47104\n"
"v_mfma_f32_16x16x32_bf16 v[24:27], a[224:227], v[128:131], 0\n"
"s_waitcnt lgkmcnt(11)\n"
"ds_read_b64_tr_b16 a[144:145], v192 offset:49152\n"
"v_mfma_f32_16x16x32_bf16 v[24:27], a[228:231], v[132:135], v[24:27]\n"
"ds_read_b64_tr_b16 a[146:147], v192 offset:51200\n"
"v_mfma_f32_16x16x32_bf16 v[24:27], a[232:235], v[136:139], v[24:27]\n"
"ds_read_b64_tr_b16 a[148:149], v192 offset:53248\n"
"v_mfma_f32_16x16x32_bf16 v[24:27], a[236:239], v[140:143], v[24:27]\n"
"ds_read_b64_tr_b16 a[150:151], v192 offset:55296\n"
"v_mfma_f32_16x16x32_bf16 v[24:27], a[240:243], v[144:147], v[24:27]\n"
"ds_read_b64_tr_b16 a[152:153], v192 offset:57344\n"
"v_cvt_pk_bf16_f32 v44, v20, v21\n"
"v_mfma_f32_16x16x32_bf16 v[24:27], a[244:247], v[148:151], v[24:27]\n"
"s_waitcnt lgkmcnt(11)\n"
"ds_read_b64_tr_b16 a[154:155], v192 offset:59392\n"
"v_cvt_pk_bf16_f32 v45, v22, v23\n"
"v_mfma_f32_16x16x32_bf16 v[24:27], a[248:251], v[152:155], v[24:27]\n"
"ds_read_b64_tr_b16 a[156:157], v192 offset:61440\n"
"v_mfma_f32_16x16x32_bf16 v[24:27], a[252:255], v[156:159], v[24:27]\n"
"ds_read_b64_tr_b16 a[158:159], v192 offset:63488\n"
"v_mfma_f32_16x16x32_bf16 v[28:31], a[224:227], v[160:163], 0\n"
"ds_read_b64_tr_b16 a[192:193], v195 offset:32768\n"
"v_mfma_f32_16x16x32_bf16 v[28:31], a[228:231], v[164:167], v[28:31]\n"
"ds_read_b64_tr_b16 a[194:195], v195 offset:34816\n"
"v_mfma_f32_16x16x32_bf16 v[28:31], a[232:235], v[168:171], v[28:31]\n"
"ds_read_b64_tr_b16 a[196:197], v195 offset:36864\n"
"v_mfma_f32_16x16x32_bf16 v[28:31], a[236:239], v[172:175], v[28:31]\n"
"ds_read_b64_tr_b16 a[198:199], v195 offset:38912\n"
"v_mfma_f32_16x16x32_bf16 v[28:31], a[240:243], v[176:179], v[28:31]\n"
"ds_read_b64_tr_b16 a[200:201], v195 offset:40960\n"
"v_cvt_pk_bf16_f32 v42, v24, v25\n"
"v_mfma_f32_16x16x32_bf16 v[28:31], a[244:247], v[180:183], v[28:31]\n"
"ds_read_b64_tr_b16 a[202:203], v195 offset:43008\n"
"v_cvt_pk_bf16_f32 v43, v26, v27\n"
"v_mfma_f32_16x16x32_bf16 v[28:31], a[248:251], v[184:187], v[28:31]\n"
"ds_read_b64_tr_b16 a[204:205], v195 offset:45056\n"
"v_mfma_f32_16x16x32_bf16 v[28:31], a[252:255], v[188:191], v[28:31]\n"
"buffer_store_dwordx4 v[40:43], v205, s[32:35], s54 offen offset:1024 sc1\n"
"ds_read_b64_tr_b16 a[206:207], v195 offset:47104\n"
"v_mfma_f32_16x16x32_bf16 v[0:3], a[128:131], v[128:131], 0\n"
"s_waitcnt lgkmcnt(11)\n"
"ds_read_b64_tr_b16 a[208:209], v195 offset:49152\n"
"v_mfma_f32_16x16x32_bf16 v[0:3], a[132:135], v[132:135], v[0:3]\n"
"ds_read_b64_tr_b16 a[210:211], v195 offset:51200\n"
"v_mfma_f32_16x16x32_bf16 v[0:3], a[136:139], v[136:139], v[0:3]\n"
"ds_read_b64_tr_b16 a[212:213], v195 offset:53248\n"
"v_mfma_f32_16x16x32_bf16 v[0:3], a[140:143], v[140:143], v[0:3]\n"
"ds_read_b64_tr_b16 a[214:215], v195 offset:55296\n"
"v_mfma_f32_16x16x32_bf16 v[0:3], a[144:147], v[144:147], v[0:3]\n"
"s_waitcnt vmcnt(10)\n"
"s_barrier\n"
"ds_read_b64_tr_b16 a[216:217], v195 offset:57344\n"
"v_cvt_pk_bf16_f32 v46, v28, v29\n"
"v_mfma_f32_16x16x32_bf16 v[0:3], a[148:151], v[148:151], v[0:3]\n"
"s_waitcnt lgkmcnt(11)\n"
"ds_read_b64_tr_b16 a[218:219], v195 offset:59392\n"
"v_cvt_pk_bf16_f32 v47, v30, v31\n"
"v_mfma_f32_16x16x32_bf16 v[0:3], a[152:155], v[152:155], v[0:3]\n"
"ds_read_b64_tr_b16 a[220:221], v195 offset:61440\n"
"v_mfma_f32_16x16x32_bf16 v[0:3], a[156:159], v[156:159], v[0:3]\n"
"buffer_store_dwordx4 v[44:47], v206, s[32:35], s54 offen offset:1024 sc1\n"
"ds_read_b64_tr_b16 a[222:223], v195 offset:63488\n"
"v_mfma_f32_16x16x32_bf16 v[4:7], a[128:131], v[160:163], 0\n"
"ds_read_b64_tr_b16 a[160:161], v192 offset:33024\n"
"v_mfma_f32_16x16x32_bf16 v[4:7], a[132:135], v[164:167], v[4:7]\n"
"ds_read_b64_tr_b16 a[162:163], v192 offset:35072\n"
"v_mfma_f32_16x16x32_bf16 v[4:7], a[136:139], v[168:171], v[4:7]\n"
"ds_read_b64_tr_b16 a[164:165], v192 offset:37120\n"
"v_mfma_f32_16x16x32_bf16 v[4:7], a[140:143], v[172:175], v[4:7]\n"
"ds_read_b64_tr_b16 a[166:167], v192 offset:39168\n"
"v_mfma_f32_16x16x32_bf16 v[4:7], a[144:147], v[176:179], v[4:7]\n"
"ds_read_b64_tr_b16 a[168:169], v192 offset:41216\n"
"v_cvt_pk_bf16_f32 v32, v0, v1\n"
"v_mfma_f32_16x16x32_bf16 v[4:7], a[148:151], v[180:183], v[4:7]\n"
"ds_read_b64_tr_b16 a[170:171], v192 offset:43264\n"
"v_cvt_pk_bf16_f32 v33, v2, v3\n"
"v_mfma_f32_16x16x32_bf16 v[4:7], a[152:155], v[184:187], v[4:7]\n"
"ds_read_b64_tr_b16 a[172:173], v192 offset:45312\n"
"v_mfma_f32_16x16x32_bf16 v[4:7], a[156:159], v[188:191], v[4:7]\n"
"ds_read_b64_tr_b16 a[174:175], v192 offset:47360\n"
"v_mfma_f32_16x16x32_bf16 v[8:11], a[192:195], v[128:131], 0\n"
"s_waitcnt lgkmcnt(11)\n"
"ds_read_b64_tr_b16 a[176:177], v192 offset:49408\n"
"v_mfma_f32_16x16x32_bf16 v[8:11], a[196:199], v[132:135], v[8:11]\n"
"ds_read_b64_tr_b16 a[178:179], v192 offset:51456\n"
"v_mfma_f32_16x16x32_bf16 v[8:11], a[200:203], v[136:139], v[8:11]\n"
"ds_read_b64_tr_b16 a[180:181], v192 offset:53504\n"
"v_mfma_f32_16x16x32_bf16 v[8:11], a[204:207], v[140:143], v[8:11]\n"
"ds_read_b64_tr_b16 a[182:183], v192 offset:55552\n"
"v_mfma_f32_16x16x32_bf16 v[8:11], a[208:211], v[144:147], v[8:11]\n"
"ds_read_b64_tr_b16 a[184:185], v192 offset:57600\n"
"v_cvt_pk_bf16_f32 v36, v4, v5\n"
"v_mfma_f32_16x16x32_bf16 v[8:11], a[212:215], v[148:151], v[8:11]\n"
"s_waitcnt lgkmcnt(11)\n"
"ds_read_b64_tr_b16 a[186:187], v192 offset:59648\n"
"v_cvt_pk_bf16_f32 v37, v6, v7\n"
"v_mfma_f32_16x16x32_bf16 v[8:11], a[216:219], v[152:155], v[8:11]\n"
"ds_read_b64_tr_b16 a[188:189], v192 offset:61696\n"
"v_mfma_f32_16x16x32_bf16 v[8:11], a[220:223], v[156:159], v[8:11]\n"
"ds_read_b64_tr_b16 a[190:191], v192 offset:63744\n"
"v_mfma_f32_16x16x32_bf16 v[12:15], a[192:195], v[160:163], 0\n"
"ds_read_b64_tr_b16 a[224:225], v195 offset:33024\n"
"v_mfma_f32_16x16x32_bf16 v[12:15], a[196:199], v[164:167], v[12:15]\n"
"ds_read_b64_tr_b16 a[226:227], v195 offset:35072\n"
"v_mfma_f32_16x16x32_bf16 v[12:15], a[200:203], v[168:171], v[12:15]\n"
"ds_read_b64_tr_b16 a[228:229], v195 offset:37120\n"
"v_mfma_f32_16x16x32_bf16 v[12:15], a[204:207], v[172:175], v[12:15]\n"
"ds_read_b64_tr_b16 a[230:231], v195 offset:39168\n"
"v_mfma_f32_16x16x32_bf16 v[12:15], a[208:211], v[176:179], v[12:15]\n"
"ds_read_b64_tr_b16 a[232:233], v195 offset:41216\n"
"v_cvt_pk_bf16_f32 v34, v8, v9\n"
"v_mfma_f32_16x16x32_bf16 v[12:15], a[212:215], v[180:183], v[12:15]\n"
"ds_read_b64_tr_b16 a[234:235], v195 offset:43264\n"
"v_cvt_pk_bf16_f32 v35, v10, v11\n"
"v_mfma_f32_16x16x32_bf16 v[12:15], a[216:219], v[184:187], v[12:15]\n"
"ds_read_b64_tr_b16 a[236:237], v195 offset:45312\n"
"v_mfma_f32_16x16x32_bf16 v[12:15], a[220:223], v[188:191], v[12:15]\n"
"ds_read_b64_tr_b16 a[238:239], v195 offset:47360\n"
"buffer_store_dwordx4 v[32:35], v205, s[32:35], s54 offen offset:2048 sc1\n"
"v_mfma_f32_16x16x32_bf16 v[16:19], a[160:163], v[128:131], 0\n"
"s_waitcnt lgkmcnt(11)\n"
"ds_read_b64_tr_b16 a[240:241], v195 offset:49408\n"
"v_mfma_f32_16x16x32_bf16 v[16:19], a[164:167], v[132:135], v[16:19]\n"
"ds_read_b64_tr_b16 a[242:243], v195 offset:51456\n"
"v_mfma_f32_16x16x32_bf16 v[16:19], a[168:171], v[136:139], v[16:19]\n"
"ds_read_b64_tr_b16 a[244:245], v195 offset:53504\n"
"v_mfma_f32_16x16x32_bf16 v[16:19], a[172:175], v[140:143], v[16:19]\n"
"ds_read_b64_tr_b16 a[246:247], v195 offset:55552\n"
"v_mfma_f32_16x16x32_bf16 v[16:19], a[176:179], v[144:147], v[16:19]\n"
"ds_read_b64_tr_b16 a[248:249], v195 offset:57600\n"
"v_cvt_pk_bf16_f32 v38, v12, v13\n"
"v_mfma_f32_16x16x32_bf16 v[16:19], a[180:183], v[148:151], v[16:19]\n"
"s_waitcnt lgkmcnt(11)\n"
"ds_read_b64_tr_b16 a[250:251], v195 offset:59648\n"
"v_cvt_pk_bf16_f32 v39, v14, v15\n"
"v_mfma_f32_16x16x32_bf16 v[16:19], a[184:187], v[152:155], v[16:19]\n"
"ds_read_b64_tr_b16 a[252:253], v195 offset:61696\n"
"v_mfma_f32_16x16x32_bf16 v[16:19], a[188:191], v[156:159], v[16:19]\n"
"ds_read_b64_tr_b16 a[254:255], v195 offset:63744\n"
"buffer_store_dwordx4 v[36:39], v206, s[32:35], s54 offen offset:2048 sc1\n"
"v_mfma_f32_16x16x32_bf16 v[20:23], a[160:163], v[160:163], 0\n"
"ds_read_b64_tr_b16 a[128:129], v193 offset:0\n"
"v_mfma_f32_16x16x32_bf16 v[20:23], a[164:167], v[164:167], v[20:23]\n"
"ds_read_b64_tr_b16 a[130:131], v193 offset:2048\n"
"v_mfma_f32_16x16x32_bf16 v[20:23], a[168:171], v[168:171], v[20:23]\n"
"ds_read_b64_tr_b16 a[132:133], v193 offset:4096\n"
"v_mfma_f32_16x16x32_bf16 v[20:23], a[172:175], v[172:175], v[20:23]\n"
"ds_read_b64_tr_b16 a[134:135], v193 offset:6144\n"
"v_mfma_f32_16x16x32_bf16 v[20:23], a[176:179], v[176:179], v[20:23]\n"
"ds_read_b64_tr_b16 a[136:137], v193 offset:8192\n"
"v_cvt_pk_bf16_f32 v40, v16, v17\n"
"v_mfma_f32_16x16x32_bf16 v[20:23], a[180:183], v[180:183], v[20:23]\n"
"ds_read_b64_tr_b16 a[138:139], v193 offset:10240\n"
"v_cvt_pk_bf16_f32 v41, v18, v19\n"
"v_mfma_f32_16x16x32_bf16 v[20:23], a[184:187], v[184:187], v[20:23]\n"
"ds_read_b64_tr_b16 a[140:141], v193 offset:12288\n"
"v_mfma_f32_16x16x32_bf16 v[20:23], a[188:191], v[188:191], v[20:23]\n"
"ds_read_b64_tr_b16 a[142:143], v193 offset:14336\n"
"v_mfma_f32_16x16x32_bf16 v[24:27], a[224:227], v[128:131], 0\n"
"s_waitcnt lgkmcnt(11)\n"
"ds_read_b64_tr_b16 a[144:145], v193 offset:16384\n"
"v_mfma_f32_16x16x32_bf16 v[24:27], a[228:231], v[132:135], v[24:27]\n"
"ds_read_b64_tr_b16 a[146:147], v193 offset:18432\n"
"v_mfma_f32_16x16x32_bf16 v[24:27], a[232:235], v[136:139], v[24:27]\n"
"ds_read_b64_tr_b16 a[148:149], v193 offset:20480\n"
"v_mfma_f32_16x16x32_bf16 v[24:27], a[236:239], v[140:143], v[24:27]\n"
"ds_read_b64_tr_b16 a[150:151], v193 offset:22528\n"
"v_mfma_f32_16x16x32_bf16 v[24:27], a[240:243], v[144:147], v[24:27]\n"
"ds_read_b64_tr_b16 a[152:153], v193 offset:24576\n"
"v_cvt_pk_bf16_f32 v44, v20, v21\n"
"v_mfma_f32_16x16x32_bf16 v[24:27], a[244:247], v[148:151], v[24:27]\n"
"s_waitcnt lgkmcnt(11)\n"
"ds_read_b64_tr_b16 a[154:155], v193 offset:26624\n"
"v_cvt_pk_bf16_f32 v45, v22, v23\n"
"v_mfma_f32_16x16x32_bf16 v[24:27], a[248:251], v[152:155], v[24:27]\n"
"ds_read_b64_tr_b16 a[156:157], v193 offset:28672\n"
"v_mfma_f32_16x16x32_bf16 v[24:27], a[252:255], v[156:159], v[24:27]\n"
"ds_read_b64_tr_b16 a[158:159], v193 offset:30720\n"
"v_mfma_f32_16x16x32_bf16 v[28:31], a[224:227], v[160:163], 0\n"
"ds_read_b64_tr_b16 a[192:193], v196 offset:0\n"
"v_mfma_f32_16x16x32_bf16 v[28:31], a[228:231], v[164:167], v[28:31]\n"
"ds_read_b64_tr_b16 a[194:195], v196 offset:2048\n"
"v_mfma_f32_16x16x32_bf16 v[28:31], a[232:235], v[168:171], v[28:31]\n"
"ds_read_b64_tr_b16 a[196:197], v196 offset:4096\n"
"v_mfma_f32_16x16x32_bf16 v[28:31], a[236:239], v[172:175], v[28:31]\n"
"ds_read_b64_tr_b16 a[198:199], v196 offset:6144\n"
"v_mfma_f32_16x16x32_bf16 v[28:31], a[240:243], v[176:179], v[28:31]\n"
"ds_read_b64_tr_b16 a[200:201], v196 offset:8192\n"
"v_cvt_pk_bf16_f32 v42, v24, v25\n"
"v_mfma_f32_16x16x32_bf16 v[28:31], a[244:247], v[180:183], v[28:31]\n"
"ds_read_b64_tr_b16 a[202:203], v196 offset:10240\n"
"v_cvt_pk_bf16_f32 v43, v26, v27\n"
"v_mfma_f32_16x16x32_bf16 v[28:31], a[248:251], v[184:187], v[28:31]\n"
"ds_read_b64_tr_b16 a[204:205], v196 offset:12288\n"
"v_mfma_f32_16x16x32_bf16 v[28:31], a[252:255], v[188:191], v[28:31]\n"
"buffer_store_dwordx4 v[40:43], v205, s[32:35], s54 offen offset:3072 sc1\n"
"ds_read_b64_tr_b16 a[206:207], v196 offset:14336\n"
"v_mfma_f32_16x16x32_bf16 v[0:3], a[128:131], v[128:131], 0\n"
"s_waitcnt lgkmcnt(11)\n"
"ds_read_b64_tr_b16 a[208:209], v196 offset:16384\n"
"v_mfma_f32_16x16x32_bf16 v[0:3], a[132:135], v[132:135], v[0:3]\n"
"ds_read_b64_tr_b16 a[210:211], v196 offset:18432\n"
"v_mfma_f32_16x16x32_bf16 v[0:3], a[136:139], v[136:139], v[0:3]\n"
"ds_read_b64_tr_b16 a[212:213], v196 offset:20480\n"
"v_mfma_f32_16x16x32_bf16 v[0:3], a[140:143], v[140:143], v[0:3]\n"
"ds_read_b64_tr_b16 a[214:215], v196 offset:22528\n"
"v_mfma_f32_16x16x32_bf16 v[0:3], a[144:147], v[144:147], v[0:3]\n"
"ds_read_b64_tr_b16 a[216:217], v196 offset:24576\n"
"v_cvt_pk_bf16_f32 v46, v28, v29\n"
"v_mfma_f32_16x16x32_bf16 v[0:3], a[148:151], v[148:151], v[0:3]\n"
"s_waitcnt lgkmcnt(11)\n"
"ds_read_b64_tr_b16 a[218:219], v196 offset:26624\n"
"v_cvt_pk_bf16_f32 v47, v30, v31\n"
"v_mfma_f32_16x16x32_bf16 v[0:3], a[152:155], v[152:155], v[0:3]\n"
"ds_read_b64_tr_b16 a[220:221], v196 offset:28672\n"
"v_mfma_f32_16x16x32_bf16 v[0:3], a[156:159], v[156:159], v[0:3]\n"
"buffer_store_dwordx4 v[44:47], v206, s[32:35], s54 offen offset:3072 sc1\n"
"ds_read_b64_tr_b16 a[222:223], v196 offset:30720\n"
"v_mfma_f32_16x16x32_bf16 v[4:7], a[128:131], v[160:163], 0\n"
"s_add_u32 s54, s54, 4096\n"
"ds_read_b64_tr_b16 a[160:161], v193 offset:256\n"
"v_mfma_f32_16x16x32_bf16 v[4:7], a[132:135], v[164:167], v[4:7]\n"
"ds_read_b64_tr_b16 a[162:163], v193 offset:2304\n"
"v_mfma_f32_16x16x32_bf16 v[4:7], a[136:139], v[168:171], v[4:7]\n"
"ds_read_b64_tr_b16 a[164:165], v193 offset:4352\n"
"v_mfma_f32_16x16x32_bf16 v[4:7], a[140:143], v[172:175], v[4:7]\n"
"ds_read_b64_tr_b16 a[166:167], v193 offset:6400\n"
"v_mfma_f32_16x16x32_bf16 v[4:7], a[144:147], v[176:179], v[4:7]\n"
"ds_read_b64_tr_b16 a[168:169], v193 offset:8448\n"
"v_cvt_pk_bf16_f32 v32, v0, v1\n"
"v_mfma_f32_16x16x32_bf16 v[4:7], a[148:151], v[180:183], v[4:7]\n"
"ds_read_b64_tr_b16 a[170:171], v193 offset:10496\n"
"v_cvt_pk_bf16_f32 v33, v2, v3\n"
"v_mfma_f32_16x16x32_bf16 v[4:7], a[152:155], v[184:187], v[4:7]\n"
"ds_read_b64_tr_b16 a[172:173], v193 offset:12544\n"
"v_mfma_f32_16x16x32_bf16 v[4:7], a[156:159], v[188:191], v[4:7]\n"
"ds_read_b64_tr_b16 a[174:175], v193 offset:14592\n"
"v_mfma_f32_16x16x32_bf16 v[8:11], a[192:195], v[128:131], 0\n"
"s_waitcnt lgkmcnt(11)\n"
"ds_read_b64_tr_b16 a[176:177], v193 offset:16640\n"
"v_mfma_f32_16x16x32_bf16 v[8:11], a[196:199], v[132:135], v[8:11]\n"
"ds_read_b64_tr_b16 a[178:179], v193 offset:18688\n"
"v_mfma_f32_16x16x32_bf16 v[8:11], a[200:203], v[136:139], v[8:11]\n"
"ds_read_b64_tr_b16 a[180:181], v193 offset:20736\n"
"v_mfma_f32_16x16x32_bf16 v[8:11], a[204:207], v[140:143], v[8:11]\n"
"ds_read_b64_tr_b16 a[182:183], v193 offset:22784\n"
"v_mfma_f32_16x16x32_bf16 v[8:11], a[208:211], v[144:147], v[8:11]\n"
"ds_read_b64_tr_b16 a[184:185], v193 offset:24832\n"
"v_cvt_pk_bf16_f32 v36, v4, v5\n"
"v_mfma_f32_16x16x32_bf16 v[8:11], a[212:215], v[148:151], v[8:11]\n"
"s_waitcnt lgkmcnt(11)\n"
"ds_read_b64_tr_b16 a[186:187], v193 offset:26880\n"
"v_cvt_pk_bf16_f32 v37, v6, v7\n"
"v_mfma_f32_16x16x32_bf16 v[8:11], a[216:219], v[152:155], v[8:11]\n"
"ds_read_b64_tr_b16 a[188:189], v193 offset:28928\n"
"v_mfma_f32_16x16x32_bf16 v[8:11], a[220:223], v[156:159], v[8:11]\n"
"ds_read_b64_tr_b16 a[190:191], v193 offset:30976\n"
"v_mfma_f32_16x16x32_bf16 v[12:15], a[192:195], v[160:163], 0\n"
"ds_read_b64_tr_b16 a[224:225], v196 offset:256\n"
"v_mfma_f32_16x16x32_bf16 v[12:15], a[196:199], v[164:167], v[12:15]\n"
"ds_read_b64_tr_b16 a[226:227], v196 offset:2304\n"
"v_mfma_f32_16x16x32_bf16 v[12:15], a[200:203], v[168:171], v[12:15]\n"
"ds_read_b64_tr_b16 a[228:229], v196 offset:4352\n"
"v_mfma_f32_16x16x32_bf16 v[12:15], a[204:207], v[172:175], v[12:15]\n"
"ds_read_b64_tr_b16 a[230:231], v196 offset:6400\n"
"v_mfma_f32_16x16x32_bf16 v[12:15], a[208:211], v[176:179], v[12:15]\n"
"ds_read_b64_tr_b16 a[232:233], v196 offset:8448\n"
"v_cvt_pk_bf16_f32 v34, v8, v9\n"
"v_mfma_f32_16x16x32_bf16 v[12:15], a[212:215], v[180:183], v[12:15]\n"
"ds_read_b64_tr_b16 a[234:235], v196 offset:10496\n"
"v_cvt_pk_bf16_f32 v35, v10, v11\n"
"v_mfma_f32_16x16x32_bf16 v[12:15], a[216:219], v[184:187], v[12:15]\n"
"ds_read_b64_tr_b16 a[236:237], v196 offset:12544\n"
"v_mfma_f32_16x16x32_bf16 v[12:15], a[220:223], v[188:191], v[12:15]\n"
"ds_read_b64_tr_b16 a[238:239], v196 offset:14592\n"
"buffer_store_dwordx4 v[32:35], v205, s[32:35], s54 offen offset:0 sc1\n"
"v_mfma_f32_16x16x32_bf16 v[16:19], a[160:163], v[128:131], 0\n"
"s_waitcnt lgkmcnt(11)\n"
"ds_read_b64_tr_b16 a[240:241], v196 offset:16640\n"
"v_mfma_f32_16x16x32_bf16 v[16:19], a[164:167], v[132:135], v[16:19]\n"
"ds_read_b64_tr_b16 a[242:243], v196 offset:18688\n"
"v_mfma_f32_16x16x32_bf16 v[16:19], a[168:171], v[136:139], v[16:19]\n"
"ds_read_b64_tr_b16 a[244:245], v196 offset:20736\n"
"v_mfma_f32_16x16x32_bf16 v[16:19], a[172:175], v[140:143], v[16:19]\n"
"ds_read_b64_tr_b16 a[246:247], v196 offset:22784\n"
"v_mfma_f32_16x16x32_bf16 v[16:19], a[176:179], v[144:147], v[16:19]\n"
"ds_read_b64_tr_b16 a[248:249], v196 offset:24832\n"
"v_cvt_pk_bf16_f32 v38, v12, v13\n"
"v_mfma_f32_16x16x32_bf16 v[16:19], a[180:183], v[148:151], v[16:19]\n"
"s_waitcnt lgkmcnt(11)\n"
"ds_read_b64_tr_b16 a[250:251], v196 offset:26880\n"
"v_cvt_pk_bf16_f32 v39, v14, v15\n"
"v_mfma_f32_16x16x32_bf16 v[16:19], a[184:187], v[152:155], v[16:19]\n"
"ds_read_b64_tr_b16 a[252:253], v196 offset:28928\n"
"v_mfma_f32_16x16x32_bf16 v[16:19], a[188:191], v[156:159], v[16:19]\n"
"ds_read_b64_tr_b16 a[254:255], v196 offset:30976\n"
"buffer_store_dwordx4 v[36:39], v206, s[32:35], s54 offen offset:0 sc1\n"
"v_mfma_f32_16x16x32_bf16 v[20:23], a[160:163], v[160:163], 0\n"
"s_nop 0\n"
"v_mfma_f32_16x16x32_bf16 v[20:23], a[164:167], v[164:167], v[20:23]\n"
"s_nop 0\n"
"v_mfma_f32_16x16x32_bf16 v[20:23], a[168:171], v[168:171], v[20:23]\n"
"s_nop 0\n"
"v_mfma_f32_16x16x32_bf16 v[20:23], a[172:175], v[172:175], v[20:23]\n"
"s_nop 0\n"
"v_mfma_f32_16x16x32_bf16 v[20:23], a[176:179], v[176:179], v[20:23]\n"
"v_cvt_pk_bf16_f32 v40, v16, v17\n"
"v_mfma_f32_16x16x32_bf16 v[20:23], a[180:183], v[180:183], v[20:23]\n"
"v_cvt_pk_bf16_f32 v41, v18, v19\n"
"v_mfma_f32_16x16x32_bf16 v[20:23], a[184:187], v[184:187], v[20:23]\n"
"s_nop 0\n"
"v_mfma_f32_16x16x32_bf16 v[20:23], a[188:191], v[188:191], v[20:23]\n"
"s_nop 0\n"
"v_mfma_f32_16x16x32_bf16 v[24:27], a[224:227], v[128:131], 0\n"
"s_waitcnt lgkmcnt(3)\n"
"v_mfma_f32_16x16x32_bf16 v[24:27], a[228:231], v[132:135], v[24:27]\n"
"s_nop 0\n"
"v_mfma_f32_16x16x32_bf16 v[24:27], a[232:235], v[136:139], v[24:27]\n"
"s_nop 0\n"
"v_mfma_f32_16x16x32_bf16 v[24:27], a[236:239], v[140:143], v[24:27]\n"
"s_nop 0\n"
"v_mfma_f32_16x16x32_bf16 v[24:27], a[240:243], v[144:147], v[24:27]\n"
"v_cvt_pk_bf16_f32 v44, v20, v21\n"
"v_mfma_f32_16x16x32_bf16 v[24:27], a[244:247], v[148:151], v[24:27]\n"
"s_waitcnt lgkmcnt(0)\n"
"v_cvt_pk_bf16_f32 v45, v22, v23\n"
"v_mfma_f32_16x16x32_bf16 v[24:27], a[248:251], v[152:155], v[24:27]\n"
"s_nop 0\n"
"v_mfma_f32_16x16x32_bf16 v[24:27], a[252:255], v[156:159], v[24:27]\n"
"s_nop 0\n"
"v_mfma_f32_16x16x32_bf16 v[28:31], a[224:227], v[160:163], 0\n"
"s_nop 0\n"
"v_mfma_f32_16x16x32_bf16 v[28:31], a[228:231], v[164:167], v[28:31]\n"
"s_nop 0\n"
"v_mfma_f32_16x16x32_bf16 v[28:31], a[232:235], v[168:171], v[28:31]\n"
"s_nop 0\n"
"v_mfma_f32_16x16x32_bf16 v[28:31], a[236:239], v[172:175], v[28:31]\n"
"s_nop 0\n"
"v_mfma_f32_16x16x32_bf16 v[28:31], a[240:243], v[176:179], v[28:31]\n"
"v_cvt_pk_bf16_f32 v42, v24, v25\n"
"v_mfma_f32_16x16x32_bf16 v[28:31], a[244:247], v[180:183], v[28:31]\n"
"v_cvt_pk_bf16_f32 v43, v26, v27\n"
"v_mfma_f32_16x16x32_bf16 v[28:31], a[248:251], v[184:187], v[28:31]\n"
"s_nop 0\n"
"v_mfma_f32_16x16x32_bf16 v[28:31], a[252:255], v[188:191], v[28:31]\n"
"buffer_store_dwordx4 v[40:43], v205, s[32:35], s54 offen offset:1024 sc1\n"
"s_nop 1\n"
"s_nop 1\n"
"s_nop 1\n"
"s_nop 1\n"
"s_nop 1\n"
"v_cvt_pk_bf16_f32 v46, v28, v29\n"
"s_nop 1\n"
"v_cvt_pk_bf16_f32 v47, v30, v31\n"
"s_nop 1\n"
"s_nop 1\n"
"buffer_store_dwordx4 v[44:47], v206, s[32:35], s54 offen offset:1024 sc1\n"
"s_nop 1\n"
"s_nop 1\n"
"s_nop 1\n"
"s_nop 1\n"
"s_nop 1\n"
"s_nop 1\n"
"s_nop 1\n"
"s_nop 1\n"
"s_branch AT_end\n"
"AT_fb:\n"
"s_waitcnt vmcnt(0) lgkmcnt(0)\n"
"s_barrier\n"
"s_mov_b32 s45, 1\n"
"s_lshl_b32 s16, s41, 12\n"
"s_mov_b32 s19, 32\n"
"v_mov_b32 v160, 0xff800000\n"
"v_mov_b32 v161, 0xff800000\n"
"v_mov_b32 v162, 0xff800000\n"
"v_mov_b32 v163, 0xff800000\n"
"AT_fbl:\n"
"s_mov_b32 m0, s18\n"
"s_nop 0\n"
"buffer_load_dwordx4 v246, s[8:11], s16 offen offset:0 lds\n"
"buffer_load_dwordx4 v246, s[8:11], s16 offen offset:1024 lds\n"
"buffer_load_dwordx4 v247, s[8:11], s16 offen offset:2048 lds\n"
"buffer_load_dwordx4 v247, s[8:11], s16 offen offset:3072 lds\n"
"s_add_u32 s16, s16, 16384\n"
"s_waitcnt vmcnt(0)\n"
"s_barrier\n"
"ds_read_b128 a[128:131], v240 offset:0\n"
"ds_read_b128 a[132:135], v240 offset:256\n"
"ds_read_b128 a[136:139], v240 offset:512\n"
"ds_read_b128 a[140:143], v240 offset:768\n"
"ds_read_b128 a[144:147], v240 offset:4096\n"
"ds_read_b128 a[148:151], v240 offset:4352\n"
"ds_read_b128 a[152:155], v240 offset:4608\n"
"ds_read_b128 a[156:159], v240 offset:4864\n"
"s_waitcnt lgkmcnt(0)\n"
"v_mfma_f32_16x16x32_bf16 v[128:131], a[128:131], v[64:67], 0\n"
"v_mfma_f32_16x16x32_bf16 v[128:131], a[132:135], v[68:71], v[128:131]\n"
"v_mfma_f32_16x16x32_bf16 v[128:131], a[136:139], v[72:75], v[128:131]\n"
"v_mfma_f32_16x16x32_bf16 v[128:131], a[140:143], v[76:79], v[128:131]\n"
"v_mfma_f32_16x16x32_bf16 v[132:135], a[144:147], v[64:67], 0\n"
"v_mfma_f32_16x16x32_bf16 v[132:135], a[148:151], v[68:71], v[132:135]\n"
"v_mfma_f32_16x16x32_bf16 v[132:135], a[152:155], v[72:75], v[132:135]\n"
"v_mfma_f32_16x16x32_bf16 v[132:135], a[156:159], v[76:79], v[132:135]\n"
"v_mfma_f32_16x16x32_bf16 v[136:139], a[128:131], v[80:83], 0\n"
"v_mfma_f32_16x16x32_bf16 v[136:139], a[132:135], v[84:87], v[136:139]\n"
"v_mfma_f32_16x16x32_bf16 v[136:139], a[136:139], v[88:91], v[136:139]\n"
"v_mfma_f32_16x16x32_bf16 v[136:139], a[140:143], v[92:95], v[136:139]\n"
"v_mfma_f32_16x16x32_bf16 v[140:143], a[144:147], v[80:83], 0\n"
"v_mfma_f32_16x16x32_bf16 v[140:143], a[148:151], v[84:87], v[140:143]\n"
"v_mfma_f32_16x16x32_bf16 v[140:143], a[152:155], v[88:91], v[140:143]\n"
"v_mfma_f32_16x16x32_bf16 v[140:143], a[156:159], v[92:95], v[140:143]\n"
"v_mfma_f32_16x16x32_bf16 v[144:147], a[128:131], v[96:99], 0\n"
"v_mfma_f32_16x16x32_bf16 v[144:147], a[132:135], v[100:103], v[144:147]\n"
"v_mfma_f32_16x16x32_bf16 v[144:147], a[136:139], v[104:107], v[144:147]\n"
"v_mfma_f32_16x16x32_bf16 v[144:147], a[140:143], v[108:111], v[144:147]\n"
"v_mfma_f32_16x16x32_bf16 v[148:151], a[144:147], v[96:99], 0\n"
"v_mfma_f32_16x16x32_bf16 v[148:151], a[148:151], v[100:103], v[148:151]\n"
"v_mfma_f32_16x16x32_bf16 v[148:151], a[152:155], v[104:107], v[148:151]\n"
"v_mfma_f32_16x16x32_bf16 v[148:151], a[156:159], v[108:111], v[148:151]\n"
"v_mfma_f32_16x16x32_bf16 v[152:155], a[128:131], v[112:115], 0\n"
"v_mfma_f32_16x16x32_bf16 v[152:155], a[132:135], v[116:119], v[152:155]\n"
"v_mfma_f32_16x16x32_bf16 v[152:155], a[136:139], v[120:123], v[152:155]\n"
"v_mfma_f32_16x16x32_bf16 v[152:155], a[140:143], v[124:127], v[152:155]\n"
"v_mfma_f32_16x16x32_bf16 v[156:159], a[144:147], v[112:115], 0\n"
"v_mfma_f32_16x16x32_bf16 v[156:159], a[148:151], v[116:119], v[156:159]\n"
"v_mfma_f32_16x16x32_bf16 v[156:159], a[152:155], v[120:123], v[156:159]\n"
"v_mfma_f32_16x16x32_bf16 v[156:159], a[156:159], v[124:127], v[156:159]\n"
"s_nop 7\n"
"v_max3_f32 v160, v160, v128, v129\n"
"v_max3_f32 v160, v160, v130, v131\n"
"v_max3_f32 v160, v160, v132, v133\n"
"v_max3_f32 v160, v160, v134, v135\n"
"v_max3_f32 v161, v161, v136, v137\n"
"v_max3_f32 v161, v161, v138, v139\n"
"v_max3_f32 v161, v161, v140, v141\n"
"v_max3_f32 v161, v161, v142, v143\n"
"v_max3_f32 v162, v162, v144, v145\n"
"v_max3_f32 v162, v162, v146, v147\n"
"v_max3_f32 v162, v162, v148, v149\n"
"v_max3_f32 v162, v162, v150, v151\n"
"v_max3_f32 v163, v163, v152, v153\n"
"v_max3_f32 v163, v163, v154, v155\n"
"v_max3_f32 v163, v163, v156, v157\n"
"v_max3_f32 v163, v163, v158, v159\n"
"ds_read_b128 a[128:131], v240 offset:8192\n"
"ds_read_b128 a[132:135], v240 offset:8448\n"
"ds_read_b128 a[136:139], v240 offset:8704\n"
"ds_read_b128 a[140:143], v240 offset:8960\n"
"ds_read_b128 a[144:147], v240 offset:12288\n"
"ds_read_b128 a[148:151], v240 offset:12544\n"
"ds_read_b128 a[152:155], v240 offset:12800\n"
"ds_read_b128 a[156:159], v240 offset:13056\n"
"s_waitcnt lgkmcnt(0)\n"
"v_mfma_f32_16x16x32_bf16 v[128:131], a[128:131], v[64:67], 0\n"
"v_mfma_f32_16x16x32_bf16 v[128:131], a[132:135], v[68:71], v[128:131]\n"
"v_mfma_f32_16x16x32_bf16 v[128:131], a[136:139], v[72:75], v[128:131]\n"
"v_mfma_f32_16x16x32_bf16 v[128:131], a[140:143], v[76:79], v[128:131]\n"
"v_mfma_f32_16x16x32_bf16 v[132:135], a[144:147], v[64:67], 0\n"
"v_mfma_f32_16x16x32_bf16 v[132:135], a[148:151], v[68:71], v[132:135]\n"
"v_mfma_f32_16x16x32_bf16 v[132:135], a[152:155], v[72:75], v[132:135]\n"
"v_mfma_f32_16x16x32_bf16 v[132:135], a[156:159], v[76:79], v[132:135]\n"
"v_mfma_f32_16x16x32_bf16 v[136:139], a[128:131], v[80:83], 0\n"
"v_mfma_f32_16x16x32_bf16 v[136:139], a[132:135], v[84:87], v[136:139]\n"
"v_mfma_f32_16x16x32_bf16 v[136:139], a[136:139], v[88:91], v[136:139]\n"
"v_mfma_f32_16x16x32_bf16 v[136:139], a[140:143], v[92:95], v[136:139]\n"
"v_mfma_f32_16x16x32_bf16 v[140:143], a[144:147], v[80:83], 0\n"
"v_mfma_f32_16x16x32_bf16 v[140:143], a[148:151], v[84:87], v[140:143]\n"
"v_mfma_f32_16x16x32_bf16 v[140:143], a[152:155], v[88:91], v[140:143]\n"
"v_mfma_f32_16x16x32_bf16 v[140:143], a[156:159], v[92:95], v[140:143]\n"
"v_mfma_f32_16x16x32_bf16 v[144:147], a[128:131], v[96:99], 0\n"
"v_mfma_f32_16x16x32_bf16 v[144:147], a[132:135], v[100:103], v[144:147]\n"
"v_mfma_f32_16x16x32_bf16 v[144:147], a[136:139], v[104:107], v[144:147]\n"
"v_mfma_f32_16x16x32_bf16 v[144:147], a[140:143], v[108:111], v[144:147]\n"
"v_mfma_f32_16x16x32_bf16 v[148:151], a[144:147], v[96:99], 0\n"
"v_mfma_f32_16x16x32_bf16 v[148:151], a[148:151], v[100:103], v[148:151]\n"
"v_mfma_f32_16x16x32_bf16 v[148:151], a[152:155], v[104:107], v[148:151]\n"
"v_mfma_f32_16x16x32_bf16 v[148:151], a[156:159], v[108:111], v[148:151]\n"
"v_mfma_f32_16x16x32_bf16 v[152:155], a[128:131], v[112:115], 0\n"
"v_mfma_f32_16x16x32_bf16 v[152:155], a[132:135], v[116:119], v[152:155]\n"
"v_mfma_f32_16x16x32_bf16 v[152:155], a[136:139], v[120:123], v[152:155]\n"
"v_mfma_f32_16x16x32_bf16 v[152:155], a[140:143], v[124:127], v[152:155]\n"
"v_mfma_f32_16x16x32_bf16 v[156:159], a[144:147], v[112:115], 0\n"
"v_mfma_f32_16x16x32_bf16 v[156:159], a[148:151], v[116:119], v[156:159]\n"
"v_mfma_f32_16x16x32_bf16 v[156:159], a[152:155], v[120:123], v[156:159]\n"
"v_mfma_f32_16x16x32_bf16 v[156:159], a[156:159], v[124:127], v[156:159]\n"
"s_nop 7\n"
"v_max3_f32 v160, v160, v128, v129\n"
"v_max3_f32 v160, v160, v130, v131\n"
"v_max3_f32 v160, v160, v132, v133\n"
"v_max3_f32 v160, v160, v134, v135\n"
"v_max3_f32 v161, v161, v136, v137\n"
"v_max3_f32 v161, v161, v138, v139\n"
"v_max3_f32 v161, v161, v140, v141\n"
"v_max3_f32 v161, v161, v142, v143\n"
"v_max3_f32 v162, v162, v144, v145\n"
"v_max3_f32 v162, v162, v146, v147\n"
"v_max3_f32 v162, v162, v148, v149\n"
"v_max3_f32 v162, v162, v150, v151\n"
"v_max3_f32 v163, v163, v152, v153\n"
"v_max3_f32 v163, v163, v154, v155\n"
"v_max3_f32 v163, v163, v156, v157\n"
"v_max3_f32 v163, v163, v158, v159\n"
"s_barrier\n"
"s_sub_u32 s19, s19, 1\n"
"s_cmp_lg_u32 s19, 0\n"
"s_cbranch_scc1 AT_fbl\n"
"v_mov_b32 v164, v160\n"
"s_nop 1\n"
"v_permlane32_swap_b32 v160, v164\n"
"v_max_f32 v160, v160, v164\n"
"v_mov_b32 v164, v160\n"
"s_nop 1\n"
"v_permlane16_swap_b32 v160, v164\n"
"v_max_f32 v160, v160, v164\n"
"v_sub_f32 v24, 0, v160\n"
"v_sub_f32 v25, 0, v160\n"
"v_sub_f32 v26, 0, v160\n"
"v_sub_f32 v27, 0, v160\n"
"v_mov_b32 v164, v161\n"
"s_nop 1\n"
"v_permlane32_swap_b32 v161, v164\n"
"v_max_f32 v161, v161, v164\n"
"v_mov_b32 v164, v161\n"
"s_nop 1\n"
"v_permlane16_swap_b32 v161, v164\n"
"v_max_f32 v161, v161, v164\n"
"v_sub_f32 v28, 0, v161\n"
"v_sub_f32 v29, 0, v161\n"
"v_sub_f32 v30, 0, v161\n"
"v_sub_f32 v31, 0, v161\n"
"v_mov_b32 v164, v162\n"
"s_nop 1\n"
"v_permlane32_swap_b32 v162, v164\n"
"v_max_f32 v162, v162, v164\n"
"v_mov_b32 v164, v162\n"
"s_nop 1\n"
"v_permlane16_swap_b32 v162, v164\n"
"v_max_f32 v162, v162, v164\n"
"v_sub_f32 v32, 0, v162\n"
"v_sub_f32 v33, 0, v162\n"
"v_sub_f32 v34, 0, v162\n"
"v_sub_f32 v35, 0, v162\n"
"v_mov_b32 v164, v163\n"
"s_nop 1\n"
"v_permlane32_swap_b32 v163, v164\n"
"v_max_f32 v163, v163, v164\n"
"v_mov_b32 v164, v163\n"
"s_nop 1\n"
"v_permlane16_swap_b32 v163, v164\n"
"v_max_f32 v163, v163, v164\n"
"v_sub_f32 v36, 0, v163\n"
"v_sub_f32 v37, 0, v163\n"
"v_sub_f32 v38, 0, v163\n"
"v_sub_f32 v39, 0, v163\n"
"v_mov_b32 v40, 0x3f803f80\n"
"v_mov_b32 v41, 0x3f803f80\n"
"v_mov_b32 v42, 0x3f803f80\n"
"v_mov_b32 v43, 0x3f803f80\n"
"s_lshl_b32 s16, s41, 12\n"
"s_mul_i32 s52, s41, 73728\n"
"s_mov_b32 m0, s18\n"
"s_nop 0\n"
"buffer_load_dwordx4 v246, s[8:11], s16 offen offset:0 lds\n"
"buffer_load_dwordx4 v246, s[8:11], s16 offen offset:1024 lds\n"
"buffer_load_dwordx4 v247, s[8:11], s16 offen offset:2048 lds\n"
"buffer_load_dwordx4 v247, s[8:11], s16 offen offset:3072 lds\n"
"s_add_u32 m0, s18, 16384\n"
"s_nop 0\n"
"buffer_load_dwordx4 v246, s[12:15], s16 offen offset:0 lds\n"
"buffer_load_dwordx4 v247, s[12:15], s16 offen offset:1024 lds\n"
"buffer_load_dwordx4 v246, s[12:15], s16 offen offset:2048 lds\n"
"buffer_load_dwordx4 v247, s[12:15], s16 offen offset:3072 lds\n"
"s_add_u32 s16, s16, 16384\n"
"s_add_u32 m0, s18, 32768\n"
"s_nop 0\n"
"buffer_load_dwordx4 v246, s[8:11], s16 offen offset:0 lds\n"
"buffer_load_dwordx4 v246, s[8:11], s16 offen offset:1024 lds\n"
"buffer_load_dwordx4 v247, s[8:11], s16 offen offset:2048 lds\n"
"buffer_load_dwordx4 v247, s[8:11], s16 offen offset:3072 lds\n"
"s_add_u32 m0, s18, 49152\n"
"s_nop 0\n"
"buffer_load_dwordx4 v246, s[12:15], s16 offen offset:0 lds\n"
"buffer_load_dwordx4 v247, s[12:15], s16 offen offset:1024 lds\n"
"buffer_load_dwordx4 v246, s[12:15], s16 offen offset:2048 lds\n"
"buffer_load_dwordx4 v247, s[12:15], s16 offen offset:3072 lds\n"
"s_add_u32 s16, s16, 16384\n"
"s_add_u32 m0, s18, 65536\n"
"s_nop 0\n"
"buffer_load_dwordx4 v246, s[8:11], s16 offen offset:0 lds\n"
"buffer_load_dwordx4 v246, s[8:11], s16 offen offset:1024 lds\n"
"buffer_load_dwordx4 v247, s[8:11], s16 offen offset:2048 lds\n"
"buffer_load_dwordx4 v247, s[8:11], s16 offen offset:3072 lds\n"
"s_add_u32 m0, s18, 81920\n"
"s_nop 0\n"
"buffer_load_dwordx4 v246, s[12:15], s16 offen offset:0 lds\n"
"buffer_load_dwordx4 v247, s[12:15], s16 offen offset:1024 lds\n"
"buffer_load_dwordx4 v246, s[12:15], s16 offen offset:2048 lds\n"
"buffer_load_dwordx4 v247, s[12:15], s16 offen offset:3072 lds\n"
"s_add_u32 s16, s16, 16384\n"
"s_add_u32 m0, s18, 98304\n"
"s_nop 0\n"
"buffer_load_dwordx4 v246, s[8:11], s16 offen offset:0 lds\n"
"buffer_load_dwordx4 v246, s[8:11], s16 offen offset:1024 lds\n"
"buffer_load_dwordx4 v247, s[8:11], s16 offen offset:2048 lds\n"
"buffer_load_dwordx4 v247, s[8:11], s16 offen offset:3072 lds\n"
"s_add_u32 m0, s18, 114688\n"
"s_nop 0\n"
"buffer_load_dwordx4 v246, s[12:15], s16 offen offset:0 lds\n"
"buffer_load_dwordx4 v247, s[12:15], s16 offen offset:1024 lds\n"
"buffer_load_dwordx4 v246, s[12:15], s16 offen offset:2048 lds\n"
"buffer_load_dwordx4 v247, s[12:15], s16 offen offset:3072 lds\n"
"s_add_u32 s16, s16, 16384\n"
"s_add_u32 m0, s53, 131072\n"
"s_nop 0\n"
"buffer_load_dwordx4 v204, s[28:31], s52 offen offset:0 lds\n"
"s_add_u32 s52, s52, 8192\n"
"buffer_load_dwordx4 v204, s[28:31], s52 offen offset:1024 lds\n"
"s_add_u32 s52, s52, 8192\n"
"buffer_load_dwordx4 v204, s[28:31], s52 offen offset:2048 lds\n"
"s_add_u32 s52, s52, 8192\n"
"buffer_load_dwordx4 v204, s[28:31], s52 offen offset:3072 lds\n"
"s_add_u32 s52, s52, 12288\n"
"s_add_u32 m0, s53, 135168\n"
"s_nop 0\n"
"buffer_load_dwordx4 v204, s[28:31], s52 offen offset:0 lds\n"
"s_add_u32 s52, s52, 8192\n"
"buffer_load_dwordx4 v204, s[28:31], s52 offen offset:1024 lds\n"
"s_add_u32 s52, s52, 8192\n"
"buffer_load_dwordx4 v204, s[28:31], s52 offen offset:2048 lds\n"
"s_add_u32 s52, s52, 8192\n"
"buffer_load_dwordx4 v204, s[28:31], s52 offen offset:3072 lds\n"
"s_sub_u32 s52, s52, 61312\n"
"s_waitcnt vmcnt(32)\n"
"s_barrier\n"
"ds_read_b128 a[128:131], v240 offset:0\n"
"ds_read_b128 a[132:135], v240 offset:256\n"
"ds_read_b128 a[136:139], v240 offset:512\n"
"ds_read_b128 a[140:143], v240 offset:768\n"
"ds_read_b128 a[144:147], v240 offset:4096\n"
"ds_read_b128 a[148:151], v240 offset:4352\n"
"ds_read_b128 a[152:155], v240 offset:4608\n"
"ds_read_b128 a[156:159], v240 offset:4864\n"
"ds_read_b64_tr_b16 a[192:193], v242 offset:16384\n"
"ds_read_b64_tr_b16 a[194:195], v242 offset:20480\n"
"ds_read_b64_tr_b16 a[196:197], v243 offset:16384\n"
"ds_read_b64_tr_b16 a[198:199], v243 offset:20480\n"
"ds_read_b64_tr_b16 a[200:201], v242 offset:16640\n"
"ds_read_b64_tr_b16 a[202:203], v242 offset:20736\n"
"ds_read_b64_tr_b16 a[204:205], v243 offset:16640\n"
"ds_read_b64_tr_b16 a[206:207], v243 offset:20736\n"
"ds_read_b64_tr_b16 a[208:209], v242 offset:16896\n"
"ds_read_b64_tr_b16 a[210:211], v242 offset:20992\n"
"ds_read_b64_tr_b16 a[212:213], v243 offset:16896\n"
"ds_read_b64_tr_b16 a[214:215], v243 offset:20992\n"
"ds_read_b64_tr_b16 a[216:217], v242 offset:17152\n"
"ds_read_b64_tr_b16 a[218:219], v242 offset:21248\n"
"ds_read_b64_tr_b16 a[220:221], v243 offset:17152\n"
"ds_read_b64_tr_b16 a[222:223], v243 offset:21248\n"
"s_waitcnt lgkmcnt(0)\n"
"s_branch AT_main\n"
"AT_end:\n"
  :: "s"(__builtin_amdgcn_kernarg_segment_ptr()), "s"(blockIdx.x), "s"(wid), "s"(ldsb) : "v0", "v1", "v2", "v3", "v4", "v5", "v6", "v7", "v8", "v9", "v10", "v11", "v12", "v13", "v14", "v15", "v16", "v17", "v18", "v19", "v20", "v21", "v22", "v23", "v24", "v25", "v26", "v27", "v28", "v29", "v30", "v31", "v32", "v33", "v34", "v35", "v36", "v37", "v38", "v39", "v40", "v41", "v42", "v43", "v44", "v45", "v46", "v47", "v48", "v49", "v50", "v51", "v52", "v53", "v54", "v55", "v56", "v57", "v58", "v59", "v60", "v61", "v62", "v63", "v64", "v65", "v66", "v67", "v68", "v69", "v70", "v71", "v72", "v73", "v74", "v75", "v76", "v77", "v78", "v79", "v80", "v81", "v82", "v83", "v84", "v85", "v86", "v87", "v88", "v89", "v90", "v91", "v92", "v93", "v94", "v95", "v96", "v97", "v98", "v99", "v100", "v101", "v102", "v103", "v104", "v105", "v106", "v107", "v108", "v109", "v110", "v111", "v112", "v113", "v114", "v115", "v116", "v117", "v118", "v119", "v120", "v121", "v122", "v123", "v124", "v125", "v126", "v127", "v128", "v129", "v130", "v131", "v132", "v133", "v134", "v135", "v136", "v137", "v138", "v139", "v140", "v141", "v142", "v143", "v144", "v145", "v146", "v147", "v148", "v149", "v150", "v151", "v152", "v153", "v154", "v155", "v156", "v157", "v158", "v159", "v160", "v161", "v162", "v163", "v164", "v165", "v166", "v167", "v168", "v169", "v170", "v171", "v172", "v173", "v174", "v175", "v176", "v177", "v178", "v179", "v180", "v181", "v182", "v183", "v184", "v185", "v186", "v187", "v188", "v189", "v190", "v191", "v192", "v193", "v194", "v195", "v196", "v197", "v198", "v199", "v200", "v201", "v202", "v203", "v204", "v205", "v206", "v207", "v208", "v209", "v210", "v211", "v212", "v213", "v214", "v215", "v216", "v217", "v218", "v219", "v220", "v221", "v222", "v223", "v224", "v225", "v226", "v227", "v228", "v229", "v230", "v231", "v232", "v233", "v234", "v235", "v236", "v237", "v238", "v239", "v240", "v241", "v242", "v243", "v244", "v245", "v246", "v247", "v248", "v249", "v250", "v251", "v252", "v253", "v254", "v255", "a0", "a1", "a2", "a3", "a4", "a5", "a6", "a7", "a8", "a9", "a10", "a11", "a12", "a13", "a14", "a15", "a16", "a17", "a18", "a19", "a20", "a21", "a22", "a23", "a24", "a25", "a26", "a27", "a28", "a29", "a30", "a31", "a32", "a33", "a34", "a35", "a36", "a37", "a38", "a39", "a40", "a41", "a42", "a43", "a44", "a45", "a46", "a47", "a48", "a49", "a50", "a51", "a52", "a53", "a54", "a55", "a56", "a57", "a58", "a59", "a60", "a61", "a62", "a63", "a64", "a65", "a66", "a67", "a68", "a69", "a70", "a71", "a72", "a73", "a74", "a75", "a76", "a77", "a78", "a79", "a80", "a81", "a82", "a83", "a84", "a85", "a86", "a87", "a88", "a89", "a90", "a91", "a92", "a93", "a94", "a95", "a96", "a97", "a98", "a99", "a100", "a101", "a102", "a103", "a104", "a105", "a106", "a107", "a108", "a109", "a110", "a111", "a112", "a113", "a114", "a115", "a116", "a117", "a118", "a119", "a120", "a121", "a122", "a123", "a124", "a125", "a126", "a127", "a128", "a129", "a130", "a131", "a132", "a133", "a134", "a135", "a136", "a137", "a138", "a139", "a140", "a141", "a142", "a143", "a144", "a145", "a146", "a147", "a148", "a149", "a150", "a151", "a152", "a153", "a154", "a155", "a156", "a157", "a158", "a159", "a160", "a161", "a162", "a163", "a164", "a165", "a166", "a167", "a168", "a169", "a170", "a171", "a172", "a173", "a174", "a175", "a176", "a177", "a178", "a179", "a180", "a181", "a182", "a183", "a184", "a185", "a186", "a187", "a188", "a189", "a190", "a191", "a192", "a193", "a194", "a195", "a196", "a197", "a198", "a199", "a200", "a201", "a202", "a203", "a204", "a205", "a206", "a207", "a208", "a209", "a210", "a211", "a212", "a213", "a214", "a215", "a216", "a217", "a218", "a219", "a220", "a221", "a222", "a223", "a224", "a225", "a226", "a227", "a228", "a229", "a230", "a231", "a232", "a233", "a234", "a235", "a236", "a237", "a238", "a239", "a240", "a241", "a242", "a243", "a244", "a245", "a246", "a247", "a248", "a249", "a250", "a251", "a252", "a253", "a254", "a255", "s8", "s9", "s10", "s11", "s12", "s13", "s14", "s15", "s16", "s17", "s18", "s19", "s20", "s21", "s22", "s23", "s24", "s25", "s26", "s27", "s28", "s29", "s30", "s31", "s32", "s33", "s34", "s35", "s36", "s37", "s38", "s39", "s40", "s41", "s42", "s43", "s44", "s45", "s46", "s47", "s48", "s49", "s50", "s51", "s52", "s53", "s54", "s55", "s56", "s57", "s58", "s59", "s60", "s61", "s62", "s63", "s64", "s65", "s66", "s67", "s68", "s69", "s70", "s71", "s72", "s73", "s74", "s75", "s76", "s77", "s78", "s79", "s80", "s81", "s82", "s83", "s84", "s85", "s86", "s87", "s88", "s89", "s90", "s91", "s92", "s93", "s94", "s95", "s96", "s97", "s98", "s99", "s100", "s101", "vcc", "scc", "memory");
}

__global__ __launch_bounds__(576) void k_post(const char* __restrict__ part, const unsigned short* __restrict__ x, const float* __restrict__ mod,
                                              const unsigned short* __restrict__ wr, unsigned short* __restrict__ out,
                                              float* __restrict__ h2, float* __restrict__ rout, float* __restrict__ x1) {
  KST0(2);
  __shared__ float red[3][9][8];
  const int tid = threadIdx.x, a = tid & 7, seg = tid >> 3, u = seg >> 2, g = seg & 3, wid = tid >> 6, lane = tid & 63;
  const int blk = blockIdx.x, row = blk * 8 + a;
  const char* pp = part + ((size_t)((blk >> 1) * 18 + u) * 1024 + (16 * g + 8 * (blk & 1) + a) * 16);
  u32x4 pv[16];
#pragma unroll
  for (int p = 0; p < 16; ++p) pv[p] = __builtin_nontemporal_load((const u32x4*)(pp + (size_t)p * (SQ * DM * 2)));
  const int n0 = 32 * u + 4 * g, n1 = n0 + 16;
  const u32x2 xa = *(const u32x2*)(x + (size_t)row * DM + n0), xb = *(const u32x2*)(x + (size_t)row * DM + n1);
  const u32x2 wa = *(const u32x2*)(wr + n0), wb = *(const u32x2*)(wr + n1);
  f32x4 m[4][2];
#pragma unroll
  for (int k = 0; k < 4; ++k) { m[k][0] = *(const f32x4*)(mod + (2 + k) * DM + n0); m[k][1] = *(const f32x4*)(mod + (2 + k) * DM + n1); }
  float acc[8] = {};
#pragma unroll
  for (int p = 0; p < 16; ++p)
#pragma unroll
    for (int e = 0; e < 4; ++e) { acc[2 * e] += bflo(pv[p][e]); acc[2 * e + 1] += bfhi(pv[p][e]); }
  const float xv[8] = {bflo(xa[0]), bfhi(xa[0]), bflo(xa[1]), bfhi(xa[1]), bflo(xb[0]), bfhi(xb[0]), bflo(xb[1]), bfhi(xb[1])};
  const float wv[8] = {bflo(wa[0]), bfhi(wa[0]), bflo(wa[1]), bfhi(wa[1]), bflo(wb[0]), bfhi(wb[0]), bflo(wb[1]), bfhi(wb[1])};
  float v[8]; float s = 0.f;
#pragma unroll
  for (int e = 0; e < 8; ++e) { v[e] = xv[e] + m[0][e >> 2][e & 3] * acc[e]; s += v[e]; }
  s += __shfl_xor(s, 8); s += __shfl_xor(s, 16); s += __shfl_xor(s, 32);
  if (lane < 8) red[0][wid][lane] = s;
  __syncthreads();
  float mean = 0.f;
#pragma unroll
  for (int w = 0; w < 9; ++w) mean += red[0][w][a];
  mean *= (1.f / DM);
  float q = 0.f;
#pragma unroll
  for (int e = 0; e < 8; ++e) { const float d = v[e] - mean; q += d * d; }
  q += __shfl_xor(q, 8); q += __shfl_xor(q, 16); q += __shfl_xor(q, 32);
  if (lane < 8) red[1][wid][lane] = q;
  __syncthreads();
  float var = 0.f;
#pragma unroll
  for (int w = 0; w < 9; ++w) var += red[1][w][a];
  const float rstd = 1.f / sqrtf(var * (1.f / DM) + 1e-5f);
  float r = 0.f, t[8], o[8];
#pragma unroll
  for (int e = 0; e < 8; ++e) {
    t[e] = (v[e] - mean) * rstd * (1.f + m[2][e >> 2][e & 3]) + m[1][e >> 2][e & 3];
    r += t[e] * wv[e];
    o[e] = v[e] + m[3][e >> 2][e & 3] * t[e];
  }
  r += __shfl_xor(r, 8); r += __shfl_xor(r, 16); r += __shfl_xor(r, 32);
  if (lane < 8) red[2][wid][lane] = r;
  *(u32x2*)(out + (size_t)row * DM + n0) = u32x2{cvtpk(o[0], o[1]), cvtpk(o[2], o[3])};
  *(u32x2*)(out + (size_t)row * DM + n1) = u32x2{cvtpk(o[4], o[5]), cvtpk(o[6], o[7])};
  __syncthreads();
  float rr = 0.f;
#pragma unroll
  for (int w = 0; w < 9; ++w) rr += red[2][w][a];
  if (tid < 8) rout[row] = rr;
  int rank = 0;
#pragma unroll
  for (int k = 1; k < 8; ++k) { const int ao = a ^ k; const float ro = __shfl_xor(rr, k); rank += (ro > rr || (ro == rr && ao < a)) ? 1 : 0; }
  if (rank < 2) {
    *(f32x4*)(h2 + (size_t)row * DM + n0) = f32x4{t[0], t[1], t[2], t[3]}; *(f32x4*)(h2 + (size_t)row * DM + n1) = f32x4{t[4], t[5], t[6], t[7]};
    *(f32x4*)(x1 + (size_t)row * DM + n0) = f32x4{v[0], v[1], v[2], v[3]}; *(f32x4*)(x1 + (size_t)row * DM + n1) = f32x4{v[4], v[5], v[6], v[7]};
  }
  KST1(2);
}

constexpr int MOE_BLK = FF / 32;
__global__ __launch_bounds__(256) void k_moe1(const float* __restrict__ rin, const float* __restrict__ h2, const unsigned short* __restrict__ wg,
                                              const unsigned short* __restrict__ wu, const unsigned short* __restrict__ wd, float* __restrict__ yp, int* __restrict__ selg) {
  KST0(3);
  __shared__ float xs[2][DM];
  __shared__ float red[64][4][33];
  __shared__ float act[2][32];
  __shared__ float dred[3][2][DM];
  __shared__ int sidx[2];
  __shared__ float wv[2][4]; __shared__ int wi[2][4];
  const int tid = threadIdx.x, lane = tid & 63, wid = tid >> 6;
  const int bb = blockIdx.x, slice = ((bb >> 4) << 4) | ((bb & 7) << 1) | ((bb >> 3) & 1);
  const int f0 = slice * 32;
  const int cg = tid & 3, ks = tid >> 2;
  bf16x8 wgr[9], wur[9];
#pragma unroll
  for (int i = 0; i < 9; ++i) { const size_t off = (size_t)(ks * 9 + i) * FF + f0 + cg * 8; wgr[i] = *(const bf16x8*)(wg + off); wur[i] = *(const bf16x8*)(wu + off); }
  const int dcg = tid % 72, drs = tid / 72;
  const int dr0 = drs * 11, dr1 = drs == 2 ? 32 : dr0 + 11;
  bf16x8 wdr[11];
  if (tid < 216) {
#pragma unroll
    for (int i = 0; i < 11; ++i) { int r = dr0 + i; if (r < dr1) wdr[i] = *(const bf16x8*)(wd + (size_t)(f0 + r) * DM + dcg * 8); }
  }
  float b0 = -INFINITY, b1 = -INFINITY; int i0 = 0x7fffffff, i1 = 0x7fffffff;
#pragma unroll
  for (int j = 0; j < SQ / 256; ++j) { const int i = tid + j * 256; float v = rin[i];
    if (v > b0) { b1 = b0; i1 = i0; b0 = v; i0 = i; } else if (v > b1) { b1 = v; i1 = i; } }
#pragma unroll
  for (int o = 32; o > 0; o >>= 1) {
    float c0 = __shfl_xor(b0, o), c1 = __shfl_xor(b1, o); int j0 = __shfl_xor(i0, o), j1 = __shfl_xor(i1, o);
    bool a_first = (b0 > c0) || (b0 == c0 && i0 < j0);
    float n0 = a_first ? b0 : c0; int m0 = a_first ? i0 : j0;
    float sa = a_first ? b1 : b0; int ia = a_first ? i1 : i0;
    float sb = a_first ? c0 : c1; int ib = a_first ? j0 : j1;
    bool s_first = (sa > sb) || (sa == sb && ia < ib);
    b0 = n0; i0 = m0; b1 = s_first ? sa : sb; i1 = s_first ? ia : ib;
  }
  if (lane == 0) { wv[0][wid] = b0; wi[0][wid] = i0; wv[1][wid] = b1; wi[1][wid] = i1; }
  __syncthreads();
  if (tid == 0) {
    float t0 = -INFINITY, t1 = -INFINITY; int k0 = 0x7fffffff, k1 = 0x7fffffff;
    for (int w = 0; w < 4; ++w) for (int s = 0; s < 2; ++s) { float v = wv[s][w]; int id = wi[s][w];
      if (v > t0 || (v == t0 && id < k0)) { t1 = t0; k1 = k0; t0 = v; k0 = id; } else if (v > t1 || (v == t1 && id < k1)) { t1 = v; k1 = id; } }
    sidx[0] = k0; sidx[1] = k1;
    if (blockIdx.x == 0) { selg[0] = k0; selg[1] = k1; }
  }
  __syncthreads();
  for (int i = tid; i < 2 * DM; i += 256) { int t = i / DM, k = i % DM; xs[t][k] = h2[(size_t)sidx[t] * DM + k]; }
  __syncthreads();
  {
    float g0[8] = {}, g1[8] = {}, u0[8] = {}, u1[8] = {};
#pragma unroll
    for (int i = 0; i < 9; ++i) { const float x0 = xs[0][ks * 9 + i], x1v = xs[1][ks * 9 + i];
#pragma unroll
      for (int e = 0; e < 8; ++e) { float a = bf2f((unsigned short)wgr[i][e]), b = bf2f((unsigned short)wur[i][e]);
        g0[e] += x0 * a; g1[e] += x1v * a; u0[e] += x0 * b; u1[e] += x1v * b; } }
#pragma unroll
    for (int e = 0; e < 8; ++e) { red[ks][0][cg * 8 + e] = g0[e]; red[ks][1][cg * 8 + e] = g1[e]; red[ks][2][cg * 8 + e] = u0[e]; red[ks][3][cg * 8 + e] = u1[e]; }
  }
  __syncthreads();
  if (tid < 64) { int t = tid >> 5, cc = tid & 31; float g = 0, u = 0;
#pragma unroll 16
    for (int s = 0; s < 64; ++s) { g += red[s][t][cc]; u += red[s][2 + t][cc]; }
    float sg = g / (1.f + __expf(-g));
    act[t][cc] = sg * u; }
  __syncthreads();
  if (tid < 216) {
    float y0[8] = {}, y1[8] = {};
#pragma unroll
    for (int i = 0; i < 11; ++i) { int r = dr0 + i; if (r < dr1) { const float a0 = act[0][r], a1 = act[1][r];
#pragma unroll
        for (int e = 0; e < 8; ++e) { float w = bf2f((unsigned short)wdr[i][e]); y0[e] += a0 * w; y1[e] += a1 * w; } } }
#pragma unroll
    for (int e = 0; e < 8; ++e) { dred[drs][0][dcg * 8 + e] = y0[e]; dred[drs][1][dcg * 8 + e] = y1[e]; }
  }
  __syncthreads();
  for (int i = tid; i < 2 * DM; i += 256) { int t = i / DM, n = i % DM; yp[(size_t)blockIdx.x * 2 * DM + i] = dred[0][t][n] + dred[1][t][n] + dred[2][t][n]; }
  KST1(3);
}
__global__ __launch_bounds__(256) void k_moe2(const float* __restrict__ yp, const int* __restrict__ selg, const float* __restrict__ rin,
                                              const float* __restrict__ h2, const float* __restrict__ mod,
                                              unsigned short* __restrict__ out, const float* __restrict__ x1) {
  KST0(4);
  __shared__ float red[8][33];
  const int tid = threadIdx.x, o = tid & 31, g = tid >> 5, i = blockIdx.x * 32 + o;
  float v[18];
#pragma unroll
  for (int j = 0; j < 18; ++j) v[j] = yp[(size_t)(g * 18 + j) * 2 * DM + i];
  const int t = i / DM, n = i % DM, row = selg[t];
  float rr = 0.f, hv = 0.f, xv = 0.f, gb = 0.f;
  if (g == 0) { rr = rin[row]; hv = h2[(size_t)row * DM + n]; xv = x1[(size_t)row * DM + n]; gb = mod[5 * DM + n]; }
  float a = 0.f;
#pragma unroll
  for (int j = 0; j < 18; ++j) a += v[j];
  red[g][o] = a;
  __syncthreads();
  if (g == 0) {
    float y = 0.f;
#pragma unroll
    for (int k = 0; k < 8; ++k) y += red[k][o];
    const float sg = 1.f / (1.f + __expf(-rr));
    out[(size_t)row * DM + n] = f2bf(xv + gb * (hv + y * sg));
  }
  KST1(4);
}

extern "C" void kernel_launch(void* const* d_in, const int* in_sizes, int n_in, void* d_out, int out_size, void* d_ws, size_t ws_size,
                              hipStream_t stream) {
  const unsigned short* x    = (const unsigned short*)d_in[0];
  const unsigned short* wr   = (const unsigned short*)d_in[6];
  const unsigned short* wg   = (const unsigned short*)d_in[7];
  const unsigned short* wu   = (const unsigned short*)d_in[8];
  const unsigned short* wd   = (const unsigned short*)d_in[9];
  const unsigned short* c    = (const unsigned short*)d_in[10];
  const unsigned short* wmod = (const unsigned short*)d_in[11];
  char* ws = (char*)d_ws;
  float* mod = (float*)(ws + WS_MOD); int* sel = (int*)(ws + WS_CNT); float* rbuf = (float*)(ws + WS_R);
  float* h2 = (float*)(ws + WS_H2); float* yp = (float*)(ws + WS_YP); float* x1 = (float*)(ws + WS_X1);
  unsigned short* out = (unsigned short*)d_out;

  hipFuncSetAttribute((const void*)k_attn, hipFuncAttributeMaxDynamicSharedMemorySize, 163840);
  k_attn<<<256, 256, 163840, stream>>>(d_in[0], d_in[3], d_in[1], d_in[2], d_in[4], c, wmod, ws, wg, wu, wd, d_out);
  k_post<<<256, 576, 0, stream>>>(ws + WS_PART, x, mod, wr, out, h2, rbuf, x1);
  k_moe1<<<MOE_BLK, 256, 0, stream>>>(rbuf, h2, wg, wu, wd, yp, sel);
  k_moe2<<<36, 256, 0, stream>>>(yp, sel, rbuf, h2, mod, out, x1);
}
```

```cpp
#include <hip/hip_runtime.h>
#include <stdint.h>

typedef short  bf16x8 __attribute__((ext_vector_type(8)));
typedef float  f32x4  __attribute__((ext_vector_type(4)));
typedef unsigned u32x4 __attribute__((ext_vector_type(4)));
typedef unsigned u32x2 __attribute__((ext_vector_type(2)));
typedef unsigned u32x16 __attribute__((ext_vector_type(16)));

constexpr int SQ = 2048, DM = 576, HKV = 16, FF = 4608;

__device__ __forceinline__ float bf2f(unsigned short u) { return __uint_as_float(((unsigned)u) << 16); }
__device__ __forceinline__ float bflo(unsigned u) { return __uint_as_float(u << 16); }
__device__ __forceinline__ float bfhi(unsigned u) { return __uint_as_float(u & 0xffff0000u); }
__device__ __forceinline__ unsigned cvtpk(float lo, float hi) {
  unsigned r; asm("v_cvt_pk_bf16_f32 %0, %1, %2" : "=v"(r) : "v"(lo), "v"(hi)); return r;
}
__device__ __forceinline__ unsigned short f2bf(float f) { return (unsigned short)(cvtpk(f, f) & 0xffffu); }
__device__ __forceinline__ float rbf(float f) { return bflo(cvtpk(f, f)); }

constexpr size_t WS_FLAG = 0;
constexpr size_t WS_R    = 20480;
constexpr size_t WS_GRAN = 32768;
constexpr size_t WS_EPOCH = 65536;
constexpr size_t WS_ST   = 69632;
constexpr size_t WS_PART = 139264;
constexpr size_t WS_CAND = 16384;
constexpr size_t WS_ROW  = WS_PART + (size_t)HKV * SQ * DM * 2;
constexpr size_t WS_Y    = WS_ROW + (size_t)SQ * DM * 16;
constexpr size_t WS_END  = WS_Y + 72 * 192 * 128;

#define TST(k)
#define TAIL_DELAY 40
typedef float f32x2 __attribute__((ext_vector_type(2)));
typedef __amdgpu_buffer_rsrc_t rsrc_t;
#define DI __device__ __forceinline__
#define MEMBAR() asm volatile("" ::: "memory")
constexpr int SC1 = 16;
constexpr unsigned SPIN_CAP = 1u << 19;
DI rsrc_t mk_rsrc(const void* p, unsigned bytes) { return __builtin_amdgcn_make_buffer_rsrc((void*)p, (short)0, (int)bytes, 0x20000); }
template <int AUX> DI u32x4 ld128(rsrc_t r, unsigned v, unsigned s) { return __builtin_amdgcn_raw_buffer_load_b128(r, v, s, AUX); }
template <int AUX> DI u32x2 ld64(rsrc_t r, unsigned v, unsigned s) { return __builtin_amdgcn_raw_buffer_load_b64(r, v, s, AUX); }
template <int AUX> DI void st128(u32x4 x, rsrc_t r, unsigned v, unsigned s) { __builtin_amdgcn_raw_buffer_store_b128(x, r, v, s, AUX); }
template <int AUX> DI void st64(u32x2 x, rsrc_t r, unsigned v, unsigned s) { __builtin_amdgcn_raw_buffer_store_b64(x, r, v, s, AUX); }
template <int N> DI unsigned ror_u(unsigned x) { return (unsigned)__builtin_amdgcn_mov_dpp((int)x, 0x120 + N, 0xf, 0xf, true); }
template <int N> DI float ror_f(float x) { return __uint_as_float(ror_u<N>(__float_as_uint(x))); }
DI float sum16_32(float x) {
  auto s = __builtin_amdgcn_permlane16_swap(__float_as_uint(x), __float_as_uint(x), false, false); x = __uint_as_float(s[0]) + __uint_as_float(s[1]);
  auto t = __builtin_amdgcn_permlane32_swap(__float_as_uint(x), __float_as_uint(x), false, false); return __uint_as_float(t[0]) + __uint_as_float(t[1]);
}
DI unsigned umax(unsigned a, unsigned b) { return a > b ? a : b; }
DI unsigned umin(unsigned a, unsigned b) { return a < b ? a : b; }
DI void top2(unsigned& k1, unsigned& k2, unsigned p1, unsigned p2) { k2 = umax(umax(k2, p2), umin(k1, p1)); k1 = umax(k1, p1); }
DI void lds_barrier() { asm volatile("s_waitcnt lgkmcnt(0)\n\ts_barrier" ::: "memory"); }

DI void tail_body(char* lds, const unsigned tag, const unsigned ob, const unsigned wid, const unsigned blk, const unsigned short* x, const unsigned short* wr,
                  char* ws, unsigned short* out, const u32x16 (&wv16)[8]) {
  float* tb   = (float*)lds;
  float* red  = (float*)(lds + 16384);
  unsigned* sel = (unsigned*)(lds + 17408);
  float* act  = (float*)(lds + 17536);
  float* fin  = (float*)(lds + 17920);
  unsigned lane; asm volatile("v_mbcnt_lo_u32_b32 %0, -1, 0\n\tv_mbcnt_hi_u32_b32 %0, -1, %0" : "=v"(lane));
  const unsigned tid = wid * 64 + lane;
  TST(0);
  const rsrc_t rw = mk_rsrc(ws, (unsigned)WS_PART);
  const bool has_slice = blk < 192;
#define WG_(j, e) wv16[(4 * (j) + (e)) >> 4][(4 * (j) + (e)) & 15]
#define WU_(j, e) wv16[(36 + 4 * (j) + (e)) >> 4][(36 + 4 * (j) + (e)) & 15]
#define WD_(j, e) wv16[(72 + 2 * (j) + (e)) >> 4][(72 + 2 * (j) + (e)) & 15]
  if (wid == 3) {
    for (unsigned spin = 0;; ++spin) {
      const unsigned f = __builtin_amdgcn_raw_buffer_load_b32(rw, (lane & 15) * 64, (unsigned)WS_FLAG + (ob >> 4) * 1024, SC1);
      if (!__any(f != tag) || spin > SPIN_CAP) break;
      __builtin_amdgcn_s_sleep(2); MEMBAR();
    }
  } else {
    const unsigned vg = tid < 144 ? tid * 32 : 0x40000000u;
    const u32x2 wv = ld64<0>(mk_rsrc(wr, DM * 2), tid * 8, 0);
    u32x4 m[4][2];
    for (unsigned spin = 0;; ++spin) {
      unsigned bad = 0;
#pragma unroll
      for (int k = 0; k < 4; ++k)
#pragma unroll
        for (int h = 0; h < 2; ++h) { m[k][h] = ld128<SC1>(rw, vg + 16 * h, (unsigned)WS_GRAN + (2 + k) * DM * 8); bad |= (m[k][h][1] ^ tag) | (m[k][h][3] ^ tag); }
      if (!__any(tid < 144 && bad != 0) || spin > SPIN_CAP) break;
      __builtin_amdgcn_s_sleep(2); MEMBAR();
    }
    if (tid < 144) {
      const float w4[4] = {bflo(wv[0]), bfhi(wv[0]), bflo(wv[1]), bfhi(wv[1])};
      f32x4 ga, u, cg, sb, gb, sw;
#pragma unroll
      for (int e = 0; e < 4; ++e) {
        ga[e] = __uint_as_float(m[0][e >> 1][2 * (e & 1)]); sb[e] = __uint_as_float(m[1][e >> 1][2 * (e & 1)]);
        cg[e] = 1.f + __uint_as_float(m[2][e >> 1][2 * (e & 1)]); gb[e] = __uint_as_float(m[3][e >> 1][2 * (e & 1)]);
        u[e] = cg[e] * w4[e]; sw[e] = sb[e] * w4[e];
      }
      *(f32x4*)(tb + 0 * DM + 4 * tid) = ga; *(f32x4*)(tb + 1 * DM + 4 * tid) = u; *(f32x4*)(tb + 2 * DM + 4 * tid) = cg;
      *(f32x4*)(tb + 3 * DM + 4 * tid) = sb; *(f32x4*)(tb + 4 * DM + 4 * tid) = gb; *(f32x4*)(tb + 5 * DM + 4 * tid) = sw;
    }
  }
  lds_barrier();
  TST(1);
  const unsigned a = lane & 7, sg = tid >> 3, row = ob * 8 + a;
  float v[3][8];
  float s0 = 0.f, s1 = 0.f, s2 = 0.f, s3 = 0.f, s4 = 0.f;
  if (wid < 3) {
    const rsrc_t rp = mk_rsrc(ws + WS_PART, (unsigned)(HKV * SQ * DM * 2));
    const unsigned vp = ((ob >> 1) * 18 + (sg >> 2)) * 1024 + (16 * (sg & 3) + 8 * (ob & 1) + a) * 16;
    const rsrc_t rx = mk_rsrc(x, SQ * DM * 2);
    const unsigned n0 = 32 * (sg >> 2) + 4 * (sg & 3);
    u32x2 xv[3][2]; u32x4 pv[3][16];
#pragma unroll
    for (int i = 0; i < 3; ++i) { xv[i][0] = ld64<0>(rx, (row * DM + n0) * 2, i * 384); xv[i][1] = ld64<0>(rx, (row * DM + n0) * 2 + 32, i * 384); }
#pragma unroll
    for (int i = 0; i < 3; ++i)
#pragma unroll
      for (int p = 0; p < 16; ++p) pv[i][p] = ld128<SC1>(rp, vp, (unsigned)p * (SQ * DM * 2) + i * 6144);
    MEMBAR();
#pragma unroll
    for (int i = 0; i < 3; ++i) {
      float acc[8] = {};
#pragma unroll
      for (int p = 0; p < 16; ++p)
#pragma unroll
        for (int e = 0; e < 4; ++e) { acc[2 * e] += bflo(pv[i][p][e]); acc[2 * e + 1] += bfhi(pv[i][p][e]); }
#pragma unroll
      for (int h = 0; h < 2; ++h) {
        const unsigned n = n0 + 192 * i + 16 * h;
        const f32x4 ga = *(const f32x4*)(tb + n), u = *(const f32x4*)(tb + DM + n), sw = *(const f32x4*)(tb + 5 * DM + n);
        const float x4[4] = {bflo(xv[i][h][0]), bfhi(xv[i][h][0]), bflo(xv[i][h][1]), bfhi(xv[i][h][1])};
#pragma unroll
        for (int e = 0; e < 4; ++e) { const float t = x4[e] + ga[e] * acc[4 * h + e]; v[i][4 * h + e] = t; s0 += t; s1 += t * t; s2 += t * u[e]; s3 += u[e]; s4 += sw[e]; }
      }
    }
    s0 = sum16_32(s0 + ror_f<8>(s0)); s1 = sum16_32(s1 + ror_f<8>(s1)); s2 = sum16_32(s2 + ror_f<8>(s2)); s3 = sum16_32(s3 + ror_f<8>(s3)); s4 = sum16_32(s4 + ror_f<8>(s4));
    if (lane < 8) { *(f32x4*)(red + (wid * 8 + a) * 8) = f32x4{s0, s1, s2, s3}; red[(wid * 8 + a) * 8 + 4] = s4; }
  }
  lds_barrier();
  TST(2);
  float mean, rstd; unsigned key;
  {
    const f32x4 r0 = *(const f32x4*)(red + a * 8), r1 = *(const f32x4*)(red + (8 + a) * 8), r2 = *(const f32x4*)(red + (16 + a) * 8);
    const float c0 = red[a * 8 + 4] + red[(8 + a) * 8 + 4] + red[(16 + a) * 8 + 4];
    mean = (r0[0] + r1[0] + r2[0]) * (1.f / DM);
    const float var = fmaxf((r0[1] + r1[1] + r2[1]) * (1.f / DM) - mean * mean, 0.f);
    rstd = __builtin_amdgcn_rsqf(var + 1e-5f);
    const float r = rstd * ((r0[2] + r1[2] + r2[2]) - mean * (r0[3] + r1[3] + r2[3])) + c0;
    const unsigned rb = __float_as_uint(r);
    key = (((rb & 0x80000000u) ? ~rb : (rb | 0x80000000u)) & ~0x7FFu) | (2047u - row);
  }
  unsigned l1 = key, l2 = 0;
  top2(l1, l2, ror_u<1>(l1), ror_u<1>(l2)); top2(l1, l2, ror_u<2>(l1), ror_u<2>(l2)); top2(l1, l2, ror_u<4>(l1), ror_u<4>(l2));
  const bool cand = key >= l2;
  u32x2 op[3][2]; float o32[3][8];
  const rsrc_t rrow = mk_rsrc(ws + WS_ROW, (unsigned)(SQ * DM * 16));
  const rsrc_t ro = mk_rsrc(out, SQ * DM * 2);
  const unsigned n0 = 32 * (sg >> 2) + 4 * (sg & 3);
  if (wid == 3) {
    if (lane == 0) st128<SC1>(u32x4{l1, tag, l2, tag}, rw, ob * 16, (unsigned)WS_CAND);
    unsigned k1, k2;
    for (unsigned spin = 0;; ++spin) {
      u32x4 c[4]; unsigned bad = 0;
#pragma unroll
      for (int k = 0; k < 4; ++k) c[k] = ld128<SC1>(rw, lane * 16, (unsigned)WS_CAND + 1024 * k);
      k1 = 0; k2 = 0;
#pragma unroll
      for (int k = 0; k < 4; ++k) { bad |= (c[k][1] ^ tag) | (c[k][3] ^ tag); top2(k1, k2, c[k][0], c[k][2]); }
      if (!__any(bad != 0) || spin > SPIN_CAP) break;
      __builtin_amdgcn_s_sleep(1); MEMBAR();
    }
    top2(k1, k2, ror_u<1>(k1), ror_u<1>(k2)); top2(k1, k2, ror_u<2>(k1), ror_u<2>(k2)); top2(k1, k2, ror_u<4>(k1), ror_u<4>(k2)); top2(k1, k2, ror_u<8>(k1), ror_u<8>(k2));
    { auto s = __builtin_amdgcn_permlane16_swap(k1, k1, false, false); auto t = __builtin_amdgcn_permlane16_swap(k2, k2, false, false); k1 = s[0]; k2 = t[0]; top2(k1, k2, s[1], t[1]); }
    { auto s = __builtin_amdgcn_permlane32_swap(k1, k1, false, false); auto t = __builtin_amdgcn_permlane32_swap(k2, k2, false, false); k1 = s[0]; k2 = t[0]; top2(k1, k2, s[1], t[1]); }
    if (lane == 0) *(u32x2*)sel = u32x2{k1, k2};
  } else {
#pragma unroll
    for (int i = 0; i < 3; ++i)
#pragma unroll
      for (int h = 0; h < 2; ++h) {
        const unsigned n = n0 + 192 * i + 16 * h;
        const f32x4 cg = *(const f32x4*)(tb + 2 * DM + n), sb = *(const f32x4*)(tb + 3 * DM + n), gb = *(const f32x4*)(tb + 4 * DM + n);
        float t[4];
#pragma unroll
        for (int e = 0; e < 4; ++e) { t[e] = (v[i][4 * h + e] - mean) * rstd * cg[e] + sb[e]; o32[i][4 * h + e] = v[i][4 * h + e] + gb[e] * t[e]; }
        if (cand) {
          st128<SC1>(u32x4{__float_as_uint(t[0]), tag, __float_as_uint(t[1]), tag}, rrow, (row * 2 * DM + n) * 8, 0);
          st128<SC1>(u32x4{__float_as_uint(t[2]), tag, __float_as_uint(t[3]), tag}, rrow, (row * 2 * DM + n) * 8 + 16, 0);
        }
        op[i][h] = u32x2{cvtpk(o32[i][4 * h], o32[i][4 * h + 1]), cvtpk(o32[i][4 * h + 2], o32[i][4 * h + 3])};
      }
    if (!cand) {
#pragma unroll
      for (int i = 0; i < 3; ++i)
#pragma unroll
        for (int h = 0; h < 2; ++h) st64<0>(op[i][h], ro, (row * DM + n0) * 2 + 32 * h, i * 384);
    }
  }
  lds_barrier();
  TST(3);
  const unsigned k1 = __builtin_amdgcn_readfirstlane(sel[0]), k2 = __builtin_amdgcn_readfirstlane(sel[1]);
  const unsigned row1 = 2047u - (k1 & 0x7FFu), row2 = 2047u - (k2 & 0x7FFu);
  if (wid < 3) {
    float hx[2][9];
    if (has_slice) {
      for (unsigned spin = 0;; ++spin) {
        unsigned bad = 0;
#pragma unroll
        for (int j = 0; j < 9; ++j) {
          const u32x2 g1 = ld64<SC1>(rrow, lane * 8 + j * 512, row1 * (2 * DM * 8)), g2 = ld64<SC1>(rrow, lane * 8 + j * 512, row2 * (2 * DM * 8));
          hx[0][j] = __uint_as_float(g1[0]); hx[1][j] = __uint_as_float(g2[0]); bad |= (g1[1] ^ tag) | (g2[1] ^ tag);
        }
        if (!__any(bad != 0) || spin > SPIN_CAP) break;
        __builtin_amdgcn_s_sleep(1); MEMBAR();
      }
    }
    if (cand) {
      if (key == k1 || key == k2) {
#pragma unroll
        for (int i = 0; i < 3; ++i)
#pragma unroll
          for (int q = 0; q < 4; ++q)
            st128<SC1>(u32x4{__float_as_uint(o32[i][2 * q]), tag, __float_as_uint(o32[i][2 * q + 1]), tag}, rrow, (row * 2 * DM + DM + n0 + 192 * i + 16 * (q >> 1) + 2 * (q & 1)) * 8, 0);
      } else {
#pragma unroll
        for (int i = 0; i < 3; ++i)
#pragma unroll
          for (int h = 0; h < 2; ++h) st64<0>(op[i][h], ro, (row * DM + n0) * 2 + 32 * h, i * 384);
      }
    }
    if (has_slice) {
      float g[2][8] = {}, u[2][8] = {};
#pragma unroll
      for (int j = 0; j < 9; ++j)
#pragma unroll
        for (int e = 0; e < 4; ++e) {
          const float a0 = bflo(WG_(j, e)), a1 = bfhi(WG_(j, e)), b0 = bflo(WU_(j, e)), b1 = bfhi(WU_(j, e));
#pragma unroll
          for (int t = 0; t < 2; ++t) { g[t][2 * e] += hx[t][j] * a0; g[t][2 * e + 1] += hx[t][j] * a1; u[t][2 * e] += hx[t][j] * b0; u[t][2 * e + 1] += hx[t][j] * b1; }
        }
      float g8[8], u8[8], g4[4], u4[4];
#pragma unroll
      for (int e = 0; e < 8; ++e) {
        auto s = __builtin_amdgcn_permlane32_swap(__float_as_uint(g[0][e]), __float_as_uint(g[1][e]), false, false); g8[e] = __uint_as_float(s[0]) + __uint_as_float(s[1]);
        auto t = __builtin_amdgcn_permlane32_swap(__float_as_uint(u[0][e]), __float_as_uint(u[1][e]), false, false); u8[e] = __uint_as_float(t[0]) + __uint_as_float(t[1]);
      }
#pragma unroll
      for (int e = 0; e < 4; ++e) {
        auto s = __builtin_amdgcn_permlane16_swap(__float_as_uint(g8[e]), __float_as_uint(g8[e + 4]), false, false); g4[e] = __uint_as_float(s[0]) + __uint_as_float(s[1]);
        auto t = __builtin_amdgcn_permlane16_swap(__float_as_uint(u8[e]), __float_as_uint(u8[e + 4]), false, false); u4[e] = __uint_as_float(t[0]) + __uint_as_float(t[1]);
      }
      f32x4 ac;
#pragma unroll
      for (int e = 0; e < 4; ++e) {
        float gg = g4[e], uu = u4[e];
        gg += ror_f<1>(gg); uu += ror_f<1>(uu); gg += ror_f<2>(gg); uu += ror_f<2>(uu); gg += ror_f<4>(gg); uu += ror_f<4>(uu); gg += ror_f<8>(gg); uu += ror_f<8>(uu);
        ac[e] = gg * __builtin_amdgcn_rcpf(1.f + __expf(-gg)) * uu;
      }
      if ((lane & 15) == 0) *(f32x4*)(act + (lane >> 5) * 24 + wid * 8 + ((lane >> 4) & 1) * 4) = ac;
    }
  }
  lds_barrier();
  TST(4);
  const rsrc_t ry = mk_rsrc(ws + WS_Y, 72 * 192 * 128);
  if (has_slice && tid < 144) {
    float y[2][4] = {};
#pragma unroll
    for (int j4 = 0; j4 < 6; ++j4) {
      const f32x4 a0 = *(const f32x4*)(act + 4 * j4), a1 = *(const f32x4*)(act + 24 + 4 * j4);
#pragma unroll
      for (int e = 0; e < 4; ++e) {
        const u32x2 w = {WD_(4 * j4 + e, 0), WD_(4 * j4 + e, 1)}; const float w4[4] = {bflo(w[0]), bfhi(w[0]), bflo(w[1]), bfhi(w[1])};
#pragma unroll
        for (int q = 0; q < 4; ++q) { y[0][q] += a0[e] * w4[q]; y[1][q] += a1[e] * w4[q]; }
      }
    }
#pragma unroll
    for (int t = 0; t < 2; ++t) {
      const unsigned vy = ((36 * t + (tid >> 2)) * 192 + blk) * 128 + (tid & 3) * 32;
      st128<SC1>(u32x4{__float_as_uint(y[t][0]), tag, __float_as_uint(y[t][1]), tag}, ry, vy, 0);
      st128<SC1>(u32x4{__float_as_uint(y[t][2]), tag, __float_as_uint(y[t][3]), tag}, ry, vy + 16, 0);
    }
  }
  TST(5);
  if (blk >= 184) {
    const unsigned j = blk - 184, tk = j >= 36 ? 1u : 0u, nb = 16 * (j - 36 * tk), rr = tk ? row2 : row1, kk = tk ? k2 : k1;
    if (!has_slice) __builtin_amdgcn_s_sleep(TAIL_DELAY);
    float y0, y1, ov = 0.f;
    for (unsigned spin = 0;; ++spin) {
      u32x4 c[6]; unsigned bad = 0;
#pragma unroll
      for (int k = 0; k < 6; ++k) c[k] = ld128<SC1>(ry, tid * 16, j * (192 * 128) + 4096 * k);
      const u32x2 og = ld64<SC1>(rrow, (lane & 15) * 8, (rr * 2 * DM + DM + nb) * 8);
      y0 = 0.f; y1 = 0.f;
#pragma unroll
      for (int k = 0; k < 6; ++k) { bad |= (c[k][1] ^ tag) | (c[k][3] ^ tag); y0 += __uint_as_float(c[k][0]); y1 += __uint_as_float(c[k][2]); }
      bad |= og[1] ^ tag; ov = __uint_as_float(og[0]);
      if (!__any(bad != 0) || spin > SPIN_CAP) break;
      __builtin_amdgcn_s_sleep(2); MEMBAR();
    }
    y0 = sum16_32(y0 + ror_f<8>(y0)); y1 = sum16_32(y1 + ror_f<8>(y1));
    if (lane < 8) *(f32x2*)(fin + wid * 16 + 2 * lane) = f32x2{y0, y1};
    lds_barrier();
    if (tid < 16) {
      const float y = fin[tid] + fin[16 + tid] + fin[32 + tid] + fin[48 + tid];
      const unsigned kb = kk & ~0x7FFu; const float r = __uint_as_float((kb & 0x80000000u) ? (kb & 0x7FFFFFFFu) : ~(kb | 0x7FFu));
      const float sg2 = __builtin_amdgcn_rcpf(1.f + __expf(-r));
      out[rr * DM + nb + tid] = f2bf(ov + tb[4 * DM + nb + tid] * (y * sg2));
    }
    TST(6);
  }
}

struct KArgs { const void *x, *wq, *kc, *vc, *wo, *c, *wmod; char* ws; const void *wg, *wu, *wd; unsigned short* out; const unsigned short* wr; };
typedef const __attribute__((address_space(4))) KArgs* kargs_t;
__global__ __launch_bounds__(256, 1) void k_main(const void* x, const void* wq, const void* kc, const void* vc, const void* wo, const void* c, const void* wmod, void* ws,
                                                 const void* wg, const void* wu, const void* wd, void* out, const void* wr) {
  extern __shared__ __attribute__((aligned(16))) char lds[];
  unsigned wid = __builtin_amdgcn_readfirstlane(threadIdx.x >> 6);
  const unsigned ldsb = (unsigned)(size_t)lds;
  kargs_t kp = (kargs_t)__builtin_amdgcn_kernarg_segment_ptr();
  unsigned tag, ob;
  u32x16 w0, w1, w2, w3, w4, w5, w6, w7;
  asm volatile(
"AT_start:\n"
"s_load_dwordx16 s[64:79], %[kp], 0\n"
"s_load_dwordx8 s[80:87], %[kp], 64\n"
"s_mov_b32 s42, %[blk]\n"
"s_mov_b32 s41, %[wid]\n"
"s_mov_b32 s40, %[lds]\n"
"s_mov_b32 s45, 0\n"
"v_mbcnt_lo_u32_b32 v248, -1, 0\n"
"v_mbcnt_hi_u32_b32 v248, -1, v248\n"
"s_and_b32 s56, s42, 3\n"
"s_lshr_b32 s57, s42, 3\n"
"s_and_b32 s58, s57, 3\n"
"s_lshl_b32 s56, s56, 2\n"
"s_add_u32 s43, s56, s58\n"
"s_lshr_b32 s44, s57, 2\n"
"s_and_b32 s56, s42, 7\n"
"s_lshr_b32 s56, s56, 2\n"
"s_lshl_b32 s56, s56, 3\n"
"s_add_u32 s44, s44, s56\n"
"s_lshr_b32 s56, s42, 5\n"
"s_lshl_b32 s56, s56, 3\n"
"s_and_b32 s58, s42, 7\n"
"s_add_u32 s56, s56, s58\n"
"s_lshl_b32 s56, s56, 2\n"
"s_and_b32 s57, s57, 3\n"
"s_add_u32 s55, s56, s57\n"
"v_lshrrev_b32 v226, 4, v248\n"
"v_bfe_u32 v227, v248, 2, 2\n"
"v_and_b32 v228, 3, v248\n"
"v_and_b32 v229, 15, v248\n"
"v_lshrrev_b32 v230, 3, v248\n"
"v_and_b32 v231, 7, v248\n"
"s_lshl_b32 s56, s41, 6\n"
"v_add_u32 v234, s56, v248\n"
"v_lshrrev_b32 v232, 1, v234\n"
"v_and_b32 v233, 1, v234\n"
"v_lshlrev_b32 v236, 1, v232\n"
"v_mul_u32_u24 v232, 6912, v232\n"
"v_lshl_add_u32 v235, v233, 4, v232\n"
"s_lshl_b32 s56, s55, 5\n"
"s_add_u32 s57, s56, 884736\n"
"s_add_u32 s58, s56, 1769472\n"
"s_add_u32 s59, s56, 2654208\n"
"s_add_u32 s60, s56, 3538944\n"
"v_lshrrev_b32 v232, 1, v230\n"
"v_xor_b32 v232, v232, v231\n"
"v_mul_u32_u24 v233, 1152, v230\n"
"v_lshl_add_u32 v222, v232, 4, v233\n"
"v_xor_b32 v232, 4, v232\n"
"v_lshl_add_u32 v223, v232, 4, v233\n"
"s_lshl_b32 s61, s44, 2\n"
"s_add_u32 s61, s61, s41\n"
"s_mul_i32 s46, s61, 36864\n"
"s_add_u32 s47, s46, 9216\n"
"s_add_u32 s48, s46, 18432\n"
"s_add_u32 s49, s46, 27648\n"
"s_mul_i32 s54, s61, 36864\n"
"s_mul_i32 s63, s41, 36864\n"
"s_add_u32 s63, s63, s40\n"
"s_cmp_lt_u32 s55, 216\n"
"s_cselect_b32 s10, 3981312, 0\n"
"s_getpc_b64 s[28:29]\n"
"AT_pc0:\n"
"s_sub_u32 s28, s28, AT_pc0-AT_start\n"
"s_subb_u32 s29, s29, 0\n"
"s_and_b32 s29, s29, 0xffff\n"
"s_mov_b32 s30, AT_kend-AT_start\n"
"s_mov_b32 s31, 0x20000\n"
"s_cmp_ge_u32 s42, 224\n"
"s_cselect_b32 s30, s30, 0\n"
"s_lshr_b32 s52, s42, 3\n"
"s_lshl_b32 s52, s52, 2\n"
"s_add_u32 s52, s52, s41\n"
"s_and_b32 s52, s52, 15\n"
"s_lshl_b32 s52, s52, 12\n"
"v_lshlrev_b32 v214, 6, v248\n"
"buffer_load_dword v215, v214, s[28:31], s52 offen\n"
"s_add_u32 s52, s52, 65536\n"
"buffer_load_dword v215, v214, s[28:31], s52 offen\n"
"s_waitcnt lgkmcnt(0)\n"
"s_load_dword s17, s[78:79], 65536\n"
"s_mov_b32 s8, s76\n"
"s_and_b32 s9, s77, 0xffff\n"
"s_mov_b32 s11, 0x20000\n"
"s_mov_b32 s12, s74\n"
"s_and_b32 s13, s75, 0xffff\n"
"s_mov_b32 s14, 1152\n"
"s_mov_b32 s15, 0x20000\n"
"buffer_load_ushort v164, v236, s[12:15], 0 offen offset:0\n"
"buffer_load_ushort v165, v236, s[12:15], 0 offen offset:256\n"
"buffer_load_ushort v166, v236, s[12:15], 0 offen offset:512\n"
"buffer_load_ushort v167, v236, s[12:15], 0 offen offset:768\n"
"buffer_load_ushort v168, v236, s[12:15], 0 offen offset:1024\n"
"buffer_load_dwordx4 v[144:147], v235, s[8:11], s56 offen\n"
"buffer_load_dwordx4 v[148:151], v235, s[8:11], s57 offen\n"
"buffer_load_dwordx4 v[152:155], v235, s[8:11], s58 offen\n"
"buffer_load_dwordx4 v[156:159], v235, s[8:11], s59 offen\n"
"buffer_load_dwordx4 v[160:163], v235, s[8:11], s60 offen\n"
"s_mov_b32 s20, s64\n"
"s_and_b32 s21, s65, 0xffff\n"
"s_mov_b32 s22, 2359296\n"
"s_mov_b32 s23, 0x20000\n"
"s_mov_b32 m0, s63\n"
"s_nop 0\n"
"buffer_load_dwordx4 v222, s[20:23], s46 offen offset:0 lds\n"
"s_add_u32 m0, s63, 1024\n"
"s_nop 0\n"
"buffer_load_dwordx4 v223, s[20:23], s47 offen offset:0 lds\n"
"s_add_u32 m0, s63, 2048\n"
"s_nop 0\n"
"buffer_load_dwordx4 v222, s[20:23], s48 offen offset:0 lds\n"
"s_add_u32 m0, s63, 3072\n"
"s_nop 0\n"
"buffer_load_dwordx4 v223, s[20:23], s49 offen offset:0 lds\n"
"s_add_u32 m0, s63, 3968\n"
"s_nop 0\n"
"buffer_load_dwordx4 v222, s[20:23], s46 offen offset:128 lds\n"
"s_add_u32 m0, s63, 4992\n"
"s_nop 0\n"
"buffer_load_dwordx4 v223, s[20:23], s47 offen offset:128 lds\n"
"s_add_u32 m0, s63, 6016\n"
"s_nop 0\n"
"buffer_load_dwordx4 v222, s[20:23], s48 offen offset:128 lds\n"
"s_add_u32 m0, s63, 7040\n"
"s_nop 0\n"
"buffer_load_dwordx4 v223, s[20:23], s49 offen offset:128 lds\n"
"s_mov_b32 s24, s66\n"
"s_and_b32 s25, s67, 0xffff\n"
"s_mov_b32 s26, 4718592\n"
"s_mov_b32 s27, 0x20000\n"
"s_mov_b32 s36, s78\n"
"s_and_b32 s37, s79, 0xffff\n"
"s_mov_b32 s38, 65536\n"
"s_mov_b32 s39, 0x20000\n"
"s_mul_i32 s56, s43, 294912\n"
"s_add_u32 s28, s72, s56\n"
"s_addc_u32 s29, s73, 0\n"
"s_and_b32 s29, s29, 0xffff\n"
"s_mov_b32 s30, 294912\n"
"s_mov_b32 s31, 0x20000\n"
"s_mul_i32 s56, s43, 2359296\n"
"s_add_u32 s56, s56, 139264\n"
"s_add_u32 s32, s78, s56\n"
"s_addc_u32 s33, s79, 0\n"
"s_and_b32 s33, s33, 0xffff\n"
"s_mov_b32 s34, 2359296\n"
"s_mov_b32 s35, 0x20000\n"
"s_lshl_b32 s16, s41, 12\n"
"s_add_u32 s18, s40, s16\n"
"s_lshl_b32 s56, s41, 13\n"
"s_add_u32 s53, s40, s56\n"
"s_mul_i32 s52, s41, 73728\n"
"s_lshl_b32 s50, s41, 17\n"
"s_lshl_b32 s56, s43, 9\n"
"s_add_u32 s50, s50, s56\n"
"s_lshl_b32 s56, s41, 11\n"
"s_add_u32 s51, s40, s56\n"
"s_mov_b32 s61, 32768\n"
"s_mov_b32 s62, 37376\n"
"v_lshlrev_b32 v232, 13, v227\n"
"v_lshl_add_u32 v232, v226, 6, v232\n"
"v_lshl_add_u32 v217, v228, 4, v232\n"
"v_and_b32 v232, 2, v227\n"
"v_xor_b32 v232, v232, v226\n"
"v_lshlrev_b32 v233, 10, v227\n"
"v_lshl_add_u32 v233, v228, 6, v233\n"
"v_lshl_add_u32 v233, v232, 4, v233\n"
"v_add_u32 v240, s40, v233\n"
"v_add_u32 v241, 0x10000, v240\n"
"v_lshlrev_b32 v233, 10, v226\n"
"v_lshl_add_u32 v233, v227, 6, v233\n"
"v_lshl_add_u32 v233, v228, 3, v233\n"
"v_add_u32 v233, s40, v233\n"
"v_and_b32 v232, 1, v226\n"
"v_lshl_add_u32 v242, v232, 5, v233\n"
"v_xor_b32 v243, 32, v242\n"
"v_add_u32 v244, 0x10000, v242\n"
"v_add_u32 v245, 0x10000, v243\n"
"v_lshlrev_b32 v233, 8, v227\n"
"v_lshl_add_u32 v233, v226, 6, v233\n"
"v_lshl_add_u32 v246, v228, 4, v233\n"
"v_xor_b32 v247, 32, v246\n"
"v_mul_u32_u24 v233, 16392, v232\n"
"v_lshrrev_b32 v232, 1, v226\n"
"v_lshl_add_u32 v233, v232, 11, v233\n"
"v_lshl_add_u32 v233, v227, 6, v233\n"
"v_lshl_add_u32 v233, v228, 4, v233\n"
"v_add_u32 v218, s40, v233\n"
"v_add_u32 v219, 65568, v218\n"
"v_lshrrev_b32 v232, 1, v229\n"
"v_xor_b32 v232, v232, v226\n"
"v_lshlrev_b32 v233, 7, v229\n"
"v_lshl_add_u32 v233, v232, 4, v233\n"
"v_add_u32 v220, s63, v233\n"
"v_xor_b32 v221, 64, v220\n"
"s_add_u32 s56, s40, 148480\n"
"v_lshl_add_u32 v224, v226, 4, s56\n"
"v_lshrrev_b32 v232, 3, v234\n"
"v_lshlrev_b32 v233, 8, v232\n"
"v_and_b32 v232, 1, v234\n"
"v_lshl_add_u32 v233, v232, 6, v233\n"
"v_bfe_u32 v232, v234, 1, 2\n"
"v_lshl_add_u32 v233, v232, 4, v233\n"
"v_add_u32 v216, s56, v233\n"
"v_lshlrev_b32 v225, 5, v234\n"
"v_mov_b32 v232, 0x40000000\n"
"v_cmp_gt_u32 vcc, 144, v234\n"
"s_nop 1\n"
"v_cndmask_b32 v225, v232, v225, vcc\n"
"s_lshl_b32 s56, s41, 8\n"
"s_add_u32 s57, s40, 147456\n"
"s_add_u32 s56, s56, s57\n"
"v_and_b32 v232, 1, v248\n"
"v_lshlrev_b32 v232, 5, v232\n"
"v_lshl_add_u32 v232, v226, 6, v232\n"
"v_add_u32 v238, s56, v232\n"
"v_lshlrev_b32 v232, 8, v226\n"
"v_lshl_add_u32 v232, v229, 2, v232\n"
"v_add_u32 v239, s57, v232\n"
"v_mov_b32 v237, 0xffff0000\n"
"v_mov_b32 v176, 0\n"
"v_mov_b32 v177, 0\n"
"v_mov_b32 v178, 0\n"
"v_mov_b32 v179, 0\n"
"v_mov_b32 v180, 0\n"
"v_mov_b32 v181, 0\n"
"v_mov_b32 v182, 0\n"
"v_mov_b32 v183, 0\n"
"s_waitcnt vmcnt(13)\n"
"v_lshlrev_b32 v164, 16, v164\n"
"v_mul_f32 v184, 0xbfb8aa3b, v164\n"
"v_lshlrev_b32 v165, 16, v165\n"
"v_mul_f32 v185, 0xbfb8aa3b, v165\n"
"v_lshlrev_b32 v166, 16, v166\n"
"v_mul_f32 v186, 0xbfb8aa3b, v166\n"
"v_lshlrev_b32 v167, 16, v167\n"
"v_mul_f32 v187, 0xbfb8aa3b, v167\n"
"v_lshlrev_b32 v168, 16, v168\n"
"v_mul_f32 v188, 0xbfb8aa3b, v168\n"
"v_exp_f32 v184, v184\n"
"s_add_u32 m0, s63, 7936\n"
"s_nop 0\n"
"buffer_load_dwordx4 v222, s[20:23], s46 offen offset:256 lds\n"
"v_exp_f32 v185, v185\n"
"v_exp_f32 v186, v186\n"
"v_exp_f32 v187, v187\n"
"v_exp_f32 v188, v188\n"
"v_add_f32 v184, 1.0, v184\n"
"v_add_f32 v185, 1.0, v185\n"
"v_add_f32 v186, 1.0, v186\n"
"v_add_f32 v187, 1.0, v187\n"
"v_add_f32 v188, 1.0, v188\n"
"v_rcp_f32 v184, v184\n"
"v_rcp_f32 v185, v185\n"
"v_rcp_f32 v186, v186\n"
"s_add_u32 m0, s63, 8960\n"
"s_nop 0\n"
"buffer_load_dwordx4 v223, s[20:23], s47 offen offset:256 lds\n"
"v_rcp_f32 v187, v187\n"
"v_rcp_f32 v188, v188\n"
"v_mul_f32 v164, v164, v184\n"
"v_mul_f32 v165, v165, v185\n"
"v_mul_f32 v166, v166, v186\n"
"v_mul_f32 v167, v167, v187\n"
"v_mul_f32 v168, v168, v188\n"
"v_cvt_pk_bf16_f32 v164, v164, v164\n"
"v_and_b32 v164, v237, v164\n"
"v_cvt_pk_bf16_f32 v165, v165, v165\n"
"v_and_b32 v165, v237, v165\n"
"v_cvt_pk_bf16_f32 v166, v166, v166\n"
"s_add_u32 m0, s63, 9984\n"
"s_nop 0\n"
"buffer_load_dwordx4 v222, s[20:23], s48 offen offset:256 lds\n"
"v_and_b32 v166, v237, v166\n"
"v_cvt_pk_bf16_f32 v167, v167, v167\n"
"v_and_b32 v167, v237, v167\n"
"v_cvt_pk_bf16_f32 v168, v168, v168\n"
"v_and_b32 v168, v237, v168\n"
"s_waitcnt vmcnt(15)\n"
"v_lshlrev_b32 v184, 16, v144\n"
"v_and_b32 v185, v237, v144\n"
"v_lshlrev_b32 v186, 16, v145\n"
"v_and_b32 v187, v237, v145\n"
"v_lshlrev_b32 v188, 16, v146\n"
"v_and_b32 v189, v237, v146\n"
"s_add_u32 m0, s63, 11008\n"
"s_nop 0\n"
"buffer_load_dwordx4 v223, s[20:23], s49 offen offset:256 lds\n"
"v_lshlrev_b32 v190, 16, v147\n"
"v_and_b32 v191, v237, v147\n"
"v_fmac_f32 v176, v164, v184\n"
"v_fmac_f32 v177, v164, v185\n"
"v_fmac_f32 v178, v164, v186\n"
"v_fmac_f32 v179, v164, v187\n"
"v_fmac_f32 v180, v164, v188\n"
"v_fmac_f32 v181, v164, v189\n"
"v_fmac_f32 v182, v164, v190\n"
"v_fmac_f32 v183, v164, v191\n"
"s_waitcnt vmcnt(15)\n"
"v_lshlrev_b32 v184, 16, v148\n"
"s_add_u32 m0, s63, 11904\n"
"s_nop 0\n"
"buffer_load_dwordx4 v222, s[20:23], s46 offen offset:384 lds\n"
"v_and_b32 v185, v237, v148\n"
"v_lshlrev_b32 v186, 16, v149\n"
"v_and_b32 v187, v237, v149\n"
"v_lshlrev_b32 v188, 16, v150\n"
"v_and_b32 v189, v237, v150\n"
"v_lshlrev_b32 v190, 16, v151\n"
"v_and_b32 v191, v237, v151\n"
"v_fmac_f32 v176, v165, v184\n"
"v_fmac_f32 v177, v165, v185\n"
"v_fmac_f32 v178, v165, v186\n"
"v_fmac_f32 v179, v165, v187\n"
"v_fmac_f32 v180, v165, v188\n"
"s_add_u32 m0, s63, 12928\n"
"s_nop 0\n"
"buffer_load_dwordx4 v223, s[20:23], s47 offen offset:384 lds\n"
"v_fmac_f32 v181, v165, v189\n"
"v_fmac_f32 v182, v165, v190\n"
"v_fmac_f32 v183, v165, v191\n"
"s_waitcnt vmcnt(16)\n"
"v_lshlrev_b32 v184, 16, v152\n"
"v_and_b32 v185, v237, v152\n"
"v_lshlrev_b32 v186, 16, v153\n"
"v_and_b32 v187, v237, v153\n"
"v_lshlrev_b32 v188, 16, v154\n"
"v_and_b32 v189, v237, v154\n"
"v_lshlrev_b32 v190, 16, v155\n"
"v_and_b32 v191, v237, v155\n"
"s_add_u32 m0, s63, 13952\n"
"s_nop 0\n"
"buffer_load_dwordx4 v222, s[20:23], s48 offen offset:384 lds\n"
"v_fmac_f32 v176, v166, v184\n"
"v_fmac_f32 v177, v166, v185\n"
"v_fmac_f32 v178, v166, v186\n"
"v_fmac_f32 v179, v166, v187\n"
"v_fmac_f32 v180, v166, v188\n"
"v_fmac_f32 v181, v166, v189\n"
"v_fmac_f32 v182, v166, v190\n"
"v_fmac_f32 v183, v166, v191\n"
"s_waitcnt vmcnt(16)\n"
"v_lshlrev_b32 v184, 16, v156\n"
"v_and_b32 v185, v237, v156\n"
"v_lshlrev_b32 v186, 16, v157\n"
"s_add_u32 m0, s63, 14976\n"
"s_nop 0\n"
"buffer_load_dwordx4 v223, s[20:23], s49 offen offset:384 lds\n"
"v_and_b32 v187, v237, v157\n"
"v_lshlrev_b32 v188, 16, v158\n"
"v_and_b32 v189, v237, v158\n"
"v_lshlrev_b32 v190, 16, v159\n"
"v_and_b32 v191, v237, v159\n"
"v_fmac_f32 v176, v167, v184\n"
"v_fmac_f32 v177, v167, v185\n"
"v_fmac_f32 v178, v167, v186\n"
"v_fmac_f32 v179, v167, v187\n"
"v_fmac_f32 v180, v167, v188\n"
"v_fmac_f32 v181, v167, v189\n"
"v_fmac_f32 v182, v167, v190\n"
"s_add_u32 m0, s63, 15872\n"
"s_nop 0\n"
"buffer_load_dwordx4 v222, s[20:23], s46 offen offset:512 lds\n"
"v_fmac_f32 v183, v167, v191\n"
"s_waitcnt vmcnt(17)\n"
"v_lshlrev_b32 v184, 16, v160\n"
"v_and_b32 v185, v237, v160\n"
"v_lshlrev_b32 v186, 16, v161\n"
"v_and_b32 v187, v237, v161\n"
"v_lshlrev_b32 v188, 16, v162\n"
"v_and_b32 v189, v237, v162\n"
"v_lshlrev_b32 v190, 16, v163\n"
"v_and_b32 v191, v237, v163\n"
"v_fmac_f32 v176, v168, v184\n"
"v_fmac_f32 v177, v168, v185\n"
"s_add_u32 m0, s63, 16896\n"
"s_nop 0\n"
"buffer_load_dwordx4 v223, s[20:23], s47 offen offset:512 lds\n"
"v_fmac_f32 v178, v168, v186\n"
"v_fmac_f32 v179, v168, v187\n"
"v_fmac_f32 v180, v168, v188\n"
"v_fmac_f32 v181, v168, v189\n"
"v_fmac_f32 v182, v168, v190\n"
"v_fmac_f32 v183, v168, v191\n"
"v_add_f32_dpp v176, v176, v176 row_ror:2 row_mask:0xf bank_mask:0xf\n"
"v_add_f32_dpp v177, v177, v177 row_ror:2 row_mask:0xf bank_mask:0xf\n"
"v_add_f32_dpp v178, v178, v178 row_ror:2 row_mask:0xf bank_mask:0xf\n"
"v_add_f32_dpp v179, v179, v179 row_ror:2 row_mask:0xf bank_mask:0xf\n"
"v_add_f32_dpp v180, v180, v180 row_ror:2 row_mask:0xf bank_mask:0xf\n"
"v_add_f32_dpp v181, v181, v181 row_ror:2 row_mask:0xf bank_mask:0xf\n"
"s_add_u32 m0, s63, 17920\n"
"s_nop 0\n"
"buffer_load_dwordx4 v222, s[20:23], s48 offen offset:512 lds\n"
"v_add_f32_dpp v182, v182, v182 row_ror:2 row_mask:0xf bank_mask:0xf\n"
"v_add_f32_dpp v183, v183, v183 row_ror:2 row_mask:0xf bank_mask:0xf\n"
"v_add_f32_dpp v176, v176, v176 row_ror:4 row_mask:0xf bank_mask:0xf\n"
"v_add_f32_dpp v177, v177, v177 row_ror:4 row_mask:0xf bank_mask:0xf\n"
"v_add_f32_dpp v178, v178, v178 row_ror:4 row_mask:0xf bank_mask:0xf\n"
"v_add_f32_dpp v179, v179, v179 row_ror:4 row_mask:0xf bank_mask:0xf\n"
"v_add_f32_dpp v180, v180, v180 row_ror:4 row_mask:0xf bank_mask:0xf\n"
"v_add_f32_dpp v181, v181, v181 row_ror:4 row_mask:0xf bank_mask:0xf\n"
"v_add_f32_dpp v182, v182, v182 row_ror:4 row_mask:0xf bank_mask:0xf\n"
"v_add_f32_dpp v183, v183, v183 row_ror:4 row_mask:0xf bank_mask:0xf\n"
"v_add_f32_dpp v176, v176, v176 row_ror:8 row_mask:0xf bank_mask:0xf\n"
"v_add_f32_dpp v177, v177, v177 row_ror:8 row_mask:0xf bank_mask:0xf\n"
"s_add_u32 m0, s63, 18944\n"
"s_nop 0\n"
"buffer_load_dwordx4 v223, s[20:23], s49 offen offset:512 lds\n"
"v_add_f32_dpp v178, v178, v178 row_ror:8 row_mask:0xf bank_mask:0xf\n"
"v_add_f32_dpp v179, v179, v179 row_ror:8 row_mask:0xf bank_mask:0xf\n"
"v_add_f32_dpp v180, v180, v180 row_ror:8 row_mask:0xf bank_mask:0xf\n"
"v_add_f32_dpp v181, v181, v181 row_ror:8 row_mask:0xf bank_mask:0xf\n"
"v_add_f32_dpp v182, v182, v182 row_ror:8 row_mask:0xf bank_mask:0xf\n"
"v_add_f32_dpp v183, v183, v183 row_ror:8 row_mask:0xf bank_mask:0xf\n"
"s_mov_b32 exec_lo, 0x00030003\n"
"s_mov_b32 exec_hi, 0x00030003\n"
"ds_write_b128 v238, v[176:179]\n"
"ds_write_b128 v238, v[180:183] offset:16\n"
"s_mov_b64 exec, -1\n"
"s_waitcnt lgkmcnt(0)\n"
"s_barrier\n"
"ds_read_b32 v184, v239 offset:0\n"
"ds_read_b32 v185, v239 offset:64\n"
"ds_read_b32 v186, v239 offset:128\n"
"ds_read_b32 v187, v239 offset:192\n"
"s_add_u32 s17, s17, 1\n"
"s_lshl_b32 s56, s55, 7\n"
"s_add_u32 s56, s56, 32768\n"
"s_lshl_b32 s58, s41, 2\n"
"s_lshl_b32 s58, 15, s58\n"
"v_lshlrev_b32 v190, 3, v248\n"
"v_mov_b32 v189, s17\n"
"s_waitcnt lgkmcnt(2)\n"
"v_add_f32 v184, v184, v185\n"
"s_waitcnt lgkmcnt(0)\n"
"v_add_f32 v186, v186, v187\n"
"v_add_f32 v184, v184, v186\n"
"v_mov_b32 v185, v184\n"
"s_nop 1\n"
"v_permlane32_swap_b32 v184, v185\n"
"v_add_f32 v184, v184, v185\n"
"v_mov_b32 v185, v184\n"
"s_nop 1\n"
"v_permlane16_swap_b32 v184, v185\n"
"v_add_f32 v184, v184, v185\n"
"v_cvt_pk_bf16_f32 v188, v184, v184\n"
"v_and_b32 v188, v237, v188\n"
"s_mov_b32 exec_lo, s58\n"
"s_mov_b32 exec_hi, 0\n"
"buffer_store_dwordx2 v[188:189], v190, s[36:39], s56 offen sc1\n"
"s_mov_b64 exec, -1\n"
"s_add_u32 m0, s63, 19840\n"
"s_nop 0\n"
"buffer_load_dwordx4 v222, s[20:23], s46 offen offset:640 lds\n"
"s_add_u32 m0, s63, 20864\n"
"s_nop 0\n"
"buffer_load_dwordx4 v223, s[20:23], s47 offen offset:640 lds\n"
"s_add_u32 m0, s63, 21888\n"
"s_nop 0\n"
"buffer_load_dwordx4 v222, s[20:23], s48 offen offset:640 lds\n"
"s_add_u32 m0, s63, 22912\n"
"s_nop 0\n"
"buffer_load_dwordx4 v223, s[20:23], s49 offen offset:640 lds\n"
"s_add_u32 m0, s63, 23808\n"
"s_nop 0\n"
"buffer_load_dwordx4 v222, s[20:23], s46 offen offset:768 lds\n"
"s_add_u32 m0, s63, 24832\n"
"s_nop 0\n"
"buffer_load_dwordx4 v223, s[20:23], s47 offen offset:768 lds\n"
"s_add_u32 m0, s63, 25856\n"
"s_nop 0\n"
"buffer_load_dwordx4 v222, s[20:23], s48 offen offset:768 lds\n"
"s_add_u32 m0, s63, 26880\n"
"s_nop 0\n"
"buffer_load_dwordx4 v223, s[20:23], s49 offen offset:768 lds\n"
"s_add_u32 m0, s63, 27776\n"
"s_nop 0\n"
"buffer_load_dwordx4 v222, s[20:23], s46 offen offset:896 lds\n"
"s_add_u32 m0, s63, 28800\n"
"s_nop 0\n"
"buffer_load_dwordx4 v223, s[20:23], s47 offen offset:896 lds\n"
"s_add_u32 m0, s63, 29824\n"
"s_nop 0\n"
"buffer_load_dwordx4 v222, s[20:23], s48 offen offset:896 lds\n"
"s_add_u32 m0, s63, 30848\n"
"s_nop 0\n"
"buffer_load_dwordx4 v223, s[20:23], s49 offen offset:896 lds\n"
"s_add_u32 m0, s63, 31744\n"
"s_nop 0\n"
"buffer_load_dwordx4 v222, s[20:23], s46 offen offset:1024 lds\n"
"s_add_u32 m0, s63, 32768\n"
"s_nop 0\n"
"buffer_load_dwordx4 v223, s[20:23], s47 offen offset:1024 lds\n"
"s_add_u32 m0, s63, 33792\n"
"s_nop 0\n"
"buffer_load_dwordx4 v222, s[20:23], s48 offen offset:1024 lds\n"
"s_add_u32 m0, s63, 34816\n"
"s_nop 0\n"
"buffer_load_dwordx4 v223, s[20:23], s49 offen offset:1024 lds\n"
"s_lshl_b32 s56, s43, 19\n"
"s_add_u32 s8, s68, s56\n"
"s_addc_u32 s9, s69, 0\n"
"s_and_b32 s9, s9, 0xffff\n"
"s_mov_b32 s10, 524288\n"
"s_mov_b32 s11, 0x20000\n"
"s_add_u32 s12, s70, s56\n"
"s_addc_u32 s13, s71, 0\n"
"s_and_b32 s13, s13, 0xffff\n"
"s_mov_b32 s14, 524288\n"
"s_mov_b32 s15, 0x20000\n"
"v_mov_b32 v236, 0x3f803f80\n"
"v_mov_b32 v200, 0\n"
"v_mov_b32 v202, 0\n"
"v_mov_b32 v204, 0\n"
"v_mov_b32 v206, 0\n"
"v_mov_b32 v208, 0\n"
"v_mov_b32 v210, 0\n"
"v_mov_b32 v212, 0\n"
"v_mov_b32 v214, 0\n"
"s_waitcnt vmcnt(33)\n"
"ds_read_b128 v[0:3], v220 offset:0\n"
"ds_read_b128 v[4:7], v220 offset:2048\n"
"ds_read_b128 v[8:11], v221 offset:0\n"
"ds_read_b128 v[12:15], v221 offset:2048\n"
"s_waitcnt vmcnt(29)\n"
"ds_read_b128 v[16:19], v220 offset:4096\n"
"ds_read_b128 v[20:23], v220 offset:6144\n"
"ds_read_b128 v[24:27], v221 offset:4096\n"
"ds_read_b128 v[28:31], v221 offset:6144\n"
"s_waitcnt lgkmcnt(7)\n"
"v_dot2c_f32_bf16 v200, v0, v236\n"
"v_dot2c_f32_bf16 v202, v0, v0\n"
"s_waitcnt lgkmcnt(6)\n"
"v_dot2c_f32_bf16 v204, v4, v236\n"
"v_dot2c_f32_bf16 v206, v4, v4\n"
"s_waitcnt lgkmcnt(5)\n"
"v_dot2c_f32_bf16 v208, v8, v236\n"
"v_dot2c_f32_bf16 v210, v8, v8\n"
"s_waitcnt lgkmcnt(4)\n"
"v_dot2c_f32_bf16 v212, v12, v236\n"
"v_dot2c_f32_bf16 v214, v12, v12\n"
"v_dot2c_f32_bf16 v200, v1, v236\n"
"v_dot2c_f32_bf16 v202, v1, v1\n"
"v_dot2c_f32_bf16 v204, v5, v236\n"
"v_dot2c_f32_bf16 v206, v5, v5\n"
"v_dot2c_f32_bf16 v208, v9, v236\n"
"v_dot2c_f32_bf16 v210, v9, v9\n"
"v_dot2c_f32_bf16 v212, v13, v236\n"
"v_dot2c_f32_bf16 v214, v13, v13\n"
"v_dot2c_f32_bf16 v200, v2, v236\n"
"v_dot2c_f32_bf16 v202, v2, v2\n"
"v_dot2c_f32_bf16 v204, v6, v236\n"
"v_dot2c_f32_bf16 v206, v6, v6\n"
"v_dot2c_f32_bf16 v208, v10, v236\n"
"v_dot2c_f32_bf16 v210, v10, v10\n"
"v_dot2c_f32_bf16 v212, v14, v236\n"
"v_dot2c_f32_bf16 v214, v14, v14\n"
"v_dot2c_f32_bf16 v200, v3, v236\n"
"v_dot2c_f32_bf16 v202, v3, v3\n"
"v_dot2c_f32_bf16 v204, v7, v236\n"
"v_dot2c_f32_bf16 v206, v7, v7\n"
"v_dot2c_f32_bf16 v208, v11, v236\n"
"v_dot2c_f32_bf16 v210, v11, v11\n"
"v_dot2c_f32_bf16 v212, v15, v236\n"
"v_dot2c_f32_bf16 v214, v15, v15\n"
"s_waitcnt vmcnt(25)\n"
"ds_read_b128 v[32:35], v220 offset:8192\n"
"ds_read_b128 v[36:39], v220 offset:10240\n"
"ds_read_b128 v[40:43], v221 offset:8192\n"
"ds_read_b128 v[44:47], v221 offset:10240\n"
"s_waitcnt lgkmcnt(7)\n"
"v_dot2c_f32_bf16 v200, v16, v236\n"
"v_dot2c_f32_bf16 v202, v16, v16\n"
"s_waitcnt lgkmcnt(6)\n"
"v_dot2c_f32_bf16 v204, v20, v236\n"
"v_dot2c_f32_bf16 v206, v20, v20\n"
"s_waitcnt lgkmcnt(5)\n"
"v_dot2c_f32_bf16 v208, v24, v236\n"
"v_dot2c_f32_bf16 v210, v24, v24\n"
"s_waitcnt lgkmcnt(4)\n"
"v_dot2c_f32_bf16 v212, v28, v236\n"
"v_dot2c_f32_bf16 v214, v28, v28\n"
"v_dot2c_f32_bf16 v200, v17, v236\n"
"v_dot2c_f32_bf16 v202, v17, v17\n"
"v_dot2c_f32_bf16 v204, v21, v236\n"
"v_dot2c_f32_bf16 v206, v21, v21\n"
"v_dot2c_f32_bf16 v208, v25, v236\n"
"v_dot2c_f32_bf16 v210, v25, v25\n"
"v_dot2c_f32_bf16 v212, v29, v236\n"
"v_dot2c_f32_bf16 v214, v29, v29\n"
"v_dot2c_f32_bf16 v200, v18, v236\n"
"v_dot2c_f32_bf16 v202, v18, v18\n"
"v_dot2c_f32_bf16 v204, v22, v236\n"
"v_dot2c_f32_bf16 v206, v22, v22\n"
"v_dot2c_f32_bf16 v208, v26, v236\n"
"v_dot2c_f32_bf16 v210, v26, v26\n"
"v_dot2c_f32_bf16 v212, v30, v236\n"
"v_dot2c_f32_bf16 v214, v30, v30\n"
"v_dot2c_f32_bf16 v200, v19, v236\n"
"v_dot2c_f32_bf16 v202, v19, v19\n"
"v_dot2c_f32_bf16 v204, v23, v236\n"
"v_dot2c_f32_bf16 v206, v23, v23\n"
"v_dot2c_f32_bf16 v208, v27, v236\n"
"v_dot2c_f32_bf16 v210, v27, v27\n"
"v_dot2c_f32_bf16 v212, v31, v236\n"
"v_dot2c_f32_bf16 v214, v31, v31\n"
"s_waitcnt vmcnt(21)\n"
"ds_read_b128 v[48:51], v220 offset:12288\n"
"ds_read_b128 v[52:55], v220 offset:14336\n"
"ds_read_b128 v[56:59], v221 offset:12288\n"
"ds_read_b128 v[60:63], v221 offset:14336\n"
"s_waitcnt lgkmcnt(7)\n"
"v_dot2c_f32_bf16 v200, v32, v236\n"
"v_dot2c_f32_bf16 v202, v32, v32\n"
"s_waitcnt lgkmcnt(6)\n"
"v_dot2c_f32_bf16 v204, v36, v236\n"
"v_dot2c_f32_bf16 v206, v36, v36\n"
"s_waitcnt lgkmcnt(5)\n"
"v_dot2c_f32_bf16 v208, v40, v236\n"
"v_dot2c_f32_bf16 v210, v40, v40\n"
"s_waitcnt lgkmcnt(4)\n"
"v_dot2c_f32_bf16 v212, v44, v236\n"
"v_dot2c_f32_bf16 v214, v44, v44\n"
"v_dot2c_f32_bf16 v200, v33, v236\n"
"v_dot2c_f32_bf16 v202, v33, v33\n"
"v_dot2c_f32_bf16 v204, v37, v236\n"
"v_dot2c_f32_bf16 v206, v37, v37\n"
"v_dot2c_f32_bf16 v208, v41, v236\n"
"v_dot2c_f32_bf16 v210, v41, v41\n"
"v_dot2c_f32_bf16 v212, v45, v236\n"
"v_dot2c_f32_bf16 v214, v45, v45\n"
"v_dot2c_f32_bf16 v200, v34, v236\n"
"v_dot2c_f32_bf16 v202, v34, v34\n"
"v_dot2c_f32_bf16 v204, v38, v236\n"
"v_dot2c_f32_bf16 v206, v38, v38\n"
"v_dot2c_f32_bf16 v208, v42, v236\n"
"v_dot2c_f32_bf16 v210, v42, v42\n"
"v_dot2c_f32_bf16 v212, v46, v236\n"
"v_dot2c_f32_bf16 v214, v46, v46\n"
"v_dot2c_f32_bf16 v200, v35, v236\n"
"v_dot2c_f32_bf16 v202, v35, v35\n"
"v_dot2c_f32_bf16 v204, v39, v236\n"
"v_dot2c_f32_bf16 v206, v39, v39\n"
"v_dot2c_f32_bf16 v208, v43, v236\n"
"v_dot2c_f32_bf16 v210, v43, v43\n"
"v_dot2c_f32_bf16 v212, v47, v236\n"
"v_dot2c_f32_bf16 v214, v47, v47\n"
"s_waitcnt vmcnt(17)\n"
"ds_read_b128 v[64:67], v220 offset:16384\n"
"ds_read_b128 v[68:71], v220 offset:18432\n"
"ds_read_b128 v[72:75], v221 offset:16384\n"
"ds_read_b128 v[76:79], v221 offset:18432\n"
"s_waitcnt lgkmcnt(7)\n"
"v_dot2c_f32_bf16 v200, v48, v236\n"
"v_dot2c_f32_bf16 v202, v48, v48\n"
"s_waitcnt lgkmcnt(6)\n"
"v_dot2c_f32_bf16 v204, v52, v236\n"
"v_dot2c_f32_bf16 v206, v52, v52\n"
"s_waitcnt lgkmcnt(5)\n"
"v_dot2c_f32_bf16 v208, v56, v236\n"
"v_dot2c_f32_bf16 v210, v56, v56\n"
"s_waitcnt lgkmcnt(4)\n"
"v_dot2c_f32_bf16 v212, v60, v236\n"
"v_dot2c_f32_bf16 v214, v60, v60\n"
"v_dot2c_f32_bf16 v200, v49, v236\n"
"v_dot2c_f32_bf16 v202, v49, v49\n"
"v_dot2c_f32_bf16 v204, v53, v236\n"
"v_dot2c_f32_bf16 v206, v53, v53\n"
"v_dot2c_f32_bf16 v208, v57, v236\n"
"v_dot2c_f32_bf16 v210, v57, v57\n"
"v_dot2c_f32_bf16 v212, v61, v236\n"
"v_dot2c_f32_bf16 v214, v61, v61\n"
"v_dot2c_f32_bf16 v200, v50, v236\n"
"v_dot2c_f32_bf16 v202, v50, v50\n"
"v_dot2c_f32_bf16 v204, v54, v236\n"
"v_dot2c_f32_bf16 v206, v54, v54\n"
"v_dot2c_f32_bf16 v208, v58, v236\n"
"v_dot2c_f32_bf16 v210, v58, v58\n"
"v_dot2c_f32_bf16 v212, v62, v236\n"
"v_dot2c_f32_bf16 v214, v62, v62\n"
"v_dot2c_f32_bf16 v200, v51, v236\n"
"v_dot2c_f32_bf16 v202, v51, v51\n"
"v_dot2c_f32_bf16 v204, v55, v236\n"
"v_dot2c_f32_bf16 v206, v55, v55\n"
"v_dot2c_f32_bf16 v208, v59, v236\n"
"v_dot2c_f32_bf16 v210, v59, v59\n"
"v_dot2c_f32_bf16 v212, v63, v236\n"
"v_dot2c_f32_bf16 v214, v63, v63\n"
"s_waitcnt vmcnt(12)\n"
"ds_read_b128 v[80:83], v220 offset:20480\n"
"ds_read_b128 v[84:87], v220 offset:22528\n"
"ds_read_b128 v[88:91], v221 offset:20480\n"
"ds_read_b128 v[92:95], v221 offset:22528\n"
"s_waitcnt lgkmcnt(7)\n"
"v_dot2c_f32_bf16 v200, v64, v236\n"
"v_dot2c_f32_bf16 v202, v64, v64\n"
"s_waitcnt lgkmcnt(6)\n"
"v_dot2c_f32_bf16 v204, v68, v236\n"
"v_dot2c_f32_bf16 v206, v68, v68\n"
"s_waitcnt lgkmcnt(5)\n"
"v_dot2c_f32_bf16 v208, v72, v236\n"
"v_dot2c_f32_bf16 v210, v72, v72\n"
"s_waitcnt lgkmcnt(4)\n"
"v_dot2c_f32_bf16 v212, v76, v236\n"
"v_dot2c_f32_bf16 v214, v76, v76\n"
"v_dot2c_f32_bf16 v200, v65, v236\n"
"v_dot2c_f32_bf16 v202, v65, v65\n"
"v_dot2c_f32_bf16 v204, v69, v236\n"
"v_dot2c_f32_bf16 v206, v69, v69\n"
"v_dot2c_f32_bf16 v208, v73, v236\n"
"v_dot2c_f32_bf16 v210, v73, v73\n"
"v_dot2c_f32_bf16 v212, v77, v236\n"
"v_dot2c_f32_bf16 v214, v77, v77\n"
"v_dot2c_f32_bf16 v200, v66, v236\n"
"v_dot2c_f32_bf16 v202, v66, v66\n"
"v_dot2c_f32_bf16 v204, v70, v236\n"
"v_dot2c_f32_bf16 v206, v70, v70\n"
"v_dot2c_f32_bf16 v208, v74, v236\n"
"v_dot2c_f32_bf16 v210, v74, v74\n"
"v_dot2c_f32_bf16 v212, v78, v236\n"
"v_dot2c_f32_bf16 v214, v78, v78\n"
"v_dot2c_f32_bf16 v200, v67, v236\n"
"v_dot2c_f32_bf16 v202, v67, v67\n"
"v_dot2c_f32_bf16 v204, v71, v236\n"
"v_dot2c_f32_bf16 v206, v71, v71\n"
"v_dot2c_f32_bf16 v208, v75, v236\n"
"v_dot2c_f32_bf16 v210, v75, v75\n"
"v_dot2c_f32_bf16 v212, v79, v236\n"
"v_dot2c_f32_bf16 v214, v79, v79\n"
"s_waitcnt vmcnt(8)\n"
"ds_read_b128 v[96:99], v220 offset:24576\n"
"ds_read_b128 v[100:103], v220 offset:26624\n"
"ds_read_b128 v[104:107], v221 offset:24576\n"
"ds_read_b128 v[108:111], v221 offset:26624\n"
"s_waitcnt lgkmcnt(7)\n"
"v_dot2c_f32_bf16 v200, v80, v236\n"
"v_dot2c_f32_bf16 v202, v80, v80\n"
"s_waitcnt lgkmcnt(6)\n"
"v_dot2c_f32_bf16 v204, v84, v236\n"
"v_dot2c_f32_bf16 v206, v84, v84\n"
"s_waitcnt lgkmcnt(5)\n"
"v_dot2c_f32_bf16 v208, v88, v236\n"
"v_dot2c_f32_bf16 v210, v88, v88\n"
"s_waitcnt lgkmcnt(4)\n"
"v_dot2c_f32_bf16 v212, v92, v236\n"
"v_dot2c_f32_bf16 v214, v92, v92\n"
"v_dot2c_f32_bf16 v200, v81, v236\n"
"v_dot2c_f32_bf16 v202, v81, v81\n"
"v_dot2c_f32_bf16 v204, v85, v236\n"
"v_dot2c_f32_bf16 v206, v85, v85\n"
"v_dot2c_f32_bf16 v208, v89, v236\n"
"v_dot2c_f32_bf16 v210, v89, v89\n"
"v_dot2c_f32_bf16 v212, v93, v236\n"
"v_dot2c_f32_bf16 v214, v93, v93\n"
"v_dot2c_f32_bf16 v200, v82, v236\n"
"v_dot2c_f32_bf16 v202, v82, v82\n"
"v_dot2c_f32_bf16 v204, v86, v236\n"
"v_dot2c_f32_bf16 v206, v86, v86\n"
"v_dot2c_f32_bf16 v208, v90, v236\n"
"v_dot2c_f32_bf16 v210, v90, v90\n"
"v_dot2c_f32_bf16 v212, v94, v236\n"
"v_dot2c_f32_bf16 v214, v94, v94\n"
"v_dot2c_f32_bf16 v200, v83, v236\n"
"v_dot2c_f32_bf16 v202, v83, v83\n"
"v_dot2c_f32_bf16 v204, v87, v236\n"
"v_dot2c_f32_bf16 v206, v87, v87\n"
"v_dot2c_f32_bf16 v208, v91, v236\n"
"v_dot2c_f32_bf16 v210, v91, v91\n"
"v_dot2c_f32_bf16 v212, v95, v236\n"
"v_dot2c_f32_bf16 v214, v95, v95\n"
"s_waitcnt vmcnt(4)\n"
"ds_read_b128 v[112:115], v220 offset:28672\n"
"ds_read_b128 v[116:119], v220 offset:30720\n"
"ds_read_b128 v[120:123], v221 offset:28672\n"
"ds_read_b128 v[124:127], v221 offset:30720\n"
"s_waitcnt lgkmcnt(7)\n"
"v_dot2c_f32_bf16 v200, v96, v236\n"
"v_dot2c_f32_bf16 v202, v96, v96\n"
"s_waitcnt lgkmcnt(6)\n"
"v_dot2c_f32_bf16 v204, v100, v236\n"
"v_dot2c_f32_bf16 v206, v100, v100\n"
"s_waitcnt lgkmcnt(5)\n"
"v_dot2c_f32_bf16 v208, v104, v236\n"
"v_dot2c_f32_bf16 v210, v104, v104\n"
"s_waitcnt lgkmcnt(4)\n"
"v_dot2c_f32_bf16 v212, v108, v236\n"
"v_dot2c_f32_bf16 v214, v108, v108\n"
"v_dot2c_f32_bf16 v200, v97, v236\n"
"v_dot2c_f32_bf16 v202, v97, v97\n"
"v_dot2c_f32_bf16 v204, v101, v236\n"
"v_dot2c_f32_bf16 v206, v101, v101\n"
"v_dot2c_f32_bf16 v208, v105, v236\n"
"v_dot2c_f32_bf16 v210, v105, v105\n"
"v_dot2c_f32_bf16 v212, v109, v236\n"
"v_dot2c_f32_bf16 v214, v109, v109\n"
"v_dot2c_f32_bf16 v200, v98, v236\n"
"v_dot2c_f32_bf16 v202, v98, v98\n"
"v_dot2c_f32_bf16 v204, v102, v236\n"
"v_dot2c_f32_bf16 v206, v102, v102\n"
"v_dot2c_f32_bf16 v208, v106, v236\n"
"v_dot2c_f32_bf16 v210, v106, v106\n"
"v_dot2c_f32_bf16 v212, v110, v236\n"
"v_dot2c_f32_bf16 v214, v110, v110\n"
"v_dot2c_f32_bf16 v200, v99, v236\n"
"v_dot2c_f32_bf16 v202, v99, v99\n"
"v_dot2c_f32_bf16 v204, v103, v236\n"
"v_dot2c_f32_bf16 v206, v103, v103\n"
"v_dot2c_f32_bf16 v208, v107, v236\n"
"v_dot2c_f32_bf16 v210, v107, v107\n"
"v_dot2c_f32_bf16 v212, v111, v236\n"
"v_dot2c_f32_bf16 v214, v111, v111\n"
"s_waitcnt vmcnt(0)\n"
"ds_read_b128 v[128:131], v220 offset:32768\n"
"ds_read_b128 v[132:135], v220 offset:34816\n"
"ds_read_b128 v[136:139], v221 offset:32768\n"
"ds_read_b128 v[140:143], v221 offset:34816\n"
"s_waitcnt lgkmcnt(7)\n"
"v_dot2c_f32_bf16 v200, v112, v236\n"
"v_dot2c_f32_bf16 v202, v112, v112\n"
"s_waitcnt lgkmcnt(6)\n"
"v_dot2c_f32_bf16 v204, v116, v236\n"
"v_dot2c_f32_bf16 v206, v116, v116\n"
"s_waitcnt lgkmcnt(5)\n"
"v_dot2c_f32_bf16 v208, v120, v236\n"
"v_dot2c_f32_bf16 v210, v120, v120\n"
"s_waitcnt lgkmcnt(4)\n"
"v_dot2c_f32_bf16 v212, v124, v236\n"
"v_dot2c_f32_bf16 v214, v124, v124\n"
"v_dot2c_f32_bf16 v200, v113, v236\n"
"v_dot2c_f32_bf16 v202, v113, v113\n"
"v_dot2c_f32_bf16 v204, v117, v236\n"
"v_dot2c_f32_bf16 v206, v117, v117\n"
"v_dot2c_f32_bf16 v208, v121, v236\n"
"v_dot2c_f32_bf16 v210, v121, v121\n"
"v_dot2c_f32_bf16 v212, v125, v236\n"
"v_dot2c_f32_bf16 v214, v125, v125\n"
"v_dot2c_f32_bf16 v200, v114, v236\n"
"v_dot2c_f32_bf16 v202, v114, v114\n"
"v_dot2c_f32_bf16 v204, v118, v236\n"
"v_dot2c_f32_bf16 v206, v118, v118\n"
"v_dot2c_f32_bf16 v208, v122, v236\n"
"v_dot2c_f32_bf16 v210, v122, v122\n"
"v_dot2c_f32_bf16 v212, v126, v236\n"
"v_dot2c_f32_bf16 v214, v126, v126\n"
"v_dot2c_f32_bf16 v200, v115, v236\n"
"v_dot2c_f32_bf16 v202, v115, v115\n"
"v_dot2c_f32_bf16 v204, v119, v236\n"
"v_dot2c_f32_bf16 v206, v119, v119\n"
"v_dot2c_f32_bf16 v208, v123, v236\n"
"v_dot2c_f32_bf16 v210, v123, v123\n"
"v_dot2c_f32_bf16 v212, v127, v236\n"
"v_dot2c_f32_bf16 v214, v127, v127\n"
"s_waitcnt lgkmcnt(0)\n"
"s_barrier\n"
"s_add_u32 m0, s51, 98352\n"
"s_nop 0\n"
"buffer_load_dwordx4 v217, s[24:27], s50 offen offset:0 lds\n"
"s_add_u32 m0, s51, 106288\n"
"s_nop 0\n"
"buffer_load_dwordx4 v217, s[24:27], s50 offen offset:256 lds\n"
"s_add_u32 s50, s50, 32768\n"
"v_dot2c_f32_bf16 v200, v128, v236\n"
"v_dot2c_f32_bf16 v202, v128, v128\n"
"v_dot2c_f32_bf16 v204, v132, v236\n"
"v_dot2c_f32_bf16 v206, v132, v132\n"
"v_dot2c_f32_bf16 v208, v136, v236\n"
"v_dot2c_f32_bf16 v210, v136, v136\n"
"v_dot2c_f32_bf16 v212, v140, v236\n"
"v_dot2c_f32_bf16 v214, v140, v140\n"
"v_dot2c_f32_bf16 v200, v129, v236\n"
"v_dot2c_f32_bf16 v202, v129, v129\n"
"v_dot2c_f32_bf16 v204, v133, v236\n"
"v_dot2c_f32_bf16 v206, v133, v133\n"
"v_dot2c_f32_bf16 v208, v137, v236\n"
"v_dot2c_f32_bf16 v210, v137, v137\n"
"v_dot2c_f32_bf16 v212, v141, v236\n"
"v_dot2c_f32_bf16 v214, v141, v141\n"
"v_dot2c_f32_bf16 v200, v130, v236\n"
"v_dot2c_f32_bf16 v202, v130, v130\n"
"v_dot2c_f32_bf16 v204, v134, v236\n"
"v_dot2c_f32_bf16 v206, v134, v134\n"
"v_dot2c_f32_bf16 v208, v138, v236\n"
"v_dot2c_f32_bf16 v210, v138, v138\n"
"v_dot2c_f32_bf16 v212, v142, v236\n"
"v_dot2c_f32_bf16 v214, v142, v142\n"
"v_dot2c_f32_bf16 v200, v131, v236\n"
"v_dot2c_f32_bf16 v202, v131, v131\n"
"v_dot2c_f32_bf16 v204, v135, v236\n"
"v_dot2c_f32_bf16 v206, v135, v135\n"
"v_dot2c_f32_bf16 v208, v139, v236\n"
"v_dot2c_f32_bf16 v210, v139, v139\n"
"v_dot2c_f32_bf16 v212, v143, v236\n"
"v_dot2c_f32_bf16 v214, v143, v143\n"
"s_nop 2\n"
"v_add_f32 v200, v200, v208\n"
"v_add_f32 v202, v202, v210\n"
"v_add_f32 v204, v204, v212\n"
"v_add_f32 v206, v206, v214\n"
"v_mov_b32 v208, v200\n"
"v_mov_b32 v209, v202\n"
"v_mov_b32 v210, v204\n"
"v_mov_b32 v211, v206\n"
"s_nop 0\n"
"v_permlane32_swap_b32 v200, v208\n"
"v_permlane32_swap_b32 v202, v209\n"
"v_permlane32_swap_b32 v204, v210\n"
"v_permlane32_swap_b32 v206, v211\n"
"v_add_f32 v200, v200, v208\n"
"v_add_f32 v202, v202, v209\n"
"v_add_f32 v204, v204, v210\n"
"v_add_f32 v206, v206, v211\n"
"v_mov_b32 v208, v200\n"
"v_mov_b32 v209, v202\n"
"v_mov_b32 v210, v204\n"
"v_mov_b32 v211, v206\n"
"s_nop 0\n"
"v_permlane16_swap_b32 v200, v208\n"
"v_permlane16_swap_b32 v202, v209\n"
"v_permlane16_swap_b32 v204, v210\n"
"v_permlane16_swap_b32 v206, v211\n"
"v_add_f32 v200, v200, v208\n"
"v_add_f32 v202, v202, v209\n"
"v_add_f32 v204, v204, v210\n"
"v_add_f32 v206, v206, v211\n"
"v_mul_f32 v200, 0x3ae38e39, v200\n"
"v_mul_f32 v202, 0x3ae38e39, v202\n"
"v_fma_f32 v202, -v200, v200, v202\n"
"v_max_f32 v202, 0, v202\n"
"v_add_f32 v202, 0x3727c5ac, v202\n"
"v_mul_f32 v204, 0x3ae38e39, v204\n"
"v_mul_f32 v206, 0x3ae38e39, v206\n"
"v_fma_f32 v206, -v204, v204, v206\n"
"v_max_f32 v206, 0, v206\n"
"v_add_f32 v206, 0x3727c5ac, v206\n"
"v_rsq_f32 v201, v202\n"
"v_rsq_f32 v205, v206\n"
"s_nop 0\n"
"v_mul_f32 v202, v200, v201\n"
"v_sub_f32 v202, 0, v202\n"
"v_mov_b32 v200, v201\n"
"v_mul_f32 v206, v204, v205\n"
"v_sub_f32 v206, 0, v206\n"
"v_mov_b32 v204, v205\n"
"v_lshlrev_b32 v176, 16, v0\n"
"v_and_b32 v177, v237, v0\n"
"v_pk_fma_f32 v[176:177], v[176:177], v[200:201], v[202:203] op_sel_hi:[1,0,0]\n"
"v_cvt_pk_f16_f32 v0, v176, v177\n"
"v_lshlrev_b32 v178, 16, v1\n"
"v_and_b32 v179, v237, v1\n"
"v_pk_fma_f32 v[178:179], v[178:179], v[200:201], v[202:203] op_sel_hi:[1,0,0]\n"
"v_cvt_pk_f16_f32 v1, v178, v179\n"
"v_lshlrev_b32 v180, 16, v2\n"
"v_and_b32 v181, v237, v2\n"
"v_pk_fma_f32 v[180:181], v[180:181], v[200:201], v[202:203] op_sel_hi:[1,0,0]\n"
"v_cvt_pk_f16_f32 v2, v180, v181\n"
"v_lshlrev_b32 v182, 16, v3\n"
"v_and_b32 v183, v237, v3\n"
"v_pk_fma_f32 v[182:183], v[182:183], v[200:201], v[202:203] op_sel_hi:[1,0,0]\n"
"v_cvt_pk_f16_f32 v3, v182, v183\n"
"v_lshlrev_b32 v184, 16, v4\n"
"v_and_b32 v185, v237, v4\n"
"v_pk_fma_f32 v[184:185], v[184:185], v[204:205], v[206:207] op_sel_hi:[1,0,0]\n"
"v_cvt_pk_f16_f32 v4, v184, v185\n"
"v_lshlrev_b32 v186, 16, v5\n"
"s_add_u32 m0, s51, 99376\n"
"s_nop 0\n"
"buffer_load_dwordx4 v217, s[24:27], s50 offen offset:0 lds\n"
"v_and_b32 v187, v237, v5\n"
"v_pk_fma_f32 v[186:187], v[186:187], v[204:205], v[206:207] op_sel_hi:[1,0,0]\n"
"v_cvt_pk_f16_f32 v5, v186, v187\n"
"v_lshlrev_b32 v188, 16, v6\n"
"v_and_b32 v189, v237, v6\n"
"v_pk_fma_f32 v[188:189], v[188:189], v[204:205], v[206:207] op_sel_hi:[1,0,0]\n"
"v_cvt_pk_f16_f32 v6, v188, v189\n"
"v_lshlrev_b32 v190, 16, v7\n"
"v_and_b32 v191, v237, v7\n"
"v_pk_fma_f32 v[190:191], v[190:191], v[204:205], v[206:207] op_sel_hi:[1,0,0]\n"
"v_cvt_pk_f16_f32 v7, v190, v191\n"
"v_lshlrev_b32 v176, 16, v8\n"
"v_and_b32 v177, v237, v8\n"
"v_pk_fma_f32 v[176:177], v[176:177], v[200:201], v[202:203] op_sel_hi:[1,0,0]\n"
"v_cvt_pk_f16_f32 v8, v176, v177\n"
"v_lshlrev_b32 v178, 16, v9\n"
"v_and_b32 v179, v237, v9\n"
"v_pk_fma_f32 v[178:179], v[178:179], v[200:201], v[202:203] op_sel_hi:[1,0,0]\n"
"v_cvt_pk_f16_f32 v9, v178, v179\n"
"v_lshlrev_b32 v180, 16, v10\n"
"v_and_b32 v181, v237, v10\n"
"s_add_u32 m0, s51, 107312\n"
"s_nop 0\n"
"buffer_load_dwordx4 v217, s[24:27], s50 offen offset:256 lds\n"
"s_add_u32 s50, s50, 32768\n"
"v_pk_fma_f32 v[180:181], v[180:181], v[200:201], v[202:203] op_sel_hi:[1,0,0]\n"
"v_cvt_pk_f16_f32 v10, v180, v181\n"
"v_lshlrev_b32 v182, 16, v11\n"
"v_and_b32 v183, v237, v11\n"
"v_pk_fma_f32 v[182:183], v[182:183], v[200:201], v[202:203] op_sel_hi:[1,0,0]\n"
"v_cvt_pk_f16_f32 v11, v182, v183\n"
"v_lshlrev_b32 v184, 16, v12\n"
"v_and_b32 v185, v237, v12\n"
"v_pk_fma_f32 v[184:185], v[184:185], v[204:205], v[206:207] op_sel_hi:[1,0,0]\n"
"v_cvt_pk_f16_f32 v12, v184, v185\n"
"v_lshlrev_b32 v186, 16, v13\n"
"v_and_b32 v187, v237, v13\n"
"v_pk_fma_f32 v[186:187], v[186:187], v[204:205], v[206:207] op_sel_hi:[1,0,0]\n"
"v_cvt_pk_f16_f32 v13, v186, v187\n"
"v_lshlrev_b32 v188, 16, v14\n"
"v_and_b32 v189, v237, v14\n"
"v_pk_fma_f32 v[188:189], v[188:189], v[204:205], v[206:207] op_sel_hi:[1,0,0]\n"
"v_cvt_pk_f16_f32 v14, v188, v189\n"
"v_lshlrev_b32 v190, 16, v15\n"
"v_and_b32 v191, v237, v15\n"
"v_pk_fma_f32 v[190:191], v[190:191], v[204:205], v[206:207] op_sel_hi:[1,0,0]\n"
"s_add_u32 m0, s51, 114744\n"
"s_nop 0\n"
"buffer_load_dwordx4 v217, s[24:27], s50 offen offset:0 lds\n"
"v_cvt_pk_f16_f32 v15, v190, v191\n"
"v_lshlrev_b32 v176, 16, v16\n"
"v_and_b32 v177, v237, v16\n"
"v_pk_fma_f32 v[176:177], v[176:177], v[200:201], v[202:203] op_sel_hi:[1,0,0]\n"
"v_cvt_pk_f16_f32 v16, v176, v177\n"
"v_lshlrev_b32 v178, 16, v17\n"
"v_and_b32 v179, v237, v17\n"
"v_pk_fma_f32 v[178:179], v[178:179], v[200:201], v[202:203] op_sel_hi:[1,0,0]\n"
"v_cvt_pk_f16_f32 v17, v178, v179\n"
"v_lshlrev_b32 v180, 16, v18\n"
"v_and_b32 v181, v237, v18\n"
"v_pk_fma_f32 v[180:181], v[180:181], v[200:201], v[202:203] op_sel_hi:[1,0,0]\n"
"v_cvt_pk_f16_f32 v18, v180, v181\n"
"v_lshlrev_b32 v182, 16, v19\n"
"v_and_b32 v183, v237, v19\n"
"v_pk_fma_f32 v[182:183], v[182:183], v[200:201], v[202:203] op_sel_hi:[1,0,0]\n"
"v_cvt_pk_f16_f32 v19, v182, v183\n"
"v_lshlrev_b32 v184, 16, v20\n"
"v_and_b32 v185, v237, v20\n"
"v_pk_fma_f32 v[184:185], v[184:185], v[204:205], v[206:207] op_sel_hi:[1,0,0]\n"
"v_cvt_pk_f16_f32 v20, v184, v185\n"
"v_lshlrev_b32 v186, 16, v21\n"
"s_add_u32 m0, s51, 122680\n"
"s_nop 0\n"
"buffer_load_dwordx4 v217, s[24:27], s50 offen offset:256 lds\n"
"s_add_u32 s50, s50, 32768\n"
"v_and_b32 v187, v237, v21\n"
"v_pk_fma_f32 v[186:187], v[186:187], v[204:205], v[206:207] op_sel_hi:[1,0,0]\n"
"v_cvt_pk_f16_f32 v21, v186, v187\n"
"v_lshlrev_b32 v188, 16, v22\n"
"v_and_b32 v189, v237, v22\n"
"v_pk_fma_f32 v[188:189], v[188:189], v[204:205], v[206:207] op_sel_hi:[1,0,0]\n"
"v_cvt_pk_f16_f32 v22, v188, v189\n"
"v_lshlrev_b32 v190, 16, v23\n"
"v_and_b32 v191, v237, v23\n"
"v_pk_fma_f32 v[190:191], v[190:191], v[204:205], v[206:207] op_sel_hi:[1,0,0]\n"
"v_cvt_pk_f16_f32 v23, v190, v191\n"
"v_lshlrev_b32 v176, 16, v24\n"
"v_and_b32 v177, v237, v24\n"
"v_pk_fma_f32 v[176:177], v[176:177], v[200:201], v[202:203] op_sel_hi:[1,0,0]\n"
"v_cvt_pk_f16_f32 v24, v176, v177\n"
"v_lshlrev_b32 v178, 16, v25\n"
"v_and_b32 v179, v237, v25\n"
"v_pk_fma_f32 v[178:179], v[178:179], v[200:201], v[202:203] op_sel_hi:[1,0,0]\n"
"v_cvt_pk_f16_f32 v25, v178, v179\n"
"v_lshlrev_b32 v180, 16, v26\n"
"v_and_b32 v181, v237, v26\n"
"s_add_u32 m0, s51, 115768\n"
"s_nop 0\n"
"buffer_load_dwordx4 v217, s[24:27], s50 offen offset:0 lds\n"
"v_pk_fma_f32 v[180:181], v[180:181], v[200:201], v[202:203] op_sel_hi:[1,0,0]\n"
"v_cvt_pk_f16_f32 v26, v180, v181\n"
"v_lshlrev_b32 v182, 16, v27\n"
"v_and_b32 v183, v237, v27\n"
"v_pk_fma_f32 v[182:183], v[182:183], v[200:201], v[202:203] op_sel_hi:[1,0,0]\n"
"v_cvt_pk_f16_f32 v27, v182, v183\n"
"v_lshlrev_b32 v184, 16, v28\n"
"v_and_b32 v185, v237, v28\n"
"v_pk_fma_f32 v[184:185], v[184:185], v[204:205], v[206:207] op_sel_hi:[1,0,0]\n"
"v_cvt_pk_f16_f32 v28, v184, v185\n"
"v_lshlrev_b32 v186, 16, v29\n"
"v_and_b32 v187, v237, v29\n"
"v_pk_fma_f32 v[186:187], v[186:187], v[204:205], v[206:207] op_sel_hi:[1,0,0]\n"
"v_cvt_pk_f16_f32 v29, v186, v187\n"
"v_lshlrev_b32 v188, 16, v30\n"
"v_and_b32 v189, v237, v30\n"
"v_pk_fma_f32 v[188:189], v[188:189], v[204:205], v[206:207] op_sel_hi:[1,0,0]\n"
"v_cvt_pk_f16_f32 v30, v188, v189\n"
"v_lshlrev_b32 v190, 16, v31\n"
"v_and_b32 v191, v237, v31\n"
"v_pk_fma_f32 v[190:191], v[190:191], v[204:205], v[206:207] op_sel_hi:[1,0,0]\n"
"s_add_u32 m0, s51, 123704\n"
"s_nop 0\n"
"buffer_load_dwordx4 v217, s[24:27], s50 offen offset:256 lds\n"
"s_add_u32 s50, s50, 425984\n"
"v_cvt_pk_f16_f32 v31, v190, v191\n"
"buffer_load_dwordx4 v[144:147], v225, s[36:39], s61 offen offset:0 sc1\n"
"buffer_load_dwordx4 v[148:151], v225, s[36:39], s61 offen offset:16 sc1\n"
"buffer_load_dwordx4 v[152:155], v225, s[36:39], s62 offen offset:0 sc1\n"
"buffer_load_dwordx4 v[156:159], v225, s[36:39], s62 offen offset:16 sc1\n"
"v_lshlrev_b32 v176, 16, v32\n"
"v_and_b32 v177, v237, v32\n"
"v_pk_fma_f32 v[176:177], v[176:177], v[200:201], v[202:203] op_sel_hi:[1,0,0]\n"
"v_cvt_pk_f16_f32 v32, v176, v177\n"
"v_lshlrev_b32 v178, 16, v33\n"
"v_and_b32 v179, v237, v33\n"
"v_pk_fma_f32 v[178:179], v[178:179], v[200:201], v[202:203] op_sel_hi:[1,0,0]\n"
"v_cvt_pk_f16_f32 v33, v178, v179\n"
"v_lshlrev_b32 v180, 16, v34\n"
"v_and_b32 v181, v237, v34\n"
"v_pk_fma_f32 v[180:181], v[180:181], v[200:201], v[202:203] op_sel_hi:[1,0,0]\n"
"v_cvt_pk_f16_f32 v34, v180, v181\n"
"v_lshlrev_b32 v182, 16, v35\n"
"v_and_b32 v183, v237, v35\n"
"v_pk_fma_f32 v[182:183], v[182:183], v[200:201], v[202:203] op_sel_hi:[1,0,0]\n"
"v_cvt_pk_f16_f32 v35, v182, v183\n"
"v_lshlrev_b32 v184, 16, v36\n"
"v_and_b32 v185, v237, v36\n"
"v_pk_fma_f32 v[184:185], v[184:185], v[204:205], v[206:207] op_sel_hi:[1,0,0]\n"
"v_cvt_pk_f16_f32 v36, v184, v185\n"
"v_lshlrev_b32 v186, 16, v37\n"
"s_mov_b32 m0, s51\n"
"s_nop 0\n"
"buffer_load_dwordx4 v217, s[24:27], s50 offen offset:0 lds\n"
"v_and_b32 v187, v237, v37\n"
"v_pk_fma_f32 v[186:187], v[186:187], v[204:205], v[206:207] op_sel_hi:[1,0,0]\n"
"v_cvt_pk_f16_f32 v37, v186, v187\n"
"v_lshlrev_b32 v188, 16, v38\n"
"v_and_b32 v189, v237, v38\n"
"v_pk_fma_f32 v[188:189], v[188:189], v[204:205], v[206:207] op_sel_hi:[1,0,0]\n"
"v_cvt_pk_f16_f32 v38, v188, v189\n"
"v_lshlrev_b32 v190, 16, v39\n"
"v_and_b32 v191, v237, v39\n"
"v_pk_fma_f32 v[190:191], v[190:191], v[204:205], v[206:207] op_sel_hi:[1,0,0]\n"
"v_cvt_pk_f16_f32 v39, v190, v191\n"
"v_lshlrev_b32 v176, 16, v40\n"
"v_and_b32 v177, v237, v40\n"
"v_pk_fma_f32 v[176:177], v[176:177], v[200:201], v[202:203] op_sel_hi:[1,0,0]\n"
"v_cvt_pk_f16_f32 v40, v176, v177\n"
"v_lshlrev_b32 v178, 16, v41\n"
"v_and_b32 v179, v237, v41\n"
"v_pk_fma_f32 v[178:179], v[178:179], v[200:201], v[202:203] op_sel_hi:[1,0,0]\n"
"v_cvt_pk_f16_f32 v41, v178, v179\n"
"v_lshlrev_b32 v180, 16, v42\n"
"v_and_b32 v181, v237, v42\n"
"s_add_u32 m0, s51, 7936\n"
"s_nop 0\n"
"buffer_load_dwordx4 v217, s[24:27], s50 offen offset:256 lds\n"
"s_add_u32 s50, s50, 32768\n"
"v_pk_fma_f32 v[180:181], v[180:181], v[200:201], v[202:203] op_sel_hi:[1,0,0]\n"
"v_cvt_pk_f16_f32 v42, v180, v181\n"
"v_lshlrev_b32 v182, 16, v43\n"
"v_and_b32 v183, v237, v43\n"
"v_pk_fma_f32 v[182:183], v[182:183], v[200:201], v[202:203] op_sel_hi:[1,0,0]\n"
"v_cvt_pk_f16_f32 v43, v182, v183\n"
"v_lshlrev_b32 v184, 16, v44\n"
"v_and_b32 v185, v237, v44\n"
"v_pk_fma_f32 v[184:185], v[184:185], v[204:205], v[206:207] op_sel_hi:[1,0,0]\n"
"v_cvt_pk_f16_f32 v44, v184, v185\n"
"v_lshlrev_b32 v186, 16, v45\n"
"v_and_b32 v187, v237, v45\n"
"v_pk_fma_f32 v[186:187], v[186:187], v[204:205], v[206:207] op_sel_hi:[1,0,0]\n"
"v_cvt_pk_f16_f32 v45, v186, v187\n"
"v_lshlrev_b32 v188, 16, v46\n"
"v_and_b32 v189, v237, v46\n"
"v_pk_fma_f32 v[188:189], v[188:189], v[204:205], v[206:207] op_sel_hi:[1,0,0]\n"
"v_cvt_pk_f16_f32 v46, v188, v189\n"
"v_lshlrev_b32 v190, 16, v47\n"
"v_and_b32 v191, v237, v47\n"
"v_pk_fma_f32 v[190:191], v[190:191], v[204:205], v[206:207] op_sel_hi:[1,0,0]\n"
"s_add_u32 m0, s51, 1024\n"
"s_nop 0\n"
"buffer_load_dwordx4 v217, s[24:27], s50 offen offset:0 lds\n"
"v_cvt_pk_f16_f32 v47, v190, v191\n"
"v_lshlrev_b32 v176, 16, v48\n"
"v_and_b32 v177, v237, v48\n"
"v_pk_fma_f32 v[176:177], v[176:177], v[200:201], v[202:203] op_sel_hi:[1,0,0]\n"
"v_cvt_pk_f16_f32 v48, v176, v177\n"
"v_lshlrev_b32 v178, 16, v49\n"
"v_and_b32 v179, v237, v49\n"
"v_pk_fma_f32 v[178:179], v[178:179], v[200:201], v[202:203] op_sel_hi:[1,0,0]\n"
"v_cvt_pk_f16_f32 v49, v178, v179\n"
"v_lshlrev_b32 v180, 16, v50\n"
"v_and_b32 v181, v237, v50\n"
"v_pk_fma_f32 v[180:181], v[180:181], v[200:201], v[202:203] op_sel_hi:[1,0,0]\n"
"v_cvt_pk_f16_f32 v50, v180, v181\n"
"v_lshlrev_b32 v182, 16, v51\n"
"v_and_b32 v183, v237, v51\n"
"v_pk_fma_f32 v[182:183], v[182:183], v[200:201], v[202:203] op_sel_hi:[1,0,0]\n"
"v_cvt_pk_f16_f32 v51, v182, v183\n"
"v_lshlrev_b32 v184, 16, v52\n"
"v_and_b32 v185, v237, v52\n"
"v_pk_fma_f32 v[184:185], v[184:185], v[204:205], v[206:207] op_sel_hi:[1,0,0]\n"
"v_cvt_pk_f16_f32 v52, v184, v185\n"
"v_lshlrev_b32 v186, 16, v53\n"
"s_add_u32 m0, s51, 8960\n"
"s_nop 0\n"
"buffer_load_dwordx4 v217, s[24:27], s50 offen offset:256 lds\n"
"s_add_u32 s50, s50, 32768\n"
"v_and_b32 v187, v237, v53\n"
"v_pk_fma_f32 v[186:187], v[186:187], v[204:205], v[206:207] op_sel_hi:[1,0,0]\n"
"v_cvt_pk_f16_f32 v53, v186, v187\n"
"v_lshlrev_b32 v188, 16, v54\n"
"v_and_b32 v189, v237, v54\n"
"v_pk_fma_f32 v[188:189], v[188:189], v[204:205], v[206:207] op_sel_hi:[1,0,0]\n"
"v_cvt_pk_f16_f32 v54, v188, v189\n"
"v_lshlrev_b32 v190, 16, v55\n"
"v_and_b32 v191, v237, v55\n"
"v_pk_fma_f32 v[190:191], v[190:191], v[204:205], v[206:207] op_sel_hi:[1,0,0]\n"
"v_cvt_pk_f16_f32 v55, v190, v191\n"
"v_lshlrev_b32 v176, 16, v56\n"
"v_and_b32 v177, v237, v56\n"
"v_pk_fma_f32 v[176:177], v[176:177], v[200:201], v[202:203] op_sel_hi:[1,0,0]\n"
"v_cvt_pk_f16_f32 v56, v176, v177\n"
"v_lshlrev_b32 v178, 16, v57\n"
"v_and_b32 v179, v237, v57\n"
"v_pk_fma_f32 v[178:179], v[178:179], v[200:201], v[202:203] op_sel_hi:[1,0,0]\n"
"v_cvt_pk_f16_f32 v57, v178, v179\n"
"v_lshlrev_b32 v180, 16, v58\n"
"v_and_b32 v181, v237, v58\n"
"s_add_u32 m0, s51, 16392\n"
"s_nop 0\n"
"buffer_load_dwordx4 v217, s[24:27], s50 offen offset:0 lds\n"
"v_pk_fma_f32 v[180:181], v[180:181], v[200:201], v[202:203] op_sel_hi:[1,0,0]\n"
"v_cvt_pk_f16_f32 v58, v180, v181\n"
"v_lshlrev_b32 v182, 16, v59\n"
"v_and_b32 v183, v237, v59\n"
"v_pk_fma_f32 v[182:183], v[182:183], v[200:201], v[202:203] op_sel_hi:[1,0,0]\n"
"v_cvt_pk_f16_f32 v59, v182, v183\n"
"v_lshlrev_b32 v184, 16, v60\n"
"v_and_b32 v185, v237, v60\n"
"v_pk_fma_f32 v[184:185], v[184:185], v[204:205], v[206:207] op_sel_hi:[1,0,0]\n"
"v_cvt_pk_f16_f32 v60, v184, v185\n"
"v_lshlrev_b32 v186, 16, v61\n"
"v_and_b32 v187, v237, v61\n"
"v_pk_fma_f32 v[186:187], v[186:187], v[204:205], v[206:207] op_sel_hi:[1,0,0]\n"
"v_cvt_pk_f16_f32 v61, v186, v187\n"
"v_lshlrev_b32 v188, 16, v62\n"
"v_and_b32 v189, v237, v62\n"
"v_pk_fma_f32 v[188:189], v[188:189], v[204:205], v[206:207] op_sel_hi:[1,0,0]\n"
"v_cvt_pk_f16_f32 v62, v188, v189\n"
"v_lshlrev_b32 v190, 16, v63\n"
"v_and_b32 v191, v237, v63\n"
"v_pk_fma_f32 v[190:191], v[190:191], v[204:205], v[206:207] op_sel_hi:[1,0,0]\n"
"s_add_u32 m0, s51, 24328\n"
"s_nop 0\n"
"buffer_load_dwordx4 v217, s[24:27], s50 offen offset:256 lds\n"
"s_add_u32 s50, s50, 32768\n"
"v_cvt_pk_f16_f32 v63, v190, v191\n"
"v_lshlrev_b32 v176, 16, v64\n"
"v_and_b32 v177, v237, v64\n"
"v_pk_fma_f32 v[176:177], v[176:177], v[200:201], v[202:203] op_sel_hi:[1,0,0]\n"
"v_cvt_pk_f16_f32 v64, v176, v177\n"
"v_lshlrev_b32 v178, 16, v65\n"
"v_and_b32 v179, v237, v65\n"
"v_pk_fma_f32 v[178:179], v[178:179], v[200:201], v[202:203] op_sel_hi:[1,0,0]\n"
"v_cvt_pk_f16_f32 v65, v178, v179\n"
"v_lshlrev_b32 v180, 16, v66\n"
"v_and_b32 v181, v237, v66\n"
"v_pk_fma_f32 v[180:181], v[180:181], v[200:201], v[202:203] op_sel_hi:[1,0,0]\n"
"v_cvt_pk_f16_f32 v66, v180, v181\n"
"v_lshlrev_b32 v182, 16, v67\n"
"v_and_b32 v183, v237, v67\n"
"v_pk_fma_f32 v[182:183], v[182:183], v[200:201], v[202:203] op_sel_hi:[1,0,0]\n"
"v_cvt_pk_f16_f32 v67, v182, v183\n"
"v_lshlrev_b32 v184, 16, v68\n"
"v_and_b32 v185, v237, v68\n"
"v_pk_fma_f32 v[184:185], v[184:185], v[204:205], v[206:207] op_sel_hi:[1,0,0]\n"
"v_cvt_pk_f16_f32 v68, v184, v185\n"
"v_lshlrev_b32 v186, 16, v69\n"
"s_add_u32 m0, s51, 17416\n"
"s_nop 0\n"
"buffer_load_dwordx4 v217, s[24:27], s50 offen offset:0 lds\n"
"v_and_b32 v187, v237, v69\n"
"v_pk_fma_f32 v[186:187], v[186:187], v[204:205], v[206:207] op_sel_hi:[1,0,0]\n"
"v_cvt_pk_f16_f32 v69, v186, v187\n"
"v_lshlrev_b32 v188, 16, v70\n"
"v_and_b32 v189, v237, v70\n"
"v_pk_fma_f32 v[188:189], v[188:189], v[204:205], v[206:207] op_sel_hi:[1,0,0]\n"
"v_cvt_pk_f16_f32 v70, v188, v189\n"
"v_lshlrev_b32 v190, 16, v71\n"
"v_and_b32 v191, v237, v71\n"
"v_pk_fma_f32 v[190:191], v[190:191], v[204:205], v[206:207] op_sel_hi:[1,0,0]\n"
"v_cvt_pk_f16_f32 v71, v190, v191\n"
"v_lshlrev_b32 v176, 16, v72\n"
"v_and_b32 v177, v237, v72\n"
"v_pk_fma_f32 v[176:177], v[176:177], v[200:201], v[202:203] op_sel_hi:[1,0,0]\n"
"v_cvt_pk_f16_f32 v72, v176, v177\n"
"v_lshlrev_b32 v178, 16, v73\n"
"v_and_b32 v179, v237, v73\n"
"v_pk_fma_f32 v[178:179], v[178:179], v[200:201], v[202:203] op_sel_hi:[1,0,0]\n"
"v_cvt_pk_f16_f32 v73, v178, v179\n"
"v_lshlrev_b32 v180, 16, v74\n"
"v_and_b32 v181, v237, v74\n"
"s_add_u32 m0, s51, 25352\n"
"s_nop 0\n"
"buffer_load_dwordx4 v217, s[24:27], s50 offen offset:256 lds\n"
"s_add_u32 s50, s50, 425984\n"
"v_pk_fma_f32 v[180:181], v[180:181], v[200:201], v[202:203] op_sel_hi:[1,0,0]\n"
"v_cvt_pk_f16_f32 v74, v180, v181\n"
"v_lshlrev_b32 v182, 16, v75\n"
"v_and_b32 v183, v237, v75\n"
"v_pk_fma_f32 v[182:183], v[182:183], v[200:201], v[202:203] op_sel_hi:[1,0,0]\n"
"v_cvt_pk_f16_f32 v75, v182, v183\n"
"v_lshlrev_b32 v184, 16, v76\n"
"v_and_b32 v185, v237, v76\n"
"v_pk_fma_f32 v[184:185], v[184:185], v[204:205], v[206:207] op_sel_hi:[1,0,0]\n"
"v_cvt_pk_f16_f32 v76, v184, v185\n"
"v_lshlrev_b32 v186, 16, v77\n"
"v_and_b32 v187, v237, v77\n"
"v_pk_fma_f32 v[186:187], v[186:187], v[204:205], v[206:207] op_sel_hi:[1,0,0]\n"
"v_cvt_pk_f16_f32 v77, v186, v187\n"
"v_lshlrev_b32 v188, 16, v78\n"
"v_and_b32 v189, v237, v78\n"
"v_pk_fma_f32 v[188:189], v[188:189], v[204:205], v[206:207] op_sel_hi:[1,0,0]\n"
"v_cvt_pk_f16_f32 v78, v188, v189\n"
"v_lshlrev_b32 v190, 16, v79\n"
"v_and_b32 v191, v237, v79\n"
"v_pk_fma_f32 v[190:191], v[190:191], v[204:205], v[206:207] op_sel_hi:[1,0,0]\n"
"s_add_u32 m0, s51, 32784\n"
"s_nop 0\n"
"buffer_load_dwordx4 v217, s[24:27], s50 offen offset:0 lds\n"
"v_cvt_pk_f16_f32 v79, v190, v191\n"
"v_lshlrev_b32 v176, 16, v80\n"
"v_and_b32 v177, v237, v80\n"
"v_pk_fma_f32 v[176:177], v[176:177], v[200:201], v[202:203] op_sel_hi:[1,0,0]\n"
"v_cvt_pk_f16_f32 v80, v176, v177\n"
"v_lshlrev_b32 v178, 16, v81\n"
"v_and_b32 v179, v237, v81\n"
"v_pk_fma_f32 v[178:179], v[178:179], v[200:201], v[202:203] op_sel_hi:[1,0,0]\n"
"v_cvt_pk_f16_f32 v81, v178, v179\n"
"v_lshlrev_b32 v180, 16, v82\n"
"v_and_b32 v181, v237, v82\n"
"v_pk_fma_f32 v[180:181], v[180:181], v[200:201], v[202:203] op_sel_hi:[1,0,0]\n"
"v_cvt_pk_f16_f32 v82, v180, v181\n"
"v_lshlrev_b32 v182, 16, v83\n"
"v_and_b32 v183, v237, v83\n"
"v_pk_fma_f32 v[182:183], v[182:183], v[200:201], v[202:203] op_sel_hi:[1,0,0]\n"
"v_cvt_pk_f16_f32 v83, v182, v183\n"
"v_lshlrev_b32 v184, 16, v84\n"
"v_and_b32 v185, v237, v84\n"
"v_pk_fma_f32 v[184:185], v[184:185], v[204:205], v[206:207] op_sel_hi:[1,0,0]\n"
"v_cvt_pk_f16_f32 v84, v184, v185\n"
"v_lshlrev_b32 v186, 16, v85\n"
"s_add_u32 m0, s51, 40720\n"
"s_nop 0\n"
"buffer_load_dwordx4 v217, s[24:27], s50 offen offset:256 lds\n"
"s_add_u32 s50, s50, 32768\n"
"v_and_b32 v187, v237, v85\n"
"v_pk_fma_f32 v[186:187], v[186:187], v[204:205], v[206:207] op_sel_hi:[1,0,0]\n"
"v_cvt_pk_f16_f32 v85, v186, v187\n"
"v_lshlrev_b32 v188, 16, v86\n"
"v_and_b32 v189, v237, v86\n"
"v_pk_fma_f32 v[188:189], v[188:189], v[204:205], v[206:207] op_sel_hi:[1,0,0]\n"
"v_cvt_pk_f16_f32 v86, v188, v189\n"
"v_lshlrev_b32 v190, 16, v87\n"
"v_and_b32 v191, v237, v87\n"
"v_pk_fma_f32 v[190:191], v[190:191], v[204:205], v[206:207] op_sel_hi:[1,0,0]\n"
"v_cvt_pk_f16_f32 v87, v190, v191\n"
"v_lshlrev_b32 v176, 16, v88\n"
"v_and_b32 v177, v237, v88\n"
"v_pk_fma_f32 v[176:177], v[176:177], v[200:201], v[202:203] op_sel_hi:[1,0,0]\n"
"v_cvt_pk_f16_f32 v88, v176, v177\n"
"v_lshlrev_b32 v178, 16, v89\n"
"v_and_b32 v179, v237, v89\n"
"v_pk_fma_f32 v[178:179], v[178:179], v[200:201], v[202:203] op_sel_hi:[1,0,0]\n"
"v_cvt_pk_f16_f32 v89, v178, v179\n"
"v_lshlrev_b32 v180, 16, v90\n"
"v_and_b32 v181, v237, v90\n"
"s_add_u32 m0, s51, 33808\n"
"s_nop 0\n"
"buffer_load_dwordx4 v217, s[24:27], s50 offen offset:0 lds\n"
"v_pk_fma_f32 v[180:181], v[180:181], v[200:201], v[202:203] op_sel_hi:[1,0,0]\n"
"v_cvt_pk_f16_f32 v90, v180, v181\n"
"v_lshlrev_b32 v182, 16, v91\n"
"v_and_b32 v183, v237, v91\n"
"v_pk_fma_f32 v[182:183], v[182:183], v[200:201], v[202:203] op_sel_hi:[1,0,0]\n"
"v_cvt_pk_f16_f32 v91, v182, v183\n"
"v_lshlrev_b32 v184, 16, v92\n"
"v_and_b32 v185, v237, v92\n"
"v_pk_fma_f32 v[184:185], v[184:185], v[204:205], v[206:207] op_sel_hi:[1,0,0]\n"
"v_cvt_pk_f16_f32 v92, v184, v185\n"
"v_lshlrev_b32 v186, 16, v93\n"
"v_and_b32 v187, v237, v93\n"
"v_pk_fma_f32 v[186:187], v[186:187], v[204:205], v[206:207] op_sel_hi:[1,0,0]\n"
"v_cvt_pk_f16_f32 v93, v186, v187\n"
"v_lshlrev_b32 v188, 16, v94\n"
"v_and_b32 v189, v237, v94\n"
"v_pk_fma_f32 v[188:189], v[188:189], v[204:205], v[206:207] op_sel_hi:[1,0,0]\n"
"v_cvt_pk_f16_f32 v94, v188, v189\n"
"v_lshlrev_b32 v190, 16, v95\n"
"v_and_b32 v191, v237, v95\n"
"v_pk_fma_f32 v[190:191], v[190:191], v[204:205], v[206:207] op_sel_hi:[1,0,0]\n"
"s_add_u32 m0, s51, 41744\n"
"s_nop 0\n"
"buffer_load_dwordx4 v217, s[24:27], s50 offen offset:256 lds\n"
"s_add_u32 s50, s50, 32768\n"
"v_cvt_pk_f16_f32 v95, v190, v191\n"
"v_lshlrev_b32 v176, 16, v96\n"
"v_and_b32 v177, v237, v96\n"
"v_pk_fma_f32 v[176:177], v[176:177], v[200:201], v[202:203] op_sel_hi:[1,0,0]\n"
"v_cvt_pk_f16_f32 v96, v176, v177\n"
"v_lshlrev_b32 v178, 16, v97\n"
"v_and_b32 v179, v237, v97\n"
"v_pk_fma_f32 v[178:179], v[178:179], v[200:201], v[202:203] op_sel_hi:[1,0,0]\n"
"v_cvt_pk_f16_f32 v97, v178, v179\n"
"v_lshlrev_b32 v180, 16, v98\n"
"v_and_b32 v181, v237, v98\n"
"v_pk_fma_f32 v[180:181], v[180:181], v[200:201], v[202:203] op_sel_hi:[1,0,0]\n"
"v_cvt_pk_f16_f32 v98, v180, v181\n"
"v_lshlrev_b32 v182, 16, v99\n"
"v_and_b32 v183, v237, v99\n"
"v_pk_fma_f32 v[182:183], v[182:183], v[200:201], v[202:203] op_sel_hi:[1,0,0]\n"
"v_cvt_pk_f16_f32 v99, v182, v183\n"
"v_lshlrev_b32 v184, 16, v100\n"
"v_and_b32 v185, v237, v100\n"
"v_pk_fma_f32 v[184:185], v[184:185], v[204:205], v[206:207] op_sel_hi:[1,0,0]\n"
"v_cvt_pk_f16_f32 v100, v184, v185\n"
"v_lshlrev_b32 v186, 16, v101\n"
"s_add_u32 m0, s51, 49176\n"
"s_nop 0\n"
"buffer_load_dwordx4 v217, s[24:27], s50 offen offset:0 lds\n"
"v_and_b32 v187, v237, v101\n"
"v_pk_fma_f32 v[186:187], v[186:187], v[204:205], v[206:207] op_sel_hi:[1,0,0]\n"
"v_cvt_pk_f16_f32 v101, v186, v187\n"
"v_lshlrev_b32 v188, 16, v102\n"
"v_and_b32 v189, v237, v102\n"
"v_pk_fma_f32 v[188:189], v[188:189], v[204:205], v[206:207] op_sel_hi:[1,0,0]\n"
"v_cvt_pk_f16_f32 v102, v188, v189\n"
"v_lshlrev_b32 v190, 16, v103\n"
"v_and_b32 v191, v237, v103\n"
"v_pk_fma_f32 v[190:191], v[190:191], v[204:205], v[206:207] op_sel_hi:[1,0,0]\n"
"v_cvt_pk_f16_f32 v103, v190, v191\n"
"v_lshlrev_b32 v176, 16, v104\n"
"v_and_b32 v177, v237, v104\n"
"v_pk_fma_f32 v[176:177], v[176:177], v[200:201], v[202:203] op_sel_hi:[1,0,0]\n"
"v_cvt_pk_f16_f32 v104, v176, v177\n"
"v_lshlrev_b32 v178, 16, v105\n"
"v_and_b32 v179, v237, v105\n"
"v_pk_fma_f32 v[178:179], v[178:179], v[200:201], v[202:203] op_sel_hi:[1,0,0]\n"
"v_cvt_pk_f16_f32 v105, v178, v179\n"
"v_lshlrev_b32 v180, 16, v106\n"
"v_and_b32 v181, v237, v106\n"
"s_add_u32 m0, s51, 57112\n"
"s_nop 0\n"
"buffer_load_dwordx4 v217, s[24:27], s50 offen offset:256 lds\n"
"s_add_u32 s50, s50, 32768\n"
"v_pk_fma_f32 v[180:181], v[180:181], v[200:201], v[202:203] op_sel_hi:[1,0,0]\n"
"v_cvt_pk_f16_f32 v106, v180, v181\n"
"v_lshlrev_b32 v182, 16, v107\n"
"v_and_b32 v183, v237, v107\n"
"v_pk_fma_f32 v[182:183], v[182:183], v[200:201], v[202:203] op_sel_hi:[1,0,0]\n"
"v_cvt_pk_f16_f32 v107, v182, v183\n"
"v_lshlrev_b32 v184, 16, v108\n"
"v_and_b32 v185, v237, v108\n"
"v_pk_fma_f32 v[184:185], v[184:185], v[204:205], v[206:207] op_sel_hi:[1,0,0]\n"
"v_cvt_pk_f16_f32 v108, v184, v185\n"
"v_lshlrev_b32 v186, 16, v109\n"
"v_and_b32 v187, v237, v109\n"
"v_pk_fma_f32 v[186:187], v[186:187], v[204:205], v[206:207] op_sel_hi:[1,0,0]\n"
"v_cvt_pk_f16_f32 v109, v186, v187\n"
"v_lshlrev_b32 v188, 16, v110\n"
"v_and_b32 v189, v237, v110\n"
"v_pk_fma_f32 v[188:189], v[188:189], v[204:205], v[206:207] op_sel_hi:[1,0,0]\n"
"v_cvt_pk_f16_f32 v110, v188, v189\n"
"v_lshlrev_b32 v190, 16, v111\n"
"v_and_b32 v191, v237, v111\n"
"v_pk_fma_f32 v[190:191], v[190:191], v[204:205], v[206:207] op_sel_hi:[1,0,0]\n"
"s_add_u32 m0, s51, 50200\n"
"s_nop 0\n"
"buffer_load_dwordx4 v217, s[24:27], s50 offen offset:0 lds\n"
"v_cvt_pk_f16_f32 v111, v190, v191\n"
"v_lshlrev_b32 v176, 16, v112\n"
"v_and_b32 v177, v237, v112\n"
"v_pk_fma_f32 v[176:177], v[176:177], v[200:201], v[202:203] op_sel_hi:[1,0,0]\n"
"v_cvt_pk_f16_f32 v112, v176, v177\n"
"v_lshlrev_b32 v178, 16, v113\n"
"v_and_b32 v179, v237, v113\n"
"v_pk_fma_f32 v[178:179], v[178:179], v[200:201], v[202:203] op_sel_hi:[1,0,0]\n"
"v_cvt_pk_f16_f32 v113, v178, v179\n"
"v_lshlrev_b32 v180, 16, v114\n"
"v_and_b32 v181, v237, v114\n"
"v_pk_fma_f32 v[180:181], v[180:181], v[200:201], v[202:203] op_sel_hi:[1,0,0]\n"
"v_cvt_pk_f16_f32 v114, v180, v181\n"
"v_lshlrev_b32 v182, 16, v115\n"
"v_and_b32 v183, v237, v115\n"
"v_pk_fma_f32 v[182:183], v[182:183], v[200:201], v[202:203] op_sel_hi:[1,0,0]\n"
"v_cvt_pk_f16_f32 v115, v182, v183\n"
"v_lshlrev_b32 v184, 16, v116\n"
"v_and_b32 v185, v237, v116\n"
"v_pk_fma_f32 v[184:185], v[184:185], v[204:205], v[206:207] op_sel_hi:[1,0,0]\n"
"v_cvt_pk_f16_f32 v116, v184, v185\n"
"v_lshlrev_b32 v186, 16, v117\n"
"s_add_u32 m0, s51, 58136\n"
"s_nop 0\n"
"buffer_load_dwordx4 v217, s[24:27], s50 offen offset:256 lds\n"
"s_add_u32 s50, s50, 425984\n"
"v_and_b32 v187, v237, v117\n"
"v_pk_fma_f32 v[186:187], v[186:187], v[204:205], v[206:207] op_sel_hi:[1,0,0]\n"
"v_cvt_pk_f16_f32 v117, v186, v187\n"
"v_lshlrev_b32 v188, 16, v118\n"
"v_and_b32 v189, v237, v118\n"
"v_pk_fma_f32 v[188:189], v[188:189], v[204:205], v[206:207] op_sel_hi:[1,0,0]\n"
"v_cvt_pk_f16_f32 v118, v188, v189\n"
"v_lshlrev_b32 v190, 16, v119\n"
"v_and_b32 v191, v237, v119\n"
"v_pk_fma_f32 v[190:191], v[190:191], v[204:205], v[206:207] op_sel_hi:[1,0,0]\n"
"v_cvt_pk_f16_f32 v119, v190, v191\n"
"v_lshlrev_b32 v176, 16, v120\n"
"v_and_b32 v177, v237, v120\n"
"v_pk_fma_f32 v[176:177], v[176:177], v[200:201], v[202:203] op_sel_hi:[1,0,0]\n"
"v_cvt_pk_f16_f32 v120, v176, v177\n"
"v_lshlrev_b32 v178, 16, v121\n"
"v_and_b32 v179, v237, v121\n"
"v_pk_fma_f32 v[178:179], v[178:179], v[200:201], v[202:203] op_sel_hi:[1,0,0]\n"
"v_cvt_pk_f16_f32 v121, v178, v179\n"
"v_lshlrev_b32 v180, 16, v122\n"
"v_and_b32 v181, v237, v122\n"
"v_pk_fma_f32 v[180:181], v[180:181], v[200:201], v[202:203] op_sel_hi:[1,0,0]\n"
"v_cvt_pk_f16_f32 v122, v180, v181\n"
"v_lshlrev_b32 v182, 16, v123\n"
"v_and_b32 v183, v237, v123\n"
"v_pk_fma_f32 v[182:183], v[182:183], v[200:201], v[202:203] op_sel_hi:[1,0,0]\n"
"v_cvt_pk_f16_f32 v123, v182, v183\n"
"v_lshlrev_b32 v184, 16, v124\n"
"v_and_b32 v185, v237, v124\n"
"v_pk_fma_f32 v[184:185], v[184:185], v[204:205], v[206:207] op_sel_hi:[1,0,0]\n"
"v_cvt_pk_f16_f32 v124, v184, v185\n"
"v_lshlrev_b32 v186, 16, v125\n"
"v_and_b32 v187, v237, v125\n"
"v_pk_fma_f32 v[186:187], v[186:187], v[204:205], v[206:207] op_sel_hi:[1,0,0]\n"
"v_cvt_pk_f16_f32 v125, v186, v187\n"
"v_lshlrev_b32 v188, 16, v126\n"
"v_and_b32 v189, v237, v126\n"
"v_pk_fma_f32 v[188:189], v[188:189], v[204:205], v[206:207] op_sel_hi:[1,0,0]\n"
"v_cvt_pk_f16_f32 v126, v188, v189\n"
"v_lshlrev_b32 v190, 16, v127\n"
"v_and_b32 v191, v237, v127\n"
"v_pk_fma_f32 v[190:191], v[190:191], v[204:205], v[206:207] op_sel_hi:[1,0,0]\n"
"v_cvt_pk_f16_f32 v127, v190, v191\n"
"v_lshlrev_b32 v176, 16, v128\n"
"v_and_b32 v177, v237, v128\n"
"v_pk_fma_f32 v[176:177], v[176:177], v[200:201], v[202:203] op_sel_hi:[1,0,0]\n"
"v_cvt_pk_f16_f32 v128, v176, v177\n"
"v_lshlrev_b32 v178, 16, v129\n"
"v_and_b32 v179, v237, v129\n"
"v_pk_fma_f32 v[178:179], v[178:179], v[200:201], v[202:203] op_sel_hi:[1,0,0]\n"
"v_cvt_pk_f16_f32 v129, v178, v179\n"
"v_lshlrev_b32 v180, 16, v130\n"
"v_and_b32 v181, v237, v130\n"
"v_pk_fma_f32 v[180:181], v[180:181], v[200:201], v[202:203] op_sel_hi:[1,0,0]\n"
"v_cvt_pk_f16_f32 v130, v180, v181\n"
"v_lshlrev_b32 v182, 16, v131\n"
"v_and_b32 v183, v237, v131\n"
"v_pk_fma_f32 v[182:183], v[182:183], v[200:201], v[202:203] op_sel_hi:[1,0,0]\n"
"v_cvt_pk_f16_f32 v131, v182, v183\n"
"v_lshlrev_b32 v184, 16, v132\n"
"v_and_b32 v185, v237, v132\n"
"v_pk_fma_f32 v[184:185], v[184:185], v[204:205], v[206:207] op_sel_hi:[1,0,0]\n"
"v_cvt_pk_f16_f32 v132, v184, v185\n"
"v_lshlrev_b32 v186, 16, v133\n"
"v_and_b32 v187, v237, v133\n"
"v_pk_fma_f32 v[186:187], v[186:187], v[204:205], v[206:207] op_sel_hi:[1,0,0]\n"
"v_cvt_pk_f16_f32 v133, v186, v187\n"
"v_lshlrev_b32 v188, 16, v134\n"
"v_and_b32 v189, v237, v134\n"
"v_pk_fma_f32 v[188:189], v[188:189], v[204:205], v[206:207] op_sel_hi:[1,0,0]\n"
"v_cvt_pk_f16_f32 v134, v188, v189\n"
"v_lshlrev_b32 v190, 16, v135\n"
"v_and_b32 v191, v237, v135\n"
"v_pk_fma_f32 v[190:191], v[190:191], v[204:205], v[206:207] op_sel_hi:[1,0,0]\n"
"v_cvt_pk_f16_f32 v135, v190, v191\n"
"v_lshlrev_b32 v176, 16, v136\n"
"v_and_b32 v177, v237, v136\n"
"v_pk_fma_f32 v[176:177], v[176:177], v[200:201], v[202:203] op_sel_hi:[1,0,0]\n"
"v_cvt_pk_f16_f32 v136, v176, v177\n"
"v_lshlrev_b32 v178, 16, v137\n"
"v_and_b32 v179, v237, v137\n"
"v_pk_fma_f32 v[178:179], v[178:179], v[200:201], v[202:203] op_sel_hi:[1,0,0]\n"
"v_cvt_pk_f16_f32 v137, v178, v179\n"
"v_lshlrev_b32 v180, 16, v138\n"
"v_and_b32 v181, v237, v138\n"
"v_pk_fma_f32 v[180:181], v[180:181], v[200:201], v[202:203] op_sel_hi:[1,0,0]\n"
"v_cvt_pk_f16_f32 v138, v180, v181\n"
"v_lshlrev_b32 v182, 16, v139\n"
"v_and_b32 v183, v237, v139\n"
"v_pk_fma_f32 v[182:183], v[182:183], v[200:201], v[202:203] op_sel_hi:[1,0,0]\n"
"v_cvt_pk_f16_f32 v139, v182, v183\n"
"v_lshlrev_b32 v184, 16, v140\n"
"v_and_b32 v185, v237, v140\n"
"v_pk_fma_f32 v[184:185], v[184:185], v[204:205], v[206:207] op_sel_hi:[1,0,0]\n"
"v_cvt_pk_f16_f32 v140, v184, v185\n"
"v_lshlrev_b32 v186, 16, v141\n"
"v_and_b32 v187, v237, v141\n"
"v_pk_fma_f32 v[186:187], v[186:187], v[204:205], v[206:207] op_sel_hi:[1,0,0]\n"
"v_cvt_pk_f16_f32 v141, v186, v187\n"
"v_lshlrev_b32 v188, 16, v142\n"
"v_and_b32 v189, v237, v142\n"
"v_pk_fma_f32 v[188:189], v[188:189], v[204:205], v[206:207] op_sel_hi:[1,0,0]\n"
"v_cvt_pk_f16_f32 v142, v188, v189\n"
"v_lshlrev_b32 v190, 16, v143\n"
"v_and_b32 v191, v237, v143\n"
"v_pk_fma_f32 v[190:191], v[190:191], v[204:205], v[206:207] op_sel_hi:[1,0,0]\n"
"v_cvt_pk_f16_f32 v143, v190, v191\n"
"s_mov_b32 s19, 0x80000\n"
"s_waitcnt vmcnt(16)\n"
"AT_gchk:\n"
"v_xor_b32 v176, s17, v145\n"
"v_xor_b32 v177, s17, v147\n"
"v_xor_b32 v178, s17, v149\n"
"v_xor_b32 v179, s17, v151\n"
"v_xor_b32 v180, s17, v153\n"
"v_xor_b32 v181, s17, v155\n"
"v_xor_b32 v182, s17, v157\n"
"v_xor_b32 v183, s17, v159\n"
"v_or3_b32 v176, v176, v177, v178\n"
"v_or3_b32 v179, v179, v180, v181\n"
"v_or3_b32 v176, v176, v182, v183\n"
"v_or_b32 v176, v176, v179\n"
"v_cmp_ne_u32 s[56:57], 0, v176\n"
"v_cmp_gt_u32 vcc, 144, v234\n"
"s_nop 1\n"
"s_and_b64 vcc, vcc, s[56:57]\n"
"s_cbranch_vccz AT_gok\n"
"s_sub_u32 s19, s19, 1\n"
"s_cmp_eq_u32 s19, 0\n"
"s_cbranch_scc1 AT_gok\n"
"s_sleep 2\n"
"buffer_load_dwordx4 v[144:147], v225, s[36:39], s61 offen offset:0 sc1\n"
"buffer_load_dwordx4 v[148:151], v225, s[36:39], s61 offen offset:16 sc1\n"
"buffer_load_dwordx4 v[152:155], v225, s[36:39], s62 offen offset:0 sc1\n"
"buffer_load_dwordx4 v[156:159], v225, s[36:39], s62 offen offset:16 sc1\n"
"s_waitcnt vmcnt(0)\n"
"s_branch AT_gchk\n"
"AT_gok:\n"
"v_mov_b32 v236, 0x3e0293ee\n"
"v_mul_f32 v176, v236, v144\n"
"v_fma_f32 v180, v152, v236, v236\n"
"v_mul_f32 v177, v236, v146\n"
"v_fma_f32 v181, v154, v236, v236\n"
"v_mul_f32 v178, v236, v148\n"
"v_fma_f32 v182, v156, v236, v236\n"
"v_mul_f32 v179, v236, v150\n"
"v_fma_f32 v183, v158, v236, v236\n"
"ds_write_b128 v216, v[180:183]\n"
"ds_write_b128 v216, v[176:179] offset:128\n"
"s_waitcnt vmcnt(20) lgkmcnt(0)\n"
"s_barrier\n"
"ds_read_b128 v[144:147], v224 offset:0\n"
"ds_read_b128 v[148:151], v224 offset:64\n"
"ds_read_b128 v[152:155], v224 offset:128\n"
"ds_read_b128 v[156:159], v224 offset:192\n"
"ds_read_b128 v[160:163], v224 offset:256\n"
"ds_read_b128 v[164:167], v224 offset:320\n"
"ds_read_b128 v[168:171], v224 offset:384\n"
"ds_read_b128 v[172:175], v224 offset:448\n"
"ds_read_b64_tr_b16 a[160:161], v219 offset:32784\n"
"s_waitcnt lgkmcnt(6)\n"
"v_fma_mix_f32 v176, v0, v144, v152 op_sel:[0,0,0] op_sel_hi:[1,0,0]\n"
"v_fma_mix_f32 v177, v0, v145, v153 op_sel:[1,0,0] op_sel_hi:[1,0,0]\n"
"v_fma_mix_f32 v178, v1, v146, v154 op_sel:[0,0,0] op_sel_hi:[1,0,0]\n"
"v_fma_mix_f32 v179, v1, v147, v155 op_sel:[1,0,0] op_sel_hi:[1,0,0]\n"
"ds_read_b64_tr_b16 a[162:163], v219 offset:33808\n"
"s_waitcnt lgkmcnt(6)\n"
"v_fma_mix_f32 v180, v2, v148, v156 op_sel:[0,0,0] op_sel_hi:[1,0,0]\n"
"v_fma_mix_f32 v181, v2, v149, v157 op_sel:[1,0,0] op_sel_hi:[1,0,0]\n"
"v_fma_mix_f32 v182, v3, v150, v158 op_sel:[0,0,0] op_sel_hi:[1,0,0]\n"
"v_fma_mix_f32 v183, v3, v151, v159 op_sel:[1,0,0] op_sel_hi:[1,0,0]\n"
"ds_read_b64_tr_b16 a[164:165], v219 offset:36880\n"
"v_cvt_pk_bf16_f32 v0, v176, v177\n"
"v_cvt_pk_bf16_f32 v1, v178, v179\n"
"v_cvt_pk_bf16_f32 v2, v180, v181\n"
"v_cvt_pk_bf16_f32 v3, v182, v183\n"
"ds_read_b64_tr_b16 a[166:167], v219 offset:37904\n"
"v_fma_mix_f32 v184, v4, v144, v152 op_sel:[0,0,0] op_sel_hi:[1,0,0]\n"
"v_fma_mix_f32 v185, v4, v145, v153 op_sel:[1,0,0] op_sel_hi:[1,0,0]\n"
"v_fma_mix_f32 v186, v5, v146, v154 op_sel:[0,0,0] op_sel_hi:[1,0,0]\n"
"v_fma_mix_f32 v187, v5, v147, v155 op_sel:[1,0,0] op_sel_hi:[1,0,0]\n"
"ds_read_b64_tr_b16 a[168:169], v219 offset:32792\n"
"v_fma_mix_f32 v188, v6, v148, v156 op_sel:[0,0,0] op_sel_hi:[1,0,0]\n"
"v_fma_mix_f32 v189, v6, v149, v157 op_sel:[1,0,0] op_sel_hi:[1,0,0]\n"
"v_fma_mix_f32 v190, v7, v150, v158 op_sel:[0,0,0] op_sel_hi:[1,0,0]\n"
"v_fma_mix_f32 v191, v7, v151, v159 op_sel:[1,0,0] op_sel_hi:[1,0,0]\n"
"ds_read_b64_tr_b16 a[170:171], v219 offset:33816\n"
"ds_read_b128 v[144:147], v224 offset:512\n"
"ds_read_b128 v[148:151], v224 offset:576\n"
"ds_read_b128 v[152:155], v224 offset:640\n"
"ds_read_b128 v[156:159], v224 offset:704\n"
"ds_read_b64_tr_b16 a[172:173], v219 offset:36888\n"
"v_cvt_pk_bf16_f32 v4, v184, v185\n"
"v_cvt_pk_bf16_f32 v5, v186, v187\n"
"v_cvt_pk_bf16_f32 v6, v188, v189\n"
"v_cvt_pk_bf16_f32 v7, v190, v191\n"
"ds_read_b64_tr_b16 a[174:175], v219 offset:37912\n"
"s_waitcnt lgkmcnt(13)\n"
"v_fma_mix_f32 v176, v8, v160, v168 op_sel:[0,0,0] op_sel_hi:[1,0,0]\n"
"v_fma_mix_f32 v177, v8, v161, v169 op_sel:[1,0,0] op_sel_hi:[1,0,0]\n"
"v_fma_mix_f32 v178, v9, v162, v170 op_sel:[0,0,0] op_sel_hi:[1,0,0]\n"
"v_fma_mix_f32 v179, v9, v163, v171 op_sel:[1,0,0] op_sel_hi:[1,0,0]\n"
"ds_read_b64_tr_b16 a[176:177], v219 offset:33040\n"
"s_waitcnt lgkmcnt(13)\n"
"v_fma_mix_f32 v180, v10, v164, v172 op_sel:[0,0,0] op_sel_hi:[1,0,0]\n"
"v_fma_mix_f32 v181, v10, v165, v173 op_sel:[1,0,0] op_sel_hi:[1,0,0]\n"
"v_fma_mix_f32 v182, v11, v166, v174 op_sel:[0,0,0] op_sel_hi:[1,0,0]\n"
"v_fma_mix_f32 v183, v11, v167, v175 op_sel:[1,0,0] op_sel_hi:[1,0,0]\n"
"ds_read_b64_tr_b16 a[178:179], v219 offset:34064\n"
"v_cvt_pk_bf16_f32 v8, v176, v177\n"
"v_cvt_pk_bf16_f32 v9, v178, v179\n"
"v_cvt_pk_bf16_f32 v10, v180, v181\n"
"v_cvt_pk_bf16_f32 v11, v182, v183\n"
"ds_read_b64_tr_b16 a[180:181], v219 offset:37136\n"
"v_fma_mix_f32 v184, v12, v160, v168 op_sel:[0,0,0] op_sel_hi:[1,0,0]\n"
"v_fma_mix_f32 v185, v12, v161, v169 op_sel:[1,0,0] op_sel_hi:[1,0,0]\n"
"v_fma_mix_f32 v186, v13, v162, v170 op_sel:[0,0,0] op_sel_hi:[1,0,0]\n"
"v_fma_mix_f32 v187, v13, v163, v171 op_sel:[1,0,0] op_sel_hi:[1,0,0]\n"
"ds_read_b64_tr_b16 a[182:183], v219 offset:38160\n"
"v_fma_mix_f32 v188, v14, v164, v172 op_sel:[0,0,0] op_sel_hi:[1,0,0]\n"
"v_fma_mix_f32 v189, v14, v165, v173 op_sel:[1,0,0] op_sel_hi:[1,0,0]\n"
"v_fma_mix_f32 v190, v15, v166, v174 op_sel:[0,0,0] op_sel_hi:[1,0,0]\n"
"v_fma_mix_f32 v191, v15, v167, v175 op_sel:[1,0,0] op_sel_hi:[1,0,0]\n"
"ds_read_b64_tr_b16 a[184:185], v219 offset:33048\n"
"ds_read_b128 v[160:163], v224 offset:768\n"
"ds_read_b128 v[164:167], v224 offset:832\n"
"ds_read_b128 v[168:171], v224 offset:896\n"
"ds_read_b128 v[172:175], v224 offset:960\n"
"ds_read_b64_tr_b16 a[186:187], v219 offset:34072\n"
"v_cvt_pk_bf16_f32 v12, v184, v185\n"
"v_cvt_pk_bf16_f32 v13, v186, v187\n"
"v_cvt_pk_bf16_f32 v14, v188, v189\n"
"v_cvt_pk_bf16_f32 v15, v190, v191\n"
"ds_read_b64_tr_b16 a[188:189], v219 offset:37144\n"
"s_waitcnt lgkmcnt(14)\n"
"v_fma_mix_f32 v176, v16, v144, v152 op_sel:[0,0,0] op_sel_hi:[1,0,0]\n"
"v_fma_mix_f32 v177, v16, v145, v153 op_sel:[1,0,0] op_sel_hi:[1,0,0]\n"
"v_fma_mix_f32 v178, v17, v146, v154 op_sel:[0,0,0] op_sel_hi:[1,0,0]\n"
"v_fma_mix_f32 v179, v17, v147, v155 op_sel:[1,0,0] op_sel_hi:[1,0,0]\n"
"ds_read_b64_tr_b16 a[190:191], v219 offset:38168\n"
"s_waitcnt lgkmcnt(14)\n"
"v_fma_mix_f32 v180, v18, v148, v156 op_sel:[0,0,0] op_sel_hi:[1,0,0]\n"
"v_fma_mix_f32 v181, v18, v149, v157 op_sel:[1,0,0] op_sel_hi:[1,0,0]\n"
"v_fma_mix_f32 v182, v19, v150, v158 op_sel:[0,0,0] op_sel_hi:[1,0,0]\n"
"v_fma_mix_f32 v183, v19, v151, v159 op_sel:[1,0,0] op_sel_hi:[1,0,0]\n"
"ds_read_b64_tr_b16 a[224:225], v219 offset:33296\n"
"v_cvt_pk_bf16_f32 v16, v176, v177\n"
"v_cvt_pk_bf16_f32 v17, v178, v179\n"
"v_cvt_pk_bf16_f32 v18, v180, v181\n"
"v_cvt_pk_bf16_f32 v19, v182, v183\n"
"ds_read_b64_tr_b16 a[226:227], v219 offset:34320\n"
"v_fma_mix_f32 v184, v20, v144, v152 op_sel:[0,0,0] op_sel_hi:[1,0,0]\n"
"v_fma_mix_f32 v185, v20, v145, v153 op_sel:[1,0,0] op_sel_hi:[1,0,0]\n"
"v_fma_mix_f32 v186, v21, v146, v154 op_sel:[0,0,0] op_sel_hi:[1,0,0]\n"
"v_fma_mix_f32 v187, v21, v147, v155 op_sel:[1,0,0] op_sel_hi:[1,0,0]\n"
"ds_read_b64_tr_b16 a[228:229], v219 offset:37392\n"
"v_fma_mix_f32 v188, v22, v148, v156 op_sel:[0,0,0] op_sel_hi:[1,0,0]\n"
"v_fma_mix_f32 v189, v22, v149, v157 op_sel:[1,0,0] op_sel_hi:[1,0,0]\n"
"v_fma_mix_f32 v190, v23, v150, v158 op_sel:[0,0,0] op_sel_hi:[1,0,0]\n"
"v_fma_mix_f32 v191, v23, v151, v159 op_sel:[1,0,0] op_sel_hi:[1,0,0]\n"
"ds_read_b64_tr_b16 a[230:231], v219 offset:38416\n"
"ds_read_b128 v[144:147], v224 offset:1024\n"
"ds_read_b128 v[148:151], v224 offset:1088\n"
"ds_read_b128 v[152:155], v224 offset:1152\n"
"ds_read_b128 v[156:159], v224 offset:1216\n"
"v_mfma_f32_16x16x32_bf16 a[0:3], a[160:163], v[0:3], 0\n"
"ds_read_b64_tr_b16 a[232:233], v219 offset:33304\n"
"v_cvt_pk_bf16_f32 v20, v184, v185\n"
"v_mfma_f32_16x16x32_bf16 a[0:3], a[164:167], v[8:11], a[0:3]\n"
"ds_read_b64_tr_b16 a[234:235], v219 offset:34328\n"
"v_cvt_pk_bf16_f32 v21, v186, v187\n"
"v_mfma_f32_16x16x32_bf16 a[4:7], a[160:163], v[4:7], 0\n"
"s_waitcnt vmcnt(8)\n"
"s_barrier\n"
"ds_read_b64_tr_b16 a[236:237], v219 offset:37400\n"
"v_cvt_pk_bf16_f32 v22, v188, v189\n"
"v_mfma_f32_16x16x32_bf16 a[4:7], a[164:167], v[12:15], a[4:7]\n"
"ds_read_b64_tr_b16 a[238:239], v219 offset:38424\n"
"v_cvt_pk_bf16_f32 v23, v190, v191\n"
"v_mfma_f32_16x16x32_bf16 a[8:11], a[168:171], v[0:3], 0\n"
"s_waitcnt lgkmcnt(14)\n"
"s_add_u32 m0, s51, 65568\n"
"ds_read_b64_tr_b16 a[240:241], v219 offset:33552\n"
"v_fma_mix_f32 v176, v24, v160, v168 op_sel:[0,0,0] op_sel_hi:[1,0,0]\n"
"v_mfma_f32_16x16x32_bf16 a[8:11], a[172:175], v[8:11], a[8:11]\n"
"ds_read_b64_tr_b16 a[242:243], v219 offset:34576\n"
"buffer_load_dwordx4 v217, s[24:27], s50 offen offset:0 lds\n"
"v_mfma_f32_16x16x32_bf16 a[12:15], a[168:171], v[4:7], 0\n"
"ds_read_b64_tr_b16 a[244:245], v219 offset:37648\n"
"v_fma_mix_f32 v177, v24, v161, v169 op_sel:[1,0,0] op_sel_hi:[1,0,0]\n"
"v_mfma_f32_16x16x32_bf16 a[12:15], a[172:175], v[12:15], a[12:15]\n"
"ds_read_b64_tr_b16 a[246:247], v219 offset:38672\n"
"v_fma_mix_f32 v178, v25, v162, v170 op_sel:[0,0,0] op_sel_hi:[1,0,0]\n"
"v_mfma_f32_16x16x32_bf16 a[16:19], a[176:179], v[0:3], 0\n"
"ds_read_b64_tr_b16 a[248:249], v219 offset:33560\n"
"v_fma_mix_f32 v179, v25, v163, v171 op_sel:[1,0,0] op_sel_hi:[1,0,0]\n"
"v_mfma_f32_16x16x32_bf16 a[16:19], a[180:183], v[8:11], a[16:19]\n"
"ds_read_b64_tr_b16 a[250:251], v219 offset:34584\n"
"v_fma_mix_f32 v180, v26, v164, v172 op_sel:[0,0,0] op_sel_hi:[1,0,0]\n"
"v_mfma_f32_16x16x32_bf16 a[20:23], a[176:179], v[4:7], 0\n"
"s_add_u32 m0, s51, 73504\n"
"ds_read_b64_tr_b16 a[252:253], v219 offset:37656\n"
"v_fma_mix_f32 v181, v26, v165, v173 op_sel:[1,0,0] op_sel_hi:[1,0,0]\n"
"v_mfma_f32_16x16x32_bf16 a[20:23], a[180:183], v[12:15], a[20:23]\n"
"ds_read_b64_tr_b16 a[254:255], v219 offset:38680\n"
"buffer_load_dwordx4 v217, s[24:27], s50 offen offset:256 lds\n"
"v_mfma_f32_16x16x32_bf16 a[24:27], a[184:187], v[0:3], 0\n"
"s_waitcnt lgkmcnt(11)\n"
"s_add_u32 s50, s50, 32768\n"
"ds_read_b64_tr_b16 a[160:161], v219 offset:40976\n"
"v_fma_mix_f32 v182, v27, v166, v174 op_sel:[0,0,0] op_sel_hi:[1,0,0]\n"
"v_mfma_f32_16x16x32_bf16 a[24:27], a[188:191], v[8:11], a[24:27]\n"
"ds_read_b64_tr_b16 a[162:163], v219 offset:42000\n"
"v_fma_mix_f32 v183, v27, v167, v175 op_sel:[1,0,0] op_sel_hi:[1,0,0]\n"
"v_mfma_f32_16x16x32_bf16 a[28:31], a[184:187], v[4:7], 0\n"
"ds_read_b64_tr_b16 a[164:165], v219 offset:45072\n"
"v_cvt_pk_bf16_f32 v24, v176, v177\n"
"v_mfma_f32_16x16x32_bf16 a[28:31], a[188:191], v[12:15], a[28:31]\n"
"ds_read_b64_tr_b16 a[166:167], v219 offset:46096\n"
"v_cvt_pk_bf16_f32 v25, v178, v179\n"
"v_mfma_f32_16x16x32_bf16 a[32:35], a[224:227], v[0:3], 0\n"
"s_add_u32 m0, s51, 66592\n"
"ds_read_b64_tr_b16 a[168:169], v219 offset:40984\n"
"v_cvt_pk_bf16_f32 v26, v180, v181\n"
"v_mfma_f32_16x16x32_bf16 a[32:35], a[228:231], v[8:11], a[32:35]\n"
"ds_read_b64_tr_b16 a[170:171], v219 offset:42008\n"
"buffer_load_dwordx4 v217, s[24:27], s50 offen offset:0 lds\n"
"v_mfma_f32_16x16x32_bf16 a[36:39], a[224:227], v[4:7], 0\n"
"ds_read_b64_tr_b16 a[172:173], v219 offset:45080\n"
"v_cvt_pk_bf16_f32 v27, v182, v183\n"
"v_mfma_f32_16x16x32_bf16 a[36:39], a[228:231], v[12:15], a[36:39]\n"
"s_waitcnt lgkmcnt(11)\n"
"ds_read_b64_tr_b16 a[174:175], v219 offset:46104\n"
"v_fma_mix_f32 v184, v28, v160, v168 op_sel:[0,0,0] op_sel_hi:[1,0,0]\n"
"v_mfma_f32_16x16x32_bf16 a[40:43], a[232:235], v[0:3], 0\n"
"ds_read_b64_tr_b16 a[176:177], v219 offset:41232\n"
"v_fma_mix_f32 v185, v28, v161, v169 op_sel:[1,0,0] op_sel_hi:[1,0,0]\n"
"v_mfma_f32_16x16x32_bf16 a[40:43], a[236:239], v[8:11], a[40:43]\n"
"ds_read_b64_tr_b16 a[178:179], v219 offset:42256\n"
"v_fma_mix_f32 v186, v29, v162, v170 op_sel:[0,0,0] op_sel_hi:[1,0,0]\n"
"v_mfma_f32_16x16x32_bf16 a[44:47], a[232:235], v[4:7], 0\n"
"s_add_u32 m0, s51, 74528\n"
"ds_read_b64_tr_b16 a[180:181], v219 offset:45328\n"
"v_fma_mix_f32 v187, v29, v163, v171 op_sel:[1,0,0] op_sel_hi:[1,0,0]\n"
"v_mfma_f32_16x16x32_bf16 a[44:47], a[236:239], v[12:15], a[44:47]\n"
"ds_read_b64_tr_b16 a[182:183], v219 offset:46352\n"
"buffer_load_dwordx4 v217, s[24:27], s50 offen offset:256 lds\n"
"v_mfma_f32_16x16x32_bf16 a[48:51], a[240:243], v[0:3], 0\n"
"s_add_u32 s50, s50, 32768\n"
"ds_read_b64_tr_b16 a[184:185], v219 offset:41240\n"
"v_fma_mix_f32 v188, v30, v164, v172 op_sel:[0,0,0] op_sel_hi:[1,0,0]\n"
"v_mfma_f32_16x16x32_bf16 a[48:51], a[244:247], v[8:11], a[48:51]\n"
"ds_read_b64_tr_b16 a[186:187], v219 offset:42264\n"
"v_fma_mix_f32 v189, v30, v165, v173 op_sel:[1,0,0] op_sel_hi:[1,0,0]\n"
"v_mfma_f32_16x16x32_bf16 a[52:55], a[240:243], v[4:7], 0\n"
"ds_read_b64_tr_b16 a[188:189], v219 offset:45336\n"
"v_fma_mix_f32 v190, v31, v166, v174 op_sel:[0,0,0] op_sel_hi:[1,0,0]\n"
"v_mfma_f32_16x16x32_bf16 a[52:55], a[244:247], v[12:15], a[52:55]\n"
"s_waitcnt lgkmcnt(11)\n"
"ds_read_b64_tr_b16 a[190:191], v219 offset:46360\n"
"v_fma_mix_f32 v191, v31, v167, v175 op_sel:[1,0,0] op_sel_hi:[1,0,0]\n"
"v_mfma_f32_16x16x32_bf16 a[56:59], a[248:251], v[0:3], 0\n"
"s_add_u32 m0, s51, 81960\n"
"ds_read_b64_tr_b16 a[224:225], v219 offset:41488\n"
"ds_read_b128 v[160:163], v224 offset:1280\n"
"v_mfma_f32_16x16x32_bf16 a[56:59], a[252:255], v[8:11], a[56:59]\n"
"ds_read_b64_tr_b16 a[226:227], v219 offset:42512\n"
"buffer_load_dwordx4 v217, s[24:27], s50 offen offset:0 lds\n"
"v_mfma_f32_16x16x32_bf16 a[60:63], a[248:251], v[4:7], 0\n"
"ds_read_b64_tr_b16 a[228:229], v219 offset:45584\n"
"ds_read_b128 v[164:167], v224 offset:1344\n"
"v_mfma_f32_16x16x32_bf16 a[60:63], a[252:255], v[12:15], a[60:63]\n"
"ds_read_b64_tr_b16 a[230:231], v219 offset:46608\n"
"ds_read_b128 v[168:171], v224 offset:1408\n"
"v_mfma_f32_16x16x32_bf16 a[64:67], a[160:163], v[0:3], 0\n"
"ds_read_b64_tr_b16 a[232:233], v219 offset:41496\n"
"ds_read_b128 v[172:175], v224 offset:1472\n"
"v_mfma_f32_16x16x32_bf16 a[64:67], a[164:167], v[8:11], a[64:67]\n"
"ds_read_b64_tr_b16 a[234:235], v219 offset:42520\n"
"v_cvt_pk_bf16_f32 v28, v184, v185\n"
"v_mfma_f32_16x16x32_bf16 a[68:71], a[160:163], v[4:7], 0\n"
"s_add_u32 m0, s51, 89896\n"
"ds_read_b64_tr_b16 a[236:237], v219 offset:45592\n"
"v_cvt_pk_bf16_f32 v29, v186, v187\n"
"v_mfma_f32_16x16x32_bf16 a[68:71], a[164:167], v[12:15], a[68:71]\n"
"s_waitcnt lgkmcnt(14)\n"
"ds_read_b64_tr_b16 a[238:239], v219 offset:46616\n"
"buffer_load_dwordx4 v217, s[24:27], s50 offen offset:256 lds\n"
"v_mfma_f32_16x16x32_bf16 a[72:75], a[168:171], v[0:3], 0\n"
"s_add_u32 s50, s50, 32768\n"
"ds_read_b64_tr_b16 a[240:241], v219 offset:41744\n"
"v_cvt_pk_bf16_f32 v30, v188, v189\n"
"v_mfma_f32_16x16x32_bf16 a[72:75], a[172:175], v[8:11], a[72:75]\n"
"ds_read_b64_tr_b16 a[242:243], v219 offset:42768\n"
"v_cvt_pk_bf16_f32 v31, v190, v191\n"
"v_mfma_f32_16x16x32_bf16 a[76:79], a[168:171], v[4:7], 0\n"
"ds_read_b64_tr_b16 a[244:245], v219 offset:45840\n"
"v_fma_mix_f32 v176, v32, v144, v152 op_sel:[0,0,0] op_sel_hi:[1,0,0]\n"
"v_mfma_f32_16x16x32_bf16 a[76:79], a[172:175], v[12:15], a[76:79]\n"
"ds_read_b64_tr_b16 a[246:247], v219 offset:46864\n"
"v_fma_mix_f32 v177, v32, v145, v153 op_sel:[1,0,0] op_sel_hi:[1,0,0]\n"
"v_mfma_f32_16x16x32_bf16 a[80:83], a[176:179], v[0:3], 0\n"
"s_add_u32 m0, s51, 82984\n"
"ds_read_b64_tr_b16 a[248:249], v219 offset:41752\n"
"v_fma_mix_f32 v178, v33, v146, v154 op_sel:[0,0,0] op_sel_hi:[1,0,0]\n"
"v_mfma_f32_16x16x32_bf16 a[80:83], a[180:183], v[8:11], a[80:83]\n"
"ds_read_b64_tr_b16 a[250:251], v219 offset:42776\n"
"buffer_load_dwordx4 v217, s[24:27], s50 offen offset:0 lds\n"
"v_mfma_f32_16x16x32_bf16 a[84:87], a[176:179], v[4:7], 0\n"
"ds_read_b64_tr_b16 a[252:253], v219 offset:45848\n"
"v_fma_mix_f32 v179, v33, v147, v155 op_sel:[1,0,0] op_sel_hi:[1,0,0]\n"
"v_mfma_f32_16x16x32_bf16 a[84:87], a[180:183], v[12:15], a[84:87]\n"
"s_waitcnt lgkmcnt(12)\n"
"ds_read_b64_tr_b16 a[254:255], v219 offset:46872\n"
"v_fma_mix_f32 v180, v34, v148, v156 op_sel:[0,0,0] op_sel_hi:[1,0,0]\n"
"v_mfma_f32_16x16x32_bf16 a[88:91], a[184:187], v[0:3], 0\n"
"ds_read_b64_tr_b16 a[160:161], v218 offset:0\n"
"v_fma_mix_f32 v181, v34, v149, v157 op_sel:[1,0,0] op_sel_hi:[1,0,0]\n"
"v_mfma_f32_16x16x32_bf16 a[88:91], a[188:191], v[8:11], a[88:91]\n"
"ds_read_b64_tr_b16 a[162:163], v218 offset:1024\n"
"v_fma_mix_f32 v182, v35, v150, v158 op_sel:[0,0,0] op_sel_hi:[1,0,0]\n"
"v_mfma_f32_16x16x32_bf16 a[92:95], a[184:187], v[4:7], 0\n"
"s_add_u32 m0, s51, 90920\n"
"ds_read_b64_tr_b16 a[164:165], v218 offset:4096\n"
"v_fma_mix_f32 v183, v35, v151, v159 op_sel:[1,0,0] op_sel_hi:[1,0,0]\n"
"v_mfma_f32_16x16x32_bf16 a[92:95], a[188:191], v[12:15], a[92:95]\n"
"buffer_load_dwordx4 v217, s[24:27], s50 offen offset:256 lds\n"
"ds_read_b64_tr_b16 a[166:167], v218 offset:5120\n"
"v_mfma_f32_16x16x32_bf16 a[96:99], a[224:227], v[0:3], 0\n"
"s_add_u32 s50, s50, 425984\n"
"ds_read_b64_tr_b16 a[168:169], v218 offset:8\n"
"v_cvt_pk_bf16_f32 v32, v176, v177\n"
"v_mfma_f32_16x16x32_bf16 a[96:99], a[228:231], v[8:11], a[96:99]\n"
"ds_read_b64_tr_b16 a[170:171], v218 offset:1032\n"
"v_cvt_pk_bf16_f32 v33, v178, v179\n"
"v_mfma_f32_16x16x32_bf16 a[100:103], a[224:227], v[4:7], 0\n"
"ds_read_b64_tr_b16 a[172:173], v218 offset:4104\n"
"v_cvt_pk_bf16_f32 v34, v180, v181\n"
"v_mfma_f32_16x16x32_bf16 a[100:103], a[228:231], v[12:15], a[100:103]\n"
"s_waitcnt lgkmcnt(11)\n"
"ds_read_b64_tr_b16 a[174:175], v218 offset:5128\n"
"v_cvt_pk_bf16_f32 v35, v182, v183\n"
"v_mfma_f32_16x16x32_bf16 a[104:107], a[232:235], v[0:3], 0\n"
"ds_read_b64_tr_b16 a[176:177], v218 offset:256\n"
"v_fma_mix_f32 v184, v36, v144, v152 op_sel:[0,0,0] op_sel_hi:[1,0,0]\n"
"v_mfma_f32_16x16x32_bf16 a[104:107], a[236:239], v[8:11], a[104:107]\n"
"ds_read_b64_tr_b16 a[178:179], v218 offset:1280\n"
"v_fma_mix_f32 v185, v36, v145, v153 op_sel:[1,0,0] op_sel_hi:[1,0,0]\n"
"v_mfma_f32_16x16x32_bf16 a[108:111], a[232:235], v[4:7], 0\n"
"ds_read_b64_tr_b16 a[180:181], v218 offset:4352\n"
"v_fma_mix_f32 v186, v37, v146, v154 op_sel:[0,0,0] op_sel_hi:[1,0,0]\n"
"v_mfma_f32_16x16x32_bf16 a[108:111], a[236:239], v[12:15], a[108:111]\n"
"ds_read_b64_tr_b16 a[182:183], v218 offset:5376\n"
"v_fma_mix_f32 v187, v37, v147, v155 op_sel:[1,0,0] op_sel_hi:[1,0,0]\n"
"v_mfma_f32_16x16x32_bf16 a[112:115], a[240:243], v[0:3], 0\n"
"ds_read_b64_tr_b16 a[184:185], v218 offset:264\n"
"v_fma_mix_f32 v188, v38, v148, v156 op_sel:[0,0,0] op_sel_hi:[1,0,0]\n"
"v_mfma_f32_16x16x32_bf16 a[112:115], a[244:247], v[8:11], a[112:115]\n"
"ds_read_b64_tr_b16 a[186:187], v218 offset:1288\n"
"v_fma_mix_f32 v189, v38, v149, v157 op_sel:[1,0,0] op_sel_hi:[1,0,0]\n"
"v_mfma_f32_16x16x32_bf16 a[116:119], a[240:243], v[4:7], 0\n"
"ds_read_b64_tr_b16 a[188:189], v218 offset:4360\n"
"v_fma_mix_f32 v190, v39, v150, v158 op_sel:[0,0,0] op_sel_hi:[1,0,0]\n"
"v_mfma_f32_16x16x32_bf16 a[116:119], a[244:247], v[12:15], a[116:119]\n"
"s_waitcnt lgkmcnt(11)\n"
"ds_read_b64_tr_b16 a[190:191], v218 offset:5384\n"
"v_fma_mix_f32 v191, v39, v151, v159 op_sel:[1,0,0] op_sel_hi:[1,0,0]\n"
"v_mfma_f32_16x16x32_bf16 a[120:123], a[248:251], v[0:3], 0\n"
"ds_read_b64_tr_b16 a[224:225], v218 offset:512\n"
"ds_read_b128 v[144:147], v224 offset:1536\n"
"v_mfma_f32_16x16x32_bf16 a[120:123], a[252:255], v[8:11], a[120:123]\n"
"ds_read_b64_tr_b16 a[226:227], v218 offset:1536\n"
"ds_read_b128 v[148:151], v224 offset:1600\n"
"v_mfma_f32_16x16x32_bf16 a[124:127], a[248:251], v[4:7], 0\n"
"ds_read_b64_tr_b16 a[228:229], v218 offset:4608\n"
"ds_read_b128 v[152:155], v224 offset:1664\n"
"v_mfma_f32_16x16x32_bf16 a[124:127], a[252:255], v[12:15], a[124:127]\n"
"ds_read_b64_tr_b16 a[230:231], v218 offset:5632\n"
"ds_read_b128 v[156:159], v224 offset:1728\n"
"v_mfma_f32_16x16x32_bf16 a[0:3], a[160:163], v[16:19], a[0:3]\n"
"ds_read_b64_tr_b16 a[232:233], v218 offset:520\n"
"v_cvt_pk_bf16_f32 v36, v184, v185\n"
"v_mfma_f32_16x16x32_bf16 a[0:3], a[164:167], v[24:27], a[0:3]\n"
"ds_read_b64_tr_b16 a[234:235], v218 offset:1544\n"
"v_cvt_pk_bf16_f32 v37, v186, v187\n"
"v_mfma_f32_16x16x32_bf16 a[4:7], a[160:163], v[20:23], a[4:7]\n"
"s_waitcnt vmcnt(8)\n"
"s_barrier\n"
"ds_read_b64_tr_b16 a[236:237], v218 offset:4616\n"
"v_cvt_pk_bf16_f32 v38, v188, v189\n"
"v_mfma_f32_16x16x32_bf16 a[4:7], a[164:167], v[28:31], a[4:7]\n"
"s_waitcnt lgkmcnt(14)\n"
"ds_read_b64_tr_b16 a[238:239], v218 offset:5640\n"
"v_cvt_pk_bf16_f32 v39, v190, v191\n"
"v_mfma_f32_16x16x32_bf16 a[8:11], a[168:171], v[16:19], a[8:11]\n"
"s_add_u32 m0, s51, 98352\n"
"ds_read_b64_tr_b16 a[240:241], v218 offset:768\n"
"v_fma_mix_f32 v176, v40, v160, v168 op_sel:[0,0,0] op_sel_hi:[1,0,0]\n"
"v_mfma_f32_16x16x32_bf16 a[8:11], a[172:175], v[24:27], a[8:11]\n"
"ds_read_b64_tr_b16 a[242:243], v218 offset:1792\n"
"buffer_load_dwordx4 v217, s[24:27], s50 offen offset:0 lds\n"
"v_mfma_f32_16x16x32_bf16 a[12:15], a[168:171], v[20:23], a[12:15]\n"
"ds_read_b64_tr_b16 a[244:245], v218 offset:4864\n"
"v_fma_mix_f32 v177, v40, v161, v169 op_sel:[1,0,0] op_sel_hi:[1,0,0]\n"
"v_mfma_f32_16x16x32_bf16 a[12:15], a[172:175], v[28:31], a[12:15]\n"
"ds_read_b64_tr_b16 a[246:247], v218 offset:5888\n"
"v_fma_mix_f32 v178, v41, v162, v170 op_sel:[0,0,0] op_sel_hi:[1,0,0]\n"
"v_mfma_f32_16x16x32_bf16 a[16:19], a[176:179], v[16:19], a[16:19]\n"
"ds_read_b64_tr_b16 a[248:249], v218 offset:776\n"
"v_fma_mix_f32 v179, v41, v163, v171 op_sel:[1,0,0] op_sel_hi:[1,0,0]\n"
"v_mfma_f32_16x16x32_bf16 a[16:19], a[180:183], v[24:27], a[16:19]\n"
"ds_read_b64_tr_b16 a[250:251], v218 offset:1800\n"
"v_fma_mix_f32 v180, v42, v164, v172 op_sel:[0,0,0] op_sel_hi:[1,0,0]\n"
"v_mfma_f32_16x16x32_bf16 a[20:23], a[176:179], v[20:23], a[20:23]\n"
"s_add_u32 m0, s51, 106288\n"
"ds_read_b64_tr_b16 a[252:253], v218 offset:4872\n"
"v_fma_mix_f32 v181, v42, v165, v173 op_sel:[1,0,0] op_sel_hi:[1,0,0]\n"
"v_mfma_f32_16x16x32_bf16 a[20:23], a[180:183], v[28:31], a[20:23]\n"
"s_waitcnt lgkmcnt(11)\n"
"ds_read_b64_tr_b16 a[254:255], v218 offset:5896\n"
"buffer_load_dwordx4 v217, s[24:27], s50 offen offset:256 lds\n"
"v_mfma_f32_16x16x32_bf16 a[24:27], a[184:187], v[16:19], a[24:27]\n"
"s_add_u32 s50, s50, 32768\n"
"ds_read_b64_tr_b16 a[160:161], v218 offset:8192\n"
"v_fma_mix_f32 v182, v43, v166, v174 op_sel:[0,0,0] op_sel_hi:[1,0,0]\n"
"v_mfma_f32_16x16x32_bf16 a[24:27], a[188:191], v[24:27], a[24:27]\n"
"ds_read_b64_tr_b16 a[162:163], v218 offset:9216\n"
"v_fma_mix_f32 v183, v43, v167, v175 op_sel:[1,0,0] op_sel_hi:[1,0,0]\n"
"v_mfma_f32_16x16x32_bf16 a[28:31], a[184:187], v[20:23], a[28:31]\n"
"ds_read_b64_tr_b16 a[164:165], v218 offset:12288\n"
"v_cvt_pk_bf16_f32 v40, v176, v177\n"
"v_mfma_f32_16x16x32_bf16 a[28:31], a[188:191], v[28:31], a[28:31]\n"
"ds_read_b64_tr_b16 a[166:167], v218 offset:13312\n"
"v_cvt_pk_bf16_f32 v41, v178, v179\n"
"v_mfma_f32_16x16x32_bf16 a[32:35], a[224:227], v[16:19], a[32:35]\n"
"s_add_u32 m0, s51, 99376\n"
"ds_read_b64_tr_b16 a[168:169], v218 offset:8200\n"
"v_cvt_pk_bf16_f32 v42, v180, v181\n"
"v_mfma_f32_16x16x32_bf16 a[32:35], a[228:231], v[24:27], a[32:35]\n"
"ds_read_b64_tr_b16 a[170:171], v218 offset:9224\n"
"buffer_load_dwordx4 v217, s[24:27], s50 offen offset:0 lds\n"
"v_mfma_f32_16x16x32_bf16 a[36:39], a[224:227], v[20:23], a[36:39]\n"
"ds_read_b64_tr_b16 a[172:173], v218 offset:12296\n"
"v_cvt_pk_bf16_f32 v43, v182, v183\n"
"v_mfma_f32_16x16x32_bf16 a[36:39], a[228:231], v[28:31], a[36:39]\n"
"s_waitcnt lgkmcnt(11)\n"
"ds_read_b64_tr_b16 a[174:175], v218 offset:13320\n"
"v_fma_mix_f32 v184, v44, v160, v168 op_sel:[0,0,0] op_sel_hi:[1,0,0]\n"
"v_mfma_f32_16x16x32_bf16 a[40:43], a[232:235], v[16:19], a[40:43]\n"
"ds_read_b64_tr_b16 a[176:177], v218 offset:8448\n"
"v_fma_mix_f32 v185, v44, v161, v169 op_sel:[1,0,0] op_sel_hi:[1,0,0]\n"
"v_mfma_f32_16x16x32_bf16 a[40:43], a[236:239], v[24:27], a[40:43]\n"
"ds_read_b64_tr_b16 a[178:179], v218 offset:9472\n"
"v_fma_mix_f32 v186, v45, v162, v170 op_sel:[0,0,0] op_sel_hi:[1,0,0]\n"
"v_mfma_f32_16x16x32_bf16 a[44:47], a[232:235], v[20:23], a[44:47]\n"
"s_add_u32 m0, s51, 107312\n"
"ds_read_b64_tr_b16 a[180:181], v218 offset:12544\n"
"v_fma_mix_f32 v187, v45, v163, v171 op_sel:[1,0,0] op_sel_hi:[1,0,0]\n"
"v_mfma_f32_16x16x32_bf16 a[44:47], a[236:239], v[28:31], a[44:47]\n"
"ds_read_b64_tr_b16 a[182:183], v218 offset:13568\n"
"buffer_load_dwordx4 v217, s[24:27], s50 offen offset:256 lds\n"
"v_mfma_f32_16x16x32_bf16 a[48:51], a[240:243], v[16:19], a[48:51]\n"
"s_add_u32 s50, s50, 32768\n"
"ds_read_b64_tr_b16 a[184:185], v218 offset:8456\n"
"v_fma_mix_f32 v188, v46, v164, v172 op_sel:[0,0,0] op_sel_hi:[1,0,0]\n"
"v_mfma_f32_16x16x32_bf16 a[48:51], a[244:247], v[24:27], a[48:51]\n"
"ds_read_b64_tr_b16 a[186:187], v218 offset:9480\n"
"v_fma_mix_f32 v189, v46, v165, v173 op_sel:[1,0,0] op_sel_hi:[1,0,0]\n"
"v_mfma_f32_16x16x32_bf16 a[52:55], a[240:243], v[20:23], a[52:55]\n"
"ds_read_b64_tr_b16 a[188:189], v218 offset:12552\n"
"v_fma_mix_f32 v190, v47, v166, v174 op_sel:[0,0,0] op_sel_hi:[1,0,0]\n"
"v_mfma_f32_16x16x32_bf16 a[52:55], a[244:247], v[28:31], a[52:55]\n"
"s_waitcnt lgkmcnt(11)\n"
"ds_read_b64_tr_b16 a[190:191], v218 offset:13576\n"
"v_fma_mix_f32 v191, v47, v167, v175 op_sel:[1,0,0] op_sel_hi:[1,0,0]\n"
"v_mfma_f32_16x16x32_bf16 a[56:59], a[248:251], v[16:19], a[56:59]\n"
"s_add_u32 m0, s51, 114744\n"
"ds_read_b64_tr_b16 a[224:225], v218 offset:8704\n"
"ds_read_b128 v[160:163], v224 offset:1792\n"
"v_mfma_f32_16x16x32_bf16 a[56:59], a[252:255], v[24:27], a[56:59]\n"
"ds_read_b64_tr_b16 a[226:227], v218 offset:9728\n"
"buffer_load_dwordx4 v217, s[24:27], s50 offen offset:0 lds\n"
"v_mfma_f32_16x16x32_bf16 a[60:63], a[248:251], v[20:23], a[60:63]\n"
"ds_read_b64_tr_b16 a[228:229], v218 offset:12800\n"
"ds_read_b128 v[164:167], v224 offset:1856\n"
"v_mfma_f32_16x16x32_bf16 a[60:63], a[252:255], v[28:31], a[60:63]\n"
"ds_read_b64_tr_b16 a[230:231], v218 offset:13824\n"
"ds_read_b128 v[168:171], v224 offset:1920\n"
"v_mfma_f32_16x16x32_bf16 a[64:67], a[160:163], v[16:19], a[64:67]\n"
"ds_read_b64_tr_b16 a[232:233], v218 offset:8712\n"
"ds_read_b128 v[172:175], v224 offset:1984\n"
"v_mfma_f32_16x16x32_bf16 a[64:67], a[164:167], v[24:27], a[64:67]\n"
"ds_read_b64_tr_b16 a[234:235], v218 offset:9736\n"
"v_cvt_pk_bf16_f32 v44, v184, v185\n"
"v_mfma_f32_16x16x32_bf16 a[68:71], a[160:163], v[20:23], a[68:71]\n"
"s_add_u32 m0, s51, 122680\n"
"ds_read_b64_tr_b16 a[236:237], v218 offset:12808\n"
"v_cvt_pk_bf16_f32 v45, v186, v187\n"
"v_mfma_f32_16x16x32_bf16 a[68:71], a[164:167], v[28:31], a[68:71]\n"
"s_waitcnt lgkmcnt(14)\n"
"ds_read_b64_tr_b16 a[238:239], v218 offset:13832\n"
"buffer_load_dwordx4 v217, s[24:27], s50 offen offset:256 lds\n"
"v_mfma_f32_16x16x32_bf16 a[72:75], a[168:171], v[16:19], a[72:75]\n"
"s_add_u32 s50, s50, 32768\n"
"ds_read_b64_tr_b16 a[240:241], v218 offset:8960\n"
"v_cvt_pk_bf16_f32 v46, v188, v189\n"
"v_mfma_f32_16x16x32_bf16 a[72:75], a[172:175], v[24:27], a[72:75]\n"
"ds_read_b64_tr_b16 a[242:243], v218 offset:9984\n"
"v_cvt_pk_bf16_f32 v47, v190, v191\n"
"v_mfma_f32_16x16x32_bf16 a[76:79], a[168:171], v[20:23], a[76:79]\n"
"ds_read_b64_tr_b16 a[244:245], v218 offset:13056\n"
"v_fma_mix_f32 v176, v48, v144, v152 op_sel:[0,0,0] op_sel_hi:[1,0,0]\n"
"v_mfma_f32_16x16x32_bf16 a[76:79], a[172:175], v[28:31], a[76:79]\n"
"ds_read_b64_tr_b16 a[246:247], v218 offset:14080\n"
"v_fma_mix_f32 v177, v48, v145, v153 op_sel:[1,0,0] op_sel_hi:[1,0,0]\n"
"v_mfma_f32_16x16x32_bf16 a[80:83], a[176:179], v[16:19], a[80:83]\n"
"s_add_u32 m0, s51, 115768\n"
"ds_read_b64_tr_b16 a[248:249], v218 offset:8968\n"
"v_fma_mix_f32 v178, v49, v146, v154 op_sel:[0,0,0] op_sel_hi:[1,0,0]\n"
"v_mfma_f32_16x16x32_bf16 a[80:83], a[180:183], v[24:27], a[80:83]\n"
"ds_read_b64_tr_b16 a[250:251], v218 offset:9992\n"
"buffer_load_dwordx4 v217, s[24:27], s50 offen offset:0 lds\n"
"v_mfma_f32_16x16x32_bf16 a[84:87], a[176:179], v[20:23], a[84:87]\n"
"ds_read_b64_tr_b16 a[252:253], v218 offset:13064\n"
"v_fma_mix_f32 v179, v49, v147, v155 op_sel:[1,0,0] op_sel_hi:[1,0,0]\n"
"v_mfma_f32_16x16x32_bf16 a[84:87], a[180:183], v[28:31], a[84:87]\n"
"s_waitcnt lgkmcnt(12)\n"
"ds_read_b64_tr_b16 a[254:255], v218 offset:14088\n"
"v_fma_mix_f32 v180, v50, v148, v156 op_sel:[0,0,0] op_sel_hi:[1,0,0]\n"
"v_mfma_f32_16x16x32_bf16 a[88:91], a[184:187], v[16:19], a[88:91]\n"
"ds_read_b64_tr_b16 a[160:161], v218 offset:32784\n"
"v_fma_mix_f32 v181, v50, v149, v157 op_sel:[1,0,0] op_sel_hi:[1,0,0]\n"
"v_mfma_f32_16x16x32_bf16 a[88:91], a[188:191], v[24:27], a[88:91]\n"
"ds_read_b64_tr_b16 a[162:163], v218 offset:33808\n"
"v_fma_mix_f32 v182, v51, v150, v158 op_sel:[0,0,0] op_sel_hi:[1,0,0]\n"
"v_mfma_f32_16x16x32_bf16 a[92:95], a[184:187], v[20:23], a[92:95]\n"
"s_add_u32 m0, s51, 123704\n"
"ds_read_b64_tr_b16 a[164:165], v218 offset:36880\n"
"v_fma_mix_f32 v183, v51, v151, v159 op_sel:[1,0,0] op_sel_hi:[1,0,0]\n"
"v_mfma_f32_16x16x32_bf16 a[92:95], a[188:191], v[28:31], a[92:95]\n"
"buffer_load_dwordx4 v217, s[24:27], s50 offen offset:256 lds\n"
"ds_read_b64_tr_b16 a[166:167], v218 offset:37904\n"
"v_mfma_f32_16x16x32_bf16 a[96:99], a[224:227], v[16:19], a[96:99]\n"
"s_add_u32 s50, s50, 425984\n"
"ds_read_b64_tr_b16 a[168:169], v218 offset:32792\n"
"v_cvt_pk_bf16_f32 v48, v176, v177\n"
"v_mfma_f32_16x16x32_bf16 a[96:99], a[228:231], v[24:27], a[96:99]\n"
"ds_read_b64_tr_b16 a[170:171], v218 offset:33816\n"
"v_cvt_pk_bf16_f32 v49, v178, v179\n"
"v_mfma_f32_16x16x32_bf16 a[100:103], a[224:227], v[20:23], a[100:103]\n"
"ds_read_b64_tr_b16 a[172:173], v218 offset:36888\n"
"v_cvt_pk_bf16_f32 v50, v180, v181\n"
"v_mfma_f32_16x16x32_bf16 a[100:103], a[228:231], v[28:31], a[100:103]\n"
"s_waitcnt lgkmcnt(11)\n"
"ds_read_b64_tr_b16 a[174:175], v218 offset:37912\n"
"v_cvt_pk_bf16_f32 v51, v182, v183\n"
"v_mfma_f32_16x16x32_bf16 a[104:107], a[232:235], v[16:19], a[104:107]\n"
"ds_read_b64_tr_b16 a[176:177], v218 offset:33040\n"
"v_fma_mix_f32 v184, v52, v144, v152 op_sel:[0,0,0] op_sel_hi:[1,0,0]\n"
"v_mfma_f32_16x16x32_bf16 a[104:107], a[236:239], v[24:27], a[104:107]\n"
"ds_read_b64_tr_b16 a[178:179], v218 offset:34064\n"
"v_fma_mix_f32 v185, v52, v145, v153 op_sel:[1,0,0] op_sel_hi:[1,0,0]\n"
"v_mfma_f32_16x16x32_bf16 a[108:111], a[232:235], v[20:23], a[108:111]\n"
"ds_read_b64_tr_b16 a[180:181], v218 offset:37136\n"
"v_fma_mix_f32 v186, v53, v146, v154 op_sel:[0,0,0] op_sel_hi:[1,0,0]\n"
"v_mfma_f32_16x16x32_bf16 a[108:111], a[236:239], v[28:31], a[108:111]\n"
"ds_read_b64_tr_b16 a[182:183], v218 offset:38160\n"
"v_fma_mix_f32 v187, v53, v147, v155 op_sel:[1,0,0] op_sel_hi:[1,0,0]\n"
"v_mfma_f32_16x16x32_bf16 a[112:115], a[240:243], v[16:19], a[112:115]\n"
"ds_read_b64_tr_b16 a[184:185], v218 offset:33048\n"
"v_fma_mix_f32 v188, v54, v148, v156 op_sel:[0,0,0] op_sel_hi:[1,0,0]\n"
"v_mfma_f32_16x16x32_bf16 a[112:115], a[244:247], v[24:27], a[112:115]\n"
"ds_read_b64_tr_b16 a[186:187], v218 offset:34072\n"
"v_fma_mix_f32 v189, v54, v149, v157 op_sel:[1,0,0] op_sel_hi:[1,0,0]\n"
"v_mfma_f32_16x16x32_bf16 a[116:119], a[240:243], v[20:23], a[116:119]\n"
"ds_read_b64_tr_b16 a[188:189], v218 offset:37144\n"
"v_fma_mix_f32 v190, v55, v150, v158 op_sel:[0,0,0] op_sel_hi:[1,0,0]\n"
"v_mfma_f32_16x16x32_bf16 a[116:119], a[244:247], v[28:31], a[116:119]\n"
"s_waitcnt lgkmcnt(11)\n"
"ds_read_b64_tr_b16 a[190:191], v218 offset:38168\n"
"v_fma_mix_f32 v191, v55, v151, v159 op_sel:[1,0,0] op_sel_hi:[1,0,0]\n"
"v_mfma_f32_16x16x32_bf16 a[120:123], a[248:251], v[16:19], a[120:123]\n"
"ds_read_b64_tr_b16 a[224:225], v218 offset:33296\n"
"ds_read_b128 v[144:147], v224 offset:2048\n"
"v_mfma_f32_16x16x32_bf16 a[120:123], a[252:255], v[24:27], a[120:123]\n"
"ds_read_b64_tr_b16 a[226:227], v218 offset:34320\n"
"ds_read_b128 v[148:151], v224 offset:2112\n"
"v_mfma_f32_16x16x32_bf16 a[124:127], a[248:251], v[20:23], a[124:127]\n"
"ds_read_b64_tr_b16 a[228:229], v218 offset:37392\n"
"ds_read_b128 v[152:155], v224 offset:2176\n"
"v_mfma_f32_16x16x32_bf16 a[124:127], a[252:255], v[28:31], a[124:127]\n"
"ds_read_b64_tr_b16 a[230:231], v218 offset:38416\n"
"ds_read_b128 v[156:159], v224 offset:2240\n"
"v_mfma_f32_16x16x32_bf16 a[0:3], a[160:163], v[32:35], a[0:3]\n"
"ds_read_b64_tr_b16 a[232:233], v218 offset:33304\n"
"v_cvt_pk_bf16_f32 v52, v184, v185\n"
"v_mfma_f32_16x16x32_bf16 a[0:3], a[164:167], v[40:43], a[0:3]\n"
"ds_read_b64_tr_b16 a[234:235], v218 offset:34328\n"
"v_cvt_pk_bf16_f32 v53, v186, v187\n"
"v_mfma_f32_16x16x32_bf16 a[4:7], a[160:163], v[36:39], a[4:7]\n"
"s_waitcnt vmcnt(8)\n"
"s_barrier\n"
"ds_read_b64_tr_b16 a[236:237], v218 offset:37400\n"
"v_cvt_pk_bf16_f32 v54, v188, v189\n"
"v_mfma_f32_16x16x32_bf16 a[4:7], a[164:167], v[44:47], a[4:7]\n"
"s_waitcnt lgkmcnt(14)\n"
"ds_read_b64_tr_b16 a[238:239], v218 offset:38424\n"
"v_cvt_pk_bf16_f32 v55, v190, v191\n"
"v_mfma_f32_16x16x32_bf16 a[8:11], a[168:171], v[32:35], a[8:11]\n"
"s_mov_b32 m0, s51\n"
"ds_read_b64_tr_b16 a[240:241], v218 offset:33552\n"
"v_fma_mix_f32 v176, v56, v160, v168 op_sel:[0,0,0] op_sel_hi:[1,0,0]\n"
"v_mfma_f32_16x16x32_bf16 a[8:11], a[172:175], v[40:43], a[8:11]\n"
"ds_read_b64_tr_b16 a[242:243], v218 offset:34576\n"
"buffer_load_dwordx4 v217, s[24:27], s50 offen offset:0 lds\n"
"v_mfma_f32_16x16x32_bf16 a[12:15], a[168:171], v[36:39], a[12:15]\n"
"ds_read_b64_tr_b16 a[244:245], v218 offset:37648\n"
"v_fma_mix_f32 v177, v56, v161, v169 op_sel:[1,0,0] op_sel_hi:[1,0,0]\n"
"v_mfma_f32_16x16x32_bf16 a[12:15], a[172:175], v[44:47], a[12:15]\n"
"ds_read_b64_tr_b16 a[246:247], v218 offset:38672\n"
"v_fma_mix_f32 v178, v57, v162, v170 op_sel:[0,0,0] op_sel_hi:[1,0,0]\n"
"v_mfma_f32_16x16x32_bf16 a[16:19], a[176:179], v[32:35], a[16:19]\n"
"ds_read_b64_tr_b16 a[248:249], v218 offset:33560\n"
"v_fma_mix_f32 v179, v57, v163, v171 op_sel:[1,0,0] op_sel_hi:[1,0,0]\n"
"v_mfma_f32_16x16x32_bf16 a[16:19], a[180:183], v[40:43], a[16:19]\n"
"ds_read_b64_tr_b16 a[250:251], v218 offset:34584\n"
"v_fma_mix_f32 v180, v58, v164, v172 op_sel:[0,0,0] op_sel_hi:[1,0,0]\n"
"v_mfma_f32_16x16x32_bf16 a[20:23], a[176:179], v[36:39], a[20:23]\n"
"s_add_u32 m0, s51, 7936\n"
"ds_read_b64_tr_b16 a[252:253], v218 offset:37656\n"
"v_fma_mix_f32 v181, v58, v165, v173 op_sel:[1,0,0] op_sel_hi:[1,0,0]\n"
"v_mfma_f32_16x16x32_bf16 a[20:23], a[180:183], v[44:47], a[20:23]\n"
"s_waitcnt lgkmcnt(11)\n"
"ds_read_b64_tr_b16 a[254:255], v218 offset:38680\n"
"buffer_load_dwordx4 v217, s[24:27], s50 offen offset:256 lds\n"
"v_mfma_f32_16x16x32_bf16 a[24:27], a[184:187], v[32:35], a[24:27]\n"
"s_add_u32 s50, s50, 32768\n"
"ds_read_b64_tr_b16 a[160:161], v218 offset:40976\n"
"v_fma_mix_f32 v182, v59, v166, v174 op_sel:[0,0,0] op_sel_hi:[1,0,0]\n"
"v_mfma_f32_16x16x32_bf16 a[24:27], a[188:191], v[40:43], a[24:27]\n"
"ds_read_b64_tr_b16 a[162:163], v218 offset:42000\n"
"v_fma_mix_f32 v183, v59, v167, v175 op_sel:[1,0,0] op_sel_hi:[1,0,0]\n"
"v_mfma_f32_16x16x32_bf16 a[28:31], a[184:187], v[36:39], a[28:31]\n"
"ds_read_b64_tr_b16 a[164:165], v218 offset:45072\n"
"v_cvt_pk_bf16_f32 v56, v176, v177\n"
"v_mfma_f32_16x16x32_bf16 a[28:31], a[188:191], v[44:47], a[28:31]\n"
"ds_read_b64_tr_b16 a[166:167], v218 offset:46096\n"
"v_cvt_pk_bf16_f32 v57, v178, v179\n"
"v_mfma_f32_16x16x32_bf16 a[32:35], a[224:227], v[32:35], a[32:35]\n"
"s_add_u32 m0, s51, 1024\n"
"ds_read_b64_tr_b16 a[168:169], v218 offset:40984\n"
"v_cvt_pk_bf16_f32 v58, v180, v181\n"
"v_mfma_f32_16x16x32_bf16 a[32:35], a[228:231], v[40:43], a[32:35]\n"
"ds_read_b64_tr_b16 a[170:171], v218 offset:42008\n"
"buffer_load_dwordx4 v217, s[24:27], s50 offen offset:0 lds\n"
"v_mfma_f32_16x16x32_bf16 a[36:39], a[224:227], v[36:39], a[36:39]\n"
"ds_read_b64_tr_b16 a[172:173], v218 offset:45080\n"
"v_cvt_pk_bf16_f32 v59, v182, v183\n"
"v_mfma_f32_16x16x32_bf16 a[36:39], a[228:231], v[44:47], a[36:39]\n"
"s_waitcnt lgkmcnt(11)\n"
"ds_read_b64_tr_b16 a[174:175], v218 offset:46104\n"
"v_fma_mix_f32 v184, v60, v160, v168 op_sel:[0,0,0] op_sel_hi:[1,0,0]\n"
"v_mfma_f32_16x16x32_bf16 a[40:43], a[232:235], v[32:35], a[40:43]\n"
"ds_read_b64_tr_b16 a[176:177], v218 offset:41232\n"
"v_fma_mix_f32 v185, v60, v161, v169 op_sel:[1,0,0] op_sel_hi:[1,0,0]\n"
"v_mfma_f32_16x16x32_bf16 a[40:43], a[236:239], v[40:43], a[40:43]\n"
"ds_read_b64_tr_b16 a[178:179], v218 offset:42256\n"
"v_fma_mix_f32 v186, v61, v162, v170 op_sel:[0,0,0] op_sel_hi:[1,0,0]\n"
"v_mfma_f32_16x16x32_bf16 a[44:47], a[232:235], v[36:39], a[44:47]\n"
"s_add_u32 m0, s51, 8960\n"
"ds_read_b64_tr_b16 a[180:181], v218 offset:45328\n"
"v_fma_mix_f32 v187, v61, v163, v171 op_sel:[1,0,0] op_sel_hi:[1,0,0]\n"
"v_mfma_f32_16x16x32_bf16 a[44:47], a[236:239], v[44:47], a[44:47]\n"
"ds_read_b64_tr_b16 a[182:183], v218 offset:46352\n"
"buffer_load_dwordx4 v217, s[24:27], s50 offen offset:256 lds\n"
"v_mfma_f32_16x16x32_bf16 a[48:51], a[240:243], v[32:35], a[48:51]\n"
"s_add_u32 s50, s50, 32768\n"
"ds_read_b64_tr_b16 a[184:185], v218 offset:41240\n"
"v_fma_mix_f32 v188, v62, v164, v172 op_sel:[0,0,0] op_sel_hi:[1,0,0]\n"
"v_mfma_f32_16x16x32_bf16 a[48:51], a[244:247], v[40:43], a[48:51]\n"
"ds_read_b64_tr_b16 a[186:187], v218 offset:42264\n"
"v_fma_mix_f32 v189, v62, v165, v173 op_sel:[1,0,0] op_sel_hi:[1,0,0]\n"
"v_mfma_f32_16x16x32_bf16 a[52:55], a[240:243], v[36:39], a[52:55]\n"
"ds_read_b64_tr_b16 a[188:189], v218 offset:45336\n"
"v_fma_mix_f32 v190, v63, v166, v174 op_sel:[0,0,0] op_sel_hi:[1,0,0]\n"
"v_mfma_f32_16x16x32_bf16 a[52:55], a[244:247], v[44:47], a[52:55]\n"
"s_waitcnt lgkmcnt(11)\n"
"ds_read_b64_tr_b16 a[190:191], v218 offset:46360\n"
"v_fma_mix_f32 v191, v63, v167, v175 op_sel:[1,0,0] op_sel_hi:[1,0,0]\n"
"v_mfma_f32_16x16x32_bf16 a[56:59], a[248:251], v[32:35], a[56:59]\n"
"s_add_u32 m0, s51, 16392\n"
"ds_read_b64_tr_b16 a[224:225], v218 offset:41488\n"
"ds_read_b128 v[160:163], v224 offset:2304\n"
"v_mfma_f32_16x16x32_bf16 a[56:59], a[252:255], v[40:43], a[56:59]\n"
"ds_read_b64_tr_b16 a[226:227], v218 offset:42512\n"
"buffer_load_dwordx4 v217, s[24:27], s50 offen offset:0 lds\n"
"v_mfma_f32_16x16x32_bf16 a[60:63], a[248:251], v[36:39], a[60:63]\n"
"ds_read_b64_tr_b16 a[228:229], v218 offset:45584\n"
"ds_read_b128 v[164:167], v224 offset:2368\n"
"v_mfma_f32_16x16x32_bf16 a[60:63], a[252:255], v[44:47], a[60:63]\n"
"ds_read_b64_tr_b16 a[230:231], v218 offset:46608\n"
"ds_read_b128 v[168:171], v224 offset:2432\n"
"v_mfma_f32_16x16x32_bf16 a[64:67], a[160:163], v[32:35], a[64:67]\n"
"ds_read_b64_tr_b16 a[232:233], v218 offset:41496\n"
"ds_read_b128 v[172:175], v224 offset:2496\n"
"v_mfma_f32_16x16x32_bf16 a[64:67], a[164:167], v[40:43], a[64:67]\n"
"ds_read_b64_tr_b16 a[234:235], v218 offset:42520\n"
"v_cvt_pk_bf16_f32 v60, v184, v185\n"
"v_mfma_f32_16x16x32_bf16 a[68:71], a[160:163], v[36:39], a[68:71]\n"
"s_add_u32 m0, s51, 24328\n"
"ds_read_b64_tr_b16 a[236:237], v218 offset:45592\n"
"v_cvt_pk_bf16_f32 v61, v186, v187\n"
"v_mfma_f32_16x16x32_bf16 a[68:71], a[164:167], v[44:47], a[68:71]\n"
"s_waitcnt lgkmcnt(14)\n"
"ds_read_b64_tr_b16 a[238:239], v218 offset:46616\n"
"buffer_load_dwordx4 v217, s[24:27], s50 offen offset:256 lds\n"
"v_mfma_f32_16x16x32_bf16 a[72:75], a[168:171], v[32:35], a[72:75]\n"
"s_add_u32 s50, s50, 32768\n"
"ds_read_b64_tr_b16 a[240:241], v218 offset:41744\n"
"v_cvt_pk_bf16_f32 v62, v188, v189\n"
"v_mfma_f32_16x16x32_bf16 a[72:75], a[172:175], v[40:43], a[72:75]\n"
"ds_read_b64_tr_b16 a[242:243], v218 offset:42768\n"
"v_cvt_pk_bf16_f32 v63, v190, v191\n"
"v_mfma_f32_16x16x32_bf16 a[76:79], a[168:171], v[36:39], a[76:79]\n"
"ds_read_b64_tr_b16 a[244:245], v218 offset:45840\n"
"v_fma_mix_f32 v176, v64, v144, v152 op_sel:[0,0,0] op_sel_hi:[1,0,0]\n"
"v_mfma_f32_16x16x32_bf16 a[76:79], a[172:175], v[44:47], a[76:79]\n"
"ds_read_b64_tr_b16 a[246:247], v218 offset:46864\n"
"v_fma_mix_f32 v177, v64, v145, v153 op_sel:[1,0,0] op_sel_hi:[1,0,0]\n"
"v_mfma_f32_16x16x32_bf16 a[80:83], a[176:179], v[32:35], a[80:83]\n"
"s_add_u32 m0, s51, 17416\n"
"ds_read_b64_tr_b16 a[248:249], v218 offset:41752\n"
"v_fma_mix_f32 v178, v65, v146, v154 op_sel:[0,0,0] op_sel_hi:[1,0,0]\n"
"v_mfma_f32_16x16x32_bf16 a[80:83], a[180:183], v[40:43], a[80:83]\n"
"ds_read_b64_tr_b16 a[250:251], v218 offset:42776\n"
"buffer_load_dwordx4 v217, s[24:27], s50 offen offset:0 lds\n"
"v_mfma_f32_16x16x32_bf16 a[84:87], a[176:179], v[36:39], a[84:87]\n"
"ds_read_b64_tr_b16 a[252:253], v218 offset:45848\n"
"v_fma_mix_f32 v179, v65, v147, v155 op_sel:[1,0,0] op_sel_hi:[1,0,0]\n"
"v_mfma_f32_16x16x32_bf16 a[84:87], a[180:183], v[44:47], a[84:87]\n"
"s_waitcnt lgkmcnt(12)\n"
"ds_read_b64_tr_b16 a[254:255], v218 offset:46872\n"
"v_fma_mix_f32 v180, v66, v148, v156 op_sel:[0,0,0] op_sel_hi:[1,0,0]\n"
"v_mfma_f32_16x16x32_bf16 a[88:91], a[184:187], v[32:35], a[88:91]\n"
"ds_read_b64_tr_b16 a[160:161], v219 offset:0\n"
"v_fma_mix_f32 v181, v66, v149, v157 op_sel:[1,0,0] op_sel_hi:[1,0,0]\n"
"v_mfma_f32_16x16x32_bf16 a[88:91], a[188:191], v[40:43], a[88:91]\n"
"ds_read_b64_tr_b16 a[162:163], v219 offset:1024\n"
"v_fma_mix_f32 v182, v67, v150, v158 op_sel:[0,0,0] op_sel_hi:[1,0,0]\n"
"v_mfma_f32_16x16x32_bf16 a[92:95], a[184:187], v[36:39], a[92:95]\n"
"s_add_u32 m0, s51, 25352\n"
"ds_read_b64_tr_b16 a[164:165], v219 offset:4096\n"
"v_fma_mix_f32 v183, v67, v151, v159 op_sel:[1,0,0] op_sel_hi:[1,0,0]\n"
"v_mfma_f32_16x16x32_bf16 a[92:95], a[188:191], v[44:47], a[92:95]\n"
"buffer_load_dwordx4 v217, s[24:27], s50 offen offset:256 lds\n"
"ds_read_b64_tr_b16 a[166:167], v219 offset:5120\n"
"v_mfma_f32_16x16x32_bf16 a[96:99], a[224:227], v[32:35], a[96:99]\n"
"s_add_u32 s50, s50, 425984\n"
"ds_read_b64_tr_b16 a[168:169], v219 offset:8\n"
"v_cvt_pk_bf16_f32 v64, v176, v177\n"
"v_mfma_f32_16x16x32_bf16 a[96:99], a[228:231], v[40:43], a[96:99]\n"
"ds_read_b64_tr_b16 a[170:171], v219 offset:1032\n"
"v_cvt_pk_bf16_f32 v65, v178, v179\n"
"v_mfma_f32_16x16x32_bf16 a[100:103], a[224:227], v[36:39], a[100:103]\n"
"ds_read_b64_tr_b16 a[172:173], v219 offset:4104\n"
"v_cvt_pk_bf16_f32 v66, v180, v181\n"
"v_mfma_f32_16x16x32_bf16 a[100:103], a[228:231], v[44:47], a[100:103]\n"
"s_waitcnt lgkmcnt(11)\n"
"ds_read_b64_tr_b16 a[174:175], v219 offset:5128\n"
"v_cvt_pk_bf16_f32 v67, v182, v183\n"
"v_mfma_f32_16x16x32_bf16 a[104:107], a[232:235], v[32:35], a[104:107]\n"
"ds_read_b64_tr_b16 a[176:177], v219 offset:256\n"
"v_fma_mix_f32 v184, v68, v144, v152 op_sel:[0,0,0] op_sel_hi:[1,0,0]\n"
"v_mfma_f32_16x16x32_bf16 a[104:107], a[236:239], v[40:43], a[104:107]\n"
"ds_read_b64_tr_b16 a[178:179], v219 offset:1280\n"
"v_fma_mix_f32 v185, v68, v145, v153 op_sel:[1,0,0] op_sel_hi:[1,0,0]\n"
"v_mfma_f32_16x16x32_bf16 a[108:111], a[232:235], v[36:39], a[108:111]\n"
"ds_read_b64_tr_b16 a[180:181], v219 offset:4352\n"
"v_fma_mix_f32 v186, v69, v146, v154 op_sel:[0,0,0] op_sel_hi:[1,0,0]\n"
"v_mfma_f32_16x16x32_bf16 a[108:111], a[236:239], v[44:47], a[108:111]\n"
"ds_read_b64_tr_b16 a[182:183], v219 offset:5376\n"
"v_fma_mix_f32 v187, v69, v147, v155 op_sel:[1,0,0] op_sel_hi:[1,0,0]\n"
"v_mfma_f32_16x16x32_bf16 a[112:115], a[240:243], v[32:35], a[112:115]\n"
"ds_read_b64_tr_b16 a[184:185], v219 offset:264\n"
"v_fma_mix_f32 v188, v70, v148, v156 op_sel:[0,0,0] op_sel_hi:[1,0,0]\n"
"v_mfma_f32_16x16x32_bf16 a[112:115], a[244:247], v[40:43], a[112:115]\n"
"ds_read_b64_tr_b16 a[186:187], v219 offset:1288\n"
"v_fma_mix_f32 v189, v70, v149, v157 op_sel:[1,0,0] op_sel_hi:[1,0,0]\n"
"v_mfma_f32_16x16x32_bf16 a[116:119], a[240:243], v[36:39], a[116:119]\n"
"ds_read_b64_tr_b16 a[188:189], v219 offset:4360\n"
"v_fma_mix_f32 v190, v71, v150, v158 op_sel:[0,0,0] op_sel_hi:[1,0,0]\n"
"v_mfma_f32_16x16x32_bf16 a[116:119], a[244:247], v[44:47], a[116:119]\n"
"s_waitcnt lgkmcnt(11)\n"
"ds_read_b64_tr_b16 a[190:191], v219 offset:5384\n"
"v_fma_mix_f32 v191, v71, v151, v159 op_sel:[1,0,0] op_sel_hi:[1,0,0]\n"
"v_mfma_f32_16x16x32_bf16 a[120:123], a[248:251], v[32:35], a[120:123]\n"
"ds_read_b64_tr_b16 a[224:225], v219 offset:512\n"
"ds_read_b128 v[144:147], v224 offset:2560\n"
"v_mfma_f32_16x16x32_bf16 a[120:123], a[252:255], v[40:43], a[120:123]\n"
"ds_read_b64_tr_b16 a[226:227], v219 offset:1536\n"
"ds_read_b128 v[148:151], v224 offset:2624\n"
"v_mfma_f32_16x16x32_bf16 a[124:127], a[248:251], v[36:39], a[124:127]\n"
"ds_read_b64_tr_b16 a[228:229], v219 offset:4608\n"
"ds_read_b128 v[152:155], v224 offset:2688\n"
"v_mfma_f32_16x16x32_bf16 a[124:127], a[252:255], v[44:47], a[124:127]\n"
"ds_read_b64_tr_b16 a[230:231], v219 offset:5632\n"
"ds_read_b128 v[156:159], v224 offset:2752\n"
"v_mfma_f32_16x16x32_bf16 a[0:3], a[160:163], v[48:51], a[0:3]\n"
"ds_read_b64_tr_b16 a[232:233], v219 offset:520\n"
"v_cvt_pk_bf16_f32 v68, v184, v185\n"
"v_mfma_f32_16x16x32_bf16 a[0:3], a[164:167], v[56:59], a[0:3]\n"
"ds_read_b64_tr_b16 a[234:235], v219 offset:1544\n"
"v_cvt_pk_bf16_f32 v69, v186, v187\n"
"v_mfma_f32_16x16x32_bf16 a[4:7], a[160:163], v[52:55], a[4:7]\n"
"s_waitcnt vmcnt(8)\n"
"s_barrier\n"
"ds_read_b64_tr_b16 a[236:237], v219 offset:4616\n"
"v_cvt_pk_bf16_f32 v70, v188, v189\n"
"v_mfma_f32_16x16x32_bf16 a[4:7], a[164:167], v[60:63], a[4:7]\n"
"s_waitcnt lgkmcnt(14)\n"
"ds_read_b64_tr_b16 a[238:239], v219 offset:5640\n"
"v_cvt_pk_bf16_f32 v71, v190, v191\n"
"v_mfma_f32_16x16x32_bf16 a[8:11], a[168:171], v[48:51], a[8:11]\n"
"s_add_u32 m0, s51, 32784\n"
"ds_read_b64_tr_b16 a[240:241], v219 offset:768\n"
"v_fma_mix_f32 v176, v72, v160, v168 op_sel:[0,0,0] op_sel_hi:[1,0,0]\n"
"v_mfma_f32_16x16x32_bf16 a[8:11], a[172:175], v[56:59], a[8:11]\n"
"ds_read_b64_tr_b16 a[242:243], v219 offset:1792\n"
"buffer_load_dwordx4 v217, s[24:27], s50 offen offset:0 lds\n"
"v_mfma_f32_16x16x32_bf16 a[12:15], a[168:171], v[52:55], a[12:15]\n"
"ds_read_b64_tr_b16 a[244:245], v219 offset:4864\n"
"v_fma_mix_f32 v177, v72, v161, v169 op_sel:[1,0,0] op_sel_hi:[1,0,0]\n"
"v_mfma_f32_16x16x32_bf16 a[12:15], a[172:175], v[60:63], a[12:15]\n"
"ds_read_b64_tr_b16 a[246:247], v219 offset:5888\n"
"v_fma_mix_f32 v178, v73, v162, v170 op_sel:[0,0,0] op_sel_hi:[1,0,0]\n"
"v_mfma_f32_16x16x32_bf16 a[16:19], a[176:179], v[48:51], a[16:19]\n"
"ds_read_b64_tr_b16 a[248:249], v219 offset:776\n"
"v_fma_mix_f32 v179, v73, v163, v171 op_sel:[1,0,0] op_sel_hi:[1,0,0]\n"
"v_mfma_f32_16x16x32_bf16 a[16:19], a[180:183], v[56:59], a[16:19]\n"
"ds_read_b64_tr_b16 a[250:251], v219 offset:1800\n"
"v_fma_mix_f32 v180, v74, v164, v172 op_sel:[0,0,0] op_sel_hi:[1,0,0]\n"
"v_mfma_f32_16x16x32_bf16 a[20:23], a[176:179], v[52:55], a[20:23]\n"
"s_add_u32 m0, s51, 40720\n"
"ds_read_b64_tr_b16 a[252:253], v219 offset:4872\n"
"v_fma_mix_f32 v181, v74, v165, v173 op_sel:[1,0,0] op_sel_hi:[1,0,0]\n"
"v_mfma_f32_16x16x32_bf16 a[20:23], a[180:183], v[60:63], a[20:23]\n"
"s_waitcnt lgkmcnt(11)\n"
"ds_read_b64_tr_b16 a[254:255], v219 offset:5896\n"
"buffer_load_dwordx4 v217, s[24:27], s50 offen offset:256 lds\n"
"v_mfma_f32_16x16x32_bf16 a[24:27], a[184:187], v[48:51], a[24:27]\n"
"s_add_u32 s50, s50, 32768\n"
"ds_read_b64_tr_b16 a[160:161], v219 offset:8192\n"
"v_fma_mix_f32 v182, v75, v166, v174 op_sel:[0,0,0] op_sel_hi:[1,0,0]\n"
"v_mfma_f32_16x16x32_bf16 a[24:27], a[188:191], v[56:59], a[24:27]\n"
"ds_read_b64_tr_b16 a[162:163], v219 offset:9216\n"
"v_fma_mix_f32 v183, v75, v167, v175 op_sel:[1,0,0] op_sel_hi:[1,0,0]\n"
"v_mfma_f32_16x16x32_bf16 a[28:31], a[184:187], v[52:55], a[28:31]\n"
"ds_read_b64_tr_b16 a[164:165], v219 offset:12288\n"
"v_cvt_pk_bf16_f32 v72, v176, v177\n"
"v_mfma_f32_16x16x32_bf16 a[28:31], a[188:191], v[60:63], a[28:31]\n"
"ds_read_b64_tr_b16 a[166:167], v219 offset:13312\n"
"v_cvt_pk_bf16_f32 v73, v178, v179\n"
"v_mfma_f32_16x16x32_bf16 a[32:35], a[224:227], v[48:51], a[32:35]\n"
"s_add_u32 m0, s51, 33808\n"
"ds_read_b64_tr_b16 a[168:169], v219 offset:8200\n"
"v_cvt_pk_bf16_f32 v74, v180, v181\n"
"v_mfma_f32_16x16x32_bf16 a[32:35], a[228:231], v[56:59], a[32:35]\n"
"ds_read_b64_tr_b16 a[170:171], v219 offset:9224\n"
"buffer_load_dwordx4 v217, s[24:27], s50 offen offset:0 lds\n"
"v_mfma_f32_16x16x32_bf16 a[36:39], a[224:227], v[52:55], a[36:39]\n"
"ds_read_b64_tr_b16 a[172:173], v219 offset:12296\n"
"v_cvt_pk_bf16_f32 v75, v182, v183\n"
"v_mfma_f32_16x16x32_bf16 a[36:39], a[228:231], v[60:63], a[36:39]\n"
"s_waitcnt lgkmcnt(11)\n"
"ds_read_b64_tr_b16 a[174:175], v219 offset:13320\n"
"v_fma_mix_f32 v184, v76, v160, v168 op_sel:[0,0,0] op_sel_hi:[1,0,0]\n"
"v_mfma_f32_16x16x32_bf16 a[40:43], a[232:235], v[48:51], a[40:43]\n"
"ds_read_b64_tr_b16 a[176:177], v219 offset:8448\n"
"v_fma_mix_f32 v185, v76, v161, v169 op_sel:[1,0,0] op_sel_hi:[1,0,0]\n"
"v_mfma_f32_16x16x32_bf16 a[40:43], a[236:239], v[56:59], a[40:43]\n"
"ds_read_b64_tr_b16 a[178:179], v219 offset:9472\n"
"v_fma_mix_f32 v186, v77, v162, v170 op_sel:[0,0,0] op_sel_hi:[1,0,0]\n"
"v_mfma_f32_16x16x32_bf16 a[44:47], a[232:235], v[52:55], a[44:47]\n"
"s_add_u32 m0, s51, 41744\n"
"ds_read_b64_tr_b16 a[180:181], v219 offset:12544\n"
"v_fma_mix_f32 v187, v77, v163, v171 op_sel:[1,0,0] op_sel_hi:[1,0,0]\n"
"v_mfma_f32_16x16x32_bf16 a[44:47], a[236:239], v[60:63], a[44:47]\n"
"ds_read_b64_tr_b16 a[182:183], v219 offset:13568\n"
"buffer_load_dwordx4 v217, s[24:27], s50 offen offset:256 lds\n"
"v_mfma_f32_16x16x32_bf16 a[48:51], a[240:243], v[48:51], a[48:51]\n"
"s_add_u32 s50, s50, 32768\n"
"ds_read_b64_tr_b16 a[184:185], v219 offset:8456\n"
"v_fma_mix_f32 v188, v78, v164, v172 op_sel:[0,0,0] op_sel_hi:[1,0,0]\n"
"v_mfma_f32_16x16x32_bf16 a[48:51], a[244:247], v[56:59], a[48:51]\n"
"ds_read_b64_tr_b16 a[186:187], v219 offset:9480\n"
"v_fma_mix_f32 v189, v78, v165, v173 op_sel:[1,0,0] op_sel_hi:[1,0,0]\n"
"v_mfma_f32_16x16x32_bf16 a[52:55], a[240:243], v[52:55], a[52:55]\n"
"ds_read_b64_tr_b16 a[188:189], v219 offset:12552\n"
"v_fma_mix_f32 v190, v79, v166, v174 op_sel:[0,0,0] op_sel_hi:[1,0,0]\n"
"v_mfma_f32_16x16x32_bf16 a[52:55], a[244:247], v[60:63], a[52:55]\n"
"s_waitcnt lgkmcnt(11)\n"
"ds_read_b64_tr_b16 a[190:191], v219 offset:13576\n"
"v_fma_mix_f32 v191, v79, v167, v175 op_sel:[1,0,0] op_sel_hi:[1,0,0]\n"
"v_mfma_f32_16x16x32_bf16 a[56:59], a[248:251], v[48:51], a[56:59]\n"
"s_add_u32 m0, s51, 49176\n"
"ds_read_b64_tr_b16 a[224:225], v219 offset:8704\n"
"ds_read_b128 v[160:163], v224 offset:2816\n"
"v_mfma_f32_16x16x32_bf16 a[56:59], a[252:255], v[56:59], a[56:59]\n"
"ds_read_b64_tr_b16 a[226:227], v219 offset:9728\n"
"buffer_load_dwordx4 v217, s[24:27], s50 offen offset:0 lds\n"
"v_mfma_f32_16x16x32_bf16 a[60:63], a[248:251], v[52:55], a[60:63]\n"
"ds_read_b64_tr_b16 a[228:229], v219 offset:12800\n"
"ds_read_b128 v[164:167], v224 offset:2880\n"
"v_mfma_f32_16x16x32_bf16 a[60:63], a[252:255], v[60:63], a[60:63]\n"
"ds_read_b64_tr_b16 a[230:231], v219 offset:13824\n"
"ds_read_b128 v[168:171], v224 offset:2944\n"
"v_mfma_f32_16x16x32_bf16 a[64:67], a[160:163], v[48:51], a[64:67]\n"
"ds_read_b64_tr_b16 a[232:233], v219 offset:8712\n"
"ds_read_b128 v[172:175], v224 offset:3008\n"
"v_mfma_f32_16x16x32_bf16 a[64:67], a[164:167], v[56:59], a[64:67]\n"
"ds_read_b64_tr_b16 a[234:235], v219 offset:9736\n"
"v_cvt_pk_bf16_f32 v76, v184, v185\n"
"v_mfma_f32_16x16x32_bf16 a[68:71], a[160:163], v[52:55], a[68:71]\n"
"s_add_u32 m0, s51, 57112\n"
"ds_read_b64_tr_b16 a[236:237], v219 offset:12808\n"
"v_cvt_pk_bf16_f32 v77, v186, v187\n"
"v_mfma_f32_16x16x32_bf16 a[68:71], a[164:167], v[60:63], a[68:71]\n"
"s_waitcnt lgkmcnt(14)\n"
"ds_read_b64_tr_b16 a[238:239], v219 offset:13832\n"
"buffer_load_dwordx4 v217, s[24:27], s50 offen offset:256 lds\n"
"v_mfma_f32_16x16x32_bf16 a[72:75], a[168:171], v[48:51], a[72:75]\n"
"s_add_u32 s50, s50, 32768\n"
"ds_read_b64_tr_b16 a[240:241], v219 offset:8960\n"
"v_cvt_pk_bf16_f32 v78, v188, v189\n"
"v_mfma_f32_16x16x32_bf16 a[72:75], a[172:175], v[56:59], a[72:75]\n"
"ds_read_b64_tr_b16 a[242:243], v219 offset:9984\n"
"v_cvt_pk_bf16_f32 v79, v190, v191\n"
"v_mfma_f32_16x16x32_bf16 a[76:79], a[168:171], v[52:55], a[76:79]\n"
"ds_read_b64_tr_b16 a[244:245], v219 offset:13056\n"
"v_fma_mix_f32 v176, v80, v144, v152 op_sel:[0,0,0] op_sel_hi:[1,0,0]\n"
"v_mfma_f32_16x16x32_bf16 a[76:79], a[172:175], v[60:63], a[76:79]\n"
"ds_read_b64_tr_b16 a[246:247], v219 offset:14080\n"
"v_fma_mix_f32 v177, v80, v145, v153 op_sel:[1,0,0] op_sel_hi:[1,0,0]\n"
"v_mfma_f32_16x16x32_bf16 a[80:83], a[176:179], v[48:51], a[80:83]\n"
"s_add_u32 m0, s51, 50200\n"
"ds_read_b64_tr_b16 a[248:249], v219 offset:8968\n"
"v_fma_mix_f32 v178, v81, v146, v154 op_sel:[0,0,0] op_sel_hi:[1,0,0]\n"
"v_mfma_f32_16x16x32_bf16 a[80:83], a[180:183], v[56:59], a[80:83]\n"
"ds_read_b64_tr_b16 a[250:251], v219 offset:9992\n"
"buffer_load_dwordx4 v217, s[24:27], s50 offen offset:0 lds\n"
"v_mfma_f32_16x16x32_bf16 a[84:87], a[176:179], v[52:55], a[84:87]\n"
"ds_read_b64_tr_b16 a[252:253], v219 offset:13064\n"
"v_fma_mix_f32 v179, v81, v147, v155 op_sel:[1,0,0] op_sel_hi:[1,0,0]\n"
"v_mfma_f32_16x16x32_bf16 a[84:87], a[180:183], v[60:63], a[84:87]\n"
"s_waitcnt lgkmcnt(12)\n"
"ds_read_b64_tr_b16 a[254:255], v219 offset:14088\n"
"v_fma_mix_f32 v180, v82, v148, v156 op_sel:[0,0,0] op_sel_hi:[1,0,0]\n"
"v_mfma_f32_16x16x32_bf16 a[88:91], a[184:187], v[48:51], a[88:91]\n"
"ds_read_b64_tr_b16 a[160:161], v219 offset:32784\n"
"v_fma_mix_f32 v181, v82, v149, v157 op_sel:[1,0,0] op_sel_hi:[1,0,0]\n"
"v_mfma_f32_16x16x32_bf16 a[88:91], a[188:191], v[56:59], a[88:91]\n"
"ds_read_b64_tr_b16 a[162:163], v219 offset:33808\n"
"v_fma_mix_f32 v182, v83, v150, v158 op_sel:[0,0,0] op_sel_hi:[1,0,0]\n"
"v_mfma_f32_16x16x32_bf16 a[92:95], a[184:187], v[52:55], a[92:95]\n"
"s_add_u32 m0, s51, 58136\n"
"ds_read_b64_tr_b16 a[164:165], v219 offset:36880\n"
"v_fma_mix_f32 v183, v83, v151, v159 op_sel:[1,0,0] op_sel_hi:[1,0,0]\n"
"v_mfma_f32_16x16x32_bf16 a[92:95], a[188:191], v[60:63], a[92:95]\n"
"buffer_load_dwordx4 v217, s[24:27], s50 offen offset:256 lds\n"
"ds_read_b64_tr_b16 a[166:167], v219 offset:37904\n"
"v_mfma_f32_16x16x32_bf16 a[96:99], a[224:227], v[48:51], a[96:99]\n"
"s_add_u32 s50, s50, 425984\n"
"ds_read_b64_tr_b16 a[168:169], v219 offset:32792\n"
"v_cvt_pk_bf16_f32 v80, v176, v177\n"
"v_mfma_f32_16x16x32_bf16 a[96:99], a[228:231], v[56:59], a[96:99]\n"
"ds_read_b64_tr_b16 a[170:171], v219 offset:33816\n"
"v_cvt_pk_bf16_f32 v81, v178, v179\n"
"v_mfma_f32_16x16x32_bf16 a[100:103], a[224:227], v[52:55], a[100:103]\n"
"ds_read_b64_tr_b16 a[172:173], v219 offset:36888\n"
"v_cvt_pk_bf16_f32 v82, v180, v181\n"
"v_mfma_f32_16x16x32_bf16 a[100:103], a[228:231], v[60:63], a[100:103]\n"
"s_waitcnt lgkmcnt(11)\n"
"ds_read_b64_tr_b16 a[174:175], v219 offset:37912\n"
"v_cvt_pk_bf16_f32 v83, v182, v183\n"
"v_mfma_f32_16x16x32_bf16 a[104:107], a[232:235], v[48:51], a[104:107]\n"
"ds_read_b64_tr_b16 a[176:177], v219 offset:33040\n"
"v_fma_mix_f32 v184, v84, v144, v152 op_sel:[0,0,0] op_sel_hi:[1,0,0]\n"
"v_mfma_f32_16x16x32_bf16 a[104:107], a[236:239], v[56:59], a[104:107]\n"
"ds_read_b64_tr_b16 a[178:179], v219 offset:34064\n"
"v_fma_mix_f32 v185, v84, v145, v153 op_sel:[1,0,0] op_sel_hi:[1,0,0]\n"
"v_mfma_f32_16x16x32_bf16 a[108:111], a[232:235], v[52:55], a[108:111]\n"
"ds_read_b64_tr_b16 a[180:181], v219 offset:37136\n"
"v_fma_mix_f32 v186, v85, v146, v154 op_sel:[0,0,0] op_sel_hi:[1,0,0]\n"
"v_mfma_f32_16x16x32_bf16 a[108:111], a[236:239], v[60:63], a[108:111]\n"
"ds_read_b64_tr_b16 a[182:183], v219 offset:38160\n"
"v_fma_mix_f32 v187, v85, v147, v155 op_sel:[1,0,0] op_sel_hi:[1,0,0]\n"
"v_mfma_f32_16x16x32_bf16 a[112:115], a[240:243], v[48:51], a[112:115]\n"
"ds_read_b64_tr_b16 a[184:185], v219 offset:33048\n"
"v_fma_mix_f32 v188, v86, v148, v156 op_sel:[0,0,0] op_sel_hi:[1,0,0]\n"
"v_mfma_f32_16x16x32_bf16 a[112:115], a[244:247], v[56:59], a[112:115]\n"
"ds_read_b64_tr_b16 a[186:187], v219 offset:34072\n"
"v_fma_mix_f32 v189, v86, v149, v157 op_sel:[1,0,0] op_sel_hi:[1,0,0]\n"
"v_mfma_f32_16x16x32_bf16 a[116:119], a[240:243], v[52:55], a[116:119]\n"
"ds_read_b64_tr_b16 a[188:189], v219 offset:37144\n"
"v_fma_mix_f32 v190, v87, v150, v158 op_sel:[0,0,0] op_sel_hi:[1,0,0]\n"
"v_mfma_f32_16x16x32_bf16 a[116:119], a[244:247], v[60:63], a[116:119]\n"
"s_waitcnt lgkmcnt(11)\n"
"ds_read_b64_tr_b16 a[190:191], v219 offset:38168\n"
"v_fma_mix_f32 v191, v87, v151, v159 op_sel:[1,0,0] op_sel_hi:[1,0,0]\n"
"v_mfma_f32_16x16x32_bf16 a[120:123], a[248:251], v[48:51], a[120:123]\n"
"ds_read_b64_tr_b16 a[224:225], v219 offset:33296\n"
"ds_read_b128 v[144:147], v224 offset:3072\n"
"v_mfma_f32_16x16x32_bf16 a[120:123], a[252:255], v[56:59], a[120:123]\n"
"ds_read_b64_tr_b16 a[226:227], v219 offset:34320\n"
"ds_read_b128 v[148:151], v224 offset:3136\n"
"v_mfma_f32_16x16x32_bf16 a[124:127], a[248:251], v[52:55], a[124:127]\n"
"ds_read_b64_tr_b16 a[228:229], v219 offset:37392\n"
"ds_read_b128 v[152:155], v224 offset:3200\n"
"v_mfma_f32_16x16x32_bf16 a[124:127], a[252:255], v[60:63], a[124:127]\n"
"ds_read_b64_tr_b16 a[230:231], v219 offset:38416\n"
"ds_read_b128 v[156:159], v224 offset:3264\n"
"v_mfma_f32_16x16x32_bf16 a[0:3], a[160:163], v[64:67], a[0:3]\n"
"ds_read_b64_tr_b16 a[232:233], v219 offset:33304\n"
"v_cvt_pk_bf16_f32 v84, v184, v185\n"
"v_mfma_f32_16x16x32_bf16 a[0:3], a[164:167], v[72:75], a[0:3]\n"
"ds_read_b64_tr_b16 a[234:235], v219 offset:34328\n"
"v_cvt_pk_bf16_f32 v85, v186, v187\n"
"v_mfma_f32_16x16x32_bf16 a[4:7], a[160:163], v[68:71], a[4:7]\n"
"s_waitcnt vmcnt(8)\n"
"s_barrier\n"
"ds_read_b64_tr_b16 a[236:237], v219 offset:37400\n"
"v_cvt_pk_bf16_f32 v86, v188, v189\n"
"v_mfma_f32_16x16x32_bf16 a[4:7], a[164:167], v[76:79], a[4:7]\n"
"s_waitcnt lgkmcnt(14)\n"
"ds_read_b64_tr_b16 a[238:239], v219 offset:38424\n"
"v_cvt_pk_bf16_f32 v87, v190, v191\n"
"v_mfma_f32_16x16x32_bf16 a[8:11], a[168:171], v[64:67], a[8:11]\n"
"s_add_u32 m0, s51, 65568\n"
"ds_read_b64_tr_b16 a[240:241], v219 offset:33552\n"
"v_fma_mix_f32 v176, v88, v160, v168 op_sel:[0,0,0] op_sel_hi:[1,0,0]\n"
"v_mfma_f32_16x16x32_bf16 a[8:11], a[172:175], v[72:75], a[8:11]\n"
"ds_read_b64_tr_b16 a[242:243], v219 offset:34576\n"
"buffer_load_dwordx4 v217, s[24:27], s50 offen offset:0 lds\n"
"v_mfma_f32_16x16x32_bf16 a[12:15], a[168:171], v[68:71], a[12:15]\n"
"ds_read_b64_tr_b16 a[244:245], v219 offset:37648\n"
"v_fma_mix_f32 v177, v88, v161, v169 op_sel:[1,0,0] op_sel_hi:[1,0,0]\n"
"v_mfma_f32_16x16x32_bf16 a[12:15], a[172:175], v[76:79], a[12:15]\n"
"ds_read_b64_tr_b16 a[246:247], v219 offset:38672\n"
"v_fma_mix_f32 v178, v89, v162, v170 op_sel:[0,0,0] op_sel_hi:[1,0,0]\n"
"v_mfma_f32_16x16x32_bf16 a[16:19], a[176:179], v[64:67], a[16:19]\n"
"ds_read_b64_tr_b16 a[248:249], v219 offset:33560\n"
"v_fma_mix_f32 v179, v89, v163, v171 op_sel:[1,0,0] op_sel_hi:[1,0,0]\n"
"v_mfma_f32_16x16x32_bf16 a[16:19], a[180:183], v[72:75], a[16:19]\n"
"ds_read_b64_tr_b16 a[250:251], v219 offset:34584\n"
"v_fma_mix_f32 v180, v90, v164, v172 op_sel:[0,0,0] op_sel_hi:[1,0,0]\n"
"v_mfma_f32_16x16x32_bf16 a[20:23], a[176:179], v[68:71], a[20:23]\n"
"s_add_u32 m0, s51, 73504\n"
"ds_read_b64_tr_b16 a[252:253], v219 offset:37656\n"
"v_fma_mix_f32 v181, v90, v165, v173 op_sel:[1,0,0] op_sel_hi:[1,0,0]\n"
"v_mfma_f32_16x16x32_bf16 a[20:23], a[180:183], v[76:79], a[20:23]\n"
"s_waitcnt lgkmcnt(11)\n"
"ds_read_b64_tr_b16 a[254:255], v219 offset:38680\n"
"buffer_load_dwordx4 v217, s[24:27], s50 offen offset:256 lds\n"
"v_mfma_f32_16x16x32_bf16 a[24:27], a[184:187], v[64:67], a[24:27]\n"
"s_add_u32 s50, s50, 32768\n"
"ds_read_b64_tr_b16 a[160:161], v219 offset:40976\n"
"v_fma_mix_f32 v182, v91, v166, v174 op_sel:[0,0,0] op_sel_hi:[1,0,0]\n"
"v_mfma_f32_16x16x32_bf16 a[24:27], a[188:191], v[72:75], a[24:27]\n"
"ds_read_b64_tr_b16 a[162:163], v219 offset:42000\n"
"v_fma_mix_f32 v183, v91, v167, v175 op_sel:[1,0,0] op_sel_hi:[1,0,0]\n"
"v_mfma_f32_16x16x32_bf16 a[28:31], a[184:187], v[68:71], a[28:31]\n"
"ds_read_b64_tr_b16 a[164:165], v219 offset:45072\n"
"v_cvt_pk_bf16_f32 v88, v176, v177\n"
"v_mfma_f32_16x16x32_bf16 a[28:31], a[188:191], v[76:79], a[28:31]\n"
"ds_read_b64_tr_b16 a[166:167], v219 offset:46096\n"
"v_cvt_pk_bf16_f32 v89, v178, v179\n"
"v_mfma_f32_16x16x32_bf16 a[32:35], a[224:227], v[64:67], a[32:35]\n"
"s_add_u32 m0, s51, 66592\n"
"ds_read_b64_tr_b16 a[168:169], v219 offset:40984\n"
"v_cvt_pk_bf16_f32 v90, v180, v181\n"
"v_mfma_f32_16x16x32_bf16 a[32:35], a[228:231], v[72:75], a[32:35]\n"
"ds_read_b64_tr_b16 a[170:171], v219 offset:42008\n"
"buffer_load_dwordx4 v217, s[24:27], s50 offen offset:0 lds\n"
"v_mfma_f32_16x16x32_bf16 a[36:39], a[224:227], v[68:71], a[36:39]\n"
"ds_read_b64_tr_b16 a[172:173], v219 offset:45080\n"
"v_cvt_pk_bf16_f32 v91, v182, v183\n"
"v_mfma_f32_16x16x32_bf16 a[36:39], a[228:231], v[76:79], a[36:39]\n"
"s_waitcnt lgkmcnt(11)\n"
"ds_read_b64_tr_b16 a[174:175], v219 offset:46104\n"
"v_fma_mix_f32 v184, v92, v160, v168 op_sel:[0,0,0] op_sel_hi:[1,0,0]\n"
"v_mfma_f32_16x16x32_bf16 a[40:43], a[232:235], v[64:67], a[40:43]\n"
"ds_read_b64_tr_b16 a[176:177], v219 offset:41232\n"
"v_fma_mix_f32 v185, v92, v161, v169 op_sel:[1,0,0] op_sel_hi:[1,0,0]\n"
"v_mfma_f32_16x16x32_bf16 a[40:43], a[236:239], v[72:75], a[40:43]\n"
"ds_read_b64_tr_b16 a[178:179], v219 offset:42256\n"
"v_fma_mix_f32 v186, v93, v162, v170 op_sel:[0,0,0] op_sel_hi:[1,0,0]\n"
"v_mfma_f32_16x16x32_bf16 a[44:47], a[232:235], v[68:71], a[44:47]\n"
"s_add_u32 m0, s51, 74528\n"
"ds_read_b64_tr_b16 a[180:181], v219 offset:45328\n"
"v_fma_mix_f32 v187, v93, v163, v171 op_sel:[1,0,0] op_sel_hi:[1,0,0]\n"
"v_mfma_f32_16x16x32_bf16 a[44:47], a[236:239], v[76:79], a[44:47]\n"
"ds_read_b64_tr_b16 a[182:183], v219 offset:46352\n"
"buffer_load_dwordx4 v217, s[24:27], s50 offen offset:256 lds\n"
"v_mfma_f32_16x16x32_bf16 a[48:51], a[240:243], v[64:67], a[48:51]\n"
"s_add_u32 s50, s50, 32768\n"
"ds_read_b64_tr_b16 a[184:185], v219 offset:41240\n"
"v_fma_mix_f32 v188, v94, v164, v172 op_sel:[0,0,0] op_sel_hi:[1,0,0]\n"
"v_mfma_f32_16x16x32_bf16 a[48:51], a[244:247], v[72:75], a[48:51]\n"
"ds_read_b64_tr_b16 a[186:187], v219 offset:42264\n"
"v_fma_mix_f32 v189, v94, v165, v173 op_sel:[1,0,0] op_sel_hi:[1,0,0]\n"
"v_mfma_f32_16x16x32_bf16 a[52:55], a[240:243], v[68:71], a[52:55]\n"
"ds_read_b64_tr_b16 a[188:189], v219 offset:45336\n"
"v_fma_mix_f32 v190, v95, v166, v174 op_sel:[0,0,0] op_sel_hi:[1,0,0]\n"
"v_mfma_f32_16x16x32_bf16 a[52:55], a[244:247], v[76:79], a[52:55]\n"
"s_waitcnt lgkmcnt(11)\n"
"ds_read_b64_tr_b16 a[190:191], v219 offset:46360\n"
"v_fma_mix_f32 v191, v95, v167, v175 op_sel:[1,0,0] op_sel_hi:[1,0,0]\n"
"v_mfma_f32_16x16x32_bf16 a[56:59], a[248:251], v[64:67], a[56:59]\n"
"s_add_u32 m0, s51, 81960\n"
"ds_read_b64_tr_b16 a[224:225], v219 offset:41488\n"
"ds_read_b128 v[160:163], v224 offset:3328\n"
"v_mfma_f32_16x16x32_bf16 a[56:59], a[252:255], v[72:75], a[56:59]\n"
"ds_read_b64_tr_b16 a[226:227], v219 offset:42512\n"
"buffer_load_dwordx4 v217, s[24:27], s50 offen offset:0 lds\n"
"v_mfma_f32_16x16x32_bf16 a[60:63], a[248:251], v[68:71], a[60:63]\n"
"ds_read_b64_tr_b16 a[228:229], v219 offset:45584\n"
"ds_read_b128 v[164:167], v224 offset:3392\n"
"v_mfma_f32_16x16x32_bf16 a[60:63], a[252:255], v[76:79], a[60:63]\n"
"ds_read_b64_tr_b16 a[230:231], v219 offset:46608\n"
"ds_read_b128 v[168:171], v224 offset:3456\n"
"v_mfma_f32_16x16x32_bf16 a[64:67], a[160:163], v[64:67], a[64:67]\n"
"ds_read_b64_tr_b16 a[232:233], v219 offset:41496\n"
"ds_read_b128 v[172:175], v224 offset:3520\n"
"v_mfma_f32_16x16x32_bf16 a[64:67], a[164:167], v[72:75], a[64:67]\n"
"ds_read_b64_tr_b16 a[234:235], v219 offset:42520\n"
"v_cvt_pk_bf16_f32 v92, v184, v185\n"
"v_mfma_f32_16x16x32_bf16 a[68:71], a[160:163], v[68:71], a[68:71]\n"
"s_add_u32 m0, s51, 89896\n"
"ds_read_b64_tr_b16 a[236:237], v219 offset:45592\n"
"v_cvt_pk_bf16_f32 v93, v186, v187\n"
"v_mfma_f32_16x16x32_bf16 a[68:71], a[164:167], v[76:79], a[68:71]\n"
"s_waitcnt lgkmcnt(14)\n"
"ds_read_b64_tr_b16 a[238:239], v219 offset:46616\n"
"buffer_load_dwordx4 v217, s[24:27], s50 offen offset:256 lds\n"
"v_mfma_f32_16x16x32_bf16 a[72:75], a[168:171], v[64:67], a[72:75]\n"
"s_add_u32 s50, s50, 32768\n"
"ds_read_b64_tr_b16 a[240:241], v219 offset:41744\n"
"v_cvt_pk_bf16_f32 v94, v188, v189\n"
"v_mfma_f32_16x16x32_bf16 a[72:75], a[172:175], v[72:75], a[72:75]\n"
"ds_read_b64_tr_b16 a[242:243], v219 offset:42768\n"
"v_cvt_pk_bf16_f32 v95, v190, v191\n"
"v_mfma_f32_16x16x32_bf16 a[76:79], a[168:171], v[68:71], a[76:79]\n"
"ds_read_b64_tr_b16 a[244:245], v219 offset:45840\n"
"v_fma_mix_f32 v176, v96, v144, v152 op_sel:[0,0,0] op_sel_hi:[1,0,0]\n"
"v_mfma_f32_16x16x32_bf16 a[76:79], a[172:175], v[76:79], a[76:79]\n"
"ds_read_b64_tr_b16 a[246:247], v219 offset:46864\n"
"v_fma_mix_f32 v177, v96, v145, v153 op_sel:[1,0,0] op_sel_hi:[1,0,0]\n"
"v_mfma_f32_16x16x32_bf16 a[80:83], a[176:179], v[64:67], a[80:83]\n"
"s_add_u32 m0, s51, 82984\n"
"ds_read_b64_tr_b16 a[248:249], v219 offset:41752\n"
"v_fma_mix_f32 v178, v97, v146, v154 op_sel:[0,0,0] op_sel_hi:[1,0,0]\n"
"v_mfma_f32_16x16x32_bf16 a[80:83], a[180:183], v[72:75], a[80:83]\n"
"ds_read_b64_tr_b16 a[250:251], v219 offset:42776\n"
"buffer_load_dwordx4 v217, s[24:27], s50 offen offset:0 lds\n"
"v_mfma_f32_16x16x32_bf16 a[84:87], a[176:179], v[68:71], a[84:87]\n"
"ds_read_b64_tr_b16 a[252:253], v219 offset:45848\n"
"v_fma_mix_f32 v179, v97, v147, v155 op_sel:[1,0,0] op_sel_hi:[1,0,0]\n"
"v_mfma_f32_16x16x32_bf16 a[84:87], a[180:183], v[76:79], a[84:87]\n"
"s_waitcnt lgkmcnt(12)\n"
"ds_read_b64_tr_b16 a[254:255], v219 offset:46872\n"
"v_fma_mix_f32 v180, v98, v148, v156 op_sel:[0,0,0] op_sel_hi:[1,0,0]\n"
"v_mfma_f32_16x16x32_bf16 a[88:91], a[184:187], v[64:67], a[88:91]\n"
"ds_read_b64_tr_b16 a[160:161], v218 offset:0\n"
"v_fma_mix_f32 v181, v98, v149, v157 op_sel:[1,0,0] op_sel_hi:[1,0,0]\n"
"v_mfma_f32_16x16x32_bf16 a[88:91], a[188:191], v[72:75], a[88:91]\n"
"ds_read_b64_tr_b16 a[162:163], v218 offset:1024\n"
"v_fma_mix_f32 v182, v99, v150, v158 op_sel:[0,0,0] op_sel_hi:[1,0,0]\n"
"v_mfma_f32_16x16x32_bf16 a[92:95], a[184:187], v[68:71], a[92:95]\n"
"s_add_u32 m0, s51, 90920\n"
"ds_read_b64_tr_b16 a[164:165], v218 offset:4096\n"
"v_fma_mix_f32 v183, v99, v151, v159 op_sel:[1,0,0] op_sel_hi:[1,0,0]\n"
"v_mfma_f32_16x16x32_bf16 a[92:95], a[188:191], v[76:79], a[92:95]\n"
"buffer_load_dwordx4 v217, s[24:27], s50 offen offset:256 lds\n"
"ds_read_b64_tr_b16 a[166:167], v218 offset:5120\n"
"v_mfma_f32_16x16x32_bf16 a[96:99], a[224:227], v[64:67], a[96:99]\n"
"s_add_u32 s50, s50, 425984\n"
"ds_read_b64_tr_b16 a[168:169], v218 offset:8\n"
"v_cvt_pk_bf16_f32 v96, v176, v177\n"
"v_mfma_f32_16x16x32_bf16 a[96:99], a[228:231], v[72:75], a[96:99]\n"
"ds_read_b64_tr_b16 a[170:171], v218 offset:1032\n"
"v_cvt_pk_bf16_f32 v97, v178, v179\n"
"v_mfma_f32_16x16x32_bf16 a[100:103], a[224:227], v[68:71], a[100:103]\n"
"ds_read_b64_tr_b16 a[172:173], v218 offset:4104\n"
"v_cvt_pk_bf16_f32 v98, v180, v181\n"
"v_mfma_f32_16x16x32_bf16 a[100:103], a[228:231], v[76:79], a[100:103]\n"
"s_waitcnt lgkmcnt(11)\n"
"ds_read_b64_tr_b16 a[174:175], v218 offset:5128\n"
"v_cvt_pk_bf16_f32 v99, v182, v183\n"
"v_mfma_f32_16x16x32_bf16 a[104:107], a[232:235], v[64:67], a[104:107]\n"
"ds_read_b64_tr_b16 a[176:177], v218 offset:256\n"
"v_fma_mix_f32 v184, v100, v144, v152 op_sel:[0,0,0] op_sel_hi:[1,0,0]\n"
"v_mfma_f32_16x16x32_bf16 a[104:107], a[236:239], v[72:75], a[104:107]\n"
"ds_read_b64_tr_b16 a[178:179], v218 offset:1280\n"
"v_fma_mix_f32 v185, v100, v145, v153 op_sel:[1,0,0] op_sel_hi:[1,0,0]\n"
"v_mfma_f32_16x16x32_bf16 a[108:111], a[232:235], v[68:71], a[108:111]\n"
"ds_read_b64_tr_b16 a[180:181], v218 offset:4352\n"
"v_fma_mix_f32 v186, v101, v146, v154 op_sel:[0,0,0] op_sel_hi:[1,0,0]\n"
"v_mfma_f32_16x16x32_bf16 a[108:111], a[236:239], v[76:79], a[108:111]\n"
"ds_read_b64_tr_b16 a[182:183], v218 offset:5376\n"
"v_fma_mix_f32 v187, v101, v147, v155 op_sel:[1,0,0] op_sel_hi:[1,0,0]\n"
"v_mfma_f32_16x16x32_bf16 a[112:115], a[240:243], v[64:67], a[112:115]\n"
"ds_read_b64_tr_b16 a[184:185], v218 offset:264\n"
"v_fma_mix_f32 v188, v102, v148, v156 op_sel:[0,0,0] op_sel_hi:[1,0,0]\n"
"v_mfma_f32_16x16x32_bf16 a[112:115], a[244:247], v[72:75], a[112:115]\n"
"ds_read_b64_tr_b16 a[186:187], v218 offset:1288\n"
"v_fma_mix_f32 v189, v102, v149, v157 op_sel:[1,0,0] op_sel_hi:[1,0,0]\n"
"v_mfma_f32_16x16x32_bf16 a[116:119], a[240:243], v[68:71], a[116:119]\n"
"ds_read_b64_tr_b16 a[188:189], v218 offset:4360\n"
"v_fma_mix_f32 v190, v103, v150, v158 op_sel:[0,0,0] op_sel_hi:[1,0,0]\n"
"v_mfma_f32_16x16x32_bf16 a[116:119], a[244:247], v[76:79], a[116:119]\n"
"s_waitcnt lgkmcnt(11)\n"
"ds_read_b64_tr_b16 a[190:191], v218 offset:5384\n"
"v_fma_mix_f32 v191, v103, v151, v159 op_sel:[1,0,0] op_sel_hi:[1,0,0]\n"
"v_mfma_f32_16x16x32_bf16 a[120:123], a[248:251], v[64:67], a[120:123]\n"
"ds_read_b64_tr_b16 a[224:225], v218 offset:512\n"
"ds_read_b128 v[144:147], v224 offset:3584\n"
"v_mfma_f32_16x16x32_bf16 a[120:123], a[252:255], v[72:75], a[120:123]\n"
"ds_read_b64_tr_b16 a[226:227], v218 offset:1536\n"
"ds_read_b128 v[148:151], v224 offset:3648\n"
"v_mfma_f32_16x16x32_bf16 a[124:127], a[248:251], v[68:71], a[124:127]\n"
"ds_read_b64_tr_b16 a[228:229], v218 offset:4608\n"
"ds_read_b128 v[152:155], v224 offset:3712\n"
"v_mfma_f32_16x16x32_bf16 a[124:127], a[252:255], v[76:79], a[124:127]\n"
"ds_read_b64_tr_b16 a[230:231], v218 offset:5632\n"
"ds_read_b128 v[156:159], v224 offset:3776\n"
"v_mfma_f32_16x16x32_bf16 a[0:3], a[160:163], v[80:83], a[0:3]\n"
"ds_read_b64_tr_b16 a[232:233], v218 offset:520\n"
"v_cvt_pk_bf16_f32 v100, v184, v185\n"
"v_mfma_f32_16x16x32_bf16 a[0:3], a[164:167], v[88:91], a[0:3]\n"
"ds_read_b64_tr_b16 a[234:235], v218 offset:1544\n"
"v_cvt_pk_bf16_f32 v101, v186, v187\n"
"v_mfma_f32_16x16x32_bf16 a[4:7], a[160:163], v[84:87], a[4:7]\n"
"s_waitcnt vmcnt(8)\n"
"s_barrier\n"
"ds_read_b64_tr_b16 a[236:237], v218 offset:4616\n"
"v_cvt_pk_bf16_f32 v102, v188, v189\n"
"v_mfma_f32_16x16x32_bf16 a[4:7], a[164:167], v[92:95], a[4:7]\n"
"s_waitcnt lgkmcnt(14)\n"
"ds_read_b64_tr_b16 a[238:239], v218 offset:5640\n"
"v_cvt_pk_bf16_f32 v103, v190, v191\n"
"v_mfma_f32_16x16x32_bf16 a[8:11], a[168:171], v[80:83], a[8:11]\n"
"s_add_u32 m0, s51, 98352\n"
"ds_read_b64_tr_b16 a[240:241], v218 offset:768\n"
"v_fma_mix_f32 v176, v104, v160, v168 op_sel:[0,0,0] op_sel_hi:[1,0,0]\n"
"v_mfma_f32_16x16x32_bf16 a[8:11], a[172:175], v[88:91], a[8:11]\n"
"ds_read_b64_tr_b16 a[242:243], v218 offset:1792\n"
"buffer_load_dwordx4 v217, s[24:27], s50 offen offset:0 lds\n"
"v_mfma_f32_16x16x32_bf16 a[12:15], a[168:171], v[84:87], a[12:15]\n"
"ds_read_b64_tr_b16 a[244:245], v218 offset:4864\n"
"v_fma_mix_f32 v177, v104, v161, v169 op_sel:[1,0,0] op_sel_hi:[1,0,0]\n"
"v_mfma_f32_16x16x32_bf16 a[12:15], a[172:175], v[92:95], a[12:15]\n"
"ds_read_b64_tr_b16 a[246:247], v218 offset:5888\n"
"v_fma_mix_f32 v178, v105, v162, v170 op_sel:[0,0,0] op_sel_hi:[1,0,0]\n"
"v_mfma_f32_16x16x32_bf16 a[16:19], a[176:179], v[80:83], a[16:19]\n"
"ds_read_b64_tr_b16 a[248:249], v218 offset:776\n"
"v_fma_mix_f32 v179, v105, v163, v171 op_sel:[1,0,0] op_sel_hi:[1,0,0]\n"
"v_mfma_f32_16x16x32_bf16 a[16:19], a[180:183], v[88:91], a[16:19]\n"
"ds_read_b64_tr_b16 a[250:251], v218 offset:1800\n"
"v_fma_mix_f32 v180, v106, v164, v172 op_sel:[0,0,0] op_sel_hi:[1,0,0]\n"
"v_mfma_f32_16x16x32_bf16 a[20:23], a[176:179], v[84:87], a[20:23]\n"
"s_add_u32 m0, s51, 106288\n"
"ds_read_b64_tr_b16 a[252:253], v218 offset:4872\n"
"v_fma_mix_f32 v181, v106, v165, v173 op_sel:[1,0,0] op_sel_hi:[1,0,0]\n"
"v_mfma_f32_16x16x32_bf16 a[20:23], a[180:183], v[92:95], a[20:23]\n"
"s_waitcnt lgkmcnt(11)\n"
"ds_read_b64_tr_b16 a[254:255], v218 offset:5896\n"
"buffer_load_dwordx4 v217, s[24:27], s50 offen offset:256 lds\n"
"v_mfma_f32_16x16x32_bf16 a[24:27], a[184:187], v[80:83], a[24:27]\n"
"s_add_u32 s50, s50, 32768\n"
"ds_read_b64_tr_b16 a[160:161], v218 offset:8192\n"
"v_fma_mix_f32 v182, v107, v166, v174 op_sel:[0,0,0] op_sel_hi:[1,0,0]\n"
"v_mfma_f32_16x16x32_bf16 a[24:27], a[188:191], v[88:91], a[24:27]\n"
"ds_read_b64_tr_b16 a[162:163], v218 offset:9216\n"
"v_fma_mix_f32 v183, v107, v167, v175 op_sel:[1,0,0] op_sel_hi:[1,0,0]\n"
"v_mfma_f32_16x16x32_bf16 a[28:31], a[184:187], v[84:87], a[28:31]\n"
"ds_read_b64_tr_b16 a[164:165], v218 offset:12288\n"
"v_cvt_pk_bf16_f32 v104, v176, v177\n"
"v_mfma_f32_16x16x32_bf16 a[28:31], a[188:191], v[92:95], a[28:31]\n"
"ds_read_b64_tr_b16 a[166:167], v218 offset:13312\n"
"v_cvt_pk_bf16_f32 v105, v178, v179\n"
"v_mfma_f32_16x16x32_bf16 a[32:35], a[224:227], v[80:83], a[32:35]\n"
"s_add_u32 m0, s51, 99376\n"
"ds_read_b64_tr_b16 a[168:169], v218 offset:8200\n"
"v_cvt_pk_bf16_f32 v106, v180, v181\n"
"v_mfma_f32_16x16x32_bf16 a[32:35], a[228:231], v[88:91], a[32:35]\n"
"ds_read_b64_tr_b16 a[170:171], v218 offset:9224\n"
"buffer_load_dwordx4 v217, s[24:27], s50 offen offset:0 lds\n"
"v_mfma_f32_16x16x32_bf16 a[36:39], a[224:227], v[84:87], a[36:39]\n"
"ds_read_b64_tr_b16 a[172:173], v218 offset:12296\n"
"v_cvt_pk_bf16_f32 v107, v182, v183\n"
"v_mfma_f32_16x16x32_bf16 a[36:39], a[228:231], v[92:95], a[36:39]\n"
"s_waitcnt lgkmcnt(11)\n"
"ds_read_b64_tr_b16 a[174:175], v218 offset:13320\n"
"v_fma_mix_f32 v184, v108, v160, v168 op_sel:[0,0,0] op_sel_hi:[1,0,0]\n"
"v_mfma_f32_16x16x32_bf16 a[40:43], a[232:235], v[80:83], a[40:43]\n"
"ds_read_b64_tr_b16 a[176:177], v218 offset:8448\n"
"v_fma_mix_f32 v185, v108, v161, v169 op_sel:[1,0,0] op_sel_hi:[1,0,0]\n"
"v_mfma_f32_16x16x32_bf16 a[40:43], a[236:239], v[88:91], a[40:43]\n"
"ds_read_b64_tr_b16 a[178:179], v218 offset:9472\n"
"v_fma_mix_f32 v186, v109, v162, v170 op_sel:[0,0,0] op_sel_hi:[1,0,0]\n"
"v_mfma_f32_16x16x32_bf16 a[44:47], a[232:235], v[84:87], a[44:47]\n"
"s_add_u32 m0, s51, 107312\n"
"ds_read_b64_tr_b16 a[180:181], v218 offset:12544\n"
"v_fma_mix_f32 v187, v109, v163, v171 op_sel:[1,0,0] op_sel_hi:[1,0,0]\n"
"v_mfma_f32_16x16x32_bf16 a[44:47], a[236:239], v[92:95], a[44:47]\n"
"ds_read_b64_tr_b16 a[182:183], v218 offset:13568\n"
"buffer_load_dwordx4 v217, s[24:27], s50 offen offset:256 lds\n"
"v_mfma_f32_16x16x32_bf16 a[48:51], a[240:243], v[80:83], a[48:51]\n"
"s_add_u32 s50, s50, 32768\n"
"ds_read_b64_tr_b16 a[184:185], v218 offset:8456\n"
"v_fma_mix_f32 v188, v110, v164, v172 op_sel:[0,0,0] op_sel_hi:[1,0,0]\n"
"v_mfma_f32_16x16x32_bf16 a[48:51], a[244:247], v[88:91], a[48:51]\n"
"ds_read_b64_tr_b16 a[186:187], v218 offset:9480\n"
"v_fma_mix_f32 v189, v110, v165, v173 op_sel:[1,0,0] op_sel_hi:[1,0,0]\n"
"v_mfma_f32_16x16x32_bf16 a[52:55], a[240:243], v[84:87], a[52:55]\n"
"ds_read_b64_tr_b16 a[188:189], v218 offset:12552\n"
"v_fma_mix_f32 v190, v111, v166, v174 op_sel:[0,0,0] op_sel_hi:[1,0,0]\n"
"v_mfma_f32_16x16x32_bf16 a[52:55], a[244:247], v[92:95], a[52:55]\n"
"s_waitcnt lgkmcnt(11)\n"
"ds_read_b64_tr_b16 a[190:191], v218 offset:13576\n"
"v_fma_mix_f32 v191, v111, v167, v175 op_sel:[1,0,0] op_sel_hi:[1,0,0]\n"
"v_mfma_f32_16x16x32_bf16 a[56:59], a[248:251], v[80:83], a[56:59]\n"
"s_add_u32 m0, s51, 114744\n"
"ds_read_b64_tr_b16 a[224:225], v218 offset:8704\n"
"ds_read_b128 v[160:163], v224 offset:3840\n"
"v_mfma_f32_16x16x32_bf16 a[56:59], a[252:255], v[88:91], a[56:59]\n"
"ds_read_b64_tr_b16 a[226:227], v218 offset:9728\n"
"buffer_load_dwordx4 v217, s[24:27], s50 offen offset:0 lds\n"
"v_mfma_f32_16x16x32_bf16 a[60:63], a[248:251], v[84:87], a[60:63]\n"
"ds_read_b64_tr_b16 a[228:229], v218 offset:12800\n"
"ds_read_b128 v[164:167], v224 offset:3904\n"
"v_mfma_f32_16x16x32_bf16 a[60:63], a[252:255], v[92:95], a[60:63]\n"
"ds_read_b64_tr_b16 a[230:231], v218 offset:13824\n"
"ds_read_b128 v[168:171], v224 offset:3968\n"
"v_mfma_f32_16x16x32_bf16 a[64:67], a[160:163], v[80:83], a[64:67]\n"
"ds_read_b64_tr_b16 a[232:233], v218 offset:8712\n"
"ds_read_b128 v[172:175], v224 offset:4032\n"
"v_mfma_f32_16x16x32_bf16 a[64:67], a[164:167], v[88:91], a[64:67]\n"
"ds_read_b64_tr_b16 a[234:235], v218 offset:9736\n"
"v_cvt_pk_bf16_f32 v108, v184, v185\n"
"v_mfma_f32_16x16x32_bf16 a[68:71], a[160:163], v[84:87], a[68:71]\n"
"s_add_u32 m0, s51, 122680\n"
"ds_read_b64_tr_b16 a[236:237], v218 offset:12808\n"
"v_cvt_pk_bf16_f32 v109, v186, v187\n"
"v_mfma_f32_16x16x32_bf16 a[68:71], a[164:167], v[92:95], a[68:71]\n"
"s_waitcnt lgkmcnt(14)\n"
"ds_read_b64_tr_b16 a[238:239], v218 offset:13832\n"
"buffer_load_dwordx4 v217, s[24:27], s50 offen offset:256 lds\n"
"v_mfma_f32_16x16x32_bf16 a[72:75], a[168:171], v[80:83], a[72:75]\n"
"s_add_u32 s50, s50, 32768\n"
"ds_read_b64_tr_b16 a[240:241], v218 offset:8960\n"
"v_cvt_pk_bf16_f32 v110, v188, v189\n"
"v_mfma_f32_16x16x32_bf16 a[72:75], a[172:175], v[88:91], a[72:75]\n"
"ds_read_b64_tr_b16 a[242:243], v218 offset:9984\n"
"v_cvt_pk_bf16_f32 v111, v190, v191\n"
"v_mfma_f32_16x16x32_bf16 a[76:79], a[168:171], v[84:87], a[76:79]\n"
"ds_read_b64_tr_b16 a[244:245], v218 offset:13056\n"
"v_fma_mix_f32 v176, v112, v144, v152 op_sel:[0,0,0] op_sel_hi:[1,0,0]\n"
"v_mfma_f32_16x16x32_bf16 a[76:79], a[172:175], v[92:95], a[76:79]\n"
"ds_read_b64_tr_b16 a[246:247], v218 offset:14080\n"
"v_fma_mix_f32 v177, v112, v145, v153 op_sel:[1,0,0] op_sel_hi:[1,0,0]\n"
"v_mfma_f32_16x16x32_bf16 a[80:83], a[176:179], v[80:83], a[80:83]\n"
"s_add_u32 m0, s51, 115768\n"
"ds_read_b64_tr_b16 a[248:249], v218 offset:8968\n"
"v_fma_mix_f32 v178, v113, v146, v154 op_sel:[0,0,0] op_sel_hi:[1,0,0]\n"
"v_mfma_f32_16x16x32_bf16 a[80:83], a[180:183], v[88:91], a[80:83]\n"
"ds_read_b64_tr_b16 a[250:251], v218 offset:9992\n"
"buffer_load_dwordx4 v217, s[24:27], s50 offen offset:0 lds\n"
"v_mfma_f32_16x16x32_bf16 a[84:87], a[176:179], v[84:87], a[84:87]\n"
"ds_read_b64_tr_b16 a[252:253], v218 offset:13064\n"
"v_fma_mix_f32 v179, v113, v147, v155 op_sel:[1,0,0] op_sel_hi:[1,0,0]\n"
"v_mfma_f32_16x16x32_bf16 a[84:87], a[180:183], v[92:95], a[84:87]\n"
"s_waitcnt lgkmcnt(12)\n"
"ds_read_b64_tr_b16 a[254:255], v218 offset:14088\n"
"v_fma_mix_f32 v180, v114, v148, v156 op_sel:[0,0,0] op_sel_hi:[1,0,0]\n"
"v_mfma_f32_16x16x32_bf16 a[88:91], a[184:187], v[80:83], a[88:91]\n"
"ds_read_b64_tr_b16 a[160:161], v218 offset:32784\n"
"v_fma_mix_f32 v181, v114, v149, v157 op_sel:[1,0,0] op_sel_hi:[1,0,0]\n"
"v_mfma_f32_16x16x32_bf16 a[88:91], a[188:191], v[88:91], a[88:91]\n"
"ds_read_b64_tr_b16 a[162:163], v218 offset:33808\n"
"v_fma_mix_f32 v182, v115, v150, v158 op_sel:[0,0,0] op_sel_hi:[1,0,0]\n"
"v_mfma_f32_16x16x32_bf16 a[92:95], a[184:187], v[84:87], a[92:95]\n"
"s_add_u32 m0, s51, 123704\n"
"ds_read_b64_tr_b16 a[164:165], v218 offset:36880\n"
"v_fma_mix_f32 v183, v115, v151, v159 op_sel:[1,0,0] op_sel_hi:[1,0,0]\n"
"v_mfma_f32_16x16x32_bf16 a[92:95], a[188:191], v[92:95], a[92:95]\n"
"buffer_load_dwordx4 v217, s[24:27], s50 offen offset:256 lds\n"
"ds_read_b64_tr_b16 a[166:167], v218 offset:37904\n"
"v_mfma_f32_16x16x32_bf16 a[96:99], a[224:227], v[80:83], a[96:99]\n"
"s_add_u32 s50, s50, 425984\n"
"ds_read_b64_tr_b16 a[168:169], v218 offset:32792\n"
"v_cvt_pk_bf16_f32 v112, v176, v177\n"
"v_mfma_f32_16x16x32_bf16 a[96:99], a[228:231], v[88:91], a[96:99]\n"
"ds_read_b64_tr_b16 a[170:171], v218 offset:33816\n"
"v_cvt_pk_bf16_f32 v113, v178, v179\n"
"v_mfma_f32_16x16x32_bf16 a[100:103], a[224:227], v[84:87], a[100:103]\n"
"ds_read_b64_tr_b16 a[172:173], v218 offset:36888\n"
"v_cvt_pk_bf16_f32 v114, v180, v181\n"
"v_mfma_f32_16x16x32_bf16 a[100:103], a[228:231], v[92:95], a[100:103]\n"
"s_waitcnt lgkmcnt(11)\n"
"ds_read_b64_tr_b16 a[174:175], v218 offset:37912\n"
"v_cvt_pk_bf16_f32 v115, v182, v183\n"
"v_mfma_f32_16x16x32_bf16 a[104:107], a[232:235], v[80:83], a[104:107]\n"
"ds_read_b64_tr_b16 a[176:177], v218 offset:33040\n"
"v_fma_mix_f32 v184, v116, v144, v152 op_sel:[0,0,0] op_sel_hi:[1,0,0]\n"
"v_mfma_f32_16x16x32_bf16 a[104:107], a[236:239], v[88:91], a[104:107]\n"
"ds_read_b64_tr_b16 a[178:179], v218 offset:34064\n"
"v_fma_mix_f32 v185, v116, v145, v153 op_sel:[1,0,0] op_sel_hi:[1,0,0]\n"
"v_mfma_f32_16x16x32_bf16 a[108:111], a[232:235], v[84:87], a[108:111]\n"
"ds_read_b64_tr_b16 a[180:181], v218 offset:37136\n"
"v_fma_mix_f32 v186, v117, v146, v154 op_sel:[0,0,0] op_sel_hi:[1,0,0]\n"
"v_mfma_f32_16x16x32_bf16 a[108:111], a[236:239], v[92:95], a[108:111]\n"
"ds_read_b64_tr_b16 a[182:183], v218 offset:38160\n"
"v_fma_mix_f32 v187, v117, v147, v155 op_sel:[1,0,0] op_sel_hi:[1,0,0]\n"
"v_mfma_f32_16x16x32_bf16 a[112:115], a[240:243], v[80:83], a[112:115]\n"
"ds_read_b64_tr_b16 a[184:185], v218 offset:33048\n"
"v_fma_mix_f32 v188, v118, v148, v156 op_sel:[0,0,0] op_sel_hi:[1,0,0]\n"
"v_mfma_f32_16x16x32_bf16 a[112:115], a[244:247], v[88:91], a[112:115]\n"
"ds_read_b64_tr_b16 a[186:187], v218 offset:34072\n"
"v_fma_mix_f32 v189, v118, v149, v157 op_sel:[1,0,0] op_sel_hi:[1,0,0]\n"
"v_mfma_f32_16x16x32_bf16 a[116:119], a[240:243], v[84:87], a[116:119]\n"
"ds_read_b64_tr_b16 a[188:189], v218 offset:37144\n"
"v_fma_mix_f32 v190, v119, v150, v158 op_sel:[0,0,0] op_sel_hi:[1,0,0]\n"
"v_mfma_f32_16x16x32_bf16 a[116:119], a[244:247], v[92:95], a[116:119]\n"
"s_waitcnt lgkmcnt(11)\n"
"ds_read_b64_tr_b16 a[190:191], v218 offset:38168\n"
"v_fma_mix_f32 v191, v119, v151, v159 op_sel:[1,0,0] op_sel_hi:[1,0,0]\n"
"v_mfma_f32_16x16x32_bf16 a[120:123], a[248:251], v[80:83], a[120:123]\n"
"ds_read_b64_tr_b16 a[224:225], v218 offset:33296\n"
"ds_read_b128 v[144:147], v224 offset:4096\n"
"v_mfma_f32_16x16x32_bf16 a[120:123], a[252:255], v[88:91], a[120:123]\n"
"ds_read_b64_tr_b16 a[226:227], v218 offset:34320\n"
"ds_read_b128 v[148:151], v224 offset:4160\n"
"v_mfma_f32_16x16x32_bf16 a[124:127], a[248:251], v[84:87], a[124:127]\n"
"ds_read_b64_tr_b16 a[228:229], v218 offset:37392\n"
"ds_read_b128 v[152:155], v224 offset:4224\n"
"v_mfma_f32_16x16x32_bf16 a[124:127], a[252:255], v[92:95], a[124:127]\n"
"ds_read_b64_tr_b16 a[230:231], v218 offset:38416\n"
"ds_read_b128 v[156:159], v224 offset:4288\n"
"v_mfma_f32_16x16x32_bf16 a[0:3], a[160:163], v[96:99], a[0:3]\n"
"ds_read_b64_tr_b16 a[232:233], v218 offset:33304\n"
"v_cvt_pk_bf16_f32 v116, v184, v185\n"
"v_mfma_f32_16x16x32_bf16 a[0:3], a[164:167], v[104:107], a[0:3]\n"
"ds_read_b64_tr_b16 a[234:235], v218 offset:34328\n"
"v_cvt_pk_bf16_f32 v117, v186, v187\n"
"v_mfma_f32_16x16x32_bf16 a[4:7], a[160:163], v[100:103], a[4:7]\n"
"s_waitcnt vmcnt(8)\n"
"s_barrier\n"
"ds_read_b64_tr_b16 a[236:237], v218 offset:37400\n"
"v_cvt_pk_bf16_f32 v118, v188, v189\n"
"v_mfma_f32_16x16x32_bf16 a[4:7], a[164:167], v[108:111], a[4:7]\n"
"s_waitcnt lgkmcnt(14)\n"
"ds_read_b64_tr_b16 a[238:239], v218 offset:38424\n"
"v_cvt_pk_bf16_f32 v119, v190, v191\n"
"v_mfma_f32_16x16x32_bf16 a[8:11], a[168:171], v[96:99], a[8:11]\n"
"s_mov_b32 m0, s18\n"
"ds_read_b64_tr_b16 a[240:241], v218 offset:33552\n"
"v_fma_mix_f32 v176, v120, v160, v168 op_sel:[0,0,0] op_sel_hi:[1,0,0]\n"
"v_mfma_f32_16x16x32_bf16 a[8:11], a[172:175], v[104:107], a[8:11]\n"
"ds_read_b64_tr_b16 a[242:243], v218 offset:34576\n"
"buffer_load_dwordx4 v246, s[8:11], s16 offen offset:0 lds\n"
"v_mfma_f32_16x16x32_bf16 a[12:15], a[168:171], v[100:103], a[12:15]\n"
"ds_read_b64_tr_b16 a[244:245], v218 offset:37648\n"
"v_fma_mix_f32 v177, v120, v161, v169 op_sel:[1,0,0] op_sel_hi:[1,0,0]\n"
"v_mfma_f32_16x16x32_bf16 a[12:15], a[172:175], v[108:111], a[12:15]\n"
"ds_read_b64_tr_b16 a[246:247], v218 offset:38672\n"
"v_fma_mix_f32 v178, v121, v162, v170 op_sel:[0,0,0] op_sel_hi:[1,0,0]\n"
"v_mfma_f32_16x16x32_bf16 a[16:19], a[176:179], v[96:99], a[16:19]\n"
"ds_read_b64_tr_b16 a[248:249], v218 offset:33560\n"
"v_fma_mix_f32 v179, v121, v163, v171 op_sel:[1,0,0] op_sel_hi:[1,0,0]\n"
"v_mfma_f32_16x16x32_bf16 a[16:19], a[180:183], v[104:107], a[16:19]\n"
"ds_read_b64_tr_b16 a[250:251], v218 offset:34584\n"
"v_fma_mix_f32 v180, v122, v164, v172 op_sel:[0,0,0] op_sel_hi:[1,0,0]\n"
"v_mfma_f32_16x16x32_bf16 a[20:23], a[176:179], v[100:103], a[20:23]\n"
"ds_read_b64_tr_b16 a[252:253], v218 offset:37656\n"
"v_fma_mix_f32 v181, v122, v165, v173 op_sel:[1,0,0] op_sel_hi:[1,0,0]\n"
"v_mfma_f32_16x16x32_bf16 a[20:23], a[180:183], v[108:111], a[20:23]\n"
"s_waitcnt lgkmcnt(11)\n"
"ds_read_b64_tr_b16 a[254:255], v218 offset:38680\n"
"buffer_load_dwordx4 v246, s[8:11], s16 offen offset:1024 lds\n"
"v_mfma_f32_16x16x32_bf16 a[24:27], a[184:187], v[96:99], a[24:27]\n"
"ds_read_b64_tr_b16 a[160:161], v218 offset:40976\n"
"v_fma_mix_f32 v182, v123, v166, v174 op_sel:[0,0,0] op_sel_hi:[1,0,0]\n"
"v_mfma_f32_16x16x32_bf16 a[24:27], a[188:191], v[104:107], a[24:27]\n"
"ds_read_b64_tr_b16 a[162:163], v218 offset:42000\n"
"v_fma_mix_f32 v183, v123, v167, v175 op_sel:[1,0,0] op_sel_hi:[1,0,0]\n"
"v_mfma_f32_16x16x32_bf16 a[28:31], a[184:187], v[100:103], a[28:31]\n"
"ds_read_b64_tr_b16 a[164:165], v218 offset:45072\n"
"v_cvt_pk_bf16_f32 v120, v176, v177\n"
"v_mfma_f32_16x16x32_bf16 a[28:31], a[188:191], v[108:111], a[28:31]\n"
"ds_read_b64_tr_b16 a[166:167], v218 offset:46096\n"
"v_cvt_pk_bf16_f32 v121, v178, v179\n"
"v_mfma_f32_16x16x32_bf16 a[32:35], a[224:227], v[96:99], a[32:35]\n"
"ds_read_b64_tr_b16 a[168:169], v218 offset:40984\n"
"v_cvt_pk_bf16_f32 v122, v180, v181\n"
"v_mfma_f32_16x16x32_bf16 a[32:35], a[228:231], v[104:107], a[32:35]\n"
"ds_read_b64_tr_b16 a[170:171], v218 offset:42008\n"
"buffer_load_dwordx4 v247, s[8:11], s16 offen offset:2048 lds\n"
"v_mfma_f32_16x16x32_bf16 a[36:39], a[224:227], v[100:103], a[36:39]\n"
"ds_read_b64_tr_b16 a[172:173], v218 offset:45080\n"
"v_cvt_pk_bf16_f32 v123, v182, v183\n"
"v_mfma_f32_16x16x32_bf16 a[36:39], a[228:231], v[108:111], a[36:39]\n"
"s_waitcnt lgkmcnt(11)\n"
"ds_read_b64_tr_b16 a[174:175], v218 offset:46104\n"
"v_fma_mix_f32 v184, v124, v160, v168 op_sel:[0,0,0] op_sel_hi:[1,0,0]\n"
"v_mfma_f32_16x16x32_bf16 a[40:43], a[232:235], v[96:99], a[40:43]\n"
"ds_read_b64_tr_b16 a[176:177], v218 offset:41232\n"
"v_fma_mix_f32 v185, v124, v161, v169 op_sel:[1,0,0] op_sel_hi:[1,0,0]\n"
"v_mfma_f32_16x16x32_bf16 a[40:43], a[236:239], v[104:107], a[40:43]\n"
"ds_read_b64_tr_b16 a[178:179], v218 offset:42256\n"
"v_fma_mix_f32 v186, v125, v162, v170 op_sel:[0,0,0] op_sel_hi:[1,0,0]\n"
"v_mfma_f32_16x16x32_bf16 a[44:47], a[232:235], v[100:103], a[44:47]\n"
"ds_read_b64_tr_b16 a[180:181], v218 offset:45328\n"
"v_fma_mix_f32 v187, v125, v163, v171 op_sel:[1,0,0] op_sel_hi:[1,0,0]\n"
"v_mfma_f32_16x16x32_bf16 a[44:47], a[236:239], v[108:111], a[44:47]\n"
"ds_read_b64_tr_b16 a[182:183], v218 offset:46352\n"
"buffer_load_dwordx4 v247, s[8:11], s16 offen offset:3072 lds\n"
"v_mfma_f32_16x16x32_bf16 a[48:51], a[240:243], v[96:99], a[48:51]\n"
"ds_read_b64_tr_b16 a[184:185], v218 offset:41240\n"
"v_fma_mix_f32 v188, v126, v164, v172 op_sel:[0,0,0] op_sel_hi:[1,0,0]\n"
"v_mfma_f32_16x16x32_bf16 a[48:51], a[244:247], v[104:107], a[48:51]\n"
"ds_read_b64_tr_b16 a[186:187], v218 offset:42264\n"
"v_fma_mix_f32 v189, v126, v165, v173 op_sel:[1,0,0] op_sel_hi:[1,0,0]\n"
"v_mfma_f32_16x16x32_bf16 a[52:55], a[240:243], v[100:103], a[52:55]\n"
"ds_read_b64_tr_b16 a[188:189], v218 offset:45336\n"
"v_fma_mix_f32 v190, v127, v166, v174 op_sel:[0,0,0] op_sel_hi:[1,0,0]\n"
"v_mfma_f32_16x16x32_bf16 a[52:55], a[244:247], v[108:111], a[52:55]\n"
"s_waitcnt lgkmcnt(11)\n"
"ds_read_b64_tr_b16 a[190:191], v218 offset:46360\n"
"v_fma_mix_f32 v191, v127, v167, v175 op_sel:[1,0,0] op_sel_hi:[1,0,0]\n"
"v_mfma_f32_16x16x32_bf16 a[56:59], a[248:251], v[96:99], a[56:59]\n"
"s_add_u32 m0, s18, 16384\n"
"ds_read_b64_tr_b16 a[224:225], v218 offset:41488\n"
"ds_read_b128 v[160:163], v224 offset:4352\n"
"v_mfma_f32_16x16x32_bf16 a[56:59], a[252:255], v[104:107], a[56:59]\n"
"ds_read_b64_tr_b16 a[226:227], v218 offset:42512\n"
"buffer_load_dwordx4 v246, s[12:15], s16 offen offset:0 lds\n"
"v_mfma_f32_16x16x32_bf16 a[60:63], a[248:251], v[100:103], a[60:63]\n"
"ds_read_b64_tr_b16 a[228:229], v218 offset:45584\n"
"ds_read_b128 v[164:167], v224 offset:4416\n"
"v_mfma_f32_16x16x32_bf16 a[60:63], a[252:255], v[108:111], a[60:63]\n"
"ds_read_b64_tr_b16 a[230:231], v218 offset:46608\n"
"ds_read_b128 v[168:171], v224 offset:4480\n"
"v_mfma_f32_16x16x32_bf16 a[64:67], a[160:163], v[96:99], a[64:67]\n"
"ds_read_b64_tr_b16 a[232:233], v218 offset:41496\n"
"ds_read_b128 v[172:175], v224 offset:4544\n"
"v_mfma_f32_16x16x32_bf16 a[64:67], a[164:167], v[104:107], a[64:67]\n"
"ds_read_b64_tr_b16 a[234:235], v218 offset:42520\n"
"v_cvt_pk_bf16_f32 v124, v184, v185\n"
"v_mfma_f32_16x16x32_bf16 a[68:71], a[160:163], v[100:103], a[68:71]\n"
"ds_read_b64_tr_b16 a[236:237], v218 offset:45592\n"
"v_cvt_pk_bf16_f32 v125, v186, v187\n"
"v_mfma_f32_16x16x32_bf16 a[68:71], a[164:167], v[108:111], a[68:71]\n"
"s_waitcnt lgkmcnt(14)\n"
"ds_read_b64_tr_b16 a[238:239], v218 offset:46616\n"
"buffer_load_dwordx4 v247, s[12:15], s16 offen offset:1024 lds\n"
"v_mfma_f32_16x16x32_bf16 a[72:75], a[168:171], v[96:99], a[72:75]\n"
"ds_read_b64_tr_b16 a[240:241], v218 offset:41744\n"
"v_cvt_pk_bf16_f32 v126, v188, v189\n"
"v_mfma_f32_16x16x32_bf16 a[72:75], a[172:175], v[104:107], a[72:75]\n"
"ds_read_b64_tr_b16 a[242:243], v218 offset:42768\n"
"v_cvt_pk_bf16_f32 v127, v190, v191\n"
"v_mfma_f32_16x16x32_bf16 a[76:79], a[168:171], v[100:103], a[76:79]\n"
"ds_read_b64_tr_b16 a[244:245], v218 offset:45840\n"
"v_fma_mix_f32 v176, v128, v144, v152 op_sel:[0,0,0] op_sel_hi:[1,0,0]\n"
"v_mfma_f32_16x16x32_bf16 a[76:79], a[172:175], v[108:111], a[76:79]\n"
"ds_read_b64_tr_b16 a[246:247], v218 offset:46864\n"
"v_fma_mix_f32 v177, v128, v145, v153 op_sel:[1,0,0] op_sel_hi:[1,0,0]\n"
"v_mfma_f32_16x16x32_bf16 a[80:83], a[176:179], v[96:99], a[80:83]\n"
"ds_read_b64_tr_b16 a[248:249], v218 offset:41752\n"
"v_fma_mix_f32 v178, v129, v146, v154 op_sel:[0,0,0] op_sel_hi:[1,0,0]\n"
"v_mfma_f32_16x16x32_bf16 a[80:83], a[180:183], v[104:107], a[80:83]\n"
"ds_read_b64_tr_b16 a[250:251], v218 offset:42776\n"
"buffer_load_dwordx4 v246, s[12:15], s16 offen offset:2048 lds\n"
"v_mfma_f32_16x16x32_bf16 a[84:87], a[176:179], v[100:103], a[84:87]\n"
"ds_read_b64_tr_b16 a[252:253], v218 offset:45848\n"
"v_fma_mix_f32 v179, v129, v147, v155 op_sel:[1,0,0] op_sel_hi:[1,0,0]\n"
"v_mfma_f32_16x16x32_bf16 a[84:87], a[180:183], v[108:111], a[84:87]\n"
"s_waitcnt lgkmcnt(12)\n"
"ds_read_b64_tr_b16 a[254:255], v218 offset:46872\n"
"v_fma_mix_f32 v180, v130, v148, v156 op_sel:[0,0,0] op_sel_hi:[1,0,0]\n"
"v_mfma_f32_16x16x32_bf16 a[88:91], a[184:187], v[96:99], a[88:91]\n"
"ds_read_b64_tr_b16 a[160:161], v219 offset:0\n"
"v_fma_mix_f32 v181, v130, v149, v157 op_sel:[1,0,0] op_sel_hi:[1,0,0]\n"
"v_mfma_f32_16x16x32_bf16 a[88:91], a[188:191], v[104:107], a[88:91]\n"
"ds_read_b64_tr_b16 a[162:163], v219 offset:1024\n"
"v_fma_mix_f32 v182, v131, v150, v158 op_sel:[0,0,0] op_sel_hi:[1,0,0]\n"
"v_mfma_f32_16x16x32_bf16 a[92:95], a[184:187], v[100:103], a[92:95]\n"
"ds_read_b64_tr_b16 a[164:165], v219 offset:4096\n"
"v_fma_mix_f32 v183, v131, v151, v159 op_sel:[1,0,0] op_sel_hi:[1,0,0]\n"
"v_mfma_f32_16x16x32_bf16 a[92:95], a[188:191], v[108:111], a[92:95]\n"
"buffer_load_dwordx4 v247, s[12:15], s16 offen offset:3072 lds\n"
"ds_read_b64_tr_b16 a[166:167], v219 offset:5120\n"
"v_mfma_f32_16x16x32_bf16 a[96:99], a[224:227], v[96:99], a[96:99]\n"
"s_add_u32 s16, s16, 16384\n"
"ds_read_b64_tr_b16 a[168:169], v219 offset:8\n"
"v_cvt_pk_bf16_f32 v128, v176, v177\n"
"v_mfma_f32_16x16x32_bf16 a[96:99], a[228:231], v[104:107], a[96:99]\n"
"ds_read_b64_tr_b16 a[170:171], v219 offset:1032\n"
"v_cvt_pk_bf16_f32 v129, v178, v179\n"
"v_mfma_f32_16x16x32_bf16 a[100:103], a[224:227], v[100:103], a[100:103]\n"
"ds_read_b64_tr_b16 a[172:173], v219 offset:4104\n"
"v_cvt_pk_bf16_f32 v130, v180, v181\n"
"v_mfma_f32_16x16x32_bf16 a[100:103], a[228:231], v[108:111], a[100:103]\n"
"s_waitcnt lgkmcnt(11)\n"
"ds_read_b64_tr_b16 a[174:175], v219 offset:5128\n"
"v_cvt_pk_bf16_f32 v131, v182, v183\n"
"v_mfma_f32_16x16x32_bf16 a[104:107], a[232:235], v[96:99], a[104:107]\n"
"ds_read_b64_tr_b16 a[176:177], v219 offset:256\n"
"v_fma_mix_f32 v184, v132, v144, v152 op_sel:[0,0,0] op_sel_hi:[1,0,0]\n"
"v_mfma_f32_16x16x32_bf16 a[104:107], a[236:239], v[104:107], a[104:107]\n"
"ds_read_b64_tr_b16 a[178:179], v219 offset:1280\n"
"v_fma_mix_f32 v185, v132, v145, v153 op_sel:[1,0,0] op_sel_hi:[1,0,0]\n"
"v_mfma_f32_16x16x32_bf16 a[108:111], a[232:235], v[100:103], a[108:111]\n"
"ds_read_b64_tr_b16 a[180:181], v219 offset:4352\n"
"v_fma_mix_f32 v186, v133, v146, v154 op_sel:[0,0,0] op_sel_hi:[1,0,0]\n"
"v_mfma_f32_16x16x32_bf16 a[108:111], a[236:239], v[108:111], a[108:111]\n"
"ds_read_b64_tr_b16 a[182:183], v219 offset:5376\n"
"v_fma_mix_f32 v187, v133, v147, v155 op_sel:[1,0,0] op_sel_hi:[1,0,0]\n"
"v_mfma_f32_16x16x32_bf16 a[112:115], a[240:243], v[96:99], a[112:115]\n"
"ds_read_b64_tr_b16 a[184:185], v219 offset:264\n"
"v_fma_mix_f32 v188, v134, v148, v156 op_sel:[0,0,0] op_sel_hi:[1,0,0]\n"
"v_mfma_f32_16x16x32_bf16 a[112:115], a[244:247], v[104:107], a[112:115]\n"
"ds_read_b64_tr_b16 a[186:187], v219 offset:1288\n"
"v_fma_mix_f32 v189, v134, v149, v157 op_sel:[1,0,0] op_sel_hi:[1,0,0]\n"
"v_mfma_f32_16x16x32_bf16 a[116:119], a[240:243], v[100:103], a[116:119]\n"
"ds_read_b64_tr_b16 a[188:189], v219 offset:4360\n"
"v_fma_mix_f32 v190, v135, v150, v158 op_sel:[0,0,0] op_sel_hi:[1,0,0]\n"
"v_mfma_f32_16x16x32_bf16 a[116:119], a[244:247], v[108:111], a[116:119]\n"
"s_waitcnt lgkmcnt(11)\n"
"ds_read_b64_tr_b16 a[190:191], v219 offset:5384\n"
"v_fma_mix_f32 v191, v135, v151, v159 op_sel:[1,0,0] op_sel_hi:[1,0,0]\n"
"v_mfma_f32_16x16x32_bf16 a[120:123], a[248:251], v[96:99], a[120:123]\n"
"ds_read_b64_tr_b16 a[224:225], v219 offset:512\n"
"v_cvt_pk_bf16_f32 v132, v184, v185\n"
"v_mfma_f32_16x16x32_bf16 a[120:123], a[252:255], v[104:107], a[120:123]\n"
"ds_read_b64_tr_b16 a[226:227], v219 offset:1536\n"
"v_cvt_pk_bf16_f32 v133, v186, v187\n"
"v_mfma_f32_16x16x32_bf16 a[124:127], a[248:251], v[100:103], a[124:127]\n"
"ds_read_b64_tr_b16 a[228:229], v219 offset:4608\n"
"v_cvt_pk_bf16_f32 v134, v188, v189\n"
"v_mfma_f32_16x16x32_bf16 a[124:127], a[252:255], v[108:111], a[124:127]\n"
"ds_read_b64_tr_b16 a[230:231], v219 offset:5632\n"
"v_cvt_pk_bf16_f32 v135, v190, v191\n"
"v_mfma_f32_16x16x32_bf16 a[0:3], a[160:163], v[112:115], a[0:3]\n"
"ds_read_b64_tr_b16 a[232:233], v219 offset:520\n"
"v_fma_mix_f32 v176, v136, v160, v168 op_sel:[0,0,0] op_sel_hi:[1,0,0]\n"
"v_mfma_f32_16x16x32_bf16 a[0:3], a[164:167], v[120:123], a[0:3]\n"
"ds_read_b64_tr_b16 a[234:235], v219 offset:1544\n"
"v_fma_mix_f32 v177, v136, v161, v169 op_sel:[1,0,0] op_sel_hi:[1,0,0]\n"
"v_mfma_f32_16x16x32_bf16 a[4:7], a[160:163], v[116:119], a[4:7]\n"
"s_waitcnt vmcnt(8)\n"
"s_barrier\n"
"ds_read_b64_tr_b16 a[236:237], v219 offset:4616\n"
"v_fma_mix_f32 v178, v137, v162, v170 op_sel:[0,0,0] op_sel_hi:[1,0,0]\n"
"v_mfma_f32_16x16x32_bf16 a[4:7], a[164:167], v[124:127], a[4:7]\n"
"s_waitcnt lgkmcnt(11)\n"
"ds_read_b64_tr_b16 a[238:239], v219 offset:5640\n"
"v_fma_mix_f32 v179, v137, v163, v171 op_sel:[1,0,0] op_sel_hi:[1,0,0]\n"
"v_mfma_f32_16x16x32_bf16 a[8:11], a[168:171], v[112:115], a[8:11]\n"
"s_add_u32 m0, s18, 32768\n"
"ds_read_b64_tr_b16 a[240:241], v219 offset:768\n"
"v_fma_mix_f32 v180, v138, v164, v172 op_sel:[0,0,0] op_sel_hi:[1,0,0]\n"
"v_mfma_f32_16x16x32_bf16 a[8:11], a[172:175], v[120:123], a[8:11]\n"
"ds_read_b64_tr_b16 a[242:243], v219 offset:1792\n"
"buffer_load_dwordx4 v246, s[8:11], s16 offen offset:0 lds\n"
"v_mfma_f32_16x16x32_bf16 a[12:15], a[168:171], v[116:119], a[12:15]\n"
"ds_read_b64_tr_b16 a[244:245], v219 offset:4864\n"
"v_fma_mix_f32 v181, v138, v165, v173 op_sel:[1,0,0] op_sel_hi:[1,0,0]\n"
"v_mfma_f32_16x16x32_bf16 a[12:15], a[172:175], v[124:127], a[12:15]\n"
"ds_read_b64_tr_b16 a[246:247], v219 offset:5888\n"
"v_fma_mix_f32 v182, v139, v166, v174 op_sel:[0,0,0] op_sel_hi:[1,0,0]\n"
"v_mfma_f32_16x16x32_bf16 a[16:19], a[176:179], v[112:115], a[16:19]\n"
"ds_read_b64_tr_b16 a[248:249], v219 offset:776\n"
"v_fma_mix_f32 v183, v139, v167, v175 op_sel:[1,0,0] op_sel_hi:[1,0,0]\n"
"v_mfma_f32_16x16x32_bf16 a[16:19], a[180:183], v[120:123], a[16:19]\n"
"ds_read_b64_tr_b16 a[250:251], v219 offset:1800\n"
"v_cvt_pk_bf16_f32 v136, v176, v177\n"
"v_mfma_f32_16x16x32_bf16 a[20:23], a[176:179], v[116:119], a[20:23]\n"
"ds_read_b64_tr_b16 a[252:253], v219 offset:4872\n"
"v_cvt_pk_bf16_f32 v137, v178, v179\n"
"v_mfma_f32_16x16x32_bf16 a[20:23], a[180:183], v[124:127], a[20:23]\n"
"s_waitcnt lgkmcnt(11)\n"
"ds_read_b64_tr_b16 a[254:255], v219 offset:5896\n"
"buffer_load_dwordx4 v246, s[8:11], s16 offen offset:1024 lds\n"
"v_mfma_f32_16x16x32_bf16 a[24:27], a[184:187], v[112:115], a[24:27]\n"
"ds_read_b64_tr_b16 a[160:161], v219 offset:8192\n"
"v_cvt_pk_bf16_f32 v138, v180, v181\n"
"v_mfma_f32_16x16x32_bf16 a[24:27], a[188:191], v[120:123], a[24:27]\n"
"ds_read_b64_tr_b16 a[162:163], v219 offset:9216\n"
"v_cvt_pk_bf16_f32 v139, v182, v183\n"
"v_mfma_f32_16x16x32_bf16 a[28:31], a[184:187], v[116:119], a[28:31]\n"
"ds_read_b64_tr_b16 a[164:165], v219 offset:12288\n"
"v_fma_mix_f32 v184, v140, v160, v168 op_sel:[0,0,0] op_sel_hi:[1,0,0]\n"
"v_mfma_f32_16x16x32_bf16 a[28:31], a[188:191], v[124:127], a[28:31]\n"
"ds_read_b64_tr_b16 a[166:167], v219 offset:13312\n"
"v_fma_mix_f32 v185, v140, v161, v169 op_sel:[1,0,0] op_sel_hi:[1,0,0]\n"
"v_mfma_f32_16x16x32_bf16 a[32:35], a[224:227], v[112:115], a[32:35]\n"
"ds_read_b64_tr_b16 a[168:169], v219 offset:8200\n"
"v_fma_mix_f32 v186, v141, v162, v170 op_sel:[0,0,0] op_sel_hi:[1,0,0]\n"
"v_mfma_f32_16x16x32_bf16 a[32:35], a[228:231], v[120:123], a[32:35]\n"
"ds_read_b64_tr_b16 a[170:171], v219 offset:9224\n"
"buffer_load_dwordx4 v247, s[8:11], s16 offen offset:2048 lds\n"
"v_mfma_f32_16x16x32_bf16 a[36:39], a[224:227], v[116:119], a[36:39]\n"
"ds_read_b64_tr_b16 a[172:173], v219 offset:12296\n"
"v_fma_mix_f32 v187, v141, v163, v171 op_sel:[1,0,0] op_sel_hi:[1,0,0]\n"
"v_mfma_f32_16x16x32_bf16 a[36:39], a[228:231], v[124:127], a[36:39]\n"
"s_waitcnt lgkmcnt(11)\n"
"ds_read_b64_tr_b16 a[174:175], v219 offset:13320\n"
"v_fma_mix_f32 v188, v142, v164, v172 op_sel:[0,0,0] op_sel_hi:[1,0,0]\n"
"v_mfma_f32_16x16x32_bf16 a[40:43], a[232:235], v[112:115], a[40:43]\n"
"ds_read_b64_tr_b16 a[176:177], v219 offset:8448\n"
"v_fma_mix_f32 v189, v142, v165, v173 op_sel:[1,0,0] op_sel_hi:[1,0,0]\n"
"v_mfma_f32_16x16x32_bf16 a[40:43], a[236:239], v[120:123], a[40:43]\n"
"ds_read_b64_tr_b16 a[178:179], v219 offset:9472\n"
"v_fma_mix_f32 v190, v143, v166, v174 op_sel:[0,0,0] op_sel_hi:[1,0,0]\n"
"v_mfma_f32_16x16x32_bf16 a[44:47], a[232:235], v[116:119], a[44:47]\n"
"ds_read_b64_tr_b16 a[180:181], v219 offset:12544\n"
"v_fma_mix_f32 v191, v143, v167, v175 op_sel:[1,0,0] op_sel_hi:[1,0,0]\n"
"v_mfma_f32_16x16x32_bf16 a[44:47], a[236:239], v[124:127], a[44:47]\n"
"ds_read_b64_tr_b16 a[182:183], v219 offset:13568\n"
"buffer_load_dwordx4 v247, s[8:11], s16 offen offset:3072 lds\n"
"v_mfma_f32_16x16x32_bf16 a[48:51], a[240:243], v[112:115], a[48:51]\n"
"ds_read_b64_tr_b16 a[184:185], v219 offset:8456\n"
"v_cvt_pk_bf16_f32 v140, v184, v185\n"
"v_mfma_f32_16x16x32_bf16 a[48:51], a[244:247], v[120:123], a[48:51]\n"
"ds_read_b64_tr_b16 a[186:187], v219 offset:9480\n"
"v_cvt_pk_bf16_f32 v141, v186, v187\n"
"v_mfma_f32_16x16x32_bf16 a[52:55], a[240:243], v[116:119], a[52:55]\n"
"ds_read_b64_tr_b16 a[188:189], v219 offset:12552\n"
"v_cvt_pk_bf16_f32 v142, v188, v189\n"
"v_mfma_f32_16x16x32_bf16 a[52:55], a[244:247], v[124:127], a[52:55]\n"
"s_waitcnt lgkmcnt(11)\n"
"ds_read_b64_tr_b16 a[190:191], v219 offset:13576\n"
"v_cvt_pk_bf16_f32 v143, v190, v191\n"
"v_mfma_f32_16x16x32_bf16 a[56:59], a[248:251], v[112:115], a[56:59]\n"
"s_add_u32 m0, s18, 49152\n"
"ds_read_b64_tr_b16 a[224:225], v219 offset:8704\n"
"v_mfma_f32_16x16x32_bf16 a[56:59], a[252:255], v[120:123], a[56:59]\n"
"ds_read_b64_tr_b16 a[226:227], v219 offset:9728\n"
"buffer_load_dwordx4 v246, s[12:15], s16 offen offset:0 lds\n"
"v_mfma_f32_16x16x32_bf16 a[60:63], a[248:251], v[116:119], a[60:63]\n"
"ds_read_b64_tr_b16 a[228:229], v219 offset:12800\n"
"v_mfma_f32_16x16x32_bf16 a[60:63], a[252:255], v[124:127], a[60:63]\n"
"ds_read_b64_tr_b16 a[230:231], v219 offset:13824\n"
"v_mfma_f32_16x16x32_bf16 a[64:67], a[160:163], v[112:115], a[64:67]\n"
"ds_read_b64_tr_b16 a[232:233], v219 offset:8712\n"
"v_mfma_f32_16x16x32_bf16 a[64:67], a[164:167], v[120:123], a[64:67]\n"
"ds_read_b64_tr_b16 a[234:235], v219 offset:9736\n"
"v_mfma_f32_16x16x32_bf16 a[68:71], a[160:163], v[116:119], a[68:71]\n"
"ds_read_b64_tr_b16 a[236:237], v219 offset:12808\n"
"v_mfma_f32_16x16x32_bf16 a[68:71], a[164:167], v[124:127], a[68:71]\n"
"s_waitcnt lgkmcnt(11)\n"
"ds_read_b64_tr_b16 a[238:239], v219 offset:13832\n"
"buffer_load_dwordx4 v247, s[12:15], s16 offen offset:1024 lds\n"
"v_mfma_f32_16x16x32_bf16 a[72:75], a[168:171], v[112:115], a[72:75]\n"
"ds_read_b64_tr_b16 a[240:241], v219 offset:8960\n"
"v_mfma_f32_16x16x32_bf16 a[72:75], a[172:175], v[120:123], a[72:75]\n"
"ds_read_b64_tr_b16 a[242:243], v219 offset:9984\n"
"v_mfma_f32_16x16x32_bf16 a[76:79], a[168:171], v[116:119], a[76:79]\n"
"ds_read_b64_tr_b16 a[244:245], v219 offset:13056\n"
"v_mfma_f32_16x16x32_bf16 a[76:79], a[172:175], v[124:127], a[76:79]\n"
"ds_read_b64_tr_b16 a[246:247], v219 offset:14080\n"
"v_mfma_f32_16x16x32_bf16 a[80:83], a[176:179], v[112:115], a[80:83]\n"
"ds_read_b64_tr_b16 a[248:249], v219 offset:8968\n"
"v_mfma_f32_16x16x32_bf16 a[80:83], a[180:183], v[120:123], a[80:83]\n"
"ds_read_b64_tr_b16 a[250:251], v219 offset:9992\n"
"buffer_load_dwordx4 v246, s[12:15], s16 offen offset:2048 lds\n"
"v_mfma_f32_16x16x32_bf16 a[84:87], a[176:179], v[116:119], a[84:87]\n"
"ds_read_b64_tr_b16 a[252:253], v219 offset:13064\n"
"v_mfma_f32_16x16x32_bf16 a[84:87], a[180:183], v[124:127], a[84:87]\n"
"s_waitcnt lgkmcnt(11)\n"
"ds_read_b64_tr_b16 a[254:255], v219 offset:14088\n"
"v_mfma_f32_16x16x32_bf16 a[88:91], a[184:187], v[112:115], a[88:91]\n"
"ds_read_b64_tr_b16 a[160:161], v219 offset:32784\n"
"v_mfma_f32_16x16x32_bf16 a[88:91], a[188:191], v[120:123], a[88:91]\n"
"ds_read_b64_tr_b16 a[162:163], v219 offset:33808\n"
"v_mfma_f32_16x16x32_bf16 a[92:95], a[184:187], v[116:119], a[92:95]\n"
"ds_read_b64_tr_b16 a[164:165], v219 offset:36880\n"
"v_mfma_f32_16x16x32_bf16 a[92:95], a[188:191], v[124:127], a[92:95]\n"
"buffer_load_dwordx4 v247, s[12:15], s16 offen offset:3072 lds\n"
"ds_read_b64_tr_b16 a[166:167], v219 offset:37904\n"
"v_mfma_f32_16x16x32_bf16 a[96:99], a[224:227], v[112:115], a[96:99]\n"
"s_add_u32 s16, s16, 16384\n"
"ds_read_b64_tr_b16 a[168:169], v219 offset:32792\n"
"v_mfma_f32_16x16x32_bf16 a[96:99], a[228:231], v[120:123], a[96:99]\n"
"ds_read_b64_tr_b16 a[170:171], v219 offset:33816\n"
"v_mfma_f32_16x16x32_bf16 a[100:103], a[224:227], v[116:119], a[100:103]\n"
"ds_read_b64_tr_b16 a[172:173], v219 offset:36888\n"
"v_mfma_f32_16x16x32_bf16 a[100:103], a[228:231], v[124:127], a[100:103]\n"
"s_waitcnt lgkmcnt(11)\n"
"ds_read_b64_tr_b16 a[174:175], v219 offset:37912\n"
"v_mfma_f32_16x16x32_bf16 a[104:107], a[232:235], v[112:115], a[104:107]\n"
"ds_read_b64_tr_b16 a[176:177], v219 offset:33040\n"
"v_mfma_f32_16x16x32_bf16 a[104:107], a[236:239], v[120:123], a[104:107]\n"
"ds_read_b64_tr_b16 a[178:179], v219 offset:34064\n"
"v_mfma_f32_16x16x32_bf16 a[108:111], a[232:235], v[116:119], a[108:111]\n"
"ds_read_b64_tr_b16 a[180:181], v219 offset:37136\n"
"v_mfma_f32_16x16x32_bf16 a[108:111], a[236:239], v[124:127], a[108:111]\n"
"ds_read_b64_tr_b16 a[182:183], v219 offset:38160\n"
"v_mfma_f32_16x16x32_bf16 a[112:115], a[240:243], v[112:115], a[112:115]\n"
"ds_read_b64_tr_b16 a[184:185], v219 offset:33048\n"
"v_mfma_f32_16x16x32_bf16 a[112:115], a[244:247], v[120:123], a[112:115]\n"
"ds_read_b64_tr_b16 a[186:187], v219 offset:34072\n"
"v_mfma_f32_16x16x32_bf16 a[116:119], a[240:243], v[116:119], a[116:119]\n"
"ds_read_b64_tr_b16 a[188:189], v219 offset:37144\n"
"v_mfma_f32_16x16x32_bf16 a[116:119], a[244:247], v[124:127], a[116:119]\n"
"s_waitcnt lgkmcnt(11)\n"
"ds_read_b64_tr_b16 a[190:191], v219 offset:38168\n"
"v_mfma_f32_16x16x32_bf16 a[120:123], a[248:251], v[112:115], a[120:123]\n"
"ds_read_b64_tr_b16 a[224:225], v219 offset:33296\n"
"v_mfma_f32_16x16x32_bf16 a[120:123], a[252:255], v[120:123], a[120:123]\n"
"ds_read_b64_tr_b16 a[226:227], v219 offset:34320\n"
"v_mfma_f32_16x16x32_bf16 a[124:127], a[248:251], v[116:119], a[124:127]\n"
"ds_read_b64_tr_b16 a[228:229], v219 offset:37392\n"
"v_mfma_f32_16x16x32_bf16 a[124:127], a[252:255], v[124:127], a[124:127]\n"
"ds_read_b64_tr_b16 a[230:231], v219 offset:38416\n"
"v_mfma_f32_16x16x32_bf16 a[0:3], a[160:163], v[128:131], a[0:3]\n"
"ds_read_b64_tr_b16 a[232:233], v219 offset:33304\n"
"v_mfma_f32_16x16x32_bf16 a[0:3], a[164:167], v[136:139], a[0:3]\n"
"ds_read_b64_tr_b16 a[234:235], v219 offset:34328\n"
"v_mfma_f32_16x16x32_bf16 a[4:7], a[160:163], v[132:135], a[4:7]\n"
"s_waitcnt vmcnt(8)\n"
"s_barrier\n"
"ds_read_b64_tr_b16 a[236:237], v219 offset:37400\n"
"v_mfma_f32_16x16x32_bf16 a[4:7], a[164:167], v[140:143], a[4:7]\n"
"s_waitcnt lgkmcnt(11)\n"
"ds_read_b64_tr_b16 a[238:239], v219 offset:38424\n"
"v_mfma_f32_16x16x32_bf16 a[8:11], a[168:171], v[128:131], a[8:11]\n"
"s_add_u32 m0, s18, 65536\n"
"ds_read_b64_tr_b16 a[240:241], v219 offset:33552\n"
"v_mfma_f32_16x16x32_bf16 a[8:11], a[172:175], v[136:139], a[8:11]\n"
"ds_read_b64_tr_b16 a[242:243], v219 offset:34576\n"
"buffer_load_dwordx4 v246, s[8:11], s16 offen offset:0 lds\n"
"v_mfma_f32_16x16x32_bf16 a[12:15], a[168:171], v[132:135], a[12:15]\n"
"ds_read_b64_tr_b16 a[244:245], v219 offset:37648\n"
"v_accvgpr_read_b32 v176, a0\n"
"v_mfma_f32_16x16x32_bf16 a[12:15], a[172:175], v[140:143], a[12:15]\n"
"ds_read_b64_tr_b16 a[246:247], v219 offset:38672\n"
"v_accvgpr_read_b32 v177, a1\n"
"v_mfma_f32_16x16x32_bf16 a[16:19], a[176:179], v[128:131], a[16:19]\n"
"ds_read_b64_tr_b16 a[248:249], v219 offset:33560\n"
"v_accvgpr_read_b32 v178, a2\n"
"v_mfma_f32_16x16x32_bf16 a[16:19], a[180:183], v[136:139], a[16:19]\n"
"ds_read_b64_tr_b16 a[250:251], v219 offset:34584\n"
"v_accvgpr_read_b32 v179, a3\n"
"v_mfma_f32_16x16x32_bf16 a[20:23], a[176:179], v[132:135], a[20:23]\n"
"ds_read_b64_tr_b16 a[252:253], v219 offset:37656\n"
"v_cvt_pk_bf16_f32 v64, v176, v177\n"
"v_mfma_f32_16x16x32_bf16 a[20:23], a[180:183], v[140:143], a[20:23]\n"
"s_waitcnt lgkmcnt(11)\n"
"ds_read_b64_tr_b16 a[254:255], v219 offset:38680\n"
"buffer_load_dwordx4 v246, s[8:11], s16 offen offset:1024 lds\n"
"v_mfma_f32_16x16x32_bf16 a[24:27], a[184:187], v[128:131], a[24:27]\n"
"ds_read_b64_tr_b16 a[160:161], v219 offset:40976\n"
"v_cvt_pk_bf16_f32 v65, v178, v179\n"
"v_mfma_f32_16x16x32_bf16 a[24:27], a[188:191], v[136:139], a[24:27]\n"
"ds_read_b64_tr_b16 a[162:163], v219 offset:42000\n"
"v_accvgpr_read_b32 v180, a4\n"
"v_mfma_f32_16x16x32_bf16 a[28:31], a[184:187], v[132:135], a[28:31]\n"
"ds_read_b64_tr_b16 a[164:165], v219 offset:45072\n"
"v_accvgpr_read_b32 v181, a5\n"
"v_mfma_f32_16x16x32_bf16 a[28:31], a[188:191], v[140:143], a[28:31]\n"
"ds_read_b64_tr_b16 a[166:167], v219 offset:46096\n"
"v_accvgpr_read_b32 v182, a6\n"
"v_mfma_f32_16x16x32_bf16 a[32:35], a[224:227], v[128:131], a[32:35]\n"
"ds_read_b64_tr_b16 a[168:169], v219 offset:40984\n"
"v_accvgpr_read_b32 v183, a7\n"
"v_mfma_f32_16x16x32_bf16 a[32:35], a[228:231], v[136:139], a[32:35]\n"
"ds_read_b64_tr_b16 a[170:171], v219 offset:42008\n"
"buffer_load_dwordx4 v247, s[8:11], s16 offen offset:2048 lds\n"
"v_mfma_f32_16x16x32_bf16 a[36:39], a[224:227], v[132:135], a[36:39]\n"
"ds_read_b64_tr_b16 a[172:173], v219 offset:45080\n"
"v_cvt_pk_bf16_f32 v96, v180, v181\n"
"v_mfma_f32_16x16x32_bf16 a[36:39], a[228:231], v[140:143], a[36:39]\n"
"s_waitcnt lgkmcnt(11)\n"
"ds_read_b64_tr_b16 a[174:175], v219 offset:46104\n"
"v_cvt_pk_bf16_f32 v97, v182, v183\n"
"v_mfma_f32_16x16x32_bf16 a[40:43], a[232:235], v[128:131], a[40:43]\n"
"ds_read_b64_tr_b16 a[176:177], v219 offset:41232\n"
"v_accvgpr_read_b32 v184, a8\n"
"v_mfma_f32_16x16x32_bf16 a[40:43], a[236:239], v[136:139], a[40:43]\n"
"ds_read_b64_tr_b16 a[178:179], v219 offset:42256\n"
"v_accvgpr_read_b32 v185, a9\n"
"v_mfma_f32_16x16x32_bf16 a[44:47], a[232:235], v[132:135], a[44:47]\n"
"ds_read_b64_tr_b16 a[180:181], v219 offset:45328\n"
"v_accvgpr_read_b32 v186, a10\n"
"v_mfma_f32_16x16x32_bf16 a[44:47], a[236:239], v[140:143], a[44:47]\n"
"ds_read_b64_tr_b16 a[182:183], v219 offset:46352\n"
"buffer_load_dwordx4 v247, s[8:11], s16 offen offset:3072 lds\n"
"v_mfma_f32_16x16x32_bf16 a[48:51], a[240:243], v[128:131], a[48:51]\n"
"ds_read_b64_tr_b16 a[184:185], v219 offset:41240\n"
"v_accvgpr_read_b32 v187, a11\n"
"v_mfma_f32_16x16x32_bf16 a[48:51], a[244:247], v[136:139], a[48:51]\n"
"ds_read_b64_tr_b16 a[186:187], v219 offset:42264\n"
"v_cvt_pk_bf16_f32 v66, v184, v185\n"
"v_mfma_f32_16x16x32_bf16 a[52:55], a[240:243], v[132:135], a[52:55]\n"
"ds_read_b64_tr_b16 a[188:189], v219 offset:45336\n"
"v_cvt_pk_bf16_f32 v67, v186, v187\n"
"v_mfma_f32_16x16x32_bf16 a[52:55], a[244:247], v[140:143], a[52:55]\n"
"s_waitcnt lgkmcnt(11)\n"
"ds_read_b64_tr_b16 a[190:191], v219 offset:46360\n"
"v_accvgpr_read_b32 v188, a12\n"
"v_mfma_f32_16x16x32_bf16 a[56:59], a[248:251], v[128:131], a[56:59]\n"
"s_add_u32 m0, s18, 81920\n"
"ds_read_b64_tr_b16 a[224:225], v219 offset:41488\n"
"v_accvgpr_read_b32 v189, a13\n"
"v_mfma_f32_16x16x32_bf16 a[56:59], a[252:255], v[136:139], a[56:59]\n"
"ds_read_b64_tr_b16 a[226:227], v219 offset:42512\n"
"buffer_load_dwordx4 v246, s[12:15], s16 offen offset:0 lds\n"
"v_mfma_f32_16x16x32_bf16 a[60:63], a[248:251], v[132:135], a[60:63]\n"
"ds_read_b64_tr_b16 a[228:229], v219 offset:45584\n"
"v_accvgpr_read_b32 v190, a14\n"
"v_mfma_f32_16x16x32_bf16 a[60:63], a[252:255], v[140:143], a[60:63]\n"
"ds_read_b64_tr_b16 a[230:231], v219 offset:46608\n"
"v_accvgpr_read_b32 v191, a15\n"
"v_mfma_f32_16x16x32_bf16 a[64:67], a[160:163], v[128:131], a[64:67]\n"
"ds_read_b64_tr_b16 a[232:233], v219 offset:41496\n"
"v_cvt_pk_bf16_f32 v98, v188, v189\n"
"v_mfma_f32_16x16x32_bf16 a[64:67], a[164:167], v[136:139], a[64:67]\n"
"ds_read_b64_tr_b16 a[234:235], v219 offset:42520\n"
"v_cvt_pk_bf16_f32 v99, v190, v191\n"
"v_mfma_f32_16x16x32_bf16 a[68:71], a[160:163], v[132:135], a[68:71]\n"
"ds_read_b64_tr_b16 a[236:237], v219 offset:45592\n"
"v_accvgpr_read_b32 v176, a16\n"
"v_mfma_f32_16x16x32_bf16 a[68:71], a[164:167], v[140:143], a[68:71]\n"
"s_waitcnt lgkmcnt(11)\n"
"ds_read_b64_tr_b16 a[238:239], v219 offset:46616\n"
"buffer_load_dwordx4 v247, s[12:15], s16 offen offset:1024 lds\n"
"v_mfma_f32_16x16x32_bf16 a[72:75], a[168:171], v[128:131], a[72:75]\n"
"ds_read_b64_tr_b16 a[240:241], v219 offset:41744\n"
"v_accvgpr_read_b32 v177, a17\n"
"v_mfma_f32_16x16x32_bf16 a[72:75], a[172:175], v[136:139], a[72:75]\n"
"ds_read_b64_tr_b16 a[242:243], v219 offset:42768\n"
"v_accvgpr_read_b32 v178, a18\n"
"v_mfma_f32_16x16x32_bf16 a[76:79], a[168:171], v[132:135], a[76:79]\n"
"ds_read_b64_tr_b16 a[244:245], v219 offset:45840\n"
"v_accvgpr_read_b32 v179, a19\n"
"v_mfma_f32_16x16x32_bf16 a[76:79], a[172:175], v[140:143], a[76:79]\n"
"ds_read_b64_tr_b16 a[246:247], v219 offset:46864\n"
"v_cvt_pk_bf16_f32 v68, v176, v177\n"
"v_mfma_f32_16x16x32_bf16 a[80:83], a[176:179], v[128:131], a[80:83]\n"
"ds_read_b64_tr_b16 a[248:249], v219 offset:41752\n"
"v_cvt_pk_bf16_f32 v69, v178, v179\n"
"v_mfma_f32_16x16x32_bf16 a[80:83], a[180:183], v[136:139], a[80:83]\n"
"ds_read_b64_tr_b16 a[250:251], v219 offset:42776\n"
"buffer_load_dwordx4 v246, s[12:15], s16 offen offset:2048 lds\n"
"v_mfma_f32_16x16x32_bf16 a[84:87], a[176:179], v[132:135], a[84:87]\n"
"ds_read_b64_tr_b16 a[252:253], v219 offset:45848\n"
"v_accvgpr_read_b32 v180, a20\n"
"v_mfma_f32_16x16x32_bf16 a[84:87], a[180:183], v[140:143], a[84:87]\n"
"s_waitcnt lgkmcnt(11)\n"
"ds_read_b64_tr_b16 a[254:255], v219 offset:46872\n"
"v_accvgpr_read_b32 v181, a21\n"
"v_mfma_f32_16x16x32_bf16 a[88:91], a[184:187], v[128:131], a[88:91]\n"
"ds_read_b128 a[128:131], v240 offset:0\n"
"v_accvgpr_read_b32 v182, a22\n"
"v_mfma_f32_16x16x32_bf16 a[88:91], a[188:191], v[136:139], a[88:91]\n"
"ds_read_b128 a[132:135], v240 offset:256\n"
"v_accvgpr_read_b32 v183, a23\n"
"v_mfma_f32_16x16x32_bf16 a[92:95], a[184:187], v[132:135], a[92:95]\n"
"ds_read_b128 a[136:139], v240 offset:512\n"
"v_cvt_pk_bf16_f32 v100, v180, v181\n"
"v_mfma_f32_16x16x32_bf16 a[92:95], a[188:191], v[140:143], a[92:95]\n"
"buffer_load_dwordx4 v247, s[12:15], s16 offen offset:3072 lds\n"
"ds_read_b128 a[140:143], v240 offset:768\n"
"v_mfma_f32_16x16x32_bf16 a[96:99], a[224:227], v[128:131], a[96:99]\n"
"s_add_u32 s16, s16, 16384\n"
"ds_read_b128 a[144:147], v240 offset:4096\n"
"v_cvt_pk_bf16_f32 v101, v182, v183\n"
"v_mfma_f32_16x16x32_bf16 a[96:99], a[228:231], v[136:139], a[96:99]\n"
"ds_read_b128 a[148:151], v240 offset:4352\n"
"v_accvgpr_read_b32 v184, a24\n"
"v_mfma_f32_16x16x32_bf16 a[100:103], a[224:227], v[132:135], a[100:103]\n"
"ds_read_b128 a[152:155], v240 offset:4608\n"
"v_accvgpr_read_b32 v185, a25\n"
"v_mfma_f32_16x16x32_bf16 a[100:103], a[228:231], v[140:143], a[100:103]\n"
"s_waitcnt lgkmcnt(11)\n"
"ds_read_b128 a[156:159], v240 offset:4864\n"
"v_accvgpr_read_b32 v186, a26\n"
"v_mfma_f32_16x16x32_bf16 a[104:107], a[232:235], v[128:131], a[104:107]\n"
"ds_read_b64_tr_b16 a[192:193], v242 offset:16384\n"
"v_accvgpr_read_b32 v187, a27\n"
"v_mfma_f32_16x16x32_bf16 a[104:107], a[236:239], v[136:139], a[104:107]\n"
"ds_read_b64_tr_b16 a[194:195], v242 offset:20480\n"
"v_cvt_pk_bf16_f32 v70, v184, v185\n"
"v_mfma_f32_16x16x32_bf16 a[108:111], a[232:235], v[132:135], a[108:111]\n"
"ds_read_b64_tr_b16 a[196:197], v243 offset:16384\n"
"v_cvt_pk_bf16_f32 v71, v186, v187\n"
"v_mfma_f32_16x16x32_bf16 a[108:111], a[236:239], v[140:143], a[108:111]\n"
"ds_read_b64_tr_b16 a[198:199], v243 offset:20480\n"
"v_accvgpr_read_b32 v188, a28\n"
"v_mfma_f32_16x16x32_bf16 a[112:115], a[240:243], v[128:131], a[112:115]\n"
"ds_read_b64_tr_b16 a[200:201], v242 offset:16640\n"
"v_accvgpr_read_b32 v189, a29\n"
"v_mfma_f32_16x16x32_bf16 a[112:115], a[244:247], v[136:139], a[112:115]\n"
"ds_read_b64_tr_b16 a[202:203], v242 offset:20736\n"
"v_accvgpr_read_b32 v190, a30\n"
"v_mfma_f32_16x16x32_bf16 a[116:119], a[240:243], v[132:135], a[116:119]\n"
"ds_read_b64_tr_b16 a[204:205], v243 offset:16640\n"
"v_accvgpr_read_b32 v191, a31\n"
"v_mfma_f32_16x16x32_bf16 a[116:119], a[244:247], v[140:143], a[116:119]\n"
"s_waitcnt lgkmcnt(11)\n"
"ds_read_b64_tr_b16 a[206:207], v243 offset:20736\n"
"v_cvt_pk_bf16_f32 v102, v188, v189\n"
"v_mfma_f32_16x16x32_bf16 a[120:123], a[248:251], v[128:131], a[120:123]\n"
"ds_read_b64_tr_b16 a[208:209], v242 offset:16896\n"
"v_cvt_pk_bf16_f32 v103, v190, v191\n"
"v_mfma_f32_16x16x32_bf16 a[120:123], a[252:255], v[136:139], a[120:123]\n"
"ds_read_b64_tr_b16 a[210:211], v242 offset:20992\n"
"v_accvgpr_read_b32 v176, a32\n"
"v_mfma_f32_16x16x32_bf16 a[124:127], a[248:251], v[132:135], a[124:127]\n"
"ds_read_b64_tr_b16 a[212:213], v243 offset:16896\n"
"v_accvgpr_read_b32 v177, a33\n"
"v_mfma_f32_16x16x32_bf16 a[124:127], a[252:255], v[140:143], a[124:127]\n"
"ds_read_b64_tr_b16 a[214:215], v243 offset:20992\n"
"v_accvgpr_read_b32 v178, a34\n"
"ds_read_b64_tr_b16 a[216:217], v242 offset:17152\n"
"v_accvgpr_read_b32 v179, a35\n"
"v_cvt_pk_bf16_f32 v72, v176, v177\n"
"v_cvt_pk_bf16_f32 v73, v178, v179\n"
"ds_read_b64_tr_b16 a[218:219], v242 offset:21248\n"
"v_accvgpr_read_b32 v180, a36\n"
"v_accvgpr_read_b32 v181, a37\n"
"v_accvgpr_read_b32 v182, a38\n"
"ds_read_b64_tr_b16 a[220:221], v243 offset:17152\n"
"v_accvgpr_read_b32 v183, a39\n"
"v_cvt_pk_bf16_f32 v104, v180, v181\n"
"v_cvt_pk_bf16_f32 v105, v182, v183\n"
"ds_read_b64_tr_b16 a[222:223], v243 offset:21248\n"
"v_accvgpr_read_b32 v184, a40\n"
"v_accvgpr_read_b32 v185, a41\n"
"v_accvgpr_read_b32 v186, a42\n"
"v_accvgpr_read_b32 v187, a43\n"
"v_cvt_pk_bf16_f32 v74, v184, v185\n"
"v_cvt_pk_bf16_f32 v75, v186, v187\n"
"v_accvgpr_read_b32 v188, a44\n"
"v_accvgpr_read_b32 v189, a45\n"
"v_accvgpr_read_b32 v190, a46\n"
"v_accvgpr_read_b32 v191, a47\n"
"v_cvt_pk_bf16_f32 v106, v188, v189\n"
"v_cvt_pk_bf16_f32 v107, v190, v191\n"
"v_accvgpr_read_b32 v176, a48\n"
"v_accvgpr_read_b32 v177, a49\n"
"v_accvgpr_read_b32 v178, a50\n"
"v_accvgpr_read_b32 v179, a51\n"
"v_cvt_pk_bf16_f32 v76, v176, v177\n"
"v_cvt_pk_bf16_f32 v77, v178, v179\n"
"v_accvgpr_read_b32 v180, a52\n"
"v_accvgpr_read_b32 v181, a53\n"
"v_accvgpr_read_b32 v182, a54\n"
"v_accvgpr_read_b32 v183, a55\n"
"v_cvt_pk_bf16_f32 v108, v180, v181\n"
"v_cvt_pk_bf16_f32 v109, v182, v183\n"
"v_accvgpr_read_b32 v184, a56\n"
"v_accvgpr_read_b32 v185, a57\n"
"v_accvgpr_read_b32 v186, a58\n"
"v_accvgpr_read_b32 v187, a59\n"
"v_cvt_pk_bf16_f32 v78, v184, v185\n"
"v_cvt_pk_bf16_f32 v79, v186, v187\n"
"v_accvgpr_read_b32 v188, a60\n"
"v_accvgpr_read_b32 v189, a61\n"
"v_accvgpr_read_b32 v190, a62\n"
"v_accvgpr_read_b32 v191, a63\n"
"v_cvt_pk_bf16_f32 v110, v188, v189\n"
"v_cvt_pk_bf16_f32 v111, v190, v191\n"
"v_accvgpr_read_b32 v176, a64\n"
"v_accvgpr_read_b32 v177, a65\n"
"v_accvgpr_read_b32 v178, a66\n"
"v_accvgpr_read_b32 v179, a67\n"
"v_cvt_pk_bf16_f32 v80, v176, v177\n"
"v_cvt_pk_bf16_f32 v81, v178, v179\n"
"v_accvgpr_read_b32 v180, a68\n"
"v_accvgpr_read_b32 v181, a69\n"
"v_accvgpr_read_b32 v182, a70\n"
"v_accvgpr_read_b32 v183, a71\n"
"v_cvt_pk_bf16_f32 v112, v180, v181\n"
"v_cvt_pk_bf16_f32 v113, v182, v183\n"
"v_accvgpr_read_b32 v184, a72\n"
"v_accvgpr_read_b32 v185, a73\n"
"v_accvgpr_read_b32 v186, a74\n"
"v_accvgpr_read_b32 v187, a75\n"
"v_cvt_pk_bf16_f32 v82, v184, v185\n"
"v_cvt_pk_bf16_f32 v83, v186, v187\n"
"v_accvgpr_read_b32 v188, a76\n"
"v_accvgpr_read_b32 v189, a77\n"
"v_accvgpr_read_b32 v190, a78\n"
"v_accvgpr_read_b32 v191, a79\n"
"v_cvt_pk_bf16_f32 v114, v188, v189\n"
"v_cvt_pk_bf16_f32 v115, v190, v191\n"
"v_accvgpr_read_b32 v176, a80\n"
"v_accvgpr_read_b32 v177, a81\n"
"v_accvgpr_read_b32 v178, a82\n"
"v_accvgpr_read_b32 v179, a83\n"
"v_cvt_pk_bf16_f32 v84, v176, v177\n"
"v_cvt_pk_bf16_f32 v85, v178, v179\n"
"v_accvgpr_read_b32 v180, a84\n"
"v_accvgpr_read_b32 v181, a85\n"
"v_accvgpr_read_b32 v182, a86\n"
"v_accvgpr_read_b32 v183, a87\n"
"v_cvt_pk_bf16_f32 v116, v180, v181\n"
"v_cvt_pk_bf16_f32 v117, v182, v183\n"
"v_accvgpr_read_b32 v184, a88\n"
"v_accvgpr_read_b32 v185, a89\n"
"v_accvgpr_read_b32 v186, a90\n"
"v_accvgpr_read_b32 v187, a91\n"
"v_cvt_pk_bf16_f32 v86, v184, v185\n"
"v_cvt_pk_bf16_f32 v87, v186, v187\n"
"v_accvgpr_read_b32 v188, a92\n"
"v_accvgpr_read_b32 v189, a93\n"
"v_accvgpr_read_b32 v190, a94\n"
"v_accvgpr_read_b32 v191, a95\n"
"v_cvt_pk_bf16_f32 v118, v188, v189\n"
"v_cvt_pk_bf16_f32 v119, v190, v191\n"
"v_accvgpr_read_b32 v176, a96\n"
"v_accvgpr_read_b32 v177, a97\n"
"v_accvgpr_read_b32 v178, a98\n"
"v_accvgpr_read_b32 v179, a99\n"
"v_cvt_pk_bf16_f32 v88, v176, v177\n"
"v_cvt_pk_bf16_f32 v89, v178, v179\n"
"v_accvgpr_read_b32 v180, a100\n"
"v_accvgpr_read_b32 v181, a101\n"
"v_accvgpr_read_b32 v182, a102\n"
"v_accvgpr_read_b32 v183, a103\n"
"v_cvt_pk_bf16_f32 v120, v180, v181\n"
"v_cvt_pk_bf16_f32 v121, v182, v183\n"
"v_accvgpr_read_b32 v184, a104\n"
"v_accvgpr_read_b32 v185, a105\n"
"v_accvgpr_read_b32 v186, a106\n"
"v_accvgpr_read_b32 v187, a107\n"
"v_cvt_pk_bf16_f32 v90, v184, v185\n"
"v_cvt_pk_bf16_f32 v91, v186, v187\n"
"v_accvgpr_read_b32 v188, a108\n"
"v_accvgpr_read_b32 v189, a109\n"
"v_accvgpr_read_b32 v190, a110\n"
"v_accvgpr_read_b32 v191, a111\n"
"v_cvt_pk_bf16_f32 v122, v188, v189\n"
"v_cvt_pk_bf16_f32 v123, v190, v191\n"
"v_accvgpr_read_b32 v176, a112\n"
"v_accvgpr_read_b32 v177, a113\n"
"v_accvgpr_read_b32 v178, a114\n"
"v_accvgpr_read_b32 v179, a115\n"
"v_cvt_pk_bf16_f32 v92, v176, v177\n"
"v_cvt_pk_bf16_f32 v93, v178, v179\n"
"v_accvgpr_read_b32 v180, a116\n"
"v_accvgpr_read_b32 v181, a117\n"
"v_accvgpr_read_b32 v182, a118\n"
"v_accvgpr_read_b32 v183, a119\n"
"v_cvt_pk_bf16_f32 v124, v180, v181\n"
"v_cvt_pk_bf16_f32 v125, v182, v183\n"
"v_accvgpr_read_b32 v184, a120\n"
"v_accvgpr_read_b32 v185, a121\n"
"v_accvgpr_read_b32 v186, a122\n"
"v_accvgpr_read_b32 v187, a123\n"
"v_cvt_pk_bf16_f32 v94, v184, v185\n"
"v_cvt_pk_bf16_f32 v95, v186, v187\n"
"v_accvgpr_read_b32 v188, a124\n"
"v_accvgpr_read_b32 v189, a125\n"
"v_accvgpr_read_b32 v190, a126\n"
"v_accvgpr_read_b32 v191, a127\n"
"v_cvt_pk_bf16_f32 v126, v188, v189\n"
"v_cvt_pk_bf16_f32 v127, v190, v191\n"
"s_barrier\n"
"s_add_u32 s56, s40, 98304\n"
"s_nop 1\n"
"v_mfma_f32_16x16x32_bf16 v[128:131], a[128:131], v[64:67], 0\n"
"s_add_u32 m0, s18, 98304\n"
"v_and_b32 v232, 1, v226\n"
"v_lshlrev_b32 v204, 6, v232\n"
"v_mfma_f32_16x16x32_bf16 v[128:131], a[132:135], v[68:71], v[128:131]\n"
"buffer_load_dwordx4 v246, s[8:11], s16 offen offset:0 lds\n"
"v_lshlrev_b32 v192, 5, v232\n"
"v_mfma_f32_16x16x32_bf16 v[128:131], a[136:139], v[72:75], v[128:131]\n"
"v_and_b32 v232, 2, v226\n"
"v_xor_b32 v233, v232, v228\n"
"v_mfma_f32_16x16x32_bf16 v[128:131], a[140:143], v[76:79], v[128:131]\n"
"s_waitcnt lgkmcnt(12)\n"
"buffer_load_dwordx4 v246, s[8:11], s16 offen offset:1024 lds\n"
"v_lshl_add_u32 v204, v233, 4, v204\n"
"v_mfma_f32_16x16x32_bf16 v[132:135], a[144:147], v[64:67], 0\n"
"v_lshl_add_u32 v232, v232, 1, v227\n"
"v_mul_u32_u24 v232, 1152, v232\n"
"v_mfma_f32_16x16x32_bf16 v[132:135], a[148:151], v[68:71], v[132:135]\n"
"buffer_load_dwordx4 v247, s[8:11], s16 offen offset:2048 lds\n"
"v_add_u32 v204, v232, v204\n"
"v_mfma_f32_16x16x32_bf16 v[132:135], a[152:155], v[72:75], v[132:135]\n"
"v_lshl_add_u32 v192, v226, 9, v192\n"
"v_lshl_add_u32 v192, v227, 6, v192\n"
"v_mfma_f32_16x16x32_bf16 v[132:135], a[156:159], v[76:79], v[132:135]\n"
"buffer_load_dwordx4 v247, s[8:11], s16 offen offset:3072 lds\n"
"v_lshl_add_u32 v192, v228, 3, v192\n"
"v_mfma_f32_16x16x32_bf16 v[136:139], a[128:131], v[80:83], 0\n"
"s_add_u32 m0, s18, 114688\n"
"v_add_u32 v192, s40, v192\n"
"v_xor_b32 v195, 32, v192\n"
"v_mfma_f32_16x16x32_bf16 v[136:139], a[132:135], v[84:87], v[136:139]\n"
"buffer_load_dwordx4 v246, s[12:15], s16 offen offset:0 lds\n"
"v_add_u32 v193, 65536, v192\n"
"v_mfma_f32_16x16x32_bf16 v[136:139], a[136:139], v[88:91], v[136:139]\n"
"v_add_u32 v196, 65536, v195\n"
"v_add_u32 v194, 131072, v192\n"
"v_mfma_f32_16x16x32_bf16 v[136:139], a[140:143], v[92:95], v[136:139]\n"
"buffer_load_dwordx4 v247, s[12:15], s16 offen offset:1024 lds\n"
"v_add_u32 v197, 131072, v195\n"
"v_mfma_f32_16x16x32_bf16 v[140:143], a[144:147], v[80:83], 0\n"
"v_lshlrev_b32 v205, 4, v248\n"
"v_add_u32 v206, 18432, v205\n"
"v_mfma_f32_16x16x32_bf16 v[140:143], a[148:151], v[84:87], v[140:143]\n"
"buffer_load_dwordx4 v246, s[12:15], s16 offen offset:2048 lds\n"
"v_lshl_add_u32 v207, v228, 2, s56\n"
"v_mfma_f32_16x16x32_bf16 v[140:143], a[152:155], v[88:91], v[140:143]\n"
"v_mov_b32 v40, 0x3f803f80\n"
"v_mov_b32 v41, 0x3f803f80\n"
"v_mfma_f32_16x16x32_bf16 v[140:143], a[156:159], v[92:95], v[140:143]\n"
"buffer_load_dwordx4 v247, s[12:15], s16 offen offset:3072 lds\n"
"v_mov_b32 v42, 0x3f803f80\n"
"v_mfma_f32_16x16x32_bf16 v[144:147], a[128:131], v[96:99], 0\n"
"s_add_u32 s16, s16, 16384\n"
"s_add_u32 m0, s53, 131072\n"
"v_mov_b32 v43, 0x3f803f80\n"
"v_mfma_f32_16x16x32_bf16 v[144:147], a[132:135], v[100:103], v[144:147]\n"
"buffer_load_dwordx4 v204, s[28:31], s52 offen offset:0 lds\n"
"v_mfma_f32_16x16x32_bf16 v[144:147], a[136:139], v[104:107], v[144:147]\n"
"s_add_u32 s52, s52, 8192\n"
"v_mfma_f32_16x16x32_bf16 v[144:147], a[140:143], v[108:111], v[144:147]\n"
"buffer_load_dwordx4 v204, s[28:31], s52 offen offset:1024 lds\n"
"v_mfma_f32_16x16x32_bf16 v[148:151], a[144:147], v[96:99], 0\n"
"s_add_u32 s52, s52, 8192\n"
"v_mfma_f32_16x16x32_bf16 v[148:151], a[148:151], v[100:103], v[148:151]\n"
"buffer_load_dwordx4 v204, s[28:31], s52 offen offset:2048 lds\n"
"v_mfma_f32_16x16x32_bf16 v[148:151], a[152:155], v[104:107], v[148:151]\n"
"s_add_u32 s52, s52, 8192\n"
"v_mfma_f32_16x16x32_bf16 v[148:151], a[156:159], v[108:111], v[148:151]\n"
"buffer_load_dwordx4 v204, s[28:31], s52 offen offset:3072 lds\n"
"v_mfma_f32_16x16x32_bf16 v[152:155], a[128:131], v[112:115], 0\n"
"s_add_u32 s52, s52, 12288\n"
"s_add_u32 m0, s53, 135168\n"
"v_mfma_f32_16x16x32_bf16 v[152:155], a[132:135], v[116:119], v[152:155]\n"
"buffer_load_dwordx4 v204, s[28:31], s52 offen offset:0 lds\n"
"v_mfma_f32_16x16x32_bf16 v[152:155], a[136:139], v[120:123], v[152:155]\n"
"s_add_u32 s52, s52, 8192\n"
"v_mfma_f32_16x16x32_bf16 v[152:155], a[140:143], v[124:127], v[152:155]\n"
"buffer_load_dwordx4 v204, s[28:31], s52 offen offset:1024 lds\n"
"v_mfma_f32_16x16x32_bf16 v[156:159], a[144:147], v[112:115], 0\n"
"s_add_u32 s52, s52, 8192\n"
"v_mfma_f32_16x16x32_bf16 v[156:159], a[148:151], v[116:119], v[156:159]\n"
"buffer_load_dwordx4 v204, s[28:31], s52 offen offset:2048 lds\n"
"v_mfma_f32_16x16x32_bf16 v[156:159], a[152:155], v[120:123], v[156:159]\n"
"s_add_u32 s52, s52, 8192\n"
"v_mfma_f32_16x16x32_bf16 v[156:159], a[156:159], v[124:127], v[156:159]\n"
"buffer_load_dwordx4 v204, s[28:31], s52 offen offset:3072 lds\n"
"s_sub_u32 s52, s52, 61312\n"
"v_max3_f32 v160, v128, v129, v130\n"
"v_max3_f32 v160, v160, v131, v132\n"
"v_max3_f32 v160, v160, v133, v134\n"
"v_max_f32 v160, v160, v135\n"
"v_max3_f32 v161, v136, v137, v138\n"
"v_max3_f32 v161, v161, v139, v140\n"
"v_max3_f32 v161, v161, v141, v142\n"
"v_max_f32 v161, v161, v143\n"
"v_max3_f32 v162, v144, v145, v146\n"
"v_max3_f32 v162, v162, v147, v148\n"
"v_max3_f32 v162, v162, v149, v150\n"
"v_max_f32 v162, v162, v151\n"
"v_max3_f32 v163, v152, v153, v154\n"
"v_max3_f32 v163, v163, v155, v156\n"
"v_max3_f32 v163, v163, v157, v158\n"
"v_max_f32 v163, v163, v159\n"
"v_mov_b32 v164, v160\n"
"v_mov_b32 v165, v161\n"
"v_mov_b32 v166, v162\n"
"v_mov_b32 v167, v163\n"
"s_nop 0\n"
"v_permlane32_swap_b32 v160, v164\n"
"v_permlane32_swap_b32 v161, v165\n"
"v_permlane32_swap_b32 v162, v166\n"
"v_permlane32_swap_b32 v163, v167\n"
"v_max_f32 v160, v160, v164\n"
"v_max_f32 v161, v161, v165\n"
"v_max_f32 v162, v162, v166\n"
"v_max_f32 v163, v163, v167\n"
"v_mov_b32 v164, v160\n"
"v_mov_b32 v165, v161\n"
"v_mov_b32 v166, v162\n"
"v_mov_b32 v167, v163\n"
"s_nop 0\n"
"v_permlane16_swap_b32 v160, v164\n"
"v_permlane16_swap_b32 v161, v165\n"
"v_permlane16_swap_b32 v162, v166\n"
"v_permlane16_swap_b32 v163, v167\n"
"v_max_f32 v160, v160, v164\n"
"v_max_f32 v161, v161, v165\n"
"v_max_f32 v162, v162, v166\n"
"v_max_f32 v163, v163, v167\n"
"v_sub_f32 v24, 0xc1f00000, v160\n"
"v_sub_f32 v25, 0xc1f00000, v160\n"
"v_sub_f32 v26, 0xc1f00000, v160\n"
"v_sub_f32 v27, 0xc1f00000, v160\n"
"v_sub_f32 v28, 0xc1f00000, v161\n"
"v_sub_f32 v29, 0xc1f00000, v161\n"
"v_sub_f32 v30, 0xc1f00000, v161\n"
"v_sub_f32 v31, 0xc1f00000, v161\n"
"v_sub_f32 v32, 0xc1f00000, v162\n"
"v_sub_f32 v33, 0xc1f00000, v162\n"
"v_sub_f32 v34, 0xc1f00000, v162\n"
"v_sub_f32 v35, 0xc1f00000, v162\n"
"v_sub_f32 v36, 0xc1f00000, v163\n"
"v_sub_f32 v37, 0xc1f00000, v163\n"
"v_sub_f32 v38, 0xc1f00000, v163\n"
"v_sub_f32 v39, 0xc1f00000, v163\n"
"s_nop 1\n"
"s_waitcnt lgkmcnt(0)\n"
"AT_main:\n"
"v_mfma_f32_16x16x32_bf16 v[0:3], a[128:131], v[64:67], v[24:27]\n"
"ds_read_b128 a[160:163], v240 offset:8192\n"
"v_mfma_f32_16x16x32_bf16 v[0:3], a[132:135], v[68:71], v[0:3]\n"
"s_nop 0\n"
"v_mfma_f32_16x16x32_bf16 v[0:3], a[136:139], v[72:75], v[0:3]\n"
"ds_read_b128 a[164:167], v240 offset:8448\n"
"v_mfma_f32_16x16x32_bf16 v[0:3], a[140:143], v[76:79], v[0:3]\n"
"s_nop 0\n"
"v_mfma_f32_16x16x32_bf16 v[4:7], a[144:147], v[64:67], v[24:27]\n"
"ds_read_b128 a[168:171], v240 offset:8704\n"
"v_mfma_f32_16x16x32_bf16 v[4:7], a[148:151], v[68:71], v[4:7]\n"
"s_nop 0\n"
"v_mfma_f32_16x16x32_bf16 v[4:7], a[152:155], v[72:75], v[4:7]\n"
"ds_read_b128 a[172:175], v240 offset:8960\n"
"v_mfma_f32_16x16x32_bf16 v[4:7], a[156:159], v[76:79], v[4:7]\n"
"s_nop 0\n"
"s_nop 1\n"
"v_exp_f32 v0, v0\n"
"s_nop 1\n"
"ds_read_b128 a[176:179], v240 offset:12288\n"
"s_nop 1\n"
"v_exp_f32 v1, v1\n"
"s_nop 1\n"
"ds_read_b128 a[180:183], v240 offset:12544\n"
"v_cvt_pk_bf16_f32 v16, v0, v1\n"
"s_nop 1\n"
"v_exp_f32 v2, v2\n"
"s_nop 1\n"
"ds_read_b128 a[184:187], v240 offset:12800\n"
"s_nop 1\n"
"v_exp_f32 v3, v3\n"
"s_nop 1\n"
"v_cvt_pk_bf16_f32 v17, v2, v3\n"
"s_nop 1\n"
"v_exp_f32 v4, v4\n"
"v_mfma_f32_16x16x32_bf16 v[8:11], a[128:131], v[80:83], v[28:31]\n"
"ds_read_b128 a[188:191], v240 offset:13056\n"
"v_mfma_f32_16x16x32_bf16 v[8:11], a[132:135], v[84:87], v[8:11]\n"
"v_exp_f32 v5, v5\n"
"v_mfma_f32_16x16x32_bf16 v[8:11], a[136:139], v[88:91], v[8:11]\n"
"ds_read_b64_tr_b16 a[224:225], v242 offset:24576\n"
"v_cvt_pk_bf16_f32 v18, v4, v5\n"
"v_mfma_f32_16x16x32_bf16 v[8:11], a[140:143], v[92:95], v[8:11]\n"
"v_exp_f32 v6, v6\n"
"v_mfma_f32_16x16x32_bf16 v[12:15], a[144:147], v[80:83], v[28:31]\n"
"ds_read_b64_tr_b16 a[226:227], v242 offset:28672\n"
"v_mfma_f32_16x16x32_bf16 v[12:15], a[148:151], v[84:87], v[12:15]\n"
"v_exp_f32 v7, v7\n"
"v_mfma_f32_16x16x32_bf16 v[12:15], a[152:155], v[88:91], v[12:15]\n"
"ds_read_b64_tr_b16 a[228:229], v243 offset:24576\n"
"v_cvt_pk_bf16_f32 v19, v6, v7\n"
"v_mfma_f32_16x16x32_bf16 v[12:15], a[156:159], v[92:95], v[12:15]\n"
"s_nop 0\n"
"v_mfma_f32_16x16x32_bf16 a[0:3], a[192:195], v[16:19], 0\n"
"v_exp_f32 v8, v8\n"
"v_mfma_f32_16x16x32_bf16 a[4:7], a[196:199], v[16:19], 0\n"
"ds_read_b64_tr_b16 a[230:231], v243 offset:28672\n"
"v_mfma_f32_16x16x32_bf16 a[8:11], a[200:203], v[16:19], 0\n"
"v_exp_f32 v9, v9\n"
"v_mfma_f32_16x16x32_bf16 a[12:15], a[204:207], v[16:19], 0\n"
"ds_read_b64_tr_b16 a[232:233], v242 offset:24832\n"
"v_cvt_pk_bf16_f32 v20, v8, v9\n"
"v_mfma_f32_16x16x32_bf16 a[16:19], a[208:211], v[16:19], 0\n"
"v_exp_f32 v10, v10\n"
"v_mfma_f32_16x16x32_bf16 a[20:23], a[212:215], v[16:19], 0\n"
"ds_read_b64_tr_b16 a[234:235], v242 offset:28928\n"
"v_mfma_f32_16x16x32_bf16 a[24:27], a[216:219], v[16:19], 0\n"
"v_exp_f32 v11, v11\n"
"v_mfma_f32_16x16x32_bf16 a[28:31], a[220:223], v[16:19], 0\n"
"v_cvt_pk_bf16_f32 v21, v10, v11\n"
"v_mfma_f32_16x16x32_bf16 v[44:47], v[40:43], v[16:19], 0\n"
"v_exp_f32 v12, v12\n"
"v_mfma_f32_16x16x32_bf16 v[0:3], a[128:131], v[96:99], v[32:35]\n"
"ds_read_b64_tr_b16 a[236:237], v243 offset:24832\n"
"v_mfma_f32_16x16x32_bf16 v[0:3], a[132:135], v[100:103], v[0:3]\n"
"v_exp_f32 v13, v13\n"
"v_mfma_f32_16x16x32_bf16 v[0:3], a[136:139], v[104:107], v[0:3]\n"
"ds_read_b64_tr_b16 a[238:239], v243 offset:28928\n"
"v_cvt_pk_bf16_f32 v22, v12, v13\n"
"v_mfma_f32_16x16x32_bf16 v[0:3], a[140:143], v[108:111], v[0:3]\n"
"v_exp_f32 v14, v14\n"
"v_mfma_f32_16x16x32_bf16 v[4:7], a[144:147], v[96:99], v[32:35]\n"
"ds_read_b64_tr_b16 a[240:241], v242 offset:25088\n"
"v_mfma_f32_16x16x32_bf16 v[4:7], a[148:151], v[100:103], v[4:7]\n"
"v_exp_f32 v15, v15\n"
"v_mfma_f32_16x16x32_bf16 v[4:7], a[152:155], v[104:107], v[4:7]\n"
"ds_read_b64_tr_b16 a[242:243], v242 offset:29184\n"
"v_cvt_pk_bf16_f32 v23, v14, v15\n"
"v_mfma_f32_16x16x32_bf16 v[4:7], a[156:159], v[108:111], v[4:7]\n"
"s_nop 0\n"
"v_mfma_f32_16x16x32_bf16 a[32:35], a[192:195], v[20:23], 0\n"
"v_exp_f32 v0, v0\n"
"v_mfma_f32_16x16x32_bf16 a[36:39], a[196:199], v[20:23], 0\n"
"ds_read_b64_tr_b16 a[244:245], v243 offset:25088\n"
"v_mfma_f32_16x16x32_bf16 a[40:43], a[200:203], v[20:23], 0\n"
"v_exp_f32 v1, v1\n"
"v_mfma_f32_16x16x32_bf16 a[44:47], a[204:207], v[20:23], 0\n"
"ds_read_b64_tr_b16 a[246:247], v243 offset:29184\n"
"v_cvt_pk_bf16_f32 v16, v0, v1\n"
"v_mfma_f32_16x16x32_bf16 a[48:51], a[208:211], v[20:23], 0\n"
"v_exp_f32 v2, v2\n"
"v_mfma_f32_16x16x32_bf16 a[52:55], a[212:215], v[20:23], 0\n"
"ds_read_b64_tr_b16 a[248:249], v242 offset:25344\n"
"v_mfma_f32_16x16x32_bf16 a[56:59], a[216:219], v[20:23], 0\n"
"v_exp_f32 v3, v3\n"
"v_mfma_f32_16x16x32_bf16 a[60:63], a[220:223], v[20:23], 0\n"
"v_cvt_pk_bf16_f32 v17, v2, v3\n"
"v_mfma_f32_16x16x32_bf16 v[48:51], v[40:43], v[20:23], 0\n"
"v_exp_f32 v4, v4\n"
"v_mfma_f32_16x16x32_bf16 v[8:11], a[128:131], v[112:115], v[36:39]\n"
"ds_read_b64_tr_b16 a[250:251], v242 offset:29440\n"
"v_mfma_f32_16x16x32_bf16 v[8:11], a[132:135], v[116:119], v[8:11]\n"
"v_exp_f32 v5, v5\n"
"v_mfma_f32_16x16x32_bf16 v[8:11], a[136:139], v[120:123], v[8:11]\n"
"ds_read_b64_tr_b16 a[252:253], v243 offset:25344\n"
"v_cvt_pk_bf16_f32 v18, v4, v5\n"
"v_mfma_f32_16x16x32_bf16 v[8:11], a[140:143], v[124:127], v[8:11]\n"
"v_exp_f32 v6, v6\n"
"v_mfma_f32_16x16x32_bf16 v[12:15], a[144:147], v[112:115], v[36:39]\n"
"ds_read_b64_tr_b16 a[254:255], v243 offset:29440\n"
"v_mfma_f32_16x16x32_bf16 v[12:15], a[148:151], v[116:119], v[12:15]\n"
"v_exp_f32 v7, v7\n"
"v_mfma_f32_16x16x32_bf16 v[12:15], a[152:155], v[120:123], v[12:15]\n"
"v_cvt_pk_bf16_f32 v19, v6, v7\n"
"v_mfma_f32_16x16x32_bf16 v[12:15], a[156:159], v[124:127], v[12:15]\n"
"s_nop 0\n"
"v_mfma_f32_16x16x32_bf16 a[64:67], a[192:195], v[16:19], 0\n"
"v_exp_f32 v8, v8\n"
"v_mfma_f32_16x16x32_bf16 a[68:71], a[196:199], v[16:19], 0\n"
"s_nop 0\n"
"v_mfma_f32_16x16x32_bf16 a[72:75], a[200:203], v[16:19], 0\n"
"v_exp_f32 v9, v9\n"
"v_mfma_f32_16x16x32_bf16 a[76:79], a[204:207], v[16:19], 0\n"
"v_cvt_pk_bf16_f32 v20, v8, v9\n"
"v_mfma_f32_16x16x32_bf16 a[80:83], a[208:211], v[16:19], 0\n"
"v_exp_f32 v10, v10\n"
"v_mfma_f32_16x16x32_bf16 a[84:87], a[212:215], v[16:19], 0\n"
"s_nop 0\n"
"v_mfma_f32_16x16x32_bf16 a[88:91], a[216:219], v[16:19], 0\n"
"v_exp_f32 v11, v11\n"
"v_mfma_f32_16x16x32_bf16 a[92:95], a[220:223], v[16:19], 0\n"
"s_waitcnt vmcnt(24) lgkmcnt(0)\n"
"s_barrier\n"
"v_cvt_pk_bf16_f32 v21, v10, v11\n"
"v_mfma_f32_16x16x32_bf16 v[52:55], v[40:43], v[16:19], 0\n"
"v_exp_f32 v12, v12\n"
"v_mfma_f32_16x16x32_bf16 v[0:3], a[160:163], v[64:67], v[24:27]\n"
"ds_read_b128 a[128:131], v240 offset:32768\n"
"v_mfma_f32_16x16x32_bf16 v[0:3], a[164:167], v[68:71], v[0:3]\n"
"v_exp_f32 v13, v13\n"
"v_mfma_f32_16x16x32_bf16 v[0:3], a[168:171], v[72:75], v[0:3]\n"
"ds_read_b128 a[132:135], v240 offset:33024\n"
"v_cvt_pk_bf16_f32 v22, v12, v13\n"
"v_mfma_f32_16x16x32_bf16 v[0:3], a[172:175], v[76:79], v[0:3]\n"
"v_exp_f32 v14, v14\n"
"v_mfma_f32_16x16x32_bf16 v[4:7], a[176:179], v[64:67], v[24:27]\n"
"ds_read_b128 a[136:139], v240 offset:33280\n"
"v_mfma_f32_16x16x32_bf16 v[4:7], a[180:183], v[68:71], v[4:7]\n"
"v_exp_f32 v15, v15\n"
"v_mfma_f32_16x16x32_bf16 v[4:7], a[184:187], v[72:75], v[4:7]\n"
"s_mov_b32 m0, s18\n"
"ds_read_b128 a[140:143], v240 offset:33536\n"
"v_cvt_pk_bf16_f32 v23, v14, v15\n"
"v_mfma_f32_16x16x32_bf16 v[4:7], a[188:191], v[76:79], v[4:7]\n"
"buffer_load_dwordx4 v246, s[8:11], s16 offen offset:0 lds\n"
"v_mfma_f32_16x16x32_bf16 a[96:99], a[192:195], v[20:23], 0\n"
"v_exp_f32 v0, v0\n"
"v_mfma_f32_16x16x32_bf16 a[100:103], a[196:199], v[20:23], 0\n"
"ds_read_b128 a[144:147], v240 offset:36864\n"
"v_mfma_f32_16x16x32_bf16 a[104:107], a[200:203], v[20:23], 0\n"
"v_exp_f32 v1, v1\n"
"v_mfma_f32_16x16x32_bf16 a[108:111], a[204:207], v[20:23], 0\n"
"ds_read_b128 a[148:151], v240 offset:37120\n"
"v_cvt_pk_bf16_f32 v16, v0, v1\n"
"v_mfma_f32_16x16x32_bf16 a[112:115], a[208:211], v[20:23], 0\n"
"v_exp_f32 v2, v2\n"
"v_mfma_f32_16x16x32_bf16 a[116:119], a[212:215], v[20:23], 0\n"
"ds_read_b128 a[152:155], v240 offset:37376\n"
"v_mfma_f32_16x16x32_bf16 a[120:123], a[216:219], v[20:23], 0\n"
"v_exp_f32 v3, v3\n"
"v_mfma_f32_16x16x32_bf16 a[124:127], a[220:223], v[20:23], 0\n"
"v_cvt_pk_bf16_f32 v17, v2, v3\n"
"v_mfma_f32_16x16x32_bf16 v[56:59], v[40:43], v[20:23], 0\n"
"v_exp_f32 v4, v4\n"
"v_mfma_f32_16x16x32_bf16 v[8:11], a[160:163], v[80:83], v[28:31]\n"
"ds_read_b128 a[156:159], v240 offset:37632\n"
"v_mfma_f32_16x16x32_bf16 v[8:11], a[164:167], v[84:87], v[8:11]\n"
"v_exp_f32 v5, v5\n"
"v_mfma_f32_16x16x32_bf16 v[8:11], a[168:171], v[88:91], v[8:11]\n"
"ds_read_b64_tr_b16 a[192:193], v242 offset:49152\n"
"v_cvt_pk_bf16_f32 v18, v4, v5\n"
"v_mfma_f32_16x16x32_bf16 v[8:11], a[172:175], v[92:95], v[8:11]\n"
"v_exp_f32 v6, v6\n"
"v_mfma_f32_16x16x32_bf16 v[12:15], a[176:179], v[80:83], v[28:31]\n"
"ds_read_b64_tr_b16 a[194:195], v242 offset:53248\n"
"v_mfma_f32_16x16x32_bf16 v[12:15], a[180:183], v[84:87], v[12:15]\n"
"v_exp_f32 v7, v7\n"
"v_mfma_f32_16x16x32_bf16 v[12:15], a[184:187], v[88:91], v[12:15]\n"
"ds_read_b64_tr_b16 a[196:197], v243 offset:49152\n"
"v_cvt_pk_bf16_f32 v19, v6, v7\n"
"v_mfma_f32_16x16x32_bf16 v[12:15], a[188:191], v[92:95], v[12:15]\n"
"s_waitcnt lgkmcnt(11)\n"
"buffer_load_dwordx4 v246, s[8:11], s16 offen offset:1024 lds\n"
"v_mfma_f32_16x16x32_bf16 a[0:3], a[224:227], v[16:19], a[0:3]\n"
"v_exp_f32 v8, v8\n"
"v_mfma_f32_16x16x32_bf16 a[4:7], a[228:231], v[16:19], a[4:7]\n"
"ds_read_b64_tr_b16 a[198:199], v243 offset:53248\n"
"v_mfma_f32_16x16x32_bf16 a[8:11], a[232:235], v[16:19], a[8:11]\n"
"v_exp_f32 v9, v9\n"
"v_mfma_f32_16x16x32_bf16 a[12:15], a[236:239], v[16:19], a[12:15]\n"
"ds_read_b64_tr_b16 a[200:201], v242 offset:49408\n"
"v_cvt_pk_bf16_f32 v20, v8, v9\n"
"v_mfma_f32_16x16x32_bf16 a[16:19], a[240:243], v[16:19], a[16:19]\n"
"v_exp_f32 v10, v10\n"
"v_mfma_f32_16x16x32_bf16 a[20:23], a[244:247], v[16:19], a[20:23]\n"
"ds_read_b64_tr_b16 a[202:203], v242 offset:53504\n"
"v_mfma_f32_16x16x32_bf16 a[24:27], a[248:251], v[16:19], a[24:27]\n"
"v_exp_f32 v11, v11\n"
"v_mfma_f32_16x16x32_bf16 a[28:31], a[252:255], v[16:19], a[28:31]\n"
"v_cvt_pk_bf16_f32 v21, v10, v11\n"
"v_mfma_f32_16x16x32_bf16 v[44:47], v[40:43], v[16:19], v[44:47]\n"
"v_exp_f32 v12, v12\n"
"v_mfma_f32_16x16x32_bf16 v[0:3], a[160:163], v[96:99], v[32:35]\n"
"ds_read_b64_tr_b16 a[204:205], v243 offset:49408\n"
"v_mfma_f32_16x16x32_bf16 v[0:3], a[164:167], v[100:103], v[0:3]\n"
"v_exp_f32 v13, v13\n"
"v_mfma_f32_16x16x32_bf16 v[0:3], a[168:171], v[104:107], v[0:3]\n"
"ds_read_b64_tr_b16 a[206:207], v243 offset:53504\n"
"v_cvt_pk_bf16_f32 v22, v12, v13\n"
"v_mfma_f32_16x16x32_bf16 v[0:3], a[172:175], v[108:111], v[0:3]\n"
"v_exp_f32 v14, v14\n"
"v_mfma_f32_16x16x32_bf16 v[4:7], a[176:179], v[96:99], v[32:35]\n"
"ds_read_b64_tr_b16 a[208:209], v242 offset:49664\n"
"v_mfma_f32_16x16x32_bf16 v[4:7], a[180:183], v[100:103], v[4:7]\n"
"v_exp_f32 v15, v15\n"
"v_mfma_f32_16x16x32_bf16 v[4:7], a[184:187], v[104:107], v[4:7]\n"
"ds_read_b64_tr_b16 a[210:211], v242 offset:53760\n"
"v_cvt_pk_bf16_f32 v23, v14, v15\n"
"v_mfma_f32_16x16x32_bf16 v[4:7], a[188:191], v[108:111], v[4:7]\n"
"buffer_load_dwordx4 v247, s[8:11], s16 offen offset:2048 lds\n"
"v_mfma_f32_16x16x32_bf16 a[32:35], a[224:227], v[20:23], a[32:35]\n"
"v_exp_f32 v0, v0\n"
"v_mfma_f32_16x16x32_bf16 a[36:39], a[228:231], v[20:23], a[36:39]\n"
"ds_read_b64_tr_b16 a[212:213], v243 offset:49664\n"
"v_mfma_f32_16x16x32_bf16 a[40:43], a[232:235], v[20:23], a[40:43]\n"
"v_exp_f32 v1, v1\n"
"v_mfma_f32_16x16x32_bf16 a[44:47], a[236:239], v[20:23], a[44:47]\n"
"ds_read_b64_tr_b16 a[214:215], v243 offset:53760\n"
"v_cvt_pk_bf16_f32 v16, v0, v1\n"
"v_mfma_f32_16x16x32_bf16 a[48:51], a[240:243], v[20:23], a[48:51]\n"
"v_exp_f32 v2, v2\n"
"v_mfma_f32_16x16x32_bf16 a[52:55], a[244:247], v[20:23], a[52:55]\n"
"ds_read_b64_tr_b16 a[216:217], v242 offset:49920\n"
"v_mfma_f32_16x16x32_bf16 a[56:59], a[248:251], v[20:23], a[56:59]\n"
"v_exp_f32 v3, v3\n"
"v_mfma_f32_16x16x32_bf16 a[60:63], a[252:255], v[20:23], a[60:63]\n"
"v_cvt_pk_bf16_f32 v17, v2, v3\n"
"v_mfma_f32_16x16x32_bf16 v[48:51], v[40:43], v[20:23], v[48:51]\n"
"v_exp_f32 v4, v4\n"
"v_mfma_f32_16x16x32_bf16 v[8:11], a[160:163], v[112:115], v[36:39]\n"
"ds_read_b64_tr_b16 a[218:219], v242 offset:54016\n"
"v_mfma_f32_16x16x32_bf16 v[8:11], a[164:167], v[116:119], v[8:11]\n"
"v_exp_f32 v5, v5\n"
"v_mfma_f32_16x16x32_bf16 v[8:11], a[168:171], v[120:123], v[8:11]\n"
"ds_read_b64_tr_b16 a[220:221], v243 offset:49920\n"
"v_cvt_pk_bf16_f32 v18, v4, v5\n"
"v_mfma_f32_16x16x32_bf16 v[8:11], a[172:175], v[124:127], v[8:11]\n"
"v_exp_f32 v6, v6\n"
"v_mfma_f32_16x16x32_bf16 v[12:15], a[176:179], v[112:115], v[36:39]\n"
"ds_read_b64_tr_b16 a[222:223], v243 offset:54016\n"
"v_mfma_f32_16x16x32_bf16 v[12:15], a[180:183], v[116:119], v[12:15]\n"
"v_exp_f32 v7, v7\n"
"v_mfma_f32_16x16x32_bf16 v[12:15], a[184:187], v[120:123], v[12:15]\n"
"v_cvt_pk_bf16_f32 v19, v6, v7\n"
"v_mfma_f32_16x16x32_bf16 v[12:15], a[188:191], v[124:127], v[12:15]\n"
"buffer_load_dwordx4 v247, s[8:11], s16 offen offset:3072 lds\n"
"v_mfma_f32_16x16x32_bf16 a[64:67], a[224:227], v[16:19], a[64:67]\n"
"v_exp_f32 v8, v8\n"
"v_mfma_f32_16x16x32_bf16 a[68:71], a[228:231], v[16:19], a[68:71]\n"
"s_nop 0\n"
"v_mfma_f32_16x16x32_bf16 a[72:75], a[232:235], v[16:19], a[72:75]\n"
"v_exp_f32 v9, v9\n"
"v_mfma_f32_16x16x32_bf16 a[76:79], a[236:239], v[16:19], a[76:79]\n"
"v_cvt_pk_bf16_f32 v20, v8, v9\n"
"v_mfma_f32_16x16x32_bf16 a[80:83], a[240:243], v[16:19], a[80:83]\n"
"v_exp_f32 v10, v10\n"
"v_mfma_f32_16x16x32_bf16 a[84:87], a[244:247], v[16:19], a[84:87]\n"
"s_nop 0\n"
"v_mfma_f32_16x16x32_bf16 a[88:91], a[248:251], v[16:19], a[88:91]\n"
"v_exp_f32 v11, v11\n"
"v_mfma_f32_16x16x32_bf16 a[92:95], a[252:255], v[16:19], a[92:95]\n"
"s_waitcnt lgkmcnt(8)\n"
"v_cvt_pk_bf16_f32 v21, v10, v11\n"
"v_mfma_f32_16x16x32_bf16 v[52:55], v[40:43], v[16:19], v[52:55]\n"
"v_exp_f32 v12, v12\n"
"v_mfma_f32_16x16x32_bf16 v[0:3], a[128:131], v[64:67], v[24:27]\n"
"ds_read_b128 a[160:163], v240 offset:40960\n"
"v_mfma_f32_16x16x32_bf16 v[0:3], a[132:135], v[68:71], v[0:3]\n"
"v_exp_f32 v13, v13\n"
"v_mfma_f32_16x16x32_bf16 v[0:3], a[136:139], v[72:75], v[0:3]\n"
"ds_read_b128 a[164:167], v240 offset:41216\n"
"v_cvt_pk_bf16_f32 v22, v12, v13\n"
"v_mfma_f32_16x16x32_bf16 v[0:3], a[140:143], v[76:79], v[0:3]\n"
"v_exp_f32 v14, v14\n"
"v_mfma_f32_16x16x32_bf16 v[4:7], a[144:147], v[64:67], v[24:27]\n"
"ds_read_b128 a[168:171], v240 offset:41472\n"
"v_mfma_f32_16x16x32_bf16 v[4:7], a[148:151], v[68:71], v[4:7]\n"
"v_exp_f32 v15, v15\n"
"v_mfma_f32_16x16x32_bf16 v[4:7], a[152:155], v[72:75], v[4:7]\n"
"s_add_u32 m0, s18, 16384\n"
"ds_read_b128 a[172:175], v240 offset:41728\n"
"v_cvt_pk_bf16_f32 v23, v14, v15\n"
"v_mfma_f32_16x16x32_bf16 v[4:7], a[156:159], v[76:79], v[4:7]\n"
"buffer_load_dwordx4 v246, s[12:15], s16 offen offset:0 lds\n"
"v_mfma_f32_16x16x32_bf16 a[96:99], a[224:227], v[20:23], a[96:99]\n"
"v_exp_f32 v0, v0\n"
"v_mfma_f32_16x16x32_bf16 a[100:103], a[228:231], v[20:23], a[100:103]\n"
"ds_read_b128 a[176:179], v240 offset:45056\n"
"v_mfma_f32_16x16x32_bf16 a[104:107], a[232:235], v[20:23], a[104:107]\n"
"v_exp_f32 v1, v1\n"
"v_mfma_f32_16x16x32_bf16 a[108:111], a[236:239], v[20:23], a[108:111]\n"
"ds_read_b128 a[180:183], v240 offset:45312\n"
"v_cvt_pk_bf16_f32 v16, v0, v1\n"
"v_mfma_f32_16x16x32_bf16 a[112:115], a[240:243], v[20:23], a[112:115]\n"
"v_exp_f32 v2, v2\n"
"v_mfma_f32_16x16x32_bf16 a[116:119], a[244:247], v[20:23], a[116:119]\n"
"ds_read_b128 a[184:187], v240 offset:45568\n"
"v_mfma_f32_16x16x32_bf16 a[120:123], a[248:251], v[20:23], a[120:123]\n"
"v_exp_f32 v3, v3\n"
"v_mfma_f32_16x16x32_bf16 a[124:127], a[252:255], v[20:23], a[124:127]\n"
"v_cvt_pk_bf16_f32 v17, v2, v3\n"
"v_mfma_f32_16x16x32_bf16 v[56:59], v[40:43], v[20:23], v[56:59]\n"
"v_exp_f32 v4, v4\n"
"v_mfma_f32_16x16x32_bf16 v[8:11], a[128:131], v[80:83], v[28:31]\n"
"ds_read_b128 a[188:191], v240 offset:45824\n"
"v_mfma_f32_16x16x32_bf16 v[8:11], a[132:135], v[84:87], v[8:11]\n"
"v_exp_f32 v5, v5\n"
"v_mfma_f32_16x16x32_bf16 v[8:11], a[136:139], v[88:91], v[8:11]\n"
"ds_read_b64_tr_b16 a[224:225], v242 offset:57344\n"
"v_cvt_pk_bf16_f32 v18, v4, v5\n"
"v_mfma_f32_16x16x32_bf16 v[8:11], a[140:143], v[92:95], v[8:11]\n"
"v_exp_f32 v6, v6\n"
"v_mfma_f32_16x16x32_bf16 v[12:15], a[144:147], v[80:83], v[28:31]\n"
"ds_read_b64_tr_b16 a[226:227], v242 offset:61440\n"
"v_mfma_f32_16x16x32_bf16 v[12:15], a[148:151], v[84:87], v[12:15]\n"
"v_exp_f32 v7, v7\n"
"v_mfma_f32_16x16x32_bf16 v[12:15], a[152:155], v[88:91], v[12:15]\n"
"ds_read_b64_tr_b16 a[228:229], v243 offset:57344\n"
"v_cvt_pk_bf16_f32 v19, v6, v7\n"
"v_mfma_f32_16x16x32_bf16 v[12:15], a[156:159], v[92:95], v[12:15]\n"
"s_waitcnt lgkmcnt(11)\n"
"buffer_load_dwordx4 v247, s[12:15], s16 offen offset:1024 lds\n"
"v_mfma_f32_16x16x32_bf16 a[0:3], a[192:195], v[16:19], a[0:3]\n"
"v_exp_f32 v8, v8\n"
"v_mfma_f32_16x16x32_bf16 a[4:7], a[196:199], v[16:19], a[4:7]\n"
"ds_read_b64_tr_b16 a[230:231], v243 offset:61440\n"
"v_mfma_f32_16x16x32_bf16 a[8:11], a[200:203], v[16:19], a[8:11]\n"
"v_exp_f32 v9, v9\n"
"v_mfma_f32_16x16x32_bf16 a[12:15], a[204:207], v[16:19], a[12:15]\n"
"ds_read_b64_tr_b16 a[232:233], v242 offset:57600\n"
"v_cvt_pk_bf16_f32 v20, v8, v9\n"
"v_mfma_f32_16x16x32_bf16 a[16:19], a[208:211], v[16:19], a[16:19]\n"
"v_exp_f32 v10, v10\n"
"v_mfma_f32_16x16x32_bf16 a[20:23], a[212:215], v[16:19], a[20:23]\n"
"ds_read_b64_tr_b16 a[234:235], v242 offset:61696\n"
"v_mfma_f32_16x16x32_bf16 a[24:27], a[216:219], v[16:19], a[24:27]\n"
"v_exp_f32 v11, v11\n"
"v_mfma_f32_16x16x32_bf16 a[28:31], a[220:223], v[16:19], a[28:31]\n"
"v_cvt_pk_bf16_f32 v21, v10, v11\n"
"v_mfma_f32_16x16x32_bf16 v[44:47], v[40:43], v[16:19], v[44:47]\n"
"v_exp_f32 v12, v12\n"
"v_mfma_f32_16x16x32_bf16 v[0:3], a[128:131], v[96:99], v[32:35]\n"
"ds_read_b64_tr_b16 a[236:237], v243 offset:57600\n"
"v_mfma_f32_16x16x32_bf16 v[0:3], a[132:135], v[100:103], v[0:3]\n"
"v_exp_f32 v13, v13\n"
"v_mfma_f32_16x16x32_bf16 v[0:3], a[136:139], v[104:107], v[0:3]\n"
"ds_read_b64_tr_b16 a[238:239], v243 offset:61696\n"
"v_cvt_pk_bf16_f32 v22, v12, v13\n"
"v_mfma_f32_16x16x32_bf16 v[0:3], a[140:143], v[108:111], v[0:3]\n"
"v_exp_f32 v14, v14\n"
"v_mfma_f32_16x16x32_bf16 v[4:7], a[144:147], v[96:99], v[32:35]\n"
"ds_read_b64_tr_b16 a[240:241], v242 offset:57856\n"
"v_mfma_f32_16x16x32_bf16 v[4:7], a[148:151], v[100:103], v[4:7]\n"
"v_exp_f32 v15, v15\n"
"v_mfma_f32_16x16x32_bf16 v[4:7], a[152:155], v[104:107], v[4:7]\n"
"ds_read_b64_tr_b16 a[242:243], v242 offset:61952\n"
"v_cvt_pk_bf16_f32 v23, v14, v15\n"
"v_mfma_f32_16x16x32_bf16 v[4:7], a[156:159], v[108:111], v[4:7]\n"
"buffer_load_dwordx4 v246, s[12:15], s16 offen offset:2048 lds\n"
"v_mfma_f32_16x16x32_bf16 a[32:35], a[192:195], v[20:23], a[32:35]\n"
"v_exp_f32 v0, v0\n"
"v_mfma_f32_16x16x32_bf16 a[36:39], a[196:199], v[20:23], a[36:39]\n"
"ds_read_b64_tr_b16 a[244:245], v243 offset:57856\n"
"v_mfma_f32_16x16x32_bf16 a[40:43], a[200:203], v[20:23], a[40:43]\n"
"v_exp_f32 v1, v1\n"
"v_mfma_f32_16x16x32_bf16 a[44:47], a[204:207], v[20:23], a[44:47]\n"
"ds_read_b64_tr_b16 a[246:247], v243 offset:61952\n"
"v_cvt_pk_bf16_f32 v16, v0, v1\n"
"v_mfma_f32_16x16x32_bf16 a[48:51], a[208:211], v[20:23], a[48:51]\n"
"v_exp_f32 v2, v2\n"
"v_mfma_f32_16x16x32_bf16 a[52:55], a[212:215], v[20:23], a[52:55]\n"
"ds_read_b64_tr_b16 a[248:249], v242 offset:58112\n"
"v_mfma_f32_16x16x32_bf16 a[56:59], a[216:219], v[20:23], a[56:59]\n"
"v_exp_f32 v3, v3\n"
"v_mfma_f32_16x16x32_bf16 a[60:63], a[220:223], v[20:23], a[60:63]\n"
"v_cvt_pk_bf16_f32 v17, v2, v3\n"
"v_mfma_f32_16x16x32_bf16 v[48:51], v[40:43], v[20:23], v[48:51]\n"
"v_exp_f32 v4, v4\n"
"v_mfma_f32_16x16x32_bf16 v[8:11], a[128:131], v[112:115], v[36:39]\n"
"ds_read_b64_tr_b16 a[250:251], v242 offset:62208\n"
"v_mfma_f32_16x16x32_bf16 v[8:11], a[132:135], v[116:119], v[8:11]\n"
"v_exp_f32 v5, v5\n"
"v_mfma_f32_16x16x32_bf16 v[8:11], a[136:139], v[120:123], v[8:11]\n"
"ds_read_b64_tr_b16 a[252:253], v243 offset:58112\n"
"v_cvt_pk_bf16_f32 v18, v4, v5\n"
"v_mfma_f32_16x16x32_bf16 v[8:11], a[140:143], v[124:127], v[8:11]\n"
"v_exp_f32 v6, v6\n"
"v_mfma_f32_16x16x32_bf16 v[12:15], a[144:147], v[112:115], v[36:39]\n"
"ds_read_b64_tr_b16 a[254:255], v243 offset:62208\n"
"v_mfma_f32_16x16x32_bf16 v[12:15], a[148:151], v[116:119], v[12:15]\n"
"v_exp_f32 v7, v7\n"
"v_mfma_f32_16x16x32_bf16 v[12:15], a[152:155], v[120:123], v[12:15]\n"
"v_cvt_pk_bf16_f32 v19, v6, v7\n"
"v_mfma_f32_16x16x32_bf16 v[12:15], a[156:159], v[124:127], v[12:15]\n"
"buffer_load_dwordx4 v247, s[12:15], s16 offen offset:3072 lds\n"
"v_mfma_f32_16x16x32_bf16 a[64:67], a[192:195], v[16:19], a[64:67]\n"
"s_add_u32 s16, s16, 16384\n"
"v_exp_f32 v8, v8\n"
"v_mfma_f32_16x16x32_bf16 a[68:71], a[196:199], v[16:19], a[68:71]\n"
"s_nop 0\n"
"v_mfma_f32_16x16x32_bf16 a[72:75], a[200:203], v[16:19], a[72:75]\n"
"v_exp_f32 v9, v9\n"
"v_mfma_f32_16x16x32_bf16 a[76:79], a[204:207], v[16:19], a[76:79]\n"
"v_cvt_pk_bf16_f32 v20, v8, v9\n"
"v_mfma_f32_16x16x32_bf16 a[80:83], a[208:211], v[16:19], a[80:83]\n"
"v_exp_f32 v10, v10\n"
"v_mfma_f32_16x16x32_bf16 a[84:87], a[212:215], v[16:19], a[84:87]\n"
"s_nop 0\n"
"v_mfma_f32_16x16x32_bf16 a[88:91], a[216:219], v[16:19], a[88:91]\n"
"v_exp_f32 v11, v11\n"
"v_mfma_f32_16x16x32_bf16 a[92:95], a[220:223], v[16:19], a[92:95]\n"
"s_waitcnt vmcnt(24) lgkmcnt(0)\n"
"s_barrier\n"
"v_cvt_pk_bf16_f32 v21, v10, v11\n"
"v_mfma_f32_16x16x32_bf16 v[52:55], v[40:43], v[16:19], v[52:55]\n"
"v_exp_f32 v12, v12\n"
"s_mov_b32 s19, 6\n"
".p2align 6\n"
"AT_loop:\n"
"v_mfma_f32_16x16x32_bf16 v[0:3], a[160:163], v[64:67], v[24:27]\n"
"ds_read_b128 a[128:131], v241 offset:0\n"
"v_mfma_f32_16x16x32_bf16 v[0:3], a[164:167], v[68:71], v[0:3]\n"
"v_exp_f32 v13, v13\n"
"v_mfma_f32_16x16x32_bf16 v[0:3], a[168:171], v[72:75], v[0:3]\n"
"ds_read_b128 a[132:135], v241 offset:256\n"
"v_cvt_pk_bf16_f32 v22, v12, v13\n"
"v_mfma_f32_16x16x32_bf16 v[0:3], a[172:175], v[76:79], v[0:3]\n"
"v_exp_f32 v14, v14\n"
"v_mfma_f32_16x16x32_bf16 v[4:7], a[176:179], v[64:67], v[24:27]\n"
"ds_read_b128 a[136:139], v241 offset:512\n"
"v_mfma_f32_16x16x32_bf16 v[4:7], a[180:183], v[68:71], v[4:7]\n"
"v_exp_f32 v15, v15\n"
"v_mfma_f32_16x16x32_bf16 v[4:7], a[184:187], v[72:75], v[4:7]\n"
"s_add_u32 m0, s18, 32768\n"
"ds_read_b128 a[140:143], v241 offset:768\n"
"v_cvt_pk_bf16_f32 v23, v14, v15\n"
"v_mfma_f32_16x16x32_bf16 v[4:7], a[188:191], v[76:79], v[4:7]\n"
"buffer_load_dwordx4 v246, s[8:11], s16 offen offset:0 lds\n"
"v_mfma_f32_16x16x32_bf16 a[96:99], a[192:195], v[20:23], a[96:99]\n"
"v_exp_f32 v0, v0\n"
"v_mfma_f32_16x16x32_bf16 a[100:103], a[196:199], v[20:23], a[100:103]\n"
"ds_read_b128 a[144:147], v241 offset:4096\n"
"v_mfma_f32_16x16x32_bf16 a[104:107], a[200:203], v[20:23], a[104:107]\n"
"v_exp_f32 v1, v1\n"
"v_mfma_f32_16x16x32_bf16 a[108:111], a[204:207], v[20:23], a[108:111]\n"
"ds_read_b128 a[148:151], v241 offset:4352\n"
"v_cvt_pk_bf16_f32 v16, v0, v1\n"
"v_mfma_f32_16x16x32_bf16 a[112:115], a[208:211], v[20:23], a[112:115]\n"
"v_exp_f32 v2, v2\n"
"v_mfma_f32_16x16x32_bf16 a[116:119], a[212:215], v[20:23], a[116:119]\n"
"ds_read_b128 a[152:155], v241 offset:4608\n"
"v_mfma_f32_16x16x32_bf16 a[120:123], a[216:219], v[20:23], a[120:123]\n"
"v_exp_f32 v3, v3\n"
"v_mfma_f32_16x16x32_bf16 a[124:127], a[220:223], v[20:23], a[124:127]\n"
"v_cvt_pk_bf16_f32 v17, v2, v3\n"
"v_mfma_f32_16x16x32_bf16 v[56:59], v[40:43], v[20:23], v[56:59]\n"
"v_exp_f32 v4, v4\n"
"v_mfma_f32_16x16x32_bf16 v[8:11], a[160:163], v[80:83], v[28:31]\n"
"ds_read_b128 a[156:159], v241 offset:4864\n"
"v_mfma_f32_16x16x32_bf16 v[8:11], a[164:167], v[84:87], v[8:11]\n"
"v_exp_f32 v5, v5\n"
"v_mfma_f32_16x16x32_bf16 v[8:11], a[168:171], v[88:91], v[8:11]\n"
"ds_read_b64_tr_b16 a[192:193], v244 offset:16384\n"
"v_cvt_pk_bf16_f32 v18, v4, v5\n"
"v_mfma_f32_16x16x32_bf16 v[8:11], a[172:175], v[92:95], v[8:11]\n"
"v_exp_f32 v6, v6\n"
"v_mfma_f32_16x16x32_bf16 v[12:15], a[176:179], v[80:83], v[28:31]\n"
"ds_read_b64_tr_b16 a[194:195], v244 offset:20480\n"
"v_mfma_f32_16x16x32_bf16 v[12:15], a[180:183], v[84:87], v[12:15]\n"
"v_exp_f32 v7, v7\n"
"v_mfma_f32_16x16x32_bf16 v[12:15], a[184:187], v[88:91], v[12:15]\n"
"ds_read_b64_tr_b16 a[196:197], v245 offset:16384\n"
"v_cvt_pk_bf16_f32 v19, v6, v7\n"
"v_mfma_f32_16x16x32_bf16 v[12:15], a[188:191], v[92:95], v[12:15]\n"
"s_waitcnt lgkmcnt(11)\n"
"buffer_load_dwordx4 v246, s[8:11], s16 offen offset:1024 lds\n"
"v_mfma_f32_16x16x32_bf16 a[0:3], a[224:227], v[16:19], a[0:3]\n"
"v_exp_f32 v8, v8\n"
"v_mfma_f32_16x16x32_bf16 a[4:7], a[228:231], v[16:19], a[4:7]\n"
"ds_read_b64_tr_b16 a[198:199], v245 offset:20480\n"
"v_mfma_f32_16x16x32_bf16 a[8:11], a[232:235], v[16:19], a[8:11]\n"
"v_exp_f32 v9, v9\n"
"v_mfma_f32_16x16x32_bf16 a[12:15], a[236:239], v[16:19], a[12:15]\n"
"ds_read_b64_tr_b16 a[200:201], v244 offset:16640\n"
"v_cvt_pk_bf16_f32 v20, v8, v9\n"
"v_mfma_f32_16x16x32_bf16 a[16:19], a[240:243], v[16:19], a[16:19]\n"
"v_exp_f32 v10, v10\n"
"v_mfma_f32_16x16x32_bf16 a[20:23], a[244:247], v[16:19], a[20:23]\n"
"ds_read_b64_tr_b16 a[202:203], v244 offset:20736\n"
"v_mfma_f32_16x16x32_bf16 a[24:27], a[248:251], v[16:19], a[24:27]\n"
"v_exp_f32 v11, v11\n"
"v_mfma_f32_16x16x32_bf16 a[28:31], a[252:255], v[16:19], a[28:31]\n"
"v_cvt_pk_bf16_f32 v21, v10, v11\n"
"v_mfma_f32_16x16x32_bf16 v[44:47], v[40:43], v[16:19], v[44:47]\n"
"v_exp_f32 v12, v12\n"
"v_mfma_f32_16x16x32_bf16 v[0:3], a[160:163], v[96:99], v[32:35]\n"
"ds_read_b64_tr_b16 a[204:205], v245 offset:16640\n"
"v_mfma_f32_16x16x32_bf16 v[0:3], a[164:167], v[100:103], v[0:3]\n"
"v_exp_f32 v13, v13\n"
"v_mfma_f32_16x16x32_bf16 v[0:3], a[168:171], v[104:107], v[0:3]\n"
"ds_read_b64_tr_b16 a[206:207], v245 offset:20736\n"
"v_cvt_pk_bf16_f32 v22, v12, v13\n"
"v_mfma_f32_16x16x32_bf16 v[0:3], a[172:175], v[108:111], v[0:3]\n"
"v_exp_f32 v14, v14\n"
"v_mfma_f32_16x16x32_bf16 v[4:7], a[176:179], v[96:99], v[32:35]\n"
"ds_read_b64_tr_b16 a[208:209], v244 offset:16896\n"
"v_mfma_f32_16x16x32_bf16 v[4:7], a[180:183], v[100:103], v[4:7]\n"
"v_exp_f32 v15, v15\n"
"v_mfma_f32_16x16x32_bf16 v[4:7], a[184:187], v[104:107], v[4:7]\n"
"ds_read_b64_tr_b16 a[210:211], v244 offset:20992\n"
"v_cvt_pk_bf16_f32 v23, v14, v15\n"
"v_mfma_f32_16x16x32_bf16 v[4:7], a[188:191], v[108:111], v[4:7]\n"
"buffer_load_dwordx4 v247, s[8:11], s16 offen offset:2048 lds\n"
"v_mfma_f32_16x16x32_bf16 a[32:35], a[224:227], v[20:23], a[32:35]\n"
"v_exp_f32 v0, v0\n"
"v_mfma_f32_16x16x32_bf16 a[36:39], a[228:231], v[20:23], a[36:39]\n"
"ds_read_b64_tr_b16 a[212:213], v245 offset:16896\n"
"v_mfma_f32_16x16x32_bf16 a[40:43], a[232:235], v[20:23], a[40:43]\n"
"v_exp_f32 v1, v1\n"
"v_mfma_f32_16x16x32_bf16 a[44:47], a[236:239], v[20:23], a[44:47]\n"
"ds_read_b64_tr_b16 a[214:215], v245 offset:20992\n"
"v_cvt_pk_bf16_f32 v16, v0, v1\n"
"v_mfma_f32_16x16x32_bf16 a[48:51], a[240:243], v[20:23], a[48:51]\n"
"v_exp_f32 v2, v2\n"
"v_mfma_f32_16x16x32_bf16 a[52:55], a[244:247], v[20:23], a[52:55]\n"
"ds_read_b64_tr_b16 a[216:217], v244 offset:17152\n"
"v_mfma_f32_16x16x32_bf16 a[56:59], a[248:251], v[20:23], a[56:59]\n"
"v_exp_f32 v3, v3\n"
"v_mfma_f32_16x16x32_bf16 a[60:63], a[252:255], v[20:23], a[60:63]\n"
"v_cvt_pk_bf16_f32 v17, v2, v3\n"
"v_mfma_f32_16x16x32_bf16 v[48:51], v[40:43], v[20:23], v[48:51]\n"
"v_exp_f32 v4, v4\n"
"v_mfma_f32_16x16x32_bf16 v[8:11], a[160:163], v[112:115], v[36:39]\n"
"ds_read_b64_tr_b16 a[218:219], v244 offset:21248\n"
"v_mfma_f32_16x16x32_bf16 v[8:11], a[164:167], v[116:119], v[8:11]\n"
"v_exp_f32 v5, v5\n"
"v_mfma_f32_16x16x32_bf16 v[8:11], a[168:171], v[120:123], v[8:11]\n"
"ds_read_b64_tr_b16 a[220:221], v245 offset:17152\n"
"v_cvt_pk_bf16_f32 v18, v4, v5\n"
"v_mfma_f32_16x16x32_bf16 v[8:11], a[172:175], v[124:127], v[8:11]\n"
"v_exp_f32 v6, v6\n"
"v_mfma_f32_16x16x32_bf16 v[12:15], a[176:179], v[112:115], v[36:39]\n"
"ds_read_b64_tr_b16 a[222:223], v245 offset:21248\n"
"v_mfma_f32_16x16x32_bf16 v[12:15], a[180:183], v[116:119], v[12:15]\n"
"v_exp_f32 v7, v7\n"
"v_mfma_f32_16x16x32_bf16 v[12:15], a[184:187], v[120:123], v[12:15]\n"
"v_cvt_pk_bf16_f32 v19, v6, v7\n"
"v_mfma_f32_16x16x32_bf16 v[12:15], a[188:191], v[124:127], v[12:15]\n"
"buffer_load_dwordx4 v247, s[8:11], s16 offen offset:3072 lds\n"
"v_mfma_f32_16x16x32_bf16 a[64:67], a[224:227], v[16:19], a[64:67]\n"
"v_exp_f32 v8, v8\n"
"v_mfma_f32_16x16x32_bf16 a[68:71], a[228:231], v[16:19], a[68:71]\n"
"s_nop 0\n"
"v_mfma_f32_16x16x32_bf16 a[72:75], a[232:235], v[16:19], a[72:75]\n"
"v_exp_f32 v9, v9\n"
"v_mfma_f32_16x16x32_bf16 a[76:79], a[236:239], v[16:19], a[76:79]\n"
"v_cvt_pk_bf16_f32 v20, v8, v9\n"
"v_mfma_f32_16x16x32_bf16 a[80:83], a[240:243], v[16:19], a[80:83]\n"
"v_exp_f32 v10, v10\n"
"v_mfma_f32_16x16x32_bf16 a[84:87], a[244:247], v[16:19], a[84:87]\n"
"s_nop 0\n"
"v_mfma_f32_16x16x32_bf16 a[88:91], a[248:251], v[16:19], a[88:91]\n"
"v_exp_f32 v11, v11\n"
"v_mfma_f32_16x16x32_bf16 a[92:95], a[252:255], v[16:19], a[92:95]\n"
"s_waitcnt lgkmcnt(8)\n"
"v_cvt_pk_bf16_f32 v21, v10, v11\n"
"v_mfma_f32_16x16x32_bf16 v[52:55], v[40:43], v[16:19], v[52:55]\n"
"v_exp_f32 v12, v12\n"
"v_mfma_f32_16x16x32_bf16 v[0:3], a[128:131], v[64:67], v[24:27]\n"
"ds_read_b128 a[160:163], v241 offset:8192\n"
"v_mfma_f32_16x16x32_bf16 v[0:3], a[132:135], v[68:71], v[0:3]\n"
"v_exp_f32 v13, v13\n"
"v_mfma_f32_16x16x32_bf16 v[0:3], a[136:139], v[72:75], v[0:3]\n"
"ds_read_b128 a[164:167], v241 offset:8448\n"
"v_cvt_pk_bf16_f32 v22, v12, v13\n"
"v_mfma_f32_16x16x32_bf16 v[0:3], a[140:143], v[76:79], v[0:3]\n"
"v_exp_f32 v14, v14\n"
"v_mfma_f32_16x16x32_bf16 v[4:7], a[144:147], v[64:67], v[24:27]\n"
"ds_read_b128 a[168:171], v241 offset:8704\n"
"v_mfma_f32_16x16x32_bf16 v[4:7], a[148:151], v[68:71], v[4:7]\n"
"v_exp_f32 v15, v15\n"
"v_mfma_f32_16x16x32_bf16 v[4:7], a[152:155], v[72:75], v[4:7]\n"
"s_add_u32 m0, s18, 49152\n"
"ds_read_b128 a[172:175], v241 offset:8960\n"
"v_cvt_pk_bf16_f32 v23, v14, v15\n"
"v_mfma_f32_16x16x32_bf16 v[4:7], a[156:159], v[76:79], v[4:7]\n"
"buffer_load_dwordx4 v246, s[12:15], s16 offen offset:0 lds\n"
"v_mfma_f32_16x16x32_bf16 a[96:99], a[224:227], v[20:23], a[96:99]\n"
"v_exp_f32 v0, v0\n"
"v_mfma_f32_16x16x32_bf16 a[100:103], a[228:231], v[20:23], a[100:103]\n"
"ds_read_b128 a[176:179], v241 offset:12288\n"
"v_mfma_f32_16x16x32_bf16 a[104:107], a[232:235], v[20:23], a[104:107]\n"
"v_exp_f32 v1, v1\n"
"v_mfma_f32_16x16x32_bf16 a[108:111], a[236:239], v[20:23], a[108:111]\n"
"ds_read_b128 a[180:183], v241 offset:12544\n"
"v_cvt_pk_bf16_f32 v16, v0, v1\n"
"v_mfma_f32_16x16x32_bf16 a[112:115], a[240:243], v[20:23], a[112:115]\n"
"v_exp_f32 v2, v2\n"
"v_mfma_f32_16x16x32_bf16 a[116:119], a[244:247], v[20:23], a[116:119]\n"
"ds_read_b128 a[184:187], v241 offset:12800\n"
"v_mfma_f32_16x16x32_bf16 a[120:123], a[248:251], v[20:23], a[120:123]\n"
"v_exp_f32 v3, v3\n"
"v_mfma_f32_16x16x32_bf16 a[124:127], a[252:255], v[20:23], a[124:127]\n"
"v_cvt_pk_bf16_f32 v17, v2, v3\n"
"v_mfma_f32_16x16x32_bf16 v[56:59], v[40:43], v[20:23], v[56:59]\n"
"v_exp_f32 v4, v4\n"
"v_mfma_f32_16x16x32_bf16 v[8:11], a[128:131], v[80:83], v[28:31]\n"
"ds_read_b128 a[188:191], v241 offset:13056\n"
"v_mfma_f32_16x16x32_bf16 v[8:11], a[132:135], v[84:87], v[8:11]\n"
"v_exp_f32 v5, v5\n"
"v_mfma_f32_16x16x32_bf16 v[8:11], a[136:139], v[88:91], v[8:11]\n"
"ds_read_b64_tr_b16 a[224:225], v244 offset:24576\n"
"v_cvt_pk_bf16_f32 v18, v4, v5\n"
"v_mfma_f32_16x16x32_bf16 v[8:11], a[140:143], v[92:95], v[8:11]\n"
"v_exp_f32 v6, v6\n"
"v_mfma_f32_16x16x32_bf16 v[12:15], a[144:147], v[80:83], v[28:31]\n"
"ds_read_b64_tr_b16 a[226:227], v244 offset:28672\n"
"v_mfma_f32_16x16x32_bf16 v[12:15], a[148:151], v[84:87], v[12:15]\n"
"v_exp_f32 v7, v7\n"
"v_mfma_f32_16x16x32_bf16 v[12:15], a[152:155], v[88:91], v[12:15]\n"
"ds_read_b64_tr_b16 a[228:229], v245 offset:24576\n"
"v_cvt_pk_bf16_f32 v19, v6, v7\n"
"v_mfma_f32_16x16x32_bf16 v[12:15], a[156:159], v[92:95], v[12:15]\n"
"s_waitcnt lgkmcnt(11)\n"
"buffer_load_dwordx4 v247, s[12:15], s16 offen offset:1024 lds\n"
"v_mfma_f32_16x16x32_bf16 a[0:3], a[192:195], v[16:19], a[0:3]\n"
"v_exp_f32 v8, v8\n"
"v_mfma_f32_16x16x32_bf16 a[4:7], a[196:199], v[16:19], a[4:7]\n"
"ds_read_b64_tr_b16 a[230:231], v245 offset:28672\n"
"v_mfma_f32_16x16x32_bf16 a[8:11], a[200:203], v[16:19], a[8:11]\n"
"v_exp_f32 v9, v9\n"
"v_mfma_f32_16x16x32_bf16 a[12:15], a[204:207], v[16:19], a[12:15]\n"
"ds_read_b64_tr_b16 a[232:233], v244 offset:24832\n"
"v_cvt_pk_bf16_f32 v20, v8, v9\n"
"v_mfma_f32_16x16x32_bf16 a[16:19], a[208:211], v[16:19], a[16:19]\n"
"v_exp_f32 v10, v10\n"
"v_mfma_f32_16x16x32_bf16 a[20:23], a[212:215], v[16:19], a[20:23]\n"
"ds_read_b64_tr_b16 a[234:235], v244 offset:28928\n"
"v_mfma_f32_16x16x32_bf16 a[24:27], a[216:219], v[16:19], a[24:27]\n"
"v_exp_f32 v11, v11\n"
"v_mfma_f32_16x16x32_bf16 a[28:31], a[220:223], v[16:19], a[28:31]\n"
"v_cvt_pk_bf16_f32 v21, v10, v11\n"
"v_mfma_f32_16x16x32_bf16 v[44:47], v[40:43], v[16:19], v[44:47]\n"
"v_exp_f32 v12, v12\n"
"v_mfma_f32_16x16x32_bf16 v[0:3], a[128:131], v[96:99], v[32:35]\n"
"ds_read_b64_tr_b16 a[236:237], v245 offset:24832\n"
"v_mfma_f32_16x16x32_bf16 v[0:3], a[132:135], v[100:103], v[0:3]\n"
"v_exp_f32 v13, v13\n"
"v_mfma_f32_16x16x32_bf16 v[0:3], a[136:139], v[104:107], v[0:3]\n"
"ds_read_b64_tr_b16 a[238:239], v245 offset:28928\n"
"v_cvt_pk_bf16_f32 v22, v12, v13\n"
"v_mfma_f32_16x16x32_bf16 v[0:3], a[140:143], v[108:111], v[0:3]\n"
"v_exp_f32 v14, v14\n"
"v_mfma_f32_16x16x32_bf16 v[4:7], a[144:147], v[96:99], v[32:35]\n"
"ds_read_b64_tr_b16 a[240:241], v244 offset:25088\n"
"v_mfma_f32_16x16x32_bf16 v[4:7], a[148:151], v[100:103], v[4:7]\n"
"v_exp_f32 v15, v15\n"
"v_mfma_f32_16x16x32_bf16 v[4:7], a[152:155], v[104:107], v[4:7]\n"
"ds_read_b64_tr_b16 a[242:243], v244 offset:29184\n"
"v_cvt_pk_bf16_f32 v23, v14, v15\n"
"v_mfma_f32_16x16x32_bf16 v[4:7], a[156:159], v[108:111], v[4:7]\n"
"buffer_load_dwordx4 v246, s[12:15], s16 offen offset:2048 lds\n"
"v_mfma_f32_16x16x32_bf16 a[32:35], a[192:195], v[20:23], a[32:35]\n"
"v_exp_f32 v0, v0\n"
"v_mfma_f32_16x16x32_bf16 a[36:39], a[196:199], v[20:23], a[36:39]\n"
"ds_read_b64_tr_b16 a[244:245], v245 offset:25088\n"
"v_mfma_f32_16x16x32_bf16 a[40:43], a[200:203], v[20:23], a[40:43]\n"
"v_exp_f32 v1, v1\n"
"v_mfma_f32_16x16x32_bf16 a[44:47], a[204:207], v[20:23], a[44:47]\n"
"ds_read_b64_tr_b16 a[246:247], v245 offset:29184\n"
"v_cvt_pk_bf16_f32 v16, v0, v1\n"
"v_mfma_f32_16x16x32_bf16 a[48:51], a[208:211], v[20:23], a[48:51]\n"
"v_exp_f32 v2, v2\n"
"v_mfma_f32_16x16x32_bf16 a[52:55], a[212:215], v[20:23], a[52:55]\n"
"ds_read_b64_tr_b16 a[248:249], v244 offset:25344\n"
"v_mfma_f32_16x16x32_bf16 a[56:59], a[216:219], v[20:23], a[56:59]\n"
"v_exp_f32 v3, v3\n"
"v_mfma_f32_16x16x32_bf16 a[60:63], a[220:223], v[20:23], a[60:63]\n"
"v_cvt_pk_bf16_f32 v17, v2, v3\n"
"v_mfma_f32_16x16x32_bf16 v[48:51], v[40:43], v[20:23], v[48:51]\n"
"v_exp_f32 v4, v4\n"
"v_mfma_f32_16x16x32_bf16 v[8:11], a[128:131], v[112:115], v[36:39]\n"
"ds_read_b64_tr_b16 a[250:251], v244 offset:29440\n"
"v_mfma_f32_16x16x32_bf16 v[8:11], a[132:135], v[116:119], v[8:11]\n"
"v_exp_f32 v5, v5\n"
"v_mfma_f32_16x16x32_bf16 v[8:11], a[136:139], v[120:123], v[8:11]\n"
"ds_read_b64_tr_b16 a[252:253], v245 offset:25344\n"
"v_cvt_pk_bf16_f32 v18, v4, v5\n"
"v_mfma_f32_16x16x32_bf16 v[8:11], a[140:143], v[124:127], v[8:11]\n"
"v_exp_f32 v6, v6\n"
"v_mfma_f32_16x16x32_bf16 v[12:15], a[144:147], v[112:115], v[36:39]\n"
"ds_read_b64_tr_b16 a[254:255], v245 offset:29440\n"
"v_mfma_f32_16x16x32_bf16 v[12:15], a[148:151], v[116:119], v[12:15]\n"
"v_exp_f32 v7, v7\n"
"v_mfma_f32_16x16x32_bf16 v[12:15], a[152:155], v[120:123], v[12:15]\n"
"v_cvt_pk_bf16_f32 v19, v6, v7\n"
"v_mfma_f32_16x16x32_bf16 v[12:15], a[156:159], v[124:127], v[12:15]\n"
"buffer_load_dwordx4 v247, s[12:15], s16 offen offset:3072 lds\n"
"v_mfma_f32_16x16x32_bf16 a[64:67], a[192:195], v[16:19], a[64:67]\n"
"s_add_u32 s16, s16, 16384\n"
"v_exp_f32 v8, v8\n"
"v_mfma_f32_16x16x32_bf16 a[68:71], a[196:199], v[16:19], a[68:71]\n"
"s_nop 0\n"
"v_mfma_f32_16x16x32_bf16 a[72:75], a[200:203], v[16:19], a[72:75]\n"
"v_exp_f32 v9, v9\n"
"v_mfma_f32_16x16x32_bf16 a[76:79], a[204:207], v[16:19], a[76:79]\n"
"v_cvt_pk_bf16_f32 v20, v8, v9\n"
"v_mfma_f32_16x16x32_bf16 a[80:83], a[208:211], v[16:19], a[80:83]\n"
"v_exp_f32 v10, v10\n"
"v_mfma_f32_16x16x32_bf16 a[84:87], a[212:215], v[16:19], a[84:87]\n"
"s_nop 0\n"
"v_mfma_f32_16x16x32_bf16 a[88:91], a[216:219], v[16:19], a[88:91]\n"
"v_exp_f32 v11, v11\n"
"v_mfma_f32_16x16x32_bf16 a[92:95], a[220:223], v[16:19], a[92:95]\n"
"s_waitcnt vmcnt(16) lgkmcnt(0)\n"
"s_barrier\n"
"v_cvt_pk_bf16_f32 v21, v10, v11\n"
"v_mfma_f32_16x16x32_bf16 v[52:55], v[40:43], v[16:19], v[52:55]\n"
"v_exp_f32 v12, v12\n"
"v_mfma_f32_16x16x32_bf16 v[0:3], a[160:163], v[64:67], v[24:27]\n"
"ds_read_b128 a[128:131], v241 offset:32768\n"
"v_mfma_f32_16x16x32_bf16 v[0:3], a[164:167], v[68:71], v[0:3]\n"
"v_exp_f32 v13, v13\n"
"v_mfma_f32_16x16x32_bf16 v[0:3], a[168:171], v[72:75], v[0:3]\n"
"ds_read_b128 a[132:135], v241 offset:33024\n"
"v_cvt_pk_bf16_f32 v22, v12, v13\n"
"v_mfma_f32_16x16x32_bf16 v[0:3], a[172:175], v[76:79], v[0:3]\n"
"v_exp_f32 v14, v14\n"
"v_mfma_f32_16x16x32_bf16 v[4:7], a[176:179], v[64:67], v[24:27]\n"
"ds_read_b128 a[136:139], v241 offset:33280\n"
"v_mfma_f32_16x16x32_bf16 v[4:7], a[180:183], v[68:71], v[4:7]\n"
"v_exp_f32 v15, v15\n"
"v_mfma_f32_16x16x32_bf16 v[4:7], a[184:187], v[72:75], v[4:7]\n"
"s_add_u32 m0, s18, 65536\n"
"ds_read_b128 a[140:143], v241 offset:33536\n"
"v_cvt_pk_bf16_f32 v23, v14, v15\n"
"v_mfma_f32_16x16x32_bf16 v[4:7], a[188:191], v[76:79], v[4:7]\n"
"buffer_load_dwordx4 v246, s[8:11], s16 offen offset:0 lds\n"
"v_mfma_f32_16x16x32_bf16 a[96:99], a[192:195], v[20:23], a[96:99]\n"
"v_exp_f32 v0, v0\n"
"v_mfma_f32_16x16x32_bf16 a[100:103], a[196:199], v[20:23], a[100:103]\n"
"ds_read_b128 a[144:147], v241 offset:36864\n"
"v_mfma_f32_16x16x32_bf16 a[104:107], a[200:203], v[20:23], a[104:107]\n"
"v_exp_f32 v1, v1\n"
"v_mfma_f32_16x16x32_bf16 a[108:111], a[204:207], v[20:23], a[108:111]\n"
"ds_read_b128 a[148:151], v241 offset:37120\n"
"v_cvt_pk_bf16_f32 v16, v0, v1\n"
"v_mfma_f32_16x16x32_bf16 a[112:115], a[208:211], v[20:23], a[112:115]\n"
"v_exp_f32 v2, v2\n"
"v_mfma_f32_16x16x32_bf16 a[116:119], a[212:215], v[20:23], a[116:119]\n"
"ds_read_b128 a[152:155], v241 offset:37376\n"
"v_mfma_f32_16x16x32_bf16 a[120:123], a[216:219], v[20:23], a[120:123]\n"
"v_exp_f32 v3, v3\n"
"v_mfma_f32_16x16x32_bf16 a[124:127], a[220:223], v[20:23], a[124:127]\n"
"v_cvt_pk_bf16_f32 v17, v2, v3\n"
"v_mfma_f32_16x16x32_bf16 v[56:59], v[40:43], v[20:23], v[56:59]\n"
"v_exp_f32 v4, v4\n"
"v_mfma_f32_16x16x32_bf16 v[8:11], a[160:163], v[80:83], v[28:31]\n"
"ds_read_b128 a[156:159], v241 offset:37632\n"
"v_mfma_f32_16x16x32_bf16 v[8:11], a[164:167], v[84:87], v[8:11]\n"
"v_exp_f32 v5, v5\n"
"v_mfma_f32_16x16x32_bf16 v[8:11], a[168:171], v[88:91], v[8:11]\n"
"ds_read_b64_tr_b16 a[192:193], v244 offset:49152\n"
"v_cvt_pk_bf16_f32 v18, v4, v5\n"
"v_mfma_f32_16x16x32_bf16 v[8:11], a[172:175], v[92:95], v[8:11]\n"
"v_exp_f32 v6, v6\n"
"v_mfma_f32_16x16x32_bf16 v[12:15], a[176:179], v[80:83], v[28:31]\n"
"ds_read_b64_tr_b16 a[194:195], v244 offset:53248\n"
"v_mfma_f32_16x16x32_bf16 v[12:15], a[180:183], v[84:87], v[12:15]\n"
"v_exp_f32 v7, v7\n"
"v_mfma_f32_16x16x32_bf16 v[12:15], a[184:187], v[88:91], v[12:15]\n"
"ds_read_b64_tr_b16 a[196:197], v245 offset:49152\n"
"v_cvt_pk_bf16_f32 v19, v6, v7\n"
"v_mfma_f32_16x16x32_bf16 v[12:15], a[188:191], v[92:95], v[12:15]\n"
"s_waitcnt lgkmcnt(11)\n"
"buffer_load_dwordx4 v246, s[8:11], s16 offen offset:1024 lds\n"
"v_mfma_f32_16x16x32_bf16 a[0:3], a[224:227], v[16:19], a[0:3]\n"
"v_exp_f32 v8, v8\n"
"v_mfma_f32_16x16x32_bf16 a[4:7], a[228:231], v[16:19], a[4:7]\n"
"ds_read_b64_tr_b16 a[198:199], v245 offset:53248\n"
"v_mfma_f32_16x16x32_bf16 a[8:11], a[232:235], v[16:19], a[8:11]\n"
"v_exp_f32 v9, v9\n"
"v_mfma_f32_16x16x32_bf16 a[12:15], a[236:239], v[16:19], a[12:15]\n"
"ds_read_b64_tr_b16 a[200:201], v244 offset:49408\n"
"v_cvt_pk_bf16_f32 v20, v8, v9\n"
"v_mfma_f32_16x16x32_bf16 a[16:19], a[240:243], v[16:19], a[16:19]\n"
"v_exp_f32 v10, v10\n"
"v_mfma_f32_16x16x32_bf16 a[20:23], a[244:247], v[16:19], a[20:23]\n"
"ds_read_b64_tr_b16 a[202:203], v244 offset:53504\n"
"v_mfma_f32_16x16x32_bf16 a[24:27], a[248:251], v[16:19], a[24:27]\n"
"v_exp_f32 v11, v11\n"
"v_mfma_f32_16x16x32_bf16 a[28:31], a[252:255], v[16:19], a[28:31]\n"
"v_cvt_pk_bf16_f32 v21, v10, v11\n"
"v_mfma_f32_16x16x32_bf16 v[44:47], v[40:43], v[16:19], v[44:47]\n"
"v_exp_f32 v12, v12\n"
"v_mfma_f32_16x16x32_bf16 v[0:3], a[160:163], v[96:99], v[32:35]\n"
"ds_read_b64_tr_b16 a[204:205], v245 offset:49408\n"
"v_mfma_f32_16x16x32_bf16 v[0:3], a[164:167], v[100:103], v[0:3]\n"
"v_exp_f32 v13, v13\n"
"v_mfma_f32_16x16x32_bf16 v[0:3], a[168:171], v[104:107], v[0:3]\n"
"ds_read_b64_tr_b16 a[206:207], v245 offset:53504\n"
"v_cvt_pk_bf16_f32 v22, v12, v13\n"
"v_mfma_f32_16x16x32_bf16 v[0:3], a[172:175], v[108:111], v[0:3]\n"
"v_exp_f32 v14, v14\n"
"v_mfma_f32_16x16x32_bf16 v[4:7], a[176:179], v[96:99], v[32:35]\n"
"ds_read_b64_tr_b16 a[208:209], v244 offset:49664\n"
"v_mfma_f32_16x16x32_bf16 v[4:7], a[180:183], v[100:103], v[4:7]\n"
"v_exp_f32 v15, v15\n"
"v_mfma_f32_16x16x32_bf16 v[4:7], a[184:187], v[104:107], v[4:7]\n"
"ds_read_b64_tr_b16 a[210:211], v244 offset:53760\n"
"v_cvt_pk_bf16_f32 v23, v14, v15\n"
"v_mfma_f32_16x16x32_bf16 v[4:7], a[188:191], v[108:111], v[4:7]\n"
"buffer_load_dwordx4 v247, s[8:11], s16 offen offset:2048 lds\n"
"v_mfma_f32_16x16x32_bf16 a[32:35], a[224:227], v[20:23], a[32:35]\n"
"v_exp_f32 v0, v0\n"
"v_mfma_f32_16x16x32_bf16 a[36:39], a[228:231], v[20:23], a[36:39]\n"
"ds_read_b64_tr_b16 a[212:213], v245 offset:49664\n"
"v_mfma_f32_16x16x32_bf16 a[40:43], a[232:235], v[20:23], a[40:43]\n"
"v_exp_f32 v1, v1\n"
"v_mfma_f32_16x16x32_bf16 a[44:47], a[236:239], v[20:23], a[44:47]\n"
"ds_read_b64_tr_b16 a[214:215], v245 offset:53760\n"
"v_cvt_pk_bf16_f32 v16, v0, v1\n"
"v_mfma_f32_16x16x32_bf16 a[48:51], a[240:243], v[20:23], a[48:51]\n"
"v_exp_f32 v2, v2\n"
"v_mfma_f32_16x16x32_bf16 a[52:55], a[244:247], v[20:23], a[52:55]\n"
"ds_read_b64_tr_b16 a[216:217], v244 offset:49920\n"
"v_mfma_f32_16x16x32_bf16 a[56:59], a[248:251], v[20:23], a[56:59]\n"
"v_exp_f32 v3, v3\n"
"v_mfma_f32_16x16x32_bf16 a[60:63], a[252:255], v[20:23], a[60:63]\n"
"v_cvt_pk_bf16_f32 v17, v2, v3\n"
"v_mfma_f32_16x16x32_bf16 v[48:51], v[40:43], v[20:23], v[48:51]\n"
"v_exp_f32 v4, v4\n"
"v_mfma_f32_16x16x32_bf16 v[8:11], a[160:163], v[112:115], v[36:39]\n"
"ds_read_b64_tr_b16 a[218:219], v244 offset:54016\n"
"v_mfma_f32_16x16x32_bf16 v[8:11], a[164:167], v[116:119], v[8:11]\n"
"v_exp_f32 v5, v5\n"
"v_mfma_f32_16x16x32_bf16 v[8:11], a[168:171], v[120:123], v[8:11]\n"
"ds_read_b64_tr_b16 a[220:221], v245 offset:49920\n"
"v_cvt_pk_bf16_f32 v18, v4, v5\n"
"v_mfma_f32_16x16x32_bf16 v[8:11], a[172:175], v[124:127], v[8:11]\n"
"v_exp_f32 v6, v6\n"
"v_mfma_f32_16x16x32_bf16 v[12:15], a[176:179], v[112:115], v[36:39]\n"
"ds_read_b64_tr_b16 a[222:223], v245 offset:54016\n"
"v_mfma_f32_16x16x32_bf16 v[12:15], a[180:183], v[116:119], v[12:15]\n"
"v_exp_f32 v7, v7\n"
"v_mfma_f32_16x16x32_bf16 v[12:15], a[184:187], v[120:123], v[12:15]\n"
"v_cvt_pk_bf16_f32 v19, v6, v7\n"
"v_mfma_f32_16x16x32_bf16 v[12:15], a[188:191], v[124:127], v[12:15]\n"
"buffer_load_dwordx4 v247, s[8:11], s16 offen offset:3072 lds\n"
"v_mfma_f32_16x16x32_bf16 a[64:67], a[224:227], v[16:19], a[64:67]\n"
"v_exp_f32 v8, v8\n"
"v_mfma_f32_16x16x32_bf16 a[68:71], a[228:231], v[16:19], a[68:71]\n"
"s_nop 0\n"
"v_mfma_f32_16x16x32_bf16 a[72:75], a[232:235], v[16:19], a[72:75]\n"
"v_exp_f32 v9, v9\n"
"v_mfma_f32_16x16x32_bf16 a[76:79], a[236:239], v[16:19], a[76:79]\n"
"v_cvt_pk_bf16_f32 v20, v8, v9\n"
"v_mfma_f32_16x16x32_bf16 a[80:83], a[240:243], v[16:19], a[80:83]\n"
"v_exp_f32 v10, v10\n"
"v_mfma_f32_16x16x32_bf16 a[84:87], a[244:247], v[16:19], a[84:87]\n"
"s_nop 0\n"
"v_mfma_f32_16x16x32_bf16 a[88:91], a[248:251], v[16:19], a[88:91]\n"
"v_exp_f32 v11, v11\n"
"v_mfma_f32_16x16x32_bf16 a[92:95], a[252:255], v[16:19], a[92:95]\n"
"s_waitcnt lgkmcnt(8)\n"
"v_cvt_pk_bf16_f32 v21, v10, v11\n"
"v_mfma_f32_16x16x32_bf16 v[52:55], v[40:43], v[16:19], v[52:55]\n"
"v_exp_f32 v12, v12\n"
"v_mfma_f32_16x16x32_bf16 v[0:3], a[128:131], v[64:67], v[24:27]\n"
"ds_read_b128 a[160:163], v241 offset:40960\n"
"v_mfma_f32_16x16x32_bf16 v[0:3], a[132:135], v[68:71], v[0:3]\n"
"v_exp_f32 v13, v13\n"
"v_mfma_f32_16x16x32_bf16 v[0:3], a[136:139], v[72:75], v[0:3]\n"
"ds_read_b128 a[164:167], v241 offset:41216\n"
"v_cvt_pk_bf16_f32 v22, v12, v13\n"
"v_mfma_f32_16x16x32_bf16 v[0:3], a[140:143], v[76:79], v[0:3]\n"
"v_exp_f32 v14, v14\n"
"v_mfma_f32_16x16x32_bf16 v[4:7], a[144:147], v[64:67], v[24:27]\n"
"ds_read_b128 a[168:171], v241 offset:41472\n"
"v_mfma_f32_16x16x32_bf16 v[4:7], a[148:151], v[68:71], v[4:7]\n"
"v_exp_f32 v15, v15\n"
"v_mfma_f32_16x16x32_bf16 v[4:7], a[152:155], v[72:75], v[4:7]\n"
"s_add_u32 m0, s18, 81920\n"
"ds_read_b128 a[172:175], v241 offset:41728\n"
"v_cvt_pk_bf16_f32 v23, v14, v15\n"
"v_mfma_f32_16x16x32_bf16 v[4:7], a[156:159], v[76:79], v[4:7]\n"
"buffer_load_dwordx4 v246, s[12:15], s16 offen offset:0 lds\n"
"v_mfma_f32_16x16x32_bf16 a[96:99], a[224:227], v[20:23], a[96:99]\n"
"v_exp_f32 v0, v0\n"
"v_mfma_f32_16x16x32_bf16 a[100:103], a[228:231], v[20:23], a[100:103]\n"
"ds_read_b128 a[176:179], v241 offset:45056\n"
"v_mfma_f32_16x16x32_bf16 a[104:107], a[232:235], v[20:23], a[104:107]\n"
"v_exp_f32 v1, v1\n"
"v_mfma_f32_16x16x32_bf16 a[108:111], a[236:239], v[20:23], a[108:111]\n"
"ds_read_b128 a[180:183], v241 offset:45312\n"
"v_cvt_pk_bf16_f32 v16, v0, v1\n"
"v_mfma_f32_16x16x32_bf16 a[112:115], a[240:243], v[20:23], a[112:115]\n"
"v_exp_f32 v2, v2\n"
"v_mfma_f32_16x16x32_bf16 a[116:119], a[244:247], v[20:23], a[116:119]\n"
"ds_read_b128 a[184:187], v241 offset:45568\n"
"v_mfma_f32_16x16x32_bf16 a[120:123], a[248:251], v[20:23], a[120:123]\n"
"v_exp_f32 v3, v3\n"
"v_mfma_f32_16x16x32_bf16 a[124:127], a[252:255], v[20:23], a[124:127]\n"
"v_cvt_pk_bf16_f32 v17, v2, v3\n"
"v_mfma_f32_16x16x32_bf16 v[56:59], v[40:43], v[20:23], v[56:59]\n"
"v_exp_f32 v4, v4\n"
"v_mfma_f32_16x16x32_bf16 v[8:11], a[128:131], v[80:83], v[28:31]\n"
"ds_read_b128 a[188:191], v241 offset:45824\n"
"v_mfma_f32_16x16x32_bf16 v[8:11], a[132:135], v[84:87], v[8:11]\n"
"v_exp_f32 v5, v5\n"
"v_mfma_f32_16x16x32_bf16 v[8:11], a[136:139], v[88:91], v[8:11]\n"
"ds_read_b64_tr_b16 a[224:225], v244 offset:57344\n"
"v_cvt_pk_bf16_f32 v18, v4, v5\n"
"v_mfma_f32_16x16x32_bf16 v[8:11], a[140:143], v[92:95], v[8:11]\n"
"v_exp_f32 v6, v6\n"
"v_mfma_f32_16x16x32_bf16 v[12:15], a[144:147], v[80:83], v[28:31]\n"
"ds_read_b64_tr_b16 a[226:227], v244 offset:61440\n"
"v_mfma_f32_16x16x32_bf16 v[12:15], a[148:151], v[84:87], v[12:15]\n"
"v_exp_f32 v7, v7\n"
"v_mfma_f32_16x16x32_bf16 v[12:15], a[152:155], v[88:91], v[12:15]\n"
"ds_read_b64_tr_b16 a[228:229], v245 offset:57344\n"
"v_cvt_pk_bf16_f32 v19, v6, v7\n"
"v_mfma_f32_16x16x32_bf16 v[12:15], a[156:159], v[92:95], v[12:15]\n"
"s_waitcnt lgkmcnt(11)\n"
"buffer_load_dwordx4 v247, s[12:15], s16 offen offset:1024 lds\n"
"v_mfma_f32_16x16x32_bf16 a[0:3], a[192:195], v[16:19], a[0:3]\n"
"v_exp_f32 v8, v8\n"
"v_mfma_f32_16x16x32_bf16 a[4:7], a[196:199], v[16:19], a[4:7]\n"
"ds_read_b64_tr_b16 a[230:231], v245 offset:61440\n"
"v_mfma_f32_16x16x32_bf16 a[8:11], a[200:203], v[16:19], a[8:11]\n"
"v_exp_f32 v9, v9\n"
"v_mfma_f32_16x16x32_bf16 a[12:15], a[204:207], v[16:19], a[12:15]\n"
"ds_read_b64_tr_b16 a[232:233], v244 offset:57600\n"
"v_cvt_pk_bf16_f32 v20, v8, v9\n"
"v_mfma_f32_16x16x32_bf16 a[16:19], a[208:211], v[16:19], a[16:19]\n"
"v_exp_f32 v10, v10\n"
"v_mfma_f32_16x16x32_bf16 a[20:23], a[212:215], v[16:19], a[20:23]\n"
"ds_read_b64_tr_b16 a[234:235], v244 offset:61696\n"
"v_mfma_f32_16x16x32_bf16 a[24:27], a[216:219], v[16:19], a[24:27]\n"
"v_exp_f32 v11, v11\n"
"v_mfma_f32_16x16x32_bf16 a[28:31], a[220:223], v[16:19], a[28:31]\n"
"v_cvt_pk_bf16_f32 v21, v10, v11\n"
"v_mfma_f32_16x16x32_bf16 v[44:47], v[40:43], v[16:19], v[44:47]\n"
"v_exp_f32 v12, v12\n"
"v_mfma_f32_16x16x32_bf16 v[0:3], a[128:131], v[96:99], v[32:35]\n"
"ds_read_b64_tr_b16 a[236:237], v245 offset:57600\n"
"v_mfma_f32_16x16x32_bf16 v[0:3], a[132:135], v[100:103], v[0:3]\n"
"v_exp_f32 v13, v13\n"
"v_mfma_f32_16x16x32_bf16 v[0:3], a[136:139], v[104:107], v[0:3]\n"
"ds_read_b64_tr_b16 a[238:239], v245 offset:61696\n"
"v_cvt_pk_bf16_f32 v22, v12, v13\n"
"v_mfma_f32_16x16x32_bf16 v[0:3], a[140:143], v[108:111], v[0:3]\n"
"v_exp_f32 v14, v14\n"
"v_mfma_f32_16x16x32_bf16 v[4:7], a[144:147], v[96:99], v[32:35]\n"
"ds_read_b64_tr_b16 a[240:241], v244 offset:57856\n"
"v_mfma_f32_16x16x32_bf16 v[4:7], a[148:151], v[100:103], v[4:7]\n"
"v_exp_f32 v15, v15\n"
"v_mfma_f32_16x16x32_bf16 v[4:7], a[152:155], v[104:107], v[4:7]\n"
"ds_read_b64_tr_b16 a[242:243], v244 offset:61952\n"
"v_cvt_pk_bf16_f32 v23, v14, v15\n"
"v_mfma_f32_16x16x32_bf16 v[4:7], a[156:159], v[108:111], v[4:7]\n"
"buffer_load_dwordx4 v246, s[12:15], s16 offen offset:2048 lds\n"
"v_mfma_f32_16x16x32_bf16 a[32:35], a[192:195], v[20:23], a[32:35]\n"
"v_exp_f32 v0, v0\n"
"v_mfma_f32_16x16x32_bf16 a[36:39], a[196:199], v[20:23], a[36:39]\n"
"ds_read_b64_tr_b16 a[244:245], v245 offset:57856\n"
"v_mfma_f32_16x16x32_bf16 a[40:43], a[200:203], v[20:23], a[40:43]\n"
"v_exp_f32 v1, v1\n"
"v_mfma_f32_16x16x32_bf16 a[44:47], a[204:207], v[20:23], a[44:47]\n"
"ds_read_b64_tr_b16 a[246:247], v245 offset:61952\n"
"v_cvt_pk_bf16_f32 v16, v0, v1\n"
"v_mfma_f32_16x16x32_bf16 a[48:51], a[208:211], v[20:23], a[48:51]\n"
"v_exp_f32 v2, v2\n"
"v_mfma_f32_16x16x32_bf16 a[52:55], a[212:215], v[20:23], a[52:55]\n"
"ds_read_b64_tr_b16 a[248:249], v244 offset:58112\n"
"v_mfma_f32_16x16x32_bf16 a[56:59], a[216:219], v[20:23], a[56:59]\n"
"v_exp_f32 v3, v3\n"
"v_mfma_f32_16x16x32_bf16 a[60:63], a[220:223], v[20:23], a[60:63]\n"
"v_cvt_pk_bf16_f32 v17, v2, v3\n"
"v_mfma_f32_16x16x32_bf16 v[48:51], v[40:43], v[20:23], v[48:51]\n"
"v_exp_f32 v4, v4\n"
"v_mfma_f32_16x16x32_bf16 v[8:11], a[128:131], v[112:115], v[36:39]\n"
"ds_read_b64_tr_b16 a[250:251], v244 offset:62208\n"
"v_mfma_f32_16x16x32_bf16 v[8:11], a[132:135], v[116:119], v[8:11]\n"
"v_exp_f32 v5, v5\n"
"v_mfma_f32_16x16x32_bf16 v[8:11], a[136:139], v[120:123], v[8:11]\n"
"ds_read_b64_tr_b16 a[252:253], v245 offset:58112\n"
"v_cvt_pk_bf16_f32 v18, v4, v5\n"
"v_mfma_f32_16x16x32_bf16 v[8:11], a[140:143], v[124:127], v[8:11]\n"
"v_exp_f32 v6, v6\n"
"v_mfma_f32_16x16x32_bf16 v[12:15], a[144:147], v[112:115], v[36:39]\n"
"ds_read_b64_tr_b16 a[254:255], v245 offset:62208\n"
"v_mfma_f32_16x16x32_bf16 v[12:15], a[148:151], v[116:119], v[12:15]\n"
"v_exp_f32 v7, v7\n"
"v_mfma_f32_16x16x32_bf16 v[12:15], a[152:155], v[120:123], v[12:15]\n"
"v_cvt_pk_bf16_f32 v19, v6, v7\n"
"v_mfma_f32_16x16x32_bf16 v[12:15], a[156:159], v[124:127], v[12:15]\n"
"buffer_load_dwordx4 v247, s[12:15], s16 offen offset:3072 lds\n"
"v_mfma_f32_16x16x32_bf16 a[64:67], a[192:195], v[16:19], a[64:67]\n"
"s_add_u32 s16, s16, 16384\n"
"v_exp_f32 v8, v8\n"
"v_mfma_f32_16x16x32_bf16 a[68:71], a[196:199], v[16:19], a[68:71]\n"
"s_nop 0\n"
"v_mfma_f32_16x16x32_bf16 a[72:75], a[200:203], v[16:19], a[72:75]\n"
"v_exp_f32 v9, v9\n"
"v_mfma_f32_16x16x32_bf16 a[76:79], a[204:207], v[16:19], a[76:79]\n"
"v_cvt_pk_bf16_f32 v20, v8, v9\n"
"v_mfma_f32_16x16x32_bf16 a[80:83], a[208:211], v[16:19], a[80:83]\n"
"v_exp_f32 v10, v10\n"
"v_mfma_f32_16x16x32_bf16 a[84:87], a[212:215], v[16:19], a[84:87]\n"
"s_nop 0\n"
"v_mfma_f32_16x16x32_bf16 a[88:91], a[216:219], v[16:19], a[88:91]\n"
"v_exp_f32 v11, v11\n"
"v_mfma_f32_16x16x32_bf16 a[92:95], a[220:223], v[16:19], a[92:95]\n"
"s_waitcnt vmcnt(16) lgkmcnt(0)\n"
"s_barrier\n"
"v_cvt_pk_bf16_f32 v21, v10, v11\n"
"v_mfma_f32_16x16x32_bf16 v[52:55], v[40:43], v[16:19], v[52:55]\n"
"v_exp_f32 v12, v12\n"
"v_mfma_f32_16x16x32_bf16 v[0:3], a[160:163], v[64:67], v[24:27]\n"
"ds_read_b128 a[128:131], v240 offset:0\n"
"v_mfma_f32_16x16x32_bf16 v[0:3], a[164:167], v[68:71], v[0:3]\n"
"v_exp_f32 v13, v13\n"
"v_mfma_f32_16x16x32_bf16 v[0:3], a[168:171], v[72:75], v[0:3]\n"
"ds_read_b128 a[132:135], v240 offset:256\n"
"v_cvt_pk_bf16_f32 v22, v12, v13\n"
"v_mfma_f32_16x16x32_bf16 v[0:3], a[172:175], v[76:79], v[0:3]\n"
"v_exp_f32 v14, v14\n"
"v_mfma_f32_16x16x32_bf16 v[4:7], a[176:179], v[64:67], v[24:27]\n"
"ds_read_b128 a[136:139], v240 offset:512\n"
"v_mfma_f32_16x16x32_bf16 v[4:7], a[180:183], v[68:71], v[4:7]\n"
"v_exp_f32 v15, v15\n"
"v_mfma_f32_16x16x32_bf16 v[4:7], a[184:187], v[72:75], v[4:7]\n"
"s_add_u32 m0, s18, 98304\n"
"ds_read_b128 a[140:143], v240 offset:768\n"
"v_cvt_pk_bf16_f32 v23, v14, v15\n"
"v_mfma_f32_16x16x32_bf16 v[4:7], a[188:191], v[76:79], v[4:7]\n"
"buffer_load_dwordx4 v246, s[8:11], s16 offen offset:0 lds\n"
"v_mfma_f32_16x16x32_bf16 a[96:99], a[192:195], v[20:23], a[96:99]\n"
"v_exp_f32 v0, v0\n"
"v_mfma_f32_16x16x32_bf16 a[100:103], a[196:199], v[20:23], a[100:103]\n"
"ds_read_b128 a[144:147], v240 offset:4096\n"
"v_mfma_f32_16x16x32_bf16 a[104:107], a[200:203], v[20:23], a[104:107]\n"
"v_exp_f32 v1, v1\n"
"v_mfma_f32_16x16x32_bf16 a[108:111], a[204:207], v[20:23], a[108:111]\n"
"ds_read_b128 a[148:151], v240 offset:4352\n"
"v_cvt_pk_bf16_f32 v16, v0, v1\n"
"v_mfma_f32_16x16x32_bf16 a[112:115], a[208:211], v[20:23], a[112:115]\n"
"v_exp_f32 v2, v2\n"
"v_mfma_f32_16x16x32_bf16 a[116:119], a[212:215], v[20:23], a[116:119]\n"
"ds_read_b128 a[152:155], v240 offset:4608\n"
"v_mfma_f32_16x16x32_bf16 a[120:123], a[216:219], v[20:23], a[120:123]\n"
"v_exp_f32 v3, v3\n"
"v_mfma_f32_16x16x32_bf16 a[124:127], a[220:223], v[20:23], a[124:127]\n"
"v_cvt_pk_bf16_f32 v17, v2, v3\n"
"v_mfma_f32_16x16x32_bf16 v[56:59], v[40:43], v[20:23], v[56:59]\n"
"v_exp_f32 v4, v4\n"
"v_mfma_f32_16x16x32_bf16 v[8:11], a[160:163], v[80:83], v[28:31]\n"
"ds_read_b128 a[156:159], v240 offset:4864\n"
"v_mfma_f32_16x16x32_bf16 v[8:11], a[164:167], v[84:87], v[8:11]\n"
"v_exp_f32 v5, v5\n"
"v_mfma_f32_16x16x32_bf16 v[8:11], a[168:171], v[88:91], v[8:11]\n"
"ds_read_b64_tr_b16 a[192:193], v242 offset:16384\n"
"v_cvt_pk_bf16_f32 v18, v4, v5\n"
"v_mfma_f32_16x16x32_bf16 v[8:11], a[172:175], v[92:95], v[8:11]\n"
"v_exp_f32 v6, v6\n"
"v_mfma_f32_16x16x32_bf16 v[12:15], a[176:179], v[80:83], v[28:31]\n"
"ds_read_b64_tr_b16 a[194:195], v242 offset:20480\n"
"v_mfma_f32_16x16x32_bf16 v[12:15], a[180:183], v[84:87], v[12:15]\n"
"v_exp_f32 v7, v7\n"
"v_mfma_f32_16x16x32_bf16 v[12:15], a[184:187], v[88:91], v[12:15]\n"
"ds_read_b64_tr_b16 a[196:197], v243 offset:16384\n"
"v_cvt_pk_bf16_f32 v19, v6, v7\n"
"v_mfma_f32_16x16x32_bf16 v[12:15], a[188:191], v[92:95], v[12:15]\n"
"s_waitcnt lgkmcnt(11)\n"
"buffer_load_dwordx4 v246, s[8:11], s16 offen offset:1024 lds\n"
"v_mfma_f32_16x16x32_bf16 a[0:3], a[224:227], v[16:19], a[0:3]\n"
"v_exp_f32 v8, v8\n"
"v_mfma_f32_16x16x32_bf16 a[4:7], a[228:231], v[16:19], a[4:7]\n"
"ds_read_b64_tr_b16 a[198:199], v243 offset:20480\n"
"v_mfma_f32_16x16x32_bf16 a[8:11], a[232:235], v[16:19], a[8:11]\n"
"v_exp_f32 v9, v9\n"
"v_mfma_f32_16x16x32_bf16 a[12:15], a[236:239], v[16:19], a[12:15]\n"
"ds_read_b64_tr_b16 a[200:201], v242 offset:16640\n"
"v_cvt_pk_bf16_f32 v20, v8, v9\n"
"v_mfma_f32_16x16x32_bf16 a[16:19], a[240:243], v[16:19], a[16:19]\n"
"v_exp_f32 v10, v10\n"
"v_mfma_f32_16x16x32_bf16 a[20:23], a[244:247], v[16:19], a[20:23]\n"
"ds_read_b64_tr_b16 a[202:203], v242 offset:20736\n"
"v_mfma_f32_16x16x32_bf16 a[24:27], a[248:251], v[16:19], a[24:27]\n"
"v_exp_f32 v11, v11\n"
"v_mfma_f32_16x16x32_bf16 a[28:31], a[252:255], v[16:19], a[28:31]\n"
"v_cvt_pk_bf16_f32 v21, v10, v11\n"
"v_mfma_f32_16x16x32_bf16 v[44:47], v[40:43], v[16:19], v[44:47]\n"
"v_exp_f32 v12, v12\n"
"v_mfma_f32_16x16x32_bf16 v[0:3], a[160:163], v[96:99], v[32:35]\n"
"ds_read_b64_tr_b16 a[204:205], v243 offset:16640\n"
"v_mfma_f32_16x16x32_bf16 v[0:3], a[164:167], v[100:103], v[0:3]\n"
"v_exp_f32 v13, v13\n"
"v_mfma_f32_16x16x32_bf16 v[0:3], a[168:171], v[104:107], v[0:3]\n"
"ds_read_b64_tr_b16 a[206:207], v243 offset:20736\n"
"v_cvt_pk_bf16_f32 v22, v12, v13\n"
"v_mfma_f32_16x16x32_bf16 v[0:3], a[172:175], v[108:111], v[0:3]\n"
"v_exp_f32 v14, v14\n"
"v_mfma_f32_16x16x32_bf16 v[4:7], a[176:179], v[96:99], v[32:35]\n"
"ds_read_b64_tr_b16 a[208:209], v242 offset:16896\n"
"v_mfma_f32_16x16x32_bf16 v[4:7], a[180:183], v[100:103], v[4:7]\n"
"v_exp_f32 v15, v15\n"
"v_mfma_f32_16x16x32_bf16 v[4:7], a[184:187], v[104:107], v[4:7]\n"
"ds_read_b64_tr_b16 a[210:211], v242 offset:20992\n"
"v_cvt_pk_bf16_f32 v23, v14, v15\n"
"v_mfma_f32_16x16x32_bf16 v[4:7], a[188:191], v[108:111], v[4:7]\n"
"buffer_load_dwordx4 v247, s[8:11], s16 offen offset:2048 lds\n"
"v_mfma_f32_16x16x32_bf16 a[32:35], a[224:227], v[20:23], a[32:35]\n"
"v_exp_f32 v0, v0\n"
"v_mfma_f32_16x16x32_bf16 a[36:39], a[228:231], v[20:23], a[36:39]\n"
"ds_read_b64_tr_b16 a[212:213], v243 offset:16896\n"
"v_mfma_f32_16x16x32_bf16 a[40:43], a[232:235], v[20:23], a[40:43]\n"
"v_exp_f32 v1, v1\n"
"v_mfma_f32_16x16x32_bf16 a[44:47], a[236:239], v[20:23], a[44:47]\n"
"ds_read_b64_tr_b16 a[214:215], v243 offset:20992\n"
"v_cvt_pk_bf16_f32 v16, v0, v1\n"
"v_mfma_f32_16x16x32_bf16 a[48:51], a[240:243], v[20:23], a[48:51]\n"
"v_exp_f32 v2, v2\n"
"v_mfma_f32_16x16x32_bf16 a[52:55], a[244:247], v[20:23], a[52:55]\n"
"ds_read_b64_tr_b16 a[216:217], v242 offset:17152\n"
"v_mfma_f32_16x16x32_bf16 a[56:59], a[248:251], v[20:23], a[56:59]\n"
"v_exp_f32 v3, v3\n"
"v_mfma_f32_16x16x32_bf16 a[60:63], a[252:255], v[20:23], a[60:63]\n"
"v_cvt_pk_bf16_f32 v17, v2, v3\n"
"v_mfma_f32_16x16x32_bf16 v[48:51], v[40:43], v[20:23], v[48:51]\n"
"v_exp_f32 v4, v4\n"
"v_mfma_f32_16x16x32_bf16 v[8:11], a[160:163], v[112:115], v[36:39]\n"
"ds_read_b64_tr_b16 a[218:219], v242 offset:21248\n"
"v_mfma_f32_16x16x32_bf16 v[8:11], a[164:167], v[116:119], v[8:11]\n"
"v_exp_f32 v5, v5\n"
"v_mfma_f32_16x16x32_bf16 v[8:11], a[168:171], v[120:123], v[8:11]\n"
"ds_read_b64_tr_b16 a[220:221], v243 offset:17152\n"
"v_cvt_pk_bf16_f32 v18, v4, v5\n"
"v_mfma_f32_16x16x32_bf16 v[8:11], a[172:175], v[124:127], v[8:11]\n"
"v_exp_f32 v6, v6\n"
"v_mfma_f32_16x16x32_bf16 v[12:15], a[176:179], v[112:115], v[36:39]\n"
"ds_read_b64_tr_b16 a[222:223], v243 offset:21248\n"
"v_mfma_f32_16x16x32_bf16 v[12:15], a[180:183], v[116:119], v[12:15]\n"
"v_exp_f32 v7, v7\n"
"v_mfma_f32_16x16x32_bf16 v[12:15], a[184:187], v[120:123], v[12:15]\n"
"v_cvt_pk_bf16_f32 v19, v6, v7\n"
"v_mfma_f32_16x16x32_bf16 v[12:15], a[188:191], v[124:127], v[12:15]\n"
"buffer_load_dwordx4 v247, s[8:11], s16 offen offset:3072 lds\n"
"v_mfma_f32_16x16x32_bf16 a[64:67], a[224:227], v[16:19], a[64:67]\n"
"v_exp_f32 v8, v8\n"
"v_mfma_f32_16x16x32_bf16 a[68:71], a[228:231], v[16:19], a[68:71]\n"
"s_nop 0\n"
"v_mfma_f32_16x16x32_bf16 a[72:75], a[232:235], v[16:19], a[72:75]\n"
"v_exp_f32 v9, v9\n"
"v_mfma_f32_16x16x32_bf16 a[76:79], a[236:239], v[16:19], a[76:79]\n"
"v_cvt_pk_bf16_f32 v20, v8, v9\n"
"v_mfma_f32_16x16x32_bf16 a[80:83], a[240:243], v[16:19], a[80:83]\n"
"v_exp_f32 v10, v10\n"
"v_mfma_f32_16x16x32_bf16 a[84:87], a[244:247], v[16:19], a[84:87]\n"
"s_nop 0\n"
"v_mfma_f32_16x16x32_bf16 a[88:91], a[248:251], v[16:19], a[88:91]\n"
"v_exp_f32 v11, v11\n"
"v_mfma_f32_16x16x32_bf16 a[92:95], a[252:255], v[16:19], a[92:95]\n"
"s_waitcnt lgkmcnt(8)\n"
"v_cvt_pk_bf16_f32 v21, v10, v11\n"
"v_mfma_f32_16x16x32_bf16 v[52:55], v[40:43], v[16:19], v[52:55]\n"
"v_exp_f32 v12, v12\n"
"v_mfma_f32_16x16x32_bf16 v[0:3], a[128:131], v[64:67], v[24:27]\n"
"ds_read_b128 a[160:163], v240 offset:8192\n"
"v_mfma_f32_16x16x32_bf16 v[0:3], a[132:135], v[68:71], v[0:3]\n"
"v_exp_f32 v13, v13\n"
"v_mfma_f32_16x16x32_bf16 v[0:3], a[136:139], v[72:75], v[0:3]\n"
"ds_read_b128 a[164:167], v240 offset:8448\n"
"v_cvt_pk_bf16_f32 v22, v12, v13\n"
"v_mfma_f32_16x16x32_bf16 v[0:3], a[140:143], v[76:79], v[0:3]\n"
"v_exp_f32 v14, v14\n"
"v_mfma_f32_16x16x32_bf16 v[4:7], a[144:147], v[64:67], v[24:27]\n"
"ds_read_b128 a[168:171], v240 offset:8704\n"
"v_mfma_f32_16x16x32_bf16 v[4:7], a[148:151], v[68:71], v[4:7]\n"
"v_exp_f32 v15, v15\n"
"v_mfma_f32_16x16x32_bf16 v[4:7], a[152:155], v[72:75], v[4:7]\n"
"s_add_u32 m0, s18, 114688\n"
"ds_read_b128 a[172:175], v240 offset:8960\n"
"v_cvt_pk_bf16_f32 v23, v14, v15\n"
"v_mfma_f32_16x16x32_bf16 v[4:7], a[156:159], v[76:79], v[4:7]\n"
"buffer_load_dwordx4 v246, s[12:15], s16 offen offset:0 lds\n"
"v_mfma_f32_16x16x32_bf16 a[96:99], a[224:227], v[20:23], a[96:99]\n"
"v_exp_f32 v0, v0\n"
"v_mfma_f32_16x16x32_bf16 a[100:103], a[228:231], v[20:23], a[100:103]\n"
"ds_read_b128 a[176:179], v240 offset:12288\n"
"v_mfma_f32_16x16x32_bf16 a[104:107], a[232:235], v[20:23], a[104:107]\n"
"v_exp_f32 v1, v1\n"
"v_mfma_f32_16x16x32_bf16 a[108:111], a[236:239], v[20:23], a[108:111]\n"
"ds_read_b128 a[180:183], v240 offset:12544\n"
"v_cvt_pk_bf16_f32 v16, v0, v1\n"
"v_mfma_f32_16x16x32_bf16 a[112:115], a[240:243], v[20:23], a[112:115]\n"
"v_exp_f32 v2, v2\n"
"v_mfma_f32_16x16x32_bf16 a[116:119], a[244:247], v[20:23], a[116:119]\n"
"ds_read_b128 a[184:187], v240 offset:12800\n"
"v_mfma_f32_16x16x32_bf16 a[120:123], a[248:251], v[20:23], a[120:123]\n"
"v_exp_f32 v3, v3\n"
"v_mfma_f32_16x16x32_bf16 a[124:127], a[252:255], v[20:23], a[124:127]\n"
"v_cvt_pk_bf16_f32 v17, v2, v3\n"
"v_mfma_f32_16x16x32_bf16 v[56:59], v[40:43], v[20:23], v[56:59]\n"
"v_exp_f32 v4, v4\n"
"v_mfma_f32_16x16x32_bf16 v[8:11], a[128:131], v[80:83], v[28:31]\n"
"ds_read_b128 a[188:191], v240 offset:13056\n"
"v_mfma_f32_16x16x32_bf16 v[8:11], a[132:135], v[84:87], v[8:11]\n"
"v_exp_f32 v5, v5\n"
"v_mfma_f32_16x16x32_bf16 v[8:11], a[136:139], v[88:91], v[8:11]\n"
"ds_read_b64_tr_b16 a[224:225], v242 offset:24576\n"
"v_cvt_pk_bf16_f32 v18, v4, v5\n"
"v_mfma_f32_16x16x32_bf16 v[8:11], a[140:143], v[92:95], v[8:11]\n"
"v_exp_f32 v6, v6\n"
"v_mfma_f32_16x16x32_bf16 v[12:15], a[144:147], v[80:83], v[28:31]\n"
"ds_read_b64_tr_b16 a[226:227], v242 offset:28672\n"
"v_mfma_f32_16x16x32_bf16 v[12:15], a[148:151], v[84:87], v[12:15]\n"
"v_exp_f32 v7, v7\n"
"v_mfma_f32_16x16x32_bf16 v[12:15], a[152:155], v[88:91], v[12:15]\n"
"ds_read_b64_tr_b16 a[228:229], v243 offset:24576\n"
"v_cvt_pk_bf16_f32 v19, v6, v7\n"
"v_mfma_f32_16x16x32_bf16 v[12:15], a[156:159], v[92:95], v[12:15]\n"
"s_waitcnt lgkmcnt(11)\n"
"buffer_load_dwordx4 v247, s[12:15], s16 offen offset:1024 lds\n"
"v_mfma_f32_16x16x32_bf16 a[0:3], a[192:195], v[16:19], a[0:3]\n"
"v_exp_f32 v8, v8\n"
"v_mfma_f32_16x16x32_bf16 a[4:7], a[196:199], v[16:19], a[4:7]\n"
"ds_read_b64_tr_b16 a[230:231], v243 offset:28672\n"
"v_mfma_f32_16x16x32_bf16 a[8:11], a[200:203], v[16:19], a[8:11]\n"
"v_exp_f32 v9, v9\n"
"v_mfma_f32_16x16x32_bf16 a[12:15], a[204:207], v[16:19], a[12:15]\n"
"ds_read_b64_tr_b16 a[232:233], v242 offset:24832\n"
"v_cvt_pk_bf16_f32 v20, v8, v9\n"
"v_mfma_f32_16x16x32_bf16 a[16:19], a[208:211], v[16:19], a[16:19]\n"
"v_exp_f32 v10, v10\n"
"v_mfma_f32_16x16x32_bf16 a[20:23], a[212:215], v[16:19], a[20:23]\n"
"ds_read_b64_tr_b16 a[234:235], v242 offset:28928\n"
"v_mfma_f32_16x16x32_bf16 a[24:27], a[216:219], v[16:19], a[24:27]\n"
"v_exp_f32 v11, v11\n"
"v_mfma_f32_16x16x32_bf16 a[28:31], a[220:223], v[16:19], a[28:31]\n"
"v_cvt_pk_bf16_f32 v21, v10, v11\n"
"v_mfma_f32_16x16x32_bf16 v[44:47], v[40:43], v[16:19], v[44:47]\n"
"v_exp_f32 v12, v12\n"
"v_mfma_f32_16x16x32_bf16 v[0:3], a[128:131], v[96:99], v[32:35]\n"
"ds_read_b64_tr_b16 a[236:237], v243 offset:24832\n"
"v_mfma_f32_16x16x32_bf16 v[0:3], a[132:135], v[100:103], v[0:3]\n"
"v_exp_f32 v13, v13\n"
"v_mfma_f32_16x16x32_bf16 v[0:3], a[136:139], v[104:107], v[0:3]\n"
"ds_read_b64_tr_b16 a[238:239], v243 offset:28928\n"
"v_cvt_pk_bf16_f32 v22, v12, v13\n"
"v_mfma_f32_16x16x32_bf16 v[0:3], a[140:143], v[108:111], v[0:3]\n"
"v_exp_f32 v14, v14\n"
"v_mfma_f32_16x16x32_bf16 v[4:7], a[144:147], v[96:99], v[32:35]\n"
"ds_read_b64_tr_b16 a[240:241], v242 offset:25088\n"
"v_mfma_f32_16x16x32_bf16 v[4:7], a[148:151], v[100:103], v[4:7]\n"
"v_exp_f32 v15, v15\n"
"v_mfma_f32_16x16x32_bf16 v[4:7], a[152:155], v[104:107], v[4:7]\n"
"ds_read_b64_tr_b16 a[242:243], v242 offset:29184\n"
"v_cvt_pk_bf16_f32 v23, v14, v15\n"
"v_mfma_f32_16x16x32_bf16 v[4:7], a[156:159], v[108:111], v[4:7]\n"
"buffer_load_dwordx4 v246, s[12:15], s16 offen offset:2048 lds\n"
"v_mfma_f32_16x16x32_bf16 a[32:35], a[192:195], v[20:23], a[32:35]\n"
"v_exp_f32 v0, v0\n"
"v_mfma_f32_16x16x32_bf16 a[36:39], a[196:199], v[20:23], a[36:39]\n"
"ds_read_b64_tr_b16 a[244:245], v243 offset:25088\n"
"v_mfma_f32_16x16x32_bf16 a[40:43], a[200:203], v[20:23], a[40:43]\n"
"v_exp_f32 v1, v1\n"
"v_mfma_f32_16x16x32_bf16 a[44:47], a[204:207], v[20:23], a[44:47]\n"
"ds_read_b64_tr_b16 a[246:247], v243 offset:29184\n"
"v_cvt_pk_bf16_f32 v16, v0, v1\n"
"v_mfma_f32_16x16x32_bf16 a[48:51], a[208:211], v[20:23], a[48:51]\n"
"v_exp_f32 v2, v2\n"
"v_mfma_f32_16x16x32_bf16 a[52:55], a[212:215], v[20:23], a[52:55]\n"
"ds_read_b64_tr_b16 a[248:249], v242 offset:25344\n"
"v_mfma_f32_16x16x32_bf16 a[56:59], a[216:219], v[20:23], a[56:59]\n"
"v_exp_f32 v3, v3\n"
"v_mfma_f32_16x16x32_bf16 a[60:63], a[220:223], v[20:23], a[60:63]\n"
"v_cvt_pk_bf16_f32 v17, v2, v3\n"
"v_mfma_f32_16x16x32_bf16 v[48:51], v[40:43], v[20:23], v[48:51]\n"
"v_exp_f32 v4, v4\n"
"v_mfma_f32_16x16x32_bf16 v[8:11], a[128:131], v[112:115], v[36:39]\n"
"ds_read_b64_tr_b16 a[250:251], v242 offset:29440\n"
"v_mfma_f32_16x16x32_bf16 v[8:11], a[132:135], v[116:119], v[8:11]\n"
"v_exp_f32 v5, v5\n"
"v_mfma_f32_16x16x32_bf16 v[8:11], a[136:139], v[120:123], v[8:11]\n"
"ds_read_b64_tr_b16 a[252:253], v243 offset:25344\n"
"v_cvt_pk_bf16_f32 v18, v4, v5\n"
"v_mfma_f32_16x16x32_bf16 v[8:11], a[140:143], v[124:127], v[8:11]\n"
"v_exp_f32 v6, v6\n"
"v_mfma_f32_16x16x32_bf16 v[12:15], a[144:147], v[112:115], v[36:39]\n"
"ds_read_b64_tr_b16 a[254:255], v243 offset:29440\n"
"v_mfma_f32_16x16x32_bf16 v[12:15], a[148:151], v[116:119], v[12:15]\n"
"v_exp_f32 v7, v7\n"
"v_mfma_f32_16x16x32_bf16 v[12:15], a[152:155], v[120:123], v[12:15]\n"
"v_cvt_pk_bf16_f32 v19, v6, v7\n"
"v_mfma_f32_16x16x32_bf16 v[12:15], a[156:159], v[124:127], v[12:15]\n"
"buffer_load_dwordx4 v247, s[12:15], s16 offen offset:3072 lds\n"
"v_mfma_f32_16x16x32_bf16 a[64:67], a[192:195], v[16:19], a[64:67]\n"
"s_add_u32 s16, s16, 16384\n"
"v_exp_f32 v8, v8\n"
"v_mfma_f32_16x16x32_bf16 a[68:71], a[196:199], v[16:19], a[68:71]\n"
"s_nop 0\n"
"v_mfma_f32_16x16x32_bf16 a[72:75], a[200:203], v[16:19], a[72:75]\n"
"v_exp_f32 v9, v9\n"
"v_mfma_f32_16x16x32_bf16 a[76:79], a[204:207], v[16:19], a[76:79]\n"
"v_cvt_pk_bf16_f32 v20, v8, v9\n"
"v_mfma_f32_16x16x32_bf16 a[80:83], a[208:211], v[16:19], a[80:83]\n"
"v_exp_f32 v10, v10\n"
"v_mfma_f32_16x16x32_bf16 a[84:87], a[212:215], v[16:19], a[84:87]\n"
"s_nop 0\n"
"v_mfma_f32_16x16x32_bf16 a[88:91], a[216:219], v[16:19], a[88:91]\n"
"v_exp_f32 v11, v11\n"
"v_mfma_f32_16x16x32_bf16 a[92:95], a[220:223], v[16:19], a[92:95]\n"
"s_waitcnt vmcnt(16) lgkmcnt(0)\n"
"s_barrier\n"
"v_cvt_pk_bf16_f32 v21, v10, v11\n"
"v_mfma_f32_16x16x32_bf16 v[52:55], v[40:43], v[16:19], v[52:55]\n"
"v_exp_f32 v12, v12\n"
"v_mfma_f32_16x16x32_bf16 v[0:3], a[160:163], v[64:67], v[24:27]\n"
"ds_read_b128 a[128:131], v240 offset:32768\n"
"v_mfma_f32_16x16x32_bf16 v[0:3], a[164:167], v[68:71], v[0:3]\n"
"v_exp_f32 v13, v13\n"
"v_mfma_f32_16x16x32_bf16 v[0:3], a[168:171], v[72:75], v[0:3]\n"
"ds_read_b128 a[132:135], v240 offset:33024\n"
"v_cvt_pk_bf16_f32 v22, v12, v13\n"
"v_mfma_f32_16x16x32_bf16 v[0:3], a[172:175], v[76:79], v[0:3]\n"
"v_exp_f32 v14, v14\n"
"v_mfma_f32_16x16x32_bf16 v[4:7], a[176:179], v[64:67], v[24:27]\n"
"ds_read_b128 a[136:139], v240 offset:33280\n"
"v_mfma_f32_16x16x32_bf16 v[4:7], a[180:183], v[68:71], v[4:7]\n"
"v_exp_f32 v15, v15\n"
"v_mfma_f32_16x16x32_bf16 v[4:7], a[184:187], v[72:75], v[4:7]\n"
"s_mov_b32 m0, s18\n"
"ds_read_b128 a[140:143], v240 offset:33536\n"
"v_cvt_pk_bf16_f32 v23, v14, v15\n"
"v_mfma_f32_16x16x32_bf16 v[4:7], a[188:191], v[76:79], v[4:7]\n"
"buffer_load_dwordx4 v246, s[8:11], s16 offen offset:0 lds\n"
"v_mfma_f32_16x16x32_bf16 a[96:99], a[192:195], v[20:23], a[96:99]\n"
"v_exp_f32 v0, v0\n"
"v_mfma_f32_16x16x32_bf16 a[100:103], a[196:199], v[20:23], a[100:103]\n"
"ds_read_b128 a[144:147], v240 offset:36864\n"
"v_mfma_f32_16x16x32_bf16 a[104:107], a[200:203], v[20:23], a[104:107]\n"
"v_exp_f32 v1, v1\n"
"v_mfma_f32_16x16x32_bf16 a[108:111], a[204:207], v[20:23], a[108:111]\n"
"ds_read_b128 a[148:151], v240 offset:37120\n"
"v_cvt_pk_bf16_f32 v16, v0, v1\n"
"v_mfma_f32_16x16x32_bf16 a[112:115], a[208:211], v[20:23], a[112:115]\n"
"v_exp_f32 v2, v2\n"
"v_mfma_f32_16x16x32_bf16 a[116:119], a[212:215], v[20:23], a[116:119]\n"
"ds_read_b128 a[152:155], v240 offset:37376\n"
"v_mfma_f32_16x16x32_bf16 a[120:123], a[216:219], v[20:23], a[120:123]\n"
"v_exp_f32 v3, v3\n"
"v_mfma_f32_16x16x32_bf16 a[124:127], a[220:223], v[20:23], a[124:127]\n"
"v_cvt_pk_bf16_f32 v17, v2, v3\n"
"v_mfma_f32_16x16x32_bf16 v[56:59], v[40:43], v[20:23], v[56:59]\n"
"v_exp_f32 v4, v4\n"
"v_mfma_f32_16x16x32_bf16 v[8:11], a[160:163], v[80:83], v[28:31]\n"
"ds_read_b128 a[156:159], v240 offset:37632\n"
"v_mfma_f32_16x16x32_bf16 v[8:11], a[164:167], v[84:87], v[8:11]\n"
"v_exp_f32 v5, v5\n"
"v_mfma_f32_16x16x32_bf16 v[8:11], a[168:171], v[88:91], v[8:11]\n"
"ds_read_b64_tr_b16 a[192:193], v242 offset:49152\n"
"v_cvt_pk_bf16_f32 v18, v4, v5\n"
"v_mfma_f32_16x16x32_bf16 v[8:11], a[172:175], v[92:95], v[8:11]\n"
"v_exp_f32 v6, v6\n"
"v_mfma_f32_16x16x32_bf16 v[12:15], a[176:179], v[80:83], v[28:31]\n"
"ds_read_b64_tr_b16 a[194:195], v242 offset:53248\n"
"v_mfma_f32_16x16x32_bf16 v[12:15], a[180:183], v[84:87], v[12:15]\n"
"v_exp_f32 v7, v7\n"
"v_mfma_f32_16x16x32_bf16 v[12:15], a[184:187], v[88:91], v[12:15]\n"
"ds_read_b64_tr_b16 a[196:197], v243 offset:49152\n"
"v_cvt_pk_bf16_f32 v19, v6, v7\n"
"v_mfma_f32_16x16x32_bf16 v[12:15], a[188:191], v[92:95], v[12:15]\n"
"s_waitcnt lgkmcnt(11)\n"
"buffer_load_dwordx4 v246, s[8:11], s16 offen offset:1024 lds\n"
"v_mfma_f32_16x16x32_bf16 a[0:3], a[224:227], v[16:19], a[0:3]\n"
"v_exp_f32 v8, v8\n"
"v_mfma_f32_16x16x32_bf16 a[4:7], a[228:231], v[16:19], a[4:7]\n"
"ds_read_b64_tr_b16 a[198:199], v243 offset:53248\n"
"v_mfma_f32_16x16x32_bf16 a[8:11], a[232:235], v[16:19], a[8:11]\n"
"v_exp_f32 v9, v9\n"
"v_mfma_f32_16x16x32_bf16 a[12:15], a[236:239], v[16:19], a[12:15]\n"
"ds_read_b64_tr_b16 a[200:201], v242 offset:49408\n"
"v_cvt_pk_bf16_f32 v20, v8, v9\n"
"v_mfma_f32_16x16x32_bf16 a[16:19], a[240:243], v[16:19], a[16:19]\n"
"v_exp_f32 v10, v10\n"
"v_mfma_f32_16x16x32_bf16 a[20:23], a[244:247], v[16:19], a[20:23]\n"
"ds_read_b64_tr_b16 a[202:203], v242 offset:53504\n"
"v_mfma_f32_16x16x32_bf16 a[24:27], a[248:251], v[16:19], a[24:27]\n"
"v_exp_f32 v11, v11\n"
"v_mfma_f32_16x16x32_bf16 a[28:31], a[252:255], v[16:19], a[28:31]\n"
"v_cvt_pk_bf16_f32 v21, v10, v11\n"
"v_mfma_f32_16x16x32_bf16 v[44:47], v[40:43], v[16:19], v[44:47]\n"
"v_exp_f32 v12, v12\n"
"v_mfma_f32_16x16x32_bf16 v[0:3], a[160:163], v[96:99], v[32:35]\n"
"ds_read_b64_tr_b16 a[204:205], v243 offset:49408\n"
"v_mfma_f32_16x16x32_bf16 v[0:3], a[164:167], v[100:103], v[0:3]\n"
"v_exp_f32 v13, v13\n"
"v_mfma_f32_16x16x32_bf16 v[0:3], a[168:171], v[104:107], v[0:3]\n"
"ds_read_b64_tr_b16 a[206:207], v243 offset:53504\n"
"v_cvt_pk_bf16_f32 v22, v12, v13\n"
"v_mfma_f32_16x16x32_bf16 v[0:3], a[172:175], v[108:111], v[0:3]\n"
"v_exp_f32 v14, v14\n"
"v_mfma_f32_16x16x32_bf16 v[4:7], a[176:179], v[96:99], v[32:35]\n"
"ds_read_b64_tr_b16 a[208:209], v242 offset:49664\n"
"v_mfma_f32_16x16x32_bf16 v[4:7], a[180:183], v[100:103], v[4:7]\n"
"v_exp_f32 v15, v15\n"
"v_mfma_f32_16x16x32_bf16 v[4:7], a[184:187], v[104:107], v[4:7]\n"
"ds_read_b64_tr_b16 a[210:211], v242 offset:53760\n"
"v_cvt_pk_bf16_f32 v23, v14, v15\n"
"v_mfma_f32_16x16x32_bf16 v[4:7], a[188:191], v[108:111], v[4:7]\n"
"buffer_load_dwordx4 v247, s[8:11], s16 offen offset:2048 lds\n"
"v_mfma_f32_16x16x32_bf16 a[32:35], a[224:227], v[20:23], a[32:35]\n"
"v_exp_f32 v0, v0\n"
"v_mfma_f32_16x16x32_bf16 a[36:39], a[228:231], v[20:23], a[36:39]\n"
"ds_read_b64_tr_b16 a[212:213], v243 offset:49664\n"
"v_mfma_f32_16x16x32_bf16 a[40:43], a[232:235], v[20:23], a[40:43]\n"
"v_exp_f32 v1, v1\n"
"v_mfma_f32_16x16x32_bf16 a[44:47], a[236:239], v[20:23], a[44:47]\n"
"ds_read_b64_tr_b16 a[214:215], v243 offset:53760\n"
"v_cvt_pk_bf16_f32 v16, v0, v1\n"
"v_mfma_f32_16x16x32_bf16 a[48:51], a[240:243], v[20:23], a[48:51]\n"
"v_exp_f32 v2, v2\n"
"v_mfma_f32_16x16x32_bf16 a[52:55], a[244:247], v[20:23], a[52:55]\n"
"ds_read_b64_tr_b16 a[216:217], v242 offset:49920\n"
"v_mfma_f32_16x16x32_bf16 a[56:59], a[248:251], v[20:23], a[56:59]\n"
"v_exp_f32 v3, v3\n"
"v_mfma_f32_16x16x32_bf16 a[60:63], a[252:255], v[20:23], a[60:63]\n"
"v_cvt_pk_bf16_f32 v17, v2, v3\n"
"v_mfma_f32_16x16x32_bf16 v[48:51], v[40:43], v[20:23], v[48:51]\n"
"v_exp_f32 v4, v4\n"
"v_mfma_f32_16x16x32_bf16 v[8:11], a[160:163], v[112:115], v[36:39]\n"
"ds_read_b64_tr_b16 a[218:219], v242 offset:54016\n"
"v_mfma_f32_16x16x32_bf16 v[8:11], a[164:167], v[116:119], v[8:11]\n"
"v_exp_f32 v5, v5\n"
"v_mfma_f32_16x16x32_bf16 v[8:11], a[168:171], v[120:123], v[8:11]\n"
"ds_read_b64_tr_b16 a[220:221], v243 offset:49920\n"
"v_cvt_pk_bf16_f32 v18, v4, v5\n"
"v_mfma_f32_16x16x32_bf16 v[8:11], a[172:175], v[124:127], v[8:11]\n"
"v_exp_f32 v6, v6\n"
"v_mfma_f32_16x16x32_bf16 v[12:15], a[176:179], v[112:115], v[36:39]\n"
"ds_read_b64_tr_b16 a[222:223], v243 offset:54016\n"
"v_mfma_f32_16x16x32_bf16 v[12:15], a[180:183], v[116:119], v[12:15]\n"
"v_exp_f32 v7, v7\n"
"v_mfma_f32_16x16x32_bf16 v[12:15], a[184:187], v[120:123], v[12:15]\n"
"v_cvt_pk_bf16_f32 v19, v6, v7\n"
"v_mfma_f32_16x16x32_bf16 v[12:15], a[188:191], v[124:127], v[12:15]\n"
"buffer_load_dwordx4 v247, s[8:11], s16 offen offset:3072 lds\n"
"v_mfma_f32_16x16x32_bf16 a[64:67], a[224:227], v[16:19], a[64:67]\n"
"v_exp_f32 v8, v8\n"
"v_mfma_f32_16x16x32_bf16 a[68:71], a[228:231], v[16:19], a[68:71]\n"
"s_nop 0\n"
"v_mfma_f32_16x16x32_bf16 a[72:75], a[232:235], v[16:19], a[72:75]\n"
"v_exp_f32 v9, v9\n"
"v_mfma_f32_16x16x32_bf16 a[76:79], a[236:239], v[16:19], a[76:79]\n"
"v_cvt_pk_bf16_f32 v20, v8, v9\n"
"v_mfma_f32_16x16x32_bf16 a[80:83], a[240:243], v[16:19], a[80:83]\n"
"v_exp_f32 v10, v10\n"
"v_mfma_f32_16x16x32_bf16 a[84:87], a[244:247], v[16:19], a[84:87]\n"
"s_nop 0\n"
"v_mfma_f32_16x16x32_bf16 a[88:91], a[248:251], v[16:19], a[88:91]\n"
"v_exp_f32 v11, v11\n"
"v_mfma_f32_16x16x32_bf16 a[92:95], a[252:255], v[16:19], a[92:95]\n"
"s_waitcnt lgkmcnt(8)\n"
"v_cvt_pk_bf16_f32 v21, v10, v11\n"
"v_mfma_f32_16x16x32_bf16 v[52:55], v[40:43], v[16:19], v[52:55]\n"
"v_exp_f32 v12, v12\n"
"v_mfma_f32_16x16x32_bf16 v[0:3], a[128:131], v[64:67], v[24:27]\n"
"ds_read_b128 a[160:163], v240 offset:40960\n"
"v_mfma_f32_16x16x32_bf16 v[0:3], a[132:135], v[68:71], v[0:3]\n"
"v_exp_f32 v13, v13\n"
"v_mfma_f32_16x16x32_bf16 v[0:3], a[136:139], v[72:75], v[0:3]\n"
"ds_read_b128 a[164:167], v240 offset:41216\n"
"v_cvt_pk_bf16_f32 v22, v12, v13\n"
"v_mfma_f32_16x16x32_bf16 v[0:3], a[140:143], v[76:79], v[0:3]\n"
"v_exp_f32 v14, v14\n"
"v_mfma_f32_16x16x32_bf16 v[4:7], a[144:147], v[64:67], v[24:27]\n"
"ds_read_b128 a[168:171], v240 offset:41472\n"
"v_mfma_f32_16x16x32_bf16 v[4:7], a[148:151], v[68:71], v[4:7]\n"
"v_exp_f32 v15, v15\n"
"v_mfma_f32_16x16x32_bf16 v[4:7], a[152:155], v[72:75], v[4:7]\n"
"s_add_u32 m0, s18, 16384\n"
"ds_read_b128 a[172:175], v240 offset:41728\n"
"v_cvt_pk_bf16_f32 v23, v14, v15\n"
"v_mfma_f32_16x16x32_bf16 v[4:7], a[156:159], v[76:79], v[4:7]\n"
"buffer_load_dwordx4 v246, s[12:15], s16 offen offset:0 lds\n"
"v_mfma_f32_16x16x32_bf16 a[96:99], a[224:227], v[20:23], a[96:99]\n"
"v_exp_f32 v0, v0\n"
"v_mfma_f32_16x16x32_bf16 a[100:103], a[228:231], v[20:23], a[100:103]\n"
"ds_read_b128 a[176:179], v240 offset:45056\n"
"v_mfma_f32_16x16x32_bf16 a[104:107], a[232:235], v[20:23], a[104:107]\n"
"v_exp_f32 v1, v1\n"
"v_mfma_f32_16x16x32_bf16 a[108:111], a[236:239], v[20:23], a[108:111]\n"
"ds_read_b128 a[180:183], v240 offset:45312\n"
"v_cvt_pk_bf16_f32 v16, v0, v1\n"
"v_mfma_f32_16x16x32_bf16 a[112:115], a[240:243], v[20:23], a[112:115]\n"
"v_exp_f32 v2, v2\n"
"v_mfma_f32_16x16x32_bf16 a[116:119], a[244:247], v[20:23], a[116:119]\n"
"ds_read_b128 a[184:187], v240 offset:45568\n"
"v_mfma_f32_16x16x32_bf16 a[120:123], a[248:251], v[20:23], a[120:123]\n"
"v_exp_f32 v3, v3\n"
"v_mfma_f32_16x16x32_bf16 a[124:127], a[252:255], v[20:23], a[124:127]\n"
"v_cvt_pk_bf16_f32 v17, v2, v3\n"
"v_mfma_f32_16x16x32_bf16 v[56:59], v[40:43], v[20:23], v[56:59]\n"
"v_exp_f32 v4, v4\n"
"v_mfma_f32_16x16x32_bf16 v[8:11], a[128:131], v[80:83], v[28:31]\n"
"ds_read_b128 a[188:191], v240 offset:45824\n"
"v_mfma_f32_16x16x32_bf16 v[8:11], a[132:135], v[84:87], v[8:11]\n"
"v_exp_f32 v5, v5\n"
"v_mfma_f32_16x16x32_bf16 v[8:11], a[136:139], v[88:91], v[8:11]\n"
"ds_read_b64_tr_b16 a[224:225], v242 offset:57344\n"
"v_cvt_pk_bf16_f32 v18, v4, v5\n"
"v_mfma_f32_16x16x32_bf16 v[8:11], a[140:143], v[92:95], v[8:11]\n"
"v_exp_f32 v6, v6\n"
"v_mfma_f32_16x16x32_bf16 v[12:15], a[144:147], v[80:83], v[28:31]\n"
"ds_read_b64_tr_b16 a[226:227], v242 offset:61440\n"
"v_mfma_f32_16x16x32_bf16 v[12:15], a[148:151], v[84:87], v[12:15]\n"
"v_exp_f32 v7, v7\n"
"v_mfma_f32_16x16x32_bf16 v[12:15], a[152:155], v[88:91], v[12:15]\n"
"ds_read_b64_tr_b16 a[228:229], v243 offset:57344\n"
"v_cvt_pk_bf16_f32 v19, v6, v7\n"
"v_mfma_f32_16x16x32_bf16 v[12:15], a[156:159], v[92:95], v[12:15]\n"
"s_waitcnt lgkmcnt(11)\n"
"buffer_load_dwordx4 v247, s[12:15], s16 offen offset:1024 lds\n"
"v_mfma_f32_16x16x32_bf16 a[0:3], a[192:195], v[16:19], a[0:3]\n"
"v_exp_f32 v8, v8\n"
"v_mfma_f32_16x16x32_bf16 a[4:7], a[196:199], v[16:19], a[4:7]\n"
"ds_read_b64_tr_b16 a[230:231], v243 offset:61440\n"
"v_mfma_f32_16x16x32_bf16 a[8:11], a[200:203], v[16:19], a[8:11]\n"
"v_exp_f32 v9, v9\n"
"v_mfma_f32_16x16x32_bf16 a[12:15], a[204:207], v[16:19], a[12:15]\n"
"ds_read_b64_tr_b16 a[232:233], v242 offset:57600\n"
"v_cvt_pk_bf16_f32 v20, v8, v9\n"
"v_mfma_f32_16x16x32_bf16 a[16:19], a[208:211], v[16:19], a[16:19]\n"
"v_exp_f32 v10, v10\n"
"v_mfma_f32_16x16x32_bf16 a[20:23], a[212:215], v[16:19], a[20:23]\n"
"ds_read_b64_tr_b16 a[234:235], v242 offset:61696\n"
"v_mfma_f32_16x16x32_bf16 a[24:27], a[216:219], v[16:19], a[24:27]\n"
"v_exp_f32 v11, v11\n"
"v_mfma_f32_16x16x32_bf16 a[28:31], a[220:223], v[16:19], a[28:31]\n"
"v_cvt_pk_bf16_f32 v21, v10, v11\n"
"v_mfma_f32_16x16x32_bf16 v[44:47], v[40:43], v[16:19], v[44:47]\n"
"v_exp_f32 v12, v12\n"
"v_mfma_f32_16x16x32_bf16 v[0:3], a[128:131], v[96:99], v[32:35]\n"
"ds_read_b64_tr_b16 a[236:237], v243 offset:57600\n"
"v_mfma_f32_16x16x32_bf16 v[0:3], a[132:135], v[100:103], v[0:3]\n"
"v_exp_f32 v13, v13\n"
"v_mfma_f32_16x16x32_bf16 v[0:3], a[136:139], v[104:107], v[0:3]\n"
"ds_read_b64_tr_b16 a[238:239], v243 offset:61696\n"
"v_cvt_pk_bf16_f32 v22, v12, v13\n"
"v_mfma_f32_16x16x32_bf16 v[0:3], a[140:143], v[108:111], v[0:3]\n"
"v_exp_f32 v14, v14\n"
"v_mfma_f32_16x16x32_bf16 v[4:7], a[144:147], v[96:99], v[32:35]\n"
"ds_read_b64_tr_b16 a[240:241], v242 offset:57856\n"
"v_mfma_f32_16x16x32_bf16 v[4:7], a[148:151], v[100:103], v[4:7]\n"
"v_exp_f32 v15, v15\n"
"v_mfma_f32_16x16x32_bf16 v[4:7], a[152:155], v[104:107], v[4:7]\n"
"ds_read_b64_tr_b16 a[242:243], v242 offset:61952\n"
"v_cvt_pk_bf16_f32 v23, v14, v15\n"
"v_mfma_f32_16x16x32_bf16 v[4:7], a[156:159], v[108:111], v[4:7]\n"
"buffer_load_dwordx4 v246, s[12:15], s16 offen offset:2048 lds\n"
"v_mfma_f32_16x16x32_bf16 a[32:35], a[192:195], v[20:23], a[32:35]\n"
"v_exp_f32 v0, v0\n"
"v_mfma_f32_16x16x32_bf16 a[36:39], a[196:199], v[20:23], a[36:39]\n"
"ds_read_b64_tr_b16 a[244:245], v243 offset:57856\n"
"v_mfma_f32_16x16x32_bf16 a[40:43], a[200:203], v[20:23], a[40:43]\n"
"v_exp_f32 v1, v1\n"
"v_mfma_f32_16x16x32_bf16 a[44:47], a[204:207], v[20:23], a[44:47]\n"
"ds_read_b64_tr_b16 a[246:247], v243 offset:61952\n"
"v_cvt_pk_bf16_f32 v16, v0, v1\n"
"v_mfma_f32_16x16x32_bf16 a[48:51], a[208:211], v[20:23], a[48:51]\n"
"v_exp_f32 v2, v2\n"
"v_mfma_f32_16x16x32_bf16 a[52:55], a[212:215], v[20:23], a[52:55]\n"
"ds_read_b64_tr_b16 a[248:249], v242 offset:58112\n"
"v_mfma_f32_16x16x32_bf16 a[56:59], a[216:219], v[20:23], a[56:59]\n"
"v_exp_f32 v3, v3\n"
"v_mfma_f32_16x16x32_bf16 a[60:63], a[220:223], v[20:23], a[60:63]\n"
"v_cvt_pk_bf16_f32 v17, v2, v3\n"
"v_mfma_f32_16x16x32_bf16 v[48:51], v[40:43], v[20:23], v[48:51]\n"
"v_exp_f32 v4, v4\n"
"v_mfma_f32_16x16x32_bf16 v[8:11], a[128:131], v[112:115], v[36:39]\n"
"ds_read_b64_tr_b16 a[250:251], v242 offset:62208\n"
"v_mfma_f32_16x16x32_bf16 v[8:11], a[132:135], v[116:119], v[8:11]\n"
"v_exp_f32 v5, v5\n"
"v_mfma_f32_16x16x32_bf16 v[8:11], a[136:139], v[120:123], v[8:11]\n"
"ds_read_b64_tr_b16 a[252:253], v243 offset:58112\n"
"v_cvt_pk_bf16_f32 v18, v4, v5\n"
"v_mfma_f32_16x16x32_bf16 v[8:11], a[140:143], v[124:127], v[8:11]\n"
"v_exp_f32 v6, v6\n"
"v_mfma_f32_16x16x32_bf16 v[12:15], a[144:147], v[112:115], v[36:39]\n"
"ds_read_b64_tr_b16 a[254:255], v243 offset:62208\n"
"v_mfma_f32_16x16x32_bf16 v[12:15], a[148:151], v[116:119], v[12:15]\n"
"v_exp_f32 v7, v7\n"
"v_mfma_f32_16x16x32_bf16 v[12:15], a[152:155], v[120:123], v[12:15]\n"
"v_cvt_pk_bf16_f32 v19, v6, v7\n"
"v_mfma_f32_16x16x32_bf16 v[12:15], a[156:159], v[124:127], v[12:15]\n"
"buffer_load_dwordx4 v247, s[12:15], s16 offen offset:3072 lds\n"
"v_mfma_f32_16x16x32_bf16 a[64:67], a[192:195], v[16:19], a[64:67]\n"
"s_add_u32 s16, s16, 16384\n"
"v_exp_f32 v8, v8\n"
"v_mfma_f32_16x16x32_bf16 a[68:71], a[196:199], v[16:19], a[68:71]\n"
"s_nop 0\n"
"v_mfma_f32_16x16x32_bf16 a[72:75], a[200:203], v[16:19], a[72:75]\n"
"v_exp_f32 v9, v9\n"
"v_mfma_f32_16x16x32_bf16 a[76:79], a[204:207], v[16:19], a[76:79]\n"
"v_cvt_pk_bf16_f32 v20, v8, v9\n"
"v_mfma_f32_16x16x32_bf16 a[80:83], a[208:211], v[16:19], a[80:83]\n"
"s_sub_u32 s19, s19, 1\n"
"v_exp_f32 v10, v10\n"
"v_mfma_f32_16x16x32_bf16 a[84:87], a[212:215], v[16:19], a[84:87]\n"
"s_cmp_lg_u32 s19, 0\n"
"v_mfma_f32_16x16x32_bf16 a[88:91], a[216:219], v[16:19], a[88:91]\n"
"v_exp_f32 v11, v11\n"
"v_mfma_f32_16x16x32_bf16 a[92:95], a[220:223], v[16:19], a[92:95]\n"
"s_waitcnt vmcnt(16) lgkmcnt(0)\n"
"s_barrier\n"
"v_cvt_pk_bf16_f32 v21, v10, v11\n"
"v_mfma_f32_16x16x32_bf16 v[52:55], v[40:43], v[16:19], v[52:55]\n"
"v_exp_f32 v12, v12\n"
"s_cbranch_scc1 AT_loop\n"
"v_lshlrev_b32 v214, 6, v248\n"
"v_lshrrev_b32 v215, 3, v248\n"
"v_mul_u32_u24 v215, 1152, v215\n"
"v_and_b32 v213, 7, v248\n"
"v_lshl_add_u32 v215, v213, 7, v215\n"
"s_and_b32 s46, s44, 7\n"
"s_lshl_b32 s46, s46, 2\n"
"s_add_u32 s46, s46, s41\n"
"s_mul_i32 s46, s46, 9216\n"
"s_getpc_b64 s[20:21]\n"
"AT_pc1:\n"
"s_sub_u32 s20, s20, AT_pc1-AT_start\n"
"s_subb_u32 s21, s21, 0\n"
"s_and_b32 s21, s21, 0xffff\n"
"s_mov_b32 s22, AT_kend-AT_start\n"
"s_mov_b32 s23, 0x20000\n"
"s_lshr_b32 s47, s42, 3\n"
"s_lshl_b32 s47, s47, 2\n"
"s_add_u32 s47, s47, s41\n"
"s_and_b32 s47, s47, 31\n"
"s_lshl_b32 s47, s47, 12\n"
"s_and_b32 s48, s42, 7\n"
"s_mul_i32 s48, s48, 576\n"
"s_lshr_b32 s62, s42, 3\n"
"s_mul_i32 s62, s62, 24\n"
"s_add_u32 s48, s48, s62\n"
"s_mov_b32 s24, s80\n"
"s_and_b32 s25, s81, 0xffff\n"
"s_mov_b32 s26, 5308416\n"
"s_mov_b32 s27, 0x20000\n"
"s_cmp_lt_u32 s42, 192\n"
"s_cselect_b32 s26, s26, 0\n"
"s_mov_b32 s64, s82\n"
"s_and_b32 s65, s83, 0xffff\n"
"s_mov_b32 s66, 5308416\n"
"s_mov_b32 s67, 0x20000\n"
"s_cmp_lt_u32 s42, 192\n"
"s_cselect_b32 s66, s66, 0\n"
"s_mov_b32 s68, s84\n"
"s_and_b32 s69, s85, 0xffff\n"
"s_mov_b32 s70, 5308416\n"
"s_mov_b32 s71, 0x20000\n"
"s_cmp_lt_u32 s42, 192\n"
"s_cselect_b32 s70, s70, 0\n"
"v_mul_u32_u24 v251, 9216, v248\n"
"s_mul_i32 s62, s41, 589824\n"
"s_lshl_b32 s72, s48, 1\n"
"s_add_u32 s72, s72, s62\n"
"s_add_u32 s73, s72, 2359296\n"
"s_add_u32 s74, s72, 4718592\n"
"s_mul_i32 s50, s48, 1152\n"
"s_lshl_b32 s62, s41, 13\n"
"s_add_u32 s75, s50, s62\n"
"s_add_u32 s76, s75, 4096\n"
"s_lshl_b32 s62, s41, 3\n"
"s_add_u32 s49, s48, s62\n"
"s_lshl_b32 s49, s49, 1\n"
"s_cmp_lt_u32 s41, 3\n"
"s_cselect_b32 s62, 0, 0x40000000\n"
"v_or_b32 v252, s62, v251\n"
"s_lshl_b32 s62, s41, 6\n"
"v_add_u32 v253, s62, v248\n"
"v_mov_b32 v254, 0x40000000\n"
"v_cmp_gt_u32 vcc, 144, v253\n"
"v_lshlrev_b32 v253, 3, v253\n"
"s_nop 0\n"
"v_cndmask_b32 v253, v254, v253, vcc\n"
"s_or_b32 s56, s42, s41\n"
"s_cmp_lg_u32 s56, 0\n"
"s_cbranch_scc1 AT_h1\n"
"v_lshlrev_b32 v212, 7, v248\n"
"s_mov_b32 s57, 32768\n"
"s_mov_b32 s58, 40960\n"
"s_mov_b32 s59, 49152\n"
"s_mov_b32 s60, 57344\n"
"buffer_load_dword v208, v212, s[36:39], s57 offen offset:4 sc1\n"
"buffer_load_dword v209, v212, s[36:39], s58 offen offset:4 sc1\n"
"buffer_load_dword v210, v212, s[36:39], s59 offen offset:4 sc1\n"
"buffer_load_dword v211, v212, s[36:39], s60 offen offset:4 sc1\n"
"AT_h1:\n"
"v_mfma_f32_16x16x32_bf16 v[0:3], a[160:163], v[64:67], v[24:27]\n"
"ds_read_b128 a[128:131], v241 offset:0\n"
"v_mfma_f32_16x16x32_bf16 v[0:3], a[164:167], v[68:71], v[0:3]\n"
"v_exp_f32 v13, v13\n"
"v_mfma_f32_16x16x32_bf16 v[0:3], a[168:171], v[72:75], v[0:3]\n"
"ds_read_b128 a[132:135], v241 offset:256\n"
"v_cvt_pk_bf16_f32 v22, v12, v13\n"
"v_mfma_f32_16x16x32_bf16 v[0:3], a[172:175], v[76:79], v[0:3]\n"
"v_exp_f32 v14, v14\n"
"v_mfma_f32_16x16x32_bf16 v[4:7], a[176:179], v[64:67], v[24:27]\n"
"ds_read_b128 a[136:139], v241 offset:512\n"
"v_mfma_f32_16x16x32_bf16 v[4:7], a[180:183], v[68:71], v[4:7]\n"
"v_exp_f32 v15, v15\n"
"v_mfma_f32_16x16x32_bf16 v[4:7], a[184:187], v[72:75], v[4:7]\n"
"s_add_u32 m0, s18, 32768\n"
"ds_read_b128 a[140:143], v241 offset:768\n"
"v_cvt_pk_bf16_f32 v23, v14, v15\n"
"v_mfma_f32_16x16x32_bf16 v[4:7], a[188:191], v[76:79], v[4:7]\n"
"buffer_load_dwordx4 v246, s[8:11], s16 offen offset:0 lds\n"
"v_mfma_f32_16x16x32_bf16 a[96:99], a[192:195], v[20:23], a[96:99]\n"
"v_exp_f32 v0, v0\n"
"v_mfma_f32_16x16x32_bf16 a[100:103], a[196:199], v[20:23], a[100:103]\n"
"ds_read_b128 a[144:147], v241 offset:4096\n"
"v_mfma_f32_16x16x32_bf16 a[104:107], a[200:203], v[20:23], a[104:107]\n"
"v_exp_f32 v1, v1\n"
"v_mfma_f32_16x16x32_bf16 a[108:111], a[204:207], v[20:23], a[108:111]\n"
"ds_read_b128 a[148:151], v241 offset:4352\n"
"v_cvt_pk_bf16_f32 v16, v0, v1\n"
"v_mfma_f32_16x16x32_bf16 a[112:115], a[208:211], v[20:23], a[112:115]\n"
"v_exp_f32 v2, v2\n"
"v_mfma_f32_16x16x32_bf16 a[116:119], a[212:215], v[20:23], a[116:119]\n"
"ds_read_b128 a[152:155], v241 offset:4608\n"
"v_mfma_f32_16x16x32_bf16 a[120:123], a[216:219], v[20:23], a[120:123]\n"
"v_exp_f32 v3, v3\n"
"v_mfma_f32_16x16x32_bf16 a[124:127], a[220:223], v[20:23], a[124:127]\n"
"v_cvt_pk_bf16_f32 v17, v2, v3\n"
"v_mfma_f32_16x16x32_bf16 v[56:59], v[40:43], v[20:23], v[56:59]\n"
"v_exp_f32 v4, v4\n"
"v_mfma_f32_16x16x32_bf16 v[8:11], a[160:163], v[80:83], v[28:31]\n"
"ds_read_b128 a[156:159], v241 offset:4864\n"
"v_mfma_f32_16x16x32_bf16 v[8:11], a[164:167], v[84:87], v[8:11]\n"
"v_exp_f32 v5, v5\n"
"v_mfma_f32_16x16x32_bf16 v[8:11], a[168:171], v[88:91], v[8:11]\n"
"ds_read_b64_tr_b16 a[192:193], v244 offset:16384\n"
"v_cvt_pk_bf16_f32 v18, v4, v5\n"
"v_mfma_f32_16x16x32_bf16 v[8:11], a[172:175], v[92:95], v[8:11]\n"
"v_exp_f32 v6, v6\n"
"v_mfma_f32_16x16x32_bf16 v[12:15], a[176:179], v[80:83], v[28:31]\n"
"ds_read_b64_tr_b16 a[194:195], v244 offset:20480\n"
"v_mfma_f32_16x16x32_bf16 v[12:15], a[180:183], v[84:87], v[12:15]\n"
"v_exp_f32 v7, v7\n"
"v_mfma_f32_16x16x32_bf16 v[12:15], a[184:187], v[88:91], v[12:15]\n"
"ds_read_b64_tr_b16 a[196:197], v245 offset:16384\n"
"v_cvt_pk_bf16_f32 v19, v6, v7\n"
"v_mfma_f32_16x16x32_bf16 v[12:15], a[188:191], v[92:95], v[12:15]\n"
"s_waitcnt lgkmcnt(11)\n"
"buffer_load_dwordx4 v246, s[8:11], s16 offen offset:1024 lds\n"
"v_mfma_f32_16x16x32_bf16 a[0:3], a[224:227], v[16:19], a[0:3]\n"
"v_exp_f32 v8, v8\n"
"v_mfma_f32_16x16x32_bf16 a[4:7], a[228:231], v[16:19], a[4:7]\n"
"ds_read_b64_tr_b16 a[198:199], v245 offset:20480\n"
"v_mfma_f32_16x16x32_bf16 a[8:11], a[232:235], v[16:19], a[8:11]\n"
"v_exp_f32 v9, v9\n"
"v_mfma_f32_16x16x32_bf16 a[12:15], a[236:239], v[16:19], a[12:15]\n"
"ds_read_b64_tr_b16 a[200:201], v244 offset:16640\n"
"v_cvt_pk_bf16_f32 v20, v8, v9\n"
"v_mfma_f32_16x16x32_bf16 a[16:19], a[240:243], v[16:19], a[16:19]\n"
"v_exp_f32 v10, v10\n"
"v_mfma_f32_16x16x32_bf16 a[20:23], a[244:247], v[16:19], a[20:23]\n"
"ds_read_b64_tr_b16 a[202:203], v244 offset:20736\n"
"v_mfma_f32_16x16x32_bf16 a[24:27], a[248:251], v[16:19], a[24:27]\n"
"v_exp_f32 v11, v11\n"
"v_mfma_f32_16x16x32_bf16 a[28:31], a[252:255], v[16:19], a[28:31]\n"
"v_cvt_pk_bf16_f32 v21, v10, v11\n"
"v_mfma_f32_16x16x32_bf16 v[44:47], v[40:43], v[16:19], v[44:47]\n"
"v_exp_f32 v12, v12\n"
"v_mfma_f32_16x16x32_bf16 v[0:3], a[160:163], v[96:99], v[32:35]\n"
"ds_read_b64_tr_b16 a[204:205], v245 offset:16640\n"
"v_mfma_f32_16x16x32_bf16 v[0:3], a[164:167], v[100:103], v[0:3]\n"
"v_exp_f32 v13, v13\n"
"v_mfma_f32_16x16x32_bf16 v[0:3], a[168:171], v[104:107], v[0:3]\n"
"ds_read_b64_tr_b16 a[206:207], v245 offset:20736\n"
"v_cvt_pk_bf16_f32 v22, v12, v13\n"
"v_mfma_f32_16x16x32_bf16 v[0:3], a[172:175], v[108:111], v[0:3]\n"
"v_exp_f32 v14, v14\n"
"v_mfma_f32_16x16x32_bf16 v[4:7], a[176:179], v[96:99], v[32:35]\n"
"ds_read_b64_tr_b16 a[208:209], v244 offset:16896\n"
"v_mfma_f32_16x16x32_bf16 v[4:7], a[180:183], v[100:103], v[4:7]\n"
"v_exp_f32 v15, v15\n"
"v_mfma_f32_16x16x32_bf16 v[4:7], a[184:187], v[104:107], v[4:7]\n"
"ds_read_b64_tr_b16 a[210:211], v244 offset:20992\n"
"v_cvt_pk_bf16_f32 v23, v14, v15\n"
"v_mfma_f32_16x16x32_bf16 v[4:7], a[188:191], v[108:111], v[4:7]\n"
"buffer_load_dwordx4 v247, s[8:11], s16 offen offset:2048 lds\n"
"v_mfma_f32_16x16x32_bf16 a[32:35], a[224:227], v[20:23], a[32:35]\n"
"v_exp_f32 v0, v0\n"
"v_mfma_f32_16x16x32_bf16 a[36:39], a[228:231], v[20:23], a[36:39]\n"
"ds_read_b64_tr_b16 a[212:213], v245 offset:16896\n"
"v_mfma_f32_16x16x32_bf16 a[40:43], a[232:235], v[20:23], a[40:43]\n"
"v_exp_f32 v1, v1\n"
"v_mfma_f32_16x16x32_bf16 a[44:47], a[236:239], v[20:23], a[44:47]\n"
"ds_read_b64_tr_b16 a[214:215], v245 offset:20992\n"
"v_cvt_pk_bf16_f32 v16, v0, v1\n"
"v_mfma_f32_16x16x32_bf16 a[48:51], a[240:243], v[20:23], a[48:51]\n"
"v_exp_f32 v2, v2\n"
"v_mfma_f32_16x16x32_bf16 a[52:55], a[244:247], v[20:23], a[52:55]\n"
"ds_read_b64_tr_b16 a[216:217], v244 offset:17152\n"
"v_mfma_f32_16x16x32_bf16 a[56:59], a[248:251], v[20:23], a[56:59]\n"
"v_exp_f32 v3, v3\n"
"v_mfma_f32_16x16x32_bf16 a[60:63], a[252:255], v[20:23], a[60:63]\n"
"v_cvt_pk_bf16_f32 v17, v2, v3\n"
"v_mfma_f32_16x16x32_bf16 v[48:51], v[40:43], v[20:23], v[48:51]\n"
"v_exp_f32 v4, v4\n"
"v_mfma_f32_16x16x32_bf16 v[8:11], a[160:163], v[112:115], v[36:39]\n"
"ds_read_b64_tr_b16 a[218:219], v244 offset:21248\n"
"v_mfma_f32_16x16x32_bf16 v[8:11], a[164:167], v[116:119], v[8:11]\n"
"v_exp_f32 v5, v5\n"
"v_mfma_f32_16x16x32_bf16 v[8:11], a[168:171], v[120:123], v[8:11]\n"
"ds_read_b64_tr_b16 a[220:221], v245 offset:17152\n"
"v_cvt_pk_bf16_f32 v18, v4, v5\n"
"v_mfma_f32_16x16x32_bf16 v[8:11], a[172:175], v[124:127], v[8:11]\n"
"v_exp_f32 v6, v6\n"
"v_mfma_f32_16x16x32_bf16 v[12:15], a[176:179], v[112:115], v[36:39]\n"
"ds_read_b64_tr_b16 a[222:223], v245 offset:21248\n"
"v_mfma_f32_16x16x32_bf16 v[12:15], a[180:183], v[116:119], v[12:15]\n"
"v_exp_f32 v7, v7\n"
"v_mfma_f32_16x16x32_bf16 v[12:15], a[184:187], v[120:123], v[12:15]\n"
"v_cvt_pk_bf16_f32 v19, v6, v7\n"
"v_mfma_f32_16x16x32_bf16 v[12:15], a[188:191], v[124:127], v[12:15]\n"
"buffer_load_dwordx4 v247, s[8:11], s16 offen offset:3072 lds\n"
"v_mfma_f32_16x16x32_bf16 a[64:67], a[224:227], v[16:19], a[64:67]\n"
"v_exp_f32 v8, v8\n"
"v_mfma_f32_16x16x32_bf16 a[68:71], a[228:231], v[16:19], a[68:71]\n"
"buffer_load_dword v250, v215, s[28:31], s46 offen offset:128\n"
"v_mfma_f32_16x16x32_bf16 a[72:75], a[232:235], v[16:19], a[72:75]\n"
"v_exp_f32 v9, v9\n"
"v_mfma_f32_16x16x32_bf16 a[76:79], a[236:239], v[16:19], a[76:79]\n"
"v_cvt_pk_bf16_f32 v20, v8, v9\n"
"v_mfma_f32_16x16x32_bf16 a[80:83], a[240:243], v[16:19], a[80:83]\n"
"v_exp_f32 v10, v10\n"
"v_mfma_f32_16x16x32_bf16 a[84:87], a[244:247], v[16:19], a[84:87]\n"
"s_nop 0\n"
"v_mfma_f32_16x16x32_bf16 a[88:91], a[248:251], v[16:19], a[88:91]\n"
"v_exp_f32 v11, v11\n"
"v_mfma_f32_16x16x32_bf16 a[92:95], a[252:255], v[16:19], a[92:95]\n"
"s_waitcnt lgkmcnt(8)\n"
"v_cvt_pk_bf16_f32 v21, v10, v11\n"
"v_mfma_f32_16x16x32_bf16 v[52:55], v[40:43], v[16:19], v[52:55]\n"
"v_exp_f32 v12, v12\n"
"v_mfma_f32_16x16x32_bf16 v[0:3], a[128:131], v[64:67], v[24:27]\n"
"ds_read_b128 a[160:163], v241 offset:8192\n"
"v_mfma_f32_16x16x32_bf16 v[0:3], a[132:135], v[68:71], v[0:3]\n"
"v_exp_f32 v13, v13\n"
"v_mfma_f32_16x16x32_bf16 v[0:3], a[136:139], v[72:75], v[0:3]\n"
"ds_read_b128 a[164:167], v241 offset:8448\n"
"v_cvt_pk_bf16_f32 v22, v12, v13\n"
"v_mfma_f32_16x16x32_bf16 v[0:3], a[140:143], v[76:79], v[0:3]\n"
"v_exp_f32 v14, v14\n"
"v_mfma_f32_16x16x32_bf16 v[4:7], a[144:147], v[64:67], v[24:27]\n"
"ds_read_b128 a[168:171], v241 offset:8704\n"
"v_mfma_f32_16x16x32_bf16 v[4:7], a[148:151], v[68:71], v[4:7]\n"
"v_exp_f32 v15, v15\n"
"v_mfma_f32_16x16x32_bf16 v[4:7], a[152:155], v[72:75], v[4:7]\n"
"s_add_u32 m0, s18, 49152\n"
"ds_read_b128 a[172:175], v241 offset:8960\n"
"v_cvt_pk_bf16_f32 v23, v14, v15\n"
"v_mfma_f32_16x16x32_bf16 v[4:7], a[156:159], v[76:79], v[4:7]\n"
"buffer_load_dwordx4 v246, s[12:15], s16 offen offset:0 lds\n"
"v_mfma_f32_16x16x32_bf16 a[96:99], a[224:227], v[20:23], a[96:99]\n"
"v_exp_f32 v0, v0\n"
"v_mfma_f32_16x16x32_bf16 a[100:103], a[228:231], v[20:23], a[100:103]\n"
"ds_read_b128 a[176:179], v241 offset:12288\n"
"v_mfma_f32_16x16x32_bf16 a[104:107], a[232:235], v[20:23], a[104:107]\n"
"v_exp_f32 v1, v1\n"
"v_mfma_f32_16x16x32_bf16 a[108:111], a[236:239], v[20:23], a[108:111]\n"
"ds_read_b128 a[180:183], v241 offset:12544\n"
"v_cvt_pk_bf16_f32 v16, v0, v1\n"
"v_mfma_f32_16x16x32_bf16 a[112:115], a[240:243], v[20:23], a[112:115]\n"
"v_exp_f32 v2, v2\n"
"v_mfma_f32_16x16x32_bf16 a[116:119], a[244:247], v[20:23], a[116:119]\n"
"ds_read_b128 a[184:187], v241 offset:12800\n"
"v_mfma_f32_16x16x32_bf16 a[120:123], a[248:251], v[20:23], a[120:123]\n"
"v_exp_f32 v3, v3\n"
"v_mfma_f32_16x16x32_bf16 a[124:127], a[252:255], v[20:23], a[124:127]\n"
"v_cvt_pk_bf16_f32 v17, v2, v3\n"
"v_mfma_f32_16x16x32_bf16 v[56:59], v[40:43], v[20:23], v[56:59]\n"
"v_exp_f32 v4, v4\n"
"v_mfma_f32_16x16x32_bf16 v[8:11], a[128:131], v[80:83], v[28:31]\n"
"ds_read_b128 a[188:191], v241 offset:13056\n"
"v_mfma_f32_16x16x32_bf16 v[8:11], a[132:135], v[84:87], v[8:11]\n"
"v_exp_f32 v5, v5\n"
"v_mfma_f32_16x16x32_bf16 v[8:11], a[136:139], v[88:91], v[8:11]\n"
"ds_read_b64_tr_b16 a[224:225], v244 offset:24576\n"
"v_cvt_pk_bf16_f32 v18, v4, v5\n"
"v_mfma_f32_16x16x32_bf16 v[8:11], a[140:143], v[92:95], v[8:11]\n"
"v_exp_f32 v6, v6\n"
"v_mfma_f32_16x16x32_bf16 v[12:15], a[144:147], v[80:83], v[28:31]\n"
"ds_read_b64_tr_b16 a[226:227], v244 offset:28672\n"
"v_mfma_f32_16x16x32_bf16 v[12:15], a[148:151], v[84:87], v[12:15]\n"
"v_exp_f32 v7, v7\n"
"v_mfma_f32_16x16x32_bf16 v[12:15], a[152:155], v[88:91], v[12:15]\n"
"ds_read_b64_tr_b16 a[228:229], v245 offset:24576\n"
"v_cvt_pk_bf16_f32 v19, v6, v7\n"
"v_mfma_f32_16x16x32_bf16 v[12:15], a[156:159], v[92:95], v[12:15]\n"
"s_waitcnt lgkmcnt(11)\n"
"buffer_load_dwordx4 v247, s[12:15], s16 offen offset:1024 lds\n"
"v_mfma_f32_16x16x32_bf16 a[0:3], a[192:195], v[16:19], a[0:3]\n"
"v_exp_f32 v8, v8\n"
"v_mfma_f32_16x16x32_bf16 a[4:7], a[196:199], v[16:19], a[4:7]\n"
"ds_read_b64_tr_b16 a[230:231], v245 offset:28672\n"
"v_mfma_f32_16x16x32_bf16 a[8:11], a[200:203], v[16:19], a[8:11]\n"
"v_exp_f32 v9, v9\n"
"v_mfma_f32_16x16x32_bf16 a[12:15], a[204:207], v[16:19], a[12:15]\n"
"ds_read_b64_tr_b16 a[232:233], v244 offset:24832\n"
"v_cvt_pk_bf16_f32 v20, v8, v9\n"
"v_mfma_f32_16x16x32_bf16 a[16:19], a[208:211], v[16:19], a[16:19]\n"
"v_exp_f32 v10, v10\n"
"v_mfma_f32_16x16x32_bf16 a[20:23], a[212:215], v[16:19], a[20:23]\n"
"ds_read_b64_tr_b16 a[234:235], v244 offset:28928\n"
"v_mfma_f32_16x16x32_bf16 a[24:27], a[216:219], v[16:19], a[24:27]\n"
"v_exp_f32 v11, v11\n"
"v_mfma_f32_16x16x32_bf16 a[28:31], a[220:223], v[16:19], a[28:31]\n"
"v_cvt_pk_bf16_f32 v21, v10, v11\n"
"v_mfma_f32_16x16x32_bf16 v[44:47], v[40:43], v[16:19], v[44:47]\n"
"v_exp_f32 v12, v12\n"
"v_mfma_f32_16x16x32_bf16 v[0:3], a[128:131], v[96:99], v[32:35]\n"
"ds_read_b64_tr_b16 a[236:237], v245 offset:24832\n"
"v_mfma_f32_16x16x32_bf16 v[0:3], a[132:135], v[100:103], v[0:3]\n"
"v_exp_f32 v13, v13\n"
"v_mfma_f32_16x16x32_bf16 v[0:3], a[136:139], v[104:107], v[0:3]\n"
"ds_read_b64_tr_b16 a[238:239], v245 offset:28928\n"
"v_cvt_pk_bf16_f32 v22, v12, v13\n"
"v_mfma_f32_16x16x32_bf16 v[0:3], a[140:143], v[108:111], v[0:3]\n"
"v_exp_f32 v14, v14\n"
"v_mfma_f32_16x16x32_bf16 v[4:7], a[144:147], v[96:99], v[32:35]\n"
"ds_read_b64_tr_b16 a[240:241], v244 offset:25088\n"
"v_mfma_f32_16x16x32_bf16 v[4:7], a[148:151], v[100:103], v[4:7]\n"
"v_exp_f32 v15, v15\n"
"v_mfma_f32_16x16x32_bf16 v[4:7], a[152:155], v[104:107], v[4:7]\n"
"ds_read_b64_tr_b16 a[242:243], v244 offset:29184\n"
"v_cvt_pk_bf16_f32 v23, v14, v15\n"
"v_mfma_f32_16x16x32_bf16 v[4:7], a[156:159], v[108:111], v[4:7]\n"
"buffer_load_dwordx4 v246, s[12:15], s16 offen offset:2048 lds\n"
"v_mfma_f32_16x16x32_bf16 a[32:35], a[192:195], v[20:23], a[32:35]\n"
"v_exp_f32 v0, v0\n"
"v_mfma_f32_16x16x32_bf16 a[36:39], a[196:199], v[20:23], a[36:39]\n"
"ds_read_b64_tr_b16 a[244:245], v245 offset:25088\n"
"v_mfma_f32_16x16x32_bf16 a[40:43], a[200:203], v[20:23], a[40:43]\n"
"v_exp_f32 v1, v1\n"
"v_mfma_f32_16x16x32_bf16 a[44:47], a[204:207], v[20:23], a[44:47]\n"
"ds_read_b64_tr_b16 a[246:247], v245 offset:29184\n"
"v_cvt_pk_bf16_f32 v16, v0, v1\n"
"v_mfma_f32_16x16x32_bf16 a[48:51], a[208:211], v[20:23], a[48:51]\n"
"v_exp_f32 v2, v2\n"
"v_mfma_f32_16x16x32_bf16 a[52:55], a[212:215], v[20:23], a[52:55]\n"
"ds_read_b64_tr_b16 a[248:249], v244 offset:25344\n"
"v_mfma_f32_16x16x32_bf16 a[56:59], a[216:219], v[20:23], a[56:59]\n"
"v_exp_f32 v3, v3\n"
"v_mfma_f32_16x16x32_bf16 a[60:63], a[220:223], v[20:23], a[60:63]\n"
"v_cvt_pk_bf16_f32 v17, v2, v3\n"
"v_mfma_f32_16x16x32_bf16 v[48:51], v[40:43], v[20:23], v[48:51]\n"
"v_exp_f32 v4, v4\n"
"v_mfma_f32_16x16x32_bf16 v[8:11], a[128:131], v[112:115], v[36:39]\n"
"ds_read_b64_tr_b16 a[250:251], v244 offset:29440\n"
"v_mfma_f32_16x16x32_bf16 v[8:11], a[132:135], v[116:119], v[8:11]\n"
"v_exp_f32 v5, v5\n"
"v_mfma_f32_16x16x32_bf16 v[8:11], a[136:139], v[120:123], v[8:11]\n"
"ds_read_b64_tr_b16 a[252:253], v245 offset:25344\n"
"v_cvt_pk_bf16_f32 v18, v4, v5\n"
"v_mfma_f32_16x16x32_bf16 v[8:11], a[140:143], v[124:127], v[8:11]\n"
"v_exp_f32 v6, v6\n"
"v_mfma_f32_16x16x32_bf16 v[12:15], a[144:147], v[112:115], v[36:39]\n"
"ds_read_b64_tr_b16 a[254:255], v245 offset:29440\n"
"v_mfma_f32_16x16x32_bf16 v[12:15], a[148:151], v[116:119], v[12:15]\n"
"v_exp_f32 v7, v7\n"
"v_mfma_f32_16x16x32_bf16 v[12:15], a[152:155], v[120:123], v[12:15]\n"
"v_cvt_pk_bf16_f32 v19, v6, v7\n"
"v_mfma_f32_16x16x32_bf16 v[12:15], a[156:159], v[124:127], v[12:15]\n"
"buffer_load_dwordx4 v247, s[12:15], s16 offen offset:3072 lds\n"
"v_mfma_f32_16x16x32_bf16 a[64:67], a[192:195], v[16:19], a[64:67]\n"
"s_add_u32 s16, s16, 16384\n"
"v_exp_f32 v8, v8\n"
"v_mfma_f32_16x16x32_bf16 a[68:71], a[196:199], v[16:19], a[68:71]\n"
"buffer_load_dword v250, v215, s[28:31], s46 offen offset:192\n"
"v_mfma_f32_16x16x32_bf16 a[72:75], a[200:203], v[16:19], a[72:75]\n"
"v_exp_f32 v9, v9\n"
"v_mfma_f32_16x16x32_bf16 a[76:79], a[204:207], v[16:19], a[76:79]\n"
"v_cvt_pk_bf16_f32 v20, v8, v9\n"
"v_mfma_f32_16x16x32_bf16 a[80:83], a[208:211], v[16:19], a[80:83]\n"
"v_exp_f32 v10, v10\n"
"v_mfma_f32_16x16x32_bf16 a[84:87], a[212:215], v[16:19], a[84:87]\n"
"s_nop 0\n"
"v_mfma_f32_16x16x32_bf16 a[88:91], a[216:219], v[16:19], a[88:91]\n"
"v_exp_f32 v11, v11\n"
"v_mfma_f32_16x16x32_bf16 a[92:95], a[220:223], v[16:19], a[92:95]\n"
"s_waitcnt vmcnt(18) lgkmcnt(0)\n"
"s_barrier\n"
"v_cvt_pk_bf16_f32 v21, v10, v11\n"
"v_mfma_f32_16x16x32_bf16 v[52:55], v[40:43], v[16:19], v[52:55]\n"
"v_exp_f32 v12, v12\n"
"v_mfma_f32_16x16x32_bf16 v[0:3], a[160:163], v[64:67], v[24:27]\n"
"ds_read_b128 a[128:131], v241 offset:32768\n"
"v_mfma_f32_16x16x32_bf16 v[0:3], a[164:167], v[68:71], v[0:3]\n"
"v_exp_f32 v13, v13\n"
"v_mfma_f32_16x16x32_bf16 v[0:3], a[168:171], v[72:75], v[0:3]\n"
"ds_read_b128 a[132:135], v241 offset:33024\n"
"v_cvt_pk_bf16_f32 v22, v12, v13\n"
"v_mfma_f32_16x16x32_bf16 v[0:3], a[172:175], v[76:79], v[0:3]\n"
"v_exp_f32 v14, v14\n"
"v_mfma_f32_16x16x32_bf16 v[4:7], a[176:179], v[64:67], v[24:27]\n"
"ds_read_b128 a[136:139], v241 offset:33280\n"
"v_mfma_f32_16x16x32_bf16 v[4:7], a[180:183], v[68:71], v[4:7]\n"
"v_exp_f32 v15, v15\n"
"v_mfma_f32_16x16x32_bf16 v[4:7], a[184:187], v[72:75], v[4:7]\n"
"s_add_u32 m0, s18, 65536\n"
"ds_read_b128 a[140:143], v241 offset:33536\n"
"v_cvt_pk_bf16_f32 v23, v14, v15\n"
"v_mfma_f32_16x16x32_bf16 v[4:7], a[188:191], v[76:79], v[4:7]\n"
"buffer_load_dwordx4 v246, s[8:11], s16 offen offset:0 lds\n"
"v_mfma_f32_16x16x32_bf16 a[96:99], a[192:195], v[20:23], a[96:99]\n"
"v_exp_f32 v0, v0\n"
"v_mfma_f32_16x16x32_bf16 a[100:103], a[196:199], v[20:23], a[100:103]\n"
"ds_read_b128 a[144:147], v241 offset:36864\n"
"v_mfma_f32_16x16x32_bf16 a[104:107], a[200:203], v[20:23], a[104:107]\n"
"v_exp_f32 v1, v1\n"
"v_mfma_f32_16x16x32_bf16 a[108:111], a[204:207], v[20:23], a[108:111]\n"
"ds_read_b128 a[148:151], v241 offset:37120\n"
"v_cvt_pk_bf16_f32 v16, v0, v1\n"
"v_mfma_f32_16x16x32_bf16 a[112:115], a[208:211], v[20:23], a[112:115]\n"
"v_exp_f32 v2, v2\n"
"v_mfma_f32_16x16x32_bf16 a[116:119], a[212:215], v[20:23], a[116:119]\n"
"ds_read_b128 a[152:155], v241 offset:37376\n"
"v_mfma_f32_16x16x32_bf16 a[120:123], a[216:219], v[20:23], a[120:123]\n"
"v_exp_f32 v3, v3\n"
"v_mfma_f32_16x16x32_bf16 a[124:127], a[220:223], v[20:23], a[124:127]\n"
"v_cvt_pk_bf16_f32 v17, v2, v3\n"
"v_mfma_f32_16x16x32_bf16 v[56:59], v[40:43], v[20:23], v[56:59]\n"
"v_exp_f32 v4, v4\n"
"v_mfma_f32_16x16x32_bf16 v[8:11], a[160:163], v[80:83], v[28:31]\n"
"ds_read_b128 a[156:159], v241 offset:37632\n"
"v_mfma_f32_16x16x32_bf16 v[8:11], a[164:167], v[84:87], v[8:11]\n"
"v_exp_f32 v5, v5\n"
"v_mfma_f32_16x16x32_bf16 v[8:11], a[168:171], v[88:91], v[8:11]\n"
"ds_read_b64_tr_b16 a[192:193], v244 offset:49152\n"
"v_cvt_pk_bf16_f32 v18, v4, v5\n"
"v_mfma_f32_16x16x32_bf16 v[8:11], a[172:175], v[92:95], v[8:11]\n"
"v_exp_f32 v6, v6\n"
"v_mfma_f32_16x16x32_bf16 v[12:15], a[176:179], v[80:83], v[28:31]\n"
"ds_read_b64_tr_b16 a[194:195], v244 offset:53248\n"
"v_mfma_f32_16x16x32_bf16 v[12:15], a[180:183], v[84:87], v[12:15]\n"
"v_exp_f32 v7, v7\n"
"v_mfma_f32_16x16x32_bf16 v[12:15], a[184:187], v[88:91], v[12:15]\n"
"ds_read_b64_tr_b16 a[196:197], v245 offset:49152\n"
"v_cvt_pk_bf16_f32 v19, v6, v7\n"
"v_mfma_f32_16x16x32_bf16 v[12:15], a[188:191], v[92:95], v[12:15]\n"
"s_waitcnt lgkmcnt(11)\n"
"buffer_load_dwordx4 v246, s[8:11], s16 offen offset:1024 lds\n"
"v_mfma_f32_16x16x32_bf16 a[0:3], a[224:227], v[16:19], a[0:3]\n"
"v_exp_f32 v8, v8\n"
"v_mfma_f32_16x16x32_bf16 a[4:7], a[228:231], v[16:19], a[4:7]\n"
"ds_read_b64_tr_b16 a[198:199], v245 offset:53248\n"
"v_mfma_f32_16x16x32_bf16 a[8:11], a[232:235], v[16:19], a[8:11]\n"
"v_exp_f32 v9, v9\n"
"v_mfma_f32_16x16x32_bf16 a[12:15], a[236:239], v[16:19], a[12:15]\n"
"ds_read_b64_tr_b16 a[200:201], v244 offset:49408\n"
"v_cvt_pk_bf16_f32 v20, v8, v9\n"
"v_mfma_f32_16x16x32_bf16 a[16:19], a[240:243], v[16:19], a[16:19]\n"
"v_exp_f32 v10, v10\n"
"v_mfma_f32_16x16x32_bf16 a[20:23], a[244:247], v[16:19], a[20:23]\n"
"ds_read_b64_tr_b16 a[202:203], v244 offset:53504\n"
"v_mfma_f32_16x16x32_bf16 a[24:27], a[248:251], v[16:19], a[24:27]\n"
"v_exp_f32 v11, v11\n"
"v_mfma_f32_16x16x32_bf16 a[28:31], a[252:255], v[16:19], a[28:31]\n"
"v_cvt_pk_bf16_f32 v21, v10, v11\n"
"v_mfma_f32_16x16x32_bf16 v[44:47], v[40:43], v[16:19], v[44:47]\n"
"v_exp_f32 v12, v12\n"
"v_mfma_f32_16x16x32_bf16 v[0:3], a[160:163], v[96:99], v[32:35]\n"
"ds_read_b64_tr_b16 a[204:205], v245 offset:49408\n"
"v_mfma_f32_16x16x32_bf16 v[0:3], a[164:167], v[100:103], v[0:3]\n"
"v_exp_f32 v13, v13\n"
"v_mfma_f32_16x16x32_bf16 v[0:3], a[168:171], v[104:107], v[0:3]\n"
"ds_read_b64_tr_b16 a[206:207], v245 offset:53504\n"
"v_cvt_pk_bf16_f32 v22, v12, v13\n"
"v_mfma_f32_16x16x32_bf16 v[0:3], a[172:175], v[108:111], v[0:3]\n"
"v_exp_f32 v14, v14\n"
"v_mfma_f32_16x16x32_bf16 v[4:7], a[176:179], v[96:99], v[32:35]\n"
"ds_read_b64_tr_b16 a[208:209], v244 offset:49664\n"
"v_mfma_f32_16x16x32_bf16 v[4:7], a[180:183], v[100:103], v[4:7]\n"
"v_exp_f32 v15, v15\n"
"v_mfma_f32_16x16x32_bf16 v[4:7], a[184:187], v[104:107], v[4:7]\n"
"ds_read_b64_tr_b16 a[210:211], v244 offset:53760\n"
"v_cvt_pk_bf16_f32 v23, v14, v15\n"
"v_mfma_f32_16x16x32_bf16 v[4:7], a[188:191], v[108:111], v[4:7]\n"
"buffer_load_dwordx4 v247, s[8:11], s16 offen offset:2048 lds\n"
"v_mfma_f32_16x16x32_bf16 a[32:35], a[224:227], v[20:23], a[32:35]\n"
"v_exp_f32 v0, v0\n"
"v_mfma_f32_16x16x32_bf16 a[36:39], a[228:231], v[20:23], a[36:39]\n"
"ds_read_b64_tr_b16 a[212:213], v245 offset:49664\n"
"v_mfma_f32_16x16x32_bf16 a[40:43], a[232:235], v[20:23], a[40:43]\n"
"v_exp_f32 v1, v1\n"
"v_mfma_f32_16x16x32_bf16 a[44:47], a[236:239], v[20:23], a[44:47]\n"
"ds_read_b64_tr_b16 a[214:215], v245 offset:53760\n"
"v_cvt_pk_bf16_f32 v16, v0, v1\n"
"v_mfma_f32_16x16x32_bf16 a[48:51], a[240:243], v[20:23], a[48:51]\n"
"v_exp_f32 v2, v2\n"
"v_mfma_f32_16x16x32_bf16 a[52:55], a[244:247], v[20:23], a[52:55]\n"
"ds_read_b64_tr_b16 a[216:217], v244 offset:49920\n"
"v_mfma_f32_16x16x32_bf16 a[56:59], a[248:251], v[20:23], a[56:59]\n"
"v_exp_f32 v3, v3\n"
"v_mfma_f32_16x16x32_bf16 a[60:63], a[252:255], v[20:23], a[60:63]\n"
"v_cvt_pk_bf16_f32 v17, v2, v3\n"
"v_mfma_f32_16x16x32_bf16 v[48:51], v[40:43], v[20:23], v[48:51]\n"
"v_exp_f32 v4, v4\n"
"v_mfma_f32_16x16x32_bf16 v[8:11], a[160:163], v[112:115], v[36:39]\n"
"ds_read_b64_tr_b16 a[218:219], v244 offset:54016\n"
"v_mfma_f32_16x16x32_bf16 v[8:11], a[164:167], v[116:119], v[8:11]\n"
"v_exp_f32 v5, v5\n"
"v_mfma_f32_16x16x32_bf16 v[8:11], a[168:171], v[120:123], v[8:11]\n"
"ds_read_b64_tr_b16 a[220:221], v245 offset:49920\n"
"v_cvt_pk_bf16_f32 v18, v4, v5\n"
"v_mfma_f32_16x16x32_bf16 v[8:11], a[172:175], v[124:127], v[8:11]\n"
"v_exp_f32 v6, v6\n"
"v_mfma_f32_16x16x32_bf16 v[12:15], a[176:179], v[112:115], v[36:39]\n"
"ds_read_b64_tr_b16 a[222:223], v245 offset:54016\n"
"v_mfma_f32_16x16x32_bf16 v[12:15], a[180:183], v[116:119], v[12:15]\n"
"v_exp_f32 v7, v7\n"
"v_mfma_f32_16x16x32_bf16 v[12:15], a[184:187], v[120:123], v[12:15]\n"
"v_cvt_pk_bf16_f32 v19, v6, v7\n"
"v_mfma_f32_16x16x32_bf16 v[12:15], a[188:191], v[124:127], v[12:15]\n"
"buffer_load_dwordx4 v247, s[8:11], s16 offen offset:3072 lds\n"
"v_mfma_f32_16x16x32_bf16 a[64:67], a[224:227], v[16:19], a[64:67]\n"
"v_exp_f32 v8, v8\n"
"v_mfma_f32_16x16x32_bf16 a[68:71], a[228:231], v[16:19], a[68:71]\n"
"buffer_load_dword v250, v214, s[20:23], s47 offen\n"
"v_mfma_f32_16x16x32_bf16 a[72:75], a[232:235], v[16:19], a[72:75]\n"
"v_exp_f32 v9, v9\n"
"v_mfma_f32_16x16x32_bf16 a[76:79], a[236:239], v[16:19], a[76:79]\n"
"v_cvt_pk_bf16_f32 v20, v8, v9\n"
"v_mfma_f32_16x16x32_bf16 a[80:83], a[240:243], v[16:19], a[80:83]\n"
"v_exp_f32 v10, v10\n"
"v_mfma_f32_16x16x32_bf16 a[84:87], a[244:247], v[16:19], a[84:87]\n"
"s_nop 0\n"
"v_mfma_f32_16x16x32_bf16 a[88:91], a[248:251], v[16:19], a[88:91]\n"
"v_exp_f32 v11, v11\n"
"v_mfma_f32_16x16x32_bf16 a[92:95], a[252:255], v[16:19], a[92:95]\n"
"s_waitcnt lgkmcnt(8)\n"
"v_cvt_pk_bf16_f32 v21, v10, v11\n"
"v_mfma_f32_16x16x32_bf16 v[52:55], v[40:43], v[16:19], v[52:55]\n"
"v_exp_f32 v12, v12\n"
"v_mfma_f32_16x16x32_bf16 v[0:3], a[128:131], v[64:67], v[24:27]\n"
"ds_read_b128 a[160:163], v241 offset:40960\n"
"v_mfma_f32_16x16x32_bf16 v[0:3], a[132:135], v[68:71], v[0:3]\n"
"v_exp_f32 v13, v13\n"
"v_mfma_f32_16x16x32_bf16 v[0:3], a[136:139], v[72:75], v[0:3]\n"
"ds_read_b128 a[164:167], v241 offset:41216\n"
"v_cvt_pk_bf16_f32 v22, v12, v13\n"
"v_mfma_f32_16x16x32_bf16 v[0:3], a[140:143], v[76:79], v[0:3]\n"
"v_exp_f32 v14, v14\n"
"v_mfma_f32_16x16x32_bf16 v[4:7], a[144:147], v[64:67], v[24:27]\n"
"ds_read_b128 a[168:171], v241 offset:41472\n"
"v_mfma_f32_16x16x32_bf16 v[4:7], a[148:151], v[68:71], v[4:7]\n"
"v_exp_f32 v15, v15\n"
"v_mfma_f32_16x16x32_bf16 v[4:7], a[152:155], v[72:75], v[4:7]\n"
"s_add_u32 m0, s18, 81920\n"
"ds_read_b128 a[172:175], v241 offset:41728\n"
"v_cvt_pk_bf16_f32 v23, v14, v15\n"
"v_mfma_f32_16x16x32_bf16 v[4:7], a[156:159], v[76:79], v[4:7]\n"
"buffer_load_dwordx4 v246, s[12:15], s16 offen offset:0 lds\n"
"v_mfma_f32_16x16x32_bf16 a[96:99], a[224:227], v[20:23], a[96:99]\n"
"v_exp_f32 v0, v0\n"
"v_mfma_f32_16x16x32_bf16 a[100:103], a[228:231], v[20:23], a[100:103]\n"
"ds_read_b128 a[176:179], v241 offset:45056\n"
"v_mfma_f32_16x16x32_bf16 a[104:107], a[232:235], v[20:23], a[104:107]\n"
"v_exp_f32 v1, v1\n"
"v_mfma_f32_16x16x32_bf16 a[108:111], a[236:239], v[20:23], a[108:111]\n"
"ds_read_b128 a[180:183], v241 offset:45312\n"
"v_cvt_pk_bf16_f32 v16, v0, v1\n"
"v_mfma_f32_16x16x32_bf16 a[112:115], a[240:243], v[20:23], a[112:115]\n"
"v_exp_f32 v2, v2\n"
"v_mfma_f32_16x16x32_bf16 a[116:119], a[244:247], v[20:23], a[116:119]\n"
"ds_read_b128 a[184:187], v241 offset:45568\n"
"v_mfma_f32_16x16x32_bf16 a[120:123], a[248:251], v[20:23], a[120:123]\n"
"v_exp_f32 v3, v3\n"
"v_mfma_f32_16x16x32_bf16 a[124:127], a[252:255], v[20:23], a[124:127]\n"
"v_cvt_pk_bf16_f32 v17, v2, v3\n"
"v_mfma_f32_16x16x32_bf16 v[56:59], v[40:43], v[20:23], v[56:59]\n"
"v_exp_f32 v4, v4\n"
"v_mfma_f32_16x16x32_bf16 v[8:11], a[128:131], v[80:83], v[28:31]\n"
"ds_read_b128 a[188:191], v241 offset:45824\n"
"v_mfma_f32_16x16x32_bf16 v[8:11], a[132:135], v[84:87], v[8:11]\n"
"v_exp_f32 v5, v5\n"
"v_mfma_f32_16x16x32_bf16 v[8:11], a[136:139], v[88:91], v[8:11]\n"
"ds_read_b64_tr_b16 a[224:225], v244 offset:57344\n"
"v_cvt_pk_bf16_f32 v18, v4, v5\n"
"v_mfma_f32_16x16x32_bf16 v[8:11], a[140:143], v[92:95], v[8:11]\n"
"v_exp_f32 v6, v6\n"
"v_mfma_f32_16x16x32_bf16 v[12:15], a[144:147], v[80:83], v[28:31]\n"
"ds_read_b64_tr_b16 a[226:227], v244 offset:61440\n"
"v_mfma_f32_16x16x32_bf16 v[12:15], a[148:151], v[84:87], v[12:15]\n"
"v_exp_f32 v7, v7\n"
"v_mfma_f32_16x16x32_bf16 v[12:15], a[152:155], v[88:91], v[12:15]\n"
"ds_read_b64_tr_b16 a[228:229], v245 offset:57344\n"
"v_cvt_pk_bf16_f32 v19, v6, v7\n"
"v_mfma_f32_16x16x32_bf16 v[12:15], a[156:159], v[92:95], v[12:15]\n"
"s_waitcnt lgkmcnt(11)\n"
"buffer_load_dwordx4 v247, s[12:15], s16 offen offset:1024 lds\n"
"v_mfma_f32_16x16x32_bf16 a[0:3], a[192:195], v[16:19], a[0:3]\n"
"v_exp_f32 v8, v8\n"
"v_mfma_f32_16x16x32_bf16 a[4:7], a[196:199], v[16:19], a[4:7]\n"
"ds_read_b64_tr_b16 a[230:231], v245 offset:61440\n"
"v_mfma_f32_16x16x32_bf16 a[8:11], a[200:203], v[16:19], a[8:11]\n"
"v_exp_f32 v9, v9\n"
"v_mfma_f32_16x16x32_bf16 a[12:15], a[204:207], v[16:19], a[12:15]\n"
"ds_read_b64_tr_b16 a[232:233], v244 offset:57600\n"
"v_cvt_pk_bf16_f32 v20, v8, v9\n"
"v_mfma_f32_16x16x32_bf16 a[16:19], a[208:211], v[16:19], a[16:19]\n"
"v_exp_f32 v10, v10\n"
"v_mfma_f32_16x16x32_bf16 a[20:23], a[212:215], v[16:19], a[20:23]\n"
"ds_read_b64_tr_b16 a[234:235], v244 offset:61696\n"
"v_mfma_f32_16x16x32_bf16 a[24:27], a[216:219], v[16:19], a[24:27]\n"
"v_exp_f32 v11, v11\n"
"v_mfma_f32_16x16x32_bf16 a[28:31], a[220:223], v[16:19], a[28:31]\n"
"v_cvt_pk_bf16_f32 v21, v10, v11\n"
"v_mfma_f32_16x16x32_bf16 v[44:47], v[40:43], v[16:19], v[44:47]\n"
"v_exp_f32 v12, v12\n"
"v_mfma_f32_16x16x32_bf16 v[0:3], a[128:131], v[96:99], v[32:35]\n"
"ds_read_b64_tr_b16 a[236:237], v245 offset:57600\n"
"v_mfma_f32_16x16x32_bf16 v[0:3], a[132:135], v[100:103], v[0:3]\n"
"v_exp_f32 v13, v13\n"
"v_mfma_f32_16x16x32_bf16 v[0:3], a[136:139], v[104:107], v[0:3]\n"
"ds_read_b64_tr_b16 a[238:239], v245 offset:61696\n"
"v_cvt_pk_bf16_f32 v22, v12, v13\n"
"v_mfma_f32_16x16x32_bf16 v[0:3], a[140:143], v[108:111], v[0:3]\n"
"v_exp_f32 v14, v14\n"
"v_mfma_f32_16x16x32_bf16 v[4:7], a[144:147], v[96:99], v[32:35]\n"
"ds_read_b64_tr_b16 a[240:241], v244 offset:57856\n"
"v_mfma_f32_16x16x32_bf16 v[4:7], a[148:151], v[100:103], v[4:7]\n"
"v_exp_f32 v15, v15\n"
"v_mfma_f32_16x16x32_bf16 v[4:7], a[152:155], v[104:107], v[4:7]\n"
"ds_read_b64_tr_b16 a[242:243], v244 offset:61952\n"
"v_cvt_pk_bf16_f32 v23, v14, v15\n"
"v_mfma_f32_16x16x32_bf16 v[4:7], a[156:159], v[108:111], v[4:7]\n"
"buffer_load_dwordx4 v246, s[12:15], s16 offen offset:2048 lds\n"
"v_mfma_f32_16x16x32_bf16 a[32:35], a[192:195], v[20:23], a[32:35]\n"
"v_exp_f32 v0, v0\n"
"v_mfma_f32_16x16x32_bf16 a[36:39], a[196:199], v[20:23], a[36:39]\n"
"ds_read_b64_tr_b16 a[244:245], v245 offset:57856\n"
"v_mfma_f32_16x16x32_bf16 a[40:43], a[200:203], v[20:23], a[40:43]\n"
"v_exp_f32 v1, v1\n"
"v_mfma_f32_16x16x32_bf16 a[44:47], a[204:207], v[20:23], a[44:47]\n"
"ds_read_b64_tr_b16 a[246:247], v245 offset:61952\n"
"v_cvt_pk_bf16_f32 v16, v0, v1\n"
"v_mfma_f32_16x16x32_bf16 a[48:51], a[208:211], v[20:23], a[48:51]\n"
"v_exp_f32 v2, v2\n"
"v_mfma_f32_16x16x32_bf16 a[52:55], a[212:215], v[20:23], a[52:55]\n"
"ds_read_b64_tr_b16 a[248:249], v244 offset:58112\n"
"v_mfma_f32_16x16x32_bf16 a[56:59], a[216:219], v[20:23], a[56:59]\n"
"v_exp_f32 v3, v3\n"
"v_mfma_f32_16x16x32_bf16 a[60:63], a[220:223], v[20:23], a[60:63]\n"
"v_cvt_pk_bf16_f32 v17, v2, v3\n"
"v_mfma_f32_16x16x32_bf16 v[48:51], v[40:43], v[20:23], v[48:51]\n"
"v_exp_f32 v4, v4\n"
"v_mfma_f32_16x16x32_bf16 v[8:11], a[128:131], v[112:115], v[36:39]\n"
"ds_read_b64_tr_b16 a[250:251], v244 offset:62208\n"
"v_mfma_f32_16x16x32_bf16 v[8:11], a[132:135], v[116:119], v[8:11]\n"
"v_exp_f32 v5, v5\n"
"v_mfma_f32_16x16x32_bf16 v[8:11], a[136:139], v[120:123], v[8:11]\n"
"ds_read_b64_tr_b16 a[252:253], v245 offset:58112\n"
"v_cvt_pk_bf16_f32 v18, v4, v5\n"
"v_mfma_f32_16x16x32_bf16 v[8:11], a[140:143], v[124:127], v[8:11]\n"
"v_exp_f32 v6, v6\n"
"v_mfma_f32_16x16x32_bf16 v[12:15], a[144:147], v[112:115], v[36:39]\n"
"ds_read_b64_tr_b16 a[254:255], v245 offset:62208\n"
"v_mfma_f32_16x16x32_bf16 v[12:15], a[148:151], v[116:119], v[12:15]\n"
"v_exp_f32 v7, v7\n"
"v_mfma_f32_16x16x32_bf16 v[12:15], a[152:155], v[120:123], v[12:15]\n"
"v_cvt_pk_bf16_f32 v19, v6, v7\n"
"v_mfma_f32_16x16x32_bf16 v[12:15], a[156:159], v[124:127], v[12:15]\n"
"buffer_load_dwordx4 v247, s[12:15], s16 offen offset:3072 lds\n"
"v_mfma_f32_16x16x32_bf16 a[64:67], a[192:195], v[16:19], a[64:67]\n"
"s_add_u32 s16, s16, 16384\n"
"v_exp_f32 v8, v8\n"
"v_mfma_f32_16x16x32_bf16 a[68:71], a[196:199], v[16:19], a[68:71]\n"
"buffer_load_dword v250, v251, s[24:27], s72 offen\n"
"v_mfma_f32_16x16x32_bf16 a[72:75], a[200:203], v[16:19], a[72:75]\n"
"v_exp_f32 v9, v9\n"
"v_mfma_f32_16x16x32_bf16 a[76:79], a[204:207], v[16:19], a[76:79]\n"
"v_cvt_pk_bf16_f32 v20, v8, v9\n"
"v_mfma_f32_16x16x32_bf16 a[80:83], a[208:211], v[16:19], a[80:83]\n"
"v_exp_f32 v10, v10\n"
"v_mfma_f32_16x16x32_bf16 a[84:87], a[212:215], v[16:19], a[84:87]\n"
"s_nop 0\n"
"v_mfma_f32_16x16x32_bf16 a[88:91], a[216:219], v[16:19], a[88:91]\n"
"v_exp_f32 v11, v11\n"
"v_mfma_f32_16x16x32_bf16 a[92:95], a[220:223], v[16:19], a[92:95]\n"
"s_waitcnt vmcnt(20) lgkmcnt(0)\n"
"s_barrier\n"
"v_cvt_pk_bf16_f32 v21, v10, v11\n"
"v_mfma_f32_16x16x32_bf16 v[52:55], v[40:43], v[16:19], v[52:55]\n"
"v_exp_f32 v12, v12\n"
"s_cmp_lg_u32 s56, 0\n"
"s_cbranch_scc1 AT_h3\n"
"s_mov_b32 s61, 0x80000\n"
"AT_h2:\n"
"v_xor_b32 v208, s17, v208\n"
"v_xor_b32 v209, s17, v209\n"
"v_xor_b32 v210, s17, v210\n"
"v_xor_b32 v211, s17, v211\n"
"v_or3_b32 v208, v208, v209, v210\n"
"v_or_b32 v208, v208, v211\n"
"v_cmp_ne_u32 vcc, 0, v208\n"
"s_sub_u32 s61, s61, 1\n"
"s_cbranch_vccz AT_h4\n"
"s_cmp_eq_u32 s61, 0\n"
"s_cbranch_scc1 AT_h4\n"
"s_sleep 2\n"
"buffer_load_dword v208, v212, s[36:39], s57 offen offset:4 sc1\n"
"buffer_load_dword v209, v212, s[36:39], s58 offen offset:4 sc1\n"
"buffer_load_dword v210, v212, s[36:39], s59 offen offset:4 sc1\n"
"buffer_load_dword v211, v212, s[36:39], s60 offen offset:4 sc1\n"
"s_waitcnt vmcnt(0)\n"
"s_branch AT_h2\n"
"AT_h4:\n"
"v_mov_b32 v208, s17\n"
"v_mov_b32 v209, 65536\n"
"s_mov_b64 exec, 1\n"
"global_store_dword v209, v208, s[78:79] sc1\n"
"s_mov_b64 exec, -1\n"
"AT_h3:\n"
"v_mfma_f32_16x16x32_bf16 v[0:3], a[160:163], v[64:67], v[24:27]\n"
"ds_read_b128 a[128:131], v240 offset:0\n"
"v_mfma_f32_16x16x32_bf16 v[0:3], a[164:167], v[68:71], v[0:3]\n"
"v_exp_f32 v13, v13\n"
"v_mfma_f32_16x16x32_bf16 v[0:3], a[168:171], v[72:75], v[0:3]\n"
"ds_read_b128 a[132:135], v240 offset:256\n"
"v_cvt_pk_bf16_f32 v22, v12, v13\n"
"v_mfma_f32_16x16x32_bf16 v[0:3], a[172:175], v[76:79], v[0:3]\n"
"v_exp_f32 v14, v14\n"
"v_mfma_f32_16x16x32_bf16 v[4:7], a[176:179], v[64:67], v[24:27]\n"
"ds_read_b128 a[136:139], v240 offset:512\n"
"v_mfma_f32_16x16x32_bf16 v[4:7], a[180:183], v[68:71], v[4:7]\n"
"v_exp_f32 v15, v15\n"
"v_mfma_f32_16x16x32_bf16 v[4:7], a[184:187], v[72:75], v[4:7]\n"
"s_add_u32 m0, s18, 98304\n"
"ds_read_b128 a[140:143], v240 offset:768\n"
"v_cvt_pk_bf16_f32 v23, v14, v15\n"
"v_mfma_f32_16x16x32_bf16 v[4:7], a[188:191], v[76:79], v[4:7]\n"
"buffer_load_dwordx4 v246, s[8:11], s16 offen offset:0 lds\n"
"v_mfma_f32_16x16x32_bf16 a[96:99], a[192:195], v[20:23], a[96:99]\n"
"v_exp_f32 v0, v0\n"
"v_mfma_f32_16x16x32_bf16 a[100:103], a[196:199], v[20:23], a[100:103]\n"
"ds_read_b128 a[144:147], v240 offset:4096\n"
"v_mfma_f32_16x16x32_bf16 a[104:107], a[200:203], v[20:23], a[104:107]\n"
"v_exp_f32 v1, v1\n"
"v_mfma_f32_16x16x32_bf16 a[108:111], a[204:207], v[20:23], a[108:111]\n"
"ds_read_b128 a[148:151], v240 offset:4352\n"
"v_cvt_pk_bf16_f32 v16, v0, v1\n"
"v_mfma_f32_16x16x32_bf16 a[112:115], a[208:211], v[20:23], a[112:115]\n"
"v_exp_f32 v2, v2\n"
"v_mfma_f32_16x16x32_bf16 a[116:119], a[212:215], v[20:23], a[116:119]\n"
"ds_read_b128 a[152:155], v240 offset:4608\n"
"v_mfma_f32_16x16x32_bf16 a[120:123], a[216:219], v[20:23], a[120:123]\n"
"v_exp_f32 v3, v3\n"
"v_mfma_f32_16x16x32_bf16 a[124:127], a[220:223], v[20:23], a[124:127]\n"
"v_cvt_pk_bf16_f32 v17, v2, v3\n"
"v_mfma_f32_16x16x32_bf16 v[56:59], v[40:43], v[20:23], v[56:59]\n"
"v_exp_f32 v4, v4\n"
"v_mfma_f32_16x16x32_bf16 v[8:11], a[160:163], v[80:83], v[28:31]\n"
"ds_read_b128 a[156:159], v240 offset:4864\n"
"v_mfma_f32_16x16x32_bf16 v[8:11], a[164:167], v[84:87], v[8:11]\n"
"v_exp_f32 v5, v5\n"
"v_mfma_f32_16x16x32_bf16 v[8:11], a[168:171], v[88:91], v[8:11]\n"
"ds_read_b64_tr_b16 a[192:193], v242 offset:16384\n"
"v_cvt_pk_bf16_f32 v18, v4, v5\n"
"v_mfma_f32_16x16x32_bf16 v[8:11], a[172:175], v[92:95], v[8:11]\n"
"v_exp_f32 v6, v6\n"
"v_mfma_f32_16x16x32_bf16 v[12:15], a[176:179], v[80:83], v[28:31]\n"
"ds_read_b64_tr_b16 a[194:195], v242 offset:20480\n"
"v_mfma_f32_16x16x32_bf16 v[12:15], a[180:183], v[84:87], v[12:15]\n"
"v_exp_f32 v7, v7\n"
"v_mfma_f32_16x16x32_bf16 v[12:15], a[184:187], v[88:91], v[12:15]\n"
"ds_read_b64_tr_b16 a[196:197], v243 offset:16384\n"
"v_cvt_pk_bf16_f32 v19, v6, v7\n"
"v_mfma_f32_16x16x32_bf16 v[12:15], a[188:191], v[92:95], v[12:15]\n"
"s_waitcnt lgkmcnt(11)\n"
"buffer_load_dwordx4 v246, s[8:11], s16 offen offset:1024 lds\n"
"v_mfma_f32_16x16x32_bf16 a[0:3], a[224:227], v[16:19], a[0:3]\n"
"v_exp_f32 v8, v8\n"
"v_mfma_f32_16x16x32_bf16 a[4:7], a[228:231], v[16:19], a[4:7]\n"
"ds_read_b64_tr_b16 a[198:199], v243 offset:20480\n"
"v_mfma_f32_16x16x32_bf16 a[8:11], a[232:235], v[16:19], a[8:11]\n"
"v_exp_f32 v9, v9\n"
"v_mfma_f32_16x16x32_bf16 a[12:15], a[236:239], v[16:19], a[12:15]\n"
"ds_read_b64_tr_b16 a[200:201], v242 offset:16640\n"
"v_cvt_pk_bf16_f32 v20, v8, v9\n"
"v_mfma_f32_16x16x32_bf16 a[16:19], a[240:243], v[16:19], a[16:19]\n"
"v_exp_f32 v10, v10\n"
"v_mfma_f32_16x16x32_bf16 a[20:23], a[244:247], v[16:19], a[20:23]\n"
"ds_read_b64_tr_b16 a[202:203], v242 offset:20736\n"
"v_mfma_f32_16x16x32_bf16 a[24:27], a[248:251], v[16:19], a[24:27]\n"
"v_exp_f32 v11, v11\n"
"v_mfma_f32_16x16x32_bf16 a[28:31], a[252:255], v[16:19], a[28:31]\n"
"v_cvt_pk_bf16_f32 v21, v10, v11\n"
"v_mfma_f32_16x16x32_bf16 v[44:47], v[40:43], v[16:19], v[44:47]\n"
"v_exp_f32 v12, v12\n"
"v_mfma_f32_16x16x32_bf16 v[0:3], a[160:163], v[96:99], v[32:35]\n"
"ds_read_b64_tr_b16 a[204:205], v243 offset:16640\n"
"v_mfma_f32_16x16x32_bf16 v[0:3], a[164:167], v[100:103], v[0:3]\n"
"v_exp_f32 v13, v13\n"
"v_mfma_f32_16x16x32_bf16 v[0:3], a[168:171], v[104:107], v[0:3]\n"
"ds_read_b64_tr_b16 a[206:207], v243 offset:20736\n"
"v_cvt_pk_bf16_f32 v22, v12, v13\n"
"v_mfma_f32_16x16x32_bf16 v[0:3], a[172:175], v[108:111], v[0:3]\n"
"v_exp_f32 v14, v14\n"
"v_mfma_f32_16x16x32_bf16 v[4:7], a[176:179], v[96:99], v[32:35]\n"
"ds_read_b64_tr_b16 a[208:209], v242 offset:16896\n"
"v_mfma_f32_16x16x32_bf16 v[4:7], a[180:183], v[100:103], v[4:7]\n"
"v_exp_f32 v15, v15\n"
"v_mfma_f32_16x16x32_bf16 v[4:7], a[184:187], v[104:107], v[4:7]\n"
"ds_read_b64_tr_b16 a[210:211], v242 offset:20992\n"
"v_cvt_pk_bf16_f32 v23, v14, v15\n"
"v_mfma_f32_16x16x32_bf16 v[4:7], a[188:191], v[108:111], v[4:7]\n"
"buffer_load_dwordx4 v247, s[8:11], s16 offen offset:2048 lds\n"
"v_mfma_f32_16x16x32_bf16 a[32:35], a[224:227], v[20:23], a[32:35]\n"
"v_exp_f32 v0, v0\n"
"v_mfma_f32_16x16x32_bf16 a[36:39], a[228:231], v[20:23], a[36:39]\n"
"ds_read_b64_tr_b16 a[212:213], v243 offset:16896\n"
"v_mfma_f32_16x16x32_bf16 a[40:43], a[232:235], v[20:23], a[40:43]\n"
"v_exp_f32 v1, v1\n"
"v_mfma_f32_16x16x32_bf16 a[44:47], a[236:239], v[20:23], a[44:47]\n"
"ds_read_b64_tr_b16 a[214:215], v243 offset:20992\n"
"v_cvt_pk_bf16_f32 v16, v0, v1\n"
"v_mfma_f32_16x16x32_bf16 a[48:51], a[240:243], v[20:23], a[48:51]\n"
"v_exp_f32 v2, v2\n"
"v_mfma_f32_16x16x32_bf16 a[52:55], a[244:247], v[20:23], a[52:55]\n"
"ds_read_b64_tr_b16 a[216:217], v242 offset:17152\n"
"v_mfma_f32_16x16x32_bf16 a[56:59], a[248:251], v[20:23], a[56:59]\n"
"v_exp_f32 v3, v3\n"
"v_mfma_f32_16x16x32_bf16 a[60:63], a[252:255], v[20:23], a[60:63]\n"
"v_cvt_pk_bf16_f32 v17, v2, v3\n"
"v_mfma_f32_16x16x32_bf16 v[48:51], v[40:43], v[20:23], v[48:51]\n"
"v_exp_f32 v4, v4\n"
"v_mfma_f32_16x16x32_bf16 v[8:11], a[160:163], v[112:115], v[36:39]\n"
"ds_read_b64_tr_b16 a[218:219], v242 offset:21248\n"
"v_mfma_f32_16x16x32_bf16 v[8:11], a[164:167], v[116:119], v[8:11]\n"
"v_exp_f32 v5, v5\n"
"v_mfma_f32_16x16x32_bf16 v[8:11], a[168:171], v[120:123], v[8:11]\n"
"ds_read_b64_tr_b16 a[220:221], v243 offset:17152\n"
"v_cvt_pk_bf16_f32 v18, v4, v5\n"
"v_mfma_f32_16x16x32_bf16 v[8:11], a[172:175], v[124:127], v[8:11]\n"
"v_exp_f32 v6, v6\n"
"v_mfma_f32_16x16x32_bf16 v[12:15], a[176:179], v[112:115], v[36:39]\n"
"ds_read_b64_tr_b16 a[222:223], v243 offset:21248\n"
"v_mfma_f32_16x16x32_bf16 v[12:15], a[180:183], v[116:119], v[12:15]\n"
"v_exp_f32 v7, v7\n"
"v_mfma_f32_16x16x32_bf16 v[12:15], a[184:187], v[120:123], v[12:15]\n"
"v_cvt_pk_bf16_f32 v19, v6, v7\n"
"v_mfma_f32_16x16x32_bf16 v[12:15], a[188:191], v[124:127], v[12:15]\n"
"buffer_load_dwordx4 v247, s[8:11], s16 offen offset:3072 lds\n"
"v_mfma_f32_16x16x32_bf16 a[64:67], a[224:227], v[16:19], a[64:67]\n"
"v_exp_f32 v8, v8\n"
"v_mfma_f32_16x16x32_bf16 a[68:71], a[228:231], v[16:19], a[68:71]\n"
"buffer_load_dword v250, v251, s[64:67], s72 offen\n"
"v_mfma_f32_16x16x32_bf16 a[72:75], a[232:235], v[16:19], a[72:75]\n"
"v_exp_f32 v9, v9\n"
"v_mfma_f32_16x16x32_bf16 a[76:79], a[236:239], v[16:19], a[76:79]\n"
"v_cvt_pk_bf16_f32 v20, v8, v9\n"
"v_mfma_f32_16x16x32_bf16 a[80:83], a[240:243], v[16:19], a[80:83]\n"
"v_exp_f32 v10, v10\n"
"v_mfma_f32_16x16x32_bf16 a[84:87], a[244:247], v[16:19], a[84:87]\n"
"s_nop 0\n"
"v_mfma_f32_16x16x32_bf16 a[88:91], a[248:251], v[16:19], a[88:91]\n"
"v_exp_f32 v11, v11\n"
"v_mfma_f32_16x16x32_bf16 a[92:95], a[252:255], v[16:19], a[92:95]\n"
"s_waitcnt lgkmcnt(8)\n"
"v_cvt_pk_bf16_f32 v21, v10, v11\n"
"v_mfma_f32_16x16x32_bf16 v[52:55], v[40:43], v[16:19], v[52:55]\n"
"v_exp_f32 v12, v12\n"
"v_mfma_f32_16x16x32_bf16 v[0:3], a[128:131], v[64:67], v[24:27]\n"
"ds_read_b128 a[160:163], v240 offset:8192\n"
"v_mfma_f32_16x16x32_bf16 v[0:3], a[132:135], v[68:71], v[0:3]\n"
"v_exp_f32 v13, v13\n"
"v_mfma_f32_16x16x32_bf16 v[0:3], a[136:139], v[72:75], v[0:3]\n"
"ds_read_b128 a[164:167], v240 offset:8448\n"
"v_cvt_pk_bf16_f32 v22, v12, v13\n"
"v_mfma_f32_16x16x32_bf16 v[0:3], a[140:143], v[76:79], v[0:3]\n"
"v_exp_f32 v14, v14\n"
"v_mfma_f32_16x16x32_bf16 v[4:7], a[144:147], v[64:67], v[24:27]\n"
"ds_read_b128 a[168:171], v240 offset:8704\n"
"v_mfma_f32_16x16x32_bf16 v[4:7], a[148:151], v[68:71], v[4:7]\n"
"v_exp_f32 v15, v15\n"
"v_mfma_f32_16x16x32_bf16 v[4:7], a[152:155], v[72:75], v[4:7]\n"
"s_add_u32 m0, s18, 114688\n"
"ds_read_b128 a[172:175], v240 offset:8960\n"
"v_cvt_pk_bf16_f32 v23, v14, v15\n"
"v_mfma_f32_16x16x32_bf16 v[4:7], a[156:159], v[76:79], v[4:7]\n"
"buffer_load_dwordx4 v246, s[12:15], s16 offen offset:0 lds\n"
"v_mfma_f32_16x16x32_bf16 a[96:99], a[224:227], v[20:23], a[96:99]\n"
"v_exp_f32 v0, v0\n"
"v_mfma_f32_16x16x32_bf16 a[100:103], a[228:231], v[20:23], a[100:103]\n"
"ds_read_b128 a[176:179], v240 offset:12288\n"
"v_mfma_f32_16x16x32_bf16 a[104:107], a[232:235], v[20:23], a[104:107]\n"
"v_exp_f32 v1, v1\n"
"v_mfma_f32_16x16x32_bf16 a[108:111], a[236:239], v[20:23], a[108:111]\n"
"ds_read_b128 a[180:183], v240 offset:12544\n"
"v_cvt_pk_bf16_f32 v16, v0, v1\n"
"v_mfma_f32_16x16x32_bf16 a[112:115], a[240:243], v[20:23], a[112:115]\n"
"v_exp_f32 v2, v2\n"
"v_mfma_f32_16x16x32_bf16 a[116:119], a[244:247], v[20:23], a[116:119]\n"
"ds_read_b128 a[184:187], v240 offset:12800\n"
"v_mfma_f32_16x16x32_bf16 a[120:123], a[248:251], v[20:23], a[120:123]\n"
"v_exp_f32 v3, v3\n"
"v_mfma_f32_16x16x32_bf16 a[124:127], a[252:255], v[20:23], a[124:127]\n"
"v_cvt_pk_bf16_f32 v17, v2, v3\n"
"v_mfma_f32_16x16x32_bf16 v[56:59], v[40:43], v[20:23], v[56:59]\n"
"v_exp_f32 v4, v4\n"
"v_mfma_f32_16x16x32_bf16 v[8:11], a[128:131], v[80:83], v[28:31]\n"
"ds_read_b128 a[188:191], v240 offset:13056\n"
"v_mfma_f32_16x16x32_bf16 v[8:11], a[132:135], v[84:87], v[8:11]\n"
"v_exp_f32 v5, v5\n"
"v_mfma_f32_16x16x32_bf16 v[8:11], a[136:139], v[88:91], v[8:11]\n"
"ds_read_b64_tr_b16 a[224:225], v242 offset:24576\n"
"v_cvt_pk_bf16_f32 v18, v4, v5\n"
"v_mfma_f32_16x16x32_bf16 v[8:11], a[140:143], v[92:95], v[8:11]\n"
"v_exp_f32 v6, v6\n"
"v_mfma_f32_16x16x32_bf16 v[12:15], a[144:147], v[80:83], v[28:31]\n"
"ds_read_b64_tr_b16 a[226:227], v242 offset:28672\n"
"v_mfma_f32_16x16x32_bf16 v[12:15], a[148:151], v[84:87], v[12:15]\n"
"v_exp_f32 v7, v7\n"
"v_mfma_f32_16x16x32_bf16 v[12:15], a[152:155], v[88:91], v[12:15]\n"
"ds_read_b64_tr_b16 a[228:229], v243 offset:24576\n"
"v_cvt_pk_bf16_f32 v19, v6, v7\n"
"v_mfma_f32_16x16x32_bf16 v[12:15], a[156:159], v[92:95], v[12:15]\n"
"s_waitcnt lgkmcnt(11)\n"
"buffer_load_dwordx4 v247, s[12:15], s16 offen offset:1024 lds\n"
"v_mfma_f32_16x16x32_bf16 a[0:3], a[192:195], v[16:19], a[0:3]\n"
"v_exp_f32 v8, v8\n"
"v_mfma_f32_16x16x32_bf16 a[4:7], a[196:199], v[16:19], a[4:7]\n"
"ds_read_b64_tr_b16 a[230:231], v243 offset:28672\n"
"v_mfma_f32_16x16x32_bf16 a[8:11], a[200:203], v[16:19], a[8:11]\n"
"v_exp_f32 v9, v9\n"
"v_mfma_f32_16x16x32_bf16 a[12:15], a[204:207], v[16:19], a[12:15]\n"
"ds_read_b64_tr_b16 a[232:233], v242 offset:24832\n"
"v_cvt_pk_bf16_f32 v20, v8, v9\n"
"v_mfma_f32_16x16x32_bf16 a[16:19], a[208:211], v[16:19], a[16:19]\n"
"v_exp_f32 v10, v10\n"
"v_mfma_f32_16x16x32_bf16 a[20:23], a[212:215], v[16:19], a[20:23]\n"
"ds_read_b64_tr_b16 a[234:235], v242 offset:28928\n"
"v_mfma_f32_16x16x32_bf16 a[24:27], a[216:219], v[16:19], a[24:27]\n"
"v_exp_f32 v11, v11\n"
"v_mfma_f32_16x16x32_bf16 a[28:31], a[220:223], v[16:19], a[28:31]\n"
"v_cvt_pk_bf16_f32 v21, v10, v11\n"
"v_mfma_f32_16x16x32_bf16 v[44:47], v[40:43], v[16:19], v[44:47]\n"
"v_exp_f32 v12, v12\n"
"v_mfma_f32_16x16x32_bf16 v[0:3], a[128:131], v[96:99], v[32:35]\n"
"ds_read_b64_tr_b16 a[236:237], v243 offset:24832\n"
"v_mfma_f32_16x16x32_bf16 v[0:3], a[132:135], v[100:103], v[0:3]\n"
"v_exp_f32 v13, v13\n"
"v_mfma_f32_16x16x32_bf16 v[0:3], a[136:139], v[104:107], v[0:3]\n"
"ds_read_b64_tr_b16 a[238:239], v243 offset:28928\n"
"v_cvt_pk_bf16_f32 v22, v12, v13\n"
"v_mfma_f32_16x16x32_bf16 v[0:3], a[140:143], v[108:111], v[0:3]\n"
"v_exp_f32 v14, v14\n"
"v_mfma_f32_16x16x32_bf16 v[4:7], a[144:147], v[96:99], v[32:35]\n"
"ds_read_b64_tr_b16 a[240:241], v242 offset:25088\n"
"v_mfma_f32_16x16x32_bf16 v[4:7], a[148:151], v[100:103], v[4:7]\n"
"v_exp_f32 v15, v15\n"
"v_mfma_f32_16x16x32_bf16 v[4:7], a[152:155], v[104:107], v[4:7]\n"
"ds_read_b64_tr_b16 a[242:243], v242 offset:29184\n"
"v_cvt_pk_bf16_f32 v23, v14, v15\n"
"v_mfma_f32_16x16x32_bf16 v[4:7], a[156:159], v[108:111], v[4:7]\n"
"buffer_load_dwordx4 v246, s[12:15], s16 offen offset:2048 lds\n"
"v_mfma_f32_16x16x32_bf16 a[32:35], a[192:195], v[20:23], a[32:35]\n"
"v_exp_f32 v0, v0\n"
"v_mfma_f32_16x16x32_bf16 a[36:39], a[196:199], v[20:23], a[36:39]\n"
"ds_read_b64_tr_b16 a[244:245], v243 offset:25088\n"
"v_mfma_f32_16x16x32_bf16 a[40:43], a[200:203], v[20:23], a[40:43]\n"
"v_exp_f32 v1, v1\n"
"v_mfma_f32_16x16x32_bf16 a[44:47], a[204:207], v[20:23], a[44:47]\n"
"ds_read_b64_tr_b16 a[246:247], v243 offset:29184\n"
"v_cvt_pk_bf16_f32 v16, v0, v1\n"
"v_mfma_f32_16x16x32_bf16 a[48:51], a[208:211], v[20:23], a[48:51]\n"
"v_exp_f32 v2, v2\n"
"v_mfma_f32_16x16x32_bf16 a[52:55], a[212:215], v[20:23], a[52:55]\n"
"ds_read_b64_tr_b16 a[248:249], v242 offset:25344\n"
"v_mfma_f32_16x16x32_bf16 a[56:59], a[216:219], v[20:23], a[56:59]\n"
"v_exp_f32 v3, v3\n"
"v_mfma_f32_16x16x32_bf16 a[60:63], a[220:223], v[20:23], a[60:63]\n"
"v_cvt_pk_bf16_f32 v17, v2, v3\n"
"v_mfma_f32_16x16x32_bf16 v[48:51], v[40:43], v[20:23], v[48:51]\n"
"v_exp_f32 v4, v4\n"
"v_mfma_f32_16x16x32_bf16 v[8:11], a[128:131], v[112:115], v[36:39]\n"
"ds_read_b64_tr_b16 a[250:251], v242 offset:29440\n"
"v_mfma_f32_16x16x32_bf16 v[8:11], a[132:135], v[116:119], v[8:11]\n"
"v_exp_f32 v5, v5\n"
"v_mfma_f32_16x16x32_bf16 v[8:11], a[136:139], v[120:123], v[8:11]\n"
"ds_read_b64_tr_b16 a[252:253], v243 offset:25344\n"
"v_cvt_pk_bf16_f32 v18, v4, v5\n"
"v_mfma_f32_16x16x32_bf16 v[8:11], a[140:143], v[124:127], v[8:11]\n"
"v_exp_f32 v6, v6\n"
"v_mfma_f32_16x16x32_bf16 v[12:15], a[144:147], v[112:115], v[36:39]\n"
"ds_read_b64_tr_b16 a[254:255], v243 offset:29440\n"
"v_mfma_f32_16x16x32_bf16 v[12:15], a[148:151], v[116:119], v[12:15]\n"
"v_exp_f32 v7, v7\n"
"v_mfma_f32_16x16x32_bf16 v[12:15], a[152:155], v[120:123], v[12:15]\n"
"v_cvt_pk_bf16_f32 v19, v6, v7\n"
"v_mfma_f32_16x16x32_bf16 v[12:15], a[156:159], v[124:127], v[12:15]\n"
"buffer_load_dwordx4 v247, s[12:15], s16 offen offset:3072 lds\n"
"v_mfma_f32_16x16x32_bf16 a[64:67], a[192:195], v[16:19], a[64:67]\n"
"s_add_u32 s16, s16, 16384\n"
"v_exp_f32 v8, v8\n"
"v_mfma_f32_16x16x32_bf16 a[68:71], a[196:199], v[16:19], a[68:71]\n"
"buffer_load_dword v250, v251, s[24:27], s73 offen\n"
"v_mfma_f32_16x16x32_bf16 a[72:75], a[200:203], v[16:19], a[72:75]\n"
"v_exp_f32 v9, v9\n"
"v_mfma_f32_16x16x32_bf16 a[76:79], a[204:207], v[16:19], a[76:79]\n"
"v_cvt_pk_bf16_f32 v20, v8, v9\n"
"v_mfma_f32_16x16x32_bf16 a[80:83], a[208:211], v[16:19], a[80:83]\n"
"v_exp_f32 v10, v10\n"
"v_mfma_f32_16x16x32_bf16 a[84:87], a[212:215], v[16:19], a[84:87]\n"
"s_nop 0\n"
"v_mfma_f32_16x16x32_bf16 a[88:91], a[216:219], v[16:19], a[88:91]\n"
"v_exp_f32 v11, v11\n"
"v_mfma_f32_16x16x32_bf16 a[92:95], a[220:223], v[16:19], a[92:95]\n"
"s_waitcnt vmcnt(21) lgkmcnt(0)\n"
"s_barrier\n"
"v_cvt_pk_bf16_f32 v21, v10, v11\n"
"v_mfma_f32_16x16x32_bf16 v[52:55], v[40:43], v[16:19], v[52:55]\n"
"v_exp_f32 v12, v12\n"
"v_mfma_f32_16x16x32_bf16 v[0:3], a[160:163], v[64:67], v[24:27]\n"
"ds_read_b128 a[128:131], v240 offset:32768\n"
"v_mfma_f32_16x16x32_bf16 v[0:3], a[164:167], v[68:71], v[0:3]\n"
"v_exp_f32 v13, v13\n"
"v_mfma_f32_16x16x32_bf16 v[0:3], a[168:171], v[72:75], v[0:3]\n"
"ds_read_b128 a[132:135], v240 offset:33024\n"
"v_cvt_pk_bf16_f32 v22, v12, v13\n"
"v_mfma_f32_16x16x32_bf16 v[0:3], a[172:175], v[76:79], v[0:3]\n"
"v_exp_f32 v14, v14\n"
"v_mfma_f32_16x16x32_bf16 v[4:7], a[176:179], v[64:67], v[24:27]\n"
"ds_read_b128 a[136:139], v240 offset:33280\n"
"v_mfma_f32_16x16x32_bf16 v[4:7], a[180:183], v[68:71], v[4:7]\n"
"v_exp_f32 v15, v15\n"
"v_mfma_f32_16x16x32_bf16 v[4:7], a[184:187], v[72:75], v[4:7]\n"
"s_mov_b32 m0, s53\n"
"ds_read_b128 a[140:143], v240 offset:33536\n"
"v_cvt_pk_bf16_f32 v23, v14, v15\n"
"v_mfma_f32_16x16x32_bf16 v[4:7], a[188:191], v[76:79], v[4:7]\n"
"buffer_load_dwordx4 v204, s[28:31], s52 offen offset:0 lds\n"
"v_mfma_f32_16x16x32_bf16 a[96:99], a[192:195], v[20:23], a[96:99]\n"
"s_add_u32 s52, s52, 8192\n"
"v_exp_f32 v0, v0\n"
"v_mfma_f32_16x16x32_bf16 a[100:103], a[196:199], v[20:23], a[100:103]\n"
"ds_read_b128 a[144:147], v240 offset:36864\n"
"v_mfma_f32_16x16x32_bf16 a[104:107], a[200:203], v[20:23], a[104:107]\n"
"v_exp_f32 v1, v1\n"
"v_mfma_f32_16x16x32_bf16 a[108:111], a[204:207], v[20:23], a[108:111]\n"
"ds_read_b128 a[148:151], v240 offset:37120\n"
"v_cvt_pk_bf16_f32 v16, v0, v1\n"
"v_mfma_f32_16x16x32_bf16 a[112:115], a[208:211], v[20:23], a[112:115]\n"
"v_exp_f32 v2, v2\n"
"v_mfma_f32_16x16x32_bf16 a[116:119], a[212:215], v[20:23], a[116:119]\n"
"ds_read_b128 a[152:155], v240 offset:37376\n"
"v_mfma_f32_16x16x32_bf16 a[120:123], a[216:219], v[20:23], a[120:123]\n"
"v_exp_f32 v3, v3\n"
"v_mfma_f32_16x16x32_bf16 a[124:127], a[220:223], v[20:23], a[124:127]\n"
"v_cvt_pk_bf16_f32 v17, v2, v3\n"
"v_mfma_f32_16x16x32_bf16 v[56:59], v[40:43], v[20:23], v[56:59]\n"
"v_exp_f32 v4, v4\n"
"v_mfma_f32_16x16x32_bf16 v[8:11], a[160:163], v[80:83], v[28:31]\n"
"ds_read_b128 a[156:159], v240 offset:37632\n"
"v_mfma_f32_16x16x32_bf16 v[8:11], a[164:167], v[84:87], v[8:11]\n"
"v_exp_f32 v5, v5\n"
"v_mfma_f32_16x16x32_bf16 v[8:11], a[168:171], v[88:91], v[8:11]\n"
"ds_read_b64_tr_b16 a[192:193], v242 offset:49152\n"
"v_cvt_pk_bf16_f32 v18, v4, v5\n"
"v_mfma_f32_16x16x32_bf16 v[8:11], a[172:175], v[92:95], v[8:11]\n"
"v_exp_f32 v6, v6\n"
"v_mfma_f32_16x16x32_bf16 v[12:15], a[176:179], v[80:83], v[28:31]\n"
"ds_read_b64_tr_b16 a[194:195], v242 offset:53248\n"
"v_mfma_f32_16x16x32_bf16 v[12:15], a[180:183], v[84:87], v[12:15]\n"
"v_exp_f32 v7, v7\n"
"v_mfma_f32_16x16x32_bf16 v[12:15], a[184:187], v[88:91], v[12:15]\n"
"ds_read_b64_tr_b16 a[196:197], v243 offset:49152\n"
"v_cvt_pk_bf16_f32 v19, v6, v7\n"
"v_mfma_f32_16x16x32_bf16 v[12:15], a[188:191], v[92:95], v[12:15]\n"
"s_waitcnt lgkmcnt(11)\n"
"buffer_load_dwordx4 v204, s[28:31], s52 offen offset:1024 lds\n"
"v_mfma_f32_16x16x32_bf16 a[0:3], a[224:227], v[16:19], a[0:3]\n"
"s_add_u32 s52, s52, 8192\n"
"v_exp_f32 v8, v8\n"
"v_mfma_f32_16x16x32_bf16 a[4:7], a[228:231], v[16:19], a[4:7]\n"
"ds_read_b64_tr_b16 a[198:199], v243 offset:53248\n"
"v_mfma_f32_16x16x32_bf16 a[8:11], a[232:235], v[16:19], a[8:11]\n"
"v_exp_f32 v9, v9\n"
"v_mfma_f32_16x16x32_bf16 a[12:15], a[236:239], v[16:19], a[12:15]\n"
"ds_read_b64_tr_b16 a[200:201], v242 offset:49408\n"
"v_cvt_pk_bf16_f32 v20, v8, v9\n"
"v_mfma_f32_16x16x32_bf16 a[16:19], a[240:243], v[16:19], a[16:19]\n"
"v_exp_f32 v10, v10\n"
"v_mfma_f32_16x16x32_bf16 a[20:23], a[244:247], v[16:19], a[20:23]\n"
"ds_read_b64_tr_b16 a[202:203], v242 offset:53504\n"
"v_mfma_f32_16x16x32_bf16 a[24:27], a[248:251], v[16:19], a[24:27]\n"
"v_exp_f32 v11, v11\n"
"v_mfma_f32_16x16x32_bf16 a[28:31], a[252:255], v[16:19], a[28:31]\n"
"v_cvt_pk_bf16_f32 v21, v10, v11\n"
"v_mfma_f32_16x16x32_bf16 v[44:47], v[40:43], v[16:19], v[44:47]\n"
"v_exp_f32 v12, v12\n"
"v_mfma_f32_16x16x32_bf16 v[0:3], a[160:163], v[96:99], v[32:35]\n"
"ds_read_b64_tr_b16 a[204:205], v243 offset:49408\n"
"v_mfma_f32_16x16x32_bf16 v[0:3], a[164:167], v[100:103], v[0:3]\n"
"v_exp_f32 v13, v13\n"
"v_mfma_f32_16x16x32_bf16 v[0:3], a[168:171], v[104:107], v[0:3]\n"
"ds_read_b64_tr_b16 a[206:207], v243 offset:53504\n"
"v_cvt_pk_bf16_f32 v22, v12, v13\n"
"v_mfma_f32_16x16x32_bf16 v[0:3], a[172:175], v[108:111], v[0:3]\n"
"v_exp_f32 v14, v14\n"
"v_mfma_f32_16x16x32_bf16 v[4:7], a[176:179], v[96:99], v[32:35]\n"
"ds_read_b64_tr_b16 a[208:209], v242 offset:49664\n"
"v_mfma_f32_16x16x32_bf16 v[4:7], a[180:183], v[100:103], v[4:7]\n"
"v_exp_f32 v15, v15\n"
"v_mfma_f32_16x16x32_bf16 v[4:7], a[184:187], v[104:107], v[4:7]\n"
"ds_read_b64_tr_b16 a[210:211], v242 offset:53760\n"
"v_cvt_pk_bf16_f32 v23, v14, v15\n"
"v_mfma_f32_16x16x32_bf16 v[4:7], a[188:191], v[108:111], v[4:7]\n"
"buffer_load_dwordx4 v204, s[28:31], s52 offen offset:2048 lds\n"
"v_mfma_f32_16x16x32_bf16 a[32:35], a[224:227], v[20:23], a[32:35]\n"
"s_add_u32 s52, s52, 8192\n"
"v_exp_f32 v0, v0\n"
"v_mfma_f32_16x16x32_bf16 a[36:39], a[228:231], v[20:23], a[36:39]\n"
"ds_read_b64_tr_b16 a[212:213], v243 offset:49664\n"
"v_mfma_f32_16x16x32_bf16 a[40:43], a[232:235], v[20:23], a[40:43]\n"
"v_exp_f32 v1, v1\n"
"v_mfma_f32_16x16x32_bf16 a[44:47], a[236:239], v[20:23], a[44:47]\n"
"ds_read_b64_tr_b16 a[214:215], v243 offset:53760\n"
"v_cvt_pk_bf16_f32 v16, v0, v1\n"
"v_mfma_f32_16x16x32_bf16 a[48:51], a[240:243], v[20:23], a[48:51]\n"
"v_exp_f32 v2, v2\n"
"v_mfma_f32_16x16x32_bf16 a[52:55], a[244:247], v[20:23], a[52:55]\n"
"ds_read_b64_tr_b16 a[216:217], v242 offset:49920\n"
"v_mfma_f32_16x16x32_bf16 a[56:59], a[248:251], v[20:23], a[56:59]\n"
"v_exp_f32 v3, v3\n"
"v_mfma_f32_16x16x32_bf16 a[60:63], a[252:255], v[20:23], a[60:63]\n"
"v_cvt_pk_bf16_f32 v17, v2, v3\n"
"v_mfma_f32_16x16x32_bf16 v[48:51], v[40:43], v[20:23], v[48:51]\n"
"v_exp_f32 v4, v4\n"
"v_mfma_f32_16x16x32_bf16 v[8:11], a[160:163], v[112:115], v[36:39]\n"
"ds_read_b64_tr_b16 a[218:219], v242 offset:54016\n"
"v_mfma_f32_16x16x32_bf16 v[8:11], a[164:167], v[116:119], v[8:11]\n"
"v_exp_f32 v5, v5\n"
"v_mfma_f32_16x16x32_bf16 v[8:11], a[168:171], v[120:123], v[8:11]\n"
"ds_read_b64_tr_b16 a[220:221], v243 offset:49920\n"
"v_cvt_pk_bf16_f32 v18, v4, v5\n"
"v_mfma_f32_16x16x32_bf16 v[8:11], a[172:175], v[124:127], v[8:11]\n"
"v_exp_f32 v6, v6\n"
"v_mfma_f32_16x16x32_bf16 v[12:15], a[176:179], v[112:115], v[36:39]\n"
"ds_read_b64_tr_b16 a[222:223], v243 offset:54016\n"
"v_mfma_f32_16x16x32_bf16 v[12:15], a[180:183], v[116:119], v[12:15]\n"
"v_exp_f32 v7, v7\n"
"v_mfma_f32_16x16x32_bf16 v[12:15], a[184:187], v[120:123], v[12:15]\n"
"v_cvt_pk_bf16_f32 v19, v6, v7\n"
"v_mfma_f32_16x16x32_bf16 v[12:15], a[188:191], v[124:127], v[12:15]\n"
"buffer_load_dwordx4 v204, s[28:31], s52 offen offset:3072 lds\n"
"v_mfma_f32_16x16x32_bf16 a[64:67], a[224:227], v[16:19], a[64:67]\n"
"s_add_u32 s52, s52, 12288\n"
"v_exp_f32 v8, v8\n"
"v_mfma_f32_16x16x32_bf16 a[68:71], a[228:231], v[16:19], a[68:71]\n"
"buffer_load_dword v250, v251, s[64:67], s73 offen\n"
"v_mfma_f32_16x16x32_bf16 a[72:75], a[232:235], v[16:19], a[72:75]\n"
"v_exp_f32 v9, v9\n"
"v_mfma_f32_16x16x32_bf16 a[76:79], a[236:239], v[16:19], a[76:79]\n"
"v_cvt_pk_bf16_f32 v20, v8, v9\n"
"v_mfma_f32_16x16x32_bf16 a[80:83], a[240:243], v[16:19], a[80:83]\n"
"v_exp_f32 v10, v10\n"
"v_mfma_f32_16x16x32_bf16 a[84:87], a[244:247], v[16:19], a[84:87]\n"
"s_nop 0\n"
"v_mfma_f32_16x16x32_bf16 a[88:91], a[248:251], v[16:19], a[88:91]\n"
"v_exp_f32 v11, v11\n"
"v_mfma_f32_16x16x32_bf16 a[92:95], a[252:255], v[16:19], a[92:95]\n"
"s_waitcnt lgkmcnt(8)\n"
"v_cvt_pk_bf16_f32 v21, v10, v11\n"
"v_mfma_f32_16x16x32_bf16 v[52:55], v[40:43], v[16:19], v[52:55]\n"
"v_exp_f32 v12, v12\n"
"v_mfma_f32_16x16x32_bf16 v[0:3], a[128:131], v[64:67], v[24:27]\n"
"ds_read_b128 a[160:163], v240 offset:40960\n"
"v_mfma_f32_16x16x32_bf16 v[0:3], a[132:135], v[68:71], v[0:3]\n"
"v_exp_f32 v13, v13\n"
"v_mfma_f32_16x16x32_bf16 v[0:3], a[136:139], v[72:75], v[0:3]\n"
"ds_read_b128 a[164:167], v240 offset:41216\n"
"v_cvt_pk_bf16_f32 v22, v12, v13\n"
"v_mfma_f32_16x16x32_bf16 v[0:3], a[140:143], v[76:79], v[0:3]\n"
"v_exp_f32 v14, v14\n"
"v_mfma_f32_16x16x32_bf16 v[4:7], a[144:147], v[64:67], v[24:27]\n"
"ds_read_b128 a[168:171], v240 offset:41472\n"
"v_mfma_f32_16x16x32_bf16 v[4:7], a[148:151], v[68:71], v[4:7]\n"
"v_exp_f32 v15, v15\n"
"v_mfma_f32_16x16x32_bf16 v[4:7], a[152:155], v[72:75], v[4:7]\n"
"s_add_u32 m0, s53, 4096\n"
"ds_read_b128 a[172:175], v240 offset:41728\n"
"v_cvt_pk_bf16_f32 v23, v14, v15\n"
"v_mfma_f32_16x16x32_bf16 v[4:7], a[156:159], v[76:79], v[4:7]\n"
"buffer_load_dwordx4 v204, s[28:31], s52 offen offset:0 lds\n"
"v_mfma_f32_16x16x32_bf16 a[96:99], a[224:227], v[20:23], a[96:99]\n"
"s_add_u32 s52, s52, 8192\n"
"v_exp_f32 v0, v0\n"
"v_mfma_f32_16x16x32_bf16 a[100:103], a[228:231], v[20:23], a[100:103]\n"
"ds_read_b128 a[176:179], v240 offset:45056\n"
"v_mfma_f32_16x16x32_bf16 a[104:107], a[232:235], v[20:23], a[104:107]\n"
"v_exp_f32 v1, v1\n"
"v_mfma_f32_16x16x32_bf16 a[108:111], a[236:239], v[20:23], a[108:111]\n"
"ds_read_b128 a[180:183], v240 offset:45312\n"
"v_cvt_pk_bf16_f32 v16, v0, v1\n"
"v_mfma_f32_16x16x32_bf16 a[112:115], a[240:243], v[20:23], a[112:115]\n"
"v_exp_f32 v2, v2\n"
"v_mfma_f32_16x16x32_bf16 a[116:119], a[244:247], v[20:23], a[116:119]\n"
"ds_read_b128 a[184:187], v240 offset:45568\n"
"v_mfma_f32_16x16x32_bf16 a[120:123], a[248:251], v[20:23], a[120:123]\n"
"v_exp_f32 v3, v3\n"
"v_mfma_f32_16x16x32_bf16 a[124:127], a[252:255], v[20:23], a[124:127]\n"
"v_cvt_pk_bf16_f32 v17, v2, v3\n"
"v_mfma_f32_16x16x32_bf16 v[56:59], v[40:43], v[20:23], v[56:59]\n"
"v_exp_f32 v4, v4\n"
"v_mfma_f32_16x16x32_bf16 v[8:11], a[128:131], v[80:83], v[28:31]\n"
"ds_read_b128 a[188:191], v240 offset:45824\n"
"v_mfma_f32_16x16x32_bf16 v[8:11], a[132:135], v[84:87], v[8:11]\n"
"v_exp_f32 v5, v5\n"
"v_mfma_f32_16x16x32_bf16 v[8:11], a[136:139], v[88:91], v[8:11]\n"
"ds_read_b64_tr_b16 a[224:225], v242 offset:57344\n"
"v_cvt_pk_bf16_f32 v18, v4, v5\n"
"v_mfma_f32_16x16x32_bf16 v[8:11], a[140:143], v[92:95], v[8:11]\n"
"v_exp_f32 v6, v6\n"
"v_mfma_f32_16x16x32_bf16 v[12:15], a[144:147], v[80:83], v[28:31]\n"
"ds_read_b64_tr_b16 a[226:227], v242 offset:61440\n"
"v_mfma_f32_16x16x32_bf16 v[12:15], a[148:151], v[84:87], v[12:15]\n"
"v_exp_f32 v7, v7\n"
"v_mfma_f32_16x16x32_bf16 v[12:15], a[152:155], v[88:91], v[12:15]\n"
"ds_read_b64_tr_b16 a[228:229], v243 offset:57344\n"
"v_cvt_pk_bf16_f32 v19, v6, v7\n"
"v_mfma_f32_16x16x32_bf16 v[12:15], a[156:159], v[92:95], v[12:15]\n"
"s_waitcnt lgkmcnt(11)\n"
"buffer_load_dwordx4 v204, s[28:31], s52 offen offset:1024 lds\n"
"v_mfma_f32_16x16x32_bf16 a[0:3], a[192:195], v[16:19], a[0:3]\n"
"s_add_u32 s52, s52, 8192\n"
"v_exp_f32 v8, v8\n"
"v_mfma_f32_16x16x32_bf16 a[4:7], a[196:199], v[16:19], a[4:7]\n"
"ds_read_b64_tr_b16 a[230:231], v243 offset:61440\n"
"v_mfma_f32_16x16x32_bf16 a[8:11], a[200:203], v[16:19], a[8:11]\n"
"v_exp_f32 v9, v9\n"
"v_mfma_f32_16x16x32_bf16 a[12:15], a[204:207], v[16:19], a[12:15]\n"
"ds_read_b64_tr_b16 a[232:233], v242 offset:57600\n"
"v_cvt_pk_bf16_f32 v20, v8, v9\n"
"v_mfma_f32_16x16x32_bf16 a[16:19], a[208:211], v[16:19], a[16:19]\n"
"v_exp_f32 v10, v10\n"
"v_mfma_f32_16x16x32_bf16 a[20:23], a[212:215], v[16:19], a[20:23]\n"
"ds_read_b64_tr_b16 a[234:235], v242 offset:61696\n"
"v_mfma_f32_16x16x32_bf16 a[24:27], a[216:219], v[16:19], a[24:27]\n"
"v_exp_f32 v11, v11\n"
"v_mfma_f32_16x16x32_bf16 a[28:31], a[220:223], v[16:19], a[28:31]\n"
"v_cvt_pk_bf16_f32 v21, v10, v11\n"
"v_mfma_f32_16x16x32_bf16 v[44:47], v[40:43], v[16:19], v[44:47]\n"
"v_exp_f32 v12, v12\n"
"v_mfma_f32_16x16x32_bf16 v[0:3], a[128:131], v[96:99], v[32:35]\n"
"ds_read_b64_tr_b16 a[236:237], v243 offset:57600\n"
"v_mfma_f32_16x16x32_bf16 v[0:3], a[132:135], v[100:103], v[0:3]\n"
"v_exp_f32 v13, v13\n"
"v_mfma_f32_16x16x32_bf16 v[0:3], a[136:139], v[104:107], v[0:3]\n"
"ds_read_b64_tr_b16 a[238:239], v243 offset:61696\n"
"v_cvt_pk_bf16_f32 v22, v12, v13\n"
"v_mfma_f32_16x16x32_bf16 v[0:3], a[140:143], v[108:111], v[0:3]\n"
"v_exp_f32 v14, v14\n"
"v_mfma_f32_16x16x32_bf16 v[4:7], a[144:147], v[96:99], v[32:35]\n"
"ds_read_b64_tr_b16 a[240:241], v242 offset:57856\n"
"v_mfma_f32_16x16x32_bf16 v[4:7], a[148:151], v[100:103], v[4:7]\n"
"v_exp_f32 v15, v15\n"
"v_mfma_f32_16x16x32_bf16 v[4:7], a[152:155], v[104:107], v[4:7]\n"
"ds_read_b64_tr_b16 a[242:243], v242 offset:61952\n"
"v_cvt_pk_bf16_f32 v23, v14, v15\n"
"v_mfma_f32_16x16x32_bf16 v[4:7], a[156:159], v[108:111], v[4:7]\n"
"buffer_load_dwordx4 v204, s[28:31], s52 offen offset:2048 lds\n"
"v_mfma_f32_16x16x32_bf16 a[32:35], a[192:195], v[20:23], a[32:35]\n"
"s_add_u32 s52, s52, 8192\n"
"v_exp_f32 v0, v0\n"
"v_mfma_f32_16x16x32_bf16 a[36:39], a[196:199], v[20:23], a[36:39]\n"
"ds_read_b64_tr_b16 a[244:245], v243 offset:57856\n"
"v_mfma_f32_16x16x32_bf16 a[40:43], a[200:203], v[20:23], a[40:43]\n"
"v_exp_f32 v1, v1\n"
"v_mfma_f32_16x16x32_bf16 a[44:47], a[204:207], v[20:23], a[44:47]\n"
"ds_read_b64_tr_b16 a[246:247], v243 offset:61952\n"
"v_cvt_pk_bf16_f32 v16, v0, v1\n"
"v_mfma_f32_16x16x32_bf16 a[48:51], a[208:211], v[20:23], a[48:51]\n"
"v_exp_f32 v2, v2\n"
"v_mfma_f32_16x16x32_bf16 a[52:55], a[212:215], v[20:23], a[52:55]\n"
"ds_read_b64_tr_b16 a[248:249], v242 offset:58112\n"
"v_mfma_f32_16x16x32_bf16 a[56:59], a[216:219], v[20:23], a[56:59]\n"
"v_exp_f32 v3, v3\n"
"v_mfma_f32_16x16x32_bf16 a[60:63], a[220:223], v[20:23], a[60:63]\n"
"v_cvt_pk_bf16_f32 v17, v2, v3\n"
"v_mfma_f32_16x16x32_bf16 v[48:51], v[40:43], v[20:23], v[48:51]\n"
"v_exp_f32 v4, v4\n"
"v_mfma_f32_16x16x32_bf16 v[8:11], a[128:131], v[112:115], v[36:39]\n"
"ds_read_b64_tr_b16 a[250:251], v242 offset:62208\n"
"v_mfma_f32_16x16x32_bf16 v[8:11], a[132:135], v[116:119], v[8:11]\n"
"v_exp_f32 v5, v5\n"
"v_mfma_f32_16x16x32_bf16 v[8:11], a[136:139], v[120:123], v[8:11]\n"
"ds_read_b64_tr_b16 a[252:253], v243 offset:58112\n"
"v_cvt_pk_bf16_f32 v18, v4, v5\n"
"v_mfma_f32_16x16x32_bf16 v[8:11], a[140:143], v[124:127], v[8:11]\n"
"v_exp_f32 v6, v6\n"
"v_mfma_f32_16x16x32_bf16 v[12:15], a[144:147], v[112:115], v[36:39]\n"
"ds_read_b64_tr_b16 a[254:255], v243 offset:62208\n"
"v_mfma_f32_16x16x32_bf16 v[12:15], a[148:151], v[116:119], v[12:15]\n"
"v_exp_f32 v7, v7\n"
"v_mfma_f32_16x16x32_bf16 v[12:15], a[152:155], v[120:123], v[12:15]\n"
"v_cvt_pk_bf16_f32 v19, v6, v7\n"
"v_mfma_f32_16x16x32_bf16 v[12:15], a[156:159], v[124:127], v[12:15]\n"
"buffer_load_dwordx4 v204, s[28:31], s52 offen offset:3072 lds\n"
"v_mfma_f32_16x16x32_bf16 a[64:67], a[192:195], v[16:19], a[64:67]\n"
"s_sub_u32 s52, s52, 61312\n"
"v_exp_f32 v8, v8\n"
"v_mfma_f32_16x16x32_bf16 a[68:71], a[196:199], v[16:19], a[68:71]\n"
"buffer_load_dword v250, v251, s[24:27], s74 offen\n"
"v_mfma_f32_16x16x32_bf16 a[72:75], a[200:203], v[16:19], a[72:75]\n"
"v_exp_f32 v9, v9\n"
"v_mfma_f32_16x16x32_bf16 a[76:79], a[204:207], v[16:19], a[76:79]\n"
"v_cvt_pk_bf16_f32 v20, v8, v9\n"
"v_mfma_f32_16x16x32_bf16 a[80:83], a[208:211], v[16:19], a[80:83]\n"
"v_exp_f32 v10, v10\n"
"v_mfma_f32_16x16x32_bf16 a[84:87], a[212:215], v[16:19], a[84:87]\n"
"s_nop 0\n"
"v_mfma_f32_16x16x32_bf16 a[88:91], a[216:219], v[16:19], a[88:91]\n"
"v_exp_f32 v11, v11\n"
"v_mfma_f32_16x16x32_bf16 a[92:95], a[220:223], v[16:19], a[92:95]\n"
"s_waitcnt vmcnt(21) lgkmcnt(0)\n"
"s_barrier\n"
"v_cvt_pk_bf16_f32 v21, v10, v11\n"
"v_mfma_f32_16x16x32_bf16 v[52:55], v[40:43], v[16:19], v[52:55]\n"
"v_exp_f32 v12, v12\n"
"v_mfma_f32_16x16x32_bf16 v[0:3], a[160:163], v[64:67], v[24:27]\n"
"ds_read_b128 a[128:131], v241 offset:0\n"
"v_mfma_f32_16x16x32_bf16 v[0:3], a[164:167], v[68:71], v[0:3]\n"
"v_exp_f32 v13, v13\n"
"v_mfma_f32_16x16x32_bf16 v[0:3], a[168:171], v[72:75], v[0:3]\n"
"ds_read_b128 a[132:135], v241 offset:256\n"
"v_cvt_pk_bf16_f32 v22, v12, v13\n"
"v_mfma_f32_16x16x32_bf16 v[0:3], a[172:175], v[76:79], v[0:3]\n"
"v_exp_f32 v14, v14\n"
"v_mfma_f32_16x16x32_bf16 v[4:7], a[176:179], v[64:67], v[24:27]\n"
"ds_read_b128 a[136:139], v241 offset:512\n"
"v_mfma_f32_16x16x32_bf16 v[4:7], a[180:183], v[68:71], v[4:7]\n"
"v_exp_f32 v15, v15\n"
"v_mfma_f32_16x16x32_bf16 v[4:7], a[184:187], v[72:75], v[4:7]\n"
"s_add_u32 m0, s53, 32768\n"
"ds_read_b128 a[140:143], v241 offset:768\n"
"v_cvt_pk_bf16_f32 v23, v14, v15\n"
"v_mfma_f32_16x16x32_bf16 v[4:7], a[188:191], v[76:79], v[4:7]\n"
"buffer_load_dwordx4 v204, s[28:31], s52 offen offset:0 lds\n"
"v_mfma_f32_16x16x32_bf16 a[96:99], a[192:195], v[20:23], a[96:99]\n"
"s_add_u32 s52, s52, 8192\n"
"v_exp_f32 v0, v0\n"
"v_mfma_f32_16x16x32_bf16 a[100:103], a[196:199], v[20:23], a[100:103]\n"
"ds_read_b128 a[144:147], v241 offset:4096\n"
"v_mfma_f32_16x16x32_bf16 a[104:107], a[200:203], v[20:23], a[104:107]\n"
"v_exp_f32 v1, v1\n"
"v_mfma_f32_16x16x32_bf16 a[108:111], a[204:207], v[20:23], a[108:111]\n"
"ds_read_b128 a[148:151], v241 offset:4352\n"
"v_cvt_pk_bf16_f32 v16, v0, v1\n"
"v_mfma_f32_16x16x32_bf16 a[112:115], a[208:211], v[20:23], a[112:115]\n"
"v_exp_f32 v2, v2\n"
"v_mfma_f32_16x16x32_bf16 a[116:119], a[212:215], v[20:23], a[116:119]\n"
"ds_read_b128 a[152:155], v241 offset:4608\n"
"v_mfma_f32_16x16x32_bf16 a[120:123], a[216:219], v[20:23], a[120:123]\n"
"v_exp_f32 v3, v3\n"
"v_mfma_f32_16x16x32_bf16 a[124:127], a[220:223], v[20:23], a[124:127]\n"
"v_cvt_pk_bf16_f32 v17, v2, v3\n"
"v_mfma_f32_16x16x32_bf16 v[56:59], v[40:43], v[20:23], v[56:59]\n"
"v_exp_f32 v4, v4\n"
"v_mfma_f32_16x16x32_bf16 v[8:11], a[160:163], v[80:83], v[28:31]\n"
"ds_read_b128 a[156:159], v241 offset:4864\n"
"v_mfma_f32_16x16x32_bf16 v[8:11], a[164:167], v[84:87], v[8:11]\n"
"v_exp_f32 v5, v5\n"
"v_mfma_f32_16x16x32_bf16 v[8:11], a[168:171], v[88:91], v[8:11]\n"
"ds_read_b64_tr_b16 a[192:193], v244 offset:16384\n"
"v_cvt_pk_bf16_f32 v18, v4, v5\n"
"v_mfma_f32_16x16x32_bf16 v[8:11], a[172:175], v[92:95], v[8:11]\n"
"v_exp_f32 v6, v6\n"
"v_mfma_f32_16x16x32_bf16 v[12:15], a[176:179], v[80:83], v[28:31]\n"
"ds_read_b64_tr_b16 a[194:195], v244 offset:20480\n"
"v_mfma_f32_16x16x32_bf16 v[12:15], a[180:183], v[84:87], v[12:15]\n"
"v_exp_f32 v7, v7\n"
"v_mfma_f32_16x16x32_bf16 v[12:15], a[184:187], v[88:91], v[12:15]\n"
"ds_read_b64_tr_b16 a[196:197], v245 offset:16384\n"
"v_cvt_pk_bf16_f32 v19, v6, v7\n"
"v_mfma_f32_16x16x32_bf16 v[12:15], a[188:191], v[92:95], v[12:15]\n"
"s_waitcnt lgkmcnt(11)\n"
"buffer_load_dwordx4 v204, s[28:31], s52 offen offset:1024 lds\n"
"v_mfma_f32_16x16x32_bf16 a[0:3], a[224:227], v[16:19], a[0:3]\n"
"s_add_u32 s52, s52, 8192\n"
"v_exp_f32 v8, v8\n"
"v_mfma_f32_16x16x32_bf16 a[4:7], a[228:231], v[16:19], a[4:7]\n"
"ds_read_b64_tr_b16 a[198:199], v245 offset:20480\n"
"v_mfma_f32_16x16x32_bf16 a[8:11], a[232:235], v[16:19], a[8:11]\n"
"v_exp_f32 v9, v9\n"
"v_mfma_f32_16x16x32_bf16 a[12:15], a[236:239], v[16:19], a[12:15]\n"
"ds_read_b64_tr_b16 a[200:201], v244 offset:16640\n"
"v_cvt_pk_bf16_f32 v20, v8, v9\n"
"v_mfma_f32_16x16x32_bf16 a[16:19], a[240:243], v[16:19], a[16:19]\n"
"v_exp_f32 v10, v10\n"
"v_mfma_f32_16x16x32_bf16 a[20:23], a[244:247], v[16:19], a[20:23]\n"
"ds_read_b64_tr_b16 a[202:203], v244 offset:20736\n"
"v_mfma_f32_16x16x32_bf16 a[24:27], a[248:251], v[16:19], a[24:27]\n"
"v_exp_f32 v11, v11\n"
"v_mfma_f32_16x16x32_bf16 a[28:31], a[252:255], v[16:19], a[28:31]\n"
"v_cvt_pk_bf16_f32 v21, v10, v11\n"
"v_mfma_f32_16x16x32_bf16 v[44:47], v[40:43], v[16:19], v[44:47]\n"
"v_exp_f32 v12, v12\n"
"v_mfma_f32_16x16x32_bf16 v[0:3], a[160:163], v[96:99], v[32:35]\n"
"ds_read_b64_tr_b16 a[204:205], v245 offset:16640\n"
"v_mfma_f32_16x16x32_bf16 v[0:3], a[164:167], v[100:103], v[0:3]\n"
"v_exp_f32 v13, v13\n"
"v_mfma_f32_16x16x32_bf16 v[0:3], a[168:171], v[104:107], v[0:3]\n"
"ds_read_b64_tr_b16 a[206:207], v245 offset:20736\n"
"v_cvt_pk_bf16_f32 v22, v12, v13\n"
"v_mfma_f32_16x16x32_bf16 v[0:3], a[172:175], v[108:111], v[0:3]\n"
"v_exp_f32 v14, v14\n"
"v_mfma_f32_16x16x32_bf16 v[4:7], a[176:179], v[96:99], v[32:35]\n"
"ds_read_b64_tr_b16 a[208:209], v244 offset:16896\n"
"v_mfma_f32_16x16x32_bf16 v[4:7], a[180:183], v[100:103], v[4:7]\n"
"v_exp_f32 v15, v15\n"
"v_mfma_f32_16x16x32_bf16 v[4:7], a[184:187], v[104:107], v[4:7]\n"
"ds_read_b64_tr_b16 a[210:211], v244 offset:20992\n"
"v_cvt_pk_bf16_f32 v23, v14, v15\n"
"v_mfma_f32_16x16x32_bf16 v[4:7], a[188:191], v[108:111], v[4:7]\n"
"buffer_load_dwordx4 v204, s[28:31], s52 offen offset:2048 lds\n"
"v_mfma_f32_16x16x32_bf16 a[32:35], a[224:227], v[20:23], a[32:35]\n"
"s_add_u32 s52, s52, 8192\n"
"v_exp_f32 v0, v0\n"
"v_mfma_f32_16x16x32_bf16 a[36:39], a[228:231], v[20:23], a[36:39]\n"
"ds_read_b64_tr_b16 a[212:213], v245 offset:16896\n"
"v_mfma_f32_16x16x32_bf16 a[40:43], a[232:235], v[20:23], a[40:43]\n"
"v_exp_f32 v1, v1\n"
"v_mfma_f32_16x16x32_bf16 a[44:47], a[236:239], v[20:23], a[44:47]\n"
"ds_read_b64_tr_b16 a[214:215], v245 offset:20992\n"
"v_cvt_pk_bf16_f32 v16, v0, v1\n"
"v_mfma_f32_16x16x32_bf16 a[48:51], a[240:243], v[20:23], a[48:51]\n"
"v_exp_f32 v2, v2\n"
"v_mfma_f32_16x16x32_bf16 a[52:55], a[244:247], v[20:23], a[52:55]\n"
"ds_read_b64_tr_b16 a[216:217], v244 offset:17152\n"
"v_mfma_f32_16x16x32_bf16 a[56:59], a[248:251], v[20:23], a[56:59]\n"
"v_exp_f32 v3, v3\n"
"v_mfma_f32_16x16x32_bf16 a[60:63], a[252:255], v[20:23], a[60:63]\n"
"v_cvt_pk_bf16_f32 v17, v2, v3\n"
"v_mfma_f32_16x16x32_bf16 v[48:51], v[40:43], v[20:23], v[48:51]\n"
"v_exp_f32 v4, v4\n"
"v_mfma_f32_16x16x32_bf16 v[8:11], a[160:163], v[112:115], v[36:39]\n"
"ds_read_b64_tr_b16 a[218:219], v244 offset:21248\n"
"v_mfma_f32_16x16x32_bf16 v[8:11], a[164:167], v[116:119], v[8:11]\n"
"v_exp_f32 v5, v5\n"
"v_mfma_f32_16x16x32_bf16 v[8:11], a[168:171], v[120:123], v[8:11]\n"
"ds_read_b64_tr_b16 a[220:221], v245 offset:17152\n"
"v_cvt_pk_bf16_f32 v18, v4, v5\n"
"v_mfma_f32_16x16x32_bf16 v[8:11], a[172:175], v[124:127], v[8:11]\n"
"v_exp_f32 v6, v6\n"
"v_mfma_f32_16x16x32_bf16 v[12:15], a[176:179], v[112:115], v[36:39]\n"
"ds_read_b64_tr_b16 a[222:223], v245 offset:21248\n"
"v_mfma_f32_16x16x32_bf16 v[12:15], a[180:183], v[116:119], v[12:15]\n"
"v_exp_f32 v7, v7\n"
"v_mfma_f32_16x16x32_bf16 v[12:15], a[184:187], v[120:123], v[12:15]\n"
"v_cvt_pk_bf16_f32 v19, v6, v7\n"
"v_mfma_f32_16x16x32_bf16 v[12:15], a[188:191], v[124:127], v[12:15]\n"
"buffer_load_dwordx4 v204, s[28:31], s52 offen offset:3072 lds\n"
"v_mfma_f32_16x16x32_bf16 a[64:67], a[224:227], v[16:19], a[64:67]\n"
"s_add_u32 s52, s52, 12288\n"
"v_exp_f32 v8, v8\n"
"v_mfma_f32_16x16x32_bf16 a[68:71], a[228:231], v[16:19], a[68:71]\n"
"buffer_load_dword v250, v251, s[64:67], s74 offen\n"
"v_mfma_f32_16x16x32_bf16 a[72:75], a[232:235], v[16:19], a[72:75]\n"
"v_exp_f32 v9, v9\n"
"v_mfma_f32_16x16x32_bf16 a[76:79], a[236:239], v[16:19], a[76:79]\n"
"v_cvt_pk_bf16_f32 v20, v8, v9\n"
"v_mfma_f32_16x16x32_bf16 a[80:83], a[240:243], v[16:19], a[80:83]\n"
"v_exp_f32 v10, v10\n"
"v_mfma_f32_16x16x32_bf16 a[84:87], a[244:247], v[16:19], a[84:87]\n"
"s_nop 0\n"
"v_mfma_f32_16x16x32_bf16 a[88:91], a[248:251], v[16:19], a[88:91]\n"
"v_exp_f32 v11, v11\n"
"v_mfma_f32_16x16x32_bf16 a[92:95], a[252:255], v[16:19], a[92:95]\n"
"s_waitcnt lgkmcnt(8)\n"
"v_cvt_pk_bf16_f32 v21, v10, v11\n"
"v_mfma_f32_16x16x32_bf16 v[52:55], v[40:43], v[16:19], v[52:55]\n"
"v_exp_f32 v12, v12\n"
"v_mfma_f32_16x16x32_bf16 v[0:3], a[128:131], v[64:67], v[24:27]\n"
"ds_read_b128 a[160:163], v241 offset:8192\n"
"v_mfma_f32_16x16x32_bf16 v[0:3], a[132:135], v[68:71], v[0:3]\n"
"v_exp_f32 v13, v13\n"
"v_mfma_f32_16x16x32_bf16 v[0:3], a[136:139], v[72:75], v[0:3]\n"
"ds_read_b128 a[164:167], v241 offset:8448\n"
"v_cvt_pk_bf16_f32 v22, v12, v13\n"
"v_mfma_f32_16x16x32_bf16 v[0:3], a[140:143], v[76:79], v[0:3]\n"
"v_exp_f32 v14, v14\n"
"v_mfma_f32_16x16x32_bf16 v[4:7], a[144:147], v[64:67], v[24:27]\n"
"ds_read_b128 a[168:171], v241 offset:8704\n"
"v_mfma_f32_16x16x32_bf16 v[4:7], a[148:151], v[68:71], v[4:7]\n"
"v_exp_f32 v15, v15\n"
"v_mfma_f32_16x16x32_bf16 v[4:7], a[152:155], v[72:75], v[4:7]\n"
"s_add_u32 m0, s53, 36864\n"
"ds_read_b128 a[172:175], v241 offset:8960\n"
"v_cvt_pk_bf16_f32 v23, v14, v15\n"
"v_mfma_f32_16x16x32_bf16 v[4:7], a[156:159], v[76:79], v[4:7]\n"
"buffer_load_dwordx4 v204, s[28:31], s52 offen offset:0 lds\n"
"v_mfma_f32_16x16x32_bf16 a[96:99], a[224:227], v[20:23], a[96:99]\n"
"s_add_u32 s52, s52, 8192\n"
"v_exp_f32 v0, v0\n"
"v_mfma_f32_16x16x32_bf16 a[100:103], a[228:231], v[20:23], a[100:103]\n"
"ds_read_b128 a[176:179], v241 offset:12288\n"
"v_mfma_f32_16x16x32_bf16 a[104:107], a[232:235], v[20:23], a[104:107]\n"
"v_exp_f32 v1, v1\n"
"v_mfma_f32_16x16x32_bf16 a[108:111], a[236:239], v[20:23], a[108:111]\n"
"ds_read_b128 a[180:183], v241 offset:12544\n"
"v_cvt_pk_bf16_f32 v16, v0, v1\n"
"v_mfma_f32_16x16x32_bf16 a[112:115], a[240:243], v[20:23], a[112:115]\n"
"v_exp_f32 v2, v2\n"
"v_mfma_f32_16x16x32_bf16 a[116:119], a[244:247], v[20:23], a[116:119]\n"
"ds_read_b128 a[184:187], v241 offset:12800\n"
"v_mfma_f32_16x16x32_bf16 a[120:123], a[248:251], v[20:23], a[120:123]\n"
"v_exp_f32 v3, v3\n"
"v_mfma_f32_16x16x32_bf16 a[124:127], a[252:255], v[20:23], a[124:127]\n"
"v_cvt_pk_bf16_f32 v17, v2, v3\n"
"v_mfma_f32_16x16x32_bf16 v[56:59], v[40:43], v[20:23], v[56:59]\n"
"v_exp_f32 v4, v4\n"
"v_mfma_f32_16x16x32_bf16 v[8:11], a[128:131], v[80:83], v[28:31]\n"
"ds_read_b128 a[188:191], v241 offset:13056\n"
"v_mfma_f32_16x16x32_bf16 v[8:11], a[132:135], v[84:87], v[8:11]\n"
"v_exp_f32 v5, v5\n"
"v_mfma_f32_16x16x32_bf16 v[8:11], a[136:139], v[88:91], v[8:11]\n"
"ds_read_b64_tr_b16 a[224:225], v244 offset:24576\n"
"v_cvt_pk_bf16_f32 v18, v4, v5\n"
"v_mfma_f32_16x16x32_bf16 v[8:11], a[140:143], v[92:95], v[8:11]\n"
"v_exp_f32 v6, v6\n"
"v_mfma_f32_16x16x32_bf16 v[12:15], a[144:147], v[80:83], v[28:31]\n"
"ds_read_b64_tr_b16 a[226:227], v244 offset:28672\n"
"v_mfma_f32_16x16x32_bf16 v[12:15], a[148:151], v[84:87], v[12:15]\n"
"v_exp_f32 v7, v7\n"
"v_mfma_f32_16x16x32_bf16 v[12:15], a[152:155], v[88:91], v[12:15]\n"
"ds_read_b64_tr_b16 a[228:229], v245 offset:24576\n"
"v_cvt_pk_bf16_f32 v19, v6, v7\n"
"v_mfma_f32_16x16x32_bf16 v[12:15], a[156:159], v[92:95], v[12:15]\n"
"s_waitcnt lgkmcnt(11)\n"
"buffer_load_dwordx4 v204, s[28:31], s52 offen offset:1024 lds\n"
"v_mfma_f32_16x16x32_bf16 a[0:3], a[192:195], v[16:19], a[0:3]\n"
"s_add_u32 s52, s52, 8192\n"
"v_exp_f32 v8, v8\n"
"v_mfma_f32_16x16x32_bf16 a[4:7], a[196:199], v[16:19], a[4:7]\n"
"ds_read_b64_tr_b16 a[230:231], v245 offset:28672\n"
"v_mfma_f32_16x16x32_bf16 a[8:11], a[200:203], v[16:19], a[8:11]\n"
"v_exp_f32 v9, v9\n"
"v_mfma_f32_16x16x32_bf16 a[12:15], a[204:207], v[16:19], a[12:15]\n"
"ds_read_b64_tr_b16 a[232:233], v244 offset:24832\n"
"v_cvt_pk_bf16_f32 v20, v8, v9\n"
"v_mfma_f32_16x16x32_bf16 a[16:19], a[208:211], v[16:19], a[16:19]\n"
"v_exp_f32 v10, v10\n"
"v_mfma_f32_16x16x32_bf16 a[20:23], a[212:215], v[16:19], a[20:23]\n"
"ds_read_b64_tr_b16 a[234:235], v244 offset:28928\n"
"v_mfma_f32_16x16x32_bf16 a[24:27], a[216:219], v[16:19], a[24:27]\n"
"v_exp_f32 v11, v11\n"
"v_mfma_f32_16x16x32_bf16 a[28:31], a[220:223], v[16:19], a[28:31]\n"
"v_cvt_pk_bf16_f32 v21, v10, v11\n"
"v_mfma_f32_16x16x32_bf16 v[44:47], v[40:43], v[16:19], v[44:47]\n"
"v_exp_f32 v12, v12\n"
"v_mfma_f32_16x16x32_bf16 v[0:3], a[128:131], v[96:99], v[32:35]\n"
"ds_read_b64_tr_b16 a[236:237], v245 offset:24832\n"
"v_mfma_f32_16x16x32_bf16 v[0:3], a[132:135], v[100:103], v[0:3]\n"
"v_exp_f32 v13, v13\n"
"v_mfma_f32_16x16x32_bf16 v[0:3], a[136:139], v[104:107], v[0:3]\n"
"ds_read_b64_tr_b16 a[238:239], v245 offset:28928\n"
"v_cvt_pk_bf16_f32 v22, v12, v13\n"
"v_mfma_f32_16x16x32_bf16 v[0:3], a[140:143], v[108:111], v[0:3]\n"
"v_exp_f32 v14, v14\n"
"v_mfma_f32_16x16x32_bf16 v[4:7], a[144:147], v[96:99], v[32:35]\n"
"ds_read_b64_tr_b16 a[240:241], v244 offset:25088\n"
"v_mfma_f32_16x16x32_bf16 v[4:7], a[148:151], v[100:103], v[4:7]\n"
"v_exp_f32 v15, v15\n"
"v_mfma_f32_16x16x32_bf16 v[4:7], a[152:155], v[104:107], v[4:7]\n"
"ds_read_b64_tr_b16 a[242:243], v244 offset:29184\n"
"v_cvt_pk_bf16_f32 v23, v14, v15\n"
"v_mfma_f32_16x16x32_bf16 v[4:7], a[156:159], v[108:111], v[4:7]\n"
"buffer_load_dwordx4 v204, s[28:31], s52 offen offset:2048 lds\n"
"v_mfma_f32_16x16x32_bf16 a[32:35], a[192:195], v[20:23], a[32:35]\n"
"s_add_u32 s52, s52, 8192\n"
"v_exp_f32 v0, v0\n"
"v_mfma_f32_16x16x32_bf16 a[36:39], a[196:199], v[20:23], a[36:39]\n"
"ds_read_b64_tr_b16 a[244:245], v245 offset:25088\n"
"v_mfma_f32_16x16x32_bf16 a[40:43], a[200:203], v[20:23], a[40:43]\n"
"v_exp_f32 v1, v1\n"
"v_mfma_f32_16x16x32_bf16 a[44:47], a[204:207], v[20:23], a[44:47]\n"
"ds_read_b64_tr_b16 a[246:247], v245 offset:29184\n"
"v_cvt_pk_bf16_f32 v16, v0, v1\n"
"v_mfma_f32_16x16x32_bf16 a[48:51], a[208:211], v[20:23], a[48:51]\n"
"v_exp_f32 v2, v2\n"
"v_mfma_f32_16x16x32_bf16 a[52:55], a[212:215], v[20:23], a[52:55]\n"
"ds_read_b64_tr_b16 a[248:249], v244 offset:25344\n"
"v_mfma_f32_16x16x32_bf16 a[56:59], a[216:219], v[20:23], a[56:59]\n"
"v_exp_f32 v3, v3\n"
"v_mfma_f32_16x16x32_bf16 a[60:63], a[220:223], v[20:23], a[60:63]\n"
"v_cvt_pk_bf16_f32 v17, v2, v3\n"
"v_mfma_f32_16x16x32_bf16 v[48:51], v[40:43], v[20:23], v[48:51]\n"
"v_exp_f32 v4, v4\n"
"v_mfma_f32_16x16x32_bf16 v[8:11], a[128:131], v[112:115], v[36:39]\n"
"ds_read_b64_tr_b16 a[250:251], v244 offset:29440\n"
"v_mfma_f32_16x16x32_bf16 v[8:11], a[132:135], v[116:119], v[8:11]\n"
"v_exp_f32 v5, v5\n"
"v_mfma_f32_16x16x32_bf16 v[8:11], a[136:139], v[120:123], v[8:11]\n"
"ds_read_b64_tr_b16 a[252:253], v245 offset:25344\n"
"v_cvt_pk_bf16_f32 v18, v4, v5\n"
"v_mfma_f32_16x16x32_bf16 v[8:11], a[140:143], v[124:127], v[8:11]\n"
"v_exp_f32 v6, v6\n"
"v_mfma_f32_16x16x32_bf16 v[12:15], a[144:147], v[112:115], v[36:39]\n"
"ds_read_b64_tr_b16 a[254:255], v245 offset:29440\n"
"v_mfma_f32_16x16x32_bf16 v[12:15], a[148:151], v[116:119], v[12:15]\n"
"v_exp_f32 v7, v7\n"
"v_mfma_f32_16x16x32_bf16 v[12:15], a[152:155], v[120:123], v[12:15]\n"
"v_cvt_pk_bf16_f32 v19, v6, v7\n"
"v_mfma_f32_16x16x32_bf16 v[12:15], a[156:159], v[124:127], v[12:15]\n"
"buffer_load_dwordx4 v204, s[28:31], s52 offen offset:3072 lds\n"
"v_mfma_f32_16x16x32_bf16 a[64:67], a[192:195], v[16:19], a[64:67]\n"
"s_sub_u32 s52, s52, 61312\n"
"v_exp_f32 v8, v8\n"
"v_mfma_f32_16x16x32_bf16 a[68:71], a[196:199], v[16:19], a[68:71]\n"
"buffer_load_dword v250, v214, s[68:71], s75 offen\n"
"v_mfma_f32_16x16x32_bf16 a[72:75], a[200:203], v[16:19], a[72:75]\n"
"v_exp_f32 v9, v9\n"
"v_mfma_f32_16x16x32_bf16 a[76:79], a[204:207], v[16:19], a[76:79]\n"
"v_cvt_pk_bf16_f32 v20, v8, v9\n"
"v_mfma_f32_16x16x32_bf16 a[80:83], a[208:211], v[16:19], a[80:83]\n"
"v_exp_f32 v10, v10\n"
"v_mfma_f32_16x16x32_bf16 a[84:87], a[212:215], v[16:19], a[84:87]\n"
"s_nop 0\n"
"v_mfma_f32_16x16x32_bf16 a[88:91], a[216:219], v[16:19], a[88:91]\n"
"v_exp_f32 v11, v11\n"
"v_mfma_f32_16x16x32_bf16 a[92:95], a[220:223], v[16:19], a[92:95]\n"
"s_waitcnt vmcnt(21) lgkmcnt(0)\n"
"s_barrier\n"
"v_cvt_pk_bf16_f32 v21, v10, v11\n"
"v_mfma_f32_16x16x32_bf16 v[52:55], v[40:43], v[16:19], v[52:55]\n"
"v_exp_f32 v12, v12\n"
"v_mfma_f32_16x16x32_bf16 v[0:3], a[160:163], v[64:67], v[24:27]\n"
"ds_read_b128 a[128:131], v241 offset:32768\n"
"v_mfma_f32_16x16x32_bf16 v[0:3], a[164:167], v[68:71], v[0:3]\n"
"v_exp_f32 v13, v13\n"
"v_mfma_f32_16x16x32_bf16 v[0:3], a[168:171], v[72:75], v[0:3]\n"
"ds_read_b128 a[132:135], v241 offset:33024\n"
"v_cvt_pk_bf16_f32 v22, v12, v13\n"
"v_mfma_f32_16x16x32_bf16 v[0:3], a[172:175], v[76:79], v[0:3]\n"
"v_exp_f32 v14, v14\n"
"v_mfma_f32_16x16x32_bf16 v[4:7], a[176:179], v[64:67], v[24:27]\n"
"ds_read_b128 a[136:139], v241 offset:33280\n"
"v_mfma_f32_16x16x32_bf16 v[4:7], a[180:183], v[68:71], v[4:7]\n"
"v_exp_f32 v15, v15\n"
"v_mfma_f32_16x16x32_bf16 v[4:7], a[184:187], v[72:75], v[4:7]\n"
"s_add_u32 m0, s53, 65536\n"
"ds_read_b128 a[140:143], v241 offset:33536\n"
"v_cvt_pk_bf16_f32 v23, v14, v15\n"
"v_mfma_f32_16x16x32_bf16 v[4:7], a[188:191], v[76:79], v[4:7]\n"
"buffer_load_dwordx4 v204, s[28:31], s52 offen offset:0 lds\n"
"v_mfma_f32_16x16x32_bf16 a[96:99], a[192:195], v[20:23], a[96:99]\n"
"s_add_u32 s52, s52, 8192\n"
"v_exp_f32 v0, v0\n"
"v_mfma_f32_16x16x32_bf16 a[100:103], a[196:199], v[20:23], a[100:103]\n"
"ds_read_b128 a[144:147], v241 offset:36864\n"
"v_mfma_f32_16x16x32_bf16 a[104:107], a[200:203], v[20:23], a[104:107]\n"
"v_exp_f32 v1, v1\n"
"v_mfma_f32_16x16x32_bf16 a[108:111], a[204:207], v[20:23], a[108:111]\n"
"ds_read_b128 a[148:151], v241 offset:37120\n"
"v_cvt_pk_bf16_f32 v16, v0, v1\n"
"v_mfma_f32_16x16x32_bf16 a[112:115], a[208:211], v[20:23], a[112:115]\n"
"v_exp_f32 v2, v2\n"
"v_mfma_f32_16x16x32_bf16 a[116:119], a[212:215], v[20:23], a[116:119]\n"
"ds_read_b128 a[152:155], v241 offset:37376\n"
"v_mfma_f32_16x16x32_bf16 a[120:123], a[216:219], v[20:23], a[120:123]\n"
"v_exp_f32 v3, v3\n"
"v_mfma_f32_16x16x32_bf16 a[124:127], a[220:223], v[20:23], a[124:127]\n"
"v_cvt_pk_bf16_f32 v17, v2, v3\n"
"v_mfma_f32_16x16x32_bf16 v[56:59], v[40:43], v[20:23], v[56:59]\n"
"v_exp_f32 v4, v4\n"
"v_mfma_f32_16x16x32_bf16 v[8:11], a[160:163], v[80:83], v[28:31]\n"
"ds_read_b128 a[156:159], v241 offset:37632\n"
"v_mfma_f32_16x16x32_bf16 v[8:11], a[164:167], v[84:87], v[8:11]\n"
"v_exp_f32 v5, v5\n"
"v_mfma_f32_16x16x32_bf16 v[8:11], a[168:171], v[88:91], v[8:11]\n"
"ds_read_b64_tr_b16 a[192:193], v244 offset:49152\n"
"v_cvt_pk_bf16_f32 v18, v4, v5\n"
"v_mfma_f32_16x16x32_bf16 v[8:11], a[172:175], v[92:95], v[8:11]\n"
"v_exp_f32 v6, v6\n"
"v_mfma_f32_16x16x32_bf16 v[12:15], a[176:179], v[80:83], v[28:31]\n"
"ds_read_b64_tr_b16 a[194:195], v244 offset:53248\n"
"v_mfma_f32_16x16x32_bf16 v[12:15], a[180:183], v[84:87], v[12:15]\n"
"v_exp_f32 v7, v7\n"
"v_mfma_f32_16x16x32_bf16 v[12:15], a[184:187], v[88:91], v[12:15]\n"
"ds_read_b64_tr_b16 a[196:197], v245 offset:49152\n"
"v_cvt_pk_bf16_f32 v19, v6, v7\n"
"v_mfma_f32_16x16x32_bf16 v[12:15], a[188:191], v[92:95], v[12:15]\n"
"s_waitcnt lgkmcnt(11)\n"
"buffer_load_dwordx4 v204, s[28:31], s52 offen offset:1024 lds\n"
"v_mfma_f32_16x16x32_bf16 a[0:3], a[224:227], v[16:19], a[0:3]\n"
"s_add_u32 s52, s52, 8192\n"
"v_exp_f32 v8, v8\n"
"v_mfma_f32_16x16x32_bf16 a[4:7], a[228:231], v[16:19], a[4:7]\n"
"ds_read_b64_tr_b16 a[198:199], v245 offset:53248\n"
"v_mfma_f32_16x16x32_bf16 a[8:11], a[232:235], v[16:19], a[8:11]\n"
"v_exp_f32 v9, v9\n"
"v_mfma_f32_16x16x32_bf16 a[12:15], a[236:239], v[16:19], a[12:15]\n"
"ds_read_b64_tr_b16 a[200:201], v244 offset:49408\n"
"v_cvt_pk_bf16_f32 v20, v8, v9\n"
"v_mfma_f32_16x16x32_bf16 a[16:19], a[240:243], v[16:19], a[16:19]\n"
"v_exp_f32 v10, v10\n"
"v_mfma_f32_16x16x32_bf16 a[20:23], a[244:247], v[16:19], a[20:23]\n"
"ds_read_b64_tr_b16 a[202:203], v244 offset:53504\n"
"v_mfma_f32_16x16x32_bf16 a[24:27], a[248:251], v[16:19], a[24:27]\n"
"v_exp_f32 v11, v11\n"
"v_mfma_f32_16x16x32_bf16 a[28:31], a[252:255], v[16:19], a[28:31]\n"
"v_cvt_pk_bf16_f32 v21, v10, v11\n"
"v_mfma_f32_16x16x32_bf16 v[44:47], v[40:43], v[16:19], v[44:47]\n"
"v_exp_f32 v12, v12\n"
"v_mfma_f32_16x16x32_bf16 v[0:3], a[160:163], v[96:99], v[32:35]\n"
"ds_read_b64_tr_b16 a[204:205], v245 offset:49408\n"
"v_mfma_f32_16x16x32_bf16 v[0:3], a[164:167], v[100:103], v[0:3]\n"
"v_exp_f32 v13, v13\n"
"v_mfma_f32_16x16x32_bf16 v[0:3], a[168:171], v[104:107], v[0:3]\n"
"ds_read_b64_tr_b16 a[206:207], v245 offset:53504\n"
"v_cvt_pk_bf16_f32 v22, v12, v13\n"
"v_mfma_f32_16x16x32_bf16 v[0:3], a[172:175], v[108:111], v[0:3]\n"
"v_exp_f32 v14, v14\n"
"v_mfma_f32_16x16x32_bf16 v[4:7], a[176:179], v[96:99], v[32:35]\n"
"ds_read_b64_tr_b16 a[208:209], v244 offset:49664\n"
"v_mfma_f32_16x16x32_bf16 v[4:7], a[180:183], v[100:103], v[4:7]\n"
"v_exp_f32 v15, v15\n"
"v_mfma_f32_16x16x32_bf16 v[4:7], a[184:187], v[104:107], v[4:7]\n"
"ds_read_b64_tr_b16 a[210:211], v244 offset:53760\n"
"v_cvt_pk_bf16_f32 v23, v14, v15\n"
"v_mfma_f32_16x16x32_bf16 v[4:7], a[188:191], v[108:111], v[4:7]\n"
"buffer_load_dwordx4 v204, s[28:31], s52 offen offset:2048 lds\n"
"v_mfma_f32_16x16x32_bf16 a[32:35], a[224:227], v[20:23], a[32:35]\n"
"s_add_u32 s52, s52, 8192\n"
"v_exp_f32 v0, v0\n"
"v_mfma_f32_16x16x32_bf16 a[36:39], a[228:231], v[20:23], a[36:39]\n"
"ds_read_b64_tr_b16 a[212:213], v245 offset:49664\n"
"v_mfma_f32_16x16x32_bf16 a[40:43], a[232:235], v[20:23], a[40:43]\n"
"v_exp_f32 v1, v1\n"
"v_mfma_f32_16x16x32_bf16 a[44:47], a[236:239], v[20:23], a[44:47]\n"
"ds_read_b64_tr_b16 a[214:215], v245 offset:53760\n"
"v_cvt_pk_bf16_f32 v16, v0, v1\n"
"v_mfma_f32_16x16x32_bf16 a[48:51], a[240:243], v[20:23], a[48:51]\n"
"v_exp_f32 v2, v2\n"
"v_mfma_f32_16x16x32_bf16 a[52:55], a[244:247], v[20:23], a[52:55]\n"
"ds_read_b64_tr_b16 a[216:217], v244 offset:49920\n"
"v_mfma_f32_16x16x32_bf16 a[56:59], a[248:251], v[20:23], a[56:59]\n"
"v_exp_f32 v3, v3\n"
"v_mfma_f32_16x16x32_bf16 a[60:63], a[252:255], v[20:23], a[60:63]\n"
"v_cvt_pk_bf16_f32 v17, v2, v3\n"
"v_mfma_f32_16x16x32_bf16 v[48:51], v[40:43], v[20:23], v[48:51]\n"
"v_exp_f32 v4, v4\n"
"v_mfma_f32_16x16x32_bf16 v[8:11], a[160:163], v[112:115], v[36:39]\n"
"ds_read_b64_tr_b16 a[218:219], v244 offset:54016\n"
"v_mfma_f32_16x16x32_bf16 v[8:11], a[164:167], v[116:119], v[8:11]\n"
"v_exp_f32 v5, v5\n"
"v_mfma_f32_16x16x32_bf16 v[8:11], a[168:171], v[120:123], v[8:11]\n"
"ds_read_b64_tr_b16 a[220:221], v245 offset:49920\n"
"v_cvt_pk_bf16_f32 v18, v4, v5\n"
"v_mfma_f32_16x16x32_bf16 v[8:11], a[172:175], v[124:127], v[8:11]\n"
"v_exp_f32 v6, v6\n"
"v_mfma_f32_16x16x32_bf16 v[12:15], a[176:179], v[112:115], v[36:39]\n"
"ds_read_b64_tr_b16 a[222:223], v245 offset:54016\n"
"v_mfma_f32_16x16x32_bf16 v[12:15], a[180:183], v[116:119], v[12:15]\n"
"v_exp_f32 v7, v7\n"
"v_mfma_f32_16x16x32_bf16 v[12:15], a[184:187], v[120:123], v[12:15]\n"
"v_cvt_pk_bf16_f32 v19, v6, v7\n"
"v_mfma_f32_16x16x32_bf16 v[12:15], a[188:191], v[124:127], v[12:15]\n"
"buffer_load_dwordx4 v204, s[28:31], s52 offen offset:3072 lds\n"
"v_mfma_f32_16x16x32_bf16 a[64:67], a[224:227], v[16:19], a[64:67]\n"
"s_add_u32 s52, s52, 12288\n"
"v_exp_f32 v8, v8\n"
"v_mfma_f32_16x16x32_bf16 a[68:71], a[228:231], v[16:19], a[68:71]\n"
"buffer_load_dword v250, v214, s[68:71], s76 offen\n"
"v_mfma_f32_16x16x32_bf16 a[72:75], a[232:235], v[16:19], a[72:75]\n"
"v_exp_f32 v9, v9\n"
"v_mfma_f32_16x16x32_bf16 a[76:79], a[236:239], v[16:19], a[76:79]\n"
"v_cvt_pk_bf16_f32 v20, v8, v9\n"
"v_mfma_f32_16x16x32_bf16 a[80:83], a[240:243], v[16:19], a[80:83]\n"
"v_exp_f32 v10, v10\n"
"v_mfma_f32_16x16x32_bf16 a[84:87], a[244:247], v[16:19], a[84:87]\n"
"s_nop 0\n"
"v_mfma_f32_16x16x32_bf16 a[88:91], a[248:251], v[16:19], a[88:91]\n"
"v_exp_f32 v11, v11\n"
"v_mfma_f32_16x16x32_bf16 a[92:95], a[252:255], v[16:19], a[92:95]\n"
"s_waitcnt lgkmcnt(8)\n"
"v_cvt_pk_bf16_f32 v21, v10, v11\n"
"v_mfma_f32_16x16x32_bf16 v[52:55], v[40:43], v[16:19], v[52:55]\n"
"v_exp_f32 v12, v12\n"
"v_mfma_f32_16x16x32_bf16 v[0:3], a[128:131], v[64:67], v[24:27]\n"
"ds_read_b128 a[160:163], v241 offset:40960\n"
"v_mfma_f32_16x16x32_bf16 v[0:3], a[132:135], v[68:71], v[0:3]\n"
"v_exp_f32 v13, v13\n"
"v_mfma_f32_16x16x32_bf16 v[0:3], a[136:139], v[72:75], v[0:3]\n"
"ds_read_b128 a[164:167], v241 offset:41216\n"
"v_cvt_pk_bf16_f32 v22, v12, v13\n"
"v_mfma_f32_16x16x32_bf16 v[0:3], a[140:143], v[76:79], v[0:3]\n"
"v_exp_f32 v14, v14\n"
"v_mfma_f32_16x16x32_bf16 v[4:7], a[144:147], v[64:67], v[24:27]\n"
"ds_read_b128 a[168:171], v241 offset:41472\n"
"v_mfma_f32_16x16x32_bf16 v[4:7], a[148:151], v[68:71], v[4:7]\n"
"v_exp_f32 v15, v15\n"
"v_mfma_f32_16x16x32_bf16 v[4:7], a[152:155], v[72:75], v[4:7]\n"
"s_add_u32 m0, s53, 69632\n"
"ds_read_b128 a[172:175], v241 offset:41728\n"
"v_cvt_pk_bf16_f32 v23, v14, v15\n"
"v_mfma_f32_16x16x32_bf16 v[4:7], a[156:159], v[76:79], v[4:7]\n"
"buffer_load_dwordx4 v204, s[28:31], s52 offen offset:0 lds\n"
"v_mfma_f32_16x16x32_bf16 a[96:99], a[224:227], v[20:23], a[96:99]\n"
"s_add_u32 s52, s52, 8192\n"
"v_exp_f32 v0, v0\n"
"v_mfma_f32_16x16x32_bf16 a[100:103], a[228:231], v[20:23], a[100:103]\n"
"ds_read_b128 a[176:179], v241 offset:45056\n"
"v_mfma_f32_16x16x32_bf16 a[104:107], a[232:235], v[20:23], a[104:107]\n"
"v_exp_f32 v1, v1\n"
"v_mfma_f32_16x16x32_bf16 a[108:111], a[236:239], v[20:23], a[108:111]\n"
"ds_read_b128 a[180:183], v241 offset:45312\n"
"v_cvt_pk_bf16_f32 v16, v0, v1\n"
"v_mfma_f32_16x16x32_bf16 a[112:115], a[240:243], v[20:23], a[112:115]\n"
"v_exp_f32 v2, v2\n"
"v_mfma_f32_16x16x32_bf16 a[116:119], a[244:247], v[20:23], a[116:119]\n"
"ds_read_b128 a[184:187], v241 offset:45568\n"
"v_mfma_f32_16x16x32_bf16 a[120:123], a[248:251], v[20:23], a[120:123]\n"
"v_exp_f32 v3, v3\n"
"v_mfma_f32_16x16x32_bf16 a[124:127], a[252:255], v[20:23], a[124:127]\n"
"v_cvt_pk_bf16_f32 v17, v2, v3\n"
"v_mfma_f32_16x16x32_bf16 v[56:59], v[40:43], v[20:23], v[56:59]\n"
"v_exp_f32 v4, v4\n"
"v_mfma_f32_16x16x32_bf16 v[8:11], a[128:131], v[80:83], v[28:31]\n"
"ds_read_b128 a[188:191], v241 offset:45824\n"
"v_mfma_f32_16x16x32_bf16 v[8:11], a[132:135], v[84:87], v[8:11]\n"
"v_exp_f32 v5, v5\n"
"v_mfma_f32_16x16x32_bf16 v[8:11], a[136:139], v[88:91], v[8:11]\n"
"ds_read_b64_tr_b16 a[224:225], v244 offset:57344\n"
"v_cvt_pk_bf16_f32 v18, v4, v5\n"
"v_mfma_f32_16x16x32_bf16 v[8:11], a[140:143], v[92:95], v[8:11]\n"
"v_exp_f32 v6, v6\n"
"v_mfma_f32_16x16x32_bf16 v[12:15], a[144:147], v[80:83], v[28:31]\n"
"ds_read_b64_tr_b16 a[226:227], v244 offset:61440\n"
"v_mfma_f32_16x16x32_bf16 v[12:15], a[148:151], v[84:87], v[12:15]\n"
"v_exp_f32 v7, v7\n"
"v_mfma_f32_16x16x32_bf16 v[12:15], a[152:155], v[88:91], v[12:15]\n"
"ds_read_b64_tr_b16 a[228:229], v245 offset:57344\n"
"v_cvt_pk_bf16_f32 v19, v6, v7\n"
"v_mfma_f32_16x16x32_bf16 v[12:15], a[156:159], v[92:95], v[12:15]\n"
"s_waitcnt lgkmcnt(11)\n"
"buffer_load_dwordx4 v204, s[28:31], s52 offen offset:1024 lds\n"
"v_mfma_f32_16x16x32_bf16 a[0:3], a[192:195], v[16:19], a[0:3]\n"
"s_add_u32 s52, s52, 8192\n"
"v_exp_f32 v8, v8\n"
"v_mfma_f32_16x16x32_bf16 a[4:7], a[196:199], v[16:19], a[4:7]\n"
"ds_read_b64_tr_b16 a[230:231], v245 offset:61440\n"
"v_mfma_f32_16x16x32_bf16 a[8:11], a[200:203], v[16:19], a[8:11]\n"
"v_exp_f32 v9, v9\n"
"v_mfma_f32_16x16x32_bf16 a[12:15], a[204:207], v[16:19], a[12:15]\n"
"ds_read_b64_tr_b16 a[232:233], v244 offset:57600\n"
"v_cvt_pk_bf16_f32 v20, v8, v9\n"
"v_mfma_f32_16x16x32_bf16 a[16:19], a[208:211], v[16:19], a[16:19]\n"
"v_exp_f32 v10, v10\n"
"v_mfma_f32_16x16x32_bf16 a[20:23], a[212:215], v[16:19], a[20:23]\n"
"ds_read_b64_tr_b16 a[234:235], v244 offset:61696\n"
"v_mfma_f32_16x16x32_bf16 a[24:27], a[216:219], v[16:19], a[24:27]\n"
"v_exp_f32 v11, v11\n"
"v_mfma_f32_16x16x32_bf16 a[28:31], a[220:223], v[16:19], a[28:31]\n"
"v_cvt_pk_bf16_f32 v21, v10, v11\n"
"v_mfma_f32_16x16x32_bf16 v[44:47], v[40:43], v[16:19], v[44:47]\n"
"v_exp_f32 v12, v12\n"
"v_mfma_f32_16x16x32_bf16 v[0:3], a[128:131], v[96:99], v[32:35]\n"
"ds_read_b64_tr_b16 a[236:237], v245 offset:57600\n"
"v_mfma_f32_16x16x32_bf16 v[0:3], a[132:135], v[100:103], v[0:3]\n"
"v_exp_f32 v13, v13\n"
"v_mfma_f32_16x16x32_bf16 v[0:3], a[136:139], v[104:107], v[0:3]\n"
"ds_read_b64_tr_b16 a[238:239], v245 offset:61696\n"
"v_cvt_pk_bf16_f32 v22, v12, v13\n"
"v_mfma_f32_16x16x32_bf16 v[0:3], a[140:143], v[108:111], v[0:3]\n"
"v_exp_f32 v14, v14\n"
"v_mfma_f32_16x16x32_bf16 v[4:7], a[144:147], v[96:99], v[32:35]\n"
"ds_read_b64_tr_b16 a[240:241], v244 offset:57856\n"
"v_mfma_f32_16x16x32_bf16 v[4:7], a[148:151], v[100:103], v[4:7]\n"
"v_exp_f32 v15, v15\n"
"v_mfma_f32_16x16x32_bf16 v[4:7], a[152:155], v[104:107], v[4:7]\n"
"ds_read_b64_tr_b16 a[242:243], v244 offset:61952\n"
"v_cvt_pk_bf16_f32 v23, v14, v15\n"
"v_mfma_f32_16x16x32_bf16 v[4:7], a[156:159], v[108:111], v[4:7]\n"
"buffer_load_dwordx4 v204, s[28:31], s52 offen offset:2048 lds\n"
"v_mfma_f32_16x16x32_bf16 a[32:35], a[192:195], v[20:23], a[32:35]\n"
"s_add_u32 s52, s52, 8192\n"
"v_exp_f32 v0, v0\n"
"v_mfma_f32_16x16x32_bf16 a[36:39], a[196:199], v[20:23], a[36:39]\n"
"ds_read_b64_tr_b16 a[244:245], v245 offset:57856\n"
"v_mfma_f32_16x16x32_bf16 a[40:43], a[200:203], v[20:23], a[40:43]\n"
"v_exp_f32 v1, v1\n"
"v_mfma_f32_16x16x32_bf16 a[44:47], a[204:207], v[20:23], a[44:47]\n"
"ds_read_b64_tr_b16 a[246:247], v245 offset:61952\n"
"v_cvt_pk_bf16_f32 v16, v0, v1\n"
"v_mfma_f32_16x16x32_bf16 a[48:51], a[208:211], v[20:23], a[48:51]\n"
"v_exp_f32 v2, v2\n"
"v_mfma_f32_16x16x32_bf16 a[52:55], a[212:215], v[20:23], a[52:55]\n"
"ds_read_b64_tr_b16 a[248:249], v244 offset:58112\n"
"v_mfma_f32_16x16x32_bf16 a[56:59], a[216:219], v[20:23], a[56:59]\n"
"v_exp_f32 v3, v3\n"
"v_mfma_f32_16x16x32_bf16 a[60:63], a[220:223], v[20:23], a[60:63]\n"
"v_cvt_pk_bf16_f32 v17, v2, v3\n"
"v_mfma_f32_16x16x32_bf16 v[48:51], v[40:43], v[20:23], v[48:51]\n"
"v_exp_f32 v4, v4\n"
"v_mfma_f32_16x16x32_bf16 v[8:11], a[128:131], v[112:115], v[36:39]\n"
"ds_read_b64_tr_b16 a[250:251], v244 offset:62208\n"
"v_mfma_f32_16x16x32_bf16 v[8:11], a[132:135], v[116:119], v[8:11]\n"
"v_exp_f32 v5, v5\n"
"v_mfma_f32_16x16x32_bf16 v[8:11], a[136:139], v[120:123], v[8:11]\n"
"ds_read_b64_tr_b16 a[252:253], v245 offset:58112\n"
"v_cvt_pk_bf16_f32 v18, v4, v5\n"
"v_mfma_f32_16x16x32_bf16 v[8:11], a[140:143], v[124:127], v[8:11]\n"
"v_exp_f32 v6, v6\n"
"v_mfma_f32_16x16x32_bf16 v[12:15], a[144:147], v[112:115], v[36:39]\n"
"ds_read_b64_tr_b16 a[254:255], v245 offset:62208\n"
"v_mfma_f32_16x16x32_bf16 v[12:15], a[148:151], v[116:119], v[12:15]\n"
"v_exp_f32 v7, v7\n"
"v_mfma_f32_16x16x32_bf16 v[12:15], a[152:155], v[120:123], v[12:15]\n"
"v_cvt_pk_bf16_f32 v19, v6, v7\n"
"v_mfma_f32_16x16x32_bf16 v[12:15], a[156:159], v[124:127], v[12:15]\n"
"buffer_load_dwordx4 v204, s[28:31], s52 offen offset:3072 lds\n"
"v_mfma_f32_16x16x32_bf16 a[64:67], a[192:195], v[16:19], a[64:67]\n"
"s_sub_u32 s52, s52, 61312\n"
"v_exp_f32 v8, v8\n"
"v_mfma_f32_16x16x32_bf16 a[68:71], a[196:199], v[16:19], a[68:71]\n"
"s_nop 0\n"
"v_mfma_f32_16x16x32_bf16 a[72:75], a[200:203], v[16:19], a[72:75]\n"
"v_exp_f32 v9, v9\n"
"v_mfma_f32_16x16x32_bf16 a[76:79], a[204:207], v[16:19], a[76:79]\n"
"v_cvt_pk_bf16_f32 v20, v8, v9\n"
"v_mfma_f32_16x16x32_bf16 a[80:83], a[208:211], v[16:19], a[80:83]\n"
"v_exp_f32 v10, v10\n"
"v_mfma_f32_16x16x32_bf16 a[84:87], a[212:215], v[16:19], a[84:87]\n"
"s_nop 0\n"
"v_mfma_f32_16x16x32_bf16 a[88:91], a[216:219], v[16:19], a[88:91]\n"
"v_exp_f32 v11, v11\n"
"v_mfma_f32_16x16x32_bf16 a[92:95], a[220:223], v[16:19], a[92:95]\n"
"s_waitcnt vmcnt(20) lgkmcnt(0)\n"
"s_barrier\n"
"v_cvt_pk_bf16_f32 v21, v10, v11\n"
"v_mfma_f32_16x16x32_bf16 v[52:55], v[40:43], v[16:19], v[52:55]\n"
"v_exp_f32 v12, v12\n"
"v_mfma_f32_16x16x32_bf16 v[0:3], a[160:163], v[64:67], v[24:27]\n"
"ds_read_b64_tr_b16 a[128:129], v194 offset:0\n"
"v_mfma_f32_16x16x32_bf16 v[0:3], a[164:167], v[68:71], v[0:3]\n"
"v_exp_f32 v13, v13\n"
"v_mfma_f32_16x16x32_bf16 v[0:3], a[168:171], v[72:75], v[0:3]\n"
"ds_read_b64_tr_b16 a[130:131], v194 offset:2048\n"
"v_cvt_pk_bf16_f32 v22, v12, v13\n"
"v_mfma_f32_16x16x32_bf16 v[0:3], a[172:175], v[76:79], v[0:3]\n"
"v_exp_f32 v14, v14\n"
"v_mfma_f32_16x16x32_bf16 v[4:7], a[176:179], v[64:67], v[24:27]\n"
"ds_read_b64_tr_b16 a[132:133], v194 offset:4096\n"
"v_mfma_f32_16x16x32_bf16 v[4:7], a[180:183], v[68:71], v[4:7]\n"
"v_exp_f32 v15, v15\n"
"v_mfma_f32_16x16x32_bf16 v[4:7], a[184:187], v[72:75], v[4:7]\n"
"ds_read_b64_tr_b16 a[134:135], v194 offset:6144\n"
"v_cvt_pk_bf16_f32 v23, v14, v15\n"
"v_mfma_f32_16x16x32_bf16 v[4:7], a[188:191], v[76:79], v[4:7]\n"
"ds_read_b64_tr_b16 a[136:137], v194 offset:8192\n"
"v_mfma_f32_16x16x32_bf16 a[96:99], a[192:195], v[20:23], a[96:99]\n"
"v_exp_f32 v0, v0\n"
"v_mfma_f32_16x16x32_bf16 a[100:103], a[196:199], v[20:23], a[100:103]\n"
"ds_read_b64_tr_b16 a[138:139], v194 offset:10240\n"
"v_mfma_f32_16x16x32_bf16 a[104:107], a[200:203], v[20:23], a[104:107]\n"
"v_exp_f32 v1, v1\n"
"v_mfma_f32_16x16x32_bf16 a[108:111], a[204:207], v[20:23], a[108:111]\n"
"ds_read_b64_tr_b16 a[140:141], v194 offset:12288\n"
"v_cvt_pk_bf16_f32 v16, v0, v1\n"
"v_mfma_f32_16x16x32_bf16 a[112:115], a[208:211], v[20:23], a[112:115]\n"
"v_exp_f32 v2, v2\n"
"v_mfma_f32_16x16x32_bf16 a[116:119], a[212:215], v[20:23], a[116:119]\n"
"ds_read_b64_tr_b16 a[142:143], v194 offset:14336\n"
"v_mfma_f32_16x16x32_bf16 a[120:123], a[216:219], v[20:23], a[120:123]\n"
"v_exp_f32 v3, v3\n"
"v_mfma_f32_16x16x32_bf16 a[124:127], a[220:223], v[20:23], a[124:127]\n"
"v_cvt_pk_bf16_f32 v17, v2, v3\n"
"v_mfma_f32_16x16x32_bf16 v[56:59], v[40:43], v[20:23], v[56:59]\n"
"v_exp_f32 v4, v4\n"
"v_mfma_f32_16x16x32_bf16 v[8:11], a[160:163], v[80:83], v[28:31]\n"
"ds_read_b64_tr_b16 a[144:145], v194 offset:16384\n"
"v_mfma_f32_16x16x32_bf16 v[8:11], a[164:167], v[84:87], v[8:11]\n"
"v_exp_f32 v5, v5\n"
"v_mfma_f32_16x16x32_bf16 v[8:11], a[168:171], v[88:91], v[8:11]\n"
"ds_read_b64_tr_b16 a[146:147], v194 offset:18432\n"
"v_cvt_pk_bf16_f32 v18, v4, v5\n"
"v_mfma_f32_16x16x32_bf16 v[8:11], a[172:175], v[92:95], v[8:11]\n"
"v_exp_f32 v6, v6\n"
"v_mfma_f32_16x16x32_bf16 v[12:15], a[176:179], v[80:83], v[28:31]\n"
"ds_read_b64_tr_b16 a[148:149], v194 offset:20480\n"
"v_mfma_f32_16x16x32_bf16 v[12:15], a[180:183], v[84:87], v[12:15]\n"
"v_exp_f32 v7, v7\n"
"v_mfma_f32_16x16x32_bf16 v[12:15], a[184:187], v[88:91], v[12:15]\n"
"ds_read_b64_tr_b16 a[150:151], v194 offset:22528\n"
"v_cvt_pk_bf16_f32 v19, v6, v7\n"
"v_mfma_f32_16x16x32_bf16 v[12:15], a[188:191], v[92:95], v[12:15]\n"
"ds_read_b64_tr_b16 a[152:153], v194 offset:24576\n"
"v_mfma_f32_16x16x32_bf16 a[0:3], a[224:227], v[16:19], a[0:3]\n"
"v_exp_f32 v8, v8\n"
"v_mfma_f32_16x16x32_bf16 a[4:7], a[228:231], v[16:19], a[4:7]\n"
"ds_read_b64_tr_b16 a[154:155], v194 offset:26624\n"
"v_mfma_f32_16x16x32_bf16 a[8:11], a[232:235], v[16:19], a[8:11]\n"
"v_exp_f32 v9, v9\n"
"v_mfma_f32_16x16x32_bf16 a[12:15], a[236:239], v[16:19], a[12:15]\n"
"ds_read_b64_tr_b16 a[156:157], v194 offset:28672\n"
"v_cvt_pk_bf16_f32 v20, v8, v9\n"
"v_mfma_f32_16x16x32_bf16 a[16:19], a[240:243], v[16:19], a[16:19]\n"
"v_exp_f32 v10, v10\n"
"v_mfma_f32_16x16x32_bf16 a[20:23], a[244:247], v[16:19], a[20:23]\n"
"ds_read_b64_tr_b16 a[158:159], v194 offset:30720\n"
"v_mfma_f32_16x16x32_bf16 a[24:27], a[248:251], v[16:19], a[24:27]\n"
"v_exp_f32 v11, v11\n"
"v_mfma_f32_16x16x32_bf16 a[28:31], a[252:255], v[16:19], a[28:31]\n"
"v_cvt_pk_bf16_f32 v21, v10, v11\n"
"v_mfma_f32_16x16x32_bf16 v[44:47], v[40:43], v[16:19], v[44:47]\n"
"v_exp_f32 v12, v12\n"
"v_mfma_f32_16x16x32_bf16 v[0:3], a[160:163], v[96:99], v[32:35]\n"
"ds_read_b64_tr_b16 a[192:193], v197 offset:0\n"
"v_mfma_f32_16x16x32_bf16 v[0:3], a[164:167], v[100:103], v[0:3]\n"
"v_exp_f32 v13, v13\n"
"v_mfma_f32_16x16x32_bf16 v[0:3], a[168:171], v[104:107], v[0:3]\n"
"ds_read_b64_tr_b16 a[194:195], v197 offset:2048\n"
"v_cvt_pk_bf16_f32 v22, v12, v13\n"
"v_mfma_f32_16x16x32_bf16 v[0:3], a[172:175], v[108:111], v[0:3]\n"
"v_exp_f32 v14, v14\n"
"v_mfma_f32_16x16x32_bf16 v[4:7], a[176:179], v[96:99], v[32:35]\n"
"ds_read_b64_tr_b16 a[196:197], v197 offset:4096\n"
"v_mfma_f32_16x16x32_bf16 v[4:7], a[180:183], v[100:103], v[4:7]\n"
"v_exp_f32 v15, v15\n"
"v_mfma_f32_16x16x32_bf16 v[4:7], a[184:187], v[104:107], v[4:7]\n"
"ds_read_b64_tr_b16 a[198:199], v197 offset:6144\n"
"v_cvt_pk_bf16_f32 v23, v14, v15\n"
"v_mfma_f32_16x16x32_bf16 v[4:7], a[188:191], v[108:111], v[4:7]\n"
"ds_read_b64_tr_b16 a[200:201], v197 offset:8192\n"
"v_mfma_f32_16x16x32_bf16 a[32:35], a[224:227], v[20:23], a[32:35]\n"
"v_exp_f32 v0, v0\n"
"v_mfma_f32_16x16x32_bf16 a[36:39], a[228:231], v[20:23], a[36:39]\n"
"ds_read_b64_tr_b16 a[202:203], v197 offset:10240\n"
"v_mfma_f32_16x16x32_bf16 a[40:43], a[232:235], v[20:23], a[40:43]\n"
"v_exp_f32 v1, v1\n"
"v_mfma_f32_16x16x32_bf16 a[44:47], a[236:239], v[20:23], a[44:47]\n"
"ds_read_b64_tr_b16 a[204:205], v197 offset:12288\n"
"v_cvt_pk_bf16_f32 v16, v0, v1\n"
"v_mfma_f32_16x16x32_bf16 a[48:51], a[240:243], v[20:23], a[48:51]\n"
"v_exp_f32 v2, v2\n"
"v_mfma_f32_16x16x32_bf16 a[52:55], a[244:247], v[20:23], a[52:55]\n"
"ds_read_b64_tr_b16 a[206:207], v197 offset:14336\n"
"v_mfma_f32_16x16x32_bf16 a[56:59], a[248:251], v[20:23], a[56:59]\n"
"v_exp_f32 v3, v3\n"
"v_mfma_f32_16x16x32_bf16 a[60:63], a[252:255], v[20:23], a[60:63]\n"
"v_cvt_pk_bf16_f32 v17, v2, v3\n"
"v_mfma_f32_16x16x32_bf16 v[48:51], v[40:43], v[20:23], v[48:51]\n"
"v_exp_f32 v4, v4\n"
"v_mfma_f32_16x16x32_bf16 v[8:11], a[160:163], v[112:115], v[36:39]\n"
"s_nop 0\n"
"v_mfma_f32_16x16x32_bf16 v[8:11], a[164:167], v[116:119], v[8:11]\n"
"v_exp_f32 v5, v5\n"
"v_mfma_f32_16x16x32_bf16 v[8:11], a[168:171], v[120:123], v[8:11]\n"
"v_cvt_pk_bf16_f32 v18, v4, v5\n"
"v_mfma_f32_16x16x32_bf16 v[8:11], a[172:175], v[124:127], v[8:11]\n"
"v_exp_f32 v6, v6\n"
"v_mfma_f32_16x16x32_bf16 v[12:15], a[176:179], v[112:115], v[36:39]\n"
"s_nop 0\n"
"v_mfma_f32_16x16x32_bf16 v[12:15], a[180:183], v[116:119], v[12:15]\n"
"v_exp_f32 v7, v7\n"
"v_mfma_f32_16x16x32_bf16 v[12:15], a[184:187], v[120:123], v[12:15]\n"
"v_cvt_pk_bf16_f32 v19, v6, v7\n"
"v_mfma_f32_16x16x32_bf16 v[12:15], a[188:191], v[124:127], v[12:15]\n"
"s_nop 0\n"
"v_mfma_f32_16x16x32_bf16 a[64:67], a[224:227], v[16:19], a[64:67]\n"
"v_exp_f32 v8, v8\n"
"v_mfma_f32_16x16x32_bf16 a[68:71], a[228:231], v[16:19], a[68:71]\n"
"s_nop 0\n"
"v_mfma_f32_16x16x32_bf16 a[72:75], a[232:235], v[16:19], a[72:75]\n"
"v_exp_f32 v9, v9\n"
"v_mfma_f32_16x16x32_bf16 a[76:79], a[236:239], v[16:19], a[76:79]\n"
"v_cvt_pk_bf16_f32 v20, v8, v9\n"
"v_mfma_f32_16x16x32_bf16 a[80:83], a[240:243], v[16:19], a[80:83]\n"
"v_exp_f32 v10, v10\n"
"v_mfma_f32_16x16x32_bf16 a[84:87], a[244:247], v[16:19], a[84:87]\n"
"s_nop 0\n"
"v_mfma_f32_16x16x32_bf16 a[88:91], a[248:251], v[16:19], a[88:91]\n"
"v_exp_f32 v11, v11\n"
"v_mfma_f32_16x16x32_bf16 a[92:95], a[252:255], v[16:19], a[92:95]\n"
"v_cvt_pk_bf16_f32 v21, v10, v11\n"
"v_mfma_f32_16x16x32_bf16 v[52:55], v[40:43], v[16:19], v[52:55]\n"
"v_exp_f32 v12, v12\n"
"s_nop 0\n"
"v_exp_f32 v13, v13\n"
"s_nop 0\n"
"v_cvt_pk_bf16_f32 v22, v12, v13\n"
"s_nop 0\n"
"v_exp_f32 v14, v14\n"
"s_nop 0\n"
"s_nop 0\n"
"v_exp_f32 v15, v15\n"
"s_nop 0\n"
"v_cvt_pk_bf16_f32 v23, v14, v15\n"
"s_nop 0\n"
"s_nop 0\n"
"s_nop 1\n"
"v_mfma_f32_16x16x32_bf16 a[96:99], a[224:227], v[20:23], a[96:99]\n"
"v_mfma_f32_16x16x32_bf16 a[100:103], a[228:231], v[20:23], a[100:103]\n"
"v_mfma_f32_16x16x32_bf16 a[104:107], a[232:235], v[20:23], a[104:107]\n"
"v_mfma_f32_16x16x32_bf16 a[108:111], a[236:239], v[20:23], a[108:111]\n"
"v_mfma_f32_16x16x32_bf16 a[112:115], a[240:243], v[20:23], a[112:115]\n"
"v_mfma_f32_16x16x32_bf16 a[116:119], a[244:247], v[20:23], a[116:119]\n"
"v_mfma_f32_16x16x32_bf16 a[120:123], a[248:251], v[20:23], a[120:123]\n"
"v_mfma_f32_16x16x32_bf16 a[124:127], a[252:255], v[20:23], a[124:127]\n"
"v_mfma_f32_16x16x32_bf16 v[56:59], v[40:43], v[20:23], v[56:59]\n"
"s_nop 7\n"
"v_mov_b32 v213, 0x5f800000\n"
"v_cmp_nle_f32 s[56:57], v44, v213\n"
"v_cmp_nle_f32 s[58:59], v48, v213\n"
"v_cmp_nle_f32 s[60:61], v52, v213\n"
"v_cmp_nle_f32 s[62:63], v56, v213\n"
"v_rcp_f32 v212, v44\n"
"v_rcp_f32 v214, v48\n"
"v_rcp_f32 v216, v52\n"
"v_rcp_f32 v218, v56\n"
"s_or_b64 s[56:57], s[56:57], s[58:59]\n"
"s_or_b64 s[60:61], s[60:61], s[62:63]\n"
"s_or_b64 s[56:57], s[56:57], s[60:61]\n"
"s_cselect_b32 s56, 1, 0\n"
"s_cmp_eq_u32 s45, 0\n"
"s_cselect_b32 s56, s56, 0\n"
"s_lshl_b32 s57, s41, 2\n"
"s_add_u32 s57, s57, s40\n"
"s_add_u32 s57, s57, 98304\n"
"v_mov_b32 v236, s56\n"
"v_mov_b32 v237, s57\n"
"ds_write_b32 v237, v236\n"
"v_accvgpr_read_b32 v220, a0\n"
"v_accvgpr_read_b32 v221, a1\n"
"v_accvgpr_read_b32 v222, a2\n"
"v_accvgpr_read_b32 v223, a3\n"
"v_pk_mul_f32 v[220:221], v[220:221], v[212:213] op_sel_hi:[1,0]\n"
"v_pk_mul_f32 v[222:223], v[222:223], v[212:213] op_sel_hi:[1,0]\n"
"v_cvt_pk_bf16_f32 v128, v220, v221\n"
"v_cvt_pk_bf16_f32 v129, v222, v223\n"
"v_accvgpr_read_b32 v224, a4\n"
"v_accvgpr_read_b32 v225, a5\n"
"v_accvgpr_read_b32 v226, a6\n"
"v_accvgpr_read_b32 v227, a7\n"
"v_pk_mul_f32 v[224:225], v[224:225], v[212:213] op_sel_hi:[1,0]\n"
"v_pk_mul_f32 v[226:227], v[226:227], v[212:213] op_sel_hi:[1,0]\n"
"v_cvt_pk_bf16_f32 v130, v224, v225\n"
"v_cvt_pk_bf16_f32 v131, v226, v227\n"
"v_accvgpr_read_b32 v228, a8\n"
"v_accvgpr_read_b32 v229, a9\n"
"v_accvgpr_read_b32 v230, a10\n"
"v_accvgpr_read_b32 v231, a11\n"
"v_pk_mul_f32 v[228:229], v[228:229], v[212:213] op_sel_hi:[1,0]\n"
"v_pk_mul_f32 v[230:231], v[230:231], v[212:213] op_sel_hi:[1,0]\n"
"v_cvt_pk_bf16_f32 v132, v228, v229\n"
"v_cvt_pk_bf16_f32 v133, v230, v231\n"
"v_accvgpr_read_b32 v232, a12\n"
"v_accvgpr_read_b32 v233, a13\n"
"v_accvgpr_read_b32 v234, a14\n"
"v_accvgpr_read_b32 v235, a15\n"
"v_pk_mul_f32 v[232:233], v[232:233], v[212:213] op_sel_hi:[1,0]\n"
"v_pk_mul_f32 v[234:235], v[234:235], v[212:213] op_sel_hi:[1,0]\n"
"v_cvt_pk_bf16_f32 v134, v232, v233\n"
"v_cvt_pk_bf16_f32 v135, v234, v235\n"
"v_accvgpr_read_b32 v220, a16\n"
"v_accvgpr_read_b32 v221, a17\n"
"v_accvgpr_read_b32 v222, a18\n"
"v_accvgpr_read_b32 v223, a19\n"
"v_pk_mul_f32 v[220:221], v[220:221], v[212:213] op_sel_hi:[1,0]\n"
"v_pk_mul_f32 v[222:223], v[222:223], v[212:213] op_sel_hi:[1,0]\n"
"v_cvt_pk_bf16_f32 v136, v220, v221\n"
"v_cvt_pk_bf16_f32 v137, v222, v223\n"
"v_accvgpr_read_b32 v224, a20\n"
"v_accvgpr_read_b32 v225, a21\n"
"v_accvgpr_read_b32 v226, a22\n"
"v_accvgpr_read_b32 v227, a23\n"
"v_pk_mul_f32 v[224:225], v[224:225], v[212:213] op_sel_hi:[1,0]\n"
"v_pk_mul_f32 v[226:227], v[226:227], v[212:213] op_sel_hi:[1,0]\n"
"v_cvt_pk_bf16_f32 v138, v224, v225\n"
"v_cvt_pk_bf16_f32 v139, v226, v227\n"
"v_accvgpr_read_b32 v228, a24\n"
"v_accvgpr_read_b32 v229, a25\n"
"v_accvgpr_read_b32 v230, a26\n"
"v_accvgpr_read_b32 v231, a27\n"
"v_pk_mul_f32 v[228:229], v[228:229], v[212:213] op_sel_hi:[1,0]\n"
"v_pk_mul_f32 v[230:231], v[230:231], v[212:213] op_sel_hi:[1,0]\n"
"v_cvt_pk_bf16_f32 v140, v228, v229\n"
"v_cvt_pk_bf16_f32 v141, v230, v231\n"
"v_accvgpr_read_b32 v232, a28\n"
"v_accvgpr_read_b32 v233, a29\n"
"v_accvgpr_read_b32 v234, a30\n"
"v_accvgpr_read_b32 v235, a31\n"
"v_pk_mul_f32 v[232:233], v[232:233], v[212:213] op_sel_hi:[1,0]\n"
"v_pk_mul_f32 v[234:235], v[234:235], v[212:213] op_sel_hi:[1,0]\n"
"v_cvt_pk_bf16_f32 v142, v232, v233\n"
"v_cvt_pk_bf16_f32 v143, v234, v235\n"
"v_accvgpr_read_b32 v220, a32\n"
"v_accvgpr_read_b32 v221, a33\n"
"v_accvgpr_read_b32 v222, a34\n"
"v_accvgpr_read_b32 v223, a35\n"
"v_pk_mul_f32 v[220:221], v[220:221], v[214:215] op_sel_hi:[1,0]\n"
"v_pk_mul_f32 v[222:223], v[222:223], v[214:215] op_sel_hi:[1,0]\n"
"v_cvt_pk_bf16_f32 v144, v220, v221\n"
"v_cvt_pk_bf16_f32 v145, v222, v223\n"
"v_accvgpr_read_b32 v224, a36\n"
"v_accvgpr_read_b32 v225, a37\n"
"v_accvgpr_read_b32 v226, a38\n"
"v_accvgpr_read_b32 v227, a39\n"
"v_pk_mul_f32 v[224:225], v[224:225], v[214:215] op_sel_hi:[1,0]\n"
"v_pk_mul_f32 v[226:227], v[226:227], v[214:215] op_sel_hi:[1,0]\n"
"v_cvt_pk_bf16_f32 v146, v224, v225\n"
"v_cvt_pk_bf16_f32 v147, v226, v227\n"
"v_accvgpr_read_b32 v228, a40\n"
"v_accvgpr_read_b32 v229, a41\n"
"v_accvgpr_read_b32 v230, a42\n"
"v_accvgpr_read_b32 v231, a43\n"
"v_pk_mul_f32 v[228:229], v[228:229], v[214:215] op_sel_hi:[1,0]\n"
"v_pk_mul_f32 v[230:231], v[230:231], v[214:215] op_sel_hi:[1,0]\n"
"v_cvt_pk_bf16_f32 v148, v228, v229\n"
"v_cvt_pk_bf16_f32 v149, v230, v231\n"
"v_accvgpr_read_b32 v232, a44\n"
"v_accvgpr_read_b32 v233, a45\n"
"v_accvgpr_read_b32 v234, a46\n"
"v_accvgpr_read_b32 v235, a47\n"
"v_pk_mul_f32 v[232:233], v[232:233], v[214:215] op_sel_hi:[1,0]\n"
"v_pk_mul_f32 v[234:235], v[234:235], v[214:215] op_sel_hi:[1,0]\n"
"v_cvt_pk_bf16_f32 v150, v232, v233\n"
"v_cvt_pk_bf16_f32 v151, v234, v235\n"
"v_accvgpr_read_b32 v220, a48\n"
"v_accvgpr_read_b32 v221, a49\n"
"v_accvgpr_read_b32 v222, a50\n"
"v_accvgpr_read_b32 v223, a51\n"
"v_pk_mul_f32 v[220:221], v[220:221], v[214:215] op_sel_hi:[1,0]\n"
"v_pk_mul_f32 v[222:223], v[222:223], v[214:215] op_sel_hi:[1,0]\n"
"v_cvt_pk_bf16_f32 v152, v220, v221\n"
"v_cvt_pk_bf16_f32 v153, v222, v223\n"
"v_accvgpr_read_b32 v224, a52\n"
"v_accvgpr_read_b32 v225, a53\n"
"v_accvgpr_read_b32 v226, a54\n"
"v_accvgpr_read_b32 v227, a55\n"
"v_pk_mul_f32 v[224:225], v[224:225], v[214:215] op_sel_hi:[1,0]\n"
"v_pk_mul_f32 v[226:227], v[226:227], v[214:215] op_sel_hi:[1,0]\n"
"v_cvt_pk_bf16_f32 v154, v224, v225\n"
"v_cvt_pk_bf16_f32 v155, v226, v227\n"
"v_accvgpr_read_b32 v228, a56\n"
"v_accvgpr_read_b32 v229, a57\n"
"v_accvgpr_read_b32 v230, a58\n"
"v_accvgpr_read_b32 v231, a59\n"
"v_pk_mul_f32 v[228:229], v[228:229], v[214:215] op_sel_hi:[1,0]\n"
"v_pk_mul_f32 v[230:231], v[230:231], v[214:215] op_sel_hi:[1,0]\n"
"v_cvt_pk_bf16_f32 v156, v228, v229\n"
"v_cvt_pk_bf16_f32 v157, v230, v231\n"
"v_accvgpr_read_b32 v232, a60\n"
"v_accvgpr_read_b32 v233, a61\n"
"v_accvgpr_read_b32 v234, a62\n"
"v_accvgpr_read_b32 v235, a63\n"
"v_pk_mul_f32 v[232:233], v[232:233], v[214:215] op_sel_hi:[1,0]\n"
"v_pk_mul_f32 v[234:235], v[234:235], v[214:215] op_sel_hi:[1,0]\n"
"v_cvt_pk_bf16_f32 v158, v232, v233\n"
"v_cvt_pk_bf16_f32 v159, v234, v235\n"
"v_accvgpr_read_b32 v220, a64\n"
"v_accvgpr_read_b32 v221, a65\n"
"v_accvgpr_read_b32 v222, a66\n"
"v_accvgpr_read_b32 v223, a67\n"
"v_pk_mul_f32 v[220:221], v[220:221], v[216:217] op_sel_hi:[1,0]\n"
"v_pk_mul_f32 v[222:223], v[222:223], v[216:217] op_sel_hi:[1,0]\n"
"v_cvt_pk_bf16_f32 v160, v220, v221\n"
"v_cvt_pk_bf16_f32 v161, v222, v223\n"
"v_accvgpr_read_b32 v224, a68\n"
"v_accvgpr_read_b32 v225, a69\n"
"v_accvgpr_read_b32 v226, a70\n"
"v_accvgpr_read_b32 v227, a71\n"
"v_pk_mul_f32 v[224:225], v[224:225], v[216:217] op_sel_hi:[1,0]\n"
"v_pk_mul_f32 v[226:227], v[226:227], v[216:217] op_sel_hi:[1,0]\n"
"v_cvt_pk_bf16_f32 v162, v224, v225\n"
"v_cvt_pk_bf16_f32 v163, v226, v227\n"
"v_accvgpr_read_b32 v228, a72\n"
"v_accvgpr_read_b32 v229, a73\n"
"v_accvgpr_read_b32 v230, a74\n"
"v_accvgpr_read_b32 v231, a75\n"
"v_pk_mul_f32 v[228:229], v[228:229], v[216:217] op_sel_hi:[1,0]\n"
"v_pk_mul_f32 v[230:231], v[230:231], v[216:217] op_sel_hi:[1,0]\n"
"v_cvt_pk_bf16_f32 v164, v228, v229\n"
"v_cvt_pk_bf16_f32 v165, v230, v231\n"
"v_accvgpr_read_b32 v232, a76\n"
"v_accvgpr_read_b32 v233, a77\n"
"v_accvgpr_read_b32 v234, a78\n"
"v_accvgpr_read_b32 v235, a79\n"
"v_pk_mul_f32 v[232:233], v[232:233], v[216:217] op_sel_hi:[1,0]\n"
"v_pk_mul_f32 v[234:235], v[234:235], v[216:217] op_sel_hi:[1,0]\n"
"v_cvt_pk_bf16_f32 v166, v232, v233\n"
"v_cvt_pk_bf16_f32 v167, v234, v235\n"
"v_accvgpr_read_b32 v220, a80\n"
"v_accvgpr_read_b32 v221, a81\n"
"v_accvgpr_read_b32 v222, a82\n"
"v_accvgpr_read_b32 v223, a83\n"
"v_pk_mul_f32 v[220:221], v[220:221], v[216:217] op_sel_hi:[1,0]\n"
"v_pk_mul_f32 v[222:223], v[222:223], v[216:217] op_sel_hi:[1,0]\n"
"v_cvt_pk_bf16_f32 v168, v220, v221\n"
"v_cvt_pk_bf16_f32 v169, v222, v223\n"
"v_accvgpr_read_b32 v224, a84\n"
"v_accvgpr_read_b32 v225, a85\n"
"v_accvgpr_read_b32 v226, a86\n"
"v_accvgpr_read_b32 v227, a87\n"
"v_pk_mul_f32 v[224:225], v[224:225], v[216:217] op_sel_hi:[1,0]\n"
"v_pk_mul_f32 v[226:227], v[226:227], v[216:217] op_sel_hi:[1,0]\n"
"v_cvt_pk_bf16_f32 v170, v224, v225\n"
"v_cvt_pk_bf16_f32 v171, v226, v227\n"
"v_accvgpr_read_b32 v228, a88\n"
"v_accvgpr_read_b32 v229, a89\n"
"v_accvgpr_read_b32 v230, a90\n"
"v_accvgpr_read_b32 v231, a91\n"
"v_pk_mul_f32 v[228:229], v[228:229], v[216:217] op_sel_hi:[1,0]\n"
"v_pk_mul_f32 v[230:231], v[230:231], v[216:217] op_sel_hi:[1,0]\n"
"v_cvt_pk_bf16_f32 v172, v228, v229\n"
"v_cvt_pk_bf16_f32 v173, v230, v231\n"
"v_accvgpr_read_b32 v232, a92\n"
"v_accvgpr_read_b32 v233, a93\n"
"v_accvgpr_read_b32 v234, a94\n"
"v_accvgpr_read_b32 v235, a95\n"
"v_pk_mul_f32 v[232:233], v[232:233], v[216:217] op_sel_hi:[1,0]\n"
"v_pk_mul_f32 v[234:235], v[234:235], v[216:217] op_sel_hi:[1,0]\n"
"v_cvt_pk_bf16_f32 v174, v232, v233\n"
"v_cvt_pk_bf16_f32 v175, v234, v235\n"
"v_accvgpr_read_b32 v220, a96\n"
"v_accvgpr_read_b32 v221, a97\n"
"v_accvgpr_read_b32 v222, a98\n"
"v_accvgpr_read_b32 v223, a99\n"
"v_pk_mul_f32 v[220:221], v[220:221], v[218:219] op_sel_hi:[1,0]\n"
"v_pk_mul_f32 v[222:223], v[222:223], v[218:219] op_sel_hi:[1,0]\n"
"v_cvt_pk_bf16_f32 v176, v220, v221\n"
"v_cvt_pk_bf16_f32 v177, v222, v223\n"
"v_accvgpr_read_b32 v224, a100\n"
"v_accvgpr_read_b32 v225, a101\n"
"v_accvgpr_read_b32 v226, a102\n"
"v_accvgpr_read_b32 v227, a103\n"
"v_pk_mul_f32 v[224:225], v[224:225], v[218:219] op_sel_hi:[1,0]\n"
"v_pk_mul_f32 v[226:227], v[226:227], v[218:219] op_sel_hi:[1,0]\n"
"v_cvt_pk_bf16_f32 v178, v224, v225\n"
"v_cvt_pk_bf16_f32 v179, v226, v227\n"
"v_accvgpr_read_b32 v228, a104\n"
"v_accvgpr_read_b32 v229, a105\n"
"v_accvgpr_read_b32 v230, a106\n"
"v_accvgpr_read_b32 v231, a107\n"
"v_pk_mul_f32 v[228:229], v[228:229], v[218:219] op_sel_hi:[1,0]\n"
"v_pk_mul_f32 v[230:231], v[230:231], v[218:219] op_sel_hi:[1,0]\n"
"v_cvt_pk_bf16_f32 v180, v228, v229\n"
"v_cvt_pk_bf16_f32 v181, v230, v231\n"
"v_accvgpr_read_b32 v232, a108\n"
"v_accvgpr_read_b32 v233, a109\n"
"v_accvgpr_read_b32 v234, a110\n"
"v_accvgpr_read_b32 v235, a111\n"
"v_pk_mul_f32 v[232:233], v[232:233], v[218:219] op_sel_hi:[1,0]\n"
"v_pk_mul_f32 v[234:235], v[234:235], v[218:219] op_sel_hi:[1,0]\n"
"v_cvt_pk_bf16_f32 v182, v232, v233\n"
"v_cvt_pk_bf16_f32 v183, v234, v235\n"
"v_accvgpr_read_b32 v220, a112\n"
"v_accvgpr_read_b32 v221, a113\n"
"v_accvgpr_read_b32 v222, a114\n"
"v_accvgpr_read_b32 v223, a115\n"
"v_pk_mul_f32 v[220:221], v[220:221], v[218:219] op_sel_hi:[1,0]\n"
"v_pk_mul_f32 v[222:223], v[222:223], v[218:219] op_sel_hi:[1,0]\n"
"v_cvt_pk_bf16_f32 v184, v220, v221\n"
"v_cvt_pk_bf16_f32 v185, v222, v223\n"
"v_accvgpr_read_b32 v224, a116\n"
"v_accvgpr_read_b32 v225, a117\n"
"v_accvgpr_read_b32 v226, a118\n"
"v_accvgpr_read_b32 v227, a119\n"
"v_pk_mul_f32 v[224:225], v[224:225], v[218:219] op_sel_hi:[1,0]\n"
"v_pk_mul_f32 v[226:227], v[226:227], v[218:219] op_sel_hi:[1,0]\n"
"v_cvt_pk_bf16_f32 v186, v224, v225\n"
"v_cvt_pk_bf16_f32 v187, v226, v227\n"
"v_accvgpr_read_b32 v228, a120\n"
"v_accvgpr_read_b32 v229, a121\n"
"v_accvgpr_read_b32 v230, a122\n"
"v_accvgpr_read_b32 v231, a123\n"
"v_pk_mul_f32 v[228:229], v[228:229], v[218:219] op_sel_hi:[1,0]\n"
"v_pk_mul_f32 v[230:231], v[230:231], v[218:219] op_sel_hi:[1,0]\n"
"v_cvt_pk_bf16_f32 v188, v228, v229\n"
"v_cvt_pk_bf16_f32 v189, v230, v231\n"
"v_accvgpr_read_b32 v232, a124\n"
"v_accvgpr_read_b32 v233, a125\n"
"v_accvgpr_read_b32 v234, a126\n"
"v_accvgpr_read_b32 v235, a127\n"
"v_pk_mul_f32 v[232:233], v[232:233], v[218:219] op_sel_hi:[1,0]\n"
"v_pk_mul_f32 v[234:235], v[234:235], v[218:219] op_sel_hi:[1,0]\n"
"v_cvt_pk_bf16_f32 v190, v232, v233\n"
"v_cvt_pk_bf16_f32 v191, v234, v235\n"
"s_waitcnt lgkmcnt(0)\n"
"s_nop 0\n"
"v_mfma_f32_16x16x32_bf16 v[0:3], a[128:131], v[128:131], 0\n"
"ds_read_b64_tr_b16 a[208:209], v197 offset:16384\n"
"v_mfma_f32_16x16x32_bf16 v[0:3], a[132:135], v[132:135], v[0:3]\n"
"ds_read_b64_tr_b16 a[210:211], v197 offset:18432\n"
"v_mfma_f32_16x16x32_bf16 v[0:3], a[136:139], v[136:139], v[0:3]\n"
"ds_read_b64_tr_b16 a[212:213], v197 offset:20480\n"
"v_mfma_f32_16x16x32_bf16 v[0:3], a[140:143], v[140:143], v[0:3]\n"
"ds_read_b64_tr_b16 a[214:215], v197 offset:22528\n"
"v_mfma_f32_16x16x32_bf16 v[0:3], a[144:147], v[144:147], v[0:3]\n"
"s_waitcnt vmcnt(20)\n"
"s_barrier\n"
"ds_read_b64_tr_b16 a[216:217], v197 offset:24576\n"
"v_mfma_f32_16x16x32_bf16 v[0:3], a[148:151], v[148:151], v[0:3]\n"
"ds_read_b64_tr_b16 a[218:219], v197 offset:26624\n"
"ds_read_b32 v236, v207\n"
"v_mfma_f32_16x16x32_bf16 v[0:3], a[152:155], v[152:155], v[0:3]\n"
"ds_read_b64_tr_b16 a[220:221], v197 offset:28672\n"
"v_mfma_f32_16x16x32_bf16 v[0:3], a[156:159], v[156:159], v[0:3]\n"
"ds_read_b64_tr_b16 a[222:223], v197 offset:30720\n"
"v_mfma_f32_16x16x32_bf16 v[4:7], a[128:131], v[160:163], 0\n"
"ds_read_b64_tr_b16 a[160:161], v194 offset:256\n"
"v_mfma_f32_16x16x32_bf16 v[4:7], a[132:135], v[164:167], v[4:7]\n"
"ds_read_b64_tr_b16 a[162:163], v194 offset:2304\n"
"v_mfma_f32_16x16x32_bf16 v[4:7], a[136:139], v[168:171], v[4:7]\n"
"ds_read_b64_tr_b16 a[164:165], v194 offset:4352\n"
"v_mfma_f32_16x16x32_bf16 v[4:7], a[140:143], v[172:175], v[4:7]\n"
"ds_read_b64_tr_b16 a[166:167], v194 offset:6400\n"
"v_mfma_f32_16x16x32_bf16 v[4:7], a[144:147], v[176:179], v[4:7]\n"
"ds_read_b64_tr_b16 a[168:169], v194 offset:8448\n"
"v_cvt_pk_bf16_f32 v32, v0, v1\n"
"v_mfma_f32_16x16x32_bf16 v[4:7], a[148:151], v[180:183], v[4:7]\n"
"ds_read_b64_tr_b16 a[170:171], v194 offset:10496\n"
"v_cvt_pk_bf16_f32 v33, v2, v3\n"
"v_mfma_f32_16x16x32_bf16 v[4:7], a[152:155], v[184:187], v[4:7]\n"
"ds_read_b64_tr_b16 a[172:173], v194 offset:12544\n"
"v_mfma_f32_16x16x32_bf16 v[4:7], a[156:159], v[188:191], v[4:7]\n"
"ds_read_b64_tr_b16 a[174:175], v194 offset:14592\n"
"v_mfma_f32_16x16x32_bf16 v[8:11], a[192:195], v[128:131], 0\n"
"s_waitcnt lgkmcnt(10)\n"
"ds_read_b64_tr_b16 a[176:177], v194 offset:16640\n"
"v_cmp_ne_u32 vcc, 0, v236\n"
"v_mfma_f32_16x16x32_bf16 v[8:11], a[196:199], v[132:135], v[8:11]\n"
"ds_read_b64_tr_b16 a[178:179], v194 offset:18688\n"
"v_mfma_f32_16x16x32_bf16 v[8:11], a[200:203], v[136:139], v[8:11]\n"
"s_cbranch_vccnz AT_fb\n"
"ds_read_b64_tr_b16 a[180:181], v194 offset:20736\n"
"v_mfma_f32_16x16x32_bf16 v[8:11], a[204:207], v[140:143], v[8:11]\n"
"ds_read_b64_tr_b16 a[182:183], v194 offset:22784\n"
"v_mfma_f32_16x16x32_bf16 v[8:11], a[208:211], v[144:147], v[8:11]\n"
"ds_read_b64_tr_b16 a[184:185], v194 offset:24832\n"
"v_cvt_pk_bf16_f32 v36, v4, v5\n"
"v_mfma_f32_16x16x32_bf16 v[8:11], a[212:215], v[148:151], v[8:11]\n"
"ds_read_b64_tr_b16 a[186:187], v194 offset:26880\n"
"v_cvt_pk_bf16_f32 v37, v6, v7\n"
"v_mfma_f32_16x16x32_bf16 v[8:11], a[216:219], v[152:155], v[8:11]\n"
"s_waitcnt lgkmcnt(11)\n"
"ds_read_b64_tr_b16 a[188:189], v194 offset:28928\n"
"v_mfma_f32_16x16x32_bf16 v[8:11], a[220:223], v[156:159], v[8:11]\n"
"ds_read_b64_tr_b16 a[190:191], v194 offset:30976\n"
"v_mfma_f32_16x16x32_bf16 v[12:15], a[192:195], v[160:163], 0\n"
"ds_read_b64_tr_b16 a[224:225], v197 offset:256\n"
"v_mfma_f32_16x16x32_bf16 v[12:15], a[196:199], v[164:167], v[12:15]\n"
"ds_read_b64_tr_b16 a[226:227], v197 offset:2304\n"
"v_mfma_f32_16x16x32_bf16 v[12:15], a[200:203], v[168:171], v[12:15]\n"
"ds_read_b64_tr_b16 a[228:229], v197 offset:4352\n"
"buffer_load_dwordx4 a[0:3], v252, s[24:27], s49 offen\n"
"v_mfma_f32_16x16x32_bf16 v[12:15], a[204:207], v[172:175], v[12:15]\n"
"ds_read_b64_tr_b16 a[230:231], v197 offset:6400\n"
"v_mfma_f32_16x16x32_bf16 v[12:15], a[208:211], v[176:179], v[12:15]\n"
"ds_read_b64_tr_b16 a[232:233], v197 offset:8448\n"
"v_cvt_pk_bf16_f32 v34, v8, v9\n"
"v_mfma_f32_16x16x32_bf16 v[12:15], a[212:215], v[180:183], v[12:15]\n"
"ds_read_b64_tr_b16 a[234:235], v197 offset:10496\n"
"v_cvt_pk_bf16_f32 v35, v10, v11\n"
"v_mfma_f32_16x16x32_bf16 v[12:15], a[216:219], v[184:187], v[12:15]\n"
"ds_read_b64_tr_b16 a[236:237], v197 offset:12544\n"
"v_mfma_f32_16x16x32_bf16 v[12:15], a[220:223], v[188:191], v[12:15]\n"
"ds_read_b64_tr_b16 a[238:239], v197 offset:14592\n"
"buffer_store_dwordx4 v[32:35], v205, s[32:35], s54 offen offset:0 sc1\n"
"v_mfma_f32_16x16x32_bf16 v[16:19], a[160:163], v[128:131], 0\n"
"s_waitcnt lgkmcnt(11)\n"
"ds_read_b64_tr_b16 a[240:241], v197 offset:16640\n"
"v_mfma_f32_16x16x32_bf16 v[16:19], a[164:167], v[132:135], v[16:19]\n"
"ds_read_b64_tr_b16 a[242:243], v197 offset:18688\n"
"buffer_load_dwordx4 a[36:39], v252, s[64:67], s49 offen\n"
"v_mfma_f32_16x16x32_bf16 v[16:19], a[168:171], v[136:139], v[16:19]\n"
"s_add_u32 s49, s49, 589824\n"
"ds_read_b64_tr_b16 a[244:245], v197 offset:20736\n"
"v_mfma_f32_16x16x32_bf16 v[16:19], a[172:175], v[140:143], v[16:19]\n"
"ds_read_b64_tr_b16 a[246:247], v197 offset:22784\n"
"v_mfma_f32_16x16x32_bf16 v[16:19], a[176:179], v[144:147], v[16:19]\n"
"ds_read_b64_tr_b16 a[248:249], v197 offset:24832\n"
"v_cvt_pk_bf16_f32 v38, v12, v13\n"
"v_mfma_f32_16x16x32_bf16 v[16:19], a[180:183], v[148:151], v[16:19]\n"
"s_waitcnt lgkmcnt(11)\n"
"ds_read_b64_tr_b16 a[250:251], v197 offset:26880\n"
"v_cvt_pk_bf16_f32 v39, v14, v15\n"
"v_mfma_f32_16x16x32_bf16 v[16:19], a[184:187], v[152:155], v[16:19]\n"
"ds_read_b64_tr_b16 a[252:253], v197 offset:28928\n"
"v_mfma_f32_16x16x32_bf16 v[16:19], a[188:191], v[156:159], v[16:19]\n"
"ds_read_b64_tr_b16 a[254:255], v197 offset:30976\n"
"buffer_store_dwordx4 v[36:39], v206, s[32:35], s54 offen offset:0 sc1\n"
"v_mfma_f32_16x16x32_bf16 v[20:23], a[160:163], v[160:163], 0\n"
"ds_read_b64_tr_b16 a[128:129], v192 offset:0\n"
"v_mfma_f32_16x16x32_bf16 v[20:23], a[164:167], v[164:167], v[20:23]\n"
"ds_read_b64_tr_b16 a[130:131], v192 offset:2048\n"
"buffer_load_dwordx4 a[4:7], v252, s[24:27], s49 offen\n"
"v_mfma_f32_16x16x32_bf16 v[20:23], a[168:171], v[168:171], v[20:23]\n"
"ds_read_b64_tr_b16 a[132:133], v192 offset:4096\n"
"v_mfma_f32_16x16x32_bf16 v[20:23], a[172:175], v[172:175], v[20:23]\n"
"ds_read_b64_tr_b16 a[134:135], v192 offset:6144\n"
"v_mfma_f32_16x16x32_bf16 v[20:23], a[176:179], v[176:179], v[20:23]\n"
"ds_read_b64_tr_b16 a[136:137], v192 offset:8192\n"
"v_cvt_pk_bf16_f32 v40, v16, v17\n"
"v_mfma_f32_16x16x32_bf16 v[20:23], a[180:183], v[180:183], v[20:23]\n"
"ds_read_b64_tr_b16 a[138:139], v192 offset:10240\n"
"v_cvt_pk_bf16_f32 v41, v18, v19\n"
"v_mfma_f32_16x16x32_bf16 v[20:23], a[184:187], v[184:187], v[20:23]\n"
"ds_read_b64_tr_b16 a[140:141], v192 offset:12288\n"
"v_mfma_f32_16x16x32_bf16 v[20:23], a[188:191], v[188:191], v[20:23]\n"
"ds_read_b64_tr_b16 a[142:143], v192 offset:14336\n"
"buffer_load_dwordx4 a[40:43], v252, s[64:67], s49 offen\n"
"v_mfma_f32_16x16x32_bf16 v[24:27], a[224:227], v[128:131], 0\n"
"s_waitcnt lgkmcnt(11)\n"
"s_add_u32 s49, s49, 589824\n"
"ds_read_b64_tr_b16 a[144:145], v192 offset:16384\n"
"v_mfma_f32_16x16x32_bf16 v[24:27], a[228:231], v[132:135], v[24:27]\n"
"ds_read_b64_tr_b16 a[146:147], v192 offset:18432\n"
"v_mfma_f32_16x16x32_bf16 v[24:27], a[232:235], v[136:139], v[24:27]\n"
"ds_read_b64_tr_b16 a[148:149], v192 offset:20480\n"
"v_mfma_f32_16x16x32_bf16 v[24:27], a[236:239], v[140:143], v[24:27]\n"
"ds_read_b64_tr_b16 a[150:151], v192 offset:22528\n"
"v_mfma_f32_16x16x32_bf16 v[24:27], a[240:243], v[144:147], v[24:27]\n"
"ds_read_b64_tr_b16 a[152:153], v192 offset:24576\n"
"v_cvt_pk_bf16_f32 v44, v20, v21\n"
"v_mfma_f32_16x16x32_bf16 v[24:27], a[244:247], v[148:151], v[24:27]\n"
"s_waitcnt lgkmcnt(11)\n"
"ds_read_b64_tr_b16 a[154:155], v192 offset:26624\n"
"v_cvt_pk_bf16_f32 v45, v22, v23\n"
"v_mfma_f32_16x16x32_bf16 v[24:27], a[248:251], v[152:155], v[24:27]\n"
"ds_read_b64_tr_b16 a[156:157], v192 offset:28672\n"
"buffer_load_dwordx4 a[8:11], v252, s[24:27], s49 offen\n"
"v_mfma_f32_16x16x32_bf16 v[24:27], a[252:255], v[156:159], v[24:27]\n"
"ds_read_b64_tr_b16 a[158:159], v192 offset:30720\n"
"v_mfma_f32_16x16x32_bf16 v[28:31], a[224:227], v[160:163], 0\n"
"ds_read_b64_tr_b16 a[192:193], v195 offset:0\n"
"v_mfma_f32_16x16x32_bf16 v[28:31], a[228:231], v[164:167], v[28:31]\n"
"ds_read_b64_tr_b16 a[194:195], v195 offset:2048\n"
"v_mfma_f32_16x16x32_bf16 v[28:31], a[232:235], v[168:171], v[28:31]\n"
"ds_read_b64_tr_b16 a[196:197], v195 offset:4096\n"
"v_mfma_f32_16x16x32_bf16 v[28:31], a[236:239], v[172:175], v[28:31]\n"
"ds_read_b64_tr_b16 a[198:199], v195 offset:6144\n"
"v_mfma_f32_16x16x32_bf16 v[28:31], a[240:243], v[176:179], v[28:31]\n"
"ds_read_b64_tr_b16 a[200:201], v195 offset:8192\n"
"v_cvt_pk_bf16_f32 v42, v24, v25\n"
"v_mfma_f32_16x16x32_bf16 v[28:31], a[244:247], v[180:183], v[28:31]\n"
"ds_read_b64_tr_b16 a[202:203], v195 offset:10240\n"
"v_cvt_pk_bf16_f32 v43, v26, v27\n"
"v_mfma_f32_16x16x32_bf16 v[28:31], a[248:251], v[184:187], v[28:31]\n"
"ds_read_b64_tr_b16 a[204:205], v195 offset:12288\n"
"v_mfma_f32_16x16x32_bf16 v[28:31], a[252:255], v[188:191], v[28:31]\n"
"buffer_store_dwordx4 v[40:43], v205, s[32:35], s54 offen offset:1024 sc1\n"
"ds_read_b64_tr_b16 a[206:207], v195 offset:14336\n"
"v_mfma_f32_16x16x32_bf16 v[0:3], a[128:131], v[128:131], 0\n"
"s_waitcnt lgkmcnt(11)\n"
"ds_read_b64_tr_b16 a[208:209], v195 offset:16384\n"
"v_mfma_f32_16x16x32_bf16 v[0:3], a[132:135], v[132:135], v[0:3]\n"
"ds_read_b64_tr_b16 a[210:211], v195 offset:18432\n"
"buffer_load_dwordx4 a[44:47], v252, s[64:67], s49 offen\n"
"v_mfma_f32_16x16x32_bf16 v[0:3], a[136:139], v[136:139], v[0:3]\n"
"s_add_u32 s49, s49, 589824\n"
"ds_read_b64_tr_b16 a[212:213], v195 offset:20480\n"
"v_mfma_f32_16x16x32_bf16 v[0:3], a[140:143], v[140:143], v[0:3]\n"
"ds_read_b64_tr_b16 a[214:215], v195 offset:22528\n"
"v_mfma_f32_16x16x32_bf16 v[0:3], a[144:147], v[144:147], v[0:3]\n"
"s_waitcnt vmcnt(19)\n"
"s_barrier\n"
"ds_read_b64_tr_b16 a[216:217], v195 offset:24576\n"
"v_cvt_pk_bf16_f32 v46, v28, v29\n"
"v_mfma_f32_16x16x32_bf16 v[0:3], a[148:151], v[148:151], v[0:3]\n"
"s_waitcnt lgkmcnt(11)\n"
"s_add_u32 m0, s53, 98304\n"
"ds_read_b64_tr_b16 a[218:219], v195 offset:26624\n"
"v_cvt_pk_bf16_f32 v47, v30, v31\n"
"v_mfma_f32_16x16x32_bf16 v[0:3], a[152:155], v[152:155], v[0:3]\n"
"ds_read_b64_tr_b16 a[220:221], v195 offset:28672\n"
"buffer_load_dwordx4 v204, s[28:31], s52 offen offset:0 lds\n"
"v_mfma_f32_16x16x32_bf16 v[0:3], a[156:159], v[156:159], v[0:3]\n"
"s_add_u32 s52, s52, 8192\n"
"buffer_store_dwordx4 v[44:47], v206, s[32:35], s54 offen offset:1024 sc1\n"
"ds_read_b64_tr_b16 a[222:223], v195 offset:30720\n"
"v_mfma_f32_16x16x32_bf16 v[4:7], a[128:131], v[160:163], 0\n"
"ds_read_b64_tr_b16 a[160:161], v192 offset:256\n"
"v_mfma_f32_16x16x32_bf16 v[4:7], a[132:135], v[164:167], v[4:7]\n"
"ds_read_b64_tr_b16 a[162:163], v192 offset:2304\n"
"v_mfma_f32_16x16x32_bf16 v[4:7], a[136:139], v[168:171], v[4:7]\n"
"ds_read_b64_tr_b16 a[164:165], v192 offset:4352\n"
"buffer_load_dwordx4 v204, s[28:31], s52 offen offset:1024 lds\n"
"v_mfma_f32_16x16x32_bf16 v[4:7], a[140:143], v[172:175], v[4:7]\n"
"s_add_u32 s52, s52, 8192\n"
"ds_read_b64_tr_b16 a[166:167], v192 offset:6400\n"
"v_mfma_f32_16x16x32_bf16 v[4:7], a[144:147], v[176:179], v[4:7]\n"
"ds_read_b64_tr_b16 a[168:169], v192 offset:8448\n"
"v_cvt_pk_bf16_f32 v32, v0, v1\n"
"v_mfma_f32_16x16x32_bf16 v[4:7], a[148:151], v[180:183], v[4:7]\n"
"ds_read_b64_tr_b16 a[170:171], v192 offset:10496\n"
"v_cvt_pk_bf16_f32 v33, v2, v3\n"
"v_mfma_f32_16x16x32_bf16 v[4:7], a[152:155], v[184:187], v[4:7]\n"
"ds_read_b64_tr_b16 a[172:173], v192 offset:12544\n"
"buffer_load_dwordx4 v204, s[28:31], s52 offen offset:2048 lds\n"
"v_mfma_f32_16x16x32_bf16 v[4:7], a[156:159], v[188:191], v[4:7]\n"
"s_add_u32 s52, s52, 8192\n"
"ds_read_b64_tr_b16 a[174:175], v192 offset:14592\n"
"v_mfma_f32_16x16x32_bf16 v[8:11], a[192:195], v[128:131], 0\n"
"s_waitcnt lgkmcnt(11)\n"
"ds_read_b64_tr_b16 a[176:177], v192 offset:16640\n"
"buffer_load_dwordx4 a[12:15], v252, s[24:27], s49 offen\n"
"v_mfma_f32_16x16x32_bf16 v[8:11], a[196:199], v[132:135], v[8:11]\n"
"ds_read_b64_tr_b16 a[178:179], v192 offset:18688\n"
"v_mfma_f32_16x16x32_bf16 v[8:11], a[200:203], v[136:139], v[8:11]\n"
"ds_read_b64_tr_b16 a[180:181], v192 offset:20736\n"
"buffer_load_dwordx4 v204, s[28:31], s52 offen offset:3072 lds\n"
"v_mfma_f32_16x16x32_bf16 v[8:11], a[204:207], v[140:143], v[8:11]\n"
"s_add_u32 s52, s52, 12288\n"
"ds_read_b64_tr_b16 a[182:183], v192 offset:22784\n"
"v_mfma_f32_16x16x32_bf16 v[8:11], a[208:211], v[144:147], v[8:11]\n"
"ds_read_b64_tr_b16 a[184:185], v192 offset:24832\n"
"v_cvt_pk_bf16_f32 v36, v4, v5\n"
"v_mfma_f32_16x16x32_bf16 v[8:11], a[212:215], v[148:151], v[8:11]\n"
"s_waitcnt lgkmcnt(11)\n"
"s_add_u32 m0, s53, 102400\n"
"ds_read_b64_tr_b16 a[186:187], v192 offset:26880\n"
"v_cvt_pk_bf16_f32 v37, v6, v7\n"
"v_mfma_f32_16x16x32_bf16 v[8:11], a[216:219], v[152:155], v[8:11]\n"
"ds_read_b64_tr_b16 a[188:189], v192 offset:28928\n"
"buffer_load_dwordx4 v204, s[28:31], s52 offen offset:0 lds\n"
"v_mfma_f32_16x16x32_bf16 v[8:11], a[220:223], v[156:159], v[8:11]\n"
"s_add_u32 s52, s52, 8192\n"
"ds_read_b64_tr_b16 a[190:191], v192 offset:30976\n"
"v_mfma_f32_16x16x32_bf16 v[12:15], a[192:195], v[160:163], 0\n"
"ds_read_b64_tr_b16 a[224:225], v195 offset:256\n"
"buffer_load_dwordx4 a[48:51], v252, s[64:67], s49 offen\n"
"v_mfma_f32_16x16x32_bf16 v[12:15], a[196:199], v[164:167], v[12:15]\n"
"s_add_u32 s49, s49, 589824\n"
"ds_read_b64_tr_b16 a[226:227], v195 offset:2304\n"
"v_mfma_f32_16x16x32_bf16 v[12:15], a[200:203], v[168:171], v[12:15]\n"
"ds_read_b64_tr_b16 a[228:229], v195 offset:4352\n"
"buffer_load_dwordx4 v204, s[28:31], s52 offen offset:1024 lds\n"
"v_mfma_f32_16x16x32_bf16 v[12:15], a[204:207], v[172:175], v[12:15]\n"
"s_add_u32 s52, s52, 8192\n"
"ds_read_b64_tr_b16 a[230:231], v195 offset:6400\n"
"v_mfma_f32_16x16x32_bf16 v[12:15], a[208:211], v[176:179], v[12:15]\n"
"ds_read_b64_tr_b16 a[232:233], v195 offset:8448\n"
"v_cvt_pk_bf16_f32 v34, v8, v9\n"
"v_mfma_f32_16x16x32_bf16 v[12:15], a[212:215], v[180:183], v[12:15]\n"
"ds_read_b64_tr_b16 a[234:235], v195 offset:10496\n"
"v_cvt_pk_bf16_f32 v35, v10, v11\n"
"v_mfma_f32_16x16x32_bf16 v[12:15], a[216:219], v[184:187], v[12:15]\n"
"ds_read_b64_tr_b16 a[236:237], v195 offset:12544\n"
"buffer_load_dwordx4 v204, s[28:31], s52 offen offset:2048 lds\n"
"v_mfma_f32_16x16x32_bf16 v[12:15], a[220:223], v[188:191], v[12:15]\n"
"s_add_u32 s52, s52, 8192\n"
"ds_read_b64_tr_b16 a[238:239], v195 offset:14592\n"
"buffer_store_dwordx4 v[32:35], v205, s[32:35], s54 offen offset:2048 sc1\n"
"v_mfma_f32_16x16x32_bf16 v[16:19], a[160:163], v[128:131], 0\n"
"s_waitcnt lgkmcnt(11)\n"
"ds_read_b64_tr_b16 a[240:241], v195 offset:16640\n"
"v_mfma_f32_16x16x32_bf16 v[16:19], a[164:167], v[132:135], v[16:19]\n"
"ds_read_b64_tr_b16 a[242:243], v195 offset:18688\n"
"v_mfma_f32_16x16x32_bf16 v[16:19], a[168:171], v[136:139], v[16:19]\n"
"ds_read_b64_tr_b16 a[244:245], v195 offset:20736\n"
"buffer_load_dwordx4 v204, s[28:31], s52 offen offset:3072 lds\n"
"v_mfma_f32_16x16x32_bf16 v[16:19], a[172:175], v[140:143], v[16:19]\n"
"s_sub_u32 s52, s52, 61312\n"
"ds_read_b64_tr_b16 a[246:247], v195 offset:22784\n"
"v_mfma_f32_16x16x32_bf16 v[16:19], a[176:179], v[144:147], v[16:19]\n"
"ds_read_b64_tr_b16 a[248:249], v195 offset:24832\n"
"v_cvt_pk_bf16_f32 v38, v12, v13\n"
"v_mfma_f32_16x16x32_bf16 v[16:19], a[180:183], v[148:151], v[16:19]\n"
"s_waitcnt lgkmcnt(11)\n"
"s_add_u32 m0, s53, 131072\n"
"ds_read_b64_tr_b16 a[250:251], v195 offset:26880\n"
"v_cvt_pk_bf16_f32 v39, v14, v15\n"
"v_mfma_f32_16x16x32_bf16 v[16:19], a[184:187], v[152:155], v[16:19]\n"
"ds_read_b64_tr_b16 a[252:253], v195 offset:28928\n"
"buffer_load_dwordx4 v204, s[28:31], s52 offen offset:0 lds\n"
"v_mfma_f32_16x16x32_bf16 v[16:19], a[188:191], v[156:159], v[16:19]\n"
"s_add_u32 s52, s52, 8192\n"
"ds_read_b64_tr_b16 a[254:255], v195 offset:30976\n"
"buffer_store_dwordx4 v[36:39], v206, s[32:35], s54 offen offset:2048 sc1\n"
"v_mfma_f32_16x16x32_bf16 v[20:23], a[160:163], v[160:163], 0\n"
"ds_read_b64_tr_b16 a[128:129], v192 offset:32768\n"
"v_mfma_f32_16x16x32_bf16 v[20:23], a[164:167], v[164:167], v[20:23]\n"
"ds_read_b64_tr_b16 a[130:131], v192 offset:34816\n"
"v_mfma_f32_16x16x32_bf16 v[20:23], a[168:171], v[168:171], v[20:23]\n"
"ds_read_b64_tr_b16 a[132:133], v192 offset:36864\n"
"buffer_load_dwordx4 v204, s[28:31], s52 offen offset:1024 lds\n"
"v_mfma_f32_16x16x32_bf16 v[20:23], a[172:175], v[172:175], v[20:23]\n"
"s_add_u32 s52, s52, 8192\n"
"ds_read_b64_tr_b16 a[134:135], v192 offset:38912\n"
"v_mfma_f32_16x16x32_bf16 v[20:23], a[176:179], v[176:179], v[20:23]\n"
"ds_read_b64_tr_b16 a[136:137], v192 offset:40960\n"
"v_cvt_pk_bf16_f32 v40, v16, v17\n"
"v_mfma_f32_16x16x32_bf16 v[20:23], a[180:183], v[180:183], v[20:23]\n"
"ds_read_b64_tr_b16 a[138:139], v192 offset:43008\n"
"v_cvt_pk_bf16_f32 v41, v18, v19\n"
"v_mfma_f32_16x16x32_bf16 v[20:23], a[184:187], v[184:187], v[20:23]\n"
"ds_read_b64_tr_b16 a[140:141], v192 offset:45056\n"
"buffer_load_dwordx4 v204, s[28:31], s52 offen offset:2048 lds\n"
"v_mfma_f32_16x16x32_bf16 v[20:23], a[188:191], v[188:191], v[20:23]\n"
"s_add_u32 s52, s52, 8192\n"
"ds_read_b64_tr_b16 a[142:143], v192 offset:47104\n"
"v_mfma_f32_16x16x32_bf16 v[24:27], a[224:227], v[128:131], 0\n"
"s_waitcnt lgkmcnt(11)\n"
"ds_read_b64_tr_b16 a[144:145], v192 offset:49152\n"
"buffer_load_dwordx4 a[16:19], v252, s[24:27], s49 offen\n"
"v_mfma_f32_16x16x32_bf16 v[24:27], a[228:231], v[132:135], v[24:27]\n"
"ds_read_b64_tr_b16 a[146:147], v192 offset:51200\n"
"v_mfma_f32_16x16x32_bf16 v[24:27], a[232:235], v[136:139], v[24:27]\n"
"ds_read_b64_tr_b16 a[148:149], v192 offset:53248\n"
"buffer_load_dwordx4 v204, s[28:31], s52 offen offset:3072 lds\n"
"v_mfma_f32_16x16x32_bf16 v[24:27], a[236:239], v[140:143], v[24:27]\n"
"s_add_u32 s52, s52, 12288\n"
"ds_read_b64_tr_b16 a[150:151], v192 offset:55296\n"
"v_mfma_f32_16x16x32_bf16 v[24:27], a[240:243], v[144:147], v[24:27]\n"
"ds_read_b64_tr_b16 a[152:153], v192 offset:57344\n"
"v_cvt_pk_bf16_f32 v44, v20, v21\n"
"v_mfma_f32_16x16x32_bf16 v[24:27], a[244:247], v[148:151], v[24:27]\n"
"s_waitcnt lgkmcnt(11)\n"
"s_add_u32 m0, s53, 135168\n"
"ds_read_b64_tr_b16 a[154:155], v192 offset:59392\n"
"v_cvt_pk_bf16_f32 v45, v22, v23\n"
"v_mfma_f32_16x16x32_bf16 v[24:27], a[248:251], v[152:155], v[24:27]\n"
"ds_read_b64_tr_b16 a[156:157], v192 offset:61440\n"
"buffer_load_dwordx4 v204, s[28:31], s52 offen offset:0 lds\n"
"v_mfma_f32_16x16x32_bf16 v[24:27], a[252:255], v[156:159], v[24:27]\n"
"s_add_u32 s52, s52, 8192\n"
"ds_read_b64_tr_b16 a[158:159], v192 offset:63488\n"
"v_mfma_f32_16x16x32_bf16 v[28:31], a[224:227], v[160:163], 0\n"
"ds_read_b64_tr_b16 a[192:193], v195 offset:32768\n"
"buffer_load_dwordx4 a[52:55], v252, s[64:67], s49 offen\n"
"v_mfma_f32_16x16x32_bf16 v[28:31], a[228:231], v[164:167], v[28:31]\n"
"s_add_u32 s49, s49, 589824\n"
"ds_read_b64_tr_b16 a[194:195], v195 offset:34816\n"
"v_mfma_f32_16x16x32_bf16 v[28:31], a[232:235], v[168:171], v[28:31]\n"
"ds_read_b64_tr_b16 a[196:197], v195 offset:36864\n"
"buffer_load_dwordx4 v204, s[28:31], s52 offen offset:1024 lds\n"
"v_mfma_f32_16x16x32_bf16 v[28:31], a[236:239], v[172:175], v[28:31]\n"
"s_add_u32 s52, s52, 8192\n"
"ds_read_b64_tr_b16 a[198:199], v195 offset:38912\n"
"v_mfma_f32_16x16x32_bf16 v[28:31], a[240:243], v[176:179], v[28:31]\n"
"ds_read_b64_tr_b16 a[200:201], v195 offset:40960\n"
"v_cvt_pk_bf16_f32 v42, v24, v25\n"
"v_mfma_f32_16x16x32_bf16 v[28:31], a[244:247], v[180:183], v[28:31]\n"
"ds_read_b64_tr_b16 a[202:203], v195 offset:43008\n"
"v_cvt_pk_bf16_f32 v43, v26, v27\n"
"v_mfma_f32_16x16x32_bf16 v[28:31], a[248:251], v[184:187], v[28:31]\n"
"ds_read_b64_tr_b16 a[204:205], v195 offset:45056\n"
"buffer_load_dwordx4 v204, s[28:31], s52 offen offset:2048 lds\n"
"v_mfma_f32_16x16x32_bf16 v[28:31], a[252:255], v[188:191], v[28:31]\n"
"s_add_u32 s52, s52, 8192\n"
"buffer_store_dwordx4 v[40:43], v205, s[32:35], s54 offen offset:3072 sc1\n"
"ds_read_b64_tr_b16 a[206:207], v195 offset:47104\n"
"v_mfma_f32_16x16x32_bf16 v[0:3], a[128:131], v[128:131], 0\n"
"s_waitcnt lgkmcnt(11)\n"
"ds_read_b64_tr_b16 a[208:209], v195 offset:49152\n"
"v_mfma_f32_16x16x32_bf16 v[0:3], a[132:135], v[132:135], v[0:3]\n"
"ds_read_b64_tr_b16 a[210:211], v195 offset:51200\n"
"v_mfma_f32_16x16x32_bf16 v[0:3], a[136:139], v[136:139], v[0:3]\n"
"ds_read_b64_tr_b16 a[212:213], v195 offset:53248\n"
"buffer_load_dwordx4 v204, s[28:31], s52 offen offset:3072 lds\n"
"v_mfma_f32_16x16x32_bf16 v[0:3], a[140:143], v[140:143], v[0:3]\n"
"s_sub_u32 s52, s52, 61312\n"
"ds_read_b64_tr_b16 a[214:215], v195 offset:55296\n"
"v_mfma_f32_16x16x32_bf16 v[0:3], a[144:147], v[144:147], v[0:3]\n"
"s_waitcnt vmcnt(33)\n"
"s_barrier\n"
"ds_read_b64_tr_b16 a[216:217], v195 offset:57344\n"
"v_cvt_pk_bf16_f32 v46, v28, v29\n"
"v_mfma_f32_16x16x32_bf16 v[0:3], a[148:151], v[148:151], v[0:3]\n"
"s_waitcnt lgkmcnt(11)\n"
"s_mov_b32 m0, s53\n"
"ds_read_b64_tr_b16 a[218:219], v195 offset:59392\n"
"v_cvt_pk_bf16_f32 v47, v30, v31\n"
"v_mfma_f32_16x16x32_bf16 v[0:3], a[152:155], v[152:155], v[0:3]\n"
"ds_read_b64_tr_b16 a[220:221], v195 offset:61440\n"
"buffer_load_dwordx4 v204, s[28:31], s52 offen offset:0 lds\n"
"v_mfma_f32_16x16x32_bf16 v[0:3], a[156:159], v[156:159], v[0:3]\n"
"s_add_u32 s52, s52, 8192\n"
"buffer_store_dwordx4 v[44:47], v206, s[32:35], s54 offen offset:3072 sc1\n"
"ds_read_b64_tr_b16 a[222:223], v195 offset:63488\n"
"v_mfma_f32_16x16x32_bf16 v[4:7], a[128:131], v[160:163], 0\n"
"s_add_u32 s54, s54, 4096\n"
"ds_read_b64_tr_b16 a[160:161], v192 offset:33024\n"
"v_mfma_f32_16x16x32_bf16 v[4:7], a[132:135], v[164:167], v[4:7]\n"
"ds_read_b64_tr_b16 a[162:163], v192 offset:35072\n"
"v_mfma_f32_16x16x32_bf16 v[4:7], a[136:139], v[168:171], v[4:7]\n"
"ds_read_b64_tr_b16 a[164:165], v192 offset:37120\n"
"buffer_load_dwordx4 v204, s[28:31], s52 offen offset:1024 lds\n"
"v_mfma_f32_16x16x32_bf16 v[4:7], a[140:143], v[172:175], v[4:7]\n"
"s_add_u32 s52, s52, 8192\n"
"ds_read_b64_tr_b16 a[166:167], v192 offset:39168\n"
"v_mfma_f32_16x16x32_bf16 v[4:7], a[144:147], v[176:179], v[4:7]\n"
"ds_read_b64_tr_b16 a[168:169], v192 offset:41216\n"
"v_cvt_pk_bf16_f32 v32, v0, v1\n"
"v_mfma_f32_16x16x32_bf16 v[4:7], a[148:151], v[180:183], v[4:7]\n"
"ds_read_b64_tr_b16 a[170:171], v192 offset:43264\n"
"v_cvt_pk_bf16_f32 v33, v2, v3\n"
"v_mfma_f32_16x16x32_bf16 v[4:7], a[152:155], v[184:187], v[4:7]\n"
"ds_read_b64_tr_b16 a[172:173], v192 offset:45312\n"
"buffer_load_dwordx4 v204, s[28:31], s52 offen offset:2048 lds\n"
"v_mfma_f32_16x16x32_bf16 v[4:7], a[156:159], v[188:191], v[4:7]\n"
"s_add_u32 s52, s52, 8192\n"
"ds_read_b64_tr_b16 a[174:175], v192 offset:47360\n"
"v_mfma_f32_16x16x32_bf16 v[8:11], a[192:195], v[128:131], 0\n"
"s_waitcnt lgkmcnt(11)\n"
"ds_read_b64_tr_b16 a[176:177], v192 offset:49408\n"
"buffer_load_dwordx4 a[20:23], v252, s[24:27], s49 offen\n"
"v_mfma_f32_16x16x32_bf16 v[8:11], a[196:199], v[132:135], v[8:11]\n"
"ds_read_b64_tr_b16 a[178:179], v192 offset:51456\n"
"v_mfma_f32_16x16x32_bf16 v[8:11], a[200:203], v[136:139], v[8:11]\n"
"ds_read_b64_tr_b16 a[180:181], v192 offset:53504\n"
"buffer_load_dwordx4 v204, s[28:31], s52 offen offset:3072 lds\n"
"v_mfma_f32_16x16x32_bf16 v[8:11], a[204:207], v[140:143], v[8:11]\n"
"s_add_u32 s52, s52, 12288\n"
"ds_read_b64_tr_b16 a[182:183], v192 offset:55552\n"
"v_mfma_f32_16x16x32_bf16 v[8:11], a[208:211], v[144:147], v[8:11]\n"
"ds_read_b64_tr_b16 a[184:185], v192 offset:57600\n"
"v_cvt_pk_bf16_f32 v36, v4, v5\n"
"v_mfma_f32_16x16x32_bf16 v[8:11], a[212:215], v[148:151], v[8:11]\n"
"s_waitcnt lgkmcnt(11)\n"
"s_add_u32 m0, s53, 4096\n"
"ds_read_b64_tr_b16 a[186:187], v192 offset:59648\n"
"v_cvt_pk_bf16_f32 v37, v6, v7\n"
"v_mfma_f32_16x16x32_bf16 v[8:11], a[216:219], v[152:155], v[8:11]\n"
"ds_read_b64_tr_b16 a[188:189], v192 offset:61696\n"
"buffer_load_dwordx4 v204, s[28:31], s52 offen offset:0 lds\n"
"v_mfma_f32_16x16x32_bf16 v[8:11], a[220:223], v[156:159], v[8:11]\n"
"s_add_u32 s52, s52, 8192\n"
"ds_read_b64_tr_b16 a[190:191], v192 offset:63744\n"
"v_mfma_f32_16x16x32_bf16 v[12:15], a[192:195], v[160:163], 0\n"
"ds_read_b64_tr_b16 a[224:225], v195 offset:33024\n"
"buffer_load_dwordx4 a[56:59], v252, s[64:67], s49 offen\n"
"v_mfma_f32_16x16x32_bf16 v[12:15], a[196:199], v[164:167], v[12:15]\n"
"s_add_u32 s49, s49, 589824\n"
"ds_read_b64_tr_b16 a[226:227], v195 offset:35072\n"
"v_mfma_f32_16x16x32_bf16 v[12:15], a[200:203], v[168:171], v[12:15]\n"
"ds_read_b64_tr_b16 a[228:229], v195 offset:37120\n"
"buffer_load_dwordx4 v204, s[28:31], s52 offen offset:1024 lds\n"
"v_mfma_f32_16x16x32_bf16 v[12:15], a[204:207], v[172:175], v[12:15]\n"
"s_add_u32 s52, s52, 8192\n"
"ds_read_b64_tr_b16 a[230:231], v195 offset:39168\n"
"v_mfma_f32_16x16x32_bf16 v[12:15], a[208:211], v[176:179], v[12:15]\n"
"ds_read_b64_tr_b16 a[232:233], v195 offset:41216\n"
"v_cvt_pk_bf16_f32 v34, v8, v9\n"
"v_mfma_f32_16x16x32_bf16 v[12:15], a[212:215], v[180:183], v[12:15]\n"
"ds_read_b64_tr_b16 a[234:235], v195 offset:43264\n"
"v_cvt_pk_bf16_f32 v35, v10, v11\n"
"v_mfma_f32_16x16x32_bf16 v[12:15], a[216:219], v[184:187], v[12:15]\n"
"ds_read_b64_tr_b16 a[236:237], v195 offset:45312\n"
"buffer_load_dwordx4 v204, s[28:31], s52 offen offset:2048 lds\n"
"v_mfma_f32_16x16x32_bf16 v[12:15], a[220:223], v[188:191], v[12:15]\n"
"s_add_u32 s52, s52, 8192\n"
"ds_read_b64_tr_b16 a[238:239], v195 offset:47360\n"
"buffer_store_dwordx4 v[32:35], v205, s[32:35], s54 offen offset:0 sc1\n"
"v_mfma_f32_16x16x32_bf16 v[16:19], a[160:163], v[128:131], 0\n"
"s_waitcnt lgkmcnt(11)\n"
"ds_read_b64_tr_b16 a[240:241], v195 offset:49408\n"
"v_mfma_f32_16x16x32_bf16 v[16:19], a[164:167], v[132:135], v[16:19]\n"
"ds_read_b64_tr_b16 a[242:243], v195 offset:51456\n"
"v_mfma_f32_16x16x32_bf16 v[16:19], a[168:171], v[136:139], v[16:19]\n"
"ds_read_b64_tr_b16 a[244:245], v195 offset:53504\n"
"buffer_load_dwordx4 v204, s[28:31], s52 offen offset:3072 lds\n"
"v_mfma_f32_16x16x32_bf16 v[16:19], a[172:175], v[140:143], v[16:19]\n"
"s_sub_u32 s52, s52, 61312\n"
"ds_read_b64_tr_b16 a[246:247], v195 offset:55552\n"
"v_mfma_f32_16x16x32_bf16 v[16:19], a[176:179], v[144:147], v[16:19]\n"
"ds_read_b64_tr_b16 a[248:249], v195 offset:57600\n"
"v_cvt_pk_bf16_f32 v38, v12, v13\n"
"v_mfma_f32_16x16x32_bf16 v[16:19], a[180:183], v[148:151], v[16:19]\n"
"s_waitcnt lgkmcnt(11)\n"
"ds_read_b64_tr_b16 a[250:251], v195 offset:59648\n"
"v_cvt_pk_bf16_f32 v39, v14, v15\n"
"v_mfma_f32_16x16x32_bf16 v[16:19], a[184:187], v[152:155], v[16:19]\n"
"ds_read_b64_tr_b16 a[252:253], v195 offset:61696\n"
"v_mfma_f32_16x16x32_bf16 v[16:19], a[188:191], v[156:159], v[16:19]\n"
"ds_read_b64_tr_b16 a[254:255], v195 offset:63744\n"
"buffer_store_dwordx4 v[36:39], v206, s[32:35], s54 offen offset:0 sc1\n"
"v_mfma_f32_16x16x32_bf16 v[20:23], a[160:163], v[160:163], 0\n"
"ds_read_b64_tr_b16 a[128:129], v193 offset:0\n"
"v_mfma_f32_16x16x32_bf16 v[20:23], a[164:167], v[164:167], v[20:23]\n"
"ds_read_b64_tr_b16 a[130:131], v193 offset:2048\n"
"buffer_load_dwordx4 a[24:27], v252, s[24:27], s49 offen\n"
"v_mfma_f32_16x16x32_bf16 v[20:23], a[168:171], v[168:171], v[20:23]\n"
"ds_read_b64_tr_b16 a[132:133], v193 offset:4096\n"
"v_mfma_f32_16x16x32_bf16 v[20:23], a[172:175], v[172:175], v[20:23]\n"
"ds_read_b64_tr_b16 a[134:135], v193 offset:6144\n"
"v_mfma_f32_16x16x32_bf16 v[20:23], a[176:179], v[176:179], v[20:23]\n"
"ds_read_b64_tr_b16 a[136:137], v193 offset:8192\n"
"v_cvt_pk_bf16_f32 v40, v16, v17\n"
"v_mfma_f32_16x16x32_bf16 v[20:23], a[180:183], v[180:183], v[20:23]\n"
"ds_read_b64_tr_b16 a[138:139], v193 offset:10240\n"
"v_cvt_pk_bf16_f32 v41, v18, v19\n"
"v_mfma_f32_16x16x32_bf16 v[20:23], a[184:187], v[184:187], v[20:23]\n"
"ds_read_b64_tr_b16 a[140:141], v193 offset:12288\n"
"v_mfma_f32_16x16x32_bf16 v[20:23], a[188:191], v[188:191], v[20:23]\n"
"ds_read_b64_tr_b16 a[142:143], v193 offset:14336\n"
"buffer_load_dwordx4 a[60:63], v252, s[64:67], s49 offen\n"
"v_mfma_f32_16x16x32_bf16 v[24:27], a[224:227], v[128:131], 0\n"
"s_waitcnt lgkmcnt(11)\n"
"s_add_u32 s49, s49, 589824\n"
"ds_read_b64_tr_b16 a[144:145], v193 offset:16384\n"
"v_mfma_f32_16x16x32_bf16 v[24:27], a[228:231], v[132:135], v[24:27]\n"
"ds_read_b64_tr_b16 a[146:147], v193 offset:18432\n"
"v_mfma_f32_16x16x32_bf16 v[24:27], a[232:235], v[136:139], v[24:27]\n"
"ds_read_b64_tr_b16 a[148:149], v193 offset:20480\n"
"v_mfma_f32_16x16x32_bf16 v[24:27], a[236:239], v[140:143], v[24:27]\n"
"ds_read_b64_tr_b16 a[150:151], v193 offset:22528\n"
"v_mfma_f32_16x16x32_bf16 v[24:27], a[240:243], v[144:147], v[24:27]\n"
"ds_read_b64_tr_b16 a[152:153], v193 offset:24576\n"
"v_cvt_pk_bf16_f32 v44, v20, v21\n"
"v_mfma_f32_16x16x32_bf16 v[24:27], a[244:247], v[148:151], v[24:27]\n"
"s_waitcnt lgkmcnt(11)\n"
"ds_read_b64_tr_b16 a[154:155], v193 offset:26624\n"
"v_cvt_pk_bf16_f32 v45, v22, v23\n"
"v_mfma_f32_16x16x32_bf16 v[24:27], a[248:251], v[152:155], v[24:27]\n"
"ds_read_b64_tr_b16 a[156:157], v193 offset:28672\n"
"buffer_load_dwordx4 a[28:31], v252, s[24:27], s49 offen\n"
"v_mfma_f32_16x16x32_bf16 v[24:27], a[252:255], v[156:159], v[24:27]\n"
"ds_read_b64_tr_b16 a[158:159], v193 offset:30720\n"
"v_mfma_f32_16x16x32_bf16 v[28:31], a[224:227], v[160:163], 0\n"
"ds_read_b64_tr_b16 a[192:193], v196 offset:0\n"
"v_mfma_f32_16x16x32_bf16 v[28:31], a[228:231], v[164:167], v[28:31]\n"
"ds_read_b64_tr_b16 a[194:195], v196 offset:2048\n"
"v_mfma_f32_16x16x32_bf16 v[28:31], a[232:235], v[168:171], v[28:31]\n"
"ds_read_b64_tr_b16 a[196:197], v196 offset:4096\n"
"v_mfma_f32_16x16x32_bf16 v[28:31], a[236:239], v[172:175], v[28:31]\n"
"ds_read_b64_tr_b16 a[198:199], v196 offset:6144\n"
"v_mfma_f32_16x16x32_bf16 v[28:31], a[240:243], v[176:179], v[28:31]\n"
"ds_read_b64_tr_b16 a[200:201], v196 offset:8192\n"
"v_cvt_pk_bf16_f32 v42, v24, v25\n"
"v_mfma_f32_16x16x32_bf16 v[28:31], a[244:247], v[180:183], v[28:31]\n"
"ds_read_b64_tr_b16 a[202:203], v196 offset:10240\n"
"v_cvt_pk_bf16_f32 v43, v26, v27\n"
"v_mfma_f32_16x16x32_bf16 v[28:31], a[248:251], v[184:187], v[28:31]\n"
"ds_read_b64_tr_b16 a[204:205], v196 offset:12288\n"
"v_mfma_f32_16x16x32_bf16 v[28:31], a[252:255], v[188:191], v[28:31]\n"
"buffer_store_dwordx4 v[40:43], v205, s[32:35], s54 offen offset:1024 sc1\n"
"ds_read_b64_tr_b16 a[206:207], v196 offset:14336\n"
"v_mfma_f32_16x16x32_bf16 v[0:3], a[128:131], v[128:131], 0\n"
"s_waitcnt lgkmcnt(11)\n"
"ds_read_b64_tr_b16 a[208:209], v196 offset:16384\n"
"v_mfma_f32_16x16x32_bf16 v[0:3], a[132:135], v[132:135], v[0:3]\n"
"ds_read_b64_tr_b16 a[210:211], v196 offset:18432\n"
"buffer_load_dwordx4 a[64:67], v252, s[64:67], s49 offen\n"
"v_mfma_f32_16x16x32_bf16 v[0:3], a[136:139], v[136:139], v[0:3]\n"
"s_add_u32 s49, s49, 589824\n"
"ds_read_b64_tr_b16 a[212:213], v196 offset:20480\n"
"v_mfma_f32_16x16x32_bf16 v[0:3], a[140:143], v[140:143], v[0:3]\n"
"ds_read_b64_tr_b16 a[214:215], v196 offset:22528\n"
"v_mfma_f32_16x16x32_bf16 v[0:3], a[144:147], v[144:147], v[0:3]\n"
"s_waitcnt vmcnt(30)\n"
"s_barrier\n"
"ds_read_b64_tr_b16 a[216:217], v196 offset:24576\n"
"v_cvt_pk_bf16_f32 v46, v28, v29\n"
"v_mfma_f32_16x16x32_bf16 v[0:3], a[148:151], v[148:151], v[0:3]\n"
"s_waitcnt lgkmcnt(11)\n"
"s_add_u32 m0, s53, 32768\n"
"ds_read_b64_tr_b16 a[218:219], v196 offset:26624\n"
"v_cvt_pk_bf16_f32 v47, v30, v31\n"
"v_mfma_f32_16x16x32_bf16 v[0:3], a[152:155], v[152:155], v[0:3]\n"
"ds_read_b64_tr_b16 a[220:221], v196 offset:28672\n"
"buffer_load_dwordx4 v204, s[28:31], s52 offen offset:0 lds\n"
"v_mfma_f32_16x16x32_bf16 v[0:3], a[156:159], v[156:159], v[0:3]\n"
"s_add_u32 s52, s52, 8192\n"
"buffer_store_dwordx4 v[44:47], v206, s[32:35], s54 offen offset:1024 sc1\n"
"ds_read_b64_tr_b16 a[222:223], v196 offset:30720\n"
"v_mfma_f32_16x16x32_bf16 v[4:7], a[128:131], v[160:163], 0\n"
"ds_read_b64_tr_b16 a[160:161], v193 offset:256\n"
"v_mfma_f32_16x16x32_bf16 v[4:7], a[132:135], v[164:167], v[4:7]\n"
"ds_read_b64_tr_b16 a[162:163], v193 offset:2304\n"
"v_mfma_f32_16x16x32_bf16 v[4:7], a[136:139], v[168:171], v[4:7]\n"
"ds_read_b64_tr_b16 a[164:165], v193 offset:4352\n"
"buffer_load_dwordx4 v204, s[28:31], s52 offen offset:1024 lds\n"
"v_mfma_f32_16x16x32_bf16 v[4:7], a[140:143], v[172:175], v[4:7]\n"
"s_add_u32 s52, s52, 8192\n"
"ds_read_b64_tr_b16 a[166:167], v193 offset:6400\n"
"v_mfma_f32_16x16x32_bf16 v[4:7], a[144:147], v[176:179], v[4:7]\n"
"ds_read_b64_tr_b16 a[168:169], v193 offset:8448\n"
"v_cvt_pk_bf16_f32 v32, v0, v1\n"
"v_mfma_f32_16x16x32_bf16 v[4:7], a[148:151], v[180:183], v[4:7]\n"
"ds_read_b64_tr_b16 a[170:171], v193 offset:10496\n"
"v_cvt_pk_bf16_f32 v33, v2, v3\n"
"v_mfma_f32_16x16x32_bf16 v[4:7], a[152:155], v[184:187], v[4:7]\n"
"ds_read_b64_tr_b16 a[172:173], v193 offset:12544\n"
"buffer_load_dwordx4 v204, s[28:31], s52 offen offset:2048 lds\n"
"v_mfma_f32_16x16x32_bf16 v[4:7], a[156:159], v[188:191], v[4:7]\n"
"s_add_u32 s52, s52, 8192\n"
"ds_read_b64_tr_b16 a[174:175], v193 offset:14592\n"
"v_mfma_f32_16x16x32_bf16 v[8:11], a[192:195], v[128:131], 0\n"
"s_waitcnt lgkmcnt(11)\n"
"ds_read_b64_tr_b16 a[176:177], v193 offset:16640\n"
"buffer_load_dwordx4 a[32:35], v252, s[24:27], s49 offen\n"
"v_mfma_f32_16x16x32_bf16 v[8:11], a[196:199], v[132:135], v[8:11]\n"
"ds_read_b64_tr_b16 a[178:179], v193 offset:18688\n"
"v_mfma_f32_16x16x32_bf16 v[8:11], a[200:203], v[136:139], v[8:11]\n"
"ds_read_b64_tr_b16 a[180:181], v193 offset:20736\n"
"buffer_load_dwordx4 v204, s[28:31], s52 offen offset:3072 lds\n"
"v_mfma_f32_16x16x32_bf16 v[8:11], a[204:207], v[140:143], v[8:11]\n"
"s_add_u32 s52, s52, 12288\n"
"ds_read_b64_tr_b16 a[182:183], v193 offset:22784\n"
"v_mfma_f32_16x16x32_bf16 v[8:11], a[208:211], v[144:147], v[8:11]\n"
"ds_read_b64_tr_b16 a[184:185], v193 offset:24832\n"
"v_cvt_pk_bf16_f32 v36, v4, v5\n"
"v_mfma_f32_16x16x32_bf16 v[8:11], a[212:215], v[148:151], v[8:11]\n"
"s_waitcnt lgkmcnt(11)\n"
"s_add_u32 m0, s53, 36864\n"
"ds_read_b64_tr_b16 a[186:187], v193 offset:26880\n"
"v_cvt_pk_bf16_f32 v37, v6, v7\n"
"v_mfma_f32_16x16x32_bf16 v[8:11], a[216:219], v[152:155], v[8:11]\n"
"ds_read_b64_tr_b16 a[188:189], v193 offset:28928\n"
"buffer_load_dwordx4 v204, s[28:31], s52 offen offset:0 lds\n"
"v_mfma_f32_16x16x32_bf16 v[8:11], a[220:223], v[156:159], v[8:11]\n"
"s_add_u32 s52, s52, 8192\n"
"ds_read_b64_tr_b16 a[190:191], v193 offset:30976\n"
"v_mfma_f32_16x16x32_bf16 v[12:15], a[192:195], v[160:163], 0\n"
"ds_read_b64_tr_b16 a[224:225], v196 offset:256\n"
"buffer_load_dwordx4 a[68:71], v252, s[64:67], s49 offen\n"
"v_mfma_f32_16x16x32_bf16 v[12:15], a[196:199], v[164:167], v[12:15]\n"
"s_add_u32 s49, s49, 589824\n"
"ds_read_b64_tr_b16 a[226:227], v196 offset:2304\n"
"v_mfma_f32_16x16x32_bf16 v[12:15], a[200:203], v[168:171], v[12:15]\n"
"ds_read_b64_tr_b16 a[228:229], v196 offset:4352\n"
"buffer_load_dwordx4 v204, s[28:31], s52 offen offset:1024 lds\n"
"v_mfma_f32_16x16x32_bf16 v[12:15], a[204:207], v[172:175], v[12:15]\n"
"s_add_u32 s52, s52, 8192\n"
"ds_read_b64_tr_b16 a[230:231], v196 offset:6400\n"
"v_mfma_f32_16x16x32_bf16 v[12:15], a[208:211], v[176:179], v[12:15]\n"
"ds_read_b64_tr_b16 a[232:233], v196 offset:8448\n"
"v_cvt_pk_bf16_f32 v34, v8, v9\n"
"v_mfma_f32_16x16x32_bf16 v[12:15], a[212:215], v[180:183], v[12:15]\n"
"ds_read_b64_tr_b16 a[234:235], v196 offset:10496\n"
"v_cvt_pk_bf16_f32 v35, v10, v11\n"
"v_mfma_f32_16x16x32_bf16 v[12:15], a[216:219], v[184:187], v[12:15]\n"
"ds_read_b64_tr_b16 a[236:237], v196 offset:12544\n"
"buffer_load_dwordx4 v204, s[28:31], s52 offen offset:2048 lds\n"
"v_mfma_f32_16x16x32_bf16 v[12:15], a[220:223], v[188:191], v[12:15]\n"
"s_add_u32 s52, s52, 8192\n"
"ds_read_b64_tr_b16 a[238:239], v196 offset:14592\n"
"buffer_store_dwordx4 v[32:35], v205, s[32:35], s54 offen offset:2048 sc1\n"
"v_mfma_f32_16x16x32_bf16 v[16:19], a[160:163], v[128:131], 0\n"
"s_waitcnt lgkmcnt(11)\n"
"ds_read_b64_tr_b16 a[240:241], v196 offset:16640\n"
"v_mfma_f32_16x16x32_bf16 v[16:19], a[164:167], v[132:135], v[16:19]\n"
"ds_read_b64_tr_b16 a[242:243], v196 offset:18688\n"
"v_mfma_f32_16x16x32_bf16 v[16:19], a[168:171], v[136:139], v[16:19]\n"
"ds_read_b64_tr_b16 a[244:245], v196 offset:20736\n"
"buffer_load_dwordx4 v204, s[28:31], s52 offen offset:3072 lds\n"
"v_mfma_f32_16x16x32_bf16 v[16:19], a[172:175], v[140:143], v[16:19]\n"
"s_sub_u32 s52, s52, 61312\n"
"ds_read_b64_tr_b16 a[246:247], v196 offset:22784\n"
"v_mfma_f32_16x16x32_bf16 v[16:19], a[176:179], v[144:147], v[16:19]\n"
"ds_read_b64_tr_b16 a[248:249], v196 offset:24832\n"
"v_cvt_pk_bf16_f32 v38, v12, v13\n"
"v_mfma_f32_16x16x32_bf16 v[16:19], a[180:183], v[148:151], v[16:19]\n"
"s_waitcnt lgkmcnt(11)\n"
"ds_read_b64_tr_b16 a[250:251], v196 offset:26880\n"
"v_cvt_pk_bf16_f32 v39, v14, v15\n"
"v_mfma_f32_16x16x32_bf16 v[16:19], a[184:187], v[152:155], v[16:19]\n"
"ds_read_b64_tr_b16 a[252:253], v196 offset:28928\n"
"v_mfma_f32_16x16x32_bf16 v[16:19], a[188:191], v[156:159], v[16:19]\n"
"ds_read_b64_tr_b16 a[254:255], v196 offset:30976\n"
"buffer_store_dwordx4 v[36:39], v206, s[32:35], s54 offen offset:2048 sc1\n"
"v_mfma_f32_16x16x32_bf16 v[20:23], a[160:163], v[160:163], 0\n"
"ds_read_b64_tr_b16 a[128:129], v193 offset:32768\n"
"v_mfma_f32_16x16x32_bf16 v[20:23], a[164:167], v[164:167], v[20:23]\n"
"ds_read_b64_tr_b16 a[130:131], v193 offset:34816\n"
"buffer_load_dwordx2 a[72:73], v253, s[68:71], s50 offen offset:0\n"
"v_mfma_f32_16x16x32_bf16 v[20:23], a[168:171], v[168:171], v[20:23]\n"
"ds_read_b64_tr_b16 a[132:133], v193 offset:36864\n"
"v_mfma_f32_16x16x32_bf16 v[20:23], a[172:175], v[172:175], v[20:23]\n"
"ds_read_b64_tr_b16 a[134:135], v193 offset:38912\n"
"v_mfma_f32_16x16x32_bf16 v[20:23], a[176:179], v[176:179], v[20:23]\n"
"ds_read_b64_tr_b16 a[136:137], v193 offset:40960\n"
"v_cvt_pk_bf16_f32 v40, v16, v17\n"
"v_mfma_f32_16x16x32_bf16 v[20:23], a[180:183], v[180:183], v[20:23]\n"
"ds_read_b64_tr_b16 a[138:139], v193 offset:43008\n"
"v_cvt_pk_bf16_f32 v41, v18, v19\n"
"v_mfma_f32_16x16x32_bf16 v[20:23], a[184:187], v[184:187], v[20:23]\n"
"ds_read_b64_tr_b16 a[140:141], v193 offset:45056\n"
"v_mfma_f32_16x16x32_bf16 v[20:23], a[188:191], v[188:191], v[20:23]\n"
"ds_read_b64_tr_b16 a[142:143], v193 offset:47104\n"
"buffer_load_dwordx2 a[74:75], v253, s[68:71], s50 offen offset:1152\n"
"v_mfma_f32_16x16x32_bf16 v[24:27], a[224:227], v[128:131], 0\n"
"s_waitcnt lgkmcnt(11)\n"
"ds_read_b64_tr_b16 a[144:145], v193 offset:49152\n"
"v_mfma_f32_16x16x32_bf16 v[24:27], a[228:231], v[132:135], v[24:27]\n"
"ds_read_b64_tr_b16 a[146:147], v193 offset:51200\n"
"v_mfma_f32_16x16x32_bf16 v[24:27], a[232:235], v[136:139], v[24:27]\n"
"ds_read_b64_tr_b16 a[148:149], v193 offset:53248\n"
"v_mfma_f32_16x16x32_bf16 v[24:27], a[236:239], v[140:143], v[24:27]\n"
"ds_read_b64_tr_b16 a[150:151], v193 offset:55296\n"
"v_mfma_f32_16x16x32_bf16 v[24:27], a[240:243], v[144:147], v[24:27]\n"
"ds_read_b64_tr_b16 a[152:153], v193 offset:57344\n"
"v_cvt_pk_bf16_f32 v44, v20, v21\n"
"v_mfma_f32_16x16x32_bf16 v[24:27], a[244:247], v[148:151], v[24:27]\n"
"s_waitcnt lgkmcnt(11)\n"
"ds_read_b64_tr_b16 a[154:155], v193 offset:59392\n"
"v_cvt_pk_bf16_f32 v45, v22, v23\n"
"v_mfma_f32_16x16x32_bf16 v[24:27], a[248:251], v[152:155], v[24:27]\n"
"ds_read_b64_tr_b16 a[156:157], v193 offset:61440\n"
"buffer_load_dwordx2 a[76:77], v253, s[68:71], s50 offen offset:2304\n"
"v_mfma_f32_16x16x32_bf16 v[24:27], a[252:255], v[156:159], v[24:27]\n"
"s_add_u32 s50, s50, 3456\n"
"ds_read_b64_tr_b16 a[158:159], v193 offset:63488\n"
"v_mfma_f32_16x16x32_bf16 v[28:31], a[224:227], v[160:163], 0\n"
"ds_read_b64_tr_b16 a[192:193], v196 offset:32768\n"
"v_mfma_f32_16x16x32_bf16 v[28:31], a[228:231], v[164:167], v[28:31]\n"
"ds_read_b64_tr_b16 a[194:195], v196 offset:34816\n"
"v_mfma_f32_16x16x32_bf16 v[28:31], a[232:235], v[168:171], v[28:31]\n"
"ds_read_b64_tr_b16 a[196:197], v196 offset:36864\n"
"v_mfma_f32_16x16x32_bf16 v[28:31], a[236:239], v[172:175], v[28:31]\n"
"ds_read_b64_tr_b16 a[198:199], v196 offset:38912\n"
"v_mfma_f32_16x16x32_bf16 v[28:31], a[240:243], v[176:179], v[28:31]\n"
"ds_read_b64_tr_b16 a[200:201], v196 offset:40960\n"
"v_cvt_pk_bf16_f32 v42, v24, v25\n"
"v_mfma_f32_16x16x32_bf16 v[28:31], a[244:247], v[180:183], v[28:31]\n"
"ds_read_b64_tr_b16 a[202:203], v196 offset:43008\n"
"v_cvt_pk_bf16_f32 v43, v26, v27\n"
"v_mfma_f32_16x16x32_bf16 v[28:31], a[248:251], v[184:187], v[28:31]\n"
"ds_read_b64_tr_b16 a[204:205], v196 offset:45056\n"
"v_mfma_f32_16x16x32_bf16 v[28:31], a[252:255], v[188:191], v[28:31]\n"
"buffer_store_dwordx4 v[40:43], v205, s[32:35], s54 offen offset:3072 sc1\n"
"ds_read_b64_tr_b16 a[206:207], v196 offset:47104\n"
"v_mfma_f32_16x16x32_bf16 v[0:3], a[128:131], v[128:131], 0\n"
"s_waitcnt lgkmcnt(11)\n"
"ds_read_b64_tr_b16 a[208:209], v196 offset:49152\n"
"v_mfma_f32_16x16x32_bf16 v[0:3], a[132:135], v[132:135], v[0:3]\n"
"ds_read_b64_tr_b16 a[210:211], v196 offset:51200\n"
"buffer_load_dwordx2 a[78:79], v253, s[68:71], s50 offen offset:0\n"
"v_mfma_f32_16x16x32_bf16 v[0:3], a[136:139], v[136:139], v[0:3]\n"
"ds_read_b64_tr_b16 a[212:213], v196 offset:53248\n"
"v_mfma_f32_16x16x32_bf16 v[0:3], a[140:143], v[140:143], v[0:3]\n"
"ds_read_b64_tr_b16 a[214:215], v196 offset:55296\n"
"v_mfma_f32_16x16x32_bf16 v[0:3], a[144:147], v[144:147], v[0:3]\n"
"s_waitcnt vmcnt(36)\n"
"s_barrier\n"
"ds_read_b64_tr_b16 a[216:217], v196 offset:57344\n"
"v_cvt_pk_bf16_f32 v46, v28, v29\n"
"v_mfma_f32_16x16x32_bf16 v[0:3], a[148:151], v[148:151], v[0:3]\n"
"s_waitcnt lgkmcnt(11)\n"
"s_add_u32 m0, s53, 65536\n"
"ds_read_b64_tr_b16 a[218:219], v196 offset:59392\n"
"v_cvt_pk_bf16_f32 v47, v30, v31\n"
"v_mfma_f32_16x16x32_bf16 v[0:3], a[152:155], v[152:155], v[0:3]\n"
"ds_read_b64_tr_b16 a[220:221], v196 offset:61440\n"
"buffer_load_dwordx4 v204, s[28:31], s52 offen offset:0 lds\n"
"v_mfma_f32_16x16x32_bf16 v[0:3], a[156:159], v[156:159], v[0:3]\n"
"s_add_u32 s52, s52, 8192\n"
"buffer_store_dwordx4 v[44:47], v206, s[32:35], s54 offen offset:3072 sc1\n"
"ds_read_b64_tr_b16 a[222:223], v196 offset:63488\n"
"v_mfma_f32_16x16x32_bf16 v[4:7], a[128:131], v[160:163], 0\n"
"s_add_u32 s54, s54, 4096\n"
"ds_read_b64_tr_b16 a[160:161], v193 offset:33024\n"
"v_mfma_f32_16x16x32_bf16 v[4:7], a[132:135], v[164:167], v[4:7]\n"
"ds_read_b64_tr_b16 a[162:163], v193 offset:35072\n"
"v_mfma_f32_16x16x32_bf16 v[4:7], a[136:139], v[168:171], v[4:7]\n"
"ds_read_b64_tr_b16 a[164:165], v193 offset:37120\n"
"buffer_load_dwordx4 v204, s[28:31], s52 offen offset:1024 lds\n"
"v_mfma_f32_16x16x32_bf16 v[4:7], a[140:143], v[172:175], v[4:7]\n"
"s_add_u32 s52, s52, 8192\n"
"ds_read_b64_tr_b16 a[166:167], v193 offset:39168\n"
"v_mfma_f32_16x16x32_bf16 v[4:7], a[144:147], v[176:179], v[4:7]\n"
"ds_read_b64_tr_b16 a[168:169], v193 offset:41216\n"
"v_cvt_pk_bf16_f32 v32, v0, v1\n"
"v_mfma_f32_16x16x32_bf16 v[4:7], a[148:151], v[180:183], v[4:7]\n"
"ds_read_b64_tr_b16 a[170:171], v193 offset:43264\n"
"v_cvt_pk_bf16_f32 v33, v2, v3\n"
"v_mfma_f32_16x16x32_bf16 v[4:7], a[152:155], v[184:187], v[4:7]\n"
"ds_read_b64_tr_b16 a[172:173], v193 offset:45312\n"
"buffer_load_dwordx4 v204, s[28:31], s52 offen offset:2048 lds\n"
"v_mfma_f32_16x16x32_bf16 v[4:7], a[156:159], v[188:191], v[4:7]\n"
"s_add_u32 s52, s52, 8192\n"
"ds_read_b64_tr_b16 a[174:175], v193 offset:47360\n"
"v_mfma_f32_16x16x32_bf16 v[8:11], a[192:195], v[128:131], 0\n"
"s_waitcnt lgkmcnt(11)\n"
"ds_read_b64_tr_b16 a[176:177], v193 offset:49408\n"
"buffer_load_dwordx2 a[80:81], v253, s[68:71], s50 offen offset:1152\n"
"v_mfma_f32_16x16x32_bf16 v[8:11], a[196:199], v[132:135], v[8:11]\n"
"ds_read_b64_tr_b16 a[178:179], v193 offset:51456\n"
"v_mfma_f32_16x16x32_bf16 v[8:11], a[200:203], v[136:139], v[8:11]\n"
"ds_read_b64_tr_b16 a[180:181], v193 offset:53504\n"
"buffer_load_dwordx4 v204, s[28:31], s52 offen offset:3072 lds\n"
"v_mfma_f32_16x16x32_bf16 v[8:11], a[204:207], v[140:143], v[8:11]\n"
"s_add_u32 s52, s52, 12288\n"
"ds_read_b64_tr_b16 a[182:183], v193 offset:55552\n"
"v_mfma_f32_16x16x32_bf16 v[8:11], a[208:211], v[144:147], v[8:11]\n"
"ds_read_b64_tr_b16 a[184:185], v193 offset:57600\n"
"v_cvt_pk_bf16_f32 v36, v4, v5\n"
"v_mfma_f32_16x16x32_bf16 v[8:11], a[212:215], v[148:151], v[8:11]\n"
"s_waitcnt lgkmcnt(11)\n"
"s_add_u32 m0, s53, 69632\n"
"ds_read_b64_tr_b16 a[186:187], v193 offset:59648\n"
"v_cvt_pk_bf16_f32 v37, v6, v7\n"
"v_mfma_f32_16x16x32_bf16 v[8:11], a[216:219], v[152:155], v[8:11]\n"
"ds_read_b64_tr_b16 a[188:189], v193 offset:61696\n"
"buffer_load_dwordx4 v204, s[28:31], s52 offen offset:0 lds\n"
"v_mfma_f32_16x16x32_bf16 v[8:11], a[220:223], v[156:159], v[8:11]\n"
"s_add_u32 s52, s52, 8192\n"
"ds_read_b64_tr_b16 a[190:191], v193 offset:63744\n"
"v_mfma_f32_16x16x32_bf16 v[12:15], a[192:195], v[160:163], 0\n"
"ds_read_b64_tr_b16 a[224:225], v196 offset:33024\n"
"buffer_load_dwordx2 a[82:83], v253, s[68:71], s50 offen offset:2304\n"
"v_mfma_f32_16x16x32_bf16 v[12:15], a[196:199], v[164:167], v[12:15]\n"
"s_add_u32 s50, s50, 3456\n"
"ds_read_b64_tr_b16 a[226:227], v196 offset:35072\n"
"v_mfma_f32_16x16x32_bf16 v[12:15], a[200:203], v[168:171], v[12:15]\n"
"ds_read_b64_tr_b16 a[228:229], v196 offset:37120\n"
"buffer_load_dwordx4 v204, s[28:31], s52 offen offset:1024 lds\n"
"v_mfma_f32_16x16x32_bf16 v[12:15], a[204:207], v[172:175], v[12:15]\n"
"s_add_u32 s52, s52, 8192\n"
"ds_read_b64_tr_b16 a[230:231], v196 offset:39168\n"
"v_mfma_f32_16x16x32_bf16 v[12:15], a[208:211], v[176:179], v[12:15]\n"
"ds_read_b64_tr_b16 a[232:233], v196 offset:41216\n"
"v_cvt_pk_bf16_f32 v34, v8, v9\n"
"v_mfma_f32_16x16x32_bf16 v[12:15], a[212:215], v[180:183], v[12:15]\n"
"ds_read_b64_tr_b16 a[234:235], v196 offset:43264\n"
"v_cvt_pk_bf16_f32 v35, v10, v11\n"
"v_mfma_f32_16x16x32_bf16 v[12:15], a[216:219], v[184:187], v[12:15]\n"
"ds_read_b64_tr_b16 a[236:237], v196 offset:45312\n"
"buffer_load_dwordx4 v204, s[28:31], s52 offen offset:2048 lds\n"
"v_mfma_f32_16x16x32_bf16 v[12:15], a[220:223], v[188:191], v[12:15]\n"
"s_add_u32 s52, s52, 8192\n"
"ds_read_b64_tr_b16 a[238:239], v196 offset:47360\n"
"buffer_store_dwordx4 v[32:35], v205, s[32:35], s54 offen offset:0 sc1\n"
"v_mfma_f32_16x16x32_bf16 v[16:19], a[160:163], v[128:131], 0\n"
"s_waitcnt lgkmcnt(11)\n"
"ds_read_b64_tr_b16 a[240:241], v196 offset:49408\n"
"v_mfma_f32_16x16x32_bf16 v[16:19], a[164:167], v[132:135], v[16:19]\n"
"ds_read_b64_tr_b16 a[242:243], v196 offset:51456\n"
"v_mfma_f32_16x16x32_bf16 v[16:19], a[168:171], v[136:139], v[16:19]\n"
"ds_read_b64_tr_b16 a[244:245], v196 offset:53504\n"
"buffer_load_dwordx4 v204, s[28:31], s52 offen offset:3072 lds\n"
"v_mfma_f32_16x16x32_bf16 v[16:19], a[172:175], v[140:143], v[16:19]\n"
"s_sub_u32 s52, s52, 61312\n"
"ds_read_b64_tr_b16 a[246:247], v196 offset:55552\n"
"v_mfma_f32_16x16x32_bf16 v[16:19], a[176:179], v[144:147], v[16:19]\n"
"ds_read_b64_tr_b16 a[248:249], v196 offset:57600\n"
"v_cvt_pk_bf16_f32 v38, v12, v13\n"
"v_mfma_f32_16x16x32_bf16 v[16:19], a[180:183], v[148:151], v[16:19]\n"
"s_waitcnt lgkmcnt(11)\n"
"ds_read_b64_tr_b16 a[250:251], v196 offset:59648\n"
"v_cvt_pk_bf16_f32 v39, v14, v15\n"
"v_mfma_f32_16x16x32_bf16 v[16:19], a[184:187], v[152:155], v[16:19]\n"
"ds_read_b64_tr_b16 a[252:253], v196 offset:61696\n"
"v_mfma_f32_16x16x32_bf16 v[16:19], a[188:191], v[156:159], v[16:19]\n"
"ds_read_b64_tr_b16 a[254:255], v196 offset:63744\n"
"buffer_store_dwordx4 v[36:39], v206, s[32:35], s54 offen offset:0 sc1\n"
"v_mfma_f32_16x16x32_bf16 v[20:23], a[160:163], v[160:163], 0\n"
"ds_read_b64_tr_b16 a[128:129], v194 offset:0\n"
"v_mfma_f32_16x16x32_bf16 v[20:23], a[164:167], v[164:167], v[20:23]\n"
"ds_read_b64_tr_b16 a[130:131], v194 offset:2048\n"
"buffer_load_dwordx2 a[84:85], v253, s[68:71], s50 offen offset:0\n"
"v_mfma_f32_16x16x32_bf16 v[20:23], a[168:171], v[168:171], v[20:23]\n"
"ds_read_b64_tr_b16 a[132:133], v194 offset:4096\n"
"v_mfma_f32_16x16x32_bf16 v[20:23], a[172:175], v[172:175], v[20:23]\n"
"ds_read_b64_tr_b16 a[134:135], v194 offset:6144\n"
"v_mfma_f32_16x16x32_bf16 v[20:23], a[176:179], v[176:179], v[20:23]\n"
"ds_read_b64_tr_b16 a[136:137], v194 offset:8192\n"
"v_cvt_pk_bf16_f32 v40, v16, v17\n"
"v_mfma_f32_16x16x32_bf16 v[20:23], a[180:183], v[180:183], v[20:23]\n"
"ds_read_b64_tr_b16 a[138:139], v194 offset:10240\n"
"v_cvt_pk_bf16_f32 v41, v18, v19\n"
"v_mfma_f32_16x16x32_bf16 v[20:23], a[184:187], v[184:187], v[20:23]\n"
"ds_read_b64_tr_b16 a[140:141], v194 offset:12288\n"
"v_mfma_f32_16x16x32_bf16 v[20:23], a[188:191], v[188:191], v[20:23]\n"
"ds_read_b64_tr_b16 a[142:143], v194 offset:14336\n"
"buffer_load_dwordx2 a[86:87], v253, s[68:71], s50 offen offset:1152\n"
"v_mfma_f32_16x16x32_bf16 v[24:27], a[224:227], v[128:131], 0\n"
"s_waitcnt lgkmcnt(11)\n"
"ds_read_b64_tr_b16 a[144:145], v194 offset:16384\n"
"v_mfma_f32_16x16x32_bf16 v[24:27], a[228:231], v[132:135], v[24:27]\n"
"ds_read_b64_tr_b16 a[146:147], v194 offset:18432\n"
"v_mfma_f32_16x16x32_bf16 v[24:27], a[232:235], v[136:139], v[24:27]\n"
"ds_read_b64_tr_b16 a[148:149], v194 offset:20480\n"
"v_mfma_f32_16x16x32_bf16 v[24:27], a[236:239], v[140:143], v[24:27]\n"
"ds_read_b64_tr_b16 a[150:151], v194 offset:22528\n"
"v_mfma_f32_16x16x32_bf16 v[24:27], a[240:243], v[144:147], v[24:27]\n"
"ds_read_b64_tr_b16 a[152:153], v194 offset:24576\n"
"v_cvt_pk_bf16_f32 v44, v20, v21\n"
"v_mfma_f32_16x16x32_bf16 v[24:27], a[244:247], v[148:151], v[24:27]\n"
"s_waitcnt lgkmcnt(11)\n"
"ds_read_b64_tr_b16 a[154:155], v194 offset:26624\n"
"v_cvt_pk_bf16_f32 v45, v22, v23\n"
"v_mfma_f32_16x16x32_bf16 v[24:27], a[248:251], v[152:155], v[24:27]\n"
"ds_read_b64_tr_b16 a[156:157], v194 offset:28672\n"
"buffer_load_dwordx2 a[88:89], v253, s[68:71], s50 offen offset:2304\n"
"v_mfma_f32_16x16x32_bf16 v[24:27], a[252:255], v[156:159], v[24:27]\n"
"s_add_u32 s50, s50, 3456\n"
"ds_read_b64_tr_b16 a[158:159], v194 offset:30720\n"
"v_mfma_f32_16x16x32_bf16 v[28:31], a[224:227], v[160:163], 0\n"
"ds_read_b64_tr_b16 a[192:193], v197 offset:0\n"
"v_mfma_f32_16x16x32_bf16 v[28:31], a[228:231], v[164:167], v[28:31]\n"
"ds_read_b64_tr_b16 a[194:195], v197 offset:2048\n"
"v_mfma_f32_16x16x32_bf16 v[28:31], a[232:235], v[168:171], v[28:31]\n"
"ds_read_b64_tr_b16 a[196:197], v197 offset:4096\n"
"v_mfma_f32_16x16x32_bf16 v[28:31], a[236:239], v[172:175], v[28:31]\n"
"ds_read_b64_tr_b16 a[198:199], v197 offset:6144\n"
"v_mfma_f32_16x16x32_bf16 v[28:31], a[240:243], v[176:179], v[28:31]\n"
"ds_read_b64_tr_b16 a[200:201], v197 offset:8192\n"
"v_cvt_pk_bf16_f32 v42, v24, v25\n"
"v_mfma_f32_16x16x32_bf16 v[28:31], a[244:247], v[180:183], v[28:31]\n"
"ds_read_b64_tr_b16 a[202:203], v197 offset:10240\n"
"v_cvt_pk_bf16_f32 v43, v26, v27\n"
"v_mfma_f32_16x16x32_bf16 v[28:31], a[248:251], v[184:187], v[28:31]\n"
"ds_read_b64_tr_b16 a[204:205], v197 offset:12288\n"
"v_mfma_f32_16x16x32_bf16 v[28:31], a[252:255], v[188:191], v[28:31]\n"
"buffer_store_dwordx4 v[40:43], v205, s[32:35], s54 offen offset:1024 sc1\n"
"ds_read_b64_tr_b16 a[206:207], v197 offset:14336\n"
"v_mfma_f32_16x16x32_bf16 v[0:3], a[128:131], v[128:131], 0\n"
"s_waitcnt lgkmcnt(11)\n"
"ds_read_b64_tr_b16 a[208:209], v197 offset:16384\n"
"v_mfma_f32_16x16x32_bf16 v[0:3], a[132:135], v[132:135], v[0:3]\n"
"ds_read_b64_tr_b16 a[210:211], v197 offset:18432\n"
"buffer_load_dwordx2 a[90:91], v253, s[68:71], s50 offen offset:0\n"
"v_mfma_f32_16x16x32_bf16 v[0:3], a[136:139], v[136:139], v[0:3]\n"
"ds_read_b64_tr_b16 a[212:213], v197 offset:20480\n"
"v_mfma_f32_16x16x32_bf16 v[0:3], a[140:143], v[140:143], v[0:3]\n"
"ds_read_b64_tr_b16 a[214:215], v197 offset:22528\n"
"v_mfma_f32_16x16x32_bf16 v[0:3], a[144:147], v[144:147], v[0:3]\n"
"s_waitcnt vmcnt(42)\n"
"s_barrier\n"
"ds_read_b64_tr_b16 a[216:217], v197 offset:24576\n"
"v_cvt_pk_bf16_f32 v46, v28, v29\n"
"v_mfma_f32_16x16x32_bf16 v[0:3], a[148:151], v[148:151], v[0:3]\n"
"s_waitcnt lgkmcnt(11)\n"
"ds_read_b64_tr_b16 a[218:219], v197 offset:26624\n"
"v_cvt_pk_bf16_f32 v47, v30, v31\n"
"v_mfma_f32_16x16x32_bf16 v[0:3], a[152:155], v[152:155], v[0:3]\n"
"ds_read_b64_tr_b16 a[220:221], v197 offset:28672\n"
"v_mfma_f32_16x16x32_bf16 v[0:3], a[156:159], v[156:159], v[0:3]\n"
"buffer_store_dwordx4 v[44:47], v206, s[32:35], s54 offen offset:1024 sc1\n"
"ds_read_b64_tr_b16 a[222:223], v197 offset:30720\n"
"v_mfma_f32_16x16x32_bf16 v[4:7], a[128:131], v[160:163], 0\n"
"ds_read_b64_tr_b16 a[160:161], v194 offset:256\n"
"v_mfma_f32_16x16x32_bf16 v[4:7], a[132:135], v[164:167], v[4:7]\n"
"ds_read_b64_tr_b16 a[162:163], v194 offset:2304\n"
"buffer_load_dwordx2 a[92:93], v253, s[68:71], s50 offen offset:1152\n"
"v_mfma_f32_16x16x32_bf16 v[4:7], a[136:139], v[168:171], v[4:7]\n"
"ds_read_b64_tr_b16 a[164:165], v194 offset:4352\n"
"v_mfma_f32_16x16x32_bf16 v[4:7], a[140:143], v[172:175], v[4:7]\n"
"ds_read_b64_tr_b16 a[166:167], v194 offset:6400\n"
"v_mfma_f32_16x16x32_bf16 v[4:7], a[144:147], v[176:179], v[4:7]\n"
"ds_read_b64_tr_b16 a[168:169], v194 offset:8448\n"
"v_cvt_pk_bf16_f32 v32, v0, v1\n"
"v_mfma_f32_16x16x32_bf16 v[4:7], a[148:151], v[180:183], v[4:7]\n"
"ds_read_b64_tr_b16 a[170:171], v194 offset:10496\n"
"v_cvt_pk_bf16_f32 v33, v2, v3\n"
"v_mfma_f32_16x16x32_bf16 v[4:7], a[152:155], v[184:187], v[4:7]\n"
"ds_read_b64_tr_b16 a[172:173], v194 offset:12544\n"
"v_mfma_f32_16x16x32_bf16 v[4:7], a[156:159], v[188:191], v[4:7]\n"
"ds_read_b64_tr_b16 a[174:175], v194 offset:14592\n"
"buffer_load_dwordx2 a[94:95], v253, s[68:71], s50 offen offset:2304\n"
"v_mfma_f32_16x16x32_bf16 v[8:11], a[192:195], v[128:131], 0\n"
"s_waitcnt lgkmcnt(11)\n"
"s_add_u32 s50, s50, 3456\n"
"ds_read_b64_tr_b16 a[176:177], v194 offset:16640\n"
"v_mfma_f32_16x16x32_bf16 v[8:11], a[196:199], v[132:135], v[8:11]\n"
"ds_read_b64_tr_b16 a[178:179], v194 offset:18688\n"
"v_mfma_f32_16x16x32_bf16 v[8:11], a[200:203], v[136:139], v[8:11]\n"
"ds_read_b64_tr_b16 a[180:181], v194 offset:20736\n"
"v_mfma_f32_16x16x32_bf16 v[8:11], a[204:207], v[140:143], v[8:11]\n"
"ds_read_b64_tr_b16 a[182:183], v194 offset:22784\n"
"v_mfma_f32_16x16x32_bf16 v[8:11], a[208:211], v[144:147], v[8:11]\n"
"ds_read_b64_tr_b16 a[184:185], v194 offset:24832\n"
"v_cvt_pk_bf16_f32 v36, v4, v5\n"
"v_mfma_f32_16x16x32_bf16 v[8:11], a[212:215], v[148:151], v[8:11]\n"
"s_waitcnt lgkmcnt(11)\n"
"ds_read_b64_tr_b16 a[186:187], v194 offset:26880\n"
"v_cvt_pk_bf16_f32 v37, v6, v7\n"
"v_mfma_f32_16x16x32_bf16 v[8:11], a[216:219], v[152:155], v[8:11]\n"
"ds_read_b64_tr_b16 a[188:189], v194 offset:28928\n"
"buffer_load_dwordx2 a[96:97], v253, s[68:71], s50 offen offset:0\n"
"v_mfma_f32_16x16x32_bf16 v[8:11], a[220:223], v[156:159], v[8:11]\n"
"ds_read_b64_tr_b16 a[190:191], v194 offset:30976\n"
"v_mfma_f32_16x16x32_bf16 v[12:15], a[192:195], v[160:163], 0\n"
"ds_read_b64_tr_b16 a[224:225], v197 offset:256\n"
"v_mfma_f32_16x16x32_bf16 v[12:15], a[196:199], v[164:167], v[12:15]\n"
"ds_read_b64_tr_b16 a[226:227], v197 offset:2304\n"
"v_mfma_f32_16x16x32_bf16 v[12:15], a[200:203], v[168:171], v[12:15]\n"
"ds_read_b64_tr_b16 a[228:229], v197 offset:4352\n"
"v_mfma_f32_16x16x32_bf16 v[12:15], a[204:207], v[172:175], v[12:15]\n"
"ds_read_b64_tr_b16 a[230:231], v197 offset:6400\n"
"v_mfma_f32_16x16x32_bf16 v[12:15], a[208:211], v[176:179], v[12:15]\n"
"ds_read_b64_tr_b16 a[232:233], v197 offset:8448\n"
"v_cvt_pk_bf16_f32 v34, v8, v9\n"
"v_mfma_f32_16x16x32_bf16 v[12:15], a[212:215], v[180:183], v[12:15]\n"
"ds_read_b64_tr_b16 a[234:235], v197 offset:10496\n"
"v_cvt_pk_bf16_f32 v35, v10, v11\n"
"v_mfma_f32_16x16x32_bf16 v[12:15], a[216:219], v[184:187], v[12:15]\n"
"ds_read_b64_tr_b16 a[236:237], v197 offset:12544\n"
"v_mfma_f32_16x16x32_bf16 v[12:15], a[220:223], v[188:191], v[12:15]\n"
"ds_read_b64_tr_b16 a[238:239], v197 offset:14592\n"
"buffer_store_dwordx4 v[32:35], v205, s[32:35], s54 offen offset:2048 sc1\n"
"v_mfma_f32_16x16x32_bf16 v[16:19], a[160:163], v[128:131], 0\n"
"s_waitcnt lgkmcnt(11)\n"
"ds_read_b64_tr_b16 a[240:241], v197 offset:16640\n"
"v_mfma_f32_16x16x32_bf16 v[16:19], a[164:167], v[132:135], v[16:19]\n"
"ds_read_b64_tr_b16 a[242:243], v197 offset:18688\n"
"buffer_load_dwordx2 a[98:99], v253, s[68:71], s50 offen offset:1152\n"
"v_mfma_f32_16x16x32_bf16 v[16:19], a[168:171], v[136:139], v[16:19]\n"
"ds_read_b64_tr_b16 a[244:245], v197 offset:20736\n"
"v_mfma_f32_16x16x32_bf16 v[16:19], a[172:175], v[140:143], v[16:19]\n"
"ds_read_b64_tr_b16 a[246:247], v197 offset:22784\n"
"v_mfma_f32_16x16x32_bf16 v[16:19], a[176:179], v[144:147], v[16:19]\n"
"ds_read_b64_tr_b16 a[248:249], v197 offset:24832\n"
"v_cvt_pk_bf16_f32 v38, v12, v13\n"
"v_mfma_f32_16x16x32_bf16 v[16:19], a[180:183], v[148:151], v[16:19]\n"
"s_waitcnt lgkmcnt(11)\n"
"ds_read_b64_tr_b16 a[250:251], v197 offset:26880\n"
"v_cvt_pk_bf16_f32 v39, v14, v15\n"
"v_mfma_f32_16x16x32_bf16 v[16:19], a[184:187], v[152:155], v[16:19]\n"
"ds_read_b64_tr_b16 a[252:253], v197 offset:28928\n"
"v_mfma_f32_16x16x32_bf16 v[16:19], a[188:191], v[156:159], v[16:19]\n"
"ds_read_b64_tr_b16 a[254:255], v197 offset:30976\n"
"buffer_store_dwordx4 v[36:39], v206, s[32:35], s54 offen offset:2048 sc1\n"
"v_mfma_f32_16x16x32_bf16 v[20:23], a[160:163], v[160:163], 0\n"
"ds_read_b64_tr_b16 a[128:129], v192 offset:0\n"
"v_mfma_f32_16x16x32_bf16 v[20:23], a[164:167], v[164:167], v[20:23]\n"
"ds_read_b64_tr_b16 a[130:131], v192 offset:2048\n"
"buffer_load_dwordx2 a[100:101], v253, s[68:71], s50 offen offset:2304\n"
"v_mfma_f32_16x16x32_bf16 v[20:23], a[168:171], v[168:171], v[20:23]\n"
"s_add_u32 s50, s50, 3456\n"
"ds_read_b64_tr_b16 a[132:133], v192 offset:4096\n"
"v_mfma_f32_16x16x32_bf16 v[20:23], a[172:175], v[172:175], v[20:23]\n"
"ds_read_b64_tr_b16 a[134:135], v192 offset:6144\n"
"v_mfma_f32_16x16x32_bf16 v[20:23], a[176:179], v[176:179], v[20:23]\n"
"ds_read_b64_tr_b16 a[136:137], v192 offset:8192\n"
"v_cvt_pk_bf16_f32 v40, v16, v17\n"
"v_mfma_f32_16x16x32_bf16 v[20:23], a[180:183], v[180:183], v[20:23]\n"
"ds_read_b64_tr_b16 a[138:139], v192 offset:10240\n"
"v_cvt_pk_bf16_f32 v41, v18, v19\n"
"v_mfma_f32_16x16x32_bf16 v[20:23], a[184:187], v[184:187], v[20:23]\n"
"ds_read_b64_tr_b16 a[140:141], v192 offset:12288\n"
"v_mfma_f32_16x16x32_bf16 v[20:23], a[188:191], v[188:191], v[20:23]\n"
"ds_read_b64_tr_b16 a[142:143], v192 offset:14336\n"
"buffer_load_dwordx2 a[102:103], v253, s[68:71], s50 offen offset:0\n"
"v_mfma_f32_16x16x32_bf16 v[24:27], a[224:227], v[128:131], 0\n"
"s_waitcnt lgkmcnt(11)\n"
"ds_read_b64_tr_b16 a[144:145], v192 offset:16384\n"
"v_mfma_f32_16x16x32_bf16 v[24:27], a[228:231], v[132:135], v[24:27]\n"
"ds_read_b64_tr_b16 a[146:147], v192 offset:18432\n"
"v_mfma_f32_16x16x32_bf16 v[24:27], a[232:235], v[136:139], v[24:27]\n"
"ds_read_b64_tr_b16 a[148:149], v192 offset:20480\n"
"v_mfma_f32_16x16x32_bf16 v[24:27], a[236:239], v[140:143], v[24:27]\n"
"ds_read_b64_tr_b16 a[150:151], v192 offset:22528\n"
"v_mfma_f32_16x16x32_bf16 v[24:27], a[240:243], v[144:147], v[24:27]\n"
"ds_read_b64_tr_b16 a[152:153], v192 offset:24576\n"
"v_cvt_pk_bf16_f32 v44, v20, v21\n"
"v_mfma_f32_16x16x32_bf16 v[24:27], a[244:247], v[148:151], v[24:27]\n"
"s_waitcnt lgkmcnt(11)\n"
"ds_read_b64_tr_b16 a[154:155], v192 offset:26624\n"
"v_cvt_pk_bf16_f32 v45, v22, v23\n"
"v_mfma_f32_16x16x32_bf16 v[24:27], a[248:251], v[152:155], v[24:27]\n"
"ds_read_b64_tr_b16 a[156:157], v192 offset:28672\n"
"buffer_load_dwordx2 a[104:105], v253, s[68:71], s50 offen offset:1152\n"
"v_mfma_f32_16x16x32_bf16 v[24:27], a[252:255], v[156:159], v[24:27]\n"
"ds_read_b64_tr_b16 a[158:159], v192 offset:30720\n"
"v_mfma_f32_16x16x32_bf16 v[28:31], a[224:227], v[160:163], 0\n"
"ds_read_b64_tr_b16 a[192:193], v195 offset:0\n"
"v_mfma_f32_16x16x32_bf16 v[28:31], a[228:231], v[164:167], v[28:31]\n"
"ds_read_b64_tr_b16 a[194:195], v195 offset:2048\n"
"v_mfma_f32_16x16x32_bf16 v[28:31], a[232:235], v[168:171], v[28:31]\n"
"ds_read_b64_tr_b16 a[196:197], v195 offset:4096\n"
"v_mfma_f32_16x16x32_bf16 v[28:31], a[236:239], v[172:175], v[28:31]\n"
"ds_read_b64_tr_b16 a[198:199], v195 offset:6144\n"
"v_mfma_f32_16x16x32_bf16 v[28:31], a[240:243], v[176:179], v[28:31]\n"
"ds_read_b64_tr_b16 a[200:201], v195 offset:8192\n"
"v_cvt_pk_bf16_f32 v42, v24, v25\n"
"v_mfma_f32_16x16x32_bf16 v[28:31], a[244:247], v[180:183], v[28:31]\n"
"ds_read_b64_tr_b16 a[202:203], v195 offset:10240\n"
"v_cvt_pk_bf16_f32 v43, v26, v27\n"
"v_mfma_f32_16x16x32_bf16 v[28:31], a[248:251], v[184:187], v[28:31]\n"
"ds_read_b64_tr_b16 a[204:205], v195 offset:12288\n"
"v_mfma_f32_16x16x32_bf16 v[28:31], a[252:255], v[188:191], v[28:31]\n"
"buffer_store_dwordx4 v[40:43], v205, s[32:35], s54 offen offset:3072 sc1\n"
"ds_read_b64_tr_b16 a[206:207], v195 offset:14336\n"
"v_mfma_f32_16x16x32_bf16 v[0:3], a[128:131], v[128:131], 0\n"
"s_waitcnt lgkmcnt(11)\n"
"ds_read_b64_tr_b16 a[208:209], v195 offset:16384\n"
"v_mfma_f32_16x16x32_bf16 v[0:3], a[132:135], v[132:135], v[0:3]\n"
"ds_read_b64_tr_b16 a[210:211], v195 offset:18432\n"
"buffer_load_dwordx2 a[106:107], v253, s[68:71], s50 offen offset:2304\n"
"v_mfma_f32_16x16x32_bf16 v[0:3], a[136:139], v[136:139], v[0:3]\n"
"s_add_u32 s50, s50, 3456\n"
"ds_read_b64_tr_b16 a[212:213], v195 offset:20480\n"
"v_mfma_f32_16x16x32_bf16 v[0:3], a[140:143], v[140:143], v[0:3]\n"
"ds_read_b64_tr_b16 a[214:215], v195 offset:22528\n"
"v_mfma_f32_16x16x32_bf16 v[0:3], a[144:147], v[144:147], v[0:3]\n"
"s_waitcnt vmcnt(36)\n"
"s_barrier\n"
"ds_read_b64_tr_b16 a[216:217], v195 offset:24576\n"
"v_cvt_pk_bf16_f32 v46, v28, v29\n"
"v_mfma_f32_16x16x32_bf16 v[0:3], a[148:151], v[148:151], v[0:3]\n"
"s_waitcnt lgkmcnt(11)\n"
"ds_read_b64_tr_b16 a[218:219], v195 offset:26624\n"
"v_cvt_pk_bf16_f32 v47, v30, v31\n"
"v_mfma_f32_16x16x32_bf16 v[0:3], a[152:155], v[152:155], v[0:3]\n"
"ds_read_b64_tr_b16 a[220:221], v195 offset:28672\n"
"v_mfma_f32_16x16x32_bf16 v[0:3], a[156:159], v[156:159], v[0:3]\n"
"buffer_store_dwordx4 v[44:47], v206, s[32:35], s54 offen offset:3072 sc1\n"
"ds_read_b64_tr_b16 a[222:223], v195 offset:30720\n"
"v_mfma_f32_16x16x32_bf16 v[4:7], a[128:131], v[160:163], 0\n"
"s_add_u32 s54, s54, 4096\n"
"ds_read_b64_tr_b16 a[160:161], v192 offset:256\n"
"v_mfma_f32_16x16x32_bf16 v[4:7], a[132:135], v[164:167], v[4:7]\n"
"ds_read_b64_tr_b16 a[162:163], v192 offset:2304\n"
"buffer_load_dwordx2 a[108:109], v253, s[68:71], s50 offen offset:0\n"
"v_mfma_f32_16x16x32_bf16 v[4:7], a[136:139], v[168:171], v[4:7]\n"
"ds_read_b64_tr_b16 a[164:165], v192 offset:4352\n"
"v_mfma_f32_16x16x32_bf16 v[4:7], a[140:143], v[172:175], v[4:7]\n"
"ds_read_b64_tr_b16 a[166:167], v192 offset:6400\n"
"v_mfma_f32_16x16x32_bf16 v[4:7], a[144:147], v[176:179], v[4:7]\n"
"ds_read_b64_tr_b16 a[168:169], v192 offset:8448\n"
"v_cvt_pk_bf16_f32 v32, v0, v1\n"
"v_mfma_f32_16x16x32_bf16 v[4:7], a[148:151], v[180:183], v[4:7]\n"
"ds_read_b64_tr_b16 a[170:171], v192 offset:10496\n"
"v_cvt_pk_bf16_f32 v33, v2, v3\n"
"v_mfma_f32_16x16x32_bf16 v[4:7], a[152:155], v[184:187], v[4:7]\n"
"ds_read_b64_tr_b16 a[172:173], v192 offset:12544\n"
"v_mfma_f32_16x16x32_bf16 v[4:7], a[156:159], v[188:191], v[4:7]\n"
"ds_read_b64_tr_b16 a[174:175], v192 offset:14592\n"
"buffer_load_dwordx2 a[110:111], v253, s[68:71], s50 offen offset:1152\n"
"v_mfma_f32_16x16x32_bf16 v[8:11], a[192:195], v[128:131], 0\n"
"s_waitcnt lgkmcnt(11)\n"
"ds_read_b64_tr_b16 a[176:177], v192 offset:16640\n"
"v_mfma_f32_16x16x32_bf16 v[8:11], a[196:199], v[132:135], v[8:11]\n"
"ds_read_b64_tr_b16 a[178:179], v192 offset:18688\n"
"v_mfma_f32_16x16x32_bf16 v[8:11], a[200:203], v[136:139], v[8:11]\n"
"ds_read_b64_tr_b16 a[180:181], v192 offset:20736\n"
"v_mfma_f32_16x16x32_bf16 v[8:11], a[204:207], v[140:143], v[8:11]\n"
"ds_read_b64_tr_b16 a[182:183], v192 offset:22784\n"
"v_mfma_f32_16x16x32_bf16 v[8:11], a[208:211], v[144:147], v[8:11]\n"
"ds_read_b64_tr_b16 a[184:185], v192 offset:24832\n"
"v_cvt_pk_bf16_f32 v36, v4, v5\n"
"v_mfma_f32_16x16x32_bf16 v[8:11], a[212:215], v[148:151], v[8:11]\n"
"s_waitcnt lgkmcnt(11)\n"
"ds_read_b64_tr_b16 a[186:187], v192 offset:26880\n"
"v_cvt_pk_bf16_f32 v37, v6, v7\n"
"v_mfma_f32_16x16x32_bf16 v[8:11], a[216:219], v[152:155], v[8:11]\n"
"ds_read_b64_tr_b16 a[188:189], v192 offset:28928\n"
"buffer_load_dwordx2 a[112:113], v253, s[68:71], s50 offen offset:2304\n"
"v_mfma_f32_16x16x32_bf16 v[8:11], a[220:223], v[156:159], v[8:11]\n"
"s_add_u32 s50, s50, 3456\n"
"ds_read_b64_tr_b16 a[190:191], v192 offset:30976\n"
"v_mfma_f32_16x16x32_bf16 v[12:15], a[192:195], v[160:163], 0\n"
"ds_read_b64_tr_b16 a[224:225], v195 offset:256\n"
"v_mfma_f32_16x16x32_bf16 v[12:15], a[196:199], v[164:167], v[12:15]\n"
"ds_read_b64_tr_b16 a[226:227], v195 offset:2304\n"
"v_mfma_f32_16x16x32_bf16 v[12:15], a[200:203], v[168:171], v[12:15]\n"
"ds_read_b64_tr_b16 a[228:229], v195 offset:4352\n"
"v_mfma_f32_16x16x32_bf16 v[12:15], a[204:207], v[172:175], v[12:15]\n"
"ds_read_b64_tr_b16 a[230:231], v195 offset:6400\n"
"v_mfma_f32_16x16x32_bf16 v[12:15], a[208:211], v[176:179], v[12:15]\n"
"ds_read_b64_tr_b16 a[232:233], v195 offset:8448\n"
"v_cvt_pk_bf16_f32 v34, v8, v9\n"
"v_mfma_f32_16x16x32_bf16 v[12:15], a[212:215], v[180:183], v[12:15]\n"
"ds_read_b64_tr_b16 a[234:235], v195 offset:10496\n"
"v_cvt_pk_bf16_f32 v35, v10, v11\n"
"v_mfma_f32_16x16x32_bf16 v[12:15], a[216:219], v[184:187], v[12:15]\n"
"ds_read_b64_tr_b16 a[236:237], v195 offset:12544\n"
"v_mfma_f32_16x16x32_bf16 v[12:15], a[220:223], v[188:191], v[12:15]\n"
"ds_read_b64_tr_b16 a[238:239], v195 offset:14592\n"
"buffer_store_dwordx4 v[32:35], v205, s[32:35], s54 offen offset:0 sc1\n"
"v_mfma_f32_16x16x32_bf16 v[16:19], a[160:163], v[128:131], 0\n"
"s_waitcnt lgkmcnt(11)\n"
"ds_read_b64_tr_b16 a[240:241], v195 offset:16640\n"
"v_mfma_f32_16x16x32_bf16 v[16:19], a[164:167], v[132:135], v[16:19]\n"
"ds_read_b64_tr_b16 a[242:243], v195 offset:18688\n"
"buffer_load_dwordx2 a[114:115], v253, s[68:71], s50 offen offset:0\n"
"v_mfma_f32_16x16x32_bf16 v[16:19], a[168:171], v[136:139], v[16:19]\n"
"ds_read_b64_tr_b16 a[244:245], v195 offset:20736\n"
"v_mfma_f32_16x16x32_bf16 v[16:19], a[172:175], v[140:143], v[16:19]\n"
"ds_read_b64_tr_b16 a[246:247], v195 offset:22784\n"
"v_mfma_f32_16x16x32_bf16 v[16:19], a[176:179], v[144:147], v[16:19]\n"
"ds_read_b64_tr_b16 a[248:249], v195 offset:24832\n"
"v_cvt_pk_bf16_f32 v38, v12, v13\n"
"v_mfma_f32_16x16x32_bf16 v[16:19], a[180:183], v[148:151], v[16:19]\n"
"s_waitcnt lgkmcnt(11)\n"
"ds_read_b64_tr_b16 a[250:251], v195 offset:26880\n"
"v_cvt_pk_bf16_f32 v39, v14, v15\n"
"v_mfma_f32_16x16x32_bf16 v[16:19], a[184:187], v[152:155], v[16:19]\n"
"ds_read_b64_tr_b16 a[252:253], v195 offset:28928\n"
"v_mfma_f32_16x16x32_bf16 v[16:19], a[188:191], v[156:159], v[16:19]\n"
"ds_read_b64_tr_b16 a[254:255], v195 offset:30976\n"
"buffer_store_dwordx4 v[36:39], v206, s[32:35], s54 offen offset:0 sc1\n"
"v_mfma_f32_16x16x32_bf16 v[20:23], a[160:163], v[160:163], 0\n"
"ds_read_b64_tr_b16 a[128:129], v192 offset:32768\n"
"v_mfma_f32_16x16x32_bf16 v[20:23], a[164:167], v[164:167], v[20:23]\n"
"ds_read_b64_tr_b16 a[130:131], v192 offset:34816\n"
"buffer_load_dwordx2 a[116:117], v253, s[68:71], s50 offen offset:1152\n"
"v_mfma_f32_16x16x32_bf16 v[20:23], a[168:171], v[168:171], v[20:23]\n"
"ds_read_b64_tr_b16 a[132:133], v192 offset:36864\n"
"v_mfma_f32_16x16x32_bf16 v[20:23], a[172:175], v[172:175], v[20:23]\n"
"ds_read_b64_tr_b16 a[134:135], v192 offset:38912\n"
"v_mfma_f32_16x16x32_bf16 v[20:23], a[176:179], v[176:179], v[20:23]\n"
"ds_read_b64_tr_b16 a[136:137], v192 offset:40960\n"
"v_cvt_pk_bf16_f32 v40, v16, v17\n"
"v_mfma_f32_16x16x32_bf16 v[20:23], a[180:183], v[180:183], v[20:23]\n"
"ds_read_b64_tr_b16 a[138:139], v192 offset:43008\n"
"v_cvt_pk_bf16_f32 v41, v18, v19\n"
"v_mfma_f32_16x16x32_bf16 v[20:23], a[184:187], v[184:187], v[20:23]\n"
"ds_read_b64_tr_b16 a[140:141], v192 offset:45056\n"
"v_mfma_f32_16x16x32_bf16 v[20:23], a[188:191], v[188:191], v[20:23]\n"
"ds_read_b64_tr_b16 a[142:143], v192 offset:47104\n"
"buffer_load_dwordx2 a[118:119], v253, s[68:71], s50 offen offset:2304\n"
"v_mfma_f32_16x16x32_bf16 v[24:27], a[224:227], v[128:131], 0\n"
"s_waitcnt lgkmcnt(11)\n"
"s_add_u32 s50, s50, 3456\n"
"ds_read_b64_tr_b16 a[144:145], v192 offset:49152\n"
"v_mfma_f32_16x16x32_bf16 v[24:27], a[228:231], v[132:135], v[24:27]\n"
"ds_read_b64_tr_b16 a[146:147], v192 offset:51200\n"
"v_mfma_f32_16x16x32_bf16 v[24:27], a[232:235], v[136:139], v[24:27]\n"
"ds_read_b64_tr_b16 a[148:149], v192 offset:53248\n"
"v_mfma_f32_16x16x32_bf16 v[24:27], a[236:239], v[140:143], v[24:27]\n"
"ds_read_b64_tr_b16 a[150:151], v192 offset:55296\n"
"v_mfma_f32_16x16x32_bf16 v[24:27], a[240:243], v[144:147], v[24:27]\n"
"ds_read_b64_tr_b16 a[152:153], v192 offset:57344\n"
"v_cvt_pk_bf16_f32 v44, v20, v21\n"
"v_mfma_f32_16x16x32_bf16 v[24:27], a[244:247], v[148:151], v[24:27]\n"
"s_waitcnt lgkmcnt(11)\n"
"ds_read_b64_tr_b16 a[154:155], v192 offset:59392\n"
"v_cvt_pk_bf16_f32 v45, v22, v23\n"
"v_mfma_f32_16x16x32_bf16 v[24:27], a[248:251], v[152:155], v[24:27]\n"
"ds_read_b64_tr_b16 a[156:157], v192 offset:61440\n"
"v_mfma_f32_16x16x32_bf16 v[24:27], a[252:255], v[156:159], v[24:27]\n"
"ds_read_b64_tr_b16 a[158:159], v192 offset:63488\n"
"v_mfma_f32_16x16x32_bf16 v[28:31], a[224:227], v[160:163], 0\n"
"ds_read_b64_tr_b16 a[192:193], v195 offset:32768\n"
"v_mfma_f32_16x16x32_bf16 v[28:31], a[228:231], v[164:167], v[28:31]\n"
"ds_read_b64_tr_b16 a[194:195], v195 offset:34816\n"
"v_mfma_f32_16x16x32_bf16 v[28:31], a[232:235], v[168:171], v[28:31]\n"
"ds_read_b64_tr_b16 a[196:197], v195 offset:36864\n"
"v_mfma_f32_16x16x32_bf16 v[28:31], a[236:239], v[172:175], v[28:31]\n"
"ds_read_b64_tr_b16 a[198:199], v195 offset:38912\n"
"v_mfma_f32_16x16x32_bf16 v[28:31], a[240:243], v[176:179], v[28:31]\n"
"ds_read_b64_tr_b16 a[200:201], v195 offset:40960\n"
"v_cvt_pk_bf16_f32 v42, v24, v25\n"
"v_mfma_f32_16x16x32_bf16 v[28:31], a[244:247], v[180:183], v[28:31]\n"
"ds_read_b64_tr_b16 a[202:203], v195 offset:43008\n"
"v_cvt_pk_bf16_f32 v43, v26, v27\n"
"v_mfma_f32_16x16x32_bf16 v[28:31], a[248:251], v[184:187], v[28:31]\n"
"ds_read_b64_tr_b16 a[204:205], v195 offset:45056\n"
"v_mfma_f32_16x16x32_bf16 v[28:31], a[252:255], v[188:191], v[28:31]\n"
"buffer_store_dwordx4 v[40:43], v205, s[32:35], s54 offen offset:1024 sc1\n"
"ds_read_b64_tr_b16 a[206:207], v195 offset:47104\n"
"v_mfma_f32_16x16x32_bf16 v[0:3], a[128:131], v[128:131], 0\n"
"s_waitcnt lgkmcnt(11)\n"
"ds_read_b64_tr_b16 a[208:209], v195 offset:49152\n"
"v_mfma_f32_16x16x32_bf16 v[0:3], a[132:135], v[132:135], v[0:3]\n"
"ds_read_b64_tr_b16 a[210:211], v195 offset:51200\n"
"v_mfma_f32_16x16x32_bf16 v[0:3], a[136:139], v[136:139], v[0:3]\n"
"ds_read_b64_tr_b16 a[212:213], v195 offset:53248\n"
"v_mfma_f32_16x16x32_bf16 v[0:3], a[140:143], v[140:143], v[0:3]\n"
"ds_read_b64_tr_b16 a[214:215], v195 offset:55296\n"
"v_mfma_f32_16x16x32_bf16 v[0:3], a[144:147], v[144:147], v[0:3]\n"
"s_waitcnt vmcnt(28)\n"
"s_barrier\n"
"ds_read_b64_tr_b16 a[216:217], v195 offset:57344\n"
"v_cvt_pk_bf16_f32 v46, v28, v29\n"
"v_mfma_f32_16x16x32_bf16 v[0:3], a[148:151], v[148:151], v[0:3]\n"
"s_waitcnt lgkmcnt(11)\n"
"ds_read_b64_tr_b16 a[218:219], v195 offset:59392\n"
"v_cvt_pk_bf16_f32 v47, v30, v31\n"
"v_mfma_f32_16x16x32_bf16 v[0:3], a[152:155], v[152:155], v[0:3]\n"
"ds_read_b64_tr_b16 a[220:221], v195 offset:61440\n"
"v_mfma_f32_16x16x32_bf16 v[0:3], a[156:159], v[156:159], v[0:3]\n"
"buffer_store_dwordx4 v[44:47], v206, s[32:35], s54 offen offset:1024 sc1\n"
"ds_read_b64_tr_b16 a[222:223], v195 offset:63488\n"
"v_mfma_f32_16x16x32_bf16 v[4:7], a[128:131], v[160:163], 0\n"
"ds_read_b64_tr_b16 a[160:161], v192 offset:33024\n"
"v_mfma_f32_16x16x32_bf16 v[4:7], a[132:135], v[164:167], v[4:7]\n"
"ds_read_b64_tr_b16 a[162:163], v192 offset:35072\n"
"v_mfma_f32_16x16x32_bf16 v[4:7], a[136:139], v[168:171], v[4:7]\n"
"ds_read_b64_tr_b16 a[164:165], v192 offset:37120\n"
"v_mfma_f32_16x16x32_bf16 v[4:7], a[140:143], v[172:175], v[4:7]\n"
"ds_read_b64_tr_b16 a[166:167], v192 offset:39168\n"
"v_mfma_f32_16x16x32_bf16 v[4:7], a[144:147], v[176:179], v[4:7]\n"
"ds_read_b64_tr_b16 a[168:169], v192 offset:41216\n"
"v_cvt_pk_bf16_f32 v32, v0, v1\n"
"v_mfma_f32_16x16x32_bf16 v[4:7], a[148:151], v[180:183], v[4:7]\n"
"ds_read_b64_tr_b16 a[170:171], v192 offset:43264\n"
"v_cvt_pk_bf16_f32 v33, v2, v3\n"
"v_mfma_f32_16x16x32_bf16 v[4:7], a[152:155], v[184:187], v[4:7]\n"
"ds_read_b64_tr_b16 a[172:173], v192 offset:45312\n"
"v_mfma_f32_16x16x32_bf16 v[4:7], a[156:159], v[188:191], v[4:7]\n"
"ds_read_b64_tr_b16 a[174:175], v192 offset:47360\n"
"v_mfma_f32_16x16x32_bf16 v[8:11], a[192:195], v[128:131], 0\n"
"s_waitcnt lgkmcnt(11)\n"
"ds_read_b64_tr_b16 a[176:177], v192 offset:49408\n"
"v_mfma_f32_16x16x32_bf16 v[8:11], a[196:199], v[132:135], v[8:11]\n"
"ds_read_b64_tr_b16 a[178:179], v192 offset:51456\n"
"v_mfma_f32_16x16x32_bf16 v[8:11], a[200:203], v[136:139], v[8:11]\n"
"ds_read_b64_tr_b16 a[180:181], v192 offset:53504\n"
"v_mfma_f32_16x16x32_bf16 v[8:11], a[204:207], v[140:143], v[8:11]\n"
"ds_read_b64_tr_b16 a[182:183], v192 offset:55552\n"
"v_mfma_f32_16x16x32_bf16 v[8:11], a[208:211], v[144:147], v[8:11]\n"
"ds_read_b64_tr_b16 a[184:185], v192 offset:57600\n"
"v_cvt_pk_bf16_f32 v36, v4, v5\n"
"v_mfma_f32_16x16x32_bf16 v[8:11], a[212:215], v[148:151], v[8:11]\n"
"s_waitcnt lgkmcnt(11)\n"
"ds_read_b64_tr_b16 a[186:187], v192 offset:59648\n"
"v_cvt_pk_bf16_f32 v37, v6, v7\n"
"v_mfma_f32_16x16x32_bf16 v[8:11], a[216:219], v[152:155], v[8:11]\n"
"ds_read_b64_tr_b16 a[188:189], v192 offset:61696\n"
"v_mfma_f32_16x16x32_bf16 v[8:11], a[220:223], v[156:159], v[8:11]\n"
"ds_read_b64_tr_b16 a[190:191], v192 offset:63744\n"
"v_mfma_f32_16x16x32_bf16 v[12:15], a[192:195], v[160:163], 0\n"
"ds_read_b64_tr_b16 a[224:225], v195 offset:33024\n"
"v_mfma_f32_16x16x32_bf16 v[12:15], a[196:199], v[164:167], v[12:15]\n"
"ds_read_b64_tr_b16 a[226:227], v195 offset:35072\n"
"v_mfma_f32_16x16x32_bf16 v[12:15], a[200:203], v[168:171], v[12:15]\n"
"ds_read_b64_tr_b16 a[228:229], v195 offset:37120\n"
"v_mfma_f32_16x16x32_bf16 v[12:15], a[204:207], v[172:175], v[12:15]\n"
"ds_read_b64_tr_b16 a[230:231], v195 offset:39168\n"
"v_mfma_f32_16x16x32_bf16 v[12:15], a[208:211], v[176:179], v[12:15]\n"
"ds_read_b64_tr_b16 a[232:233], v195 offset:41216\n"
"v_cvt_pk_bf16_f32 v34, v8, v9\n"
"v_mfma_f32_16x16x32_bf16 v[12:15], a[212:215], v[180:183], v[12:15]\n"
"ds_read_b64_tr_b16 a[234:235], v195 offset:43264\n"
"v_cvt_pk_bf16_f32 v35, v10, v11\n"
"v_mfma_f32_16x16x32_bf16 v[12:15], a[216:219], v[184:187], v[12:15]\n"
"ds_read_b64_tr_b16 a[236:237], v195 offset:45312\n"
"v_mfma_f32_16x16x32_bf16 v[12:15], a[220:223], v[188:191], v[12:15]\n"
"ds_read_b64_tr_b16 a[238:239], v195 offset:47360\n"
"buffer_store_dwordx4 v[32:35], v205, s[32:35], s54 offen offset:2048 sc1\n"
"v_mfma_f32_16x16x32_bf16 v[16:19], a[160:163], v[128:131], 0\n"
"s_waitcnt lgkmcnt(11)\n"
"ds_read_b64_tr_b16 a[240:241], v195 offset:49408\n"
"v_mfma_f32_16x16x32_bf16 v[16:19], a[164:167], v[132:135], v[16:19]\n"
"ds_read_b64_tr_b16 a[242:243], v195 offset:51456\n"
"v_mfma_f32_16x16x32_bf16 v[16:19], a[168:171], v[136:139], v[16:19]\n"
"ds_read_b64_tr_b16 a[244:245], v195 offset:53504\n"
"v_mfma_f32_16x16x32_bf16 v[16:19], a[172:175], v[140:143], v[16:19]\n"
"ds_read_b64_tr_b16 a[246:247], v195 offset:55552\n"
"v_mfma_f32_16x16x32_bf16 v[16:19], a[176:179], v[144:147], v[16:19]\n"
"ds_read_b64_tr_b16 a[248:249], v195 offset:57600\n"
"v_cvt_pk_bf16_f32 v38, v12, v13\n"
"v_mfma_f32_16x16x32_bf16 v[16:19], a[180:183], v[148:151], v[16:19]\n"
"s_waitcnt lgkmcnt(11)\n"
"ds_read_b64_tr_b16 a[250:251], v195 offset:59648\n"
"v_cvt_pk_bf16_f32 v39, v14, v15\n"
"v_mfma_f32_16x16x32_bf16 v[16:19], a[184:187], v[152:155], v[16:19]\n"
"ds_read_b64_tr_b16 a[252:253], v195 offset:61696\n"
"v_mfma_f32_16x16x32_bf16 v[16:19], a[188:191], v[156:159], v[16:19]\n"
"ds_read_b64_tr_b16 a[254:255], v195 offset:63744\n"
"buffer_store_dwordx4 v[36:39], v206, s[32:35], s54 offen offset:2048 sc1\n"
"v_mfma_f32_16x16x32_bf16 v[20:23], a[160:163], v[160:163], 0\n"
"ds_read_b64_tr_b16 a[128:129], v193 offset:0\n"
"v_mfma_f32_16x16x32_bf16 v[20:23], a[164:167], v[164:167], v[20:23]\n"
"ds_read_b64_tr_b16 a[130:131], v193 offset:2048\n"
"v_mfma_f32_16x16x32_bf16 v[20:23], a[168:171], v[168:171], v[20:23]\n"
"ds_read_b64_tr_b16 a[132:133], v193 offset:4096\n"
"v_mfma_f32_16x16x32_bf16 v[20:23], a[172:175], v[172:175], v[20:23]\n"
"ds_read_b64_tr_b16 a[134:135], v193 offset:6144\n"
"v_mfma_f32_16x16x32_bf16 v[20:23], a[176:179], v[176:179], v[20:23]\n"
"ds_read_b64_tr_b16 a[136:137], v193 offset:8192\n"
"v_cvt_pk_bf16_f32 v40, v16, v17\n"
"v_mfma_f32_16x16x32_bf16 v[20:23], a[180:183], v[180:183], v[20:23]\n"
"ds_read_b64_tr_b16 a[138:139], v193 offset:10240\n"
"v_cvt_pk_bf16_f32 v41, v18, v19\n"
"v_mfma_f32_16x16x32_bf16 v[20:23], a[184:187], v[184:187], v[20:23]\n"
"ds_read_b64_tr_b16 a[140:141], v193 offset:12288\n"
"v_mfma_f32_16x16x32_bf16 v[20:23], a[188:191], v[188:191], v[20:23]\n"
"ds_read_b64_tr_b16 a[142:143], v193 offset:14336\n"
"v_mfma_f32_16x16x32_bf16 v[24:27], a[224:227], v[128:131], 0\n"
"s_waitcnt lgkmcnt(11)\n"
"ds_read_b64_tr_b16 a[144:145], v193 offset:16384\n"
"v_mfma_f32_16x16x32_bf16 v[24:27], a[228:231], v[132:135], v[24:27]\n"
"ds_read_b64_tr_b16 a[146:147], v193 offset:18432\n"
"v_mfma_f32_16x16x32_bf16 v[24:27], a[232:235], v[136:139], v[24:27]\n"
"ds_read_b64_tr_b16 a[148:149], v193 offset:20480\n"
"v_mfma_f32_16x16x32_bf16 v[24:27], a[236:239], v[140:143], v[24:27]\n"
"ds_read_b64_tr_b16 a[150:151], v193 offset:22528\n"
"v_mfma_f32_16x16x32_bf16 v[24:27], a[240:243], v[144:147], v[24:27]\n"
"ds_read_b64_tr_b16 a[152:153], v193 offset:24576\n"
"v_cvt_pk_bf16_f32 v44, v20, v21\n"
"v_mfma_f32_16x16x32_bf16 v[24:27], a[244:247], v[148:151], v[24:27]\n"
"s_waitcnt lgkmcnt(11)\n"
"ds_read_b64_tr_b16 a[154:155], v193 offset:26624\n"
"v_cvt_pk_bf16_f32 v45, v22, v23\n"
"v_mfma_f32_16x16x32_bf16 v[24:27], a[248:251], v[152:155], v[24:27]\n"
"ds_read_b64_tr_b16 a[156:157], v193 offset:28672\n"
"v_mfma_f32_16x16x32_bf16 v[24:27], a[252:255], v[156:159], v[24:27]\n"
"ds_read_b64_tr_b16 a[158:159], v193 offset:30720\n"
"v_mfma_f32_16x16x32_bf16 v[28:31], a[224:227], v[160:163], 0\n"
"ds_read_b64_tr_b16 a[192:193], v196 offset:0\n"
"v_mfma_f32_16x16x32_bf16 v[28:31], a[228:231], v[164:167], v[28:31]\n"
"ds_read_b64_tr_b16 a[194:195], v196 offset:2048\n"
"v_mfma_f32_16x16x32_bf16 v[28:31], a[232:235], v[168:171], v[28:31]\n"
"ds_read_b64_tr_b16 a[196:197], v196 offset:4096\n"
"v_mfma_f32_16x16x32_bf16 v[28:31], a[236:239], v[172:175], v[28:31]\n"
"ds_read_b64_tr_b16 a[198:199], v196 offset:6144\n"
"v_mfma_f32_16x16x32_bf16 v[28:31], a[240:243], v[176:179], v[28:31]\n"
"ds_read_b64_tr_b16 a[200:201], v196 offset:8192\n"
"v_cvt_pk_bf16_f32 v42, v24, v25\n"
"v_mfma_f32_16x16x32_bf16 v[28:31], a[244:247], v[180:183], v[28:31]\n"
"ds_read_b64_tr_b16 a[202:203], v196 offset:10240\n"
"v_cvt_pk_bf16_f32 v43, v26, v27\n"
"v_mfma_f32_16x16x32_bf16 v[28:31], a[248:251], v[184:187], v[28:31]\n"
"ds_read_b64_tr_b16 a[204:205], v196 offset:12288\n"
"v_mfma_f32_16x16x32_bf16 v[28:31], a[252:255], v[188:191], v[28:31]\n"
"buffer_store_dwordx4 v[40:43], v205, s[32:35], s54 offen offset:3072 sc1\n"
"ds_read_b64_tr_b16 a[206:207], v196 offset:14336\n"
"v_mfma_f32_16x16x32_bf16 v[0:3], a[128:131], v[128:131], 0\n"
"s_waitcnt lgkmcnt(11)\n"
"ds_read_b64_tr_b16 a[208:209], v196 offset:16384\n"
"v_mfma_f32_16x16x32_bf16 v[0:3], a[132:135], v[132:135], v[0:3]\n"
"ds_read_b64_tr_b16 a[210:211], v196 offset:18432\n"
"v_mfma_f32_16x16x32_bf16 v[0:3], a[136:139], v[136:139], v[0:3]\n"
"ds_read_b64_tr_b16 a[212:213], v196 offset:20480\n"
"v_mfma_f32_16x16x32_bf16 v[0:3], a[140:143], v[140:143], v[0:3]\n"
"ds_read_b64_tr_b16 a[214:215], v196 offset:22528\n"
"v_mfma_f32_16x16x32_bf16 v[0:3], a[144:147], v[144:147], v[0:3]\n"
"ds_read_b64_tr_b16 a[216:217], v196 offset:24576\n"
"v_cvt_pk_bf16_f32 v46, v28, v29\n"
"v_mfma_f32_16x16x32_bf16 v[0:3], a[148:151], v[148:151], v[0:3]\n"
"s_waitcnt lgkmcnt(11)\n"
"ds_read_b64_tr_b16 a[218:219], v196 offset:26624\n"
"v_cvt_pk_bf16_f32 v47, v30, v31\n"
"v_mfma_f32_16x16x32_bf16 v[0:3], a[152:155], v[152:155], v[0:3]\n"
"ds_read_b64_tr_b16 a[220:221], v196 offset:28672\n"
"v_mfma_f32_16x16x32_bf16 v[0:3], a[156:159], v[156:159], v[0:3]\n"
"buffer_store_dwordx4 v[44:47], v206, s[32:35], s54 offen offset:3072 sc1\n"
"ds_read_b64_tr_b16 a[222:223], v196 offset:30720\n"
"v_mfma_f32_16x16x32_bf16 v[4:7], a[128:131], v[160:163], 0\n"
"s_add_u32 s54, s54, 4096\n"
"ds_read_b64_tr_b16 a[160:161], v193 offset:256\n"
"v_mfma_f32_16x16x32_bf16 v[4:7], a[132:135], v[164:167], v[4:7]\n"
"ds_read_b64_tr_b16 a[162:163], v193 offset:2304\n"
"v_mfma_f32_16x16x32_bf16 v[4:7], a[136:139], v[168:171], v[4:7]\n"
"ds_read_b64_tr_b16 a[164:165], v193 offset:4352\n"
"v_mfma_f32_16x16x32_bf16 v[4:7], a[140:143], v[172:175], v[4:7]\n"
"ds_read_b64_tr_b16 a[166:167], v193 offset:6400\n"
"v_mfma_f32_16x16x32_bf16 v[4:7], a[144:147], v[176:179], v[4:7]\n"
"ds_read_b64_tr_b16 a[168:169], v193 offset:8448\n"
"v_cvt_pk_bf16_f32 v32, v0, v1\n"
"v_mfma_f32_16x16x32_bf16 v[4:7], a[148:151], v[180:183], v[4:7]\n"
"ds_read_b64_tr_b16 a[170:171], v193 offset:10496\n"
"v_cvt_pk_bf16_f32 v33, v2, v3\n"
"v_mfma_f32_16x16x32_bf16 v[4:7], a[152:155], v[184:187], v[4:7]\n"
"ds_read_b64_tr_b16 a[172:173], v193 offset:12544\n"
"v_mfma_f32_16x16x32_bf16 v[4:7], a[156:159], v[188:191], v[4:7]\n"
"ds_read_b64_tr_b16 a[174:175], v193 offset:14592\n"
"v_mfma_f32_16x16x32_bf16 v[8:11], a[192:195], v[128:131], 0\n"
"s_waitcnt lgkmcnt(11)\n"
"ds_read_b64_tr_b16 a[176:177], v193 offset:16640\n"
"v_mfma_f32_16x16x32_bf16 v[8:11], a[196:199], v[132:135], v[8:11]\n"
"ds_read_b64_tr_b16 a[178:179], v193 offset:18688\n"
"v_mfma_f32_16x16x32_bf16 v[8:11], a[200:203], v[136:139], v[8:11]\n"
"ds_read_b64_tr_b16 a[180:181], v193 offset:20736\n"
"v_mfma_f32_16x16x32_bf16 v[8:11], a[204:207], v[140:143], v[8:11]\n"
"ds_read_b64_tr_b16 a[182:183], v193 offset:22784\n"
"v_mfma_f32_16x16x32_bf16 v[8:11], a[208:211], v[144:147], v[8:11]\n"
"ds_read_b64_tr_b16 a[184:185], v193 offset:24832\n"
"v_cvt_pk_bf16_f32 v36, v4, v5\n"
"v_mfma_f32_16x16x32_bf16 v[8:11], a[212:215], v[148:151], v[8:11]\n"
"s_waitcnt lgkmcnt(11)\n"
"ds_read_b64_tr_b16 a[186:187], v193 offset:26880\n"
"v_cvt_pk_bf16_f32 v37, v6, v7\n"
"v_mfma_f32_16x16x32_bf16 v[8:11], a[216:219], v[152:155], v[8:11]\n"
"ds_read_b64_tr_b16 a[188:189], v193 offset:28928\n"
"v_mfma_f32_16x16x32_bf16 v[8:11], a[220:223], v[156:159], v[8:11]\n"
"ds_read_b64_tr_b16 a[190:191], v193 offset:30976\n"
"v_mfma_f32_16x16x32_bf16 v[12:15], a[192:195], v[160:163], 0\n"
"ds_read_b64_tr_b16 a[224:225], v196 offset:256\n"
"v_mfma_f32_16x16x32_bf16 v[12:15], a[196:199], v[164:167], v[12:15]\n"
"ds_read_b64_tr_b16 a[226:227], v196 offset:2304\n"
"v_mfma_f32_16x16x32_bf16 v[12:15], a[200:203], v[168:171], v[12:15]\n"
"ds_read_b64_tr_b16 a[228:229], v196 offset:4352\n"
"v_mfma_f32_16x16x32_bf16 v[12:15], a[204:207], v[172:175], v[12:15]\n"
"ds_read_b64_tr_b16 a[230:231], v196 offset:6400\n"
"v_mfma_f32_16x16x32_bf16 v[12:15], a[208:211], v[176:179], v[12:15]\n"
"ds_read_b64_tr_b16 a[232:233], v196 offset:8448\n"
"v_cvt_pk_bf16_f32 v34, v8, v9\n"
"v_mfma_f32_16x16x32_bf16 v[12:15], a[212:215], v[180:183], v[12:15]\n"
"ds_read_b64_tr_b16 a[234:235], v196 offset:10496\n"
"v_cvt_pk_bf16_f32 v35, v10, v11\n"
"v_mfma_f32_16x16x32_bf16 v[12:15], a[216:219], v[184:187], v[12:15]\n"
"ds_read_b64_tr_b16 a[236:237], v196 offset:12544\n"
"v_mfma_f32_16x16x32_bf16 v[12:15], a[220:223], v[188:191], v[12:15]\n"
"ds_read_b64_tr_b16 a[238:239], v196 offset:14592\n"
"buffer_store_dwordx4 v[32:35], v205, s[32:35], s54 offen offset:0 sc1\n"
"v_mfma_f32_16x16x32_bf16 v[16:19], a[160:163], v[128:131], 0\n"
"s_waitcnt lgkmcnt(11)\n"
"ds_read_b64_tr_b16 a[240:241], v196 offset:16640\n"
"v_mfma_f32_16x16x32_bf16 v[16:19], a[164:167], v[132:135], v[16:19]\n"
"ds_read_b64_tr_b16 a[242:243], v196 offset:18688\n"
"v_mfma_f32_16x16x32_bf16 v[16:19], a[168:171], v[136:139], v[16:19]\n"
"ds_read_b64_tr_b16 a[244:245], v196 offset:20736\n"
"v_mfma_f32_16x16x32_bf16 v[16:19], a[172:175], v[140:143], v[16:19]\n"
"ds_read_b64_tr_b16 a[246:247], v196 offset:22784\n"
"v_mfma_f32_16x16x32_bf16 v[16:19], a[176:179], v[144:147], v[16:19]\n"
"ds_read_b64_tr_b16 a[248:249], v196 offset:24832\n"
"v_cvt_pk_bf16_f32 v38, v12, v13\n"
"v_mfma_f32_16x16x32_bf16 v[16:19], a[180:183], v[148:151], v[16:19]\n"
"s_waitcnt lgkmcnt(11)\n"
"ds_read_b64_tr_b16 a[250:251], v196 offset:26880\n"
"v_cvt_pk_bf16_f32 v39, v14, v15\n"
"v_mfma_f32_16x16x32_bf16 v[16:19], a[184:187], v[152:155], v[16:19]\n"
"ds_read_b64_tr_b16 a[252:253], v196 offset:28928\n"
"v_mfma_f32_16x16x32_bf16 v[16:19], a[188:191], v[156:159], v[16:19]\n"
"ds_read_b64_tr_b16 a[254:255], v196 offset:30976\n"
"buffer_store_dwordx4 v[36:39], v206, s[32:35], s54 offen offset:0 sc1\n"
"v_mfma_f32_16x16x32_bf16 v[20:23], a[160:163], v[160:163], 0\n"
"s_nop 0\n"
"v_mfma_f32_16x16x32_bf16 v[20:23], a[164:167], v[164:167], v[20:23]\n"
"s_nop 0\n"
"v_mfma_f32_16x16x32_bf16 v[20:23], a[168:171], v[168:171], v[20:23]\n"
"s_nop 0\n"
"v_mfma_f32_16x16x32_bf16 v[20:23], a[172:175], v[172:175], v[20:23]\n"
"s_nop 0\n"
"v_mfma_f32_16x16x32_bf16 v[20:23], a[176:179], v[176:179], v[20:23]\n"
"v_cvt_pk_bf16_f32 v40, v16, v17\n"
"v_mfma_f32_16x16x32_bf16 v[20:23], a[180:183], v[180:183], v[20:23]\n"
"v_cvt_pk_bf16_f32 v41, v18, v19\n"
"v_mfma_f32_16x16x32_bf16 v[20:23], a[184:187], v[184:187], v[20:23]\n"
"s_nop 0\n"
"v_mfma_f32_16x16x32_bf16 v[20:23], a[188:191], v[188:191], v[20:23]\n"
"s_nop 0\n"
"v_mfma_f32_16x16x32_bf16 v[24:27], a[224:227], v[128:131], 0\n"
"s_waitcnt lgkmcnt(3)\n"
"v_mfma_f32_16x16x32_bf16 v[24:27], a[228:231], v[132:135], v[24:27]\n"
"s_nop 0\n"
"v_mfma_f32_16x16x32_bf16 v[24:27], a[232:235], v[136:139], v[24:27]\n"
"s_nop 0\n"
"v_mfma_f32_16x16x32_bf16 v[24:27], a[236:239], v[140:143], v[24:27]\n"
"s_nop 0\n"
"v_mfma_f32_16x16x32_bf16 v[24:27], a[240:243], v[144:147], v[24:27]\n"
"v_cvt_pk_bf16_f32 v44, v20, v21\n"
"v_mfma_f32_16x16x32_bf16 v[24:27], a[244:247], v[148:151], v[24:27]\n"
"s_waitcnt lgkmcnt(0)\n"
"v_cvt_pk_bf16_f32 v45, v22, v23\n"
"v_mfma_f32_16x16x32_bf16 v[24:27], a[248:251], v[152:155], v[24:27]\n"
"s_nop 0\n"
"v_mfma_f32_16x16x32_bf16 v[24:27], a[252:255], v[156:159], v[24:27]\n"
"s_nop 0\n"
"v_mfma_f32_16x16x32_bf16 v[28:31], a[224:227], v[160:163], 0\n"
"s_nop 0\n"
"v_mfma_f32_16x16x32_bf16 v[28:31], a[228:231], v[164:167], v[28:31]\n"
"s_nop 0\n"
"v_mfma_f32_16x16x32_bf16 v[28:31], a[232:235], v[168:171], v[28:31]\n"
"s_nop 0\n"
"v_mfma_f32_16x16x32_bf16 v[28:31], a[236:239], v[172:175], v[28:31]\n"
"s_nop 0\n"
"v_mfma_f32_16x16x32_bf16 v[28:31], a[240:243], v[176:179], v[28:31]\n"
"v_cvt_pk_bf16_f32 v42, v24, v25\n"
"v_mfma_f32_16x16x32_bf16 v[28:31], a[244:247], v[180:183], v[28:31]\n"
"v_cvt_pk_bf16_f32 v43, v26, v27\n"
"v_mfma_f32_16x16x32_bf16 v[28:31], a[248:251], v[184:187], v[28:31]\n"
"s_nop 0\n"
"v_mfma_f32_16x16x32_bf16 v[28:31], a[252:255], v[188:191], v[28:31]\n"
"buffer_store_dwordx4 v[40:43], v205, s[32:35], s54 offen offset:1024 sc1\n"
"s_nop 1\n"
"s_nop 1\n"
"s_nop 1\n"
"s_nop 1\n"
"s_nop 1\n"
"v_cvt_pk_bf16_f32 v46, v28, v29\n"
"s_nop 1\n"
"v_cvt_pk_bf16_f32 v47, v30, v31\n"
"s_nop 1\n"
"s_nop 1\n"
"buffer_store_dwordx4 v[44:47], v206, s[32:35], s54 offen offset:1024 sc1\n"
"s_nop 1\n"
"s_nop 1\n"
"s_nop 1\n"
"s_nop 1\n"
"s_nop 1\n"
"s_nop 1\n"
"s_nop 1\n"
"s_nop 1\n"
"s_lshl_b32 s56, s44, 4\n"
"s_add_u32 s56, s56, s43\n"
"s_lshl_b32 s57, s56, 6\n"
"v_mov_b32 v0, s17\n"
"v_mov_b32 v1, 0\n"
"s_waitcnt vmcnt(0)\n"
"s_barrier\n"
"s_cmp_lg_u32 s41, 3\n"
"s_cbranch_scc1 AT_end\n"
"s_mov_b64 exec, 1\n"
"buffer_store_dword v0, v1, s[36:39], s57 offen sc1\n"
"s_mov_b64 exec, -1\n"
"s_branch AT_end\n"
"AT_fb:\n"
"s_waitcnt vmcnt(0) lgkmcnt(0)\n"
"s_barrier\n"
"s_mov_b32 s45, 1\n"
"s_lshl_b32 s16, s41, 12\n"
"s_mov_b32 s19, 32\n"
"v_mov_b32 v160, 0xff800000\n"
"v_mov_b32 v161, 0xff800000\n"
"v_mov_b32 v162, 0xff800000\n"
"v_mov_b32 v163, 0xff800000\n"
"AT_fbl:\n"
"s_mov_b32 m0, s18\n"
"s_nop 0\n"
"buffer_load_dwordx4 v246, s[8:11], s16 offen offset:0 lds\n"
"buffer_load_dwordx4 v246, s[8:11], s16 offen offset:1024 lds\n"
"buffer_load_dwordx4 v247, s[8:11], s16 offen offset:2048 lds\n"
"buffer_load_dwordx4 v247, s[8:11], s16 offen offset:3072 lds\n"
"s_add_u32 s16, s16, 16384\n"
"s_waitcnt vmcnt(0)\n"
"s_barrier\n"
"ds_read_b128 a[128:131], v240 offset:0\n"
"ds_read_b128 a[132:135], v240 offset:256\n"
"ds_read_b128 a[136:139], v240 offset:512\n"
"ds_read_b128 a[140:143], v240 offset:768\n"
"ds_read_b128 a[144:147], v240 offset:4096\n"
"ds_read_b128 a[148:151], v240 offset:4352\n"
"ds_read_b128 a[152:155], v240 offset:4608\n"
"ds_read_b128 a[156:159], v240 offset:4864\n"
"s_waitcnt lgkmcnt(0)\n"
"v_mfma_f32_16x16x32_bf16 v[128:131], a[128:131], v[64:67], 0\n"
"v_mfma_f32_16x16x32_bf16 v[128:131], a[132:135], v[68:71], v[128:131]\n"
"v_mfma_f32_16x16x32_bf16 v[128:131], a[136:139], v[72:75], v[128:131]\n"
"v_mfma_f32_16x16x32_bf16 v[128:131], a[140:143], v[76:79], v[128:131]\n"
"v_mfma_f32_16x16x32_bf16 v[132:135], a[144:147], v[64:67], 0\n"
"v_mfma_f32_16x16x32_bf16 v[132:135], a[148:151], v[68:71], v[132:135]\n"
"v_mfma_f32_16x16x32_bf16 v[132:135], a[152:155], v[72:75], v[132:135]\n"
"v_mfma_f32_16x16x32_bf16 v[132:135], a[156:159], v[76:79], v[132:135]\n"
"v_mfma_f32_16x16x32_bf16 v[136:139], a[128:131], v[80:83], 0\n"
"v_mfma_f32_16x16x32_bf16 v[136:139], a[132:135], v[84:87], v[136:139]\n"
"v_mfma_f32_16x16x32_bf16 v[136:139], a[136:139], v[88:91], v[136:139]\n"
"v_mfma_f32_16x16x32_bf16 v[136:139], a[140:143], v[92:95], v[136:139]\n"
"v_mfma_f32_16x16x32_bf16 v[140:143], a[144:147], v[80:83], 0\n"
"v_mfma_f32_16x16x32_bf16 v[140:143], a[148:151], v[84:87], v[140:143]\n"
"v_mfma_f32_16x16x32_bf16 v[140:143], a[152:155], v[88:91], v[140:143]\n"
"v_mfma_f32_16x16x32_bf16 v[140:143], a[156:159], v[92:95], v[140:143]\n"
"v_mfma_f32_16x16x32_bf16 v[144:147], a[128:131], v[96:99], 0\n"
"v_mfma_f32_16x16x32_bf16 v[144:147], a[132:135], v[100:103], v[144:147]\n"
"v_mfma_f32_16x16x32_bf16 v[144:147], a[136:139], v[104:107], v[144:147]\n"
"v_mfma_f32_16x16x32_bf16 v[144:147], a[140:143], v[108:111], v[144:147]\n"
"v_mfma_f32_16x16x32_bf16 v[148:151], a[144:147], v[96:99], 0\n"
"v_mfma_f32_16x16x32_bf16 v[148:151], a[148:151], v[100:103], v[148:151]\n"
"v_mfma_f32_16x16x32_bf16 v[148:151], a[152:155], v[104:107], v[148:151]\n"
"v_mfma_f32_16x16x32_bf16 v[148:151], a[156:159], v[108:111], v[148:151]\n"
"v_mfma_f32_16x16x32_bf16 v[152:155], a[128:131], v[112:115], 0\n"
"v_mfma_f32_16x16x32_bf16 v[152:155], a[132:135], v[116:119], v[152:155]\n"
"v_mfma_f32_16x16x32_bf16 v[152:155], a[136:139], v[120:123], v[152:155]\n"
"v_mfma_f32_16x16x32_bf16 v[152:155], a[140:143], v[124:127], v[152:155]\n"
"v_mfma_f32_16x16x32_bf16 v[156:159], a[144:147], v[112:115], 0\n"
"v_mfma_f32_16x16x32_bf16 v[156:159], a[148:151], v[116:119], v[156:159]\n"
"v_mfma_f32_16x16x32_bf16 v[156:159], a[152:155], v[120:123], v[156:159]\n"
"v_mfma_f32_16x16x32_bf16 v[156:159], a[156:159], v[124:127], v[156:159]\n"
"s_nop 7\n"
"v_max3_f32 v160, v160, v128, v129\n"
"v_max3_f32 v160, v160, v130, v131\n"
"v_max3_f32 v160, v160, v132, v133\n"
"v_max3_f32 v160, v160, v134, v135\n"
"v_max3_f32 v161, v161, v136, v137\n"
"v_max3_f32 v161, v161, v138, v139\n"
"v_max3_f32 v161, v161, v140, v141\n"
"v_max3_f32 v161, v161, v142, v143\n"
"v_max3_f32 v162, v162, v144, v145\n"
"v_max3_f32 v162, v162, v146, v147\n"
"v_max3_f32 v162, v162, v148, v149\n"
"v_max3_f32 v162, v162, v150, v151\n"
"v_max3_f32 v163, v163, v152, v153\n"
"v_max3_f32 v163, v163, v154, v155\n"
"v_max3_f32 v163, v163, v156, v157\n"
"v_max3_f32 v163, v163, v158, v159\n"
"ds_read_b128 a[128:131], v240 offset:8192\n"
"ds_read_b128 a[132:135], v240 offset:8448\n"
"ds_read_b128 a[136:139], v240 offset:8704\n"
"ds_read_b128 a[140:143], v240 offset:8960\n"
"ds_read_b128 a[144:147], v240 offset:12288\n"
"ds_read_b128 a[148:151], v240 offset:12544\n"
"ds_read_b128 a[152:155], v240 offset:12800\n"
"ds_read_b128 a[156:159], v240 offset:13056\n"
"s_waitcnt lgkmcnt(0)\n"
"v_mfma_f32_16x16x32_bf16 v[128:131], a[128:131], v[64:67], 0\n"
"v_mfma_f32_16x16x32_bf16 v[128:131], a[132:135], v[68:71], v[128:131]\n"
"v_mfma_f32_16x16x32_bf16 v[128:131], a[136:139], v[72:75], v[128:131]\n"
"v_mfma_f32_16x16x32_bf16 v[128:131], a[140:143], v[76:79], v[128:131]\n"
"v_mfma_f32_16x16x32_bf16 v[132:135], a[144:147], v[64:67], 0\n"
"v_mfma_f32_16x16x32_bf16 v[132:135], a[148:151], v[68:71], v[132:135]\n"
"v_mfma_f32_16x16x32_bf16 v[132:135], a[152:155], v[72:75], v[132:135]\n"
"v_mfma_f32_16x16x32_bf16 v[132:135], a[156:159], v[76:79], v[132:135]\n"
"v_mfma_f32_16x16x32_bf16 v[136:139], a[128:131], v[80:83], 0\n"
"v_mfma_f32_16x16x32_bf16 v[136:139], a[132:135], v[84:87], v[136:139]\n"
"v_mfma_f32_16x16x32_bf16 v[136:139], a[136:139], v[88:91], v[136:139]\n"
"v_mfma_f32_16x16x32_bf16 v[136:139], a[140:143], v[92:95], v[136:139]\n"
"v_mfma_f32_16x16x32_bf16 v[140:143], a[144:147], v[80:83], 0\n"
"v_mfma_f32_16x16x32_bf16 v[140:143], a[148:151], v[84:87], v[140:143]\n"
"v_mfma_f32_16x16x32_bf16 v[140:143], a[152:155], v[88:91], v[140:143]\n"
"v_mfma_f32_16x16x32_bf16 v[140:143], a[156:159], v[92:95], v[140:143]\n"
"v_mfma_f32_16x16x32_bf16 v[144:147], a[128:131], v[96:99], 0\n"
"v_mfma_f32_16x16x32_bf16 v[144:147], a[132:135], v[100:103], v[144:147]\n"
"v_mfma_f32_16x16x32_bf16 v[144:147], a[136:139], v[104:107], v[144:147]\n"
"v_mfma_f32_16x16x32_bf16 v[144:147], a[140:143], v[108:111], v[144:147]\n"
"v_mfma_f32_16x16x32_bf16 v[148:151], a[144:147], v[96:99], 0\n"
"v_mfma_f32_16x16x32_bf16 v[148:151], a[148:151], v[100:103], v[148:151]\n"
"v_mfma_f32_16x16x32_bf16 v[148:151], a[152:155], v[104:107], v[148:151]\n"
"v_mfma_f32_16x16x32_bf16 v[148:151], a[156:159], v[108:111], v[148:151]\n"
"v_mfma_f32_16x16x32_bf16 v[152:155], a[128:131], v[112:115], 0\n"
"v_mfma_f32_16x16x32_bf16 v[152:155], a[132:135], v[116:119], v[152:155]\n"
"v_mfma_f32_16x16x32_bf16 v[152:155], a[136:139], v[120:123], v[152:155]\n"
"v_mfma_f32_16x16x32_bf16 v[152:155], a[140:143], v[124:127], v[152:155]\n"
"v_mfma_f32_16x16x32_bf16 v[156:159], a[144:147], v[112:115], 0\n"
"v_mfma_f32_16x16x32_bf16 v[156:159], a[148:151], v[116:119], v[156:159]\n"
"v_mfma_f32_16x16x32_bf16 v[156:159], a[152:155], v[120:123], v[156:159]\n"
"v_mfma_f32_16x16x32_bf16 v[156:159], a[156:159], v[124:127], v[156:159]\n"
"s_nop 7\n"
"v_max3_f32 v160, v160, v128, v129\n"
"v_max3_f32 v160, v160, v130, v131\n"
"v_max3_f32 v160, v160, v132, v133\n"
"v_max3_f32 v160, v160, v134, v135\n"
"v_max3_f32 v161, v161, v136, v137\n"
"v_max3_f32 v161, v161, v138, v139\n"
"v_max3_f32 v161, v161, v140, v141\n"
"v_max3_f32 v161, v161, v142, v143\n"
"v_max3_f32 v162, v162, v144, v145\n"
"v_max3_f32 v162, v162, v146, v147\n"
"v_max3_f32 v162, v162, v148, v149\n"
"v_max3_f32 v162, v162, v150, v151\n"
"v_max3_f32 v163, v163, v152, v153\n"
"v_max3_f32 v163, v163, v154, v155\n"
"v_max3_f32 v163, v163, v156, v157\n"
"v_max3_f32 v163, v163, v158, v159\n"
"s_barrier\n"
"s_sub_u32 s19, s19, 1\n"
"s_cmp_lg_u32 s19, 0\n"
"s_cbranch_scc1 AT_fbl\n"
"v_mov_b32 v164, v160\n"
"s_nop 1\n"
"v_permlane32_swap_b32 v160, v164\n"
"v_max_f32 v160, v160, v164\n"
"v_mov_b32 v164, v160\n"
"s_nop 1\n"
"v_permlane16_swap_b32 v160, v164\n"
"v_max_f32 v160, v160, v164\n"
"v_sub_f32 v24, 0, v160\n"
"v_sub_f32 v25, 0, v160\n"
"v_sub_f32 v26, 0, v160\n"
"v_sub_f32 v27, 0, v160\n"
"v_mov_b32 v164, v161\n"
"s_nop 1\n"
"v_permlane32_swap_b32 v161, v164\n"
"v_max_f32 v161, v161, v164\n"
"v_mov_b32 v164, v161\n"
"s_nop 1\n"
"v_permlane16_swap_b32 v161, v164\n"
"v_max_f32 v161, v161, v164\n"
"v_sub_f32 v28, 0, v161\n"
"v_sub_f32 v29, 0, v161\n"
"v_sub_f32 v30, 0, v161\n"
"v_sub_f32 v31, 0, v161\n"
"v_mov_b32 v164, v162\n"
"s_nop 1\n"
"v_permlane32_swap_b32 v162, v164\n"
"v_max_f32 v162, v162, v164\n"
"v_mov_b32 v164, v162\n"
"s_nop 1\n"
"v_permlane16_swap_b32 v162, v164\n"
"v_max_f32 v162, v162, v164\n"
"v_sub_f32 v32, 0, v162\n"
"v_sub_f32 v33, 0, v162\n"
"v_sub_f32 v34, 0, v162\n"
"v_sub_f32 v35, 0, v162\n"
"v_mov_b32 v164, v163\n"
"s_nop 1\n"
"v_permlane32_swap_b32 v163, v164\n"
"v_max_f32 v163, v163, v164\n"
"v_mov_b32 v164, v163\n"
"s_nop 1\n"
"v_permlane16_swap_b32 v163, v164\n"
"v_max_f32 v163, v163, v164\n"
"v_sub_f32 v36, 0, v163\n"
"v_sub_f32 v37, 0, v163\n"
"v_sub_f32 v38, 0, v163\n"
"v_sub_f32 v39, 0, v163\n"
"v_mov_b32 v40, 0x3f803f80\n"
"v_mov_b32 v41, 0x3f803f80\n"
"v_mov_b32 v42, 0x3f803f80\n"
"v_mov_b32 v43, 0x3f803f80\n"
"s_lshl_b32 s16, s41, 12\n"
"s_mul_i32 s52, s41, 73728\n"
"s_mov_b32 m0, s18\n"
"s_nop 0\n"
"buffer_load_dwordx4 v246, s[8:11], s16 offen offset:0 lds\n"
"buffer_load_dwordx4 v246, s[8:11], s16 offen offset:1024 lds\n"
"buffer_load_dwordx4 v247, s[8:11], s16 offen offset:2048 lds\n"
"buffer_load_dwordx4 v247, s[8:11], s16 offen offset:3072 lds\n"
"s_add_u32 m0, s18, 16384\n"
"s_nop 0\n"
"buffer_load_dwordx4 v246, s[12:15], s16 offen offset:0 lds\n"
"buffer_load_dwordx4 v247, s[12:15], s16 offen offset:1024 lds\n"
"buffer_load_dwordx4 v246, s[12:15], s16 offen offset:2048 lds\n"
"buffer_load_dwordx4 v247, s[12:15], s16 offen offset:3072 lds\n"
"s_add_u32 s16, s16, 16384\n"
"s_add_u32 m0, s18, 32768\n"
"s_nop 0\n"
"buffer_load_dwordx4 v246, s[8:11], s16 offen offset:0 lds\n"
"buffer_load_dwordx4 v246, s[8:11], s16 offen offset:1024 lds\n"
"buffer_load_dwordx4 v247, s[8:11], s16 offen offset:2048 lds\n"
"buffer_load_dwordx4 v247, s[8:11], s16 offen offset:3072 lds\n"
"s_add_u32 m0, s18, 49152\n"
"s_nop 0\n"
"buffer_load_dwordx4 v246, s[12:15], s16 offen offset:0 lds\n"
"buffer_load_dwordx4 v247, s[12:15], s16 offen offset:1024 lds\n"
"buffer_load_dwordx4 v246, s[12:15], s16 offen offset:2048 lds\n"
"buffer_load_dwordx4 v247, s[12:15], s16 offen offset:3072 lds\n"
"s_add_u32 s16, s16, 16384\n"
"s_add_u32 m0, s18, 65536\n"
"s_nop 0\n"
"buffer_load_dwordx4 v246, s[8:11], s16 offen offset:0 lds\n"
"buffer_load_dwordx4 v246, s[8:11], s16 offen offset:1024 lds\n"
"buffer_load_dwordx4 v247, s[8:11], s16 offen offset:2048 lds\n"
"buffer_load_dwordx4 v247, s[8:11], s16 offen offset:3072 lds\n"
"s_add_u32 m0, s18, 81920\n"
"s_nop 0\n"
"buffer_load_dwordx4 v246, s[12:15], s16 offen offset:0 lds\n"
"buffer_load_dwordx4 v247, s[12:15], s16 offen offset:1024 lds\n"
"buffer_load_dwordx4 v246, s[12:15], s16 offen offset:2048 lds\n"
"buffer_load_dwordx4 v247, s[12:15], s16 offen offset:3072 lds\n"
"s_add_u32 s16, s16, 16384\n"
"s_add_u32 m0, s18, 98304\n"
"s_nop 0\n"
"buffer_load_dwordx4 v246, s[8:11], s16 offen offset:0 lds\n"
"buffer_load_dwordx4 v246, s[8:11], s16 offen offset:1024 lds\n"
"buffer_load_dwordx4 v247, s[8:11], s16 offen offset:2048 lds\n"
"buffer_load_dwordx4 v247, s[8:11], s16 offen offset:3072 lds\n"
"s_add_u32 m0, s18, 114688\n"
"s_nop 0\n"
"buffer_load_dwordx4 v246, s[12:15], s16 offen offset:0 lds\n"
"buffer_load_dwordx4 v247, s[12:15], s16 offen offset:1024 lds\n"
"buffer_load_dwordx4 v246, s[12:15], s16 offen offset:2048 lds\n"
"buffer_load_dwordx4 v247, s[12:15], s16 offen offset:3072 lds\n"
"s_add_u32 s16, s16, 16384\n"
"s_add_u32 m0, s53, 131072\n"
"s_nop 0\n"
"buffer_load_dwordx4 v204, s[28:31], s52 offen offset:0 lds\n"
"s_add_u32 s52, s52, 8192\n"
"buffer_load_dwordx4 v204, s[28:31], s52 offen offset:1024 lds\n"
"s_add_u32 s52, s52, 8192\n"
"buffer_load_dwordx4 v204, s[28:31], s52 offen offset:2048 lds\n"
"s_add_u32 s52, s52, 8192\n"
"buffer_load_dwordx4 v204, s[28:31], s52 offen offset:3072 lds\n"
"s_add_u32 s52, s52, 12288\n"
"s_add_u32 m0, s53, 135168\n"
"s_nop 0\n"
"buffer_load_dwordx4 v204, s[28:31], s52 offen offset:0 lds\n"
"s_add_u32 s52, s52, 8192\n"
"buffer_load_dwordx4 v204, s[28:31], s52 offen offset:1024 lds\n"
"s_add_u32 s52, s52, 8192\n"
"buffer_load_dwordx4 v204, s[28:31], s52 offen offset:2048 lds\n"
"s_add_u32 s52, s52, 8192\n"
"buffer_load_dwordx4 v204, s[28:31], s52 offen offset:3072 lds\n"
"s_sub_u32 s52, s52, 61312\n"
"s_waitcnt vmcnt(32)\n"
"s_barrier\n"
"ds_read_b128 a[128:131], v240 offset:0\n"
"ds_read_b128 a[132:135], v240 offset:256\n"
"ds_read_b128 a[136:139], v240 offset:512\n"
"ds_read_b128 a[140:143], v240 offset:768\n"
"ds_read_b128 a[144:147], v240 offset:4096\n"
"ds_read_b128 a[148:151], v240 offset:4352\n"
"ds_read_b128 a[152:155], v240 offset:4608\n"
"ds_read_b128 a[156:159], v240 offset:4864\n"
"ds_read_b64_tr_b16 a[192:193], v242 offset:16384\n"
"ds_read_b64_tr_b16 a[194:195], v242 offset:20480\n"
"ds_read_b64_tr_b16 a[196:197], v243 offset:16384\n"
"ds_read_b64_tr_b16 a[198:199], v243 offset:20480\n"
"ds_read_b64_tr_b16 a[200:201], v242 offset:16640\n"
"ds_read_b64_tr_b16 a[202:203], v242 offset:20736\n"
"ds_read_b64_tr_b16 a[204:205], v243 offset:16640\n"
"ds_read_b64_tr_b16 a[206:207], v243 offset:20736\n"
"ds_read_b64_tr_b16 a[208:209], v242 offset:16896\n"
"ds_read_b64_tr_b16 a[210:211], v242 offset:20992\n"
"ds_read_b64_tr_b16 a[212:213], v243 offset:16896\n"
"ds_read_b64_tr_b16 a[214:215], v243 offset:20992\n"
"ds_read_b64_tr_b16 a[216:217], v242 offset:17152\n"
"ds_read_b64_tr_b16 a[218:219], v242 offset:21248\n"
"ds_read_b64_tr_b16 a[220:221], v243 offset:17152\n"
"ds_read_b64_tr_b16 a[222:223], v243 offset:21248\n"
"s_waitcnt lgkmcnt(0)\n"
"s_branch AT_main\n"
"AT_end:\n"
"s_mov_b32 %[tag], s17\n"
"s_mov_b32 %[ob], s56\n"
  : [tag] "=s"(tag), [ob] "=s"(ob), [kp] "+s"(kp), [wid] "+s"(wid), "={a[0:15]}"(w0), "={a[16:31]}"(w1), "={a[32:47]}"(w2), "={a[48:63]}"(w3), "={a[64:79]}"(w4), "={a[80:95]}"(w5),
    "={a[96:111]}"(w6), "={a[112:127]}"(w7)
  : [blk] "s"(blockIdx.x), [lds] "s"(ldsb) : "v0", "v1", "v2", "v3", "v4", "v5", "v6", "v7", "v8", "v9", "v10", "v11", "v12", "v13", "v14", "v15", "v16", "v17", "v18", "v19", "v20", "v21", "v22", "v23", "v24", "v25", "v26", "v27", "v28", "v29", "v30", "v31", "v32", "v33", "v34", "v35", "v36", "v37", "v38", "v39", "v40", "v41", "v42", "v43", "v44", "v45", "v46", "v47", "v48", "v49", "v50", "v51", "v52", "v53", "v54", "v55", "v56", "v57", "v58", "v59", "v60", "v61", "v62", "v63", "v64", "v65", "v66", "v67", "v68", "v69", "v70", "v71", "v72", "v73", "v74", "v75", "v76", "v77", "v78", "v79", "v80", "v81", "v82", "v83", "v84", "v85", "v86", "v87", "v88", "v89", "v90", "v91", "v92", "v93", "v94", "v95", "v96", "v97", "v98", "v99", "v100", "v101", "v102", "v103", "v104", "v105", "v106", "v107", "v108", "v109", "v110", "v111", "v112", "v113", "v114", "v115", "v116", "v117", "v118", "v119", "v120", "v121", "v122", "v123", "v124", "v125", "v126", "v127", "v128", "v129", "v130", "v131", "v132", "v133", "v134", "v135", "v136", "v137", "v138", "v139", "v140", "v141", "v142", "v143", "v144", "v145", "v146", "v147", "v148", "v149", "v150", "v151", "v152", "v153", "v154", "v155", "v156", "v157", "v158", "v159", "v160", "v161", "v162", "v163", "v164", "v165", "v166", "v167", "v168", "v169", "v170", "v171", "v172", "v173", "v174", "v175", "v176", "v177", "v178", "v179", "v180", "v181", "v182", "v183", "v184", "v185", "v186", "v187", "v188", "v189", "v190", "v191", "v192", "v193", "v194", "v195", "v196", "v197", "v198", "v199", "v200", "v201", "v202", "v203", "v204", "v205", "v206", "v207", "v208", "v209", "v210", "v211", "v212", "v213", "v214", "v215", "v216", "v217", "v218", "v219", "v220", "v221", "v222", "v223", "v224", "v225", "v226", "v227", "v228", "v229", "v230", "v231", "v232", "v233", "v234", "v235", "v236", "v237", "v238", "v239", "v240", "v241", "v242", "v243", "v244", "v245", "v246", "v247", "v248", "v249", "v250", "v251", "v252", "v253", "v254", "v255", "a128", "a129", "a130", "a131", "a132", "a133", "a134", "a135", "a136", "a137", "a138", "a139", "a140", "a141", "a142", "a143", "a144", "a145", "a146", "a147", "a148", "a149", "a150", "a151", "a152", "a153", "a154", "a155", "a156", "a157", "a158", "a159", "a160", "a161", "a162", "a163", "a164", "a165", "a166", "a167", "a168", "a169", "a170", "a171", "a172", "a173", "a174", "a175", "a176", "a177", "a178", "a179", "a180", "a181", "a182", "a183", "a184", "a185", "a186", "a187", "a188", "a189", "a190", "a191", "a192", "a193", "a194", "a195", "a196", "a197", "a198", "a199", "a200", "a201", "a202", "a203", "a204", "a205", "a206", "a207", "a208", "a209", "a210", "a211", "a212", "a213", "a214", "a215", "a216", "a217", "a218", "a219", "a220", "a221", "a222", "a223", "a224", "a225", "a226", "a227", "a228", "a229", "a230", "a231", "a232", "a233", "a234", "a235", "a236", "a237", "a238", "a239", "a240", "a241", "a242", "a243", "a244", "a245", "a246", "a247", "a248", "a249", "a250", "a251", "a252", "a253", "a254", "a255", "s8", "s9", "s10", "s11", "s12", "s13", "s14", "s15", "s16", "s17", "s18", "s19", "s20", "s21", "s22", "s23", "s24", "s25", "s26", "s27", "s28", "s29", "s30", "s31", "s32", "s33", "s34", "s35", "s36", "s37", "s38", "s39", "s40", "s41", "s42", "s43", "s44", "s45", "s46", "s47", "s48", "s49", "s50", "s51", "s52", "s53", "s54", "s55", "s56", "s57", "s58", "s59", "s60", "s61", "s62", "s63", "s64", "s65", "s66", "s67", "s68", "s69", "s70", "s71", "s72", "s73", "s74", "s75", "s76", "s77", "s78", "s79", "s80", "s81", "s82", "s83", "s84", "s85", "s86", "s87", "vcc", "scc", "memory");
  const u32x16 wv[8] = {w0, w1, w2, w3, w4, w5, w6, w7};
  tail_body(lds, tag, ob, wid, blockIdx.x, (const unsigned short*)kp->x, kp->wr, kp->ws, kp->out, wv);
  asm volatile("AT_kend:");
}

extern "C" void kernel_launch(void* const* d_in, const int* in_sizes, int n_in, void* d_out, int out_size, void* d_ws, size_t ws_size,
                              hipStream_t stream) {
  (void)hipFuncSetAttribute((const void*)k_main, hipFuncAttributeMaxDynamicSharedMemorySize, 163840);
  k_main<<<256, 256, 163840, stream>>>(d_in[0], d_in[3], d_in[1], d_in[2], d_in[4], d_in[10], d_in[11], d_ws, d_in[7], d_in[8], d_in[9], d_out, d_in[6]);
}
```

```cpp
#include <hip/hip_runtime.h>
#include <stdint.h>


constexpr int SQ = 2048, DM = 576, HKV = 16, FF = 4608;

constexpr size_t WS_FLAG = 0;
constexpr size_t WS_R    = 20480;
constexpr size_t WS_GRAN = 32768;
constexpr size_t WS_EPOCH = 65536;
constexpr size_t WS_ST   = 69632;
constexpr size_t WS_PART = 139264;
constexpr size_t WS_CAND = 16384;
constexpr size_t WS_ROW  = WS_PART + (size_t)HKV * SQ * DM * 2;
constexpr size_t WS_Y    = WS_ROW + (size_t)SQ * DM * 16;
constexpr size_t WS_END  = WS_Y + 72 * 192 * 128;

struct Buf { const void* p; unsigned bytes; unsigned fmt; };
struct KArgs { Buf k, v, x, wq, wo, ws, c, wmod, wg, wu, wd, out, wr; };

__global__ __launch_bounds__(256, 1) void k_main(KArgs args) {
  extern __shared__ __attribute__((aligned(16))) char lds[];
  const unsigned wid = __builtin_amdgcn_readfirstlane(threadIdx.x >> 6);
  const unsigned ldsb = (unsigned)(size_t)lds;
  asm volatile(
"AT_start:\n"
"s_load_dwordx16 s[24:39], %[kp], 64\n"
"s_load_dwordx16 s[8:23], %[kp], 0\n"
"s_load_dwordx16 s[64:79], %[kp], 128\n"
"s_load_dwordx4 s[80:83], %[kp], 192\n"
"s_mov_b32 s42, %[blk]\n"
"s_mov_b32 s41, %[wid]\n"
"s_mov_b32 s40, %[lds]\n"
"s_mov_b32 s45, 0\n"
"v_mbcnt_lo_u32_b32 v248, -1, 0\n"
"v_mbcnt_hi_u32_b32 v248, -1, v248\n"
"s_and_b32 s56, s42, 3\n"
"s_lshr_b32 s57, s42, 3\n"
"s_and_b32 s58, s57, 3\n"
"s_lshl_b32 s56, s56, 2\n"
"s_add_u32 s43, s56, s58\n"
"s_lshr_b32 s44, s57, 2\n"
"s_and_b32 s56, s42, 7\n"
"s_lshr_b32 s56, s56, 2\n"
"s_lshl_b32 s56, s56, 3\n"
"s_add_u32 s44, s44, s56\n"
"s_lshr_b32 s56, s42, 5\n"
"s_lshl_b32 s56, s56, 3\n"
"s_and_b32 s58, s42, 7\n"
"s_add_u32 s56, s56, s58\n"
"s_lshl_b32 s56, s56, 2\n"
"s_and_b32 s57, s57, 3\n"
"s_add_u32 s55, s56, s57\n"
"v_lshrrev_b32 v226, 4, v248\n"
"v_bfe_u32 v227, v248, 2, 2\n"
"v_and_b32 v228, 3, v248\n"
"v_and_b32 v229, 15, v248\n"
"v_lshrrev_b32 v230, 3, v248\n"
"v_and_b32 v231, 7, v248\n"
"s_lshl_b32 s56, s41, 6\n"
"v_add_u32 v234, s56, v248\n"
"v_lshrrev_b32 v232, 1, v234\n"
"v_and_b32 v233, 1, v234\n"
"v_lshlrev_b32 v236, 1, v232\n"
"v_mul_u32_u24 v232, 6912, v232\n"
"v_lshl_add_u32 v235, v233, 4, v232\n"
"s_lshl_b32 s56, s55, 5\n"
"s_add_u32 s57, s56, 884736\n"
"s_add_u32 s58, s56, 1769472\n"
"s_add_u32 s59, s56, 2654208\n"
"s_add_u32 s60, s56, 3538944\n"
"v_lshrrev_b32 v232, 1, v230\n"
"v_xor_b32 v232, v232, v231\n"
"v_mul_u32_u24 v233, 1152, v230\n"
"v_lshl_add_u32 v222, v232, 4, v233\n"
"v_xor_b32 v232, 4, v232\n"
"v_lshl_add_u32 v223, v232, 4, v233\n"
"s_lshl_b32 s61, s44, 2\n"
"s_add_u32 s61, s61, s41\n"
"s_mul_i32 s46, s61, 36864\n"
"s_add_u32 s47, s46, 9216\n"
"s_add_u32 s48, s46, 18432\n"
"s_add_u32 s49, s46, 27648\n"
"s_mul_i32 s54, s61, 36864\n"
"s_mul_i32 s62, s43, 2359296\n"
"s_add_u32 s54, s54, s62\n"
"s_add_u32 s54, s54, 139264\n"
"s_mul_i32 s63, s41, 36864\n"
"s_add_u32 s63, s63, s40\n"
"s_getpc_b64 s[84:85]\n"
"AT_pc0:\n"
"s_sub_u32 s84, s84, AT_pc0-AT_start\n"
"s_subb_u32 s85, s85, 0\n"
"s_and_b32 s85, s85, 0xffff\n"
"s_mov_b32 s86, AT_kend-AT_start\n"
"s_mov_b32 s87, 0x20000\n"
"s_cmp_ge_u32 s42, 224\n"
"s_cselect_b32 s86, s86, 0\n"
"s_lshr_b32 s52, s42, 3\n"
"s_lshl_b32 s52, s52, 2\n"
"s_add_u32 s52, s52, s41\n"
"s_and_b32 s52, s52, 15\n"
"s_lshl_b32 s52, s52, 12\n"
"v_lshlrev_b32 v214, 6, v248\n"
"buffer_load_dword v250, v214, s[84:87], s52 offen\n"
"s_add_u32 s52, s52, 65536\n"
"buffer_load_dword v250, v214, s[84:87], s52 offen\n"
"s_waitcnt lgkmcnt(0)\n"
"s_load_dword s85, s[28:29], 65536\n"
"s_cmp_lt_u32 s55, 216\n"
"s_cselect_b32 s38, s38, 0\n"
"buffer_load_ushort v164, v236, s[32:35], 0 offen offset:0\n"
"buffer_load_ushort v165, v236, s[32:35], 0 offen offset:256\n"
"buffer_load_ushort v166, v236, s[32:35], 0 offen offset:512\n"
"buffer_load_ushort v167, v236, s[32:35], 0 offen offset:768\n"
"buffer_load_ushort v168, v236, s[32:35], 0 offen offset:1024\n"
"buffer_load_dwordx4 v[144:147], v235, s[36:39], s56 offen\n"
"buffer_load_dwordx4 v[148:151], v235, s[36:39], s57 offen\n"
"buffer_load_dwordx4 v[152:155], v235, s[36:39], s58 offen\n"
"buffer_load_dwordx4 v[156:159], v235, s[36:39], s59 offen\n"
"buffer_load_dwordx4 v[160:163], v235, s[36:39], s60 offen\n"
"s_mov_b32 m0, s63\n"
"s_nop 0\n"
"buffer_load_dwordx4 v222, s[16:19], s46 offen offset:0 lds\n"
"s_add_u32 m0, s63, 1024\n"
"s_nop 0\n"
"buffer_load_dwordx4 v223, s[16:19], s47 offen offset:0 lds\n"
"s_add_u32 m0, s63, 2048\n"
"s_nop 0\n"
"buffer_load_dwordx4 v222, s[16:19], s48 offen offset:0 lds\n"
"s_add_u32 m0, s63, 3072\n"
"s_nop 0\n"
"buffer_load_dwordx4 v223, s[16:19], s49 offen offset:0 lds\n"
"s_add_u32 m0, s63, 3968\n"
"s_nop 0\n"
"buffer_load_dwordx4 v222, s[16:19], s46 offen offset:128 lds\n"
"s_add_u32 m0, s63, 4992\n"
"s_nop 0\n"
"buffer_load_dwordx4 v223, s[16:19], s47 offen offset:128 lds\n"
"s_add_u32 m0, s63, 6016\n"
"s_nop 0\n"
"buffer_load_dwordx4 v222, s[16:19], s48 offen offset:128 lds\n"
"s_add_u32 m0, s63, 7040\n"
"s_nop 0\n"
"buffer_load_dwordx4 v223, s[16:19], s49 offen offset:128 lds\n"
"s_lshl_b32 s56, s41, 12\n"
"s_add_u32 s86, s40, s56\n"
"s_lshl_b32 s56, s41, 13\n"
"s_add_u32 s53, s40, s56\n"
"s_lshl_b32 s84, s41, 12\n"
"s_lshl_b32 s56, s43, 19\n"
"s_add_u32 s84, s84, s56\n"
"s_lshl_b32 s56, s43, 2\n"
"s_add_u32 s56, s56, s41\n"
"s_mul_i32 s52, s56, 73728\n"
"s_lshl_b32 s50, s41, 17\n"
"s_lshl_b32 s56, s43, 9\n"
"s_add_u32 s50, s50, s56\n"
"s_lshl_b32 s56, s41, 11\n"
"s_add_u32 s51, s40, s56\n"
"s_mov_b32 s61, 32768\n"
"s_mov_b32 s62, 37376\n"
"v_lshlrev_b32 v232, 13, v227\n"
"v_lshl_add_u32 v232, v226, 6, v232\n"
"v_lshl_add_u32 v217, v228, 4, v232\n"
"v_and_b32 v232, 2, v227\n"
"v_xor_b32 v232, v232, v226\n"
"v_lshlrev_b32 v233, 10, v227\n"
"v_lshl_add_u32 v233, v228, 6, v233\n"
"v_lshl_add_u32 v233, v232, 4, v233\n"
"v_add_u32 v240, s40, v233\n"
"v_add_u32 v241, 0x10000, v240\n"
"v_lshlrev_b32 v233, 10, v226\n"
"v_lshl_add_u32 v233, v227, 6, v233\n"
"v_lshl_add_u32 v233, v228, 3, v233\n"
"v_add_u32 v233, s40, v233\n"
"v_and_b32 v232, 1, v226\n"
"v_lshl_add_u32 v242, v232, 5, v233\n"
"v_xor_b32 v243, 32, v242\n"
"v_add_u32 v244, 0x10000, v242\n"
"v_add_u32 v245, 0x10000, v243\n"
"v_lshlrev_b32 v233, 8, v227\n"
"v_lshl_add_u32 v233, v226, 6, v233\n"
"v_lshl_add_u32 v246, v228, 4, v233\n"
"v_xor_b32 v247, 32, v246\n"
"v_mul_u32_u24 v233, 16392, v232\n"
"v_lshrrev_b32 v232, 1, v226\n"
"v_lshl_add_u32 v233, v232, 11, v233\n"
"v_lshl_add_u32 v233, v227, 6, v233\n"
"v_lshl_add_u32 v233, v228, 4, v233\n"
"v_add_u32 v218, s40, v233\n"
"v_add_u32 v219, 65568, v218\n"
"v_lshrrev_b32 v232, 1, v229\n"
"v_xor_b32 v232, v232, v226\n"
"v_lshlrev_b32 v233, 7, v229\n"
"v_lshl_add_u32 v233, v232, 4, v233\n"
"v_add_u32 v220, s63, v233\n"
"v_xor_b32 v221, 64, v220\n"
"s_add_u32 s56, s40, 148480\n"
"v_lshl_add_u32 v224, v226, 4, s56\n"
"v_lshrrev_b32 v232, 3, v234\n"
"v_lshlrev_b32 v233, 8, v232\n"
"v_and_b32 v232, 1, v234\n"
"v_lshl_add_u32 v233, v232, 6, v233\n"
"v_bfe_u32 v232, v234, 1, 2\n"
"v_lshl_add_u32 v233, v232, 4, v233\n"
"v_add_u32 v216, s56, v233\n"
"v_lshlrev_b32 v225, 5, v234\n"
"v_mov_b32 v232, 0x40000000\n"
"v_cmp_gt_u32 vcc, 144, v234\n"
"s_nop 1\n"
"v_cndmask_b32 v225, v232, v225, vcc\n"
"s_lshl_b32 s56, s41, 8\n"
"s_add_u32 s57, s40, 147456\n"
"s_add_u32 s56, s56, s57\n"
"v_and_b32 v232, 1, v248\n"
"v_lshlrev_b32 v232, 5, v232\n"
"v_lshl_add_u32 v232, v226, 6, v232\n"
"v_add_u32 v238, s56, v232\n"
"v_lshlrev_b32 v232, 8, v226\n"
"v_lshl_add_u32 v232, v229, 2, v232\n"
"v_add_u32 v239, s57, v232\n"
"v_mov_b32 v237, 0xffff0000\n"
"v_mov_b32 v176, 0\n"
"v_mov_b32 v177, 0\n"
"v_mov_b32 v178, 0\n"
"v_mov_b32 v179, 0\n"
"v_mov_b32 v180, 0\n"
"v_mov_b32 v181, 0\n"
"v_mov_b32 v182, 0\n"
"v_mov_b32 v183, 0\n"
"s_waitcnt vmcnt(13)\n"
"v_lshlrev_b32 v164, 16, v164\n"
"v_mul_f32 v184, 0xbfb8aa3b, v164\n"
"v_lshlrev_b32 v165, 16, v165\n"
"v_mul_f32 v185, 0xbfb8aa3b, v165\n"
"v_lshlrev_b32 v166, 16, v166\n"
"v_mul_f32 v186, 0xbfb8aa3b, v166\n"
"v_lshlrev_b32 v167, 16, v167\n"
"v_mul_f32 v187, 0xbfb8aa3b, v167\n"
"v_lshlrev_b32 v168, 16, v168\n"
"v_mul_f32 v188, 0xbfb8aa3b, v168\n"
"v_exp_f32 v184, v184\n"
"s_add_u32 m0, s63, 7936\n"
"s_nop 0\n"
"buffer_load_dwordx4 v222, s[16:19], s46 offen offset:256 lds\n"
"v_exp_f32 v185, v185\n"
"v_exp_f32 v186, v186\n"
"v_exp_f32 v187, v187\n"
"v_exp_f32 v188, v188\n"
"v_add_f32 v184, 1.0, v184\n"
"v_add_f32 v185, 1.0, v185\n"
"v_add_f32 v186, 1.0, v186\n"
"v_add_f32 v187, 1.0, v187\n"
"v_add_f32 v188, 1.0, v188\n"
"v_rcp_f32 v184, v184\n"
"v_rcp_f32 v185, v185\n"
"v_rcp_f32 v186, v186\n"
"s_add_u32 m0, s63, 8960\n"
"s_nop 0\n"
"buffer_load_dwordx4 v223, s[16:19], s47 offen offset:256 lds\n"
"v_rcp_f32 v187, v187\n"
"v_rcp_f32 v188, v188\n"
"v_mul_f32 v164, v164, v184\n"
"v_mul_f32 v165, v165, v185\n"
"v_mul_f32 v166, v166, v186\n"
"v_mul_f32 v167, v167, v187\n"
"v_mul_f32 v168, v168, v188\n"
"v_cvt_pk_bf16_f32 v164, v164, v164\n"
"v_and_b32 v164, v237, v164\n"
"v_cvt_pk_bf16_f32 v165, v165, v165\n"
"v_and_b32 v165, v237, v165\n"
"v_cvt_pk_bf16_f32 v166, v166, v166\n"
"s_add_u32 m0, s63, 9984\n"
"s_nop 0\n"
"buffer_load_dwordx4 v222, s[16:19], s48 offen offset:256 lds\n"
"v_and_b32 v166, v237, v166\n"
"v_cvt_pk_bf16_f32 v167, v167, v167\n"
"v_and_b32 v167, v237, v167\n"
"v_cvt_pk_bf16_f32 v168, v168, v168\n"
"v_and_b32 v168, v237, v168\n"
"s_waitcnt vmcnt(15)\n"
"v_lshlrev_b32 v184, 16, v144\n"
"v_and_b32 v185, v237, v144\n"
"v_lshlrev_b32 v186, 16, v145\n"
"v_and_b32 v187, v237, v145\n"
"v_lshlrev_b32 v188, 16, v146\n"
"v_and_b32 v189, v237, v146\n"
"s_add_u32 m0, s63, 11008\n"
"s_nop 0\n"
"buffer_load_dwordx4 v223, s[16:19], s49 offen offset:256 lds\n"
"v_lshlrev_b32 v190, 16, v147\n"
"v_and_b32 v191, v237, v147\n"
"v_fmac_f32 v176, v164, v184\n"
"v_fmac_f32 v177, v164, v185\n"
"v_fmac_f32 v178, v164, v186\n"
"v_fmac_f32 v179, v164, v187\n"
"v_fmac_f32 v180, v164, v188\n"
"v_fmac_f32 v181, v164, v189\n"
"v_fmac_f32 v182, v164, v190\n"
"v_fmac_f32 v183, v164, v191\n"
"s_waitcnt vmcnt(15)\n"
"v_lshlrev_b32 v184, 16, v148\n"
"s_add_u32 m0, s63, 11904\n"
"s_nop 0\n"
"buffer_load_dwordx4 v222, s[16:19], s46 offen offset:384 lds\n"
"v_and_b32 v185, v237, v148\n"
"v_lshlrev_b32 v186, 16, v149\n"
"v_and_b32 v187, v237, v149\n"
"v_lshlrev_b32 v188, 16, v150\n"
"v_and_b32 v189, v237, v150\n"
"v_lshlrev_b32 v190, 16, v151\n"
"v_and_b32 v191, v237, v151\n"
"v_fmac_f32 v176, v165, v184\n"
"v_fmac_f32 v177, v165, v185\n"
"v_fmac_f32 v178, v165, v186\n"
"v_fmac_f32 v179, v165, v187\n"
"v_fmac_f32 v180, v165, v188\n"
"s_add_u32 m0, s63, 12928\n"
"s_nop 0\n"
"buffer_load_dwordx4 v223, s[16:19], s47 offen offset:384 lds\n"
"v_fmac_f32 v181, v165, v189\n"
"v_fmac_f32 v182, v165, v190\n"
"v_fmac_f32 v183, v165, v191\n"
"s_waitcnt vmcnt(16)\n"
"v_lshlrev_b32 v184, 16, v152\n"
"v_and_b32 v185, v237, v152\n"
"v_lshlrev_b32 v186, 16, v153\n"
"v_and_b32 v187, v237, v153\n"
"v_lshlrev_b32 v188, 16, v154\n"
"v_and_b32 v189, v237, v154\n"
"v_lshlrev_b32 v190, 16, v155\n"
"v_and_b32 v191, v237, v155\n"
"s_add_u32 m0, s63, 13952\n"
"s_nop 0\n"
"buffer_load_dwordx4 v222, s[16:19], s48 offen offset:384 lds\n"
"v_fmac_f32 v176, v166, v184\n"
"v_fmac_f32 v177, v166, v185\n"
"v_fmac_f32 v178, v166, v186\n"
"v_fmac_f32 v179, v166, v187\n"
"v_fmac_f32 v180, v166, v188\n"
"v_fmac_f32 v181, v166, v189\n"
"v_fmac_f32 v182, v166, v190\n"
"v_fmac_f32 v183, v166, v191\n"
"s_waitcnt vmcnt(16)\n"
"v_lshlrev_b32 v184, 16, v156\n"
"v_and_b32 v185, v237, v156\n"
"v_lshlrev_b32 v186, 16, v157\n"
"s_add_u32 m0, s63, 14976\n"
"s_nop 0\n"
"buffer_load_dwordx4 v223, s[16:19], s49 offen offset:384 lds\n"
"v_and_b32 v187, v237, v157\n"
"v_lshlrev_b32 v188, 16, v158\n"
"v_and_b32 v189, v237, v158\n"
"v_lshlrev_b32 v190, 16, v159\n"
"v_and_b32 v191, v237, v159\n"
"v_fmac_f32 v176, v167, v184\n"
"v_fmac_f32 v177, v167, v185\n"
"v_fmac_f32 v178, v167, v186\n"
"v_fmac_f32 v179, v167, v187\n"
"v_fmac_f32 v180, v167, v188\n"
"v_fmac_f32 v181, v167, v189\n"
"v_fmac_f32 v182, v167, v190\n"
"s_add_u32 m0, s63, 15872\n"
"s_nop 0\n"
"buffer_load_dwordx4 v222, s[16:19], s46 offen offset:512 lds\n"
"v_fmac_f32 v183, v167, v191\n"
"s_waitcnt vmcnt(17)\n"
"v_lshlrev_b32 v184, 16, v160\n"
"v_and_b32 v185, v237, v160\n"
"v_lshlrev_b32 v186, 16, v161\n"
"v_and_b32 v187, v237, v161\n"
"v_lshlrev_b32 v188, 16, v162\n"
"v_and_b32 v189, v237, v162\n"
"v_lshlrev_b32 v190, 16, v163\n"
"v_and_b32 v191, v237, v163\n"
"v_fmac_f32 v176, v168, v184\n"
"v_fmac_f32 v177, v168, v185\n"
"s_add_u32 m0, s63, 16896\n"
"s_nop 0\n"
"buffer_load_dwordx4 v223, s[16:19], s47 offen offset:512 lds\n"
"v_fmac_f32 v178, v168, v186\n"
"v_fmac_f32 v179, v168, v187\n"
"v_fmac_f32 v180, v168, v188\n"
"v_fmac_f32 v181, v168, v189\n"
"v_fmac_f32 v182, v168, v190\n"
"v_fmac_f32 v183, v168, v191\n"
"v_add_f32_dpp v176, v176, v176 row_ror:2 row_mask:0xf bank_mask:0xf\n"
"v_add_f32_dpp v177, v177, v177 row_ror:2 row_mask:0xf bank_mask:0xf\n"
"v_add_f32_dpp v178, v178, v178 row_ror:2 row_mask:0xf bank_mask:0xf\n"
"v_add_f32_dpp v179, v179, v179 row_ror:2 row_mask:0xf bank_mask:0xf\n"
"v_add_f32_dpp v180, v180, v180 row_ror:2 row_mask:0xf bank_mask:0xf\n"
"v_add_f32_dpp v181, v181, v181 row_ror:2 row_mask:0xf bank_mask:0xf\n"
"s_add_u32 m0, s63, 17920\n"
"s_nop 0\n"
"buffer_load_dwordx4 v222, s[16:19], s48 offen offset:512 lds\n"
"v_add_f32_dpp v182, v182, v182 row_ror:2 row_mask:0xf bank_mask:0xf\n"
"v_add_f32_dpp v183, v183, v183 row_ror:2 row_mask:0xf bank_mask:0xf\n"
"v_add_f32_dpp v176, v176, v176 row_ror:4 row_mask:0xf bank_mask:0xf\n"
"v_add_f32_dpp v177, v177, v177 row_ror:4 row_mask:0xf bank_mask:0xf\n"
"v_add_f32_dpp v178, v178, v178 row_ror:4 row_mask:0xf bank_mask:0xf\n"
"v_add_f32_dpp v179, v179, v179 row_ror:4 row_mask:0xf bank_mask:0xf\n"
"v_add_f32_dpp v180, v180, v180 row_ror:4 row_mask:0xf bank_mask:0xf\n"
"v_add_f32_dpp v181, v181, v181 row_ror:4 row_mask:0xf bank_mask:0xf\n"
"v_add_f32_dpp v182, v182, v182 row_ror:4 row_mask:0xf bank_mask:0xf\n"
"v_add_f32_dpp v183, v183, v183 row_ror:4 row_mask:0xf bank_mask:0xf\n"
"v_add_f32_dpp v176, v176, v176 row_ror:8 row_mask:0xf bank_mask:0xf\n"
"v_add_f32_dpp v177, v177, v177 row_ror:8 row_mask:0xf bank_mask:0xf\n"
"s_add_u32 m0, s63, 18944\n"
"s_nop 0\n"
"buffer_load_dwordx4 v223, s[16:19], s49 offen offset:512 lds\n"
"v_add_f32_dpp v178, v178, v178 row_ror:8 row_mask:0xf bank_mask:0xf\n"
"v_add_f32_dpp v179, v179, v179 row_ror:8 row_mask:0xf bank_mask:0xf\n"
"v_add_f32_dpp v180, v180, v180 row_ror:8 row_mask:0xf bank_mask:0xf\n"
"v_add_f32_dpp v181, v181, v181 row_ror:8 row_mask:0xf bank_mask:0xf\n"
"v_add_f32_dpp v182, v182, v182 row_ror:8 row_mask:0xf bank_mask:0xf\n"
"v_add_f32_dpp v183, v183, v183 row_ror:8 row_mask:0xf bank_mask:0xf\n"
"s_mov_b32 exec_lo, 0x00030003\n"
"s_mov_b32 exec_hi, 0x00030003\n"
"ds_write_b128 v238, v[176:179]\n"
"ds_write_b128 v238, v[180:183] offset:16\n"
"s_mov_b64 exec, -1\n"
"s_waitcnt lgkmcnt(0)\n"
"s_barrier\n"
"ds_read_b32 v184, v239 offset:0\n"
"ds_read_b32 v185, v239 offset:64\n"
"ds_read_b32 v186, v239 offset:128\n"
"ds_read_b32 v187, v239 offset:192\n"
"s_add_u32 s85, s85, 1\n"
"s_lshl_b32 s56, s55, 7\n"
"s_add_u32 s56, s56, 32768\n"
"s_lshl_b32 s58, s41, 2\n"
"s_lshl_b32 s58, 15, s58\n"
"v_lshlrev_b32 v190, 3, v248\n"
"v_mov_b32 v189, s85\n"
"s_waitcnt lgkmcnt(2)\n"
"v_add_f32 v184, v184, v185\n"
"s_waitcnt lgkmcnt(0)\n"
"v_add_f32 v186, v186, v187\n"
"v_add_f32 v184, v184, v186\n"
"v_mov_b32 v185, v184\n"
"s_nop 1\n"
"v_permlane32_swap_b32 v184, v185\n"
"v_add_f32 v184, v184, v185\n"
"v_mov_b32 v185, v184\n"
"s_nop 1\n"
"v_permlane16_swap_b32 v184, v185\n"
"v_add_f32 v184, v184, v185\n"
"v_cvt_pk_bf16_f32 v188, v184, v184\n"
"v_and_b32 v188, v237, v188\n"
"s_mov_b32 exec_lo, s58\n"
"s_mov_b32 exec_hi, 0\n"
"buffer_store_dwordx2 v[188:189], v190, s[28:31], s56 offen sc1\n"
"s_mov_b64 exec, -1\n"
"s_add_u32 m0, s63, 19840\n"
"s_nop 0\n"
"buffer_load_dwordx4 v222, s[16:19], s46 offen offset:640 lds\n"
"s_add_u32 m0, s63, 20864\n"
"s_nop 0\n"
"buffer_load_dwordx4 v223, s[16:19], s47 offen offset:640 lds\n"
"s_add_u32 m0, s63, 21888\n"
"s_nop 0\n"
"buffer_load_dwordx4 v222, s[16:19], s48 offen offset:640 lds\n"
"s_add_u32 m0, s63, 22912\n"
"s_nop 0\n"
"buffer_load_dwordx4 v223, s[16:19], s49 offen offset:640 lds\n"
"s_add_u32 m0, s63, 23808\n"
"s_nop 0\n"
"buffer_load_dwordx4 v222, s[16:19], s46 offen offset:768 lds\n"
"s_add_u32 m0, s63, 24832\n"
"s_nop 0\n"
"buffer_load_dwordx4 v223, s[16:19], s47 offen offset:768 lds\n"
"s_add_u32 m0, s63, 25856\n"
"s_nop 0\n"
"buffer_load_dwordx4 v222, s[16:19], s48 offen offset:768 lds\n"
"s_add_u32 m0, s63, 26880\n"
"s_nop 0\n"
"buffer_load_dwordx4 v223, s[16:19], s49 offen offset:768 lds\n"
"s_add_u32 m0, s63, 27776\n"
"s_nop 0\n"
"buffer_load_dwordx4 v222, s[16:19], s46 offen offset:896 lds\n"
"s_add_u32 m0, s63, 28800\n"
"s_nop 0\n"
"buffer_load_dwordx4 v223, s[16:19], s47 offen offset:896 lds\n"
"s_add_u32 m0, s63, 29824\n"
"s_nop 0\n"
"buffer_load_dwordx4 v222, s[16:19], s48 offen offset:896 lds\n"
"s_add_u32 m0, s63, 30848\n"
"s_nop 0\n"
"buffer_load_dwordx4 v223, s[16:19], s49 offen offset:896 lds\n"
"s_add_u32 m0, s63, 31744\n"
"s_nop 0\n"
"buffer_load_dwordx4 v222, s[16:19], s46 offen offset:1024 lds\n"
"s_add_u32 m0, s63, 32768\n"
"s_nop 0\n"
"buffer_load_dwordx4 v223, s[16:19], s47 offen offset:1024 lds\n"
"s_add_u32 m0, s63, 33792\n"
"s_nop 0\n"
"buffer_load_dwordx4 v222, s[16:19], s48 offen offset:1024 lds\n"
"s_add_u32 m0, s63, 34816\n"
"s_nop 0\n"
"buffer_load_dwordx4 v223, s[16:19], s49 offen offset:1024 lds\n"
"v_mov_b32 v236, 0x3f803f80\n"
"v_mov_b32 v200, 0\n"
"v_mov_b32 v202, 0\n"
"v_mov_b32 v204, 0\n"
"v_mov_b32 v206, 0\n"
"v_mov_b32 v208, 0\n"
"v_mov_b32 v210, 0\n"
"v_mov_b32 v212, 0\n"
"v_mov_b32 v214, 0\n"
"s_waitcnt vmcnt(33)\n"
"ds_read_b128 v[0:3], v220 offset:0\n"
"ds_read_b128 v[4:7], v220 offset:2048\n"
"ds_read_b128 v[8:11], v221 offset:0\n"
"ds_read_b128 v[12:15], v221 offset:2048\n"
"s_waitcnt vmcnt(29)\n"
"ds_read_b128 v[16:19], v220 offset:4096\n"
"ds_read_b128 v[20:23], v220 offset:6144\n"
"ds_read_b128 v[24:27], v221 offset:4096\n"
"ds_read_b128 v[28:31], v221 offset:6144\n"
"s_waitcnt lgkmcnt(7)\n"
"v_dot2c_f32_bf16 v200, v0, v236\n"
"v_dot2c_f32_bf16 v202, v0, v0\n"
"s_waitcnt lgkmcnt(6)\n"
"v_dot2c_f32_bf16 v204, v4, v236\n"
"v_dot2c_f32_bf16 v206, v4, v4\n"
"s_waitcnt lgkmcnt(5)\n"
"v_dot2c_f32_bf16 v208, v8, v236\n"
"v_dot2c_f32_bf16 v210, v8, v8\n"
"s_waitcnt lgkmcnt(4)\n"
"v_dot2c_f32_bf16 v212, v12, v236\n"
"v_dot2c_f32_bf16 v214, v12, v12\n"
"v_dot2c_f32_bf16 v200, v1, v236\n"
"v_dot2c_f32_bf16 v202, v1, v1\n"
"v_dot2c_f32_bf16 v204, v5, v236\n"
"v_dot2c_f32_bf16 v206, v5, v5\n"
"v_dot2c_f32_bf16 v208, v9, v236\n"
"v_dot2c_f32_bf16 v210, v9, v9\n"
"v_dot2c_f32_bf16 v212, v13, v236\n"
"v_dot2c_f32_bf16 v214, v13, v13\n"
"v_dot2c_f32_bf16 v200, v2, v236\n"
"v_dot2c_f32_bf16 v202, v2, v2\n"
"v_dot2c_f32_bf16 v204, v6, v236\n"
"v_dot2c_f32_bf16 v206, v6, v6\n"
"v_dot2c_f32_bf16 v208, v10, v236\n"
"v_dot2c_f32_bf16 v210, v10, v10\n"
"v_dot2c_f32_bf16 v212, v14, v236\n"
"v_dot2c_f32_bf16 v214, v14, v14\n"
"v_dot2c_f32_bf16 v200, v3, v236\n"
"v_dot2c_f32_bf16 v202, v3, v3\n"
"v_dot2c_f32_bf16 v204, v7, v236\n"
"v_dot2c_f32_bf16 v206, v7, v7\n"
"v_dot2c_f32_bf16 v208, v11, v236\n"
"v_dot2c_f32_bf16 v210, v11, v11\n"
"v_dot2c_f32_bf16 v212, v15, v236\n"
"v_dot2c_f32_bf16 v214, v15, v15\n"
"s_waitcnt vmcnt(25)\n"
"ds_read_b128 v[32:35], v220 offset:8192\n"
"ds_read_b128 v[36:39], v220 offset:10240\n"
"ds_read_b128 v[40:43], v221 offset:8192\n"
"ds_read_b128 v[44:47], v221 offset:10240\n"
"s_waitcnt lgkmcnt(7)\n"
"v_dot2c_f32_bf16 v200, v16, v236\n"
"v_dot2c_f32_bf16 v202, v16, v16\n"
"s_waitcnt lgkmcnt(6)\n"
"v_dot2c_f32_bf16 v204, v20, v236\n"
"v_dot2c_f32_bf16 v206, v20, v20\n"
"s_waitcnt lgkmcnt(5)\n"
"v_dot2c_f32_bf16 v208, v24, v236\n"
"v_dot2c_f32_bf16 v210, v24, v24\n"
"s_waitcnt lgkmcnt(4)\n"
"v_dot2c_f32_bf16 v212, v28, v236\n"
"v_dot2c_f32_bf16 v214, v28, v28\n"
"v_dot2c_f32_bf16 v200, v17, v236\n"
"v_dot2c_f32_bf16 v202, v17, v17\n"
"v_dot2c_f32_bf16 v204, v21, v236\n"
"v_dot2c_f32_bf16 v206, v21, v21\n"
"v_dot2c_f32_bf16 v208, v25, v236\n"
"v_dot2c_f32_bf16 v210, v25, v25\n"
"v_dot2c_f32_bf16 v212, v29, v236\n"
"v_dot2c_f32_bf16 v214, v29, v29\n"
"v_dot2c_f32_bf16 v200, v18, v236\n"
"v_dot2c_f32_bf16 v202, v18, v18\n"
"v_dot2c_f32_bf16 v204, v22, v236\n"
"v_dot2c_f32_bf16 v206, v22, v22\n"
"v_dot2c_f32_bf16 v208, v26, v236\n"
"v_dot2c_f32_bf16 v210, v26, v26\n"
"v_dot2c_f32_bf16 v212, v30, v236\n"
"v_dot2c_f32_bf16 v214, v30, v30\n"
"v_dot2c_f32_bf16 v200, v19, v236\n"
"v_dot2c_f32_bf16 v202, v19, v19\n"
"v_dot2c_f32_bf16 v204, v23, v236\n"
"v_dot2c_f32_bf16 v206, v23, v23\n"
"v_dot2c_f32_bf16 v208, v27, v236\n"
"v_dot2c_f32_bf16 v210, v27, v27\n"
"v_dot2c_f32_bf16 v212, v31, v236\n"
"v_dot2c_f32_bf16 v214, v31, v31\n"
"s_waitcnt vmcnt(21)\n"
"ds_read_b128 v[48:51], v220 offset:12288\n"
"ds_read_b128 v[52:55], v220 offset:14336\n"
"ds_read_b128 v[56:59], v221 offset:12288\n"
"ds_read_b128 v[60:63], v221 offset:14336\n"
"s_waitcnt lgkmcnt(7)\n"
"v_dot2c_f32_bf16 v200, v32, v236\n"
"v_dot2c_f32_bf16 v202, v32, v32\n"
"s_waitcnt lgkmcnt(6)\n"
"v_dot2c_f32_bf16 v204, v36, v236\n"
"v_dot2c_f32_bf16 v206, v36, v36\n"
"s_waitcnt lgkmcnt(5)\n"
"v_dot2c_f32_bf16 v208, v40, v236\n"
"v_dot2c_f32_bf16 v210, v40, v40\n"
"s_waitcnt lgkmcnt(4)\n"
"v_dot2c_f32_bf16 v212, v44, v236\n"
"v_dot2c_f32_bf16 v214, v44, v44\n"
"v_dot2c_f32_bf16 v200, v33, v236\n"
"v_dot2c_f32_bf16 v202, v33, v33\n"
"v_dot2c_f32_bf16 v204, v37, v236\n"
"v_dot2c_f32_bf16 v206, v37, v37\n"
"v_dot2c_f32_bf16 v208, v41, v236\n"
"v_dot2c_f32_bf16 v210, v41, v41\n"
"v_dot2c_f32_bf16 v212, v45, v236\n"
"v_dot2c_f32_bf16 v214, v45, v45\n"
"v_dot2c_f32_bf16 v200, v34, v236\n"
"v_dot2c_f32_bf16 v202, v34, v34\n"
"v_dot2c_f32_bf16 v204, v38, v236\n"
"v_dot2c_f32_bf16 v206, v38, v38\n"
"v_dot2c_f32_bf16 v208, v42, v236\n"
"v_dot2c_f32_bf16 v210, v42, v42\n"
"v_dot2c_f32_bf16 v212, v46, v236\n"
"v_dot2c_f32_bf16 v214, v46, v46\n"
"v_dot2c_f32_bf16 v200, v35, v236\n"
"v_dot2c_f32_bf16 v202, v35, v35\n"
"v_dot2c_f32_bf16 v204, v39, v236\n"
"v_dot2c_f32_bf16 v206, v39, v39\n"
"v_dot2c_f32_bf16 v208, v43, v236\n"
"v_dot2c_f32_bf16 v210, v43, v43\n"
"v_dot2c_f32_bf16 v212, v47, v236\n"
"v_dot2c_f32_bf16 v214, v47, v47\n"
"s_waitcnt vmcnt(17)\n"
"ds_read_b128 v[64:67], v220 offset:16384\n"
"ds_read_b128 v[68:71], v220 offset:18432\n"
"ds_read_b128 v[72:75], v221 offset:16384\n"
"ds_read_b128 v[76:79], v221 offset:18432\n"
"s_waitcnt lgkmcnt(7)\n"
"v_dot2c_f32_bf16 v200, v48, v236\n"
"v_dot2c_f32_bf16 v202, v48, v48\n"
"s_waitcnt lgkmcnt(6)\n"
"v_dot2c_f32_bf16 v204, v52, v236\n"
"v_dot2c_f32_bf16 v206, v52, v52\n"
"s_waitcnt lgkmcnt(5)\n"
"v_dot2c_f32_bf16 v208, v56, v236\n"
"v_dot2c_f32_bf16 v210, v56, v56\n"
"s_waitcnt lgkmcnt(4)\n"
"v_dot2c_f32_bf16 v212, v60, v236\n"
"v_dot2c_f32_bf16 v214, v60, v60\n"
"v_dot2c_f32_bf16 v200, v49, v236\n"
"v_dot2c_f32_bf16 v202, v49, v49\n"
"v_dot2c_f32_bf16 v204, v53, v236\n"
"v_dot2c_f32_bf16 v206, v53, v53\n"
"v_dot2c_f32_bf16 v208, v57, v236\n"
"v_dot2c_f32_bf16 v210, v57, v57\n"
"v_dot2c_f32_bf16 v212, v61, v236\n"
"v_dot2c_f32_bf16 v214, v61, v61\n"
"v_dot2c_f32_bf16 v200, v50, v236\n"
"v_dot2c_f32_bf16 v202, v50, v50\n"
"v_dot2c_f32_bf16 v204, v54, v236\n"
"v_dot2c_f32_bf16 v206, v54, v54\n"
"v_dot2c_f32_bf16 v208, v58, v236\n"
"v_dot2c_f32_bf16 v210, v58, v58\n"
"v_dot2c_f32_bf16 v212, v62, v236\n"
"v_dot2c_f32_bf16 v214, v62, v62\n"
"v_dot2c_f32_bf16 v200, v51, v236\n"
"v_dot2c_f32_bf16 v202, v51, v51\n"
"v_dot2c_f32_bf16 v204, v55, v236\n"
"v_dot2c_f32_bf16 v206, v55, v55\n"
"v_dot2c_f32_bf16 v208, v59, v236\n"
"v_dot2c_f32_bf16 v210, v59, v59\n"
"v_dot2c_f32_bf16 v212, v63, v236\n"
"v_dot2c_f32_bf16 v214, v63, v63\n"
"s_waitcnt vmcnt(12)\n"
"ds_read_b128 v[80:83], v220 offset:20480\n"
"ds_read_b128 v[84:87], v220 offset:22528\n"
"ds_read_b128 v[88:91], v221 offset:20480\n"
"ds_read_b128 v[92:95], v221 offset:22528\n"
"s_waitcnt lgkmcnt(7)\n"
"v_dot2c_f32_bf16 v200, v64, v236\n"
"v_dot2c_f32_bf16 v202, v64, v64\n"
"s_waitcnt lgkmcnt(6)\n"
"v_dot2c_f32_bf16 v204, v68, v236\n"
"v_dot2c_f32_bf16 v206, v68, v68\n"
"s_waitcnt lgkmcnt(5)\n"
"v_dot2c_f32_bf16 v208, v72, v236\n"
"v_dot2c_f32_bf16 v210, v72, v72\n"
"s_waitcnt lgkmcnt(4)\n"
"v_dot2c_f32_bf16 v212, v76, v236\n"
"v_dot2c_f32_bf16 v214, v76, v76\n"
"v_dot2c_f32_bf16 v200, v65, v236\n"
"v_dot2c_f32_bf16 v202, v65, v65\n"
"v_dot2c_f32_bf16 v204, v69, v236\n"
"v_dot2c_f32_bf16 v206, v69, v69\n"
"v_dot2c_f32_bf16 v208, v73, v236\n"
"v_dot2c_f32_bf16 v210, v73, v73\n"
"v_dot2c_f32_bf16 v212, v77, v236\n"
"v_dot2c_f32_bf16 v214, v77, v77\n"
"v_dot2c_f32_bf16 v200, v66, v236\n"
"v_dot2c_f32_bf16 v202, v66, v66\n"
"v_dot2c_f32_bf16 v204, v70, v236\n"
"v_dot2c_f32_bf16 v206, v70, v70\n"
"v_dot2c_f32_bf16 v208, v74, v236\n"
"v_dot2c_f32_bf16 v210, v74, v74\n"
"v_dot2c_f32_bf16 v212, v78, v236\n"
"v_dot2c_f32_bf16 v214, v78, v78\n"
"v_dot2c_f32_bf16 v200, v67, v236\n"
"v_dot2c_f32_bf16 v202, v67, v67\n"
"v_dot2c_f32_bf16 v204, v71, v236\n"
"v_dot2c_f32_bf16 v206, v71, v71\n"
"v_dot2c_f32_bf16 v208, v75, v236\n"
"v_dot2c_f32_bf16 v210, v75, v75\n"
"v_dot2c_f32_bf16 v212, v79, v236\n"
"v_dot2c_f32_bf16 v214, v79, v79\n"
"s_waitcnt vmcnt(8)\n"
"ds_read_b128 v[96:99], v220 offset:24576\n"
"ds_read_b128 v[100:103], v220 offset:26624\n"
"ds_read_b128 v[104:107], v221 offset:24576\n"
"ds_read_b128 v[108:111], v221 offset:26624\n"
"s_waitcnt lgkmcnt(7)\n"
"v_dot2c_f32_bf16 v200, v80, v236\n"
"v_dot2c_f32_bf16 v202, v80, v80\n"
"s_waitcnt lgkmcnt(6)\n"
"v_dot2c_f32_bf16 v204, v84, v236\n"
"v_dot2c_f32_bf16 v206, v84, v84\n"
"s_waitcnt lgkmcnt(5)\n"
"v_dot2c_f32_bf16 v208, v88, v236\n"
"v_dot2c_f32_bf16 v210, v88, v88\n"
"s_waitcnt lgkmcnt(4)\n"
"v_dot2c_f32_bf16 v212, v92, v236\n"
"v_dot2c_f32_bf16 v214, v92, v92\n"
"v_dot2c_f32_bf16 v200, v81, v236\n"
"v_dot2c_f32_bf16 v202, v81, v81\n"
"v_dot2c_f32_bf16 v204, v85, v236\n"
"v_dot2c_f32_bf16 v206, v85, v85\n"
"v_dot2c_f32_bf16 v208, v89, v236\n"
"v_dot2c_f32_bf16 v210, v89, v89\n"
"v_dot2c_f32_bf16 v212, v93, v236\n"
"v_dot2c_f32_bf16 v214, v93, v93\n"
"v_dot2c_f32_bf16 v200, v82, v236\n"
"v_dot2c_f32_bf16 v202, v82, v82\n"
"v_dot2c_f32_bf16 v204, v86, v236\n"
"v_dot2c_f32_bf16 v206, v86, v86\n"
"v_dot2c_f32_bf16 v208, v90, v236\n"
"v_dot2c_f32_bf16 v210, v90, v90\n"
"v_dot2c_f32_bf16 v212, v94, v236\n"
"v_dot2c_f32_bf16 v214, v94, v94\n"
"v_dot2c_f32_bf16 v200, v83, v236\n"
"v_dot2c_f32_bf16 v202, v83, v83\n"
"v_dot2c_f32_bf16 v204, v87, v236\n"
"v_dot2c_f32_bf16 v206, v87, v87\n"
"v_dot2c_f32_bf16 v208, v91, v236\n"
"v_dot2c_f32_bf16 v210, v91, v91\n"
"v_dot2c_f32_bf16 v212, v95, v236\n"
"v_dot2c_f32_bf16 v214, v95, v95\n"
"s_waitcnt vmcnt(4)\n"
"ds_read_b128 v[112:115], v220 offset:28672\n"
"ds_read_b128 v[116:119], v220 offset:30720\n"
"ds_read_b128 v[120:123], v221 offset:28672\n"
"ds_read_b128 v[124:127], v221 offset:30720\n"
"s_waitcnt lgkmcnt(7)\n"
"v_dot2c_f32_bf16 v200, v96, v236\n"
"v_dot2c_f32_bf16 v202, v96, v96\n"
"s_waitcnt lgkmcnt(6)\n"
"v_dot2c_f32_bf16 v204, v100, v236\n"
"v_dot2c_f32_bf16 v206, v100, v100\n"
"s_waitcnt lgkmcnt(5)\n"
"v_dot2c_f32_bf16 v208, v104, v236\n"
"v_dot2c_f32_bf16 v210, v104, v104\n"
"s_waitcnt lgkmcnt(4)\n"
"v_dot2c_f32_bf16 v212, v108, v236\n"
"v_dot2c_f32_bf16 v214, v108, v108\n"
"v_dot2c_f32_bf16 v200, v97, v236\n"
"v_dot2c_f32_bf16 v202, v97, v97\n"
"v_dot2c_f32_bf16 v204, v101, v236\n"
"v_dot2c_f32_bf16 v206, v101, v101\n"
"v_dot2c_f32_bf16 v208, v105, v236\n"
"v_dot2c_f32_bf16 v210, v105, v105\n"
"v_dot2c_f32_bf16 v212, v109, v236\n"
"v_dot2c_f32_bf16 v214, v109, v109\n"
"v_dot2c_f32_bf16 v200, v98, v236\n"
"v_dot2c_f32_bf16 v202, v98, v98\n"
"v_dot2c_f32_bf16 v204, v102, v236\n"
"v_dot2c_f32_bf16 v206, v102, v102\n"
"v_dot2c_f32_bf16 v208, v106, v236\n"
"v_dot2c_f32_bf16 v210, v106, v106\n"
"v_dot2c_f32_bf16 v212, v110, v236\n"
"v_dot2c_f32_bf16 v214, v110, v110\n"
"v_dot2c_f32_bf16 v200, v99, v236\n"
"v_dot2c_f32_bf16 v202, v99, v99\n"
"v_dot2c_f32_bf16 v204, v103, v236\n"
"v_dot2c_f32_bf16 v206, v103, v103\n"
"v_dot2c_f32_bf16 v208, v107, v236\n"
"v_dot2c_f32_bf16 v210, v107, v107\n"
"v_dot2c_f32_bf16 v212, v111, v236\n"
"v_dot2c_f32_bf16 v214, v111, v111\n"
"s_waitcnt vmcnt(0)\n"
"ds_read_b128 v[128:131], v220 offset:32768\n"
"ds_read_b128 v[132:135], v220 offset:34816\n"
"ds_read_b128 v[136:139], v221 offset:32768\n"
"ds_read_b128 v[140:143], v221 offset:34816\n"
"s_waitcnt lgkmcnt(7)\n"
"v_dot2c_f32_bf16 v200, v112, v236\n"
"v_dot2c_f32_bf16 v202, v112, v112\n"
"s_waitcnt lgkmcnt(6)\n"
"v_dot2c_f32_bf16 v204, v116, v236\n"
"v_dot2c_f32_bf16 v206, v116, v116\n"
"s_waitcnt lgkmcnt(5)\n"
"v_dot2c_f32_bf16 v208, v120, v236\n"
"v_dot2c_f32_bf16 v210, v120, v120\n"
"s_waitcnt lgkmcnt(4)\n"
"v_dot2c_f32_bf16 v212, v124, v236\n"
"v_dot2c_f32_bf16 v214, v124, v124\n"
"v_dot2c_f32_bf16 v200, v113, v236\n"
"v_dot2c_f32_bf16 v202, v113, v113\n"
"v_dot2c_f32_bf16 v204, v117, v236\n"
"v_dot2c_f32_bf16 v206, v117, v117\n"
"v_dot2c_f32_bf16 v208, v121, v236\n"
"v_dot2c_f32_bf16 v210, v121, v121\n"
"v_dot2c_f32_bf16 v212, v125, v236\n"
"v_dot2c_f32_bf16 v214, v125, v125\n"
"v_dot2c_f32_bf16 v200, v114, v236\n"
"v_dot2c_f32_bf16 v202, v114, v114\n"
"v_dot2c_f32_bf16 v204, v118, v236\n"
"v_dot2c_f32_bf16 v206, v118, v118\n"
"v_dot2c_f32_bf16 v208, v122, v236\n"
"v_dot2c_f32_bf16 v210, v122, v122\n"
"v_dot2c_f32_bf16 v212, v126, v236\n"
"v_dot2c_f32_bf16 v214, v126, v126\n"
"v_dot2c_f32_bf16 v200, v115, v236\n"
"v_dot2c_f32_bf16 v202, v115, v115\n"
"v_dot2c_f32_bf16 v204, v119, v236\n"
"v_dot2c_f32_bf16 v206, v119, v119\n"
"v_dot2c_f32_bf16 v208, v123, v236\n"
"v_dot2c_f32_bf16 v210, v123, v123\n"
"v_dot2c_f32_bf16 v212, v127, v236\n"
"v_dot2c_f32_bf16 v214, v127, v127\n"
"s_waitcnt lgkmcnt(0)\n"
"s_barrier\n"
"s_add_u32 m0, s51, 98352\n"
"s_nop 0\n"
"buffer_load_dwordx4 v217, s[20:23], s50 offen offset:0 lds\n"
"s_add_u32 m0, s51, 106288\n"
"s_nop 0\n"
"buffer_load_dwordx4 v217, s[20:23], s50 offen offset:256 lds\n"
"s_add_u32 s50, s50, 32768\n"
"v_dot2c_f32_bf16 v200, v128, v236\n"
"v_dot2c_f32_bf16 v202, v128, v128\n"
"v_dot2c_f32_bf16 v204, v132, v236\n"
"v_dot2c_f32_bf16 v206, v132, v132\n"
"v_dot2c_f32_bf16 v208, v136, v236\n"
"v_dot2c_f32_bf16 v210, v136, v136\n"
"v_dot2c_f32_bf16 v212, v140, v236\n"
"v_dot2c_f32_bf16 v214, v140, v140\n"
"v_dot2c_f32_bf16 v200, v129, v236\n"
"v_dot2c_f32_bf16 v202, v129, v129\n"
"v_dot2c_f32_bf16 v204, v133, v236\n"
"v_dot2c_f32_bf16 v206, v133, v133\n"
"v_dot2c_f32_bf16 v208, v137, v236\n"
"v_dot2c_f32_bf16 v210, v137, v137\n"
"v_dot2c_f32_bf16 v212, v141, v236\n"
"v_dot2c_f32_bf16 v214, v141, v141\n"
"v_dot2c_f32_bf16 v200, v130, v236\n"
"v_dot2c_f32_bf16 v202, v130, v130\n"
"v_dot2c_f32_bf16 v204, v134, v236\n"
"v_dot2c_f32_bf16 v206, v134, v134\n"
"v_dot2c_f32_bf16 v208, v138, v236\n"
"v_dot2c_f32_bf16 v210, v138, v138\n"
"v_dot2c_f32_bf16 v212, v142, v236\n"
"v_dot2c_f32_bf16 v214, v142, v142\n"
"v_dot2c_f32_bf16 v200, v131, v236\n"
"v_dot2c_f32_bf16 v202, v131, v131\n"
"v_dot2c_f32_bf16 v204, v135, v236\n"
"v_dot2c_f32_bf16 v206, v135, v135\n"
"v_dot2c_f32_bf16 v208, v139, v236\n"
"v_dot2c_f32_bf16 v210, v139, v139\n"
"v_dot2c_f32_bf16 v212, v143, v236\n"
"v_dot2c_f32_bf16 v214, v143, v143\n"
"s_nop 2\n"
"v_add_f32 v200, v200, v208\n"
"v_add_f32 v202, v202, v210\n"
"v_add_f32 v204, v204, v212\n"
"v_add_f32 v206, v206, v214\n"
"v_mov_b32 v208, v200\n"
"v_mov_b32 v209, v202\n"
"v_mov_b32 v210, v204\n"
"v_mov_b32 v211, v206\n"
"s_nop 0\n"
"v_permlane32_swap_b32 v200, v208\n"
"v_permlane32_swap_b32 v202, v209\n"
"v_permlane32_swap_b32 v204, v210\n"
"v_permlane32_swap_b32 v206, v211\n"
"v_add_f32 v200, v200, v208\n"
"v_add_f32 v202, v202, v209\n"
"v_add_f32 v204, v204, v210\n"
"v_add_f32 v206, v206, v211\n"
"v_mov_b32 v208, v200\n"
"v_mov_b32 v209, v202\n"
"v_mov_b32 v210, v204\n"
"v_mov_b32 v211, v206\n"
"s_nop 0\n"
"v_permlane16_swap_b32 v200, v208\n"
"v_permlane16_swap_b32 v202, v209\n"
"v_permlane16_swap_b32 v204, v210\n"
"v_permlane16_swap_b32 v206, v211\n"
"v_add_f32 v200, v200, v208\n"
"v_add_f32 v202, v202, v209\n"
"v_add_f32 v204, v204, v210\n"
"v_add_f32 v206, v206, v211\n"
"v_mul_f32 v200, 0x3ae38e39, v200\n"
"v_mul_f32 v202, 0x3ae38e39, v202\n"
"v_fma_f32 v202, -v200, v200, v202\n"
"v_max_f32 v202, 0, v202\n"
"v_add_f32 v202, 0x3727c5ac, v202\n"
"v_mul_f32 v204, 0x3ae38e39, v204\n"
"v_mul_f32 v206, 0x3ae38e39, v206\n"
"v_fma_f32 v206, -v204, v204, v206\n"
"v_max_f32 v206, 0, v206\n"
"v_add_f32 v206, 0x3727c5ac, v206\n"
"v_rsq_f32 v201, v202\n"
"v_rsq_f32 v205, v206\n"
"s_nop 0\n"
"v_mul_f32 v202, v200, v201\n"
"v_sub_f32 v202, 0, v202\n"
"v_mov_b32 v200, v201\n"
"v_mul_f32 v206, v204, v205\n"
"v_sub_f32 v206, 0, v206\n"
"v_mov_b32 v204, v205\n"
"v_lshlrev_b32 v176, 16, v0\n"
"v_and_b32 v177, v237, v0\n"
"v_pk_fma_f32 v[176:177], v[176:177], v[200:201], v[202:203] op_sel_hi:[1,0,0]\n"
"v_cvt_pk_f16_f32 v0, v176, v177\n"
"v_lshlrev_b32 v178, 16, v1\n"
"v_and_b32 v179, v237, v1\n"
"v_pk_fma_f32 v[178:179], v[178:179], v[200:201], v[202:203] op_sel_hi:[1,0,0]\n"
"v_cvt_pk_f16_f32 v1, v178, v179\n"
"v_lshlrev_b32 v180, 16, v2\n"
"v_and_b32 v181, v237, v2\n"
"v_pk_fma_f32 v[180:181], v[180:181], v[200:201], v[202:203] op_sel_hi:[1,0,0]\n"
"v_cvt_pk_f16_f32 v2, v180, v181\n"
"v_lshlrev_b32 v182, 16, v3\n"
"v_and_b32 v183, v237, v3\n"
"v_pk_fma_f32 v[182:183], v[182:183], v[200:201], v[202:203] op_sel_hi:[1,0,0]\n"
"v_cvt_pk_f16_f32 v3, v182, v183\n"
"v_lshlrev_b32 v184, 16, v4\n"
"v_and_b32 v185, v237, v4\n"
"v_pk_fma_f32 v[184:185], v[184:185], v[204:205], v[206:207] op_sel_hi:[1,0,0]\n"
"v_cvt_pk_f16_f32 v4, v184, v185\n"
"v_lshlrev_b32 v186, 16, v5\n"
"s_add_u32 m0, s51, 99376\n"
"s_nop 0\n"
"buffer_load_dwordx4 v217, s[20:23], s50 offen offset:0 lds\n"
"v_and_b32 v187, v237, v5\n"
"v_pk_fma_f32 v[186:187], v[186:187], v[204:205], v[206:207] op_sel_hi:[1,0,0]\n"
"v_cvt_pk_f16_f32 v5, v186, v187\n"
"v_lshlrev_b32 v188, 16, v6\n"
"v_and_b32 v189, v237, v6\n"
"v_pk_fma_f32 v[188:189], v[188:189], v[204:205], v[206:207] op_sel_hi:[1,0,0]\n"
"v_cvt_pk_f16_f32 v6, v188, v189\n"
"v_lshlrev_b32 v190, 16, v7\n"
"v_and_b32 v191, v237, v7\n"
"v_pk_fma_f32 v[190:191], v[190:191], v[204:205], v[206:207] op_sel_hi:[1,0,0]\n"
"v_cvt_pk_f16_f32 v7, v190, v191\n"
"v_lshlrev_b32 v176, 16, v8\n"
"v_and_b32 v177, v237, v8\n"
"v_pk_fma_f32 v[176:177], v[176:177], v[200:201], v[202:203] op_sel_hi:[1,0,0]\n"
"v_cvt_pk_f16_f32 v8, v176, v177\n"
"v_lshlrev_b32 v178, 16, v9\n"
"v_and_b32 v179, v237, v9\n"
"v_pk_fma_f32 v[178:179], v[178:179], v[200:201], v[202:203] op_sel_hi:[1,0,0]\n"
"v_cvt_pk_f16_f32 v9, v178, v179\n"
"v_lshlrev_b32 v180, 16, v10\n"
"v_and_b32 v181, v237, v10\n"
"s_add_u32 m0, s51, 107312\n"
"s_nop 0\n"
"buffer_load_dwordx4 v217, s[20:23], s50 offen offset:256 lds\n"
"s_add_u32 s50, s50, 32768\n"
"v_pk_fma_f32 v[180:181], v[180:181], v[200:201], v[202:203] op_sel_hi:[1,0,0]\n"
"v_cvt_pk_f16_f32 v10, v180, v181\n"
"v_lshlrev_b32 v182, 16, v11\n"
"v_and_b32 v183, v237, v11\n"
"v_pk_fma_f32 v[182:183], v[182:183], v[200:201], v[202:203] op_sel_hi:[1,0,0]\n"
"v_cvt_pk_f16_f32 v11, v182, v183\n"
"v_lshlrev_b32 v184, 16, v12\n"
"v_and_b32 v185, v237, v12\n"
"v_pk_fma_f32 v[184:185], v[184:185], v[204:205], v[206:207] op_sel_hi:[1,0,0]\n"
"v_cvt_pk_f16_f32 v12, v184, v185\n"
"v_lshlrev_b32 v186, 16, v13\n"
"v_and_b32 v187, v237, v13\n"
"v_pk_fma_f32 v[186:187], v[186:187], v[204:205], v[206:207] op_sel_hi:[1,0,0]\n"
"v_cvt_pk_f16_f32 v13, v186, v187\n"
"v_lshlrev_b32 v188, 16, v14\n"
"v_and_b32 v189, v237, v14\n"
"v_pk_fma_f32 v[188:189], v[188:189], v[204:205], v[206:207] op_sel_hi:[1,0,0]\n"
"v_cvt_pk_f16_f32 v14, v188, v189\n"
"v_lshlrev_b32 v190, 16, v15\n"
"v_and_b32 v191, v237, v15\n"
"v_pk_fma_f32 v[190:191], v[190:191], v[204:205], v[206:207] op_sel_hi:[1,0,0]\n"
"s_add_u32 m0, s51, 114744\n"
"s_nop 0\n"
"buffer_load_dwordx4 v217, s[20:23], s50 offen offset:0 lds\n"
"v_cvt_pk_f16_f32 v15, v190, v191\n"
"v_lshlrev_b32 v176, 16, v16\n"
"v_and_b32 v177, v237, v16\n"
"v_pk_fma_f32 v[176:177], v[176:177], v[200:201], v[202:203] op_sel_hi:[1,0,0]\n"
"v_cvt_pk_f16_f32 v16, v176, v177\n"
"v_lshlrev_b32 v178, 16, v17\n"
"v_and_b32 v179, v237, v17\n"
"v_pk_fma_f32 v[178:179], v[178:179], v[200:201], v[202:203] op_sel_hi:[1,0,0]\n"
"v_cvt_pk_f16_f32 v17, v178, v179\n"
"v_lshlrev_b32 v180, 16, v18\n"
"v_and_b32 v181, v237, v18\n"
"v_pk_fma_f32 v[180:181], v[180:181], v[200:201], v[202:203] op_sel_hi:[1,0,0]\n"
"v_cvt_pk_f16_f32 v18, v180, v181\n"
"v_lshlrev_b32 v182, 16, v19\n"
"v_and_b32 v183, v237, v19\n"
"v_pk_fma_f32 v[182:183], v[182:183], v[200:201], v[202:203] op_sel_hi:[1,0,0]\n"
"v_cvt_pk_f16_f32 v19, v182, v183\n"
"v_lshlrev_b32 v184, 16, v20\n"
"v_and_b32 v185, v237, v20\n"
"v_pk_fma_f32 v[184:185], v[184:185], v[204:205], v[206:207] op_sel_hi:[1,0,0]\n"
"v_cvt_pk_f16_f32 v20, v184, v185\n"
"v_lshlrev_b32 v186, 16, v21\n"
"s_add_u32 m0, s51, 122680\n"
"s_nop 0\n"
"buffer_load_dwordx4 v217, s[20:23], s50 offen offset:256 lds\n"
"s_add_u32 s50, s50, 32768\n"
"v_and_b32 v187, v237, v21\n"
"v_pk_fma_f32 v[186:187], v[186:187], v[204:205], v[206:207] op_sel_hi:[1,0,0]\n"
"v_cvt_pk_f16_f32 v21, v186, v187\n"
"v_lshlrev_b32 v188, 16, v22\n"
"v_and_b32 v189, v237, v22\n"
"v_pk_fma_f32 v[188:189], v[188:189], v[204:205], v[206:207] op_sel_hi:[1,0,0]\n"
"v_cvt_pk_f16_f32 v22, v188, v189\n"
"v_lshlrev_b32 v190, 16, v23\n"
"v_and_b32 v191, v237, v23\n"
"v_pk_fma_f32 v[190:191], v[190:191], v[204:205], v[206:207] op_sel_hi:[1,0,0]\n"
"v_cvt_pk_f16_f32 v23, v190, v191\n"
"v_lshlrev_b32 v176, 16, v24\n"
"v_and_b32 v177, v237, v24\n"
"v_pk_fma_f32 v[176:177], v[176:177], v[200:201], v[202:203] op_sel_hi:[1,0,0]\n"
"v_cvt_pk_f16_f32 v24, v176, v177\n"
"v_lshlrev_b32 v178, 16, v25\n"
"v_and_b32 v179, v237, v25\n"
"v_pk_fma_f32 v[178:179], v[178:179], v[200:201], v[202:203] op_sel_hi:[1,0,0]\n"
"v_cvt_pk_f16_f32 v25, v178, v179\n"
"v_lshlrev_b32 v180, 16, v26\n"
"v_and_b32 v181, v237, v26\n"
"s_add_u32 m0, s51, 115768\n"
"s_nop 0\n"
"buffer_load_dwordx4 v217, s[20:23], s50 offen offset:0 lds\n"
"v_pk_fma_f32 v[180:181], v[180:181], v[200:201], v[202:203] op_sel_hi:[1,0,0]\n"
"v_cvt_pk_f16_f32 v26, v180, v181\n"
"v_lshlrev_b32 v182, 16, v27\n"
"v_and_b32 v183, v237, v27\n"
"v_pk_fma_f32 v[182:183], v[182:183], v[200:201], v[202:203] op_sel_hi:[1,0,0]\n"
"v_cvt_pk_f16_f32 v27, v182, v183\n"
"v_lshlrev_b32 v184, 16, v28\n"
"v_and_b32 v185, v237, v28\n"
"v_pk_fma_f32 v[184:185], v[184:185], v[204:205], v[206:207] op_sel_hi:[1,0,0]\n"
"v_cvt_pk_f16_f32 v28, v184, v185\n"
"v_lshlrev_b32 v186, 16, v29\n"
"v_and_b32 v187, v237, v29\n"
"v_pk_fma_f32 v[186:187], v[186:187], v[204:205], v[206:207] op_sel_hi:[1,0,0]\n"
"v_cvt_pk_f16_f32 v29, v186, v187\n"
"v_lshlrev_b32 v188, 16, v30\n"
"v_and_b32 v189, v237, v30\n"
"v_pk_fma_f32 v[188:189], v[188:189], v[204:205], v[206:207] op_sel_hi:[1,0,0]\n"
"v_cvt_pk_f16_f32 v30, v188, v189\n"
"v_lshlrev_b32 v190, 16, v31\n"
"v_and_b32 v191, v237, v31\n"
"v_pk_fma_f32 v[190:191], v[190:191], v[204:205], v[206:207] op_sel_hi:[1,0,0]\n"
"s_add_u32 m0, s51, 123704\n"
"s_nop 0\n"
"buffer_load_dwordx4 v217, s[20:23], s50 offen offset:256 lds\n"
"s_add_u32 s50, s50, 425984\n"
"v_cvt_pk_f16_f32 v31, v190, v191\n"
"buffer_load_dwordx4 v[144:147], v225, s[28:31], s61 offen offset:0 sc1\n"
"buffer_load_dwordx4 v[148:151], v225, s[28:31], s61 offen offset:16 sc1\n"
"buffer_load_dwordx4 v[152:155], v225, s[28:31], s62 offen offset:0 sc1\n"
"buffer_load_dwordx4 v[156:159], v225, s[28:31], s62 offen offset:16 sc1\n"
"v_lshlrev_b32 v176, 16, v32\n"
"v_and_b32 v177, v237, v32\n"
"v_pk_fma_f32 v[176:177], v[176:177], v[200:201], v[202:203] op_sel_hi:[1,0,0]\n"
"v_cvt_pk_f16_f32 v32, v176, v177\n"
"v_lshlrev_b32 v178, 16, v33\n"
"v_and_b32 v179, v237, v33\n"
"v_pk_fma_f32 v[178:179], v[178:179], v[200:201], v[202:203] op_sel_hi:[1,0,0]\n"
"v_cvt_pk_f16_f32 v33, v178, v179\n"
"v_lshlrev_b32 v180, 16, v34\n"
"v_and_b32 v181, v237, v34\n"
"v_pk_fma_f32 v[180:181], v[180:181], v[200:201], v[202:203] op_sel_hi:[1,0,0]\n"
"v_cvt_pk_f16_f32 v34, v180, v181\n"
"v_lshlrev_b32 v182, 16, v35\n"
"v_and_b32 v183, v237, v35\n"
"v_pk_fma_f32 v[182:183], v[182:183], v[200:201], v[202:203] op_sel_hi:[1,0,0]\n"
"v_cvt_pk_f16_f32 v35, v182, v183\n"
"v_lshlrev_b32 v184, 16, v36\n"
"v_and_b32 v185, v237, v36\n"
"v_pk_fma_f32 v[184:185], v[184:185], v[204:205], v[206:207] op_sel_hi:[1,0,0]\n"
"v_cvt_pk_f16_f32 v36, v184, v185\n"
"v_lshlrev_b32 v186, 16, v37\n"
"s_mov_b32 m0, s51\n"
"s_nop 0\n"
"buffer_load_dwordx4 v217, s[20:23], s50 offen offset:0 lds\n"
"v_and_b32 v187, v237, v37\n"
"v_pk_fma_f32 v[186:187], v[186:187], v[204:205], v[206:207] op_sel_hi:[1,0,0]\n"
"v_cvt_pk_f16_f32 v37, v186, v187\n"
"v_lshlrev_b32 v188, 16, v38\n"
"v_and_b32 v189, v237, v38\n"
"v_pk_fma_f32 v[188:189], v[188:189], v[204:205], v[206:207] op_sel_hi:[1,0,0]\n"
"v_cvt_pk_f16_f32 v38, v188, v189\n"
"v_lshlrev_b32 v190, 16, v39\n"
"v_and_b32 v191, v237, v39\n"
"v_pk_fma_f32 v[190:191], v[190:191], v[204:205], v[206:207] op_sel_hi:[1,0,0]\n"
"v_cvt_pk_f16_f32 v39, v190, v191\n"
"v_lshlrev_b32 v176, 16, v40\n"
"v_and_b32 v177, v237, v40\n"
"v_pk_fma_f32 v[176:177], v[176:177], v[200:201], v[202:203] op_sel_hi:[1,0,0]\n"
"v_cvt_pk_f16_f32 v40, v176, v177\n"
"v_lshlrev_b32 v178, 16, v41\n"
"v_and_b32 v179, v237, v41\n"
"v_pk_fma_f32 v[178:179], v[178:179], v[200:201], v[202:203] op_sel_hi:[1,0,0]\n"
"v_cvt_pk_f16_f32 v41, v178, v179\n"
"v_lshlrev_b32 v180, 16, v42\n"
"v_and_b32 v181, v237, v42\n"
"s_add_u32 m0, s51, 7936\n"
"s_nop 0\n"
"buffer_load_dwordx4 v217, s[20:23], s50 offen offset:256 lds\n"
"s_add_u32 s50, s50, 32768\n"
"v_pk_fma_f32 v[180:181], v[180:181], v[200:201], v[202:203] op_sel_hi:[1,0,0]\n"
"v_cvt_pk_f16_f32 v42, v180, v181\n"
"v_lshlrev_b32 v182, 16, v43\n"
"v_and_b32 v183, v237, v43\n"
"v_pk_fma_f32 v[182:183], v[182:183], v[200:201], v[202:203] op_sel_hi:[1,0,0]\n"
"v_cvt_pk_f16_f32 v43, v182, v183\n"
"v_lshlrev_b32 v184, 16, v44\n"
"v_and_b32 v185, v237, v44\n"
"v_pk_fma_f32 v[184:185], v[184:185], v[204:205], v[206:207] op_sel_hi:[1,0,0]\n"
"v_cvt_pk_f16_f32 v44, v184, v185\n"
"v_lshlrev_b32 v186, 16, v45\n"
"v_and_b32 v187, v237, v45\n"
"v_pk_fma_f32 v[186:187], v[186:187], v[204:205], v[206:207] op_sel_hi:[1,0,0]\n"
"v_cvt_pk_f16_f32 v45, v186, v187\n"
"v_lshlrev_b32 v188, 16, v46\n"
"v_and_b32 v189, v237, v46\n"
"v_pk_fma_f32 v[188:189], v[188:189], v[204:205], v[206:207] op_sel_hi:[1,0,0]\n"
"v_cvt_pk_f16_f32 v46, v188, v189\n"
"v_lshlrev_b32 v190, 16, v47\n"
"v_and_b32 v191, v237, v47\n"
"v_pk_fma_f32 v[190:191], v[190:191], v[204:205], v[206:207] op_sel_hi:[1,0,0]\n"
"s_add_u32 m0, s51, 1024\n"
"s_nop 0\n"
"buffer_load_dwordx4 v217, s[20:23], s50 offen offset:0 lds\n"
"v_cvt_pk_f16_f32 v47, v190, v191\n"
"v_lshlrev_b32 v176, 16, v48\n"
"v_and_b32 v177, v237, v48\n"
"v_pk_fma_f32 v[176:177], v[176:177], v[200:201], v[202:203] op_sel_hi:[1,0,0]\n"
"v_cvt_pk_f16_f32 v48, v176, v177\n"
"v_lshlrev_b32 v178, 16, v49\n"
"v_and_b32 v179, v237, v49\n"
"v_pk_fma_f32 v[178:179], v[178:179], v[200:201], v[202:203] op_sel_hi:[1,0,0]\n"
"v_cvt_pk_f16_f32 v49, v178, v179\n"
"v_lshlrev_b32 v180, 16, v50\n"
"v_and_b32 v181, v237, v50\n"
"v_pk_fma_f32 v[180:181], v[180:181], v[200:201], v[202:203] op_sel_hi:[1,0,0]\n"
"v_cvt_pk_f16_f32 v50, v180, v181\n"
"v_lshlrev_b32 v182, 16, v51\n"
"v_and_b32 v183, v237, v51\n"
"v_pk_fma_f32 v[182:183], v[182:183], v[200:201], v[202:203] op_sel_hi:[1,0,0]\n"
"v_cvt_pk_f16_f32 v51, v182, v183\n"
"v_lshlrev_b32 v184, 16, v52\n"
"v_and_b32 v185, v237, v52\n"
"v_pk_fma_f32 v[184:185], v[184:185], v[204:205], v[206:207] op_sel_hi:[1,0,0]\n"
"v_cvt_pk_f16_f32 v52, v184, v185\n"
"v_lshlrev_b32 v186, 16, v53\n"
"s_add_u32 m0, s51, 8960\n"
"s_nop 0\n"
"buffer_load_dwordx4 v217, s[20:23], s50 offen offset:256 lds\n"
"s_add_u32 s50, s50, 32768\n"
"v_and_b32 v187, v237, v53\n"
"v_pk_fma_f32 v[186:187], v[186:187], v[204:205], v[206:207] op_sel_hi:[1,0,0]\n"
"v_cvt_pk_f16_f32 v53, v186, v187\n"
"v_lshlrev_b32 v188, 16, v54\n"
"v_and_b32 v189, v237, v54\n"
"v_pk_fma_f32 v[188:189], v[188:189], v[204:205], v[206:207] op_sel_hi:[1,0,0]\n"
"v_cvt_pk_f16_f32 v54, v188, v189\n"
"v_lshlrev_b32 v190, 16, v55\n"
"v_and_b32 v191, v237, v55\n"
"v_pk_fma_f32 v[190:191], v[190:191], v[204:205], v[206:207] op_sel_hi:[1,0,0]\n"
"v_cvt_pk_f16_f32 v55, v190, v191\n"
"v_lshlrev_b32 v176, 16, v56\n"
"v_and_b32 v177, v237, v56\n"
"v_pk_fma_f32 v[176:177], v[176:177], v[200:201], v[202:203] op_sel_hi:[1,0,0]\n"
"v_cvt_pk_f16_f32 v56, v176, v177\n"
"v_lshlrev_b32 v178, 16, v57\n"
"v_and_b32 v179, v237, v57\n"
"v_pk_fma_f32 v[178:179], v[178:179], v[200:201], v[202:203] op_sel_hi:[1,0,0]\n"
"v_cvt_pk_f16_f32 v57, v178, v179\n"
"v_lshlrev_b32 v180, 16, v58\n"
"v_and_b32 v181, v237, v58\n"
"s_add_u32 m0, s51, 16392\n"
"s_nop 0\n"
"buffer_load_dwordx4 v217, s[20:23], s50 offen offset:0 lds\n"
"v_pk_fma_f32 v[180:181], v[180:181], v[200:201], v[202:203] op_sel_hi:[1,0,0]\n"
"v_cvt_pk_f16_f32 v58, v180, v181\n"
"v_lshlrev_b32 v182, 16, v59\n"
"v_and_b32 v183, v237, v59\n"
"v_pk_fma_f32 v[182:183], v[182:183], v[200:201], v[202:203] op_sel_hi:[1,0,0]\n"
"v_cvt_pk_f16_f32 v59, v182, v183\n"
"v_lshlrev_b32 v184, 16, v60\n"
"v_and_b32 v185, v237, v60\n"
"v_pk_fma_f32 v[184:185], v[184:185], v[204:205], v[206:207] op_sel_hi:[1,0,0]\n"
"v_cvt_pk_f16_f32 v60, v184, v185\n"
"v_lshlrev_b32 v186, 16, v61\n"
"v_and_b32 v187, v237, v61\n"
"v_pk_fma_f32 v[186:187], v[186:187], v[204:205], v[206:207] op_sel_hi:[1,0,0]\n"
"v_cvt_pk_f16_f32 v61, v186, v187\n"
"v_lshlrev_b32 v188, 16, v62\n"
"v_and_b32 v189, v237, v62\n"
"v_pk_fma_f32 v[188:189], v[188:189], v[204:205], v[206:207] op_sel_hi:[1,0,0]\n"
"v_cvt_pk_f16_f32 v62, v188, v189\n"
"v_lshlrev_b32 v190, 16, v63\n"
"v_and_b32 v191, v237, v63\n"
"v_pk_fma_f32 v[190:191], v[190:191], v[204:205], v[206:207] op_sel_hi:[1,0,0]\n"
"s_add_u32 m0, s51, 24328\n"
"s_nop 0\n"
"buffer_load_dwordx4 v217, s[20:23], s50 offen offset:256 lds\n"
"s_add_u32 s50, s50, 32768\n"
"v_cvt_pk_f16_f32 v63, v190, v191\n"
"v_lshlrev_b32 v176, 16, v64\n"
"v_and_b32 v177, v237, v64\n"
"v_pk_fma_f32 v[176:177], v[176:177], v[200:201], v[202:203] op_sel_hi:[1,0,0]\n"
"v_cvt_pk_f16_f32 v64, v176, v177\n"
"v_lshlrev_b32 v178, 16, v65\n"
"v_and_b32 v179, v237, v65\n"
"v_pk_fma_f32 v[178:179], v[178:179], v[200:201], v[202:203] op_sel_hi:[1,0,0]\n"
"v_cvt_pk_f16_f32 v65, v178, v179\n"
"v_lshlrev_b32 v180, 16, v66\n"
"v_and_b32 v181, v237, v66\n"
"v_pk_fma_f32 v[180:181], v[180:181], v[200:201], v[202:203] op_sel_hi:[1,0,0]\n"
"v_cvt_pk_f16_f32 v66, v180, v181\n"
"v_lshlrev_b32 v182, 16, v67\n"
"v_and_b32 v183, v237, v67\n"
"v_pk_fma_f32 v[182:183], v[182:183], v[200:201], v[202:203] op_sel_hi:[1,0,0]\n"
"v_cvt_pk_f16_f32 v67, v182, v183\n"
"v_lshlrev_b32 v184, 16, v68\n"
"v_and_b32 v185, v237, v68\n"
"v_pk_fma_f32 v[184:185], v[184:185], v[204:205], v[206:207] op_sel_hi:[1,0,0]\n"
"v_cvt_pk_f16_f32 v68, v184, v185\n"
"v_lshlrev_b32 v186, 16, v69\n"
"s_add_u32 m0, s51, 17416\n"
"s_nop 0\n"
"buffer_load_dwordx4 v217, s[20:23], s50 offen offset:0 lds\n"
"v_and_b32 v187, v237, v69\n"
"v_pk_fma_f32 v[186:187], v[186:187], v[204:205], v[206:207] op_sel_hi:[1,0,0]\n"
"v_cvt_pk_f16_f32 v69, v186, v187\n"
"v_lshlrev_b32 v188, 16, v70\n"
"v_and_b32 v189, v237, v70\n"
"v_pk_fma_f32 v[188:189], v[188:189], v[204:205], v[206:207] op_sel_hi:[1,0,0]\n"
"v_cvt_pk_f16_f32 v70, v188, v189\n"
"v_lshlrev_b32 v190, 16, v71\n"
"v_and_b32 v191, v237, v71\n"
"v_pk_fma_f32 v[190:191], v[190:191], v[204:205], v[206:207] op_sel_hi:[1,0,0]\n"
"v_cvt_pk_f16_f32 v71, v190, v191\n"
"v_lshlrev_b32 v176, 16, v72\n"
"v_and_b32 v177, v237, v72\n"
"v_pk_fma_f32 v[176:177], v[176:177], v[200:201], v[202:203] op_sel_hi:[1,0,0]\n"
"v_cvt_pk_f16_f32 v72, v176, v177\n"
"v_lshlrev_b32 v178, 16, v73\n"
"v_and_b32 v179, v237, v73\n"
"v_pk_fma_f32 v[178:179], v[178:179], v[200:201], v[202:203] op_sel_hi:[1,0,0]\n"
"v_cvt_pk_f16_f32 v73, v178, v179\n"
"v_lshlrev_b32 v180, 16, v74\n"
"v_and_b32 v181, v237, v74\n"
"s_add_u32 m0, s51, 25352\n"
"s_nop 0\n"
"buffer_load_dwordx4 v217, s[20:23], s50 offen offset:256 lds\n"
"s_add_u32 s50, s50, 425984\n"
"v_pk_fma_f32 v[180:181], v[180:181], v[200:201], v[202:203] op_sel_hi:[1,0,0]\n"
"v_cvt_pk_f16_f32 v74, v180, v181\n"
"v_lshlrev_b32 v182, 16, v75\n"
"v_and_b32 v183, v237, v75\n"
"v_pk_fma_f32 v[182:183], v[182:183], v[200:201], v[202:203] op_sel_hi:[1,0,0]\n"
"v_cvt_pk_f16_f32 v75, v182, v183\n"
"v_lshlrev_b32 v184, 16, v76\n"
"v_and_b32 v185, v237, v76\n"
"v_pk_fma_f32 v[184:185], v[184:185], v[204:205], v[206:207] op_sel_hi:[1,0,0]\n"
"v_cvt_pk_f16_f32 v76, v184, v185\n"
"v_lshlrev_b32 v186, 16, v77\n"
"v_and_b32 v187, v237, v77\n"
"v_pk_fma_f32 v[186:187], v[186:187], v[204:205], v[206:207] op_sel_hi:[1,0,0]\n"
"v_cvt_pk_f16_f32 v77, v186, v187\n"
"v_lshlrev_b32 v188, 16, v78\n"
"v_and_b32 v189, v237, v78\n"
"v_pk_fma_f32 v[188:189], v[188:189], v[204:205], v[206:207] op_sel_hi:[1,0,0]\n"
"v_cvt_pk_f16_f32 v78, v188, v189\n"
"v_lshlrev_b32 v190, 16, v79\n"
"v_and_b32 v191, v237, v79\n"
"v_pk_fma_f32 v[190:191], v[190:191], v[204:205], v[206:207] op_sel_hi:[1,0,0]\n"
"s_add_u32 m0, s51, 32784\n"
"s_nop 0\n"
"buffer_load_dwordx4 v217, s[20:23], s50 offen offset:0 lds\n"
"v_cvt_pk_f16_f32 v79, v190, v191\n"
"v_lshlrev_b32 v176, 16, v80\n"
"v_and_b32 v177, v237, v80\n"
"v_pk_fma_f32 v[176:177], v[176:177], v[200:201], v[202:203] op_sel_hi:[1,0,0]\n"
"v_cvt_pk_f16_f32 v80, v176, v177\n"
"v_lshlrev_b32 v178, 16, v81\n"
"v_and_b32 v179, v237, v81\n"
"v_pk_fma_f32 v[178:179], v[178:179], v[200:201], v[202:203] op_sel_hi:[1,0,0]\n"
"v_cvt_pk_f16_f32 v81, v178, v179\n"
"v_lshlrev_b32 v180, 16, v82\n"
"v_and_b32 v181, v237, v82\n"
"v_pk_fma_f32 v[180:181], v[180:181], v[200:201], v[202:203] op_sel_hi:[1,0,0]\n"
"v_cvt_pk_f16_f32 v82, v180, v181\n"
"v_lshlrev_b32 v182, 16, v83\n"
"v_and_b32 v183, v237, v83\n"
"v_pk_fma_f32 v[182:183], v[182:183], v[200:201], v[202:203] op_sel_hi:[1,0,0]\n"
"v_cvt_pk_f16_f32 v83, v182, v183\n"
"v_lshlrev_b32 v184, 16, v84\n"
"v_and_b32 v185, v237, v84\n"
"v_pk_fma_f32 v[184:185], v[184:185], v[204:205], v[206:207] op_sel_hi:[1,0,0]\n"
"v_cvt_pk_f16_f32 v84, v184, v185\n"
"v_lshlrev_b32 v186, 16, v85\n"
"s_add_u32 m0, s51, 40720\n"
"s_nop 0\n"
"buffer_load_dwordx4 v217, s[20:23], s50 offen offset:256 lds\n"
"s_add_u32 s50, s50, 32768\n"
"v_and_b32 v187, v237, v85\n"
"v_pk_fma_f32 v[186:187], v[186:187], v[204:205], v[206:207] op_sel_hi:[1,0,0]\n"
"v_cvt_pk_f16_f32 v85, v186, v187\n"
"v_lshlrev_b32 v188, 16, v86\n"
"v_and_b32 v189, v237, v86\n"
"v_pk_fma_f32 v[188:189], v[188:189], v[204:205], v[206:207] op_sel_hi:[1,0,0]\n"
"v_cvt_pk_f16_f32 v86, v188, v189\n"
"v_lshlrev_b32 v190, 16, v87\n"
"v_and_b32 v191, v237, v87\n"
"v_pk_fma_f32 v[190:191], v[190:191], v[204:205], v[206:207] op_sel_hi:[1,0,0]\n"
"v_cvt_pk_f16_f32 v87, v190, v191\n"
"v_lshlrev_b32 v176, 16, v88\n"
"v_and_b32 v177, v237, v88\n"
"v_pk_fma_f32 v[176:177], v[176:177], v[200:201], v[202:203] op_sel_hi:[1,0,0]\n"
"v_cvt_pk_f16_f32 v88, v176, v177\n"
"v_lshlrev_b32 v178, 16, v89\n"
"v_and_b32 v179, v237, v89\n"
"v_pk_fma_f32 v[178:179], v[178:179], v[200:201], v[202:203] op_sel_hi:[1,0,0]\n"
"v_cvt_pk_f16_f32 v89, v178, v179\n"
"v_lshlrev_b32 v180, 16, v90\n"
"v_and_b32 v181, v237, v90\n"
"s_add_u32 m0, s51, 33808\n"
"s_nop 0\n"
"buffer_load_dwordx4 v217, s[20:23], s50 offen offset:0 lds\n"
"v_pk_fma_f32 v[180:181], v[180:181], v[200:201], v[202:203] op_sel_hi:[1,0,0]\n"
"v_cvt_pk_f16_f32 v90, v180, v181\n"
"v_lshlrev_b32 v182, 16, v91\n"
"v_and_b32 v183, v237, v91\n"
"v_pk_fma_f32 v[182:183], v[182:183], v[200:201], v[202:203] op_sel_hi:[1,0,0]\n"
"v_cvt_pk_f16_f32 v91, v182, v183\n"
"v_lshlrev_b32 v184, 16, v92\n"
"v_and_b32 v185, v237, v92\n"
"v_pk_fma_f32 v[184:185], v[184:185], v[204:205], v[206:207] op_sel_hi:[1,0,0]\n"
"v_cvt_pk_f16_f32 v92, v184, v185\n"
"v_lshlrev_b32 v186, 16, v93\n"
"v_and_b32 v187, v237, v93\n"
"v_pk_fma_f32 v[186:187], v[186:187], v[204:205], v[206:207] op_sel_hi:[1,0,0]\n"
"v_cvt_pk_f16_f32 v93, v186, v187\n"
"v_lshlrev_b32 v188, 16, v94\n"
"v_and_b32 v189, v237, v94\n"
"v_pk_fma_f32 v[188:189], v[188:189], v[204:205], v[206:207] op_sel_hi:[1,0,0]\n"
"v_cvt_pk_f16_f32 v94, v188, v189\n"
"v_lshlrev_b32 v190, 16, v95\n"
"v_and_b32 v191, v237, v95\n"
"v_pk_fma_f32 v[190:191], v[190:191], v[204:205], v[206:207] op_sel_hi:[1,0,0]\n"
"s_add_u32 m0, s51, 41744\n"
"s_nop 0\n"
"buffer_load_dwordx4 v217, s[20:23], s50 offen offset:256 lds\n"
"s_add_u32 s50, s50, 32768\n"
"v_cvt_pk_f16_f32 v95, v190, v191\n"
"v_lshlrev_b32 v176, 16, v96\n"
"v_and_b32 v177, v237, v96\n"
"v_pk_fma_f32 v[176:177], v[176:177], v[200:201], v[202:203] op_sel_hi:[1,0,0]\n"
"v_cvt_pk_f16_f32 v96, v176, v177\n"
"v_lshlrev_b32 v178, 16, v97\n"
"v_and_b32 v179, v237, v97\n"
"v_pk_fma_f32 v[178:179], v[178:179], v[200:201], v[202:203] op_sel_hi:[1,0,0]\n"
"v_cvt_pk_f16_f32 v97, v178, v179\n"
"v_lshlrev_b32 v180, 16, v98\n"
"v_and_b32 v181, v237, v98\n"
"v_pk_fma_f32 v[180:181], v[180:181], v[200:201], v[202:203] op_sel_hi:[1,0,0]\n"
"v_cvt_pk_f16_f32 v98, v180, v181\n"
"v_lshlrev_b32 v182, 16, v99\n"
"v_and_b32 v183, v237, v99\n"
"v_pk_fma_f32 v[182:183], v[182:183], v[200:201], v[202:203] op_sel_hi:[1,0,0]\n"
"v_cvt_pk_f16_f32 v99, v182, v183\n"
"v_lshlrev_b32 v184, 16, v100\n"
"v_and_b32 v185, v237, v100\n"
"v_pk_fma_f32 v[184:185], v[184:185], v[204:205], v[206:207] op_sel_hi:[1,0,0]\n"
"v_cvt_pk_f16_f32 v100, v184, v185\n"
"v_lshlrev_b32 v186, 16, v101\n"
"s_add_u32 m0, s51, 49176\n"
"s_nop 0\n"
"buffer_load_dwordx4 v217, s[20:23], s50 offen offset:0 lds\n"
"v_and_b32 v187, v237, v101\n"
"v_pk_fma_f32 v[186:187], v[186:187], v[204:205], v[206:207] op_sel_hi:[1,0,0]\n"
"v_cvt_pk_f16_f32 v101, v186, v187\n"
"v_lshlrev_b32 v188, 16, v102\n"
"v_and_b32 v189, v237, v102\n"
"v_pk_fma_f32 v[188:189], v[188:189], v[204:205], v[206:207] op_sel_hi:[1,0,0]\n"
"v_cvt_pk_f16_f32 v102, v188, v189\n"
"v_lshlrev_b32 v190, 16, v103\n"
"v_and_b32 v191, v237, v103\n"
"v_pk_fma_f32 v[190:191], v[190:191], v[204:205], v[206:207] op_sel_hi:[1,0,0]\n"
"v_cvt_pk_f16_f32 v103, v190, v191\n"
"v_lshlrev_b32 v176, 16, v104\n"
"v_and_b32 v177, v237, v104\n"
"v_pk_fma_f32 v[176:177], v[176:177], v[200:201], v[202:203] op_sel_hi:[1,0,0]\n"
"v_cvt_pk_f16_f32 v104, v176, v177\n"
"v_lshlrev_b32 v178, 16, v105\n"
"v_and_b32 v179, v237, v105\n"
"v_pk_fma_f32 v[178:179], v[178:179], v[200:201], v[202:203] op_sel_hi:[1,0,0]\n"
"v_cvt_pk_f16_f32 v105, v178, v179\n"
"v_lshlrev_b32 v180, 16, v106\n"
"v_and_b32 v181, v237, v106\n"
"s_add_u32 m0, s51, 57112\n"
"s_nop 0\n"
"buffer_load_dwordx4 v217, s[20:23], s50 offen offset:256 lds\n"
"s_add_u32 s50, s50, 32768\n"
"v_pk_fma_f32 v[180:181], v[180:181], v[200:201], v[202:203] op_sel_hi:[1,0,0]\n"
"v_cvt_pk_f16_f32 v106, v180, v181\n"
"v_lshlrev_b32 v182, 16, v107\n"
"v_and_b32 v183, v237, v107\n"
"v_pk_fma_f32 v[182:183], v[182:183], v[200:201], v[202:203] op_sel_hi:[1,0,0]\n"
"v_cvt_pk_f16_f32 v107, v182, v183\n"
"v_lshlrev_b32 v184, 16, v108\n"
"v_and_b32 v185, v237, v108\n"
"v_pk_fma_f32 v[184:185], v[184:185], v[204:205], v[206:207] op_sel_hi:[1,0,0]\n"
"v_cvt_pk_f16_f32 v108, v184, v185\n"
"v_lshlrev_b32 v186, 16, v109\n"
"v_and_b32 v187, v237, v109\n"
"v_pk_fma_f32 v[186:187], v[186:187], v[204:205], v[206:207] op_sel_hi:[1,0,0]\n"
"v_cvt_pk_f16_f32 v109, v186, v187\n"
"v_lshlrev_b32 v188, 16, v110\n"
"v_and_b32 v189, v237, v110\n"
"v_pk_fma_f32 v[188:189], v[188:189], v[204:205], v[206:207] op_sel_hi:[1,0,0]\n"
"v_cvt_pk_f16_f32 v110, v188, v189\n"
"v_lshlrev_b32 v190, 16, v111\n"
"v_and_b32 v191, v237, v111\n"
"v_pk_fma_f32 v[190:191], v[190:191], v[204:205], v[206:207] op_sel_hi:[1,0,0]\n"
"s_add_u32 m0, s51, 50200\n"
"s_nop 0\n"
"buffer_load_dwordx4 v217, s[20:23], s50 offen offset:0 lds\n"
"v_cvt_pk_f16_f32 v111, v190, v191\n"
"v_lshlrev_b32 v176, 16, v112\n"
"v_and_b32 v177, v237, v112\n"
"v_pk_fma_f32 v[176:177], v[176:177], v[200:201], v[202:203] op_sel_hi:[1,0,0]\n"
"v_cvt_pk_f16_f32 v112, v176, v177\n"
"v_lshlrev_b32 v178, 16, v113\n"
"v_and_b32 v179, v237, v113\n"
"v_pk_fma_f32 v[178:179], v[178:179], v[200:201], v[202:203] op_sel_hi:[1,0,0]\n"
"v_cvt_pk_f16_f32 v113, v178, v179\n"
"v_lshlrev_b32 v180, 16, v114\n"
"v_and_b32 v181, v237, v114\n"
"v_pk_fma_f32 v[180:181], v[180:181], v[200:201], v[202:203] op_sel_hi:[1,0,0]\n"
"v_cvt_pk_f16_f32 v114, v180, v181\n"
"v_lshlrev_b32 v182, 16, v115\n"
"v_and_b32 v183, v237, v115\n"
"v_pk_fma_f32 v[182:183], v[182:183], v[200:201], v[202:203] op_sel_hi:[1,0,0]\n"
"v_cvt_pk_f16_f32 v115, v182, v183\n"
"v_lshlrev_b32 v184, 16, v116\n"
"v_and_b32 v185, v237, v116\n"
"v_pk_fma_f32 v[184:185], v[184:185], v[204:205], v[206:207] op_sel_hi:[1,0,0]\n"
"v_cvt_pk_f16_f32 v116, v184, v185\n"
"v_lshlrev_b32 v186, 16, v117\n"
"s_add_u32 m0, s51, 58136\n"
"s_nop 0\n"
"buffer_load_dwordx4 v217, s[20:23], s50 offen offset:256 lds\n"
"s_add_u32 s50, s50, 425984\n"
"v_and_b32 v187, v237, v117\n"
"v_pk_fma_f32 v[186:187], v[186:187], v[204:205], v[206:207] op_sel_hi:[1,0,0]\n"
"v_cvt_pk_f16_f32 v117, v186, v187\n"
"v_lshlrev_b32 v188, 16, v118\n"
"v_and_b32 v189, v237, v118\n"
"v_pk_fma_f32 v[188:189], v[188:189], v[204:205], v[206:207] op_sel_hi:[1,0,0]\n"
"v_cvt_pk_f16_f32 v118, v188, v189\n"
"v_lshlrev_b32 v190, 16, v119\n"
"v_and_b32 v191, v237, v119\n"
"v_pk_fma_f32 v[190:191], v[190:191], v[204:205], v[206:207] op_sel_hi:[1,0,0]\n"
"v_cvt_pk_f16_f32 v119, v190, v191\n"
"v_lshlrev_b32 v176, 16, v120\n"
"v_and_b32 v177, v237, v120\n"
"v_pk_fma_f32 v[176:177], v[176:177], v[200:201], v[202:203] op_sel_hi:[1,0,0]\n"
"v_cvt_pk_f16_f32 v120, v176, v177\n"
"v_lshlrev_b32 v178, 16, v121\n"
"v_and_b32 v179, v237, v121\n"
"v_pk_fma_f32 v[178:179], v[178:179], v[200:201], v[202:203] op_sel_hi:[1,0,0]\n"
"v_cvt_pk_f16_f32 v121, v178, v179\n"
"v_lshlrev_b32 v180, 16, v122\n"
"v_and_b32 v181, v237, v122\n"
"v_pk_fma_f32 v[180:181], v[180:181], v[200:201], v[202:203] op_sel_hi:[1,0,0]\n"
"v_cvt_pk_f16_f32 v122, v180, v181\n"
"v_lshlrev_b32 v182, 16, v123\n"
"v_and_b32 v183, v237, v123\n"
"v_pk_fma_f32 v[182:183], v[182:183], v[200:201], v[202:203] op_sel_hi:[1,0,0]\n"
"v_cvt_pk_f16_f32 v123, v182, v183\n"
"v_lshlrev_b32 v184, 16, v124\n"
"v_and_b32 v185, v237, v124\n"
"v_pk_fma_f32 v[184:185], v[184:185], v[204:205], v[206:207] op_sel_hi:[1,0,0]\n"
"v_cvt_pk_f16_f32 v124, v184, v185\n"
"v_lshlrev_b32 v186, 16, v125\n"
"v_and_b32 v187, v237, v125\n"
"v_pk_fma_f32 v[186:187], v[186:187], v[204:205], v[206:207] op_sel_hi:[1,0,0]\n"
"v_cvt_pk_f16_f32 v125, v186, v187\n"
"v_lshlrev_b32 v188, 16, v126\n"
"v_and_b32 v189, v237, v126\n"
"v_pk_fma_f32 v[188:189], v[188:189], v[204:205], v[206:207] op_sel_hi:[1,0,0]\n"
"v_cvt_pk_f16_f32 v126, v188, v189\n"
"v_lshlrev_b32 v190, 16, v127\n"
"v_and_b32 v191, v237, v127\n"
"v_pk_fma_f32 v[190:191], v[190:191], v[204:205], v[206:207] op_sel_hi:[1,0,0]\n"
"v_cvt_pk_f16_f32 v127, v190, v191\n"
"v_lshlrev_b32 v176, 16, v128\n"
"v_and_b32 v177, v237, v128\n"
"v_pk_fma_f32 v[176:177], v[176:177], v[200:201], v[202:203] op_sel_hi:[1,0,0]\n"
"v_cvt_pk_f16_f32 v128, v176, v177\n"
"v_lshlrev_b32 v178, 16, v129\n"
"v_and_b32 v179, v237, v129\n"
"v_pk_fma_f32 v[178:179], v[178:179], v[200:201], v[202:203] op_sel_hi:[1,0,0]\n"
"v_cvt_pk_f16_f32 v129, v178, v179\n"
"v_lshlrev_b32 v180, 16, v130\n"
"v_and_b32 v181, v237, v130\n"
"v_pk_fma_f32 v[180:181], v[180:181], v[200:201], v[202:203] op_sel_hi:[1,0,0]\n"
"v_cvt_pk_f16_f32 v130, v180, v181\n"
"v_lshlrev_b32 v182, 16, v131\n"
"v_and_b32 v183, v237, v131\n"
"v_pk_fma_f32 v[182:183], v[182:183], v[200:201], v[202:203] op_sel_hi:[1,0,0]\n"
"v_cvt_pk_f16_f32 v131, v182, v183\n"
"v_lshlrev_b32 v184, 16, v132\n"
"v_and_b32 v185, v237, v132\n"
"v_pk_fma_f32 v[184:185], v[184:185], v[204:205], v[206:207] op_sel_hi:[1,0,0]\n"
"v_cvt_pk_f16_f32 v132, v184, v185\n"
"v_lshlrev_b32 v186, 16, v133\n"
"v_and_b32 v187, v237, v133\n"
"v_pk_fma_f32 v[186:187], v[186:187], v[204:205], v[206:207] op_sel_hi:[1,0,0]\n"
"v_cvt_pk_f16_f32 v133, v186, v187\n"
"v_lshlrev_b32 v188, 16, v134\n"
"v_and_b32 v189, v237, v134\n"
"v_pk_fma_f32 v[188:189], v[188:189], v[204:205], v[206:207] op_sel_hi:[1,0,0]\n"
"v_cvt_pk_f16_f32 v134, v188, v189\n"
"v_lshlrev_b32 v190, 16, v135\n"
"v_and_b32 v191, v237, v135\n"
"v_pk_fma_f32 v[190:191], v[190:191], v[204:205], v[206:207] op_sel_hi:[1,0,0]\n"
"v_cvt_pk_f16_f32 v135, v190, v191\n"
"v_lshlrev_b32 v176, 16, v136\n"
"v_and_b32 v177, v237, v136\n"
"v_pk_fma_f32 v[176:177], v[176:177], v[200:201], v[202:203] op_sel_hi:[1,0,0]\n"
"v_cvt_pk_f16_f32 v136, v176, v177\n"
"v_lshlrev_b32 v178, 16, v137\n"
"v_and_b32 v179, v237, v137\n"
"v_pk_fma_f32 v[178:179], v[178:179], v[200:201], v[202:203] op_sel_hi:[1,0,0]\n"
"v_cvt_pk_f16_f32 v137, v178, v179\n"
"v_lshlrev_b32 v180, 16, v138\n"
"v_and_b32 v181, v237, v138\n"
"v_pk_fma_f32 v[180:181], v[180:181], v[200:201], v[202:203] op_sel_hi:[1,0,0]\n"
"v_cvt_pk_f16_f32 v138, v180, v181\n"
"v_lshlrev_b32 v182, 16, v139\n"
"v_and_b32 v183, v237, v139\n"
"v_pk_fma_f32 v[182:183], v[182:183], v[200:201], v[202:203] op_sel_hi:[1,0,0]\n"
"v_cvt_pk_f16_f32 v139, v182, v183\n"
"v_lshlrev_b32 v184, 16, v140\n"
"v_and_b32 v185, v237, v140\n"
"v_pk_fma_f32 v[184:185], v[184:185], v[204:205], v[206:207] op_sel_hi:[1,0,0]\n"
"v_cvt_pk_f16_f32 v140, v184, v185\n"
"v_lshlrev_b32 v186, 16, v141\n"
"v_and_b32 v187, v237, v141\n"
"v_pk_fma_f32 v[186:187], v[186:187], v[204:205], v[206:207] op_sel_hi:[1,0,0]\n"
"v_cvt_pk_f16_f32 v141, v186, v187\n"
"v_lshlrev_b32 v188, 16, v142\n"
"v_and_b32 v189, v237, v142\n"
"v_pk_fma_f32 v[188:189], v[188:189], v[204:205], v[206:207] op_sel_hi:[1,0,0]\n"
"v_cvt_pk_f16_f32 v142, v188, v189\n"
"v_lshlrev_b32 v190, 16, v143\n"
"v_and_b32 v191, v237, v143\n"
"v_pk_fma_f32 v[190:191], v[190:191], v[204:205], v[206:207] op_sel_hi:[1,0,0]\n"
"v_cvt_pk_f16_f32 v143, v190, v191\n"
"s_mov_b32 s87, 0x80000\n"
"s_waitcnt vmcnt(16)\n"
"AT_gchk:\n"
"v_xor_b32 v176, s85, v145\n"
"v_xor_b32 v177, s85, v147\n"
"v_xor_b32 v178, s85, v149\n"
"v_xor_b32 v179, s85, v151\n"
"v_xor_b32 v180, s85, v153\n"
"v_xor_b32 v181, s85, v155\n"
"v_xor_b32 v182, s85, v157\n"
"v_xor_b32 v183, s85, v159\n"
"v_or3_b32 v176, v176, v177, v178\n"
"v_or3_b32 v179, v179, v180, v181\n"
"v_or3_b32 v176, v176, v182, v183\n"
"v_or_b32 v176, v176, v179\n"
"v_cmp_ne_u32 s[56:57], 0, v176\n"
"v_cmp_gt_u32 vcc, 144, v234\n"
"s_nop 1\n"
"s_and_b64 vcc, vcc, s[56:57]\n"
"s_cbranch_vccz AT_gok\n"
"s_sub_u32 s87, s87, 1\n"
"s_cmp_eq_u32 s87, 0\n"
"s_cbranch_scc1 AT_gok\n"
"s_sleep 2\n"
"buffer_load_dwordx4 v[144:147], v225, s[28:31], s61 offen offset:0 sc1\n"
"buffer_load_dwordx4 v[148:151], v225, s[28:31], s61 offen offset:16 sc1\n"
"buffer_load_dwordx4 v[152:155], v225, s[28:31], s62 offen offset:0 sc1\n"
"buffer_load_dwordx4 v[156:159], v225, s[28:31], s62 offen offset:16 sc1\n"
"s_waitcnt vmcnt(0)\n"
"s_branch AT_gchk\n"
"AT_gok:\n"
"v_mov_b32 v236, 0x3e0293ee\n"
"v_mul_f32 v176, v236, v144\n"
"v_fma_f32 v180, v152, v236, v236\n"
"v_mul_f32 v177, v236, v146\n"
"v_fma_f32 v181, v154, v236, v236\n"
"v_mul_f32 v178, v236, v148\n"
"v_fma_f32 v182, v156, v236, v236\n"
"v_mul_f32 v179, v236, v150\n"
"v_fma_f32 v183, v158, v236, v236\n"
"ds_write_b128 v216, v[180:183]\n"
"ds_write_b128 v216, v[176:179] offset:128\n"
"s_waitcnt vmcnt(20) lgkmcnt(0)\n"
"s_barrier\n"
"ds_read_b128 v[144:147], v224 offset:0\n"
"ds_read_b128 v[148:151], v224 offset:64\n"
"ds_read_b128 v[152:155], v224 offset:128\n"
"ds_read_b128 v[156:159], v224 offset:192\n"
"ds_read_b128 v[160:163], v224 offset:256\n"
"ds_read_b128 v[164:167], v224 offset:320\n"
"ds_read_b128 v[168:171], v224 offset:384\n"
"ds_read_b128 v[172:175], v224 offset:448\n"
"ds_read_b64_tr_b16 a[160:161], v219 offset:32784\n"
"s_waitcnt lgkmcnt(6)\n"
"v_fma_mix_f32 v176, v0, v144, v152 op_sel:[0,0,0] op_sel_hi:[1,0,0]\n"
"v_fma_mix_f32 v177, v0, v145, v153 op_sel:[1,0,0] op_sel_hi:[1,0,0]\n"
"v_fma_mix_f32 v178, v1, v146, v154 op_sel:[0,0,0] op_sel_hi:[1,0,0]\n"
"v_fma_mix_f32 v179, v1, v147, v155 op_sel:[1,0,0] op_sel_hi:[1,0,0]\n"
"ds_read_b64_tr_b16 a[162:163], v219 offset:33808\n"
"s_waitcnt lgkmcnt(6)\n"
"v_fma_mix_f32 v180, v2, v148, v156 op_sel:[0,0,0] op_sel_hi:[1,0,0]\n"
"v_fma_mix_f32 v181, v2, v149, v157 op_sel:[1,0,0] op_sel_hi:[1,0,0]\n"
"v_fma_mix_f32 v182, v3, v150, v158 op_sel:[0,0,0] op_sel_hi:[1,0,0]\n"
"v_fma_mix_f32 v183, v3, v151, v159 op_sel:[1,0,0] op_sel_hi:[1,0,0]\n"
"ds_read_b64_tr_b16 a[164:165], v219 offset:36880\n"
"v_cvt_pk_bf16_f32 v0, v176, v177\n"
"v_cvt_pk_bf16_f32 v1, v178, v179\n"
"v_cvt_pk_bf16_f32 v2, v180, v181\n"
"v_cvt_pk_bf16_f32 v3, v182, v183\n"
"ds_read_b64_tr_b16 a[166:167], v219 offset:37904\n"
"v_fma_mix_f32 v184, v4, v144, v152 op_sel:[0,0,0] op_sel_hi:[1,0,0]\n"
"v_fma_mix_f32 v185, v4, v145, v153 op_sel:[1,0,0] op_sel_hi:[1,0,0]\n"
"v_fma_mix_f32 v186, v5, v146, v154 op_sel:[0,0,0] op_sel_hi:[1,0,0]\n"
"v_fma_mix_f32 v187, v5, v147, v155 op_sel:[1,0,0] op_sel_hi:[1,0,0]\n"
"ds_read_b64_tr_b16 a[168:169], v219 offset:32792\n"
"v_fma_mix_f32 v188, v6, v148, v156 op_sel:[0,0,0] op_sel_hi:[1,0,0]\n"
"v_fma_mix_f32 v189, v6, v149, v157 op_sel:[1,0,0] op_sel_hi:[1,0,0]\n"
"v_fma_mix_f32 v190, v7, v150, v158 op_sel:[0,0,0] op_sel_hi:[1,0,0]\n"
"v_fma_mix_f32 v191, v7, v151, v159 op_sel:[1,0,0] op_sel_hi:[1,0,0]\n"
"ds_read_b64_tr_b16 a[170:171], v219 offset:33816\n"
"ds_read_b128 v[144:147], v224 offset:512\n"
"ds_read_b128 v[148:151], v224 offset:576\n"
"ds_read_b128 v[152:155], v224 offset:640\n"
"ds_read_b128 v[156:159], v224 offset:704\n"
"ds_read_b64_tr_b16 a[172:173], v219 offset:36888\n"
"v_cvt_pk_bf16_f32 v4, v184, v185\n"
"v_cvt_pk_bf16_f32 v5, v186, v187\n"
"v_cvt_pk_bf16_f32 v6, v188, v189\n"
"v_cvt_pk_bf16_f32 v7, v190, v191\n"
"ds_read_b64_tr_b16 a[174:175], v219 offset:37912\n"
"s_waitcnt lgkmcnt(13)\n"
"v_fma_mix_f32 v176, v8, v160, v168 op_sel:[0,0,0] op_sel_hi:[1,0,0]\n"
"v_fma_mix_f32 v177, v8, v161, v169 op_sel:[1,0,0] op_sel_hi:[1,0,0]\n"
"v_fma_mix_f32 v178, v9, v162, v170 op_sel:[0,0,0] op_sel_hi:[1,0,0]\n"
"v_fma_mix_f32 v179, v9, v163, v171 op_sel:[1,0,0] op_sel_hi:[1,0,0]\n"
"ds_read_b64_tr_b16 a[176:177], v219 offset:33040\n"
"s_waitcnt lgkmcnt(13)\n"
"v_fma_mix_f32 v180, v10, v164, v172 op_sel:[0,0,0] op_sel_hi:[1,0,0]\n"
"v_fma_mix_f32 v181, v10, v165, v173 op_sel:[1,0,0] op_sel_hi:[1,0,0]\n"
"v_fma_mix_f32 v182, v11, v166, v174 op_sel:[0,0,0] op_sel_hi:[1,0,0]\n"
"v_fma_mix_f32 v183, v11, v167, v175 op_sel:[1,0,0] op_sel_hi:[1,0,0]\n"
"ds_read_b64_tr_b16 a[178:179], v219 offset:34064\n"
"v_cvt_pk_bf16_f32 v8, v176, v177\n"
"v_cvt_pk_bf16_f32 v9, v178, v179\n"
"v_cvt_pk_bf16_f32 v10, v180, v181\n"
"v_cvt_pk_bf16_f32 v11, v182, v183\n"
"ds_read_b64_tr_b16 a[180:181], v219 offset:37136\n"
"v_fma_mix_f32 v184, v12, v160, v168 op_sel:[0,0,0] op_sel_hi:[1,0,0]\n"
"v_fma_mix_f32 v185, v12, v161, v169 op_sel:[1,0,0] op_sel_hi:[1,0,0]\n"
"v_fma_mix_f32 v186, v13, v162, v170 op_sel:[0,0,0] op_sel_hi:[1,0,0]\n"
"v_fma_mix_f32 v187, v13, v163, v171 op_sel:[1,0,0] op_sel_hi:[1,0,0]\n"
"ds_read_b64_tr_b16 a[182:183], v219 offset:38160\n"
"v_fma_mix_f32 v188, v14, v164, v172 op_sel:[0,0,0] op_sel_hi:[1,0,0]\n"
"v_fma_mix_f32 v189, v14, v165, v173 op_sel:[1,0,0] op_sel_hi:[1,0,0]\n"
"v_fma_mix_f32 v190, v15, v166, v174 op_sel:[0,0,0] op_sel_hi:[1,0,0]\n"
"v_fma_mix_f32 v191, v15, v167, v175 op_sel:[1,0,0] op_sel_hi:[1,0,0]\n"
"ds_read_b64_tr_b16 a[184:185], v219 offset:33048\n"
"ds_read_b128 v[160:163], v224 offset:768\n"
"ds_read_b128 v[164:167], v224 offset:832\n"
"ds_read_b128 v[168:171], v224 offset:896\n"
"ds_read_b128 v[172:175], v224 offset:960\n"
"ds_read_b64_tr_b16 a[186:187], v219 offset:34072\n"
"v_cvt_pk_bf16_f32 v12, v184, v185\n"
"v_cvt_pk_bf16_f32 v13, v186, v187\n"
"v_cvt_pk_bf16_f32 v14, v188, v189\n"
"v_cvt_pk_bf16_f32 v15, v190, v191\n"
"ds_read_b64_tr_b16 a[188:189], v219 offset:37144\n"
"s_waitcnt lgkmcnt(14)\n"
"v_fma_mix_f32 v176, v16, v144, v152 op_sel:[0,0,0] op_sel_hi:[1,0,0]\n"
"v_fma_mix_f32 v177, v16, v145, v153 op_sel:[1,0,0] op_sel_hi:[1,0,0]\n"
"v_fma_mix_f32 v178, v17, v146, v154 op_sel:[0,0,0] op_sel_hi:[1,0,0]\n"
"v_fma_mix_f32 v179, v17, v147, v155 op_sel:[1,0,0] op_sel_hi:[1,0,0]\n"
"ds_read_b64_tr_b16 a[190:191], v219 offset:38168\n"
"s_waitcnt lgkmcnt(14)\n"
"v_fma_mix_f32 v180, v18, v148, v156 op_sel:[0,0,0] op_sel_hi:[1,0,0]\n"
"v_fma_mix_f32 v181, v18, v149, v157 op_sel:[1,0,0] op_sel_hi:[1,0,0]\n"
"v_fma_mix_f32 v182, v19, v150, v158 op_sel:[0,0,0] op_sel_hi:[1,0,0]\n"
"v_fma_mix_f32 v183, v19, v151, v159 op_sel:[1,0,0] op_sel_hi:[1,0,0]\n"
"ds_read_b64_tr_b16 a[224:225], v219 offset:33296\n"
"v_cvt_pk_bf16_f32 v16, v176, v177\n"
"v_cvt_pk_bf16_f32 v17, v178, v179\n"
"v_cvt_pk_bf16_f32 v18, v180, v181\n"
"v_cvt_pk_bf16_f32 v19, v182, v183\n"
"ds_read_b64_tr_b16 a[226:227], v219 offset:34320\n"
"v_fma_mix_f32 v184, v20, v144, v152 op_sel:[0,0,0] op_sel_hi:[1,0,0]\n"
"v_fma_mix_f32 v185, v20, v145, v153 op_sel:[1,0,0] op_sel_hi:[1,0,0]\n"
"v_fma_mix_f32 v186, v21, v146, v154 op_sel:[0,0,0] op_sel_hi:[1,0,0]\n"
"v_fma_mix_f32 v187, v21, v147, v155 op_sel:[1,0,0] op_sel_hi:[1,0,0]\n"
"ds_read_b64_tr_b16 a[228:229], v219 offset:37392\n"
"v_fma_mix_f32 v188, v22, v148, v156 op_sel:[0,0,0] op_sel_hi:[1,0,0]\n"
"v_fma_mix_f32 v189, v22, v149, v157 op_sel:[1,0,0] op_sel_hi:[1,0,0]\n"
"v_fma_mix_f32 v190, v23, v150, v158 op_sel:[0,0,0] op_sel_hi:[1,0,0]\n"
"v_fma_mix_f32 v191, v23, v151, v159 op_sel:[1,0,0] op_sel_hi:[1,0,0]\n"
"ds_read_b64_tr_b16 a[230:231], v219 offset:38416\n"
"ds_read_b128 v[144:147], v224 offset:1024\n"
"ds_read_b128 v[148:151], v224 offset:1088\n"
"ds_read_b128 v[152:155], v224 offset:1152\n"
"ds_read_b128 v[156:159], v224 offset:1216\n"
"v_mfma_f32_16x16x32_bf16 a[0:3], a[160:163], v[0:3], 0\n"
"ds_read_b64_tr_b16 a[232:233], v219 offset:33304\n"
"v_cvt_pk_bf16_f32 v20, v184, v185\n"
"v_mfma_f32_16x16x32_bf16 a[0:3], a[164:167], v[8:11], a[0:3]\n"
"ds_read_b64_tr_b16 a[234:235], v219 offset:34328\n"
"v_cvt_pk_bf16_f32 v21, v186, v187\n"
"v_mfma_f32_16x16x32_bf16 a[4:7], a[160:163], v[4:7], 0\n"
"s_waitcnt vmcnt(8)\n"
"s_barrier\n"
"ds_read_b64_tr_b16 a[236:237], v219 offset:37400\n"
"v_cvt_pk_bf16_f32 v22, v188, v189\n"
"v_mfma_f32_16x16x32_bf16 a[4:7], a[164:167], v[12:15], a[4:7]\n"
"ds_read_b64_tr_b16 a[238:239], v219 offset:38424\n"
"v_cvt_pk_bf16_f32 v23, v190, v191\n"
"v_mfma_f32_16x16x32_bf16 a[8:11], a[168:171], v[0:3], 0\n"
"s_waitcnt lgkmcnt(14)\n"
"s_add_u32 m0, s51, 65568\n"
"ds_read_b64_tr_b16 a[240:241], v219 offset:33552\n"
"v_fma_mix_f32 v176, v24, v160, v168 op_sel:[0,0,0] op_sel_hi:[1,0,0]\n"
"v_mfma_f32_16x16x32_bf16 a[8:11], a[172:175], v[8:11], a[8:11]\n"
"ds_read_b64_tr_b16 a[242:243], v219 offset:34576\n"
"buffer_load_dwordx4 v217, s[20:23], s50 offen offset:0 lds\n"
"v_mfma_f32_16x16x32_bf16 a[12:15], a[168:171], v[4:7], 0\n"
"ds_read_b64_tr_b16 a[244:245], v219 offset:37648\n"
"v_fma_mix_f32 v177, v24, v161, v169 op_sel:[1,0,0] op_sel_hi:[1,0,0]\n"
"v_mfma_f32_16x16x32_bf16 a[12:15], a[172:175], v[12:15], a[12:15]\n"
"ds_read_b64_tr_b16 a[246:247], v219 offset:38672\n"
"v_fma_mix_f32 v178, v25, v162, v170 op_sel:[0,0,0] op_sel_hi:[1,0,0]\n"
"v_mfma_f32_16x16x32_bf16 a[16:19], a[176:179], v[0:3], 0\n"
"ds_read_b64_tr_b16 a[248:249], v219 offset:33560\n"
"v_fma_mix_f32 v179, v25, v163, v171 op_sel:[1,0,0] op_sel_hi:[1,0,0]\n"
"v_mfma_f32_16x16x32_bf16 a[16:19], a[180:183], v[8:11], a[16:19]\n"
"ds_read_b64_tr_b16 a[250:251], v219 offset:34584\n"
"v_fma_mix_f32 v180, v26, v164, v172 op_sel:[0,0,0] op_sel_hi:[1,0,0]\n"
"v_mfma_f32_16x16x32_bf16 a[20:23], a[176:179], v[4:7], 0\n"
"s_add_u32 m0, s51, 73504\n"
"ds_read_b64_tr_b16 a[252:253], v219 offset:37656\n"
"v_fma_mix_f32 v181, v26, v165, v173 op_sel:[1,0,0] op_sel_hi:[1,0,0]\n"
"v_mfma_f32_16x16x32_bf16 a[20:23], a[180:183], v[12:15], a[20:23]\n"
"ds_read_b64_tr_b16 a[254:255], v219 offset:38680\n"
"buffer_load_dwordx4 v217, s[20:23], s50 offen offset:256 lds\n"
"v_mfma_f32_16x16x32_bf16 a[24:27], a[184:187], v[0:3], 0\n"
"s_waitcnt lgkmcnt(11)\n"
"s_add_u32 s50, s50, 32768\n"
"ds_read_b64_tr_b16 a[160:161], v219 offset:40976\n"
"v_fma_mix_f32 v182, v27, v166, v174 op_sel:[0,0,0] op_sel_hi:[1,0,0]\n"
"v_mfma_f32_16x16x32_bf16 a[24:27], a[188:191], v[8:11], a[24:27]\n"
"ds_read_b64_tr_b16 a[162:163], v219 offset:42000\n"
"v_fma_mix_f32 v183, v27, v167, v175 op_sel:[1,0,0] op_sel_hi:[1,0,0]\n"
"v_mfma_f32_16x16x32_bf16 a[28:31], a[184:187], v[4:7], 0\n"
"ds_read_b64_tr_b16 a[164:165], v219 offset:45072\n"
"v_cvt_pk_bf16_f32 v24, v176, v177\n"
"v_mfma_f32_16x16x32_bf16 a[28:31], a[188:191], v[12:15], a[28:31]\n"
"ds_read_b64_tr_b16 a[166:167], v219 offset:46096\n"
"v_cvt_pk_bf16_f32 v25, v178, v179\n"
"v_mfma_f32_16x16x32_bf16 a[32:35], a[224:227], v[0:3], 0\n"
"s_add_u32 m0, s51, 66592\n"
"ds_read_b64_tr_b16 a[168:169], v219 offset:40984\n"
"v_cvt_pk_bf16_f32 v26, v180, v181\n"
"v_mfma_f32_16x16x32_bf16 a[32:35], a[228:231], v[8:11], a[32:35]\n"
"ds_read_b64_tr_b16 a[170:171], v219 offset:42008\n"
"buffer_load_dwordx4 v217, s[20:23], s50 offen offset:0 lds\n"
"v_mfma_f32_16x16x32_bf16 a[36:39], a[224:227], v[4:7], 0\n"
"ds_read_b64_tr_b16 a[172:173], v219 offset:45080\n"
"v_cvt_pk_bf16_f32 v27, v182, v183\n"
"v_mfma_f32_16x16x32_bf16 a[36:39], a[228:231], v[12:15], a[36:39]\n"
"s_waitcnt lgkmcnt(11)\n"
"ds_read_b64_tr_b16 a[174:175], v219 offset:46104\n"
"v_fma_mix_f32 v184, v28, v160, v168 op_sel:[0,0,0] op_sel_hi:[1,0,0]\n"
"v_mfma_f32_16x16x32_bf16 a[40:43], a[232:235], v[0:3], 0\n"
"ds_read_b64_tr_b16 a[176:177], v219 offset:41232\n"
"v_fma_mix_f32 v185, v28, v161, v169 op_sel:[1,0,0] op_sel_hi:[1,0,0]\n"
"v_mfma_f32_16x16x32_bf16 a[40:43], a[236:239], v[8:11], a[40:43]\n"
"ds_read_b64_tr_b16 a[178:179], v219 offset:42256\n"
"v_fma_mix_f32 v186, v29, v162, v170 op_sel:[0,0,0] op_sel_hi:[1,0,0]\n"
"v_mfma_f32_16x16x32_bf16 a[44:47], a[232:235], v[4:7], 0\n"
"s_add_u32 m0, s51, 74528\n"
"ds_read_b64_tr_b16 a[180:181], v219 offset:45328\n"
"v_fma_mix_f32 v187, v29, v163, v171 op_sel:[1,0,0] op_sel_hi:[1,0,0]\n"
"v_mfma_f32_16x16x32_bf16 a[44:47], a[236:239], v[12:15], a[44:47]\n"
"ds_read_b64_tr_b16 a[182:183], v219 offset:46352\n"
"buffer_load_dwordx4 v217, s[20:23], s50 offen offset:256 lds\n"
"v_mfma_f32_16x16x32_bf16 a[48:51], a[240:243], v[0:3], 0\n"
"s_add_u32 s50, s50, 32768\n"
"ds_read_b64_tr_b16 a[184:185], v219 offset:41240\n"
"v_fma_mix_f32 v188, v30, v164, v172 op_sel:[0,0,0] op_sel_hi:[1,0,0]\n"
"v_mfma_f32_16x16x32_bf16 a[48:51], a[244:247], v[8:11], a[48:51]\n"
"ds_read_b64_tr_b16 a[186:187], v219 offset:42264\n"
"v_fma_mix_f32 v189, v30, v165, v173 op_sel:[1,0,0] op_sel_hi:[1,0,0]\n"
"v_mfma_f32_16x16x32_bf16 a[52:55], a[240:243], v[4:7], 0\n"
"ds_read_b64_tr_b16 a[188:189], v219 offset:45336\n"
"v_fma_mix_f32 v190, v31, v166, v174 op_sel:[0,0,0] op_sel_hi:[1,0,0]\n"
"v_mfma_f32_16x16x32_bf16 a[52:55], a[244:247], v[12:15], a[52:55]\n"
"s_waitcnt lgkmcnt(11)\n"
"ds_read_b64_tr_b16 a[190:191], v219 offset:46360\n"
"v_fma_mix_f32 v191, v31, v167, v175 op_sel:[1,0,0] op_sel_hi:[1,0,0]\n"
"v_mfma_f32_16x16x32_bf16 a[56:59], a[248:251], v[0:3], 0\n"
"s_add_u32 m0, s51, 81960\n"
"ds_read_b64_tr_b16 a[224:225], v219 offset:41488\n"
"ds_read_b128 v[160:163], v224 offset:1280\n"
"v_mfma_f32_16x16x32_bf16 a[56:59], a[252:255], v[8:11], a[56:59]\n"
"ds_read_b64_tr_b16 a[226:227], v219 offset:42512\n"
"buffer_load_dwordx4 v217, s[20:23], s50 offen offset:0 lds\n"
"v_mfma_f32_16x16x32_bf16 a[60:63], a[248:251], v[4:7], 0\n"
"ds_read_b64_tr_b16 a[228:229], v219 offset:45584\n"
"ds_read_b128 v[164:167], v224 offset:1344\n"
"v_mfma_f32_16x16x32_bf16 a[60:63], a[252:255], v[12:15], a[60:63]\n"
"ds_read_b64_tr_b16 a[230:231], v219 offset:46608\n"
"ds_read_b128 v[168:171], v224 offset:1408\n"
"v_mfma_f32_16x16x32_bf16 a[64:67], a[160:163], v[0:3], 0\n"
"ds_read_b64_tr_b16 a[232:233], v219 offset:41496\n"
"ds_read_b128 v[172:175], v224 offset:1472\n"
"v_mfma_f32_16x16x32_bf16 a[64:67], a[164:167], v[8:11], a[64:67]\n"
"ds_read_b64_tr_b16 a[234:235], v219 offset:42520\n"
"v_cvt_pk_bf16_f32 v28, v184, v185\n"
"v_mfma_f32_16x16x32_bf16 a[68:71], a[160:163], v[4:7], 0\n"
"s_add_u32 m0, s51, 89896\n"
"ds_read_b64_tr_b16 a[236:237], v219 offset:45592\n"
"v_cvt_pk_bf16_f32 v29, v186, v187\n"
"v_mfma_f32_16x16x32_bf16 a[68:71], a[164:167], v[12:15], a[68:71]\n"
"s_waitcnt lgkmcnt(14)\n"
"ds_read_b64_tr_b16 a[238:239], v219 offset:46616\n"
"buffer_load_dwordx4 v217, s[20:23], s50 offen offset:256 lds\n"
"v_mfma_f32_16x16x32_bf16 a[72:75], a[168:171], v[0:3], 0\n"
"s_add_u32 s50, s50, 32768\n"
"ds_read_b64_tr_b16 a[240:241], v219 offset:41744\n"
"v_cvt_pk_bf16_f32 v30, v188, v189\n"
"v_mfma_f32_16x16x32_bf16 a[72:75], a[172:175], v[8:11], a[72:75]\n"
"ds_read_b64_tr_b16 a[242:243], v219 offset:42768\n"
"v_cvt_pk_bf16_f32 v31, v190, v191\n"
"v_mfma_f32_16x16x32_bf16 a[76:79], a[168:171], v[4:7], 0\n"
"ds_read_b64_tr_b16 a[244:245], v219 offset:45840\n"
"v_fma_mix_f32 v176, v32, v144, v152 op_sel:[0,0,0] op_sel_hi:[1,0,0]\n"
"v_mfma_f32_16x16x32_bf16 a[76:79], a[172:175], v[12:15], a[76:79]\n"
"ds_read_b64_tr_b16 a[246:247], v219 offset:46864\n"
"v_fma_mix_f32 v177, v32, v145, v153 op_sel:[1,0,0] op_sel_hi:[1,0,0]\n"
"v_mfma_f32_16x16x32_bf16 a[80:83], a[176:179], v[0:3], 0\n"
"s_add_u32 m0, s51, 82984\n"
"ds_read_b64_tr_b16 a[248:249], v219 offset:41752\n"
"v_fma_mix_f32 v178, v33, v146, v154 op_sel:[0,0,0] op_sel_hi:[1,0,0]\n"
"v_mfma_f32_16x16x32_bf16 a[80:83], a[180:183], v[8:11], a[80:83]\n"
"ds_read_b64_tr_b16 a[250:251], v219 offset:42776\n"
"buffer_load_dwordx4 v217, s[20:23], s50 offen offset:0 lds\n"
"v_mfma_f32_16x16x32_bf16 a[84:87], a[176:179], v[4:7], 0\n"
"ds_read_b64_tr_b16 a[252:253], v219 offset:45848\n"
"v_fma_mix_f32 v179, v33, v147, v155 op_sel:[1,0,0] op_sel_hi:[1,0,0]\n"
"v_mfma_f32_16x16x32_bf16 a[84:87], a[180:183], v[12:15], a[84:87]\n"
"s_waitcnt lgkmcnt(12)\n"
"ds_read_b64_tr_b16 a[254:255], v219 offset:46872\n"
"v_fma_mix_f32 v180, v34, v148, v156 op_sel:[0,0,0] op_sel_hi:[1,0,0]\n"
"v_mfma_f32_16x16x32_bf16 a[88:91], a[184:187], v[0:3], 0\n"
"ds_read_b64_tr_b16 a[160:161], v218 offset:0\n"
"v_fma_mix_f32 v181, v34, v149, v157 op_sel:[1,0,0] op_sel_hi:[1,0,0]\n"
"v_mfma_f32_16x16x32_bf16 a[88:91], a[188:191], v[8:11], a[88:91]\n"
"ds_read_b64_tr_b16 a[162:163], v218 offset:1024\n"
"v_fma_mix_f32 v182, v35, v150, v158 op_sel:[0,0,0] op_sel_hi:[1,0,0]\n"
"v_mfma_f32_16x16x32_bf16 a[92:95], a[184:187], v[4:7], 0\n"
"s_add_u32 m0, s51, 90920\n"
"ds_read_b64_tr_b16 a[164:165], v218 offset:4096\n"
"v_fma_mix_f32 v183, v35, v151, v159 op_sel:[1,0,0] op_sel_hi:[1,0,0]\n"
"v_mfma_f32_16x16x32_bf16 a[92:95], a[188:191], v[12:15], a[92:95]\n"
"buffer_load_dwordx4 v217, s[20:23], s50 offen offset:256 lds\n"
"ds_read_b64_tr_b16 a[166:167], v218 offset:5120\n"
"v_mfma_f32_16x16x32_bf16 a[96:99], a[224:227], v[0:3], 0\n"
"s_add_u32 s50, s50, 425984\n"
"ds_read_b64_tr_b16 a[168:169], v218 offset:8\n"
"v_cvt_pk_bf16_f32 v32, v176, v177\n"
"v_mfma_f32_16x16x32_bf16 a[96:99], a[228:231], v[8:11], a[96:99]\n"
"ds_read_b64_tr_b16 a[170:171], v218 offset:1032\n"
"v_cvt_pk_bf16_f32 v33, v178, v179\n"
"v_mfma_f32_16x16x32_bf16 a[100:103], a[224:227], v[4:7], 0\n"
"ds_read_b64_tr_b16 a[172:173], v218 offset:4104\n"
"v_cvt_pk_bf16_f32 v34, v180, v181\n"
"v_mfma_f32_16x16x32_bf16 a[100:103], a[228:231], v[12:15], a[100:103]\n"
"s_waitcnt lgkmcnt(11)\n"
"ds_read_b64_tr_b16 a[174:175], v218 offset:5128\n"
"v_cvt_pk_bf16_f32 v35, v182, v183\n"
"v_mfma_f32_16x16x32_bf16 a[104:107], a[232:235], v[0:3], 0\n"
"ds_read_b64_tr_b16 a[176:177], v218 offset:256\n"
"v_fma_mix_f32 v184, v36, v144, v152 op_sel:[0,0,0] op_sel_hi:[1,0,0]\n"
"v_mfma_f32_16x16x32_bf16 a[104:107], a[236:239], v[8:11], a[104:107]\n"
"ds_read_b64_tr_b16 a[178:179], v218 offset:1280\n"
"v_fma_mix_f32 v185, v36, v145, v153 op_sel:[1,0,0] op_sel_hi:[1,0,0]\n"
"v_mfma_f32_16x16x32_bf16 a[108:111], a[232:235], v[4:7], 0\n"
"ds_read_b64_tr_b16 a[180:181], v218 offset:4352\n"
"v_fma_mix_f32 v186, v37, v146, v154 op_sel:[0,0,0] op_sel_hi:[1,0,0]\n"
"v_mfma_f32_16x16x32_bf16 a[108:111], a[236:239], v[12:15], a[108:111]\n"
"ds_read_b64_tr_b16 a[182:183], v218 offset:5376\n"
"v_fma_mix_f32 v187, v37, v147, v155 op_sel:[1,0,0] op_sel_hi:[1,0,0]\n"
"v_mfma_f32_16x16x32_bf16 a[112:115], a[240:243], v[0:3], 0\n"
"ds_read_b64_tr_b16 a[184:185], v218 offset:264\n"
"v_fma_mix_f32 v188, v38, v148, v156 op_sel:[0,0,0] op_sel_hi:[1,0,0]\n"
"v_mfma_f32_16x16x32_bf16 a[112:115], a[244:247], v[8:11], a[112:115]\n"
"ds_read_b64_tr_b16 a[186:187], v218 offset:1288\n"
"v_fma_mix_f32 v189, v38, v149, v157 op_sel:[1,0,0] op_sel_hi:[1,0,0]\n"
"v_mfma_f32_16x16x32_bf16 a[116:119], a[240:243], v[4:7], 0\n"
"ds_read_b64_tr_b16 a[188:189], v218 offset:4360\n"
"v_fma_mix_f32 v190, v39, v150, v158 op_sel:[0,0,0] op_sel_hi:[1,0,0]\n"
"v_mfma_f32_16x16x32_bf16 a[116:119], a[244:247], v[12:15], a[116:119]\n"
"s_waitcnt lgkmcnt(11)\n"
"ds_read_b64_tr_b16 a[190:191], v218 offset:5384\n"
"v_fma_mix_f32 v191, v39, v151, v159 op_sel:[1,0,0] op_sel_hi:[1,0,0]\n"
"v_mfma_f32_16x16x32_bf16 a[120:123], a[248:251], v[0:3], 0\n"
"ds_read_b64_tr_b16 a[224:225], v218 offset:512\n"
"ds_read_b128 v[144:147], v224 offset:1536\n"
"v_mfma_f32_16x16x32_bf16 a[120:123], a[252:255], v[8:11], a[120:123]\n"
"ds_read_b64_tr_b16 a[226:227], v218 offset:1536\n"
"ds_read_b128 v[148:151], v224 offset:1600\n"
"v_mfma_f32_16x16x32_bf16 a[124:127], a[248:251], v[4:7], 0\n"
"ds_read_b64_tr_b16 a[228:229], v218 offset:4608\n"
"ds_read_b128 v[152:155], v224 offset:1664\n"
"v_mfma_f32_16x16x32_bf16 a[124:127], a[252:255], v[12:15], a[124:127]\n"
"ds_read_b64_tr_b16 a[230:231], v218 offset:5632\n"
"ds_read_b128 v[156:159], v224 offset:1728\n"
"v_mfma_f32_16x16x32_bf16 a[0:3], a[160:163], v[16:19], a[0:3]\n"
"ds_read_b64_tr_b16 a[232:233], v218 offset:520\n"
"v_cvt_pk_bf16_f32 v36, v184, v185\n"
"v_mfma_f32_16x16x32_bf16 a[0:3], a[164:167], v[24:27], a[0:3]\n"
"ds_read_b64_tr_b16 a[234:235], v218 offset:1544\n"
"v_cvt_pk_bf16_f32 v37, v186, v187\n"
"v_mfma_f32_16x16x32_bf16 a[4:7], a[160:163], v[20:23], a[4:7]\n"
"s_waitcnt vmcnt(8)\n"
"s_barrier\n"
"ds_read_b64_tr_b16 a[236:237], v218 offset:4616\n"
"v_cvt_pk_bf16_f32 v38, v188, v189\n"
"v_mfma_f32_16x16x32_bf16 a[4:7], a[164:167], v[28:31], a[4:7]\n"
"s_waitcnt lgkmcnt(14)\n"
"ds_read_b64_tr_b16 a[238:239], v218 offset:5640\n"
"v_cvt_pk_bf16_f32 v39, v190, v191\n"
"v_mfma_f32_16x16x32_bf16 a[8:11], a[168:171], v[16:19], a[8:11]\n"
"s_add_u32 m0, s51, 98352\n"
"ds_read_b64_tr_b16 a[240:241], v218 offset:768\n"
"v_fma_mix_f32 v176, v40, v160, v168 op_sel:[0,0,0] op_sel_hi:[1,0,0]\n"
"v_mfma_f32_16x16x32_bf16 a[8:11], a[172:175], v[24:27], a[8:11]\n"
"ds_read_b64_tr_b16 a[242:243], v218 offset:1792\n"
"buffer_load_dwordx4 v217, s[20:23], s50 offen offset:0 lds\n"
"v_mfma_f32_16x16x32_bf16 a[12:15], a[168:171], v[20:23], a[12:15]\n"
"ds_read_b64_tr_b16 a[244:245], v218 offset:4864\n"
"v_fma_mix_f32 v177, v40, v161, v169 op_sel:[1,0,0] op_sel_hi:[1,0,0]\n"
"v_mfma_f32_16x16x32_bf16 a[12:15], a[172:175], v[28:31], a[12:15]\n"
"ds_read_b64_tr_b16 a[246:247], v218 offset:5888\n"
"v_fma_mix_f32 v178, v41, v162, v170 op_sel:[0,0,0] op_sel_hi:[1,0,0]\n"
"v_mfma_f32_16x16x32_bf16 a[16:19], a[176:179], v[16:19], a[16:19]\n"
"ds_read_b64_tr_b16 a[248:249], v218 offset:776\n"
"v_fma_mix_f32 v179, v41, v163, v171 op_sel:[1,0,0] op_sel_hi:[1,0,0]\n"
"v_mfma_f32_16x16x32_bf16 a[16:19], a[180:183], v[24:27], a[16:19]\n"
"ds_read_b64_tr_b16 a[250:251], v218 offset:1800\n"
"v_fma_mix_f32 v180, v42, v164, v172 op_sel:[0,0,0] op_sel_hi:[1,0,0]\n"
"v_mfma_f32_16x16x32_bf16 a[20:23], a[176:179], v[20:23], a[20:23]\n"
"s_add_u32 m0, s51, 106288\n"
"ds_read_b64_tr_b16 a[252:253], v218 offset:4872\n"
"v_fma_mix_f32 v181, v42, v165, v173 op_sel:[1,0,0] op_sel_hi:[1,0,0]\n"
"v_mfma_f32_16x16x32_bf16 a[20:23], a[180:183], v[28:31], a[20:23]\n"
"s_waitcnt lgkmcnt(11)\n"
"ds_read_b64_tr_b16 a[254:255], v218 offset:5896\n"
"buffer_load_dwordx4 v217, s[20:23], s50 offen offset:256 lds\n"
"v_mfma_f32_16x16x32_bf16 a[24:27], a[184:187], v[16:19], a[24:27]\n"
"s_add_u32 s50, s50, 32768\n"
"ds_read_b64_tr_b16 a[160:161], v218 offset:8192\n"
"v_fma_mix_f32 v182, v43, v166, v174 op_sel:[0,0,0] op_sel_hi:[1,0,0]\n"
"v_mfma_f32_16x16x32_bf16 a[24:27], a[188:191], v[24:27], a[24:27]\n"
"ds_read_b64_tr_b16 a[162:163], v218 offset:9216\n"
"v_fma_mix_f32 v183, v43, v167, v175 op_sel:[1,0,0] op_sel_hi:[1,0,0]\n"
"v_mfma_f32_16x16x32_bf16 a[28:31], a[184:187], v[20:23], a[28:31]\n"
"ds_read_b64_tr_b16 a[164:165], v218 offset:12288\n"
"v_cvt_pk_bf16_f32 v40, v176, v177\n"
"v_mfma_f32_16x16x32_bf16 a[28:31], a[188:191], v[28:31], a[28:31]\n"
"ds_read_b64_tr_b16 a[166:167], v218 offset:13312\n"
"v_cvt_pk_bf16_f32 v41, v178, v179\n"
"v_mfma_f32_16x16x32_bf16 a[32:35], a[224:227], v[16:19], a[32:35]\n"
"s_add_u32 m0, s51, 99376\n"
"ds_read_b64_tr_b16 a[168:169], v218 offset:8200\n"
"v_cvt_pk_bf16_f32 v42, v180, v181\n"
"v_mfma_f32_16x16x32_bf16 a[32:35], a[228:231], v[24:27], a[32:35]\n"
"ds_read_b64_tr_b16 a[170:171], v218 offset:9224\n"
"buffer_load_dwordx4 v217, s[20:23], s50 offen offset:0 lds\n"
"v_mfma_f32_16x16x32_bf16 a[36:39], a[224:227], v[20:23], a[36:39]\n"
"ds_read_b64_tr_b16 a[172:173], v218 offset:12296\n"
"v_cvt_pk_bf16_f32 v43, v182, v183\n"
"v_mfma_f32_16x16x32_bf16 a[36:39], a[228:231], v[28:31], a[36:39]\n"
"s_waitcnt lgkmcnt(11)\n"
"ds_read_b64_tr_b16 a[174:175], v218 offset:13320\n"
"v_fma_mix_f32 v184, v44, v160, v168 op_sel:[0,0,0] op_sel_hi:[1,0,0]\n"
"v_mfma_f32_16x16x32_bf16 a[40:43], a[232:235], v[16:19], a[40:43]\n"
"ds_read_b64_tr_b16 a[176:177], v218 offset:8448\n"
"v_fma_mix_f32 v185, v44, v161, v169 op_sel:[1,0,0] op_sel_hi:[1,0,0]\n"
"v_mfma_f32_16x16x32_bf16 a[40:43], a[236:239], v[24:27], a[40:43]\n"
"ds_read_b64_tr_b16 a[178:179], v218 offset:9472\n"
"v_fma_mix_f32 v186, v45, v162, v170 op_sel:[0,0,0] op_sel_hi:[1,0,0]\n"
"v_mfma_f32_16x16x32_bf16 a[44:47], a[232:235], v[20:23], a[44:47]\n"
"s_add_u32 m0, s51, 107312\n"
"ds_read_b64_tr_b16 a[180:181], v218 offset:12544\n"
"v_fma_mix_f32 v187, v45, v163, v171 op_sel:[1,0,0] op_sel_hi:[1,0,0]\n"
"v_mfma_f32_16x16x32_bf16 a[44:47], a[236:239], v[28:31], a[44:47]\n"
"ds_read_b64_tr_b16 a[182:183], v218 offset:13568\n"
"buffer_load_dwordx4 v217, s[20:23], s50 offen offset:256 lds\n"
"v_mfma_f32_16x16x32_bf16 a[48:51], a[240:243], v[16:19], a[48:51]\n"
"s_add_u32 s50, s50, 32768\n"
"ds_read_b64_tr_b16 a[184:185], v218 offset:8456\n"
"v_fma_mix_f32 v188, v46, v164, v172 op_sel:[0,0,0] op_sel_hi:[1,0,0]\n"
"v_mfma_f32_16x16x32_bf16 a[48:51], a[244:247], v[24:27], a[48:51]\n"
"ds_read_b64_tr_b16 a[186:187], v218 offset:9480\n"
"v_fma_mix_f32 v189, v46, v165, v173 op_sel:[1,0,0] op_sel_hi:[1,0,0]\n"
"v_mfma_f32_16x16x32_bf16 a[52:55], a[240:243], v[20:23], a[52:55]\n"
"ds_read_b64_tr_b16 a[188:189], v218 offset:12552\n"
"v_fma_mix_f32 v190, v47, v166, v174 op_sel:[0,0,0] op_sel_hi:[1,0,0]\n"
"v_mfma_f32_16x16x32_bf16 a[52:55], a[244:247], v[28:31], a[52:55]\n"
"s_waitcnt lgkmcnt(11)\n"
"ds_read_b64_tr_b16 a[190:191], v218 offset:13576\n"
"v_fma_mix_f32 v191, v47, v167, v175 op_sel:[1,0,0] op_sel_hi:[1,0,0]\n"
"v_mfma_f32_16x16x32_bf16 a[56:59], a[248:251], v[16:19], a[56:59]\n"
"s_add_u32 m0, s51, 114744\n"
"ds_read_b64_tr_b16 a[224:225], v218 offset:8704\n"
"ds_read_b128 v[160:163], v224 offset:1792\n"
"v_mfma_f32_16x16x32_bf16 a[56:59], a[252:255], v[24:27], a[56:59]\n"
"ds_read_b64_tr_b16 a[226:227], v218 offset:9728\n"
"buffer_load_dwordx4 v217, s[20:23], s50 offen offset:0 lds\n"
"v_mfma_f32_16x16x32_bf16 a[60:63], a[248:251], v[20:23], a[60:63]\n"
"ds_read_b64_tr_b16 a[228:229], v218 offset:12800\n"
"ds_read_b128 v[164:167], v224 offset:1856\n"
"v_mfma_f32_16x16x32_bf16 a[60:63], a[252:255], v[28:31], a[60:63]\n"
"ds_read_b64_tr_b16 a[230:231], v218 offset:13824\n"
"ds_read_b128 v[168:171], v224 offset:1920\n"
"v_mfma_f32_16x16x32_bf16 a[64:67], a[160:163], v[16:19], a[64:67]\n"
"ds_read_b64_tr_b16 a[232:233], v218 offset:8712\n"
"ds_read_b128 v[172:175], v224 offset:1984\n"
"v_mfma_f32_16x16x32_bf16 a[64:67], a[164:167], v[24:27], a[64:67]\n"
"ds_read_b64_tr_b16 a[234:235], v218 offset:9736\n"
"v_cvt_pk_bf16_f32 v44, v184, v185\n"
"v_mfma_f32_16x16x32_bf16 a[68:71], a[160:163], v[20:23], a[68:71]\n"
"s_add_u32 m0, s51, 122680\n"
"ds_read_b64_tr_b16 a[236:237], v218 offset:12808\n"
"v_cvt_pk_bf16_f32 v45, v186, v187\n"
"v_mfma_f32_16x16x32_bf16 a[68:71], a[164:167], v[28:31], a[68:71]\n"
"s_waitcnt lgkmcnt(14)\n"
"ds_read_b64_tr_b16 a[238:239], v218 offset:13832\n"
"buffer_load_dwordx4 v217, s[20:23], s50 offen offset:256 lds\n"
"v_mfma_f32_16x16x32_bf16 a[72:75], a[168:171], v[16:19], a[72:75]\n"
"s_add_u32 s50, s50, 32768\n"
"ds_read_b64_tr_b16 a[240:241], v218 offset:8960\n"
"v_cvt_pk_bf16_f32 v46, v188, v189\n"
"v_mfma_f32_16x16x32_bf16 a[72:75], a[172:175], v[24:27], a[72:75]\n"
"ds_read_b64_tr_b16 a[242:243], v218 offset:9984\n"
"v_cvt_pk_bf16_f32 v47, v190, v191\n"
"v_mfma_f32_16x16x32_bf16 a[76:79], a[168:171], v[20:23], a[76:79]\n"
"ds_read_b64_tr_b16 a[244:245], v218 offset:13056\n"
"v_fma_mix_f32 v176, v48, v144, v152 op_sel:[0,0,0] op_sel_hi:[1,0,0]\n"
"v_mfma_f32_16x16x32_bf16 a[76:79], a[172:175], v[28:31], a[76:79]\n"
"ds_read_b64_tr_b16 a[246:247], v218 offset:14080\n"
"v_fma_mix_f32 v177, v48, v145, v153 op_sel:[1,0,0] op_sel_hi:[1,0,0]\n"
"v_mfma_f32_16x16x32_bf16 a[80:83], a[176:179], v[16:19], a[80:83]\n"
"s_add_u32 m0, s51, 115768\n"
"ds_read_b64_tr_b16 a[248:249], v218 offset:8968\n"
"v_fma_mix_f32 v178, v49, v146, v154 op_sel:[0,0,0] op_sel_hi:[1,0,0]\n"
"v_mfma_f32_16x16x32_bf16 a[80:83], a[180:183], v[24:27], a[80:83]\n"
"ds_read_b64_tr_b16 a[250:251], v218 offset:9992\n"
"buffer_load_dwordx4 v217, s[20:23], s50 offen offset:0 lds\n"
"v_mfma_f32_16x16x32_bf16 a[84:87], a[176:179], v[20:23], a[84:87]\n"
"ds_read_b64_tr_b16 a[252:253], v218 offset:13064\n"
"v_fma_mix_f32 v179, v49, v147, v155 op_sel:[1,0,0] op_sel_hi:[1,0,0]\n"
"v_mfma_f32_16x16x32_bf16 a[84:87], a[180:183], v[28:31], a[84:87]\n"
"s_waitcnt lgkmcnt(12)\n"
"ds_read_b64_tr_b16 a[254:255], v218 offset:14088\n"
"v_fma_mix_f32 v180, v50, v148, v156 op_sel:[0,0,0] op_sel_hi:[1,0,0]\n"
"v_mfma_f32_16x16x32_bf16 a[88:91], a[184:187], v[16:19], a[88:91]\n"
"ds_read_b64_tr_b16 a[160:161], v218 offset:32784\n"
"v_fma_mix_f32 v181, v50, v149, v157 op_sel:[1,0,0] op_sel_hi:[1,0,0]\n"
"v_mfma_f32_16x16x32_bf16 a[88:91], a[188:191], v[24:27], a[88:91]\n"
"ds_read_b64_tr_b16 a[162:163], v218 offset:33808\n"
"v_fma_mix_f32 v182, v51, v150, v158 op_sel:[0,0,0] op_sel_hi:[1,0,0]\n"
"v_mfma_f32_16x16x32_bf16 a[92:95], a[184:187], v[20:23], a[92:95]\n"
"s_add_u32 m0, s51, 123704\n"
"ds_read_b64_tr_b16 a[164:165], v218 offset:36880\n"
"v_fma_mix_f32 v183, v51, v151, v159 op_sel:[1,0,0] op_sel_hi:[1,0,0]\n"
"v_mfma_f32_16x16x32_bf16 a[92:95], a[188:191], v[28:31], a[92:95]\n"
"buffer_load_dwordx4 v217, s[20:23], s50 offen offset:256 lds\n"
"ds_read_b64_tr_b16 a[166:167], v218 offset:37904\n"
"v_mfma_f32_16x16x32_bf16 a[96:99], a[224:227], v[16:19], a[96:99]\n"
"s_add_u32 s50, s50, 425984\n"
"ds_read_b64_tr_b16 a[168:169], v218 offset:32792\n"
"v_cvt_pk_bf16_f32 v48, v176, v177\n"
"v_mfma_f32_16x16x32_bf16 a[96:99], a[228:231], v[24:27], a[96:99]\n"
"ds_read_b64_tr_b16 a[170:171], v218 offset:33816\n"
"v_cvt_pk_bf16_f32 v49, v178, v179\n"
"v_mfma_f32_16x16x32_bf16 a[100:103], a[224:227], v[20:23], a[100:103]\n"
"ds_read_b64_tr_b16 a[172:173], v218 offset:36888\n"
"v_cvt_pk_bf16_f32 v50, v180, v181\n"
"v_mfma_f32_16x16x32_bf16 a[100:103], a[228:231], v[28:31], a[100:103]\n"
"s_waitcnt lgkmcnt(11)\n"
"ds_read_b64_tr_b16 a[174:175], v218 offset:37912\n"
"v_cvt_pk_bf16_f32 v51, v182, v183\n"
"v_mfma_f32_16x16x32_bf16 a[104:107], a[232:235], v[16:19], a[104:107]\n"
"ds_read_b64_tr_b16 a[176:177], v218 offset:33040\n"
"v_fma_mix_f32 v184, v52, v144, v152 op_sel:[0,0,0] op_sel_hi:[1,0,0]\n"
"v_mfma_f32_16x16x32_bf16 a[104:107], a[236:239], v[24:27], a[104:107]\n"
"ds_read_b64_tr_b16 a[178:179], v218 offset:34064\n"
"v_fma_mix_f32 v185, v52, v145, v153 op_sel:[1,0,0] op_sel_hi:[1,0,0]\n"
"v_mfma_f32_16x16x32_bf16 a[108:111], a[232:235], v[20:23], a[108:111]\n"
"ds_read_b64_tr_b16 a[180:181], v218 offset:37136\n"
"v_fma_mix_f32 v186, v53, v146, v154 op_sel:[0,0,0] op_sel_hi:[1,0,0]\n"
"v_mfma_f32_16x16x32_bf16 a[108:111], a[236:239], v[28:31], a[108:111]\n"
"ds_read_b64_tr_b16 a[182:183], v218 offset:38160\n"
"v_fma_mix_f32 v187, v53, v147, v155 op_sel:[1,0,0] op_sel_hi:[1,0,0]\n"
"v_mfma_f32_16x16x32_bf16 a[112:115], a[240:243], v[16:19], a[112:115]\n"
"ds_read_b64_tr_b16 a[184:185], v218 offset:33048\n"
"v_fma_mix_f32 v188, v54, v148, v156 op_sel:[0,0,0] op_sel_hi:[1,0,0]\n"
"v_mfma_f32_16x16x32_bf16 a[112:115], a[244:247], v[24:27], a[112:115]\n"
"ds_read_b64_tr_b16 a[186:187], v218 offset:34072\n"
"v_fma_mix_f32 v189, v54, v149, v157 op_sel:[1,0,0] op_sel_hi:[1,0,0]\n"
"v_mfma_f32_16x16x32_bf16 a[116:119], a[240:243], v[20:23], a[116:119]\n"
"ds_read_b64_tr_b16 a[188:189], v218 offset:37144\n"
"v_fma_mix_f32 v190, v55, v150, v158 op_sel:[0,0,0] op_sel_hi:[1,0,0]\n"
"v_mfma_f32_16x16x32_bf16 a[116:119], a[244:247], v[28:31], a[116:119]\n"
"s_waitcnt lgkmcnt(11)\n"
"ds_read_b64_tr_b16 a[190:191], v218 offset:38168\n"
"v_fma_mix_f32 v191, v55, v151, v159 op_sel:[1,0,0] op_sel_hi:[1,0,0]\n"
"v_mfma_f32_16x16x32_bf16 a[120:123], a[248:251], v[16:19], a[120:123]\n"
"ds_read_b64_tr_b16 a[224:225], v218 offset:33296\n"
"ds_read_b128 v[144:147], v224 offset:2048\n"
"v_mfma_f32_16x16x32_bf16 a[120:123], a[252:255], v[24:27], a[120:123]\n"
"ds_read_b64_tr_b16 a[226:227], v218 offset:34320\n"
"ds_read_b128 v[148:151], v224 offset:2112\n"
"v_mfma_f32_16x16x32_bf16 a[124:127], a[248:251], v[20:23], a[124:127]\n"
"ds_read_b64_tr_b16 a[228:229], v218 offset:37392\n"
"ds_read_b128 v[152:155], v224 offset:2176\n"
"v_mfma_f32_16x16x32_bf16 a[124:127], a[252:255], v[28:31], a[124:127]\n"
"ds_read_b64_tr_b16 a[230:231], v218 offset:38416\n"
"ds_read_b128 v[156:159], v224 offset:2240\n"
"v_mfma_f32_16x16x32_bf16 a[0:3], a[160:163], v[32:35], a[0:3]\n"
"ds_read_b64_tr_b16 a[232:233], v218 offset:33304\n"
"v_cvt_pk_bf16_f32 v52, v184, v185\n"
"v_mfma_f32_16x16x32_bf16 a[0:3], a[164:167], v[40:43], a[0:3]\n"
"ds_read_b64_tr_b16 a[234:235], v218 offset:34328\n"
"v_cvt_pk_bf16_f32 v53, v186, v187\n"
"v_mfma_f32_16x16x32_bf16 a[4:7], a[160:163], v[36:39], a[4:7]\n"
"s_waitcnt vmcnt(8)\n"
"s_barrier\n"
"ds_read_b64_tr_b16 a[236:237], v218 offset:37400\n"
"v_cvt_pk_bf16_f32 v54, v188, v189\n"
"v_mfma_f32_16x16x32_bf16 a[4:7], a[164:167], v[44:47], a[4:7]\n"
"s_waitcnt lgkmcnt(14)\n"
"ds_read_b64_tr_b16 a[238:239], v218 offset:38424\n"
"v_cvt_pk_bf16_f32 v55, v190, v191\n"
"v_mfma_f32_16x16x32_bf16 a[8:11], a[168:171], v[32:35], a[8:11]\n"
"s_mov_b32 m0, s51\n"
"ds_read_b64_tr_b16 a[240:241], v218 offset:33552\n"
"v_fma_mix_f32 v176, v56, v160, v168 op_sel:[0,0,0] op_sel_hi:[1,0,0]\n"
"v_mfma_f32_16x16x32_bf16 a[8:11], a[172:175], v[40:43], a[8:11]\n"
"ds_read_b64_tr_b16 a[242:243], v218 offset:34576\n"
"buffer_load_dwordx4 v217, s[20:23], s50 offen offset:0 lds\n"
"v_mfma_f32_16x16x32_bf16 a[12:15], a[168:171], v[36:39], a[12:15]\n"
"ds_read_b64_tr_b16 a[244:245], v218 offset:37648\n"
"v_fma_mix_f32 v177, v56, v161, v169 op_sel:[1,0,0] op_sel_hi:[1,0,0]\n"
"v_mfma_f32_16x16x32_bf16 a[12:15], a[172:175], v[44:47], a[12:15]\n"
"ds_read_b64_tr_b16 a[246:247], v218 offset:38672\n"
"v_fma_mix_f32 v178, v57, v162, v170 op_sel:[0,0,0] op_sel_hi:[1,0,0]\n"
"v_mfma_f32_16x16x32_bf16 a[16:19], a[176:179], v[32:35], a[16:19]\n"
"ds_read_b64_tr_b16 a[248:249], v218 offset:33560\n"
"v_fma_mix_f32 v179, v57, v163, v171 op_sel:[1,0,0] op_sel_hi:[1,0,0]\n"
"v_mfma_f32_16x16x32_bf16 a[16:19], a[180:183], v[40:43], a[16:19]\n"
"ds_read_b64_tr_b16 a[250:251], v218 offset:34584\n"
"v_fma_mix_f32 v180, v58, v164, v172 op_sel:[0,0,0] op_sel_hi:[1,0,0]\n"
"v_mfma_f32_16x16x32_bf16 a[20:23], a[176:179], v[36:39], a[20:23]\n"
"s_add_u32 m0, s51, 7936\n"
"ds_read_b64_tr_b16 a[252:253], v218 offset:37656\n"
"v_fma_mix_f32 v181, v58, v165, v173 op_sel:[1,0,0] op_sel_hi:[1,0,0]\n"
"v_mfma_f32_16x16x32_bf16 a[20:23], a[180:183], v[44:47], a[20:23]\n"
"s_waitcnt lgkmcnt(11)\n"
"ds_read_b64_tr_b16 a[254:255], v218 offset:38680\n"
"buffer_load_dwordx4 v217, s[20:23], s50 offen offset:256 lds\n"
"v_mfma_f32_16x16x32_bf16 a[24:27], a[184:187], v[32:35], a[24:27]\n"
"s_add_u32 s50, s50, 32768\n"
"ds_read_b64_tr_b16 a[160:161], v218 offset:40976\n"
"v_fma_mix_f32 v182, v59, v166, v174 op_sel:[0,0,0] op_sel_hi:[1,0,0]\n"
"v_mfma_f32_16x16x32_bf16 a[24:27], a[188:191], v[40:43], a[24:27]\n"
"ds_read_b64_tr_b16 a[162:163], v218 offset:42000\n"
"v_fma_mix_f32 v183, v59, v167, v175 op_sel:[1,0,0] op_sel_hi:[1,0,0]\n"
"v_mfma_f32_16x16x32_bf16 a[28:31], a[184:187], v[36:39], a[28:31]\n"
"ds_read_b64_tr_b16 a[164:165], v218 offset:45072\n"
"v_cvt_pk_bf16_f32 v56, v176, v177\n"
"v_mfma_f32_16x16x32_bf16 a[28:31], a[188:191], v[44:47], a[28:31]\n"
"ds_read_b64_tr_b16 a[166:167], v218 offset:46096\n"
"v_cvt_pk_bf16_f32 v57, v178, v179\n"
"v_mfma_f32_16x16x32_bf16 a[32:35], a[224:227], v[32:35], a[32:35]\n"
"s_add_u32 m0, s51, 1024\n"
"ds_read_b64_tr_b16 a[168:169], v218 offset:40984\n"
"v_cvt_pk_bf16_f32 v58, v180, v181\n"
"v_mfma_f32_16x16x32_bf16 a[32:35], a[228:231], v[40:43], a[32:35]\n"
"ds_read_b64_tr_b16 a[170:171], v218 offset:42008\n"
"buffer_load_dwordx4 v217, s[20:23], s50 offen offset:0 lds\n"
"v_mfma_f32_16x16x32_bf16 a[36:39], a[224:227], v[36:39], a[36:39]\n"
"ds_read_b64_tr_b16 a[172:173], v218 offset:45080\n"
"v_cvt_pk_bf16_f32 v59, v182, v183\n"
"v_mfma_f32_16x16x32_bf16 a[36:39], a[228:231], v[44:47], a[36:39]\n"
"s_waitcnt lgkmcnt(11)\n"
"ds_read_b64_tr_b16 a[174:175], v218 offset:46104\n"
"v_fma_mix_f32 v184, v60, v160, v168 op_sel:[0,0,0] op_sel_hi:[1,0,0]\n"
"v_mfma_f32_16x16x32_bf16 a[40:43], a[232:235], v[32:35], a[40:43]\n"
"ds_read_b64_tr_b16 a[176:177], v218 offset:41232\n"
"v_fma_mix_f32 v185, v60, v161, v169 op_sel:[1,0,0] op_sel_hi:[1,0,0]\n"
"v_mfma_f32_16x16x32_bf16 a[40:43], a[236:239], v[40:43], a[40:43]\n"
"ds_read_b64_tr_b16 a[178:179], v218 offset:42256\n"
"v_fma_mix_f32 v186, v61, v162, v170 op_sel:[0,0,0] op_sel_hi:[1,0,0]\n"
"v_mfma_f32_16x16x32_bf16 a[44:47], a[232:235], v[36:39], a[44:47]\n"
"s_add_u32 m0, s51, 8960\n"
"ds_read_b64_tr_b16 a[180:181], v218 offset:45328\n"
"v_fma_mix_f32 v187, v61, v163, v171 op_sel:[1,0,0] op_sel_hi:[1,0,0]\n"
"v_mfma_f32_16x16x32_bf16 a[44:47], a[236:239], v[44:47], a[44:47]\n"
"ds_read_b64_tr_b16 a[182:183], v218 offset:46352\n"
"buffer_load_dwordx4 v217, s[20:23], s50 offen offset:256 lds\n"
"v_mfma_f32_16x16x32_bf16 a[48:51], a[240:243], v[32:35], a[48:51]\n"
"s_add_u32 s50, s50, 32768\n"
"ds_read_b64_tr_b16 a[184:185], v218 offset:41240\n"
"v_fma_mix_f32 v188, v62, v164, v172 op_sel:[0,0,0] op_sel_hi:[1,0,0]\n"
"v_mfma_f32_16x16x32_bf16 a[48:51], a[244:247], v[40:43], a[48:51]\n"
"ds_read_b64_tr_b16 a[186:187], v218 offset:42264\n"
"v_fma_mix_f32 v189, v62, v165, v173 op_sel:[1,0,0] op_sel_hi:[1,0,0]\n"
"v_mfma_f32_16x16x32_bf16 a[52:55], a[240:243], v[36:39], a[52:55]\n"
"ds_read_b64_tr_b16 a[188:189], v218 offset:45336\n"
"v_fma_mix_f32 v190, v63, v166, v174 op_sel:[0,0,0] op_sel_hi:[1,0,0]\n"
"v_mfma_f32_16x16x32_bf16 a[52:55], a[244:247], v[44:47], a[52:55]\n"
"s_waitcnt lgkmcnt(11)\n"
"ds_read_b64_tr_b16 a[190:191], v218 offset:46360\n"
"v_fma_mix_f32 v191, v63, v167, v175 op_sel:[1,0,0] op_sel_hi:[1,0,0]\n"
"v_mfma_f32_16x16x32_bf16 a[56:59], a[248:251], v[32:35], a[56:59]\n"
"s_add_u32 m0, s51, 16392\n"
"ds_read_b64_tr_b16 a[224:225], v218 offset:41488\n"
"ds_read_b128 v[160:163], v224 offset:2304\n"
"v_mfma_f32_16x16x32_bf16 a[56:59], a[252:255], v[40:43], a[56:59]\n"
"ds_read_b64_tr_b16 a[226:227], v218 offset:42512\n"
"buffer_load_dwordx4 v217, s[20:23], s50 offen offset:0 lds\n"
"v_mfma_f32_16x16x32_bf16 a[60:63], a[248:251], v[36:39], a[60:63]\n"
"ds_read_b64_tr_b16 a[228:229], v218 offset:45584\n"
"ds_read_b128 v[164:167], v224 offset:2368\n"
"v_mfma_f32_16x16x32_bf16 a[60:63], a[252:255], v[44:47], a[60:63]\n"
"ds_read_b64_tr_b16 a[230:231], v218 offset:46608\n"
"ds_read_b128 v[168:171], v224 offset:2432\n"
"v_mfma_f32_16x16x32_bf16 a[64:67], a[160:163], v[32:35], a[64:67]\n"
"ds_read_b64_tr_b16 a[232:233], v218 offset:41496\n"
"ds_read_b128 v[172:175], v224 offset:2496\n"
"v_mfma_f32_16x16x32_bf16 a[64:67], a[164:167], v[40:43], a[64:67]\n"
"ds_read_b64_tr_b16 a[234:235], v218 offset:42520\n"
"v_cvt_pk_bf16_f32 v60, v184, v185\n"
"v_mfma_f32_16x16x32_bf16 a[68:71], a[160:163], v[36:39], a[68:71]\n"
"s_add_u32 m0, s51, 24328\n"
"ds_read_b64_tr_b16 a[236:237], v218 offset:45592\n"
"v_cvt_pk_bf16_f32 v61, v186, v187\n"
"v_mfma_f32_16x16x32_bf16 a[68:71], a[164:167], v[44:47], a[68:71]\n"
"s_waitcnt lgkmcnt(14)\n"
"ds_read_b64_tr_b16 a[238:239], v218 offset:46616\n"
"buffer_load_dwordx4 v217, s[20:23], s50 offen offset:256 lds\n"
"v_mfma_f32_16x16x32_bf16 a[72:75], a[168:171], v[32:35], a[72:75]\n"
"s_add_u32 s50, s50, 32768\n"
"ds_read_b64_tr_b16 a[240:241], v218 offset:41744\n"
"v_cvt_pk_bf16_f32 v62, v188, v189\n"
"v_mfma_f32_16x16x32_bf16 a[72:75], a[172:175], v[40:43], a[72:75]\n"
"ds_read_b64_tr_b16 a[242:243], v218 offset:42768\n"
"v_cvt_pk_bf16_f32 v63, v190, v191\n"
"v_mfma_f32_16x16x32_bf16 a[76:79], a[168:171], v[36:39], a[76:79]\n"
"ds_read_b64_tr_b16 a[244:245], v218 offset:45840\n"
"v_fma_mix_f32 v176, v64, v144, v152 op_sel:[0,0,0] op_sel_hi:[1,0,0]\n"
"v_mfma_f32_16x16x32_bf16 a[76:79], a[172:175], v[44:47], a[76:79]\n"
"ds_read_b64_tr_b16 a[246:247], v218 offset:46864\n"
"v_fma_mix_f32 v177, v64, v145, v153 op_sel:[1,0,0] op_sel_hi:[1,0,0]\n"
"v_mfma_f32_16x16x32_bf16 a[80:83], a[176:179], v[32:35], a[80:83]\n"
"s_add_u32 m0, s51, 17416\n"
"ds_read_b64_tr_b16 a[248:249], v218 offset:41752\n"
"v_fma_mix_f32 v178, v65, v146, v154 op_sel:[0,0,0] op_sel_hi:[1,0,0]\n"
"v_mfma_f32_16x16x32_bf16 a[80:83], a[180:183], v[40:43], a[80:83]\n"
"ds_read_b64_tr_b16 a[250:251], v218 offset:42776\n"
"buffer_load_dwordx4 v217, s[20:23], s50 offen offset:0 lds\n"
"v_mfma_f32_16x16x32_bf16 a[84:87], a[176:179], v[36:39], a[84:87]\n"
"ds_read_b64_tr_b16 a[252:253], v218 offset:45848\n"
"v_fma_mix_f32 v179, v65, v147, v155 op_sel:[1,0,0] op_sel_hi:[1,0,0]\n"
"v_mfma_f32_16x16x32_bf16 a[84:87], a[180:183], v[44:47], a[84:87]\n"
"s_waitcnt lgkmcnt(12)\n"
"ds_read_b64_tr_b16 a[254:255], v218 offset:46872\n"
"v_fma_mix_f32 v180, v66, v148, v156 op_sel:[0,0,0] op_sel_hi:[1,0,0]\n"
"v_mfma_f32_16x16x32_bf16 a[88:91], a[184:187], v[32:35], a[88:91]\n"
"ds_read_b64_tr_b16 a[160:161], v219 offset:0\n"
"v_fma_mix_f32 v181, v66, v149, v157 op_sel:[1,0,0] op_sel_hi:[1,0,0]\n"
"v_mfma_f32_16x16x32_bf16 a[88:91], a[188:191], v[40:43], a[88:91]\n"
"ds_read_b64_tr_b16 a[162:163], v219 offset:1024\n"
"v_fma_mix_f32 v182, v67, v150, v158 op_sel:[0,0,0] op_sel_hi:[1,0,0]\n"
"v_mfma_f32_16x16x32_bf16 a[92:95], a[184:187], v[36:39], a[92:95]\n"
"s_add_u32 m0, s51, 25352\n"
"ds_read_b64_tr_b16 a[164:165], v219 offset:4096\n"
"v_fma_mix_f32 v183, v67, v151, v159 op_sel:[1,0,0] op_sel_hi:[1,0,0]\n"
"v_mfma_f32_16x16x32_bf16 a[92:95], a[188:191], v[44:47], a[92:95]\n"
"buffer_load_dwordx4 v217, s[20:23], s50 offen offset:256 lds\n"
"ds_read_b64_tr_b16 a[166:167], v219 offset:5120\n"
"v_mfma_f32_16x16x32_bf16 a[96:99], a[224:227], v[32:35], a[96:99]\n"
"s_add_u32 s50, s50, 425984\n"
"ds_read_b64_tr_b16 a[168:169], v219 offset:8\n"
"v_cvt_pk_bf16_f32 v64, v176, v177\n"
"v_mfma_f32_16x16x32_bf16 a[96:99], a[228:231], v[40:43], a[96:99]\n"
"ds_read_b64_tr_b16 a[170:171], v219 offset:1032\n"
"v_cvt_pk_bf16_f32 v65, v178, v179\n"
"v_mfma_f32_16x16x32_bf16 a[100:103], a[224:227], v[36:39], a[100:103]\n"
"ds_read_b64_tr_b16 a[172:173], v219 offset:4104\n"
"v_cvt_pk_bf16_f32 v66, v180, v181\n"
"v_mfma_f32_16x16x32_bf16 a[100:103], a[228:231], v[44:47], a[100:103]\n"
"s_waitcnt lgkmcnt(11)\n"
"ds_read_b64_tr_b16 a[174:175], v219 offset:5128\n"
"v_cvt_pk_bf16_f32 v67, v182, v183\n"
"v_mfma_f32_16x16x32_bf16 a[104:107], a[232:235], v[32:35], a[104:107]\n"
"ds_read_b64_tr_b16 a[176:177], v219 offset:256\n"
"v_fma_mix_f32 v184, v68, v144, v152 op_sel:[0,0,0] op_sel_hi:[1,0,0]\n"
"v_mfma_f32_16x16x32_bf16 a[104:107], a[236:239], v[40:43], a[104:107]\n"
"ds_read_b64_tr_b16 a[178:179], v219 offset:1280\n"
"v_fma_mix_f32 v185, v68, v145, v153 op_sel:[1,0,0] op_sel_hi:[1,0,0]\n"
"v_mfma_f32_16x16x32_bf16 a[108:111], a[232:235], v[36:39], a[108:111]\n"
"ds_read_b64_tr_b16 a[180:181], v219 offset:4352\n"
"v_fma_mix_f32 v186, v69, v146, v154 op_sel:[0,0,0] op_sel_hi:[1,0,0]\n"
"v_mfma_f32_16x16x32_bf16 a[108:111], a[236:239], v[44:47], a[108:111]\n"
"ds_read_b64_tr_b16 a[182:183], v219 offset:5376\n"
"v_fma_mix_f32 v187, v69, v147, v155 op_sel:[1,0,0] op_sel_hi:[1,0,0]\n"
"v_mfma_f32_16x16x32_bf16 a[112:115], a[240:243], v[32:35], a[112:115]\n"
"ds_read_b64_tr_b16 a[184:185], v219 offset:264\n"
"v_fma_mix_f32 v188, v70, v148, v156 op_sel:[0,0,0] op_sel_hi:[1,0,0]\n"
"v_mfma_f32_16x16x32_bf16 a[112:115], a[244:247], v[40:43], a[112:115]\n"
"ds_read_b64_tr_b16 a[186:187], v219 offset:1288\n"
"v_fma_mix_f32 v189, v70, v149, v157 op_sel:[1,0,0] op_sel_hi:[1,0,0]\n"
"v_mfma_f32_16x16x32_bf16 a[116:119], a[240:243], v[36:39], a[116:119]\n"
"ds_read_b64_tr_b16 a[188:189], v219 offset:4360\n"
"v_fma_mix_f32 v190, v71, v150, v158 op_sel:[0,0,0] op_sel_hi:[1,0,0]\n"
"v_mfma_f32_16x16x32_bf16 a[116:119], a[244:247], v[44:47], a[116:119]\n"
"s_waitcnt lgkmcnt(11)\n"
"ds_read_b64_tr_b16 a[190:191], v219 offset:5384\n"
"v_fma_mix_f32 v191, v71, v151, v159 op_sel:[1,0,0] op_sel_hi:[1,0,0]\n"
"v_mfma_f32_16x16x32_bf16 a[120:123], a[248:251], v[32:35], a[120:123]\n"
"ds_read_b64_tr_b16 a[224:225], v219 offset:512\n"
"ds_read_b128 v[144:147], v224 offset:2560\n"
"v_mfma_f32_16x16x32_bf16 a[120:123], a[252:255], v[40:43], a[120:123]\n"
"ds_read_b64_tr_b16 a[226:227], v219 offset:1536\n"
"ds_read_b128 v[148:151], v224 offset:2624\n"
"v_mfma_f32_16x16x32_bf16 a[124:127], a[248:251], v[36:39], a[124:127]\n"
"ds_read_b64_tr_b16 a[228:229], v219 offset:4608\n"
"ds_read_b128 v[152:155], v224 offset:2688\n"
"v_mfma_f32_16x16x32_bf16 a[124:127], a[252:255], v[44:47], a[124:127]\n"
"ds_read_b64_tr_b16 a[230:231], v219 offset:5632\n"
"ds_read_b128 v[156:159], v224 offset:2752\n"
"v_mfma_f32_16x16x32_bf16 a[0:3], a[160:163], v[48:51], a[0:3]\n"
"ds_read_b64_tr_b16 a[232:233], v219 offset:520\n"
"v_cvt_pk_bf16_f32 v68, v184, v185\n"
"v_mfma_f32_16x16x32_bf16 a[0:3], a[164:167], v[56:59], a[0:3]\n"
"ds_read_b64_tr_b16 a[234:235], v219 offset:1544\n"
"v_cvt_pk_bf16_f32 v69, v186, v187\n"
"v_mfma_f32_16x16x32_bf16 a[4:7], a[160:163], v[52:55], a[4:7]\n"
"s_waitcnt vmcnt(8)\n"
"s_barrier\n"
"ds_read_b64_tr_b16 a[236:237], v219 offset:4616\n"
"v_cvt_pk_bf16_f32 v70, v188, v189\n"
"v_mfma_f32_16x16x32_bf16 a[4:7], a[164:167], v[60:63], a[4:7]\n"
"s_waitcnt lgkmcnt(14)\n"
"ds_read_b64_tr_b16 a[238:239], v219 offset:5640\n"
"v_cvt_pk_bf16_f32 v71, v190, v191\n"
"v_mfma_f32_16x16x32_bf16 a[8:11], a[168:171], v[48:51], a[8:11]\n"
"s_add_u32 m0, s51, 32784\n"
"ds_read_b64_tr_b16 a[240:241], v219 offset:768\n"
"v_fma_mix_f32 v176, v72, v160, v168 op_sel:[0,0,0] op_sel_hi:[1,0,0]\n"
"v_mfma_f32_16x16x32_bf16 a[8:11], a[172:175], v[56:59], a[8:11]\n"
"ds_read_b64_tr_b16 a[242:243], v219 offset:1792\n"
"buffer_load_dwordx4 v217, s[20:23], s50 offen offset:0 lds\n"
"v_mfma_f32_16x16x32_bf16 a[12:15], a[168:171], v[52:55], a[12:15]\n"
"ds_read_b64_tr_b16 a[244:245], v219 offset:4864\n"
"v_fma_mix_f32 v177, v72, v161, v169 op_sel:[1,0,0] op_sel_hi:[1,0,0]\n"
"v_mfma_f32_16x16x32_bf16 a[12:15], a[172:175], v[60:63], a[12:15]\n"
"ds_read_b64_tr_b16 a[246:247], v219 offset:5888\n"
"v_fma_mix_f32 v178, v73, v162, v170 op_sel:[0,0,0] op_sel_hi:[1,0,0]\n"
"v_mfma_f32_16x16x32_bf16 a[16:19], a[176:179], v[48:51], a[16:19]\n"
"ds_read_b64_tr_b16 a[248:249], v219 offset:776\n"
"v_fma_mix_f32 v179, v73, v163, v171 op_sel:[1,0,0] op_sel_hi:[1,0,0]\n"
"v_mfma_f32_16x16x32_bf16 a[16:19], a[180:183], v[56:59], a[16:19]\n"
"ds_read_b64_tr_b16 a[250:251], v219 offset:1800\n"
"v_fma_mix_f32 v180, v74, v164, v172 op_sel:[0,0,0] op_sel_hi:[1,0,0]\n"
"v_mfma_f32_16x16x32_bf16 a[20:23], a[176:179], v[52:55], a[20:23]\n"
"s_add_u32 m0, s51, 40720\n"
"ds_read_b64_tr_b16 a[252:253], v219 offset:4872\n"
"v_fma_mix_f32 v181, v74, v165, v173 op_sel:[1,0,0] op_sel_hi:[1,0,0]\n"
"v_mfma_f32_16x16x32_bf16 a[20:23], a[180:183], v[60:63], a[20:23]\n"
"s_waitcnt lgkmcnt(11)\n"
"ds_read_b64_tr_b16 a[254:255], v219 offset:5896\n"
"buffer_load_dwordx4 v217, s[20:23], s50 offen offset:256 lds\n"
"v_mfma_f32_16x16x32_bf16 a[24:27], a[184:187], v[48:51], a[24:27]\n"
"s_add_u32 s50, s50, 32768\n"
"ds_read_b64_tr_b16 a[160:161], v219 offset:8192\n"
"v_fma_mix_f32 v182, v75, v166, v174 op_sel:[0,0,0] op_sel_hi:[1,0,0]\n"
"v_mfma_f32_16x16x32_bf16 a[24:27], a[188:191], v[56:59], a[24:27]\n"
"ds_read_b64_tr_b16 a[162:163], v219 offset:9216\n"
"v_fma_mix_f32 v183, v75, v167, v175 op_sel:[1,0,0] op_sel_hi:[1,0,0]\n"
"v_mfma_f32_16x16x32_bf16 a[28:31], a[184:187], v[52:55], a[28:31]\n"
"ds_read_b64_tr_b16 a[164:165], v219 offset:12288\n"
"v_cvt_pk_bf16_f32 v72, v176, v177\n"
"v_mfma_f32_16x16x32_bf16 a[28:31], a[188:191], v[60:63], a[28:31]\n"
"ds_read_b64_tr_b16 a[166:167], v219 offset:13312\n"
"v_cvt_pk_bf16_f32 v73, v178, v179\n"
"v_mfma_f32_16x16x32_bf16 a[32:35], a[224:227], v[48:51], a[32:35]\n"
"s_add_u32 m0, s51, 33808\n"
"ds_read_b64_tr_b16 a[168:169], v219 offset:8200\n"
"v_cvt_pk_bf16_f32 v74, v180, v181\n"
"v_mfma_f32_16x16x32_bf16 a[32:35], a[228:231], v[56:59], a[32:35]\n"
"ds_read_b64_tr_b16 a[170:171], v219 offset:9224\n"
"buffer_load_dwordx4 v217, s[20:23], s50 offen offset:0 lds\n"
"v_mfma_f32_16x16x32_bf16 a[36:39], a[224:227], v[52:55], a[36:39]\n"
"ds_read_b64_tr_b16 a[172:173], v219 offset:12296\n"
"v_cvt_pk_bf16_f32 v75, v182, v183\n"
"v_mfma_f32_16x16x32_bf16 a[36:39], a[228:231], v[60:63], a[36:39]\n"
"s_waitcnt lgkmcnt(11)\n"
"ds_read_b64_tr_b16 a[174:175], v219 offset:13320\n"
"v_fma_mix_f32 v184, v76, v160, v168 op_sel:[0,0,0] op_sel_hi:[1,0,0]\n"
"v_mfma_f32_16x16x32_bf16 a[40:43], a[232:235], v[48:51], a[40:43]\n"
"ds_read_b64_tr_b16 a[176:177], v219 offset:8448\n"
"v_fma_mix_f32 v185, v76, v161, v169 op_sel:[1,0,0] op_sel_hi:[1,0,0]\n"
"v_mfma_f32_16x16x32_bf16 a[40:43], a[236:239], v[56:59], a[40:43]\n"
"ds_read_b64_tr_b16 a[178:179], v219 offset:9472\n"
"v_fma_mix_f32 v186, v77, v162, v170 op_sel:[0,0,0] op_sel_hi:[1,0,0]\n"
"v_mfma_f32_16x16x32_bf16 a[44:47], a[232:235], v[52:55], a[44:47]\n"
"s_add_u32 m0, s51, 41744\n"
"ds_read_b64_tr_b16 a[180:181], v219 offset:12544\n"
"v_fma_mix_f32 v187, v77, v163, v171 op_sel:[1,0,0] op_sel_hi:[1,0,0]\n"
"v_mfma_f32_16x16x32_bf16 a[44:47], a[236:239], v[60:63], a[44:47]\n"
"ds_read_b64_tr_b16 a[182:183], v219 offset:13568\n"
"buffer_load_dwordx4 v217, s[20:23], s50 offen offset:256 lds\n"
"v_mfma_f32_16x16x32_bf16 a[48:51], a[240:243], v[48:51], a[48:51]\n"
"s_add_u32 s50, s50, 32768\n"
"ds_read_b64_tr_b16 a[184:185], v219 offset:8456\n"
"v_fma_mix_f32 v188, v78, v164, v172 op_sel:[0,0,0] op_sel_hi:[1,0,0]\n"
"v_mfma_f32_16x16x32_bf16 a[48:51], a[244:247], v[56:59], a[48:51]\n"
"ds_read_b64_tr_b16 a[186:187], v219 offset:9480\n"
"v_fma_mix_f32 v189, v78, v165, v173 op_sel:[1,0,0] op_sel_hi:[1,0,0]\n"
"v_mfma_f32_16x16x32_bf16 a[52:55], a[240:243], v[52:55], a[52:55]\n"
"ds_read_b64_tr_b16 a[188:189], v219 offset:12552\n"
"v_fma_mix_f32 v190, v79, v166, v174 op_sel:[0,0,0] op_sel_hi:[1,0,0]\n"
"v_mfma_f32_16x16x32_bf16 a[52:55], a[244:247], v[60:63], a[52:55]\n"
"s_waitcnt lgkmcnt(11)\n"
"ds_read_b64_tr_b16 a[190:191], v219 offset:13576\n"
"v_fma_mix_f32 v191, v79, v167, v175 op_sel:[1,0,0] op_sel_hi:[1,0,0]\n"
"v_mfma_f32_16x16x32_bf16 a[56:59], a[248:251], v[48:51], a[56:59]\n"
"s_add_u32 m0, s51, 49176\n"
"ds_read_b64_tr_b16 a[224:225], v219 offset:8704\n"
"ds_read_b128 v[160:163], v224 offset:2816\n"
"v_mfma_f32_16x16x32_bf16 a[56:59], a[252:255], v[56:59], a[56:59]\n"
"ds_read_b64_tr_b16 a[226:227], v219 offset:9728\n"
"buffer_load_dwordx4 v217, s[20:23], s50 offen offset:0 lds\n"
"v_mfma_f32_16x16x32_bf16 a[60:63], a[248:251], v[52:55], a[60:63]\n"
"ds_read_b64_tr_b16 a[228:229], v219 offset:12800\n"
"ds_read_b128 v[164:167], v224 offset:2880\n"
"v_mfma_f32_16x16x32_bf16 a[60:63], a[252:255], v[60:63], a[60:63]\n"
"ds_read_b64_tr_b16 a[230:231], v219 offset:13824\n"
"ds_read_b128 v[168:171], v224 offset:2944\n"
"v_mfma_f32_16x16x32_bf16 a[64:67], a[160:163], v[48:51], a[64:67]\n"
"ds_read_b64_tr_b16 a[232:233], v219 offset:8712\n"
"ds_read_b128 v[172:175], v224 offset:3008\n"
"v_mfma_f32_16x16x32_bf16 a[64:67], a[164:167], v[56:59], a[64:67]\n"
"ds_read_b64_tr_b16 a[234:235], v219 offset:9736\n"
"v_cvt_pk_bf16_f32 v76, v184, v185\n"
"v_mfma_f32_16x16x32_bf16 a[68:71], a[160:163], v[52:55], a[68:71]\n"
"s_add_u32 m0, s51, 57112\n"
"ds_read_b64_tr_b16 a[236:237], v219 offset:12808\n"
"v_cvt_pk_bf16_f32 v77, v186, v187\n"
"v_mfma_f32_16x16x32_bf16 a[68:71], a[164:167], v[60:63], a[68:71]\n"
"s_waitcnt lgkmcnt(14)\n"
"ds_read_b64_tr_b16 a[238:239], v219 offset:13832\n"
"buffer_load_dwordx4 v217, s[20:23], s50 offen offset:256 lds\n"
"v_mfma_f32_16x16x32_bf16 a[72:75], a[168:171], v[48:51], a[72:75]\n"
"s_add_u32 s50, s50, 32768\n"
"ds_read_b64_tr_b16 a[240:241], v219 offset:8960\n"
"v_cvt_pk_bf16_f32 v78, v188, v189\n"
"v_mfma_f32_16x16x32_bf16 a[72:75], a[172:175], v[56:59], a[72:75]\n"
"ds_read_b64_tr_b16 a[242:243], v219 offset:9984\n"
"v_cvt_pk_bf16_f32 v79, v190, v191\n"
"v_mfma_f32_16x16x32_bf16 a[76:79], a[168:171], v[52:55], a[76:79]\n"
"ds_read_b64_tr_b16 a[244:245], v219 offset:13056\n"
"v_fma_mix_f32 v176, v80, v144, v152 op_sel:[0,0,0] op_sel_hi:[1,0,0]\n"
"v_mfma_f32_16x16x32_bf16 a[76:79], a[172:175], v[60:63], a[76:79]\n"
"ds_read_b64_tr_b16 a[246:247], v219 offset:14080\n"
"v_fma_mix_f32 v177, v80, v145, v153 op_sel:[1,0,0] op_sel_hi:[1,0,0]\n"
"v_mfma_f32_16x16x32_bf16 a[80:83], a[176:179], v[48:51], a[80:83]\n"
"s_add_u32 m0, s51, 50200\n"
"ds_read_b64_tr_b16 a[248:249], v219 offset:8968\n"
"v_fma_mix_f32 v178, v81, v146, v154 op_sel:[0,0,0] op_sel_hi:[1,0,0]\n"
"v_mfma_f32_16x16x32_bf16 a[80:83], a[180:183], v[56:59], a[80:83]\n"
"ds_read_b64_tr_b16 a[250:251], v219 offset:9992\n"
"buffer_load_dwordx4 v217, s[20:23], s50 offen offset:0 lds\n"
"v_mfma_f32_16x16x32_bf16 a[84:87], a[176:179], v[52:55], a[84:87]\n"
"ds_read_b64_tr_b16 a[252:253], v219 offset:13064\n"
"v_fma_mix_f32 v179, v81, v147, v155 op_sel:[1,0,0] op_sel_hi:[1,0,0]\n"
"v_mfma_f32_16x16x32_bf16 a[84:87], a[180:183], v[60:63], a[84:87]\n"
"s_waitcnt lgkmcnt(12)\n"
"ds_read_b64_tr_b16 a[254:255], v219 offset:14088\n"
"v_fma_mix_f32 v180, v82, v148, v156 op_sel:[0,0,0] op_sel_hi:[1,0,0]\n"
"v_mfma_f32_16x16x32_bf16 a[88:91], a[184:187], v[48:51], a[88:91]\n"
"ds_read_b64_tr_b16 a[160:161], v219 offset:32784\n"
"v_fma_mix_f32 v181, v82, v149, v157 op_sel:[1,0,0] op_sel_hi:[1,0,0]\n"
"v_mfma_f32_16x16x32_bf16 a[88:91], a[188:191], v[56:59], a[88:91]\n"
"ds_read_b64_tr_b16 a[162:163], v219 offset:33808\n"
"v_fma_mix_f32 v182, v83, v150, v158 op_sel:[0,0,0] op_sel_hi:[1,0,0]\n"
"v_mfma_f32_16x16x32_bf16 a[92:95], a[184:187], v[52:55], a[92:95]\n"
"s_add_u32 m0, s51, 58136\n"
"ds_read_b64_tr_b16 a[164:165], v219 offset:36880\n"
"v_fma_mix_f32 v183, v83, v151, v159 op_sel:[1,0,0] op_sel_hi:[1,0,0]\n"
"v_mfma_f32_16x16x32_bf16 a[92:95], a[188:191], v[60:63], a[92:95]\n"
"buffer_load_dwordx4 v217, s[20:23], s50 offen offset:256 lds\n"
"ds_read_b64_tr_b16 a[166:167], v219 offset:37904\n"
"v_mfma_f32_16x16x32_bf16 a[96:99], a[224:227], v[48:51], a[96:99]\n"
"s_add_u32 s50, s50, 425984\n"
"ds_read_b64_tr_b16 a[168:169], v219 offset:32792\n"
"v_cvt_pk_bf16_f32 v80, v176, v177\n"
"v_mfma_f32_16x16x32_bf16 a[96:99], a[228:231], v[56:59], a[96:99]\n"
"ds_read_b64_tr_b16 a[170:171], v219 offset:33816\n"
"v_cvt_pk_bf16_f32 v81, v178, v179\n"
"v_mfma_f32_16x16x32_bf16 a[100:103], a[224:227], v[52:55], a[100:103]\n"
"ds_read_b64_tr_b16 a[172:173], v219 offset:36888\n"
"v_cvt_pk_bf16_f32 v82, v180, v181\n"
"v_mfma_f32_16x16x32_bf16 a[100:103], a[228:231], v[60:63], a[100:103]\n"
"s_waitcnt lgkmcnt(11)\n"
"ds_read_b64_tr_b16 a[174:175], v219 offset:37912\n"
"v_cvt_pk_bf16_f32 v83, v182, v183\n"
"v_mfma_f32_16x16x32_bf16 a[104:107], a[232:235], v[48:51], a[104:107]\n"
"ds_read_b64_tr_b16 a[176:177], v219 offset:33040\n"
"v_fma_mix_f32 v184, v84, v144, v152 op_sel:[0,0,0] op_sel_hi:[1,0,0]\n"
"v_mfma_f32_16x16x32_bf16 a[104:107], a[236:239], v[56:59], a[104:107]\n"
"ds_read_b64_tr_b16 a[178:179], v219 offset:34064\n"
"v_fma_mix_f32 v185, v84, v145, v153 op_sel:[1,0,0] op_sel_hi:[1,0,0]\n"
"v_mfma_f32_16x16x32_bf16 a[108:111], a[232:235], v[52:55], a[108:111]\n"
"ds_read_b64_tr_b16 a[180:181], v219 offset:37136\n"
"v_fma_mix_f32 v186, v85, v146, v154 op_sel:[0,0,0] op_sel_hi:[1,0,0]\n"
"v_mfma_f32_16x16x32_bf16 a[108:111], a[236:239], v[60:63], a[108:111]\n"
"ds_read_b64_tr_b16 a[182:183], v219 offset:38160\n"
"v_fma_mix_f32 v187, v85, v147, v155 op_sel:[1,0,0] op_sel_hi:[1,0,0]\n"
"v_mfma_f32_16x16x32_bf16 a[112:115], a[240:243], v[48:51], a[112:115]\n"
"ds_read_b64_tr_b16 a[184:185], v219 offset:33048\n"
"v_fma_mix_f32 v188, v86, v148, v156 op_sel:[0,0,0] op_sel_hi:[1,0,0]\n"
"v_mfma_f32_16x16x32_bf16 a[112:115], a[244:247], v[56:59], a[112:115]\n"
"ds_read_b64_tr_b16 a[186:187], v219 offset:34072\n"
"v_fma_mix_f32 v189, v86, v149, v157 op_sel:[1,0,0] op_sel_hi:[1,0,0]\n"
"v_mfma_f32_16x16x32_bf16 a[116:119], a[240:243], v[52:55], a[116:119]\n"
"ds_read_b64_tr_b16 a[188:189], v219 offset:37144\n"
"v_fma_mix_f32 v190, v87, v150, v158 op_sel:[0,0,0] op_sel_hi:[1,0,0]\n"
"v_mfma_f32_16x16x32_bf16 a[116:119], a[244:247], v[60:63], a[116:119]\n"
"s_waitcnt lgkmcnt(11)\n"
"ds_read_b64_tr_b16 a[190:191], v219 offset:38168\n"
"v_fma_mix_f32 v191, v87, v151, v159 op_sel:[1,0,0] op_sel_hi:[1,0,0]\n"
"v_mfma_f32_16x16x32_bf16 a[120:123], a[248:251], v[48:51], a[120:123]\n"
"ds_read_b64_tr_b16 a[224:225], v219 offset:33296\n"
"ds_read_b128 v[144:147], v224 offset:3072\n"
"v_mfma_f32_16x16x32_bf16 a[120:123], a[252:255], v[56:59], a[120:123]\n"
"ds_read_b64_tr_b16 a[226:227], v219 offset:34320\n"
"ds_read_b128 v[148:151], v224 offset:3136\n"
"v_mfma_f32_16x16x32_bf16 a[124:127], a[248:251], v[52:55], a[124:127]\n"
"ds_read_b64_tr_b16 a[228:229], v219 offset:37392\n"
"ds_read_b128 v[152:155], v224 offset:3200\n"
"v_mfma_f32_16x16x32_bf16 a[124:127], a[252:255], v[60:63], a[124:127]\n"
"ds_read_b64_tr_b16 a[230:231], v219 offset:38416\n"
"ds_read_b128 v[156:159], v224 offset:3264\n"
"v_mfma_f32_16x16x32_bf16 a[0:3], a[160:163], v[64:67], a[0:3]\n"
"ds_read_b64_tr_b16 a[232:233], v219 offset:33304\n"
"v_cvt_pk_bf16_f32 v84, v184, v185\n"
"v_mfma_f32_16x16x32_bf16 a[0:3], a[164:167], v[72:75], a[0:3]\n"
"ds_read_b64_tr_b16 a[234:235], v219 offset:34328\n"
"v_cvt_pk_bf16_f32 v85, v186, v187\n"
"v_mfma_f32_16x16x32_bf16 a[4:7], a[160:163], v[68:71], a[4:7]\n"
"s_waitcnt vmcnt(8)\n"
"s_barrier\n"
"ds_read_b64_tr_b16 a[236:237], v219 offset:37400\n"
"v_cvt_pk_bf16_f32 v86, v188, v189\n"
"v_mfma_f32_16x16x32_bf16 a[4:7], a[164:167], v[76:79], a[4:7]\n"
"s_waitcnt lgkmcnt(14)\n"
"ds_read_b64_tr_b16 a[238:239], v219 offset:38424\n"
"v_cvt_pk_bf16_f32 v87, v190, v191\n"
"v_mfma_f32_16x16x32_bf16 a[8:11], a[168:171], v[64:67], a[8:11]\n"
"s_add_u32 m0, s51, 65568\n"
"ds_read_b64_tr_b16 a[240:241], v219 offset:33552\n"
"v_fma_mix_f32 v176, v88, v160, v168 op_sel:[0,0,0] op_sel_hi:[1,0,0]\n"
"v_mfma_f32_16x16x32_bf16 a[8:11], a[172:175], v[72:75], a[8:11]\n"
"ds_read_b64_tr_b16 a[242:243], v219 offset:34576\n"
"buffer_load_dwordx4 v217, s[20:23], s50 offen offset:0 lds\n"
"v_mfma_f32_16x16x32_bf16 a[12:15], a[168:171], v[68:71], a[12:15]\n"
"ds_read_b64_tr_b16 a[244:245], v219 offset:37648\n"
"v_fma_mix_f32 v177, v88, v161, v169 op_sel:[1,0,0] op_sel_hi:[1,0,0]\n"
"v_mfma_f32_16x16x32_bf16 a[12:15], a[172:175], v[76:79], a[12:15]\n"
"ds_read_b64_tr_b16 a[246:247], v219 offset:38672\n"
"v_fma_mix_f32 v178, v89, v162, v170 op_sel:[0,0,0] op_sel_hi:[1,0,0]\n"
"v_mfma_f32_16x16x32_bf16 a[16:19], a[176:179], v[64:67], a[16:19]\n"
"ds_read_b64_tr_b16 a[248:249], v219 offset:33560\n"
"v_fma_mix_f32 v179, v89, v163, v171 op_sel:[1,0,0] op_sel_hi:[1,0,0]\n"
"v_mfma_f32_16x16x32_bf16 a[16:19], a[180:183], v[72:75], a[16:19]\n"
"ds_read_b64_tr_b16 a[250:251], v219 offset:34584\n"
"v_fma_mix_f32 v180, v90, v164, v172 op_sel:[0,0,0] op_sel_hi:[1,0,0]\n"
"v_mfma_f32_16x16x32_bf16 a[20:23], a[176:179], v[68:71], a[20:23]\n"
"s_add_u32 m0, s51, 73504\n"
"ds_read_b64_tr_b16 a[252:253], v219 offset:37656\n"
"v_fma_mix_f32 v181, v90, v165, v173 op_sel:[1,0,0] op_sel_hi:[1,0,0]\n"
"v_mfma_f32_16x16x32_bf16 a[20:23], a[180:183], v[76:79], a[20:23]\n"
"s_waitcnt lgkmcnt(11)\n"
"ds_read_b64_tr_b16 a[254:255], v219 offset:38680\n"
"buffer_load_dwordx4 v217, s[20:23], s50 offen offset:256 lds\n"
"v_mfma_f32_16x16x32_bf16 a[24:27], a[184:187], v[64:67], a[24:27]\n"
"s_add_u32 s50, s50, 32768\n"
"ds_read_b64_tr_b16 a[160:161], v219 offset:40976\n"
"v_fma_mix_f32 v182, v91, v166, v174 op_sel:[0,0,0] op_sel_hi:[1,0,0]\n"
"v_mfma_f32_16x16x32_bf16 a[24:27], a[188:191], v[72:75], a[24:27]\n"
"ds_read_b64_tr_b16 a[162:163], v219 offset:42000\n"
"v_fma_mix_f32 v183, v91, v167, v175 op_sel:[1,0,0] op_sel_hi:[1,0,0]\n"
"v_mfma_f32_16x16x32_bf16 a[28:31], a[184:187], v[68:71], a[28:31]\n"
"ds_read_b64_tr_b16 a[164:165], v219 offset:45072\n"
"v_cvt_pk_bf16_f32 v88, v176, v177\n"
"v_mfma_f32_16x16x32_bf16 a[28:31], a[188:191], v[76:79], a[28:31]\n"
"ds_read_b64_tr_b16 a[166:167], v219 offset:46096\n"
"v_cvt_pk_bf16_f32 v89, v178, v179\n"
"v_mfma_f32_16x16x32_bf16 a[32:35], a[224:227], v[64:67], a[32:35]\n"
"s_add_u32 m0, s51, 66592\n"
"ds_read_b64_tr_b16 a[168:169], v219 offset:40984\n"
"v_cvt_pk_bf16_f32 v90, v180, v181\n"
"v_mfma_f32_16x16x32_bf16 a[32:35], a[228:231], v[72:75], a[32:35]\n"
"ds_read_b64_tr_b16 a[170:171], v219 offset:42008\n"
"buffer_load_dwordx4 v217, s[20:23], s50 offen offset:0 lds\n"
"v_mfma_f32_16x16x32_bf16 a[36:39], a[224:227], v[68:71], a[36:39]\n"
"ds_read_b64_tr_b16 a[172:173], v219 offset:45080\n"
"v_cvt_pk_bf16_f32 v91, v182, v183\n"
"v_mfma_f32_16x16x32_bf16 a[36:39], a[228:231], v[76:79], a[36:39]\n"
"s_waitcnt lgkmcnt(11)\n"
"ds_read_b64_tr_b16 a[174:175], v219 offset:46104\n"
"v_fma_mix_f32 v184, v92, v160, v168 op_sel:[0,0,0] op_sel_hi:[1,0,0]\n"
"v_mfma_f32_16x16x32_bf16 a[40:43], a[232:235], v[64:67], a[40:43]\n"
"ds_read_b64_tr_b16 a[176:177], v219 offset:41232\n"
"v_fma_mix_f32 v185, v92, v161, v169 op_sel:[1,0,0] op_sel_hi:[1,0,0]\n"
"v_mfma_f32_16x16x32_bf16 a[40:43], a[236:239], v[72:75], a[40:43]\n"
"ds_read_b64_tr_b16 a[178:179], v219 offset:42256\n"
"v_fma_mix_f32 v186, v93, v162, v170 op_sel:[0,0,0] op_sel_hi:[1,0,0]\n"
"v_mfma_f32_16x16x32_bf16 a[44:47], a[232:235], v[68:71], a[44:47]\n"
"s_add_u32 m0, s51, 74528\n"
"ds_read_b64_tr_b16 a[180:181], v219 offset:45328\n"
"v_fma_mix_f32 v187, v93, v163, v171 op_sel:[1,0,0] op_sel_hi:[1,0,0]\n"
"v_mfma_f32_16x16x32_bf16 a[44:47], a[236:239], v[76:79], a[44:47]\n"
"ds_read_b64_tr_b16 a[182:183], v219 offset:46352\n"
"buffer_load_dwordx4 v217, s[20:23], s50 offen offset:256 lds\n"
"v_mfma_f32_16x16x32_bf16 a[48:51], a[240:243], v[64:67], a[48:51]\n"
"s_add_u32 s50, s50, 32768\n"
"ds_read_b64_tr_b16 a[184:185], v219 offset:41240\n"
"v_fma_mix_f32 v188, v94, v164, v172 op_sel:[0,0,0] op_sel_hi:[1,0,0]\n"
"v_mfma_f32_16x16x32_bf16 a[48:51], a[244:247], v[72:75], a[48:51]\n"
"ds_read_b64_tr_b16 a[186:187], v219 offset:42264\n"
"v_fma_mix_f32 v189, v94, v165, v173 op_sel:[1,0,0] op_sel_hi:[1,0,0]\n"
"v_mfma_f32_16x16x32_bf16 a[52:55], a[240:243], v[68:71], a[52:55]\n"
"ds_read_b64_tr_b16 a[188:189], v219 offset:45336\n"
"v_fma_mix_f32 v190, v95, v166, v174 op_sel:[0,0,0] op_sel_hi:[1,0,0]\n"
"v_mfma_f32_16x16x32_bf16 a[52:55], a[244:247], v[76:79], a[52:55]\n"
"s_waitcnt lgkmcnt(11)\n"
"ds_read_b64_tr_b16 a[190:191], v219 offset:46360\n"
"v_fma_mix_f32 v191, v95, v167, v175 op_sel:[1,0,0] op_sel_hi:[1,0,0]\n"
"v_mfma_f32_16x16x32_bf16 a[56:59], a[248:251], v[64:67], a[56:59]\n"
"s_add_u32 m0, s51, 81960\n"
"ds_read_b64_tr_b16 a[224:225], v219 offset:41488\n"
"ds_read_b128 v[160:163], v224 offset:3328\n"
"v_mfma_f32_16x16x32_bf16 a[56:59], a[252:255], v[72:75], a[56:59]\n"
"ds_read_b64_tr_b16 a[226:227], v219 offset:42512\n"
"buffer_load_dwordx4 v217, s[20:23], s50 offen offset:0 lds\n"
"v_mfma_f32_16x16x32_bf16 a[60:63], a[248:251], v[68:71], a[60:63]\n"
"ds_read_b64_tr_b16 a[228:229], v219 offset:45584\n"
"ds_read_b128 v[164:167], v224 offset:3392\n"
"v_mfma_f32_16x16x32_bf16 a[60:63], a[252:255], v[76:79], a[60:63]\n"
"ds_read_b64_tr_b16 a[230:231], v219 offset:46608\n"
"ds_read_b128 v[168:171], v224 offset:3456\n"
"v_mfma_f32_16x16x32_bf16 a[64:67], a[160:163], v[64:67], a[64:67]\n"
"ds_read_b64_tr_b16 a[232:233], v219 offset:41496\n"
"ds_read_b128 v[172:175], v224 offset:3520\n"
"v_mfma_f32_16x16x32_bf16 a[64:67], a[164:167], v[72:75], a[64:67]\n"
"ds_read_b64_tr_b16 a[234:235], v219 offset:42520\n"
"v_cvt_pk_bf16_f32 v92, v184, v185\n"
"v_mfma_f32_16x16x32_bf16 a[68:71], a[160:163], v[68:71], a[68:71]\n"
"s_add_u32 m0, s51, 89896\n"
"ds_read_b64_tr_b16 a[236:237], v219 offset:45592\n"
"v_cvt_pk_bf16_f32 v93, v186, v187\n"
"v_mfma_f32_16x16x32_bf16 a[68:71], a[164:167], v[76:79], a[68:71]\n"
"s_waitcnt lgkmcnt(14)\n"
"ds_read_b64_tr_b16 a[238:239], v219 offset:46616\n"
"buffer_load_dwordx4 v217, s[20:23], s50 offen offset:256 lds\n"
"v_mfma_f32_16x16x32_bf16 a[72:75], a[168:171], v[64:67], a[72:75]\n"
"s_add_u32 s50, s50, 32768\n"
"ds_read_b64_tr_b16 a[240:241], v219 offset:41744\n"
"v_cvt_pk_bf16_f32 v94, v188, v189\n"
"v_mfma_f32_16x16x32_bf16 a[72:75], a[172:175], v[72:75], a[72:75]\n"
"ds_read_b64_tr_b16 a[242:243], v219 offset:42768\n"
"v_cvt_pk_bf16_f32 v95, v190, v191\n"
"v_mfma_f32_16x16x32_bf16 a[76:79], a[168:171], v[68:71], a[76:79]\n"
"ds_read_b64_tr_b16 a[244:245], v219 offset:45840\n"
"v_fma_mix_f32 v176, v96, v144, v152 op_sel:[0,0,0] op_sel_hi:[1,0,0]\n"
"v_mfma_f32_16x16x32_bf16 a[76:79], a[172:175], v[76:79], a[76:79]\n"
"ds_read_b64_tr_b16 a[246:247], v219 offset:46864\n"
"v_fma_mix_f32 v177, v96, v145, v153 op_sel:[1,0,0] op_sel_hi:[1,0,0]\n"
"v_mfma_f32_16x16x32_bf16 a[80:83], a[176:179], v[64:67], a[80:83]\n"
"s_add_u32 m0, s51, 82984\n"
"ds_read_b64_tr_b16 a[248:249], v219 offset:41752\n"
"v_fma_mix_f32 v178, v97, v146, v154 op_sel:[0,0,0] op_sel_hi:[1,0,0]\n"
"v_mfma_f32_16x16x32_bf16 a[80:83], a[180:183], v[72:75], a[80:83]\n"
"ds_read_b64_tr_b16 a[250:251], v219 offset:42776\n"
"buffer_load_dwordx4 v217, s[20:23], s50 offen offset:0 lds\n"
"v_mfma_f32_16x16x32_bf16 a[84:87], a[176:179], v[68:71], a[84:87]\n"
"ds_read_b64_tr_b16 a[252:253], v219 offset:45848\n"
"v_fma_mix_f32 v179, v97, v147, v155 op_sel:[1,0,0] op_sel_hi:[1,0,0]\n"
"v_mfma_f32_16x16x32_bf16 a[84:87], a[180:183], v[76:79], a[84:87]\n"
"s_waitcnt lgkmcnt(12)\n"
"ds_read_b64_tr_b16 a[254:255], v219 offset:46872\n"
"v_fma_mix_f32 v180, v98, v148, v156 op_sel:[0,0,0] op_sel_hi:[1,0,0]\n"
"v_mfma_f32_16x16x32_bf16 a[88:91], a[184:187], v[64:67], a[88:91]\n"
"ds_read_b64_tr_b16 a[160:161], v218 offset:0\n"
"v_fma_mix_f32 v181, v98, v149, v157 op_sel:[1,0,0] op_sel_hi:[1,0,0]\n"
"v_mfma_f32_16x16x32_bf16 a[88:91], a[188:191], v[72:75], a[88:91]\n"
"ds_read_b64_tr_b16 a[162:163], v218 offset:1024\n"
"v_fma_mix_f32 v182, v99, v150, v158 op_sel:[0,0,0] op_sel_hi:[1,0,0]\n"
"v_mfma_f32_16x16x32_bf16 a[92:95], a[184:187], v[68:71], a[92:95]\n"
"s_add_u32 m0, s51, 90920\n"
"ds_read_b64_tr_b16 a[164:165], v218 offset:4096\n"
"v_fma_mix_f32 v183, v99, v151, v159 op_sel:[1,0,0] op_sel_hi:[1,0,0]\n"
"v_mfma_f32_16x16x32_bf16 a[92:95], a[188:191], v[76:79], a[92:95]\n"
"buffer_load_dwordx4 v217, s[20:23], s50 offen offset:256 lds\n"
"ds_read_b64_tr_b16 a[166:167], v218 offset:5120\n"
"v_mfma_f32_16x16x32_bf16 a[96:99], a[224:227], v[64:67], a[96:99]\n"
"s_add_u32 s50, s50, 425984\n"
"ds_read_b64_tr_b16 a[168:169], v218 offset:8\n"
"v_cvt_pk_bf16_f32 v96, v176, v177\n"
"v_mfma_f32_16x16x32_bf16 a[96:99], a[228:231], v[72:75], a[96:99]\n"
"ds_read_b64_tr_b16 a[170:171], v218 offset:1032\n"
"v_cvt_pk_bf16_f32 v97, v178, v179\n"
"v_mfma_f32_16x16x32_bf16 a[100:103], a[224:227], v[68:71], a[100:103]\n"
"ds_read_b64_tr_b16 a[172:173], v218 offset:4104\n"
"v_cvt_pk_bf16_f32 v98, v180, v181\n"
"v_mfma_f32_16x16x32_bf16 a[100:103], a[228:231], v[76:79], a[100:103]\n"
"s_waitcnt lgkmcnt(11)\n"
"ds_read_b64_tr_b16 a[174:175], v218 offset:5128\n"
"v_cvt_pk_bf16_f32 v99, v182, v183\n"
"v_mfma_f32_16x16x32_bf16 a[104:107], a[232:235], v[64:67], a[104:107]\n"
"ds_read_b64_tr_b16 a[176:177], v218 offset:256\n"
"v_fma_mix_f32 v184, v100, v144, v152 op_sel:[0,0,0] op_sel_hi:[1,0,0]\n"
"v_mfma_f32_16x16x32_bf16 a[104:107], a[236:239], v[72:75], a[104:107]\n"
"ds_read_b64_tr_b16 a[178:179], v218 offset:1280\n"
"v_fma_mix_f32 v185, v100, v145, v153 op_sel:[1,0,0] op_sel_hi:[1,0,0]\n"
"v_mfma_f32_16x16x32_bf16 a[108:111], a[232:235], v[68:71], a[108:111]\n"
"ds_read_b64_tr_b16 a[180:181], v218 offset:4352\n"
"v_fma_mix_f32 v186, v101, v146, v154 op_sel:[0,0,0] op_sel_hi:[1,0,0]\n"
"v_mfma_f32_16x16x32_bf16 a[108:111], a[236:239], v[76:79], a[108:111]\n"
"ds_read_b64_tr_b16 a[182:183], v218 offset:5376\n"
"v_fma_mix_f32 v187, v101, v147, v155 op_sel:[1,0,0] op_sel_hi:[1,0,0]\n"
"v_mfma_f32_16x16x32_bf16 a[112:115], a[240:243], v[64:67], a[112:115]\n"
"ds_read_b64_tr_b16 a[184:185], v218 offset:264\n"
"v_fma_mix_f32 v188, v102, v148, v156 op_sel:[0,0,0] op_sel_hi:[1,0,0]\n"
"v_mfma_f32_16x16x32_bf16 a[112:115], a[244:247], v[72:75], a[112:115]\n"
"ds_read_b64_tr_b16 a[186:187], v218 offset:1288\n"
"v_fma_mix_f32 v189, v102, v149, v157 op_sel:[1,0,0] op_sel_hi:[1,0,0]\n"
"v_mfma_f32_16x16x32_bf16 a[116:119], a[240:243], v[68:71], a[116:119]\n"
"ds_read_b64_tr_b16 a[188:189], v218 offset:4360\n"
"v_fma_mix_f32 v190, v103, v150, v158 op_sel:[0,0,0] op_sel_hi:[1,0,0]\n"
"v_mfma_f32_16x16x32_bf16 a[116:119], a[244:247], v[76:79], a[116:119]\n"
"s_waitcnt lgkmcnt(11)\n"
"ds_read_b64_tr_b16 a[190:191], v218 offset:5384\n"
"v_fma_mix_f32 v191, v103, v151, v159 op_sel:[1,0,0] op_sel_hi:[1,0,0]\n"
"v_mfma_f32_16x16x32_bf16 a[120:123], a[248:251], v[64:67], a[120:123]\n"
"ds_read_b64_tr_b16 a[224:225], v218 offset:512\n"
"ds_read_b128 v[144:147], v224 offset:3584\n"
"v_mfma_f32_16x16x32_bf16 a[120:123], a[252:255], v[72:75], a[120:123]\n"
"ds_read_b64_tr_b16 a[226:227], v218 offset:1536\n"
"ds_read_b128 v[148:151], v224 offset:3648\n"
"v_mfma_f32_16x16x32_bf16 a[124:127], a[248:251], v[68:71], a[124:127]\n"
"ds_read_b64_tr_b16 a[228:229], v218 offset:4608\n"
"ds_read_b128 v[152:155], v224 offset:3712\n"
"v_mfma_f32_16x16x32_bf16 a[124:127], a[252:255], v[76:79], a[124:127]\n"
"ds_read_b64_tr_b16 a[230:231], v218 offset:5632\n"
"ds_read_b128 v[156:159], v224 offset:3776\n"
"v_mfma_f32_16x16x32_bf16 a[0:3], a[160:163], v[80:83], a[0:3]\n"
"ds_read_b64_tr_b16 a[232:233], v218 offset:520\n"
"v_cvt_pk_bf16_f32 v100, v184, v185\n"
"v_mfma_f32_16x16x32_bf16 a[0:3], a[164:167], v[88:91], a[0:3]\n"
"ds_read_b64_tr_b16 a[234:235], v218 offset:1544\n"
"v_cvt_pk_bf16_f32 v101, v186, v187\n"
"v_mfma_f32_16x16x32_bf16 a[4:7], a[160:163], v[84:87], a[4:7]\n"
"s_waitcnt vmcnt(8)\n"
"s_barrier\n"
"ds_read_b64_tr_b16 a[236:237], v218 offset:4616\n"
"v_cvt_pk_bf16_f32 v102, v188, v189\n"
"v_mfma_f32_16x16x32_bf16 a[4:7], a[164:167], v[92:95], a[4:7]\n"
"s_waitcnt lgkmcnt(14)\n"
"ds_read_b64_tr_b16 a[238:239], v218 offset:5640\n"
"v_cvt_pk_bf16_f32 v103, v190, v191\n"
"v_mfma_f32_16x16x32_bf16 a[8:11], a[168:171], v[80:83], a[8:11]\n"
"s_add_u32 m0, s51, 98352\n"
"ds_read_b64_tr_b16 a[240:241], v218 offset:768\n"
"v_fma_mix_f32 v176, v104, v160, v168 op_sel:[0,0,0] op_sel_hi:[1,0,0]\n"
"v_mfma_f32_16x16x32_bf16 a[8:11], a[172:175], v[88:91], a[8:11]\n"
"ds_read_b64_tr_b16 a[242:243], v218 offset:1792\n"
"buffer_load_dwordx4 v217, s[20:23], s50 offen offset:0 lds\n"
"v_mfma_f32_16x16x32_bf16 a[12:15], a[168:171], v[84:87], a[12:15]\n"
"ds_read_b64_tr_b16 a[244:245], v218 offset:4864\n"
"v_fma_mix_f32 v177, v104, v161, v169 op_sel:[1,0,0] op_sel_hi:[1,0,0]\n"
"v_mfma_f32_16x16x32_bf16 a[12:15], a[172:175], v[92:95], a[12:15]\n"
"ds_read_b64_tr_b16 a[246:247], v218 offset:5888\n"
"v_fma_mix_f32 v178, v105, v162, v170 op_sel:[0,0,0] op_sel_hi:[1,0,0]\n"
"v_mfma_f32_16x16x32_bf16 a[16:19], a[176:179], v[80:83], a[16:19]\n"
"ds_read_b64_tr_b16 a[248:249], v218 offset:776\n"
"v_fma_mix_f32 v179, v105, v163, v171 op_sel:[1,0,0] op_sel_hi:[1,0,0]\n"
"v_mfma_f32_16x16x32_bf16 a[16:19], a[180:183], v[88:91], a[16:19]\n"
"ds_read_b64_tr_b16 a[250:251], v218 offset:1800\n"
"v_fma_mix_f32 v180, v106, v164, v172 op_sel:[0,0,0] op_sel_hi:[1,0,0]\n"
"v_mfma_f32_16x16x32_bf16 a[20:23], a[176:179], v[84:87], a[20:23]\n"
"s_add_u32 m0, s51, 106288\n"
"ds_read_b64_tr_b16 a[252:253], v218 offset:4872\n"
"v_fma_mix_f32 v181, v106, v165, v173 op_sel:[1,0,0] op_sel_hi:[1,0,0]\n"
"v_mfma_f32_16x16x32_bf16 a[20:23], a[180:183], v[92:95], a[20:23]\n"
"s_waitcnt lgkmcnt(11)\n"
"ds_read_b64_tr_b16 a[254:255], v218 offset:5896\n"
"buffer_load_dwordx4 v217, s[20:23], s50 offen offset:256 lds\n"
"v_mfma_f32_16x16x32_bf16 a[24:27], a[184:187], v[80:83], a[24:27]\n"
"s_add_u32 s50, s50, 32768\n"
"ds_read_b64_tr_b16 a[160:161], v218 offset:8192\n"
"v_fma_mix_f32 v182, v107, v166, v174 op_sel:[0,0,0] op_sel_hi:[1,0,0]\n"
"v_mfma_f32_16x16x32_bf16 a[24:27], a[188:191], v[88:91], a[24:27]\n"
"ds_read_b64_tr_b16 a[162:163], v218 offset:9216\n"
"v_fma_mix_f32 v183, v107, v167, v175 op_sel:[1,0,0] op_sel_hi:[1,0,0]\n"
"v_mfma_f32_16x16x32_bf16 a[28:31], a[184:187], v[84:87], a[28:31]\n"
"ds_read_b64_tr_b16 a[164:165], v218 offset:12288\n"
"v_cvt_pk_bf16_f32 v104, v176, v177\n"
"v_mfma_f32_16x16x32_bf16 a[28:31], a[188:191], v[92:95], a[28:31]\n"
"ds_read_b64_tr_b16 a[166:167], v218 offset:13312\n"
"v_cvt_pk_bf16_f32 v105, v178, v179\n"
"v_mfma_f32_16x16x32_bf16 a[32:35], a[224:227], v[80:83], a[32:35]\n"
"s_add_u32 m0, s51, 99376\n"
"ds_read_b64_tr_b16 a[168:169], v218 offset:8200\n"
"v_cvt_pk_bf16_f32 v106, v180, v181\n"
"v_mfma_f32_16x16x32_bf16 a[32:35], a[228:231], v[88:91], a[32:35]\n"
"ds_read_b64_tr_b16 a[170:171], v218 offset:9224\n"
"buffer_load_dwordx4 v217, s[20:23], s50 offen offset:0 lds\n"
"v_mfma_f32_16x16x32_bf16 a[36:39], a[224:227], v[84:87], a[36:39]\n"
"ds_read_b64_tr_b16 a[172:173], v218 offset:12296\n"
"v_cvt_pk_bf16_f32 v107, v182, v183\n"
"v_mfma_f32_16x16x32_bf16 a[36:39], a[228:231], v[92:95], a[36:39]\n"
"s_waitcnt lgkmcnt(11)\n"
"ds_read_b64_tr_b16 a[174:175], v218 offset:13320\n"
"v_fma_mix_f32 v184, v108, v160, v168 op_sel:[0,0,0] op_sel_hi:[1,0,0]\n"
"v_mfma_f32_16x16x32_bf16 a[40:43], a[232:235], v[80:83], a[40:43]\n"
"ds_read_b64_tr_b16 a[176:177], v218 offset:8448\n"
"v_fma_mix_f32 v185, v108, v161, v169 op_sel:[1,0,0] op_sel_hi:[1,0,0]\n"
"v_mfma_f32_16x16x32_bf16 a[40:43], a[236:239], v[88:91], a[40:43]\n"
"ds_read_b64_tr_b16 a[178:179], v218 offset:9472\n"
"v_fma_mix_f32 v186, v109, v162, v170 op_sel:[0,0,0] op_sel_hi:[1,0,0]\n"
"v_mfma_f32_16x16x32_bf16 a[44:47], a[232:235], v[84:87], a[44:47]\n"
"s_add_u32 m0, s51, 107312\n"
"ds_read_b64_tr_b16 a[180:181], v218 offset:12544\n"
"v_fma_mix_f32 v187, v109, v163, v171 op_sel:[1,0,0] op_sel_hi:[1,0,0]\n"
"v_mfma_f32_16x16x32_bf16 a[44:47], a[236:239], v[92:95], a[44:47]\n"
"ds_read_b64_tr_b16 a[182:183], v218 offset:13568\n"
"buffer_load_dwordx4 v217, s[20:23], s50 offen offset:256 lds\n"
"v_mfma_f32_16x16x32_bf16 a[48:51], a[240:243], v[80:83], a[48:51]\n"
"s_add_u32 s50, s50, 32768\n"
"ds_read_b64_tr_b16 a[184:185], v218 offset:8456\n"
"v_fma_mix_f32 v188, v110, v164, v172 op_sel:[0,0,0] op_sel_hi:[1,0,0]\n"
"v_mfma_f32_16x16x32_bf16 a[48:51], a[244:247], v[88:91], a[48:51]\n"
"ds_read_b64_tr_b16 a[186:187], v218 offset:9480\n"
"v_fma_mix_f32 v189, v110, v165, v173 op_sel:[1,0,0] op_sel_hi:[1,0,0]\n"
"v_mfma_f32_16x16x32_bf16 a[52:55], a[240:243], v[84:87], a[52:55]\n"
"ds_read_b64_tr_b16 a[188:189], v218 offset:12552\n"
"v_fma_mix_f32 v190, v111, v166, v174 op_sel:[0,0,0] op_sel_hi:[1,0,0]\n"
"v_mfma_f32_16x16x32_bf16 a[52:55], a[244:247], v[92:95], a[52:55]\n"
"s_waitcnt lgkmcnt(11)\n"
"ds_read_b64_tr_b16 a[190:191], v218 offset:13576\n"
"v_fma_mix_f32 v191, v111, v167, v175 op_sel:[1,0,0] op_sel_hi:[1,0,0]\n"
"v_mfma_f32_16x16x32_bf16 a[56:59], a[248:251], v[80:83], a[56:59]\n"
"s_add_u32 m0, s51, 114744\n"
"ds_read_b64_tr_b16 a[224:225], v218 offset:8704\n"
"ds_read_b128 v[160:163], v224 offset:3840\n"
"v_mfma_f32_16x16x32_bf16 a[56:59], a[252:255], v[88:91], a[56:59]\n"
"ds_read_b64_tr_b16 a[226:227], v218 offset:9728\n"
"buffer_load_dwordx4 v217, s[20:23], s50 offen offset:0 lds\n"
"v_mfma_f32_16x16x32_bf16 a[60:63], a[248:251], v[84:87], a[60:63]\n"
"ds_read_b64_tr_b16 a[228:229], v218 offset:12800\n"
"ds_read_b128 v[164:167], v224 offset:3904\n"
"v_mfma_f32_16x16x32_bf16 a[60:63], a[252:255], v[92:95], a[60:63]\n"
"ds_read_b64_tr_b16 a[230:231], v218 offset:13824\n"
"ds_read_b128 v[168:171], v224 offset:3968\n"
"v_mfma_f32_16x16x32_bf16 a[64:67], a[160:163], v[80:83], a[64:67]\n"
"ds_read_b64_tr_b16 a[232:233], v218 offset:8712\n"
"ds_read_b128 v[172:175], v224 offset:4032\n"
"v_mfma_f32_16x16x32_bf16 a[64:67], a[164:167], v[88:91], a[64:67]\n"
"ds_read_b64_tr_b16 a[234:235], v218 offset:9736\n"
"v_cvt_pk_bf16_f32 v108, v184, v185\n"
"v_mfma_f32_16x16x32_bf16 a[68:71], a[160:163], v[84:87], a[68:71]\n"
"s_add_u32 m0, s51, 122680\n"
"ds_read_b64_tr_b16 a[236:237], v218 offset:12808\n"
"v_cvt_pk_bf16_f32 v109, v186, v187\n"
"v_mfma_f32_16x16x32_bf16 a[68:71], a[164:167], v[92:95], a[68:71]\n"
"s_waitcnt lgkmcnt(14)\n"
"ds_read_b64_tr_b16 a[238:239], v218 offset:13832\n"
"buffer_load_dwordx4 v217, s[20:23], s50 offen offset:256 lds\n"
"v_mfma_f32_16x16x32_bf16 a[72:75], a[168:171], v[80:83], a[72:75]\n"
"s_add_u32 s50, s50, 32768\n"
"ds_read_b64_tr_b16 a[240:241], v218 offset:8960\n"
"v_cvt_pk_bf16_f32 v110, v188, v189\n"
"v_mfma_f32_16x16x32_bf16 a[72:75], a[172:175], v[88:91], a[72:75]\n"
"ds_read_b64_tr_b16 a[242:243], v218 offset:9984\n"
"v_cvt_pk_bf16_f32 v111, v190, v191\n"
"v_mfma_f32_16x16x32_bf16 a[76:79], a[168:171], v[84:87], a[76:79]\n"
"ds_read_b64_tr_b16 a[244:245], v218 offset:13056\n"
"v_fma_mix_f32 v176, v112, v144, v152 op_sel:[0,0,0] op_sel_hi:[1,0,0]\n"
"v_mfma_f32_16x16x32_bf16 a[76:79], a[172:175], v[92:95], a[76:79]\n"
"ds_read_b64_tr_b16 a[246:247], v218 offset:14080\n"
"v_fma_mix_f32 v177, v112, v145, v153 op_sel:[1,0,0] op_sel_hi:[1,0,0]\n"
"v_mfma_f32_16x16x32_bf16 a[80:83], a[176:179], v[80:83], a[80:83]\n"
"s_add_u32 m0, s51, 115768\n"
"ds_read_b64_tr_b16 a[248:249], v218 offset:8968\n"
"v_fma_mix_f32 v178, v113, v146, v154 op_sel:[0,0,0] op_sel_hi:[1,0,0]\n"
"v_mfma_f32_16x16x32_bf16 a[80:83], a[180:183], v[88:91], a[80:83]\n"
"ds_read_b64_tr_b16 a[250:251], v218 offset:9992\n"
"buffer_load_dwordx4 v217, s[20:23], s50 offen offset:0 lds\n"
"v_mfma_f32_16x16x32_bf16 a[84:87], a[176:179], v[84:87], a[84:87]\n"
"ds_read_b64_tr_b16 a[252:253], v218 offset:13064\n"
"v_fma_mix_f32 v179, v113, v147, v155 op_sel:[1,0,0] op_sel_hi:[1,0,0]\n"
"v_mfma_f32_16x16x32_bf16 a[84:87], a[180:183], v[92:95], a[84:87]\n"
"s_waitcnt lgkmcnt(12)\n"
"ds_read_b64_tr_b16 a[254:255], v218 offset:14088\n"
"v_fma_mix_f32 v180, v114, v148, v156 op_sel:[0,0,0] op_sel_hi:[1,0,0]\n"
"v_mfma_f32_16x16x32_bf16 a[88:91], a[184:187], v[80:83], a[88:91]\n"
"ds_read_b64_tr_b16 a[160:161], v218 offset:32784\n"
"v_fma_mix_f32 v181, v114, v149, v157 op_sel:[1,0,0] op_sel_hi:[1,0,0]\n"
"v_mfma_f32_16x16x32_bf16 a[88:91], a[188:191], v[88:91], a[88:91]\n"
"ds_read_b64_tr_b16 a[162:163], v218 offset:33808\n"
"v_fma_mix_f32 v182, v115, v150, v158 op_sel:[0,0,0] op_sel_hi:[1,0,0]\n"
"v_mfma_f32_16x16x32_bf16 a[92:95], a[184:187], v[84:87], a[92:95]\n"
"s_add_u32 m0, s51, 123704\n"
"ds_read_b64_tr_b16 a[164:165], v218 offset:36880\n"
"v_fma_mix_f32 v183, v115, v151, v159 op_sel:[1,0,0] op_sel_hi:[1,0,0]\n"
"v_mfma_f32_16x16x32_bf16 a[92:95], a[188:191], v[92:95], a[92:95]\n"
"buffer_load_dwordx4 v217, s[20:23], s50 offen offset:256 lds\n"
"ds_read_b64_tr_b16 a[166:167], v218 offset:37904\n"
"v_mfma_f32_16x16x32_bf16 a[96:99], a[224:227], v[80:83], a[96:99]\n"
"s_add_u32 s50, s50, 425984\n"
"ds_read_b64_tr_b16 a[168:169], v218 offset:32792\n"
"v_cvt_pk_bf16_f32 v112, v176, v177\n"
"v_mfma_f32_16x16x32_bf16 a[96:99], a[228:231], v[88:91], a[96:99]\n"
"ds_read_b64_tr_b16 a[170:171], v218 offset:33816\n"
"v_cvt_pk_bf16_f32 v113, v178, v179\n"
"v_mfma_f32_16x16x32_bf16 a[100:103], a[224:227], v[84:87], a[100:103]\n"
"ds_read_b64_tr_b16 a[172:173], v218 offset:36888\n"
"v_cvt_pk_bf16_f32 v114, v180, v181\n"
"v_mfma_f32_16x16x32_bf16 a[100:103], a[228:231], v[92:95], a[100:103]\n"
"s_waitcnt lgkmcnt(11)\n"
"ds_read_b64_tr_b16 a[174:175], v218 offset:37912\n"
"v_cvt_pk_bf16_f32 v115, v182, v183\n"
"v_mfma_f32_16x16x32_bf16 a[104:107], a[232:235], v[80:83], a[104:107]\n"
"ds_read_b64_tr_b16 a[176:177], v218 offset:33040\n"
"v_fma_mix_f32 v184, v116, v144, v152 op_sel:[0,0,0] op_sel_hi:[1,0,0]\n"
"v_mfma_f32_16x16x32_bf16 a[104:107], a[236:239], v[88:91], a[104:107]\n"
"ds_read_b64_tr_b16 a[178:179], v218 offset:34064\n"
"v_fma_mix_f32 v185, v116, v145, v153 op_sel:[1,0,0] op_sel_hi:[1,0,0]\n"
"v_mfma_f32_16x16x32_bf16 a[108:111], a[232:235], v[84:87], a[108:111]\n"
"ds_read_b64_tr_b16 a[180:181], v218 offset:37136\n"
"v_fma_mix_f32 v186, v117, v146, v154 op_sel:[0,0,0] op_sel_hi:[1,0,0]\n"
"v_mfma_f32_16x16x32_bf16 a[108:111], a[236:239], v[92:95], a[108:111]\n"
"ds_read_b64_tr_b16 a[182:183], v218 offset:38160\n"
"v_fma_mix_f32 v187, v117, v147, v155 op_sel:[1,0,0] op_sel_hi:[1,0,0]\n"
"v_mfma_f32_16x16x32_bf16 a[112:115], a[240:243], v[80:83], a[112:115]\n"
"ds_read_b64_tr_b16 a[184:185], v218 offset:33048\n"
"v_fma_mix_f32 v188, v118, v148, v156 op_sel:[0,0,0] op_sel_hi:[1,0,0]\n"
"v_mfma_f32_16x16x32_bf16 a[112:115], a[244:247], v[88:91], a[112:115]\n"
"ds_read_b64_tr_b16 a[186:187], v218 offset:34072\n"
"v_fma_mix_f32 v189, v118, v149, v157 op_sel:[1,0,0] op_sel_hi:[1,0,0]\n"
"v_mfma_f32_16x16x32_bf16 a[116:119], a[240:243], v[84:87], a[116:119]\n"
"ds_read_b64_tr_b16 a[188:189], v218 offset:37144\n"
"v_fma_mix_f32 v190, v119, v150, v158 op_sel:[0,0,0] op_sel_hi:[1,0,0]\n"
"v_mfma_f32_16x16x32_bf16 a[116:119], a[244:247], v[92:95], a[116:119]\n"
"s_waitcnt lgkmcnt(11)\n"
"ds_read_b64_tr_b16 a[190:191], v218 offset:38168\n"
"v_fma_mix_f32 v191, v119, v151, v159 op_sel:[1,0,0] op_sel_hi:[1,0,0]\n"
"v_mfma_f32_16x16x32_bf16 a[120:123], a[248:251], v[80:83], a[120:123]\n"
"ds_read_b64_tr_b16 a[224:225], v218 offset:33296\n"
"ds_read_b128 v[144:147], v224 offset:4096\n"
"v_mfma_f32_16x16x32_bf16 a[120:123], a[252:255], v[88:91], a[120:123]\n"
"ds_read_b64_tr_b16 a[226:227], v218 offset:34320\n"
"ds_read_b128 v[148:151], v224 offset:4160\n"
"v_mfma_f32_16x16x32_bf16 a[124:127], a[248:251], v[84:87], a[124:127]\n"
"ds_read_b64_tr_b16 a[228:229], v218 offset:37392\n"
"ds_read_b128 v[152:155], v224 offset:4224\n"
"v_mfma_f32_16x16x32_bf16 a[124:127], a[252:255], v[92:95], a[124:127]\n"
"ds_read_b64_tr_b16 a[230:231], v218 offset:38416\n"
"ds_read_b128 v[156:159], v224 offset:4288\n"
"v_mfma_f32_16x16x32_bf16 a[0:3], a[160:163], v[96:99], a[0:3]\n"
"ds_read_b64_tr_b16 a[232:233], v218 offset:33304\n"
"v_cvt_pk_bf16_f32 v116, v184, v185\n"
"v_mfma_f32_16x16x32_bf16 a[0:3], a[164:167], v[104:107], a[0:3]\n"
"ds_read_b64_tr_b16 a[234:235], v218 offset:34328\n"
"v_cvt_pk_bf16_f32 v117, v186, v187\n"
"v_mfma_f32_16x16x32_bf16 a[4:7], a[160:163], v[100:103], a[4:7]\n"
"s_waitcnt vmcnt(8)\n"
"s_barrier\n"
"ds_read_b64_tr_b16 a[236:237], v218 offset:37400\n"
"v_cvt_pk_bf16_f32 v118, v188, v189\n"
"v_mfma_f32_16x16x32_bf16 a[4:7], a[164:167], v[108:111], a[4:7]\n"
"s_waitcnt lgkmcnt(14)\n"
"ds_read_b64_tr_b16 a[238:239], v218 offset:38424\n"
"v_cvt_pk_bf16_f32 v119, v190, v191\n"
"v_mfma_f32_16x16x32_bf16 a[8:11], a[168:171], v[96:99], a[8:11]\n"
"s_mov_b32 m0, s86\n"
"ds_read_b64_tr_b16 a[240:241], v218 offset:33552\n"
"v_fma_mix_f32 v176, v120, v160, v168 op_sel:[0,0,0] op_sel_hi:[1,0,0]\n"
"v_mfma_f32_16x16x32_bf16 a[8:11], a[172:175], v[104:107], a[8:11]\n"
"ds_read_b64_tr_b16 a[242:243], v218 offset:34576\n"
"buffer_load_dwordx4 v246, s[8:11], s84 offen offset:0 lds\n"
"v_mfma_f32_16x16x32_bf16 a[12:15], a[168:171], v[100:103], a[12:15]\n"
"ds_read_b64_tr_b16 a[244:245], v218 offset:37648\n"
"v_fma_mix_f32 v177, v120, v161, v169 op_sel:[1,0,0] op_sel_hi:[1,0,0]\n"
"v_mfma_f32_16x16x32_bf16 a[12:15], a[172:175], v[108:111], a[12:15]\n"
"ds_read_b64_tr_b16 a[246:247], v218 offset:38672\n"
"v_fma_mix_f32 v178, v121, v162, v170 op_sel:[0,0,0] op_sel_hi:[1,0,0]\n"
"v_mfma_f32_16x16x32_bf16 a[16:19], a[176:179], v[96:99], a[16:19]\n"
"ds_read_b64_tr_b16 a[248:249], v218 offset:33560\n"
"v_fma_mix_f32 v179, v121, v163, v171 op_sel:[1,0,0] op_sel_hi:[1,0,0]\n"
"v_mfma_f32_16x16x32_bf16 a[16:19], a[180:183], v[104:107], a[16:19]\n"
"ds_read_b64_tr_b16 a[250:251], v218 offset:34584\n"
"v_fma_mix_f32 v180, v122, v164, v172 op_sel:[0,0,0] op_sel_hi:[1,0,0]\n"
"v_mfma_f32_16x16x32_bf16 a[20:23], a[176:179], v[100:103], a[20:23]\n"
"ds_read_b64_tr_b16 a[252:253], v218 offset:37656\n"
"v_fma_mix_f32 v181, v122, v165, v173 op_sel:[1,0,0] op_sel_hi:[1,0,0]\n"
"v_mfma_f32_16x16x32_bf16 a[20:23], a[180:183], v[108:111], a[20:23]\n"
"s_waitcnt lgkmcnt(11)\n"
"ds_read_b64_tr_b16 a[254:255], v218 offset:38680\n"
"buffer_load_dwordx4 v246, s[8:11], s84 offen offset:1024 lds\n"
"v_mfma_f32_16x16x32_bf16 a[24:27], a[184:187], v[96:99], a[24:27]\n"
"ds_read_b64_tr_b16 a[160:161], v218 offset:40976\n"
"v_fma_mix_f32 v182, v123, v166, v174 op_sel:[0,0,0] op_sel_hi:[1,0,0]\n"
"v_mfma_f32_16x16x32_bf16 a[24:27], a[188:191], v[104:107], a[24:27]\n"
"ds_read_b64_tr_b16 a[162:163], v218 offset:42000\n"
"v_fma_mix_f32 v183, v123, v167, v175 op_sel:[1,0,0] op_sel_hi:[1,0,0]\n"
"v_mfma_f32_16x16x32_bf16 a[28:31], a[184:187], v[100:103], a[28:31]\n"
"ds_read_b64_tr_b16 a[164:165], v218 offset:45072\n"
"v_cvt_pk_bf16_f32 v120, v176, v177\n"
"v_mfma_f32_16x16x32_bf16 a[28:31], a[188:191], v[108:111], a[28:31]\n"
"ds_read_b64_tr_b16 a[166:167], v218 offset:46096\n"
"v_cvt_pk_bf16_f32 v121, v178, v179\n"
"v_mfma_f32_16x16x32_bf16 a[32:35], a[224:227], v[96:99], a[32:35]\n"
"ds_read_b64_tr_b16 a[168:169], v218 offset:40984\n"
"v_cvt_pk_bf16_f32 v122, v180, v181\n"
"v_mfma_f32_16x16x32_bf16 a[32:35], a[228:231], v[104:107], a[32:35]\n"
"ds_read_b64_tr_b16 a[170:171], v218 offset:42008\n"
"buffer_load_dwordx4 v247, s[8:11], s84 offen offset:2048 lds\n"
"v_mfma_f32_16x16x32_bf16 a[36:39], a[224:227], v[100:103], a[36:39]\n"
"ds_read_b64_tr_b16 a[172:173], v218 offset:45080\n"
"v_cvt_pk_bf16_f32 v123, v182, v183\n"
"v_mfma_f32_16x16x32_bf16 a[36:39], a[228:231], v[108:111], a[36:39]\n"
"s_waitcnt lgkmcnt(11)\n"
"ds_read_b64_tr_b16 a[174:175], v218 offset:46104\n"
"v_fma_mix_f32 v184, v124, v160, v168 op_sel:[0,0,0] op_sel_hi:[1,0,0]\n"
"v_mfma_f32_16x16x32_bf16 a[40:43], a[232:235], v[96:99], a[40:43]\n"
"ds_read_b64_tr_b16 a[176:177], v218 offset:41232\n"
"v_fma_mix_f32 v185, v124, v161, v169 op_sel:[1,0,0] op_sel_hi:[1,0,0]\n"
"v_mfma_f32_16x16x32_bf16 a[40:43], a[236:239], v[104:107], a[40:43]\n"
"ds_read_b64_tr_b16 a[178:179], v218 offset:42256\n"
"v_fma_mix_f32 v186, v125, v162, v170 op_sel:[0,0,0] op_sel_hi:[1,0,0]\n"
"v_mfma_f32_16x16x32_bf16 a[44:47], a[232:235], v[100:103], a[44:47]\n"
"ds_read_b64_tr_b16 a[180:181], v218 offset:45328\n"
"v_fma_mix_f32 v187, v125, v163, v171 op_sel:[1,0,0] op_sel_hi:[1,0,0]\n"
"v_mfma_f32_16x16x32_bf16 a[44:47], a[236:239], v[108:111], a[44:47]\n"
"ds_read_b64_tr_b16 a[182:183], v218 offset:46352\n"
"buffer_load_dwordx4 v247, s[8:11], s84 offen offset:3072 lds\n"
"v_mfma_f32_16x16x32_bf16 a[48:51], a[240:243], v[96:99], a[48:51]\n"
"ds_read_b64_tr_b16 a[184:185], v218 offset:41240\n"
"v_fma_mix_f32 v188, v126, v164, v172 op_sel:[0,0,0] op_sel_hi:[1,0,0]\n"
"v_mfma_f32_16x16x32_bf16 a[48:51], a[244:247], v[104:107], a[48:51]\n"
"ds_read_b64_tr_b16 a[186:187], v218 offset:42264\n"
"v_fma_mix_f32 v189, v126, v165, v173 op_sel:[1,0,0] op_sel_hi:[1,0,0]\n"
"v_mfma_f32_16x16x32_bf16 a[52:55], a[240:243], v[100:103], a[52:55]\n"
"ds_read_b64_tr_b16 a[188:189], v218 offset:45336\n"
"v_fma_mix_f32 v190, v127, v166, v174 op_sel:[0,0,0] op_sel_hi:[1,0,0]\n"
"v_mfma_f32_16x16x32_bf16 a[52:55], a[244:247], v[108:111], a[52:55]\n"
"s_waitcnt lgkmcnt(11)\n"
"ds_read_b64_tr_b16 a[190:191], v218 offset:46360\n"
"v_fma_mix_f32 v191, v127, v167, v175 op_sel:[1,0,0] op_sel_hi:[1,0,0]\n"
"v_mfma_f32_16x16x32_bf16 a[56:59], a[248:251], v[96:99], a[56:59]\n"
"s_add_u32 m0, s86, 16384\n"
"ds_read_b64_tr_b16 a[224:225], v218 offset:41488\n"
"ds_read_b128 v[160:163], v224 offset:4352\n"
"v_mfma_f32_16x16x32_bf16 a[56:59], a[252:255], v[104:107], a[56:59]\n"
"ds_read_b64_tr_b16 a[226:227], v218 offset:42512\n"
"buffer_load_dwordx4 v246, s[12:15], s84 offen offset:0 lds\n"
"v_mfma_f32_16x16x32_bf16 a[60:63], a[248:251], v[100:103], a[60:63]\n"
"ds_read_b64_tr_b16 a[228:229], v218 offset:45584\n"
"ds_read_b128 v[164:167], v224 offset:4416\n"
"v_mfma_f32_16x16x32_bf16 a[60:63], a[252:255], v[108:111], a[60:63]\n"
"ds_read_b64_tr_b16 a[230:231], v218 offset:46608\n"
"ds_read_b128 v[168:171], v224 offset:4480\n"
"v_mfma_f32_16x16x32_bf16 a[64:67], a[160:163], v[96:99], a[64:67]\n"
"ds_read_b64_tr_b16 a[232:233], v218 offset:41496\n"
"ds_read_b128 v[172:175], v224 offset:4544\n"
"v_mfma_f32_16x16x32_bf16 a[64:67], a[164:167], v[104:107], a[64:67]\n"
"ds_read_b64_tr_b16 a[234:235], v218 offset:42520\n"
"v_cvt_pk_bf16_f32 v124, v184, v185\n"
"v_mfma_f32_16x16x32_bf16 a[68:71], a[160:163], v[100:103], a[68:71]\n"
"ds_read_b64_tr_b16 a[236:237], v218 offset:45592\n"
"v_cvt_pk_bf16_f32 v125, v186, v187\n"
"v_mfma_f32_16x16x32_bf16 a[68:71], a[164:167], v[108:111], a[68:71]\n"
"s_waitcnt lgkmcnt(14)\n"
"ds_read_b64_tr_b16 a[238:239], v218 offset:46616\n"
"buffer_load_dwordx4 v247, s[12:15], s84 offen offset:1024 lds\n"
"v_mfma_f32_16x16x32_bf16 a[72:75], a[168:171], v[96:99], a[72:75]\n"
"ds_read_b64_tr_b16 a[240:241], v218 offset:41744\n"
"v_cvt_pk_bf16_f32 v126, v188, v189\n"
"v_mfma_f32_16x16x32_bf16 a[72:75], a[172:175], v[104:107], a[72:75]\n"
"ds_read_b64_tr_b16 a[242:243], v218 offset:42768\n"
"v_cvt_pk_bf16_f32 v127, v190, v191\n"
"v_mfma_f32_16x16x32_bf16 a[76:79], a[168:171], v[100:103], a[76:79]\n"
"ds_read_b64_tr_b16 a[244:245], v218 offset:45840\n"
"v_fma_mix_f32 v176, v128, v144, v152 op_sel:[0,0,0] op_sel_hi:[1,0,0]\n"
"v_mfma_f32_16x16x32_bf16 a[76:79], a[172:175], v[108:111], a[76:79]\n"
"ds_read_b64_tr_b16 a[246:247], v218 offset:46864\n"
"v_fma_mix_f32 v177, v128, v145, v153 op_sel:[1,0,0] op_sel_hi:[1,0,0]\n"
"v_mfma_f32_16x16x32_bf16 a[80:83], a[176:179], v[96:99], a[80:83]\n"
"ds_read_b64_tr_b16 a[248:249], v218 offset:41752\n"
"v_fma_mix_f32 v178, v129, v146, v154 op_sel:[0,0,0] op_sel_hi:[1,0,0]\n"
"v_mfma_f32_16x16x32_bf16 a[80:83], a[180:183], v[104:107], a[80:83]\n"
"ds_read_b64_tr_b16 a[250:251], v218 offset:42776\n"
"buffer_load_dwordx4 v246, s[12:15], s84 offen offset:2048 lds\n"
"v_mfma_f32_16x16x32_bf16 a[84:87], a[176:179], v[100:103], a[84:87]\n"
"ds_read_b64_tr_b16 a[252:253], v218 offset:45848\n"
"v_fma_mix_f32 v179, v129, v147, v155 op_sel:[1,0,0] op_sel_hi:[1,0,0]\n"
"v_mfma_f32_16x16x32_bf16 a[84:87], a[180:183], v[108:111], a[84:87]\n"
"s_waitcnt lgkmcnt(12)\n"
"ds_read_b64_tr_b16 a[254:255], v218 offset:46872\n"
"v_fma_mix_f32 v180, v130, v148, v156 op_sel:[0,0,0] op_sel_hi:[1,0,0]\n"
"v_mfma_f32_16x16x32_bf16 a[88:91], a[184:187], v[96:99], a[88:91]\n"
"ds_read_b64_tr_b16 a[160:161], v219 offset:0\n"
"v_fma_mix_f32 v181, v130, v149, v157 op_sel:[1,0,0] op_sel_hi:[1,0,0]\n"
"v_mfma_f32_16x16x32_bf16 a[88:91], a[188:191], v[104:107], a[88:91]\n"
"ds_read_b64_tr_b16 a[162:163], v219 offset:1024\n"
"v_fma_mix_f32 v182, v131, v150, v158 op_sel:[0,0,0] op_sel_hi:[1,0,0]\n"
"v_mfma_f32_16x16x32_bf16 a[92:95], a[184:187], v[100:103], a[92:95]\n"
"ds_read_b64_tr_b16 a[164:165], v219 offset:4096\n"
"v_fma_mix_f32 v183, v131, v151, v159 op_sel:[1,0,0] op_sel_hi:[1,0,0]\n"
"v_mfma_f32_16x16x32_bf16 a[92:95], a[188:191], v[108:111], a[92:95]\n"
"buffer_load_dwordx4 v247, s[12:15], s84 offen offset:3072 lds\n"
"ds_read_b64_tr_b16 a[166:167], v219 offset:5120\n"
"v_mfma_f32_16x16x32_bf16 a[96:99], a[224:227], v[96:99], a[96:99]\n"
"s_add_u32 s84, s84, 16384\n"
"ds_read_b64_tr_b16 a[168:169], v219 offset:8\n"
"v_cvt_pk_bf16_f32 v128, v176, v177\n"
"v_mfma_f32_16x16x32_bf16 a[96:99], a[228:231], v[104:107], a[96:99]\n"
"ds_read_b64_tr_b16 a[170:171], v219 offset:1032\n"
"v_cvt_pk_bf16_f32 v129, v178, v179\n"
"v_mfma_f32_16x16x32_bf16 a[100:103], a[224:227], v[100:103], a[100:103]\n"
"ds_read_b64_tr_b16 a[172:173], v219 offset:4104\n"
"v_cvt_pk_bf16_f32 v130, v180, v181\n"
"v_mfma_f32_16x16x32_bf16 a[100:103], a[228:231], v[108:111], a[100:103]\n"
"s_waitcnt lgkmcnt(11)\n"
"ds_read_b64_tr_b16 a[174:175], v219 offset:5128\n"
"v_cvt_pk_bf16_f32 v131, v182, v183\n"
"v_mfma_f32_16x16x32_bf16 a[104:107], a[232:235], v[96:99], a[104:107]\n"
"ds_read_b64_tr_b16 a[176:177], v219 offset:256\n"
"v_fma_mix_f32 v184, v132, v144, v152 op_sel:[0,0,0] op_sel_hi:[1,0,0]\n"
"v_mfma_f32_16x16x32_bf16 a[104:107], a[236:239], v[104:107], a[104:107]\n"
"ds_read_b64_tr_b16 a[178:179], v219 offset:1280\n"
"v_fma_mix_f32 v185, v132, v145, v153 op_sel:[1,0,0] op_sel_hi:[1,0,0]\n"
"v_mfma_f32_16x16x32_bf16 a[108:111], a[232:235], v[100:103], a[108:111]\n"
"ds_read_b64_tr_b16 a[180:181], v219 offset:4352\n"
"v_fma_mix_f32 v186, v133, v146, v154 op_sel:[0,0,0] op_sel_hi:[1,0,0]\n"
"v_mfma_f32_16x16x32_bf16 a[108:111], a[236:239], v[108:111], a[108:111]\n"
"ds_read_b64_tr_b16 a[182:183], v219 offset:5376\n"
"v_fma_mix_f32 v187, v133, v147, v155 op_sel:[1,0,0] op_sel_hi:[1,0,0]\n"
"v_mfma_f32_16x16x32_bf16 a[112:115], a[240:243], v[96:99], a[112:115]\n"
"ds_read_b64_tr_b16 a[184:185], v219 offset:264\n"
"v_fma_mix_f32 v188, v134, v148, v156 op_sel:[0,0,0] op_sel_hi:[1,0,0]\n"
"v_mfma_f32_16x16x32_bf16 a[112:115], a[244:247], v[104:107], a[112:115]\n"
"ds_read_b64_tr_b16 a[186:187], v219 offset:1288\n"
"v_fma_mix_f32 v189, v134, v149, v157 op_sel:[1,0,0] op_sel_hi:[1,0,0]\n"
"v_mfma_f32_16x16x32_bf16 a[116:119], a[240:243], v[100:103], a[116:119]\n"
"ds_read_b64_tr_b16 a[188:189], v219 offset:4360\n"
"v_fma_mix_f32 v190, v135, v150, v158 op_sel:[0,0,0] op_sel_hi:[1,0,0]\n"
"v_mfma_f32_16x16x32_bf16 a[116:119], a[244:247], v[108:111], a[116:119]\n"
"s_waitcnt lgkmcnt(11)\n"
"ds_read_b64_tr_b16 a[190:191], v219 offset:5384\n"
"v_fma_mix_f32 v191, v135, v151, v159 op_sel:[1,0,0] op_sel_hi:[1,0,0]\n"
"v_mfma_f32_16x16x32_bf16 a[120:123], a[248:251], v[96:99], a[120:123]\n"
"ds_read_b64_tr_b16 a[224:225], v219 offset:512\n"
"v_cvt_pk_bf16_f32 v132, v184, v185\n"
"v_mfma_f32_16x16x32_bf16 a[120:123], a[252:255], v[104:107], a[120:123]\n"
"ds_read_b64_tr_b16 a[226:227], v219 offset:1536\n"
"v_cvt_pk_bf16_f32 v133, v186, v187\n"
"v_mfma_f32_16x16x32_bf16 a[124:127], a[248:251], v[100:103], a[124:127]\n"
"ds_read_b64_tr_b16 a[228:229], v219 offset:4608\n"
"v_cvt_pk_bf16_f32 v134, v188, v189\n"
"v_mfma_f32_16x16x32_bf16 a[124:127], a[252:255], v[108:111], a[124:127]\n"
"ds_read_b64_tr_b16 a[230:231], v219 offset:5632\n"
"v_cvt_pk_bf16_f32 v135, v190, v191\n"
"v_mfma_f32_16x16x32_bf16 a[0:3], a[160:163], v[112:115], a[0:3]\n"
"ds_read_b64_tr_b16 a[232:233], v219 offset:520\n"
"v_fma_mix_f32 v176, v136, v160, v168 op_sel:[0,0,0] op_sel_hi:[1,0,0]\n"
"v_mfma_f32_16x16x32_bf16 a[0:3], a[164:167], v[120:123], a[0:3]\n"
"ds_read_b64_tr_b16 a[234:235], v219 offset:1544\n"
"v_fma_mix_f32 v177, v136, v161, v169 op_sel:[1,0,0] op_sel_hi:[1,0,0]\n"
"v_mfma_f32_16x16x32_bf16 a[4:7], a[160:163], v[116:119], a[4:7]\n"
"s_waitcnt vmcnt(8)\n"
"s_barrier\n"
"ds_read_b64_tr_b16 a[236:237], v219 offset:4616\n"
"v_fma_mix_f32 v178, v137, v162, v170 op_sel:[0,0,0] op_sel_hi:[1,0,0]\n"
"v_mfma_f32_16x16x32_bf16 a[4:7], a[164:167], v[124:127], a[4:7]\n"
"s_waitcnt lgkmcnt(11)\n"
"ds_read_b64_tr_b16 a[238:239], v219 offset:5640\n"
"v_fma_mix_f32 v179, v137, v163, v171 op_sel:[1,0,0] op_sel_hi:[1,0,0]\n"
"v_mfma_f32_16x16x32_bf16 a[8:11], a[168:171], v[112:115], a[8:11]\n"
"s_add_u32 m0, s86, 32768\n"
"ds_read_b64_tr_b16 a[240:241], v219 offset:768\n"
"v_fma_mix_f32 v180, v138, v164, v172 op_sel:[0,0,0] op_sel_hi:[1,0,0]\n"
"v_mfma_f32_16x16x32_bf16 a[8:11], a[172:175], v[120:123], a[8:11]\n"
"ds_read_b64_tr_b16 a[242:243], v219 offset:1792\n"
"buffer_load_dwordx4 v246, s[8:11], s84 offen offset:0 lds\n"
"v_mfma_f32_16x16x32_bf16 a[12:15], a[168:171], v[116:119], a[12:15]\n"
"ds_read_b64_tr_b16 a[244:245], v219 offset:4864\n"
"v_fma_mix_f32 v181, v138, v165, v173 op_sel:[1,0,0] op_sel_hi:[1,0,0]\n"
"v_mfma_f32_16x16x32_bf16 a[12:15], a[172:175], v[124:127], a[12:15]\n"
"ds_read_b64_tr_b16 a[246:247], v219 offset:5888\n"
"v_fma_mix_f32 v182, v139, v166, v174 op_sel:[0,0,0] op_sel_hi:[1,0,0]\n"
"v_mfma_f32_16x16x32_bf16 a[16:19], a[176:179], v[112:115], a[16:19]\n"
"ds_read_b64_tr_b16 a[248:249], v219 offset:776\n"
"v_fma_mix_f32 v183, v139, v167, v175 op_sel:[1,0,0] op_sel_hi:[1,0,0]\n"
"v_mfma_f32_16x16x32_bf16 a[16:19], a[180:183], v[120:123], a[16:19]\n"
"ds_read_b64_tr_b16 a[250:251], v219 offset:1800\n"
"v_cvt_pk_bf16_f32 v136, v176, v177\n"
"v_mfma_f32_16x16x32_bf16 a[20:23], a[176:179], v[116:119], a[20:23]\n"
"ds_read_b64_tr_b16 a[252:253], v219 offset:4872\n"
"v_cvt_pk_bf16_f32 v137, v178, v179\n"
"v_mfma_f32_16x16x32_bf16 a[20:23], a[180:183], v[124:127], a[20:23]\n"
"s_waitcnt lgkmcnt(11)\n"
"ds_read_b64_tr_b16 a[254:255], v219 offset:5896\n"
"buffer_load_dwordx4 v246, s[8:11], s84 offen offset:1024 lds\n"
"v_mfma_f32_16x16x32_bf16 a[24:27], a[184:187], v[112:115], a[24:27]\n"
"ds_read_b64_tr_b16 a[160:161], v219 offset:8192\n"
"v_cvt_pk_bf16_f32 v138, v180, v181\n"
"v_mfma_f32_16x16x32_bf16 a[24:27], a[188:191], v[120:123], a[24:27]\n"
"ds_read_b64_tr_b16 a[162:163], v219 offset:9216\n"
"v_cvt_pk_bf16_f32 v139, v182, v183\n"
"v_mfma_f32_16x16x32_bf16 a[28:31], a[184:187], v[116:119], a[28:31]\n"
"ds_read_b64_tr_b16 a[164:165], v219 offset:12288\n"
"v_fma_mix_f32 v184, v140, v160, v168 op_sel:[0,0,0] op_sel_hi:[1,0,0]\n"
"v_mfma_f32_16x16x32_bf16 a[28:31], a[188:191], v[124:127], a[28:31]\n"
"ds_read_b64_tr_b16 a[166:167], v219 offset:13312\n"
"v_fma_mix_f32 v185, v140, v161, v169 op_sel:[1,0,0] op_sel_hi:[1,0,0]\n"
"v_mfma_f32_16x16x32_bf16 a[32:35], a[224:227], v[112:115], a[32:35]\n"
"ds_read_b64_tr_b16 a[168:169], v219 offset:8200\n"
"v_fma_mix_f32 v186, v141, v162, v170 op_sel:[0,0,0] op_sel_hi:[1,0,0]\n"
"v_mfma_f32_16x16x32_bf16 a[32:35], a[228:231], v[120:123], a[32:35]\n"
"ds_read_b64_tr_b16 a[170:171], v219 offset:9224\n"
"buffer_load_dwordx4 v247, s[8:11], s84 offen offset:2048 lds\n"
"v_mfma_f32_16x16x32_bf16 a[36:39], a[224:227], v[116:119], a[36:39]\n"
"ds_read_b64_tr_b16 a[172:173], v219 offset:12296\n"
"v_fma_mix_f32 v187, v141, v163, v171 op_sel:[1,0,0] op_sel_hi:[1,0,0]\n"
"v_mfma_f32_16x16x32_bf16 a[36:39], a[228:231], v[124:127], a[36:39]\n"
"s_waitcnt lgkmcnt(11)\n"
"ds_read_b64_tr_b16 a[174:175], v219 offset:13320\n"
"v_fma_mix_f32 v188, v142, v164, v172 op_sel:[0,0,0] op_sel_hi:[1,0,0]\n"
"v_mfma_f32_16x16x32_bf16 a[40:43], a[232:235], v[112:115], a[40:43]\n"
"ds_read_b64_tr_b16 a[176:177], v219 offset:8448\n"
"v_fma_mix_f32 v189, v142, v165, v173 op_sel:[1,0,0] op_sel_hi:[1,0,0]\n"
"v_mfma_f32_16x16x32_bf16 a[40:43], a[236:239], v[120:123], a[40:43]\n"
"ds_read_b64_tr_b16 a[178:179], v219 offset:9472\n"
"v_fma_mix_f32 v190, v143, v166, v174 op_sel:[0,0,0] op_sel_hi:[1,0,0]\n"
"v_mfma_f32_16x16x32_bf16 a[44:47], a[232:235], v[116:119], a[44:47]\n"
"ds_read_b64_tr_b16 a[180:181], v219 offset:12544\n"
"v_fma_mix_f32 v191, v143, v167, v175 op_sel:[1,0,0] op_sel_hi:[1,0,0]\n"
"v_mfma_f32_16x16x32_bf16 a[44:47], a[236:239], v[124:127], a[44:47]\n"
"ds_read_b64_tr_b16 a[182:183], v219 offset:13568\n"
"buffer_load_dwordx4 v247, s[8:11], s84 offen offset:3072 lds\n"
"v_mfma_f32_16x16x32_bf16 a[48:51], a[240:243], v[112:115], a[48:51]\n"
"ds_read_b64_tr_b16 a[184:185], v219 offset:8456\n"
"v_cvt_pk_bf16_f32 v140, v184, v185\n"
"v_mfma_f32_16x16x32_bf16 a[48:51], a[244:247], v[120:123], a[48:51]\n"
"ds_read_b64_tr_b16 a[186:187], v219 offset:9480\n"
"v_cvt_pk_bf16_f32 v141, v186, v187\n"
"v_mfma_f32_16x16x32_bf16 a[52:55], a[240:243], v[116:119], a[52:55]\n"
"ds_read_b64_tr_b16 a[188:189], v219 offset:12552\n"
"v_cvt_pk_bf16_f32 v142, v188, v189\n"
"v_mfma_f32_16x16x32_bf16 a[52:55], a[244:247], v[124:127], a[52:55]\n"
"s_waitcnt lgkmcnt(11)\n"
"ds_read_b64_tr_b16 a[190:191], v219 offset:13576\n"
"v_cvt_pk_bf16_f32 v143, v190, v191\n"
"v_mfma_f32_16x16x32_bf16 a[56:59], a[248:251], v[112:115], a[56:59]\n"
"s_add_u32 m0, s86, 49152\n"
"ds_read_b64_tr_b16 a[224:225], v219 offset:8704\n"
"v_mfma_f32_16x16x32_bf16 a[56:59], a[252:255], v[120:123], a[56:59]\n"
"ds_read_b64_tr_b16 a[226:227], v219 offset:9728\n"
"buffer_load_dwordx4 v246, s[12:15], s84 offen offset:0 lds\n"
"v_mfma_f32_16x16x32_bf16 a[60:63], a[248:251], v[116:119], a[60:63]\n"
"ds_read_b64_tr_b16 a[228:229], v219 offset:12800\n"
"v_mfma_f32_16x16x32_bf16 a[60:63], a[252:255], v[124:127], a[60:63]\n"
"ds_read_b64_tr_b16 a[230:231], v219 offset:13824\n"
"v_mfma_f32_16x16x32_bf16 a[64:67], a[160:163], v[112:115], a[64:67]\n"
"ds_read_b64_tr_b16 a[232:233], v219 offset:8712\n"
"v_mfma_f32_16x16x32_bf16 a[64:67], a[164:167], v[120:123], a[64:67]\n"
"ds_read_b64_tr_b16 a[234:235], v219 offset:9736\n"
"v_mfma_f32_16x16x32_bf16 a[68:71], a[160:163], v[116:119], a[68:71]\n"
"ds_read_b64_tr_b16 a[236:237], v219 offset:12808\n"
"v_mfma_f32_16x16x32_bf16 a[68:71], a[164:167], v[124:127], a[68:71]\n"
"s_waitcnt lgkmcnt(11)\n"
"ds_read_b64_tr_b16 a[238:239], v219 offset:13832\n"
"buffer_load_dwordx4 v247, s[12:15], s84 offen offset:1024 lds\n"
"v_mfma_f32_16x16x32_bf16 a[72:75], a[168:171], v[112:115], a[72:75]\n"
"ds_read_b64_tr_b16 a[240:241], v219 offset:8960\n"
"v_mfma_f32_16x16x32_bf16 a[72:75], a[172:175], v[120:123], a[72:75]\n"
"ds_read_b64_tr_b16 a[242:243], v219 offset:9984\n"
"v_mfma_f32_16x16x32_bf16 a[76:79], a[168:171], v[116:119], a[76:79]\n"
"ds_read_b64_tr_b16 a[244:245], v219 offset:13056\n"
"v_mfma_f32_16x16x32_bf16 a[76:79], a[172:175], v[124:127], a[76:79]\n"
"ds_read_b64_tr_b16 a[246:247], v219 offset:14080\n"
"v_mfma_f32_16x16x32_bf16 a[80:83], a[176:179], v[112:115], a[80:83]\n"
"ds_read_b64_tr_b16 a[248:249], v219 offset:8968\n"
"v_mfma_f32_16x16x32_bf16 a[80:83], a[180:183], v[120:123], a[80:83]\n"
"ds_read_b64_tr_b16 a[250:251], v219 offset:9992\n"
"buffer_load_dwordx4 v246, s[12:15], s84 offen offset:2048 lds\n"
"v_mfma_f32_16x16x32_bf16 a[84:87], a[176:179], v[116:119], a[84:87]\n"
"ds_read_b64_tr_b16 a[252:253], v219 offset:13064\n"
"v_mfma_f32_16x16x32_bf16 a[84:87], a[180:183], v[124:127], a[84:87]\n"
"s_waitcnt lgkmcnt(11)\n"
"ds_read_b64_tr_b16 a[254:255], v219 offset:14088\n"
"v_mfma_f32_16x16x32_bf16 a[88:91], a[184:187], v[112:115], a[88:91]\n"
"ds_read_b64_tr_b16 a[160:161], v219 offset:32784\n"
"v_mfma_f32_16x16x32_bf16 a[88:91], a[188:191], v[120:123], a[88:91]\n"
"ds_read_b64_tr_b16 a[162:163], v219 offset:33808\n"
"v_mfma_f32_16x16x32_bf16 a[92:95], a[184:187], v[116:119], a[92:95]\n"
"ds_read_b64_tr_b16 a[164:165], v219 offset:36880\n"
"v_mfma_f32_16x16x32_bf16 a[92:95], a[188:191], v[124:127], a[92:95]\n"
"buffer_load_dwordx4 v247, s[12:15], s84 offen offset:3072 lds\n"
"ds_read_b64_tr_b16 a[166:167], v219 offset:37904\n"
"v_mfma_f32_16x16x32_bf16 a[96:99], a[224:227], v[112:115], a[96:99]\n"
"s_add_u32 s84, s84, 16384\n"
"ds_read_b64_tr_b16 a[168:169], v219 offset:32792\n"
"v_mfma_f32_16x16x32_bf16 a[96:99], a[228:231], v[120:123], a[96:99]\n"
"ds_read_b64_tr_b16 a[170:171], v219 offset:33816\n"
"v_mfma_f32_16x16x32_bf16 a[100:103], a[224:227], v[116:119], a[100:103]\n"
"ds_read_b64_tr_b16 a[172:173], v219 offset:36888\n"
"v_mfma_f32_16x16x32_bf16 a[100:103], a[228:231], v[124:127], a[100:103]\n"
"s_waitcnt lgkmcnt(11)\n"
"ds_read_b64_tr_b16 a[174:175], v219 offset:37912\n"
"v_mfma_f32_16x16x32_bf16 a[104:107], a[232:235], v[112:115], a[104:107]\n"
"ds_read_b64_tr_b16 a[176:177], v219 offset:33040\n"
"v_mfma_f32_16x16x32_bf16 a[104:107], a[236:239], v[120:123], a[104:107]\n"
"ds_read_b64_tr_b16 a[178:179], v219 offset:34064\n"
"v_mfma_f32_16x16x32_bf16 a[108:111], a[232:235], v[116:119], a[108:111]\n"
"ds_read_b64_tr_b16 a[180:181], v219 offset:37136\n"
"v_mfma_f32_16x16x32_bf16 a[108:111], a[236:239], v[124:127], a[108:111]\n"
"ds_read_b64_tr_b16 a[182:183], v219 offset:38160\n"
"v_mfma_f32_16x16x32_bf16 a[112:115], a[240:243], v[112:115], a[112:115]\n"
"ds_read_b64_tr_b16 a[184:185], v219 offset:33048\n"
"v_mfma_f32_16x16x32_bf16 a[112:115], a[244:247], v[120:123], a[112:115]\n"
"ds_read_b64_tr_b16 a[186:187], v219 offset:34072\n"
"v_mfma_f32_16x16x32_bf16 a[116:119], a[240:243], v[116:119], a[116:119]\n"
"ds_read_b64_tr_b16 a[188:189], v219 offset:37144\n"
"v_mfma_f32_16x16x32_bf16 a[116:119], a[244:247], v[124:127], a[116:119]\n"
"s_waitcnt lgkmcnt(11)\n"
"ds_read_b64_tr_b16 a[190:191], v219 offset:38168\n"
"v_mfma_f32_16x16x32_bf16 a[120:123], a[248:251], v[112:115], a[120:123]\n"
"ds_read_b64_tr_b16 a[224:225], v219 offset:33296\n"
"v_mfma_f32_16x16x32_bf16 a[120:123], a[252:255], v[120:123], a[120:123]\n"
"ds_read_b64_tr_b16 a[226:227], v219 offset:34320\n"
"v_mfma_f32_16x16x32_bf16 a[124:127], a[248:251], v[116:119], a[124:127]\n"
"ds_read_b64_tr_b16 a[228:229], v219 offset:37392\n"
"v_mfma_f32_16x16x32_bf16 a[124:127], a[252:255], v[124:127], a[124:127]\n"
"ds_read_b64_tr_b16 a[230:231], v219 offset:38416\n"
"v_mfma_f32_16x16x32_bf16 a[0:3], a[160:163], v[128:131], a[0:3]\n"
"ds_read_b64_tr_b16 a[232:233], v219 offset:33304\n"
"v_mfma_f32_16x16x32_bf16 a[0:3], a[164:167], v[136:139], a[0:3]\n"
"ds_read_b64_tr_b16 a[234:235], v219 offset:34328\n"
"v_mfma_f32_16x16x32_bf16 a[4:7], a[160:163], v[132:135], a[4:7]\n"
"s_waitcnt vmcnt(8)\n"
"s_barrier\n"
"ds_read_b64_tr_b16 a[236:237], v219 offset:37400\n"
"v_mfma_f32_16x16x32_bf16 a[4:7], a[164:167], v[140:143], a[4:7]\n"
"s_waitcnt lgkmcnt(11)\n"
"ds_read_b64_tr_b16 a[238:239], v219 offset:38424\n"
"v_mfma_f32_16x16x32_bf16 a[8:11], a[168:171], v[128:131], a[8:11]\n"
"s_add_u32 m0, s86, 65536\n"
"ds_read_b64_tr_b16 a[240:241], v219 offset:33552\n"
"v_mfma_f32_16x16x32_bf16 a[8:11], a[172:175], v[136:139], a[8:11]\n"
"ds_read_b64_tr_b16 a[242:243], v219 offset:34576\n"
"buffer_load_dwordx4 v246, s[8:11], s84 offen offset:0 lds\n"
"v_mfma_f32_16x16x32_bf16 a[12:15], a[168:171], v[132:135], a[12:15]\n"
"ds_read_b64_tr_b16 a[244:245], v219 offset:37648\n"
"v_accvgpr_read_b32 v176, a0\n"
"v_mfma_f32_16x16x32_bf16 a[12:15], a[172:175], v[140:143], a[12:15]\n"
"ds_read_b64_tr_b16 a[246:247], v219 offset:38672\n"
"v_accvgpr_read_b32 v177, a1\n"
"v_mfma_f32_16x16x32_bf16 a[16:19], a[176:179], v[128:131], a[16:19]\n"
"ds_read_b64_tr_b16 a[248:249], v219 offset:33560\n"
"v_accvgpr_read_b32 v178, a2\n"
"v_mfma_f32_16x16x32_bf16 a[16:19], a[180:183], v[136:139], a[16:19]\n"
"ds_read_b64_tr_b16 a[250:251], v219 offset:34584\n"
"v_accvgpr_read_b32 v179, a3\n"
"v_mfma_f32_16x16x32_bf16 a[20:23], a[176:179], v[132:135], a[20:23]\n"
"ds_read_b64_tr_b16 a[252:253], v219 offset:37656\n"
"v_cvt_pk_bf16_f32 v64, v176, v177\n"
"v_mfma_f32_16x16x32_bf16 a[20:23], a[180:183], v[140:143], a[20:23]\n"
"s_waitcnt lgkmcnt(11)\n"
"ds_read_b64_tr_b16 a[254:255], v219 offset:38680\n"
"buffer_load_dwordx4 v246, s[8:11], s84 offen offset:1024 lds\n"
"v_mfma_f32_16x16x32_bf16 a[24:27], a[184:187], v[128:131], a[24:27]\n"
"ds_read_b64_tr_b16 a[160:161], v219 offset:40976\n"
"v_cvt_pk_bf16_f32 v65, v178, v179\n"
"v_mfma_f32_16x16x32_bf16 a[24:27], a[188:191], v[136:139], a[24:27]\n"
"ds_read_b64_tr_b16 a[162:163], v219 offset:42000\n"
"v_accvgpr_read_b32 v180, a4\n"
"v_mfma_f32_16x16x32_bf16 a[28:31], a[184:187], v[132:135], a[28:31]\n"
"ds_read_b64_tr_b16 a[164:165], v219 offset:45072\n"
"v_accvgpr_read_b32 v181, a5\n"
"v_mfma_f32_16x16x32_bf16 a[28:31], a[188:191], v[140:143], a[28:31]\n"
"ds_read_b64_tr_b16 a[166:167], v219 offset:46096\n"
"v_accvgpr_read_b32 v182, a6\n"
"v_mfma_f32_16x16x32_bf16 a[32:35], a[224:227], v[128:131], a[32:35]\n"
"ds_read_b64_tr_b16 a[168:169], v219 offset:40984\n"
"v_accvgpr_read_b32 v183, a7\n"
"v_mfma_f32_16x16x32_bf16 a[32:35], a[228:231], v[136:139], a[32:35]\n"
"ds_read_b64_tr_b16 a[170:171], v219 offset:42008\n"
"buffer_load_dwordx4 v247, s[8:11], s84 offen offset:2048 lds\n"
"v_mfma_f32_16x16x32_bf16 a[36:39], a[224:227], v[132:135], a[36:39]\n"
"ds_read_b64_tr_b16 a[172:173], v219 offset:45080\n"
"v_cvt_pk_bf16_f32 v96, v180, v181\n"
"v_mfma_f32_16x16x32_bf16 a[36:39], a[228:231], v[140:143], a[36:39]\n"
"s_waitcnt lgkmcnt(11)\n"
"ds_read_b64_tr_b16 a[174:175], v219 offset:46104\n"
"v_cvt_pk_bf16_f32 v97, v182, v183\n"
"v_mfma_f32_16x16x32_bf16 a[40:43], a[232:235], v[128:131], a[40:43]\n"
"ds_read_b64_tr_b16 a[176:177], v219 offset:41232\n"
"v_accvgpr_read_b32 v184, a8\n"
"v_mfma_f32_16x16x32_bf16 a[40:43], a[236:239], v[136:139], a[40:43]\n"
"ds_read_b64_tr_b16 a[178:179], v219 offset:42256\n"
"v_accvgpr_read_b32 v185, a9\n"
"v_mfma_f32_16x16x32_bf16 a[44:47], a[232:235], v[132:135], a[44:47]\n"
"ds_read_b64_tr_b16 a[180:181], v219 offset:45328\n"
"v_accvgpr_read_b32 v186, a10\n"
"v_mfma_f32_16x16x32_bf16 a[44:47], a[236:239], v[140:143], a[44:47]\n"
"ds_read_b64_tr_b16 a[182:183], v219 offset:46352\n"
"buffer_load_dwordx4 v247, s[8:11], s84 offen offset:3072 lds\n"
"v_mfma_f32_16x16x32_bf16 a[48:51], a[240:243], v[128:131], a[48:51]\n"
"ds_read_b64_tr_b16 a[184:185], v219 offset:41240\n"
"v_accvgpr_read_b32 v187, a11\n"
"v_mfma_f32_16x16x32_bf16 a[48:51], a[244:247], v[136:139], a[48:51]\n"
"ds_read_b64_tr_b16 a[186:187], v219 offset:42264\n"
"v_cvt_pk_bf16_f32 v66, v184, v185\n"
"v_mfma_f32_16x16x32_bf16 a[52:55], a[240:243], v[132:135], a[52:55]\n"
"ds_read_b64_tr_b16 a[188:189], v219 offset:45336\n"
"v_cvt_pk_bf16_f32 v67, v186, v187\n"
"v_mfma_f32_16x16x32_bf16 a[52:55], a[244:247], v[140:143], a[52:55]\n"
"s_waitcnt lgkmcnt(11)\n"
"ds_read_b64_tr_b16 a[190:191], v219 offset:46360\n"
"v_accvgpr_read_b32 v188, a12\n"
"v_mfma_f32_16x16x32_bf16 a[56:59], a[248:251], v[128:131], a[56:59]\n"
"s_add_u32 m0, s86, 81920\n"
"ds_read_b64_tr_b16 a[224:225], v219 offset:41488\n"
"v_accvgpr_read_b32 v189, a13\n"
"v_mfma_f32_16x16x32_bf16 a[56:59], a[252:255], v[136:139], a[56:59]\n"
"ds_read_b64_tr_b16 a[226:227], v219 offset:42512\n"
"buffer_load_dwordx4 v246, s[12:15], s84 offen offset:0 lds\n"
"v_mfma_f32_16x16x32_bf16 a[60:63], a[248:251], v[132:135], a[60:63]\n"
"ds_read_b64_tr_b16 a[228:229], v219 offset:45584\n"
"v_accvgpr_read_b32 v190, a14\n"
"v_mfma_f32_16x16x32_bf16 a[60:63], a[252:255], v[140:143], a[60:63]\n"
"ds_read_b64_tr_b16 a[230:231], v219 offset:46608\n"
"v_accvgpr_read_b32 v191, a15\n"
"v_mfma_f32_16x16x32_bf16 a[64:67], a[160:163], v[128:131], a[64:67]\n"
"ds_read_b64_tr_b16 a[232:233], v219 offset:41496\n"
"v_cvt_pk_bf16_f32 v98, v188, v189\n"
"v_mfma_f32_16x16x32_bf16 a[64:67], a[164:167], v[136:139], a[64:67]\n"
"ds_read_b64_tr_b16 a[234:235], v219 offset:42520\n"
"v_cvt_pk_bf16_f32 v99, v190, v191\n"
"v_mfma_f32_16x16x32_bf16 a[68:71], a[160:163], v[132:135], a[68:71]\n"
"ds_read_b64_tr_b16 a[236:237], v219 offset:45592\n"
"v_accvgpr_read_b32 v176, a16\n"
"v_mfma_f32_16x16x32_bf16 a[68:71], a[164:167], v[140:143], a[68:71]\n"
"s_waitcnt lgkmcnt(11)\n"
"ds_read_b64_tr_b16 a[238:239], v219 offset:46616\n"
"buffer_load_dwordx4 v247, s[12:15], s84 offen offset:1024 lds\n"
"v_mfma_f32_16x16x32_bf16 a[72:75], a[168:171], v[128:131], a[72:75]\n"
"ds_read_b64_tr_b16 a[240:241], v219 offset:41744\n"
"v_accvgpr_read_b32 v177, a17\n"
"v_mfma_f32_16x16x32_bf16 a[72:75], a[172:175], v[136:139], a[72:75]\n"
"ds_read_b64_tr_b16 a[242:243], v219 offset:42768\n"
"v_accvgpr_read_b32 v178, a18\n"
"v_mfma_f32_16x16x32_bf16 a[76:79], a[168:171], v[132:135], a[76:79]\n"
"ds_read_b64_tr_b16 a[244:245], v219 offset:45840\n"
"v_accvgpr_read_b32 v179, a19\n"
"v_mfma_f32_16x16x32_bf16 a[76:79], a[172:175], v[140:143], a[76:79]\n"
"ds_read_b64_tr_b16 a[246:247], v219 offset:46864\n"
"v_cvt_pk_bf16_f32 v68, v176, v177\n"
"v_mfma_f32_16x16x32_bf16 a[80:83], a[176:179], v[128:131], a[80:83]\n"
"ds_read_b64_tr_b16 a[248:249], v219 offset:41752\n"
"v_cvt_pk_bf16_f32 v69, v178, v179\n"
"v_mfma_f32_16x16x32_bf16 a[80:83], a[180:183], v[136:139], a[80:83]\n"
"ds_read_b64_tr_b16 a[250:251], v219 offset:42776\n"
"buffer_load_dwordx4 v246, s[12:15], s84 offen offset:2048 lds\n"
"v_mfma_f32_16x16x32_bf16 a[84:87], a[176:179], v[132:135], a[84:87]\n"
"ds_read_b64_tr_b16 a[252:253], v219 offset:45848\n"
"v_accvgpr_read_b32 v180, a20\n"
"v_mfma_f32_16x16x32_bf16 a[84:87], a[180:183], v[140:143], a[84:87]\n"
"s_waitcnt lgkmcnt(11)\n"
"ds_read_b64_tr_b16 a[254:255], v219 offset:46872\n"
"v_accvgpr_read_b32 v181, a21\n"
"v_mfma_f32_16x16x32_bf16 a[88:91], a[184:187], v[128:131], a[88:91]\n"
"ds_read_b128 a[128:131], v240 offset:0\n"
"v_accvgpr_read_b32 v182, a22\n"
"v_mfma_f32_16x16x32_bf16 a[88:91], a[188:191], v[136:139], a[88:91]\n"
"ds_read_b128 a[132:135], v240 offset:256\n"
"v_accvgpr_read_b32 v183, a23\n"
"v_mfma_f32_16x16x32_bf16 a[92:95], a[184:187], v[132:135], a[92:95]\n"
"ds_read_b128 a[136:139], v240 offset:512\n"
"v_cvt_pk_bf16_f32 v100, v180, v181\n"
"v_mfma_f32_16x16x32_bf16 a[92:95], a[188:191], v[140:143], a[92:95]\n"
"buffer_load_dwordx4 v247, s[12:15], s84 offen offset:3072 lds\n"
"ds_read_b128 a[140:143], v240 offset:768\n"
"v_mfma_f32_16x16x32_bf16 a[96:99], a[224:227], v[128:131], a[96:99]\n"
"s_add_u32 s84, s84, 16384\n"
"ds_read_b128 a[144:147], v240 offset:4096\n"
"v_cvt_pk_bf16_f32 v101, v182, v183\n"
"v_mfma_f32_16x16x32_bf16 a[96:99], a[228:231], v[136:139], a[96:99]\n"
"ds_read_b128 a[148:151], v240 offset:4352\n"
"v_accvgpr_read_b32 v184, a24\n"
"v_mfma_f32_16x16x32_bf16 a[100:103], a[224:227], v[132:135], a[100:103]\n"
"ds_read_b128 a[152:155], v240 offset:4608\n"
"v_accvgpr_read_b32 v185, a25\n"
"v_mfma_f32_16x16x32_bf16 a[100:103], a[228:231], v[140:143], a[100:103]\n"
"s_waitcnt lgkmcnt(11)\n"
"ds_read_b128 a[156:159], v240 offset:4864\n"
"v_accvgpr_read_b32 v186, a26\n"
"v_mfma_f32_16x16x32_bf16 a[104:107], a[232:235], v[128:131], a[104:107]\n"
"ds_read_b64_tr_b16 a[192:193], v242 offset:16384\n"
"v_accvgpr_read_b32 v187, a27\n"
"v_mfma_f32_16x16x32_bf16 a[104:107], a[236:239], v[136:139], a[104:107]\n"
"ds_read_b64_tr_b16 a[194:195], v242 offset:20480\n"
"v_cvt_pk_bf16_f32 v70, v184, v185\n"
"v_mfma_f32_16x16x32_bf16 a[108:111], a[232:235], v[132:135], a[108:111]\n"
"ds_read_b64_tr_b16 a[196:197], v243 offset:16384\n"
"v_cvt_pk_bf16_f32 v71, v186, v187\n"
"v_mfma_f32_16x16x32_bf16 a[108:111], a[236:239], v[140:143], a[108:111]\n"
"ds_read_b64_tr_b16 a[198:199], v243 offset:20480\n"
"v_accvgpr_read_b32 v188, a28\n"
"v_mfma_f32_16x16x32_bf16 a[112:115], a[240:243], v[128:131], a[112:115]\n"
"ds_read_b64_tr_b16 a[200:201], v242 offset:16640\n"
"v_accvgpr_read_b32 v189, a29\n"
"v_mfma_f32_16x16x32_bf16 a[112:115], a[244:247], v[136:139], a[112:115]\n"
"ds_read_b64_tr_b16 a[202:203], v242 offset:20736\n"
"v_accvgpr_read_b32 v190, a30\n"
"v_mfma_f32_16x16x32_bf16 a[116:119], a[240:243], v[132:135], a[116:119]\n"
"ds_read_b64_tr_b16 a[204:205], v243 offset:16640\n"
"v_accvgpr_read_b32 v191, a31\n"
"v_mfma_f32_16x16x32_bf16 a[116:119], a[244:247], v[140:143], a[116:119]\n"
"s_waitcnt lgkmcnt(11)\n"
"ds_read_b64_tr_b16 a[206:207], v243 offset:20736\n"
"v_cvt_pk_bf16_f32 v102, v188, v189\n"
"v_mfma_f32_16x16x32_bf16 a[120:123], a[248:251], v[128:131], a[120:123]\n"
"ds_read_b64_tr_b16 a[208:209], v242 offset:16896\n"
"v_cvt_pk_bf16_f32 v103, v190, v191\n"
"v_mfma_f32_16x16x32_bf16 a[120:123], a[252:255], v[136:139], a[120:123]\n"
"ds_read_b64_tr_b16 a[210:211], v242 offset:20992\n"
"v_accvgpr_read_b32 v176, a32\n"
"v_mfma_f32_16x16x32_bf16 a[124:127], a[248:251], v[132:135], a[124:127]\n"
"ds_read_b64_tr_b16 a[212:213], v243 offset:16896\n"
"v_accvgpr_read_b32 v177, a33\n"
"v_mfma_f32_16x16x32_bf16 a[124:127], a[252:255], v[140:143], a[124:127]\n"
"ds_read_b64_tr_b16 a[214:215], v243 offset:20992\n"
"v_accvgpr_read_b32 v178, a34\n"
"ds_read_b64_tr_b16 a[216:217], v242 offset:17152\n"
"v_accvgpr_read_b32 v179, a35\n"
"v_cvt_pk_bf16_f32 v72, v176, v177\n"
"v_cvt_pk_bf16_f32 v73, v178, v179\n"
"ds_read_b64_tr_b16 a[218:219], v242 offset:21248\n"
"v_accvgpr_read_b32 v180, a36\n"
"v_accvgpr_read_b32 v181, a37\n"
"v_accvgpr_read_b32 v182, a38\n"
"ds_read_b64_tr_b16 a[220:221], v243 offset:17152\n"
"v_accvgpr_read_b32 v183, a39\n"
"v_cvt_pk_bf16_f32 v104, v180, v181\n"
"v_cvt_pk_bf16_f32 v105, v182, v183\n"
"ds_read_b64_tr_b16 a[222:223], v243 offset:21248\n"
"v_accvgpr_read_b32 v184, a40\n"
"v_accvgpr_read_b32 v185, a41\n"
"v_accvgpr_read_b32 v186, a42\n"
"v_accvgpr_read_b32 v187, a43\n"
"v_cvt_pk_bf16_f32 v74, v184, v185\n"
"v_cvt_pk_bf16_f32 v75, v186, v187\n"
"v_accvgpr_read_b32 v188, a44\n"
"v_accvgpr_read_b32 v189, a45\n"
"v_accvgpr_read_b32 v190, a46\n"
"v_accvgpr_read_b32 v191, a47\n"
"v_cvt_pk_bf16_f32 v106, v188, v189\n"
"v_cvt_pk_bf16_f32 v107, v190, v191\n"
"v_accvgpr_read_b32 v176, a48\n"
"v_accvgpr_read_b32 v177, a49\n"
"v_accvgpr_read_b32 v178, a50\n"
"v_accvgpr_read_b32 v179, a51\n"
"v_cvt_pk_bf16_f32 v76, v176, v177\n"
"v_cvt_pk_bf16_f32 v77, v178, v179\n"
"v_accvgpr_read_b32 v180, a52\n"
"v_accvgpr_read_b32 v181, a53\n"
"v_accvgpr_read_b32 v182, a54\n"
"v_accvgpr_read_b32 v183, a55\n"
"v_cvt_pk_bf16_f32 v108, v180, v181\n"
"v_cvt_pk_bf16_f32 v109, v182, v183\n"
"v_accvgpr_read_b32 v184, a56\n"
"v_accvgpr_read_b32 v185, a57\n"
"v_accvgpr_read_b32 v186, a58\n"
"v_accvgpr_read_b32 v187, a59\n"
"v_cvt_pk_bf16_f32 v78, v184, v185\n"
"v_cvt_pk_bf16_f32 v79, v186, v187\n"
"v_accvgpr_read_b32 v188, a60\n"
"v_accvgpr_read_b32 v189, a61\n"
"v_accvgpr_read_b32 v190, a62\n"
"v_accvgpr_read_b32 v191, a63\n"
"v_cvt_pk_bf16_f32 v110, v188, v189\n"
"v_cvt_pk_bf16_f32 v111, v190, v191\n"
"v_accvgpr_read_b32 v176, a64\n"
"v_accvgpr_read_b32 v177, a65\n"
"v_accvgpr_read_b32 v178, a66\n"
"v_accvgpr_read_b32 v179, a67\n"
"v_cvt_pk_bf16_f32 v80, v176, v177\n"
"v_cvt_pk_bf16_f32 v81, v178, v179\n"
"v_accvgpr_read_b32 v180, a68\n"
"v_accvgpr_read_b32 v181, a69\n"
"v_accvgpr_read_b32 v182, a70\n"
"v_accvgpr_read_b32 v183, a71\n"
"v_cvt_pk_bf16_f32 v112, v180, v181\n"
"v_cvt_pk_bf16_f32 v113, v182, v183\n"
"v_accvgpr_read_b32 v184, a72\n"
"v_accvgpr_read_b32 v185, a73\n"
"v_accvgpr_read_b32 v186, a74\n"
"v_accvgpr_read_b32 v187, a75\n"
"v_cvt_pk_bf16_f32 v82, v184, v185\n"
"v_cvt_pk_bf16_f32 v83, v186, v187\n"
"v_accvgpr_read_b32 v188, a76\n"
"v_accvgpr_read_b32 v189, a77\n"
"v_accvgpr_read_b32 v190, a78\n"
"v_accvgpr_read_b32 v191, a79\n"
"v_cvt_pk_bf16_f32 v114, v188, v189\n"
"v_cvt_pk_bf16_f32 v115, v190, v191\n"
"v_accvgpr_read_b32 v176, a80\n"
"v_accvgpr_read_b32 v177, a81\n"
"v_accvgpr_read_b32 v178, a82\n"
"v_accvgpr_read_b32 v179, a83\n"
"v_cvt_pk_bf16_f32 v84, v176, v177\n"
"v_cvt_pk_bf16_f32 v85, v178, v179\n"
"v_accvgpr_read_b32 v180, a84\n"
"v_accvgpr_read_b32 v181, a85\n"
"v_accvgpr_read_b32 v182, a86\n"
"v_accvgpr_read_b32 v183, a87\n"
"v_cvt_pk_bf16_f32 v116, v180, v181\n"
"v_cvt_pk_bf16_f32 v117, v182, v183\n"
"v_accvgpr_read_b32 v184, a88\n"
"v_accvgpr_read_b32 v185, a89\n"
"v_accvgpr_read_b32 v186, a90\n"
"v_accvgpr_read_b32 v187, a91\n"
"v_cvt_pk_bf16_f32 v86, v184, v185\n"
"v_cvt_pk_bf16_f32 v87, v186, v187\n"
"v_accvgpr_read_b32 v188, a92\n"
"v_accvgpr_read_b32 v189, a93\n"
"v_accvgpr_read_b32 v190, a94\n"
"v_accvgpr_read_b32 v191, a95\n"
"v_cvt_pk_bf16_f32 v118, v188, v189\n"
"v_cvt_pk_bf16_f32 v119, v190, v191\n"
"v_accvgpr_read_b32 v176, a96\n"
"v_accvgpr_read_b32 v177, a97\n"
"v_accvgpr_read_b32 v178, a98\n"
"v_accvgpr_read_b32 v179, a99\n"
"v_cvt_pk_bf16_f32 v88, v176, v177\n"
"v_cvt_pk_bf16_f32 v89, v178, v179\n"
"v_accvgpr_read_b32 v180, a100\n"
"v_accvgpr_read_b32 v181, a101\n"
"v_accvgpr_read_b32 v182, a102\n"
"v_accvgpr_read_b32 v183, a103\n"
"v_cvt_pk_bf16_f32 v120, v180, v181\n"
"v_cvt_pk_bf16_f32 v121, v182, v183\n"
"v_accvgpr_read_b32 v184, a104\n"
"v_accvgpr_read_b32 v185, a105\n"
"v_accvgpr_read_b32 v186, a106\n"
"v_accvgpr_read_b32 v187, a107\n"
"v_cvt_pk_bf16_f32 v90, v184, v185\n"
"v_cvt_pk_bf16_f32 v91, v186, v187\n"
"v_accvgpr_read_b32 v188, a108\n"
"v_accvgpr_read_b32 v189, a109\n"
"v_accvgpr_read_b32 v190, a110\n"
"v_accvgpr_read_b32 v191, a111\n"
"v_cvt_pk_bf16_f32 v122, v188, v189\n"
"v_cvt_pk_bf16_f32 v123, v190, v191\n"
"v_accvgpr_read_b32 v176, a112\n"
"v_accvgpr_read_b32 v177, a113\n"
"v_accvgpr_read_b32 v178, a114\n"
"v_accvgpr_read_b32 v179, a115\n"
"v_cvt_pk_bf16_f32 v92, v176, v177\n"
"v_cvt_pk_bf16_f32 v93, v178, v179\n"
"v_accvgpr_read_b32 v180, a116\n"
"v_accvgpr_read_b32 v181, a117\n"
"v_accvgpr_read_b32 v182, a118\n"
"v_accvgpr_read_b32 v183, a119\n"
"v_cvt_pk_bf16_f32 v124, v180, v181\n"
"v_cvt_pk_bf16_f32 v125, v182, v183\n"
"v_accvgpr_read_b32 v184, a120\n"
"v_accvgpr_read_b32 v185, a121\n"
"v_accvgpr_read_b32 v186, a122\n"
"v_accvgpr_read_b32 v187, a123\n"
"v_cvt_pk_bf16_f32 v94, v184, v185\n"
"v_cvt_pk_bf16_f32 v95, v186, v187\n"
"v_accvgpr_read_b32 v188, a124\n"
"v_accvgpr_read_b32 v189, a125\n"
"v_accvgpr_read_b32 v190, a126\n"
"v_accvgpr_read_b32 v191, a127\n"
"v_cvt_pk_bf16_f32 v126, v188, v189\n"
"v_cvt_pk_bf16_f32 v127, v190, v191\n"
"s_barrier\n"
"s_add_u32 s56, s40, 98304\n"
"s_lshr_b32 s57, s42, 3\n"
"s_lshl_b32 s57, s57, 2\n"
"s_add_u32 s57, s57, s41\n"
"s_sub_u32 s57, s57, 256\n"
"s_and_b32 s58, s42, 7\n"
"s_mul_i32 s59, s57, 9216\n"
"s_mul_i32 s60, s58, 1152\n"
"s_add_u32 s59, s59, s60\n"
"s_mul_i32 s60, s57, 1152\n"
"s_lshl_b32 s61, s60, 1\n"
"s_mul_i32 s46, s58, 663552\n"
"s_lshr_b32 s47, s43, 2\n"
"s_mul_i32 s47, s47, 1179648\n"
"s_nop 1\n"
"v_mfma_f32_16x16x32_bf16 v[128:131], a[128:131], v[64:67], 0\n"
"s_add_u32 m0, s86, 98304\n"
"v_and_b32 v232, 1, v226\n"
"v_lshlrev_b32 v206, 6, v232\n"
"v_mfma_f32_16x16x32_bf16 v[128:131], a[132:135], v[68:71], v[128:131]\n"
"buffer_load_dwordx4 v246, s[8:11], s84 offen offset:0 lds\n"
"v_lshlrev_b32 v200, 5, v232\n"
"v_mfma_f32_16x16x32_bf16 v[128:131], a[136:139], v[72:75], v[128:131]\n"
"v_and_b32 v232, 2, v226\n"
"v_xor_b32 v233, v232, v228\n"
"v_mfma_f32_16x16x32_bf16 v[128:131], a[140:143], v[76:79], v[128:131]\n"
"s_waitcnt lgkmcnt(12)\n"
"buffer_load_dwordx4 v246, s[8:11], s84 offen offset:1024 lds\n"
"v_lshl_add_u32 v206, v233, 4, v206\n"
"v_mfma_f32_16x16x32_bf16 v[132:135], a[144:147], v[64:67], 0\n"
"v_lshl_add_u32 v232, v232, 1, v227\n"
"v_mul_u32_u24 v232, 1152, v232\n"
"v_mfma_f32_16x16x32_bf16 v[132:135], a[148:151], v[68:71], v[132:135]\n"
"buffer_load_dwordx4 v247, s[8:11], s84 offen offset:2048 lds\n"
"v_add_u32 v206, v232, v206\n"
"v_mfma_f32_16x16x32_bf16 v[132:135], a[152:155], v[72:75], v[132:135]\n"
"v_lshl_add_u32 v200, v226, 9, v200\n"
"v_lshl_add_u32 v200, v227, 6, v200\n"
"v_mfma_f32_16x16x32_bf16 v[132:135], a[156:159], v[76:79], v[132:135]\n"
"buffer_load_dwordx4 v247, s[8:11], s84 offen offset:3072 lds\n"
"v_lshl_add_u32 v200, v228, 3, v200\n"
"v_mfma_f32_16x16x32_bf16 v[136:139], a[128:131], v[80:83], 0\n"
"s_add_u32 m0, s86, 114688\n"
"v_add_u32 v200, s40, v200\n"
"v_xor_b32 v203, 32, v200\n"
"v_mfma_f32_16x16x32_bf16 v[136:139], a[132:135], v[84:87], v[136:139]\n"
"buffer_load_dwordx4 v246, s[12:15], s84 offen offset:0 lds\n"
"v_add_u32 v201, 65536, v200\n"
"v_mfma_f32_16x16x32_bf16 v[136:139], a[136:139], v[88:91], v[136:139]\n"
"v_add_u32 v204, 65536, v203\n"
"v_add_u32 v202, 131072, v200\n"
"v_mfma_f32_16x16x32_bf16 v[136:139], a[140:143], v[92:95], v[136:139]\n"
"buffer_load_dwordx4 v247, s[12:15], s84 offen offset:1024 lds\n"
"v_add_u32 v205, 131072, v203\n"
"v_mfma_f32_16x16x32_bf16 v[140:143], a[144:147], v[80:83], 0\n"
"v_lshlrev_b32 v207, 4, v248\n"
"v_add_u32 v208, 18432, v207\n"
"v_mfma_f32_16x16x32_bf16 v[140:143], a[148:151], v[84:87], v[140:143]\n"
"buffer_load_dwordx4 v246, s[12:15], s84 offen offset:2048 lds\n"
"v_lshl_add_u32 v209, v228, 2, s56\n"
"v_mfma_f32_16x16x32_bf16 v[140:143], a[152:155], v[88:91], v[140:143]\n"
"v_lshlrev_b32 v216, 6, v248\n"
"v_add_u32 v217, 0x40000000, v216\n"
"v_mfma_f32_16x16x32_bf16 v[140:143], a[156:159], v[92:95], v[140:143]\n"
"buffer_load_dwordx4 v247, s[12:15], s84 offen offset:3072 lds\n"
"v_cmp_gt_u32 vcc, 18, v248\n"
"v_mfma_f32_16x16x32_bf16 v[144:147], a[128:131], v[96:99], 0\n"
"s_add_u32 s84, s84, 16384\n"
"s_add_u32 m0, s53, 131072\n"
"v_cmp_gt_u32 s[62:63], 36, v248\n"
"v_nop\n"
"v_mfma_f32_16x16x32_bf16 v[144:147], a[132:135], v[100:103], v[144:147]\n"
"buffer_load_dwordx4 v206, s[24:27], s52 offen offset:0 lds\n"
"v_cndmask_b32 v215, v217, v216, vcc\n"
"v_mfma_f32_16x16x32_bf16 v[144:147], a[136:139], v[104:107], v[144:147]\n"
"s_add_u32 s52, s52, 8192\n"
"v_cndmask_b32 v217, v217, v216, s[62:63]\n"
"v_add_u32 v216, s60, v215\n"
"v_mfma_f32_16x16x32_bf16 v[144:147], a[140:143], v[108:111], v[144:147]\n"
"buffer_load_dwordx4 v206, s[24:27], s52 offen offset:1024 lds\n"
"v_add_u32 v215, s59, v215\n"
"v_mfma_f32_16x16x32_bf16 v[148:151], a[144:147], v[96:99], 0\n"
"s_add_u32 s52, s52, 8192\n"
"v_add_u32 v217, s61, v217\n"
"v_mov_b32 v40, 0x3f803f80\n"
"v_mfma_f32_16x16x32_bf16 v[148:151], a[148:151], v[100:103], v[148:151]\n"
"buffer_load_dwordx4 v206, s[24:27], s52 offen offset:2048 lds\n"
"v_mov_b32 v41, 0x3f803f80\n"
"v_mfma_f32_16x16x32_bf16 v[148:151], a[152:155], v[104:107], v[148:151]\n"
"s_add_u32 s52, s52, 8192\n"
"v_mov_b32 v42, 0x3f803f80\n"
"v_mov_b32 v43, 0x3f803f80\n"
"v_mfma_f32_16x16x32_bf16 v[148:151], a[156:159], v[108:111], v[148:151]\n"
"buffer_load_dwordx4 v206, s[24:27], s52 offen offset:3072 lds\n"
"v_mov_b32 v56, 0\n"
"v_mfma_f32_16x16x32_bf16 v[152:155], a[128:131], v[112:115], 0\n"
"s_add_u32 s52, s52, 12288\n"
"s_add_u32 m0, s53, 135168\n"
"v_mov_b32 v57, 0\n"
"v_mov_b32 v58, 0\n"
"v_mfma_f32_16x16x32_bf16 v[152:155], a[132:135], v[116:119], v[152:155]\n"
"buffer_load_dwordx4 v206, s[24:27], s52 offen offset:0 lds\n"
"v_mov_b32 v59, 0\n"
"v_mfma_f32_16x16x32_bf16 v[152:155], a[136:139], v[120:123], v[152:155]\n"
"s_add_u32 s52, s52, 8192\n"
"v_accvgpr_write_b32 a96, 0\n"
"v_accvgpr_write_b32 a97, 0\n"
"v_mfma_f32_16x16x32_bf16 v[152:155], a[140:143], v[124:127], v[152:155]\n"
"buffer_load_dwordx4 v206, s[24:27], s52 offen offset:1024 lds\n"
"v_accvgpr_write_b32 a98, 0\n"
"v_mfma_f32_16x16x32_bf16 v[156:159], a[144:147], v[112:115], 0\n"
"s_add_u32 s52, s52, 8192\n"
"v_accvgpr_write_b32 a99, 0\n"
"v_accvgpr_write_b32 a100, 0\n"
"v_mfma_f32_16x16x32_bf16 v[156:159], a[148:151], v[116:119], v[156:159]\n"
"buffer_load_dwordx4 v206, s[24:27], s52 offen offset:2048 lds\n"
"v_accvgpr_write_b32 a101, 0\n"
"v_mfma_f32_16x16x32_bf16 v[156:159], a[152:155], v[120:123], v[156:159]\n"
"s_add_u32 s52, s52, 8192\n"
"v_accvgpr_write_b32 a102, 0\n"
"v_accvgpr_write_b32 a103, 0\n"
"v_mfma_f32_16x16x32_bf16 v[156:159], a[156:159], v[124:127], v[156:159]\n"
"buffer_load_dwordx4 v206, s[24:27], s52 offen offset:3072 lds\n"
"v_accvgpr_write_b32 a104, 0\n"
"s_sub_u32 s52, s52, 61312\n"
"v_accvgpr_write_b32 a105, 0\n"
"v_accvgpr_write_b32 a106, 0\n"
"v_accvgpr_write_b32 a107, 0\n"
"v_accvgpr_write_b32 a108, 0\n"
"v_accvgpr_write_b32 a109, 0\n"
"v_accvgpr_write_b32 a110, 0\n"
"v_accvgpr_write_b32 a111, 0\n"
"v_accvgpr_write_b32 a112, 0\n"
"v_accvgpr_write_b32 a113, 0\n"
"v_accvgpr_write_b32 a114, 0\n"
"v_accvgpr_write_b32 a115, 0\n"
"v_accvgpr_write_b32 a116, 0\n"
"v_accvgpr_write_b32 a117, 0\n"
"v_accvgpr_write_b32 a118, 0\n"
"v_accvgpr_write_b32 a119, 0\n"
"v_accvgpr_write_b32 a120, 0\n"
"v_accvgpr_write_b32 a121, 0\n"
"v_accvgpr_write_b32 a122, 0\n"
"v_accvgpr_write_b32 a123, 0\n"
"v_accvgpr_write_b32 a124, 0\n"
"v_accvgpr_write_b32 a125, 0\n"
"v_accvgpr_write_b32 a126, 0\n"
"v_accvgpr_write_b32 a127, 0\n"
"v_max3_f32 v160, v128, v129, v130\n"
"v_max3_f32 v160, v160, v131, v132\n"
"v_max3_f32 v160, v160, v133, v134\n"
"v_max_f32 v160, v160, v135\n"
"v_max3_f32 v161, v136, v137, v138\n"
"v_max3_f32 v161, v161, v139, v140\n"
"v_max3_f32 v161, v161, v141, v142\n"
"v_max_f32 v161, v161, v143\n"
"v_max3_f32 v162, v144, v145, v146\n"
"v_max3_f32 v162, v162, v147, v148\n"
"v_max3_f32 v162, v162, v149, v150\n"
"v_max_f32 v162, v162, v151\n"
"v_max3_f32 v163, v152, v153, v154\n"
"v_max3_f32 v163, v163, v155, v156\n"
"v_max3_f32 v163, v163, v157, v158\n"
"v_max_f32 v163, v163, v159\n"
"v_mov_b32 v164, v160\n"
"v_mov_b32 v165, v161\n"
"v_mov_b32 v166, v162\n"
"v_mov_b32 v167, v163\n"
"s_nop 0\n"
"v_permlane32_swap_b32 v160, v164\n"
"v_permlane32_swap_b32 v161, v165\n"
"v_permlane32_swap_b32 v162, v166\n"
"v_permlane32_swap_b32 v163, v167\n"
"v_max_f32 v160, v160, v164\n"
"v_max_f32 v161, v161, v165\n"
"v_max_f32 v162, v162, v166\n"
"v_max_f32 v163, v163, v167\n"
"v_mov_b32 v164, v160\n"
"v_mov_b32 v165, v161\n"
"v_mov_b32 v166, v162\n"
"v_mov_b32 v167, v163\n"
"s_nop 0\n"
"v_permlane16_swap_b32 v160, v164\n"
"v_permlane16_swap_b32 v161, v165\n"
"v_permlane16_swap_b32 v162, v166\n"
"v_permlane16_swap_b32 v163, v167\n"
"v_max_f32 v160, v160, v164\n"
"v_max_f32 v161, v161, v165\n"
"v_max_f32 v162, v162, v166\n"
"v_max_f32 v163, v163, v167\n"
"v_sub_f32 v24, 0xc1f00000, v160\n"
"v_sub_f32 v25, 0xc1f00000, v160\n"
"v_sub_f32 v26, 0xc1f00000, v160\n"
"v_sub_f32 v27, 0xc1f00000, v160\n"
"v_sub_f32 v28, 0xc1f00000, v161\n"
"v_sub_f32 v29, 0xc1f00000, v161\n"
"v_sub_f32 v30, 0xc1f00000, v161\n"
"v_sub_f32 v31, 0xc1f00000, v161\n"
"v_sub_f32 v32, 0xc1f00000, v162\n"
"v_sub_f32 v33, 0xc1f00000, v162\n"
"v_sub_f32 v34, 0xc1f00000, v162\n"
"v_sub_f32 v35, 0xc1f00000, v162\n"
"v_sub_f32 v36, 0xc1f00000, v163\n"
"v_sub_f32 v37, 0xc1f00000, v163\n"
"v_sub_f32 v38, 0xc1f00000, v163\n"
"v_sub_f32 v39, 0xc1f00000, v163\n"
"s_nop 1\n"
"s_waitcnt lgkmcnt(0)\n"
"AT_main:\n"
"s_or_b32 s56, s42, s41\n"
"s_cmp_lg_u32 s56, 0\n"
"s_cbranch_scc1 AT_h1\n"
"v_lshlrev_b32 v214, 7, v248\n"
"s_mov_b32 s57, 32768\n"
"s_mov_b32 s58, 40960\n"
"s_mov_b32 s59, 49152\n"
"s_mov_b32 s60, 57344\n"
"buffer_load_dword v210, v214, s[28:31], s57 offen offset:4 sc1\n"
"buffer_load_dword v211, v214, s[28:31], s58 offen offset:4 sc1\n"
"buffer_load_dword v212, v214, s[28:31], s59 offen offset:4 sc1\n"
"buffer_load_dword v213, v214, s[28:31], s60 offen offset:4 sc1\n"
"AT_h1:\n"
"v_mfma_f32_16x16x32_bf16 v[0:3], a[128:131], v[64:67], v[24:27]\n"
"ds_read_b128 a[160:163], v240 offset:8192\n"
"v_mfma_f32_16x16x32_bf16 v[0:3], a[132:135], v[68:71], v[0:3]\n"
"s_nop 0\n"
"v_mfma_f32_16x16x32_bf16 v[0:3], a[136:139], v[72:75], v[0:3]\n"
"ds_read_b128 a[164:167], v240 offset:8448\n"
"v_mfma_f32_16x16x32_bf16 v[0:3], a[140:143], v[76:79], v[0:3]\n"
"s_nop 0\n"
"v_mfma_f32_16x16x32_bf16 v[4:7], a[144:147], v[64:67], v[24:27]\n"
"ds_read_b128 a[168:171], v240 offset:8704\n"
"v_mfma_f32_16x16x32_bf16 v[4:7], a[148:151], v[68:71], v[4:7]\n"
"s_nop 0\n"
"v_mfma_f32_16x16x32_bf16 v[4:7], a[152:155], v[72:75], v[4:7]\n"
"ds_read_b128 a[172:175], v240 offset:8960\n"
"v_mfma_f32_16x16x32_bf16 v[4:7], a[156:159], v[76:79], v[4:7]\n"
"s_nop 0\n"
"s_nop 1\n"
"v_exp_f32 v0, v0\n"
"s_nop 1\n"
"ds_read_b128 a[176:179], v240 offset:12288\n"
"s_nop 1\n"
"v_exp_f32 v1, v1\n"
"s_nop 1\n"
"ds_read_b128 a[180:183], v240 offset:12544\n"
"v_cvt_pk_bf16_f32 v16, v0, v1\n"
"s_nop 1\n"
"v_exp_f32 v2, v2\n"
"s_nop 1\n"
"ds_read_b128 a[184:187], v240 offset:12800\n"
"s_nop 1\n"
"v_exp_f32 v3, v3\n"
"s_nop 1\n"
"v_cvt_pk_bf16_f32 v17, v2, v3\n"
"s_nop 1\n"
"v_exp_f32 v4, v4\n"
"v_mfma_f32_16x16x32_bf16 v[8:11], a[128:131], v[80:83], v[28:31]\n"
"ds_read_b128 a[188:191], v240 offset:13056\n"
"v_mfma_f32_16x16x32_bf16 v[8:11], a[132:135], v[84:87], v[8:11]\n"
"v_exp_f32 v5, v5\n"
"v_mfma_f32_16x16x32_bf16 v[8:11], a[136:139], v[88:91], v[8:11]\n"
"ds_read_b64_tr_b16 a[224:225], v242 offset:24576\n"
"v_cvt_pk_bf16_f32 v18, v4, v5\n"
"v_mfma_f32_16x16x32_bf16 v[8:11], a[140:143], v[92:95], v[8:11]\n"
"v_exp_f32 v6, v6\n"
"v_mfma_f32_16x16x32_bf16 v[12:15], a[144:147], v[80:83], v[28:31]\n"
"ds_read_b64_tr_b16 a[226:227], v242 offset:28672\n"
"v_mfma_f32_16x16x32_bf16 v[12:15], a[148:151], v[84:87], v[12:15]\n"
"v_exp_f32 v7, v7\n"
"v_mfma_f32_16x16x32_bf16 v[12:15], a[152:155], v[88:91], v[12:15]\n"
"ds_read_b64_tr_b16 a[228:229], v243 offset:24576\n"
"v_cvt_pk_bf16_f32 v19, v6, v7\n"
"v_mfma_f32_16x16x32_bf16 v[12:15], a[156:159], v[92:95], v[12:15]\n"
"s_nop 0\n"
"v_mfma_f32_16x16x32_bf16 a[0:3], a[192:195], v[16:19], 0\n"
"v_exp_f32 v8, v8\n"
"v_mfma_f32_16x16x32_bf16 a[4:7], a[196:199], v[16:19], 0\n"
"ds_read_b64_tr_b16 a[230:231], v243 offset:28672\n"
"v_mfma_f32_16x16x32_bf16 a[8:11], a[200:203], v[16:19], 0\n"
"v_exp_f32 v9, v9\n"
"v_mfma_f32_16x16x32_bf16 a[12:15], a[204:207], v[16:19], 0\n"
"ds_read_b64_tr_b16 a[232:233], v242 offset:24832\n"
"v_cvt_pk_bf16_f32 v20, v8, v9\n"
"v_mfma_f32_16x16x32_bf16 a[16:19], a[208:211], v[16:19], 0\n"
"v_exp_f32 v10, v10\n"
"v_mfma_f32_16x16x32_bf16 a[20:23], a[212:215], v[16:19], 0\n"
"ds_read_b64_tr_b16 a[234:235], v242 offset:28928\n"
"v_mfma_f32_16x16x32_bf16 a[24:27], a[216:219], v[16:19], 0\n"
"v_exp_f32 v11, v11\n"
"v_mfma_f32_16x16x32_bf16 a[28:31], a[220:223], v[16:19], 0\n"
"v_cvt_pk_bf16_f32 v21, v10, v11\n"
"v_mfma_f32_16x16x32_bf16 v[44:47], v[40:43], v[16:19], 0\n"
"v_exp_f32 v12, v12\n"
"v_mfma_f32_16x16x32_bf16 v[0:3], a[128:131], v[96:99], v[32:35]\n"
"ds_read_b64_tr_b16 a[236:237], v243 offset:24832\n"
"v_mfma_f32_16x16x32_bf16 v[0:3], a[132:135], v[100:103], v[0:3]\n"
"v_exp_f32 v13, v13\n"
"v_mfma_f32_16x16x32_bf16 v[0:3], a[136:139], v[104:107], v[0:3]\n"
"ds_read_b64_tr_b16 a[238:239], v243 offset:28928\n"
"v_cvt_pk_bf16_f32 v22, v12, v13\n"
"v_mfma_f32_16x16x32_bf16 v[0:3], a[140:143], v[108:111], v[0:3]\n"
"v_exp_f32 v14, v14\n"
"v_mfma_f32_16x16x32_bf16 v[4:7], a[144:147], v[96:99], v[32:35]\n"
"ds_read_b64_tr_b16 a[240:241], v242 offset:25088\n"
"v_mfma_f32_16x16x32_bf16 v[4:7], a[148:151], v[100:103], v[4:7]\n"
"v_exp_f32 v15, v15\n"
"v_mfma_f32_16x16x32_bf16 v[4:7], a[152:155], v[104:107], v[4:7]\n"
"ds_read_b64_tr_b16 a[242:243], v242 offset:29184\n"
"v_cvt_pk_bf16_f32 v23, v14, v15\n"
"v_mfma_f32_16x16x32_bf16 v[4:7], a[156:159], v[108:111], v[4:7]\n"
"s_nop 0\n"
"v_mfma_f32_16x16x32_bf16 a[32:35], a[192:195], v[20:23], 0\n"
"v_exp_f32 v0, v0\n"
"v_mfma_f32_16x16x32_bf16 a[36:39], a[196:199], v[20:23], 0\n"
"ds_read_b64_tr_b16 a[244:245], v243 offset:25088\n"
"v_mfma_f32_16x16x32_bf16 a[40:43], a[200:203], v[20:23], 0\n"
"v_exp_f32 v1, v1\n"
"v_mfma_f32_16x16x32_bf16 a[44:47], a[204:207], v[20:23], 0\n"
"ds_read_b64_tr_b16 a[246:247], v243 offset:29184\n"
"v_cvt_pk_bf16_f32 v16, v0, v1\n"
"v_mfma_f32_16x16x32_bf16 a[48:51], a[208:211], v[20:23], 0\n"
"v_exp_f32 v2, v2\n"
"v_mfma_f32_16x16x32_bf16 a[52:55], a[212:215], v[20:23], 0\n"
"ds_read_b64_tr_b16 a[248:249], v242 offset:25344\n"
"v_mfma_f32_16x16x32_bf16 a[56:59], a[216:219], v[20:23], 0\n"
"v_exp_f32 v3, v3\n"
"v_mfma_f32_16x16x32_bf16 a[60:63], a[220:223], v[20:23], 0\n"
"v_cvt_pk_bf16_f32 v17, v2, v3\n"
"v_mfma_f32_16x16x32_bf16 v[48:51], v[40:43], v[20:23], 0\n"
"v_exp_f32 v4, v4\n"
"v_mfma_f32_16x16x32_bf16 v[8:11], a[128:131], v[112:115], v[36:39]\n"
"ds_read_b64_tr_b16 a[250:251], v242 offset:29440\n"
"v_mfma_f32_16x16x32_bf16 v[8:11], a[132:135], v[116:119], v[8:11]\n"
"v_exp_f32 v5, v5\n"
"v_mfma_f32_16x16x32_bf16 v[8:11], a[136:139], v[120:123], v[8:11]\n"
"ds_read_b64_tr_b16 a[252:253], v243 offset:25344\n"
"v_cvt_pk_bf16_f32 v18, v4, v5\n"
"v_mfma_f32_16x16x32_bf16 v[8:11], a[140:143], v[124:127], v[8:11]\n"
"v_exp_f32 v6, v6\n"
"v_mfma_f32_16x16x32_bf16 v[12:15], a[144:147], v[112:115], v[36:39]\n"
"ds_read_b64_tr_b16 a[254:255], v243 offset:29440\n"
"v_mfma_f32_16x16x32_bf16 v[12:15], a[148:151], v[116:119], v[12:15]\n"
"v_exp_f32 v7, v7\n"
"v_mfma_f32_16x16x32_bf16 v[12:15], a[152:155], v[120:123], v[12:15]\n"
"v_cvt_pk_bf16_f32 v19, v6, v7\n"
"v_mfma_f32_16x16x32_bf16 v[12:15], a[156:159], v[124:127], v[12:15]\n"
"s_nop 0\n"
"v_mfma_f32_16x16x32_bf16 a[64:67], a[192:195], v[16:19], 0\n"
"v_exp_f32 v8, v8\n"
"v_mfma_f32_16x16x32_bf16 a[68:71], a[196:199], v[16:19], 0\n"
"s_nop 0\n"
"v_mfma_f32_16x16x32_bf16 a[72:75], a[200:203], v[16:19], 0\n"
"v_exp_f32 v9, v9\n"
"v_mfma_f32_16x16x32_bf16 a[76:79], a[204:207], v[16:19], 0\n"
"v_cvt_pk_bf16_f32 v20, v8, v9\n"
"v_mfma_f32_16x16x32_bf16 a[80:83], a[208:211], v[16:19], 0\n"
"v_exp_f32 v10, v10\n"
"v_mfma_f32_16x16x32_bf16 a[84:87], a[212:215], v[16:19], 0\n"
"s_nop 0\n"
"v_mfma_f32_16x16x32_bf16 a[88:91], a[216:219], v[16:19], 0\n"
"v_exp_f32 v11, v11\n"
"v_mfma_f32_16x16x32_bf16 a[92:95], a[220:223], v[16:19], 0\n"
"s_waitcnt vmcnt(24) lgkmcnt(0)\n"
"s_barrier\n"
"v_cvt_pk_bf16_f32 v21, v10, v11\n"
"v_mfma_f32_16x16x32_bf16 v[52:55], v[40:43], v[16:19], 0\n"
"v_exp_f32 v12, v12\n"
"s_mov_b32 s87, 7\n"
".p2align 6\n"
"AT_loop:\n"
"v_mfma_f32_16x16x32_bf16 v[0:3], a[160:163], v[64:67], v[24:27]\n"
"ds_read_b128 a[128:131], v240 offset:32768\n"
"v_mfma_f32_16x16x32_bf16 v[0:3], a[164:167], v[68:71], v[0:3]\n"
"v_exp_f32 v13, v13\n"
"v_mfma_f32_16x16x32_bf16 v[0:3], a[168:171], v[72:75], v[0:3]\n"
"ds_read_b128 a[132:135], v240 offset:33024\n"
"v_cvt_pk_bf16_f32 v22, v12, v13\n"
"v_mfma_f32_16x16x32_bf16 v[0:3], a[172:175], v[76:79], v[0:3]\n"
"v_exp_f32 v14, v14\n"
"v_mfma_f32_16x16x32_bf16 v[4:7], a[176:179], v[64:67], v[24:27]\n"
"ds_read_b128 a[136:139], v240 offset:33280\n"
"v_mfma_f32_16x16x32_bf16 v[4:7], a[180:183], v[68:71], v[4:7]\n"
"v_exp_f32 v15, v15\n"
"v_mfma_f32_16x16x32_bf16 v[4:7], a[184:187], v[72:75], v[4:7]\n"
"s_mov_b32 m0, s86\n"
"ds_read_b128 a[140:143], v240 offset:33536\n"
"v_cvt_pk_bf16_f32 v23, v14, v15\n"
"v_mfma_f32_16x16x32_bf16 v[4:7], a[188:191], v[76:79], v[4:7]\n"
"buffer_load_dwordx4 v246, s[8:11], s84 offen offset:0 lds\n"
"v_mfma_f32_16x16x32_bf16 a[96:99], a[192:195], v[20:23], a[96:99]\n"
"v_exp_f32 v0, v0\n"
"v_mfma_f32_16x16x32_bf16 a[100:103], a[196:199], v[20:23], a[100:103]\n"
"ds_read_b128 a[144:147], v240 offset:36864\n"
"v_mfma_f32_16x16x32_bf16 a[104:107], a[200:203], v[20:23], a[104:107]\n"
"v_exp_f32 v1, v1\n"
"v_mfma_f32_16x16x32_bf16 a[108:111], a[204:207], v[20:23], a[108:111]\n"
"ds_read_b128 a[148:151], v240 offset:37120\n"
"v_cvt_pk_bf16_f32 v16, v0, v1\n"
"v_mfma_f32_16x16x32_bf16 a[112:115], a[208:211], v[20:23], a[112:115]\n"
"v_exp_f32 v2, v2\n"
"v_mfma_f32_16x16x32_bf16 a[116:119], a[212:215], v[20:23], a[116:119]\n"
"ds_read_b128 a[152:155], v240 offset:37376\n"
"v_mfma_f32_16x16x32_bf16 a[120:123], a[216:219], v[20:23], a[120:123]\n"
"v_exp_f32 v3, v3\n"
"v_mfma_f32_16x16x32_bf16 a[124:127], a[220:223], v[20:23], a[124:127]\n"
"buffer_load_dword v250, v215, s[64:67], 0 offen\n"
"v_cvt_pk_bf16_f32 v17, v2, v3\n"
"v_mfma_f32_16x16x32_bf16 v[56:59], v[40:43], v[20:23], v[56:59]\n"
"v_exp_f32 v4, v4\n"
"v_mfma_f32_16x16x32_bf16 v[8:11], a[160:163], v[80:83], v[28:31]\n"
"ds_read_b128 a[156:159], v240 offset:37632\n"
"v_mfma_f32_16x16x32_bf16 v[8:11], a[164:167], v[84:87], v[8:11]\n"
"v_exp_f32 v5, v5\n"
"v_mfma_f32_16x16x32_bf16 v[8:11], a[168:171], v[88:91], v[8:11]\n"
"ds_read_b64_tr_b16 a[192:193], v242 offset:49152\n"
"v_cvt_pk_bf16_f32 v18, v4, v5\n"
"v_mfma_f32_16x16x32_bf16 v[8:11], a[172:175], v[92:95], v[8:11]\n"
"v_exp_f32 v6, v6\n"
"v_mfma_f32_16x16x32_bf16 v[12:15], a[176:179], v[80:83], v[28:31]\n"
"ds_read_b64_tr_b16 a[194:195], v242 offset:53248\n"
"v_mfma_f32_16x16x32_bf16 v[12:15], a[180:183], v[84:87], v[12:15]\n"
"v_exp_f32 v7, v7\n"
"v_mfma_f32_16x16x32_bf16 v[12:15], a[184:187], v[88:91], v[12:15]\n"
"ds_read_b64_tr_b16 a[196:197], v243 offset:49152\n"
"v_cvt_pk_bf16_f32 v19, v6, v7\n"
"v_mfma_f32_16x16x32_bf16 v[12:15], a[188:191], v[92:95], v[12:15]\n"
"s_waitcnt lgkmcnt(11)\n"
"buffer_load_dwordx4 v246, s[8:11], s84 offen offset:1024 lds\n"
"v_mfma_f32_16x16x32_bf16 a[0:3], a[224:227], v[16:19], a[0:3]\n"
"v_exp_f32 v8, v8\n"
"v_mfma_f32_16x16x32_bf16 a[4:7], a[228:231], v[16:19], a[4:7]\n"
"ds_read_b64_tr_b16 a[198:199], v243 offset:53248\n"
"v_mfma_f32_16x16x32_bf16 a[8:11], a[232:235], v[16:19], a[8:11]\n"
"v_exp_f32 v9, v9\n"
"v_mfma_f32_16x16x32_bf16 a[12:15], a[236:239], v[16:19], a[12:15]\n"
"ds_read_b64_tr_b16 a[200:201], v242 offset:49408\n"
"v_cvt_pk_bf16_f32 v20, v8, v9\n"
"v_mfma_f32_16x16x32_bf16 a[16:19], a[240:243], v[16:19], a[16:19]\n"
"v_exp_f32 v10, v10\n"
"v_mfma_f32_16x16x32_bf16 a[20:23], a[244:247], v[16:19], a[20:23]\n"
"ds_read_b64_tr_b16 a[202:203], v242 offset:53504\n"
"v_mfma_f32_16x16x32_bf16 a[24:27], a[248:251], v[16:19], a[24:27]\n"
"v_exp_f32 v11, v11\n"
"v_mfma_f32_16x16x32_bf16 a[28:31], a[252:255], v[16:19], a[28:31]\n"
"v_cvt_pk_bf16_f32 v21, v10, v11\n"
"v_mfma_f32_16x16x32_bf16 v[44:47], v[40:43], v[16:19], v[44:47]\n"
"v_exp_f32 v12, v12\n"
"v_mfma_f32_16x16x32_bf16 v[0:3], a[160:163], v[96:99], v[32:35]\n"
"ds_read_b64_tr_b16 a[204:205], v243 offset:49408\n"
"v_mfma_f32_16x16x32_bf16 v[0:3], a[164:167], v[100:103], v[0:3]\n"
"v_exp_f32 v13, v13\n"
"v_mfma_f32_16x16x32_bf16 v[0:3], a[168:171], v[104:107], v[0:3]\n"
"ds_read_b64_tr_b16 a[206:207], v243 offset:53504\n"
"v_cvt_pk_bf16_f32 v22, v12, v13\n"
"v_mfma_f32_16x16x32_bf16 v[0:3], a[172:175], v[108:111], v[0:3]\n"
"v_exp_f32 v14, v14\n"
"v_mfma_f32_16x16x32_bf16 v[4:7], a[176:179], v[96:99], v[32:35]\n"
"ds_read_b64_tr_b16 a[208:209], v242 offset:49664\n"
"v_mfma_f32_16x16x32_bf16 v[4:7], a[180:183], v[100:103], v[4:7]\n"
"v_exp_f32 v15, v15\n"
"v_mfma_f32_16x16x32_bf16 v[4:7], a[184:187], v[104:107], v[4:7]\n"
"ds_read_b64_tr_b16 a[210:211], v242 offset:53760\n"
"v_cvt_pk_bf16_f32 v23, v14, v15\n"
"v_mfma_f32_16x16x32_bf16 v[4:7], a[188:191], v[108:111], v[4:7]\n"
"buffer_load_dwordx4 v247, s[8:11], s84 offen offset:2048 lds\n"
"v_mfma_f32_16x16x32_bf16 a[32:35], a[224:227], v[20:23], a[32:35]\n"
"v_exp_f32 v0, v0\n"
"v_mfma_f32_16x16x32_bf16 a[36:39], a[228:231], v[20:23], a[36:39]\n"
"ds_read_b64_tr_b16 a[212:213], v243 offset:49664\n"
"v_mfma_f32_16x16x32_bf16 a[40:43], a[232:235], v[20:23], a[40:43]\n"
"v_exp_f32 v1, v1\n"
"v_mfma_f32_16x16x32_bf16 a[44:47], a[236:239], v[20:23], a[44:47]\n"
"ds_read_b64_tr_b16 a[214:215], v243 offset:53760\n"
"v_cvt_pk_bf16_f32 v16, v0, v1\n"
"v_mfma_f32_16x16x32_bf16 a[48:51], a[240:243], v[20:23], a[48:51]\n"
"v_exp_f32 v2, v2\n"
"v_mfma_f32_16x16x32_bf16 a[52:55], a[244:247], v[20:23], a[52:55]\n"
"ds_read_b64_tr_b16 a[216:217], v242 offset:49920\n"
"v_mfma_f32_16x16x32_bf16 a[56:59], a[248:251], v[20:23], a[56:59]\n"
"v_exp_f32 v3, v3\n"
"v_mfma_f32_16x16x32_bf16 a[60:63], a[252:255], v[20:23], a[60:63]\n"
"v_cvt_pk_bf16_f32 v17, v2, v3\n"
"v_mfma_f32_16x16x32_bf16 v[48:51], v[40:43], v[20:23], v[48:51]\n"
"v_exp_f32 v4, v4\n"
"v_mfma_f32_16x16x32_bf16 v[8:11], a[160:163], v[112:115], v[36:39]\n"
"ds_read_b64_tr_b16 a[218:219], v242 offset:54016\n"
"v_mfma_f32_16x16x32_bf16 v[8:11], a[164:167], v[116:119], v[8:11]\n"
"v_exp_f32 v5, v5\n"
"v_mfma_f32_16x16x32_bf16 v[8:11], a[168:171], v[120:123], v[8:11]\n"
"ds_read_b64_tr_b16 a[220:221], v243 offset:49920\n"
"v_cvt_pk_bf16_f32 v18, v4, v5\n"
"v_mfma_f32_16x16x32_bf16 v[8:11], a[172:175], v[124:127], v[8:11]\n"
"v_exp_f32 v6, v6\n"
"v_mfma_f32_16x16x32_bf16 v[12:15], a[176:179], v[112:115], v[36:39]\n"
"ds_read_b64_tr_b16 a[222:223], v243 offset:54016\n"
"v_mfma_f32_16x16x32_bf16 v[12:15], a[180:183], v[116:119], v[12:15]\n"
"v_exp_f32 v7, v7\n"
"v_mfma_f32_16x16x32_bf16 v[12:15], a[184:187], v[120:123], v[12:15]\n"
"v_cvt_pk_bf16_f32 v19, v6, v7\n"
"v_mfma_f32_16x16x32_bf16 v[12:15], a[188:191], v[124:127], v[12:15]\n"
"buffer_load_dwordx4 v247, s[8:11], s84 offen offset:3072 lds\n"
"v_mfma_f32_16x16x32_bf16 a[64:67], a[224:227], v[16:19], a[64:67]\n"
"v_exp_f32 v8, v8\n"
"v_mfma_f32_16x16x32_bf16 a[68:71], a[228:231], v[16:19], a[68:71]\n"
"s_nop 0\n"
"v_mfma_f32_16x16x32_bf16 a[72:75], a[232:235], v[16:19], a[72:75]\n"
"v_exp_f32 v9, v9\n"
"v_mfma_f32_16x16x32_bf16 a[76:79], a[236:239], v[16:19], a[76:79]\n"
"v_cvt_pk_bf16_f32 v20, v8, v9\n"
"v_mfma_f32_16x16x32_bf16 a[80:83], a[240:243], v[16:19], a[80:83]\n"
"v_exp_f32 v10, v10\n"
"v_mfma_f32_16x16x32_bf16 a[84:87], a[244:247], v[16:19], a[84:87]\n"
"s_nop 0\n"
"v_mfma_f32_16x16x32_bf16 a[88:91], a[248:251], v[16:19], a[88:91]\n"
"v_exp_f32 v11, v11\n"
"v_mfma_f32_16x16x32_bf16 a[92:95], a[252:255], v[16:19], a[92:95]\n"
"s_waitcnt lgkmcnt(8)\n"
"v_cvt_pk_bf16_f32 v21, v10, v11\n"
"v_mfma_f32_16x16x32_bf16 v[52:55], v[40:43], v[16:19], v[52:55]\n"
"v_exp_f32 v12, v12\n"
"v_mfma_f32_16x16x32_bf16 v[0:3], a[128:131], v[64:67], v[24:27]\n"
"ds_read_b128 a[160:163], v240 offset:40960\n"
"v_mfma_f32_16x16x32_bf16 v[0:3], a[132:135], v[68:71], v[0:3]\n"
"v_exp_f32 v13, v13\n"
"v_mfma_f32_16x16x32_bf16 v[0:3], a[136:139], v[72:75], v[0:3]\n"
"ds_read_b128 a[164:167], v240 offset:41216\n"
"v_cvt_pk_bf16_f32 v22, v12, v13\n"
"v_mfma_f32_16x16x32_bf16 v[0:3], a[140:143], v[76:79], v[0:3]\n"
"v_exp_f32 v14, v14\n"
"v_mfma_f32_16x16x32_bf16 v[4:7], a[144:147], v[64:67], v[24:27]\n"
"ds_read_b128 a[168:171], v240 offset:41472\n"
"v_mfma_f32_16x16x32_bf16 v[4:7], a[148:151], v[68:71], v[4:7]\n"
"v_exp_f32 v15, v15\n"
"v_mfma_f32_16x16x32_bf16 v[4:7], a[152:155], v[72:75], v[4:7]\n"
"s_add_u32 m0, s86, 16384\n"
"ds_read_b128 a[172:175], v240 offset:41728\n"
"v_cvt_pk_bf16_f32 v23, v14, v15\n"
"v_mfma_f32_16x16x32_bf16 v[4:7], a[156:159], v[76:79], v[4:7]\n"
"buffer_load_dwordx4 v246, s[12:15], s84 offen offset:0 lds\n"
"v_mfma_f32_16x16x32_bf16 a[96:99], a[224:227], v[20:23], a[96:99]\n"
"v_exp_f32 v0, v0\n"
"v_mfma_f32_16x16x32_bf16 a[100:103], a[228:231], v[20:23], a[100:103]\n"
"ds_read_b128 a[176:179], v240 offset:45056\n"
"v_mfma_f32_16x16x32_bf16 a[104:107], a[232:235], v[20:23], a[104:107]\n"
"v_exp_f32 v1, v1\n"
"v_mfma_f32_16x16x32_bf16 a[108:111], a[236:239], v[20:23], a[108:111]\n"
"ds_read_b128 a[180:183], v240 offset:45312\n"
"v_cvt_pk_bf16_f32 v16, v0, v1\n"
"v_mfma_f32_16x16x32_bf16 a[112:115], a[240:243], v[20:23], a[112:115]\n"
"v_exp_f32 v2, v2\n"
"v_mfma_f32_16x16x32_bf16 a[116:119], a[244:247], v[20:23], a[116:119]\n"
"ds_read_b128 a[184:187], v240 offset:45568\n"
"v_mfma_f32_16x16x32_bf16 a[120:123], a[248:251], v[20:23], a[120:123]\n"
"v_exp_f32 v3, v3\n"
"v_mfma_f32_16x16x32_bf16 a[124:127], a[252:255], v[20:23], a[124:127]\n"
"v_cvt_pk_bf16_f32 v17, v2, v3\n"
"v_mfma_f32_16x16x32_bf16 v[56:59], v[40:43], v[20:23], v[56:59]\n"
"v_exp_f32 v4, v4\n"
"v_mfma_f32_16x16x32_bf16 v[8:11], a[128:131], v[80:83], v[28:31]\n"
"ds_read_b128 a[188:191], v240 offset:45824\n"
"v_mfma_f32_16x16x32_bf16 v[8:11], a[132:135], v[84:87], v[8:11]\n"
"v_exp_f32 v5, v5\n"
"v_mfma_f32_16x16x32_bf16 v[8:11], a[136:139], v[88:91], v[8:11]\n"
"ds_read_b64_tr_b16 a[224:225], v242 offset:57344\n"
"v_cvt_pk_bf16_f32 v18, v4, v5\n"
"v_mfma_f32_16x16x32_bf16 v[8:11], a[140:143], v[92:95], v[8:11]\n"
"v_exp_f32 v6, v6\n"
"v_mfma_f32_16x16x32_bf16 v[12:15], a[144:147], v[80:83], v[28:31]\n"
"ds_read_b64_tr_b16 a[226:227], v242 offset:61440\n"
"v_mfma_f32_16x16x32_bf16 v[12:15], a[148:151], v[84:87], v[12:15]\n"
"v_exp_f32 v7, v7\n"
"v_mfma_f32_16x16x32_bf16 v[12:15], a[152:155], v[88:91], v[12:15]\n"
"ds_read_b64_tr_b16 a[228:229], v243 offset:57344\n"
"v_cvt_pk_bf16_f32 v19, v6, v7\n"
"v_mfma_f32_16x16x32_bf16 v[12:15], a[156:159], v[92:95], v[12:15]\n"
"s_waitcnt lgkmcnt(11)\n"
"buffer_load_dwordx4 v247, s[12:15], s84 offen offset:1024 lds\n"
"v_mfma_f32_16x16x32_bf16 a[0:3], a[192:195], v[16:19], a[0:3]\n"
"v_exp_f32 v8, v8\n"
"v_mfma_f32_16x16x32_bf16 a[4:7], a[196:199], v[16:19], a[4:7]\n"
"ds_read_b64_tr_b16 a[230:231], v243 offset:61440\n"
"v_mfma_f32_16x16x32_bf16 a[8:11], a[200:203], v[16:19], a[8:11]\n"
"v_exp_f32 v9, v9\n"
"v_mfma_f32_16x16x32_bf16 a[12:15], a[204:207], v[16:19], a[12:15]\n"
"ds_read_b64_tr_b16 a[232:233], v242 offset:57600\n"
"v_cvt_pk_bf16_f32 v20, v8, v9\n"
"v_mfma_f32_16x16x32_bf16 a[16:19], a[208:211], v[16:19], a[16:19]\n"
"v_exp_f32 v10, v10\n"
"v_mfma_f32_16x16x32_bf16 a[20:23], a[212:215], v[16:19], a[20:23]\n"
"ds_read_b64_tr_b16 a[234:235], v242 offset:61696\n"
"v_mfma_f32_16x16x32_bf16 a[24:27], a[216:219], v[16:19], a[24:27]\n"
"v_exp_f32 v11, v11\n"
"v_mfma_f32_16x16x32_bf16 a[28:31], a[220:223], v[16:19], a[28:31]\n"
"v_cvt_pk_bf16_f32 v21, v10, v11\n"
"v_mfma_f32_16x16x32_bf16 v[44:47], v[40:43], v[16:19], v[44:47]\n"
"v_exp_f32 v12, v12\n"
"v_mfma_f32_16x16x32_bf16 v[0:3], a[128:131], v[96:99], v[32:35]\n"
"ds_read_b64_tr_b16 a[236:237], v243 offset:57600\n"
"v_mfma_f32_16x16x32_bf16 v[0:3], a[132:135], v[100:103], v[0:3]\n"
"v_exp_f32 v13, v13\n"
"v_mfma_f32_16x16x32_bf16 v[0:3], a[136:139], v[104:107], v[0:3]\n"
"ds_read_b64_tr_b16 a[238:239], v243 offset:61696\n"
"v_cvt_pk_bf16_f32 v22, v12, v13\n"
"v_mfma_f32_16x16x32_bf16 v[0:3], a[140:143], v[108:111], v[0:3]\n"
"v_exp_f32 v14, v14\n"
"v_mfma_f32_16x16x32_bf16 v[4:7], a[144:147], v[96:99], v[32:35]\n"
"ds_read_b64_tr_b16 a[240:241], v242 offset:57856\n"
"v_mfma_f32_16x16x32_bf16 v[4:7], a[148:151], v[100:103], v[4:7]\n"
"v_exp_f32 v15, v15\n"
"v_mfma_f32_16x16x32_bf16 v[4:7], a[152:155], v[104:107], v[4:7]\n"
"ds_read_b64_tr_b16 a[242:243], v242 offset:61952\n"
"v_cvt_pk_bf16_f32 v23, v14, v15\n"
"v_mfma_f32_16x16x32_bf16 v[4:7], a[156:159], v[108:111], v[4:7]\n"
"buffer_load_dwordx4 v246, s[12:15], s84 offen offset:2048 lds\n"
"v_mfma_f32_16x16x32_bf16 a[32:35], a[192:195], v[20:23], a[32:35]\n"
"v_exp_f32 v0, v0\n"
"v_mfma_f32_16x16x32_bf16 a[36:39], a[196:199], v[20:23], a[36:39]\n"
"ds_read_b64_tr_b16 a[244:245], v243 offset:57856\n"
"v_mfma_f32_16x16x32_bf16 a[40:43], a[200:203], v[20:23], a[40:43]\n"
"v_exp_f32 v1, v1\n"
"v_mfma_f32_16x16x32_bf16 a[44:47], a[204:207], v[20:23], a[44:47]\n"
"ds_read_b64_tr_b16 a[246:247], v243 offset:61952\n"
"v_cvt_pk_bf16_f32 v16, v0, v1\n"
"v_mfma_f32_16x16x32_bf16 a[48:51], a[208:211], v[20:23], a[48:51]\n"
"v_exp_f32 v2, v2\n"
"v_mfma_f32_16x16x32_bf16 a[52:55], a[212:215], v[20:23], a[52:55]\n"
"ds_read_b64_tr_b16 a[248:249], v242 offset:58112\n"
"v_mfma_f32_16x16x32_bf16 a[56:59], a[216:219], v[20:23], a[56:59]\n"
"v_exp_f32 v3, v3\n"
"v_mfma_f32_16x16x32_bf16 a[60:63], a[220:223], v[20:23], a[60:63]\n"
"v_cvt_pk_bf16_f32 v17, v2, v3\n"
"v_mfma_f32_16x16x32_bf16 v[48:51], v[40:43], v[20:23], v[48:51]\n"
"v_exp_f32 v4, v4\n"
"v_mfma_f32_16x16x32_bf16 v[8:11], a[128:131], v[112:115], v[36:39]\n"
"ds_read_b64_tr_b16 a[250:251], v242 offset:62208\n"
"v_mfma_f32_16x16x32_bf16 v[8:11], a[132:135], v[116:119], v[8:11]\n"
"v_exp_f32 v5, v5\n"
"v_mfma_f32_16x16x32_bf16 v[8:11], a[136:139], v[120:123], v[8:11]\n"
"ds_read_b64_tr_b16 a[252:253], v243 offset:58112\n"
"v_cvt_pk_bf16_f32 v18, v4, v5\n"
"v_mfma_f32_16x16x32_bf16 v[8:11], a[140:143], v[124:127], v[8:11]\n"
"v_exp_f32 v6, v6\n"
"v_mfma_f32_16x16x32_bf16 v[12:15], a[144:147], v[112:115], v[36:39]\n"
"ds_read_b64_tr_b16 a[254:255], v243 offset:62208\n"
"v_mfma_f32_16x16x32_bf16 v[12:15], a[148:151], v[116:119], v[12:15]\n"
"v_exp_f32 v7, v7\n"
"v_mfma_f32_16x16x32_bf16 v[12:15], a[152:155], v[120:123], v[12:15]\n"
"v_cvt_pk_bf16_f32 v19, v6, v7\n"
"v_mfma_f32_16x16x32_bf16 v[12:15], a[156:159], v[124:127], v[12:15]\n"
"buffer_load_dwordx4 v247, s[12:15], s84 offen offset:3072 lds\n"
"v_mfma_f32_16x16x32_bf16 a[64:67], a[192:195], v[16:19], a[64:67]\n"
"s_add_u32 s84, s84, 16384\n"
"v_exp_f32 v8, v8\n"
"v_mfma_f32_16x16x32_bf16 a[68:71], a[196:199], v[16:19], a[68:71]\n"
"s_nop 0\n"
"v_mfma_f32_16x16x32_bf16 a[72:75], a[200:203], v[16:19], a[72:75]\n"
"v_exp_f32 v9, v9\n"
"v_mfma_f32_16x16x32_bf16 a[76:79], a[204:207], v[16:19], a[76:79]\n"
"v_cvt_pk_bf16_f32 v20, v8, v9\n"
"v_mfma_f32_16x16x32_bf16 a[80:83], a[208:211], v[16:19], a[80:83]\n"
"v_exp_f32 v10, v10\n"
"v_mfma_f32_16x16x32_bf16 a[84:87], a[212:215], v[16:19], a[84:87]\n"
"s_nop 0\n"
"v_mfma_f32_16x16x32_bf16 a[88:91], a[216:219], v[16:19], a[88:91]\n"
"v_exp_f32 v11, v11\n"
"v_mfma_f32_16x16x32_bf16 a[92:95], a[220:223], v[16:19], a[92:95]\n"
"s_waitcnt vmcnt(18) lgkmcnt(0)\n"
"s_barrier\n"
"v_cvt_pk_bf16_f32 v21, v10, v11\n"
"v_mfma_f32_16x16x32_bf16 v[52:55], v[40:43], v[16:19], v[52:55]\n"
"v_exp_f32 v12, v12\n"
"v_mfma_f32_16x16x32_bf16 v[0:3], a[160:163], v[64:67], v[24:27]\n"
"ds_read_b128 a[128:131], v241 offset:0\n"
"v_mfma_f32_16x16x32_bf16 v[0:3], a[164:167], v[68:71], v[0:3]\n"
"v_exp_f32 v13, v13\n"
"v_mfma_f32_16x16x32_bf16 v[0:3], a[168:171], v[72:75], v[0:3]\n"
"ds_read_b128 a[132:135], v241 offset:256\n"
"v_cvt_pk_bf16_f32 v22, v12, v13\n"
"v_mfma_f32_16x16x32_bf16 v[0:3], a[172:175], v[76:79], v[0:3]\n"
"v_exp_f32 v14, v14\n"
"v_mfma_f32_16x16x32_bf16 v[4:7], a[176:179], v[64:67], v[24:27]\n"
"ds_read_b128 a[136:139], v241 offset:512\n"
"v_mfma_f32_16x16x32_bf16 v[4:7], a[180:183], v[68:71], v[4:7]\n"
"v_exp_f32 v15, v15\n"
"v_mfma_f32_16x16x32_bf16 v[4:7], a[184:187], v[72:75], v[4:7]\n"
"s_add_u32 m0, s86, 32768\n"
"ds_read_b128 a[140:143], v241 offset:768\n"
"v_cvt_pk_bf16_f32 v23, v14, v15\n"
"v_mfma_f32_16x16x32_bf16 v[4:7], a[188:191], v[76:79], v[4:7]\n"
"buffer_load_dwordx4 v246, s[8:11], s84 offen offset:0 lds\n"
"v_mfma_f32_16x16x32_bf16 a[96:99], a[192:195], v[20:23], a[96:99]\n"
"v_exp_f32 v0, v0\n"
"v_mfma_f32_16x16x32_bf16 a[100:103], a[196:199], v[20:23], a[100:103]\n"
"ds_read_b128 a[144:147], v241 offset:4096\n"
"v_mfma_f32_16x16x32_bf16 a[104:107], a[200:203], v[20:23], a[104:107]\n"
"v_exp_f32 v1, v1\n"
"v_mfma_f32_16x16x32_bf16 a[108:111], a[204:207], v[20:23], a[108:111]\n"
"ds_read_b128 a[148:151], v241 offset:4352\n"
"v_cvt_pk_bf16_f32 v16, v0, v1\n"
"v_mfma_f32_16x16x32_bf16 a[112:115], a[208:211], v[20:23], a[112:115]\n"
"v_exp_f32 v2, v2\n"
"v_mfma_f32_16x16x32_bf16 a[116:119], a[212:215], v[20:23], a[116:119]\n"
"ds_read_b128 a[152:155], v241 offset:4608\n"
"v_mfma_f32_16x16x32_bf16 a[120:123], a[216:219], v[20:23], a[120:123]\n"
"v_exp_f32 v3, v3\n"
"v_mfma_f32_16x16x32_bf16 a[124:127], a[220:223], v[20:23], a[124:127]\n"
"buffer_load_dword v250, v215, s[68:71], 0 offen\n"
"v_cvt_pk_bf16_f32 v17, v2, v3\n"
"v_mfma_f32_16x16x32_bf16 v[56:59], v[40:43], v[20:23], v[56:59]\n"
"v_exp_f32 v4, v4\n"
"v_mfma_f32_16x16x32_bf16 v[8:11], a[160:163], v[80:83], v[28:31]\n"
"ds_read_b128 a[156:159], v241 offset:4864\n"
"v_add_u32 v215, 1179648, v215\n"
"v_mfma_f32_16x16x32_bf16 v[8:11], a[164:167], v[84:87], v[8:11]\n"
"v_exp_f32 v5, v5\n"
"v_mfma_f32_16x16x32_bf16 v[8:11], a[168:171], v[88:91], v[8:11]\n"
"ds_read_b64_tr_b16 a[192:193], v244 offset:16384\n"
"v_cvt_pk_bf16_f32 v18, v4, v5\n"
"v_mfma_f32_16x16x32_bf16 v[8:11], a[172:175], v[92:95], v[8:11]\n"
"v_exp_f32 v6, v6\n"
"v_mfma_f32_16x16x32_bf16 v[12:15], a[176:179], v[80:83], v[28:31]\n"
"ds_read_b64_tr_b16 a[194:195], v244 offset:20480\n"
"v_mfma_f32_16x16x32_bf16 v[12:15], a[180:183], v[84:87], v[12:15]\n"
"v_exp_f32 v7, v7\n"
"v_mfma_f32_16x16x32_bf16 v[12:15], a[184:187], v[88:91], v[12:15]\n"
"ds_read_b64_tr_b16 a[196:197], v245 offset:16384\n"
"v_cvt_pk_bf16_f32 v19, v6, v7\n"
"v_mfma_f32_16x16x32_bf16 v[12:15], a[188:191], v[92:95], v[12:15]\n"
"s_waitcnt lgkmcnt(11)\n"
"buffer_load_dwordx4 v246, s[8:11], s84 offen offset:1024 lds\n"
"v_mfma_f32_16x16x32_bf16 a[0:3], a[224:227], v[16:19], a[0:3]\n"
"v_exp_f32 v8, v8\n"
"v_mfma_f32_16x16x32_bf16 a[4:7], a[228:231], v[16:19], a[4:7]\n"
"ds_read_b64_tr_b16 a[198:199], v245 offset:20480\n"
"v_mfma_f32_16x16x32_bf16 a[8:11], a[232:235], v[16:19], a[8:11]\n"
"v_exp_f32 v9, v9\n"
"v_mfma_f32_16x16x32_bf16 a[12:15], a[236:239], v[16:19], a[12:15]\n"
"ds_read_b64_tr_b16 a[200:201], v244 offset:16640\n"
"v_cvt_pk_bf16_f32 v20, v8, v9\n"
"v_mfma_f32_16x16x32_bf16 a[16:19], a[240:243], v[16:19], a[16:19]\n"
"v_exp_f32 v10, v10\n"
"v_mfma_f32_16x16x32_bf16 a[20:23], a[244:247], v[16:19], a[20:23]\n"
"ds_read_b64_tr_b16 a[202:203], v244 offset:20736\n"
"v_mfma_f32_16x16x32_bf16 a[24:27], a[248:251], v[16:19], a[24:27]\n"
"v_exp_f32 v11, v11\n"
"v_mfma_f32_16x16x32_bf16 a[28:31], a[252:255], v[16:19], a[28:31]\n"
"v_cvt_pk_bf16_f32 v21, v10, v11\n"
"v_mfma_f32_16x16x32_bf16 v[44:47], v[40:43], v[16:19], v[44:47]\n"
"v_exp_f32 v12, v12\n"
"v_mfma_f32_16x16x32_bf16 v[0:3], a[160:163], v[96:99], v[32:35]\n"
"ds_read_b64_tr_b16 a[204:205], v245 offset:16640\n"
"v_mfma_f32_16x16x32_bf16 v[0:3], a[164:167], v[100:103], v[0:3]\n"
"v_exp_f32 v13, v13\n"
"v_mfma_f32_16x16x32_bf16 v[0:3], a[168:171], v[104:107], v[0:3]\n"
"ds_read_b64_tr_b16 a[206:207], v245 offset:20736\n"
"v_cvt_pk_bf16_f32 v22, v12, v13\n"
"v_mfma_f32_16x16x32_bf16 v[0:3], a[172:175], v[108:111], v[0:3]\n"
"v_exp_f32 v14, v14\n"
"v_mfma_f32_16x16x32_bf16 v[4:7], a[176:179], v[96:99], v[32:35]\n"
"ds_read_b64_tr_b16 a[208:209], v244 offset:16896\n"
"v_mfma_f32_16x16x32_bf16 v[4:7], a[180:183], v[100:103], v[4:7]\n"
"v_exp_f32 v15, v15\n"
"v_mfma_f32_16x16x32_bf16 v[4:7], a[184:187], v[104:107], v[4:7]\n"
"ds_read_b64_tr_b16 a[210:211], v244 offset:20992\n"
"v_cvt_pk_bf16_f32 v23, v14, v15\n"
"v_mfma_f32_16x16x32_bf16 v[4:7], a[188:191], v[108:111], v[4:7]\n"
"buffer_load_dwordx4 v247, s[8:11], s84 offen offset:2048 lds\n"
"v_mfma_f32_16x16x32_bf16 a[32:35], a[224:227], v[20:23], a[32:35]\n"
"v_exp_f32 v0, v0\n"
"v_mfma_f32_16x16x32_bf16 a[36:39], a[228:231], v[20:23], a[36:39]\n"
"ds_read_b64_tr_b16 a[212:213], v245 offset:16896\n"
"v_mfma_f32_16x16x32_bf16 a[40:43], a[232:235], v[20:23], a[40:43]\n"
"v_exp_f32 v1, v1\n"
"v_mfma_f32_16x16x32_bf16 a[44:47], a[236:239], v[20:23], a[44:47]\n"
"ds_read_b64_tr_b16 a[214:215], v245 offset:20992\n"
"v_cvt_pk_bf16_f32 v16, v0, v1\n"
"v_mfma_f32_16x16x32_bf16 a[48:51], a[240:243], v[20:23], a[48:51]\n"
"v_exp_f32 v2, v2\n"
"v_mfma_f32_16x16x32_bf16 a[52:55], a[244:247], v[20:23], a[52:55]\n"
"ds_read_b64_tr_b16 a[216:217], v244 offset:17152\n"
"v_mfma_f32_16x16x32_bf16 a[56:59], a[248:251], v[20:23], a[56:59]\n"
"v_exp_f32 v3, v3\n"
"v_mfma_f32_16x16x32_bf16 a[60:63], a[252:255], v[20:23], a[60:63]\n"
"v_cvt_pk_bf16_f32 v17, v2, v3\n"
"v_mfma_f32_16x16x32_bf16 v[48:51], v[40:43], v[20:23], v[48:51]\n"
"v_exp_f32 v4, v4\n"
"v_mfma_f32_16x16x32_bf16 v[8:11], a[160:163], v[112:115], v[36:39]\n"
"ds_read_b64_tr_b16 a[218:219], v244 offset:21248\n"
"v_mfma_f32_16x16x32_bf16 v[8:11], a[164:167], v[116:119], v[8:11]\n"
"v_exp_f32 v5, v5\n"
"v_mfma_f32_16x16x32_bf16 v[8:11], a[168:171], v[120:123], v[8:11]\n"
"ds_read_b64_tr_b16 a[220:221], v245 offset:17152\n"
"v_cvt_pk_bf16_f32 v18, v4, v5\n"
"v_mfma_f32_16x16x32_bf16 v[8:11], a[172:175], v[124:127], v[8:11]\n"
"v_exp_f32 v6, v6\n"
"v_mfma_f32_16x16x32_bf16 v[12:15], a[176:179], v[112:115], v[36:39]\n"
"ds_read_b64_tr_b16 a[222:223], v245 offset:21248\n"
"v_mfma_f32_16x16x32_bf16 v[12:15], a[180:183], v[116:119], v[12:15]\n"
"v_exp_f32 v7, v7\n"
"v_mfma_f32_16x16x32_bf16 v[12:15], a[184:187], v[120:123], v[12:15]\n"
"v_cvt_pk_bf16_f32 v19, v6, v7\n"
"v_mfma_f32_16x16x32_bf16 v[12:15], a[188:191], v[124:127], v[12:15]\n"
"buffer_load_dwordx4 v247, s[8:11], s84 offen offset:3072 lds\n"
"v_mfma_f32_16x16x32_bf16 a[64:67], a[224:227], v[16:19], a[64:67]\n"
"v_exp_f32 v8, v8\n"
"v_mfma_f32_16x16x32_bf16 a[68:71], a[228:231], v[16:19], a[68:71]\n"
"s_nop 0\n"
"v_mfma_f32_16x16x32_bf16 a[72:75], a[232:235], v[16:19], a[72:75]\n"
"v_exp_f32 v9, v9\n"
"v_mfma_f32_16x16x32_bf16 a[76:79], a[236:239], v[16:19], a[76:79]\n"
"v_cvt_pk_bf16_f32 v20, v8, v9\n"
"v_mfma_f32_16x16x32_bf16 a[80:83], a[240:243], v[16:19], a[80:83]\n"
"v_exp_f32 v10, v10\n"
"v_mfma_f32_16x16x32_bf16 a[84:87], a[244:247], v[16:19], a[84:87]\n"
"s_nop 0\n"
"v_mfma_f32_16x16x32_bf16 a[88:91], a[248:251], v[16:19], a[88:91]\n"
"v_exp_f32 v11, v11\n"
"v_mfma_f32_16x16x32_bf16 a[92:95], a[252:255], v[16:19], a[92:95]\n"
"s_waitcnt lgkmcnt(8)\n"
"v_cvt_pk_bf16_f32 v21, v10, v11\n"
"v_mfma_f32_16x16x32_bf16 v[52:55], v[40:43], v[16:19], v[52:55]\n"
"v_exp_f32 v12, v12\n"
"v_mfma_f32_16x16x32_bf16 v[0:3], a[128:131], v[64:67], v[24:27]\n"
"ds_read_b128 a[160:163], v241 offset:8192\n"
"v_mfma_f32_16x16x32_bf16 v[0:3], a[132:135], v[68:71], v[0:3]\n"
"v_exp_f32 v13, v13\n"
"v_mfma_f32_16x16x32_bf16 v[0:3], a[136:139], v[72:75], v[0:3]\n"
"ds_read_b128 a[164:167], v241 offset:8448\n"
"v_cvt_pk_bf16_f32 v22, v12, v13\n"
"v_mfma_f32_16x16x32_bf16 v[0:3], a[140:143], v[76:79], v[0:3]\n"
"v_exp_f32 v14, v14\n"
"v_mfma_f32_16x16x32_bf16 v[4:7], a[144:147], v[64:67], v[24:27]\n"
"ds_read_b128 a[168:171], v241 offset:8704\n"
"v_mfma_f32_16x16x32_bf16 v[4:7], a[148:151], v[68:71], v[4:7]\n"
"v_exp_f32 v15, v15\n"
"v_mfma_f32_16x16x32_bf16 v[4:7], a[152:155], v[72:75], v[4:7]\n"
"s_add_u32 m0, s86, 49152\n"
"ds_read_b128 a[172:175], v241 offset:8960\n"
"v_cvt_pk_bf16_f32 v23, v14, v15\n"
"v_mfma_f32_16x16x32_bf16 v[4:7], a[156:159], v[76:79], v[4:7]\n"
"buffer_load_dwordx4 v246, s[12:15], s84 offen offset:0 lds\n"
"v_mfma_f32_16x16x32_bf16 a[96:99], a[224:227], v[20:23], a[96:99]\n"
"v_exp_f32 v0, v0\n"
"v_mfma_f32_16x16x32_bf16 a[100:103], a[228:231], v[20:23], a[100:103]\n"
"ds_read_b128 a[176:179], v241 offset:12288\n"
"v_mfma_f32_16x16x32_bf16 a[104:107], a[232:235], v[20:23], a[104:107]\n"
"v_exp_f32 v1, v1\n"
"v_mfma_f32_16x16x32_bf16 a[108:111], a[236:239], v[20:23], a[108:111]\n"
"ds_read_b128 a[180:183], v241 offset:12544\n"
"v_cvt_pk_bf16_f32 v16, v0, v1\n"
"v_mfma_f32_16x16x32_bf16 a[112:115], a[240:243], v[20:23], a[112:115]\n"
"v_exp_f32 v2, v2\n"
"v_mfma_f32_16x16x32_bf16 a[116:119], a[244:247], v[20:23], a[116:119]\n"
"ds_read_b128 a[184:187], v241 offset:12800\n"
"v_mfma_f32_16x16x32_bf16 a[120:123], a[248:251], v[20:23], a[120:123]\n"
"v_exp_f32 v3, v3\n"
"v_mfma_f32_16x16x32_bf16 a[124:127], a[252:255], v[20:23], a[124:127]\n"
"v_cvt_pk_bf16_f32 v17, v2, v3\n"
"v_mfma_f32_16x16x32_bf16 v[56:59], v[40:43], v[20:23], v[56:59]\n"
"v_exp_f32 v4, v4\n"
"v_mfma_f32_16x16x32_bf16 v[8:11], a[128:131], v[80:83], v[28:31]\n"
"ds_read_b128 a[188:191], v241 offset:13056\n"
"v_mfma_f32_16x16x32_bf16 v[8:11], a[132:135], v[84:87], v[8:11]\n"
"v_exp_f32 v5, v5\n"
"v_mfma_f32_16x16x32_bf16 v[8:11], a[136:139], v[88:91], v[8:11]\n"
"ds_read_b64_tr_b16 a[224:225], v244 offset:24576\n"
"v_cvt_pk_bf16_f32 v18, v4, v5\n"
"v_mfma_f32_16x16x32_bf16 v[8:11], a[140:143], v[92:95], v[8:11]\n"
"v_exp_f32 v6, v6\n"
"v_mfma_f32_16x16x32_bf16 v[12:15], a[144:147], v[80:83], v[28:31]\n"
"ds_read_b64_tr_b16 a[226:227], v244 offset:28672\n"
"v_mfma_f32_16x16x32_bf16 v[12:15], a[148:151], v[84:87], v[12:15]\n"
"v_exp_f32 v7, v7\n"
"v_mfma_f32_16x16x32_bf16 v[12:15], a[152:155], v[88:91], v[12:15]\n"
"ds_read_b64_tr_b16 a[228:229], v245 offset:24576\n"
"v_cvt_pk_bf16_f32 v19, v6, v7\n"
"v_mfma_f32_16x16x32_bf16 v[12:15], a[156:159], v[92:95], v[12:15]\n"
"s_waitcnt lgkmcnt(11)\n"
"buffer_load_dwordx4 v247, s[12:15], s84 offen offset:1024 lds\n"
"v_mfma_f32_16x16x32_bf16 a[0:3], a[192:195], v[16:19], a[0:3]\n"
"v_exp_f32 v8, v8\n"
"v_mfma_f32_16x16x32_bf16 a[4:7], a[196:199], v[16:19], a[4:7]\n"
"ds_read_b64_tr_b16 a[230:231], v245 offset:28672\n"
"v_mfma_f32_16x16x32_bf16 a[8:11], a[200:203], v[16:19], a[8:11]\n"
"v_exp_f32 v9, v9\n"
"v_mfma_f32_16x16x32_bf16 a[12:15], a[204:207], v[16:19], a[12:15]\n"
"ds_read_b64_tr_b16 a[232:233], v244 offset:24832\n"
"v_cvt_pk_bf16_f32 v20, v8, v9\n"
"v_mfma_f32_16x16x32_bf16 a[16:19], a[208:211], v[16:19], a[16:19]\n"
"v_exp_f32 v10, v10\n"
"v_mfma_f32_16x16x32_bf16 a[20:23], a[212:215], v[16:19], a[20:23]\n"
"ds_read_b64_tr_b16 a[234:235], v244 offset:28928\n"
"v_mfma_f32_16x16x32_bf16 a[24:27], a[216:219], v[16:19], a[24:27]\n"
"v_exp_f32 v11, v11\n"
"v_mfma_f32_16x16x32_bf16 a[28:31], a[220:223], v[16:19], a[28:31]\n"
"v_cvt_pk_bf16_f32 v21, v10, v11\n"
"v_mfma_f32_16x16x32_bf16 v[44:47], v[40:43], v[16:19], v[44:47]\n"
"v_exp_f32 v12, v12\n"
"v_mfma_f32_16x16x32_bf16 v[0:3], a[128:131], v[96:99], v[32:35]\n"
"ds_read_b64_tr_b16 a[236:237], v245 offset:24832\n"
"v_mfma_f32_16x16x32_bf16 v[0:3], a[132:135], v[100:103], v[0:3]\n"
"v_exp_f32 v13, v13\n"
"v_mfma_f32_16x16x32_bf16 v[0:3], a[136:139], v[104:107], v[0:3]\n"
"ds_read_b64_tr_b16 a[238:239], v245 offset:28928\n"
"v_cvt_pk_bf16_f32 v22, v12, v13\n"
"v_mfma_f32_16x16x32_bf16 v[0:3], a[140:143], v[108:111], v[0:3]\n"
"v_exp_f32 v14, v14\n"
"v_mfma_f32_16x16x32_bf16 v[4:7], a[144:147], v[96:99], v[32:35]\n"
"ds_read_b64_tr_b16 a[240:241], v244 offset:25088\n"
"v_mfma_f32_16x16x32_bf16 v[4:7], a[148:151], v[100:103], v[4:7]\n"
"v_exp_f32 v15, v15\n"
"v_mfma_f32_16x16x32_bf16 v[4:7], a[152:155], v[104:107], v[4:7]\n"
"ds_read_b64_tr_b16 a[242:243], v244 offset:29184\n"
"v_cvt_pk_bf16_f32 v23, v14, v15\n"
"v_mfma_f32_16x16x32_bf16 v[4:7], a[156:159], v[108:111], v[4:7]\n"
"buffer_load_dwordx4 v246, s[12:15], s84 offen offset:2048 lds\n"
"v_mfma_f32_16x16x32_bf16 a[32:35], a[192:195], v[20:23], a[32:35]\n"
"v_exp_f32 v0, v0\n"
"v_mfma_f32_16x16x32_bf16 a[36:39], a[196:199], v[20:23], a[36:39]\n"
"ds_read_b64_tr_b16 a[244:245], v245 offset:25088\n"
"v_mfma_f32_16x16x32_bf16 a[40:43], a[200:203], v[20:23], a[40:43]\n"
"v_exp_f32 v1, v1\n"
"v_mfma_f32_16x16x32_bf16 a[44:47], a[204:207], v[20:23], a[44:47]\n"
"ds_read_b64_tr_b16 a[246:247], v245 offset:29184\n"
"v_cvt_pk_bf16_f32 v16, v0, v1\n"
"v_mfma_f32_16x16x32_bf16 a[48:51], a[208:211], v[20:23], a[48:51]\n"
"v_exp_f32 v2, v2\n"
"v_mfma_f32_16x16x32_bf16 a[52:55], a[212:215], v[20:23], a[52:55]\n"
"ds_read_b64_tr_b16 a[248:249], v244 offset:25344\n"
"v_mfma_f32_16x16x32_bf16 a[56:59], a[216:219], v[20:23], a[56:59]\n"
"v_exp_f32 v3, v3\n"
"v_mfma_f32_16x16x32_bf16 a[60:63], a[220:223], v[20:23], a[60:63]\n"
"v_cvt_pk_bf16_f32 v17, v2, v3\n"
"v_mfma_f32_16x16x32_bf16 v[48:51], v[40:43], v[20:23], v[48:51]\n"
"v_exp_f32 v4, v4\n"
"v_mfma_f32_16x16x32_bf16 v[8:11], a[128:131], v[112:115], v[36:39]\n"
"ds_read_b64_tr_b16 a[250:251], v244 offset:29440\n"
"v_mfma_f32_16x16x32_bf16 v[8:11], a[132:135], v[116:119], v[8:11]\n"
"v_exp_f32 v5, v5\n"
"v_mfma_f32_16x16x32_bf16 v[8:11], a[136:139], v[120:123], v[8:11]\n"
"ds_read_b64_tr_b16 a[252:253], v245 offset:25344\n"
"v_cvt_pk_bf16_f32 v18, v4, v5\n"
"v_mfma_f32_16x16x32_bf16 v[8:11], a[140:143], v[124:127], v[8:11]\n"
"v_exp_f32 v6, v6\n"
"v_mfma_f32_16x16x32_bf16 v[12:15], a[144:147], v[112:115], v[36:39]\n"
"ds_read_b64_tr_b16 a[254:255], v245 offset:29440\n"
"v_mfma_f32_16x16x32_bf16 v[12:15], a[148:151], v[116:119], v[12:15]\n"
"v_exp_f32 v7, v7\n"
"v_mfma_f32_16x16x32_bf16 v[12:15], a[152:155], v[120:123], v[12:15]\n"
"v_cvt_pk_bf16_f32 v19, v6, v7\n"
"v_mfma_f32_16x16x32_bf16 v[12:15], a[156:159], v[124:127], v[12:15]\n"
"buffer_load_dwordx4 v247, s[12:15], s84 offen offset:3072 lds\n"
"v_mfma_f32_16x16x32_bf16 a[64:67], a[192:195], v[16:19], a[64:67]\n"
"s_add_u32 s84, s84, 16384\n"
"v_exp_f32 v8, v8\n"
"v_mfma_f32_16x16x32_bf16 a[68:71], a[196:199], v[16:19], a[68:71]\n"
"s_nop 0\n"
"v_mfma_f32_16x16x32_bf16 a[72:75], a[200:203], v[16:19], a[72:75]\n"
"v_exp_f32 v9, v9\n"
"v_mfma_f32_16x16x32_bf16 a[76:79], a[204:207], v[16:19], a[76:79]\n"
"v_cvt_pk_bf16_f32 v20, v8, v9\n"
"v_mfma_f32_16x16x32_bf16 a[80:83], a[208:211], v[16:19], a[80:83]\n"
"v_exp_f32 v10, v10\n"
"v_mfma_f32_16x16x32_bf16 a[84:87], a[212:215], v[16:19], a[84:87]\n"
"s_nop 0\n"
"v_mfma_f32_16x16x32_bf16 a[88:91], a[216:219], v[16:19], a[88:91]\n"
"v_exp_f32 v11, v11\n"
"v_mfma_f32_16x16x32_bf16 a[92:95], a[220:223], v[16:19], a[92:95]\n"
"s_waitcnt vmcnt(18) lgkmcnt(0)\n"
"s_barrier\n"
"v_cvt_pk_bf16_f32 v21, v10, v11\n"
"v_mfma_f32_16x16x32_bf16 v[52:55], v[40:43], v[16:19], v[52:55]\n"
"v_exp_f32 v12, v12\n"
"v_mfma_f32_16x16x32_bf16 v[0:3], a[160:163], v[64:67], v[24:27]\n"
"ds_read_b128 a[128:131], v241 offset:32768\n"
"v_mfma_f32_16x16x32_bf16 v[0:3], a[164:167], v[68:71], v[0:3]\n"
"v_exp_f32 v13, v13\n"
"v_mfma_f32_16x16x32_bf16 v[0:3], a[168:171], v[72:75], v[0:3]\n"
"ds_read_b128 a[132:135], v241 offset:33024\n"
"v_cvt_pk_bf16_f32 v22, v12, v13\n"
"v_mfma_f32_16x16x32_bf16 v[0:3], a[172:175], v[76:79], v[0:3]\n"
"v_exp_f32 v14, v14\n"
"v_mfma_f32_16x16x32_bf16 v[4:7], a[176:179], v[64:67], v[24:27]\n"
"ds_read_b128 a[136:139], v241 offset:33280\n"
"v_mfma_f32_16x16x32_bf16 v[4:7], a[180:183], v[68:71], v[4:7]\n"
"v_exp_f32 v15, v15\n"
"v_mfma_f32_16x16x32_bf16 v[4:7], a[184:187], v[72:75], v[4:7]\n"
"s_add_u32 m0, s86, 65536\n"
"ds_read_b128 a[140:143], v241 offset:33536\n"
"v_cvt_pk_bf16_f32 v23, v14, v15\n"
"v_mfma_f32_16x16x32_bf16 v[4:7], a[188:191], v[76:79], v[4:7]\n"
"buffer_load_dwordx4 v246, s[8:11], s84 offen offset:0 lds\n"
"v_mfma_f32_16x16x32_bf16 a[96:99], a[192:195], v[20:23], a[96:99]\n"
"v_exp_f32 v0, v0\n"
"v_mfma_f32_16x16x32_bf16 a[100:103], a[196:199], v[20:23], a[100:103]\n"
"ds_read_b128 a[144:147], v241 offset:36864\n"
"v_mfma_f32_16x16x32_bf16 a[104:107], a[200:203], v[20:23], a[104:107]\n"
"v_exp_f32 v1, v1\n"
"v_mfma_f32_16x16x32_bf16 a[108:111], a[204:207], v[20:23], a[108:111]\n"
"ds_read_b128 a[148:151], v241 offset:37120\n"
"v_cvt_pk_bf16_f32 v16, v0, v1\n"
"v_mfma_f32_16x16x32_bf16 a[112:115], a[208:211], v[20:23], a[112:115]\n"
"v_exp_f32 v2, v2\n"
"v_mfma_f32_16x16x32_bf16 a[116:119], a[212:215], v[20:23], a[116:119]\n"
"ds_read_b128 a[152:155], v241 offset:37376\n"
"v_mfma_f32_16x16x32_bf16 a[120:123], a[216:219], v[20:23], a[120:123]\n"
"v_exp_f32 v3, v3\n"
"v_mfma_f32_16x16x32_bf16 a[124:127], a[220:223], v[20:23], a[124:127]\n"
"buffer_load_dword v250, v216, s[72:75], s46 offen\n"
"v_cvt_pk_bf16_f32 v17, v2, v3\n"
"v_mfma_f32_16x16x32_bf16 v[56:59], v[40:43], v[20:23], v[56:59]\n"
"v_exp_f32 v4, v4\n"
"v_mfma_f32_16x16x32_bf16 v[8:11], a[160:163], v[80:83], v[28:31]\n"
"ds_read_b128 a[156:159], v241 offset:37632\n"
"v_add_u32 v216, 147456, v216\n"
"v_mfma_f32_16x16x32_bf16 v[8:11], a[164:167], v[84:87], v[8:11]\n"
"v_exp_f32 v5, v5\n"
"v_mfma_f32_16x16x32_bf16 v[8:11], a[168:171], v[88:91], v[8:11]\n"
"ds_read_b64_tr_b16 a[192:193], v244 offset:49152\n"
"v_cvt_pk_bf16_f32 v18, v4, v5\n"
"v_mfma_f32_16x16x32_bf16 v[8:11], a[172:175], v[92:95], v[8:11]\n"
"v_exp_f32 v6, v6\n"
"v_mfma_f32_16x16x32_bf16 v[12:15], a[176:179], v[80:83], v[28:31]\n"
"ds_read_b64_tr_b16 a[194:195], v244 offset:53248\n"
"v_mfma_f32_16x16x32_bf16 v[12:15], a[180:183], v[84:87], v[12:15]\n"
"v_exp_f32 v7, v7\n"
"v_mfma_f32_16x16x32_bf16 v[12:15], a[184:187], v[88:91], v[12:15]\n"
"ds_read_b64_tr_b16 a[196:197], v245 offset:49152\n"
"v_cvt_pk_bf16_f32 v19, v6, v7\n"
"v_mfma_f32_16x16x32_bf16 v[12:15], a[188:191], v[92:95], v[12:15]\n"
"s_waitcnt lgkmcnt(11)\n"
"buffer_load_dwordx4 v246, s[8:11], s84 offen offset:1024 lds\n"
"v_mfma_f32_16x16x32_bf16 a[0:3], a[224:227], v[16:19], a[0:3]\n"
"v_exp_f32 v8, v8\n"
"v_mfma_f32_16x16x32_bf16 a[4:7], a[228:231], v[16:19], a[4:7]\n"
"ds_read_b64_tr_b16 a[198:199], v245 offset:53248\n"
"v_mfma_f32_16x16x32_bf16 a[8:11], a[232:235], v[16:19], a[8:11]\n"
"v_exp_f32 v9, v9\n"
"v_mfma_f32_16x16x32_bf16 a[12:15], a[236:239], v[16:19], a[12:15]\n"
"ds_read_b64_tr_b16 a[200:201], v244 offset:49408\n"
"v_cvt_pk_bf16_f32 v20, v8, v9\n"
"v_mfma_f32_16x16x32_bf16 a[16:19], a[240:243], v[16:19], a[16:19]\n"
"v_exp_f32 v10, v10\n"
"v_mfma_f32_16x16x32_bf16 a[20:23], a[244:247], v[16:19], a[20:23]\n"
"ds_read_b64_tr_b16 a[202:203], v244 offset:53504\n"
"v_mfma_f32_16x16x32_bf16 a[24:27], a[248:251], v[16:19], a[24:27]\n"
"v_exp_f32 v11, v11\n"
"v_mfma_f32_16x16x32_bf16 a[28:31], a[252:255], v[16:19], a[28:31]\n"
"v_cvt_pk_bf16_f32 v21, v10, v11\n"
"v_mfma_f32_16x16x32_bf16 v[44:47], v[40:43], v[16:19], v[44:47]\n"
"v_exp_f32 v12, v12\n"
"v_mfma_f32_16x16x32_bf16 v[0:3], a[160:163], v[96:99], v[32:35]\n"
"ds_read_b64_tr_b16 a[204:205], v245 offset:49408\n"
"v_mfma_f32_16x16x32_bf16 v[0:3], a[164:167], v[100:103], v[0:3]\n"
"v_exp_f32 v13, v13\n"
"v_mfma_f32_16x16x32_bf16 v[0:3], a[168:171], v[104:107], v[0:3]\n"
"ds_read_b64_tr_b16 a[206:207], v245 offset:53504\n"
"v_cvt_pk_bf16_f32 v22, v12, v13\n"
"v_mfma_f32_16x16x32_bf16 v[0:3], a[172:175], v[108:111], v[0:3]\n"
"v_exp_f32 v14, v14\n"
"v_mfma_f32_16x16x32_bf16 v[4:7], a[176:179], v[96:99], v[32:35]\n"
"ds_read_b64_tr_b16 a[208:209], v244 offset:49664\n"
"v_mfma_f32_16x16x32_bf16 v[4:7], a[180:183], v[100:103], v[4:7]\n"
"v_exp_f32 v15, v15\n"
"v_mfma_f32_16x16x32_bf16 v[4:7], a[184:187], v[104:107], v[4:7]\n"
"ds_read_b64_tr_b16 a[210:211], v244 offset:53760\n"
"v_cvt_pk_bf16_f32 v23, v14, v15\n"
"v_mfma_f32_16x16x32_bf16 v[4:7], a[188:191], v[108:111], v[4:7]\n"
"buffer_load_dwordx4 v247, s[8:11], s84 offen offset:2048 lds\n"
"v_mfma_f32_16x16x32_bf16 a[32:35], a[224:227], v[20:23], a[32:35]\n"
"v_exp_f32 v0, v0\n"
"v_mfma_f32_16x16x32_bf16 a[36:39], a[228:231], v[20:23], a[36:39]\n"
"ds_read_b64_tr_b16 a[212:213], v245 offset:49664\n"
"v_mfma_f32_16x16x32_bf16 a[40:43], a[232:235], v[20:23], a[40:43]\n"
"v_exp_f32 v1, v1\n"
"v_mfma_f32_16x16x32_bf16 a[44:47], a[236:239], v[20:23], a[44:47]\n"
"ds_read_b64_tr_b16 a[214:215], v245 offset:53760\n"
"v_cvt_pk_bf16_f32 v16, v0, v1\n"
"v_mfma_f32_16x16x32_bf16 a[48:51], a[240:243], v[20:23], a[48:51]\n"
"v_exp_f32 v2, v2\n"
"v_mfma_f32_16x16x32_bf16 a[52:55], a[244:247], v[20:23], a[52:55]\n"
"ds_read_b64_tr_b16 a[216:217], v244 offset:49920\n"
"v_mfma_f32_16x16x32_bf16 a[56:59], a[248:251], v[20:23], a[56:59]\n"
"v_exp_f32 v3, v3\n"
"v_mfma_f32_16x16x32_bf16 a[60:63], a[252:255], v[20:23], a[60:63]\n"
"v_cvt_pk_bf16_f32 v17, v2, v3\n"
"v_mfma_f32_16x16x32_bf16 v[48:51], v[40:43], v[20:23], v[48:51]\n"
"v_exp_f32 v4, v4\n"
"v_mfma_f32_16x16x32_bf16 v[8:11], a[160:163], v[112:115], v[36:39]\n"
"ds_read_b64_tr_b16 a[218:219], v244 offset:54016\n"
"v_mfma_f32_16x16x32_bf16 v[8:11], a[164:167], v[116:119], v[8:11]\n"
"v_exp_f32 v5, v5\n"
"v_mfma_f32_16x16x32_bf16 v[8:11], a[168:171], v[120:123], v[8:11]\n"
"ds_read_b64_tr_b16 a[220:221], v245 offset:49920\n"
"v_cvt_pk_bf16_f32 v18, v4, v5\n"
"v_mfma_f32_16x16x32_bf16 v[8:11], a[172:175], v[124:127], v[8:11]\n"
"v_exp_f32 v6, v6\n"
"v_mfma_f32_16x16x32_bf16 v[12:15], a[176:179], v[112:115], v[36:39]\n"
"ds_read_b64_tr_b16 a[222:223], v245 offset:54016\n"
"v_mfma_f32_16x16x32_bf16 v[12:15], a[180:183], v[116:119], v[12:15]\n"
"v_exp_f32 v7, v7\n"
"v_mfma_f32_16x16x32_bf16 v[12:15], a[184:187], v[120:123], v[12:15]\n"
"v_cvt_pk_bf16_f32 v19, v6, v7\n"
"v_mfma_f32_16x16x32_bf16 v[12:15], a[188:191], v[124:127], v[12:15]\n"
"buffer_load_dwordx4 v247, s[8:11], s84 offen offset:3072 lds\n"
"v_mfma_f32_16x16x32_bf16 a[64:67], a[224:227], v[16:19], a[64:67]\n"
"v_exp_f32 v8, v8\n"
"v_mfma_f32_16x16x32_bf16 a[68:71], a[228:231], v[16:19], a[68:71]\n"
"s_nop 0\n"
"v_mfma_f32_16x16x32_bf16 a[72:75], a[232:235], v[16:19], a[72:75]\n"
"v_exp_f32 v9, v9\n"
"v_mfma_f32_16x16x32_bf16 a[76:79], a[236:239], v[16:19], a[76:79]\n"
"v_cvt_pk_bf16_f32 v20, v8, v9\n"
"v_mfma_f32_16x16x32_bf16 a[80:83], a[240:243], v[16:19], a[80:83]\n"
"v_exp_f32 v10, v10\n"
"v_mfma_f32_16x16x32_bf16 a[84:87], a[244:247], v[16:19], a[84:87]\n"
"s_nop 0\n"
"v_mfma_f32_16x16x32_bf16 a[88:91], a[248:251], v[16:19], a[88:91]\n"
"v_exp_f32 v11, v11\n"
"v_mfma_f32_16x16x32_bf16 a[92:95], a[252:255], v[16:19], a[92:95]\n"
"s_waitcnt lgkmcnt(8)\n"
"v_cvt_pk_bf16_f32 v21, v10, v11\n"
"v_mfma_f32_16x16x32_bf16 v[52:55], v[40:43], v[16:19], v[52:55]\n"
"v_exp_f32 v12, v12\n"
"v_mfma_f32_16x16x32_bf16 v[0:3], a[128:131], v[64:67], v[24:27]\n"
"ds_read_b128 a[160:163], v241 offset:40960\n"
"v_mfma_f32_16x16x32_bf16 v[0:3], a[132:135], v[68:71], v[0:3]\n"
"v_exp_f32 v13, v13\n"
"v_mfma_f32_16x16x32_bf16 v[0:3], a[136:139], v[72:75], v[0:3]\n"
"ds_read_b128 a[164:167], v241 offset:41216\n"
"v_cvt_pk_bf16_f32 v22, v12, v13\n"
"v_mfma_f32_16x16x32_bf16 v[0:3], a[140:143], v[76:79], v[0:3]\n"
"v_exp_f32 v14, v14\n"
"v_mfma_f32_16x16x32_bf16 v[4:7], a[144:147], v[64:67], v[24:27]\n"
"ds_read_b128 a[168:171], v241 offset:41472\n"
"v_mfma_f32_16x16x32_bf16 v[4:7], a[148:151], v[68:71], v[4:7]\n"
"v_exp_f32 v15, v15\n"
"v_mfma_f32_16x16x32_bf16 v[4:7], a[152:155], v[72:75], v[4:7]\n"
"s_add_u32 m0, s86, 81920\n"
"ds_read_b128 a[172:175], v241 offset:41728\n"
"v_cvt_pk_bf16_f32 v23, v14, v15\n"
"v_mfma_f32_16x16x32_bf16 v[4:7], a[156:159], v[76:79], v[4:7]\n"
"buffer_load_dwordx4 v246, s[12:15], s84 offen offset:0 lds\n"
"v_mfma_f32_16x16x32_bf16 a[96:99], a[224:227], v[20:23], a[96:99]\n"
"v_exp_f32 v0, v0\n"
"v_mfma_f32_16x16x32_bf16 a[100:103], a[228:231], v[20:23], a[100:103]\n"
"ds_read_b128 a[176:179], v241 offset:45056\n"
"v_mfma_f32_16x16x32_bf16 a[104:107], a[232:235], v[20:23], a[104:107]\n"
"v_exp_f32 v1, v1\n"
"v_mfma_f32_16x16x32_bf16 a[108:111], a[236:239], v[20:23], a[108:111]\n"
"ds_read_b128 a[180:183], v241 offset:45312\n"
"v_cvt_pk_bf16_f32 v16, v0, v1\n"
"v_mfma_f32_16x16x32_bf16 a[112:115], a[240:243], v[20:23], a[112:115]\n"
"v_exp_f32 v2, v2\n"
"v_mfma_f32_16x16x32_bf16 a[116:119], a[244:247], v[20:23], a[116:119]\n"
"ds_read_b128 a[184:187], v241 offset:45568\n"
"v_mfma_f32_16x16x32_bf16 a[120:123], a[248:251], v[20:23], a[120:123]\n"
"v_exp_f32 v3, v3\n"
"v_mfma_f32_16x16x32_bf16 a[124:127], a[252:255], v[20:23], a[124:127]\n"
"v_cvt_pk_bf16_f32 v17, v2, v3\n"
"v_mfma_f32_16x16x32_bf16 v[56:59], v[40:43], v[20:23], v[56:59]\n"
"v_exp_f32 v4, v4\n"
"v_mfma_f32_16x16x32_bf16 v[8:11], a[128:131], v[80:83], v[28:31]\n"
"ds_read_b128 a[188:191], v241 offset:45824\n"
"v_mfma_f32_16x16x32_bf16 v[8:11], a[132:135], v[84:87], v[8:11]\n"
"v_exp_f32 v5, v5\n"
"v_mfma_f32_16x16x32_bf16 v[8:11], a[136:139], v[88:91], v[8:11]\n"
"ds_read_b64_tr_b16 a[224:225], v244 offset:57344\n"
"v_cvt_pk_bf16_f32 v18, v4, v5\n"
"v_mfma_f32_16x16x32_bf16 v[8:11], a[140:143], v[92:95], v[8:11]\n"
"v_exp_f32 v6, v6\n"
"v_mfma_f32_16x16x32_bf16 v[12:15], a[144:147], v[80:83], v[28:31]\n"
"ds_read_b64_tr_b16 a[226:227], v244 offset:61440\n"
"v_mfma_f32_16x16x32_bf16 v[12:15], a[148:151], v[84:87], v[12:15]\n"
"v_exp_f32 v7, v7\n"
"v_mfma_f32_16x16x32_bf16 v[12:15], a[152:155], v[88:91], v[12:15]\n"
"ds_read_b64_tr_b16 a[228:229], v245 offset:57344\n"
"v_cvt_pk_bf16_f32 v19, v6, v7\n"
"v_mfma_f32_16x16x32_bf16 v[12:15], a[156:159], v[92:95], v[12:15]\n"
"s_waitcnt lgkmcnt(11)\n"
"buffer_load_dwordx4 v247, s[12:15], s84 offen offset:1024 lds\n"
"v_mfma_f32_16x16x32_bf16 a[0:3], a[192:195], v[16:19], a[0:3]\n"
"v_exp_f32 v8, v8\n"
"v_mfma_f32_16x16x32_bf16 a[4:7], a[196:199], v[16:19], a[4:7]\n"
"ds_read_b64_tr_b16 a[230:231], v245 offset:61440\n"
"v_mfma_f32_16x16x32_bf16 a[8:11], a[200:203], v[16:19], a[8:11]\n"
"v_exp_f32 v9, v9\n"
"v_mfma_f32_16x16x32_bf16 a[12:15], a[204:207], v[16:19], a[12:15]\n"
"ds_read_b64_tr_b16 a[232:233], v244 offset:57600\n"
"v_cvt_pk_bf16_f32 v20, v8, v9\n"
"v_mfma_f32_16x16x32_bf16 a[16:19], a[208:211], v[16:19], a[16:19]\n"
"v_exp_f32 v10, v10\n"
"v_mfma_f32_16x16x32_bf16 a[20:23], a[212:215], v[16:19], a[20:23]\n"
"ds_read_b64_tr_b16 a[234:235], v244 offset:61696\n"
"v_mfma_f32_16x16x32_bf16 a[24:27], a[216:219], v[16:19], a[24:27]\n"
"v_exp_f32 v11, v11\n"
"v_mfma_f32_16x16x32_bf16 a[28:31], a[220:223], v[16:19], a[28:31]\n"
"v_cvt_pk_bf16_f32 v21, v10, v11\n"
"v_mfma_f32_16x16x32_bf16 v[44:47], v[40:43], v[16:19], v[44:47]\n"
"v_exp_f32 v12, v12\n"
"v_mfma_f32_16x16x32_bf16 v[0:3], a[128:131], v[96:99], v[32:35]\n"
"ds_read_b64_tr_b16 a[236:237], v245 offset:57600\n"
"v_mfma_f32_16x16x32_bf16 v[0:3], a[132:135], v[100:103], v[0:3]\n"
"v_exp_f32 v13, v13\n"
"v_mfma_f32_16x16x32_bf16 v[0:3], a[136:139], v[104:107], v[0:3]\n"
"ds_read_b64_tr_b16 a[238:239], v245 offset:61696\n"
"v_cvt_pk_bf16_f32 v22, v12, v13\n"
"v_mfma_f32_16x16x32_bf16 v[0:3], a[140:143], v[108:111], v[0:3]\n"
"v_exp_f32 v14, v14\n"
"v_mfma_f32_16x16x32_bf16 v[4:7], a[144:147], v[96:99], v[32:35]\n"
"ds_read_b64_tr_b16 a[240:241], v244 offset:57856\n"
"v_mfma_f32_16x16x32_bf16 v[4:7], a[148:151], v[100:103], v[4:7]\n"
"v_exp_f32 v15, v15\n"
"v_mfma_f32_16x16x32_bf16 v[4:7], a[152:155], v[104:107], v[4:7]\n"
"ds_read_b64_tr_b16 a[242:243], v244 offset:61952\n"
"v_cvt_pk_bf16_f32 v23, v14, v15\n"
"v_mfma_f32_16x16x32_bf16 v[4:7], a[156:159], v[108:111], v[4:7]\n"
"buffer_load_dwordx4 v246, s[12:15], s84 offen offset:2048 lds\n"
"v_mfma_f32_16x16x32_bf16 a[32:35], a[192:195], v[20:23], a[32:35]\n"
"v_exp_f32 v0, v0\n"
"v_mfma_f32_16x16x32_bf16 a[36:39], a[196:199], v[20:23], a[36:39]\n"
"ds_read_b64_tr_b16 a[244:245], v245 offset:57856\n"
"v_mfma_f32_16x16x32_bf16 a[40:43], a[200:203], v[20:23], a[40:43]\n"
"v_exp_f32 v1, v1\n"
"v_mfma_f32_16x16x32_bf16 a[44:47], a[204:207], v[20:23], a[44:47]\n"
"ds_read_b64_tr_b16 a[246:247], v245 offset:61952\n"
"v_cvt_pk_bf16_f32 v16, v0, v1\n"
"v_mfma_f32_16x16x32_bf16 a[48:51], a[208:211], v[20:23], a[48:51]\n"
"v_exp_f32 v2, v2\n"
"v_mfma_f32_16x16x32_bf16 a[52:55], a[212:215], v[20:23], a[52:55]\n"
"ds_read_b64_tr_b16 a[248:249], v244 offset:58112\n"
"v_mfma_f32_16x16x32_bf16 a[56:59], a[216:219], v[20:23], a[56:59]\n"
"v_exp_f32 v3, v3\n"
"v_mfma_f32_16x16x32_bf16 a[60:63], a[220:223], v[20:23], a[60:63]\n"
"v_cvt_pk_bf16_f32 v17, v2, v3\n"
"v_mfma_f32_16x16x32_bf16 v[48:51], v[40:43], v[20:23], v[48:51]\n"
"v_exp_f32 v4, v4\n"
"v_mfma_f32_16x16x32_bf16 v[8:11], a[128:131], v[112:115], v[36:39]\n"
"ds_read_b64_tr_b16 a[250:251], v244 offset:62208\n"
"v_mfma_f32_16x16x32_bf16 v[8:11], a[132:135], v[116:119], v[8:11]\n"
"v_exp_f32 v5, v5\n"
"v_mfma_f32_16x16x32_bf16 v[8:11], a[136:139], v[120:123], v[8:11]\n"
"ds_read_b64_tr_b16 a[252:253], v245 offset:58112\n"
"v_cvt_pk_bf16_f32 v18, v4, v5\n"
"v_mfma_f32_16x16x32_bf16 v[8:11], a[140:143], v[124:127], v[8:11]\n"
"v_exp_f32 v6, v6\n"
"v_mfma_f32_16x16x32_bf16 v[12:15], a[144:147], v[112:115], v[36:39]\n"
"ds_read_b64_tr_b16 a[254:255], v245 offset:62208\n"
"v_mfma_f32_16x16x32_bf16 v[12:15], a[148:151], v[116:119], v[12:15]\n"
"v_exp_f32 v7, v7\n"
"v_mfma_f32_16x16x32_bf16 v[12:15], a[152:155], v[120:123], v[12:15]\n"
"v_cvt_pk_bf16_f32 v19, v6, v7\n"
"v_mfma_f32_16x16x32_bf16 v[12:15], a[156:159], v[124:127], v[12:15]\n"
"buffer_load_dwordx4 v247, s[12:15], s84 offen offset:3072 lds\n"
"v_mfma_f32_16x16x32_bf16 a[64:67], a[192:195], v[16:19], a[64:67]\n"
"s_add_u32 s84, s84, 16384\n"
"v_exp_f32 v8, v8\n"
"v_mfma_f32_16x16x32_bf16 a[68:71], a[196:199], v[16:19], a[68:71]\n"
"s_nop 0\n"
"v_mfma_f32_16x16x32_bf16 a[72:75], a[200:203], v[16:19], a[72:75]\n"
"v_exp_f32 v9, v9\n"
"v_mfma_f32_16x16x32_bf16 a[76:79], a[204:207], v[16:19], a[76:79]\n"
"v_cvt_pk_bf16_f32 v20, v8, v9\n"
"v_mfma_f32_16x16x32_bf16 a[80:83], a[208:211], v[16:19], a[80:83]\n"
"v_exp_f32 v10, v10\n"
"v_mfma_f32_16x16x32_bf16 a[84:87], a[212:215], v[16:19], a[84:87]\n"
"s_nop 0\n"
"v_mfma_f32_16x16x32_bf16 a[88:91], a[216:219], v[16:19], a[88:91]\n"
"v_exp_f32 v11, v11\n"
"v_mfma_f32_16x16x32_bf16 a[92:95], a[220:223], v[16:19], a[92:95]\n"
"s_waitcnt vmcnt(18) lgkmcnt(0)\n"
"s_barrier\n"
"v_cvt_pk_bf16_f32 v21, v10, v11\n"
"v_mfma_f32_16x16x32_bf16 v[52:55], v[40:43], v[16:19], v[52:55]\n"
"v_exp_f32 v12, v12\n"
"v_mfma_f32_16x16x32_bf16 v[0:3], a[160:163], v[64:67], v[24:27]\n"
"ds_read_b128 a[128:131], v240 offset:0\n"
"v_mfma_f32_16x16x32_bf16 v[0:3], a[164:167], v[68:71], v[0:3]\n"
"v_exp_f32 v13, v13\n"
"v_mfma_f32_16x16x32_bf16 v[0:3], a[168:171], v[72:75], v[0:3]\n"
"ds_read_b128 a[132:135], v240 offset:256\n"
"v_cvt_pk_bf16_f32 v22, v12, v13\n"
"v_mfma_f32_16x16x32_bf16 v[0:3], a[172:175], v[76:79], v[0:3]\n"
"v_exp_f32 v14, v14\n"
"v_mfma_f32_16x16x32_bf16 v[4:7], a[176:179], v[64:67], v[24:27]\n"
"ds_read_b128 a[136:139], v240 offset:512\n"
"v_mfma_f32_16x16x32_bf16 v[4:7], a[180:183], v[68:71], v[4:7]\n"
"v_exp_f32 v15, v15\n"
"v_mfma_f32_16x16x32_bf16 v[4:7], a[184:187], v[72:75], v[4:7]\n"
"s_add_u32 m0, s86, 98304\n"
"ds_read_b128 a[140:143], v240 offset:768\n"
"v_cvt_pk_bf16_f32 v23, v14, v15\n"
"v_mfma_f32_16x16x32_bf16 v[4:7], a[188:191], v[76:79], v[4:7]\n"
"buffer_load_dwordx4 v246, s[8:11], s84 offen offset:0 lds\n"
"v_mfma_f32_16x16x32_bf16 a[96:99], a[192:195], v[20:23], a[96:99]\n"
"v_exp_f32 v0, v0\n"
"v_mfma_f32_16x16x32_bf16 a[100:103], a[196:199], v[20:23], a[100:103]\n"
"ds_read_b128 a[144:147], v240 offset:4096\n"
"v_mfma_f32_16x16x32_bf16 a[104:107], a[200:203], v[20:23], a[104:107]\n"
"v_exp_f32 v1, v1\n"
"v_mfma_f32_16x16x32_bf16 a[108:111], a[204:207], v[20:23], a[108:111]\n"
"ds_read_b128 a[148:151], v240 offset:4352\n"
"v_cvt_pk_bf16_f32 v16, v0, v1\n"
"v_mfma_f32_16x16x32_bf16 a[112:115], a[208:211], v[20:23], a[112:115]\n"
"v_exp_f32 v2, v2\n"
"v_mfma_f32_16x16x32_bf16 a[116:119], a[212:215], v[20:23], a[116:119]\n"
"ds_read_b128 a[152:155], v240 offset:4608\n"
"v_mfma_f32_16x16x32_bf16 a[120:123], a[216:219], v[20:23], a[120:123]\n"
"v_exp_f32 v3, v3\n"
"v_mfma_f32_16x16x32_bf16 a[124:127], a[220:223], v[20:23], a[124:127]\n"
"buffer_load_dword v250, v217, s[24:27], s47 offen\n"
"v_cvt_pk_bf16_f32 v17, v2, v3\n"
"v_mfma_f32_16x16x32_bf16 v[56:59], v[40:43], v[20:23], v[56:59]\n"
"v_exp_f32 v4, v4\n"
"v_mfma_f32_16x16x32_bf16 v[8:11], a[160:163], v[80:83], v[28:31]\n"
"ds_read_b128 a[156:159], v240 offset:4864\n"
"v_add_u32 v217, 294912, v217\n"
"v_mfma_f32_16x16x32_bf16 v[8:11], a[164:167], v[84:87], v[8:11]\n"
"v_exp_f32 v5, v5\n"
"v_mfma_f32_16x16x32_bf16 v[8:11], a[168:171], v[88:91], v[8:11]\n"
"ds_read_b64_tr_b16 a[192:193], v242 offset:16384\n"
"v_cvt_pk_bf16_f32 v18, v4, v5\n"
"v_mfma_f32_16x16x32_bf16 v[8:11], a[172:175], v[92:95], v[8:11]\n"
"v_exp_f32 v6, v6\n"
"v_mfma_f32_16x16x32_bf16 v[12:15], a[176:179], v[80:83], v[28:31]\n"
"ds_read_b64_tr_b16 a[194:195], v242 offset:20480\n"
"v_mfma_f32_16x16x32_bf16 v[12:15], a[180:183], v[84:87], v[12:15]\n"
"v_exp_f32 v7, v7\n"
"v_mfma_f32_16x16x32_bf16 v[12:15], a[184:187], v[88:91], v[12:15]\n"
"ds_read_b64_tr_b16 a[196:197], v243 offset:16384\n"
"v_cvt_pk_bf16_f32 v19, v6, v7\n"
"v_mfma_f32_16x16x32_bf16 v[12:15], a[188:191], v[92:95], v[12:15]\n"
"s_waitcnt lgkmcnt(11)\n"
"buffer_load_dwordx4 v246, s[8:11], s84 offen offset:1024 lds\n"
"v_mfma_f32_16x16x32_bf16 a[0:3], a[224:227], v[16:19], a[0:3]\n"
"v_exp_f32 v8, v8\n"
"v_mfma_f32_16x16x32_bf16 a[4:7], a[228:231], v[16:19], a[4:7]\n"
"ds_read_b64_tr_b16 a[198:199], v243 offset:20480\n"
"v_mfma_f32_16x16x32_bf16 a[8:11], a[232:235], v[16:19], a[8:11]\n"
"v_exp_f32 v9, v9\n"
"v_mfma_f32_16x16x32_bf16 a[12:15], a[236:239], v[16:19], a[12:15]\n"
"ds_read_b64_tr_b16 a[200:201], v242 offset:16640\n"
"v_cvt_pk_bf16_f32 v20, v8, v9\n"
"v_mfma_f32_16x16x32_bf16 a[16:19], a[240:243], v[16:19], a[16:19]\n"
"v_exp_f32 v10, v10\n"
"v_mfma_f32_16x16x32_bf16 a[20:23], a[244:247], v[16:19], a[20:23]\n"
"ds_read_b64_tr_b16 a[202:203], v242 offset:20736\n"
"v_mfma_f32_16x16x32_bf16 a[24:27], a[248:251], v[16:19], a[24:27]\n"
"v_exp_f32 v11, v11\n"
"v_mfma_f32_16x16x32_bf16 a[28:31], a[252:255], v[16:19], a[28:31]\n"
"v_cvt_pk_bf16_f32 v21, v10, v11\n"
"v_mfma_f32_16x16x32_bf16 v[44:47], v[40:43], v[16:19], v[44:47]\n"
"v_exp_f32 v12, v12\n"
"v_mfma_f32_16x16x32_bf16 v[0:3], a[160:163], v[96:99], v[32:35]\n"
"ds_read_b64_tr_b16 a[204:205], v243 offset:16640\n"
"v_mfma_f32_16x16x32_bf16 v[0:3], a[164:167], v[100:103], v[0:3]\n"
"v_exp_f32 v13, v13\n"
"v_mfma_f32_16x16x32_bf16 v[0:3], a[168:171], v[104:107], v[0:3]\n"
"ds_read_b64_tr_b16 a[206:207], v243 offset:20736\n"
"v_cvt_pk_bf16_f32 v22, v12, v13\n"
"v_mfma_f32_16x16x32_bf16 v[0:3], a[172:175], v[108:111], v[0:3]\n"
"v_exp_f32 v14, v14\n"
"v_mfma_f32_16x16x32_bf16 v[4:7], a[176:179], v[96:99], v[32:35]\n"
"ds_read_b64_tr_b16 a[208:209], v242 offset:16896\n"
"v_mfma_f32_16x16x32_bf16 v[4:7], a[180:183], v[100:103], v[4:7]\n"
"v_exp_f32 v15, v15\n"
"v_mfma_f32_16x16x32_bf16 v[4:7], a[184:187], v[104:107], v[4:7]\n"
"ds_read_b64_tr_b16 a[210:211], v242 offset:20992\n"
"v_cvt_pk_bf16_f32 v23, v14, v15\n"
"v_mfma_f32_16x16x32_bf16 v[4:7], a[188:191], v[108:111], v[4:7]\n"
"buffer_load_dwordx4 v247, s[8:11], s84 offen offset:2048 lds\n"
"v_mfma_f32_16x16x32_bf16 a[32:35], a[224:227], v[20:23], a[32:35]\n"
"v_exp_f32 v0, v0\n"
"v_mfma_f32_16x16x32_bf16 a[36:39], a[228:231], v[20:23], a[36:39]\n"
"ds_read_b64_tr_b16 a[212:213], v243 offset:16896\n"
"v_mfma_f32_16x16x32_bf16 a[40:43], a[232:235], v[20:23], a[40:43]\n"
"v_exp_f32 v1, v1\n"
"v_mfma_f32_16x16x32_bf16 a[44:47], a[236:239], v[20:23], a[44:47]\n"
"ds_read_b64_tr_b16 a[214:215], v243 offset:20992\n"
"v_cvt_pk_bf16_f32 v16, v0, v1\n"
"v_mfma_f32_16x16x32_bf16 a[48:51], a[240:243], v[20:23], a[48:51]\n"
"v_exp_f32 v2, v2\n"
"v_mfma_f32_16x16x32_bf16 a[52:55], a[244:247], v[20:23], a[52:55]\n"
"ds_read_b64_tr_b16 a[216:217], v242 offset:17152\n"
"v_mfma_f32_16x16x32_bf16 a[56:59], a[248:251], v[20:23], a[56:59]\n"
"v_exp_f32 v3, v3\n"
"v_mfma_f32_16x16x32_bf16 a[60:63], a[252:255], v[20:23], a[60:63]\n"
"v_cvt_pk_bf16_f32 v17, v2, v3\n"
"v_mfma_f32_16x16x32_bf16 v[48:51], v[40:43], v[20:23], v[48:51]\n"
"v_exp_f32 v4, v4\n"
"v_mfma_f32_16x16x32_bf16 v[8:11], a[160:163], v[112:115], v[36:39]\n"
"ds_read_b64_tr_b16 a[218:219], v242 offset:21248\n"
"v_mfma_f32_16x16x32_bf16 v[8:11], a[164:167], v[116:119], v[8:11]\n"
"v_exp_f32 v5, v5\n"
"v_mfma_f32_16x16x32_bf16 v[8:11], a[168:171], v[120:123], v[8:11]\n"
"ds_read_b64_tr_b16 a[220:221], v243 offset:17152\n"
"v_cvt_pk_bf16_f32 v18, v4, v5\n"
"v_mfma_f32_16x16x32_bf16 v[8:11], a[172:175], v[124:127], v[8:11]\n"
"v_exp_f32 v6, v6\n"
"v_mfma_f32_16x16x32_bf16 v[12:15], a[176:179], v[112:115], v[36:39]\n"
"ds_read_b64_tr_b16 a[222:223], v243 offset:21248\n"
"v_mfma_f32_16x16x32_bf16 v[12:15], a[180:183], v[116:119], v[12:15]\n"
"v_exp_f32 v7, v7\n"
"v_mfma_f32_16x16x32_bf16 v[12:15], a[184:187], v[120:123], v[12:15]\n"
"v_cvt_pk_bf16_f32 v19, v6, v7\n"
"v_mfma_f32_16x16x32_bf16 v[12:15], a[188:191], v[124:127], v[12:15]\n"
"buffer_load_dwordx4 v247, s[8:11], s84 offen offset:3072 lds\n"
"v_mfma_f32_16x16x32_bf16 a[64:67], a[224:227], v[16:19], a[64:67]\n"
"v_exp_f32 v8, v8\n"
"v_mfma_f32_16x16x32_bf16 a[68:71], a[228:231], v[16:19], a[68:71]\n"
"s_nop 0\n"
"v_mfma_f32_16x16x32_bf16 a[72:75], a[232:235], v[16:19], a[72:75]\n"
"v_exp_f32 v9, v9\n"
"v_mfma_f32_16x16x32_bf16 a[76:79], a[236:239], v[16:19], a[76:79]\n"
"v_cvt_pk_bf16_f32 v20, v8, v9\n"
"v_mfma_f32_16x16x32_bf16 a[80:83], a[240:243], v[16:19], a[80:83]\n"
"v_exp_f32 v10, v10\n"
"v_mfma_f32_16x16x32_bf16 a[84:87], a[244:247], v[16:19], a[84:87]\n"
"s_nop 0\n"
"v_mfma_f32_16x16x32_bf16 a[88:91], a[248:251], v[16:19], a[88:91]\n"
"v_exp_f32 v11, v11\n"
"v_mfma_f32_16x16x32_bf16 a[92:95], a[252:255], v[16:19], a[92:95]\n"
"s_waitcnt lgkmcnt(8)\n"
"v_cvt_pk_bf16_f32 v21, v10, v11\n"
"v_mfma_f32_16x16x32_bf16 v[52:55], v[40:43], v[16:19], v[52:55]\n"
"v_exp_f32 v12, v12\n"
"v_mfma_f32_16x16x32_bf16 v[0:3], a[128:131], v[64:67], v[24:27]\n"
"ds_read_b128 a[160:163], v240 offset:8192\n"
"v_mfma_f32_16x16x32_bf16 v[0:3], a[132:135], v[68:71], v[0:3]\n"
"v_exp_f32 v13, v13\n"
"v_mfma_f32_16x16x32_bf16 v[0:3], a[136:139], v[72:75], v[0:3]\n"
"ds_read_b128 a[164:167], v240 offset:8448\n"
"v_cvt_pk_bf16_f32 v22, v12, v13\n"
"v_mfma_f32_16x16x32_bf16 v[0:3], a[140:143], v[76:79], v[0:3]\n"
"v_exp_f32 v14, v14\n"
"v_mfma_f32_16x16x32_bf16 v[4:7], a[144:147], v[64:67], v[24:27]\n"
"ds_read_b128 a[168:171], v240 offset:8704\n"
"v_mfma_f32_16x16x32_bf16 v[4:7], a[148:151], v[68:71], v[4:7]\n"
"v_exp_f32 v15, v15\n"
"v_mfma_f32_16x16x32_bf16 v[4:7], a[152:155], v[72:75], v[4:7]\n"
"s_add_u32 m0, s86, 114688\n"
"ds_read_b128 a[172:175], v240 offset:8960\n"
"v_cvt_pk_bf16_f32 v23, v14, v15\n"
"v_mfma_f32_16x16x32_bf16 v[4:7], a[156:159], v[76:79], v[4:7]\n"
"buffer_load_dwordx4 v246, s[12:15], s84 offen offset:0 lds\n"
"v_mfma_f32_16x16x32_bf16 a[96:99], a[224:227], v[20:23], a[96:99]\n"
"v_exp_f32 v0, v0\n"
"v_mfma_f32_16x16x32_bf16 a[100:103], a[228:231], v[20:23], a[100:103]\n"
"ds_read_b128 a[176:179], v240 offset:12288\n"
"v_mfma_f32_16x16x32_bf16 a[104:107], a[232:235], v[20:23], a[104:107]\n"
"v_exp_f32 v1, v1\n"
"v_mfma_f32_16x16x32_bf16 a[108:111], a[236:239], v[20:23], a[108:111]\n"
"ds_read_b128 a[180:183], v240 offset:12544\n"
"v_cvt_pk_bf16_f32 v16, v0, v1\n"
"v_mfma_f32_16x16x32_bf16 a[112:115], a[240:243], v[20:23], a[112:115]\n"
"v_exp_f32 v2, v2\n"
"v_mfma_f32_16x16x32_bf16 a[116:119], a[244:247], v[20:23], a[116:119]\n"
"ds_read_b128 a[184:187], v240 offset:12800\n"
"v_mfma_f32_16x16x32_bf16 a[120:123], a[248:251], v[20:23], a[120:123]\n"
"v_exp_f32 v3, v3\n"
"v_mfma_f32_16x16x32_bf16 a[124:127], a[252:255], v[20:23], a[124:127]\n"
"v_cvt_pk_bf16_f32 v17, v2, v3\n"
"v_mfma_f32_16x16x32_bf16 v[56:59], v[40:43], v[20:23], v[56:59]\n"
"v_exp_f32 v4, v4\n"
"v_mfma_f32_16x16x32_bf16 v[8:11], a[128:131], v[80:83], v[28:31]\n"
"ds_read_b128 a[188:191], v240 offset:13056\n"
"v_mfma_f32_16x16x32_bf16 v[8:11], a[132:135], v[84:87], v[8:11]\n"
"v_exp_f32 v5, v5\n"
"v_mfma_f32_16x16x32_bf16 v[8:11], a[136:139], v[88:91], v[8:11]\n"
"ds_read_b64_tr_b16 a[224:225], v242 offset:24576\n"
"v_cvt_pk_bf16_f32 v18, v4, v5\n"
"v_mfma_f32_16x16x32_bf16 v[8:11], a[140:143], v[92:95], v[8:11]\n"
"v_exp_f32 v6, v6\n"
"v_mfma_f32_16x16x32_bf16 v[12:15], a[144:147], v[80:83], v[28:31]\n"
"ds_read_b64_tr_b16 a[226:227], v242 offset:28672\n"
"v_mfma_f32_16x16x32_bf16 v[12:15], a[148:151], v[84:87], v[12:15]\n"
"v_exp_f32 v7, v7\n"
"v_mfma_f32_16x16x32_bf16 v[12:15], a[152:155], v[88:91], v[12:15]\n"
"ds_read_b64_tr_b16 a[228:229], v243 offset:24576\n"
"v_cvt_pk_bf16_f32 v19, v6, v7\n"
"v_mfma_f32_16x16x32_bf16 v[12:15], a[156:159], v[92:95], v[12:15]\n"
"s_waitcnt lgkmcnt(11)\n"
"buffer_load_dwordx4 v247, s[12:15], s84 offen offset:1024 lds\n"
"v_mfma_f32_16x16x32_bf16 a[0:3], a[192:195], v[16:19], a[0:3]\n"
"v_exp_f32 v8, v8\n"
"v_mfma_f32_16x16x32_bf16 a[4:7], a[196:199], v[16:19], a[4:7]\n"
"ds_read_b64_tr_b16 a[230:231], v243 offset:28672\n"
"v_mfma_f32_16x16x32_bf16 a[8:11], a[200:203], v[16:19], a[8:11]\n"
"v_exp_f32 v9, v9\n"
"v_mfma_f32_16x16x32_bf16 a[12:15], a[204:207], v[16:19], a[12:15]\n"
"ds_read_b64_tr_b16 a[232:233], v242 offset:24832\n"
"v_cvt_pk_bf16_f32 v20, v8, v9\n"
"v_mfma_f32_16x16x32_bf16 a[16:19], a[208:211], v[16:19], a[16:19]\n"
"v_exp_f32 v10, v10\n"
"v_mfma_f32_16x16x32_bf16 a[20:23], a[212:215], v[16:19], a[20:23]\n"
"ds_read_b64_tr_b16 a[234:235], v242 offset:28928\n"
"v_mfma_f32_16x16x32_bf16 a[24:27], a[216:219], v[16:19], a[24:27]\n"
"v_exp_f32 v11, v11\n"
"v_mfma_f32_16x16x32_bf16 a[28:31], a[220:223], v[16:19], a[28:31]\n"
"v_cvt_pk_bf16_f32 v21, v10, v11\n"
"v_mfma_f32_16x16x32_bf16 v[44:47], v[40:43], v[16:19], v[44:47]\n"
"v_exp_f32 v12, v12\n"
"v_mfma_f32_16x16x32_bf16 v[0:3], a[128:131], v[96:99], v[32:35]\n"
"ds_read_b64_tr_b16 a[236:237], v243 offset:24832\n"
"v_mfma_f32_16x16x32_bf16 v[0:3], a[132:135], v[100:103], v[0:3]\n"
"v_exp_f32 v13, v13\n"
"v_mfma_f32_16x16x32_bf16 v[0:3], a[136:139], v[104:107], v[0:3]\n"
"ds_read_b64_tr_b16 a[238:239], v243 offset:28928\n"
"v_cvt_pk_bf16_f32 v22, v12, v13\n"
"v_mfma_f32_16x16x32_bf16 v[0:3], a[140:143], v[108:111], v[0:3]\n"
"v_exp_f32 v14, v14\n"
"v_mfma_f32_16x16x32_bf16 v[4:7], a[144:147], v[96:99], v[32:35]\n"
"ds_read_b64_tr_b16 a[240:241], v242 offset:25088\n"
"v_mfma_f32_16x16x32_bf16 v[4:7], a[148:151], v[100:103], v[4:7]\n"
"v_exp_f32 v15, v15\n"
"v_mfma_f32_16x16x32_bf16 v[4:7], a[152:155], v[104:107], v[4:7]\n"
"ds_read_b64_tr_b16 a[242:243], v242 offset:29184\n"
"v_cvt_pk_bf16_f32 v23, v14, v15\n"
"v_mfma_f32_16x16x32_bf16 v[4:7], a[156:159], v[108:111], v[4:7]\n"
"buffer_load_dwordx4 v246, s[12:15], s84 offen offset:2048 lds\n"
"v_mfma_f32_16x16x32_bf16 a[32:35], a[192:195], v[20:23], a[32:35]\n"
"v_exp_f32 v0, v0\n"
"v_mfma_f32_16x16x32_bf16 a[36:39], a[196:199], v[20:23], a[36:39]\n"
"ds_read_b64_tr_b16 a[244:245], v243 offset:25088\n"
"v_mfma_f32_16x16x32_bf16 a[40:43], a[200:203], v[20:23], a[40:43]\n"
"v_exp_f32 v1, v1\n"
"v_mfma_f32_16x16x32_bf16 a[44:47], a[204:207], v[20:23], a[44:47]\n"
"ds_read_b64_tr_b16 a[246:247], v243 offset:29184\n"
"v_cvt_pk_bf16_f32 v16, v0, v1\n"
"v_mfma_f32_16x16x32_bf16 a[48:51], a[208:211], v[20:23], a[48:51]\n"
"v_exp_f32 v2, v2\n"
"v_mfma_f32_16x16x32_bf16 a[52:55], a[212:215], v[20:23], a[52:55]\n"
"ds_read_b64_tr_b16 a[248:249], v242 offset:25344\n"
"v_mfma_f32_16x16x32_bf16 a[56:59], a[216:219], v[20:23], a[56:59]\n"
"v_exp_f32 v3, v3\n"
"v_mfma_f32_16x16x32_bf16 a[60:63], a[220:223], v[20:23], a[60:63]\n"
"v_cvt_pk_bf16_f32 v17, v2, v3\n"
"v_mfma_f32_16x16x32_bf16 v[48:51], v[40:43], v[20:23], v[48:51]\n"
"v_exp_f32 v4, v4\n"
"v_mfma_f32_16x16x32_bf16 v[8:11], a[128:131], v[112:115], v[36:39]\n"
"ds_read_b64_tr_b16 a[250:251], v242 offset:29440\n"
"v_mfma_f32_16x16x32_bf16 v[8:11], a[132:135], v[116:119], v[8:11]\n"
"v_exp_f32 v5, v5\n"
"v_mfma_f32_16x16x32_bf16 v[8:11], a[136:139], v[120:123], v[8:11]\n"
"ds_read_b64_tr_b16 a[252:253], v243 offset:25344\n"
"v_cvt_pk_bf16_f32 v18, v4, v5\n"
"v_mfma_f32_16x16x32_bf16 v[8:11], a[140:143], v[124:127], v[8:11]\n"
"v_exp_f32 v6, v6\n"
"v_mfma_f32_16x16x32_bf16 v[12:15], a[144:147], v[112:115], v[36:39]\n"
"ds_read_b64_tr_b16 a[254:255], v243 offset:29440\n"
"v_mfma_f32_16x16x32_bf16 v[12:15], a[148:151], v[116:119], v[12:15]\n"
"v_exp_f32 v7, v7\n"
"v_mfma_f32_16x16x32_bf16 v[12:15], a[152:155], v[120:123], v[12:15]\n"
"v_cvt_pk_bf16_f32 v19, v6, v7\n"
"v_mfma_f32_16x16x32_bf16 v[12:15], a[156:159], v[124:127], v[12:15]\n"
"buffer_load_dwordx4 v247, s[12:15], s84 offen offset:3072 lds\n"
"v_mfma_f32_16x16x32_bf16 a[64:67], a[192:195], v[16:19], a[64:67]\n"
"s_add_u32 s84, s84, 16384\n"
"v_exp_f32 v8, v8\n"
"v_mfma_f32_16x16x32_bf16 a[68:71], a[196:199], v[16:19], a[68:71]\n"
"s_nop 0\n"
"v_mfma_f32_16x16x32_bf16 a[72:75], a[200:203], v[16:19], a[72:75]\n"
"v_exp_f32 v9, v9\n"
"v_mfma_f32_16x16x32_bf16 a[76:79], a[204:207], v[16:19], a[76:79]\n"
"v_cvt_pk_bf16_f32 v20, v8, v9\n"
"v_mfma_f32_16x16x32_bf16 a[80:83], a[208:211], v[16:19], a[80:83]\n"
"s_sub_u32 s87, s87, 1\n"
"v_exp_f32 v10, v10\n"
"v_mfma_f32_16x16x32_bf16 a[84:87], a[212:215], v[16:19], a[84:87]\n"
"s_cmp_lg_u32 s87, 0\n"
"v_mfma_f32_16x16x32_bf16 a[88:91], a[216:219], v[16:19], a[88:91]\n"
"v_exp_f32 v11, v11\n"
"v_mfma_f32_16x16x32_bf16 a[92:95], a[220:223], v[16:19], a[92:95]\n"
"s_waitcnt vmcnt(18) lgkmcnt(0)\n"
"s_barrier\n"
"v_cvt_pk_bf16_f32 v21, v10, v11\n"
"v_mfma_f32_16x16x32_bf16 v[52:55], v[40:43], v[16:19], v[52:55]\n"
"v_exp_f32 v12, v12\n"
"s_cbranch_scc1 AT_loop\n"
"s_cmp_lg_u32 s56, 0\n"
"s_cbranch_scc1 AT_h3\n"
"s_mov_b32 s61, 0x80000\n"
"AT_h2:\n"
"v_xor_b32 v210, s85, v210\n"
"v_xor_b32 v211, s85, v211\n"
"v_xor_b32 v212, s85, v212\n"
"v_xor_b32 v213, s85, v213\n"
"v_or3_b32 v210, v210, v211, v212\n"
"v_or_b32 v210, v210, v213\n"
"v_cmp_ne_u32 vcc, 0, v210\n"
"s_sub_u32 s61, s61, 1\n"
"s_cbranch_vccz AT_h4\n"
"s_cmp_eq_u32 s61, 0\n"
"s_cbranch_scc1 AT_h4\n"
"s_sleep 2\n"
"buffer_load_dword v210, v214, s[28:31], s57 offen offset:4 sc1\n"
"buffer_load_dword v211, v214, s[28:31], s58 offen offset:4 sc1\n"
"buffer_load_dword v212, v214, s[28:31], s59 offen offset:4 sc1\n"
"buffer_load_dword v213, v214, s[28:31], s60 offen offset:4 sc1\n"
"s_waitcnt vmcnt(0)\n"
"s_branch AT_h2\n"
"AT_h4:\n"
"v_mov_b32 v210, s85\n"
"v_mov_b32 v211, 65536\n"
"s_mov_b64 exec, 1\n"
"buffer_store_dword v210, v211, s[28:31], 0 offen sc1\n"
"s_mov_b64 exec, -1\n"
"AT_h3:\n"
"v_mfma_f32_16x16x32_bf16 v[0:3], a[160:163], v[64:67], v[24:27]\n"
"ds_read_b128 a[128:131], v240 offset:32768\n"
"v_lshlrev_b32 v216, 6, v248\n"
"v_mfma_f32_16x16x32_bf16 v[0:3], a[164:167], v[68:71], v[0:3]\n"
"v_exp_f32 v13, v13\n"
"v_mfma_f32_16x16x32_bf16 v[0:3], a[168:171], v[72:75], v[0:3]\n"
"ds_read_b128 a[132:135], v240 offset:33024\n"
"v_cvt_pk_bf16_f32 v22, v12, v13\n"
"v_mfma_f32_16x16x32_bf16 v[0:3], a[172:175], v[76:79], v[0:3]\n"
"v_exp_f32 v14, v14\n"
"v_mfma_f32_16x16x32_bf16 v[4:7], a[176:179], v[64:67], v[24:27]\n"
"ds_read_b128 a[136:139], v240 offset:33280\n"
"v_lshrrev_b32 v215, 3, v248\n"
"v_mfma_f32_16x16x32_bf16 v[4:7], a[180:183], v[68:71], v[4:7]\n"
"v_exp_f32 v15, v15\n"
"v_mfma_f32_16x16x32_bf16 v[4:7], a[184:187], v[72:75], v[4:7]\n"
"s_mov_b32 m0, s53\n"
"ds_read_b128 a[140:143], v240 offset:33536\n"
"v_cvt_pk_bf16_f32 v23, v14, v15\n"
"v_mfma_f32_16x16x32_bf16 v[4:7], a[188:191], v[76:79], v[4:7]\n"
"buffer_load_dwordx4 v206, s[24:27], s52 offen offset:0 lds\n"
"v_mul_u32_u24 v215, 1152, v215\n"
"v_mfma_f32_16x16x32_bf16 a[96:99], a[192:195], v[20:23], a[96:99]\n"
"s_add_u32 s52, s52, 8192\n"
"v_exp_f32 v0, v0\n"
"v_mfma_f32_16x16x32_bf16 a[100:103], a[196:199], v[20:23], a[100:103]\n"
"ds_read_b128 a[144:147], v240 offset:36864\n"
"v_and_b32 v217, 7, v248\n"
"v_mfma_f32_16x16x32_bf16 a[104:107], a[200:203], v[20:23], a[104:107]\n"
"v_exp_f32 v1, v1\n"
"v_mfma_f32_16x16x32_bf16 a[108:111], a[204:207], v[20:23], a[108:111]\n"
"ds_read_b128 a[148:151], v240 offset:37120\n"
"v_cvt_pk_bf16_f32 v16, v0, v1\n"
"v_mfma_f32_16x16x32_bf16 a[112:115], a[208:211], v[20:23], a[112:115]\n"
"v_exp_f32 v2, v2\n"
"v_mfma_f32_16x16x32_bf16 a[116:119], a[212:215], v[20:23], a[116:119]\n"
"ds_read_b128 a[152:155], v240 offset:37376\n"
"v_lshl_add_u32 v215, v217, 7, v215\n"
"v_mfma_f32_16x16x32_bf16 a[120:123], a[216:219], v[20:23], a[120:123]\n"
"s_and_b32 s46, s44, 7\n"
"v_exp_f32 v3, v3\n"
"v_mfma_f32_16x16x32_bf16 a[124:127], a[220:223], v[20:23], a[124:127]\n"
"s_lshl_b32 s46, s46, 2\n"
"v_cvt_pk_bf16_f32 v17, v2, v3\n"
"v_mfma_f32_16x16x32_bf16 v[56:59], v[40:43], v[20:23], v[56:59]\n"
"s_add_u32 s46, s46, s41\n"
"v_exp_f32 v4, v4\n"
"v_mfma_f32_16x16x32_bf16 v[8:11], a[160:163], v[80:83], v[28:31]\n"
"s_mul_i32 s46, s46, 9216\n"
"ds_read_b128 a[156:159], v240 offset:37632\n"
"v_mfma_f32_16x16x32_bf16 v[8:11], a[164:167], v[84:87], v[8:11]\n"
"s_mul_i32 s62, s43, 294912\n"
"v_exp_f32 v5, v5\n"
"v_mfma_f32_16x16x32_bf16 v[8:11], a[168:171], v[88:91], v[8:11]\n"
"s_add_u32 s46, s46, s62\n"
"ds_read_b64_tr_b16 a[192:193], v242 offset:49152\n"
"v_cvt_pk_bf16_f32 v18, v4, v5\n"
"v_mfma_f32_16x16x32_bf16 v[8:11], a[172:175], v[92:95], v[8:11]\n"
"v_exp_f32 v6, v6\n"
"v_mfma_f32_16x16x32_bf16 v[12:15], a[176:179], v[80:83], v[28:31]\n"
"ds_read_b64_tr_b16 a[194:195], v242 offset:53248\n"
"v_mfma_f32_16x16x32_bf16 v[12:15], a[180:183], v[84:87], v[12:15]\n"
"v_exp_f32 v7, v7\n"
"v_mfma_f32_16x16x32_bf16 v[12:15], a[184:187], v[88:91], v[12:15]\n"
"ds_read_b64_tr_b16 a[196:197], v243 offset:49152\n"
"v_cvt_pk_bf16_f32 v19, v6, v7\n"
"v_mfma_f32_16x16x32_bf16 v[12:15], a[188:191], v[92:95], v[12:15]\n"
"s_waitcnt lgkmcnt(11)\n"
"buffer_load_dwordx4 v206, s[24:27], s52 offen offset:1024 lds\n"
"v_mfma_f32_16x16x32_bf16 a[0:3], a[224:227], v[16:19], a[0:3]\n"
"s_add_u32 s52, s52, 8192\n"
"v_exp_f32 v8, v8\n"
"v_mfma_f32_16x16x32_bf16 a[4:7], a[228:231], v[16:19], a[4:7]\n"
"ds_read_b64_tr_b16 a[198:199], v243 offset:53248\n"
"v_mfma_f32_16x16x32_bf16 a[8:11], a[232:235], v[16:19], a[8:11]\n"
"v_exp_f32 v9, v9\n"
"v_mfma_f32_16x16x32_bf16 a[12:15], a[236:239], v[16:19], a[12:15]\n"
"ds_read_b64_tr_b16 a[200:201], v242 offset:49408\n"
"v_cvt_pk_bf16_f32 v20, v8, v9\n"
"v_mfma_f32_16x16x32_bf16 a[16:19], a[240:243], v[16:19], a[16:19]\n"
"v_exp_f32 v10, v10\n"
"v_mfma_f32_16x16x32_bf16 a[20:23], a[244:247], v[16:19], a[20:23]\n"
"ds_read_b64_tr_b16 a[202:203], v242 offset:53504\n"
"v_mfma_f32_16x16x32_bf16 a[24:27], a[248:251], v[16:19], a[24:27]\n"
"v_exp_f32 v11, v11\n"
"v_mfma_f32_16x16x32_bf16 a[28:31], a[252:255], v[16:19], a[28:31]\n"
"buffer_load_dword v250, v215, s[24:27], s46 offen offset:128\n"
"v_cvt_pk_bf16_f32 v21, v10, v11\n"
"v_mfma_f32_16x16x32_bf16 v[44:47], v[40:43], v[16:19], v[44:47]\n"
"v_exp_f32 v12, v12\n"
"v_mfma_f32_16x16x32_bf16 v[0:3], a[160:163], v[96:99], v[32:35]\n"
"ds_read_b64_tr_b16 a[204:205], v243 offset:49408\n"
"v_mfma_f32_16x16x32_bf16 v[0:3], a[164:167], v[100:103], v[0:3]\n"
"v_exp_f32 v13, v13\n"
"v_mfma_f32_16x16x32_bf16 v[0:3], a[168:171], v[104:107], v[0:3]\n"
"ds_read_b64_tr_b16 a[206:207], v243 offset:53504\n"
"v_cvt_pk_bf16_f32 v22, v12, v13\n"
"v_mfma_f32_16x16x32_bf16 v[0:3], a[172:175], v[108:111], v[0:3]\n"
"v_exp_f32 v14, v14\n"
"v_mfma_f32_16x16x32_bf16 v[4:7], a[176:179], v[96:99], v[32:35]\n"
"ds_read_b64_tr_b16 a[208:209], v242 offset:49664\n"
"v_mfma_f32_16x16x32_bf16 v[4:7], a[180:183], v[100:103], v[4:7]\n"
"v_exp_f32 v15, v15\n"
"v_mfma_f32_16x16x32_bf16 v[4:7], a[184:187], v[104:107], v[4:7]\n"
"ds_read_b64_tr_b16 a[210:211], v242 offset:53760\n"
"v_cvt_pk_bf16_f32 v23, v14, v15\n"
"v_mfma_f32_16x16x32_bf16 v[4:7], a[188:191], v[108:111], v[4:7]\n"
"buffer_load_dwordx4 v206, s[24:27], s52 offen offset:2048 lds\n"
"v_mfma_f32_16x16x32_bf16 a[32:35], a[224:227], v[20:23], a[32:35]\n"
"s_add_u32 s52, s52, 8192\n"
"v_exp_f32 v0, v0\n"
"v_mfma_f32_16x16x32_bf16 a[36:39], a[228:231], v[20:23], a[36:39]\n"
"ds_read_b64_tr_b16 a[212:213], v243 offset:49664\n"
"v_mfma_f32_16x16x32_bf16 a[40:43], a[232:235], v[20:23], a[40:43]\n"
"v_exp_f32 v1, v1\n"
"v_mfma_f32_16x16x32_bf16 a[44:47], a[236:239], v[20:23], a[44:47]\n"
"ds_read_b64_tr_b16 a[214:215], v243 offset:53760\n"
"v_cvt_pk_bf16_f32 v16, v0, v1\n"
"v_mfma_f32_16x16x32_bf16 a[48:51], a[240:243], v[20:23], a[48:51]\n"
"v_exp_f32 v2, v2\n"
"v_mfma_f32_16x16x32_bf16 a[52:55], a[244:247], v[20:23], a[52:55]\n"
"ds_read_b64_tr_b16 a[216:217], v242 offset:49920\n"
"v_mfma_f32_16x16x32_bf16 a[56:59], a[248:251], v[20:23], a[56:59]\n"
"v_exp_f32 v3, v3\n"
"v_mfma_f32_16x16x32_bf16 a[60:63], a[252:255], v[20:23], a[60:63]\n"
"buffer_load_dword v250, v215, s[24:27], s46 offen offset:192\n"
"v_cvt_pk_bf16_f32 v17, v2, v3\n"
"v_mfma_f32_16x16x32_bf16 v[48:51], v[40:43], v[20:23], v[48:51]\n"
"s_getpc_b64 s[32:33]\n"
"AT_pc1:\n"
"s_sub_u32 s32, s32, AT_pc1-AT_start\n"
"s_subb_u32 s33, s33, 0\n"
"v_exp_f32 v4, v4\n"
"v_mfma_f32_16x16x32_bf16 v[8:11], a[160:163], v[112:115], v[36:39]\n"
"s_and_b32 s33, s33, 0xffff\n"
"ds_read_b64_tr_b16 a[218:219], v242 offset:54016\n"
"v_mfma_f32_16x16x32_bf16 v[8:11], a[164:167], v[116:119], v[8:11]\n"
"s_mov_b32 s34, AT_kend-AT_start\n"
"v_exp_f32 v5, v5\n"
"v_mfma_f32_16x16x32_bf16 v[8:11], a[168:171], v[120:123], v[8:11]\n"
"s_mov_b32 s35, 0x20000\n"
"ds_read_b64_tr_b16 a[220:221], v243 offset:49920\n"
"v_cvt_pk_bf16_f32 v18, v4, v5\n"
"v_mfma_f32_16x16x32_bf16 v[8:11], a[172:175], v[124:127], v[8:11]\n"
"s_lshr_b32 s47, s42, 3\n"
"v_exp_f32 v6, v6\n"
"v_mfma_f32_16x16x32_bf16 v[12:15], a[176:179], v[112:115], v[36:39]\n"
"s_lshl_b32 s47, s47, 2\n"
"ds_read_b64_tr_b16 a[222:223], v243 offset:54016\n"
"v_mfma_f32_16x16x32_bf16 v[12:15], a[180:183], v[116:119], v[12:15]\n"
"s_add_u32 s47, s47, s41\n"
"v_exp_f32 v7, v7\n"
"v_mfma_f32_16x16x32_bf16 v[12:15], a[184:187], v[120:123], v[12:15]\n"
"s_and_b32 s47, s47, 31\n"
"v_cvt_pk_bf16_f32 v19, v6, v7\n"
"v_mfma_f32_16x16x32_bf16 v[12:15], a[188:191], v[124:127], v[12:15]\n"
"s_lshl_b32 s47, s47, 12\n"
"buffer_load_dwordx4 v206, s[24:27], s52 offen offset:3072 lds\n"
"v_mfma_f32_16x16x32_bf16 a[64:67], a[224:227], v[16:19], a[64:67]\n"
"s_add_u32 s52, s52, 12288\n"
"v_exp_f32 v8, v8\n"
"v_mfma_f32_16x16x32_bf16 a[68:71], a[228:231], v[16:19], a[68:71]\n"
"buffer_load_dword v250, v216, s[32:35], s47 offen\n"
"v_mfma_f32_16x16x32_bf16 a[72:75], a[232:235], v[16:19], a[72:75]\n"
"s_and_b32 s48, s42, 7\n"
"v_exp_f32 v9, v9\n"
"v_mfma_f32_16x16x32_bf16 a[76:79], a[236:239], v[16:19], a[76:79]\n"
"s_mul_i32 s48, s48, 576\n"
"v_cvt_pk_bf16_f32 v20, v8, v9\n"
"v_mfma_f32_16x16x32_bf16 a[80:83], a[240:243], v[16:19], a[80:83]\n"
"s_lshr_b32 s62, s42, 3\n"
"v_exp_f32 v10, v10\n"
"v_mfma_f32_16x16x32_bf16 a[84:87], a[244:247], v[16:19], a[84:87]\n"
"s_mul_i32 s62, s62, 24\n"
"v_mfma_f32_16x16x32_bf16 a[88:91], a[248:251], v[16:19], a[88:91]\n"
"s_add_u32 s48, s48, s62\n"
"v_exp_f32 v11, v11\n"
"v_mfma_f32_16x16x32_bf16 a[92:95], a[252:255], v[16:19], a[92:95]\n"
"s_waitcnt lgkmcnt(8)\n"
"v_cvt_pk_bf16_f32 v21, v10, v11\n"
"v_mfma_f32_16x16x32_bf16 v[52:55], v[40:43], v[16:19], v[52:55]\n"
"s_cmp_lt_u32 s42, 192\n"
"s_cselect_b32 s74, s74, 0\n"
"s_cselect_b32 s66, s66, 0\n"
"s_cselect_b32 s70, s70, 0\n"
"v_exp_f32 v12, v12\n"
"v_mfma_f32_16x16x32_bf16 v[0:3], a[128:131], v[64:67], v[24:27]\n"
"s_mul_i32 s50, s48, 1152\n"
"ds_read_b128 a[160:163], v240 offset:40960\n"
"v_mfma_f32_16x16x32_bf16 v[0:3], a[132:135], v[68:71], v[0:3]\n"
"s_lshl_b32 s62, s41, 13\n"
"v_exp_f32 v13, v13\n"
"v_mfma_f32_16x16x32_bf16 v[0:3], a[136:139], v[72:75], v[0:3]\n"
"s_add_u32 s46, s50, s62\n"
"ds_read_b128 a[164:167], v240 offset:41216\n"
"v_cvt_pk_bf16_f32 v22, v12, v13\n"
"v_mfma_f32_16x16x32_bf16 v[0:3], a[140:143], v[76:79], v[0:3]\n"
"s_add_u32 s47, s46, 4096\n"
"v_exp_f32 v14, v14\n"
"v_mfma_f32_16x16x32_bf16 v[4:7], a[144:147], v[64:67], v[24:27]\n"
"ds_read_b128 a[168:171], v240 offset:41472\n"
"v_mfma_f32_16x16x32_bf16 v[4:7], a[148:151], v[68:71], v[4:7]\n"
"v_exp_f32 v15, v15\n"
"v_mfma_f32_16x16x32_bf16 v[4:7], a[152:155], v[72:75], v[4:7]\n"
"s_add_u32 m0, s53, 4096\n"
"ds_read_b128 a[172:175], v240 offset:41728\n"
"v_cvt_pk_bf16_f32 v23, v14, v15\n"
"v_mfma_f32_16x16x32_bf16 v[4:7], a[156:159], v[76:79], v[4:7]\n"
"buffer_load_dwordx4 v206, s[24:27], s52 offen offset:0 lds\n"
"v_mfma_f32_16x16x32_bf16 a[96:99], a[224:227], v[20:23], a[96:99]\n"
"s_add_u32 s52, s52, 8192\n"
"v_exp_f32 v0, v0\n"
"v_mfma_f32_16x16x32_bf16 a[100:103], a[228:231], v[20:23], a[100:103]\n"
"ds_read_b128 a[176:179], v240 offset:45056\n"
"v_mfma_f32_16x16x32_bf16 a[104:107], a[232:235], v[20:23], a[104:107]\n"
"v_exp_f32 v1, v1\n"
"v_mfma_f32_16x16x32_bf16 a[108:111], a[236:239], v[20:23], a[108:111]\n"
"ds_read_b128 a[180:183], v240 offset:45312\n"
"v_cvt_pk_bf16_f32 v16, v0, v1\n"
"v_mfma_f32_16x16x32_bf16 a[112:115], a[240:243], v[20:23], a[112:115]\n"
"v_exp_f32 v2, v2\n"
"v_mfma_f32_16x16x32_bf16 a[116:119], a[244:247], v[20:23], a[116:119]\n"
"ds_read_b128 a[184:187], v240 offset:45568\n"
"v_mfma_f32_16x16x32_bf16 a[120:123], a[248:251], v[20:23], a[120:123]\n"
"v_exp_f32 v3, v3\n"
"v_mfma_f32_16x16x32_bf16 a[124:127], a[252:255], v[20:23], a[124:127]\n"
"buffer_load_dword v250, v216, s[72:75], s46 offen\n"
"v_cvt_pk_bf16_f32 v17, v2, v3\n"
"v_mfma_f32_16x16x32_bf16 v[56:59], v[40:43], v[20:23], v[56:59]\n"
"v_exp_f32 v4, v4\n"
"v_mfma_f32_16x16x32_bf16 v[8:11], a[128:131], v[80:83], v[28:31]\n"
"ds_read_b128 a[188:191], v240 offset:45824\n"
"v_mfma_f32_16x16x32_bf16 v[8:11], a[132:135], v[84:87], v[8:11]\n"
"v_exp_f32 v5, v5\n"
"v_mfma_f32_16x16x32_bf16 v[8:11], a[136:139], v[88:91], v[8:11]\n"
"ds_read_b64_tr_b16 a[224:225], v242 offset:57344\n"
"v_cvt_pk_bf16_f32 v18, v4, v5\n"
"v_mfma_f32_16x16x32_bf16 v[8:11], a[140:143], v[92:95], v[8:11]\n"
"v_exp_f32 v6, v6\n"
"v_mfma_f32_16x16x32_bf16 v[12:15], a[144:147], v[80:83], v[28:31]\n"
"ds_read_b64_tr_b16 a[226:227], v242 offset:61440\n"
"v_mfma_f32_16x16x32_bf16 v[12:15], a[148:151], v[84:87], v[12:15]\n"
"v_exp_f32 v7, v7\n"
"v_mfma_f32_16x16x32_bf16 v[12:15], a[152:155], v[88:91], v[12:15]\n"
"ds_read_b64_tr_b16 a[228:229], v243 offset:57344\n"
"v_cvt_pk_bf16_f32 v19, v6, v7\n"
"v_mfma_f32_16x16x32_bf16 v[12:15], a[156:159], v[92:95], v[12:15]\n"
"s_waitcnt lgkmcnt(11)\n"
"buffer_load_dwordx4 v206, s[24:27], s52 offen offset:1024 lds\n"
"v_mfma_f32_16x16x32_bf16 a[0:3], a[192:195], v[16:19], a[0:3]\n"
"s_add_u32 s52, s52, 8192\n"
"v_exp_f32 v8, v8\n"
"v_mfma_f32_16x16x32_bf16 a[4:7], a[196:199], v[16:19], a[4:7]\n"
"ds_read_b64_tr_b16 a[230:231], v243 offset:61440\n"
"v_mfma_f32_16x16x32_bf16 a[8:11], a[200:203], v[16:19], a[8:11]\n"
"v_exp_f32 v9, v9\n"
"v_mfma_f32_16x16x32_bf16 a[12:15], a[204:207], v[16:19], a[12:15]\n"
"ds_read_b64_tr_b16 a[232:233], v242 offset:57600\n"
"v_cvt_pk_bf16_f32 v20, v8, v9\n"
"v_mfma_f32_16x16x32_bf16 a[16:19], a[208:211], v[16:19], a[16:19]\n"
"v_exp_f32 v10, v10\n"
"v_mfma_f32_16x16x32_bf16 a[20:23], a[212:215], v[16:19], a[20:23]\n"
"ds_read_b64_tr_b16 a[234:235], v242 offset:61696\n"
"v_mfma_f32_16x16x32_bf16 a[24:27], a[216:219], v[16:19], a[24:27]\n"
"v_exp_f32 v11, v11\n"
"v_mfma_f32_16x16x32_bf16 a[28:31], a[220:223], v[16:19], a[28:31]\n"
"buffer_load_dword v250, v216, s[72:75], s47 offen\n"
"v_cvt_pk_bf16_f32 v21, v10, v11\n"
"v_mfma_f32_16x16x32_bf16 v[44:47], v[40:43], v[16:19], v[44:47]\n"
"v_exp_f32 v12, v12\n"
"v_mfma_f32_16x16x32_bf16 v[0:3], a[128:131], v[96:99], v[32:35]\n"
"ds_read_b64_tr_b16 a[236:237], v243 offset:57600\n"
"v_lshrrev_b32 v251, 2, v248\n"
"v_mfma_f32_16x16x32_bf16 v[0:3], a[132:135], v[100:103], v[0:3]\n"
"v_exp_f32 v13, v13\n"
"v_mfma_f32_16x16x32_bf16 v[0:3], a[136:139], v[104:107], v[0:3]\n"
"ds_read_b64_tr_b16 a[238:239], v243 offset:61696\n"
"v_cvt_pk_bf16_f32 v22, v12, v13\n"
"v_mfma_f32_16x16x32_bf16 v[0:3], a[140:143], v[108:111], v[0:3]\n"
"v_exp_f32 v14, v14\n"
"v_mfma_f32_16x16x32_bf16 v[4:7], a[144:147], v[96:99], v[32:35]\n"
"ds_read_b64_tr_b16 a[240:241], v242 offset:57856\n"
"v_mul_u32_u24 v251, 9216, v251\n"
"v_mfma_f32_16x16x32_bf16 v[4:7], a[148:151], v[100:103], v[4:7]\n"
"v_exp_f32 v15, v15\n"
"v_mfma_f32_16x16x32_bf16 v[4:7], a[152:155], v[104:107], v[4:7]\n"
"ds_read_b64_tr_b16 a[242:243], v242 offset:61952\n"
"v_cvt_pk_bf16_f32 v23, v14, v15\n"
"v_mfma_f32_16x16x32_bf16 v[4:7], a[156:159], v[108:111], v[4:7]\n"
"buffer_load_dwordx4 v206, s[24:27], s52 offen offset:2048 lds\n"
"v_and_b32 v217, 3, v248\n"
"v_mfma_f32_16x16x32_bf16 a[32:35], a[192:195], v[20:23], a[32:35]\n"
"s_add_u32 s52, s52, 8192\n"
"v_exp_f32 v0, v0\n"
"v_mfma_f32_16x16x32_bf16 a[36:39], a[196:199], v[20:23], a[36:39]\n"
"ds_read_b64_tr_b16 a[244:245], v243 offset:57856\n"
"v_add_u32 v255, 1, v217\n"
"v_mfma_f32_16x16x32_bf16 a[40:43], a[200:203], v[20:23], a[40:43]\n"
"v_exp_f32 v1, v1\n"
"v_mfma_f32_16x16x32_bf16 a[44:47], a[204:207], v[20:23], a[44:47]\n"
"ds_read_b64_tr_b16 a[246:247], v243 offset:61952\n"
"v_cvt_pk_bf16_f32 v16, v0, v1\n"
"v_mfma_f32_16x16x32_bf16 a[48:51], a[208:211], v[20:23], a[48:51]\n"
"v_exp_f32 v2, v2\n"
"v_mfma_f32_16x16x32_bf16 a[52:55], a[212:215], v[20:23], a[52:55]\n"
"ds_read_b64_tr_b16 a[248:249], v242 offset:58112\n"
"v_and_b32 v255, 4, v255\n"
"v_mfma_f32_16x16x32_bf16 a[56:59], a[216:219], v[20:23], a[56:59]\n"
"v_exp_f32 v3, v3\n"
"v_mfma_f32_16x16x32_bf16 a[60:63], a[220:223], v[20:23], a[60:63]\n"
"v_cvt_pk_bf16_f32 v17, v2, v3\n"
"v_lshlrev_b32 v255, 26, v255\n"
"v_mfma_f32_16x16x32_bf16 v[48:51], v[40:43], v[20:23], v[48:51]\n"
"v_exp_f32 v4, v4\n"
"v_mfma_f32_16x16x32_bf16 v[8:11], a[128:131], v[112:115], v[36:39]\n"
"ds_read_b64_tr_b16 a[250:251], v242 offset:62208\n"
"v_lshl_add_u32 v217, v217, 4, v255\n"
"v_mfma_f32_16x16x32_bf16 v[8:11], a[132:135], v[116:119], v[8:11]\n"
"v_exp_f32 v5, v5\n"
"v_mfma_f32_16x16x32_bf16 v[8:11], a[136:139], v[120:123], v[8:11]\n"
"ds_read_b64_tr_b16 a[252:253], v243 offset:58112\n"
"v_cvt_pk_bf16_f32 v18, v4, v5\n"
"v_mfma_f32_16x16x32_bf16 v[8:11], a[140:143], v[124:127], v[8:11]\n"
"v_exp_f32 v6, v6\n"
"v_mfma_f32_16x16x32_bf16 v[12:15], a[144:147], v[112:115], v[36:39]\n"
"ds_read_b64_tr_b16 a[254:255], v243 offset:62208\n"
"v_lshl_add_u32 v252, v251, 1, v217\n"
"v_mfma_f32_16x16x32_bf16 v[12:15], a[148:151], v[116:119], v[12:15]\n"
"v_exp_f32 v7, v7\n"
"v_mfma_f32_16x16x32_bf16 v[12:15], a[152:155], v[120:123], v[12:15]\n"
"v_cvt_pk_bf16_f32 v19, v6, v7\n"
"v_add_u32 v251, v251, v217\n"
"v_mfma_f32_16x16x32_bf16 v[12:15], a[156:159], v[124:127], v[12:15]\n"
"s_mul_i32 s62, s41, 1327104\n"
"buffer_load_dwordx4 v206, s[24:27], s52 offen offset:3072 lds\n"
"v_mfma_f32_16x16x32_bf16 a[64:67], a[192:195], v[16:19], a[64:67]\n"
"s_sub_u32 s52, s52, 61312\n"
"v_exp_f32 v8, v8\n"
"v_mfma_f32_16x16x32_bf16 a[68:71], a[196:199], v[16:19], a[68:71]\n"
"s_lshl_b32 s49, s48, 1\n"
"v_mfma_f32_16x16x32_bf16 a[72:75], a[200:203], v[16:19], a[72:75]\n"
"s_add_u32 s49, s49, s62\n"
"v_exp_f32 v9, v9\n"
"v_mfma_f32_16x16x32_bf16 a[76:79], a[204:207], v[16:19], a[76:79]\n"
"buffer_load_dwordx4 v[128:131], v252, s[64:67], s49 offen\n"
"v_cvt_pk_bf16_f32 v20, v8, v9\n"
"v_mfma_f32_16x16x32_bf16 a[80:83], a[208:211], v[16:19], a[80:83]\n"
"v_exp_f32 v10, v10\n"
"v_mfma_f32_16x16x32_bf16 a[84:87], a[212:215], v[16:19], a[84:87]\n"
"buffer_load_dwordx4 v[164:167], v252, s[68:71], s49 offen\n"
"v_mfma_f32_16x16x32_bf16 a[88:91], a[216:219], v[16:19], a[88:91]\n"
"s_add_u32 s49, s49, 9216\n"
"v_exp_f32 v11, v11\n"
"v_mfma_f32_16x16x32_bf16 a[92:95], a[220:223], v[16:19], a[92:95]\n"
"s_waitcnt vmcnt(23) lgkmcnt(0)\n"
"s_barrier\n"
"buffer_load_dwordx4 v[132:135], v252, s[64:67], s49 offen\n"
"v_cvt_pk_bf16_f32 v21, v10, v11\n"
"v_mfma_f32_16x16x32_bf16 v[52:55], v[40:43], v[16:19], v[52:55]\n"
"v_exp_f32 v12, v12\n"
"v_mfma_f32_16x16x32_bf16 v[0:3], a[160:163], v[64:67], v[24:27]\n"
"ds_read_b128 a[128:131], v241 offset:0\n"
"v_mfma_f32_16x16x32_bf16 v[0:3], a[164:167], v[68:71], v[0:3]\n"
"v_exp_f32 v13, v13\n"
"v_mfma_f32_16x16x32_bf16 v[0:3], a[168:171], v[72:75], v[0:3]\n"
"ds_read_b128 a[132:135], v241 offset:256\n"
"v_cvt_pk_bf16_f32 v22, v12, v13\n"
"v_mfma_f32_16x16x32_bf16 v[0:3], a[172:175], v[76:79], v[0:3]\n"
"v_exp_f32 v14, v14\n"
"v_mfma_f32_16x16x32_bf16 v[4:7], a[176:179], v[64:67], v[24:27]\n"
"ds_read_b128 a[136:139], v241 offset:512\n"
"v_mfma_f32_16x16x32_bf16 v[4:7], a[180:183], v[68:71], v[4:7]\n"
"v_exp_f32 v15, v15\n"
"v_mfma_f32_16x16x32_bf16 v[4:7], a[184:187], v[72:75], v[4:7]\n"
"s_add_u32 m0, s53, 32768\n"
"ds_read_b128 a[140:143], v241 offset:768\n"
"v_cvt_pk_bf16_f32 v23, v14, v15\n"
"v_mfma_f32_16x16x32_bf16 v[4:7], a[188:191], v[76:79], v[4:7]\n"
"buffer_load_dwordx4 v206, s[24:27], s52 offen offset:0 lds\n"
"v_mfma_f32_16x16x32_bf16 a[96:99], a[192:195], v[20:23], a[96:99]\n"
"s_add_u32 s52, s52, 8192\n"
"v_exp_f32 v0, v0\n"
"v_mfma_f32_16x16x32_bf16 a[100:103], a[196:199], v[20:23], a[100:103]\n"
"ds_read_b128 a[144:147], v241 offset:4096\n"
"v_mfma_f32_16x16x32_bf16 a[104:107], a[200:203], v[20:23], a[104:107]\n"
"v_exp_f32 v1, v1\n"
"v_mfma_f32_16x16x32_bf16 a[108:111], a[204:207], v[20:23], a[108:111]\n"
"ds_read_b128 a[148:151], v241 offset:4352\n"
"v_cvt_pk_bf16_f32 v16, v0, v1\n"
"v_mfma_f32_16x16x32_bf16 a[112:115], a[208:211], v[20:23], a[112:115]\n"
"v_exp_f32 v2, v2\n"
"v_mfma_f32_16x16x32_bf16 a[116:119], a[212:215], v[20:23], a[116:119]\n"
"ds_read_b128 a[152:155], v241 offset:4608\n"
"v_mfma_f32_16x16x32_bf16 a[120:123], a[216:219], v[20:23], a[120:123]\n"
"v_exp_f32 v3, v3\n"
"v_mfma_f32_16x16x32_bf16 a[124:127], a[220:223], v[20:23], a[124:127]\n"
"buffer_load_dwordx4 v[168:171], v252, s[68:71], s49 offen\n"
"v_cvt_pk_bf16_f32 v17, v2, v3\n"
"v_mfma_f32_16x16x32_bf16 v[56:59], v[40:43], v[20:23], v[56:59]\n"
"s_add_u32 s49, s49, 285696\n"
"v_exp_f32 v4, v4\n"
"v_mfma_f32_16x16x32_bf16 v[8:11], a[160:163], v[80:83], v[28:31]\n"
"ds_read_b128 a[156:159], v241 offset:4864\n"
"v_mfma_f32_16x16x32_bf16 v[8:11], a[164:167], v[84:87], v[8:11]\n"
"v_exp_f32 v5, v5\n"
"v_mfma_f32_16x16x32_bf16 v[8:11], a[168:171], v[88:91], v[8:11]\n"
"ds_read_b64_tr_b16 a[192:193], v244 offset:16384\n"
"v_cvt_pk_bf16_f32 v18, v4, v5\n"
"v_mfma_f32_16x16x32_bf16 v[8:11], a[172:175], v[92:95], v[8:11]\n"
"v_exp_f32 v6, v6\n"
"v_mfma_f32_16x16x32_bf16 v[12:15], a[176:179], v[80:83], v[28:31]\n"
"ds_read_b64_tr_b16 a[194:195], v244 offset:20480\n"
"v_mfma_f32_16x16x32_bf16 v[12:15], a[180:183], v[84:87], v[12:15]\n"
"v_exp_f32 v7, v7\n"
"v_mfma_f32_16x16x32_bf16 v[12:15], a[184:187], v[88:91], v[12:15]\n"
"ds_read_b64_tr_b16 a[196:197], v245 offset:16384\n"
"v_cvt_pk_bf16_f32 v19, v6, v7\n"
"v_mfma_f32_16x16x32_bf16 v[12:15], a[188:191], v[92:95], v[12:15]\n"
"s_waitcnt lgkmcnt(11)\n"
"buffer_load_dwordx4 v206, s[24:27], s52 offen offset:1024 lds\n"
"v_mfma_f32_16x16x32_bf16 a[0:3], a[224:227], v[16:19], a[0:3]\n"
"s_add_u32 s52, s52, 8192\n"
"v_exp_f32 v8, v8\n"
"v_mfma_f32_16x16x32_bf16 a[4:7], a[228:231], v[16:19], a[4:7]\n"
"ds_read_b64_tr_b16 a[198:199], v245 offset:20480\n"
"v_mfma_f32_16x16x32_bf16 a[8:11], a[232:235], v[16:19], a[8:11]\n"
"v_exp_f32 v9, v9\n"
"v_mfma_f32_16x16x32_bf16 a[12:15], a[236:239], v[16:19], a[12:15]\n"
"ds_read_b64_tr_b16 a[200:201], v244 offset:16640\n"
"v_cvt_pk_bf16_f32 v20, v8, v9\n"
"v_mfma_f32_16x16x32_bf16 a[16:19], a[240:243], v[16:19], a[16:19]\n"
"v_exp_f32 v10, v10\n"
"v_mfma_f32_16x16x32_bf16 a[20:23], a[244:247], v[16:19], a[20:23]\n"
"ds_read_b64_tr_b16 a[202:203], v244 offset:20736\n"
"v_mfma_f32_16x16x32_bf16 a[24:27], a[248:251], v[16:19], a[24:27]\n"
"v_exp_f32 v11, v11\n"
"v_mfma_f32_16x16x32_bf16 a[28:31], a[252:255], v[16:19], a[28:31]\n"
"buffer_load_dwordx4 v[136:139], v252, s[64:67], s49 offen\n"
"v_cvt_pk_bf16_f32 v21, v10, v11\n"
"v_mfma_f32_16x16x32_bf16 v[44:47], v[40:43], v[16:19], v[44:47]\n"
"v_exp_f32 v12, v12\n"
"v_mfma_f32_16x16x32_bf16 v[0:3], a[160:163], v[96:99], v[32:35]\n"
"ds_read_b64_tr_b16 a[204:205], v245 offset:16640\n"
"v_mfma_f32_16x16x32_bf16 v[0:3], a[164:167], v[100:103], v[0:3]\n"
"v_exp_f32 v13, v13\n"
"v_mfma_f32_16x16x32_bf16 v[0:3], a[168:171], v[104:107], v[0:3]\n"
"ds_read_b64_tr_b16 a[206:207], v245 offset:20736\n"
"v_cvt_pk_bf16_f32 v22, v12, v13\n"
"v_mfma_f32_16x16x32_bf16 v[0:3], a[172:175], v[108:111], v[0:3]\n"
"v_exp_f32 v14, v14\n"
"v_mfma_f32_16x16x32_bf16 v[4:7], a[176:179], v[96:99], v[32:35]\n"
"ds_read_b64_tr_b16 a[208:209], v244 offset:16896\n"
"v_mfma_f32_16x16x32_bf16 v[4:7], a[180:183], v[100:103], v[4:7]\n"
"v_exp_f32 v15, v15\n"
"v_mfma_f32_16x16x32_bf16 v[4:7], a[184:187], v[104:107], v[4:7]\n"
"ds_read_b64_tr_b16 a[210:211], v244 offset:20992\n"
"v_cvt_pk_bf16_f32 v23, v14, v15\n"
"v_mfma_f32_16x16x32_bf16 v[4:7], a[188:191], v[108:111], v[4:7]\n"
"buffer_load_dwordx4 v206, s[24:27], s52 offen offset:2048 lds\n"
"v_mfma_f32_16x16x32_bf16 a[32:35], a[224:227], v[20:23], a[32:35]\n"
"s_add_u32 s52, s52, 8192\n"
"v_exp_f32 v0, v0\n"
"v_mfma_f32_16x16x32_bf16 a[36:39], a[228:231], v[20:23], a[36:39]\n"
"ds_read_b64_tr_b16 a[212:213], v245 offset:16896\n"
"v_mfma_f32_16x16x32_bf16 a[40:43], a[232:235], v[20:23], a[40:43]\n"
"v_exp_f32 v1, v1\n"
"v_mfma_f32_16x16x32_bf16 a[44:47], a[236:239], v[20:23], a[44:47]\n"
"ds_read_b64_tr_b16 a[214:215], v245 offset:20992\n"
"v_cvt_pk_bf16_f32 v16, v0, v1\n"
"v_mfma_f32_16x16x32_bf16 a[48:51], a[240:243], v[20:23], a[48:51]\n"
"v_exp_f32 v2, v2\n"
"v_mfma_f32_16x16x32_bf16 a[52:55], a[244:247], v[20:23], a[52:55]\n"
"ds_read_b64_tr_b16 a[216:217], v244 offset:17152\n"
"v_mfma_f32_16x16x32_bf16 a[56:59], a[248:251], v[20:23], a[56:59]\n"
"v_exp_f32 v3, v3\n"
"v_mfma_f32_16x16x32_bf16 a[60:63], a[252:255], v[20:23], a[60:63]\n"
"buffer_load_dwordx4 v[172:175], v252, s[68:71], s49 offen\n"
"v_cvt_pk_bf16_f32 v17, v2, v3\n"
"v_mfma_f32_16x16x32_bf16 v[48:51], v[40:43], v[20:23], v[48:51]\n"
"s_add_u32 s49, s49, 9216\n"
"v_exp_f32 v4, v4\n"
"v_mfma_f32_16x16x32_bf16 v[8:11], a[160:163], v[112:115], v[36:39]\n"
"ds_read_b64_tr_b16 a[218:219], v244 offset:21248\n"
"v_mfma_f32_16x16x32_bf16 v[8:11], a[164:167], v[116:119], v[8:11]\n"
"v_exp_f32 v5, v5\n"
"v_mfma_f32_16x16x32_bf16 v[8:11], a[168:171], v[120:123], v[8:11]\n"
"ds_read_b64_tr_b16 a[220:221], v245 offset:17152\n"
"v_cvt_pk_bf16_f32 v18, v4, v5\n"
"v_mfma_f32_16x16x32_bf16 v[8:11], a[172:175], v[124:127], v[8:11]\n"
"v_exp_f32 v6, v6\n"
"v_mfma_f32_16x16x32_bf16 v[12:15], a[176:179], v[112:115], v[36:39]\n"
"ds_read_b64_tr_b16 a[222:223], v245 offset:21248\n"
"v_mfma_f32_16x16x32_bf16 v[12:15], a[180:183], v[116:119], v[12:15]\n"
"v_exp_f32 v7, v7\n"
"v_mfma_f32_16x16x32_bf16 v[12:15], a[184:187], v[120:123], v[12:15]\n"
"buffer_load_dwordx4 v[140:143], v252, s[64:67], s49 offen\n"
"v_cvt_pk_bf16_f32 v19, v6, v7\n"
"v_mfma_f32_16x16x32_bf16 v[12:15], a[188:191], v[124:127], v[12:15]\n"
"buffer_load_dwordx4 v206, s[24:27], s52 offen offset:3072 lds\n"
"v_mfma_f32_16x16x32_bf16 a[64:67], a[224:227], v[16:19], a[64:67]\n"
"s_add_u32 s52, s52, 12288\n"
"v_exp_f32 v8, v8\n"
"v_mfma_f32_16x16x32_bf16 a[68:71], a[228:231], v[16:19], a[68:71]\n"
"buffer_load_dwordx4 v[176:179], v252, s[68:71], s49 offen\n"
"v_mfma_f32_16x16x32_bf16 a[72:75], a[232:235], v[16:19], a[72:75]\n"
"s_add_u32 s49, s49, 285696\n"
"v_exp_f32 v9, v9\n"
"v_mfma_f32_16x16x32_bf16 a[76:79], a[236:239], v[16:19], a[76:79]\n"
"buffer_load_dwordx4 v[144:147], v252, s[64:67], s49 offen\n"
"v_cvt_pk_bf16_f32 v20, v8, v9\n"
"v_mfma_f32_16x16x32_bf16 a[80:83], a[240:243], v[16:19], a[80:83]\n"
"v_exp_f32 v10, v10\n"
"v_mfma_f32_16x16x32_bf16 a[84:87], a[244:247], v[16:19], a[84:87]\n"
"buffer_load_dwordx4 v[180:183], v252, s[68:71], s49 offen\n"
"v_mfma_f32_16x16x32_bf16 a[88:91], a[248:251], v[16:19], a[88:91]\n"
"s_add_u32 s49, s49, 9216\n"
"v_exp_f32 v11, v11\n"
"v_mfma_f32_16x16x32_bf16 a[92:95], a[252:255], v[16:19], a[92:95]\n"
"s_waitcnt lgkmcnt(8)\n"
"buffer_load_dwordx4 v[148:151], v252, s[64:67], s49 offen\n"
"v_cvt_pk_bf16_f32 v21, v10, v11\n"
"v_mfma_f32_16x16x32_bf16 v[52:55], v[40:43], v[16:19], v[52:55]\n"
"v_exp_f32 v12, v12\n"
"v_mfma_f32_16x16x32_bf16 v[0:3], a[128:131], v[64:67], v[24:27]\n"
"ds_read_b128 a[160:163], v241 offset:8192\n"
"v_mfma_f32_16x16x32_bf16 v[0:3], a[132:135], v[68:71], v[0:3]\n"
"v_exp_f32 v13, v13\n"
"v_mfma_f32_16x16x32_bf16 v[0:3], a[136:139], v[72:75], v[0:3]\n"
"ds_read_b128 a[164:167], v241 offset:8448\n"
"v_cvt_pk_bf16_f32 v22, v12, v13\n"
"v_mfma_f32_16x16x32_bf16 v[0:3], a[140:143], v[76:79], v[0:3]\n"
"v_exp_f32 v14, v14\n"
"v_mfma_f32_16x16x32_bf16 v[4:7], a[144:147], v[64:67], v[24:27]\n"
"ds_read_b128 a[168:171], v241 offset:8704\n"
"v_mfma_f32_16x16x32_bf16 v[4:7], a[148:151], v[68:71], v[4:7]\n"
"v_exp_f32 v15, v15\n"
"v_mfma_f32_16x16x32_bf16 v[4:7], a[152:155], v[72:75], v[4:7]\n"
"s_add_u32 m0, s53, 36864\n"
"ds_read_b128 a[172:175], v241 offset:8960\n"
"v_cvt_pk_bf16_f32 v23, v14, v15\n"
"v_mfma_f32_16x16x32_bf16 v[4:7], a[156:159], v[76:79], v[4:7]\n"
"buffer_load_dwordx4 v206, s[24:27], s52 offen offset:0 lds\n"
"v_mfma_f32_16x16x32_bf16 a[96:99], a[224:227], v[20:23], a[96:99]\n"
"s_add_u32 s52, s52, 8192\n"
"v_exp_f32 v0, v0\n"
"v_mfma_f32_16x16x32_bf16 a[100:103], a[228:231], v[20:23], a[100:103]\n"
"ds_read_b128 a[176:179], v241 offset:12288\n"
"v_mfma_f32_16x16x32_bf16 a[104:107], a[232:235], v[20:23], a[104:107]\n"
"v_exp_f32 v1, v1\n"
"v_mfma_f32_16x16x32_bf16 a[108:111], a[236:239], v[20:23], a[108:111]\n"
"ds_read_b128 a[180:183], v241 offset:12544\n"
"v_cvt_pk_bf16_f32 v16, v0, v1\n"
"v_mfma_f32_16x16x32_bf16 a[112:115], a[240:243], v[20:23], a[112:115]\n"
"v_exp_f32 v2, v2\n"
"v_mfma_f32_16x16x32_bf16 a[116:119], a[244:247], v[20:23], a[116:119]\n"
"ds_read_b128 a[184:187], v241 offset:12800\n"
"v_mfma_f32_16x16x32_bf16 a[120:123], a[248:251], v[20:23], a[120:123]\n"
"v_exp_f32 v3, v3\n"
"v_mfma_f32_16x16x32_bf16 a[124:127], a[252:255], v[20:23], a[124:127]\n"
"buffer_load_dwordx4 v[184:187], v252, s[68:71], s49 offen\n"
"v_cvt_pk_bf16_f32 v17, v2, v3\n"
"v_mfma_f32_16x16x32_bf16 v[56:59], v[40:43], v[20:23], v[56:59]\n"
"s_add_u32 s49, s49, 285696\n"
"v_exp_f32 v4, v4\n"
"v_mfma_f32_16x16x32_bf16 v[8:11], a[128:131], v[80:83], v[28:31]\n"
"ds_read_b128 a[188:191], v241 offset:13056\n"
"v_mfma_f32_16x16x32_bf16 v[8:11], a[132:135], v[84:87], v[8:11]\n"
"v_exp_f32 v5, v5\n"
"v_mfma_f32_16x16x32_bf16 v[8:11], a[136:139], v[88:91], v[8:11]\n"
"ds_read_b64_tr_b16 a[224:225], v244 offset:24576\n"
"v_cvt_pk_bf16_f32 v18, v4, v5\n"
"v_mfma_f32_16x16x32_bf16 v[8:11], a[140:143], v[92:95], v[8:11]\n"
"v_exp_f32 v6, v6\n"
"v_mfma_f32_16x16x32_bf16 v[12:15], a[144:147], v[80:83], v[28:31]\n"
"ds_read_b64_tr_b16 a[226:227], v244 offset:28672\n"
"v_mfma_f32_16x16x32_bf16 v[12:15], a[148:151], v[84:87], v[12:15]\n"
"v_exp_f32 v7, v7\n"
"v_mfma_f32_16x16x32_bf16 v[12:15], a[152:155], v[88:91], v[12:15]\n"
"ds_read_b64_tr_b16 a[228:229], v245 offset:24576\n"
"v_cvt_pk_bf16_f32 v19, v6, v7\n"
"v_mfma_f32_16x16x32_bf16 v[12:15], a[156:159], v[92:95], v[12:15]\n"
"s_waitcnt lgkmcnt(11)\n"
"buffer_load_dwordx4 v206, s[24:27], s52 offen offset:1024 lds\n"
"v_mfma_f32_16x16x32_bf16 a[0:3], a[192:195], v[16:19], a[0:3]\n"
"s_add_u32 s52, s52, 8192\n"
"v_exp_f32 v8, v8\n"
"v_mfma_f32_16x16x32_bf16 a[4:7], a[196:199], v[16:19], a[4:7]\n"
"ds_read_b64_tr_b16 a[230:231], v245 offset:28672\n"
"v_mfma_f32_16x16x32_bf16 a[8:11], a[200:203], v[16:19], a[8:11]\n"
"v_exp_f32 v9, v9\n"
"v_mfma_f32_16x16x32_bf16 a[12:15], a[204:207], v[16:19], a[12:15]\n"
"ds_read_b64_tr_b16 a[232:233], v244 offset:24832\n"
"v_cvt_pk_bf16_f32 v20, v8, v9\n"
"v_mfma_f32_16x16x32_bf16 a[16:19], a[208:211], v[16:19], a[16:19]\n"
"v_exp_f32 v10, v10\n"
"v_mfma_f32_16x16x32_bf16 a[20:23], a[212:215], v[16:19], a[20:23]\n"
"ds_read_b64_tr_b16 a[234:235], v244 offset:28928\n"
"v_mfma_f32_16x16x32_bf16 a[24:27], a[216:219], v[16:19], a[24:27]\n"
"v_exp_f32 v11, v11\n"
"v_mfma_f32_16x16x32_bf16 a[28:31], a[220:223], v[16:19], a[28:31]\n"
"buffer_load_dwordx4 v[152:155], v252, s[64:67], s49 offen\n"
"v_cvt_pk_bf16_f32 v21, v10, v11\n"
"v_mfma_f32_16x16x32_bf16 v[44:47], v[40:43], v[16:19], v[44:47]\n"
"v_exp_f32 v12, v12\n"
"v_mfma_f32_16x16x32_bf16 v[0:3], a[128:131], v[96:99], v[32:35]\n"
"ds_read_b64_tr_b16 a[236:237], v245 offset:24832\n"
"v_mfma_f32_16x16x32_bf16 v[0:3], a[132:135], v[100:103], v[0:3]\n"
"v_exp_f32 v13, v13\n"
"v_mfma_f32_16x16x32_bf16 v[0:3], a[136:139], v[104:107], v[0:3]\n"
"ds_read_b64_tr_b16 a[238:239], v245 offset:28928\n"
"v_cvt_pk_bf16_f32 v22, v12, v13\n"
"v_mfma_f32_16x16x32_bf16 v[0:3], a[140:143], v[108:111], v[0:3]\n"
"v_exp_f32 v14, v14\n"
"v_mfma_f32_16x16x32_bf16 v[4:7], a[144:147], v[96:99], v[32:35]\n"
"ds_read_b64_tr_b16 a[240:241], v244 offset:25088\n"
"v_mfma_f32_16x16x32_bf16 v[4:7], a[148:151], v[100:103], v[4:7]\n"
"v_exp_f32 v15, v15\n"
"v_mfma_f32_16x16x32_bf16 v[4:7], a[152:155], v[104:107], v[4:7]\n"
"ds_read_b64_tr_b16 a[242:243], v244 offset:29184\n"
"v_cvt_pk_bf16_f32 v23, v14, v15\n"
"v_mfma_f32_16x16x32_bf16 v[4:7], a[156:159], v[108:111], v[4:7]\n"
"buffer_load_dwordx4 v206, s[24:27], s52 offen offset:2048 lds\n"
"v_mfma_f32_16x16x32_bf16 a[32:35], a[192:195], v[20:23], a[32:35]\n"
"s_add_u32 s52, s52, 8192\n"
"v_exp_f32 v0, v0\n"
"v_mfma_f32_16x16x32_bf16 a[36:39], a[196:199], v[20:23], a[36:39]\n"
"ds_read_b64_tr_b16 a[244:245], v245 offset:25088\n"
"v_mfma_f32_16x16x32_bf16 a[40:43], a[200:203], v[20:23], a[40:43]\n"
"v_exp_f32 v1, v1\n"
"v_mfma_f32_16x16x32_bf16 a[44:47], a[204:207], v[20:23], a[44:47]\n"
"ds_read_b64_tr_b16 a[246:247], v245 offset:29184\n"
"v_cvt_pk_bf16_f32 v16, v0, v1\n"
"v_mfma_f32_16x16x32_bf16 a[48:51], a[208:211], v[20:23], a[48:51]\n"
"v_exp_f32 v2, v2\n"
"v_mfma_f32_16x16x32_bf16 a[52:55], a[212:215], v[20:23], a[52:55]\n"
"ds_read_b64_tr_b16 a[248:249], v244 offset:25344\n"
"v_mfma_f32_16x16x32_bf16 a[56:59], a[216:219], v[20:23], a[56:59]\n"
"v_exp_f32 v3, v3\n"
"v_mfma_f32_16x16x32_bf16 a[60:63], a[220:223], v[20:23], a[60:63]\n"
"buffer_load_dwordx4 v[188:191], v252, s[68:71], s49 offen\n"
"v_cvt_pk_bf16_f32 v17, v2, v3\n"
"v_mfma_f32_16x16x32_bf16 v[48:51], v[40:43], v[20:23], v[48:51]\n"
"s_add_u32 s49, s49, 9216\n"
"v_exp_f32 v4, v4\n"
"v_mfma_f32_16x16x32_bf16 v[8:11], a[128:131], v[112:115], v[36:39]\n"
"ds_read_b64_tr_b16 a[250:251], v244 offset:29440\n"
"v_mfma_f32_16x16x32_bf16 v[8:11], a[132:135], v[116:119], v[8:11]\n"
"v_exp_f32 v5, v5\n"
"v_mfma_f32_16x16x32_bf16 v[8:11], a[136:139], v[120:123], v[8:11]\n"
"ds_read_b64_tr_b16 a[252:253], v245 offset:25344\n"
"v_cvt_pk_bf16_f32 v18, v4, v5\n"
"v_mfma_f32_16x16x32_bf16 v[8:11], a[140:143], v[124:127], v[8:11]\n"
"v_exp_f32 v6, v6\n"
"v_mfma_f32_16x16x32_bf16 v[12:15], a[144:147], v[112:115], v[36:39]\n"
"ds_read_b64_tr_b16 a[254:255], v245 offset:29440\n"
"v_mfma_f32_16x16x32_bf16 v[12:15], a[148:151], v[116:119], v[12:15]\n"
"v_exp_f32 v7, v7\n"
"v_mfma_f32_16x16x32_bf16 v[12:15], a[152:155], v[120:123], v[12:15]\n"
"buffer_load_dwordx4 v[156:159], v252, s[64:67], s49 offen\n"
"v_cvt_pk_bf16_f32 v19, v6, v7\n"
"v_mfma_f32_16x16x32_bf16 v[12:15], a[156:159], v[124:127], v[12:15]\n"
"buffer_load_dwordx4 v206, s[24:27], s52 offen offset:3072 lds\n"
"v_mfma_f32_16x16x32_bf16 a[64:67], a[192:195], v[16:19], a[64:67]\n"
"s_sub_u32 s52, s52, 61312\n"
"v_exp_f32 v8, v8\n"
"v_mfma_f32_16x16x32_bf16 a[68:71], a[196:199], v[16:19], a[68:71]\n"
"buffer_load_dwordx4 v[192:195], v252, s[68:71], s49 offen\n"
"v_mfma_f32_16x16x32_bf16 a[72:75], a[200:203], v[16:19], a[72:75]\n"
"s_add_u32 s49, s49, 285696\n"
"v_exp_f32 v9, v9\n"
"v_mfma_f32_16x16x32_bf16 a[76:79], a[204:207], v[16:19], a[76:79]\n"
"buffer_load_dwordx4 v[160:163], v251, s[64:67], s49 offen\n"
"v_cvt_pk_bf16_f32 v20, v8, v9\n"
"v_mfma_f32_16x16x32_bf16 a[80:83], a[208:211], v[16:19], a[80:83]\n"
"v_exp_f32 v10, v10\n"
"v_mfma_f32_16x16x32_bf16 a[84:87], a[212:215], v[16:19], a[84:87]\n"
"buffer_load_dwordx4 v[196:199], v251, s[68:71], s49 offen\n"
"v_mfma_f32_16x16x32_bf16 a[88:91], a[216:219], v[16:19], a[88:91]\n"
"s_lshl_b32 s62, s41, 6\n"
"v_exp_f32 v11, v11\n"
"v_mfma_f32_16x16x32_bf16 a[92:95], a[220:223], v[16:19], a[92:95]\n"
"s_waitcnt vmcnt(39) lgkmcnt(0)\n"
"s_barrier\n"
"v_cvt_pk_bf16_f32 v21, v10, v11\n"
"v_add_u32 v253, s62, v248\n"
"v_mfma_f32_16x16x32_bf16 v[52:55], v[40:43], v[16:19], v[52:55]\n"
"v_exp_f32 v12, v12\n"
"v_mfma_f32_16x16x32_bf16 v[0:3], a[160:163], v[64:67], v[24:27]\n"
"ds_read_b128 a[128:131], v241 offset:32768\n"
"v_mov_b32 v255, 0x10000000\n"
"v_mfma_f32_16x16x32_bf16 v[0:3], a[164:167], v[68:71], v[0:3]\n"
"v_exp_f32 v13, v13\n"
"v_mfma_f32_16x16x32_bf16 v[0:3], a[168:171], v[72:75], v[0:3]\n"
"ds_read_b128 a[132:135], v241 offset:33024\n"
"v_cvt_pk_bf16_f32 v22, v12, v13\n"
"v_mfma_f32_16x16x32_bf16 v[0:3], a[172:175], v[76:79], v[0:3]\n"
"v_exp_f32 v14, v14\n"
"v_mfma_f32_16x16x32_bf16 v[4:7], a[176:179], v[64:67], v[24:27]\n"
"ds_read_b128 a[136:139], v241 offset:33280\n"
"v_cmp_gt_u32 vcc, 144, v253\n"
"v_mfma_f32_16x16x32_bf16 v[4:7], a[180:183], v[68:71], v[4:7]\n"
"v_exp_f32 v15, v15\n"
"v_mfma_f32_16x16x32_bf16 v[4:7], a[184:187], v[72:75], v[4:7]\n"
"s_add_u32 m0, s53, 65536\n"
"ds_read_b128 a[140:143], v241 offset:33536\n"
"v_cvt_pk_bf16_f32 v23, v14, v15\n"
"v_mfma_f32_16x16x32_bf16 v[4:7], a[188:191], v[76:79], v[4:7]\n"
"buffer_load_dwordx4 v206, s[24:27], s52 offen offset:0 lds\n"
"v_lshlrev_b32 v254, 3, v253\n"
"v_mfma_f32_16x16x32_bf16 a[96:99], a[192:195], v[20:23], a[96:99]\n"
"s_add_u32 s52, s52, 8192\n"
"v_exp_f32 v0, v0\n"
"v_mfma_f32_16x16x32_bf16 a[100:103], a[196:199], v[20:23], a[100:103]\n"
"s_nop 0\n"
"ds_read_b128 a[144:147], v241 offset:36864\n"
"v_mfma_f32_16x16x32_bf16 a[104:107], a[200:203], v[20:23], a[104:107]\n"
"v_exp_f32 v1, v1\n"
"v_mfma_f32_16x16x32_bf16 a[108:111], a[204:207], v[20:23], a[108:111]\n"
"ds_read_b128 a[148:151], v241 offset:37120\n"
"v_cvt_pk_bf16_f32 v16, v0, v1\n"
"v_mfma_f32_16x16x32_bf16 a[112:115], a[208:211], v[20:23], a[112:115]\n"
"v_exp_f32 v2, v2\n"
"v_mfma_f32_16x16x32_bf16 a[116:119], a[212:215], v[20:23], a[116:119]\n"
"ds_read_b128 a[152:155], v241 offset:37376\n"
"v_cndmask_b32 v254, v255, v254, vcc\n"
"v_mfma_f32_16x16x32_bf16 a[120:123], a[216:219], v[20:23], a[120:123]\n"
"v_exp_f32 v3, v3\n"
"v_mfma_f32_16x16x32_bf16 a[124:127], a[220:223], v[20:23], a[124:127]\n"
"v_cvt_pk_bf16_f32 v17, v2, v3\n"
"v_mfma_f32_16x16x32_bf16 v[56:59], v[40:43], v[20:23], v[56:59]\n"
"v_exp_f32 v4, v4\n"
"v_mfma_f32_16x16x32_bf16 v[8:11], a[160:163], v[80:83], v[28:31]\n"
"ds_read_b128 a[156:159], v241 offset:37632\n"
"v_mfma_f32_16x16x32_bf16 v[8:11], a[164:167], v[84:87], v[8:11]\n"
"v_exp_f32 v5, v5\n"
"v_mfma_f32_16x16x32_bf16 v[8:11], a[168:171], v[88:91], v[8:11]\n"
"ds_read_b64_tr_b16 a[192:193], v244 offset:49152\n"
"v_cvt_pk_bf16_f32 v18, v4, v5\n"
"v_mfma_f32_16x16x32_bf16 v[8:11], a[172:175], v[92:95], v[8:11]\n"
"v_exp_f32 v6, v6\n"
"v_mfma_f32_16x16x32_bf16 v[12:15], a[176:179], v[80:83], v[28:31]\n"
"ds_read_b64_tr_b16 a[194:195], v244 offset:53248\n"
"v_mfma_f32_16x16x32_bf16 v[12:15], a[180:183], v[84:87], v[12:15]\n"
"v_exp_f32 v7, v7\n"
"v_mfma_f32_16x16x32_bf16 v[12:15], a[184:187], v[88:91], v[12:15]\n"
"ds_read_b64_tr_b16 a[196:197], v245 offset:49152\n"
"v_cvt_pk_bf16_f32 v19, v6, v7\n"
"v_mfma_f32_16x16x32_bf16 v[12:15], a[188:191], v[92:95], v[12:15]\n"
"s_waitcnt lgkmcnt(11)\n"
"buffer_load_dwordx4 v206, s[24:27], s52 offen offset:1024 lds\n"
"v_mfma_f32_16x16x32_bf16 a[0:3], a[224:227], v[16:19], a[0:3]\n"
"s_add_u32 s52, s52, 8192\n"
"v_exp_f32 v8, v8\n"
"v_mfma_f32_16x16x32_bf16 a[4:7], a[228:231], v[16:19], a[4:7]\n"
"ds_read_b64_tr_b16 a[198:199], v245 offset:53248\n"
"v_mfma_f32_16x16x32_bf16 a[8:11], a[232:235], v[16:19], a[8:11]\n"
"v_exp_f32 v9, v9\n"
"v_mfma_f32_16x16x32_bf16 a[12:15], a[236:239], v[16:19], a[12:15]\n"
"ds_read_b64_tr_b16 a[200:201], v244 offset:49408\n"
"v_cvt_pk_bf16_f32 v20, v8, v9\n"
"v_mfma_f32_16x16x32_bf16 a[16:19], a[240:243], v[16:19], a[16:19]\n"
"v_exp_f32 v10, v10\n"
"v_mfma_f32_16x16x32_bf16 a[20:23], a[244:247], v[16:19], a[20:23]\n"
"ds_read_b64_tr_b16 a[202:203], v244 offset:53504\n"
"v_mfma_f32_16x16x32_bf16 a[24:27], a[248:251], v[16:19], a[24:27]\n"
"v_exp_f32 v11, v11\n"
"v_mfma_f32_16x16x32_bf16 a[28:31], a[252:255], v[16:19], a[28:31]\n"
"v_cvt_pk_bf16_f32 v21, v10, v11\n"
"v_mfma_f32_16x16x32_bf16 v[44:47], v[40:43], v[16:19], v[44:47]\n"
"v_exp_f32 v12, v12\n"
"v_mfma_f32_16x16x32_bf16 v[0:3], a[160:163], v[96:99], v[32:35]\n"
"ds_read_b64_tr_b16 a[204:205], v245 offset:49408\n"
"v_mfma_f32_16x16x32_bf16 v[0:3], a[164:167], v[100:103], v[0:3]\n"
"v_exp_f32 v13, v13\n"
"v_mfma_f32_16x16x32_bf16 v[0:3], a[168:171], v[104:107], v[0:3]\n"
"ds_read_b64_tr_b16 a[206:207], v245 offset:53504\n"
"v_cvt_pk_bf16_f32 v22, v12, v13\n"
"v_mfma_f32_16x16x32_bf16 v[0:3], a[172:175], v[108:111], v[0:3]\n"
"v_exp_f32 v14, v14\n"
"v_mfma_f32_16x16x32_bf16 v[4:7], a[176:179], v[96:99], v[32:35]\n"
"ds_read_b64_tr_b16 a[208:209], v244 offset:49664\n"
"v_mfma_f32_16x16x32_bf16 v[4:7], a[180:183], v[100:103], v[4:7]\n"
"v_exp_f32 v15, v15\n"
"v_mfma_f32_16x16x32_bf16 v[4:7], a[184:187], v[104:107], v[4:7]\n"
"ds_read_b64_tr_b16 a[210:211], v244 offset:53760\n"
"v_cvt_pk_bf16_f32 v23, v14, v15\n"
"v_mfma_f32_16x16x32_bf16 v[4:7], a[188:191], v[108:111], v[4:7]\n"
"buffer_load_dwordx4 v206, s[24:27], s52 offen offset:2048 lds\n"
"v_mfma_f32_16x16x32_bf16 a[32:35], a[224:227], v[20:23], a[32:35]\n"
"s_add_u32 s52, s52, 8192\n"
"v_exp_f32 v0, v0\n"
"v_mfma_f32_16x16x32_bf16 a[36:39], a[228:231], v[20:23], a[36:39]\n"
"ds_read_b64_tr_b16 a[212:213], v245 offset:49664\n"
"v_mfma_f32_16x16x32_bf16 a[40:43], a[232:235], v[20:23], a[40:43]\n"
"v_exp_f32 v1, v1\n"
"v_mfma_f32_16x16x32_bf16 a[44:47], a[236:239], v[20:23], a[44:47]\n"
"ds_read_b64_tr_b16 a[214:215], v245 offset:53760\n"
"v_cvt_pk_bf16_f32 v16, v0, v1\n"
"v_mfma_f32_16x16x32_bf16 a[48:51], a[240:243], v[20:23], a[48:51]\n"
"v_exp_f32 v2, v2\n"
"v_mfma_f32_16x16x32_bf16 a[52:55], a[244:247], v[20:23], a[52:55]\n"
"ds_read_b64_tr_b16 a[216:217], v244 offset:49920\n"
"v_mfma_f32_16x16x32_bf16 a[56:59], a[248:251], v[20:23], a[56:59]\n"
"v_exp_f32 v3, v3\n"
"v_mfma_f32_16x16x32_bf16 a[60:63], a[252:255], v[20:23], a[60:63]\n"
"v_cvt_pk_bf16_f32 v17, v2, v3\n"
"v_mfma_f32_16x16x32_bf16 v[48:51], v[40:43], v[20:23], v[48:51]\n"
"v_exp_f32 v4, v4\n"
"v_mfma_f32_16x16x32_bf16 v[8:11], a[160:163], v[112:115], v[36:39]\n"
"ds_read_b64_tr_b16 a[218:219], v244 offset:54016\n"
"v_mfma_f32_16x16x32_bf16 v[8:11], a[164:167], v[116:119], v[8:11]\n"
"v_exp_f32 v5, v5\n"
"v_mfma_f32_16x16x32_bf16 v[8:11], a[168:171], v[120:123], v[8:11]\n"
"ds_read_b64_tr_b16 a[220:221], v245 offset:49920\n"
"v_cvt_pk_bf16_f32 v18, v4, v5\n"
"v_mfma_f32_16x16x32_bf16 v[8:11], a[172:175], v[124:127], v[8:11]\n"
"v_exp_f32 v6, v6\n"
"v_mfma_f32_16x16x32_bf16 v[12:15], a[176:179], v[112:115], v[36:39]\n"
"ds_read_b64_tr_b16 a[222:223], v245 offset:54016\n"
"v_mfma_f32_16x16x32_bf16 v[12:15], a[180:183], v[116:119], v[12:15]\n"
"v_exp_f32 v7, v7\n"
"v_mfma_f32_16x16x32_bf16 v[12:15], a[184:187], v[120:123], v[12:15]\n"
"v_cvt_pk_bf16_f32 v19, v6, v7\n"
"v_mfma_f32_16x16x32_bf16 v[12:15], a[188:191], v[124:127], v[12:15]\n"
"buffer_load_dwordx4 v206, s[24:27], s52 offen offset:3072 lds\n"
"v_mfma_f32_16x16x32_bf16 a[64:67], a[224:227], v[16:19], a[64:67]\n"
"s_add_u32 s52, s52, 12288\n"
"v_exp_f32 v8, v8\n"
"v_mfma_f32_16x16x32_bf16 a[68:71], a[228:231], v[16:19], a[68:71]\n"
"s_nop 0\n"
"v_mfma_f32_16x16x32_bf16 a[72:75], a[232:235], v[16:19], a[72:75]\n"
"v_exp_f32 v9, v9\n"
"v_mfma_f32_16x16x32_bf16 a[76:79], a[236:239], v[16:19], a[76:79]\n"
"v_cvt_pk_bf16_f32 v20, v8, v9\n"
"v_mfma_f32_16x16x32_bf16 a[80:83], a[240:243], v[16:19], a[80:83]\n"
"v_exp_f32 v10, v10\n"
"v_mfma_f32_16x16x32_bf16 a[84:87], a[244:247], v[16:19], a[84:87]\n"
"s_nop 0\n"
"v_mfma_f32_16x16x32_bf16 a[88:91], a[248:251], v[16:19], a[88:91]\n"
"v_exp_f32 v11, v11\n"
"v_mfma_f32_16x16x32_bf16 a[92:95], a[252:255], v[16:19], a[92:95]\n"
"s_waitcnt lgkmcnt(8)\n"
"v_cvt_pk_bf16_f32 v21, v10, v11\n"
"v_mfma_f32_16x16x32_bf16 v[52:55], v[40:43], v[16:19], v[52:55]\n"
"v_exp_f32 v12, v12\n"
"v_mfma_f32_16x16x32_bf16 v[0:3], a[128:131], v[64:67], v[24:27]\n"
"ds_read_b128 a[160:163], v241 offset:40960\n"
"v_mfma_f32_16x16x32_bf16 v[0:3], a[132:135], v[68:71], v[0:3]\n"
"v_exp_f32 v13, v13\n"
"v_mfma_f32_16x16x32_bf16 v[0:3], a[136:139], v[72:75], v[0:3]\n"
"ds_read_b128 a[164:167], v241 offset:41216\n"
"v_cvt_pk_bf16_f32 v22, v12, v13\n"
"v_mfma_f32_16x16x32_bf16 v[0:3], a[140:143], v[76:79], v[0:3]\n"
"v_exp_f32 v14, v14\n"
"v_mfma_f32_16x16x32_bf16 v[4:7], a[144:147], v[64:67], v[24:27]\n"
"ds_read_b128 a[168:171], v241 offset:41472\n"
"v_mfma_f32_16x16x32_bf16 v[4:7], a[148:151], v[68:71], v[4:7]\n"
"v_exp_f32 v15, v15\n"
"v_mfma_f32_16x16x32_bf16 v[4:7], a[152:155], v[72:75], v[4:7]\n"
"s_add_u32 m0, s53, 69632\n"
"ds_read_b128 a[172:175], v241 offset:41728\n"
"v_cvt_pk_bf16_f32 v23, v14, v15\n"
"v_mfma_f32_16x16x32_bf16 v[4:7], a[156:159], v[76:79], v[4:7]\n"
"buffer_load_dwordx4 v206, s[24:27], s52 offen offset:0 lds\n"
"v_mfma_f32_16x16x32_bf16 a[96:99], a[224:227], v[20:23], a[96:99]\n"
"s_add_u32 s52, s52, 8192\n"
"v_exp_f32 v0, v0\n"
"v_mfma_f32_16x16x32_bf16 a[100:103], a[228:231], v[20:23], a[100:103]\n"
"ds_read_b128 a[176:179], v241 offset:45056\n"
"v_mfma_f32_16x16x32_bf16 a[104:107], a[232:235], v[20:23], a[104:107]\n"
"v_exp_f32 v1, v1\n"
"v_mfma_f32_16x16x32_bf16 a[108:111], a[236:239], v[20:23], a[108:111]\n"
"ds_read_b128 a[180:183], v241 offset:45312\n"
"v_cvt_pk_bf16_f32 v16, v0, v1\n"
"v_mfma_f32_16x16x32_bf16 a[112:115], a[240:243], v[20:23], a[112:115]\n"
"v_exp_f32 v2, v2\n"
"v_mfma_f32_16x16x32_bf16 a[116:119], a[244:247], v[20:23], a[116:119]\n"
"ds_read_b128 a[184:187], v241 offset:45568\n"
"v_mfma_f32_16x16x32_bf16 a[120:123], a[248:251], v[20:23], a[120:123]\n"
"v_exp_f32 v3, v3\n"
"v_mfma_f32_16x16x32_bf16 a[124:127], a[252:255], v[20:23], a[124:127]\n"
"v_cvt_pk_bf16_f32 v17, v2, v3\n"
"v_mfma_f32_16x16x32_bf16 v[56:59], v[40:43], v[20:23], v[56:59]\n"
"v_exp_f32 v4, v4\n"
"v_mfma_f32_16x16x32_bf16 v[8:11], a[128:131], v[80:83], v[28:31]\n"
"ds_read_b128 a[188:191], v241 offset:45824\n"
"v_mfma_f32_16x16x32_bf16 v[8:11], a[132:135], v[84:87], v[8:11]\n"
"v_exp_f32 v5, v5\n"
"v_mfma_f32_16x16x32_bf16 v[8:11], a[136:139], v[88:91], v[8:11]\n"
"ds_read_b64_tr_b16 a[224:225], v244 offset:57344\n"
"v_cvt_pk_bf16_f32 v18, v4, v5\n"
"v_mfma_f32_16x16x32_bf16 v[8:11], a[140:143], v[92:95], v[8:11]\n"
"v_exp_f32 v6, v6\n"
"v_mfma_f32_16x16x32_bf16 v[12:15], a[144:147], v[80:83], v[28:31]\n"
"ds_read_b64_tr_b16 a[226:227], v244 offset:61440\n"
"v_mfma_f32_16x16x32_bf16 v[12:15], a[148:151], v[84:87], v[12:15]\n"
"v_exp_f32 v7, v7\n"
"v_mfma_f32_16x16x32_bf16 v[12:15], a[152:155], v[88:91], v[12:15]\n"
"ds_read_b64_tr_b16 a[228:229], v245 offset:57344\n"
"v_cvt_pk_bf16_f32 v19, v6, v7\n"
"v_mfma_f32_16x16x32_bf16 v[12:15], a[156:159], v[92:95], v[12:15]\n"
"s_waitcnt lgkmcnt(11)\n"
"buffer_load_dwordx4 v206, s[24:27], s52 offen offset:1024 lds\n"
"v_mfma_f32_16x16x32_bf16 a[0:3], a[192:195], v[16:19], a[0:3]\n"
"s_add_u32 s52, s52, 8192\n"
"v_exp_f32 v8, v8\n"
"v_mfma_f32_16x16x32_bf16 a[4:7], a[196:199], v[16:19], a[4:7]\n"
"ds_read_b64_tr_b16 a[230:231], v245 offset:61440\n"
"v_mfma_f32_16x16x32_bf16 a[8:11], a[200:203], v[16:19], a[8:11]\n"
"v_exp_f32 v9, v9\n"
"v_mfma_f32_16x16x32_bf16 a[12:15], a[204:207], v[16:19], a[12:15]\n"
"ds_read_b64_tr_b16 a[232:233], v244 offset:57600\n"
"v_cvt_pk_bf16_f32 v20, v8, v9\n"
"v_mfma_f32_16x16x32_bf16 a[16:19], a[208:211], v[16:19], a[16:19]\n"
"v_exp_f32 v10, v10\n"
"v_mfma_f32_16x16x32_bf16 a[20:23], a[212:215], v[16:19], a[20:23]\n"
"ds_read_b64_tr_b16 a[234:235], v244 offset:61696\n"
"v_mfma_f32_16x16x32_bf16 a[24:27], a[216:219], v[16:19], a[24:27]\n"
"v_exp_f32 v11, v11\n"
"v_mfma_f32_16x16x32_bf16 a[28:31], a[220:223], v[16:19], a[28:31]\n"
"v_cvt_pk_bf16_f32 v21, v10, v11\n"
"v_mfma_f32_16x16x32_bf16 v[44:47], v[40:43], v[16:19], v[44:47]\n"
"v_exp_f32 v12, v12\n"
"v_mfma_f32_16x16x32_bf16 v[0:3], a[128:131], v[96:99], v[32:35]\n"
"ds_read_b64_tr_b16 a[236:237], v245 offset:57600\n"
"v_mfma_f32_16x16x32_bf16 v[0:3], a[132:135], v[100:103], v[0:3]\n"
"v_exp_f32 v13, v13\n"
"v_mfma_f32_16x16x32_bf16 v[0:3], a[136:139], v[104:107], v[0:3]\n"
"ds_read_b64_tr_b16 a[238:239], v245 offset:61696\n"
"v_cvt_pk_bf16_f32 v22, v12, v13\n"
"v_mfma_f32_16x16x32_bf16 v[0:3], a[140:143], v[108:111], v[0:3]\n"
"v_exp_f32 v14, v14\n"
"v_mfma_f32_16x16x32_bf16 v[4:7], a[144:147], v[96:99], v[32:35]\n"
"ds_read_b64_tr_b16 a[240:241], v244 offset:57856\n"
"v_mfma_f32_16x16x32_bf16 v[4:7], a[148:151], v[100:103], v[4:7]\n"
"v_exp_f32 v15, v15\n"
"v_mfma_f32_16x16x32_bf16 v[4:7], a[152:155], v[104:107], v[4:7]\n"
"ds_read_b64_tr_b16 a[242:243], v244 offset:61952\n"
"v_cvt_pk_bf16_f32 v23, v14, v15\n"
"v_mfma_f32_16x16x32_bf16 v[4:7], a[156:159], v[108:111], v[4:7]\n"
"buffer_load_dwordx4 v206, s[24:27], s52 offen offset:2048 lds\n"
"v_mfma_f32_16x16x32_bf16 a[32:35], a[192:195], v[20:23], a[32:35]\n"
"s_add_u32 s52, s52, 8192\n"
"v_exp_f32 v0, v0\n"
"v_mfma_f32_16x16x32_bf16 a[36:39], a[196:199], v[20:23], a[36:39]\n"
"ds_read_b64_tr_b16 a[244:245], v245 offset:57856\n"
"v_mfma_f32_16x16x32_bf16 a[40:43], a[200:203], v[20:23], a[40:43]\n"
"v_exp_f32 v1, v1\n"
"v_mfma_f32_16x16x32_bf16 a[44:47], a[204:207], v[20:23], a[44:47]\n"
"ds_read_b64_tr_b16 a[246:247], v245 offset:61952\n"
"v_cvt_pk_bf16_f32 v16, v0, v1\n"
"v_mfma_f32_16x16x32_bf16 a[48:51], a[208:211], v[20:23], a[48:51]\n"
"v_exp_f32 v2, v2\n"
"v_mfma_f32_16x16x32_bf16 a[52:55], a[212:215], v[20:23], a[52:55]\n"
"ds_read_b64_tr_b16 a[248:249], v244 offset:58112\n"
"v_mfma_f32_16x16x32_bf16 a[56:59], a[216:219], v[20:23], a[56:59]\n"
"v_exp_f32 v3, v3\n"
"v_mfma_f32_16x16x32_bf16 a[60:63], a[220:223], v[20:23], a[60:63]\n"
"v_cvt_pk_bf16_f32 v17, v2, v3\n"
"v_mfma_f32_16x16x32_bf16 v[48:51], v[40:43], v[20:23], v[48:51]\n"
"v_exp_f32 v4, v4\n"
"v_mfma_f32_16x16x32_bf16 v[8:11], a[128:131], v[112:115], v[36:39]\n"
"ds_read_b64_tr_b16 a[250:251], v244 offset:62208\n"
"v_mfma_f32_16x16x32_bf16 v[8:11], a[132:135], v[116:119], v[8:11]\n"
"v_exp_f32 v5, v5\n"
"v_mfma_f32_16x16x32_bf16 v[8:11], a[136:139], v[120:123], v[8:11]\n"
"ds_read_b64_tr_b16 a[252:253], v245 offset:58112\n"
"v_cvt_pk_bf16_f32 v18, v4, v5\n"
"v_mfma_f32_16x16x32_bf16 v[8:11], a[140:143], v[124:127], v[8:11]\n"
"v_exp_f32 v6, v6\n"
"v_mfma_f32_16x16x32_bf16 v[12:15], a[144:147], v[112:115], v[36:39]\n"
"ds_read_b64_tr_b16 a[254:255], v245 offset:62208\n"
"v_mfma_f32_16x16x32_bf16 v[12:15], a[148:151], v[116:119], v[12:15]\n"
"v_exp_f32 v7, v7\n"
"v_mfma_f32_16x16x32_bf16 v[12:15], a[152:155], v[120:123], v[12:15]\n"
"v_cvt_pk_bf16_f32 v19, v6, v7\n"
"v_mfma_f32_16x16x32_bf16 v[12:15], a[156:159], v[124:127], v[12:15]\n"
"buffer_load_dwordx4 v206, s[24:27], s52 offen offset:3072 lds\n"
"v_mfma_f32_16x16x32_bf16 a[64:67], a[192:195], v[16:19], a[64:67]\n"
"s_sub_u32 s52, s52, 61312\n"
"v_exp_f32 v8, v8\n"
"v_mfma_f32_16x16x32_bf16 a[68:71], a[196:199], v[16:19], a[68:71]\n"
"s_nop 0\n"
"v_mfma_f32_16x16x32_bf16 a[72:75], a[200:203], v[16:19], a[72:75]\n"
"v_exp_f32 v9, v9\n"
"v_mfma_f32_16x16x32_bf16 a[76:79], a[204:207], v[16:19], a[76:79]\n"
"v_cvt_pk_bf16_f32 v20, v8, v9\n"
"v_mfma_f32_16x16x32_bf16 a[80:83], a[208:211], v[16:19], a[80:83]\n"
"v_exp_f32 v10, v10\n"
"v_mfma_f32_16x16x32_bf16 a[84:87], a[212:215], v[16:19], a[84:87]\n"
"s_nop 0\n"
"v_mfma_f32_16x16x32_bf16 a[88:91], a[216:219], v[16:19], a[88:91]\n"
"v_exp_f32 v11, v11\n"
"v_mfma_f32_16x16x32_bf16 a[92:95], a[220:223], v[16:19], a[92:95]\n"
"s_waitcnt vmcnt(34) lgkmcnt(0)\n"
"s_barrier\n"
"v_cvt_pk_bf16_f32 v21, v10, v11\n"
"v_mfma_f32_16x16x32_bf16 v[52:55], v[40:43], v[16:19], v[52:55]\n"
"v_exp_f32 v12, v12\n"
"v_mfma_f32_16x16x32_bf16 v[0:3], a[160:163], v[64:67], v[24:27]\n"
"ds_read_b64_tr_b16 a[128:129], v202 offset:0\n"
"v_mfma_f32_16x16x32_bf16 v[0:3], a[164:167], v[68:71], v[0:3]\n"
"v_exp_f32 v13, v13\n"
"v_mfma_f32_16x16x32_bf16 v[0:3], a[168:171], v[72:75], v[0:3]\n"
"ds_read_b64_tr_b16 a[130:131], v202 offset:2048\n"
"v_cvt_pk_bf16_f32 v22, v12, v13\n"
"v_mfma_f32_16x16x32_bf16 v[0:3], a[172:175], v[76:79], v[0:3]\n"
"v_exp_f32 v14, v14\n"
"v_mfma_f32_16x16x32_bf16 v[4:7], a[176:179], v[64:67], v[24:27]\n"
"ds_read_b64_tr_b16 a[132:133], v202 offset:4096\n"
"v_mfma_f32_16x16x32_bf16 v[4:7], a[180:183], v[68:71], v[4:7]\n"
"v_exp_f32 v15, v15\n"
"v_mfma_f32_16x16x32_bf16 v[4:7], a[184:187], v[72:75], v[4:7]\n"
"ds_read_b64_tr_b16 a[134:135], v202 offset:6144\n"
"v_cvt_pk_bf16_f32 v23, v14, v15\n"
"v_mfma_f32_16x16x32_bf16 v[4:7], a[188:191], v[76:79], v[4:7]\n"
"ds_read_b64_tr_b16 a[136:137], v202 offset:8192\n"
"v_mfma_f32_16x16x32_bf16 a[96:99], a[192:195], v[20:23], a[96:99]\n"
"v_exp_f32 v0, v0\n"
"v_mfma_f32_16x16x32_bf16 a[100:103], a[196:199], v[20:23], a[100:103]\n"
"ds_read_b64_tr_b16 a[138:139], v202 offset:10240\n"
"v_mfma_f32_16x16x32_bf16 a[104:107], a[200:203], v[20:23], a[104:107]\n"
"v_exp_f32 v1, v1\n"
"v_mfma_f32_16x16x32_bf16 a[108:111], a[204:207], v[20:23], a[108:111]\n"
"ds_read_b64_tr_b16 a[140:141], v202 offset:12288\n"
"v_cvt_pk_bf16_f32 v16, v0, v1\n"
"v_mfma_f32_16x16x32_bf16 a[112:115], a[208:211], v[20:23], a[112:115]\n"
"v_exp_f32 v2, v2\n"
"v_mfma_f32_16x16x32_bf16 a[116:119], a[212:215], v[20:23], a[116:119]\n"
"ds_read_b64_tr_b16 a[142:143], v202 offset:14336\n"
"v_mfma_f32_16x16x32_bf16 a[120:123], a[216:219], v[20:23], a[120:123]\n"
"v_exp_f32 v3, v3\n"
"v_mfma_f32_16x16x32_bf16 a[124:127], a[220:223], v[20:23], a[124:127]\n"
"v_cvt_pk_bf16_f32 v17, v2, v3\n"
"v_mfma_f32_16x16x32_bf16 v[56:59], v[40:43], v[20:23], v[56:59]\n"
"v_exp_f32 v4, v4\n"
"v_mfma_f32_16x16x32_bf16 v[8:11], a[160:163], v[80:83], v[28:31]\n"
"ds_read_b64_tr_b16 a[144:145], v202 offset:16384\n"
"v_mfma_f32_16x16x32_bf16 v[8:11], a[164:167], v[84:87], v[8:11]\n"
"v_exp_f32 v5, v5\n"
"v_mfma_f32_16x16x32_bf16 v[8:11], a[168:171], v[88:91], v[8:11]\n"
"ds_read_b64_tr_b16 a[146:147], v202 offset:18432\n"
"v_cvt_pk_bf16_f32 v18, v4, v5\n"
"v_mfma_f32_16x16x32_bf16 v[8:11], a[172:175], v[92:95], v[8:11]\n"
"v_exp_f32 v6, v6\n"
"v_mfma_f32_16x16x32_bf16 v[12:15], a[176:179], v[80:83], v[28:31]\n"
"ds_read_b64_tr_b16 a[148:149], v202 offset:20480\n"
"v_mfma_f32_16x16x32_bf16 v[12:15], a[180:183], v[84:87], v[12:15]\n"
"v_exp_f32 v7, v7\n"
"v_mfma_f32_16x16x32_bf16 v[12:15], a[184:187], v[88:91], v[12:15]\n"
"ds_read_b64_tr_b16 a[150:151], v202 offset:22528\n"
"v_cvt_pk_bf16_f32 v19, v6, v7\n"
"v_mfma_f32_16x16x32_bf16 v[12:15], a[188:191], v[92:95], v[12:15]\n"
"ds_read_b64_tr_b16 a[152:153], v202 offset:24576\n"
"v_mfma_f32_16x16x32_bf16 a[0:3], a[224:227], v[16:19], a[0:3]\n"
"v_exp_f32 v8, v8\n"
"v_mfma_f32_16x16x32_bf16 a[4:7], a[228:231], v[16:19], a[4:7]\n"
"ds_read_b64_tr_b16 a[154:155], v202 offset:26624\n"
"v_mfma_f32_16x16x32_bf16 a[8:11], a[232:235], v[16:19], a[8:11]\n"
"v_exp_f32 v9, v9\n"
"v_mfma_f32_16x16x32_bf16 a[12:15], a[236:239], v[16:19], a[12:15]\n"
"ds_read_b64_tr_b16 a[156:157], v202 offset:28672\n"
"v_cvt_pk_bf16_f32 v20, v8, v9\n"
"v_mfma_f32_16x16x32_bf16 a[16:19], a[240:243], v[16:19], a[16:19]\n"
"v_exp_f32 v10, v10\n"
"v_mfma_f32_16x16x32_bf16 a[20:23], a[244:247], v[16:19], a[20:23]\n"
"ds_read_b64_tr_b16 a[158:159], v202 offset:30720\n"
"v_mfma_f32_16x16x32_bf16 a[24:27], a[248:251], v[16:19], a[24:27]\n"
"v_exp_f32 v11, v11\n"
"v_mfma_f32_16x16x32_bf16 a[28:31], a[252:255], v[16:19], a[28:31]\n"
"v_cvt_pk_bf16_f32 v21, v10, v11\n"
"v_mfma_f32_16x16x32_bf16 v[44:47], v[40:43], v[16:19], v[44:47]\n"
"v_exp_f32 v12, v12\n"
"v_mfma_f32_16x16x32_bf16 v[0:3], a[160:163], v[96:99], v[32:35]\n"
"ds_read_b64_tr_b16 a[192:193], v205 offset:0\n"
"v_mfma_f32_16x16x32_bf16 v[0:3], a[164:167], v[100:103], v[0:3]\n"
"v_exp_f32 v13, v13\n"
"v_mfma_f32_16x16x32_bf16 v[0:3], a[168:171], v[104:107], v[0:3]\n"
"ds_read_b64_tr_b16 a[194:195], v205 offset:2048\n"
"v_cvt_pk_bf16_f32 v22, v12, v13\n"
"v_mfma_f32_16x16x32_bf16 v[0:3], a[172:175], v[108:111], v[0:3]\n"
"v_exp_f32 v14, v14\n"
"v_mfma_f32_16x16x32_bf16 v[4:7], a[176:179], v[96:99], v[32:35]\n"
"ds_read_b64_tr_b16 a[196:197], v205 offset:4096\n"
"v_mfma_f32_16x16x32_bf16 v[4:7], a[180:183], v[100:103], v[4:7]\n"
"v_exp_f32 v15, v15\n"
"v_mfma_f32_16x16x32_bf16 v[4:7], a[184:187], v[104:107], v[4:7]\n"
"ds_read_b64_tr_b16 a[198:199], v205 offset:6144\n"
"v_cvt_pk_bf16_f32 v23, v14, v15\n"
"v_mfma_f32_16x16x32_bf16 v[4:7], a[188:191], v[108:111], v[4:7]\n"
"ds_read_b64_tr_b16 a[200:201], v205 offset:8192\n"
"v_mfma_f32_16x16x32_bf16 a[32:35], a[224:227], v[20:23], a[32:35]\n"
"v_exp_f32 v0, v0\n"
"v_mfma_f32_16x16x32_bf16 a[36:39], a[228:231], v[20:23], a[36:39]\n"
"ds_read_b64_tr_b16 a[202:203], v205 offset:10240\n"
"v_mfma_f32_16x16x32_bf16 a[40:43], a[232:235], v[20:23], a[40:43]\n"
"v_exp_f32 v1, v1\n"
"v_mfma_f32_16x16x32_bf16 a[44:47], a[236:239], v[20:23], a[44:47]\n"
"ds_read_b64_tr_b16 a[204:205], v205 offset:12288\n"
"v_cvt_pk_bf16_f32 v16, v0, v1\n"
"v_mfma_f32_16x16x32_bf16 a[48:51], a[240:243], v[20:23], a[48:51]\n"
"v_exp_f32 v2, v2\n"
"v_mfma_f32_16x16x32_bf16 a[52:55], a[244:247], v[20:23], a[52:55]\n"
"ds_read_b64_tr_b16 a[206:207], v205 offset:14336\n"
"v_mfma_f32_16x16x32_bf16 a[56:59], a[248:251], v[20:23], a[56:59]\n"
"v_exp_f32 v3, v3\n"
"v_mfma_f32_16x16x32_bf16 a[60:63], a[252:255], v[20:23], a[60:63]\n"
"v_cvt_pk_bf16_f32 v17, v2, v3\n"
"v_mfma_f32_16x16x32_bf16 v[48:51], v[40:43], v[20:23], v[48:51]\n"
"v_exp_f32 v4, v4\n"
"v_mfma_f32_16x16x32_bf16 v[8:11], a[160:163], v[112:115], v[36:39]\n"
"s_nop 0\n"
"v_mfma_f32_16x16x32_bf16 v[8:11], a[164:167], v[116:119], v[8:11]\n"
"v_exp_f32 v5, v5\n"
"v_mfma_f32_16x16x32_bf16 v[8:11], a[168:171], v[120:123], v[8:11]\n"
"v_cvt_pk_bf16_f32 v18, v4, v5\n"
"v_mfma_f32_16x16x32_bf16 v[8:11], a[172:175], v[124:127], v[8:11]\n"
"v_exp_f32 v6, v6\n"
"v_mfma_f32_16x16x32_bf16 v[12:15], a[176:179], v[112:115], v[36:39]\n"
"s_nop 0\n"
"v_mfma_f32_16x16x32_bf16 v[12:15], a[180:183], v[116:119], v[12:15]\n"
"v_exp_f32 v7, v7\n"
"v_mfma_f32_16x16x32_bf16 v[12:15], a[184:187], v[120:123], v[12:15]\n"
"v_cvt_pk_bf16_f32 v19, v6, v7\n"
"v_mfma_f32_16x16x32_bf16 v[12:15], a[188:191], v[124:127], v[12:15]\n"
"s_nop 0\n"
"v_mfma_f32_16x16x32_bf16 a[64:67], a[224:227], v[16:19], a[64:67]\n"
"v_exp_f32 v8, v8\n"
"v_mfma_f32_16x16x32_bf16 a[68:71], a[228:231], v[16:19], a[68:71]\n"
"s_nop 0\n"
"v_mfma_f32_16x16x32_bf16 a[72:75], a[232:235], v[16:19], a[72:75]\n"
"v_exp_f32 v9, v9\n"
"v_mfma_f32_16x16x32_bf16 a[76:79], a[236:239], v[16:19], a[76:79]\n"
"v_cvt_pk_bf16_f32 v20, v8, v9\n"
"v_mfma_f32_16x16x32_bf16 a[80:83], a[240:243], v[16:19], a[80:83]\n"
"v_exp_f32 v10, v10\n"
"v_mfma_f32_16x16x32_bf16 a[84:87], a[244:247], v[16:19], a[84:87]\n"
"s_nop 0\n"
"v_mfma_f32_16x16x32_bf16 a[88:91], a[248:251], v[16:19], a[88:91]\n"
"v_exp_f32 v11, v11\n"
"v_mfma_f32_16x16x32_bf16 a[92:95], a[252:255], v[16:19], a[92:95]\n"
"v_cvt_pk_bf16_f32 v21, v10, v11\n"
"v_mfma_f32_16x16x32_bf16 v[52:55], v[40:43], v[16:19], v[52:55]\n"
"v_exp_f32 v12, v12\n"
"s_nop 0\n"
"v_exp_f32 v13, v13\n"
"s_nop 0\n"
"v_cvt_pk_bf16_f32 v22, v12, v13\n"
"s_nop 0\n"
"v_exp_f32 v14, v14\n"
"s_nop 0\n"
"s_nop 0\n"
"v_exp_f32 v15, v15\n"
"s_nop 0\n"
"v_cvt_pk_bf16_f32 v23, v14, v15\n"
"s_nop 0\n"
"s_nop 0\n"
"s_nop 1\n"
"v_mfma_f32_16x16x32_bf16 a[96:99], a[224:227], v[20:23], a[96:99]\n"
"v_mfma_f32_16x16x32_bf16 a[100:103], a[228:231], v[20:23], a[100:103]\n"
"v_mfma_f32_16x16x32_bf16 a[104:107], a[232:235], v[20:23], a[104:107]\n"
"v_mfma_f32_16x16x32_bf16 a[108:111], a[236:239], v[20:23], a[108:111]\n"
"v_mfma_f32_16x16x32_bf16 a[112:115], a[240:243], v[20:23], a[112:115]\n"
"v_mfma_f32_16x16x32_bf16 a[116:119], a[244:247], v[20:23], a[116:119]\n"
"v_mfma_f32_16x16x32_bf16 a[120:123], a[248:251], v[20:23], a[120:123]\n"
"v_mfma_f32_16x16x32_bf16 a[124:127], a[252:255], v[20:23], a[124:127]\n"
"v_mfma_f32_16x16x32_bf16 v[56:59], v[40:43], v[20:23], v[56:59]\n"
"s_nop 7\n"
"v_mov_b32 v219, 0x5f800000\n"
"v_cmp_nle_f32 s[56:57], v44, v219\n"
"v_cmp_nle_f32 s[58:59], v48, v219\n"
"v_cmp_nle_f32 s[60:61], v52, v219\n"
"v_cmp_nle_f32 s[62:63], v56, v219\n"
"v_rcp_f32 v218, v44\n"
"v_rcp_f32 v220, v48\n"
"v_rcp_f32 v222, v52\n"
"v_rcp_f32 v224, v56\n"
"s_or_b64 s[56:57], s[56:57], s[58:59]\n"
"s_or_b64 s[60:61], s[60:61], s[62:63]\n"
"s_or_b64 s[56:57], s[56:57], s[60:61]\n"
"s_cselect_b32 s56, 1, 0\n"
"s_cmp_eq_u32 s45, 0\n"
"s_cselect_b32 s56, s56, 0\n"
"s_lshl_b32 s57, s41, 2\n"
"s_add_u32 s57, s57, s40\n"
"s_add_u32 s57, s57, 98304\n"
"v_mov_b32 v238, s56\n"
"v_mov_b32 v239, s57\n"
"ds_write_b32 v239, v238\n"
"v_accvgpr_read_b32 v226, a0\n"
"v_accvgpr_read_b32 v227, a1\n"
"v_accvgpr_read_b32 v228, a2\n"
"v_accvgpr_read_b32 v229, a3\n"
"v_pk_mul_f32 v[226:227], v[226:227], v[218:219] op_sel_hi:[1,0]\n"
"v_pk_mul_f32 v[228:229], v[228:229], v[218:219] op_sel_hi:[1,0]\n"
"v_cvt_pk_bf16_f32 v0, v226, v227\n"
"v_cvt_pk_bf16_f32 v1, v228, v229\n"
"v_accvgpr_read_b32 v230, a4\n"
"v_accvgpr_read_b32 v231, a5\n"
"v_accvgpr_read_b32 v232, a6\n"
"v_accvgpr_read_b32 v233, a7\n"
"v_pk_mul_f32 v[230:231], v[230:231], v[218:219] op_sel_hi:[1,0]\n"
"v_pk_mul_f32 v[232:233], v[232:233], v[218:219] op_sel_hi:[1,0]\n"
"v_cvt_pk_bf16_f32 v2, v230, v231\n"
"v_cvt_pk_bf16_f32 v3, v232, v233\n"
"v_accvgpr_read_b32 v234, a8\n"
"v_accvgpr_read_b32 v235, a9\n"
"v_accvgpr_read_b32 v236, a10\n"
"v_accvgpr_read_b32 v237, a11\n"
"v_pk_mul_f32 v[234:235], v[234:235], v[218:219] op_sel_hi:[1,0]\n"
"v_pk_mul_f32 v[236:237], v[236:237], v[218:219] op_sel_hi:[1,0]\n"
"v_cvt_pk_bf16_f32 v4, v234, v235\n"
"v_cvt_pk_bf16_f32 v5, v236, v237\n"
"v_accvgpr_read_b32 v226, a12\n"
"v_accvgpr_read_b32 v227, a13\n"
"v_accvgpr_read_b32 v228, a14\n"
"v_accvgpr_read_b32 v229, a15\n"
"v_pk_mul_f32 v[226:227], v[226:227], v[218:219] op_sel_hi:[1,0]\n"
"v_pk_mul_f32 v[228:229], v[228:229], v[218:219] op_sel_hi:[1,0]\n"
"v_cvt_pk_bf16_f32 v6, v226, v227\n"
"v_cvt_pk_bf16_f32 v7, v228, v229\n"
"v_accvgpr_read_b32 v230, a16\n"
"v_accvgpr_read_b32 v231, a17\n"
"v_accvgpr_read_b32 v232, a18\n"
"v_accvgpr_read_b32 v233, a19\n"
"v_pk_mul_f32 v[230:231], v[230:231], v[218:219] op_sel_hi:[1,0]\n"
"v_pk_mul_f32 v[232:233], v[232:233], v[218:219] op_sel_hi:[1,0]\n"
"v_cvt_pk_bf16_f32 v8, v230, v231\n"
"v_cvt_pk_bf16_f32 v9, v232, v233\n"
"v_accvgpr_read_b32 v234, a20\n"
"v_accvgpr_read_b32 v235, a21\n"
"v_accvgpr_read_b32 v236, a22\n"
"v_accvgpr_read_b32 v237, a23\n"
"v_pk_mul_f32 v[234:235], v[234:235], v[218:219] op_sel_hi:[1,0]\n"
"v_pk_mul_f32 v[236:237], v[236:237], v[218:219] op_sel_hi:[1,0]\n"
"v_cvt_pk_bf16_f32 v10, v234, v235\n"
"v_cvt_pk_bf16_f32 v11, v236, v237\n"
"v_accvgpr_read_b32 v226, a24\n"
"v_accvgpr_read_b32 v227, a25\n"
"v_accvgpr_read_b32 v228, a26\n"
"v_accvgpr_read_b32 v229, a27\n"
"v_pk_mul_f32 v[226:227], v[226:227], v[218:219] op_sel_hi:[1,0]\n"
"v_pk_mul_f32 v[228:229], v[228:229], v[218:219] op_sel_hi:[1,0]\n"
"v_cvt_pk_bf16_f32 v12, v226, v227\n"
"v_cvt_pk_bf16_f32 v13, v228, v229\n"
"v_accvgpr_read_b32 v230, a28\n"
"v_accvgpr_read_b32 v231, a29\n"
"v_accvgpr_read_b32 v232, a30\n"
"v_accvgpr_read_b32 v233, a31\n"
"v_pk_mul_f32 v[230:231], v[230:231], v[218:219] op_sel_hi:[1,0]\n"
"v_pk_mul_f32 v[232:233], v[232:233], v[218:219] op_sel_hi:[1,0]\n"
"v_cvt_pk_bf16_f32 v14, v230, v231\n"
"v_cvt_pk_bf16_f32 v15, v232, v233\n"
"v_accvgpr_read_b32 v234, a32\n"
"v_accvgpr_read_b32 v235, a33\n"
"v_accvgpr_read_b32 v236, a34\n"
"v_accvgpr_read_b32 v237, a35\n"
"v_pk_mul_f32 v[234:235], v[234:235], v[220:221] op_sel_hi:[1,0]\n"
"v_pk_mul_f32 v[236:237], v[236:237], v[220:221] op_sel_hi:[1,0]\n"
"v_cvt_pk_bf16_f32 v16, v234, v235\n"
"v_cvt_pk_bf16_f32 v17, v236, v237\n"
"v_accvgpr_read_b32 v226, a36\n"
"v_accvgpr_read_b32 v227, a37\n"
"v_accvgpr_read_b32 v228, a38\n"
"v_accvgpr_read_b32 v229, a39\n"
"v_pk_mul_f32 v[226:227], v[226:227], v[220:221] op_sel_hi:[1,0]\n"
"v_pk_mul_f32 v[228:229], v[228:229], v[220:221] op_sel_hi:[1,0]\n"
"v_cvt_pk_bf16_f32 v18, v226, v227\n"
"v_cvt_pk_bf16_f32 v19, v228, v229\n"
"v_accvgpr_read_b32 v230, a40\n"
"v_accvgpr_read_b32 v231, a41\n"
"v_accvgpr_read_b32 v232, a42\n"
"v_accvgpr_read_b32 v233, a43\n"
"v_pk_mul_f32 v[230:231], v[230:231], v[220:221] op_sel_hi:[1,0]\n"
"v_pk_mul_f32 v[232:233], v[232:233], v[220:221] op_sel_hi:[1,0]\n"
"v_cvt_pk_bf16_f32 v20, v230, v231\n"
"v_cvt_pk_bf16_f32 v21, v232, v233\n"
"v_accvgpr_read_b32 v234, a44\n"
"v_accvgpr_read_b32 v235, a45\n"
"v_accvgpr_read_b32 v236, a46\n"
"v_accvgpr_read_b32 v237, a47\n"
"v_pk_mul_f32 v[234:235], v[234:235], v[220:221] op_sel_hi:[1,0]\n"
"v_pk_mul_f32 v[236:237], v[236:237], v[220:221] op_sel_hi:[1,0]\n"
"v_cvt_pk_bf16_f32 v22, v234, v235\n"
"v_cvt_pk_bf16_f32 v23, v236, v237\n"
"v_accvgpr_read_b32 v226, a48\n"
"v_accvgpr_read_b32 v227, a49\n"
"v_accvgpr_read_b32 v228, a50\n"
"v_accvgpr_read_b32 v229, a51\n"
"v_pk_mul_f32 v[226:227], v[226:227], v[220:221] op_sel_hi:[1,0]\n"
"v_pk_mul_f32 v[228:229], v[228:229], v[220:221] op_sel_hi:[1,0]\n"
"v_cvt_pk_bf16_f32 v24, v226, v227\n"
"v_cvt_pk_bf16_f32 v25, v228, v229\n"
"v_accvgpr_read_b32 v230, a52\n"
"v_accvgpr_read_b32 v231, a53\n"
"v_accvgpr_read_b32 v232, a54\n"
"v_accvgpr_read_b32 v233, a55\n"
"v_pk_mul_f32 v[230:231], v[230:231], v[220:221] op_sel_hi:[1,0]\n"
"v_pk_mul_f32 v[232:233], v[232:233], v[220:221] op_sel_hi:[1,0]\n"
"v_cvt_pk_bf16_f32 v26, v230, v231\n"
"v_cvt_pk_bf16_f32 v27, v232, v233\n"
"v_accvgpr_read_b32 v234, a56\n"
"v_accvgpr_read_b32 v235, a57\n"
"v_accvgpr_read_b32 v236, a58\n"
"v_accvgpr_read_b32 v237, a59\n"
"v_pk_mul_f32 v[234:235], v[234:235], v[220:221] op_sel_hi:[1,0]\n"
"v_pk_mul_f32 v[236:237], v[236:237], v[220:221] op_sel_hi:[1,0]\n"
"v_cvt_pk_bf16_f32 v28, v234, v235\n"
"v_cvt_pk_bf16_f32 v29, v236, v237\n"
"v_accvgpr_read_b32 v226, a60\n"
"v_accvgpr_read_b32 v227, a61\n"
"v_accvgpr_read_b32 v228, a62\n"
"v_accvgpr_read_b32 v229, a63\n"
"v_pk_mul_f32 v[226:227], v[226:227], v[220:221] op_sel_hi:[1,0]\n"
"v_pk_mul_f32 v[228:229], v[228:229], v[220:221] op_sel_hi:[1,0]\n"
"v_cvt_pk_bf16_f32 v30, v226, v227\n"
"v_cvt_pk_bf16_f32 v31, v228, v229\n"
"s_waitcnt lgkmcnt(0)\n"
"s_barrier\n"
"ds_read_b32 v238, v209\n"
"v_accvgpr_read_b32 v230, a64\n"
"v_accvgpr_read_b32 v231, a65\n"
"v_accvgpr_read_b32 v232, a66\n"
"v_accvgpr_read_b32 v233, a67\n"
"v_pk_mul_f32 v[230:231], v[230:231], v[222:223] op_sel_hi:[1,0]\n"
"v_pk_mul_f32 v[232:233], v[232:233], v[222:223] op_sel_hi:[1,0]\n"
"v_cvt_pk_bf16_f32 v32, v230, v231\n"
"v_cvt_pk_bf16_f32 v33, v232, v233\n"
"v_accvgpr_read_b32 v234, a68\n"
"v_accvgpr_read_b32 v235, a69\n"
"v_accvgpr_read_b32 v236, a70\n"
"v_accvgpr_read_b32 v237, a71\n"
"v_pk_mul_f32 v[234:235], v[234:235], v[222:223] op_sel_hi:[1,0]\n"
"v_pk_mul_f32 v[236:237], v[236:237], v[222:223] op_sel_hi:[1,0]\n"
"v_cvt_pk_bf16_f32 v34, v234, v235\n"
"v_cvt_pk_bf16_f32 v35, v236, v237\n"
"v_accvgpr_read_b32 v226, a72\n"
"v_accvgpr_read_b32 v227, a73\n"
"v_accvgpr_read_b32 v228, a74\n"
"v_accvgpr_read_b32 v229, a75\n"
"v_pk_mul_f32 v[226:227], v[226:227], v[222:223] op_sel_hi:[1,0]\n"
"v_pk_mul_f32 v[228:229], v[228:229], v[222:223] op_sel_hi:[1,0]\n"
"v_cvt_pk_bf16_f32 v36, v226, v227\n"
"v_cvt_pk_bf16_f32 v37, v228, v229\n"
"v_accvgpr_read_b32 v230, a76\n"
"v_accvgpr_read_b32 v231, a77\n"
"v_accvgpr_read_b32 v232, a78\n"
"v_accvgpr_read_b32 v233, a79\n"
"v_pk_mul_f32 v[230:231], v[230:231], v[222:223] op_sel_hi:[1,0]\n"
"v_pk_mul_f32 v[232:233], v[232:233], v[222:223] op_sel_hi:[1,0]\n"
"v_cvt_pk_bf16_f32 v38, v230, v231\n"
"v_cvt_pk_bf16_f32 v39, v232, v233\n"
"v_accvgpr_read_b32 v234, a80\n"
"v_accvgpr_read_b32 v235, a81\n"
"v_accvgpr_read_b32 v236, a82\n"
"v_accvgpr_read_b32 v237, a83\n"
"v_pk_mul_f32 v[234:235], v[234:235], v[222:223] op_sel_hi:[1,0]\n"
"v_pk_mul_f32 v[236:237], v[236:237], v[222:223] op_sel_hi:[1,0]\n"
"v_cvt_pk_bf16_f32 v40, v234, v235\n"
"v_cvt_pk_bf16_f32 v41, v236, v237\n"
"v_accvgpr_read_b32 v226, a84\n"
"v_accvgpr_read_b32 v227, a85\n"
"v_accvgpr_read_b32 v228, a86\n"
"v_accvgpr_read_b32 v229, a87\n"
"v_pk_mul_f32 v[226:227], v[226:227], v[222:223] op_sel_hi:[1,0]\n"
"v_pk_mul_f32 v[228:229], v[228:229], v[222:223] op_sel_hi:[1,0]\n"
"v_cvt_pk_bf16_f32 v42, v226, v227\n"
"v_cvt_pk_bf16_f32 v43, v228, v229\n"
"v_accvgpr_read_b32 v230, a88\n"
"v_accvgpr_read_b32 v231, a89\n"
"v_accvgpr_read_b32 v232, a90\n"
"v_accvgpr_read_b32 v233, a91\n"
"v_pk_mul_f32 v[230:231], v[230:231], v[222:223] op_sel_hi:[1,0]\n"
"v_pk_mul_f32 v[232:233], v[232:233], v[222:223] op_sel_hi:[1,0]\n"
"v_cvt_pk_bf16_f32 v44, v230, v231\n"
"v_cvt_pk_bf16_f32 v45, v232, v233\n"
"v_accvgpr_read_b32 v234, a92\n"
"v_accvgpr_read_b32 v235, a93\n"
"v_accvgpr_read_b32 v236, a94\n"
"v_accvgpr_read_b32 v237, a95\n"
"v_pk_mul_f32 v[234:235], v[234:235], v[222:223] op_sel_hi:[1,0]\n"
"v_pk_mul_f32 v[236:237], v[236:237], v[222:223] op_sel_hi:[1,0]\n"
"v_cvt_pk_bf16_f32 v46, v234, v235\n"
"v_cvt_pk_bf16_f32 v47, v236, v237\n"
"v_accvgpr_read_b32 v226, a96\n"
"v_accvgpr_read_b32 v227, a97\n"
"v_accvgpr_read_b32 v228, a98\n"
"v_accvgpr_read_b32 v229, a99\n"
"v_pk_mul_f32 v[226:227], v[226:227], v[224:225] op_sel_hi:[1,0]\n"
"v_pk_mul_f32 v[228:229], v[228:229], v[224:225] op_sel_hi:[1,0]\n"
"v_cvt_pk_bf16_f32 v48, v226, v227\n"
"v_cvt_pk_bf16_f32 v49, v228, v229\n"
"v_accvgpr_read_b32 v230, a100\n"
"v_accvgpr_read_b32 v231, a101\n"
"v_accvgpr_read_b32 v232, a102\n"
"v_accvgpr_read_b32 v233, a103\n"
"v_pk_mul_f32 v[230:231], v[230:231], v[224:225] op_sel_hi:[1,0]\n"
"v_pk_mul_f32 v[232:233], v[232:233], v[224:225] op_sel_hi:[1,0]\n"
"v_cvt_pk_bf16_f32 v50, v230, v231\n"
"v_cvt_pk_bf16_f32 v51, v232, v233\n"
"v_accvgpr_read_b32 v234, a104\n"
"v_accvgpr_read_b32 v235, a105\n"
"v_accvgpr_read_b32 v236, a106\n"
"v_accvgpr_read_b32 v237, a107\n"
"v_pk_mul_f32 v[234:235], v[234:235], v[224:225] op_sel_hi:[1,0]\n"
"v_pk_mul_f32 v[236:237], v[236:237], v[224:225] op_sel_hi:[1,0]\n"
"v_cvt_pk_bf16_f32 v52, v234, v235\n"
"v_cvt_pk_bf16_f32 v53, v236, v237\n"
"v_accvgpr_read_b32 v226, a108\n"
"v_accvgpr_read_b32 v227, a109\n"
"v_accvgpr_read_b32 v228, a110\n"
"v_accvgpr_read_b32 v229, a111\n"
"v_pk_mul_f32 v[226:227], v[226:227], v[224:225] op_sel_hi:[1,0]\n"
"v_pk_mul_f32 v[228:229], v[228:229], v[224:225] op_sel_hi:[1,0]\n"
"v_cvt_pk_bf16_f32 v54, v226, v227\n"
"v_cvt_pk_bf16_f32 v55, v228, v229\n"
"v_accvgpr_read_b32 v230, a112\n"
"v_accvgpr_read_b32 v231, a113\n"
"v_accvgpr_read_b32 v232, a114\n"
"v_accvgpr_read_b32 v233, a115\n"
"v_pk_mul_f32 v[230:231], v[230:231], v[224:225] op_sel_hi:[1,0]\n"
"v_pk_mul_f32 v[232:233], v[232:233], v[224:225] op_sel_hi:[1,0]\n"
"v_cvt_pk_bf16_f32 v56, v230, v231\n"
"v_cvt_pk_bf16_f32 v57, v232, v233\n"
"v_accvgpr_read_b32 v234, a116\n"
"v_accvgpr_read_b32 v235, a117\n"
"v_accvgpr_read_b32 v236, a118\n"
"v_accvgpr_read_b32 v237, a119\n"
"v_pk_mul_f32 v[234:235], v[234:235], v[224:225] op_sel_hi:[1,0]\n"
"v_pk_mul_f32 v[236:237], v[236:237], v[224:225] op_sel_hi:[1,0]\n"
"v_cvt_pk_bf16_f32 v58, v234, v235\n"
"v_cvt_pk_bf16_f32 v59, v236, v237\n"
"v_accvgpr_read_b32 v226, a120\n"
"v_accvgpr_read_b32 v227, a121\n"
"v_accvgpr_read_b32 v228, a122\n"
"v_accvgpr_read_b32 v229, a123\n"
"v_pk_mul_f32 v[226:227], v[226:227], v[224:225] op_sel_hi:[1,0]\n"
"v_pk_mul_f32 v[228:229], v[228:229], v[224:225] op_sel_hi:[1,0]\n"
"v_cvt_pk_bf16_f32 v60, v226, v227\n"
"v_cvt_pk_bf16_f32 v61, v228, v229\n"
"v_accvgpr_read_b32 v230, a124\n"
"v_accvgpr_read_b32 v231, a125\n"
"v_accvgpr_read_b32 v232, a126\n"
"v_accvgpr_read_b32 v233, a127\n"
"v_pk_mul_f32 v[230:231], v[230:231], v[224:225] op_sel_hi:[1,0]\n"
"v_pk_mul_f32 v[232:233], v[232:233], v[224:225] op_sel_hi:[1,0]\n"
"v_cvt_pk_bf16_f32 v62, v230, v231\n"
"v_cvt_pk_bf16_f32 v63, v232, v233\n"
"s_waitcnt lgkmcnt(0)\n"
"v_cmp_ne_u32 vcc, 0, v238\n"
"s_nop 0\n"
"s_cbranch_vccnz AT_fb\n"
"v_mfma_f32_16x16x32_bf16 v[64:67], a[128:131], v[0:3], 0\n"
"ds_read_b64_tr_b16 a[208:209], v205 offset:16384\n"
"v_mfma_f32_16x16x32_bf16 v[64:67], a[132:135], v[4:7], v[64:67]\n"
"ds_read_b64_tr_b16 a[210:211], v205 offset:18432\n"
"v_mfma_f32_16x16x32_bf16 v[64:67], a[136:139], v[8:11], v[64:67]\n"
"ds_read_b64_tr_b16 a[212:213], v205 offset:20480\n"
"v_mfma_f32_16x16x32_bf16 v[64:67], a[140:143], v[12:15], v[64:67]\n"
"ds_read_b64_tr_b16 a[214:215], v205 offset:22528\n"
"v_mfma_f32_16x16x32_bf16 v[64:67], a[144:147], v[16:19], v[64:67]\n"
"s_waitcnt vmcnt(34)\n"
"s_barrier\n"
"ds_read_b64_tr_b16 a[216:217], v205 offset:24576\n"
"v_mfma_f32_16x16x32_bf16 v[64:67], a[148:151], v[20:23], v[64:67]\n"
"s_add_u32 m0, s53, 98304\n"
"ds_read_b64_tr_b16 a[218:219], v205 offset:26624\n"
"v_mfma_f32_16x16x32_bf16 v[64:67], a[152:155], v[24:27], v[64:67]\n"
"ds_read_b64_tr_b16 a[220:221], v205 offset:28672\n"
"buffer_load_dwordx4 v206, s[24:27], s52 offen offset:0 lds\n"
"v_mfma_f32_16x16x32_bf16 v[64:67], a[156:159], v[28:31], v[64:67]\n"
"s_add_u32 s52, s52, 8192\n"
"ds_read_b64_tr_b16 a[222:223], v205 offset:30720\n"
"v_mfma_f32_16x16x32_bf16 v[68:71], a[128:131], v[32:35], 0\n"
"s_lshl_b32 s55, s44, 4\n"
"ds_read_b64_tr_b16 a[160:161], v202 offset:256\n"
"v_mfma_f32_16x16x32_bf16 v[68:71], a[132:135], v[36:39], v[68:71]\n"
"s_add_u32 s55, s55, s43\n"
"ds_read_b64_tr_b16 a[162:163], v202 offset:2304\n"
"v_mfma_f32_16x16x32_bf16 v[68:71], a[136:139], v[40:43], v[68:71]\n"
"s_mul_i32 s46, s55, 9216\n"
"ds_read_b64_tr_b16 a[164:165], v202 offset:4352\n"
"buffer_load_dwordx4 v206, s[24:27], s52 offen offset:1024 lds\n"
"v_mfma_f32_16x16x32_bf16 v[68:71], a[140:143], v[44:47], v[68:71]\n"
"s_add_u32 s52, s52, 8192\n"
"ds_read_b64_tr_b16 a[166:167], v202 offset:6400\n"
"v_mfma_f32_16x16x32_bf16 v[68:71], a[144:147], v[48:51], v[68:71]\n"
"s_lshr_b32 s47, s55, 1\n"
"ds_read_b64_tr_b16 a[168:169], v202 offset:8448\n"
"v_cvt_pk_bf16_f32 v96, v64, v65\n"
"v_mfma_f32_16x16x32_bf16 v[68:71], a[148:151], v[52:55], v[68:71]\n"
"s_mul_i32 s47, s47, 18432\n"
"ds_read_b64_tr_b16 a[170:171], v202 offset:10496\n"
"v_cvt_pk_bf16_f32 v97, v66, v67\n"
"v_mfma_f32_16x16x32_bf16 v[68:71], a[152:155], v[56:59], v[68:71]\n"
"s_and_b32 s62, s55, 1\n"
"ds_read_b64_tr_b16 a[172:173], v202 offset:12544\n"
"buffer_load_dwordx4 v206, s[24:27], s52 offen offset:2048 lds\n"
"v_mfma_f32_16x16x32_bf16 v[68:71], a[156:159], v[60:63], v[68:71]\n"
"s_add_u32 s52, s52, 8192\n"
"ds_read_b64_tr_b16 a[174:175], v202 offset:14592\n"
"v_mfma_f32_16x16x32_bf16 v[72:75], a[192:195], v[0:3], 0\n"
"s_lshl_b32 s62, s62, 7\n"
"ds_read_b64_tr_b16 a[176:177], v202 offset:16640\n"
"v_mfma_f32_16x16x32_bf16 v[72:75], a[196:199], v[4:7], v[72:75]\n"
"s_add_u32 s47, s47, s62\n"
"ds_read_b64_tr_b16 a[178:179], v202 offset:18688\n"
"v_mfma_f32_16x16x32_bf16 v[72:75], a[200:203], v[8:11], v[72:75]\n"
"s_add_u32 s47, s47, 139264\n"
"ds_read_b64_tr_b16 a[180:181], v202 offset:20736\n"
"buffer_load_dwordx4 v206, s[24:27], s52 offen offset:3072 lds\n"
"v_mfma_f32_16x16x32_bf16 v[72:75], a[204:207], v[12:15], v[72:75]\n"
"s_waitcnt lgkmcnt(11)\n"
"s_add_u32 s52, s52, 12288\n"
"ds_read_b64_tr_b16 a[182:183], v202 offset:22784\n"
"v_mfma_f32_16x16x32_bf16 v[72:75], a[208:211], v[16:19], v[72:75]\n"
"s_mul_i32 s48, s55, 73728\n"
"ds_read_b64_tr_b16 a[184:185], v202 offset:24832\n"
"v_cvt_pk_bf16_f32 v100, v68, v69\n"
"v_mfma_f32_16x16x32_bf16 v[72:75], a[212:215], v[20:23], v[72:75]\n"
"s_add_u32 m0, s53, 102400\n"
"ds_read_b64_tr_b16 a[186:187], v202 offset:26880\n"
"v_cvt_pk_bf16_f32 v101, v70, v71\n"
"v_mfma_f32_16x16x32_bf16 v[72:75], a[216:219], v[24:27], v[72:75]\n"
"s_add_u32 s48, s48, 37888000\n"
"ds_read_b64_tr_b16 a[188:189], v202 offset:28928\n"
"buffer_load_dwordx4 v206, s[24:27], s52 offen offset:0 lds\n"
"v_mfma_f32_16x16x32_bf16 v[72:75], a[220:223], v[28:31], v[72:75]\n"
"s_add_u32 s52, s52, 8192\n"
"ds_read_b64_tr_b16 a[190:191], v202 offset:30976\n"
"v_mfma_f32_16x16x32_bf16 v[76:79], a[192:195], v[32:35], 0\n"
"s_add_u32 s62, s40, 98304\n"
"ds_read_b64_tr_b16 a[224:225], v205 offset:256\n"
"v_mfma_f32_16x16x32_bf16 v[76:79], a[196:199], v[36:39], v[76:79]\n"
"s_mov_b32 s56, 41984\n"
"ds_read_b64_tr_b16 a[226:227], v205 offset:2304\n"
"v_mfma_f32_16x16x32_bf16 v[76:79], a[200:203], v[40:43], v[76:79]\n"
"s_mov_b32 s57, 46592\n"
"ds_read_b64_tr_b16 a[228:229], v205 offset:4352\n"
"buffer_load_dwordx4 v206, s[24:27], s52 offen offset:1024 lds\n"
"v_mfma_f32_16x16x32_bf16 v[76:79], a[204:207], v[44:47], v[76:79]\n"
"s_add_u32 s52, s52, 8192\n"
"ds_read_b64_tr_b16 a[230:231], v205 offset:6400\n"
"v_mfma_f32_16x16x32_bf16 v[76:79], a[208:211], v[48:51], v[76:79]\n"
"s_mov_b32 s58, 51200\n"
"ds_read_b64_tr_b16 a[232:233], v205 offset:8448\n"
"v_cvt_pk_bf16_f32 v98, v72, v73\n"
"v_mfma_f32_16x16x32_bf16 v[76:79], a[212:215], v[52:55], v[76:79]\n"
"s_mov_b32 s59, 55808\n"
"ds_read_b64_tr_b16 a[234:235], v205 offset:10496\n"
"v_cvt_pk_bf16_f32 v99, v74, v75\n"
"v_mfma_f32_16x16x32_bf16 v[76:79], a[216:219], v[56:59], v[76:79]\n"
"ds_read_b64_tr_b16 a[236:237], v205 offset:12544\n"
"buffer_load_dwordx4 v206, s[24:27], s52 offen offset:2048 lds\n"
"v_mfma_f32_16x16x32_bf16 v[76:79], a[220:223], v[60:63], v[76:79]\n"
"s_waitcnt lgkmcnt(11)\n"
"s_add_u32 s52, s52, 8192\n"
"ds_read_b64_tr_b16 a[238:239], v205 offset:14592\n"
"buffer_store_dwordx4 v[96:99], v207, s[28:31], s54 offen offset:0 sc1\n"
"v_mfma_f32_16x16x32_bf16 v[80:83], a[160:163], v[0:3], 0\n"
"ds_read_b64_tr_b16 a[240:241], v205 offset:16640\n"
"v_and_b32 v249, 7, v248\n"
"v_mfma_f32_16x16x32_bf16 v[80:83], a[164:167], v[4:7], v[80:83]\n"
"ds_read_b64_tr_b16 a[242:243], v205 offset:18688\n"
"v_lshrrev_b32 v255, 5, v253\n"
"v_mfma_f32_16x16x32_bf16 v[80:83], a[168:171], v[8:11], v[80:83]\n"
"ds_read_b64_tr_b16 a[244:245], v205 offset:20736\n"
"buffer_load_dwordx4 v206, s[24:27], s52 offen offset:3072 lds\n"
"v_mfma_f32_16x16x32_bf16 v[80:83], a[172:175], v[12:15], v[80:83]\n"
"s_sub_u32 s52, s52, 61312\n"
"ds_read_b64_tr_b16 a[246:247], v205 offset:22784\n"
"v_bfe_u32 v246, v253, 3, 2\n"
"v_mfma_f32_16x16x32_bf16 v[80:83], a[176:179], v[16:19], v[80:83]\n"
"ds_read_b64_tr_b16 a[248:249], v205 offset:24832\n"
"v_cvt_pk_bf16_f32 v102, v76, v77\n"
"v_mfma_f32_16x16x32_bf16 v[80:83], a[180:183], v[20:23], v[80:83]\n"
"s_waitcnt lgkmcnt(11)\n"
"ds_read_b64_tr_b16 a[250:251], v205 offset:26880\n"
"v_cvt_pk_bf16_f32 v103, v78, v79\n"
"v_mfma_f32_16x16x32_bf16 v[80:83], a[184:187], v[24:27], v[80:83]\n"
"ds_read_b64_tr_b16 a[252:253], v205 offset:28928\n"
"v_lshlrev_b32 v241, 10, v255\n"
"v_mfma_f32_16x16x32_bf16 v[80:83], a[188:191], v[28:31], v[80:83]\n"
"ds_read_b64_tr_b16 a[254:255], v205 offset:30976\n"
"buffer_store_dwordx4 v[100:103], v208, s[28:31], s54 offen offset:0 sc1\n"
"v_mfma_f32_16x16x32_bf16 v[84:87], a[160:163], v[32:35], 0\n"
"ds_read_b64_tr_b16 a[128:129], v200 offset:0\n"
"v_lshl_add_u32 v241, v246, 8, v241\n"
"v_mfma_f32_16x16x32_bf16 v[84:87], a[164:167], v[36:39], v[84:87]\n"
"ds_read_b64_tr_b16 a[130:131], v200 offset:2048\n"
"v_lshl_add_u32 v241, v249, 4, v241\n"
"v_mfma_f32_16x16x32_bf16 v[84:87], a[168:171], v[40:43], v[84:87]\n"
"ds_read_b64_tr_b16 a[132:133], v200 offset:4096\n"
"v_add_u32 v242, 6144, v241\n"
"v_mfma_f32_16x16x32_bf16 v[84:87], a[172:175], v[44:47], v[84:87]\n"
"ds_read_b64_tr_b16 a[134:135], v200 offset:6144\n"
"v_add_u32 v243, 12288, v241\n"
"v_mfma_f32_16x16x32_bf16 v[84:87], a[176:179], v[48:51], v[84:87]\n"
"ds_read_b64_tr_b16 a[136:137], v200 offset:8192\n"
"v_cvt_pk_bf16_f32 v104, v80, v81\n"
"v_mfma_f32_16x16x32_bf16 v[84:87], a[180:183], v[52:55], v[84:87]\n"
"ds_read_b64_tr_b16 a[138:139], v200 offset:10240\n"
"v_cvt_pk_bf16_f32 v105, v82, v83\n"
"v_mfma_f32_16x16x32_bf16 v[84:87], a[184:187], v[56:59], v[84:87]\n"
"ds_read_b64_tr_b16 a[140:141], v200 offset:12288\n"
"v_lshlrev_b32 v255, 5, v255\n"
"v_mfma_f32_16x16x32_bf16 v[84:87], a[188:191], v[60:63], v[84:87]\n"
"ds_read_b64_tr_b16 a[142:143], v200 offset:14336\n"
"v_lshl_add_u32 v255, v246, 2, v255\n"
"v_mfma_f32_16x16x32_bf16 v[88:91], a[224:227], v[0:3], 0\n"
"s_waitcnt lgkmcnt(11)\n"
"ds_read_b64_tr_b16 a[144:145], v200 offset:16384\n"
"v_lshl_add_u32 v244, v255, 2, s62\n"
"v_mfma_f32_16x16x32_bf16 v[88:91], a[228:231], v[4:7], v[88:91]\n"
"ds_read_b64_tr_b16 a[146:147], v200 offset:18432\n"
"v_mul_u32_u24 v249, 1152, v249\n"
"v_mfma_f32_16x16x32_bf16 v[88:91], a[232:235], v[8:11], v[88:91]\n"
"ds_read_b64_tr_b16 a[148:149], v200 offset:20480\n"
"v_lshl_add_u32 v240, v255, 1, v249\n"
"v_mfma_f32_16x16x32_bf16 v[88:91], a[236:239], v[12:15], v[88:91]\n"
"ds_read_b64_tr_b16 a[150:151], v200 offset:22528\n"
"v_add_u32 v245, v249, v255\n"
"v_mfma_f32_16x16x32_bf16 v[88:91], a[240:243], v[16:19], v[88:91]\n"
"ds_read_b64_tr_b16 a[152:153], v200 offset:24576\n"
"v_cvt_pk_bf16_f32 v108, v84, v85\n"
"v_mfma_f32_16x16x32_bf16 v[88:91], a[244:247], v[20:23], v[88:91]\n"
"s_waitcnt lgkmcnt(11)\n"
"ds_read_b64_tr_b16 a[154:155], v200 offset:26624\n"
"v_cvt_pk_bf16_f32 v109, v86, v87\n"
"v_mfma_f32_16x16x32_bf16 v[88:91], a[248:251], v[24:27], v[88:91]\n"
"ds_read_b64_tr_b16 a[156:157], v200 offset:28672\n"
"v_lshlrev_b32 v245, 3, v245\n"
"v_mfma_f32_16x16x32_bf16 v[88:91], a[252:255], v[28:31], v[88:91]\n"
"ds_read_b64_tr_b16 a[158:159], v200 offset:30720\n"
"v_lshlrev_b32 v246, 2, v254\n"
"v_mfma_f32_16x16x32_bf16 v[92:95], a[224:227], v[32:35], 0\n"
"ds_read_b64_tr_b16 a[192:193], v203 offset:0\n"
"v_lshl_add_u32 v247, v253, 4, s62\n"
"v_mfma_f32_16x16x32_bf16 v[92:95], a[228:231], v[36:39], v[92:95]\n"
"ds_read_b64_tr_b16 a[194:195], v203 offset:2048\n"
"v_mfma_f32_16x16x32_bf16 v[92:95], a[232:235], v[40:43], v[92:95]\n"
"ds_read_b64_tr_b16 a[196:197], v203 offset:4096\n"
"v_mfma_f32_16x16x32_bf16 v[92:95], a[236:239], v[44:47], v[92:95]\n"
"ds_read_b64_tr_b16 a[198:199], v203 offset:6144\n"
"v_mfma_f32_16x16x32_bf16 v[92:95], a[240:243], v[48:51], v[92:95]\n"
"ds_read_b64_tr_b16 a[200:201], v203 offset:8192\n"
"v_cvt_pk_bf16_f32 v106, v88, v89\n"
"v_mfma_f32_16x16x32_bf16 v[92:95], a[244:247], v[52:55], v[92:95]\n"
"ds_read_b64_tr_b16 a[202:203], v203 offset:10240\n"
"v_cvt_pk_bf16_f32 v107, v90, v91\n"
"v_mfma_f32_16x16x32_bf16 v[92:95], a[248:251], v[56:59], v[92:95]\n"
"ds_read_b64_tr_b16 a[204:205], v203 offset:12288\n"
"v_mfma_f32_16x16x32_bf16 v[92:95], a[252:255], v[60:63], v[92:95]\n"
"buffer_store_dwordx4 v[104:107], v207, s[28:31], s54 offen offset:1024 sc1\n"
"ds_read_b64_tr_b16 a[206:207], v203 offset:14336\n"
"v_mfma_f32_16x16x32_bf16 v[64:67], a[128:131], v[0:3], 0\n"
"s_waitcnt lgkmcnt(11)\n"
"ds_read_b64_tr_b16 a[208:209], v203 offset:16384\n"
"v_mfma_f32_16x16x32_bf16 v[64:67], a[132:135], v[4:7], v[64:67]\n"
"ds_read_b64_tr_b16 a[210:211], v203 offset:18432\n"
"v_mfma_f32_16x16x32_bf16 v[64:67], a[136:139], v[8:11], v[64:67]\n"
"ds_read_b64_tr_b16 a[212:213], v203 offset:20480\n"
"v_mfma_f32_16x16x32_bf16 v[64:67], a[140:143], v[12:15], v[64:67]\n"
"ds_read_b64_tr_b16 a[214:215], v203 offset:22528\n"
"v_mfma_f32_16x16x32_bf16 v[64:67], a[144:147], v[16:19], v[64:67]\n"
"s_waitcnt vmcnt(22)\n"
"s_barrier\n"
"ds_read_b64_tr_b16 a[216:217], v203 offset:24576\n"
"v_cvt_pk_bf16_f32 v110, v92, v93\n"
"v_mfma_f32_16x16x32_bf16 v[64:67], a[148:151], v[20:23], v[64:67]\n"
"s_waitcnt lgkmcnt(11)\n"
"s_add_u32 m0, s53, 131072\n"
"ds_read_b64_tr_b16 a[218:219], v203 offset:26624\n"
"v_cvt_pk_bf16_f32 v111, v94, v95\n"
"v_mfma_f32_16x16x32_bf16 v[64:67], a[152:155], v[24:27], v[64:67]\n"
"ds_read_b64_tr_b16 a[220:221], v203 offset:28672\n"
"buffer_load_dwordx4 v206, s[24:27], s52 offen offset:0 lds\n"
"v_mfma_f32_16x16x32_bf16 v[64:67], a[156:159], v[28:31], v[64:67]\n"
"s_add_u32 s52, s52, 8192\n"
"buffer_store_dwordx4 v[108:111], v208, s[28:31], s54 offen offset:1024 sc1\n"
"ds_read_b64_tr_b16 a[222:223], v203 offset:30720\n"
"v_mfma_f32_16x16x32_bf16 v[68:71], a[128:131], v[32:35], 0\n"
"ds_read_b64_tr_b16 a[160:161], v200 offset:256\n"
"v_mfma_f32_16x16x32_bf16 v[68:71], a[132:135], v[36:39], v[68:71]\n"
"ds_read_b64_tr_b16 a[162:163], v200 offset:2304\n"
"v_mfma_f32_16x16x32_bf16 v[68:71], a[136:139], v[40:43], v[68:71]\n"
"ds_read_b64_tr_b16 a[164:165], v200 offset:4352\n"
"buffer_load_dwordx4 v206, s[24:27], s52 offen offset:1024 lds\n"
"v_mfma_f32_16x16x32_bf16 v[68:71], a[140:143], v[44:47], v[68:71]\n"
"s_add_u32 s52, s52, 8192\n"
"ds_read_b64_tr_b16 a[166:167], v200 offset:6400\n"
"v_mfma_f32_16x16x32_bf16 v[68:71], a[144:147], v[48:51], v[68:71]\n"
"ds_read_b64_tr_b16 a[168:169], v200 offset:8448\n"
"v_cvt_pk_bf16_f32 v96, v64, v65\n"
"v_mfma_f32_16x16x32_bf16 v[68:71], a[148:151], v[52:55], v[68:71]\n"
"ds_read_b64_tr_b16 a[170:171], v200 offset:10496\n"
"v_cvt_pk_bf16_f32 v97, v66, v67\n"
"v_mfma_f32_16x16x32_bf16 v[68:71], a[152:155], v[56:59], v[68:71]\n"
"ds_read_b64_tr_b16 a[172:173], v200 offset:12544\n"
"buffer_load_dwordx4 v206, s[24:27], s52 offen offset:2048 lds\n"
"v_mfma_f32_16x16x32_bf16 v[68:71], a[156:159], v[60:63], v[68:71]\n"
"s_add_u32 s52, s52, 8192\n"
"ds_read_b64_tr_b16 a[174:175], v200 offset:14592\n"
"v_mfma_f32_16x16x32_bf16 v[72:75], a[192:195], v[0:3], 0\n"
"s_waitcnt lgkmcnt(11)\n"
"ds_read_b64_tr_b16 a[176:177], v200 offset:16640\n"
"v_mfma_f32_16x16x32_bf16 v[72:75], a[196:199], v[4:7], v[72:75]\n"
"ds_read_b64_tr_b16 a[178:179], v200 offset:18688\n"
"v_mfma_f32_16x16x32_bf16 v[72:75], a[200:203], v[8:11], v[72:75]\n"
"ds_read_b64_tr_b16 a[180:181], v200 offset:20736\n"
"buffer_load_dwordx4 v206, s[24:27], s52 offen offset:3072 lds\n"
"v_mfma_f32_16x16x32_bf16 v[72:75], a[204:207], v[12:15], v[72:75]\n"
"s_add_u32 s52, s52, 12288\n"
"ds_read_b64_tr_b16 a[182:183], v200 offset:22784\n"
"v_mfma_f32_16x16x32_bf16 v[72:75], a[208:211], v[16:19], v[72:75]\n"
"ds_read_b64_tr_b16 a[184:185], v200 offset:24832\n"
"v_cvt_pk_bf16_f32 v100, v68, v69\n"
"v_mfma_f32_16x16x32_bf16 v[72:75], a[212:215], v[20:23], v[72:75]\n"
"s_waitcnt lgkmcnt(11)\n"
"s_add_u32 m0, s53, 135168\n"
"ds_read_b64_tr_b16 a[186:187], v200 offset:26880\n"
"v_cvt_pk_bf16_f32 v101, v70, v71\n"
"v_mfma_f32_16x16x32_bf16 v[72:75], a[216:219], v[24:27], v[72:75]\n"
"ds_read_b64_tr_b16 a[188:189], v200 offset:28928\n"
"buffer_load_dwordx4 v206, s[24:27], s52 offen offset:0 lds\n"
"v_mfma_f32_16x16x32_bf16 v[72:75], a[220:223], v[28:31], v[72:75]\n"
"s_add_u32 s52, s52, 8192\n"
"ds_read_b64_tr_b16 a[190:191], v200 offset:30976\n"
"v_mfma_f32_16x16x32_bf16 v[76:79], a[192:195], v[32:35], 0\n"
"ds_read_b64_tr_b16 a[224:225], v203 offset:256\n"
"v_mfma_f32_16x16x32_bf16 v[76:79], a[196:199], v[36:39], v[76:79]\n"
"ds_read_b64_tr_b16 a[226:227], v203 offset:2304\n"
"v_mfma_f32_16x16x32_bf16 v[76:79], a[200:203], v[40:43], v[76:79]\n"
"ds_read_b64_tr_b16 a[228:229], v203 offset:4352\n"
"buffer_load_dwordx4 v206, s[24:27], s52 offen offset:1024 lds\n"
"v_mfma_f32_16x16x32_bf16 v[76:79], a[204:207], v[44:47], v[76:79]\n"
"s_add_u32 s52, s52, 8192\n"
"ds_read_b64_tr_b16 a[230:231], v203 offset:6400\n"
"v_mfma_f32_16x16x32_bf16 v[76:79], a[208:211], v[48:51], v[76:79]\n"
"ds_read_b64_tr_b16 a[232:233], v203 offset:8448\n"
"v_cvt_pk_bf16_f32 v98, v72, v73\n"
"v_mfma_f32_16x16x32_bf16 v[76:79], a[212:215], v[52:55], v[76:79]\n"
"ds_read_b64_tr_b16 a[234:235], v203 offset:10496\n"
"v_cvt_pk_bf16_f32 v99, v74, v75\n"
"v_mfma_f32_16x16x32_bf16 v[76:79], a[216:219], v[56:59], v[76:79]\n"
"ds_read_b64_tr_b16 a[236:237], v203 offset:12544\n"
"buffer_load_dwordx4 v206, s[24:27], s52 offen offset:2048 lds\n"
"v_mfma_f32_16x16x32_bf16 v[76:79], a[220:223], v[60:63], v[76:79]\n"
"s_add_u32 s52, s52, 8192\n"
"ds_read_b64_tr_b16 a[238:239], v203 offset:14592\n"
"buffer_store_dwordx4 v[96:99], v207, s[28:31], s54 offen offset:2048 sc1\n"
"v_mfma_f32_16x16x32_bf16 v[80:83], a[160:163], v[0:3], 0\n"
"s_waitcnt lgkmcnt(11)\n"
"ds_read_b64_tr_b16 a[240:241], v203 offset:16640\n"
"v_mfma_f32_16x16x32_bf16 v[80:83], a[164:167], v[4:7], v[80:83]\n"
"ds_read_b64_tr_b16 a[242:243], v203 offset:18688\n"
"v_mfma_f32_16x16x32_bf16 v[80:83], a[168:171], v[8:11], v[80:83]\n"
"ds_read_b64_tr_b16 a[244:245], v203 offset:20736\n"
"buffer_load_dwordx4 v206, s[24:27], s52 offen offset:3072 lds\n"
"v_mfma_f32_16x16x32_bf16 v[80:83], a[172:175], v[12:15], v[80:83]\n"
"s_sub_u32 s52, s52, 61312\n"
"ds_read_b64_tr_b16 a[246:247], v203 offset:22784\n"
"v_mfma_f32_16x16x32_bf16 v[80:83], a[176:179], v[16:19], v[80:83]\n"
"ds_read_b64_tr_b16 a[248:249], v203 offset:24832\n"
"v_cvt_pk_bf16_f32 v102, v76, v77\n"
"v_mfma_f32_16x16x32_bf16 v[80:83], a[180:183], v[20:23], v[80:83]\n"
"s_waitcnt lgkmcnt(11)\n"
"ds_read_b64_tr_b16 a[250:251], v203 offset:26880\n"
"v_cvt_pk_bf16_f32 v103, v78, v79\n"
"v_mfma_f32_16x16x32_bf16 v[80:83], a[184:187], v[24:27], v[80:83]\n"
"ds_read_b64_tr_b16 a[252:253], v203 offset:28928\n"
"v_mfma_f32_16x16x32_bf16 v[80:83], a[188:191], v[28:31], v[80:83]\n"
"ds_read_b64_tr_b16 a[254:255], v203 offset:30976\n"
"buffer_store_dwordx4 v[100:103], v208, s[28:31], s54 offen offset:2048 sc1\n"
"v_mfma_f32_16x16x32_bf16 v[84:87], a[160:163], v[32:35], 0\n"
"ds_read_b64_tr_b16 a[128:129], v200 offset:32768\n"
"v_mfma_f32_16x16x32_bf16 v[84:87], a[164:167], v[36:39], v[84:87]\n"
"ds_read_b64_tr_b16 a[130:131], v200 offset:34816\n"
"v_mfma_f32_16x16x32_bf16 v[84:87], a[168:171], v[40:43], v[84:87]\n"
"ds_read_b64_tr_b16 a[132:133], v200 offset:36864\n"
"v_mfma_f32_16x16x32_bf16 v[84:87], a[172:175], v[44:47], v[84:87]\n"
"ds_read_b64_tr_b16 a[134:135], v200 offset:38912\n"
"v_mfma_f32_16x16x32_bf16 v[84:87], a[176:179], v[48:51], v[84:87]\n"
"ds_read_b64_tr_b16 a[136:137], v200 offset:40960\n"
"v_cvt_pk_bf16_f32 v104, v80, v81\n"
"v_mfma_f32_16x16x32_bf16 v[84:87], a[180:183], v[52:55], v[84:87]\n"
"ds_read_b64_tr_b16 a[138:139], v200 offset:43008\n"
"v_cvt_pk_bf16_f32 v105, v82, v83\n"
"v_mfma_f32_16x16x32_bf16 v[84:87], a[184:187], v[56:59], v[84:87]\n"
"ds_read_b64_tr_b16 a[140:141], v200 offset:45056\n"
"v_mfma_f32_16x16x32_bf16 v[84:87], a[188:191], v[60:63], v[84:87]\n"
"ds_read_b64_tr_b16 a[142:143], v200 offset:47104\n"
"v_mfma_f32_16x16x32_bf16 v[88:91], a[224:227], v[0:3], 0\n"
"s_waitcnt lgkmcnt(11)\n"
"ds_read_b64_tr_b16 a[144:145], v200 offset:49152\n"
"v_mfma_f32_16x16x32_bf16 v[88:91], a[228:231], v[4:7], v[88:91]\n"
"ds_read_b64_tr_b16 a[146:147], v200 offset:51200\n"
"v_mfma_f32_16x16x32_bf16 v[88:91], a[232:235], v[8:11], v[88:91]\n"
"ds_read_b64_tr_b16 a[148:149], v200 offset:53248\n"
"v_mfma_f32_16x16x32_bf16 v[88:91], a[236:239], v[12:15], v[88:91]\n"
"ds_read_b64_tr_b16 a[150:151], v200 offset:55296\n"
"v_mfma_f32_16x16x32_bf16 v[88:91], a[240:243], v[16:19], v[88:91]\n"
"ds_read_b64_tr_b16 a[152:153], v200 offset:57344\n"
"v_cvt_pk_bf16_f32 v108, v84, v85\n"
"v_mfma_f32_16x16x32_bf16 v[88:91], a[244:247], v[20:23], v[88:91]\n"
"s_waitcnt lgkmcnt(11)\n"
"ds_read_b64_tr_b16 a[154:155], v200 offset:59392\n"
"v_cvt_pk_bf16_f32 v109, v86, v87\n"
"v_mfma_f32_16x16x32_bf16 v[88:91], a[248:251], v[24:27], v[88:91]\n"
"ds_read_b64_tr_b16 a[156:157], v200 offset:61440\n"
"v_mfma_f32_16x16x32_bf16 v[88:91], a[252:255], v[28:31], v[88:91]\n"
"ds_read_b64_tr_b16 a[158:159], v200 offset:63488\n"
"v_mfma_f32_16x16x32_bf16 v[92:95], a[224:227], v[32:35], 0\n"
"ds_read_b64_tr_b16 a[192:193], v203 offset:32768\n"
"v_mfma_f32_16x16x32_bf16 v[92:95], a[228:231], v[36:39], v[92:95]\n"
"ds_read_b64_tr_b16 a[194:195], v203 offset:34816\n"
"v_mfma_f32_16x16x32_bf16 v[92:95], a[232:235], v[40:43], v[92:95]\n"
"ds_read_b64_tr_b16 a[196:197], v203 offset:36864\n"
"v_mfma_f32_16x16x32_bf16 v[92:95], a[236:239], v[44:47], v[92:95]\n"
"ds_read_b64_tr_b16 a[198:199], v203 offset:38912\n"
"v_mfma_f32_16x16x32_bf16 v[92:95], a[240:243], v[48:51], v[92:95]\n"
"ds_read_b64_tr_b16 a[200:201], v203 offset:40960\n"
"v_cvt_pk_bf16_f32 v106, v88, v89\n"
"v_mfma_f32_16x16x32_bf16 v[92:95], a[244:247], v[52:55], v[92:95]\n"
"ds_read_b64_tr_b16 a[202:203], v203 offset:43008\n"
"v_cvt_pk_bf16_f32 v107, v90, v91\n"
"v_mfma_f32_16x16x32_bf16 v[92:95], a[248:251], v[56:59], v[92:95]\n"
"ds_read_b64_tr_b16 a[204:205], v203 offset:45056\n"
"v_mfma_f32_16x16x32_bf16 v[92:95], a[252:255], v[60:63], v[92:95]\n"
"buffer_store_dwordx4 v[104:107], v207, s[28:31], s54 offen offset:3072 sc1\n"
"ds_read_b64_tr_b16 a[206:207], v203 offset:47104\n"
"v_mfma_f32_16x16x32_bf16 v[64:67], a[128:131], v[0:3], 0\n"
"s_waitcnt lgkmcnt(11)\n"
"ds_read_b64_tr_b16 a[208:209], v203 offset:49152\n"
"v_mfma_f32_16x16x32_bf16 v[64:67], a[132:135], v[4:7], v[64:67]\n"
"ds_read_b64_tr_b16 a[210:211], v203 offset:51200\n"
"v_mfma_f32_16x16x32_bf16 v[64:67], a[136:139], v[8:11], v[64:67]\n"
"ds_read_b64_tr_b16 a[212:213], v203 offset:53248\n"
"v_mfma_f32_16x16x32_bf16 v[64:67], a[140:143], v[12:15], v[64:67]\n"
"ds_read_b64_tr_b16 a[214:215], v203 offset:55296\n"
"v_mfma_f32_16x16x32_bf16 v[64:67], a[144:147], v[16:19], v[64:67]\n"
"s_waitcnt vmcnt(23)\n"
"s_barrier\n"
"ds_read_b64_tr_b16 a[216:217], v203 offset:57344\n"
"v_cvt_pk_bf16_f32 v110, v92, v93\n"
"v_mfma_f32_16x16x32_bf16 v[64:67], a[148:151], v[20:23], v[64:67]\n"
"s_waitcnt lgkmcnt(11)\n"
"s_mov_b32 m0, s53\n"
"ds_read_b64_tr_b16 a[218:219], v203 offset:59392\n"
"v_cvt_pk_bf16_f32 v111, v94, v95\n"
"v_mfma_f32_16x16x32_bf16 v[64:67], a[152:155], v[24:27], v[64:67]\n"
"ds_read_b64_tr_b16 a[220:221], v203 offset:61440\n"
"buffer_load_dwordx4 v206, s[24:27], s52 offen offset:0 lds\n"
"v_mfma_f32_16x16x32_bf16 v[64:67], a[156:159], v[28:31], v[64:67]\n"
"s_add_u32 s52, s52, 8192\n"
"buffer_store_dwordx4 v[108:111], v208, s[28:31], s54 offen offset:3072 sc1\n"
"ds_read_b64_tr_b16 a[222:223], v203 offset:63488\n"
"v_mfma_f32_16x16x32_bf16 v[68:71], a[128:131], v[32:35], 0\n"
"s_add_u32 s54, s54, 4096\n"
"ds_read_b64_tr_b16 a[160:161], v200 offset:33024\n"
"v_mfma_f32_16x16x32_bf16 v[68:71], a[132:135], v[36:39], v[68:71]\n"
"ds_read_b64_tr_b16 a[162:163], v200 offset:35072\n"
"v_mfma_f32_16x16x32_bf16 v[68:71], a[136:139], v[40:43], v[68:71]\n"
"ds_read_b64_tr_b16 a[164:165], v200 offset:37120\n"
"buffer_load_dwordx4 v206, s[24:27], s52 offen offset:1024 lds\n"
"v_mfma_f32_16x16x32_bf16 v[68:71], a[140:143], v[44:47], v[68:71]\n"
"s_add_u32 s52, s52, 8192\n"
"ds_read_b64_tr_b16 a[166:167], v200 offset:39168\n"
"v_mfma_f32_16x16x32_bf16 v[68:71], a[144:147], v[48:51], v[68:71]\n"
"ds_read_b64_tr_b16 a[168:169], v200 offset:41216\n"
"v_cvt_pk_bf16_f32 v96, v64, v65\n"
"v_mfma_f32_16x16x32_bf16 v[68:71], a[148:151], v[52:55], v[68:71]\n"
"ds_read_b64_tr_b16 a[170:171], v200 offset:43264\n"
"v_cvt_pk_bf16_f32 v97, v66, v67\n"
"v_mfma_f32_16x16x32_bf16 v[68:71], a[152:155], v[56:59], v[68:71]\n"
"ds_read_b64_tr_b16 a[172:173], v200 offset:45312\n"
"buffer_load_dwordx4 v206, s[24:27], s52 offen offset:2048 lds\n"
"v_mfma_f32_16x16x32_bf16 v[68:71], a[156:159], v[60:63], v[68:71]\n"
"s_add_u32 s52, s52, 8192\n"
"ds_read_b64_tr_b16 a[174:175], v200 offset:47360\n"
"v_mfma_f32_16x16x32_bf16 v[72:75], a[192:195], v[0:3], 0\n"
"s_waitcnt lgkmcnt(11)\n"
"ds_read_b64_tr_b16 a[176:177], v200 offset:49408\n"
"v_mfma_f32_16x16x32_bf16 v[72:75], a[196:199], v[4:7], v[72:75]\n"
"ds_read_b64_tr_b16 a[178:179], v200 offset:51456\n"
"v_mfma_f32_16x16x32_bf16 v[72:75], a[200:203], v[8:11], v[72:75]\n"
"ds_read_b64_tr_b16 a[180:181], v200 offset:53504\n"
"buffer_load_dwordx4 v206, s[24:27], s52 offen offset:3072 lds\n"
"v_mfma_f32_16x16x32_bf16 v[72:75], a[204:207], v[12:15], v[72:75]\n"
"s_add_u32 s52, s52, 12288\n"
"ds_read_b64_tr_b16 a[182:183], v200 offset:55552\n"
"v_mfma_f32_16x16x32_bf16 v[72:75], a[208:211], v[16:19], v[72:75]\n"
"ds_read_b64_tr_b16 a[184:185], v200 offset:57600\n"
"v_cvt_pk_bf16_f32 v100, v68, v69\n"
"v_mfma_f32_16x16x32_bf16 v[72:75], a[212:215], v[20:23], v[72:75]\n"
"s_waitcnt lgkmcnt(11)\n"
"s_add_u32 m0, s53, 4096\n"
"ds_read_b64_tr_b16 a[186:187], v200 offset:59648\n"
"v_cvt_pk_bf16_f32 v101, v70, v71\n"
"v_mfma_f32_16x16x32_bf16 v[72:75], a[216:219], v[24:27], v[72:75]\n"
"ds_read_b64_tr_b16 a[188:189], v200 offset:61696\n"
"buffer_load_dwordx4 v206, s[24:27], s52 offen offset:0 lds\n"
"v_mfma_f32_16x16x32_bf16 v[72:75], a[220:223], v[28:31], v[72:75]\n"
"s_add_u32 s52, s52, 8192\n"
"ds_read_b64_tr_b16 a[190:191], v200 offset:63744\n"
"v_mfma_f32_16x16x32_bf16 v[76:79], a[192:195], v[32:35], 0\n"
"ds_read_b64_tr_b16 a[224:225], v203 offset:33024\n"
"v_mfma_f32_16x16x32_bf16 v[76:79], a[196:199], v[36:39], v[76:79]\n"
"ds_read_b64_tr_b16 a[226:227], v203 offset:35072\n"
"v_mfma_f32_16x16x32_bf16 v[76:79], a[200:203], v[40:43], v[76:79]\n"
"ds_read_b64_tr_b16 a[228:229], v203 offset:37120\n"
"buffer_load_dwordx4 v206, s[24:27], s52 offen offset:1024 lds\n"
"v_mfma_f32_16x16x32_bf16 v[76:79], a[204:207], v[44:47], v[76:79]\n"
"s_add_u32 s52, s52, 8192\n"
"ds_read_b64_tr_b16 a[230:231], v203 offset:39168\n"
"v_mfma_f32_16x16x32_bf16 v[76:79], a[208:211], v[48:51], v[76:79]\n"
"ds_read_b64_tr_b16 a[232:233], v203 offset:41216\n"
"v_cvt_pk_bf16_f32 v98, v72, v73\n"
"v_mfma_f32_16x16x32_bf16 v[76:79], a[212:215], v[52:55], v[76:79]\n"
"ds_read_b64_tr_b16 a[234:235], v203 offset:43264\n"
"v_cvt_pk_bf16_f32 v99, v74, v75\n"
"v_mfma_f32_16x16x32_bf16 v[76:79], a[216:219], v[56:59], v[76:79]\n"
"ds_read_b64_tr_b16 a[236:237], v203 offset:45312\n"
"buffer_load_dwordx4 v206, s[24:27], s52 offen offset:2048 lds\n"
"v_mfma_f32_16x16x32_bf16 v[76:79], a[220:223], v[60:63], v[76:79]\n"
"s_add_u32 s52, s52, 8192\n"
"ds_read_b64_tr_b16 a[238:239], v203 offset:47360\n"
"buffer_store_dwordx4 v[96:99], v207, s[28:31], s54 offen offset:0 sc1\n"
"v_mfma_f32_16x16x32_bf16 v[80:83], a[160:163], v[0:3], 0\n"
"s_waitcnt lgkmcnt(11)\n"
"ds_read_b64_tr_b16 a[240:241], v203 offset:49408\n"
"v_mfma_f32_16x16x32_bf16 v[80:83], a[164:167], v[4:7], v[80:83]\n"
"ds_read_b64_tr_b16 a[242:243], v203 offset:51456\n"
"v_mfma_f32_16x16x32_bf16 v[80:83], a[168:171], v[8:11], v[80:83]\n"
"ds_read_b64_tr_b16 a[244:245], v203 offset:53504\n"
"buffer_load_dwordx4 v206, s[24:27], s52 offen offset:3072 lds\n"
"v_mfma_f32_16x16x32_bf16 v[80:83], a[172:175], v[12:15], v[80:83]\n"
"s_sub_u32 s52, s52, 61312\n"
"ds_read_b64_tr_b16 a[246:247], v203 offset:55552\n"
"v_mfma_f32_16x16x32_bf16 v[80:83], a[176:179], v[16:19], v[80:83]\n"
"ds_read_b64_tr_b16 a[248:249], v203 offset:57600\n"
"v_cvt_pk_bf16_f32 v102, v76, v77\n"
"v_mfma_f32_16x16x32_bf16 v[80:83], a[180:183], v[20:23], v[80:83]\n"
"s_waitcnt lgkmcnt(11)\n"
"ds_read_b64_tr_b16 a[250:251], v203 offset:59648\n"
"v_cvt_pk_bf16_f32 v103, v78, v79\n"
"v_mfma_f32_16x16x32_bf16 v[80:83], a[184:187], v[24:27], v[80:83]\n"
"ds_read_b64_tr_b16 a[252:253], v203 offset:61696\n"
"v_mfma_f32_16x16x32_bf16 v[80:83], a[188:191], v[28:31], v[80:83]\n"
"ds_read_b64_tr_b16 a[254:255], v203 offset:63744\n"
"buffer_store_dwordx4 v[100:103], v208, s[28:31], s54 offen offset:0 sc1\n"
"v_mfma_f32_16x16x32_bf16 v[84:87], a[160:163], v[32:35], 0\n"
"ds_read_b64_tr_b16 a[128:129], v201 offset:0\n"
"v_mfma_f32_16x16x32_bf16 v[84:87], a[164:167], v[36:39], v[84:87]\n"
"ds_read_b64_tr_b16 a[130:131], v201 offset:2048\n"
"v_mfma_f32_16x16x32_bf16 v[84:87], a[168:171], v[40:43], v[84:87]\n"
"ds_read_b64_tr_b16 a[132:133], v201 offset:4096\n"
"v_mfma_f32_16x16x32_bf16 v[84:87], a[172:175], v[44:47], v[84:87]\n"
"ds_read_b64_tr_b16 a[134:135], v201 offset:6144\n"
"v_mfma_f32_16x16x32_bf16 v[84:87], a[176:179], v[48:51], v[84:87]\n"
"ds_read_b64_tr_b16 a[136:137], v201 offset:8192\n"
"v_cvt_pk_bf16_f32 v104, v80, v81\n"
"v_mfma_f32_16x16x32_bf16 v[84:87], a[180:183], v[52:55], v[84:87]\n"
"ds_read_b64_tr_b16 a[138:139], v201 offset:10240\n"
"v_cvt_pk_bf16_f32 v105, v82, v83\n"
"v_mfma_f32_16x16x32_bf16 v[84:87], a[184:187], v[56:59], v[84:87]\n"
"ds_read_b64_tr_b16 a[140:141], v201 offset:12288\n"
"v_mfma_f32_16x16x32_bf16 v[84:87], a[188:191], v[60:63], v[84:87]\n"
"ds_read_b64_tr_b16 a[142:143], v201 offset:14336\n"
"v_mfma_f32_16x16x32_bf16 v[88:91], a[224:227], v[0:3], 0\n"
"s_waitcnt lgkmcnt(11)\n"
"ds_read_b64_tr_b16 a[144:145], v201 offset:16384\n"
"v_mfma_f32_16x16x32_bf16 v[88:91], a[228:231], v[4:7], v[88:91]\n"
"ds_read_b64_tr_b16 a[146:147], v201 offset:18432\n"
"v_mfma_f32_16x16x32_bf16 v[88:91], a[232:235], v[8:11], v[88:91]\n"
"ds_read_b64_tr_b16 a[148:149], v201 offset:20480\n"
"v_mfma_f32_16x16x32_bf16 v[88:91], a[236:239], v[12:15], v[88:91]\n"
"ds_read_b64_tr_b16 a[150:151], v201 offset:22528\n"
"v_mfma_f32_16x16x32_bf16 v[88:91], a[240:243], v[16:19], v[88:91]\n"
"ds_read_b64_tr_b16 a[152:153], v201 offset:24576\n"
"v_cvt_pk_bf16_f32 v108, v84, v85\n"
"v_mfma_f32_16x16x32_bf16 v[88:91], a[244:247], v[20:23], v[88:91]\n"
"s_waitcnt lgkmcnt(11)\n"
"ds_read_b64_tr_b16 a[154:155], v201 offset:26624\n"
"v_cvt_pk_bf16_f32 v109, v86, v87\n"
"v_mfma_f32_16x16x32_bf16 v[88:91], a[248:251], v[24:27], v[88:91]\n"
"ds_read_b64_tr_b16 a[156:157], v201 offset:28672\n"
"v_mfma_f32_16x16x32_bf16 v[88:91], a[252:255], v[28:31], v[88:91]\n"
"ds_read_b64_tr_b16 a[158:159], v201 offset:30720\n"
"v_mfma_f32_16x16x32_bf16 v[92:95], a[224:227], v[32:35], 0\n"
"ds_read_b64_tr_b16 a[192:193], v204 offset:0\n"
"v_mfma_f32_16x16x32_bf16 v[92:95], a[228:231], v[36:39], v[92:95]\n"
"ds_read_b64_tr_b16 a[194:195], v204 offset:2048\n"
"v_mfma_f32_16x16x32_bf16 v[92:95], a[232:235], v[40:43], v[92:95]\n"
"ds_read_b64_tr_b16 a[196:197], v204 offset:4096\n"
"v_mfma_f32_16x16x32_bf16 v[92:95], a[236:239], v[44:47], v[92:95]\n"
"ds_read_b64_tr_b16 a[198:199], v204 offset:6144\n"
"v_mfma_f32_16x16x32_bf16 v[92:95], a[240:243], v[48:51], v[92:95]\n"
"ds_read_b64_tr_b16 a[200:201], v204 offset:8192\n"
"v_cvt_pk_bf16_f32 v106, v88, v89\n"
"v_mfma_f32_16x16x32_bf16 v[92:95], a[244:247], v[52:55], v[92:95]\n"
"ds_read_b64_tr_b16 a[202:203], v204 offset:10240\n"
"v_cvt_pk_bf16_f32 v107, v90, v91\n"
"v_mfma_f32_16x16x32_bf16 v[92:95], a[248:251], v[56:59], v[92:95]\n"
"ds_read_b64_tr_b16 a[204:205], v204 offset:12288\n"
"v_mfma_f32_16x16x32_bf16 v[92:95], a[252:255], v[60:63], v[92:95]\n"
"buffer_store_dwordx4 v[104:107], v207, s[28:31], s54 offen offset:1024 sc1\n"
"ds_read_b64_tr_b16 a[206:207], v204 offset:14336\n"
"v_mfma_f32_16x16x32_bf16 v[64:67], a[128:131], v[0:3], 0\n"
"s_waitcnt lgkmcnt(11)\n"
"ds_read_b64_tr_b16 a[208:209], v204 offset:16384\n"
"v_accvgpr_write_b32 a48, v128\n"
"v_mfma_f32_16x16x32_bf16 v[64:67], a[132:135], v[4:7], v[64:67]\n"
"ds_read_b64_tr_b16 a[210:211], v204 offset:18432\n"
"v_accvgpr_write_b32 a49, v129\n"
"v_mfma_f32_16x16x32_bf16 v[64:67], a[136:139], v[8:11], v[64:67]\n"
"ds_read_b64_tr_b16 a[212:213], v204 offset:20480\n"
"v_accvgpr_write_b32 a50, v130\n"
"v_mfma_f32_16x16x32_bf16 v[64:67], a[140:143], v[12:15], v[64:67]\n"
"ds_read_b64_tr_b16 a[214:215], v204 offset:22528\n"
"v_accvgpr_write_b32 a51, v131\n"
"v_mfma_f32_16x16x32_bf16 v[64:67], a[144:147], v[16:19], v[64:67]\n"
"s_waitcnt vmcnt(26)\n"
"s_barrier\n"
"ds_read_b64_tr_b16 a[216:217], v204 offset:24576\n"
"v_cvt_pk_bf16_f32 v110, v92, v93\n"
"v_mfma_f32_16x16x32_bf16 v[64:67], a[148:151], v[20:23], v[64:67]\n"
"s_waitcnt lgkmcnt(11)\n"
"s_add_u32 m0, s53, 32768\n"
"ds_read_b64_tr_b16 a[218:219], v204 offset:26624\n"
"v_cvt_pk_bf16_f32 v111, v94, v95\n"
"v_mfma_f32_16x16x32_bf16 v[64:67], a[152:155], v[24:27], v[64:67]\n"
"ds_read_b64_tr_b16 a[220:221], v204 offset:28672\n"
"buffer_load_dwordx4 v206, s[24:27], s52 offen offset:0 lds\n"
"v_mfma_f32_16x16x32_bf16 v[64:67], a[156:159], v[28:31], v[64:67]\n"
"s_add_u32 s52, s52, 8192\n"
"buffer_store_dwordx4 v[108:111], v208, s[28:31], s54 offen offset:1024 sc1\n"
"ds_read_b64_tr_b16 a[222:223], v204 offset:30720\n"
"v_mfma_f32_16x16x32_bf16 v[68:71], a[128:131], v[32:35], 0\n"
"ds_read_b64_tr_b16 a[160:161], v201 offset:256\n"
"v_accvgpr_write_b32 a52, v132\n"
"v_mfma_f32_16x16x32_bf16 v[68:71], a[132:135], v[36:39], v[68:71]\n"
"ds_read_b64_tr_b16 a[162:163], v201 offset:2304\n"
"v_accvgpr_write_b32 a53, v133\n"
"v_mfma_f32_16x16x32_bf16 v[68:71], a[136:139], v[40:43], v[68:71]\n"
"ds_read_b64_tr_b16 a[164:165], v201 offset:4352\n"
"buffer_load_dwordx4 v206, s[24:27], s52 offen offset:1024 lds\n"
"v_mfma_f32_16x16x32_bf16 v[68:71], a[140:143], v[44:47], v[68:71]\n"
"s_add_u32 s52, s52, 8192\n"
"ds_read_b64_tr_b16 a[166:167], v201 offset:6400\n"
"v_accvgpr_write_b32 a54, v134\n"
"v_mfma_f32_16x16x32_bf16 v[68:71], a[144:147], v[48:51], v[68:71]\n"
"ds_read_b64_tr_b16 a[168:169], v201 offset:8448\n"
"v_cvt_pk_bf16_f32 v96, v64, v65\n"
"v_mfma_f32_16x16x32_bf16 v[68:71], a[148:151], v[52:55], v[68:71]\n"
"ds_read_b64_tr_b16 a[170:171], v201 offset:10496\n"
"v_cvt_pk_bf16_f32 v97, v66, v67\n"
"v_mfma_f32_16x16x32_bf16 v[68:71], a[152:155], v[56:59], v[68:71]\n"
"ds_read_b64_tr_b16 a[172:173], v201 offset:12544\n"
"buffer_load_dwordx4 v206, s[24:27], s52 offen offset:2048 lds\n"
"v_mfma_f32_16x16x32_bf16 v[68:71], a[156:159], v[60:63], v[68:71]\n"
"s_add_u32 s52, s52, 8192\n"
"ds_read_b64_tr_b16 a[174:175], v201 offset:14592\n"
"v_accvgpr_write_b32 a55, v135\n"
"v_mfma_f32_16x16x32_bf16 v[72:75], a[192:195], v[0:3], 0\n"
"s_waitcnt lgkmcnt(11)\n"
"ds_read_b64_tr_b16 a[176:177], v201 offset:16640\n"
"v_accvgpr_write_b32 a56, v136\n"
"v_mfma_f32_16x16x32_bf16 v[72:75], a[196:199], v[4:7], v[72:75]\n"
"ds_read_b64_tr_b16 a[178:179], v201 offset:18688\n"
"v_accvgpr_write_b32 a57, v137\n"
"v_mfma_f32_16x16x32_bf16 v[72:75], a[200:203], v[8:11], v[72:75]\n"
"ds_read_b64_tr_b16 a[180:181], v201 offset:20736\n"
"buffer_load_dwordx4 v206, s[24:27], s52 offen offset:3072 lds\n"
"v_mfma_f32_16x16x32_bf16 v[72:75], a[204:207], v[12:15], v[72:75]\n"
"s_add_u32 s52, s52, 12288\n"
"ds_read_b64_tr_b16 a[182:183], v201 offset:22784\n"
"v_accvgpr_write_b32 a58, v138\n"
"v_mfma_f32_16x16x32_bf16 v[72:75], a[208:211], v[16:19], v[72:75]\n"
"ds_read_b64_tr_b16 a[184:185], v201 offset:24832\n"
"v_cvt_pk_bf16_f32 v100, v68, v69\n"
"v_mfma_f32_16x16x32_bf16 v[72:75], a[212:215], v[20:23], v[72:75]\n"
"s_waitcnt lgkmcnt(11)\n"
"s_add_u32 m0, s53, 36864\n"
"ds_read_b64_tr_b16 a[186:187], v201 offset:26880\n"
"v_cvt_pk_bf16_f32 v101, v70, v71\n"
"v_mfma_f32_16x16x32_bf16 v[72:75], a[216:219], v[24:27], v[72:75]\n"
"ds_read_b64_tr_b16 a[188:189], v201 offset:28928\n"
"buffer_load_dwordx4 v206, s[24:27], s52 offen offset:0 lds\n"
"v_mfma_f32_16x16x32_bf16 v[72:75], a[220:223], v[28:31], v[72:75]\n"
"s_add_u32 s52, s52, 8192\n"
"ds_read_b64_tr_b16 a[190:191], v201 offset:30976\n"
"v_accvgpr_write_b32 a59, v139\n"
"v_mfma_f32_16x16x32_bf16 v[76:79], a[192:195], v[32:35], 0\n"
"ds_read_b64_tr_b16 a[224:225], v204 offset:256\n"
"v_accvgpr_write_b32 a60, v140\n"
"v_mfma_f32_16x16x32_bf16 v[76:79], a[196:199], v[36:39], v[76:79]\n"
"ds_read_b64_tr_b16 a[226:227], v204 offset:2304\n"
"v_accvgpr_write_b32 a61, v141\n"
"v_mfma_f32_16x16x32_bf16 v[76:79], a[200:203], v[40:43], v[76:79]\n"
"ds_read_b64_tr_b16 a[228:229], v204 offset:4352\n"
"buffer_load_dwordx4 v206, s[24:27], s52 offen offset:1024 lds\n"
"v_mfma_f32_16x16x32_bf16 v[76:79], a[204:207], v[44:47], v[76:79]\n"
"s_add_u32 s52, s52, 8192\n"
"ds_read_b64_tr_b16 a[230:231], v204 offset:6400\n"
"v_accvgpr_write_b32 a62, v142\n"
"v_mfma_f32_16x16x32_bf16 v[76:79], a[208:211], v[48:51], v[76:79]\n"
"ds_read_b64_tr_b16 a[232:233], v204 offset:8448\n"
"v_cvt_pk_bf16_f32 v98, v72, v73\n"
"v_mfma_f32_16x16x32_bf16 v[76:79], a[212:215], v[52:55], v[76:79]\n"
"ds_read_b64_tr_b16 a[234:235], v204 offset:10496\n"
"v_cvt_pk_bf16_f32 v99, v74, v75\n"
"v_mfma_f32_16x16x32_bf16 v[76:79], a[216:219], v[56:59], v[76:79]\n"
"ds_read_b64_tr_b16 a[236:237], v204 offset:12544\n"
"buffer_load_dwordx4 v206, s[24:27], s52 offen offset:2048 lds\n"
"v_mfma_f32_16x16x32_bf16 v[76:79], a[220:223], v[60:63], v[76:79]\n"
"s_add_u32 s52, s52, 8192\n"
"ds_read_b64_tr_b16 a[238:239], v204 offset:14592\n"
"buffer_store_dwordx4 v[96:99], v207, s[28:31], s54 offen offset:2048 sc1\n"
"v_mfma_f32_16x16x32_bf16 v[80:83], a[160:163], v[0:3], 0\n"
"s_waitcnt lgkmcnt(11)\n"
"ds_read_b64_tr_b16 a[240:241], v204 offset:16640\n"
"v_accvgpr_write_b32 a63, v143\n"
"v_mfma_f32_16x16x32_bf16 v[80:83], a[164:167], v[4:7], v[80:83]\n"
"ds_read_b64_tr_b16 a[242:243], v204 offset:18688\n"
"v_accvgpr_write_b32 a64, v144\n"
"v_mfma_f32_16x16x32_bf16 v[80:83], a[168:171], v[8:11], v[80:83]\n"
"ds_read_b64_tr_b16 a[244:245], v204 offset:20736\n"
"buffer_load_dwordx4 v206, s[24:27], s52 offen offset:3072 lds\n"
"v_mfma_f32_16x16x32_bf16 v[80:83], a[172:175], v[12:15], v[80:83]\n"
"s_sub_u32 s52, s52, 61312\n"
"ds_read_b64_tr_b16 a[246:247], v204 offset:22784\n"
"v_accvgpr_write_b32 a65, v145\n"
"v_mfma_f32_16x16x32_bf16 v[80:83], a[176:179], v[16:19], v[80:83]\n"
"ds_read_b64_tr_b16 a[248:249], v204 offset:24832\n"
"v_cvt_pk_bf16_f32 v102, v76, v77\n"
"v_mfma_f32_16x16x32_bf16 v[80:83], a[180:183], v[20:23], v[80:83]\n"
"s_waitcnt lgkmcnt(11)\n"
"ds_read_b64_tr_b16 a[250:251], v204 offset:26880\n"
"v_cvt_pk_bf16_f32 v103, v78, v79\n"
"v_mfma_f32_16x16x32_bf16 v[80:83], a[184:187], v[24:27], v[80:83]\n"
"ds_read_b64_tr_b16 a[252:253], v204 offset:28928\n"
"v_accvgpr_write_b32 a66, v146\n"
"v_mfma_f32_16x16x32_bf16 v[80:83], a[188:191], v[28:31], v[80:83]\n"
"ds_read_b64_tr_b16 a[254:255], v204 offset:30976\n"
"buffer_store_dwordx4 v[100:103], v208, s[28:31], s54 offen offset:2048 sc1\n"
"v_mfma_f32_16x16x32_bf16 v[84:87], a[160:163], v[32:35], 0\n"
"ds_read_b64_tr_b16 a[128:129], v201 offset:32768\n"
"v_accvgpr_write_b32 a67, v147\n"
"v_mfma_f32_16x16x32_bf16 v[84:87], a[164:167], v[36:39], v[84:87]\n"
"ds_read_b64_tr_b16 a[130:131], v201 offset:34816\n"
"v_accvgpr_write_b32 a68, v148\n"
"v_mfma_f32_16x16x32_bf16 v[84:87], a[168:171], v[40:43], v[84:87]\n"
"ds_read_b64_tr_b16 a[132:133], v201 offset:36864\n"
"v_accvgpr_write_b32 a69, v149\n"
"v_mfma_f32_16x16x32_bf16 v[84:87], a[172:175], v[44:47], v[84:87]\n"
"ds_read_b64_tr_b16 a[134:135], v201 offset:38912\n"
"v_accvgpr_write_b32 a70, v150\n"
"v_mfma_f32_16x16x32_bf16 v[84:87], a[176:179], v[48:51], v[84:87]\n"
"ds_read_b64_tr_b16 a[136:137], v201 offset:40960\n"
"v_cvt_pk_bf16_f32 v104, v80, v81\n"
"v_mfma_f32_16x16x32_bf16 v[84:87], a[180:183], v[52:55], v[84:87]\n"
"ds_read_b64_tr_b16 a[138:139], v201 offset:43008\n"
"v_cvt_pk_bf16_f32 v105, v82, v83\n"
"v_mfma_f32_16x16x32_bf16 v[84:87], a[184:187], v[56:59], v[84:87]\n"
"ds_read_b64_tr_b16 a[140:141], v201 offset:45056\n"
"v_accvgpr_write_b32 a71, v151\n"
"v_mfma_f32_16x16x32_bf16 v[84:87], a[188:191], v[60:63], v[84:87]\n"
"ds_read_b64_tr_b16 a[142:143], v201 offset:47104\n"
"v_accvgpr_write_b32 a72, v152\n"
"v_mfma_f32_16x16x32_bf16 v[88:91], a[224:227], v[0:3], 0\n"
"s_waitcnt lgkmcnt(11)\n"
"ds_read_b64_tr_b16 a[144:145], v201 offset:49152\n"
"v_accvgpr_write_b32 a73, v153\n"
"v_mfma_f32_16x16x32_bf16 v[88:91], a[228:231], v[4:7], v[88:91]\n"
"ds_read_b64_tr_b16 a[146:147], v201 offset:51200\n"
"v_accvgpr_write_b32 a74, v154\n"
"v_mfma_f32_16x16x32_bf16 v[88:91], a[232:235], v[8:11], v[88:91]\n"
"ds_read_b64_tr_b16 a[148:149], v201 offset:53248\n"
"v_accvgpr_write_b32 a75, v155\n"
"v_mfma_f32_16x16x32_bf16 v[88:91], a[236:239], v[12:15], v[88:91]\n"
"ds_read_b64_tr_b16 a[150:151], v201 offset:55296\n"
"v_accvgpr_write_b32 a76, v156\n"
"v_mfma_f32_16x16x32_bf16 v[88:91], a[240:243], v[16:19], v[88:91]\n"
"ds_read_b64_tr_b16 a[152:153], v201 offset:57344\n"
"v_cvt_pk_bf16_f32 v108, v84, v85\n"
"v_mfma_f32_16x16x32_bf16 v[88:91], a[244:247], v[20:23], v[88:91]\n"
"s_waitcnt lgkmcnt(11)\n"
"ds_read_b64_tr_b16 a[154:155], v201 offset:59392\n"
"v_cvt_pk_bf16_f32 v109, v86, v87\n"
"v_mfma_f32_16x16x32_bf16 v[88:91], a[248:251], v[24:27], v[88:91]\n"
"ds_read_b64_tr_b16 a[156:157], v201 offset:61440\n"
"v_accvgpr_write_b32 a77, v157\n"
"v_mfma_f32_16x16x32_bf16 v[88:91], a[252:255], v[28:31], v[88:91]\n"
"ds_read_b64_tr_b16 a[158:159], v201 offset:63488\n"
"v_accvgpr_write_b32 a78, v158\n"
"v_mfma_f32_16x16x32_bf16 v[92:95], a[224:227], v[32:35], 0\n"
"ds_read_b64_tr_b16 a[192:193], v204 offset:32768\n"
"v_accvgpr_write_b32 a79, v159\n"
"v_mfma_f32_16x16x32_bf16 v[92:95], a[228:231], v[36:39], v[92:95]\n"
"ds_read_b64_tr_b16 a[194:195], v204 offset:34816\n"
"v_accvgpr_write_b32 a80, v160\n"
"v_mfma_f32_16x16x32_bf16 v[92:95], a[232:235], v[40:43], v[92:95]\n"
"ds_read_b64_tr_b16 a[196:197], v204 offset:36864\n"
"v_accvgpr_write_b32 a81, v161\n"
"v_mfma_f32_16x16x32_bf16 v[92:95], a[236:239], v[44:47], v[92:95]\n"
"ds_read_b64_tr_b16 a[198:199], v204 offset:38912\n"
"v_accvgpr_write_b32 a82, v162\n"
"v_mfma_f32_16x16x32_bf16 v[92:95], a[240:243], v[48:51], v[92:95]\n"
"ds_read_b64_tr_b16 a[200:201], v204 offset:40960\n"
"v_cvt_pk_bf16_f32 v106, v88, v89\n"
"v_mfma_f32_16x16x32_bf16 v[92:95], a[244:247], v[52:55], v[92:95]\n"
"ds_read_b64_tr_b16 a[202:203], v204 offset:43008\n"
"v_cvt_pk_bf16_f32 v107, v90, v91\n"
"v_mfma_f32_16x16x32_bf16 v[92:95], a[248:251], v[56:59], v[92:95]\n"
"ds_read_b64_tr_b16 a[204:205], v204 offset:45056\n"
"v_accvgpr_write_b32 a83, v163\n"
"v_mfma_f32_16x16x32_bf16 v[92:95], a[252:255], v[60:63], v[92:95]\n"
"buffer_store_dwordx4 v[104:107], v207, s[28:31], s54 offen offset:3072 sc1\n"
"ds_read_b64_tr_b16 a[206:207], v204 offset:47104\n"
"v_mfma_f32_16x16x32_bf16 v[64:67], a[128:131], v[0:3], 0\n"
"s_waitcnt lgkmcnt(11)\n"
"ds_read_b64_tr_b16 a[208:209], v204 offset:49152\n"
"v_accvgpr_write_b32 a84, v164\n"
"v_mfma_f32_16x16x32_bf16 v[64:67], a[132:135], v[4:7], v[64:67]\n"
"ds_read_b64_tr_b16 a[210:211], v204 offset:51200\n"
"v_accvgpr_write_b32 a85, v165\n"
"v_mfma_f32_16x16x32_bf16 v[64:67], a[136:139], v[8:11], v[64:67]\n"
"ds_read_b64_tr_b16 a[212:213], v204 offset:53248\n"
"v_accvgpr_write_b32 a86, v166\n"
"v_mfma_f32_16x16x32_bf16 v[64:67], a[140:143], v[12:15], v[64:67]\n"
"ds_read_b64_tr_b16 a[214:215], v204 offset:55296\n"
"v_accvgpr_write_b32 a87, v167\n"
"v_mfma_f32_16x16x32_bf16 v[64:67], a[144:147], v[16:19], v[64:67]\n"
"s_waitcnt vmcnt(26)\n"
"s_barrier\n"
"ds_read_b64_tr_b16 a[216:217], v204 offset:57344\n"
"v_cvt_pk_bf16_f32 v110, v92, v93\n"
"v_mfma_f32_16x16x32_bf16 v[64:67], a[148:151], v[20:23], v[64:67]\n"
"s_waitcnt lgkmcnt(11)\n"
"s_add_u32 m0, s53, 65536\n"
"ds_read_b64_tr_b16 a[218:219], v204 offset:59392\n"
"v_cvt_pk_bf16_f32 v111, v94, v95\n"
"v_mfma_f32_16x16x32_bf16 v[64:67], a[152:155], v[24:27], v[64:67]\n"
"ds_read_b64_tr_b16 a[220:221], v204 offset:61440\n"
"buffer_load_dwordx4 v206, s[24:27], s52 offen offset:0 lds\n"
"v_mfma_f32_16x16x32_bf16 v[64:67], a[156:159], v[28:31], v[64:67]\n"
"s_add_u32 s52, s52, 8192\n"
"buffer_store_dwordx4 v[108:111], v208, s[28:31], s54 offen offset:3072 sc1\n"
"ds_read_b64_tr_b16 a[222:223], v204 offset:63488\n"
"v_mfma_f32_16x16x32_bf16 v[68:71], a[128:131], v[32:35], 0\n"
"s_add_u32 s54, s54, 4096\n"
"ds_read_b64_tr_b16 a[160:161], v201 offset:33024\n"
"v_accvgpr_write_b32 a88, v168\n"
"v_mfma_f32_16x16x32_bf16 v[68:71], a[132:135], v[36:39], v[68:71]\n"
"ds_read_b64_tr_b16 a[162:163], v201 offset:35072\n"
"v_accvgpr_write_b32 a89, v169\n"
"v_mfma_f32_16x16x32_bf16 v[68:71], a[136:139], v[40:43], v[68:71]\n"
"ds_read_b64_tr_b16 a[164:165], v201 offset:37120\n"
"buffer_load_dwordx4 v206, s[24:27], s52 offen offset:1024 lds\n"
"v_mfma_f32_16x16x32_bf16 v[68:71], a[140:143], v[44:47], v[68:71]\n"
"s_add_u32 s52, s52, 8192\n"
"ds_read_b64_tr_b16 a[166:167], v201 offset:39168\n"
"v_accvgpr_write_b32 a90, v170\n"
"v_mfma_f32_16x16x32_bf16 v[68:71], a[144:147], v[48:51], v[68:71]\n"
"ds_read_b64_tr_b16 a[168:169], v201 offset:41216\n"
"v_cvt_pk_bf16_f32 v96, v64, v65\n"
"v_mfma_f32_16x16x32_bf16 v[68:71], a[148:151], v[52:55], v[68:71]\n"
"ds_read_b64_tr_b16 a[170:171], v201 offset:43264\n"
"v_cvt_pk_bf16_f32 v97, v66, v67\n"
"v_mfma_f32_16x16x32_bf16 v[68:71], a[152:155], v[56:59], v[68:71]\n"
"ds_read_b64_tr_b16 a[172:173], v201 offset:45312\n"
"buffer_load_dwordx4 v206, s[24:27], s52 offen offset:2048 lds\n"
"v_mfma_f32_16x16x32_bf16 v[68:71], a[156:159], v[60:63], v[68:71]\n"
"s_add_u32 s52, s52, 8192\n"
"ds_read_b64_tr_b16 a[174:175], v201 offset:47360\n"
"v_accvgpr_write_b32 a91, v171\n"
"v_mfma_f32_16x16x32_bf16 v[72:75], a[192:195], v[0:3], 0\n"
"s_waitcnt lgkmcnt(11)\n"
"ds_read_b64_tr_b16 a[176:177], v201 offset:49408\n"
"v_accvgpr_write_b32 a92, v172\n"
"v_mfma_f32_16x16x32_bf16 v[72:75], a[196:199], v[4:7], v[72:75]\n"
"ds_read_b64_tr_b16 a[178:179], v201 offset:51456\n"
"v_accvgpr_write_b32 a93, v173\n"
"v_mfma_f32_16x16x32_bf16 v[72:75], a[200:203], v[8:11], v[72:75]\n"
"ds_read_b64_tr_b16 a[180:181], v201 offset:53504\n"
"buffer_load_dwordx4 v206, s[24:27], s52 offen offset:3072 lds\n"
"v_mfma_f32_16x16x32_bf16 v[72:75], a[204:207], v[12:15], v[72:75]\n"
"s_add_u32 s52, s52, 12288\n"
"ds_read_b64_tr_b16 a[182:183], v201 offset:55552\n"
"v_accvgpr_write_b32 a94, v174\n"
"v_mfma_f32_16x16x32_bf16 v[72:75], a[208:211], v[16:19], v[72:75]\n"
"ds_read_b64_tr_b16 a[184:185], v201 offset:57600\n"
"v_cvt_pk_bf16_f32 v100, v68, v69\n"
"v_mfma_f32_16x16x32_bf16 v[72:75], a[212:215], v[20:23], v[72:75]\n"
"s_waitcnt lgkmcnt(11)\n"
"s_add_u32 m0, s53, 69632\n"
"ds_read_b64_tr_b16 a[186:187], v201 offset:59648\n"
"v_cvt_pk_bf16_f32 v101, v70, v71\n"
"v_mfma_f32_16x16x32_bf16 v[72:75], a[216:219], v[24:27], v[72:75]\n"
"ds_read_b64_tr_b16 a[188:189], v201 offset:61696\n"
"buffer_load_dwordx4 v206, s[24:27], s52 offen offset:0 lds\n"
"v_mfma_f32_16x16x32_bf16 v[72:75], a[220:223], v[28:31], v[72:75]\n"
"s_add_u32 s52, s52, 8192\n"
"ds_read_b64_tr_b16 a[190:191], v201 offset:63744\n"
"v_accvgpr_write_b32 a95, v175\n"
"v_mfma_f32_16x16x32_bf16 v[76:79], a[192:195], v[32:35], 0\n"
"ds_read_b64_tr_b16 a[224:225], v204 offset:33024\n"
"v_accvgpr_write_b32 a96, v176\n"
"v_mfma_f32_16x16x32_bf16 v[76:79], a[196:199], v[36:39], v[76:79]\n"
"ds_read_b64_tr_b16 a[226:227], v204 offset:35072\n"
"v_accvgpr_write_b32 a97, v177\n"
"v_mfma_f32_16x16x32_bf16 v[76:79], a[200:203], v[40:43], v[76:79]\n"
"ds_read_b64_tr_b16 a[228:229], v204 offset:37120\n"
"buffer_load_dwordx4 v206, s[24:27], s52 offen offset:1024 lds\n"
"v_mfma_f32_16x16x32_bf16 v[76:79], a[204:207], v[44:47], v[76:79]\n"
"s_add_u32 s52, s52, 8192\n"
"ds_read_b64_tr_b16 a[230:231], v204 offset:39168\n"
"v_accvgpr_write_b32 a98, v178\n"
"v_mfma_f32_16x16x32_bf16 v[76:79], a[208:211], v[48:51], v[76:79]\n"
"ds_read_b64_tr_b16 a[232:233], v204 offset:41216\n"
"v_cvt_pk_bf16_f32 v98, v72, v73\n"
"v_mfma_f32_16x16x32_bf16 v[76:79], a[212:215], v[52:55], v[76:79]\n"
"ds_read_b64_tr_b16 a[234:235], v204 offset:43264\n"
"v_cvt_pk_bf16_f32 v99, v74, v75\n"
"v_mfma_f32_16x16x32_bf16 v[76:79], a[216:219], v[56:59], v[76:79]\n"
"ds_read_b64_tr_b16 a[236:237], v204 offset:45312\n"
"buffer_load_dwordx4 v206, s[24:27], s52 offen offset:2048 lds\n"
"v_mfma_f32_16x16x32_bf16 v[76:79], a[220:223], v[60:63], v[76:79]\n"
"s_add_u32 s52, s52, 8192\n"
"ds_read_b64_tr_b16 a[238:239], v204 offset:47360\n"
"buffer_store_dwordx4 v[96:99], v207, s[28:31], s54 offen offset:0 sc1\n"
"v_mfma_f32_16x16x32_bf16 v[80:83], a[160:163], v[0:3], 0\n"
"s_waitcnt lgkmcnt(11)\n"
"ds_read_b64_tr_b16 a[240:241], v204 offset:49408\n"
"v_accvgpr_write_b32 a99, v179\n"
"v_mfma_f32_16x16x32_bf16 v[80:83], a[164:167], v[4:7], v[80:83]\n"
"ds_read_b64_tr_b16 a[242:243], v204 offset:51456\n"
"v_accvgpr_write_b32 a100, v180\n"
"v_mfma_f32_16x16x32_bf16 v[80:83], a[168:171], v[8:11], v[80:83]\n"
"ds_read_b64_tr_b16 a[244:245], v204 offset:53504\n"
"buffer_load_dwordx4 v206, s[24:27], s52 offen offset:3072 lds\n"
"v_mfma_f32_16x16x32_bf16 v[80:83], a[172:175], v[12:15], v[80:83]\n"
"s_sub_u32 s52, s52, 61312\n"
"ds_read_b64_tr_b16 a[246:247], v204 offset:55552\n"
"v_accvgpr_write_b32 a101, v181\n"
"v_mfma_f32_16x16x32_bf16 v[80:83], a[176:179], v[16:19], v[80:83]\n"
"ds_read_b64_tr_b16 a[248:249], v204 offset:57600\n"
"v_cvt_pk_bf16_f32 v102, v76, v77\n"
"v_mfma_f32_16x16x32_bf16 v[80:83], a[180:183], v[20:23], v[80:83]\n"
"s_waitcnt lgkmcnt(11)\n"
"ds_read_b64_tr_b16 a[250:251], v204 offset:59648\n"
"v_cvt_pk_bf16_f32 v103, v78, v79\n"
"v_mfma_f32_16x16x32_bf16 v[80:83], a[184:187], v[24:27], v[80:83]\n"
"ds_read_b64_tr_b16 a[252:253], v204 offset:61696\n"
"v_accvgpr_write_b32 a102, v182\n"
"v_mfma_f32_16x16x32_bf16 v[80:83], a[188:191], v[28:31], v[80:83]\n"
"ds_read_b64_tr_b16 a[254:255], v204 offset:63744\n"
"buffer_store_dwordx4 v[100:103], v208, s[28:31], s54 offen offset:0 sc1\n"
"v_mfma_f32_16x16x32_bf16 v[84:87], a[160:163], v[32:35], 0\n"
"ds_read_b64_tr_b16 a[128:129], v202 offset:0\n"
"v_accvgpr_write_b32 a103, v183\n"
"v_mfma_f32_16x16x32_bf16 v[84:87], a[164:167], v[36:39], v[84:87]\n"
"ds_read_b64_tr_b16 a[130:131], v202 offset:2048\n"
"v_accvgpr_write_b32 a104, v184\n"
"v_mfma_f32_16x16x32_bf16 v[84:87], a[168:171], v[40:43], v[84:87]\n"
"ds_read_b64_tr_b16 a[132:133], v202 offset:4096\n"
"v_accvgpr_write_b32 a105, v185\n"
"v_mfma_f32_16x16x32_bf16 v[84:87], a[172:175], v[44:47], v[84:87]\n"
"ds_read_b64_tr_b16 a[134:135], v202 offset:6144\n"
"v_accvgpr_write_b32 a106, v186\n"
"v_mfma_f32_16x16x32_bf16 v[84:87], a[176:179], v[48:51], v[84:87]\n"
"ds_read_b64_tr_b16 a[136:137], v202 offset:8192\n"
"v_cvt_pk_bf16_f32 v104, v80, v81\n"
"v_mfma_f32_16x16x32_bf16 v[84:87], a[180:183], v[52:55], v[84:87]\n"
"ds_read_b64_tr_b16 a[138:139], v202 offset:10240\n"
"v_cvt_pk_bf16_f32 v105, v82, v83\n"
"v_mfma_f32_16x16x32_bf16 v[84:87], a[184:187], v[56:59], v[84:87]\n"
"ds_read_b64_tr_b16 a[140:141], v202 offset:12288\n"
"v_accvgpr_write_b32 a107, v187\n"
"v_mfma_f32_16x16x32_bf16 v[84:87], a[188:191], v[60:63], v[84:87]\n"
"ds_read_b64_tr_b16 a[142:143], v202 offset:14336\n"
"v_accvgpr_write_b32 a108, v188\n"
"v_mfma_f32_16x16x32_bf16 v[88:91], a[224:227], v[0:3], 0\n"
"s_waitcnt lgkmcnt(11)\n"
"ds_read_b64_tr_b16 a[144:145], v202 offset:16384\n"
"v_accvgpr_write_b32 a109, v189\n"
"v_mfma_f32_16x16x32_bf16 v[88:91], a[228:231], v[4:7], v[88:91]\n"
"ds_read_b64_tr_b16 a[146:147], v202 offset:18432\n"
"v_accvgpr_write_b32 a110, v190\n"
"v_mfma_f32_16x16x32_bf16 v[88:91], a[232:235], v[8:11], v[88:91]\n"
"ds_read_b64_tr_b16 a[148:149], v202 offset:20480\n"
"v_accvgpr_write_b32 a111, v191\n"
"v_mfma_f32_16x16x32_bf16 v[88:91], a[236:239], v[12:15], v[88:91]\n"
"ds_read_b64_tr_b16 a[150:151], v202 offset:22528\n"
"v_accvgpr_write_b32 a112, v192\n"
"v_mfma_f32_16x16x32_bf16 v[88:91], a[240:243], v[16:19], v[88:91]\n"
"ds_read_b64_tr_b16 a[152:153], v202 offset:24576\n"
"v_cvt_pk_bf16_f32 v108, v84, v85\n"
"v_mfma_f32_16x16x32_bf16 v[88:91], a[244:247], v[20:23], v[88:91]\n"
"s_waitcnt lgkmcnt(11)\n"
"ds_read_b64_tr_b16 a[154:155], v202 offset:26624\n"
"v_cvt_pk_bf16_f32 v109, v86, v87\n"
"v_mfma_f32_16x16x32_bf16 v[88:91], a[248:251], v[24:27], v[88:91]\n"
"ds_read_b64_tr_b16 a[156:157], v202 offset:28672\n"
"v_accvgpr_write_b32 a113, v193\n"
"v_mfma_f32_16x16x32_bf16 v[88:91], a[252:255], v[28:31], v[88:91]\n"
"ds_read_b64_tr_b16 a[158:159], v202 offset:30720\n"
"v_accvgpr_write_b32 a114, v194\n"
"v_mfma_f32_16x16x32_bf16 v[92:95], a[224:227], v[32:35], 0\n"
"ds_read_b64_tr_b16 a[192:193], v205 offset:0\n"
"v_accvgpr_write_b32 a115, v195\n"
"v_mfma_f32_16x16x32_bf16 v[92:95], a[228:231], v[36:39], v[92:95]\n"
"ds_read_b64_tr_b16 a[194:195], v205 offset:2048\n"
"v_accvgpr_write_b32 a116, v196\n"
"v_mfma_f32_16x16x32_bf16 v[92:95], a[232:235], v[40:43], v[92:95]\n"
"ds_read_b64_tr_b16 a[196:197], v205 offset:4096\n"
"v_accvgpr_write_b32 a117, v197\n"
"v_mfma_f32_16x16x32_bf16 v[92:95], a[236:239], v[44:47], v[92:95]\n"
"ds_read_b64_tr_b16 a[198:199], v205 offset:6144\n"
"v_accvgpr_write_b32 a118, v198\n"
"v_mfma_f32_16x16x32_bf16 v[92:95], a[240:243], v[48:51], v[92:95]\n"
"ds_read_b64_tr_b16 a[200:201], v205 offset:8192\n"
"v_cvt_pk_bf16_f32 v106, v88, v89\n"
"v_mfma_f32_16x16x32_bf16 v[92:95], a[244:247], v[52:55], v[92:95]\n"
"ds_read_b64_tr_b16 a[202:203], v205 offset:10240\n"
"v_cvt_pk_bf16_f32 v107, v90, v91\n"
"v_mfma_f32_16x16x32_bf16 v[92:95], a[248:251], v[56:59], v[92:95]\n"
"ds_read_b64_tr_b16 a[204:205], v205 offset:12288\n"
"v_accvgpr_write_b32 a119, v199\n"
"v_mfma_f32_16x16x32_bf16 v[92:95], a[252:255], v[60:63], v[92:95]\n"
"buffer_store_dwordx4 v[104:107], v207, s[28:31], s54 offen offset:1024 sc1\n"
"ds_read_b64_tr_b16 a[206:207], v205 offset:14336\n"
"v_mfma_f32_16x16x32_bf16 v[64:67], a[128:131], v[0:3], 0\n"
"s_waitcnt lgkmcnt(11)\n"
"ds_read_b64_tr_b16 a[208:209], v205 offset:16384\n"
"v_mfma_f32_16x16x32_bf16 v[64:67], a[132:135], v[4:7], v[64:67]\n"
"ds_read_b64_tr_b16 a[210:211], v205 offset:18432\n"
"buffer_load_dwordx2 a[0:1], v254, s[72:75], s50 offen offset:0\n"
"v_mfma_f32_16x16x32_bf16 v[64:67], a[136:139], v[8:11], v[64:67]\n"
"ds_read_b64_tr_b16 a[212:213], v205 offset:20480\n"
"v_mfma_f32_16x16x32_bf16 v[64:67], a[140:143], v[12:15], v[64:67]\n"
"ds_read_b64_tr_b16 a[214:215], v205 offset:22528\n"
"v_mfma_f32_16x16x32_bf16 v[64:67], a[144:147], v[16:19], v[64:67]\n"
"s_waitcnt vmcnt(27)\n"
"s_barrier\n"
"ds_read_b64_tr_b16 a[216:217], v205 offset:24576\n"
"v_cvt_pk_bf16_f32 v110, v92, v93\n"
"v_mfma_f32_16x16x32_bf16 v[64:67], a[148:151], v[20:23], v[64:67]\n"
"s_waitcnt lgkmcnt(11)\n"
"ds_read_b64_tr_b16 a[218:219], v205 offset:26624\n"
"v_cvt_pk_bf16_f32 v111, v94, v95\n"
"v_mfma_f32_16x16x32_bf16 v[64:67], a[152:155], v[24:27], v[64:67]\n"
"ds_read_b64_tr_b16 a[220:221], v205 offset:28672\n"
"v_mfma_f32_16x16x32_bf16 v[64:67], a[156:159], v[28:31], v[64:67]\n"
"buffer_store_dwordx4 v[108:111], v208, s[28:31], s54 offen offset:1024 sc1\n"
"ds_read_b64_tr_b16 a[222:223], v205 offset:30720\n"
"v_mfma_f32_16x16x32_bf16 v[68:71], a[128:131], v[32:35], 0\n"
"ds_read_b64_tr_b16 a[160:161], v202 offset:256\n"
"v_mfma_f32_16x16x32_bf16 v[68:71], a[132:135], v[36:39], v[68:71]\n"
"ds_read_b64_tr_b16 a[162:163], v202 offset:2304\n"
"buffer_load_dwordx2 a[2:3], v254, s[72:75], s50 offen offset:1152\n"
"v_mfma_f32_16x16x32_bf16 v[68:71], a[136:139], v[40:43], v[68:71]\n"
"ds_read_b64_tr_b16 a[164:165], v202 offset:4352\n"
"v_mfma_f32_16x16x32_bf16 v[68:71], a[140:143], v[44:47], v[68:71]\n"
"ds_read_b64_tr_b16 a[166:167], v202 offset:6400\n"
"v_mfma_f32_16x16x32_bf16 v[68:71], a[144:147], v[48:51], v[68:71]\n"
"ds_read_b64_tr_b16 a[168:169], v202 offset:8448\n"
"v_cvt_pk_bf16_f32 v96, v64, v65\n"
"v_mfma_f32_16x16x32_bf16 v[68:71], a[148:151], v[52:55], v[68:71]\n"
"ds_read_b64_tr_b16 a[170:171], v202 offset:10496\n"
"v_cvt_pk_bf16_f32 v97, v66, v67\n"
"v_mfma_f32_16x16x32_bf16 v[68:71], a[152:155], v[56:59], v[68:71]\n"
"ds_read_b64_tr_b16 a[172:173], v202 offset:12544\n"
"buffer_load_dwordx2 a[4:5], v254, s[72:75], s50 offen offset:2304\n"
"v_mfma_f32_16x16x32_bf16 v[68:71], a[156:159], v[60:63], v[68:71]\n"
"ds_read_b64_tr_b16 a[174:175], v202 offset:14592\n"
"v_mfma_f32_16x16x32_bf16 v[72:75], a[192:195], v[0:3], 0\n"
"s_waitcnt lgkmcnt(11)\n"
"ds_read_b64_tr_b16 a[176:177], v202 offset:16640\n"
"v_mfma_f32_16x16x32_bf16 v[72:75], a[196:199], v[4:7], v[72:75]\n"
"ds_read_b64_tr_b16 a[178:179], v202 offset:18688\n"
"v_mfma_f32_16x16x32_bf16 v[72:75], a[200:203], v[8:11], v[72:75]\n"
"s_add_u32 s50, s50, 3456\n"
"ds_read_b64_tr_b16 a[180:181], v202 offset:20736\n"
"v_mfma_f32_16x16x32_bf16 v[72:75], a[204:207], v[12:15], v[72:75]\n"
"ds_read_b64_tr_b16 a[182:183], v202 offset:22784\n"
"buffer_load_dwordx2 a[6:7], v254, s[72:75], s50 offen offset:0\n"
"v_mfma_f32_16x16x32_bf16 v[72:75], a[208:211], v[16:19], v[72:75]\n"
"ds_read_b64_tr_b16 a[184:185], v202 offset:24832\n"
"v_cvt_pk_bf16_f32 v100, v68, v69\n"
"v_mfma_f32_16x16x32_bf16 v[72:75], a[212:215], v[20:23], v[72:75]\n"
"s_waitcnt lgkmcnt(11)\n"
"ds_read_b64_tr_b16 a[186:187], v202 offset:26880\n"
"v_cvt_pk_bf16_f32 v101, v70, v71\n"
"v_mfma_f32_16x16x32_bf16 v[72:75], a[216:219], v[24:27], v[72:75]\n"
"ds_read_b64_tr_b16 a[188:189], v202 offset:28928\n"
"v_mfma_f32_16x16x32_bf16 v[72:75], a[220:223], v[28:31], v[72:75]\n"
"ds_read_b64_tr_b16 a[190:191], v202 offset:30976\n"
"buffer_load_dwordx2 a[8:9], v254, s[72:75], s50 offen offset:1152\n"
"v_mfma_f32_16x16x32_bf16 v[76:79], a[192:195], v[32:35], 0\n"
"ds_read_b64_tr_b16 a[224:225], v205 offset:256\n"
"v_mfma_f32_16x16x32_bf16 v[76:79], a[196:199], v[36:39], v[76:79]\n"
"ds_read_b64_tr_b16 a[226:227], v205 offset:2304\n"
"v_mfma_f32_16x16x32_bf16 v[76:79], a[200:203], v[40:43], v[76:79]\n"
"ds_read_b64_tr_b16 a[228:229], v205 offset:4352\n"
"v_mfma_f32_16x16x32_bf16 v[76:79], a[204:207], v[44:47], v[76:79]\n"
"ds_read_b64_tr_b16 a[230:231], v205 offset:6400\n"
"buffer_load_dwordx2 a[10:11], v254, s[72:75], s50 offen offset:2304\n"
"v_mfma_f32_16x16x32_bf16 v[76:79], a[208:211], v[48:51], v[76:79]\n"
"ds_read_b64_tr_b16 a[232:233], v205 offset:8448\n"
"v_cvt_pk_bf16_f32 v98, v72, v73\n"
"v_mfma_f32_16x16x32_bf16 v[76:79], a[212:215], v[52:55], v[76:79]\n"
"ds_read_b64_tr_b16 a[234:235], v205 offset:10496\n"
"v_cvt_pk_bf16_f32 v99, v74, v75\n"
"v_mfma_f32_16x16x32_bf16 v[76:79], a[216:219], v[56:59], v[76:79]\n"
"ds_read_b64_tr_b16 a[236:237], v205 offset:12544\n"
"v_mfma_f32_16x16x32_bf16 v[76:79], a[220:223], v[60:63], v[76:79]\n"
"s_add_u32 s50, s50, 3456\n"
"ds_read_b64_tr_b16 a[238:239], v205 offset:14592\n"
"buffer_store_dwordx4 v[96:99], v207, s[28:31], s54 offen offset:2048 sc1\n"
"v_mfma_f32_16x16x32_bf16 v[80:83], a[160:163], v[0:3], 0\n"
"s_waitcnt lgkmcnt(11)\n"
"ds_read_b64_tr_b16 a[240:241], v205 offset:16640\n"
"v_mfma_f32_16x16x32_bf16 v[80:83], a[164:167], v[4:7], v[80:83]\n"
"ds_read_b64_tr_b16 a[242:243], v205 offset:18688\n"
"buffer_load_dwordx2 a[12:13], v254, s[72:75], s50 offen offset:0\n"
"v_mfma_f32_16x16x32_bf16 v[80:83], a[168:171], v[8:11], v[80:83]\n"
"ds_read_b64_tr_b16 a[244:245], v205 offset:20736\n"
"v_mfma_f32_16x16x32_bf16 v[80:83], a[172:175], v[12:15], v[80:83]\n"
"ds_read_b64_tr_b16 a[246:247], v205 offset:22784\n"
"v_mfma_f32_16x16x32_bf16 v[80:83], a[176:179], v[16:19], v[80:83]\n"
"ds_read_b64_tr_b16 a[248:249], v205 offset:24832\n"
"v_cvt_pk_bf16_f32 v102, v76, v77\n"
"v_mfma_f32_16x16x32_bf16 v[80:83], a[180:183], v[20:23], v[80:83]\n"
"s_waitcnt lgkmcnt(11)\n"
"ds_read_b64_tr_b16 a[250:251], v205 offset:26880\n"
"v_cvt_pk_bf16_f32 v103, v78, v79\n"
"v_mfma_f32_16x16x32_bf16 v[80:83], a[184:187], v[24:27], v[80:83]\n"
"ds_read_b64_tr_b16 a[252:253], v205 offset:28928\n"
"v_mfma_f32_16x16x32_bf16 v[80:83], a[188:191], v[28:31], v[80:83]\n"
"ds_read_b64_tr_b16 a[254:255], v205 offset:30976\n"
"buffer_store_dwordx4 v[100:103], v208, s[28:31], s54 offen offset:2048 sc1\n"
"v_mfma_f32_16x16x32_bf16 v[84:87], a[160:163], v[32:35], 0\n"
"ds_read_b64_tr_b16 a[128:129], v200 offset:0\n"
"v_mfma_f32_16x16x32_bf16 v[84:87], a[164:167], v[36:39], v[84:87]\n"
"ds_read_b64_tr_b16 a[130:131], v200 offset:2048\n"
"buffer_load_dwordx2 a[14:15], v254, s[72:75], s50 offen offset:1152\n"
"v_mfma_f32_16x16x32_bf16 v[84:87], a[168:171], v[40:43], v[84:87]\n"
"ds_read_b64_tr_b16 a[132:133], v200 offset:4096\n"
"v_mfma_f32_16x16x32_bf16 v[84:87], a[172:175], v[44:47], v[84:87]\n"
"ds_read_b64_tr_b16 a[134:135], v200 offset:6144\n"
"v_mfma_f32_16x16x32_bf16 v[84:87], a[176:179], v[48:51], v[84:87]\n"
"ds_read_b64_tr_b16 a[136:137], v200 offset:8192\n"
"v_cvt_pk_bf16_f32 v104, v80, v81\n"
"v_mfma_f32_16x16x32_bf16 v[84:87], a[180:183], v[52:55], v[84:87]\n"
"ds_read_b64_tr_b16 a[138:139], v200 offset:10240\n"
"v_cvt_pk_bf16_f32 v105, v82, v83\n"
"v_mfma_f32_16x16x32_bf16 v[84:87], a[184:187], v[56:59], v[84:87]\n"
"ds_read_b64_tr_b16 a[140:141], v200 offset:12288\n"
"buffer_load_dwordx2 a[16:17], v254, s[72:75], s50 offen offset:2304\n"
"v_mfma_f32_16x16x32_bf16 v[84:87], a[188:191], v[60:63], v[84:87]\n"
"ds_read_b64_tr_b16 a[142:143], v200 offset:14336\n"
"v_mfma_f32_16x16x32_bf16 v[88:91], a[224:227], v[0:3], 0\n"
"s_waitcnt lgkmcnt(11)\n"
"ds_read_b64_tr_b16 a[144:145], v200 offset:16384\n"
"v_mfma_f32_16x16x32_bf16 v[88:91], a[228:231], v[4:7], v[88:91]\n"
"ds_read_b64_tr_b16 a[146:147], v200 offset:18432\n"
"v_mfma_f32_16x16x32_bf16 v[88:91], a[232:235], v[8:11], v[88:91]\n"
"s_add_u32 s50, s50, 3456\n"
"ds_read_b64_tr_b16 a[148:149], v200 offset:20480\n"
"v_mfma_f32_16x16x32_bf16 v[88:91], a[236:239], v[12:15], v[88:91]\n"
"ds_read_b64_tr_b16 a[150:151], v200 offset:22528\n"
"buffer_load_dwordx2 a[18:19], v254, s[72:75], s50 offen offset:0\n"
"v_mfma_f32_16x16x32_bf16 v[88:91], a[240:243], v[16:19], v[88:91]\n"
"ds_read_b64_tr_b16 a[152:153], v200 offset:24576\n"
"v_cvt_pk_bf16_f32 v108, v84, v85\n"
"v_mfma_f32_16x16x32_bf16 v[88:91], a[244:247], v[20:23], v[88:91]\n"
"s_waitcnt lgkmcnt(11)\n"
"ds_read_b64_tr_b16 a[154:155], v200 offset:26624\n"
"v_cvt_pk_bf16_f32 v109, v86, v87\n"
"v_mfma_f32_16x16x32_bf16 v[88:91], a[248:251], v[24:27], v[88:91]\n"
"ds_read_b64_tr_b16 a[156:157], v200 offset:28672\n"
"v_mfma_f32_16x16x32_bf16 v[88:91], a[252:255], v[28:31], v[88:91]\n"
"ds_read_b64_tr_b16 a[158:159], v200 offset:30720\n"
"buffer_load_dwordx2 a[20:21], v254, s[72:75], s50 offen offset:1152\n"
"v_mfma_f32_16x16x32_bf16 v[92:95], a[224:227], v[32:35], 0\n"
"ds_read_b64_tr_b16 a[192:193], v203 offset:0\n"
"v_mfma_f32_16x16x32_bf16 v[92:95], a[228:231], v[36:39], v[92:95]\n"
"ds_read_b64_tr_b16 a[194:195], v203 offset:2048\n"
"v_mfma_f32_16x16x32_bf16 v[92:95], a[232:235], v[40:43], v[92:95]\n"
"ds_read_b64_tr_b16 a[196:197], v203 offset:4096\n"
"v_mfma_f32_16x16x32_bf16 v[92:95], a[236:239], v[44:47], v[92:95]\n"
"ds_read_b64_tr_b16 a[198:199], v203 offset:6144\n"
"buffer_load_dwordx2 a[22:23], v254, s[72:75], s50 offen offset:2304\n"
"v_mfma_f32_16x16x32_bf16 v[92:95], a[240:243], v[48:51], v[92:95]\n"
"ds_read_b64_tr_b16 a[200:201], v203 offset:8192\n"
"v_cvt_pk_bf16_f32 v106, v88, v89\n"
"v_mfma_f32_16x16x32_bf16 v[92:95], a[244:247], v[52:55], v[92:95]\n"
"ds_read_b64_tr_b16 a[202:203], v203 offset:10240\n"
"v_cvt_pk_bf16_f32 v107, v90, v91\n"
"v_mfma_f32_16x16x32_bf16 v[92:95], a[248:251], v[56:59], v[92:95]\n"
"ds_read_b64_tr_b16 a[204:205], v203 offset:12288\n"
"v_mfma_f32_16x16x32_bf16 v[92:95], a[252:255], v[60:63], v[92:95]\n"
"s_add_u32 s50, s50, 3456\n"
"buffer_store_dwordx4 v[104:107], v207, s[28:31], s54 offen offset:3072 sc1\n"
"ds_read_b64_tr_b16 a[206:207], v203 offset:14336\n"
"v_mfma_f32_16x16x32_bf16 v[64:67], a[128:131], v[0:3], 0\n"
"s_waitcnt lgkmcnt(11)\n"
"ds_read_b64_tr_b16 a[208:209], v203 offset:16384\n"
"v_mfma_f32_16x16x32_bf16 v[64:67], a[132:135], v[4:7], v[64:67]\n"
"ds_read_b64_tr_b16 a[210:211], v203 offset:18432\n"
"buffer_load_dwordx2 a[24:25], v254, s[72:75], s50 offen offset:0\n"
"v_mfma_f32_16x16x32_bf16 v[64:67], a[136:139], v[8:11], v[64:67]\n"
"ds_read_b64_tr_b16 a[212:213], v203 offset:20480\n"
"v_mfma_f32_16x16x32_bf16 v[64:67], a[140:143], v[12:15], v[64:67]\n"
"ds_read_b64_tr_b16 a[214:215], v203 offset:22528\n"
"v_mfma_f32_16x16x32_bf16 v[64:67], a[144:147], v[16:19], v[64:67]\n"
"s_waitcnt vmcnt(31)\n"
"s_barrier\n"
"ds_read_b64_tr_b16 a[216:217], v203 offset:24576\n"
"v_cvt_pk_bf16_f32 v110, v92, v93\n"
"v_mfma_f32_16x16x32_bf16 v[64:67], a[148:151], v[20:23], v[64:67]\n"
"s_waitcnt lgkmcnt(11)\n"
"ds_read_b64_tr_b16 a[218:219], v203 offset:26624\n"
"v_cvt_pk_bf16_f32 v111, v94, v95\n"
"v_mfma_f32_16x16x32_bf16 v[64:67], a[152:155], v[24:27], v[64:67]\n"
"ds_read_b64_tr_b16 a[220:221], v203 offset:28672\n"
"v_mfma_f32_16x16x32_bf16 v[64:67], a[156:159], v[28:31], v[64:67]\n"
"buffer_store_dwordx4 v[108:111], v208, s[28:31], s54 offen offset:3072 sc1\n"
"ds_read_b64_tr_b16 a[222:223], v203 offset:30720\n"
"v_mfma_f32_16x16x32_bf16 v[68:71], a[128:131], v[32:35], 0\n"
"s_add_u32 s54, s54, 4096\n"
"ds_read_b64_tr_b16 a[160:161], v200 offset:256\n"
"v_mfma_f32_16x16x32_bf16 v[68:71], a[132:135], v[36:39], v[68:71]\n"
"ds_read_b64_tr_b16 a[162:163], v200 offset:2304\n"
"buffer_load_dwordx2 a[26:27], v254, s[72:75], s50 offen offset:1152\n"
"v_mfma_f32_16x16x32_bf16 v[68:71], a[136:139], v[40:43], v[68:71]\n"
"ds_read_b64_tr_b16 a[164:165], v200 offset:4352\n"
"v_mfma_f32_16x16x32_bf16 v[68:71], a[140:143], v[44:47], v[68:71]\n"
"ds_read_b64_tr_b16 a[166:167], v200 offset:6400\n"
"v_mfma_f32_16x16x32_bf16 v[68:71], a[144:147], v[48:51], v[68:71]\n"
"ds_read_b64_tr_b16 a[168:169], v200 offset:8448\n"
"v_cvt_pk_bf16_f32 v96, v64, v65\n"
"v_mfma_f32_16x16x32_bf16 v[68:71], a[148:151], v[52:55], v[68:71]\n"
"ds_read_b64_tr_b16 a[170:171], v200 offset:10496\n"
"v_cvt_pk_bf16_f32 v97, v66, v67\n"
"v_mfma_f32_16x16x32_bf16 v[68:71], a[152:155], v[56:59], v[68:71]\n"
"ds_read_b64_tr_b16 a[172:173], v200 offset:12544\n"
"buffer_load_dwordx2 a[28:29], v254, s[72:75], s50 offen offset:2304\n"
"v_mfma_f32_16x16x32_bf16 v[68:71], a[156:159], v[60:63], v[68:71]\n"
"ds_read_b64_tr_b16 a[174:175], v200 offset:14592\n"
"v_mfma_f32_16x16x32_bf16 v[72:75], a[192:195], v[0:3], 0\n"
"s_waitcnt lgkmcnt(11)\n"
"ds_read_b64_tr_b16 a[176:177], v200 offset:16640\n"
"v_mfma_f32_16x16x32_bf16 v[72:75], a[196:199], v[4:7], v[72:75]\n"
"ds_read_b64_tr_b16 a[178:179], v200 offset:18688\n"
"v_mfma_f32_16x16x32_bf16 v[72:75], a[200:203], v[8:11], v[72:75]\n"
"s_add_u32 s50, s50, 3456\n"
"ds_read_b64_tr_b16 a[180:181], v200 offset:20736\n"
"v_mfma_f32_16x16x32_bf16 v[72:75], a[204:207], v[12:15], v[72:75]\n"
"ds_read_b64_tr_b16 a[182:183], v200 offset:22784\n"
"buffer_load_dwordx2 a[30:31], v254, s[72:75], s50 offen offset:0\n"
"v_mfma_f32_16x16x32_bf16 v[72:75], a[208:211], v[16:19], v[72:75]\n"
"ds_read_b64_tr_b16 a[184:185], v200 offset:24832\n"
"v_cvt_pk_bf16_f32 v100, v68, v69\n"
"v_mfma_f32_16x16x32_bf16 v[72:75], a[212:215], v[20:23], v[72:75]\n"
"s_waitcnt lgkmcnt(11)\n"
"ds_read_b64_tr_b16 a[186:187], v200 offset:26880\n"
"v_cvt_pk_bf16_f32 v101, v70, v71\n"
"v_mfma_f32_16x16x32_bf16 v[72:75], a[216:219], v[24:27], v[72:75]\n"
"ds_read_b64_tr_b16 a[188:189], v200 offset:28928\n"
"v_mfma_f32_16x16x32_bf16 v[72:75], a[220:223], v[28:31], v[72:75]\n"
"ds_read_b64_tr_b16 a[190:191], v200 offset:30976\n"
"buffer_load_dwordx2 a[32:33], v254, s[72:75], s50 offen offset:1152\n"
"v_mfma_f32_16x16x32_bf16 v[76:79], a[192:195], v[32:35], 0\n"
"ds_read_b64_tr_b16 a[224:225], v203 offset:256\n"
"v_mfma_f32_16x16x32_bf16 v[76:79], a[196:199], v[36:39], v[76:79]\n"
"ds_read_b64_tr_b16 a[226:227], v203 offset:2304\n"
"v_mfma_f32_16x16x32_bf16 v[76:79], a[200:203], v[40:43], v[76:79]\n"
"ds_read_b64_tr_b16 a[228:229], v203 offset:4352\n"
"v_mfma_f32_16x16x32_bf16 v[76:79], a[204:207], v[44:47], v[76:79]\n"
"ds_read_b64_tr_b16 a[230:231], v203 offset:6400\n"
"buffer_load_dwordx2 a[34:35], v254, s[72:75], s50 offen offset:2304\n"
"v_mfma_f32_16x16x32_bf16 v[76:79], a[208:211], v[48:51], v[76:79]\n"
"ds_read_b64_tr_b16 a[232:233], v203 offset:8448\n"
"v_cvt_pk_bf16_f32 v98, v72, v73\n"
"v_mfma_f32_16x16x32_bf16 v[76:79], a[212:215], v[52:55], v[76:79]\n"
"ds_read_b64_tr_b16 a[234:235], v203 offset:10496\n"
"v_cvt_pk_bf16_f32 v99, v74, v75\n"
"v_mfma_f32_16x16x32_bf16 v[76:79], a[216:219], v[56:59], v[76:79]\n"
"ds_read_b64_tr_b16 a[236:237], v203 offset:12544\n"
"v_mfma_f32_16x16x32_bf16 v[76:79], a[220:223], v[60:63], v[76:79]\n"
"s_add_u32 s50, s50, 3456\n"
"ds_read_b64_tr_b16 a[238:239], v203 offset:14592\n"
"buffer_store_dwordx4 v[96:99], v207, s[28:31], s54 offen offset:0 sc1\n"
"v_mfma_f32_16x16x32_bf16 v[80:83], a[160:163], v[0:3], 0\n"
"s_waitcnt lgkmcnt(11)\n"
"ds_read_b64_tr_b16 a[240:241], v203 offset:16640\n"
"v_mfma_f32_16x16x32_bf16 v[80:83], a[164:167], v[4:7], v[80:83]\n"
"ds_read_b64_tr_b16 a[242:243], v203 offset:18688\n"
"buffer_load_dwordx2 a[36:37], v254, s[72:75], s50 offen offset:0\n"
"v_mfma_f32_16x16x32_bf16 v[80:83], a[168:171], v[8:11], v[80:83]\n"
"ds_read_b64_tr_b16 a[244:245], v203 offset:20736\n"
"v_mfma_f32_16x16x32_bf16 v[80:83], a[172:175], v[12:15], v[80:83]\n"
"ds_read_b64_tr_b16 a[246:247], v203 offset:22784\n"
"v_mfma_f32_16x16x32_bf16 v[80:83], a[176:179], v[16:19], v[80:83]\n"
"ds_read_b64_tr_b16 a[248:249], v203 offset:24832\n"
"v_cvt_pk_bf16_f32 v102, v76, v77\n"
"v_mfma_f32_16x16x32_bf16 v[80:83], a[180:183], v[20:23], v[80:83]\n"
"s_waitcnt lgkmcnt(11)\n"
"ds_read_b64_tr_b16 a[250:251], v203 offset:26880\n"
"v_cvt_pk_bf16_f32 v103, v78, v79\n"
"v_mfma_f32_16x16x32_bf16 v[80:83], a[184:187], v[24:27], v[80:83]\n"
"ds_read_b64_tr_b16 a[252:253], v203 offset:28928\n"
"v_mfma_f32_16x16x32_bf16 v[80:83], a[188:191], v[28:31], v[80:83]\n"
"ds_read_b64_tr_b16 a[254:255], v203 offset:30976\n"
"buffer_store_dwordx4 v[100:103], v208, s[28:31], s54 offen offset:0 sc1\n"
"v_mfma_f32_16x16x32_bf16 v[84:87], a[160:163], v[32:35], 0\n"
"ds_read_b64_tr_b16 a[128:129], v200 offset:32768\n"
"v_mfma_f32_16x16x32_bf16 v[84:87], a[164:167], v[36:39], v[84:87]\n"
"ds_read_b64_tr_b16 a[130:131], v200 offset:34816\n"
"buffer_load_dwordx2 a[38:39], v254, s[72:75], s50 offen offset:1152\n"
"v_mfma_f32_16x16x32_bf16 v[84:87], a[168:171], v[40:43], v[84:87]\n"
"ds_read_b64_tr_b16 a[132:133], v200 offset:36864\n"
"v_mfma_f32_16x16x32_bf16 v[84:87], a[172:175], v[44:47], v[84:87]\n"
"ds_read_b64_tr_b16 a[134:135], v200 offset:38912\n"
"v_mfma_f32_16x16x32_bf16 v[84:87], a[176:179], v[48:51], v[84:87]\n"
"ds_read_b64_tr_b16 a[136:137], v200 offset:40960\n"
"v_cvt_pk_bf16_f32 v104, v80, v81\n"
"v_mfma_f32_16x16x32_bf16 v[84:87], a[180:183], v[52:55], v[84:87]\n"
"ds_read_b64_tr_b16 a[138:139], v200 offset:43008\n"
"v_cvt_pk_bf16_f32 v105, v82, v83\n"
"v_mfma_f32_16x16x32_bf16 v[84:87], a[184:187], v[56:59], v[84:87]\n"
"ds_read_b64_tr_b16 a[140:141], v200 offset:45056\n"
"buffer_load_dwordx2 a[40:41], v254, s[72:75], s50 offen offset:2304\n"
"v_mfma_f32_16x16x32_bf16 v[84:87], a[188:191], v[60:63], v[84:87]\n"
"ds_read_b64_tr_b16 a[142:143], v200 offset:47104\n"
"v_mfma_f32_16x16x32_bf16 v[88:91], a[224:227], v[0:3], 0\n"
"s_waitcnt lgkmcnt(11)\n"
"ds_read_b64_tr_b16 a[144:145], v200 offset:49152\n"
"v_mfma_f32_16x16x32_bf16 v[88:91], a[228:231], v[4:7], v[88:91]\n"
"ds_read_b64_tr_b16 a[146:147], v200 offset:51200\n"
"v_mfma_f32_16x16x32_bf16 v[88:91], a[232:235], v[8:11], v[88:91]\n"
"s_add_u32 s50, s50, 3456\n"
"ds_read_b64_tr_b16 a[148:149], v200 offset:53248\n"
"v_mfma_f32_16x16x32_bf16 v[88:91], a[236:239], v[12:15], v[88:91]\n"
"ds_read_b64_tr_b16 a[150:151], v200 offset:55296\n"
"buffer_load_dwordx2 a[42:43], v254, s[72:75], s50 offen offset:0\n"
"v_mfma_f32_16x16x32_bf16 v[88:91], a[240:243], v[16:19], v[88:91]\n"
"ds_read_b64_tr_b16 a[152:153], v200 offset:57344\n"
"v_cvt_pk_bf16_f32 v108, v84, v85\n"
"v_mfma_f32_16x16x32_bf16 v[88:91], a[244:247], v[20:23], v[88:91]\n"
"s_waitcnt lgkmcnt(11)\n"
"ds_read_b64_tr_b16 a[154:155], v200 offset:59392\n"
"v_cvt_pk_bf16_f32 v109, v86, v87\n"
"v_mfma_f32_16x16x32_bf16 v[88:91], a[248:251], v[24:27], v[88:91]\n"
"ds_read_b64_tr_b16 a[156:157], v200 offset:61440\n"
"v_mfma_f32_16x16x32_bf16 v[88:91], a[252:255], v[28:31], v[88:91]\n"
"ds_read_b64_tr_b16 a[158:159], v200 offset:63488\n"
"buffer_load_dwordx2 a[44:45], v254, s[72:75], s50 offen offset:1152\n"
"v_mfma_f32_16x16x32_bf16 v[92:95], a[224:227], v[32:35], 0\n"
"ds_read_b64_tr_b16 a[192:193], v203 offset:32768\n"
"v_mfma_f32_16x16x32_bf16 v[92:95], a[228:231], v[36:39], v[92:95]\n"
"ds_read_b64_tr_b16 a[194:195], v203 offset:34816\n"
"v_mfma_f32_16x16x32_bf16 v[92:95], a[232:235], v[40:43], v[92:95]\n"
"ds_read_b64_tr_b16 a[196:197], v203 offset:36864\n"
"v_mfma_f32_16x16x32_bf16 v[92:95], a[236:239], v[44:47], v[92:95]\n"
"ds_read_b64_tr_b16 a[198:199], v203 offset:38912\n"
"buffer_load_dwordx2 a[46:47], v254, s[72:75], s50 offen offset:2304\n"
"v_mfma_f32_16x16x32_bf16 v[92:95], a[240:243], v[48:51], v[92:95]\n"
"ds_read_b64_tr_b16 a[200:201], v203 offset:40960\n"
"v_cvt_pk_bf16_f32 v106, v88, v89\n"
"v_mfma_f32_16x16x32_bf16 v[92:95], a[244:247], v[52:55], v[92:95]\n"
"ds_read_b64_tr_b16 a[202:203], v203 offset:43008\n"
"v_cvt_pk_bf16_f32 v107, v90, v91\n"
"v_mfma_f32_16x16x32_bf16 v[92:95], a[248:251], v[56:59], v[92:95]\n"
"ds_read_b64_tr_b16 a[204:205], v203 offset:45056\n"
"v_mfma_f32_16x16x32_bf16 v[92:95], a[252:255], v[60:63], v[92:95]\n"
"s_add_u32 s50, s50, 3456\n"
"buffer_store_dwordx4 v[104:107], v207, s[28:31], s54 offen offset:1024 sc1\n"
"ds_read_b64_tr_b16 a[206:207], v203 offset:47104\n"
"v_mfma_f32_16x16x32_bf16 v[64:67], a[128:131], v[0:3], 0\n"
"s_waitcnt lgkmcnt(11)\n"
"ds_read_b64_tr_b16 a[208:209], v203 offset:49152\n"
"v_mfma_f32_16x16x32_bf16 v[64:67], a[132:135], v[4:7], v[64:67]\n"
"ds_read_b64_tr_b16 a[210:211], v203 offset:51200\n"
"buffer_load_dwordx4 v[112:115], v246, s[28:31], s56 offen offset:0 sc1\n"
"v_mfma_f32_16x16x32_bf16 v[64:67], a[136:139], v[8:11], v[64:67]\n"
"ds_read_b64_tr_b16 a[212:213], v203 offset:53248\n"
"v_mfma_f32_16x16x32_bf16 v[64:67], a[140:143], v[12:15], v[64:67]\n"
"ds_read_b64_tr_b16 a[214:215], v203 offset:55296\n"
"v_mfma_f32_16x16x32_bf16 v[64:67], a[144:147], v[16:19], v[64:67]\n"
"s_waitcnt vmcnt(35)\n"
"s_barrier\n"
"ds_read_b64_tr_b16 a[216:217], v203 offset:57344\n"
"v_cvt_pk_bf16_f32 v110, v92, v93\n"
"v_mfma_f32_16x16x32_bf16 v[64:67], a[148:151], v[20:23], v[64:67]\n"
"s_waitcnt lgkmcnt(11)\n"
"ds_read_b64_tr_b16 a[218:219], v203 offset:59392\n"
"v_cvt_pk_bf16_f32 v111, v94, v95\n"
"v_mfma_f32_16x16x32_bf16 v[64:67], a[152:155], v[24:27], v[64:67]\n"
"ds_read_b64_tr_b16 a[220:221], v203 offset:61440\n"
"v_mfma_f32_16x16x32_bf16 v[64:67], a[156:159], v[28:31], v[64:67]\n"
"buffer_store_dwordx4 v[108:111], v208, s[28:31], s54 offen offset:1024 sc1\n"
"ds_read_b64_tr_b16 a[222:223], v203 offset:63488\n"
"v_mfma_f32_16x16x32_bf16 v[68:71], a[128:131], v[32:35], 0\n"
"ds_read_b64_tr_b16 a[160:161], v200 offset:33024\n"
"v_mfma_f32_16x16x32_bf16 v[68:71], a[132:135], v[36:39], v[68:71]\n"
"ds_read_b64_tr_b16 a[162:163], v200 offset:35072\n"
"buffer_load_dwordx4 v[116:119], v246, s[28:31], s56 offen offset:16 sc1\n"
"v_mfma_f32_16x16x32_bf16 v[68:71], a[136:139], v[40:43], v[68:71]\n"
"ds_read_b64_tr_b16 a[164:165], v200 offset:37120\n"
"v_mfma_f32_16x16x32_bf16 v[68:71], a[140:143], v[44:47], v[68:71]\n"
"ds_read_b64_tr_b16 a[166:167], v200 offset:39168\n"
"v_mfma_f32_16x16x32_bf16 v[68:71], a[144:147], v[48:51], v[68:71]\n"
"ds_read_b64_tr_b16 a[168:169], v200 offset:41216\n"
"v_cvt_pk_bf16_f32 v96, v64, v65\n"
"v_mfma_f32_16x16x32_bf16 v[68:71], a[148:151], v[52:55], v[68:71]\n"
"ds_read_b64_tr_b16 a[170:171], v200 offset:43264\n"
"v_cvt_pk_bf16_f32 v97, v66, v67\n"
"v_mfma_f32_16x16x32_bf16 v[68:71], a[152:155], v[56:59], v[68:71]\n"
"ds_read_b64_tr_b16 a[172:173], v200 offset:45312\n"
"buffer_load_dwordx4 v[120:123], v246, s[28:31], s57 offen offset:0 sc1\n"
"v_mfma_f32_16x16x32_bf16 v[68:71], a[156:159], v[60:63], v[68:71]\n"
"ds_read_b64_tr_b16 a[174:175], v200 offset:47360\n"
"v_mfma_f32_16x16x32_bf16 v[72:75], a[192:195], v[0:3], 0\n"
"s_waitcnt lgkmcnt(11)\n"
"ds_read_b64_tr_b16 a[176:177], v200 offset:49408\n"
"v_mfma_f32_16x16x32_bf16 v[72:75], a[196:199], v[4:7], v[72:75]\n"
"ds_read_b64_tr_b16 a[178:179], v200 offset:51456\n"
"v_mfma_f32_16x16x32_bf16 v[72:75], a[200:203], v[8:11], v[72:75]\n"
"ds_read_b64_tr_b16 a[180:181], v200 offset:53504\n"
"buffer_load_dwordx4 v[124:127], v246, s[28:31], s57 offen offset:16 sc1\n"
"v_mfma_f32_16x16x32_bf16 v[72:75], a[204:207], v[12:15], v[72:75]\n"
"ds_read_b64_tr_b16 a[182:183], v200 offset:55552\n"
"v_mfma_f32_16x16x32_bf16 v[72:75], a[208:211], v[16:19], v[72:75]\n"
"ds_read_b64_tr_b16 a[184:185], v200 offset:57600\n"
"v_cvt_pk_bf16_f32 v100, v68, v69\n"
"v_mfma_f32_16x16x32_bf16 v[72:75], a[212:215], v[20:23], v[72:75]\n"
"s_waitcnt lgkmcnt(11)\n"
"ds_read_b64_tr_b16 a[186:187], v200 offset:59648\n"
"v_cvt_pk_bf16_f32 v101, v70, v71\n"
"v_mfma_f32_16x16x32_bf16 v[72:75], a[216:219], v[24:27], v[72:75]\n"
"ds_read_b64_tr_b16 a[188:189], v200 offset:61696\n"
"buffer_load_dwordx4 v[212:215], v246, s[28:31], s58 offen offset:0 sc1\n"
"v_mfma_f32_16x16x32_bf16 v[72:75], a[220:223], v[28:31], v[72:75]\n"
"ds_read_b64_tr_b16 a[190:191], v200 offset:63744\n"
"v_mfma_f32_16x16x32_bf16 v[76:79], a[192:195], v[32:35], 0\n"
"ds_read_b64_tr_b16 a[224:225], v203 offset:33024\n"
"v_mfma_f32_16x16x32_bf16 v[76:79], a[196:199], v[36:39], v[76:79]\n"
"ds_read_b64_tr_b16 a[226:227], v203 offset:35072\n"
"v_mfma_f32_16x16x32_bf16 v[76:79], a[200:203], v[40:43], v[76:79]\n"
"ds_read_b64_tr_b16 a[228:229], v203 offset:37120\n"
"buffer_load_dwordx4 v[216:219], v246, s[28:31], s58 offen offset:16 sc1\n"
"v_mfma_f32_16x16x32_bf16 v[76:79], a[204:207], v[44:47], v[76:79]\n"
"ds_read_b64_tr_b16 a[230:231], v203 offset:39168\n"
"v_mfma_f32_16x16x32_bf16 v[76:79], a[208:211], v[48:51], v[76:79]\n"
"ds_read_b64_tr_b16 a[232:233], v203 offset:41216\n"
"v_cvt_pk_bf16_f32 v98, v72, v73\n"
"v_mfma_f32_16x16x32_bf16 v[76:79], a[212:215], v[52:55], v[76:79]\n"
"ds_read_b64_tr_b16 a[234:235], v203 offset:43264\n"
"v_cvt_pk_bf16_f32 v99, v74, v75\n"
"v_mfma_f32_16x16x32_bf16 v[76:79], a[216:219], v[56:59], v[76:79]\n"
"ds_read_b64_tr_b16 a[236:237], v203 offset:45312\n"
"v_mfma_f32_16x16x32_bf16 v[76:79], a[220:223], v[60:63], v[76:79]\n"
"ds_read_b64_tr_b16 a[238:239], v203 offset:47360\n"
"buffer_store_dwordx4 v[96:99], v207, s[28:31], s54 offen offset:2048 sc1\n"
"v_mfma_f32_16x16x32_bf16 v[80:83], a[160:163], v[0:3], 0\n"
"s_waitcnt lgkmcnt(11)\n"
"ds_read_b64_tr_b16 a[240:241], v203 offset:49408\n"
"v_mfma_f32_16x16x32_bf16 v[80:83], a[164:167], v[4:7], v[80:83]\n"
"ds_read_b64_tr_b16 a[242:243], v203 offset:51456\n"
"buffer_load_dwordx4 v[220:223], v246, s[28:31], s59 offen offset:0 sc1\n"
"v_mfma_f32_16x16x32_bf16 v[80:83], a[168:171], v[8:11], v[80:83]\n"
"ds_read_b64_tr_b16 a[244:245], v203 offset:53504\n"
"v_mfma_f32_16x16x32_bf16 v[80:83], a[172:175], v[12:15], v[80:83]\n"
"ds_read_b64_tr_b16 a[246:247], v203 offset:55552\n"
"v_mfma_f32_16x16x32_bf16 v[80:83], a[176:179], v[16:19], v[80:83]\n"
"ds_read_b64_tr_b16 a[248:249], v203 offset:57600\n"
"v_cvt_pk_bf16_f32 v102, v76, v77\n"
"v_mfma_f32_16x16x32_bf16 v[80:83], a[180:183], v[20:23], v[80:83]\n"
"s_waitcnt lgkmcnt(11)\n"
"ds_read_b64_tr_b16 a[250:251], v203 offset:59648\n"
"v_cvt_pk_bf16_f32 v103, v78, v79\n"
"v_mfma_f32_16x16x32_bf16 v[80:83], a[184:187], v[24:27], v[80:83]\n"
"ds_read_b64_tr_b16 a[252:253], v203 offset:61696\n"
"v_mfma_f32_16x16x32_bf16 v[80:83], a[188:191], v[28:31], v[80:83]\n"
"ds_read_b64_tr_b16 a[254:255], v203 offset:63744\n"
"buffer_store_dwordx4 v[100:103], v208, s[28:31], s54 offen offset:2048 sc1\n"
"v_mfma_f32_16x16x32_bf16 v[84:87], a[160:163], v[32:35], 0\n"
"ds_read_b64_tr_b16 a[128:129], v201 offset:0\n"
"v_mfma_f32_16x16x32_bf16 v[84:87], a[164:167], v[36:39], v[84:87]\n"
"ds_read_b64_tr_b16 a[130:131], v201 offset:2048\n"
"buffer_load_dwordx4 v[224:227], v246, s[28:31], s59 offen offset:16 sc1\n"
"v_mfma_f32_16x16x32_bf16 v[84:87], a[168:171], v[40:43], v[84:87]\n"
"ds_read_b64_tr_b16 a[132:133], v201 offset:4096\n"
"v_mfma_f32_16x16x32_bf16 v[84:87], a[172:175], v[44:47], v[84:87]\n"
"ds_read_b64_tr_b16 a[134:135], v201 offset:6144\n"
"v_mfma_f32_16x16x32_bf16 v[84:87], a[176:179], v[48:51], v[84:87]\n"
"ds_read_b64_tr_b16 a[136:137], v201 offset:8192\n"
"v_cvt_pk_bf16_f32 v104, v80, v81\n"
"v_mfma_f32_16x16x32_bf16 v[84:87], a[180:183], v[52:55], v[84:87]\n"
"ds_read_b64_tr_b16 a[138:139], v201 offset:10240\n"
"v_cvt_pk_bf16_f32 v105, v82, v83\n"
"v_mfma_f32_16x16x32_bf16 v[84:87], a[184:187], v[56:59], v[84:87]\n"
"ds_read_b64_tr_b16 a[140:141], v201 offset:12288\n"
"buffer_load_dwordx2 v[210:211], v254, s[80:83], 0 offen\n"
"v_mfma_f32_16x16x32_bf16 v[84:87], a[188:191], v[60:63], v[84:87]\n"
"ds_read_b64_tr_b16 a[142:143], v201 offset:14336\n"
"v_mfma_f32_16x16x32_bf16 v[88:91], a[224:227], v[0:3], 0\n"
"s_waitcnt lgkmcnt(11)\n"
"ds_read_b64_tr_b16 a[144:145], v201 offset:16384\n"
"v_mfma_f32_16x16x32_bf16 v[88:91], a[228:231], v[4:7], v[88:91]\n"
"ds_read_b64_tr_b16 a[146:147], v201 offset:18432\n"
"v_mfma_f32_16x16x32_bf16 v[88:91], a[232:235], v[8:11], v[88:91]\n"
"ds_read_b64_tr_b16 a[148:149], v201 offset:20480\n"
"buffer_load_dwordx2 v[228:229], v240, s[16:19], s46 offen offset:0\n"
"v_mfma_f32_16x16x32_bf16 v[88:91], a[236:239], v[12:15], v[88:91]\n"
"ds_read_b64_tr_b16 a[150:151], v201 offset:22528\n"
"v_mfma_f32_16x16x32_bf16 v[88:91], a[240:243], v[16:19], v[88:91]\n"
"ds_read_b64_tr_b16 a[152:153], v201 offset:24576\n"
"v_cvt_pk_bf16_f32 v108, v84, v85\n"
"v_mfma_f32_16x16x32_bf16 v[88:91], a[244:247], v[20:23], v[88:91]\n"
"s_waitcnt lgkmcnt(11)\n"
"ds_read_b64_tr_b16 a[154:155], v201 offset:26624\n"
"v_cvt_pk_bf16_f32 v109, v86, v87\n"
"v_mfma_f32_16x16x32_bf16 v[88:91], a[248:251], v[24:27], v[88:91]\n"
"ds_read_b64_tr_b16 a[156:157], v201 offset:28672\n"
"buffer_load_dwordx2 v[230:231], v240, s[16:19], s46 offen offset:32\n"
"v_mfma_f32_16x16x32_bf16 v[88:91], a[252:255], v[28:31], v[88:91]\n"
"ds_read_b64_tr_b16 a[158:159], v201 offset:30720\n"
"v_mfma_f32_16x16x32_bf16 v[92:95], a[224:227], v[32:35], 0\n"
"ds_read_b64_tr_b16 a[192:193], v204 offset:0\n"
"v_mfma_f32_16x16x32_bf16 v[92:95], a[228:231], v[36:39], v[92:95]\n"
"ds_read_b64_tr_b16 a[194:195], v204 offset:2048\n"
"v_mfma_f32_16x16x32_bf16 v[92:95], a[232:235], v[40:43], v[92:95]\n"
"ds_read_b64_tr_b16 a[196:197], v204 offset:4096\n"
"buffer_load_dwordx2 v[232:233], v240, s[16:19], s46 offen offset:384\n"
"v_mfma_f32_16x16x32_bf16 v[92:95], a[236:239], v[44:47], v[92:95]\n"
"ds_read_b64_tr_b16 a[198:199], v204 offset:6144\n"
"v_mfma_f32_16x16x32_bf16 v[92:95], a[240:243], v[48:51], v[92:95]\n"
"ds_read_b64_tr_b16 a[200:201], v204 offset:8192\n"
"v_cvt_pk_bf16_f32 v106, v88, v89\n"
"v_mfma_f32_16x16x32_bf16 v[92:95], a[244:247], v[52:55], v[92:95]\n"
"ds_read_b64_tr_b16 a[202:203], v204 offset:10240\n"
"v_cvt_pk_bf16_f32 v107, v90, v91\n"
"v_mfma_f32_16x16x32_bf16 v[92:95], a[248:251], v[56:59], v[92:95]\n"
"ds_read_b64_tr_b16 a[204:205], v204 offset:12288\n"
"v_mfma_f32_16x16x32_bf16 v[92:95], a[252:255], v[60:63], v[92:95]\n"
"buffer_store_dwordx4 v[104:107], v207, s[28:31], s54 offen offset:3072 sc1\n"
"ds_read_b64_tr_b16 a[206:207], v204 offset:14336\n"
"v_mfma_f32_16x16x32_bf16 v[64:67], a[128:131], v[0:3], 0\n"
"s_waitcnt lgkmcnt(11)\n"
"ds_read_b64_tr_b16 a[208:209], v204 offset:16384\n"
"v_mfma_f32_16x16x32_bf16 v[64:67], a[132:135], v[4:7], v[64:67]\n"
"ds_read_b64_tr_b16 a[210:211], v204 offset:18432\n"
"buffer_load_dwordx2 v[234:235], v240, s[16:19], s46 offen offset:416\n"
"v_mfma_f32_16x16x32_bf16 v[64:67], a[136:139], v[8:11], v[64:67]\n"
"ds_read_b64_tr_b16 a[212:213], v204 offset:20480\n"
"v_mfma_f32_16x16x32_bf16 v[64:67], a[140:143], v[12:15], v[64:67]\n"
"ds_read_b64_tr_b16 a[214:215], v204 offset:22528\n"
"v_mfma_f32_16x16x32_bf16 v[64:67], a[144:147], v[16:19], v[64:67]\n"
"ds_read_b64_tr_b16 a[216:217], v204 offset:24576\n"
"v_cvt_pk_bf16_f32 v110, v92, v93\n"
"v_mfma_f32_16x16x32_bf16 v[64:67], a[148:151], v[20:23], v[64:67]\n"
"s_waitcnt lgkmcnt(11)\n"
"ds_read_b64_tr_b16 a[218:219], v204 offset:26624\n"
"v_cvt_pk_bf16_f32 v111, v94, v95\n"
"v_mfma_f32_16x16x32_bf16 v[64:67], a[152:155], v[24:27], v[64:67]\n"
"ds_read_b64_tr_b16 a[220:221], v204 offset:28672\n"
"v_mfma_f32_16x16x32_bf16 v[64:67], a[156:159], v[28:31], v[64:67]\n"
"buffer_store_dwordx4 v[108:111], v208, s[28:31], s54 offen offset:3072 sc1\n"
"ds_read_b64_tr_b16 a[222:223], v204 offset:30720\n"
"v_mfma_f32_16x16x32_bf16 v[68:71], a[128:131], v[32:35], 0\n"
"s_add_u32 s54, s54, 4096\n"
"ds_read_b64_tr_b16 a[160:161], v201 offset:256\n"
"v_mfma_f32_16x16x32_bf16 v[68:71], a[132:135], v[36:39], v[68:71]\n"
"ds_read_b64_tr_b16 a[162:163], v201 offset:2304\n"
"buffer_load_dwordx2 v[236:237], v240, s[16:19], s46 offen offset:768\n"
"v_mfma_f32_16x16x32_bf16 v[68:71], a[136:139], v[40:43], v[68:71]\n"
"ds_read_b64_tr_b16 a[164:165], v201 offset:4352\n"
"v_mfma_f32_16x16x32_bf16 v[68:71], a[140:143], v[44:47], v[68:71]\n"
"ds_read_b64_tr_b16 a[166:167], v201 offset:6400\n"
"v_mfma_f32_16x16x32_bf16 v[68:71], a[144:147], v[48:51], v[68:71]\n"
"ds_read_b64_tr_b16 a[168:169], v201 offset:8448\n"
"v_cvt_pk_bf16_f32 v96, v64, v65\n"
"v_mfma_f32_16x16x32_bf16 v[68:71], a[148:151], v[52:55], v[68:71]\n"
"ds_read_b64_tr_b16 a[170:171], v201 offset:10496\n"
"v_cvt_pk_bf16_f32 v97, v66, v67\n"
"v_mfma_f32_16x16x32_bf16 v[68:71], a[152:155], v[56:59], v[68:71]\n"
"ds_read_b64_tr_b16 a[172:173], v201 offset:12544\n"
"buffer_load_dwordx2 v[238:239], v240, s[16:19], s46 offen offset:800\n"
"v_mfma_f32_16x16x32_bf16 v[68:71], a[156:159], v[60:63], v[68:71]\n"
"ds_read_b64_tr_b16 a[174:175], v201 offset:14592\n"
"v_mfma_f32_16x16x32_bf16 v[72:75], a[192:195], v[0:3], 0\n"
"s_waitcnt lgkmcnt(11)\n"
"ds_read_b64_tr_b16 a[176:177], v201 offset:16640\n"
"v_mfma_f32_16x16x32_bf16 v[72:75], a[196:199], v[4:7], v[72:75]\n"
"ds_read_b64_tr_b16 a[178:179], v201 offset:18688\n"
"v_mfma_f32_16x16x32_bf16 v[72:75], a[200:203], v[8:11], v[72:75]\n"
"ds_read_b64_tr_b16 a[180:181], v201 offset:20736\n"
"v_mfma_f32_16x16x32_bf16 v[72:75], a[204:207], v[12:15], v[72:75]\n"
"ds_read_b64_tr_b16 a[182:183], v201 offset:22784\n"
"v_mfma_f32_16x16x32_bf16 v[72:75], a[208:211], v[16:19], v[72:75]\n"
"ds_read_b64_tr_b16 a[184:185], v201 offset:24832\n"
"v_cvt_pk_bf16_f32 v100, v68, v69\n"
"v_mfma_f32_16x16x32_bf16 v[72:75], a[212:215], v[20:23], v[72:75]\n"
"s_waitcnt lgkmcnt(11)\n"
"ds_read_b64_tr_b16 a[186:187], v201 offset:26880\n"
"v_cvt_pk_bf16_f32 v101, v70, v71\n"
"v_mfma_f32_16x16x32_bf16 v[72:75], a[216:219], v[24:27], v[72:75]\n"
"ds_read_b64_tr_b16 a[188:189], v201 offset:28928\n"
"v_mfma_f32_16x16x32_bf16 v[72:75], a[220:223], v[28:31], v[72:75]\n"
"ds_read_b64_tr_b16 a[190:191], v201 offset:30976\n"
"v_mfma_f32_16x16x32_bf16 v[76:79], a[192:195], v[32:35], 0\n"
"ds_read_b64_tr_b16 a[224:225], v204 offset:256\n"
"v_mfma_f32_16x16x32_bf16 v[76:79], a[196:199], v[36:39], v[76:79]\n"
"ds_read_b64_tr_b16 a[226:227], v204 offset:2304\n"
"v_mfma_f32_16x16x32_bf16 v[76:79], a[200:203], v[40:43], v[76:79]\n"
"ds_read_b64_tr_b16 a[228:229], v204 offset:4352\n"
"v_mfma_f32_16x16x32_bf16 v[76:79], a[204:207], v[44:47], v[76:79]\n"
"ds_read_b64_tr_b16 a[230:231], v204 offset:6400\n"
"v_mfma_f32_16x16x32_bf16 v[76:79], a[208:211], v[48:51], v[76:79]\n"
"ds_read_b64_tr_b16 a[232:233], v204 offset:8448\n"
"v_cvt_pk_bf16_f32 v98, v72, v73\n"
"v_mfma_f32_16x16x32_bf16 v[76:79], a[212:215], v[52:55], v[76:79]\n"
"ds_read_b64_tr_b16 a[234:235], v204 offset:10496\n"
"v_cvt_pk_bf16_f32 v99, v74, v75\n"
"v_mfma_f32_16x16x32_bf16 v[76:79], a[216:219], v[56:59], v[76:79]\n"
"ds_read_b64_tr_b16 a[236:237], v204 offset:12544\n"
"v_mfma_f32_16x16x32_bf16 v[76:79], a[220:223], v[60:63], v[76:79]\n"
"ds_read_b64_tr_b16 a[238:239], v204 offset:14592\n"
"buffer_store_dwordx4 v[96:99], v207, s[28:31], s54 offen offset:0 sc1\n"
"v_mfma_f32_16x16x32_bf16 v[80:83], a[160:163], v[0:3], 0\n"
"s_waitcnt lgkmcnt(11)\n"
"ds_read_b64_tr_b16 a[240:241], v204 offset:16640\n"
"v_mfma_f32_16x16x32_bf16 v[80:83], a[164:167], v[4:7], v[80:83]\n"
"ds_read_b64_tr_b16 a[242:243], v204 offset:18688\n"
"v_mfma_f32_16x16x32_bf16 v[80:83], a[168:171], v[8:11], v[80:83]\n"
"ds_read_b64_tr_b16 a[244:245], v204 offset:20736\n"
"v_mfma_f32_16x16x32_bf16 v[80:83], a[172:175], v[12:15], v[80:83]\n"
"ds_read_b64_tr_b16 a[246:247], v204 offset:22784\n"
"v_mfma_f32_16x16x32_bf16 v[80:83], a[176:179], v[16:19], v[80:83]\n"
"ds_read_b64_tr_b16 a[248:249], v204 offset:24832\n"
"v_cvt_pk_bf16_f32 v102, v76, v77\n"
"v_mfma_f32_16x16x32_bf16 v[80:83], a[180:183], v[20:23], v[80:83]\n"
"s_waitcnt lgkmcnt(11)\n"
"ds_read_b64_tr_b16 a[250:251], v204 offset:26880\n"
"v_cvt_pk_bf16_f32 v103, v78, v79\n"
"v_mfma_f32_16x16x32_bf16 v[80:83], a[184:187], v[24:27], v[80:83]\n"
"ds_read_b64_tr_b16 a[252:253], v204 offset:28928\n"
"v_mfma_f32_16x16x32_bf16 v[80:83], a[188:191], v[28:31], v[80:83]\n"
"ds_read_b64_tr_b16 a[254:255], v204 offset:30976\n"
"buffer_store_dwordx4 v[100:103], v208, s[28:31], s54 offen offset:0 sc1\n"
"v_mfma_f32_16x16x32_bf16 v[84:87], a[160:163], v[32:35], 0\n"
"s_nop 0\n"
"v_mfma_f32_16x16x32_bf16 v[84:87], a[164:167], v[36:39], v[84:87]\n"
"s_nop 0\n"
"v_mfma_f32_16x16x32_bf16 v[84:87], a[168:171], v[40:43], v[84:87]\n"
"s_nop 0\n"
"v_mfma_f32_16x16x32_bf16 v[84:87], a[172:175], v[44:47], v[84:87]\n"
"s_nop 0\n"
"v_mfma_f32_16x16x32_bf16 v[84:87], a[176:179], v[48:51], v[84:87]\n"
"v_cvt_pk_bf16_f32 v104, v80, v81\n"
"v_mfma_f32_16x16x32_bf16 v[84:87], a[180:183], v[52:55], v[84:87]\n"
"v_cvt_pk_bf16_f32 v105, v82, v83\n"
"v_mfma_f32_16x16x32_bf16 v[84:87], a[184:187], v[56:59], v[84:87]\n"
"s_nop 0\n"
"v_mfma_f32_16x16x32_bf16 v[84:87], a[188:191], v[60:63], v[84:87]\n"
"s_nop 0\n"
"v_mfma_f32_16x16x32_bf16 v[88:91], a[224:227], v[0:3], 0\n"
"s_waitcnt lgkmcnt(3)\n"
"v_mfma_f32_16x16x32_bf16 v[88:91], a[228:231], v[4:7], v[88:91]\n"
"s_nop 0\n"
"v_mfma_f32_16x16x32_bf16 v[88:91], a[232:235], v[8:11], v[88:91]\n"
"s_nop 0\n"
"v_mfma_f32_16x16x32_bf16 v[88:91], a[236:239], v[12:15], v[88:91]\n"
"s_nop 0\n"
"v_mfma_f32_16x16x32_bf16 v[88:91], a[240:243], v[16:19], v[88:91]\n"
"v_cvt_pk_bf16_f32 v108, v84, v85\n"
"v_mfma_f32_16x16x32_bf16 v[88:91], a[244:247], v[20:23], v[88:91]\n"
"s_waitcnt lgkmcnt(0)\n"
"v_cvt_pk_bf16_f32 v109, v86, v87\n"
"v_mfma_f32_16x16x32_bf16 v[88:91], a[248:251], v[24:27], v[88:91]\n"
"s_nop 0\n"
"v_mfma_f32_16x16x32_bf16 v[88:91], a[252:255], v[28:31], v[88:91]\n"
"s_nop 0\n"
"v_mfma_f32_16x16x32_bf16 v[92:95], a[224:227], v[32:35], 0\n"
"s_nop 0\n"
"v_mfma_f32_16x16x32_bf16 v[92:95], a[228:231], v[36:39], v[92:95]\n"
"s_nop 0\n"
"v_mfma_f32_16x16x32_bf16 v[92:95], a[232:235], v[40:43], v[92:95]\n"
"s_nop 0\n"
"v_mfma_f32_16x16x32_bf16 v[92:95], a[236:239], v[44:47], v[92:95]\n"
"s_nop 0\n"
"v_mfma_f32_16x16x32_bf16 v[92:95], a[240:243], v[48:51], v[92:95]\n"
"v_cvt_pk_bf16_f32 v106, v88, v89\n"
"v_mfma_f32_16x16x32_bf16 v[92:95], a[244:247], v[52:55], v[92:95]\n"
"v_cvt_pk_bf16_f32 v107, v90, v91\n"
"v_mfma_f32_16x16x32_bf16 v[92:95], a[248:251], v[56:59], v[92:95]\n"
"s_nop 0\n"
"v_mfma_f32_16x16x32_bf16 v[92:95], a[252:255], v[60:63], v[92:95]\n"
"buffer_store_dwordx4 v[104:107], v207, s[28:31], s54 offen offset:1024 sc1\n"
"s_nop 1\n"
"s_nop 1\n"
"s_nop 1\n"
"s_nop 1\n"
"s_nop 1\n"
"v_cvt_pk_bf16_f32 v110, v92, v93\n"
"s_nop 1\n"
"v_cvt_pk_bf16_f32 v111, v94, v95\n"
"s_nop 1\n"
"s_nop 1\n"
"buffer_store_dwordx4 v[108:111], v208, s[28:31], s54 offen offset:1024 sc1\n"
"s_nop 1\n"
"s_nop 1\n"
"s_nop 1\n"
"s_nop 1\n"
"s_nop 1\n"
"s_nop 1\n"
"s_nop 1\n"
"s_nop 1\n"
"s_mov_b32 s87, 0x80000\n"
"v_mov_b32 v28, 0xffff0000\n"
"s_waitcnt vmcnt(4)\n"
"AT_tchk:\n"
"v_xor_b32 v26, s85, v113\n"
"v_xor_b32 v27, s85, v115\n"
"v_bitop3_b32 v26, v117, s85, v26 bitop3:0xbe\n"
"v_bitop3_b32 v27, v119, s85, v27 bitop3:0xbe\n"
"v_bitop3_b32 v26, v121, s85, v26 bitop3:0xbe\n"
"v_bitop3_b32 v27, v123, s85, v27 bitop3:0xbe\n"
"v_bitop3_b32 v26, v125, s85, v26 bitop3:0xbe\n"
"v_bitop3_b32 v27, v127, s85, v27 bitop3:0xbe\n"
"v_bitop3_b32 v26, v213, s85, v26 bitop3:0xbe\n"
"v_bitop3_b32 v27, v215, s85, v27 bitop3:0xbe\n"
"v_bitop3_b32 v26, v217, s85, v26 bitop3:0xbe\n"
"v_bitop3_b32 v27, v219, s85, v27 bitop3:0xbe\n"
"v_bitop3_b32 v26, v221, s85, v26 bitop3:0xbe\n"
"v_bitop3_b32 v27, v223, s85, v27 bitop3:0xbe\n"
"v_bitop3_b32 v26, v225, s85, v26 bitop3:0xbe\n"
"v_bitop3_b32 v27, v227, s85, v27 bitop3:0xbe\n"
"v_or_b32 v26, v26, v27\n"
"v_cmp_ne_u32 s[60:61], 0, v26\n"
"v_cmp_gt_u32 vcc, 144, v253\n"
"s_nop 1\n"
"s_and_b64 vcc, vcc, s[60:61]\n"
"s_cbranch_vccz AT_tok\n"
"s_sub_u32 s87, s87, 1\n"
"s_cmp_eq_u32 s87, 0\n"
"s_cbranch_scc1 AT_tok\n"
"s_sleep 2\n"
"buffer_load_dwordx4 v[112:115], v246, s[28:31], s56 offen offset:0 sc1\n"
"buffer_load_dwordx4 v[116:119], v246, s[28:31], s56 offen offset:16 sc1\n"
"buffer_load_dwordx4 v[120:123], v246, s[28:31], s57 offen offset:0 sc1\n"
"buffer_load_dwordx4 v[124:127], v246, s[28:31], s57 offen offset:16 sc1\n"
"buffer_load_dwordx4 v[212:215], v246, s[28:31], s58 offen offset:0 sc1\n"
"buffer_load_dwordx4 v[216:219], v246, s[28:31], s58 offen offset:16 sc1\n"
"buffer_load_dwordx4 v[220:223], v246, s[28:31], s59 offen offset:0 sc1\n"
"buffer_load_dwordx4 v[224:227], v246, s[28:31], s59 offen offset:16 sc1\n"
"s_waitcnt vmcnt(0)\n"
"s_branch AT_tchk\n"
"AT_tok:\n"
"v_lshlrev_b32 v0, 16, v210\n"
"v_and_b32 v1, v28, v210\n"
"v_lshlrev_b32 v2, 16, v211\n"
"v_and_b32 v3, v28, v211\n"
"v_mov_b32 v4, v112\n"
"v_add_f32 v12, 1.0, v212\n"
"v_mov_b32 v16, v120\n"
"v_mov_b32 v20, v220\n"
"v_mul_f32 v8, v12, v0\n"
"v_mov_b32 v5, v114\n"
"v_add_f32 v13, 1.0, v214\n"
"v_mov_b32 v17, v122\n"
"v_mov_b32 v21, v222\n"
"v_mul_f32 v9, v13, v1\n"
"v_mov_b32 v6, v116\n"
"v_add_f32 v14, 1.0, v216\n"
"v_mov_b32 v18, v124\n"
"v_mov_b32 v22, v224\n"
"v_mul_f32 v10, v14, v2\n"
"v_mov_b32 v7, v118\n"
"v_add_f32 v15, 1.0, v218\n"
"v_mov_b32 v19, v126\n"
"v_mov_b32 v23, v226\n"
"v_mul_f32 v11, v15, v3\n"
"ds_write_b128 v247, v[4:7] offset:0\n"
"ds_write_b128 v247, v[8:11] offset:4096\n"
"ds_write_b128 v247, v[12:15] offset:8192\n"
"ds_write_b128 v247, v[16:19] offset:12288\n"
"ds_write_b128 v247, v[20:23] offset:16384\n"
"v_add_f32 v24, v8, v9\n"
"v_mul_f32 v25, v16, v0\n"
"v_add_f32 v24, v24, v10\n"
"v_fmac_f32 v25, v17, v1\n"
"v_add_f32 v24, v24, v11\n"
"v_fmac_f32 v25, v18, v2\n"
"s_nop 0\n"
"v_fmac_f32 v25, v19, v3\n"
"s_nop 1\n"
"v_add_f32_dpp v24, v24, v24 row_ror:1 row_mask:0xf bank_mask:0xf\n"
"v_add_f32_dpp v25, v25, v25 row_ror:1 row_mask:0xf bank_mask:0xf\n"
"s_nop 0\n"
"v_add_f32_dpp v24, v24, v24 row_ror:2 row_mask:0xf bank_mask:0xf\n"
"v_add_f32_dpp v25, v25, v25 row_ror:2 row_mask:0xf bank_mask:0xf\n"
"s_nop 0\n"
"v_add_f32_dpp v24, v24, v24 row_ror:4 row_mask:0xf bank_mask:0xf\n"
"v_add_f32_dpp v25, v25, v25 row_ror:4 row_mask:0xf bank_mask:0xf\n"
"s_nop 0\n"
"v_add_f32_dpp v24, v24, v24 row_ror:8 row_mask:0xf bank_mask:0xf\n"
"v_add_f32_dpp v25, v25, v25 row_ror:8 row_mask:0xf bank_mask:0xf\n"
"s_nop 0\n"
"v_mov_b32 v26, v24\n"
"v_mov_b32 v27, v25\n"
"s_nop 0\n"
"v_permlane16_swap_b32 v24, v26\n"
"v_permlane16_swap_b32 v25, v27\n"
"v_add_f32 v24, v24, v26\n"
"v_add_f32 v25, v25, v27\n"
"v_mov_b32 v26, v24\n"
"v_mov_b32 v27, v25\n"
"s_nop 0\n"
"v_permlane32_swap_b32 v24, v26\n"
"v_permlane32_swap_b32 v25, v27\n"
"v_add_f32 v24, v24, v26\n"
"v_add_f32 v25, v25, v27\n"
"s_lshl_b32 s60, s41, 3\n"
"s_add_u32 s60, s60, s40\n"
"v_mov_b32 v26, s60\n"
"v_mov_b32 v27, s40\n"
"v_mov_b32 v29, 0\n"
"v_add_u32 v26, 119296, v26\n"
"v_add_u32 v227, 118784, v27\n"
"s_mov_b64 exec, 1\n"
"ds_write_b64 v26, v[24:25]\n"
"ds_write_b32 v227, v29 offset:640\n"
"s_mov_b64 exec, -1\n"
"v_mov_b32 v224, 0x3f80\n"
"v_mov_b32 v225, 0x3f800000\n"
"v_and_b32 v226, 7, v248\n"
"v_lshl_add_u32 v226, v226, 4, v227\n"
"s_waitcnt vmcnt(0) lgkmcnt(0)\n"
"s_barrier\n"
"s_mov_b32 s14, 0xfffff800\n"
"s_cmp_lg_u32 s41, 3\n"
"s_cbranch_scc1 AT_A0\n"
"s_lshl_b32 s56, s55, 6\n"
"v_mov_b32 v0, s85\n"
"v_mov_b32 v1, 0\n"
"s_mov_b64 exec, 1\n"
"buffer_store_dword v0, v1, s[28:31], s56 offen sc1\n"
"s_mov_b64 exec, -1\n"
"v_and_b32 v2, 15, v248\n"
"v_lshlrev_b32 v2, 6, v2\n"
"s_lshl_b32 s57, s44, 10\n"
"s_mov_b32 s87, 0x80000\n"
"AT_S1:\n"
"buffer_load_dword v3, v2, s[28:31], s57 offen sc1\n"
"s_waitcnt vmcnt(0)\n"
"v_cmp_eq_u32 vcc, s85, v3\n"
"s_nop 1\n"
"s_lshr_b32 s58, vcc_lo, 1\n"
"s_and_b32 s58, s58, vcc_lo\n"
"s_lshr_b32 s59, s58, 2\n"
"s_and_b32 s58, s58, s59\n"
"s_and_b32 s58, s58, 0x1111\n"
"s_sub_u32 s87, s87, 1\n"
"s_cmp_eq_u32 s87, 0\n"
"s_cselect_b32 s58, 0x1111, s58\n"
"v_mov_b32 v4, s58\n"
"ds_write_b32 v227, v4 offset:640\n"
"s_cmp_eq_u32 s58, 0x1111\n"
"s_cbranch_scc1 AT_S1x\n"
"s_sleep 2\n"
"s_branch AT_S1\n"
"AT_S1x:\n"
"s_mov_b64 s[8:9], 0\n"
"v_mov_b32 v224, 0xffff0000\n"
"v_accvgpr_read_b32 v1, a48\n"
"v_accvgpr_read_b32 v3, a49\n"
"v_accvgpr_read_b32 v5, a50\n"
"v_accvgpr_read_b32 v7, a51\n"
"v_lshlrev_b32 v0, 16, v1\n"
"v_and_b32 v1, v224, v1\n"
"v_lshlrev_b32 v2, 16, v3\n"
"v_and_b32 v3, v224, v3\n"
"v_lshlrev_b32 v4, 16, v5\n"
"v_and_b32 v5, v224, v5\n"
"v_lshlrev_b32 v6, 16, v7\n"
"v_and_b32 v7, v224, v7\n"
"v_accvgpr_read_b32 v9, a84\n"
"v_accvgpr_read_b32 v11, a85\n"
"v_accvgpr_read_b32 v13, a86\n"
"v_accvgpr_read_b32 v15, a87\n"
"v_lshlrev_b32 v8, 16, v9\n"
"v_and_b32 v9, v224, v9\n"
"v_lshlrev_b32 v10, 16, v11\n"
"v_and_b32 v11, v224, v11\n"
"v_lshlrev_b32 v12, 16, v13\n"
"v_and_b32 v13, v224, v13\n"
"v_lshlrev_b32 v14, 16, v15\n"
"v_and_b32 v15, v224, v15\n"
"v_accvgpr_read_b32 v17, a52\n"
"v_accvgpr_read_b32 v19, a53\n"
"v_accvgpr_read_b32 v21, a54\n"
"v_accvgpr_read_b32 v23, a55\n"
"v_lshlrev_b32 v16, 16, v17\n"
"v_and_b32 v17, v224, v17\n"
"v_lshlrev_b32 v18, 16, v19\n"
"v_and_b32 v19, v224, v19\n"
"v_lshlrev_b32 v20, 16, v21\n"
"v_and_b32 v21, v224, v21\n"
"v_lshlrev_b32 v22, 16, v23\n"
"v_and_b32 v23, v224, v23\n"
"v_accvgpr_read_b32 v25, a88\n"
"v_accvgpr_read_b32 v27, a89\n"
"v_accvgpr_read_b32 v29, a90\n"
"v_accvgpr_read_b32 v31, a91\n"
"v_lshlrev_b32 v24, 16, v25\n"
"v_and_b32 v25, v224, v25\n"
"v_lshlrev_b32 v26, 16, v27\n"
"v_and_b32 v27, v224, v27\n"
"v_lshlrev_b32 v28, 16, v29\n"
"v_and_b32 v29, v224, v29\n"
"v_lshlrev_b32 v30, 16, v31\n"
"v_and_b32 v31, v224, v31\n"
"v_accvgpr_read_b32 v33, a56\n"
"v_accvgpr_read_b32 v35, a57\n"
"v_accvgpr_read_b32 v37, a58\n"
"v_accvgpr_read_b32 v39, a59\n"
"v_lshlrev_b32 v32, 16, v33\n"
"v_and_b32 v33, v224, v33\n"
"v_lshlrev_b32 v34, 16, v35\n"
"v_and_b32 v35, v224, v35\n"
"v_lshlrev_b32 v36, 16, v37\n"
"v_and_b32 v37, v224, v37\n"
"v_lshlrev_b32 v38, 16, v39\n"
"v_and_b32 v39, v224, v39\n"
"v_accvgpr_read_b32 v41, a92\n"
"v_accvgpr_read_b32 v43, a93\n"
"v_accvgpr_read_b32 v45, a94\n"
"v_accvgpr_read_b32 v47, a95\n"
"v_lshlrev_b32 v40, 16, v41\n"
"v_and_b32 v41, v224, v41\n"
"v_lshlrev_b32 v42, 16, v43\n"
"v_and_b32 v43, v224, v43\n"
"v_lshlrev_b32 v44, 16, v45\n"
"v_and_b32 v45, v224, v45\n"
"v_lshlrev_b32 v46, 16, v47\n"
"v_and_b32 v47, v224, v47\n"
"v_accvgpr_read_b32 v49, a60\n"
"v_accvgpr_read_b32 v51, a61\n"
"v_accvgpr_read_b32 v53, a62\n"
"v_accvgpr_read_b32 v55, a63\n"
"v_lshlrev_b32 v48, 16, v49\n"
"v_and_b32 v49, v224, v49\n"
"v_lshlrev_b32 v50, 16, v51\n"
"v_and_b32 v51, v224, v51\n"
"v_lshlrev_b32 v52, 16, v53\n"
"v_and_b32 v53, v224, v53\n"
"v_lshlrev_b32 v54, 16, v55\n"
"v_and_b32 v55, v224, v55\n"
"v_accvgpr_read_b32 v57, a96\n"
"v_accvgpr_read_b32 v59, a97\n"
"v_accvgpr_read_b32 v61, a98\n"
"v_accvgpr_read_b32 v63, a99\n"
"v_lshlrev_b32 v56, 16, v57\n"
"v_and_b32 v57, v224, v57\n"
"v_lshlrev_b32 v58, 16, v59\n"
"v_and_b32 v59, v224, v59\n"
"v_lshlrev_b32 v60, 16, v61\n"
"v_and_b32 v61, v224, v61\n"
"v_lshlrev_b32 v62, 16, v63\n"
"v_and_b32 v63, v224, v63\n"
"v_accvgpr_read_b32 v65, a64\n"
"v_accvgpr_read_b32 v67, a65\n"
"v_accvgpr_read_b32 v69, a66\n"
"v_accvgpr_read_b32 v71, a67\n"
"v_lshlrev_b32 v64, 16, v65\n"
"v_and_b32 v65, v224, v65\n"
"v_lshlrev_b32 v66, 16, v67\n"
"v_and_b32 v67, v224, v67\n"
"v_lshlrev_b32 v68, 16, v69\n"
"v_and_b32 v69, v224, v69\n"
"v_lshlrev_b32 v70, 16, v71\n"
"v_and_b32 v71, v224, v71\n"
"v_accvgpr_read_b32 v73, a100\n"
"v_accvgpr_read_b32 v75, a101\n"
"v_accvgpr_read_b32 v77, a102\n"
"v_accvgpr_read_b32 v79, a103\n"
"v_lshlrev_b32 v72, 16, v73\n"
"v_and_b32 v73, v224, v73\n"
"v_lshlrev_b32 v74, 16, v75\n"
"v_and_b32 v75, v224, v75\n"
"v_lshlrev_b32 v76, 16, v77\n"
"v_and_b32 v77, v224, v77\n"
"v_lshlrev_b32 v78, 16, v79\n"
"v_and_b32 v79, v224, v79\n"
"v_accvgpr_read_b32 v81, a68\n"
"v_accvgpr_read_b32 v83, a69\n"
"v_accvgpr_read_b32 v85, a70\n"
"v_accvgpr_read_b32 v87, a71\n"
"v_lshlrev_b32 v80, 16, v81\n"
"v_and_b32 v81, v224, v81\n"
"v_lshlrev_b32 v82, 16, v83\n"
"v_and_b32 v83, v224, v83\n"
"v_lshlrev_b32 v84, 16, v85\n"
"v_and_b32 v85, v224, v85\n"
"v_lshlrev_b32 v86, 16, v87\n"
"v_and_b32 v87, v224, v87\n"
"v_accvgpr_read_b32 v89, a104\n"
"v_accvgpr_read_b32 v91, a105\n"
"v_accvgpr_read_b32 v93, a106\n"
"v_accvgpr_read_b32 v95, a107\n"
"v_lshlrev_b32 v88, 16, v89\n"
"v_and_b32 v89, v224, v89\n"
"v_lshlrev_b32 v90, 16, v91\n"
"v_and_b32 v91, v224, v91\n"
"v_lshlrev_b32 v92, 16, v93\n"
"v_and_b32 v93, v224, v93\n"
"v_lshlrev_b32 v94, 16, v95\n"
"v_and_b32 v95, v224, v95\n"
"v_accvgpr_read_b32 v97, a72\n"
"v_accvgpr_read_b32 v99, a73\n"
"v_accvgpr_read_b32 v101, a74\n"
"v_accvgpr_read_b32 v103, a75\n"
"v_lshlrev_b32 v96, 16, v97\n"
"v_and_b32 v97, v224, v97\n"
"v_lshlrev_b32 v98, 16, v99\n"
"v_and_b32 v99, v224, v99\n"
"v_lshlrev_b32 v100, 16, v101\n"
"v_and_b32 v101, v224, v101\n"
"v_lshlrev_b32 v102, 16, v103\n"
"v_and_b32 v103, v224, v103\n"
"v_accvgpr_read_b32 v105, a108\n"
"v_accvgpr_read_b32 v107, a109\n"
"v_accvgpr_read_b32 v109, a110\n"
"v_accvgpr_read_b32 v111, a111\n"
"v_lshlrev_b32 v104, 16, v105\n"
"v_and_b32 v105, v224, v105\n"
"v_lshlrev_b32 v106, 16, v107\n"
"v_and_b32 v107, v224, v107\n"
"v_lshlrev_b32 v108, 16, v109\n"
"v_and_b32 v109, v224, v109\n"
"v_lshlrev_b32 v110, 16, v111\n"
"v_and_b32 v111, v224, v111\n"
"v_accvgpr_read_b32 v113, a76\n"
"v_accvgpr_read_b32 v115, a77\n"
"v_accvgpr_read_b32 v117, a78\n"
"v_accvgpr_read_b32 v119, a79\n"
"v_lshlrev_b32 v112, 16, v113\n"
"v_and_b32 v113, v224, v113\n"
"v_lshlrev_b32 v114, 16, v115\n"
"v_and_b32 v115, v224, v115\n"
"v_lshlrev_b32 v116, 16, v117\n"
"v_and_b32 v117, v224, v117\n"
"v_lshlrev_b32 v118, 16, v119\n"
"v_and_b32 v119, v224, v119\n"
"v_accvgpr_read_b32 v121, a112\n"
"v_accvgpr_read_b32 v123, a113\n"
"v_accvgpr_read_b32 v125, a114\n"
"v_accvgpr_read_b32 v127, a115\n"
"v_lshlrev_b32 v120, 16, v121\n"
"v_and_b32 v121, v224, v121\n"
"v_lshlrev_b32 v122, 16, v123\n"
"v_and_b32 v123, v224, v123\n"
"v_lshlrev_b32 v124, 16, v125\n"
"v_and_b32 v125, v224, v125\n"
"v_lshlrev_b32 v126, 16, v127\n"
"v_and_b32 v127, v224, v127\n"
"v_accvgpr_read_b32 v129, a80\n"
"v_accvgpr_read_b32 v131, a81\n"
"v_accvgpr_read_b32 v133, a82\n"
"v_accvgpr_read_b32 v135, a83\n"
"v_lshlrev_b32 v128, 16, v129\n"
"v_and_b32 v129, v224, v129\n"
"v_lshlrev_b32 v130, 16, v131\n"
"v_and_b32 v131, v224, v131\n"
"v_lshlrev_b32 v132, 16, v133\n"
"v_and_b32 v133, v224, v133\n"
"v_lshlrev_b32 v134, 16, v135\n"
"v_and_b32 v135, v224, v135\n"
"v_accvgpr_read_b32 v137, a116\n"
"v_accvgpr_read_b32 v139, a117\n"
"v_accvgpr_read_b32 v141, a118\n"
"v_accvgpr_read_b32 v143, a119\n"
"v_lshlrev_b32 v136, 16, v137\n"
"v_and_b32 v137, v224, v137\n"
"v_lshlrev_b32 v138, 16, v139\n"
"v_and_b32 v139, v224, v139\n"
"v_lshlrev_b32 v140, 16, v141\n"
"v_and_b32 v141, v224, v141\n"
"v_lshlrev_b32 v142, 16, v143\n"
"v_and_b32 v143, v224, v143\n"
"v_lshrrev_b32 v242, 2, v248\n"
"v_lshlrev_b32 v241, 4, v242\n"
"v_lshlrev_b32 v242, 3, v242\n"
"v_lshrrev_b32 v243, 5, v254\n"
"v_mul_u32_u24 v243, 24576, v243\n"
"v_and_b32 v247, 3, v248\n"
"v_lshl_add_u32 v243, v247, 5, v243\n"
"v_and_b32 v244, 0x10000000, v254\n"
"v_or_b32 v243, v244, v243\n"
"v_lshrrev_b32 v244, 5, v248\n"
"v_mul_u32_u24 v244, 96, v244\n"
"v_and_b32 v246, 16, v248\n"
"v_add3_u32 v244, v244, v246, v227\n"
"v_lshl_add_u32 v244, v247, 5, v244\n"
"s_mul_i32 s63, s41, 384\n"
"v_add_u32 v244, s63, v244\n"
"v_lshl_add_u32 v249, v248, 2, v227\n"
"s_lshl_b32 s63, s41, 8\n"
"v_add_u32 v251, s63, v249\n"
"v_add_u32 v252, s63, v227\n"
"s_branch AT_B2\n"
"AT_A0:\n"
"ds_read_b128 v[200:203], v244 offset:0\n"
"ds_read_b128 v[204:207], v244 offset:64\n"
"ds_read_b128 v[208:211], v244 offset:768\n"
"ds_read_b128 v[212:215], v244 offset:832\n"
"ds_read_b128 v[216:219], v244 offset:1536\n"
"ds_read_b128 v[220:223], v244 offset:1600\n"
"s_mov_b32 s52, 0\n"
"AT_A1:\n"
"ds_read_b32 v192, v227 offset:640\n"
"s_waitcnt lgkmcnt(0)\n"
"v_readfirstlane_b32 s53, v192\n"
"s_nop 0\n"
"s_andn2_b32 s53, s53, s52\n"
"s_cmp_lg_u32 s53, 0\n"
"s_cbranch_scc1 AT_A2\n"
"s_sleep 1\n"
"s_branch AT_A1\n"
"AT_A2:\n"
"s_bcnt1_i32_b32 s54, s53\n"
"s_bitcmp1_b32 s53, 0\n"
"s_cbranch_scc0 AT_Ai0\n"
"s_mov_b32 s56, s47\n"
"s_add_u32 s57, s47, 2359296\n"
"s_add_u32 s58, s47, 4718592\n"
"s_add_u32 s59, s47, 7077888\n"
"buffer_load_dwordx4 v[0:3], v241, s[28:31], s56 offen sc1\n"
"buffer_load_dwordx4 v[4:7], v241, s[28:31], s57 offen sc1\n"
"buffer_load_dwordx4 v[8:11], v241, s[28:31], s58 offen sc1\n"
"buffer_load_dwordx4 v[12:15], v241, s[28:31], s59 offen sc1\n"
"buffer_load_dwordx4 v[16:19], v242, s[28:31], s56 offen sc1\n"
"buffer_load_dwordx4 v[20:23], v242, s[28:31], s57 offen sc1\n"
"buffer_load_dwordx4 v[24:27], v242, s[28:31], s58 offen sc1\n"
"buffer_load_dwordx4 v[28:31], v242, s[28:31], s59 offen sc1\n"
"buffer_load_dwordx4 v[32:35], v243, s[28:31], s56 offen sc1\n"
"buffer_load_dwordx4 v[36:39], v243, s[28:31], s57 offen sc1\n"
"buffer_load_dwordx4 v[40:43], v243, s[28:31], s58 offen sc1\n"
"buffer_load_dwordx4 v[44:47], v243, s[28:31], s59 offen sc1\n"
"AT_Ai0:\n"
"s_bitcmp1_b32 s53, 4\n"
"s_cbranch_scc0 AT_Ai1\n"
"s_add_u32 s56, s47, 9437184\n"
"s_add_u32 s57, s47, 11796480\n"
"s_add_u32 s58, s47, 14155776\n"
"s_add_u32 s59, s47, 16515072\n"
"buffer_load_dwordx4 v[48:51], v241, s[28:31], s56 offen sc1\n"
"buffer_load_dwordx4 v[52:55], v241, s[28:31], s57 offen sc1\n"
"buffer_load_dwordx4 v[56:59], v241, s[28:31], s58 offen sc1\n"
"buffer_load_dwordx4 v[60:63], v241, s[28:31], s59 offen sc1\n"
"buffer_load_dwordx4 v[64:67], v242, s[28:31], s56 offen sc1\n"
"buffer_load_dwordx4 v[68:71], v242, s[28:31], s57 offen sc1\n"
"buffer_load_dwordx4 v[72:75], v242, s[28:31], s58 offen sc1\n"
"buffer_load_dwordx4 v[76:79], v242, s[28:31], s59 offen sc1\n"
"buffer_load_dwordx4 v[80:83], v243, s[28:31], s56 offen sc1\n"
"buffer_load_dwordx4 v[84:87], v243, s[28:31], s57 offen sc1\n"
"buffer_load_dwordx4 v[88:91], v243, s[28:31], s58 offen sc1\n"
"buffer_load_dwordx4 v[92:95], v243, s[28:31], s59 offen sc1\n"
"AT_Ai1:\n"
"s_bitcmp1_b32 s53, 8\n"
"s_cbranch_scc0 AT_Ai2\n"
"s_add_u32 s56, s47, 18874368\n"
"s_add_u32 s57, s47, 21233664\n"
"s_add_u32 s58, s47, 23592960\n"
"s_add_u32 s59, s47, 25952256\n"
"buffer_load_dwordx4 v[96:99], v241, s[28:31], s56 offen sc1\n"
"buffer_load_dwordx4 v[100:103], v241, s[28:31], s57 offen sc1\n"
"buffer_load_dwordx4 v[104:107], v241, s[28:31], s58 offen sc1\n"
"buffer_load_dwordx4 v[108:111], v241, s[28:31], s59 offen sc1\n"
"buffer_load_dwordx4 v[112:115], v242, s[28:31], s56 offen sc1\n"
"buffer_load_dwordx4 v[116:119], v242, s[28:31], s57 offen sc1\n"
"buffer_load_dwordx4 v[120:123], v242, s[28:31], s58 offen sc1\n"
"buffer_load_dwordx4 v[124:127], v242, s[28:31], s59 offen sc1\n"
"buffer_load_dwordx4 v[128:131], v243, s[28:31], s56 offen sc1\n"
"buffer_load_dwordx4 v[132:135], v243, s[28:31], s57 offen sc1\n"
"buffer_load_dwordx4 v[136:139], v243, s[28:31], s58 offen sc1\n"
"buffer_load_dwordx4 v[140:143], v243, s[28:31], s59 offen sc1\n"
"AT_Ai2:\n"
"s_bitcmp1_b32 s53, 12\n"
"s_cbranch_scc0 AT_Ai3\n"
"s_add_u32 s56, s47, 28311552\n"
"s_add_u32 s57, s47, 30670848\n"
"s_add_u32 s58, s47, 33030144\n"
"s_add_u32 s59, s47, 35389440\n"
"buffer_load_dwordx4 v[144:147], v241, s[28:31], s56 offen sc1\n"
"buffer_load_dwordx4 v[148:151], v241, s[28:31], s57 offen sc1\n"
"buffer_load_dwordx4 v[152:155], v241, s[28:31], s58 offen sc1\n"
"buffer_load_dwordx4 v[156:159], v241, s[28:31], s59 offen sc1\n"
"buffer_load_dwordx4 v[160:163], v242, s[28:31], s56 offen sc1\n"
"buffer_load_dwordx4 v[164:167], v242, s[28:31], s57 offen sc1\n"
"buffer_load_dwordx4 v[168:171], v242, s[28:31], s58 offen sc1\n"
"buffer_load_dwordx4 v[172:175], v242, s[28:31], s59 offen sc1\n"
"buffer_load_dwordx4 v[176:179], v243, s[28:31], s56 offen sc1\n"
"buffer_load_dwordx4 v[180:183], v243, s[28:31], s57 offen sc1\n"
"buffer_load_dwordx4 v[184:187], v243, s[28:31], s58 offen sc1\n"
"buffer_load_dwordx4 v[188:191], v243, s[28:31], s59 offen sc1\n"
"AT_Ai3:\n"
"s_bitcmp1_b32 s53, 0\n"
"s_cbranch_scc0 AT_Ar0\n"
"s_sub_u32 s54, s54, 1\n"
"s_cmp_eq_u32 s54, 0\n"
"s_cbranch_scc1 AT_Aw0_0\n"
"s_cmp_eq_u32 s54, 1\n"
"s_cbranch_scc1 AT_Aw0_1\n"
"s_cmp_eq_u32 s54, 2\n"
"s_cbranch_scc1 AT_Aw0_2\n"
"s_waitcnt vmcnt(36)\n"
"s_branch AT_Ax0\n"
"AT_Aw0_2:\n"
"s_waitcnt vmcnt(24)\n"
"s_branch AT_Ax0\n"
"AT_Aw0_1:\n"
"s_waitcnt vmcnt(12)\n"
"s_branch AT_Ax0\n"
"AT_Aw0_0:\n"
"s_waitcnt vmcnt(0)\n"
"AT_Ax0:\n"
"v_dot2_f32_bf16 v192, v0, v224, 0\n"
"v_dot2_f32_bf16 v193, v0, v225, 0\n"
"v_dot2_f32_bf16 v194, v1, v224, 0\n"
"v_dot2_f32_bf16 v195, v1, v225, 0\n"
"v_dot2_f32_bf16 v196, v2, v224, 0\n"
"v_dot2_f32_bf16 v197, v2, v225, 0\n"
"v_dot2_f32_bf16 v198, v3, v224, 0\n"
"v_dot2_f32_bf16 v199, v3, v225, 0\n"
"v_dot2_f32_bf16 v192, v4, v224, v192\n"
"v_dot2_f32_bf16 v193, v4, v225, v193\n"
"v_dot2_f32_bf16 v194, v5, v224, v194\n"
"v_dot2_f32_bf16 v195, v5, v225, v195\n"
"v_dot2_f32_bf16 v196, v6, v224, v196\n"
"v_dot2_f32_bf16 v197, v6, v225, v197\n"
"v_dot2_f32_bf16 v198, v7, v224, v198\n"
"v_dot2_f32_bf16 v199, v7, v225, v199\n"
"v_dot2_f32_bf16 v192, v8, v224, v192\n"
"v_dot2_f32_bf16 v193, v8, v225, v193\n"
"v_dot2_f32_bf16 v194, v9, v224, v194\n"
"v_dot2_f32_bf16 v195, v9, v225, v195\n"
"v_dot2_f32_bf16 v196, v10, v224, v196\n"
"v_dot2_f32_bf16 v197, v10, v225, v197\n"
"v_dot2_f32_bf16 v198, v11, v224, v198\n"
"v_dot2_f32_bf16 v199, v11, v225, v199\n"
"v_dot2_f32_bf16 v0, v12, v224, v192\n"
"v_dot2_f32_bf16 v1, v12, v225, v193\n"
"v_dot2_f32_bf16 v2, v13, v224, v194\n"
"v_dot2_f32_bf16 v3, v13, v225, v195\n"
"v_dot2_f32_bf16 v4, v14, v224, v196\n"
"v_dot2_f32_bf16 v5, v14, v225, v197\n"
"v_dot2_f32_bf16 v6, v15, v224, v198\n"
"v_dot2_f32_bf16 v7, v15, v225, v199\n"
"v_dot2_f32_bf16 v192, v16, v224, 0\n"
"v_dot2_f32_bf16 v193, v16, v225, 0\n"
"v_dot2_f32_bf16 v194, v17, v224, 0\n"
"v_dot2_f32_bf16 v195, v17, v225, 0\n"
"v_dot2_f32_bf16 v196, v18, v224, 0\n"
"v_dot2_f32_bf16 v197, v18, v225, 0\n"
"v_dot2_f32_bf16 v198, v19, v224, 0\n"
"v_dot2_f32_bf16 v199, v19, v225, 0\n"
"v_dot2_f32_bf16 v192, v20, v224, v192\n"
"v_dot2_f32_bf16 v193, v20, v225, v193\n"
"v_dot2_f32_bf16 v194, v21, v224, v194\n"
"v_dot2_f32_bf16 v195, v21, v225, v195\n"
"v_dot2_f32_bf16 v196, v22, v224, v196\n"
"v_dot2_f32_bf16 v197, v22, v225, v197\n"
"v_dot2_f32_bf16 v198, v23, v224, v198\n"
"v_dot2_f32_bf16 v199, v23, v225, v199\n"
"v_dot2_f32_bf16 v192, v24, v224, v192\n"
"v_dot2_f32_bf16 v193, v24, v225, v193\n"
"v_dot2_f32_bf16 v194, v25, v224, v194\n"
"v_dot2_f32_bf16 v195, v25, v225, v195\n"
"v_dot2_f32_bf16 v196, v26, v224, v196\n"
"v_dot2_f32_bf16 v197, v26, v225, v197\n"
"v_dot2_f32_bf16 v198, v27, v224, v198\n"
"v_dot2_f32_bf16 v199, v27, v225, v199\n"
"v_dot2_f32_bf16 v16, v28, v224, v192\n"
"v_dot2_f32_bf16 v17, v28, v225, v193\n"
"v_dot2_f32_bf16 v18, v29, v224, v194\n"
"v_dot2_f32_bf16 v19, v29, v225, v195\n"
"v_dot2_f32_bf16 v20, v30, v224, v196\n"
"v_dot2_f32_bf16 v21, v30, v225, v197\n"
"v_dot2_f32_bf16 v22, v31, v224, v198\n"
"v_dot2_f32_bf16 v23, v31, v225, v199\n"
"v_dot2_f32_bf16 v192, v32, v224, 0\n"
"v_dot2_f32_bf16 v193, v32, v225, 0\n"
"v_dot2_f32_bf16 v194, v33, v224, 0\n"
"v_dot2_f32_bf16 v195, v33, v225, 0\n"
"v_dot2_f32_bf16 v196, v34, v224, 0\n"
"v_dot2_f32_bf16 v197, v34, v225, 0\n"
"v_dot2_f32_bf16 v198, v35, v224, 0\n"
"v_dot2_f32_bf16 v199, v35, v225, 0\n"
"v_dot2_f32_bf16 v192, v36, v224, v192\n"
"v_dot2_f32_bf16 v193, v36, v225, v193\n"
"v_dot2_f32_bf16 v194, v37, v224, v194\n"
"v_dot2_f32_bf16 v195, v37, v225, v195\n"
"v_dot2_f32_bf16 v196, v38, v224, v196\n"
"v_dot2_f32_bf16 v197, v38, v225, v197\n"
"v_dot2_f32_bf16 v198, v39, v224, v198\n"
"v_dot2_f32_bf16 v199, v39, v225, v199\n"
"v_dot2_f32_bf16 v192, v40, v224, v192\n"
"v_dot2_f32_bf16 v193, v40, v225, v193\n"
"v_dot2_f32_bf16 v194, v41, v224, v194\n"
"v_dot2_f32_bf16 v195, v41, v225, v195\n"
"v_dot2_f32_bf16 v196, v42, v224, v196\n"
"v_dot2_f32_bf16 v197, v42, v225, v197\n"
"v_dot2_f32_bf16 v198, v43, v224, v198\n"
"v_dot2_f32_bf16 v199, v43, v225, v199\n"
"v_dot2_f32_bf16 v32, v44, v224, v192\n"
"v_dot2_f32_bf16 v33, v44, v225, v193\n"
"v_dot2_f32_bf16 v34, v45, v224, v194\n"
"v_dot2_f32_bf16 v35, v45, v225, v195\n"
"v_dot2_f32_bf16 v36, v46, v224, v196\n"
"v_dot2_f32_bf16 v37, v46, v225, v197\n"
"v_dot2_f32_bf16 v38, v47, v224, v198\n"
"v_dot2_f32_bf16 v39, v47, v225, v199\n"
"AT_Ar0:\n"
"s_bitcmp1_b32 s53, 4\n"
"s_cbranch_scc0 AT_Ar1\n"
"s_sub_u32 s54, s54, 1\n"
"s_cmp_eq_u32 s54, 0\n"
"s_cbranch_scc1 AT_Aw1_0\n"
"s_cmp_eq_u32 s54, 1\n"
"s_cbranch_scc1 AT_Aw1_1\n"
"s_waitcnt vmcnt(24)\n"
"s_branch AT_Ax1\n"
"AT_Aw1_1:\n"
"s_waitcnt vmcnt(12)\n"
"s_branch AT_Ax1\n"
"AT_Aw1_0:\n"
"s_waitcnt vmcnt(0)\n"
"AT_Ax1:\n"
"v_dot2_f32_bf16 v192, v48, v224, 0\n"
"v_dot2_f32_bf16 v193, v48, v225, 0\n"
"v_dot2_f32_bf16 v194, v49, v224, 0\n"
"v_dot2_f32_bf16 v195, v49, v225, 0\n"
"v_dot2_f32_bf16 v196, v50, v224, 0\n"
"v_dot2_f32_bf16 v197, v50, v225, 0\n"
"v_dot2_f32_bf16 v198, v51, v224, 0\n"
"v_dot2_f32_bf16 v199, v51, v225, 0\n"
"v_dot2_f32_bf16 v192, v52, v224, v192\n"
"v_dot2_f32_bf16 v193, v52, v225, v193\n"
"v_dot2_f32_bf16 v194, v53, v224, v194\n"
"v_dot2_f32_bf16 v195, v53, v225, v195\n"
"v_dot2_f32_bf16 v196, v54, v224, v196\n"
"v_dot2_f32_bf16 v197, v54, v225, v197\n"
"v_dot2_f32_bf16 v198, v55, v224, v198\n"
"v_dot2_f32_bf16 v199, v55, v225, v199\n"
"v_dot2_f32_bf16 v192, v56, v224, v192\n"
"v_dot2_f32_bf16 v193, v56, v225, v193\n"
"v_dot2_f32_bf16 v194, v57, v224, v194\n"
"v_dot2_f32_bf16 v195, v57, v225, v195\n"
"v_dot2_f32_bf16 v196, v58, v224, v196\n"
"v_dot2_f32_bf16 v197, v58, v225, v197\n"
"v_dot2_f32_bf16 v198, v59, v224, v198\n"
"v_dot2_f32_bf16 v199, v59, v225, v199\n"
"v_dot2_f32_bf16 v48, v60, v224, v192\n"
"v_dot2_f32_bf16 v49, v60, v225, v193\n"
"v_dot2_f32_bf16 v50, v61, v224, v194\n"
"v_dot2_f32_bf16 v51, v61, v225, v195\n"
"v_dot2_f32_bf16 v52, v62, v224, v196\n"
"v_dot2_f32_bf16 v53, v62, v225, v197\n"
"v_dot2_f32_bf16 v54, v63, v224, v198\n"
"v_dot2_f32_bf16 v55, v63, v225, v199\n"
"v_dot2_f32_bf16 v192, v64, v224, 0\n"
"v_dot2_f32_bf16 v193, v64, v225, 0\n"
"v_dot2_f32_bf16 v194, v65, v224, 0\n"
"v_dot2_f32_bf16 v195, v65, v225, 0\n"
"v_dot2_f32_bf16 v196, v66, v224, 0\n"
"v_dot2_f32_bf16 v197, v66, v225, 0\n"
"v_dot2_f32_bf16 v198, v67, v224, 0\n"
"v_dot2_f32_bf16 v199, v67, v225, 0\n"
"v_dot2_f32_bf16 v192, v68, v224, v192\n"
"v_dot2_f32_bf16 v193, v68, v225, v193\n"
"v_dot2_f32_bf16 v194, v69, v224, v194\n"
"v_dot2_f32_bf16 v195, v69, v225, v195\n"
"v_dot2_f32_bf16 v196, v70, v224, v196\n"
"v_dot2_f32_bf16 v197, v70, v225, v197\n"
"v_dot2_f32_bf16 v198, v71, v224, v198\n"
"v_dot2_f32_bf16 v199, v71, v225, v199\n"
"v_dot2_f32_bf16 v192, v72, v224, v192\n"
"v_dot2_f32_bf16 v193, v72, v225, v193\n"
"v_dot2_f32_bf16 v194, v73, v224, v194\n"
"v_dot2_f32_bf16 v195, v73, v225, v195\n"
"v_dot2_f32_bf16 v196, v74, v224, v196\n"
"v_dot2_f32_bf16 v197, v74, v225, v197\n"
"v_dot2_f32_bf16 v198, v75, v224, v198\n"
"v_dot2_f32_bf16 v199, v75, v225, v199\n"
"v_dot2_f32_bf16 v64, v76, v224, v192\n"
"v_dot2_f32_bf16 v65, v76, v225, v193\n"
"v_dot2_f32_bf16 v66, v77, v224, v194\n"
"v_dot2_f32_bf16 v67, v77, v225, v195\n"
"v_dot2_f32_bf16 v68, v78, v224, v196\n"
"v_dot2_f32_bf16 v69, v78, v225, v197\n"
"v_dot2_f32_bf16 v70, v79, v224, v198\n"
"v_dot2_f32_bf16 v71, v79, v225, v199\n"
"v_dot2_f32_bf16 v192, v80, v224, 0\n"
"v_dot2_f32_bf16 v193, v80, v225, 0\n"
"v_dot2_f32_bf16 v194, v81, v224, 0\n"
"v_dot2_f32_bf16 v195, v81, v225, 0\n"
"v_dot2_f32_bf16 v196, v82, v224, 0\n"
"v_dot2_f32_bf16 v197, v82, v225, 0\n"
"v_dot2_f32_bf16 v198, v83, v224, 0\n"
"v_dot2_f32_bf16 v199, v83, v225, 0\n"
"v_dot2_f32_bf16 v192, v84, v224, v192\n"
"v_dot2_f32_bf16 v193, v84, v225, v193\n"
"v_dot2_f32_bf16 v194, v85, v224, v194\n"
"v_dot2_f32_bf16 v195, v85, v225, v195\n"
"v_dot2_f32_bf16 v196, v86, v224, v196\n"
"v_dot2_f32_bf16 v197, v86, v225, v197\n"
"v_dot2_f32_bf16 v198, v87, v224, v198\n"
"v_dot2_f32_bf16 v199, v87, v225, v199\n"
"v_dot2_f32_bf16 v192, v88, v224, v192\n"
"v_dot2_f32_bf16 v193, v88, v225, v193\n"
"v_dot2_f32_bf16 v194, v89, v224, v194\n"
"v_dot2_f32_bf16 v195, v89, v225, v195\n"
"v_dot2_f32_bf16 v196, v90, v224, v196\n"
"v_dot2_f32_bf16 v197, v90, v225, v197\n"
"v_dot2_f32_bf16 v198, v91, v224, v198\n"
"v_dot2_f32_bf16 v199, v91, v225, v199\n"
"v_dot2_f32_bf16 v80, v92, v224, v192\n"
"v_dot2_f32_bf16 v81, v92, v225, v193\n"
"v_dot2_f32_bf16 v82, v93, v224, v194\n"
"v_dot2_f32_bf16 v83, v93, v225, v195\n"
"v_dot2_f32_bf16 v84, v94, v224, v196\n"
"v_dot2_f32_bf16 v85, v94, v225, v197\n"
"v_dot2_f32_bf16 v86, v95, v224, v198\n"
"v_dot2_f32_bf16 v87, v95, v225, v199\n"
"AT_Ar1:\n"
"s_bitcmp1_b32 s53, 8\n"
"s_cbranch_scc0 AT_Ar2\n"
"s_sub_u32 s54, s54, 1\n"
"s_cmp_eq_u32 s54, 0\n"
"s_cbranch_scc1 AT_Aw2_0\n"
"s_waitcnt vmcnt(12)\n"
"s_branch AT_Ax2\n"
"AT_Aw2_0:\n"
"s_waitcnt vmcnt(0)\n"
"AT_Ax2:\n"
"v_dot2_f32_bf16 v192, v96, v224, 0\n"
"v_dot2_f32_bf16 v193, v96, v225, 0\n"
"v_dot2_f32_bf16 v194, v97, v224, 0\n"
"v_dot2_f32_bf16 v195, v97, v225, 0\n"
"v_dot2_f32_bf16 v196, v98, v224, 0\n"
"v_dot2_f32_bf16 v197, v98, v225, 0\n"
"v_dot2_f32_bf16 v198, v99, v224, 0\n"
"v_dot2_f32_bf16 v199, v99, v225, 0\n"
"v_dot2_f32_bf16 v192, v100, v224, v192\n"
"v_dot2_f32_bf16 v193, v100, v225, v193\n"
"v_dot2_f32_bf16 v194, v101, v224, v194\n"
"v_dot2_f32_bf16 v195, v101, v225, v195\n"
"v_dot2_f32_bf16 v196, v102, v224, v196\n"
"v_dot2_f32_bf16 v197, v102, v225, v197\n"
"v_dot2_f32_bf16 v198, v103, v224, v198\n"
"v_dot2_f32_bf16 v199, v103, v225, v199\n"
"v_dot2_f32_bf16 v192, v104, v224, v192\n"
"v_dot2_f32_bf16 v193, v104, v225, v193\n"
"v_dot2_f32_bf16 v194, v105, v224, v194\n"
"v_dot2_f32_bf16 v195, v105, v225, v195\n"
"v_dot2_f32_bf16 v196, v106, v224, v196\n"
"v_dot2_f32_bf16 v197, v106, v225, v197\n"
"v_dot2_f32_bf16 v198, v107, v224, v198\n"
"v_dot2_f32_bf16 v199, v107, v225, v199\n"
"v_dot2_f32_bf16 v96, v108, v224, v192\n"
"v_dot2_f32_bf16 v97, v108, v225, v193\n"
"v_dot2_f32_bf16 v98, v109, v224, v194\n"
"v_dot2_f32_bf16 v99, v109, v225, v195\n"
"v_dot2_f32_bf16 v100, v110, v224, v196\n"
"v_dot2_f32_bf16 v101, v110, v225, v197\n"
"v_dot2_f32_bf16 v102, v111, v224, v198\n"
"v_dot2_f32_bf16 v103, v111, v225, v199\n"
"v_dot2_f32_bf16 v192, v112, v224, 0\n"
"v_dot2_f32_bf16 v193, v112, v225, 0\n"
"v_dot2_f32_bf16 v194, v113, v224, 0\n"
"v_dot2_f32_bf16 v195, v113, v225, 0\n"
"v_dot2_f32_bf16 v196, v114, v224, 0\n"
"v_dot2_f32_bf16 v197, v114, v225, 0\n"
"v_dot2_f32_bf16 v198, v115, v224, 0\n"
"v_dot2_f32_bf16 v199, v115, v225, 0\n"
"v_dot2_f32_bf16 v192, v116, v224, v192\n"
"v_dot2_f32_bf16 v193, v116, v225, v193\n"
"v_dot2_f32_bf16 v194, v117, v224, v194\n"
"v_dot2_f32_bf16 v195, v117, v225, v195\n"
"v_dot2_f32_bf16 v196, v118, v224, v196\n"
"v_dot2_f32_bf16 v197, v118, v225, v197\n"
"v_dot2_f32_bf16 v198, v119, v224, v198\n"
"v_dot2_f32_bf16 v199, v119, v225, v199\n"
"v_dot2_f32_bf16 v192, v120, v224, v192\n"
"v_dot2_f32_bf16 v193, v120, v225, v193\n"
"v_dot2_f32_bf16 v194, v121, v224, v194\n"
"v_dot2_f32_bf16 v195, v121, v225, v195\n"
"v_dot2_f32_bf16 v196, v122, v224, v196\n"
"v_dot2_f32_bf16 v197, v122, v225, v197\n"
"v_dot2_f32_bf16 v198, v123, v224, v198\n"
"v_dot2_f32_bf16 v199, v123, v225, v199\n"
"v_dot2_f32_bf16 v112, v124, v224, v192\n"
"v_dot2_f32_bf16 v113, v124, v225, v193\n"
"v_dot2_f32_bf16 v114, v125, v224, v194\n"
"v_dot2_f32_bf16 v115, v125, v225, v195\n"
"v_dot2_f32_bf16 v116, v126, v224, v196\n"
"v_dot2_f32_bf16 v117, v126, v225, v197\n"
"v_dot2_f32_bf16 v118, v127, v224, v198\n"
"v_dot2_f32_bf16 v119, v127, v225, v199\n"
"v_dot2_f32_bf16 v192, v128, v224, 0\n"
"v_dot2_f32_bf16 v193, v128, v225, 0\n"
"v_dot2_f32_bf16 v194, v129, v224, 0\n"
"v_dot2_f32_bf16 v195, v129, v225, 0\n"
"v_dot2_f32_bf16 v196, v130, v224, 0\n"
"v_dot2_f32_bf16 v197, v130, v225, 0\n"
"v_dot2_f32_bf16 v198, v131, v224, 0\n"
"v_dot2_f32_bf16 v199, v131, v225, 0\n"
"v_dot2_f32_bf16 v192, v132, v224, v192\n"
"v_dot2_f32_bf16 v193, v132, v225, v193\n"
"v_dot2_f32_bf16 v194, v133, v224, v194\n"
"v_dot2_f32_bf16 v195, v133, v225, v195\n"
"v_dot2_f32_bf16 v196, v134, v224, v196\n"
"v_dot2_f32_bf16 v197, v134, v225, v197\n"
"v_dot2_f32_bf16 v198, v135, v224, v198\n"
"v_dot2_f32_bf16 v199, v135, v225, v199\n"
"v_dot2_f32_bf16 v192, v136, v224, v192\n"
"v_dot2_f32_bf16 v193, v136, v225, v193\n"
"v_dot2_f32_bf16 v194, v137, v224, v194\n"
"v_dot2_f32_bf16 v195, v137, v225, v195\n"
"v_dot2_f32_bf16 v196, v138, v224, v196\n"
"v_dot2_f32_bf16 v197, v138, v225, v197\n"
"v_dot2_f32_bf16 v198, v139, v224, v198\n"
"v_dot2_f32_bf16 v199, v139, v225, v199\n"
"v_dot2_f32_bf16 v128, v140, v224, v192\n"
"v_dot2_f32_bf16 v129, v140, v225, v193\n"
"v_dot2_f32_bf16 v130, v141, v224, v194\n"
"v_dot2_f32_bf16 v131, v141, v225, v195\n"
"v_dot2_f32_bf16 v132, v142, v224, v196\n"
"v_dot2_f32_bf16 v133, v142, v225, v197\n"
"v_dot2_f32_bf16 v134, v143, v224, v198\n"
"v_dot2_f32_bf16 v135, v143, v225, v199\n"
"AT_Ar2:\n"
"s_bitcmp1_b32 s53, 12\n"
"s_cbranch_scc0 AT_Ar3\n"
"s_sub_u32 s54, s54, 1\n"
"s_waitcnt vmcnt(0)\n"
"AT_Ax3:\n"
"v_dot2_f32_bf16 v192, v144, v224, 0\n"
"v_dot2_f32_bf16 v193, v144, v225, 0\n"
"v_dot2_f32_bf16 v194, v145, v224, 0\n"
"v_dot2_f32_bf16 v195, v145, v225, 0\n"
"v_dot2_f32_bf16 v196, v146, v224, 0\n"
"v_dot2_f32_bf16 v197, v146, v225, 0\n"
"v_dot2_f32_bf16 v198, v147, v224, 0\n"
"v_dot2_f32_bf16 v199, v147, v225, 0\n"
"v_dot2_f32_bf16 v192, v148, v224, v192\n"
"v_dot2_f32_bf16 v193, v148, v225, v193\n"
"v_dot2_f32_bf16 v194, v149, v224, v194\n"
"v_dot2_f32_bf16 v195, v149, v225, v195\n"
"v_dot2_f32_bf16 v196, v150, v224, v196\n"
"v_dot2_f32_bf16 v197, v150, v225, v197\n"
"v_dot2_f32_bf16 v198, v151, v224, v198\n"
"v_dot2_f32_bf16 v199, v151, v225, v199\n"
"v_dot2_f32_bf16 v192, v152, v224, v192\n"
"v_dot2_f32_bf16 v193, v152, v225, v193\n"
"v_dot2_f32_bf16 v194, v153, v224, v194\n"
"v_dot2_f32_bf16 v195, v153, v225, v195\n"
"v_dot2_f32_bf16 v196, v154, v224, v196\n"
"v_dot2_f32_bf16 v197, v154, v225, v197\n"
"v_dot2_f32_bf16 v198, v155, v224, v198\n"
"v_dot2_f32_bf16 v199, v155, v225, v199\n"
"v_dot2_f32_bf16 v144, v156, v224, v192\n"
"v_dot2_f32_bf16 v145, v156, v225, v193\n"
"v_dot2_f32_bf16 v146, v157, v224, v194\n"
"v_dot2_f32_bf16 v147, v157, v225, v195\n"
"v_dot2_f32_bf16 v148, v158, v224, v196\n"
"v_dot2_f32_bf16 v149, v158, v225, v197\n"
"v_dot2_f32_bf16 v150, v159, v224, v198\n"
"v_dot2_f32_bf16 v151, v159, v225, v199\n"
"v_dot2_f32_bf16 v192, v160, v224, 0\n"
"v_dot2_f32_bf16 v193, v160, v225, 0\n"
"v_dot2_f32_bf16 v194, v161, v224, 0\n"
"v_dot2_f32_bf16 v195, v161, v225, 0\n"
"v_dot2_f32_bf16 v196, v162, v224, 0\n"
"v_dot2_f32_bf16 v197, v162, v225, 0\n"
"v_dot2_f32_bf16 v198, v163, v224, 0\n"
"v_dot2_f32_bf16 v199, v163, v225, 0\n"
"v_dot2_f32_bf16 v192, v164, v224, v192\n"
"v_dot2_f32_bf16 v193, v164, v225, v193\n"
"v_dot2_f32_bf16 v194, v165, v224, v194\n"
"v_dot2_f32_bf16 v195, v165, v225, v195\n"
"v_dot2_f32_bf16 v196, v166, v224, v196\n"
"v_dot2_f32_bf16 v197, v166, v225, v197\n"
"v_dot2_f32_bf16 v198, v167, v224, v198\n"
"v_dot2_f32_bf16 v199, v167, v225, v199\n"
"v_dot2_f32_bf16 v192, v168, v224, v192\n"
"v_dot2_f32_bf16 v193, v168, v225, v193\n"
"v_dot2_f32_bf16 v194, v169, v224, v194\n"
"v_dot2_f32_bf16 v195, v169, v225, v195\n"
"v_dot2_f32_bf16 v196, v170, v224, v196\n"
"v_dot2_f32_bf16 v197, v170, v225, v197\n"
"v_dot2_f32_bf16 v198, v171, v224, v198\n"
"v_dot2_f32_bf16 v199, v171, v225, v199\n"
"v_dot2_f32_bf16 v160, v172, v224, v192\n"
"v_dot2_f32_bf16 v161, v172, v225, v193\n"
"v_dot2_f32_bf16 v162, v173, v224, v194\n"
"v_dot2_f32_bf16 v163, v173, v225, v195\n"
"v_dot2_f32_bf16 v164, v174, v224, v196\n"
"v_dot2_f32_bf16 v165, v174, v225, v197\n"
"v_dot2_f32_bf16 v166, v175, v224, v198\n"
"v_dot2_f32_bf16 v167, v175, v225, v199\n"
"v_dot2_f32_bf16 v192, v176, v224, 0\n"
"v_dot2_f32_bf16 v193, v176, v225, 0\n"
"v_dot2_f32_bf16 v194, v177, v224, 0\n"
"v_dot2_f32_bf16 v195, v177, v225, 0\n"
"v_dot2_f32_bf16 v196, v178, v224, 0\n"
"v_dot2_f32_bf16 v197, v178, v225, 0\n"
"v_dot2_f32_bf16 v198, v179, v224, 0\n"
"v_dot2_f32_bf16 v199, v179, v225, 0\n"
"v_dot2_f32_bf16 v192, v180, v224, v192\n"
"v_dot2_f32_bf16 v193, v180, v225, v193\n"
"v_dot2_f32_bf16 v194, v181, v224, v194\n"
"v_dot2_f32_bf16 v195, v181, v225, v195\n"
"v_dot2_f32_bf16 v196, v182, v224, v196\n"
"v_dot2_f32_bf16 v197, v182, v225, v197\n"
"v_dot2_f32_bf16 v198, v183, v224, v198\n"
"v_dot2_f32_bf16 v199, v183, v225, v199\n"
"v_dot2_f32_bf16 v192, v184, v224, v192\n"
"v_dot2_f32_bf16 v193, v184, v225, v193\n"
"v_dot2_f32_bf16 v194, v185, v224, v194\n"
"v_dot2_f32_bf16 v195, v185, v225, v195\n"
"v_dot2_f32_bf16 v196, v186, v224, v196\n"
"v_dot2_f32_bf16 v197, v186, v225, v197\n"
"v_dot2_f32_bf16 v198, v187, v224, v198\n"
"v_dot2_f32_bf16 v199, v187, v225, v199\n"
"v_dot2_f32_bf16 v176, v188, v224, v192\n"
"v_dot2_f32_bf16 v177, v188, v225, v193\n"
"v_dot2_f32_bf16 v178, v189, v224, v194\n"
"v_dot2_f32_bf16 v179, v189, v225, v195\n"
"v_dot2_f32_bf16 v180, v190, v224, v196\n"
"v_dot2_f32_bf16 v181, v190, v225, v197\n"
"v_dot2_f32_bf16 v182, v191, v224, v198\n"
"v_dot2_f32_bf16 v183, v191, v225, v199\n"
"AT_Ar3:\n"
"s_or_b32 s52, s52, s53\n"
"s_cmp_lg_u32 s52, 0x1111\n"
"s_cbranch_scc1 AT_A1\n"
"ds_read_b128 v[8:11], v244 offset:4096\n"
"ds_read_b128 v[12:15], v244 offset:4160\n"
"ds_read_b128 v[24:27], v244 offset:4864\n"
"ds_read_b128 v[28:31], v244 offset:4928\n"
"ds_read_b128 v[40:43], v244 offset:5632\n"
"ds_read_b128 v[44:47], v244 offset:5696\n"
"v_pk_add_f32 v[0:1], v[0:1], v[48:49]\n"
"v_pk_add_f32 v[96:97], v[96:97], v[144:145]\n"
"v_pk_add_f32 v[2:3], v[2:3], v[50:51]\n"
"v_pk_add_f32 v[98:99], v[98:99], v[146:147]\n"
"v_pk_add_f32 v[4:5], v[4:5], v[52:53]\n"
"v_pk_add_f32 v[100:101], v[100:101], v[148:149]\n"
"v_pk_add_f32 v[6:7], v[6:7], v[54:55]\n"
"v_pk_add_f32 v[102:103], v[102:103], v[150:151]\n"
"v_pk_add_f32 v[16:17], v[16:17], v[64:65]\n"
"v_pk_add_f32 v[112:113], v[112:113], v[160:161]\n"
"v_pk_add_f32 v[18:19], v[18:19], v[66:67]\n"
"v_pk_add_f32 v[114:115], v[114:115], v[162:163]\n"
"v_pk_add_f32 v[20:21], v[20:21], v[68:69]\n"
"v_pk_add_f32 v[116:117], v[116:117], v[164:165]\n"
"v_pk_add_f32 v[22:23], v[22:23], v[70:71]\n"
"v_pk_add_f32 v[118:119], v[118:119], v[166:167]\n"
"v_pk_add_f32 v[32:33], v[32:33], v[80:81]\n"
"v_pk_add_f32 v[128:129], v[128:129], v[176:177]\n"
"v_pk_add_f32 v[34:35], v[34:35], v[82:83]\n"
"v_pk_add_f32 v[130:131], v[130:131], v[178:179]\n"
"v_pk_add_f32 v[36:37], v[36:37], v[84:85]\n"
"v_pk_add_f32 v[132:133], v[132:133], v[180:181]\n"
"v_pk_add_f32 v[38:39], v[38:39], v[86:87]\n"
"v_pk_add_f32 v[134:135], v[134:135], v[182:183]\n"
"v_pk_add_f32 v[0:1], v[0:1], v[96:97]\n"
"v_pk_add_f32 v[2:3], v[2:3], v[98:99]\n"
"v_pk_add_f32 v[4:5], v[4:5], v[100:101]\n"
"v_pk_add_f32 v[6:7], v[6:7], v[102:103]\n"
"v_pk_add_f32 v[16:17], v[16:17], v[112:113]\n"
"v_pk_add_f32 v[18:19], v[18:19], v[114:115]\n"
"v_pk_add_f32 v[20:21], v[20:21], v[116:117]\n"
"v_pk_add_f32 v[22:23], v[22:23], v[118:119]\n"
"v_pk_add_f32 v[32:33], v[32:33], v[128:129]\n"
"v_pk_add_f32 v[34:35], v[34:35], v[130:131]\n"
"v_pk_add_f32 v[36:37], v[36:37], v[132:133]\n"
"v_pk_add_f32 v[38:39], v[38:39], v[134:135]\n"
"v_pk_mul_f32 v[0:1], v[0:1], v[200:201]\n"
"v_pk_mul_f32 v[2:3], v[2:3], v[202:203]\n"
"v_pk_mul_f32 v[4:5], v[4:5], v[204:205]\n"
"v_pk_mul_f32 v[6:7], v[6:7], v[206:207]\n"
"v_pk_mul_f32 v[16:17], v[16:17], v[208:209]\n"
"v_pk_mul_f32 v[18:19], v[18:19], v[210:211]\n"
"v_pk_mul_f32 v[20:21], v[20:21], v[212:213]\n"
"v_pk_mul_f32 v[22:23], v[22:23], v[214:215]\n"
"v_pk_mul_f32 v[32:33], v[32:33], v[216:217]\n"
"v_pk_mul_f32 v[34:35], v[34:35], v[218:219]\n"
"v_pk_mul_f32 v[36:37], v[36:37], v[220:221]\n"
"v_pk_mul_f32 v[38:39], v[38:39], v[222:223]\n"
"v_dot2_f32_bf16 v0, v228, v224, v0\n"
"v_dot2_f32_bf16 v1, v228, v225, v1\n"
"v_dot2_f32_bf16 v2, v229, v224, v2\n"
"v_dot2_f32_bf16 v3, v229, v225, v3\n"
"v_dot2_f32_bf16 v4, v230, v224, v4\n"
"v_dot2_f32_bf16 v5, v230, v225, v5\n"
"v_dot2_f32_bf16 v6, v231, v224, v6\n"
"v_dot2_f32_bf16 v7, v231, v225, v7\n"
"v_dot2_f32_bf16 v16, v232, v224, v16\n"
"v_dot2_f32_bf16 v17, v232, v225, v17\n"
"v_dot2_f32_bf16 v18, v233, v224, v18\n"
"v_dot2_f32_bf16 v19, v233, v225, v19\n"
"v_dot2_f32_bf16 v20, v234, v224, v20\n"
"v_dot2_f32_bf16 v21, v234, v225, v21\n"
"v_dot2_f32_bf16 v22, v235, v224, v22\n"
"v_dot2_f32_bf16 v23, v235, v225, v23\n"
"v_dot2_f32_bf16 v32, v236, v224, v32\n"
"v_dot2_f32_bf16 v33, v236, v225, v33\n"
"v_dot2_f32_bf16 v34, v237, v224, v34\n"
"v_dot2_f32_bf16 v35, v237, v225, v35\n"
"v_dot2_f32_bf16 v36, v238, v224, v36\n"
"v_dot2_f32_bf16 v37, v238, v225, v37\n"
"v_dot2_f32_bf16 v38, v239, v224, v38\n"
"v_dot2_f32_bf16 v39, v239, v225, v39\n"
"ds_read_b128 v[56:59], v244 offset:8192\n"
"ds_read_b128 v[60:63], v244 offset:8256\n"
"ds_read_b128 v[72:75], v244 offset:8960\n"
"ds_read_b128 v[76:79], v244 offset:9024\n"
"ds_read_b128 v[88:91], v244 offset:9728\n"
"ds_read_b128 v[92:95], v244 offset:9792\n"
"ds_read_b128 v[104:107], v244 offset:12288\n"
"ds_read_b128 v[108:111], v244 offset:12352\n"
"ds_read_b128 v[120:123], v244 offset:13056\n"
"ds_read_b128 v[124:127], v244 offset:13120\n"
"ds_read_b128 v[136:139], v244 offset:13824\n"
"ds_read_b128 v[140:143], v244 offset:13888\n"
"ds_read_b128 v[152:155], v244 offset:16384\n"
"ds_read_b128 v[156:159], v244 offset:16448\n"
"ds_read_b128 v[168:171], v244 offset:17152\n"
"ds_read_b128 v[172:175], v244 offset:17216\n"
"ds_read_b128 v[184:187], v244 offset:17920\n"
"ds_read_b128 v[188:191], v244 offset:17984\n"
"v_pk_mul_f32 v[194:195], v[0:1], v[0:1]\n"
"s_waitcnt lgkmcnt(14)\n"
"v_pk_mul_f32 v[196:197], v[0:1], v[8:9]\n"
"v_pk_add_f32 v[192:193], v[0:1], v[2:3]\n"
"v_pk_fma_f32 v[194:195], v[2:3], v[2:3], v[194:195]\n"
"v_pk_fma_f32 v[196:197], v[2:3], v[10:11], v[196:197]\n"
"v_pk_add_f32 v[192:193], v[192:193], v[4:5]\n"
"v_pk_fma_f32 v[194:195], v[4:5], v[4:5], v[194:195]\n"
"v_pk_fma_f32 v[196:197], v[4:5], v[12:13], v[196:197]\n"
"v_pk_add_f32 v[192:193], v[192:193], v[6:7]\n"
"v_pk_fma_f32 v[194:195], v[6:7], v[6:7], v[194:195]\n"
"v_pk_fma_f32 v[196:197], v[6:7], v[14:15], v[196:197]\n"
"v_pk_add_f32 v[192:193], v[192:193], v[16:17]\n"
"v_pk_fma_f32 v[194:195], v[16:17], v[16:17], v[194:195]\n"
"v_pk_fma_f32 v[196:197], v[16:17], v[24:25], v[196:197]\n"
"v_pk_add_f32 v[192:193], v[192:193], v[18:19]\n"
"v_pk_fma_f32 v[194:195], v[18:19], v[18:19], v[194:195]\n"
"v_pk_fma_f32 v[196:197], v[18:19], v[26:27], v[196:197]\n"
"v_pk_add_f32 v[192:193], v[192:193], v[20:21]\n"
"v_pk_fma_f32 v[194:195], v[20:21], v[20:21], v[194:195]\n"
"v_pk_fma_f32 v[196:197], v[20:21], v[28:29], v[196:197]\n"
"v_pk_add_f32 v[192:193], v[192:193], v[22:23]\n"
"v_pk_fma_f32 v[194:195], v[22:23], v[22:23], v[194:195]\n"
"v_pk_fma_f32 v[196:197], v[22:23], v[30:31], v[196:197]\n"
"v_pk_add_f32 v[192:193], v[192:193], v[32:33]\n"
"v_pk_fma_f32 v[194:195], v[32:33], v[32:33], v[194:195]\n"
"v_pk_fma_f32 v[196:197], v[32:33], v[40:41], v[196:197]\n"
"v_pk_add_f32 v[192:193], v[192:193], v[34:35]\n"
"v_pk_fma_f32 v[194:195], v[34:35], v[34:35], v[194:195]\n"
"v_pk_fma_f32 v[196:197], v[34:35], v[42:43], v[196:197]\n"
"v_pk_add_f32 v[192:193], v[192:193], v[36:37]\n"
"v_pk_fma_f32 v[194:195], v[36:37], v[36:37], v[194:195]\n"
"v_pk_fma_f32 v[196:197], v[36:37], v[44:45], v[196:197]\n"
"v_pk_add_f32 v[192:193], v[192:193], v[38:39]\n"
"v_pk_fma_f32 v[194:195], v[38:39], v[38:39], v[194:195]\n"
"v_pk_fma_f32 v[196:197], v[38:39], v[46:47], v[196:197]\n"
"v_add_f32 v192, v192, v193\n"
"v_add_f32 v193, v194, v195\n"
"v_add_f32 v194, v196, v197\n"
"s_lshl_b32 s56, s41, 7\n"
"v_add_u32 v199, s56, v226\n"
"v_add_f32_dpp v192, v192, v192 row_ror:8 row_mask:0xf bank_mask:0xf\n"
"v_add_f32_dpp v193, v193, v193 row_ror:8 row_mask:0xf bank_mask:0xf\n"
"v_add_f32_dpp v194, v194, v194 row_ror:8 row_mask:0xf bank_mask:0xf\n"
"v_mov_b32 v196, v192\n"
"v_mov_b32 v197, v193\n"
"v_mov_b32 v198, v194\n"
"v_permlane16_swap_b32 v192, v196\n"
"v_permlane16_swap_b32 v193, v197\n"
"v_permlane16_swap_b32 v194, v198\n"
"v_add_f32 v192, v192, v196\n"
"v_add_f32 v193, v193, v197\n"
"v_add_f32 v194, v194, v198\n"
"v_mov_b32 v196, v192\n"
"v_mov_b32 v197, v193\n"
"v_mov_b32 v198, v194\n"
"v_permlane32_swap_b32 v192, v196\n"
"v_permlane32_swap_b32 v193, v197\n"
"v_permlane32_swap_b32 v194, v198\n"
"v_add_f32 v192, v192, v196\n"
"v_add_f32 v193, v193, v197\n"
"v_add_f32 v194, v194, v198\n"
"s_mov_b64 exec, 0xff\n"
"ds_write_b128 v199, v[192:195]\n"
"s_mov_b64 exec, -1\n"
"v_mov_b32 v49, s85\n"
"v_mov_b32 v51, s85\n"
"v_mov_b32 v53, s85\n"
"v_mov_b32 v55, s85\n"
"v_mov_b32 v97, s85\n"
"v_mov_b32 v99, s85\n"
"v_mov_b32 v101, s85\n"
"v_mov_b32 v103, s85\n"
"v_mov_b32 v65, s85\n"
"v_mov_b32 v67, s85\n"
"v_mov_b32 v69, s85\n"
"v_mov_b32 v71, s85\n"
"v_mov_b32 v113, s85\n"
"v_mov_b32 v115, s85\n"
"v_mov_b32 v117, s85\n"
"v_mov_b32 v119, s85\n"
"v_mov_b32 v81, s85\n"
"v_mov_b32 v83, s85\n"
"v_mov_b32 v85, s85\n"
"v_mov_b32 v87, s85\n"
"v_mov_b32 v129, s85\n"
"v_mov_b32 v131, s85\n"
"v_mov_b32 v133, s85\n"
"v_mov_b32 v135, s85\n"
"AT_B2:\n"
"s_waitcnt lgkmcnt(0)\n"
"s_barrier\n"
"ds_read_b128 v[200:203], v226 offset:0\n"
"ds_read_b128 v[204:207], v226 offset:128\n"
"ds_read_b128 v[208:211], v226 offset:256\n"
"ds_read_b128 v[212:215], v227 offset:512\n"
"ds_read_b128 v[216:219], v227 offset:528\n"
"s_lshl_b32 s56, s55, 3\n"
"s_sub_u32 s56, 2047, s56\n"
"v_and_b32 v199, 7, v248\n"
"v_sub_u32 v199, s56, v199\n"
"s_waitcnt lgkmcnt(3)\n"
"v_add_f32 v200, v200, v204\n"
"v_add_f32 v201, v201, v205\n"
"v_add_f32 v202, v202, v206\n"
"s_waitcnt lgkmcnt(2)\n"
"v_add_f32 v200, v200, v208\n"
"v_add_f32 v201, v201, v209\n"
"v_add_f32 v202, v202, v210\n"
"v_mul_f32 v200, 0x3ae38e39, v200\n"
"v_mul_f32 v201, 0x3ae38e39, v201\n"
"s_waitcnt lgkmcnt(1)\n"
"v_add_f32 v212, v212, v214\n"
"v_fma_f32 v201, -v200, v200, v201\n"
"v_add_f32 v213, v213, v215\n"
"v_max_f32 v201, 0, v201\n"
"s_waitcnt lgkmcnt(0)\n"
"v_add_f32 v212, v212, v216\n"
"v_add_f32 v201, 0x3727c5ac, v201\n"
"v_add_f32 v213, v213, v217\n"
"v_rsq_f32 v192, v201\n"
"v_fma_f32 v202, -v200, v212, v202\n"
"v_fma_f32 v196, v192, v202, v213\n"
"v_mul_f32 v194, v200, v192\n"
"v_ashrrev_i32 v203, 31, v196\n"
"v_sub_f32 v194, 0, v194\n"
"v_or_b32 v203, 0x80000000, v203\n"
"v_xor_b32 v196, v196, v203\n"
"v_and_or_b32 v196, v196, s14, v199\n"
"s_nop 1\n"
"v_mov_b32_dpp v203, v196 row_ror:1 row_mask:0xf bank_mask:0xf\n"
"v_min_u32 v198, v196, v203\n"
"v_max_u32 v197, v196, v203\n"
"s_nop 0\n"
"v_mov_b32_dpp v204, v198 row_ror:2 row_mask:0xf bank_mask:0xf\n"
"v_mov_b32_dpp v203, v197 row_ror:2 row_mask:0xf bank_mask:0xf\n"
"v_min_u32 v205, v197, v203\n"
"v_max_u32 v197, v197, v203\n"
"v_max3_u32 v198, v198, v204, v205\n"
"s_nop 0\n"
"v_mov_b32_dpp v203, v197 row_ror:4 row_mask:0xf bank_mask:0xf\n"
"v_mov_b32_dpp v204, v198 row_ror:4 row_mask:0xf bank_mask:0xf\n"
"v_min_u32 v205, v197, v203\n"
"v_max_u32 v197, v197, v203\n"
"v_max3_u32 v198, v198, v204, v205\n"
"s_cmp_lg_u32 s41, 3\n"
"s_cbranch_scc1 AT_A3\n"
"v_mov_b32 v144, v197\n"
"v_mov_b32 v145, s85\n"
"v_mov_b32 v146, v198\n"
"v_mov_b32 v147, s85\n"
"s_lshl_b32 s56, s55, 4\n"
"v_mov_b32 v148, 16384\n"
"s_mov_b64 exec, 1\n"
"buffer_store_dwordx4 v[144:147], v148, s[28:31], s56 offen sc1\n"
"s_mov_b64 exec, -1\n"
"v_lshlrev_b32 v149, 4, v248\n"
"s_mov_b32 s57, 16384\n"
"s_mov_b32 s87, 0x80000\n"
"AT_S2:\n"
"buffer_load_dwordx4 v[152:155], v149, s[28:31], s57 offen offset:0 sc1\n"
"buffer_load_dwordx4 v[156:159], v149, s[28:31], s57 offen offset:1024 sc1\n"
"buffer_load_dwordx4 v[160:163], v149, s[28:31], s57 offen offset:2048 sc1\n"
"buffer_load_dwordx4 v[164:167], v149, s[28:31], s57 offen offset:3072 sc1\n"
"s_waitcnt vmcnt(0)\n"
"v_xor_b32 v150, s85, v153\n"
"v_xor_b32 v151, s85, v155\n"
"v_bitop3_b32 v150, v157, s85, v150 bitop3:0xbe\n"
"v_bitop3_b32 v151, v159, s85, v151 bitop3:0xbe\n"
"v_bitop3_b32 v150, v161, s85, v150 bitop3:0xbe\n"
"v_bitop3_b32 v151, v163, s85, v151 bitop3:0xbe\n"
"v_bitop3_b32 v150, v165, s85, v150 bitop3:0xbe\n"
"v_bitop3_b32 v151, v167, s85, v151 bitop3:0xbe\n"
"v_or_b32 v150, v150, v151\n"
"v_cmp_ne_u32 vcc, 0, v150\n"
"s_nop 0\n"
"s_cbranch_vccz AT_S3\n"
"s_sub_u32 s87, s87, 1\n"
"s_cmp_eq_u32 s87, 0\n"
"s_cbranch_scc1 AT_S3\n"
"s_sleep 1\n"
"s_branch AT_S2\n"
"AT_S3:\n"
"v_min_u32 v205, v152, v156\n"
"v_max_u32 v152, v152, v156\n"
"v_max3_u32 v154, v154, v158, v205\n"
"v_min_u32 v205, v160, v164\n"
"v_max_u32 v160, v160, v164\n"
"v_max3_u32 v162, v162, v166, v205\n"
"v_min_u32 v205, v152, v160\n"
"v_max_u32 v152, v152, v160\n"
"v_max3_u32 v154, v154, v162, v205\n"
"s_nop 0\n"
"v_mov_b32_dpp v156, v152 row_ror:1 row_mask:0xf bank_mask:0xf\n"
"v_mov_b32_dpp v158, v154 row_ror:1 row_mask:0xf bank_mask:0xf\n"
"v_min_u32 v205, v152, v156\n"
"v_max_u32 v152, v152, v156\n"
"v_max3_u32 v154, v154, v158, v205\n"
"s_nop 0\n"
"v_mov_b32_dpp v156, v152 row_ror:2 row_mask:0xf bank_mask:0xf\n"
"v_mov_b32_dpp v158, v154 row_ror:2 row_mask:0xf bank_mask:0xf\n"
"v_min_u32 v205, v152, v156\n"
"v_max_u32 v152, v152, v156\n"
"v_max3_u32 v154, v154, v158, v205\n"
"s_nop 0\n"
"v_mov_b32_dpp v156, v152 row_ror:4 row_mask:0xf bank_mask:0xf\n"
"v_mov_b32_dpp v158, v154 row_ror:4 row_mask:0xf bank_mask:0xf\n"
"v_min_u32 v205, v152, v156\n"
"v_max_u32 v152, v152, v156\n"
"v_max3_u32 v154, v154, v158, v205\n"
"s_nop 0\n"
"v_mov_b32_dpp v156, v152 row_ror:8 row_mask:0xf bank_mask:0xf\n"
"v_mov_b32_dpp v158, v154 row_ror:8 row_mask:0xf bank_mask:0xf\n"
"v_min_u32 v205, v152, v156\n"
"v_max_u32 v152, v152, v156\n"
"v_max3_u32 v154, v154, v158, v205\n"
"v_mov_b32 v156, v152\n"
"v_mov_b32 v158, v154\n"
"s_nop 0\n"
"v_permlane16_swap_b32 v152, v156\n"
"v_permlane16_swap_b32 v154, v158\n"
"v_min_u32 v205, v152, v156\n"
"v_max_u32 v152, v152, v156\n"
"v_max3_u32 v154, v154, v158, v205\n"
"v_mov_b32 v156, v152\n"
"v_mov_b32 v158, v154\n"
"s_nop 0\n"
"v_permlane32_swap_b32 v152, v156\n"
"v_permlane32_swap_b32 v154, v158\n"
"v_min_u32 v205, v152, v156\n"
"v_max_u32 v152, v152, v156\n"
"v_max3_u32 v154, v154, v158, v205\n"
"v_mov_b32 v153, v154\n"
"s_mov_b64 exec, 1\n"
"ds_write_b64 v227, v[152:153] offset:656\n"
"s_mov_b64 exec, -1\n"
"s_branch AT_B3\n"
"AT_A3:\n"
"v_cmp_ge_u32 s[8:9], v196, v198\n"
"v_pk_fma_f32 v[144:145], v[0:1], v[192:193], v[194:195] op_sel_hi:[1,0,0]\n"
"v_pk_fma_f32 v[146:147], v[2:3], v[192:193], v[194:195] op_sel_hi:[1,0,0]\n"
"v_pk_fma_f32 v[148:149], v[4:5], v[192:193], v[194:195] op_sel_hi:[1,0,0]\n"
"v_pk_fma_f32 v[150:151], v[6:7], v[192:193], v[194:195] op_sel_hi:[1,0,0]\n"
"v_pk_fma_f32 v[160:161], v[16:17], v[192:193], v[194:195] op_sel_hi:[1,0,0]\n"
"v_pk_fma_f32 v[162:163], v[18:19], v[192:193], v[194:195] op_sel_hi:[1,0,0]\n"
"v_pk_fma_f32 v[164:165], v[20:21], v[192:193], v[194:195] op_sel_hi:[1,0,0]\n"
"v_pk_fma_f32 v[166:167], v[22:23], v[192:193], v[194:195] op_sel_hi:[1,0,0]\n"
"v_pk_fma_f32 v[176:177], v[32:33], v[192:193], v[194:195] op_sel_hi:[1,0,0]\n"
"v_pk_fma_f32 v[178:179], v[34:35], v[192:193], v[194:195] op_sel_hi:[1,0,0]\n"
"v_pk_fma_f32 v[180:181], v[36:37], v[192:193], v[194:195] op_sel_hi:[1,0,0]\n"
"v_pk_fma_f32 v[182:183], v[38:39], v[192:193], v[194:195] op_sel_hi:[1,0,0]\n"
"v_fma_f32 v48, v144, v56, v104\n"
"v_fma_f32 v50, v145, v57, v105\n"
"v_fma_f32 v52, v146, v58, v106\n"
"v_fma_f32 v54, v147, v59, v107\n"
"v_fma_f32 v96, v148, v60, v108\n"
"v_fma_f32 v98, v149, v61, v109\n"
"v_fma_f32 v100, v150, v62, v110\n"
"v_fma_f32 v102, v151, v63, v111\n"
"v_fma_f32 v64, v160, v72, v120\n"
"v_fma_f32 v66, v161, v73, v121\n"
"v_fma_f32 v68, v162, v74, v122\n"
"v_fma_f32 v70, v163, v75, v123\n"
"v_fma_f32 v112, v164, v76, v124\n"
"v_fma_f32 v114, v165, v77, v125\n"
"v_fma_f32 v116, v166, v78, v126\n"
"v_fma_f32 v118, v167, v79, v127\n"
"v_fma_f32 v80, v176, v88, v136\n"
"v_fma_f32 v82, v177, v89, v137\n"
"v_fma_f32 v84, v178, v90, v138\n"
"v_fma_f32 v86, v179, v91, v139\n"
"v_fma_f32 v128, v180, v92, v140\n"
"v_fma_f32 v130, v181, v93, v141\n"
"v_fma_f32 v132, v182, v94, v142\n"
"v_fma_f32 v134, v183, v95, v143\n"
"s_mov_b64 exec, s[8:9]\n"
"buffer_store_dwordx4 v[48:51], v245, s[28:31], s48 offen offset:0 sc1\n"
"buffer_store_dwordx4 v[52:55], v245, s[28:31], s48 offen offset:16 sc1\n"
"buffer_store_dwordx4 v[96:99], v245, s[28:31], s48 offen offset:128 sc1\n"
"buffer_store_dwordx4 v[100:103], v245, s[28:31], s48 offen offset:144 sc1\n"
"buffer_store_dwordx4 v[64:67], v245, s[28:31], s48 offen offset:1536 sc1\n"
"buffer_store_dwordx4 v[68:71], v245, s[28:31], s48 offen offset:1552 sc1\n"
"buffer_store_dwordx4 v[112:115], v245, s[28:31], s48 offen offset:1664 sc1\n"
"buffer_store_dwordx4 v[116:119], v245, s[28:31], s48 offen offset:1680 sc1\n"
"buffer_store_dwordx4 v[80:83], v245, s[28:31], s48 offen offset:3072 sc1\n"
"buffer_store_dwordx4 v[84:87], v245, s[28:31], s48 offen offset:3088 sc1\n"
"buffer_store_dwordx4 v[128:131], v245, s[28:31], s48 offen offset:3200 sc1\n"
"buffer_store_dwordx4 v[132:135], v245, s[28:31], s48 offen offset:3216 sc1\n"
"s_mov_b64 exec, -1\n"
"v_fma_f32 v200, v152, v48, v0\n"
"v_fma_f32 v201, v153, v50, v1\n"
"v_fma_f32 v202, v154, v52, v2\n"
"v_fma_f32 v203, v155, v54, v3\n"
"v_fma_f32 v204, v156, v96, v4\n"
"v_fma_f32 v205, v157, v98, v5\n"
"v_fma_f32 v206, v158, v100, v6\n"
"v_fma_f32 v207, v159, v102, v7\n"
"v_fma_f32 v208, v168, v64, v16\n"
"v_fma_f32 v209, v169, v66, v17\n"
"v_fma_f32 v210, v170, v68, v18\n"
"v_fma_f32 v211, v171, v70, v19\n"
"v_fma_f32 v212, v172, v112, v20\n"
"v_fma_f32 v213, v173, v114, v21\n"
"v_fma_f32 v214, v174, v116, v22\n"
"v_fma_f32 v215, v175, v118, v23\n"
"v_fma_f32 v216, v184, v80, v32\n"
"v_fma_f32 v217, v185, v82, v33\n"
"v_fma_f32 v218, v186, v84, v34\n"
"v_fma_f32 v219, v187, v86, v35\n"
"v_fma_f32 v220, v188, v128, v36\n"
"v_fma_f32 v221, v189, v130, v37\n"
"v_fma_f32 v222, v190, v132, v38\n"
"v_fma_f32 v223, v191, v134, v39\n"
"v_cvt_pk_bf16_f32 v228, v200, v201\n"
"v_cvt_pk_bf16_f32 v229, v202, v203\n"
"v_cvt_pk_bf16_f32 v230, v204, v205\n"
"v_cvt_pk_bf16_f32 v231, v206, v207\n"
"v_cvt_pk_bf16_f32 v232, v208, v209\n"
"v_cvt_pk_bf16_f32 v233, v210, v211\n"
"v_cvt_pk_bf16_f32 v234, v212, v213\n"
"v_cvt_pk_bf16_f32 v235, v214, v215\n"
"v_cvt_pk_bf16_f32 v236, v216, v217\n"
"v_cvt_pk_bf16_f32 v237, v218, v219\n"
"v_cvt_pk_bf16_f32 v238, v220, v221\n"
"v_cvt_pk_bf16_f32 v239, v222, v223\n"
"s_not_b64 exec, s[8:9]\n"
"buffer_store_dwordx2 v[228:229], v240, s[76:79], s46 offen offset:0\n"
"buffer_store_dwordx2 v[230:231], v240, s[76:79], s46 offen offset:32\n"
"buffer_store_dwordx2 v[232:233], v240, s[76:79], s46 offen offset:384\n"
"buffer_store_dwordx2 v[234:235], v240, s[76:79], s46 offen offset:416\n"
"buffer_store_dwordx2 v[236:237], v240, s[76:79], s46 offen offset:768\n"
"buffer_store_dwordx2 v[238:239], v240, s[76:79], s46 offen offset:800\n"
"s_mov_b64 exec, -1\n"
"v_mov_b32 v225, v196\n"
"v_mov_b32 v224, 0xffff0000\n"
"v_accvgpr_read_b32 v1, a48\n"
"v_accvgpr_read_b32 v3, a49\n"
"v_accvgpr_read_b32 v5, a50\n"
"v_accvgpr_read_b32 v7, a51\n"
"v_lshlrev_b32 v0, 16, v1\n"
"v_and_b32 v1, v224, v1\n"
"v_lshlrev_b32 v2, 16, v3\n"
"v_and_b32 v3, v224, v3\n"
"v_lshlrev_b32 v4, 16, v5\n"
"v_and_b32 v5, v224, v5\n"
"v_lshlrev_b32 v6, 16, v7\n"
"v_and_b32 v7, v224, v7\n"
"v_accvgpr_read_b32 v9, a84\n"
"v_accvgpr_read_b32 v11, a85\n"
"v_accvgpr_read_b32 v13, a86\n"
"v_accvgpr_read_b32 v15, a87\n"
"v_lshlrev_b32 v8, 16, v9\n"
"v_and_b32 v9, v224, v9\n"
"v_lshlrev_b32 v10, 16, v11\n"
"v_and_b32 v11, v224, v11\n"
"v_lshlrev_b32 v12, 16, v13\n"
"v_and_b32 v13, v224, v13\n"
"v_lshlrev_b32 v14, 16, v15\n"
"v_and_b32 v15, v224, v15\n"
"v_accvgpr_read_b32 v17, a52\n"
"v_accvgpr_read_b32 v19, a53\n"
"v_accvgpr_read_b32 v21, a54\n"
"v_accvgpr_read_b32 v23, a55\n"
"v_lshlrev_b32 v16, 16, v17\n"
"v_and_b32 v17, v224, v17\n"
"v_lshlrev_b32 v18, 16, v19\n"
"v_and_b32 v19, v224, v19\n"
"v_lshlrev_b32 v20, 16, v21\n"
"v_and_b32 v21, v224, v21\n"
"v_lshlrev_b32 v22, 16, v23\n"
"v_and_b32 v23, v224, v23\n"
"v_accvgpr_read_b32 v25, a88\n"
"v_accvgpr_read_b32 v27, a89\n"
"v_accvgpr_read_b32 v29, a90\n"
"v_accvgpr_read_b32 v31, a91\n"
"v_lshlrev_b32 v24, 16, v25\n"
"v_and_b32 v25, v224, v25\n"
"v_lshlrev_b32 v26, 16, v27\n"
"v_and_b32 v27, v224, v27\n"
"v_lshlrev_b32 v28, 16, v29\n"
"v_and_b32 v29, v224, v29\n"
"v_lshlrev_b32 v30, 16, v31\n"
"v_and_b32 v31, v224, v31\n"
"v_accvgpr_read_b32 v33, a56\n"
"v_accvgpr_read_b32 v35, a57\n"
"v_accvgpr_read_b32 v37, a58\n"
"v_accvgpr_read_b32 v39, a59\n"
"v_lshlrev_b32 v32, 16, v33\n"
"v_and_b32 v33, v224, v33\n"
"v_lshlrev_b32 v34, 16, v35\n"
"v_and_b32 v35, v224, v35\n"
"v_lshlrev_b32 v36, 16, v37\n"
"v_and_b32 v37, v224, v37\n"
"v_lshlrev_b32 v38, 16, v39\n"
"v_and_b32 v39, v224, v39\n"
"v_accvgpr_read_b32 v41, a92\n"
"v_accvgpr_read_b32 v43, a93\n"
"v_accvgpr_read_b32 v45, a94\n"
"v_accvgpr_read_b32 v47, a95\n"
"v_lshlrev_b32 v40, 16, v41\n"
"v_and_b32 v41, v224, v41\n"
"v_lshlrev_b32 v42, 16, v43\n"
"v_and_b32 v43, v224, v43\n"
"v_lshlrev_b32 v44, 16, v45\n"
"v_and_b32 v45, v224, v45\n"
"v_lshlrev_b32 v46, 16, v47\n"
"v_and_b32 v47, v224, v47\n"
"v_accvgpr_read_b32 v49, a60\n"
"v_accvgpr_read_b32 v51, a61\n"
"v_accvgpr_read_b32 v53, a62\n"
"v_accvgpr_read_b32 v55, a63\n"
"v_lshlrev_b32 v48, 16, v49\n"
"v_and_b32 v49, v224, v49\n"
"v_lshlrev_b32 v50, 16, v51\n"
"v_and_b32 v51, v224, v51\n"
"v_lshlrev_b32 v52, 16, v53\n"
"v_and_b32 v53, v224, v53\n"
"v_lshlrev_b32 v54, 16, v55\n"
"v_and_b32 v55, v224, v55\n"
"v_accvgpr_read_b32 v57, a96\n"
"v_accvgpr_read_b32 v59, a97\n"
"v_accvgpr_read_b32 v61, a98\n"
"v_accvgpr_read_b32 v63, a99\n"
"v_lshlrev_b32 v56, 16, v57\n"
"v_and_b32 v57, v224, v57\n"
"v_lshlrev_b32 v58, 16, v59\n"
"v_and_b32 v59, v224, v59\n"
"v_lshlrev_b32 v60, 16, v61\n"
"v_and_b32 v61, v224, v61\n"
"v_lshlrev_b32 v62, 16, v63\n"
"v_and_b32 v63, v224, v63\n"
"v_accvgpr_read_b32 v65, a64\n"
"v_accvgpr_read_b32 v67, a65\n"
"v_accvgpr_read_b32 v69, a66\n"
"v_accvgpr_read_b32 v71, a67\n"
"v_lshlrev_b32 v64, 16, v65\n"
"v_and_b32 v65, v224, v65\n"
"v_lshlrev_b32 v66, 16, v67\n"
"v_and_b32 v67, v224, v67\n"
"v_lshlrev_b32 v68, 16, v69\n"
"v_and_b32 v69, v224, v69\n"
"v_lshlrev_b32 v70, 16, v71\n"
"v_and_b32 v71, v224, v71\n"
"v_accvgpr_read_b32 v73, a100\n"
"v_accvgpr_read_b32 v75, a101\n"
"v_accvgpr_read_b32 v77, a102\n"
"v_accvgpr_read_b32 v79, a103\n"
"v_lshlrev_b32 v72, 16, v73\n"
"v_and_b32 v73, v224, v73\n"
"v_lshlrev_b32 v74, 16, v75\n"
"v_and_b32 v75, v224, v75\n"
"v_lshlrev_b32 v76, 16, v77\n"
"v_and_b32 v77, v224, v77\n"
"v_lshlrev_b32 v78, 16, v79\n"
"v_and_b32 v79, v224, v79\n"
"v_accvgpr_read_b32 v81, a68\n"
"v_accvgpr_read_b32 v83, a69\n"
"v_accvgpr_read_b32 v85, a70\n"
"v_accvgpr_read_b32 v87, a71\n"
"v_lshlrev_b32 v80, 16, v81\n"
"v_and_b32 v81, v224, v81\n"
"v_lshlrev_b32 v82, 16, v83\n"
"v_and_b32 v83, v224, v83\n"
"v_lshlrev_b32 v84, 16, v85\n"
"v_and_b32 v85, v224, v85\n"
"v_lshlrev_b32 v86, 16, v87\n"
"v_and_b32 v87, v224, v87\n"
"v_accvgpr_read_b32 v89, a104\n"
"v_accvgpr_read_b32 v91, a105\n"
"v_accvgpr_read_b32 v93, a106\n"
"v_accvgpr_read_b32 v95, a107\n"
"v_lshlrev_b32 v88, 16, v89\n"
"v_and_b32 v89, v224, v89\n"
"v_lshlrev_b32 v90, 16, v91\n"
"v_and_b32 v91, v224, v91\n"
"v_lshlrev_b32 v92, 16, v93\n"
"v_and_b32 v93, v224, v93\n"
"v_lshlrev_b32 v94, 16, v95\n"
"v_and_b32 v95, v224, v95\n"
"v_accvgpr_read_b32 v97, a72\n"
"v_accvgpr_read_b32 v99, a73\n"
"v_accvgpr_read_b32 v101, a74\n"
"v_accvgpr_read_b32 v103, a75\n"
"v_lshlrev_b32 v96, 16, v97\n"
"v_and_b32 v97, v224, v97\n"
"v_lshlrev_b32 v98, 16, v99\n"
"v_and_b32 v99, v224, v99\n"
"v_lshlrev_b32 v100, 16, v101\n"
"v_and_b32 v101, v224, v101\n"
"v_lshlrev_b32 v102, 16, v103\n"
"v_and_b32 v103, v224, v103\n"
"v_accvgpr_read_b32 v105, a108\n"
"v_accvgpr_read_b32 v107, a109\n"
"v_accvgpr_read_b32 v109, a110\n"
"v_accvgpr_read_b32 v111, a111\n"
"v_lshlrev_b32 v104, 16, v105\n"
"v_and_b32 v105, v224, v105\n"
"v_lshlrev_b32 v106, 16, v107\n"
"v_and_b32 v107, v224, v107\n"
"v_lshlrev_b32 v108, 16, v109\n"
"v_and_b32 v109, v224, v109\n"
"v_lshlrev_b32 v110, 16, v111\n"
"v_and_b32 v111, v224, v111\n"
"v_accvgpr_read_b32 v113, a76\n"
"v_accvgpr_read_b32 v115, a77\n"
"v_accvgpr_read_b32 v117, a78\n"
"v_accvgpr_read_b32 v119, a79\n"
"v_lshlrev_b32 v112, 16, v113\n"
"v_and_b32 v113, v224, v113\n"
"v_lshlrev_b32 v114, 16, v115\n"
"v_and_b32 v115, v224, v115\n"
"v_lshlrev_b32 v116, 16, v117\n"
"v_and_b32 v117, v224, v117\n"
"v_lshlrev_b32 v118, 16, v119\n"
"v_and_b32 v119, v224, v119\n"
"v_accvgpr_read_b32 v121, a112\n"
"v_accvgpr_read_b32 v123, a113\n"
"v_accvgpr_read_b32 v125, a114\n"
"v_accvgpr_read_b32 v127, a115\n"
"v_lshlrev_b32 v120, 16, v121\n"
"v_and_b32 v121, v224, v121\n"
"v_lshlrev_b32 v122, 16, v123\n"
"v_and_b32 v123, v224, v123\n"
"v_lshlrev_b32 v124, 16, v125\n"
"v_and_b32 v125, v224, v125\n"
"v_lshlrev_b32 v126, 16, v127\n"
"v_and_b32 v127, v224, v127\n"
"v_accvgpr_read_b32 v129, a80\n"
"v_accvgpr_read_b32 v131, a81\n"
"v_accvgpr_read_b32 v133, a82\n"
"v_accvgpr_read_b32 v135, a83\n"
"v_lshlrev_b32 v128, 16, v129\n"
"v_and_b32 v129, v224, v129\n"
"v_lshlrev_b32 v130, 16, v131\n"
"v_and_b32 v131, v224, v131\n"
"v_lshlrev_b32 v132, 16, v133\n"
"v_and_b32 v133, v224, v133\n"
"v_lshlrev_b32 v134, 16, v135\n"
"v_and_b32 v135, v224, v135\n"
"v_accvgpr_read_b32 v137, a116\n"
"v_accvgpr_read_b32 v139, a117\n"
"v_accvgpr_read_b32 v141, a118\n"
"v_accvgpr_read_b32 v143, a119\n"
"v_lshlrev_b32 v136, 16, v137\n"
"v_and_b32 v137, v224, v137\n"
"v_lshlrev_b32 v138, 16, v139\n"
"v_and_b32 v139, v224, v139\n"
"v_lshlrev_b32 v140, 16, v141\n"
"v_and_b32 v141, v224, v141\n"
"v_lshlrev_b32 v142, 16, v143\n"
"v_and_b32 v143, v224, v143\n"
"v_lshrrev_b32 v242, 2, v248\n"
"v_lshlrev_b32 v241, 4, v242\n"
"v_lshlrev_b32 v242, 3, v242\n"
"v_lshrrev_b32 v243, 5, v254\n"
"v_mul_u32_u24 v243, 24576, v243\n"
"v_and_b32 v247, 3, v248\n"
"v_lshl_add_u32 v243, v247, 5, v243\n"
"v_and_b32 v244, 0x10000000, v254\n"
"v_or_b32 v243, v244, v243\n"
"v_lshrrev_b32 v244, 5, v248\n"
"v_mul_u32_u24 v244, 96, v244\n"
"v_and_b32 v246, 16, v248\n"
"v_add3_u32 v244, v244, v246, v227\n"
"v_lshl_add_u32 v244, v247, 5, v244\n"
"s_mul_i32 s63, s41, 384\n"
"v_add_u32 v244, s63, v244\n"
"v_lshl_add_u32 v249, v248, 2, v227\n"
"s_lshl_b32 s63, s41, 8\n"
"v_add_u32 v251, s63, v249\n"
"v_add_u32 v252, s63, v227\n"
"AT_B3:\n"
"s_waitcnt lgkmcnt(0)\n"
"s_barrier\n"
"ds_read_b64 v[246:247], v227 offset:656\n"
"s_mul_i32 s58, s41, 1152\n"
"s_add_u32 s58, s58, 37888000\n"
"s_waitcnt lgkmcnt(0)\n"
"v_readfirstlane_b32 s10, v246\n"
"v_readfirstlane_b32 s11, v247\n"
"s_nop 0\n"
"s_andn2_b32 s12, 0x7ff, s10\n"
"s_andn2_b32 s13, 0x7ff, s11\n"
"s_mul_i32 s56, s12, 9216\n"
"s_mul_i32 s57, s13, 9216\n"
"s_add_u32 s56, s56, s58\n"
"s_add_u32 s57, s57, s58\n"
"s_cmp_ge_u32 s42, 192\n"
"s_cbranch_scc1 AT_A4\n"
"buffer_load_dwordx4 v[144:147], v241, s[28:31], s56 offen offset:0 sc1\n"
"buffer_load_dwordx4 v[148:151], v241, s[28:31], s56 offen offset:256 sc1\n"
"buffer_load_dwordx4 v[152:155], v241, s[28:31], s56 offen offset:512 sc1\n"
"buffer_load_dwordx4 v[156:159], v241, s[28:31], s56 offen offset:768 sc1\n"
"buffer_load_dwordx2 v[160:161], v242, s[28:31], s56 offen offset:1024 sc1\n"
"buffer_load_dwordx4 v[162:165], v241, s[28:31], s57 offen offset:0 sc1\n"
"buffer_load_dwordx4 v[166:169], v241, s[28:31], s57 offen offset:256 sc1\n"
"buffer_load_dwordx4 v[170:173], v241, s[28:31], s57 offen offset:512 sc1\n"
"buffer_load_dwordx4 v[174:177], v241, s[28:31], s57 offen offset:768 sc1\n"
"buffer_load_dwordx2 v[178:179], v242, s[28:31], s57 offen offset:1024 sc1\n"
"AT_A4:\n"
"v_cmp_eq_u32 vcc, s10, v225\n"
"v_cmp_eq_u32 s[58:59], s11, v225\n"
"s_nop 0\n"
"s_or_b64 s[58:59], s[58:59], vcc\n"
"s_andn2_b64 exec, s[8:9], s[58:59]\n"
"buffer_store_dwordx2 v[228:229], v240, s[76:79], s46 offen offset:0\n"
"buffer_store_dwordx2 v[230:231], v240, s[76:79], s46 offen offset:32\n"
"buffer_store_dwordx2 v[232:233], v240, s[76:79], s46 offen offset:384\n"
"buffer_store_dwordx2 v[234:235], v240, s[76:79], s46 offen offset:416\n"
"buffer_store_dwordx2 v[236:237], v240, s[76:79], s46 offen offset:768\n"
"buffer_store_dwordx2 v[238:239], v240, s[76:79], s46 offen offset:800\n"
"s_and_b64 exec, s[8:9], s[58:59]\n"
"s_cbranch_execz AT_A5\n"
"s_add_u32 s60, s48, 4608\n"
"v_mov_b32 v181, s85\n"
"v_mov_b32 v183, s85\n"
"v_mov_b32 v185, s85\n"
"v_mov_b32 v187, s85\n"
"v_mov_b32 v189, s85\n"
"v_mov_b32 v191, s85\n"
"v_mov_b32 v193, s85\n"
"v_mov_b32 v195, s85\n"
"v_mov_b32 v180, v200\n"
"v_mov_b32 v182, v201\n"
"buffer_store_dwordx4 v[180:183], v245, s[28:31], s60 offen offset:0 sc1\n"
"v_mov_b32 v184, v202\n"
"v_mov_b32 v186, v203\n"
"buffer_store_dwordx4 v[184:187], v245, s[28:31], s60 offen offset:16 sc1\n"
"v_mov_b32 v188, v204\n"
"v_mov_b32 v190, v205\n"
"buffer_store_dwordx4 v[188:191], v245, s[28:31], s60 offen offset:128 sc1\n"
"v_mov_b32 v192, v206\n"
"v_mov_b32 v194, v207\n"
"buffer_store_dwordx4 v[192:195], v245, s[28:31], s60 offen offset:144 sc1\n"
"v_mov_b32 v180, v208\n"
"v_mov_b32 v182, v209\n"
"buffer_store_dwordx4 v[180:183], v245, s[28:31], s60 offen offset:1536 sc1\n"
"v_mov_b32 v184, v210\n"
"v_mov_b32 v186, v211\n"
"buffer_store_dwordx4 v[184:187], v245, s[28:31], s60 offen offset:1552 sc1\n"
"v_mov_b32 v188, v212\n"
"v_mov_b32 v190, v213\n"
"buffer_store_dwordx4 v[188:191], v245, s[28:31], s60 offen offset:1664 sc1\n"
"v_mov_b32 v192, v214\n"
"v_mov_b32 v194, v215\n"
"buffer_store_dwordx4 v[192:195], v245, s[28:31], s60 offen offset:1680 sc1\n"
"v_mov_b32 v180, v216\n"
"v_mov_b32 v182, v217\n"
"buffer_store_dwordx4 v[180:183], v245, s[28:31], s60 offen offset:3072 sc1\n"
"v_mov_b32 v184, v218\n"
"v_mov_b32 v186, v219\n"
"buffer_store_dwordx4 v[184:187], v245, s[28:31], s60 offen offset:3088 sc1\n"
"v_mov_b32 v188, v220\n"
"v_mov_b32 v190, v221\n"
"buffer_store_dwordx4 v[188:191], v245, s[28:31], s60 offen offset:3200 sc1\n"
"v_mov_b32 v192, v222\n"
"v_mov_b32 v194, v223\n"
"buffer_store_dwordx4 v[192:195], v245, s[28:31], s60 offen offset:3216 sc1\n"
"AT_A5:\n"
"s_mov_b64 exec, -1\n"
"s_cmp_ge_u32 s42, 192\n"
"s_cbranch_scc1 AT_F0\n"
"s_mov_b32 s87, 0x80000\n"
"s_waitcnt vmcnt(6)\n"
"AT_A6:\n"
"v_xor_b32 v212, s85, v145\n"
"v_xor_b32 v213, s85, v147\n"
"v_bitop3_b32 v212, v149, s85, v212 bitop3:0xbe\n"
"v_bitop3_b32 v213, v151, s85, v213 bitop3:0xbe\n"
"v_bitop3_b32 v212, v153, s85, v212 bitop3:0xbe\n"
"v_bitop3_b32 v213, v155, s85, v213 bitop3:0xbe\n"
"v_bitop3_b32 v212, v157, s85, v212 bitop3:0xbe\n"
"v_bitop3_b32 v213, v159, s85, v213 bitop3:0xbe\n"
"v_bitop3_b32 v212, v161, s85, v212 bitop3:0xbe\n"
"v_bitop3_b32 v213, v163, s85, v213 bitop3:0xbe\n"
"v_bitop3_b32 v212, v165, s85, v212 bitop3:0xbe\n"
"v_bitop3_b32 v213, v167, s85, v213 bitop3:0xbe\n"
"v_bitop3_b32 v212, v169, s85, v212 bitop3:0xbe\n"
"v_bitop3_b32 v213, v171, s85, v213 bitop3:0xbe\n"
"v_bitop3_b32 v212, v173, s85, v212 bitop3:0xbe\n"
"v_bitop3_b32 v213, v175, s85, v213 bitop3:0xbe\n"
"v_bitop3_b32 v212, v177, s85, v212 bitop3:0xbe\n"
"v_bitop3_b32 v213, v179, s85, v213 bitop3:0xbe\n"
"v_or_b32 v212, v212, v213\n"
"v_cmp_ne_u32 vcc, 0, v212\n"
"s_nop 0\n"
"s_cbranch_vccz AT_A7\n"
"s_sub_u32 s87, s87, 1\n"
"s_cmp_eq_u32 s87, 0\n"
"s_cbranch_scc1 AT_A7\n"
"s_sleep 1\n"
"buffer_load_dwordx4 v[144:147], v241, s[28:31], s56 offen offset:0 sc1\n"
"buffer_load_dwordx4 v[148:151], v241, s[28:31], s56 offen offset:256 sc1\n"
"buffer_load_dwordx4 v[152:155], v241, s[28:31], s56 offen offset:512 sc1\n"
"buffer_load_dwordx4 v[156:159], v241, s[28:31], s56 offen offset:768 sc1\n"
"buffer_load_dwordx2 v[160:161], v242, s[28:31], s56 offen offset:1024 sc1\n"
"buffer_load_dwordx4 v[162:165], v241, s[28:31], s57 offen offset:0 sc1\n"
"buffer_load_dwordx4 v[166:169], v241, s[28:31], s57 offen offset:256 sc1\n"
"buffer_load_dwordx4 v[170:173], v241, s[28:31], s57 offen offset:512 sc1\n"
"buffer_load_dwordx4 v[174:177], v241, s[28:31], s57 offen offset:768 sc1\n"
"buffer_load_dwordx2 v[178:179], v242, s[28:31], s57 offen offset:1024 sc1\n"
"s_waitcnt vmcnt(0)\n"
"s_branch AT_A6\n"
"AT_A7:\n"
"v_pk_mul_f32 v[180:181], v[0:1], v[144:145] op_sel_hi:[1,0]\n"
"v_pk_mul_f32 v[182:183], v[2:3], v[144:145] op_sel_hi:[1,0]\n"
"v_pk_mul_f32 v[184:185], v[4:5], v[144:145] op_sel_hi:[1,0]\n"
"v_pk_mul_f32 v[186:187], v[6:7], v[144:145] op_sel_hi:[1,0]\n"
"v_pk_mul_f32 v[196:197], v[8:9], v[144:145] op_sel_hi:[1,0]\n"
"v_pk_mul_f32 v[198:199], v[10:11], v[144:145] op_sel_hi:[1,0]\n"
"v_pk_mul_f32 v[200:201], v[12:13], v[144:145] op_sel_hi:[1,0]\n"
"v_pk_mul_f32 v[202:203], v[14:15], v[144:145] op_sel_hi:[1,0]\n"
"v_pk_mul_f32 v[188:189], v[0:1], v[162:163] op_sel_hi:[1,0]\n"
"v_pk_mul_f32 v[190:191], v[2:3], v[162:163] op_sel_hi:[1,0]\n"
"v_pk_mul_f32 v[192:193], v[4:5], v[162:163] op_sel_hi:[1,0]\n"
"v_pk_mul_f32 v[194:195], v[6:7], v[162:163] op_sel_hi:[1,0]\n"
"v_pk_mul_f32 v[204:205], v[8:9], v[162:163] op_sel_hi:[1,0]\n"
"v_pk_mul_f32 v[206:207], v[10:11], v[162:163] op_sel_hi:[1,0]\n"
"v_pk_mul_f32 v[208:209], v[12:13], v[162:163] op_sel_hi:[1,0]\n"
"v_pk_mul_f32 v[210:211], v[14:15], v[162:163] op_sel_hi:[1,0]\n"
"v_pk_fma_f32 v[180:181], v[16:17], v[146:147], v[180:181] op_sel_hi:[1,0,1]\n"
"v_pk_fma_f32 v[182:183], v[18:19], v[146:147], v[182:183] op_sel_hi:[1,0,1]\n"
"v_pk_fma_f32 v[184:185], v[20:21], v[146:147], v[184:185] op_sel_hi:[1,0,1]\n"
"v_pk_fma_f32 v[186:187], v[22:23], v[146:147], v[186:187] op_sel_hi:[1,0,1]\n"
"v_pk_fma_f32 v[196:197], v[24:25], v[146:147], v[196:197] op_sel_hi:[1,0,1]\n"
"v_pk_fma_f32 v[198:199], v[26:27], v[146:147], v[198:199] op_sel_hi:[1,0,1]\n"
"v_pk_fma_f32 v[200:201], v[28:29], v[146:147], v[200:201] op_sel_hi:[1,0,1]\n"
"v_pk_fma_f32 v[202:203], v[30:31], v[146:147], v[202:203] op_sel_hi:[1,0,1]\n"
"v_pk_fma_f32 v[188:189], v[16:17], v[164:165], v[188:189] op_sel_hi:[1,0,1]\n"
"v_pk_fma_f32 v[190:191], v[18:19], v[164:165], v[190:191] op_sel_hi:[1,0,1]\n"
"v_pk_fma_f32 v[192:193], v[20:21], v[164:165], v[192:193] op_sel_hi:[1,0,1]\n"
"v_pk_fma_f32 v[194:195], v[22:23], v[164:165], v[194:195] op_sel_hi:[1,0,1]\n"
"v_pk_fma_f32 v[204:205], v[24:25], v[164:165], v[204:205] op_sel_hi:[1,0,1]\n"
"v_pk_fma_f32 v[206:207], v[26:27], v[164:165], v[206:207] op_sel_hi:[1,0,1]\n"
"v_pk_fma_f32 v[208:209], v[28:29], v[164:165], v[208:209] op_sel_hi:[1,0,1]\n"
"v_pk_fma_f32 v[210:211], v[30:31], v[164:165], v[210:211] op_sel_hi:[1,0,1]\n"
"v_pk_fma_f32 v[180:181], v[32:33], v[148:149], v[180:181] op_sel_hi:[1,0,1]\n"
"v_pk_fma_f32 v[182:183], v[34:35], v[148:149], v[182:183] op_sel_hi:[1,0,1]\n"
"v_pk_fma_f32 v[184:185], v[36:37], v[148:149], v[184:185] op_sel_hi:[1,0,1]\n"
"v_pk_fma_f32 v[186:187], v[38:39], v[148:149], v[186:187] op_sel_hi:[1,0,1]\n"
"v_pk_fma_f32 v[196:197], v[40:41], v[148:149], v[196:197] op_sel_hi:[1,0,1]\n"
"v_pk_fma_f32 v[198:199], v[42:43], v[148:149], v[198:199] op_sel_hi:[1,0,1]\n"
"v_pk_fma_f32 v[200:201], v[44:45], v[148:149], v[200:201] op_sel_hi:[1,0,1]\n"
"v_pk_fma_f32 v[202:203], v[46:47], v[148:149], v[202:203] op_sel_hi:[1,0,1]\n"
"v_pk_fma_f32 v[188:189], v[32:33], v[166:167], v[188:189] op_sel_hi:[1,0,1]\n"
"v_pk_fma_f32 v[190:191], v[34:35], v[166:167], v[190:191] op_sel_hi:[1,0,1]\n"
"v_pk_fma_f32 v[192:193], v[36:37], v[166:167], v[192:193] op_sel_hi:[1,0,1]\n"
"v_pk_fma_f32 v[194:195], v[38:39], v[166:167], v[194:195] op_sel_hi:[1,0,1]\n"
"v_pk_fma_f32 v[204:205], v[40:41], v[166:167], v[204:205] op_sel_hi:[1,0,1]\n"
"v_pk_fma_f32 v[206:207], v[42:43], v[166:167], v[206:207] op_sel_hi:[1,0,1]\n"
"v_pk_fma_f32 v[208:209], v[44:45], v[166:167], v[208:209] op_sel_hi:[1,0,1]\n"
"v_pk_fma_f32 v[210:211], v[46:47], v[166:167], v[210:211] op_sel_hi:[1,0,1]\n"
"v_pk_fma_f32 v[180:181], v[48:49], v[150:151], v[180:181] op_sel_hi:[1,0,1]\n"
"v_pk_fma_f32 v[182:183], v[50:51], v[150:151], v[182:183] op_sel_hi:[1,0,1]\n"
"v_pk_fma_f32 v[184:185], v[52:53], v[150:151], v[184:185] op_sel_hi:[1,0,1]\n"
"v_pk_fma_f32 v[186:187], v[54:55], v[150:151], v[186:187] op_sel_hi:[1,0,1]\n"
"v_pk_fma_f32 v[196:197], v[56:57], v[150:151], v[196:197] op_sel_hi:[1,0,1]\n"
"v_pk_fma_f32 v[198:199], v[58:59], v[150:151], v[198:199] op_sel_hi:[1,0,1]\n"
"v_pk_fma_f32 v[200:201], v[60:61], v[150:151], v[200:201] op_sel_hi:[1,0,1]\n"
"v_pk_fma_f32 v[202:203], v[62:63], v[150:151], v[202:203] op_sel_hi:[1,0,1]\n"
"v_pk_fma_f32 v[188:189], v[48:49], v[168:169], v[188:189] op_sel_hi:[1,0,1]\n"
"v_pk_fma_f32 v[190:191], v[50:51], v[168:169], v[190:191] op_sel_hi:[1,0,1]\n"
"v_pk_fma_f32 v[192:193], v[52:53], v[168:169], v[192:193] op_sel_hi:[1,0,1]\n"
"v_pk_fma_f32 v[194:195], v[54:55], v[168:169], v[194:195] op_sel_hi:[1,0,1]\n"
"v_pk_fma_f32 v[204:205], v[56:57], v[168:169], v[204:205] op_sel_hi:[1,0,1]\n"
"v_pk_fma_f32 v[206:207], v[58:59], v[168:169], v[206:207] op_sel_hi:[1,0,1]\n"
"v_pk_fma_f32 v[208:209], v[60:61], v[168:169], v[208:209] op_sel_hi:[1,0,1]\n"
"v_pk_fma_f32 v[210:211], v[62:63], v[168:169], v[210:211] op_sel_hi:[1,0,1]\n"
"v_pk_fma_f32 v[180:181], v[64:65], v[152:153], v[180:181] op_sel_hi:[1,0,1]\n"
"v_pk_fma_f32 v[182:183], v[66:67], v[152:153], v[182:183] op_sel_hi:[1,0,1]\n"
"v_pk_fma_f32 v[184:185], v[68:69], v[152:153], v[184:185] op_sel_hi:[1,0,1]\n"
"v_pk_fma_f32 v[186:187], v[70:71], v[152:153], v[186:187] op_sel_hi:[1,0,1]\n"
"v_pk_fma_f32 v[196:197], v[72:73], v[152:153], v[196:197] op_sel_hi:[1,0,1]\n"
"v_pk_fma_f32 v[198:199], v[74:75], v[152:153], v[198:199] op_sel_hi:[1,0,1]\n"
"v_pk_fma_f32 v[200:201], v[76:77], v[152:153], v[200:201] op_sel_hi:[1,0,1]\n"
"v_pk_fma_f32 v[202:203], v[78:79], v[152:153], v[202:203] op_sel_hi:[1,0,1]\n"
"v_pk_fma_f32 v[188:189], v[64:65], v[170:171], v[188:189] op_sel_hi:[1,0,1]\n"
"v_pk_fma_f32 v[190:191], v[66:67], v[170:171], v[190:191] op_sel_hi:[1,0,1]\n"
"v_pk_fma_f32 v[192:193], v[68:69], v[170:171], v[192:193] op_sel_hi:[1,0,1]\n"
"v_pk_fma_f32 v[194:195], v[70:71], v[170:171], v[194:195] op_sel_hi:[1,0,1]\n"
"v_pk_fma_f32 v[204:205], v[72:73], v[170:171], v[204:205] op_sel_hi:[1,0,1]\n"
"v_pk_fma_f32 v[206:207], v[74:75], v[170:171], v[206:207] op_sel_hi:[1,0,1]\n"
"v_pk_fma_f32 v[208:209], v[76:77], v[170:171], v[208:209] op_sel_hi:[1,0,1]\n"
"v_pk_fma_f32 v[210:211], v[78:79], v[170:171], v[210:211] op_sel_hi:[1,0,1]\n"
"v_pk_fma_f32 v[180:181], v[80:81], v[154:155], v[180:181] op_sel_hi:[1,0,1]\n"
"v_pk_fma_f32 v[182:183], v[82:83], v[154:155], v[182:183] op_sel_hi:[1,0,1]\n"
"v_pk_fma_f32 v[184:185], v[84:85], v[154:155], v[184:185] op_sel_hi:[1,0,1]\n"
"v_pk_fma_f32 v[186:187], v[86:87], v[154:155], v[186:187] op_sel_hi:[1,0,1]\n"
"v_pk_fma_f32 v[196:197], v[88:89], v[154:155], v[196:197] op_sel_hi:[1,0,1]\n"
"v_pk_fma_f32 v[198:199], v[90:91], v[154:155], v[198:199] op_sel_hi:[1,0,1]\n"
"v_pk_fma_f32 v[200:201], v[92:93], v[154:155], v[200:201] op_sel_hi:[1,0,1]\n"
"v_pk_fma_f32 v[202:203], v[94:95], v[154:155], v[202:203] op_sel_hi:[1,0,1]\n"
"v_pk_fma_f32 v[188:189], v[80:81], v[172:173], v[188:189] op_sel_hi:[1,0,1]\n"
"v_pk_fma_f32 v[190:191], v[82:83], v[172:173], v[190:191] op_sel_hi:[1,0,1]\n"
"v_pk_fma_f32 v[192:193], v[84:85], v[172:173], v[192:193] op_sel_hi:[1,0,1]\n"
"v_pk_fma_f32 v[194:195], v[86:87], v[172:173], v[194:195] op_sel_hi:[1,0,1]\n"
"v_pk_fma_f32 v[204:205], v[88:89], v[172:173], v[204:205] op_sel_hi:[1,0,1]\n"
"v_pk_fma_f32 v[206:207], v[90:91], v[172:173], v[206:207] op_sel_hi:[1,0,1]\n"
"v_pk_fma_f32 v[208:209], v[92:93], v[172:173], v[208:209] op_sel_hi:[1,0,1]\n"
"v_pk_fma_f32 v[210:211], v[94:95], v[172:173], v[210:211] op_sel_hi:[1,0,1]\n"
"v_pk_fma_f32 v[180:181], v[96:97], v[156:157], v[180:181] op_sel_hi:[1,0,1]\n"
"v_pk_fma_f32 v[182:183], v[98:99], v[156:157], v[182:183] op_sel_hi:[1,0,1]\n"
"v_pk_fma_f32 v[184:185], v[100:101], v[156:157], v[184:185] op_sel_hi:[1,0,1]\n"
"v_pk_fma_f32 v[186:187], v[102:103], v[156:157], v[186:187] op_sel_hi:[1,0,1]\n"
"v_pk_fma_f32 v[196:197], v[104:105], v[156:157], v[196:197] op_sel_hi:[1,0,1]\n"
"v_pk_fma_f32 v[198:199], v[106:107], v[156:157], v[198:199] op_sel_hi:[1,0,1]\n"
"v_pk_fma_f32 v[200:201], v[108:109], v[156:157], v[200:201] op_sel_hi:[1,0,1]\n"
"v_pk_fma_f32 v[202:203], v[110:111], v[156:157], v[202:203] op_sel_hi:[1,0,1]\n"
"v_pk_fma_f32 v[188:189], v[96:97], v[174:175], v[188:189] op_sel_hi:[1,0,1]\n"
"v_pk_fma_f32 v[190:191], v[98:99], v[174:175], v[190:191] op_sel_hi:[1,0,1]\n"
"v_pk_fma_f32 v[192:193], v[100:101], v[174:175], v[192:193] op_sel_hi:[1,0,1]\n"
"v_pk_fma_f32 v[194:195], v[102:103], v[174:175], v[194:195] op_sel_hi:[1,0,1]\n"
"v_pk_fma_f32 v[204:205], v[104:105], v[174:175], v[204:205] op_sel_hi:[1,0,1]\n"
"v_pk_fma_f32 v[206:207], v[106:107], v[174:175], v[206:207] op_sel_hi:[1,0,1]\n"
"v_pk_fma_f32 v[208:209], v[108:109], v[174:175], v[208:209] op_sel_hi:[1,0,1]\n"
"v_pk_fma_f32 v[210:211], v[110:111], v[174:175], v[210:211] op_sel_hi:[1,0,1]\n"
"v_pk_fma_f32 v[180:181], v[112:113], v[158:159], v[180:181] op_sel_hi:[1,0,1]\n"
"v_pk_fma_f32 v[182:183], v[114:115], v[158:159], v[182:183] op_sel_hi:[1,0,1]\n"
"v_pk_fma_f32 v[184:185], v[116:117], v[158:159], v[184:185] op_sel_hi:[1,0,1]\n"
"v_pk_fma_f32 v[186:187], v[118:119], v[158:159], v[186:187] op_sel_hi:[1,0,1]\n"
"v_pk_fma_f32 v[196:197], v[120:121], v[158:159], v[196:197] op_sel_hi:[1,0,1]\n"
"v_pk_fma_f32 v[198:199], v[122:123], v[158:159], v[198:199] op_sel_hi:[1,0,1]\n"
"v_pk_fma_f32 v[200:201], v[124:125], v[158:159], v[200:201] op_sel_hi:[1,0,1]\n"
"v_pk_fma_f32 v[202:203], v[126:127], v[158:159], v[202:203] op_sel_hi:[1,0,1]\n"
"v_pk_fma_f32 v[188:189], v[112:113], v[176:177], v[188:189] op_sel_hi:[1,0,1]\n"
"v_pk_fma_f32 v[190:191], v[114:115], v[176:177], v[190:191] op_sel_hi:[1,0,1]\n"
"v_pk_fma_f32 v[192:193], v[116:117], v[176:177], v[192:193] op_sel_hi:[1,0,1]\n"
"v_pk_fma_f32 v[194:195], v[118:119], v[176:177], v[194:195] op_sel_hi:[1,0,1]\n"
"v_pk_fma_f32 v[204:205], v[120:121], v[176:177], v[204:205] op_sel_hi:[1,0,1]\n"
"v_pk_fma_f32 v[206:207], v[122:123], v[176:177], v[206:207] op_sel_hi:[1,0,1]\n"
"v_pk_fma_f32 v[208:209], v[124:125], v[176:177], v[208:209] op_sel_hi:[1,0,1]\n"
"v_pk_fma_f32 v[210:211], v[126:127], v[176:177], v[210:211] op_sel_hi:[1,0,1]\n"
"v_pk_fma_f32 v[180:181], v[128:129], v[160:161], v[180:181] op_sel_hi:[1,0,1]\n"
"v_pk_fma_f32 v[182:183], v[130:131], v[160:161], v[182:183] op_sel_hi:[1,0,1]\n"
"v_pk_fma_f32 v[184:185], v[132:133], v[160:161], v[184:185] op_sel_hi:[1,0,1]\n"
"v_pk_fma_f32 v[186:187], v[134:135], v[160:161], v[186:187] op_sel_hi:[1,0,1]\n"
"v_pk_fma_f32 v[196:197], v[136:137], v[160:161], v[196:197] op_sel_hi:[1,0,1]\n"
"v_pk_fma_f32 v[198:199], v[138:139], v[160:161], v[198:199] op_sel_hi:[1,0,1]\n"
"v_pk_fma_f32 v[200:201], v[140:141], v[160:161], v[200:201] op_sel_hi:[1,0,1]\n"
"v_pk_fma_f32 v[202:203], v[142:143], v[160:161], v[202:203] op_sel_hi:[1,0,1]\n"
"v_pk_fma_f32 v[188:189], v[128:129], v[178:179], v[188:189] op_sel_hi:[1,0,1]\n"
"v_pk_fma_f32 v[190:191], v[130:131], v[178:179], v[190:191] op_sel_hi:[1,0,1]\n"
"v_pk_fma_f32 v[192:193], v[132:133], v[178:179], v[192:193] op_sel_hi:[1,0,1]\n"
"v_pk_fma_f32 v[194:195], v[134:135], v[178:179], v[194:195] op_sel_hi:[1,0,1]\n"
"v_pk_fma_f32 v[204:205], v[136:137], v[178:179], v[204:205] op_sel_hi:[1,0,1]\n"
"v_pk_fma_f32 v[206:207], v[138:139], v[178:179], v[206:207] op_sel_hi:[1,0,1]\n"
"v_pk_fma_f32 v[208:209], v[140:141], v[178:179], v[208:209] op_sel_hi:[1,0,1]\n"
"v_pk_fma_f32 v[210:211], v[142:143], v[178:179], v[210:211] op_sel_hi:[1,0,1]\n"
"v_accvgpr_read_b32 v1, a0\n"
"v_accvgpr_read_b32 v3, a1\n"
"v_permlane32_swap_b32 v180, v188\n"
"v_permlane32_swap_b32 v181, v189\n"
"v_permlane32_swap_b32 v182, v190\n"
"v_permlane32_swap_b32 v183, v191\n"
"v_permlane32_swap_b32 v184, v192\n"
"v_permlane32_swap_b32 v185, v193\n"
"v_permlane32_swap_b32 v186, v194\n"
"v_permlane32_swap_b32 v187, v195\n"
"v_permlane32_swap_b32 v196, v204\n"
"v_permlane32_swap_b32 v197, v205\n"
"v_permlane32_swap_b32 v198, v206\n"
"v_permlane32_swap_b32 v199, v207\n"
"v_permlane32_swap_b32 v200, v208\n"
"v_permlane32_swap_b32 v201, v209\n"
"v_permlane32_swap_b32 v202, v210\n"
"v_permlane32_swap_b32 v203, v211\n"
"v_pk_add_f32 v[180:181], v[180:181], v[188:189]\n"
"v_pk_add_f32 v[182:183], v[182:183], v[190:191]\n"
"v_pk_add_f32 v[184:185], v[184:185], v[192:193]\n"
"v_pk_add_f32 v[186:187], v[186:187], v[194:195]\n"
"v_pk_add_f32 v[196:197], v[196:197], v[204:205]\n"
"v_pk_add_f32 v[198:199], v[198:199], v[206:207]\n"
"v_pk_add_f32 v[200:201], v[200:201], v[208:209]\n"
"v_pk_add_f32 v[202:203], v[202:203], v[210:211]\n"
"v_lshlrev_b32 v0, 16, v1\n"
"v_lshlrev_b32 v2, 16, v3\n"
"v_permlane16_swap_b32 v180, v184\n"
"v_permlane16_swap_b32 v181, v185\n"
"v_permlane16_swap_b32 v182, v186\n"
"v_permlane16_swap_b32 v183, v187\n"
"v_permlane16_swap_b32 v196, v200\n"
"v_permlane16_swap_b32 v197, v201\n"
"v_permlane16_swap_b32 v198, v202\n"
"v_permlane16_swap_b32 v199, v203\n"
"v_pk_add_f32 v[180:181], v[180:181], v[184:185]\n"
"v_pk_add_f32 v[182:183], v[182:183], v[186:187]\n"
"v_pk_add_f32 v[196:197], v[196:197], v[200:201]\n"
"v_pk_add_f32 v[198:199], v[198:199], v[202:203]\n"
"v_and_b32 v1, v224, v1\n"
"v_and_b32 v3, v224, v3\n"
"v_add_f32_dpp v180, v180, v180 row_ror:4 row_mask:0xf bank_mask:0xf\n"
"v_add_f32_dpp v181, v181, v181 row_ror:4 row_mask:0xf bank_mask:0xf\n"
"v_add_f32_dpp v182, v182, v182 row_ror:4 row_mask:0xf bank_mask:0xf\n"
"v_add_f32_dpp v183, v183, v183 row_ror:4 row_mask:0xf bank_mask:0xf\n"
"v_add_f32_dpp v196, v196, v196 row_ror:4 row_mask:0xf bank_mask:0xf\n"
"v_add_f32_dpp v197, v197, v197 row_ror:4 row_mask:0xf bank_mask:0xf\n"
"v_add_f32_dpp v198, v198, v198 row_ror:4 row_mask:0xf bank_mask:0xf\n"
"v_add_f32_dpp v199, v199, v199 row_ror:4 row_mask:0xf bank_mask:0xf\n"
"v_add_f32_dpp v180, v180, v180 row_ror:8 row_mask:0xf bank_mask:0xf\n"
"v_add_f32_dpp v181, v181, v181 row_ror:8 row_mask:0xf bank_mask:0xf\n"
"v_add_f32_dpp v182, v182, v182 row_ror:8 row_mask:0xf bank_mask:0xf\n"
"v_add_f32_dpp v183, v183, v183 row_ror:8 row_mask:0xf bank_mask:0xf\n"
"v_add_f32_dpp v196, v196, v196 row_ror:8 row_mask:0xf bank_mask:0xf\n"
"v_add_f32_dpp v197, v197, v197 row_ror:8 row_mask:0xf bank_mask:0xf\n"
"v_add_f32_dpp v198, v198, v198 row_ror:8 row_mask:0xf bank_mask:0xf\n"
"v_add_f32_dpp v199, v199, v199 row_ror:8 row_mask:0xf bank_mask:0xf\n"
"s_mov_b32 exec_lo, 0x00070007\n"
"s_mov_b32 exec_hi, 0x00070007\n"
"ds_write_b128 v244, v[180:183] offset:2048\n"
"ds_write_b128 v244, v[196:199] offset:2240\n"
"s_mov_b64 exec, -1\n"
"v_accvgpr_read_b32 v5, a2\n"
"v_accvgpr_read_b32 v7, a3\n"
"v_lshlrev_b32 v4, 16, v5\n"
"v_lshlrev_b32 v6, 16, v7\n"
"v_and_b32 v5, v224, v5\n"
"v_and_b32 v7, v224, v7\n"
"v_accvgpr_read_b32 v9, a4\n"
"v_accvgpr_read_b32 v11, a5\n"
"v_lshlrev_b32 v8, 16, v9\n"
"v_lshlrev_b32 v10, 16, v11\n"
"v_and_b32 v9, v224, v9\n"
"v_and_b32 v11, v224, v11\n"
"v_accvgpr_read_b32 v13, a6\n"
"v_accvgpr_read_b32 v15, a7\n"
"v_lshlrev_b32 v12, 16, v13\n"
"v_lshlrev_b32 v14, 16, v15\n"
"v_and_b32 v13, v224, v13\n"
"v_and_b32 v15, v224, v15\n"
"v_accvgpr_read_b32 v17, a8\n"
"v_accvgpr_read_b32 v19, a9\n"
"v_lshlrev_b32 v16, 16, v17\n"
"v_lshlrev_b32 v18, 16, v19\n"
"v_and_b32 v17, v224, v17\n"
"v_and_b32 v19, v224, v19\n"
"v_accvgpr_read_b32 v21, a10\n"
"v_accvgpr_read_b32 v23, a11\n"
"v_lshlrev_b32 v20, 16, v21\n"
"v_lshlrev_b32 v22, 16, v23\n"
"v_and_b32 v21, v224, v21\n"
"v_and_b32 v23, v224, v23\n"
"s_waitcnt lgkmcnt(0)\n"
"s_barrier\n"
"s_cmp_eq_u32 s41, 3\n"
"s_cbranch_scc1 AT_F1\n"
"ds_read_b32 v180, v249 offset:2048\n"
"ds_read_b32 v181, v249 offset:2240\n"
"ds_read_b32 v182, v249 offset:2432\n"
"ds_read_b32 v183, v249 offset:2624\n"
"ds_read_b32 v184, v249 offset:2816\n"
"ds_read_b32 v185, v249 offset:3008\n"
"ds_read_b32 v186, v249 offset:3200\n"
"ds_read_b32 v187, v249 offset:3392\n"
"v_accvgpr_read_b32 v25, a12\n"
"v_accvgpr_read_b32 v27, a13\n"
"v_lshlrev_b32 v24, 16, v25\n"
"v_lshlrev_b32 v26, 16, v27\n"
"v_and_b32 v25, v224, v25\n"
"v_and_b32 v27, v224, v27\n"
"v_accvgpr_read_b32 v29, a14\n"
"v_accvgpr_read_b32 v31, a15\n"
"v_lshlrev_b32 v28, 16, v29\n"
"v_lshlrev_b32 v30, 16, v31\n"
"v_and_b32 v29, v224, v29\n"
"v_and_b32 v31, v224, v31\n"
"v_accvgpr_read_b32 v33, a16\n"
"v_accvgpr_read_b32 v35, a17\n"
"v_lshlrev_b32 v32, 16, v33\n"
"v_lshlrev_b32 v34, 16, v35\n"
"v_and_b32 v33, v224, v33\n"
"v_and_b32 v35, v224, v35\n"
"v_accvgpr_read_b32 v37, a18\n"
"v_accvgpr_read_b32 v39, a19\n"
"v_lshlrev_b32 v36, 16, v37\n"
"v_lshlrev_b32 v38, 16, v39\n"
"v_and_b32 v37, v224, v37\n"
"v_and_b32 v39, v224, v39\n"
"s_waitcnt lgkmcnt(5)\n"
"v_add_f32 v180, v180, v182\n"
"s_waitcnt lgkmcnt(1)\n"
"v_add_f32 v184, v184, v186\n"
"v_add_f32 v181, v181, v183\n"
"v_add_f32 v180, v180, v184\n"
"s_waitcnt lgkmcnt(0)\n"
"v_add_f32 v185, v185, v187\n"
"v_mul_f32 v182, 0xbfb8aa3b, v180\n"
"v_add_f32 v181, v181, v185\n"
"v_exp_f32 v182, v182\n"
"v_mul_f32 v180, v180, v181\n"
"v_add_f32 v182, 1.0, v182\n"
"v_rcp_f32 v182, v182\n"
"v_accvgpr_read_b32 v41, a20\n"
"v_mul_f32 v180, v180, v182\n"
"ds_write_b32 v251, v180 offset:4096\n"
"ds_read_b128 v[100:103], v252 offset:4096\n"
"ds_read_b128 v[124:127], v252 offset:4192\n"
"ds_read_b128 v[104:107], v252 offset:4112\n"
"ds_read_b128 v[128:131], v252 offset:4208\n"
"ds_read_b128 v[108:111], v252 offset:4128\n"
"ds_read_b128 v[132:135], v252 offset:4224\n"
"ds_read_b128 v[112:115], v252 offset:4144\n"
"ds_read_b128 v[136:139], v252 offset:4240\n"
"ds_read_b128 v[116:119], v252 offset:4160\n"
"ds_read_b128 v[140:143], v252 offset:4256\n"
"ds_read_b128 v[120:123], v252 offset:4176\n"
"ds_read_b128 v[144:147], v252 offset:4272\n"
"s_lshl_b32 s58, s42, 7\n"
"s_add_u32 s58, s58, 56762368\n"
"s_add_u32 s59, s58, 884736\n"
"v_mov_b32 v161, s85\n"
"v_mov_b32 v163, s85\n"
"v_mov_b32 v165, s85\n"
"v_mov_b32 v167, s85\n"
"v_mov_b32 v169, s85\n"
"v_mov_b32 v171, s85\n"
"v_mov_b32 v173, s85\n"
"v_mov_b32 v175, s85\n"
"v_accvgpr_read_b32 v43, a21\n"
"v_lshlrev_b32 v40, 16, v41\n"
"v_lshlrev_b32 v42, 16, v43\n"
"v_and_b32 v41, v224, v41\n"
"v_and_b32 v43, v224, v43\n"
"v_accvgpr_read_b32 v45, a22\n"
"v_accvgpr_read_b32 v47, a23\n"
"v_lshlrev_b32 v44, 16, v45\n"
"v_lshlrev_b32 v46, 16, v47\n"
"v_and_b32 v45, v224, v45\n"
"v_and_b32 v47, v224, v47\n"
"v_accvgpr_read_b32 v49, a24\n"
"v_accvgpr_read_b32 v51, a25\n"
"v_lshlrev_b32 v48, 16, v49\n"
"v_lshlrev_b32 v50, 16, v51\n"
"v_and_b32 v49, v224, v49\n"
"v_and_b32 v51, v224, v51\n"
"v_accvgpr_read_b32 v53, a26\n"
"v_accvgpr_read_b32 v55, a27\n"
"v_lshlrev_b32 v52, 16, v53\n"
"v_lshlrev_b32 v54, 16, v55\n"
"v_and_b32 v53, v224, v53\n"
"v_and_b32 v55, v224, v55\n"
"v_accvgpr_read_b32 v57, a28\n"
"v_accvgpr_read_b32 v59, a29\n"
"v_lshlrev_b32 v56, 16, v57\n"
"v_lshlrev_b32 v58, 16, v59\n"
"v_and_b32 v57, v224, v57\n"
"v_and_b32 v59, v224, v59\n"
"v_accvgpr_read_b32 v61, a30\n"
"v_accvgpr_read_b32 v63, a31\n"
"v_lshlrev_b32 v60, 16, v61\n"
"v_lshlrev_b32 v62, 16, v63\n"
"v_and_b32 v61, v224, v61\n"
"v_and_b32 v63, v224, v63\n"
"v_accvgpr_read_b32 v65, a32\n"
"v_accvgpr_read_b32 v67, a33\n"
"v_lshlrev_b32 v64, 16, v65\n"
"v_lshlrev_b32 v66, 16, v67\n"
"v_and_b32 v65, v224, v65\n"
"v_and_b32 v67, v224, v67\n"
"v_accvgpr_read_b32 v69, a34\n"
"v_accvgpr_read_b32 v71, a35\n"
"v_lshlrev_b32 v68, 16, v69\n"
"v_lshlrev_b32 v70, 16, v71\n"
"v_and_b32 v69, v224, v69\n"
"v_and_b32 v71, v224, v71\n"
"v_accvgpr_read_b32 v73, a36\n"
"v_accvgpr_read_b32 v75, a37\n"
"v_lshlrev_b32 v72, 16, v73\n"
"v_lshlrev_b32 v74, 16, v75\n"
"v_and_b32 v73, v224, v73\n"
"v_and_b32 v75, v224, v75\n"
"v_accvgpr_read_b32 v77, a38\n"
"v_accvgpr_read_b32 v79, a39\n"
"v_lshlrev_b32 v76, 16, v77\n"
"v_lshlrev_b32 v78, 16, v79\n"
"v_and_b32 v77, v224, v77\n"
"v_and_b32 v79, v224, v79\n"
"v_accvgpr_read_b32 v81, a40\n"
"v_accvgpr_read_b32 v83, a41\n"
"v_lshlrev_b32 v80, 16, v81\n"
"v_lshlrev_b32 v82, 16, v83\n"
"v_and_b32 v81, v224, v81\n"
"v_and_b32 v83, v224, v83\n"
"v_accvgpr_read_b32 v85, a42\n"
"v_accvgpr_read_b32 v87, a43\n"
"v_lshlrev_b32 v84, 16, v85\n"
"v_lshlrev_b32 v86, 16, v87\n"
"v_and_b32 v85, v224, v85\n"
"v_and_b32 v87, v224, v87\n"
"v_accvgpr_read_b32 v89, a44\n"
"v_accvgpr_read_b32 v91, a45\n"
"v_lshlrev_b32 v88, 16, v89\n"
"v_lshlrev_b32 v90, 16, v91\n"
"v_and_b32 v89, v224, v89\n"
"v_and_b32 v91, v224, v91\n"
"v_accvgpr_read_b32 v93, a46\n"
"v_accvgpr_read_b32 v95, a47\n"
"v_lshlrev_b32 v92, 16, v93\n"
"v_lshlrev_b32 v94, 16, v95\n"
"v_and_b32 v93, v224, v93\n"
"v_and_b32 v95, v224, v95\n"
"s_waitcnt lgkmcnt(11)\n"
"v_pk_mul_f32 v[150:151], v[0:1], v[100:101] op_sel:[0,0] op_sel_hi:[1,0]\n"
"v_pk_mul_f32 v[152:153], v[2:3], v[100:101] op_sel:[0,0] op_sel_hi:[1,0]\n"
"s_waitcnt lgkmcnt(10)\n"
"v_pk_mul_f32 v[154:155], v[0:1], v[124:125] op_sel:[0,0] op_sel_hi:[1,0]\n"
"v_pk_mul_f32 v[156:157], v[2:3], v[124:125] op_sel:[0,0] op_sel_hi:[1,0]\n"
"v_pk_fma_f32 v[150:151], v[4:5], v[100:101], v[150:151] op_sel:[0,1,0] op_sel_hi:[1,1,1]\n"
"v_pk_fma_f32 v[152:153], v[6:7], v[100:101], v[152:153] op_sel:[0,1,0] op_sel_hi:[1,1,1]\n"
"v_pk_fma_f32 v[154:155], v[4:5], v[124:125], v[154:155] op_sel:[0,1,0] op_sel_hi:[1,1,1]\n"
"v_pk_fma_f32 v[156:157], v[6:7], v[124:125], v[156:157] op_sel:[0,1,0] op_sel_hi:[1,1,1]\n"
"v_pk_fma_f32 v[150:151], v[8:9], v[102:103], v[150:151] op_sel:[0,0,0] op_sel_hi:[1,0,1]\n"
"v_pk_fma_f32 v[152:153], v[10:11], v[102:103], v[152:153] op_sel:[0,0,0] op_sel_hi:[1,0,1]\n"
"v_pk_fma_f32 v[154:155], v[8:9], v[126:127], v[154:155] op_sel:[0,0,0] op_sel_hi:[1,0,1]\n"
"v_pk_fma_f32 v[156:157], v[10:11], v[126:127], v[156:157] op_sel:[0,0,0] op_sel_hi:[1,0,1]\n"
"v_pk_fma_f32 v[150:151], v[12:13], v[102:103], v[150:151] op_sel:[0,1,0] op_sel_hi:[1,1,1]\n"
"v_pk_fma_f32 v[152:153], v[14:15], v[102:103], v[152:153] op_sel:[0,1,0] op_sel_hi:[1,1,1]\n"
"v_pk_fma_f32 v[154:155], v[12:13], v[126:127], v[154:155] op_sel:[0,1,0] op_sel_hi:[1,1,1]\n"
"v_pk_fma_f32 v[156:157], v[14:15], v[126:127], v[156:157] op_sel:[0,1,0] op_sel_hi:[1,1,1]\n"
"s_waitcnt lgkmcnt(9)\n"
"v_pk_fma_f32 v[150:151], v[16:17], v[104:105], v[150:151] op_sel:[0,0,0] op_sel_hi:[1,0,1]\n"
"v_pk_fma_f32 v[152:153], v[18:19], v[104:105], v[152:153] op_sel:[0,0,0] op_sel_hi:[1,0,1]\n"
"s_waitcnt lgkmcnt(8)\n"
"v_pk_fma_f32 v[154:155], v[16:17], v[128:129], v[154:155] op_sel:[0,0,0] op_sel_hi:[1,0,1]\n"
"v_pk_fma_f32 v[156:157], v[18:19], v[128:129], v[156:157] op_sel:[0,0,0] op_sel_hi:[1,0,1]\n"
"v_pk_fma_f32 v[150:151], v[20:21], v[104:105], v[150:151] op_sel:[0,1,0] op_sel_hi:[1,1,1]\n"
"v_pk_fma_f32 v[152:153], v[22:23], v[104:105], v[152:153] op_sel:[0,1,0] op_sel_hi:[1,1,1]\n"
"v_pk_fma_f32 v[154:155], v[20:21], v[128:129], v[154:155] op_sel:[0,1,0] op_sel_hi:[1,1,1]\n"
"v_pk_fma_f32 v[156:157], v[22:23], v[128:129], v[156:157] op_sel:[0,1,0] op_sel_hi:[1,1,1]\n"
"v_pk_fma_f32 v[150:151], v[24:25], v[106:107], v[150:151] op_sel:[0,0,0] op_sel_hi:[1,0,1]\n"
"v_pk_fma_f32 v[152:153], v[26:27], v[106:107], v[152:153] op_sel:[0,0,0] op_sel_hi:[1,0,1]\n"
"v_pk_fma_f32 v[154:155], v[24:25], v[130:131], v[154:155] op_sel:[0,0,0] op_sel_hi:[1,0,1]\n"
"v_pk_fma_f32 v[156:157], v[26:27], v[130:131], v[156:157] op_sel:[0,0,0] op_sel_hi:[1,0,1]\n"
"v_pk_fma_f32 v[150:151], v[28:29], v[106:107], v[150:151] op_sel:[0,1,0] op_sel_hi:[1,1,1]\n"
"v_pk_fma_f32 v[152:153], v[30:31], v[106:107], v[152:153] op_sel:[0,1,0] op_sel_hi:[1,1,1]\n"
"v_pk_fma_f32 v[154:155], v[28:29], v[130:131], v[154:155] op_sel:[0,1,0] op_sel_hi:[1,1,1]\n"
"v_pk_fma_f32 v[156:157], v[30:31], v[130:131], v[156:157] op_sel:[0,1,0] op_sel_hi:[1,1,1]\n"
"s_waitcnt lgkmcnt(7)\n"
"v_pk_fma_f32 v[150:151], v[32:33], v[108:109], v[150:151] op_sel:[0,0,0] op_sel_hi:[1,0,1]\n"
"v_pk_fma_f32 v[152:153], v[34:35], v[108:109], v[152:153] op_sel:[0,0,0] op_sel_hi:[1,0,1]\n"
"s_waitcnt lgkmcnt(6)\n"
"v_pk_fma_f32 v[154:155], v[32:33], v[132:133], v[154:155] op_sel:[0,0,0] op_sel_hi:[1,0,1]\n"
"v_pk_fma_f32 v[156:157], v[34:35], v[132:133], v[156:157] op_sel:[0,0,0] op_sel_hi:[1,0,1]\n"
"v_pk_fma_f32 v[150:151], v[36:37], v[108:109], v[150:151] op_sel:[0,1,0] op_sel_hi:[1,1,1]\n"
"v_pk_fma_f32 v[152:153], v[38:39], v[108:109], v[152:153] op_sel:[0,1,0] op_sel_hi:[1,1,1]\n"
"v_pk_fma_f32 v[154:155], v[36:37], v[132:133], v[154:155] op_sel:[0,1,0] op_sel_hi:[1,1,1]\n"
"v_pk_fma_f32 v[156:157], v[38:39], v[132:133], v[156:157] op_sel:[0,1,0] op_sel_hi:[1,1,1]\n"
"v_pk_fma_f32 v[150:151], v[40:41], v[110:111], v[150:151] op_sel:[0,0,0] op_sel_hi:[1,0,1]\n"
"v_pk_fma_f32 v[152:153], v[42:43], v[110:111], v[152:153] op_sel:[0,0,0] op_sel_hi:[1,0,1]\n"
"v_pk_fma_f32 v[154:155], v[40:41], v[134:135], v[154:155] op_sel:[0,0,0] op_sel_hi:[1,0,1]\n"
"v_pk_fma_f32 v[156:157], v[42:43], v[134:135], v[156:157] op_sel:[0,0,0] op_sel_hi:[1,0,1]\n"
"v_pk_fma_f32 v[150:151], v[44:45], v[110:111], v[150:151] op_sel:[0,1,0] op_sel_hi:[1,1,1]\n"
"v_pk_fma_f32 v[152:153], v[46:47], v[110:111], v[152:153] op_sel:[0,1,0] op_sel_hi:[1,1,1]\n"
"v_pk_fma_f32 v[154:155], v[44:45], v[134:135], v[154:155] op_sel:[0,1,0] op_sel_hi:[1,1,1]\n"
"v_pk_fma_f32 v[156:157], v[46:47], v[134:135], v[156:157] op_sel:[0,1,0] op_sel_hi:[1,1,1]\n"
"s_waitcnt lgkmcnt(5)\n"
"v_pk_fma_f32 v[150:151], v[48:49], v[112:113], v[150:151] op_sel:[0,0,0] op_sel_hi:[1,0,1]\n"
"v_pk_fma_f32 v[152:153], v[50:51], v[112:113], v[152:153] op_sel:[0,0,0] op_sel_hi:[1,0,1]\n"
"s_waitcnt lgkmcnt(4)\n"
"v_pk_fma_f32 v[154:155], v[48:49], v[136:137], v[154:155] op_sel:[0,0,0] op_sel_hi:[1,0,1]\n"
"v_pk_fma_f32 v[156:157], v[50:51], v[136:137], v[156:157] op_sel:[0,0,0] op_sel_hi:[1,0,1]\n"
"v_pk_fma_f32 v[150:151], v[52:53], v[112:113], v[150:151] op_sel:[0,1,0] op_sel_hi:[1,1,1]\n"
"v_pk_fma_f32 v[152:153], v[54:55], v[112:113], v[152:153] op_sel:[0,1,0] op_sel_hi:[1,1,1]\n"
"v_pk_fma_f32 v[154:155], v[52:53], v[136:137], v[154:155] op_sel:[0,1,0] op_sel_hi:[1,1,1]\n"
"v_pk_fma_f32 v[156:157], v[54:55], v[136:137], v[156:157] op_sel:[0,1,0] op_sel_hi:[1,1,1]\n"
"v_pk_fma_f32 v[150:151], v[56:57], v[114:115], v[150:151] op_sel:[0,0,0] op_sel_hi:[1,0,1]\n"
"v_pk_fma_f32 v[152:153], v[58:59], v[114:115], v[152:153] op_sel:[0,0,0] op_sel_hi:[1,0,1]\n"
"v_pk_fma_f32 v[154:155], v[56:57], v[138:139], v[154:155] op_sel:[0,0,0] op_sel_hi:[1,0,1]\n"
"v_pk_fma_f32 v[156:157], v[58:59], v[138:139], v[156:157] op_sel:[0,0,0] op_sel_hi:[1,0,1]\n"
"v_pk_fma_f32 v[150:151], v[60:61], v[114:115], v[150:151] op_sel:[0,1,0] op_sel_hi:[1,1,1]\n"
"v_pk_fma_f32 v[152:153], v[62:63], v[114:115], v[152:153] op_sel:[0,1,0] op_sel_hi:[1,1,1]\n"
"v_pk_fma_f32 v[154:155], v[60:61], v[138:139], v[154:155] op_sel:[0,1,0] op_sel_hi:[1,1,1]\n"
"v_pk_fma_f32 v[156:157], v[62:63], v[138:139], v[156:157] op_sel:[0,1,0] op_sel_hi:[1,1,1]\n"
"s_waitcnt lgkmcnt(3)\n"
"v_pk_fma_f32 v[150:151], v[64:65], v[116:117], v[150:151] op_sel:[0,0,0] op_sel_hi:[1,0,1]\n"
"v_pk_fma_f32 v[152:153], v[66:67], v[116:117], v[152:153] op_sel:[0,0,0] op_sel_hi:[1,0,1]\n"
"s_waitcnt lgkmcnt(2)\n"
"v_pk_fma_f32 v[154:155], v[64:65], v[140:141], v[154:155] op_sel:[0,0,0] op_sel_hi:[1,0,1]\n"
"v_pk_fma_f32 v[156:157], v[66:67], v[140:141], v[156:157] op_sel:[0,0,0] op_sel_hi:[1,0,1]\n"
"v_pk_fma_f32 v[150:151], v[68:69], v[116:117], v[150:151] op_sel:[0,1,0] op_sel_hi:[1,1,1]\n"
"v_pk_fma_f32 v[152:153], v[70:71], v[116:117], v[152:153] op_sel:[0,1,0] op_sel_hi:[1,1,1]\n"
"v_pk_fma_f32 v[154:155], v[68:69], v[140:141], v[154:155] op_sel:[0,1,0] op_sel_hi:[1,1,1]\n"
"v_pk_fma_f32 v[156:157], v[70:71], v[140:141], v[156:157] op_sel:[0,1,0] op_sel_hi:[1,1,1]\n"
"v_pk_fma_f32 v[150:151], v[72:73], v[118:119], v[150:151] op_sel:[0,0,0] op_sel_hi:[1,0,1]\n"
"v_pk_fma_f32 v[152:153], v[74:75], v[118:119], v[152:153] op_sel:[0,0,0] op_sel_hi:[1,0,1]\n"
"v_pk_fma_f32 v[154:155], v[72:73], v[142:143], v[154:155] op_sel:[0,0,0] op_sel_hi:[1,0,1]\n"
"v_pk_fma_f32 v[156:157], v[74:75], v[142:143], v[156:157] op_sel:[0,0,0] op_sel_hi:[1,0,1]\n"
"v_pk_fma_f32 v[150:151], v[76:77], v[118:119], v[150:151] op_sel:[0,1,0] op_sel_hi:[1,1,1]\n"
"v_pk_fma_f32 v[152:153], v[78:79], v[118:119], v[152:153] op_sel:[0,1,0] op_sel_hi:[1,1,1]\n"
"v_pk_fma_f32 v[154:155], v[76:77], v[142:143], v[154:155] op_sel:[0,1,0] op_sel_hi:[1,1,1]\n"
"v_pk_fma_f32 v[156:157], v[78:79], v[142:143], v[156:157] op_sel:[0,1,0] op_sel_hi:[1,1,1]\n"
"s_waitcnt lgkmcnt(1)\n"
"v_pk_fma_f32 v[150:151], v[80:81], v[120:121], v[150:151] op_sel:[0,0,0] op_sel_hi:[1,0,1]\n"
"v_pk_fma_f32 v[152:153], v[82:83], v[120:121], v[152:153] op_sel:[0,0,0] op_sel_hi:[1,0,1]\n"
"s_waitcnt lgkmcnt(0)\n"
"v_pk_fma_f32 v[154:155], v[80:81], v[144:145], v[154:155] op_sel:[0,0,0] op_sel_hi:[1,0,1]\n"
"v_pk_fma_f32 v[156:157], v[82:83], v[144:145], v[156:157] op_sel:[0,0,0] op_sel_hi:[1,0,1]\n"
"v_pk_fma_f32 v[150:151], v[84:85], v[120:121], v[150:151] op_sel:[0,1,0] op_sel_hi:[1,1,1]\n"
"v_pk_fma_f32 v[152:153], v[86:87], v[120:121], v[152:153] op_sel:[0,1,0] op_sel_hi:[1,1,1]\n"
"v_pk_fma_f32 v[154:155], v[84:85], v[144:145], v[154:155] op_sel:[0,1,0] op_sel_hi:[1,1,1]\n"
"v_pk_fma_f32 v[156:157], v[86:87], v[144:145], v[156:157] op_sel:[0,1,0] op_sel_hi:[1,1,1]\n"
"v_pk_fma_f32 v[150:151], v[88:89], v[122:123], v[150:151] op_sel:[0,0,0] op_sel_hi:[1,0,1]\n"
"v_pk_fma_f32 v[152:153], v[90:91], v[122:123], v[152:153] op_sel:[0,0,0] op_sel_hi:[1,0,1]\n"
"v_pk_fma_f32 v[154:155], v[88:89], v[146:147], v[154:155] op_sel:[0,0,0] op_sel_hi:[1,0,1]\n"
"v_pk_fma_f32 v[156:157], v[90:91], v[146:147], v[156:157] op_sel:[0,0,0] op_sel_hi:[1,0,1]\n"
"v_pk_fma_f32 v[150:151], v[92:93], v[122:123], v[150:151] op_sel:[0,1,0] op_sel_hi:[1,1,1]\n"
"v_pk_fma_f32 v[152:153], v[94:95], v[122:123], v[152:153] op_sel:[0,1,0] op_sel_hi:[1,1,1]\n"
"v_pk_fma_f32 v[154:155], v[92:93], v[146:147], v[154:155] op_sel:[0,1,0] op_sel_hi:[1,1,1]\n"
"v_pk_fma_f32 v[156:157], v[94:95], v[146:147], v[156:157] op_sel:[0,1,0] op_sel_hi:[1,1,1]\n"
"v_mov_b32 v160, v150\n"
"v_mov_b32 v162, v151\n"
"v_mov_b32 v164, v152\n"
"v_mov_b32 v166, v153\n"
"v_mov_b32 v168, v154\n"
"v_mov_b32 v170, v155\n"
"v_mov_b32 v172, v156\n"
"v_mov_b32 v174, v157\n"
"buffer_store_dwordx4 v[160:163], v243, s[28:31], s58 offen offset:0 sc1\n"
"buffer_store_dwordx4 v[164:167], v243, s[28:31], s58 offen offset:16 sc1\n"
"buffer_store_dwordx4 v[168:171], v243, s[28:31], s59 offen offset:0 sc1\n"
"buffer_store_dwordx4 v[172:175], v243, s[28:31], s59 offen offset:16 sc1\n"
"s_branch AT_F1\n"
"AT_F0:\n"
"s_sleep 30\n"
"AT_F1:\n"
"s_cmp_lt_u32 s42, 184\n"
"s_cbranch_scc1 AT_end\n"
"s_sub_u32 s56, s42, 184\n"
"s_cmp_ge_u32 s56, 36\n"
"s_cselect_b32 s57, 36, 0\n"
"s_cselect_b32 s59, s13, s12\n"
"s_cselect_b32 s60, s11, s10\n"
"s_sub_u32 s58, s56, s57\n"
"s_lshl_b32 s58, s58, 4\n"
"s_mul_i32 s56, s56, 24576\n"
"s_add_u32 s56, s56, 56762368\n"
"s_mul_i32 s61, s59, 1152\n"
"s_add_u32 s61, s61, s58\n"
"s_lshl_b32 s61, s61, 3\n"
"s_add_u32 s61, s61, 37892608\n"
"v_lshlrev_b32 v30, 4, v253\n"
"v_add_u32 v31, 4096, v30\n"
"v_add_u32 v32, 8192, v30\n"
"v_add_u32 v33, 12288, v30\n"
"v_add_u32 v34, 16384, v30\n"
"v_add_u32 v35, 20480, v30\n"
"v_and_b32 v36, 15, v248\n"
"v_lshlrev_b32 v37, 3, v36\n"
"s_lshl_b32 s62, s58, 2\n"
"s_add_u32 s62, s62, s40\n"
"s_add_u32 s62, s62, 114688\n"
"v_lshl_add_u32 v38, v36, 2, s62\n"
"ds_read_b32 v39, v38\n"
"s_mov_b32 s87, 0x80000\n"
"AT_F2:\n"
"buffer_load_dwordx4 v[0:3], v30, s[28:31], s56 offen sc1\n"
"buffer_load_dwordx4 v[4:7], v31, s[28:31], s56 offen sc1\n"
"buffer_load_dwordx4 v[8:11], v32, s[28:31], s56 offen sc1\n"
"buffer_load_dwordx4 v[12:15], v33, s[28:31], s56 offen sc1\n"
"buffer_load_dwordx4 v[16:19], v34, s[28:31], s56 offen sc1\n"
"buffer_load_dwordx4 v[20:23], v35, s[28:31], s56 offen sc1\n"
"buffer_load_dwordx2 v[24:25], v37, s[28:31], s61 offen sc1\n"
"s_and_b32 s62, s60, s14\n"
"s_ashr_i32 s63, s62, 31\n"
"s_not_b32 s63, s63\n"
"s_and_b32 s63, s63, s14\n"
"s_or_b32 s63, s63, 0x80000000\n"
"s_xor_b32 s62, s62, s63\n"
"v_mov_b32 v40, s62\n"
"v_mul_f32 v40, 0xbfb8aa3b, v40\n"
"v_exp_f32 v40, v40\n"
"s_nop 0\n"
"v_add_f32 v40, 1.0, v40\n"
"v_rcp_f32 v40, v40\n"
"s_waitcnt vmcnt(0) lgkmcnt(0)\n"
"v_xor_b32 v26, s85, v1\n"
"v_xor_b32 v27, s85, v3\n"
"v_bitop3_b32 v26, v5, s85, v26 bitop3:0xbe\n"
"v_bitop3_b32 v27, v7, s85, v27 bitop3:0xbe\n"
"v_bitop3_b32 v26, v9, s85, v26 bitop3:0xbe\n"
"v_bitop3_b32 v27, v11, s85, v27 bitop3:0xbe\n"
"v_bitop3_b32 v26, v13, s85, v26 bitop3:0xbe\n"
"v_bitop3_b32 v27, v15, s85, v27 bitop3:0xbe\n"
"v_bitop3_b32 v26, v17, s85, v26 bitop3:0xbe\n"
"v_bitop3_b32 v27, v19, s85, v27 bitop3:0xbe\n"
"v_bitop3_b32 v26, v21, s85, v26 bitop3:0xbe\n"
"v_bitop3_b32 v27, v23, s85, v27 bitop3:0xbe\n"
"v_bitop3_b32 v26, v25, s85, v26 bitop3:0xbe\n"
"v_or_b32 v26, v26, v27\n"
"v_cmp_ne_u32 vcc, 0, v26\n"
"s_nop 0\n"
"s_cbranch_vccz AT_F3\n"
"s_sub_u32 s87, s87, 1\n"
"s_cmp_eq_u32 s87, 0\n"
"s_cbranch_scc1 AT_F3\n"
"s_sleep 2\n"
"s_branch AT_F2\n"
"AT_F3:\n"
"v_add_f32 v0, v0, v4\n"
"v_add_f32 v2, v2, v6\n"
"v_add_f32 v8, v8, v12\n"
"v_add_f32 v10, v10, v14\n"
"v_add_f32 v16, v16, v20\n"
"v_add_f32 v18, v18, v22\n"
"v_add_f32 v0, v0, v8\n"
"v_add_f32 v2, v2, v10\n"
"v_add_f32 v0, v0, v16\n"
"v_add_f32 v1, v2, v18\n"
"v_mul_f32 v40, v40, v39\n"
"s_lshl_b32 s62, s41, 6\n"
"v_add3_u32 v28, v37, v227, s62\n"
"v_add_f32_dpp v0, v0, v0 row_ror:8 row_mask:0xf bank_mask:0xf\n"
"v_add_f32_dpp v1, v1, v1 row_ror:8 row_mask:0xf bank_mask:0xf\n"
"s_nop 0\n"
"v_mov_b32 v2, v0\n"
"v_mov_b32 v3, v1\n"
"s_nop 0\n"
"v_permlane16_swap_b32 v0, v2\n"
"v_permlane16_swap_b32 v1, v3\n"
"v_add_f32 v0, v0, v2\n"
"v_add_f32 v1, v1, v3\n"
"v_mov_b32 v2, v0\n"
"v_mov_b32 v3, v1\n"
"s_nop 0\n"
"v_permlane32_swap_b32 v0, v2\n"
"v_permlane32_swap_b32 v1, v3\n"
"v_add_f32 v0, v0, v2\n"
"v_add_f32 v1, v1, v3\n"
"s_mov_b64 exec, 0xff\n"
"ds_write_b64 v28, v[0:1] offset:1024\n"
"s_mov_b64 exec, -1\n"
"v_lshl_add_u32 v28, v36, 2, v227\n"
"s_waitcnt lgkmcnt(0)\n"
"s_barrier\n"
"s_cmp_lg_u32 s41, 0\n"
"s_cbranch_scc1 AT_end\n"
"ds_read_b32 v0, v28 offset:1024\n"
"ds_read_b32 v1, v28 offset:1088\n"
"ds_read_b32 v2, v28 offset:1152\n"
"ds_read_b32 v3, v28 offset:1216\n"
"s_mul_i32 s61, s59, 576\n"
"s_add_u32 s61, s61, s58\n"
"s_lshl_b32 s61, s61, 1\n"
"v_lshlrev_b32 v5, 1, v36\n"
"s_waitcnt lgkmcnt(0)\n"
"v_add_f32 v0, v0, v1\n"
"v_add_f32 v2, v2, v3\n"
"v_add_f32 v0, v0, v2\n"
"v_fma_f32 v0, v0, v40, v24\n"
"v_cvt_pk_bf16_f32 v0, v0, v0\n"
"s_mov_b64 exec, 0xffff\n"
"buffer_store_short v0, v5, s[76:79], s61 offen\n"
"s_branch AT_end\n"
"AT_fb:\n"
"s_waitcnt vmcnt(0) lgkmcnt(0)\n"
"s_barrier\n"
"s_mov_b32 s45, 1\n"
"s_mov_b32 s87, 32\n"
"s_lshl_b32 s84, s41, 12\n"
"s_lshl_b32 s56, s43, 19\n"
"s_add_u32 s84, s84, s56\n"
"s_lshl_b32 s56, s43, 2\n"
"s_add_u32 s56, s56, s41\n"
"s_mul_i32 s52, s56, 73728\n"
"v_mov_b32 v160, 0xff800000\n"
"v_mov_b32 v161, 0xff800000\n"
"v_mov_b32 v162, 0xff800000\n"
"v_mov_b32 v163, 0xff800000\n"
"AT_fbl:\n"
"s_mov_b32 m0, s86\n"
"s_nop 0\n"
"buffer_load_dwordx4 v246, s[8:11], s84 offen offset:0 lds\n"
"buffer_load_dwordx4 v246, s[8:11], s84 offen offset:1024 lds\n"
"buffer_load_dwordx4 v247, s[8:11], s84 offen offset:2048 lds\n"
"buffer_load_dwordx4 v247, s[8:11], s84 offen offset:3072 lds\n"
"s_add_u32 s84, s84, 16384\n"
"s_waitcnt vmcnt(0)\n"
"s_barrier\n"
"ds_read_b128 a[128:131], v240 offset:0\n"
"ds_read_b128 a[132:135], v240 offset:256\n"
"ds_read_b128 a[136:139], v240 offset:512\n"
"ds_read_b128 a[140:143], v240 offset:768\n"
"ds_read_b128 a[144:147], v240 offset:4096\n"
"ds_read_b128 a[148:151], v240 offset:4352\n"
"ds_read_b128 a[152:155], v240 offset:4608\n"
"ds_read_b128 a[156:159], v240 offset:4864\n"
"s_waitcnt lgkmcnt(0)\n"
"v_mfma_f32_16x16x32_bf16 v[128:131], a[128:131], v[64:67], 0\n"
"v_mfma_f32_16x16x32_bf16 v[128:131], a[132:135], v[68:71], v[128:131]\n"
"v_mfma_f32_16x16x32_bf16 v[128:131], a[136:139], v[72:75], v[128:131]\n"
"v_mfma_f32_16x16x32_bf16 v[128:131], a[140:143], v[76:79], v[128:131]\n"
"v_mfma_f32_16x16x32_bf16 v[132:135], a[144:147], v[64:67], 0\n"
"v_mfma_f32_16x16x32_bf16 v[132:135], a[148:151], v[68:71], v[132:135]\n"
"v_mfma_f32_16x16x32_bf16 v[132:135], a[152:155], v[72:75], v[132:135]\n"
"v_mfma_f32_16x16x32_bf16 v[132:135], a[156:159], v[76:79], v[132:135]\n"
"v_mfma_f32_16x16x32_bf16 v[136:139], a[128:131], v[80:83], 0\n"
"v_mfma_f32_16x16x32_bf16 v[136:139], a[132:135], v[84:87], v[136:139]\n"
"v_mfma_f32_16x16x32_bf16 v[136:139], a[136:139], v[88:91], v[136:139]\n"
"v_mfma_f32_16x16x32_bf16 v[136:139], a[140:143], v[92:95], v[136:139]\n"
"v_mfma_f32_16x16x32_bf16 v[140:143], a[144:147], v[80:83], 0\n"
"v_mfma_f32_16x16x32_bf16 v[140:143], a[148:151], v[84:87], v[140:143]\n"
"v_mfma_f32_16x16x32_bf16 v[140:143], a[152:155], v[88:91], v[140:143]\n"
"v_mfma_f32_16x16x32_bf16 v[140:143], a[156:159], v[92:95], v[140:143]\n"
"v_mfma_f32_16x16x32_bf16 v[144:147], a[128:131], v[96:99], 0\n"
"v_mfma_f32_16x16x32_bf16 v[144:147], a[132:135], v[100:103], v[144:147]\n"
"v_mfma_f32_16x16x32_bf16 v[144:147], a[136:139], v[104:107], v[144:147]\n"
"v_mfma_f32_16x16x32_bf16 v[144:147], a[140:143], v[108:111], v[144:147]\n"
"v_mfma_f32_16x16x32_bf16 v[148:151], a[144:147], v[96:99], 0\n"
"v_mfma_f32_16x16x32_bf16 v[148:151], a[148:151], v[100:103], v[148:151]\n"
"v_mfma_f32_16x16x32_bf16 v[148:151], a[152:155], v[104:107], v[148:151]\n"
"v_mfma_f32_16x16x32_bf16 v[148:151], a[156:159], v[108:111], v[148:151]\n"
"v_mfma_f32_16x16x32_bf16 v[152:155], a[128:131], v[112:115], 0\n"
"v_mfma_f32_16x16x32_bf16 v[152:155], a[132:135], v[116:119], v[152:155]\n"
"v_mfma_f32_16x16x32_bf16 v[152:155], a[136:139], v[120:123], v[152:155]\n"
"v_mfma_f32_16x16x32_bf16 v[152:155], a[140:143], v[124:127], v[152:155]\n"
"v_mfma_f32_16x16x32_bf16 v[156:159], a[144:147], v[112:115], 0\n"
"v_mfma_f32_16x16x32_bf16 v[156:159], a[148:151], v[116:119], v[156:159]\n"
"v_mfma_f32_16x16x32_bf16 v[156:159], a[152:155], v[120:123], v[156:159]\n"
"v_mfma_f32_16x16x32_bf16 v[156:159], a[156:159], v[124:127], v[156:159]\n"
"s_nop 7\n"
"v_max3_f32 v160, v160, v128, v129\n"
"v_max3_f32 v160, v160, v130, v131\n"
"v_max3_f32 v160, v160, v132, v133\n"
"v_max3_f32 v160, v160, v134, v135\n"
"v_max3_f32 v161, v161, v136, v137\n"
"v_max3_f32 v161, v161, v138, v139\n"
"v_max3_f32 v161, v161, v140, v141\n"
"v_max3_f32 v161, v161, v142, v143\n"
"v_max3_f32 v162, v162, v144, v145\n"
"v_max3_f32 v162, v162, v146, v147\n"
"v_max3_f32 v162, v162, v148, v149\n"
"v_max3_f32 v162, v162, v150, v151\n"
"v_max3_f32 v163, v163, v152, v153\n"
"v_max3_f32 v163, v163, v154, v155\n"
"v_max3_f32 v163, v163, v156, v157\n"
"v_max3_f32 v163, v163, v158, v159\n"
"ds_read_b128 a[128:131], v240 offset:8192\n"
"ds_read_b128 a[132:135], v240 offset:8448\n"
"ds_read_b128 a[136:139], v240 offset:8704\n"
"ds_read_b128 a[140:143], v240 offset:8960\n"
"ds_read_b128 a[144:147], v240 offset:12288\n"
"ds_read_b128 a[148:151], v240 offset:12544\n"
"ds_read_b128 a[152:155], v240 offset:12800\n"
"ds_read_b128 a[156:159], v240 offset:13056\n"
"s_waitcnt lgkmcnt(0)\n"
"v_mfma_f32_16x16x32_bf16 v[128:131], a[128:131], v[64:67], 0\n"
"v_mfma_f32_16x16x32_bf16 v[128:131], a[132:135], v[68:71], v[128:131]\n"
"v_mfma_f32_16x16x32_bf16 v[128:131], a[136:139], v[72:75], v[128:131]\n"
"v_mfma_f32_16x16x32_bf16 v[128:131], a[140:143], v[76:79], v[128:131]\n"
"v_mfma_f32_16x16x32_bf16 v[132:135], a[144:147], v[64:67], 0\n"
"v_mfma_f32_16x16x32_bf16 v[132:135], a[148:151], v[68:71], v[132:135]\n"
"v_mfma_f32_16x16x32_bf16 v[132:135], a[152:155], v[72:75], v[132:135]\n"
"v_mfma_f32_16x16x32_bf16 v[132:135], a[156:159], v[76:79], v[132:135]\n"
"v_mfma_f32_16x16x32_bf16 v[136:139], a[128:131], v[80:83], 0\n"
"v_mfma_f32_16x16x32_bf16 v[136:139], a[132:135], v[84:87], v[136:139]\n"
"v_mfma_f32_16x16x32_bf16 v[136:139], a[136:139], v[88:91], v[136:139]\n"
"v_mfma_f32_16x16x32_bf16 v[136:139], a[140:143], v[92:95], v[136:139]\n"
"v_mfma_f32_16x16x32_bf16 v[140:143], a[144:147], v[80:83], 0\n"
"v_mfma_f32_16x16x32_bf16 v[140:143], a[148:151], v[84:87], v[140:143]\n"
"v_mfma_f32_16x16x32_bf16 v[140:143], a[152:155], v[88:91], v[140:143]\n"
"v_mfma_f32_16x16x32_bf16 v[140:143], a[156:159], v[92:95], v[140:143]\n"
"v_mfma_f32_16x16x32_bf16 v[144:147], a[128:131], v[96:99], 0\n"
"v_mfma_f32_16x16x32_bf16 v[144:147], a[132:135], v[100:103], v[144:147]\n"
"v_mfma_f32_16x16x32_bf16 v[144:147], a[136:139], v[104:107], v[144:147]\n"
"v_mfma_f32_16x16x32_bf16 v[144:147], a[140:143], v[108:111], v[144:147]\n"
"v_mfma_f32_16x16x32_bf16 v[148:151], a[144:147], v[96:99], 0\n"
"v_mfma_f32_16x16x32_bf16 v[148:151], a[148:151], v[100:103], v[148:151]\n"
"v_mfma_f32_16x16x32_bf16 v[148:151], a[152:155], v[104:107], v[148:151]\n"
"v_mfma_f32_16x16x32_bf16 v[148:151], a[156:159], v[108:111], v[148:151]\n"
"v_mfma_f32_16x16x32_bf16 v[152:155], a[128:131], v[112:115], 0\n"
"v_mfma_f32_16x16x32_bf16 v[152:155], a[132:135], v[116:119], v[152:155]\n"
"v_mfma_f32_16x16x32_bf16 v[152:155], a[136:139], v[120:123], v[152:155]\n"
"v_mfma_f32_16x16x32_bf16 v[152:155], a[140:143], v[124:127], v[152:155]\n"
"v_mfma_f32_16x16x32_bf16 v[156:159], a[144:147], v[112:115], 0\n"
"v_mfma_f32_16x16x32_bf16 v[156:159], a[148:151], v[116:119], v[156:159]\n"
"v_mfma_f32_16x16x32_bf16 v[156:159], a[152:155], v[120:123], v[156:159]\n"
"v_mfma_f32_16x16x32_bf16 v[156:159], a[156:159], v[124:127], v[156:159]\n"
"s_nop 7\n"
"v_max3_f32 v160, v160, v128, v129\n"
"v_max3_f32 v160, v160, v130, v131\n"
"v_max3_f32 v160, v160, v132, v133\n"
"v_max3_f32 v160, v160, v134, v135\n"
"v_max3_f32 v161, v161, v136, v137\n"
"v_max3_f32 v161, v161, v138, v139\n"
"v_max3_f32 v161, v161, v140, v141\n"
"v_max3_f32 v161, v161, v142, v143\n"
"v_max3_f32 v162, v162, v144, v145\n"
"v_max3_f32 v162, v162, v146, v147\n"
"v_max3_f32 v162, v162, v148, v149\n"
"v_max3_f32 v162, v162, v150, v151\n"
"v_max3_f32 v163, v163, v152, v153\n"
"v_max3_f32 v163, v163, v154, v155\n"
"v_max3_f32 v163, v163, v156, v157\n"
"v_max3_f32 v163, v163, v158, v159\n"
"s_barrier\n"
"s_sub_u32 s87, s87, 1\n"
"s_cmp_lg_u32 s87, 0\n"
"s_cbranch_scc1 AT_fbl\n"
"v_mov_b32 v164, v160\n"
"s_nop 1\n"
"v_permlane32_swap_b32 v160, v164\n"
"v_max_f32 v160, v160, v164\n"
"v_mov_b32 v164, v160\n"
"s_nop 1\n"
"v_permlane16_swap_b32 v160, v164\n"
"v_max_f32 v160, v160, v164\n"
"v_sub_f32 v24, 0, v160\n"
"v_sub_f32 v25, 0, v160\n"
"v_sub_f32 v26, 0, v160\n"
"v_sub_f32 v27, 0, v160\n"
"v_mov_b32 v164, v161\n"
"s_nop 1\n"
"v_permlane32_swap_b32 v161, v164\n"
"v_max_f32 v161, v161, v164\n"
"v_mov_b32 v164, v161\n"
"s_nop 1\n"
"v_permlane16_swap_b32 v161, v164\n"
"v_max_f32 v161, v161, v164\n"
"v_sub_f32 v28, 0, v161\n"
"v_sub_f32 v29, 0, v161\n"
"v_sub_f32 v30, 0, v161\n"
"v_sub_f32 v31, 0, v161\n"
"v_mov_b32 v164, v162\n"
"s_nop 1\n"
"v_permlane32_swap_b32 v162, v164\n"
"v_max_f32 v162, v162, v164\n"
"v_mov_b32 v164, v162\n"
"s_nop 1\n"
"v_permlane16_swap_b32 v162, v164\n"
"v_max_f32 v162, v162, v164\n"
"v_sub_f32 v32, 0, v162\n"
"v_sub_f32 v33, 0, v162\n"
"v_sub_f32 v34, 0, v162\n"
"v_sub_f32 v35, 0, v162\n"
"v_mov_b32 v164, v163\n"
"s_nop 1\n"
"v_permlane32_swap_b32 v163, v164\n"
"v_max_f32 v163, v163, v164\n"
"v_mov_b32 v164, v163\n"
"s_nop 1\n"
"v_permlane16_swap_b32 v163, v164\n"
"v_max_f32 v163, v163, v164\n"
"v_sub_f32 v36, 0, v163\n"
"v_sub_f32 v37, 0, v163\n"
"v_sub_f32 v38, 0, v163\n"
"v_sub_f32 v39, 0, v163\n"
"v_mov_b32 v40, 0x3f803f80\n"
"v_mov_b32 v41, 0x3f803f80\n"
"v_mov_b32 v42, 0x3f803f80\n"
"v_mov_b32 v43, 0x3f803f80\n"
"v_mov_b32 v56, 0\n"
"v_mov_b32 v57, 0\n"
"v_mov_b32 v58, 0\n"
"v_mov_b32 v59, 0\n"
"v_accvgpr_write_b32 a96, 0\n"
"v_accvgpr_write_b32 a97, 0\n"
"v_accvgpr_write_b32 a98, 0\n"
"v_accvgpr_write_b32 a99, 0\n"
"v_accvgpr_write_b32 a100, 0\n"
"v_accvgpr_write_b32 a101, 0\n"
"v_accvgpr_write_b32 a102, 0\n"
"v_accvgpr_write_b32 a103, 0\n"
"v_accvgpr_write_b32 a104, 0\n"
"v_accvgpr_write_b32 a105, 0\n"
"v_accvgpr_write_b32 a106, 0\n"
"v_accvgpr_write_b32 a107, 0\n"
"v_accvgpr_write_b32 a108, 0\n"
"v_accvgpr_write_b32 a109, 0\n"
"v_accvgpr_write_b32 a110, 0\n"
"v_accvgpr_write_b32 a111, 0\n"
"v_accvgpr_write_b32 a112, 0\n"
"v_accvgpr_write_b32 a113, 0\n"
"v_accvgpr_write_b32 a114, 0\n"
"v_accvgpr_write_b32 a115, 0\n"
"v_accvgpr_write_b32 a116, 0\n"
"v_accvgpr_write_b32 a117, 0\n"
"v_accvgpr_write_b32 a118, 0\n"
"v_accvgpr_write_b32 a119, 0\n"
"v_accvgpr_write_b32 a120, 0\n"
"v_accvgpr_write_b32 a121, 0\n"
"v_accvgpr_write_b32 a122, 0\n"
"v_accvgpr_write_b32 a123, 0\n"
"v_accvgpr_write_b32 a124, 0\n"
"v_accvgpr_write_b32 a125, 0\n"
"v_accvgpr_write_b32 a126, 0\n"
"v_accvgpr_write_b32 a127, 0\n"
"s_lshl_b32 s84, s41, 12\n"
"s_lshl_b32 s56, s43, 19\n"
"s_add_u32 s84, s84, s56\n"
"s_lshl_b32 s56, s43, 2\n"
"s_add_u32 s56, s56, s41\n"
"s_mul_i32 s52, s56, 73728\n"
"v_mov_b32 v215, 0x40000000\n"
"v_mov_b32 v216, 0x40000000\n"
"v_mov_b32 v217, 0x40000000\n"
"s_mov_b32 s46, 0\n"
"s_mov_b32 s47, 0\n"
"s_mov_b32 m0, s86\n"
"s_nop 0\n"
"buffer_load_dwordx4 v246, s[8:11], s84 offen offset:0 lds\n"
"buffer_load_dwordx4 v246, s[8:11], s84 offen offset:1024 lds\n"
"buffer_load_dwordx4 v247, s[8:11], s84 offen offset:2048 lds\n"
"buffer_load_dwordx4 v247, s[8:11], s84 offen offset:3072 lds\n"
"s_add_u32 m0, s86, 16384\n"
"s_nop 0\n"
"buffer_load_dwordx4 v246, s[12:15], s84 offen offset:0 lds\n"
"buffer_load_dwordx4 v247, s[12:15], s84 offen offset:1024 lds\n"
"buffer_load_dwordx4 v246, s[12:15], s84 offen offset:2048 lds\n"
"buffer_load_dwordx4 v247, s[12:15], s84 offen offset:3072 lds\n"
"s_add_u32 s84, s84, 16384\n"
"s_add_u32 m0, s86, 32768\n"
"s_nop 0\n"
"buffer_load_dwordx4 v246, s[8:11], s84 offen offset:0 lds\n"
"buffer_load_dwordx4 v246, s[8:11], s84 offen offset:1024 lds\n"
"buffer_load_dwordx4 v247, s[8:11], s84 offen offset:2048 lds\n"
"buffer_load_dwordx4 v247, s[8:11], s84 offen offset:3072 lds\n"
"s_add_u32 m0, s86, 49152\n"
"s_nop 0\n"
"buffer_load_dwordx4 v246, s[12:15], s84 offen offset:0 lds\n"
"buffer_load_dwordx4 v247, s[12:15], s84 offen offset:1024 lds\n"
"buffer_load_dwordx4 v246, s[12:15], s84 offen offset:2048 lds\n"
"buffer_load_dwordx4 v247, s[12:15], s84 offen offset:3072 lds\n"
"s_add_u32 s84, s84, 16384\n"
"s_add_u32 m0, s86, 65536\n"
"s_nop 0\n"
"buffer_load_dwordx4 v246, s[8:11], s84 offen offset:0 lds\n"
"buffer_load_dwordx4 v246, s[8:11], s84 offen offset:1024 lds\n"
"buffer_load_dwordx4 v247, s[8:11], s84 offen offset:2048 lds\n"
"buffer_load_dwordx4 v247, s[8:11], s84 offen offset:3072 lds\n"
"s_add_u32 m0, s86, 81920\n"
"s_nop 0\n"
"buffer_load_dwordx4 v246, s[12:15], s84 offen offset:0 lds\n"
"buffer_load_dwordx4 v247, s[12:15], s84 offen offset:1024 lds\n"
"buffer_load_dwordx4 v246, s[12:15], s84 offen offset:2048 lds\n"
"buffer_load_dwordx4 v247, s[12:15], s84 offen offset:3072 lds\n"
"s_add_u32 s84, s84, 16384\n"
"s_add_u32 m0, s86, 98304\n"
"s_nop 0\n"
"buffer_load_dwordx4 v246, s[8:11], s84 offen offset:0 lds\n"
"buffer_load_dwordx4 v246, s[8:11], s84 offen offset:1024 lds\n"
"buffer_load_dwordx4 v247, s[8:11], s84 offen offset:2048 lds\n"
"buffer_load_dwordx4 v247, s[8:11], s84 offen offset:3072 lds\n"
"s_add_u32 m0, s86, 114688\n"
"s_nop 0\n"
"buffer_load_dwordx4 v246, s[12:15], s84 offen offset:0 lds\n"
"buffer_load_dwordx4 v247, s[12:15], s84 offen offset:1024 lds\n"
"buffer_load_dwordx4 v246, s[12:15], s84 offen offset:2048 lds\n"
"buffer_load_dwordx4 v247, s[12:15], s84 offen offset:3072 lds\n"
"s_add_u32 s84, s84, 16384\n"
"s_add_u32 m0, s53, 131072\n"
"s_nop 0\n"
"buffer_load_dwordx4 v206, s[24:27], s52 offen offset:0 lds\n"
"s_add_u32 s52, s52, 8192\n"
"buffer_load_dwordx4 v206, s[24:27], s52 offen offset:1024 lds\n"
"s_add_u32 s52, s52, 8192\n"
"buffer_load_dwordx4 v206, s[24:27], s52 offen offset:2048 lds\n"
"s_add_u32 s52, s52, 8192\n"
"buffer_load_dwordx4 v206, s[24:27], s52 offen offset:3072 lds\n"
"s_add_u32 s52, s52, 12288\n"
"s_add_u32 m0, s53, 135168\n"
"s_nop 0\n"
"buffer_load_dwordx4 v206, s[24:27], s52 offen offset:0 lds\n"
"s_add_u32 s52, s52, 8192\n"
"buffer_load_dwordx4 v206, s[24:27], s52 offen offset:1024 lds\n"
"s_add_u32 s52, s52, 8192\n"
"buffer_load_dwordx4 v206, s[24:27], s52 offen offset:2048 lds\n"
"s_add_u32 s52, s52, 8192\n"
"buffer_load_dwordx4 v206, s[24:27], s52 offen offset:3072 lds\n"
"s_sub_u32 s52, s52, 61312\n"
"s_waitcnt vmcnt(32)\n"
"s_barrier\n"
"ds_read_b128 a[128:131], v240 offset:0\n"
"ds_read_b128 a[132:135], v240 offset:256\n"
"ds_read_b128 a[136:139], v240 offset:512\n"
"ds_read_b128 a[140:143], v240 offset:768\n"
"ds_read_b128 a[144:147], v240 offset:4096\n"
"ds_read_b128 a[148:151], v240 offset:4352\n"
"ds_read_b128 a[152:155], v240 offset:4608\n"
"ds_read_b128 a[156:159], v240 offset:4864\n"
"ds_read_b64_tr_b16 a[192:193], v242 offset:16384\n"
"ds_read_b64_tr_b16 a[194:195], v242 offset:20480\n"
"ds_read_b64_tr_b16 a[196:197], v243 offset:16384\n"
"ds_read_b64_tr_b16 a[198:199], v243 offset:20480\n"
"ds_read_b64_tr_b16 a[200:201], v242 offset:16640\n"
"ds_read_b64_tr_b16 a[202:203], v242 offset:20736\n"
"ds_read_b64_tr_b16 a[204:205], v243 offset:16640\n"
"ds_read_b64_tr_b16 a[206:207], v243 offset:20736\n"
"ds_read_b64_tr_b16 a[208:209], v242 offset:16896\n"
"ds_read_b64_tr_b16 a[210:211], v242 offset:20992\n"
"ds_read_b64_tr_b16 a[212:213], v243 offset:16896\n"
"ds_read_b64_tr_b16 a[214:215], v243 offset:20992\n"
"ds_read_b64_tr_b16 a[216:217], v242 offset:17152\n"
"ds_read_b64_tr_b16 a[218:219], v242 offset:21248\n"
"ds_read_b64_tr_b16 a[220:221], v243 offset:17152\n"
"ds_read_b64_tr_b16 a[222:223], v243 offset:21248\n"
"s_waitcnt lgkmcnt(0)\n"
"s_branch AT_main\n"
"AT_end:\n"
"AT_kend:\n"
  :: [kp] "s"(__builtin_amdgcn_kernarg_segment_ptr()), [blk] "s"(blockIdx.x), [wid] "s"(wid), [lds] "s"(ldsb) : "v0", "v1", "v2", "v3", "v4", "v5", "v6", "v7", "v8", "v9", "v10", "v11", "v12", "v13", "v14", "v15", "v16", "v17", "v18", "v19", "v20", "v21", "v22", "v23", "v24", "v25", "v26", "v27", "v28", "v29", "v30", "v31", "v32", "v33", "v34", "v35", "v36", "v37", "v38", "v39", "v40", "v41", "v42", "v43", "v44", "v45", "v46", "v47", "v48", "v49", "v50", "v51", "v52", "v53", "v54", "v55", "v56", "v57", "v58", "v59", "v60", "v61", "v62", "v63", "v64", "v65", "v66", "v67", "v68", "v69", "v70", "v71", "v72", "v73", "v74", "v75", "v76", "v77", "v78", "v79", "v80", "v81", "v82", "v83", "v84", "v85", "v86", "v87", "v88", "v89", "v90", "v91", "v92", "v93", "v94", "v95", "v96", "v97", "v98", "v99", "v100", "v101", "v102", "v103", "v104", "v105", "v106", "v107", "v108", "v109", "v110", "v111", "v112", "v113", "v114", "v115", "v116", "v117", "v118", "v119", "v120", "v121", "v122", "v123", "v124", "v125", "v126", "v127", "v128", "v129", "v130", "v131", "v132", "v133", "v134", "v135", "v136", "v137", "v138", "v139", "v140", "v141", "v142", "v143", "v144", "v145", "v146", "v147", "v148", "v149", "v150", "v151", "v152", "v153", "v154", "v155", "v156", "v157", "v158", "v159", "v160", "v161", "v162", "v163", "v164", "v165", "v166", "v167", "v168", "v169", "v170", "v171", "v172", "v173", "v174", "v175", "v176", "v177", "v178", "v179", "v180", "v181", "v182", "v183", "v184", "v185", "v186", "v187", "v188", "v189", "v190", "v191", "v192", "v193", "v194", "v195", "v196", "v197", "v198", "v199", "v200", "v201", "v202", "v203", "v204", "v205", "v206", "v207", "v208", "v209", "v210", "v211", "v212", "v213", "v214", "v215", "v216", "v217", "v218", "v219", "v220", "v221", "v222", "v223", "v224", "v225", "v226", "v227", "v228", "v229", "v230", "v231", "v232", "v233", "v234", "v235", "v236", "v237", "v238", "v239", "v240", "v241", "v242", "v243", "v244", "v245", "v246", "v247", "v248", "v249", "v250", "v251", "v252", "v253", "v254", "v255", "a0", "a1", "a2", "a3", "a4", "a5", "a6", "a7", "a8", "a9", "a10", "a11", "a12", "a13", "a14", "a15", "a16", "a17", "a18", "a19", "a20", "a21", "a22", "a23", "a24", "a25", "a26", "a27", "a28", "a29", "a30", "a31", "a32", "a33", "a34", "a35", "a36", "a37", "a38", "a39", "a40", "a41", "a42", "a43", "a44", "a45", "a46", "a47", "a48", "a49", "a50", "a51", "a52", "a53", "a54", "a55", "a56", "a57", "a58", "a59", "a60", "a61", "a62", "a63", "a64", "a65", "a66", "a67", "a68", "a69", "a70", "a71", "a72", "a73", "a74", "a75", "a76", "a77", "a78", "a79", "a80", "a81", "a82", "a83", "a84", "a85", "a86", "a87", "a88", "a89", "a90", "a91", "a92", "a93", "a94", "a95", "a96", "a97", "a98", "a99", "a100", "a101", "a102", "a103", "a104", "a105", "a106", "a107", "a108", "a109", "a110", "a111", "a112", "a113", "a114", "a115", "a116", "a117", "a118", "a119", "a120", "a121", "a122", "a123", "a124", "a125", "a126", "a127", "a128", "a129", "a130", "a131", "a132", "a133", "a134", "a135", "a136", "a137", "a138", "a139", "a140", "a141", "a142", "a143", "a144", "a145", "a146", "a147", "a148", "a149", "a150", "a151", "a152", "a153", "a154", "a155", "a156", "a157", "a158", "a159", "a160", "a161", "a162", "a163", "a164", "a165", "a166", "a167", "a168", "a169", "a170", "a171", "a172", "a173", "a174", "a175", "a176", "a177", "a178", "a179", "a180", "a181", "a182", "a183", "a184", "a185", "a186", "a187", "a188", "a189", "a190", "a191", "a192", "a193", "a194", "a195", "a196", "a197", "a198", "a199", "a200", "a201", "a202", "a203", "a204", "a205", "a206", "a207", "a208", "a209", "a210", "a211", "a212", "a213", "a214", "a215", "a216", "a217", "a218", "a219", "a220", "a221", "a222", "a223", "a224", "a225", "a226", "a227", "a228", "a229", "a230", "a231", "a232", "a233", "a234", "a235", "a236", "a237", "a238", "a239", "a240", "a241", "a242", "a243", "a244", "a245", "a246", "a247", "a248", "a249", "a250", "a251", "a252", "a253", "a254", "a255", "s8", "s9", "s10", "s11", "s12", "s13", "s14", "s15", "s16", "s17", "s18", "s19", "s20", "s21", "s22", "s23", "s24", "s25", "s26", "s27", "s28", "s29", "s30", "s31", "s32", "s33", "s34", "s35", "s36", "s37", "s38", "s39", "s40", "s41", "s42", "s43", "s44", "s45", "s46", "s47", "s48", "s49", "s50", "s51", "s52", "s53", "s54", "s55", "s56", "s57", "s58", "s59", "s60", "s61", "s62", "s63", "s64", "s65", "s66", "s67", "s68", "s69", "s70", "s71", "s72", "s73", "s74", "s75", "s76", "s77", "s78", "s79", "s80", "s81", "s82", "s83", "s84", "s85", "s86", "s87", "s88", "s89", "s90", "s91", "vcc", "scc", "memory");
}

extern "C" void kernel_launch(void* const* d_in, const int* in_sizes, int n_in, void* d_out, int out_size, void* d_ws, size_t ws_size,
                              hipStream_t stream) {
  const unsigned F = 0x20000u;
  KArgs a;
  a.k = {d_in[1], HKV * SQ * 256u, F}; a.v = {d_in[2], HKV * SQ * 256u, F}; a.x = {d_in[0], SQ * DM * 2u, F}; a.wq = {d_in[3], DM * 4096u * 2u, F};
  a.wo = {d_in[4], DM * 4096u * 2u, F}; a.ws = {d_ws, (unsigned)WS_END, F}; a.c = {d_in[10], DM * 2u, F}; a.wmod = {d_in[11], DM * 6u * DM * 2u, F};
  a.wg = {d_in[7], DM * FF * 2u, F}; a.wu = {d_in[8], DM * FF * 2u, F}; a.wd = {d_in[9], DM * FF * 2u, F}; a.out = {d_out, SQ * DM * 2u, F}; a.wr = {d_in[6], DM * 2u, F};
  (void)hipFuncSetAttribute((const void*)k_main, hipFuncAttributeMaxDynamicSharedMemorySize, 163840);
  k_main<<<256, 256, 163840, stream>>>(a);
}
```

```cpp
#include <hip/hip_runtime.h>
#include <stdint.h>


constexpr int SQ = 2048, DM = 576, HKV = 16, FF = 4608;

constexpr size_t WS_FLAG = 0;
constexpr size_t WS_R    = 20480;
constexpr size_t WS_GRAN = 32768;
constexpr size_t WS_EPOCH = 65536;
constexpr size_t WS_ST   = 69632;
constexpr size_t WS_PART = 139264;
constexpr size_t WS_CAND = 16384;
constexpr size_t WS_ROW  = WS_PART + (size_t)HKV * SQ * DM * 2;
constexpr size_t WS_Y    = WS_ROW + (size_t)SQ * DM * 16;
constexpr size_t WS_END  = WS_Y + 72 * 192 * 128;

struct Buf { const void* p; unsigned bytes; unsigned fmt; };
struct KArgs { Buf k, v, x, wq, wo, ws, c, wmod, wg, wu, wd, out, wr; };

__global__ __launch_bounds__(256, 1) void k_main(KArgs args) {
  extern __shared__ __attribute__((aligned(16))) char lds[];
  const unsigned wid = __builtin_amdgcn_readfirstlane(threadIdx.x >> 6);
  const unsigned ldsb = (unsigned)(size_t)lds;
  asm volatile(
"AT_start:\n"
"s_load_dwordx16 s[24:39], %[kp], 64\n"
"s_load_dwordx16 s[8:23], %[kp], 0\n"
"s_load_dwordx16 s[64:79], %[kp], 128\n"
"s_load_dwordx4 s[80:83], %[kp], 192\n"
"s_mov_b32 s42, %[blk]\n"
"s_mov_b32 s41, %[wid]\n"
"s_mov_b32 s40, %[lds]\n"
"s_mov_b32 s45, 0\n"
"v_mbcnt_lo_u32_b32 v248, -1, 0\n"
"v_mbcnt_hi_u32_b32 v248, -1, v248\n"
"s_and_b32 s56, s42, 3\n"
"s_lshr_b32 s57, s42, 3\n"
"s_and_b32 s58, s57, 3\n"
"s_lshl_b32 s56, s56, 2\n"
"s_add_u32 s43, s56, s58\n"
"s_lshr_b32 s44, s57, 2\n"
"s_and_b32 s56, s42, 7\n"
"s_lshr_b32 s56, s56, 2\n"
"s_lshl_b32 s56, s56, 3\n"
"s_add_u32 s44, s44, s56\n"
"s_lshr_b32 s56, s42, 5\n"
"s_lshl_b32 s56, s56, 3\n"
"s_and_b32 s58, s42, 7\n"
"s_add_u32 s56, s56, s58\n"
"s_lshl_b32 s56, s56, 2\n"
"s_and_b32 s57, s57, 3\n"
"s_add_u32 s55, s56, s57\n"
"v_lshrrev_b32 v226, 4, v248\n"
"v_bfe_u32 v227, v248, 2, 2\n"
"v_and_b32 v228, 3, v248\n"
"v_and_b32 v229, 15, v248\n"
"v_lshrrev_b32 v230, 3, v248\n"
"v_and_b32 v231, 7, v248\n"
"s_lshl_b32 s56, s41, 6\n"
"v_add_u32 v234, s56, v248\n"
"v_lshrrev_b32 v232, 1, v234\n"
"v_and_b32 v233, 1, v234\n"
"v_lshlrev_b32 v236, 1, v232\n"
"v_mul_u32_u24 v232, 6912, v232\n"
"v_lshl_add_u32 v235, v233, 4, v232\n"
"s_lshl_b32 s56, s55, 5\n"
"s_add_u32 s57, s56, 884736\n"
"s_add_u32 s58, s56, 1769472\n"
"s_add_u32 s59, s56, 2654208\n"
"s_add_u32 s60, s56, 3538944\n"
"v_lshrrev_b32 v232, 1, v230\n"
"v_xor_b32 v232, v232, v231\n"
"v_mul_u32_u24 v233, 1152, v230\n"
"v_lshl_add_u32 v222, v232, 4, v233\n"
"v_xor_b32 v232, 4, v232\n"
"v_lshl_add_u32 v223, v232, 4, v233\n"
"s_lshl_b32 s61, s44, 2\n"
"s_add_u32 s61, s61, s41\n"
"s_mul_i32 s46, s61, 36864\n"
"s_add_u32 s47, s46, 9216\n"
"s_add_u32 s48, s46, 18432\n"
"s_add_u32 s49, s46, 27648\n"
"s_mul_i32 s63, s41, 36864\n"
"s_add_u32 s63, s63, s40\n"
"s_getpc_b64 s[84:85]\n"
"AT_pc0:\n"
"s_sub_u32 s84, s84, AT_pc0-AT_start\n"
"s_subb_u32 s85, s85, 0\n"
"s_and_b32 s85, s85, 0xffff\n"
"s_mov_b32 s86, AT_kend-AT_start\n"
"s_mov_b32 s87, 0x20000\n"
"s_cmp_ge_u32 s42, 224\n"
"s_cselect_b32 s86, s86, 0\n"
"s_lshr_b32 s52, s42, 3\n"
"s_lshl_b32 s52, s52, 2\n"
"s_add_u32 s52, s52, s41\n"
"s_and_b32 s52, s52, 15\n"
"s_lshl_b32 s52, s52, 12\n"
"v_lshlrev_b32 v214, 6, v248\n"
"buffer_load_dword v250, v214, s[84:87], s52 offen\n"
"s_add_u32 s52, s52, 65536\n"
"buffer_load_dword v250, v214, s[84:87], s52 offen\n"
"s_waitcnt lgkmcnt(0)\n"
"s_load_dword s85, s[28:29], 65536\n"
"s_cmp_lt_u32 s55, 216\n"
"s_cselect_b32 s38, s38, 0\n"
"buffer_load_ushort v164, v236, s[32:35], 0 offen offset:0\n"
"buffer_load_ushort v165, v236, s[32:35], 0 offen offset:256\n"
"buffer_load_ushort v166, v236, s[32:35], 0 offen offset:512\n"
"buffer_load_ushort v167, v236, s[32:35], 0 offen offset:768\n"
"buffer_load_ushort v168, v236, s[32:35], 0 offen offset:1024\n"
"buffer_load_dwordx4 v[144:147], v235, s[36:39], s56 offen\n"
"buffer_load_dwordx4 v[148:151], v235, s[36:39], s57 offen\n"
"buffer_load_dwordx4 v[152:155], v235, s[36:39], s58 offen\n"
"buffer_load_dwordx4 v[156:159], v235, s[36:39], s59 offen\n"
"buffer_load_dwordx4 v[160:163], v235, s[36:39], s60 offen\n"
"s_mov_b32 m0, s63\n"
"s_nop 0\n"
"buffer_load_dwordx4 v222, s[16:19], s46 offen offset:0 lds\n"
"s_add_u32 m0, s63, 1024\n"
"s_nop 0\n"
"buffer_load_dwordx4 v223, s[16:19], s47 offen offset:0 lds\n"
"s_add_u32 m0, s63, 2048\n"
"s_nop 0\n"
"buffer_load_dwordx4 v222, s[16:19], s48 offen offset:0 lds\n"
"s_add_u32 m0, s63, 3072\n"
"s_nop 0\n"
"buffer_load_dwordx4 v223, s[16:19], s49 offen offset:0 lds\n"
"s_add_u32 m0, s63, 3968\n"
"s_nop 0\n"
"buffer_load_dwordx4 v222, s[16:19], s46 offen offset:128 lds\n"
"s_add_u32 m0, s63, 4992\n"
"s_nop 0\n"
"buffer_load_dwordx4 v223, s[16:19], s47 offen offset:128 lds\n"
"s_add_u32 m0, s63, 6016\n"
"s_nop 0\n"
"buffer_load_dwordx4 v222, s[16:19], s48 offen offset:128 lds\n"
"s_add_u32 m0, s63, 7040\n"
"s_nop 0\n"
"buffer_load_dwordx4 v223, s[16:19], s49 offen offset:128 lds\n"
"s_lshl_b32 s56, s41, 12\n"
"s_add_u32 s86, s40, s56\n"
"s_lshl_b32 s84, s41, 12\n"
"s_lshl_b32 s56, s43, 19\n"
"s_add_u32 s84, s84, s56\n"
"s_lshl_b32 s50, s41, 17\n"
"s_lshl_b32 s56, s43, 9\n"
"s_add_u32 s50, s50, s56\n"
"s_lshl_b32 s56, s41, 11\n"
"s_add_u32 s51, s40, s56\n"
"s_add_u32 m0, s63, 7936\n"
"s_nop 0\n"
"buffer_load_dwordx4 v222, s[16:19], s46 offen offset:256 lds\n"
"s_mov_b32 s61, 32768\n"
"s_mov_b32 s62, 37376\n"
"v_lshlrev_b32 v232, 13, v227\n"
"v_lshl_add_u32 v232, v226, 6, v232\n"
"v_lshl_add_u32 v217, v228, 4, v232\n"
"v_and_b32 v232, 2, v227\n"
"v_xor_b32 v232, v232, v226\n"
"v_lshlrev_b32 v233, 10, v227\n"
"v_lshl_add_u32 v233, v228, 6, v233\n"
"v_lshl_add_u32 v233, v232, 4, v233\n"
"s_add_u32 m0, s63, 8960\n"
"s_nop 0\n"
"buffer_load_dwordx4 v223, s[16:19], s47 offen offset:256 lds\n"
"v_add_u32 v240, s40, v233\n"
"v_add_u32 v241, 0x10000, v240\n"
"v_lshlrev_b32 v233, 10, v226\n"
"v_lshl_add_u32 v233, v227, 6, v233\n"
"v_lshl_add_u32 v233, v228, 3, v233\n"
"v_add_u32 v233, s40, v233\n"
"v_and_b32 v232, 1, v226\n"
"v_lshl_add_u32 v242, v232, 5, v233\n"
"v_xor_b32 v243, 32, v242\n"
"v_add_u32 v244, 0x10000, v242\n"
"s_add_u32 m0, s63, 9984\n"
"s_nop 0\n"
"buffer_load_dwordx4 v222, s[16:19], s48 offen offset:256 lds\n"
"v_add_u32 v245, 0x10000, v243\n"
"v_lshlrev_b32 v233, 8, v227\n"
"v_lshl_add_u32 v233, v226, 6, v233\n"
"v_lshl_add_u32 v246, v228, 4, v233\n"
"v_xor_b32 v247, 32, v246\n"
"v_mul_u32_u24 v233, 16392, v232\n"
"v_lshrrev_b32 v232, 1, v226\n"
"v_lshl_add_u32 v233, v232, 11, v233\n"
"v_lshl_add_u32 v233, v227, 6, v233\n"
"v_lshl_add_u32 v233, v228, 4, v233\n"
"s_add_u32 m0, s63, 11008\n"
"s_nop 0\n"
"buffer_load_dwordx4 v223, s[16:19], s49 offen offset:256 lds\n"
"v_add_u32 v218, s40, v233\n"
"v_add_u32 v219, 65568, v218\n"
"v_lshrrev_b32 v232, 1, v229\n"
"v_xor_b32 v232, v232, v226\n"
"v_lshlrev_b32 v233, 7, v229\n"
"v_lshl_add_u32 v233, v232, 4, v233\n"
"v_add_u32 v220, s63, v233\n"
"v_xor_b32 v221, 64, v220\n"
"s_add_u32 s56, s40, 148480\n"
"v_lshl_add_u32 v224, v226, 4, s56\n"
"s_add_u32 m0, s63, 11904\n"
"s_nop 0\n"
"buffer_load_dwordx4 v222, s[16:19], s46 offen offset:384 lds\n"
"v_lshrrev_b32 v232, 3, v234\n"
"v_lshlrev_b32 v233, 8, v232\n"
"v_and_b32 v232, 1, v234\n"
"v_lshl_add_u32 v233, v232, 6, v233\n"
"v_bfe_u32 v232, v234, 1, 2\n"
"v_lshl_add_u32 v233, v232, 4, v233\n"
"v_add_u32 v216, s56, v233\n"
"v_lshlrev_b32 v225, 5, v234\n"
"v_mov_b32 v232, 0x40000000\n"
"v_cmp_gt_u32 vcc, 144, v234\n"
"s_nop 1\n"
"v_cndmask_b32 v225, v232, v225, vcc\n"
"s_lshl_b32 s56, s41, 8\n"
"s_add_u32 s57, s40, 147456\n"
"s_add_u32 s56, s56, s57\n"
"v_and_b32 v232, 1, v248\n"
"v_lshlrev_b32 v232, 5, v232\n"
"v_lshl_add_u32 v232, v226, 6, v232\n"
"v_add_u32 v238, s56, v232\n"
"v_lshlrev_b32 v232, 8, v226\n"
"s_add_u32 m0, s63, 12928\n"
"s_nop 0\n"
"buffer_load_dwordx4 v223, s[16:19], s47 offen offset:384 lds\n"
"v_lshl_add_u32 v232, v229, 2, v232\n"
"v_add_u32 v239, s57, v232\n"
"v_lshlrev_b32 v232, 6, v228\n"
"v_lshl_add_u32 v232, v226, 4, v232\n"
"v_add_u32 v254, s40, v232\n"
"s_lshr_b32 s57, s42, 3\n"
"s_lshl_b32 s57, s57, 2\n"
"s_add_u32 s57, s57, s41\n"
"s_sub_u32 s57, s57, 128\n"
"s_and_b32 s58, s42, 7\n"
"s_add_u32 m0, s63, 13952\n"
"s_nop 0\n"
"buffer_load_dwordx4 v222, s[16:19], s48 offen offset:384 lds\n"
"s_mul_i32 s59, s57, 9216\n"
"s_mul_i32 s60, s58, 1152\n"
"s_add_u32 s59, s59, s60\n"
"v_lshlrev_b32 v252, 6, v248\n"
"v_add_u32 v253, 0x40000000, v252\n"
"v_cmp_gt_u32 vcc, 18, v248\n"
"s_nop 1\n"
"v_cndmask_b32 v251, v253, v252, vcc\n"
"v_cmp_gt_u32 vcc, 36, v248\n"
"s_nop 1\n"
"v_cndmask_b32 v253, v253, v252, vcc\n"
"s_mul_i32 s60, s57, 1152\n"
"s_lshl_b32 s57, s60, 1\n"
"v_add_u32 v252, s60, v251\n"
"v_add_u32 v251, s59, v251\n"
"v_add_u32 v253, s57, v253\n"
"v_mov_b32 v237, 0xffff0000\n"
"v_mov_b32 v176, 0\n"
"v_mov_b32 v177, 0\n"
"v_mov_b32 v178, 0\n"
"s_add_u32 m0, s63, 14976\n"
"s_nop 0\n"
"buffer_load_dwordx4 v223, s[16:19], s49 offen offset:384 lds\n"
"v_mov_b32 v179, 0\n"
"v_mov_b32 v180, 0\n"
"v_mov_b32 v181, 0\n"
"v_mov_b32 v182, 0\n"
"v_mov_b32 v183, 0\n"
"v_mov_b32 v170, 0x3f803f80\n"
"v_mov_b32 v171, 0x3f803f80\n"
"v_mov_b32 v172, 0x3f803f80\n"
"v_mov_b32 v173, 0x3f803f80\n"
"s_waitcnt vmcnt(21)\n"
"v_lshlrev_b32 v164, 16, v164\n"
"v_mul_f32 v184, 0xbfb8aa3b, v164\n"
"v_lshlrev_b32 v165, 16, v165\n"
"v_mul_f32 v185, 0xbfb8aa3b, v165\n"
"v_lshlrev_b32 v166, 16, v166\n"
"v_mul_f32 v186, 0xbfb8aa3b, v166\n"
"v_lshlrev_b32 v167, 16, v167\n"
"v_mul_f32 v187, 0xbfb8aa3b, v167\n"
"v_lshlrev_b32 v168, 16, v168\n"
"v_mul_f32 v188, 0xbfb8aa3b, v168\n"
"v_exp_f32 v184, v184\n"
"s_add_u32 m0, s63, 15872\n"
"s_nop 0\n"
"buffer_load_dwordx4 v222, s[16:19], s46 offen offset:512 lds\n"
"v_exp_f32 v185, v185\n"
"v_exp_f32 v186, v186\n"
"v_exp_f32 v187, v187\n"
"v_exp_f32 v188, v188\n"
"v_add_f32 v184, 1.0, v184\n"
"v_add_f32 v185, 1.0, v185\n"
"v_add_f32 v186, 1.0, v186\n"
"v_add_f32 v187, 1.0, v187\n"
"v_add_f32 v188, 1.0, v188\n"
"v_rcp_f32 v184, v184\n"
"v_rcp_f32 v185, v185\n"
"v_rcp_f32 v186, v186\n"
"s_add_u32 m0, s63, 16896\n"
"s_nop 0\n"
"buffer_load_dwordx4 v223, s[16:19], s47 offen offset:512 lds\n"
"v_rcp_f32 v187, v187\n"
"v_rcp_f32 v188, v188\n"
"v_mul_f32 v164, v164, v184\n"
"v_mul_f32 v165, v165, v185\n"
"v_mul_f32 v166, v166, v186\n"
"v_mul_f32 v167, v167, v187\n"
"v_mul_f32 v168, v168, v188\n"
"v_cvt_pk_bf16_f32 v164, v164, v164\n"
"v_and_b32 v164, v237, v164\n"
"v_cvt_pk_bf16_f32 v165, v165, v165\n"
"v_and_b32 v165, v237, v165\n"
"v_cvt_pk_bf16_f32 v166, v166, v166\n"
"s_add_u32 m0, s63, 17920\n"
"s_nop 0\n"
"buffer_load_dwordx4 v222, s[16:19], s48 offen offset:512 lds\n"
"v_and_b32 v166, v237, v166\n"
"v_cvt_pk_bf16_f32 v167, v167, v167\n"
"v_and_b32 v167, v237, v167\n"
"v_cvt_pk_bf16_f32 v168, v168, v168\n"
"v_and_b32 v168, v237, v168\n"
"s_waitcnt vmcnt(23)\n"
"v_lshlrev_b32 v184, 16, v144\n"
"v_and_b32 v185, v237, v144\n"
"v_lshlrev_b32 v186, 16, v145\n"
"v_and_b32 v187, v237, v145\n"
"v_lshlrev_b32 v188, 16, v146\n"
"v_and_b32 v189, v237, v146\n"
"s_add_u32 m0, s63, 18944\n"
"s_nop 0\n"
"buffer_load_dwordx4 v223, s[16:19], s49 offen offset:512 lds\n"
"v_lshlrev_b32 v190, 16, v147\n"
"v_and_b32 v191, v237, v147\n"
"v_fmac_f32 v176, v164, v184\n"
"v_fmac_f32 v177, v164, v185\n"
"v_fmac_f32 v178, v164, v186\n"
"v_fmac_f32 v179, v164, v187\n"
"v_fmac_f32 v180, v164, v188\n"
"v_fmac_f32 v181, v164, v189\n"
"v_fmac_f32 v182, v164, v190\n"
"v_fmac_f32 v183, v164, v191\n"
"s_waitcnt vmcnt(23)\n"
"v_lshlrev_b32 v184, 16, v148\n"
"s_add_u32 m0, s63, 19840\n"
"s_nop 0\n"
"buffer_load_dwordx4 v222, s[16:19], s46 offen offset:640 lds\n"
"v_and_b32 v185, v237, v148\n"
"v_lshlrev_b32 v186, 16, v149\n"
"v_and_b32 v187, v237, v149\n"
"v_lshlrev_b32 v188, 16, v150\n"
"v_and_b32 v189, v237, v150\n"
"v_lshlrev_b32 v190, 16, v151\n"
"v_and_b32 v191, v237, v151\n"
"v_fmac_f32 v176, v165, v184\n"
"v_fmac_f32 v177, v165, v185\n"
"v_fmac_f32 v178, v165, v186\n"
"v_fmac_f32 v179, v165, v187\n"
"v_fmac_f32 v180, v165, v188\n"
"s_add_u32 m0, s63, 20864\n"
"s_nop 0\n"
"buffer_load_dwordx4 v223, s[16:19], s47 offen offset:640 lds\n"
"v_fmac_f32 v181, v165, v189\n"
"v_fmac_f32 v182, v165, v190\n"
"v_fmac_f32 v183, v165, v191\n"
"s_waitcnt vmcnt(24)\n"
"v_lshlrev_b32 v184, 16, v152\n"
"v_and_b32 v185, v237, v152\n"
"v_lshlrev_b32 v186, 16, v153\n"
"v_and_b32 v187, v237, v153\n"
"v_lshlrev_b32 v188, 16, v154\n"
"v_and_b32 v189, v237, v154\n"
"v_lshlrev_b32 v190, 16, v155\n"
"v_and_b32 v191, v237, v155\n"
"s_add_u32 m0, s63, 21888\n"
"s_nop 0\n"
"buffer_load_dwordx4 v222, s[16:19], s48 offen offset:640 lds\n"
"v_fmac_f32 v176, v166, v184\n"
"v_fmac_f32 v177, v166, v185\n"
"v_fmac_f32 v178, v166, v186\n"
"v_fmac_f32 v179, v166, v187\n"
"v_fmac_f32 v180, v166, v188\n"
"v_fmac_f32 v181, v166, v189\n"
"v_fmac_f32 v182, v166, v190\n"
"v_fmac_f32 v183, v166, v191\n"
"s_waitcnt vmcnt(24)\n"
"v_lshlrev_b32 v184, 16, v156\n"
"v_and_b32 v185, v237, v156\n"
"v_lshlrev_b32 v186, 16, v157\n"
"s_add_u32 m0, s63, 22912\n"
"s_nop 0\n"
"buffer_load_dwordx4 v223, s[16:19], s49 offen offset:640 lds\n"
"v_and_b32 v187, v237, v157\n"
"v_lshlrev_b32 v188, 16, v158\n"
"v_and_b32 v189, v237, v158\n"
"v_lshlrev_b32 v190, 16, v159\n"
"v_and_b32 v191, v237, v159\n"
"v_fmac_f32 v176, v167, v184\n"
"v_fmac_f32 v177, v167, v185\n"
"v_fmac_f32 v178, v167, v186\n"
"v_fmac_f32 v179, v167, v187\n"
"v_fmac_f32 v180, v167, v188\n"
"v_fmac_f32 v181, v167, v189\n"
"v_fmac_f32 v182, v167, v190\n"
"s_add_u32 m0, s63, 23808\n"
"s_nop 0\n"
"buffer_load_dwordx4 v222, s[16:19], s46 offen offset:768 lds\n"
"v_fmac_f32 v183, v167, v191\n"
"s_waitcnt vmcnt(25)\n"
"v_lshlrev_b32 v184, 16, v160\n"
"v_and_b32 v185, v237, v160\n"
"v_lshlrev_b32 v186, 16, v161\n"
"v_and_b32 v187, v237, v161\n"
"v_lshlrev_b32 v188, 16, v162\n"
"v_and_b32 v189, v237, v162\n"
"v_lshlrev_b32 v190, 16, v163\n"
"v_and_b32 v191, v237, v163\n"
"v_fmac_f32 v176, v168, v184\n"
"v_fmac_f32 v177, v168, v185\n"
"s_add_u32 m0, s63, 24832\n"
"s_nop 0\n"
"buffer_load_dwordx4 v223, s[16:19], s47 offen offset:768 lds\n"
"v_fmac_f32 v178, v168, v186\n"
"v_fmac_f32 v179, v168, v187\n"
"v_fmac_f32 v180, v168, v188\n"
"v_fmac_f32 v181, v168, v189\n"
"v_fmac_f32 v182, v168, v190\n"
"v_fmac_f32 v183, v168, v191\n"
"v_add_f32_dpp v176, v176, v176 row_ror:2 row_mask:0xf bank_mask:0xf\n"
"v_add_f32_dpp v177, v177, v177 row_ror:2 row_mask:0xf bank_mask:0xf\n"
"v_add_f32_dpp v178, v178, v178 row_ror:2 row_mask:0xf bank_mask:0xf\n"
"v_add_f32_dpp v179, v179, v179 row_ror:2 row_mask:0xf bank_mask:0xf\n"
"v_add_f32_dpp v180, v180, v180 row_ror:2 row_mask:0xf bank_mask:0xf\n"
"v_add_f32_dpp v181, v181, v181 row_ror:2 row_mask:0xf bank_mask:0xf\n"
"s_add_u32 m0, s63, 25856\n"
"s_nop 0\n"
"buffer_load_dwordx4 v222, s[16:19], s48 offen offset:768 lds\n"
"v_add_f32_dpp v182, v182, v182 row_ror:2 row_mask:0xf bank_mask:0xf\n"
"v_add_f32_dpp v183, v183, v183 row_ror:2 row_mask:0xf bank_mask:0xf\n"
"v_add_f32_dpp v176, v176, v176 row_ror:4 row_mask:0xf bank_mask:0xf\n"
"v_add_f32_dpp v177, v177, v177 row_ror:4 row_mask:0xf bank_mask:0xf\n"
"v_add_f32_dpp v178, v178, v178 row_ror:4 row_mask:0xf bank_mask:0xf\n"
"v_add_f32_dpp v179, v179, v179 row_ror:4 row_mask:0xf bank_mask:0xf\n"
"v_add_f32_dpp v180, v180, v180 row_ror:4 row_mask:0xf bank_mask:0xf\n"
"v_add_f32_dpp v181, v181, v181 row_ror:4 row_mask:0xf bank_mask:0xf\n"
"v_add_f32_dpp v182, v182, v182 row_ror:4 row_mask:0xf bank_mask:0xf\n"
"v_add_f32_dpp v183, v183, v183 row_ror:4 row_mask:0xf bank_mask:0xf\n"
"v_add_f32_dpp v176, v176, v176 row_ror:8 row_mask:0xf bank_mask:0xf\n"
"v_add_f32_dpp v177, v177, v177 row_ror:8 row_mask:0xf bank_mask:0xf\n"
"s_add_u32 m0, s63, 26880\n"
"s_nop 0\n"
"buffer_load_dwordx4 v223, s[16:19], s49 offen offset:768 lds\n"
"v_add_f32_dpp v178, v178, v178 row_ror:8 row_mask:0xf bank_mask:0xf\n"
"v_add_f32_dpp v179, v179, v179 row_ror:8 row_mask:0xf bank_mask:0xf\n"
"v_add_f32_dpp v180, v180, v180 row_ror:8 row_mask:0xf bank_mask:0xf\n"
"v_add_f32_dpp v181, v181, v181 row_ror:8 row_mask:0xf bank_mask:0xf\n"
"v_add_f32_dpp v182, v182, v182 row_ror:8 row_mask:0xf bank_mask:0xf\n"
"v_add_f32_dpp v183, v183, v183 row_ror:8 row_mask:0xf bank_mask:0xf\n"
"s_mov_b32 exec_lo, 0x00030003\n"
"s_mov_b32 exec_hi, 0x00030003\n"
"ds_write_b128 v238, v[176:179]\n"
"ds_write_b128 v238, v[180:183] offset:16\n"
"s_mov_b64 exec, -1\n"
"s_waitcnt lgkmcnt(0)\n"
"s_barrier\n"
"ds_read_b32 v184, v239 offset:0\n"
"ds_read_b32 v185, v239 offset:64\n"
"ds_read_b32 v186, v239 offset:128\n"
"ds_read_b32 v187, v239 offset:192\n"
"s_add_u32 s85, s85, 1\n"
"s_lshl_b32 s56, s55, 7\n"
"s_add_u32 s56, s56, 32768\n"
"s_lshl_b32 s58, s41, 2\n"
"s_lshl_b32 s58, 15, s58\n"
"v_lshlrev_b32 v190, 3, v248\n"
"v_mov_b32 v189, s85\n"
"s_waitcnt lgkmcnt(2)\n"
"v_add_f32 v184, v184, v185\n"
"s_waitcnt lgkmcnt(0)\n"
"v_add_f32 v186, v186, v187\n"
"v_add_f32 v184, v184, v186\n"
"v_mov_b32 v185, v184\n"
"s_nop 1\n"
"v_permlane32_swap_b32 v184, v185\n"
"v_add_f32 v184, v184, v185\n"
"v_mov_b32 v185, v184\n"
"s_nop 1\n"
"v_permlane16_swap_b32 v184, v185\n"
"v_add_f32 v184, v184, v185\n"
"v_cvt_pk_bf16_f32 v188, v184, v184\n"
"v_and_b32 v188, v237, v188\n"
"s_mov_b32 exec_lo, s58\n"
"s_mov_b32 exec_hi, 0\n"
"buffer_store_dwordx2 v[188:189], v190, s[28:31], s56 offen sc1\n"
"s_mov_b64 exec, -1\n"
"s_waitcnt vmcnt(25)\n"
"ds_read_b128 v[0:3], v220 offset:0\n"
"ds_read_b128 v[4:7], v220 offset:2048\n"
"ds_read_b128 v[8:11], v221 offset:0\n"
"ds_read_b128 v[12:15], v221 offset:2048\n"
"s_waitcnt vmcnt(21)\n"
"ds_read_b128 v[16:19], v220 offset:4096\n"
"ds_read_b128 v[20:23], v220 offset:6144\n"
"ds_read_b128 v[24:27], v221 offset:4096\n"
"ds_read_b128 v[28:31], v221 offset:6144\n"
"s_waitcnt lgkmcnt(7)\n"
"v_mfma_f32_16x16x32_bf16 v[192:195], v[170:173], v[0:3], 0\n"
"v_mfma_f32_16x16x32_bf16 v[200:203], v[0:3], v[0:3], 0\n"
"s_waitcnt lgkmcnt(6)\n"
"v_mfma_f32_16x16x32_bf16 v[196:199], v[170:173], v[4:7], 0\n"
"v_mfma_f32_16x16x32_bf16 v[204:207], v[4:7], v[4:7], 0\n"
"s_waitcnt lgkmcnt(5)\n"
"v_mfma_f32_16x16x32_bf16 v[192:195], v[170:173], v[8:11], v[192:195]\n"
"v_mfma_f32_16x16x32_bf16 v[200:203], v[8:11], v[8:11], v[200:203]\n"
"s_waitcnt lgkmcnt(4)\n"
"v_mfma_f32_16x16x32_bf16 v[196:199], v[170:173], v[12:15], v[196:199]\n"
"v_mfma_f32_16x16x32_bf16 v[204:207], v[12:15], v[12:15], v[204:207]\n"
"s_waitcnt lgkmcnt(3)\n"
"s_waitcnt vmcnt(17)\n"
"ds_read_b128 v[32:35], v220 offset:8192\n"
"ds_read_b128 v[36:39], v220 offset:10240\n"
"ds_read_b128 v[40:43], v221 offset:8192\n"
"ds_read_b128 v[44:47], v221 offset:10240\n"
"v_mfma_f32_16x16x32_bf16 v[192:195], v[170:173], v[16:19], v[192:195]\n"
"v_mfma_f32_16x16x32_bf16 v[200:203], v[16:19], v[16:19], v[200:203]\n"
"s_waitcnt lgkmcnt(6)\n"
"v_mfma_f32_16x16x32_bf16 v[196:199], v[170:173], v[20:23], v[196:199]\n"
"v_mfma_f32_16x16x32_bf16 v[204:207], v[20:23], v[20:23], v[204:207]\n"
"s_waitcnt lgkmcnt(4)\n"
"v_mfma_f32_16x16x32_bf16 v[192:195], v[170:173], v[24:27], v[192:195]\n"
"v_mfma_f32_16x16x32_bf16 v[200:203], v[24:27], v[24:27], v[200:203]\n"
"v_mfma_f32_16x16x32_bf16 v[196:199], v[170:173], v[28:31], v[196:199]\n"
"v_mfma_f32_16x16x32_bf16 v[204:207], v[28:31], v[28:31], v[204:207]\n"
"s_waitcnt lgkmcnt(3)\n"
"s_waitcnt vmcnt(13)\n"
"ds_read_b128 v[48:51], v220 offset:12288\n"
"ds_read_b128 v[52:55], v220 offset:14336\n"
"ds_read_b128 v[56:59], v221 offset:12288\n"
"ds_read_b128 v[60:63], v221 offset:14336\n"
"v_mfma_f32_16x16x32_bf16 v[192:195], v[170:173], v[32:35], v[192:195]\n"
"v_mfma_f32_16x16x32_bf16 v[200:203], v[32:35], v[32:35], v[200:203]\n"
"s_waitcnt lgkmcnt(6)\n"
"v_mfma_f32_16x16x32_bf16 v[196:199], v[170:173], v[36:39], v[196:199]\n"
"v_mfma_f32_16x16x32_bf16 v[204:207], v[36:39], v[36:39], v[204:207]\n"
"s_waitcnt lgkmcnt(4)\n"
"v_mfma_f32_16x16x32_bf16 v[192:195], v[170:173], v[40:43], v[192:195]\n"
"v_mfma_f32_16x16x32_bf16 v[200:203], v[40:43], v[40:43], v[200:203]\n"
"v_mfma_f32_16x16x32_bf16 v[196:199], v[170:173], v[44:47], v[196:199]\n"
"v_mfma_f32_16x16x32_bf16 v[204:207], v[44:47], v[44:47], v[204:207]\n"
"s_waitcnt lgkmcnt(3)\n"
"s_add_u32 m0, s63, 27776\n"
"s_nop 0\n"
"buffer_load_dwordx4 v222, s[16:19], s46 offen offset:896 lds\n"
"s_add_u32 m0, s63, 28800\n"
"s_nop 0\n"
"buffer_load_dwordx4 v223, s[16:19], s47 offen offset:896 lds\n"
"s_add_u32 m0, s63, 29824\n"
"s_nop 0\n"
"buffer_load_dwordx4 v222, s[16:19], s48 offen offset:896 lds\n"
"s_add_u32 m0, s63, 30848\n"
"s_nop 0\n"
"buffer_load_dwordx4 v223, s[16:19], s49 offen offset:896 lds\n"
"s_waitcnt vmcnt(13)\n"
"ds_read_b128 v[64:67], v220 offset:16384\n"
"ds_read_b128 v[68:71], v220 offset:18432\n"
"ds_read_b128 v[72:75], v221 offset:16384\n"
"ds_read_b128 v[76:79], v221 offset:18432\n"
"v_mfma_f32_16x16x32_bf16 v[192:195], v[170:173], v[48:51], v[192:195]\n"
"v_mfma_f32_16x16x32_bf16 v[200:203], v[48:51], v[48:51], v[200:203]\n"
"s_waitcnt lgkmcnt(6)\n"
"v_mfma_f32_16x16x32_bf16 v[196:199], v[170:173], v[52:55], v[196:199]\n"
"v_mfma_f32_16x16x32_bf16 v[204:207], v[52:55], v[52:55], v[204:207]\n"
"s_waitcnt lgkmcnt(4)\n"
"v_mfma_f32_16x16x32_bf16 v[192:195], v[170:173], v[56:59], v[192:195]\n"
"v_mfma_f32_16x16x32_bf16 v[200:203], v[56:59], v[56:59], v[200:203]\n"
"v_mfma_f32_16x16x32_bf16 v[196:199], v[170:173], v[60:63], v[196:199]\n"
"v_mfma_f32_16x16x32_bf16 v[204:207], v[60:63], v[60:63], v[204:207]\n"
"s_waitcnt lgkmcnt(3)\n"
"s_add_u32 m0, s63, 31744\n"
"s_nop 0\n"
"buffer_load_dwordx4 v222, s[16:19], s46 offen offset:1024 lds\n"
"s_add_u32 m0, s63, 32768\n"
"s_nop 0\n"
"buffer_load_dwordx4 v223, s[16:19], s47 offen offset:1024 lds\n"
"s_add_u32 m0, s63, 33792\n"
"s_nop 0\n"
"buffer_load_dwordx4 v222, s[16:19], s48 offen offset:1024 lds\n"
"s_add_u32 m0, s63, 34816\n"
"s_nop 0\n"
"buffer_load_dwordx4 v223, s[16:19], s49 offen offset:1024 lds\n"
"s_waitcnt vmcnt(13)\n"
"ds_read_b128 v[80:83], v220 offset:20480\n"
"ds_read_b128 v[84:87], v220 offset:22528\n"
"ds_read_b128 v[88:91], v221 offset:20480\n"
"ds_read_b128 v[92:95], v221 offset:22528\n"
"v_mfma_f32_16x16x32_bf16 v[192:195], v[170:173], v[64:67], v[192:195]\n"
"v_mfma_f32_16x16x32_bf16 v[200:203], v[64:67], v[64:67], v[200:203]\n"
"s_waitcnt lgkmcnt(6)\n"
"v_mfma_f32_16x16x32_bf16 v[196:199], v[170:173], v[68:71], v[196:199]\n"
"v_mfma_f32_16x16x32_bf16 v[204:207], v[68:71], v[68:71], v[204:207]\n"
"s_waitcnt lgkmcnt(4)\n"
"v_mfma_f32_16x16x32_bf16 v[192:195], v[170:173], v[72:75], v[192:195]\n"
"v_mfma_f32_16x16x32_bf16 v[200:203], v[72:75], v[72:75], v[200:203]\n"
"v_mfma_f32_16x16x32_bf16 v[196:199], v[170:173], v[76:79], v[196:199]\n"
"v_mfma_f32_16x16x32_bf16 v[204:207], v[76:79], v[76:79], v[204:207]\n"
"s_waitcnt lgkmcnt(3)\n"
"s_waitcnt vmcnt(9)\n"
"ds_read_b128 v[96:99], v220 offset:24576\n"
"ds_read_b128 v[100:103], v220 offset:26624\n"
"ds_read_b128 v[104:107], v221 offset:24576\n"
"ds_read_b128 v[108:111], v221 offset:26624\n"
"v_mfma_f32_16x16x32_bf16 v[192:195], v[170:173], v[80:83], v[192:195]\n"
"v_mfma_f32_16x16x32_bf16 v[200:203], v[80:83], v[80:83], v[200:203]\n"
"s_waitcnt lgkmcnt(6)\n"
"v_mfma_f32_16x16x32_bf16 v[196:199], v[170:173], v[84:87], v[196:199]\n"
"v_mfma_f32_16x16x32_bf16 v[204:207], v[84:87], v[84:87], v[204:207]\n"
"s_waitcnt lgkmcnt(4)\n"
"v_mfma_f32_16x16x32_bf16 v[192:195], v[170:173], v[88:91], v[192:195]\n"
"v_mfma_f32_16x16x32_bf16 v[200:203], v[88:91], v[88:91], v[200:203]\n"
"v_mfma_f32_16x16x32_bf16 v[196:199], v[170:173], v[92:95], v[196:199]\n"
"v_mfma_f32_16x16x32_bf16 v[204:207], v[92:95], v[92:95], v[204:207]\n"
"s_waitcnt lgkmcnt(3)\n"
"s_waitcnt vmcnt(4)\n"
"ds_read_b128 v[112:115], v220 offset:28672\n"
"ds_read_b128 v[116:119], v220 offset:30720\n"
"ds_read_b128 v[120:123], v221 offset:28672\n"
"ds_read_b128 v[124:127], v221 offset:30720\n"
"v_mfma_f32_16x16x32_bf16 v[192:195], v[170:173], v[96:99], v[192:195]\n"
"v_mfma_f32_16x16x32_bf16 v[200:203], v[96:99], v[96:99], v[200:203]\n"
"s_waitcnt lgkmcnt(6)\n"
"v_mfma_f32_16x16x32_bf16 v[196:199], v[170:173], v[100:103], v[196:199]\n"
"v_mfma_f32_16x16x32_bf16 v[204:207], v[100:103], v[100:103], v[204:207]\n"
"s_waitcnt lgkmcnt(4)\n"
"v_mfma_f32_16x16x32_bf16 v[192:195], v[170:173], v[104:107], v[192:195]\n"
"v_mfma_f32_16x16x32_bf16 v[200:203], v[104:107], v[104:107], v[200:203]\n"
"v_mfma_f32_16x16x32_bf16 v[196:199], v[170:173], v[108:111], v[196:199]\n"
"v_mfma_f32_16x16x32_bf16 v[204:207], v[108:111], v[108:111], v[204:207]\n"
"s_waitcnt lgkmcnt(3)\n"
"s_waitcnt vmcnt(0)\n"
"ds_read_b128 v[128:131], v220 offset:32768\n"
"ds_read_b128 v[132:135], v220 offset:34816\n"
"ds_read_b128 v[136:139], v221 offset:32768\n"
"ds_read_b128 v[140:143], v221 offset:34816\n"
"v_mfma_f32_16x16x32_bf16 v[192:195], v[170:173], v[112:115], v[192:195]\n"
"v_mfma_f32_16x16x32_bf16 v[200:203], v[112:115], v[112:115], v[200:203]\n"
"s_waitcnt lgkmcnt(6)\n"
"v_mfma_f32_16x16x32_bf16 v[196:199], v[170:173], v[116:119], v[196:199]\n"
"v_mfma_f32_16x16x32_bf16 v[204:207], v[116:119], v[116:119], v[204:207]\n"
"s_waitcnt lgkmcnt(4)\n"
"v_mfma_f32_16x16x32_bf16 v[192:195], v[170:173], v[120:123], v[192:195]\n"
"v_mfma_f32_16x16x32_bf16 v[200:203], v[120:123], v[120:123], v[200:203]\n"
"v_mfma_f32_16x16x32_bf16 v[196:199], v[170:173], v[124:127], v[196:199]\n"
"v_mfma_f32_16x16x32_bf16 v[204:207], v[124:127], v[124:127], v[204:207]\n"
"s_waitcnt lgkmcnt(0)\n"
"s_barrier\n"
"v_mfma_f32_16x16x32_bf16 v[192:195], v[170:173], v[128:131], v[192:195]\n"
"v_mfma_f32_16x16x32_bf16 v[200:203], v[128:131], v[128:131], v[200:203]\n"
"v_mfma_f32_16x16x32_bf16 v[196:199], v[170:173], v[132:135], v[196:199]\n"
"v_mfma_f32_16x16x32_bf16 v[204:207], v[132:135], v[132:135], v[204:207]\n"
"v_mfma_f32_16x16x32_bf16 v[192:195], v[170:173], v[136:139], v[192:195]\n"
"v_mfma_f32_16x16x32_bf16 v[200:203], v[136:139], v[136:139], v[200:203]\n"
"v_mfma_f32_16x16x32_bf16 v[196:199], v[170:173], v[140:143], v[196:199]\n"
"v_mfma_f32_16x16x32_bf16 v[204:207], v[140:143], v[140:143], v[204:207]\n"
"s_add_u32 m0, s51, 98352\n"
"s_nop 0\n"
"buffer_load_dwordx4 v217, s[20:23], s50 offen offset:0 lds\n"
"s_add_u32 m0, s51, 106288\n"
"s_nop 0\n"
"buffer_load_dwordx4 v217, s[20:23], s50 offen offset:256 lds\n"
"s_add_u32 s50, s50, 32768\n"
"v_cmp_eq_u32 s[56:57], 1, v228\n"
"v_cmp_eq_u32 s[58:59], 2, v228\n"
"v_cmp_eq_u32 vcc, 3, v228\n"
"s_nop 4\n"
"v_cndmask_b32 v200, v200, v201, s[56:57]\n"
"v_cndmask_b32 v200, v200, v202, s[58:59]\n"
"v_cndmask_b32 v200, v200, v203, vcc\n"
"v_cndmask_b32 v204, v204, v205, s[56:57]\n"
"v_cndmask_b32 v204, v204, v206, s[58:59]\n"
"v_cndmask_b32 v204, v204, v207, vcc\n"
"v_cmp_eq_u32 vcc, v226, v227\n"
"v_mov_b32 v208, v192\n"
"v_mov_b32 v212, v196\n"
"v_cndmask_b32 v210, 0, v200, vcc\n"
"v_cndmask_b32 v214, 0, v204, vcc\n"
"v_mov_b32 v192, v210\n"
"v_mov_b32 v193, v214\n"
"s_nop 0\n"
"v_permlane32_swap_b32 v210, v192\n"
"v_permlane32_swap_b32 v214, v193\n"
"v_add_f32 v210, v210, v192\n"
"v_add_f32 v214, v214, v193\n"
"v_mov_b32 v192, v210\n"
"v_mov_b32 v193, v214\n"
"s_nop 0\n"
"v_permlane16_swap_b32 v210, v192\n"
"v_permlane16_swap_b32 v214, v193\n"
"v_add_f32 v210, v210, v192\n"
"v_add_f32 v214, v214, v193\n"
"v_mul_f32 v208, 0x3ae38e39, v208\n"
"v_mul_f32 v210, 0x3ae38e39, v210\n"
"v_fma_f32 v210, -v208, v208, v210\n"
"v_max_f32 v210, 0, v210\n"
"v_add_f32 v210, 0x3727c5ac, v210\n"
"v_mul_f32 v212, 0x3ae38e39, v212\n"
"v_mul_f32 v214, 0x3ae38e39, v214\n"
"v_fma_f32 v214, -v212, v212, v214\n"
"v_max_f32 v214, 0, v214\n"
"v_add_f32 v214, 0x3727c5ac, v214\n"
"v_rsq_f32 v209, v210\n"
"v_rsq_f32 v213, v214\n"
"s_nop 0\n"
"v_mul_f32 v210, v208, v209\n"
"v_sub_f32 v210, 0, v210\n"
"v_mov_b32 v208, v209\n"
"v_mul_f32 v214, v212, v213\n"
"v_sub_f32 v214, 0, v214\n"
"v_mov_b32 v212, v213\n"
"v_lshlrev_b32 v176, 16, v0\n"
"v_and_b32 v177, v237, v0\n"
"v_lshlrev_b32 v178, 16, v1\n"
"v_and_b32 v179, v237, v1\n"
"v_lshlrev_b32 v180, 16, v2\n"
"v_and_b32 v181, v237, v2\n"
"v_lshlrev_b32 v182, 16, v3\n"
"v_and_b32 v183, v237, v3\n"
"v_pk_fma_f32 v[176:177], v[176:177], v[208:209], v[210:211] op_sel_hi:[1,0,0]\n"
"v_pk_fma_f32 v[178:179], v[178:179], v[208:209], v[210:211] op_sel_hi:[1,0,0]\n"
"v_pk_fma_f32 v[180:181], v[180:181], v[208:209], v[210:211] op_sel_hi:[1,0,0]\n"
"v_pk_fma_f32 v[182:183], v[182:183], v[208:209], v[210:211] op_sel_hi:[1,0,0]\n"
"v_cvt_pk_f16_f32 v0, v176, v177\n"
"v_cvt_pk_f16_f32 v1, v178, v179\n"
"v_cvt_pk_f16_f32 v2, v180, v181\n"
"v_cvt_pk_f16_f32 v3, v182, v183\n"
"v_lshlrev_b32 v184, 16, v4\n"
"v_and_b32 v185, v237, v4\n"
"v_lshlrev_b32 v186, 16, v5\n"
"v_and_b32 v187, v237, v5\n"
"v_lshlrev_b32 v188, 16, v6\n"
"s_add_u32 m0, s51, 99376\n"
"s_nop 0\n"
"buffer_load_dwordx4 v217, s[20:23], s50 offen offset:0 lds\n"
"v_and_b32 v189, v237, v6\n"
"v_lshlrev_b32 v190, 16, v7\n"
"v_and_b32 v191, v237, v7\n"
"v_pk_fma_f32 v[184:185], v[184:185], v[212:213], v[214:215] op_sel_hi:[1,0,0]\n"
"v_pk_fma_f32 v[186:187], v[186:187], v[212:213], v[214:215] op_sel_hi:[1,0,0]\n"
"v_pk_fma_f32 v[188:189], v[188:189], v[212:213], v[214:215] op_sel_hi:[1,0,0]\n"
"v_pk_fma_f32 v[190:191], v[190:191], v[212:213], v[214:215] op_sel_hi:[1,0,0]\n"
"v_cvt_pk_f16_f32 v4, v184, v185\n"
"v_cvt_pk_f16_f32 v5, v186, v187\n"
"v_cvt_pk_f16_f32 v6, v188, v189\n"
"v_cvt_pk_f16_f32 v7, v190, v191\n"
"v_lshlrev_b32 v176, 16, v8\n"
"v_and_b32 v177, v237, v8\n"
"v_lshlrev_b32 v178, 16, v9\n"
"v_and_b32 v179, v237, v9\n"
"v_lshlrev_b32 v180, 16, v10\n"
"v_and_b32 v181, v237, v10\n"
"v_lshlrev_b32 v182, 16, v11\n"
"v_and_b32 v183, v237, v11\n"
"v_pk_fma_f32 v[176:177], v[176:177], v[208:209], v[210:211] op_sel_hi:[1,0,0]\n"
"v_pk_fma_f32 v[178:179], v[178:179], v[208:209], v[210:211] op_sel_hi:[1,0,0]\n"
"s_add_u32 m0, s51, 107312\n"
"s_nop 0\n"
"buffer_load_dwordx4 v217, s[20:23], s50 offen offset:256 lds\n"
"s_add_u32 s50, s50, 32768\n"
"v_pk_fma_f32 v[180:181], v[180:181], v[208:209], v[210:211] op_sel_hi:[1,0,0]\n"
"v_pk_fma_f32 v[182:183], v[182:183], v[208:209], v[210:211] op_sel_hi:[1,0,0]\n"
"v_cvt_pk_f16_f32 v8, v176, v177\n"
"v_cvt_pk_f16_f32 v9, v178, v179\n"
"v_cvt_pk_f16_f32 v10, v180, v181\n"
"v_cvt_pk_f16_f32 v11, v182, v183\n"
"v_lshlrev_b32 v184, 16, v12\n"
"v_and_b32 v185, v237, v12\n"
"v_lshlrev_b32 v186, 16, v13\n"
"v_and_b32 v187, v237, v13\n"
"v_lshlrev_b32 v188, 16, v14\n"
"v_and_b32 v189, v237, v14\n"
"v_lshlrev_b32 v190, 16, v15\n"
"v_and_b32 v191, v237, v15\n"
"v_pk_fma_f32 v[184:185], v[184:185], v[212:213], v[214:215] op_sel_hi:[1,0,0]\n"
"v_pk_fma_f32 v[186:187], v[186:187], v[212:213], v[214:215] op_sel_hi:[1,0,0]\n"
"v_pk_fma_f32 v[188:189], v[188:189], v[212:213], v[214:215] op_sel_hi:[1,0,0]\n"
"v_pk_fma_f32 v[190:191], v[190:191], v[212:213], v[214:215] op_sel_hi:[1,0,0]\n"
"v_cvt_pk_f16_f32 v12, v184, v185\n"
"v_cvt_pk_f16_f32 v13, v186, v187\n"
"v_cvt_pk_f16_f32 v14, v188, v189\n"
"s_add_u32 m0, s51, 114744\n"
"s_nop 0\n"
"buffer_load_dwordx4 v217, s[20:23], s50 offen offset:0 lds\n"
"v_cvt_pk_f16_f32 v15, v190, v191\n"
"v_lshlrev_b32 v176, 16, v16\n"
"v_and_b32 v177, v237, v16\n"
"v_lshlrev_b32 v178, 16, v17\n"
"v_and_b32 v179, v237, v17\n"
"v_lshlrev_b32 v180, 16, v18\n"
"v_and_b32 v181, v237, v18\n"
"v_lshlrev_b32 v182, 16, v19\n"
"v_and_b32 v183, v237, v19\n"
"v_pk_fma_f32 v[176:177], v[176:177], v[208:209], v[210:211] op_sel_hi:[1,0,0]\n"
"v_pk_fma_f32 v[178:179], v[178:179], v[208:209], v[210:211] op_sel_hi:[1,0,0]\n"
"v_pk_fma_f32 v[180:181], v[180:181], v[208:209], v[210:211] op_sel_hi:[1,0,0]\n"
"v_pk_fma_f32 v[182:183], v[182:183], v[208:209], v[210:211] op_sel_hi:[1,0,0]\n"
"v_cvt_pk_f16_f32 v16, v176, v177\n"
"v_cvt_pk_f16_f32 v17, v178, v179\n"
"v_cvt_pk_f16_f32 v18, v180, v181\n"
"v_cvt_pk_f16_f32 v19, v182, v183\n"
"v_lshlrev_b32 v184, 16, v20\n"
"v_and_b32 v185, v237, v20\n"
"v_lshlrev_b32 v186, 16, v21\n"
"v_and_b32 v187, v237, v21\n"
"v_lshlrev_b32 v188, 16, v22\n"
"s_add_u32 m0, s51, 122680\n"
"s_nop 0\n"
"buffer_load_dwordx4 v217, s[20:23], s50 offen offset:256 lds\n"
"s_add_u32 s50, s50, 32768\n"
"v_and_b32 v189, v237, v22\n"
"v_lshlrev_b32 v190, 16, v23\n"
"v_and_b32 v191, v237, v23\n"
"v_pk_fma_f32 v[184:185], v[184:185], v[212:213], v[214:215] op_sel_hi:[1,0,0]\n"
"v_pk_fma_f32 v[186:187], v[186:187], v[212:213], v[214:215] op_sel_hi:[1,0,0]\n"
"v_pk_fma_f32 v[188:189], v[188:189], v[212:213], v[214:215] op_sel_hi:[1,0,0]\n"
"v_pk_fma_f32 v[190:191], v[190:191], v[212:213], v[214:215] op_sel_hi:[1,0,0]\n"
"v_cvt_pk_f16_f32 v20, v184, v185\n"
"v_cvt_pk_f16_f32 v21, v186, v187\n"
"v_cvt_pk_f16_f32 v22, v188, v189\n"
"v_cvt_pk_f16_f32 v23, v190, v191\n"
"v_lshlrev_b32 v176, 16, v24\n"
"v_and_b32 v177, v237, v24\n"
"v_lshlrev_b32 v178, 16, v25\n"
"v_and_b32 v179, v237, v25\n"
"v_lshlrev_b32 v180, 16, v26\n"
"v_and_b32 v181, v237, v26\n"
"v_lshlrev_b32 v182, 16, v27\n"
"v_and_b32 v183, v237, v27\n"
"v_pk_fma_f32 v[176:177], v[176:177], v[208:209], v[210:211] op_sel_hi:[1,0,0]\n"
"v_pk_fma_f32 v[178:179], v[178:179], v[208:209], v[210:211] op_sel_hi:[1,0,0]\n"
"s_add_u32 m0, s51, 115768\n"
"s_nop 0\n"
"buffer_load_dwordx4 v217, s[20:23], s50 offen offset:0 lds\n"
"v_pk_fma_f32 v[180:181], v[180:181], v[208:209], v[210:211] op_sel_hi:[1,0,0]\n"
"v_pk_fma_f32 v[182:183], v[182:183], v[208:209], v[210:211] op_sel_hi:[1,0,0]\n"
"v_cvt_pk_f16_f32 v24, v176, v177\n"
"v_cvt_pk_f16_f32 v25, v178, v179\n"
"v_cvt_pk_f16_f32 v26, v180, v181\n"
"v_cvt_pk_f16_f32 v27, v182, v183\n"
"v_lshlrev_b32 v184, 16, v28\n"
"v_and_b32 v185, v237, v28\n"
"v_lshlrev_b32 v186, 16, v29\n"
"v_and_b32 v187, v237, v29\n"
"v_lshlrev_b32 v188, 16, v30\n"
"v_and_b32 v189, v237, v30\n"
"v_lshlrev_b32 v190, 16, v31\n"
"v_and_b32 v191, v237, v31\n"
"v_pk_fma_f32 v[184:185], v[184:185], v[212:213], v[214:215] op_sel_hi:[1,0,0]\n"
"v_pk_fma_f32 v[186:187], v[186:187], v[212:213], v[214:215] op_sel_hi:[1,0,0]\n"
"v_pk_fma_f32 v[188:189], v[188:189], v[212:213], v[214:215] op_sel_hi:[1,0,0]\n"
"v_pk_fma_f32 v[190:191], v[190:191], v[212:213], v[214:215] op_sel_hi:[1,0,0]\n"
"v_cvt_pk_f16_f32 v28, v184, v185\n"
"v_cvt_pk_f16_f32 v29, v186, v187\n"
"v_cvt_pk_f16_f32 v30, v188, v189\n"
"s_add_u32 m0, s51, 123704\n"
"s_nop 0\n"
"buffer_load_dwordx4 v217, s[20:23], s50 offen offset:256 lds\n"
"s_add_u32 s50, s50, 425984\n"
"v_cvt_pk_f16_f32 v31, v190, v191\n"
"buffer_load_dwordx4 v[144:147], v225, s[28:31], s61 offen offset:0 sc1\n"
"buffer_load_dwordx4 v[148:151], v225, s[28:31], s61 offen offset:16 sc1\n"
"buffer_load_dwordx4 v[152:155], v225, s[28:31], s62 offen offset:0 sc1\n"
"buffer_load_dwordx4 v[156:159], v225, s[28:31], s62 offen offset:16 sc1\n"
"v_lshlrev_b32 v176, 16, v32\n"
"v_and_b32 v177, v237, v32\n"
"v_lshlrev_b32 v178, 16, v33\n"
"v_and_b32 v179, v237, v33\n"
"v_lshlrev_b32 v180, 16, v34\n"
"v_and_b32 v181, v237, v34\n"
"v_lshlrev_b32 v182, 16, v35\n"
"v_and_b32 v183, v237, v35\n"
"v_pk_fma_f32 v[176:177], v[176:177], v[208:209], v[210:211] op_sel_hi:[1,0,0]\n"
"v_pk_fma_f32 v[178:179], v[178:179], v[208:209], v[210:211] op_sel_hi:[1,0,0]\n"
"v_pk_fma_f32 v[180:181], v[180:181], v[208:209], v[210:211] op_sel_hi:[1,0,0]\n"
"v_pk_fma_f32 v[182:183], v[182:183], v[208:209], v[210:211] op_sel_hi:[1,0,0]\n"
"v_cvt_pk_f16_f32 v32, v176, v177\n"
"v_cvt_pk_f16_f32 v33, v178, v179\n"
"v_cvt_pk_f16_f32 v34, v180, v181\n"
"v_cvt_pk_f16_f32 v35, v182, v183\n"
"v_lshlrev_b32 v184, 16, v36\n"
"v_and_b32 v185, v237, v36\n"
"v_lshlrev_b32 v186, 16, v37\n"
"v_and_b32 v187, v237, v37\n"
"v_lshlrev_b32 v188, 16, v38\n"
"s_mov_b32 m0, s51\n"
"s_nop 0\n"
"buffer_load_dwordx4 v217, s[20:23], s50 offen offset:0 lds\n"
"v_and_b32 v189, v237, v38\n"
"v_lshlrev_b32 v190, 16, v39\n"
"v_and_b32 v191, v237, v39\n"
"v_pk_fma_f32 v[184:185], v[184:185], v[212:213], v[214:215] op_sel_hi:[1,0,0]\n"
"v_pk_fma_f32 v[186:187], v[186:187], v[212:213], v[214:215] op_sel_hi:[1,0,0]\n"
"v_pk_fma_f32 v[188:189], v[188:189], v[212:213], v[214:215] op_sel_hi:[1,0,0]\n"
"v_pk_fma_f32 v[190:191], v[190:191], v[212:213], v[214:215] op_sel_hi:[1,0,0]\n"
"v_cvt_pk_f16_f32 v36, v184, v185\n"
"v_cvt_pk_f16_f32 v37, v186, v187\n"
"v_cvt_pk_f16_f32 v38, v188, v189\n"
"v_cvt_pk_f16_f32 v39, v190, v191\n"
"v_lshlrev_b32 v176, 16, v40\n"
"v_and_b32 v177, v237, v40\n"
"v_lshlrev_b32 v178, 16, v41\n"
"v_and_b32 v179, v237, v41\n"
"v_lshlrev_b32 v180, 16, v42\n"
"v_and_b32 v181, v237, v42\n"
"v_lshlrev_b32 v182, 16, v43\n"
"v_and_b32 v183, v237, v43\n"
"v_pk_fma_f32 v[176:177], v[176:177], v[208:209], v[210:211] op_sel_hi:[1,0,0]\n"
"v_pk_fma_f32 v[178:179], v[178:179], v[208:209], v[210:211] op_sel_hi:[1,0,0]\n"
"s_add_u32 m0, s51, 7936\n"
"s_nop 0\n"
"buffer_load_dwordx4 v217, s[20:23], s50 offen offset:256 lds\n"
"s_add_u32 s50, s50, 32768\n"
"v_pk_fma_f32 v[180:181], v[180:181], v[208:209], v[210:211] op_sel_hi:[1,0,0]\n"
"v_pk_fma_f32 v[182:183], v[182:183], v[208:209], v[210:211] op_sel_hi:[1,0,0]\n"
"v_cvt_pk_f16_f32 v40, v176, v177\n"
"v_cvt_pk_f16_f32 v41, v178, v179\n"
"v_cvt_pk_f16_f32 v42, v180, v181\n"
"v_cvt_pk_f16_f32 v43, v182, v183\n"
"v_lshlrev_b32 v184, 16, v44\n"
"v_and_b32 v185, v237, v44\n"
"v_lshlrev_b32 v186, 16, v45\n"
"v_and_b32 v187, v237, v45\n"
"v_lshlrev_b32 v188, 16, v46\n"
"v_and_b32 v189, v237, v46\n"
"v_lshlrev_b32 v190, 16, v47\n"
"v_and_b32 v191, v237, v47\n"
"v_pk_fma_f32 v[184:185], v[184:185], v[212:213], v[214:215] op_sel_hi:[1,0,0]\n"
"v_pk_fma_f32 v[186:187], v[186:187], v[212:213], v[214:215] op_sel_hi:[1,0,0]\n"
"v_pk_fma_f32 v[188:189], v[188:189], v[212:213], v[214:215] op_sel_hi:[1,0,0]\n"
"v_pk_fma_f32 v[190:191], v[190:191], v[212:213], v[214:215] op_sel_hi:[1,0,0]\n"
"v_cvt_pk_f16_f32 v44, v184, v185\n"
"v_cvt_pk_f16_f32 v45, v186, v187\n"
"v_cvt_pk_f16_f32 v46, v188, v189\n"
"s_add_u32 m0, s51, 1024\n"
"s_nop 0\n"
"buffer_load_dwordx4 v217, s[20:23], s50 offen offset:0 lds\n"
"v_cvt_pk_f16_f32 v47, v190, v191\n"
"v_lshlrev_b32 v176, 16, v48\n"
"v_and_b32 v177, v237, v48\n"
"v_lshlrev_b32 v178, 16, v49\n"
"v_and_b32 v179, v237, v49\n"
"v_lshlrev_b32 v180, 16, v50\n"
"v_and_b32 v181, v237, v50\n"
"v_lshlrev_b32 v182, 16, v51\n"
"v_and_b32 v183, v237, v51\n"
"v_pk_fma_f32 v[176:177], v[176:177], v[208:209], v[210:211] op_sel_hi:[1,0,0]\n"
"v_pk_fma_f32 v[178:179], v[178:179], v[208:209], v[210:211] op_sel_hi:[1,0,0]\n"
"v_pk_fma_f32 v[180:181], v[180:181], v[208:209], v[210:211] op_sel_hi:[1,0,0]\n"
"v_pk_fma_f32 v[182:183], v[182:183], v[208:209], v[210:211] op_sel_hi:[1,0,0]\n"
"v_cvt_pk_f16_f32 v48, v176, v177\n"
"v_cvt_pk_f16_f32 v49, v178, v179\n"
"v_cvt_pk_f16_f32 v50, v180, v181\n"
"v_cvt_pk_f16_f32 v51, v182, v183\n"
"v_lshlrev_b32 v184, 16, v52\n"
"v_and_b32 v185, v237, v52\n"
"v_lshlrev_b32 v186, 16, v53\n"
"v_and_b32 v187, v237, v53\n"
"v_lshlrev_b32 v188, 16, v54\n"
"s_add_u32 m0, s51, 8960\n"
"s_nop 0\n"
"buffer_load_dwordx4 v217, s[20:23], s50 offen offset:256 lds\n"
"s_add_u32 s50, s50, 32768\n"
"v_and_b32 v189, v237, v54\n"
"v_lshlrev_b32 v190, 16, v55\n"
"v_and_b32 v191, v237, v55\n"
"v_pk_fma_f32 v[184:185], v[184:185], v[212:213], v[214:215] op_sel_hi:[1,0,0]\n"
"v_pk_fma_f32 v[186:187], v[186:187], v[212:213], v[214:215] op_sel_hi:[1,0,0]\n"
"v_pk_fma_f32 v[188:189], v[188:189], v[212:213], v[214:215] op_sel_hi:[1,0,0]\n"
"v_pk_fma_f32 v[190:191], v[190:191], v[212:213], v[214:215] op_sel_hi:[1,0,0]\n"
"v_cvt_pk_f16_f32 v52, v184, v185\n"
"v_cvt_pk_f16_f32 v53, v186, v187\n"
"v_cvt_pk_f16_f32 v54, v188, v189\n"
"v_cvt_pk_f16_f32 v55, v190, v191\n"
"v_lshlrev_b32 v176, 16, v56\n"
"v_and_b32 v177, v237, v56\n"
"v_lshlrev_b32 v178, 16, v57\n"
"v_and_b32 v179, v237, v57\n"
"v_lshlrev_b32 v180, 16, v58\n"
"v_and_b32 v181, v237, v58\n"
"v_lshlrev_b32 v182, 16, v59\n"
"v_and_b32 v183, v237, v59\n"
"v_pk_fma_f32 v[176:177], v[176:177], v[208:209], v[210:211] op_sel_hi:[1,0,0]\n"
"v_pk_fma_f32 v[178:179], v[178:179], v[208:209], v[210:211] op_sel_hi:[1,0,0]\n"
"s_add_u32 m0, s51, 16392\n"
"s_nop 0\n"
"buffer_load_dwordx4 v217, s[20:23], s50 offen offset:0 lds\n"
"v_pk_fma_f32 v[180:181], v[180:181], v[208:209], v[210:211] op_sel_hi:[1,0,0]\n"
"v_pk_fma_f32 v[182:183], v[182:183], v[208:209], v[210:211] op_sel_hi:[1,0,0]\n"
"v_cvt_pk_f16_f32 v56, v176, v177\n"
"v_cvt_pk_f16_f32 v57, v178, v179\n"
"v_cvt_pk_f16_f32 v58, v180, v181\n"
"v_cvt_pk_f16_f32 v59, v182, v183\n"
"v_lshlrev_b32 v184, 16, v60\n"
"v_and_b32 v185, v237, v60\n"
"v_lshlrev_b32 v186, 16, v61\n"
"v_and_b32 v187, v237, v61\n"
"v_lshlrev_b32 v188, 16, v62\n"
"v_and_b32 v189, v237, v62\n"
"v_lshlrev_b32 v190, 16, v63\n"
"v_and_b32 v191, v237, v63\n"
"v_pk_fma_f32 v[184:185], v[184:185], v[212:213], v[214:215] op_sel_hi:[1,0,0]\n"
"v_pk_fma_f32 v[186:187], v[186:187], v[212:213], v[214:215] op_sel_hi:[1,0,0]\n"
"v_pk_fma_f32 v[188:189], v[188:189], v[212:213], v[214:215] op_sel_hi:[1,0,0]\n"
"v_pk_fma_f32 v[190:191], v[190:191], v[212:213], v[214:215] op_sel_hi:[1,0,0]\n"
"v_cvt_pk_f16_f32 v60, v184, v185\n"
"v_cvt_pk_f16_f32 v61, v186, v187\n"
"v_cvt_pk_f16_f32 v62, v188, v189\n"
"s_add_u32 m0, s51, 24328\n"
"s_nop 0\n"
"buffer_load_dwordx4 v217, s[20:23], s50 offen offset:256 lds\n"
"s_add_u32 s50, s50, 32768\n"
"v_cvt_pk_f16_f32 v63, v190, v191\n"
"v_lshlrev_b32 v176, 16, v64\n"
"v_and_b32 v177, v237, v64\n"
"v_lshlrev_b32 v178, 16, v65\n"
"v_and_b32 v179, v237, v65\n"
"v_lshlrev_b32 v180, 16, v66\n"
"v_and_b32 v181, v237, v66\n"
"v_lshlrev_b32 v182, 16, v67\n"
"v_and_b32 v183, v237, v67\n"
"v_pk_fma_f32 v[176:177], v[176:177], v[208:209], v[210:211] op_sel_hi:[1,0,0]\n"
"v_pk_fma_f32 v[178:179], v[178:179], v[208:209], v[210:211] op_sel_hi:[1,0,0]\n"
"v_pk_fma_f32 v[180:181], v[180:181], v[208:209], v[210:211] op_sel_hi:[1,0,0]\n"
"v_pk_fma_f32 v[182:183], v[182:183], v[208:209], v[210:211] op_sel_hi:[1,0,0]\n"
"v_cvt_pk_f16_f32 v64, v176, v177\n"
"v_cvt_pk_f16_f32 v65, v178, v179\n"
"v_cvt_pk_f16_f32 v66, v180, v181\n"
"v_cvt_pk_f16_f32 v67, v182, v183\n"
"v_lshlrev_b32 v184, 16, v68\n"
"v_and_b32 v185, v237, v68\n"
"v_lshlrev_b32 v186, 16, v69\n"
"v_and_b32 v187, v237, v69\n"
"v_lshlrev_b32 v188, 16, v70\n"
"s_add_u32 m0, s51, 17416\n"
"s_nop 0\n"
"buffer_load_dwordx4 v217, s[20:23], s50 offen offset:0 lds\n"
"v_and_b32 v189, v237, v70\n"
"v_lshlrev_b32 v190, 16, v71\n"
"v_and_b32 v191, v237, v71\n"
"v_pk_fma_f32 v[184:185], v[184:185], v[212:213], v[214:215] op_sel_hi:[1,0,0]\n"
"v_pk_fma_f32 v[186:187], v[186:187], v[212:213], v[214:215] op_sel_hi:[1,0,0]\n"
"v_pk_fma_f32 v[188:189], v[188:189], v[212:213], v[214:215] op_sel_hi:[1,0,0]\n"
"v_pk_fma_f32 v[190:191], v[190:191], v[212:213], v[214:215] op_sel_hi:[1,0,0]\n"
"v_cvt_pk_f16_f32 v68, v184, v185\n"
"v_cvt_pk_f16_f32 v69, v186, v187\n"
"v_cvt_pk_f16_f32 v70, v188, v189\n"
"v_cvt_pk_f16_f32 v71, v190, v191\n"
"v_lshlrev_b32 v176, 16, v72\n"
"v_and_b32 v177, v237, v72\n"
"v_lshlrev_b32 v178, 16, v73\n"
"v_and_b32 v179, v237, v73\n"
"v_lshlrev_b32 v180, 16, v74\n"
"v_and_b32 v181, v237, v74\n"
"v_lshlrev_b32 v182, 16, v75\n"
"v_and_b32 v183, v237, v75\n"
"v_pk_fma_f32 v[176:177], v[176:177], v[208:209], v[210:211] op_sel_hi:[1,0,0]\n"
"v_pk_fma_f32 v[178:179], v[178:179], v[208:209], v[210:211] op_sel_hi:[1,0,0]\n"
"s_add_u32 m0, s51, 25352\n"
"s_nop 0\n"
"buffer_load_dwordx4 v217, s[20:23], s50 offen offset:256 lds\n"
"s_add_u32 s50, s50, 425984\n"
"v_pk_fma_f32 v[180:181], v[180:181], v[208:209], v[210:211] op_sel_hi:[1,0,0]\n"
"v_pk_fma_f32 v[182:183], v[182:183], v[208:209], v[210:211] op_sel_hi:[1,0,0]\n"
"v_cvt_pk_f16_f32 v72, v176, v177\n"
"v_cvt_pk_f16_f32 v73, v178, v179\n"
"v_cvt_pk_f16_f32 v74, v180, v181\n"
"v_cvt_pk_f16_f32 v75, v182, v183\n"
"v_lshlrev_b32 v184, 16, v76\n"
"v_and_b32 v185, v237, v76\n"
"v_lshlrev_b32 v186, 16, v77\n"
"v_and_b32 v187, v237, v77\n"
"v_lshlrev_b32 v188, 16, v78\n"
"v_and_b32 v189, v237, v78\n"
"v_lshlrev_b32 v190, 16, v79\n"
"v_and_b32 v191, v237, v79\n"
"v_pk_fma_f32 v[184:185], v[184:185], v[212:213], v[214:215] op_sel_hi:[1,0,0]\n"
"v_pk_fma_f32 v[186:187], v[186:187], v[212:213], v[214:215] op_sel_hi:[1,0,0]\n"
"v_pk_fma_f32 v[188:189], v[188:189], v[212:213], v[214:215] op_sel_hi:[1,0,0]\n"
"v_pk_fma_f32 v[190:191], v[190:191], v[212:213], v[214:215] op_sel_hi:[1,0,0]\n"
"v_cvt_pk_f16_f32 v76, v184, v185\n"
"v_cvt_pk_f16_f32 v77, v186, v187\n"
"v_cvt_pk_f16_f32 v78, v188, v189\n"
"s_add_u32 m0, s51, 32784\n"
"s_nop 0\n"
"buffer_load_dwordx4 v217, s[20:23], s50 offen offset:0 lds\n"
"v_cvt_pk_f16_f32 v79, v190, v191\n"
"v_lshlrev_b32 v176, 16, v80\n"
"v_and_b32 v177, v237, v80\n"
"v_lshlrev_b32 v178, 16, v81\n"
"v_and_b32 v179, v237, v81\n"
"v_lshlrev_b32 v180, 16, v82\n"
"v_and_b32 v181, v237, v82\n"
"v_lshlrev_b32 v182, 16, v83\n"
"v_and_b32 v183, v237, v83\n"
"v_pk_fma_f32 v[176:177], v[176:177], v[208:209], v[210:211] op_sel_hi:[1,0,0]\n"
"v_pk_fma_f32 v[178:179], v[178:179], v[208:209], v[210:211] op_sel_hi:[1,0,0]\n"
"v_pk_fma_f32 v[180:181], v[180:181], v[208:209], v[210:211] op_sel_hi:[1,0,0]\n"
"v_pk_fma_f32 v[182:183], v[182:183], v[208:209], v[210:211] op_sel_hi:[1,0,0]\n"
"v_cvt_pk_f16_f32 v80, v176, v177\n"
"v_cvt_pk_f16_f32 v81, v178, v179\n"
"v_cvt_pk_f16_f32 v82, v180, v181\n"
"v_cvt_pk_f16_f32 v83, v182, v183\n"
"v_lshlrev_b32 v184, 16, v84\n"
"v_and_b32 v185, v237, v84\n"
"v_lshlrev_b32 v186, 16, v85\n"
"v_and_b32 v187, v237, v85\n"
"v_lshlrev_b32 v188, 16, v86\n"
"s_add_u32 m0, s51, 40720\n"
"s_nop 0\n"
"buffer_load_dwordx4 v217, s[20:23], s50 offen offset:256 lds\n"
"s_add_u32 s50, s50, 32768\n"
"v_and_b32 v189, v237, v86\n"
"v_lshlrev_b32 v190, 16, v87\n"
"v_and_b32 v191, v237, v87\n"
"v_pk_fma_f32 v[184:185], v[184:185], v[212:213], v[214:215] op_sel_hi:[1,0,0]\n"
"v_pk_fma_f32 v[186:187], v[186:187], v[212:213], v[214:215] op_sel_hi:[1,0,0]\n"
"v_pk_fma_f32 v[188:189], v[188:189], v[212:213], v[214:215] op_sel_hi:[1,0,0]\n"
"v_pk_fma_f32 v[190:191], v[190:191], v[212:213], v[214:215] op_sel_hi:[1,0,0]\n"
"v_cvt_pk_f16_f32 v84, v184, v185\n"
"v_cvt_pk_f16_f32 v85, v186, v187\n"
"v_cvt_pk_f16_f32 v86, v188, v189\n"
"v_cvt_pk_f16_f32 v87, v190, v191\n"
"v_lshlrev_b32 v176, 16, v88\n"
"v_and_b32 v177, v237, v88\n"
"v_lshlrev_b32 v178, 16, v89\n"
"v_and_b32 v179, v237, v89\n"
"v_lshlrev_b32 v180, 16, v90\n"
"v_and_b32 v181, v237, v90\n"
"v_lshlrev_b32 v182, 16, v91\n"
"v_and_b32 v183, v237, v91\n"
"v_pk_fma_f32 v[176:177], v[176:177], v[208:209], v[210:211] op_sel_hi:[1,0,0]\n"
"v_pk_fma_f32 v[178:179], v[178:179], v[208:209], v[210:211] op_sel_hi:[1,0,0]\n"
"s_add_u32 m0, s51, 33808\n"
"s_nop 0\n"
"buffer_load_dwordx4 v217, s[20:23], s50 offen offset:0 lds\n"
"v_pk_fma_f32 v[180:181], v[180:181], v[208:209], v[210:211] op_sel_hi:[1,0,0]\n"
"v_pk_fma_f32 v[182:183], v[182:183], v[208:209], v[210:211] op_sel_hi:[1,0,0]\n"
"v_cvt_pk_f16_f32 v88, v176, v177\n"
"v_cvt_pk_f16_f32 v89, v178, v179\n"
"v_cvt_pk_f16_f32 v90, v180, v181\n"
"v_cvt_pk_f16_f32 v91, v182, v183\n"
"v_lshlrev_b32 v184, 16, v92\n"
"v_and_b32 v185, v237, v92\n"
"v_lshlrev_b32 v186, 16, v93\n"
"v_and_b32 v187, v237, v93\n"
"v_lshlrev_b32 v188, 16, v94\n"
"v_and_b32 v189, v237, v94\n"
"v_lshlrev_b32 v190, 16, v95\n"
"v_and_b32 v191, v237, v95\n"
"v_pk_fma_f32 v[184:185], v[184:185], v[212:213], v[214:215] op_sel_hi:[1,0,0]\n"
"v_pk_fma_f32 v[186:187], v[186:187], v[212:213], v[214:215] op_sel_hi:[1,0,0]\n"
"v_pk_fma_f32 v[188:189], v[188:189], v[212:213], v[214:215] op_sel_hi:[1,0,0]\n"
"v_pk_fma_f32 v[190:191], v[190:191], v[212:213], v[214:215] op_sel_hi:[1,0,0]\n"
"v_cvt_pk_f16_f32 v92, v184, v185\n"
"v_cvt_pk_f16_f32 v93, v186, v187\n"
"v_cvt_pk_f16_f32 v94, v188, v189\n"
"s_add_u32 m0, s51, 41744\n"
"s_nop 0\n"
"buffer_load_dwordx4 v217, s[20:23], s50 offen offset:256 lds\n"
"s_add_u32 s50, s50, 32768\n"
"v_cvt_pk_f16_f32 v95, v190, v191\n"
"v_lshlrev_b32 v176, 16, v96\n"
"v_and_b32 v177, v237, v96\n"
"v_lshlrev_b32 v178, 16, v97\n"
"v_and_b32 v179, v237, v97\n"
"v_lshlrev_b32 v180, 16, v98\n"
"v_and_b32 v181, v237, v98\n"
"v_lshlrev_b32 v182, 16, v99\n"
"v_and_b32 v183, v237, v99\n"
"v_pk_fma_f32 v[176:177], v[176:177], v[208:209], v[210:211] op_sel_hi:[1,0,0]\n"
"v_pk_fma_f32 v[178:179], v[178:179], v[208:209], v[210:211] op_sel_hi:[1,0,0]\n"
"v_pk_fma_f32 v[180:181], v[180:181], v[208:209], v[210:211] op_sel_hi:[1,0,0]\n"
"v_pk_fma_f32 v[182:183], v[182:183], v[208:209], v[210:211] op_sel_hi:[1,0,0]\n"
"v_cvt_pk_f16_f32 v96, v176, v177\n"
"v_cvt_pk_f16_f32 v97, v178, v179\n"
"v_cvt_pk_f16_f32 v98, v180, v181\n"
"v_cvt_pk_f16_f32 v99, v182, v183\n"
"v_lshlrev_b32 v184, 16, v100\n"
"v_and_b32 v185, v237, v100\n"
"v_lshlrev_b32 v186, 16, v101\n"
"v_and_b32 v187, v237, v101\n"
"v_lshlrev_b32 v188, 16, v102\n"
"s_add_u32 m0, s51, 49176\n"
"s_nop 0\n"
"buffer_load_dwordx4 v217, s[20:23], s50 offen offset:0 lds\n"
"v_and_b32 v189, v237, v102\n"
"v_lshlrev_b32 v190, 16, v103\n"
"v_and_b32 v191, v237, v103\n"
"v_pk_fma_f32 v[184:185], v[184:185], v[212:213], v[214:215] op_sel_hi:[1,0,0]\n"
"v_pk_fma_f32 v[186:187], v[186:187], v[212:213], v[214:215] op_sel_hi:[1,0,0]\n"
"v_pk_fma_f32 v[188:189], v[188:189], v[212:213], v[214:215] op_sel_hi:[1,0,0]\n"
"v_pk_fma_f32 v[190:191], v[190:191], v[212:213], v[214:215] op_sel_hi:[1,0,0]\n"
"v_cvt_pk_f16_f32 v100, v184, v185\n"
"v_cvt_pk_f16_f32 v101, v186, v187\n"
"v_cvt_pk_f16_f32 v102, v188, v189\n"
"v_cvt_pk_f16_f32 v103, v190, v191\n"
"v_lshlrev_b32 v176, 16, v104\n"
"v_and_b32 v177, v237, v104\n"
"v_lshlrev_b32 v178, 16, v105\n"
"v_and_b32 v179, v237, v105\n"
"v_lshlrev_b32 v180, 16, v106\n"
"v_and_b32 v181, v237, v106\n"
"v_lshlrev_b32 v182, 16, v107\n"
"v_and_b32 v183, v237, v107\n"
"v_pk_fma_f32 v[176:177], v[176:177], v[208:209], v[210:211] op_sel_hi:[1,0,0]\n"
"v_pk_fma_f32 v[178:179], v[178:179], v[208:209], v[210:211] op_sel_hi:[1,0,0]\n"
"s_add_u32 m0, s51, 57112\n"
"s_nop 0\n"
"buffer_load_dwordx4 v217, s[20:23], s50 offen offset:256 lds\n"
"s_add_u32 s50, s50, 32768\n"
"v_pk_fma_f32 v[180:181], v[180:181], v[208:209], v[210:211] op_sel_hi:[1,0,0]\n"
"v_pk_fma_f32 v[182:183], v[182:183], v[208:209], v[210:211] op_sel_hi:[1,0,0]\n"
"v_cvt_pk_f16_f32 v104, v176, v177\n"
"v_cvt_pk_f16_f32 v105, v178, v179\n"
"v_cvt_pk_f16_f32 v106, v180, v181\n"
"v_cvt_pk_f16_f32 v107, v182, v183\n"
"v_lshlrev_b32 v184, 16, v108\n"
"v_and_b32 v185, v237, v108\n"
"v_lshlrev_b32 v186, 16, v109\n"
"v_and_b32 v187, v237, v109\n"
"v_lshlrev_b32 v188, 16, v110\n"
"v_and_b32 v189, v237, v110\n"
"v_lshlrev_b32 v190, 16, v111\n"
"v_and_b32 v191, v237, v111\n"
"v_pk_fma_f32 v[184:185], v[184:185], v[212:213], v[214:215] op_sel_hi:[1,0,0]\n"
"v_pk_fma_f32 v[186:187], v[186:187], v[212:213], v[214:215] op_sel_hi:[1,0,0]\n"
"v_pk_fma_f32 v[188:189], v[188:189], v[212:213], v[214:215] op_sel_hi:[1,0,0]\n"
"v_pk_fma_f32 v[190:191], v[190:191], v[212:213], v[214:215] op_sel_hi:[1,0,0]\n"
"v_cvt_pk_f16_f32 v108, v184, v185\n"
"v_cvt_pk_f16_f32 v109, v186, v187\n"
"v_cvt_pk_f16_f32 v110, v188, v189\n"
"s_add_u32 m0, s51, 50200\n"
"s_nop 0\n"
"buffer_load_dwordx4 v217, s[20:23], s50 offen offset:0 lds\n"
"v_cvt_pk_f16_f32 v111, v190, v191\n"
"s_mov_b32 s87, 0x80000\n"
"s_waitcnt vmcnt(15)\n"
"AT_gchk:\n"
"v_xor_b32 v192, s85, v145\n"
"v_xor_b32 v193, s85, v147\n"
"v_bitop3_b32 v192, v149, s85, v192 bitop3:0xbe\n"
"v_bitop3_b32 v193, v151, s85, v193 bitop3:0xbe\n"
"v_bitop3_b32 v192, v153, s85, v192 bitop3:0xbe\n"
"v_bitop3_b32 v193, v155, s85, v193 bitop3:0xbe\n"
"v_bitop3_b32 v192, v157, s85, v192 bitop3:0xbe\n"
"v_bitop3_b32 v193, v159, s85, v193 bitop3:0xbe\n"
"v_or_b32 v192, v192, v193\n"
"v_cmp_ne_u32 s[56:57], 0, v192\n"
"v_cmp_gt_u32 vcc, 144, v234\n"
"s_nop 1\n"
"s_and_b64 vcc, vcc, s[56:57]\n"
"s_cbranch_vccz AT_gok\n"
"s_sub_u32 s87, s87, 1\n"
"s_cmp_eq_u32 s87, 0\n"
"s_cbranch_scc1 AT_gok\n"
"s_sleep 2\n"
"buffer_load_dwordx4 v[144:147], v225, s[28:31], s61 offen offset:0 sc1\n"
"buffer_load_dwordx4 v[148:151], v225, s[28:31], s61 offen offset:16 sc1\n"
"buffer_load_dwordx4 v[152:155], v225, s[28:31], s62 offen offset:0 sc1\n"
"buffer_load_dwordx4 v[156:159], v225, s[28:31], s62 offen offset:16 sc1\n"
"s_waitcnt vmcnt(0)\n"
"s_branch AT_gchk\n"
"AT_gok:\n"
"v_mov_b32 v236, 0x3e0293ee\n"
"v_mul_f32 v192, v236, v144\n"
"v_fma_f32 v196, v152, v236, v236\n"
"v_mul_f32 v193, v236, v146\n"
"v_fma_f32 v197, v154, v236, v236\n"
"v_mul_f32 v194, v236, v148\n"
"v_fma_f32 v198, v156, v236, v236\n"
"v_mul_f32 v195, v236, v150\n"
"v_fma_f32 v199, v158, v236, v236\n"
"ds_write_b128 v216, v[196:199]\n"
"ds_write_b128 v216, v[192:195] offset:128\n"
"v_lshlrev_b32 v176, 16, v112\n"
"v_and_b32 v177, v237, v112\n"
"v_lshlrev_b32 v178, 16, v113\n"
"v_and_b32 v179, v237, v113\n"
"v_lshlrev_b32 v180, 16, v114\n"
"v_and_b32 v181, v237, v114\n"
"v_lshlrev_b32 v182, 16, v115\n"
"v_and_b32 v183, v237, v115\n"
"v_pk_fma_f32 v[176:177], v[176:177], v[208:209], v[210:211] op_sel_hi:[1,0,0]\n"
"v_pk_fma_f32 v[178:179], v[178:179], v[208:209], v[210:211] op_sel_hi:[1,0,0]\n"
"v_pk_fma_f32 v[180:181], v[180:181], v[208:209], v[210:211] op_sel_hi:[1,0,0]\n"
"v_pk_fma_f32 v[182:183], v[182:183], v[208:209], v[210:211] op_sel_hi:[1,0,0]\n"
"v_cvt_pk_f16_f32 v112, v176, v177\n"
"v_cvt_pk_f16_f32 v113, v178, v179\n"
"v_cvt_pk_f16_f32 v114, v180, v181\n"
"v_cvt_pk_f16_f32 v115, v182, v183\n"
"v_lshlrev_b32 v184, 16, v116\n"
"v_and_b32 v185, v237, v116\n"
"v_lshlrev_b32 v186, 16, v117\n"
"v_and_b32 v187, v237, v117\n"
"v_lshlrev_b32 v188, 16, v118\n"
"s_add_u32 m0, s51, 58136\n"
"s_nop 0\n"
"buffer_load_dwordx4 v217, s[20:23], s50 offen offset:256 lds\n"
"s_add_u32 s50, s50, 425984\n"
"v_and_b32 v189, v237, v118\n"
"v_lshlrev_b32 v190, 16, v119\n"
"v_and_b32 v191, v237, v119\n"
"v_pk_fma_f32 v[184:185], v[184:185], v[212:213], v[214:215] op_sel_hi:[1,0,0]\n"
"v_pk_fma_f32 v[186:187], v[186:187], v[212:213], v[214:215] op_sel_hi:[1,0,0]\n"
"v_pk_fma_f32 v[188:189], v[188:189], v[212:213], v[214:215] op_sel_hi:[1,0,0]\n"
"v_pk_fma_f32 v[190:191], v[190:191], v[212:213], v[214:215] op_sel_hi:[1,0,0]\n"
"v_cvt_pk_f16_f32 v116, v184, v185\n"
"v_cvt_pk_f16_f32 v117, v186, v187\n"
"v_cvt_pk_f16_f32 v118, v188, v189\n"
"v_cvt_pk_f16_f32 v119, v190, v191\n"
"v_lshlrev_b32 v176, 16, v120\n"
"v_and_b32 v177, v237, v120\n"
"v_lshlrev_b32 v178, 16, v121\n"
"v_and_b32 v179, v237, v121\n"
"v_lshlrev_b32 v180, 16, v122\n"
"v_and_b32 v181, v237, v122\n"
"v_lshlrev_b32 v182, 16, v123\n"
"v_and_b32 v183, v237, v123\n"
"v_pk_fma_f32 v[176:177], v[176:177], v[208:209], v[210:211] op_sel_hi:[1,0,0]\n"
"v_pk_fma_f32 v[178:179], v[178:179], v[208:209], v[210:211] op_sel_hi:[1,0,0]\n"
"v_pk_fma_f32 v[180:181], v[180:181], v[208:209], v[210:211] op_sel_hi:[1,0,0]\n"
"v_pk_fma_f32 v[182:183], v[182:183], v[208:209], v[210:211] op_sel_hi:[1,0,0]\n"
"v_cvt_pk_f16_f32 v120, v176, v177\n"
"v_cvt_pk_f16_f32 v121, v178, v179\n"
"v_cvt_pk_f16_f32 v122, v180, v181\n"
"v_cvt_pk_f16_f32 v123, v182, v183\n"
"v_lshlrev_b32 v184, 16, v124\n"
"v_and_b32 v185, v237, v124\n"
"v_lshlrev_b32 v186, 16, v125\n"
"v_and_b32 v187, v237, v125\n"
"v_lshlrev_b32 v188, 16, v126\n"
"v_and_b32 v189, v237, v126\n"
"v_lshlrev_b32 v190, 16, v127\n"
"v_and_b32 v191, v237, v127\n"
"v_pk_fma_f32 v[184:185], v[184:185], v[212:213], v[214:215] op_sel_hi:[1,0,0]\n"
"v_pk_fma_f32 v[186:187], v[186:187], v[212:213], v[214:215] op_sel_hi:[1,0,0]\n"
"v_pk_fma_f32 v[188:189], v[188:189], v[212:213], v[214:215] op_sel_hi:[1,0,0]\n"
"v_pk_fma_f32 v[190:191], v[190:191], v[212:213], v[214:215] op_sel_hi:[1,0,0]\n"
"v_cvt_pk_f16_f32 v124, v184, v185\n"
"v_cvt_pk_f16_f32 v125, v186, v187\n"
"v_cvt_pk_f16_f32 v126, v188, v189\n"
"v_cvt_pk_f16_f32 v127, v190, v191\n"
"s_waitcnt vmcnt(20) lgkmcnt(0)\n"
"s_barrier\n"
"ds_read_b128 v[144:147], v224 offset:0\n"
"ds_read_b128 v[148:151], v224 offset:64\n"
"ds_read_b128 v[152:155], v224 offset:128\n"
"ds_read_b128 v[156:159], v224 offset:192\n"
"ds_read_b128 v[160:163], v224 offset:256\n"
"ds_read_b128 v[164:167], v224 offset:320\n"
"ds_read_b128 v[168:171], v224 offset:384\n"
"ds_read_b128 v[172:175], v224 offset:448\n"
"v_lshlrev_b32 v176, 16, v128\n"
"v_and_b32 v177, v237, v128\n"
"v_lshlrev_b32 v178, 16, v129\n"
"v_and_b32 v179, v237, v129\n"
"v_lshlrev_b32 v180, 16, v130\n"
"v_and_b32 v181, v237, v130\n"
"v_lshlrev_b32 v182, 16, v131\n"
"v_and_b32 v183, v237, v131\n"
"v_pk_fma_f32 v[176:177], v[176:177], v[208:209], v[210:211] op_sel_hi:[1,0,0]\n"
"v_pk_fma_f32 v[178:179], v[178:179], v[208:209], v[210:211] op_sel_hi:[1,0,0]\n"
"v_pk_fma_f32 v[180:181], v[180:181], v[208:209], v[210:211] op_sel_hi:[1,0,0]\n"
"v_pk_fma_f32 v[182:183], v[182:183], v[208:209], v[210:211] op_sel_hi:[1,0,0]\n"
"v_cvt_pk_f16_f32 v128, v176, v177\n"
"v_cvt_pk_f16_f32 v129, v178, v179\n"
"v_cvt_pk_f16_f32 v130, v180, v181\n"
"v_cvt_pk_f16_f32 v131, v182, v183\n"
"v_lshlrev_b32 v184, 16, v132\n"
"v_and_b32 v185, v237, v132\n"
"v_lshlrev_b32 v186, 16, v133\n"
"v_and_b32 v187, v237, v133\n"
"v_lshlrev_b32 v188, 16, v134\n"
"v_and_b32 v189, v237, v134\n"
"v_lshlrev_b32 v190, 16, v135\n"
"v_and_b32 v191, v237, v135\n"
"v_pk_fma_f32 v[184:185], v[184:185], v[212:213], v[214:215] op_sel_hi:[1,0,0]\n"
"v_pk_fma_f32 v[186:187], v[186:187], v[212:213], v[214:215] op_sel_hi:[1,0,0]\n"
"v_pk_fma_f32 v[188:189], v[188:189], v[212:213], v[214:215] op_sel_hi:[1,0,0]\n"
"v_pk_fma_f32 v[190:191], v[190:191], v[212:213], v[214:215] op_sel_hi:[1,0,0]\n"
"v_cvt_pk_f16_f32 v132, v184, v185\n"
"v_cvt_pk_f16_f32 v133, v186, v187\n"
"v_cvt_pk_f16_f32 v134, v188, v189\n"
"v_cvt_pk_f16_f32 v135, v190, v191\n"
"v_lshlrev_b32 v176, 16, v136\n"
"v_and_b32 v177, v237, v136\n"
"v_lshlrev_b32 v178, 16, v137\n"
"v_and_b32 v179, v237, v137\n"
"v_lshlrev_b32 v180, 16, v138\n"
"v_and_b32 v181, v237, v138\n"
"v_lshlrev_b32 v182, 16, v139\n"
"v_and_b32 v183, v237, v139\n"
"v_pk_fma_f32 v[176:177], v[176:177], v[208:209], v[210:211] op_sel_hi:[1,0,0]\n"
"v_pk_fma_f32 v[178:179], v[178:179], v[208:209], v[210:211] op_sel_hi:[1,0,0]\n"
"v_pk_fma_f32 v[180:181], v[180:181], v[208:209], v[210:211] op_sel_hi:[1,0,0]\n"
"v_pk_fma_f32 v[182:183], v[182:183], v[208:209], v[210:211] op_sel_hi:[1,0,0]\n"
"v_cvt_pk_f16_f32 v136, v176, v177\n"
"v_cvt_pk_f16_f32 v137, v178, v179\n"
"v_cvt_pk_f16_f32 v138, v180, v181\n"
"v_cvt_pk_f16_f32 v139, v182, v183\n"
"v_lshlrev_b32 v184, 16, v140\n"
"v_and_b32 v185, v237, v140\n"
"v_lshlrev_b32 v186, 16, v141\n"
"v_and_b32 v187, v237, v141\n"
"v_lshlrev_b32 v188, 16, v142\n"
"v_and_b32 v189, v237, v142\n"
"v_lshlrev_b32 v190, 16, v143\n"
"v_and_b32 v191, v237, v143\n"
"v_pk_fma_f32 v[184:185], v[184:185], v[212:213], v[214:215] op_sel_hi:[1,0,0]\n"
"v_pk_fma_f32 v[186:187], v[186:187], v[212:213], v[214:215] op_sel_hi:[1,0,0]\n"
"v_pk_fma_f32 v[188:189], v[188:189], v[212:213], v[214:215] op_sel_hi:[1,0,0]\n"
"v_pk_fma_f32 v[190:191], v[190:191], v[212:213], v[214:215] op_sel_hi:[1,0,0]\n"
"v_cvt_pk_f16_f32 v140, v184, v185\n"
"v_cvt_pk_f16_f32 v141, v186, v187\n"
"v_cvt_pk_f16_f32 v142, v188, v189\n"
"v_cvt_pk_f16_f32 v143, v190, v191\n"
"ds_read_b64_tr_b16 a[160:161], v219 offset:32784\n"
"s_waitcnt lgkmcnt(6)\n"
"v_fma_mix_f32 v176, v0, v144, v152 op_sel:[0,0,0] op_sel_hi:[1,0,0]\n"
"v_fma_mix_f32 v177, v0, v145, v153 op_sel:[1,0,0] op_sel_hi:[1,0,0]\n"
"v_fma_mix_f32 v178, v1, v146, v154 op_sel:[0,0,0] op_sel_hi:[1,0,0]\n"
"ds_read_b64_tr_b16 a[162:163], v219 offset:33808\n"
"v_fma_mix_f32 v179, v1, v147, v155 op_sel:[1,0,0] op_sel_hi:[1,0,0]\n"
"s_waitcnt lgkmcnt(6)\n"
"v_fma_mix_f32 v180, v2, v148, v156 op_sel:[0,0,0] op_sel_hi:[1,0,0]\n"
"v_fma_mix_f32 v181, v2, v149, v157 op_sel:[1,0,0] op_sel_hi:[1,0,0]\n"
"ds_read_b64_tr_b16 a[164:165], v219 offset:36880\n"
"v_fma_mix_f32 v182, v3, v150, v158 op_sel:[0,0,0] op_sel_hi:[1,0,0]\n"
"v_fma_mix_f32 v183, v3, v151, v159 op_sel:[1,0,0] op_sel_hi:[1,0,0]\n"
"v_cvt_pk_bf16_f32 v0, v176, v177\n"
"ds_read_b64_tr_b16 a[166:167], v219 offset:37904\n"
"v_cvt_pk_bf16_f32 v1, v178, v179\n"
"v_cvt_pk_bf16_f32 v2, v180, v181\n"
"v_cvt_pk_bf16_f32 v3, v182, v183\n"
"ds_read_b64_tr_b16 a[168:169], v219 offset:32792\n"
"v_fma_mix_f32 v184, v4, v144, v152 op_sel:[0,0,0] op_sel_hi:[1,0,0]\n"
"v_fma_mix_f32 v185, v4, v145, v153 op_sel:[1,0,0] op_sel_hi:[1,0,0]\n"
"v_fma_mix_f32 v186, v5, v146, v154 op_sel:[0,0,0] op_sel_hi:[1,0,0]\n"
"ds_read_b64_tr_b16 a[170:171], v219 offset:33816\n"
"v_fma_mix_f32 v187, v5, v147, v155 op_sel:[1,0,0] op_sel_hi:[1,0,0]\n"
"v_fma_mix_f32 v188, v6, v148, v156 op_sel:[0,0,0] op_sel_hi:[1,0,0]\n"
"v_fma_mix_f32 v189, v6, v149, v157 op_sel:[1,0,0] op_sel_hi:[1,0,0]\n"
"ds_read_b64_tr_b16 a[172:173], v219 offset:36888\n"
"v_fma_mix_f32 v190, v7, v150, v158 op_sel:[0,0,0] op_sel_hi:[1,0,0]\n"
"v_fma_mix_f32 v191, v7, v151, v159 op_sel:[1,0,0] op_sel_hi:[1,0,0]\n"
"v_cvt_pk_bf16_f32 v4, v184, v185\n"
"ds_read_b64_tr_b16 a[174:175], v219 offset:37912\n"
"ds_read_b128 v[144:147], v224 offset:512\n"
"v_cvt_pk_bf16_f32 v5, v186, v187\n"
"v_cvt_pk_bf16_f32 v6, v188, v189\n"
"ds_read_b64_tr_b16 a[176:177], v219 offset:33040\n"
"v_cvt_pk_bf16_f32 v7, v190, v191\n"
"s_waitcnt lgkmcnt(11)\n"
"v_fma_mix_f32 v176, v8, v160, v168 op_sel:[0,0,0] op_sel_hi:[1,0,0]\n"
"v_fma_mix_f32 v177, v8, v161, v169 op_sel:[1,0,0] op_sel_hi:[1,0,0]\n"
"ds_read_b64_tr_b16 a[178:179], v219 offset:34064\n"
"v_fma_mix_f32 v178, v9, v162, v170 op_sel:[0,0,0] op_sel_hi:[1,0,0]\n"
"v_fma_mix_f32 v179, v9, v163, v171 op_sel:[1,0,0] op_sel_hi:[1,0,0]\n"
"s_waitcnt lgkmcnt(11)\n"
"v_fma_mix_f32 v180, v10, v164, v172 op_sel:[0,0,0] op_sel_hi:[1,0,0]\n"
"ds_read_b64_tr_b16 a[180:181], v219 offset:37136\n"
"ds_read_b128 v[148:151], v224 offset:576\n"
"v_fma_mix_f32 v181, v10, v165, v173 op_sel:[1,0,0] op_sel_hi:[1,0,0]\n"
"v_fma_mix_f32 v182, v11, v166, v174 op_sel:[0,0,0] op_sel_hi:[1,0,0]\n"
"ds_read_b64_tr_b16 a[182:183], v219 offset:38160\n"
"v_fma_mix_f32 v183, v11, v167, v175 op_sel:[1,0,0] op_sel_hi:[1,0,0]\n"
"v_cvt_pk_bf16_f32 v8, v176, v177\n"
"v_cvt_pk_bf16_f32 v9, v178, v179\n"
"ds_read_b64_tr_b16 a[184:185], v219 offset:33048\n"
"v_cvt_pk_bf16_f32 v10, v180, v181\n"
"v_cvt_pk_bf16_f32 v11, v182, v183\n"
"v_fma_mix_f32 v184, v12, v160, v168 op_sel:[0,0,0] op_sel_hi:[1,0,0]\n"
"ds_read_b64_tr_b16 a[186:187], v219 offset:34072\n"
"ds_read_b128 v[152:155], v224 offset:640\n"
"v_fma_mix_f32 v185, v12, v161, v169 op_sel:[1,0,0] op_sel_hi:[1,0,0]\n"
"v_fma_mix_f32 v186, v13, v162, v170 op_sel:[0,0,0] op_sel_hi:[1,0,0]\n"
"ds_read_b64_tr_b16 a[188:189], v219 offset:37144\n"
"v_fma_mix_f32 v187, v13, v163, v171 op_sel:[1,0,0] op_sel_hi:[1,0,0]\n"
"v_fma_mix_f32 v188, v14, v164, v172 op_sel:[0,0,0] op_sel_hi:[1,0,0]\n"
"v_fma_mix_f32 v189, v14, v165, v173 op_sel:[1,0,0] op_sel_hi:[1,0,0]\n"
"ds_read_b64_tr_b16 a[190:191], v219 offset:38168\n"
"v_fma_mix_f32 v190, v15, v166, v174 op_sel:[0,0,0] op_sel_hi:[1,0,0]\n"
"v_fma_mix_f32 v191, v15, v167, v175 op_sel:[1,0,0] op_sel_hi:[1,0,0]\n"
"v_cvt_pk_bf16_f32 v12, v184, v185\n"
"ds_read_b64_tr_b16 a[224:225], v219 offset:33296\n"
"ds_read_b128 v[156:159], v224 offset:704\n"
"ds_read_b128 v[160:163], v224 offset:768\n"
"v_cvt_pk_bf16_f32 v13, v186, v187\n"
"ds_read_b64_tr_b16 a[226:227], v219 offset:34320\n"
"v_cvt_pk_bf16_f32 v14, v188, v189\n"
"v_cvt_pk_bf16_f32 v15, v190, v191\n"
"s_waitcnt lgkmcnt(6)\n"
"v_fma_mix_f32 v176, v16, v144, v152 op_sel:[0,0,0] op_sel_hi:[1,0,0]\n"
"ds_read_b64_tr_b16 a[228:229], v219 offset:37392\n"
"v_fma_mix_f32 v177, v16, v145, v153 op_sel:[1,0,0] op_sel_hi:[1,0,0]\n"
"v_fma_mix_f32 v178, v17, v146, v154 op_sel:[0,0,0] op_sel_hi:[1,0,0]\n"
"v_fma_mix_f32 v179, v17, v147, v155 op_sel:[1,0,0] op_sel_hi:[1,0,0]\n"
"ds_read_b64_tr_b16 a[230:231], v219 offset:38416\n"
"ds_read_b128 v[164:167], v224 offset:832\n"
"s_waitcnt lgkmcnt(5)\n"
"v_fma_mix_f32 v180, v18, v148, v156 op_sel:[0,0,0] op_sel_hi:[1,0,0]\n"
"v_fma_mix_f32 v181, v18, v149, v157 op_sel:[1,0,0] op_sel_hi:[1,0,0]\n"
"v_mfma_f32_16x16x32_bf16 a[0:3], a[160:163], v[0:3], 0\n"
"ds_read_b64_tr_b16 a[232:233], v219 offset:33304\n"
"v_fma_mix_f32 v182, v19, v150, v158 op_sel:[0,0,0] op_sel_hi:[1,0,0]\n"
"v_mfma_f32_16x16x32_bf16 a[0:3], a[164:167], v[8:11], a[0:3]\n"
"ds_read_b64_tr_b16 a[234:235], v219 offset:34328\n"
"v_fma_mix_f32 v183, v19, v151, v159 op_sel:[1,0,0] op_sel_hi:[1,0,0]\n"
"v_mfma_f32_16x16x32_bf16 a[4:7], a[160:163], v[4:7], 0\n"
"s_waitcnt vmcnt(8)\n"
"s_barrier\n"
"ds_read_b64_tr_b16 a[236:237], v219 offset:37400\n"
"ds_read_b128 v[168:171], v224 offset:896\n"
"v_mfma_f32_16x16x32_bf16 a[4:7], a[164:167], v[12:15], a[4:7]\n"
"ds_read_b64_tr_b16 a[238:239], v219 offset:38424\n"
"v_cvt_pk_bf16_f32 v16, v176, v177\n"
"v_mfma_f32_16x16x32_bf16 a[8:11], a[168:171], v[0:3], 0\n"
"s_add_u32 m0, s51, 65568\n"
"ds_read_b64_tr_b16 a[240:241], v219 offset:33552\n"
"v_cvt_pk_bf16_f32 v17, v178, v179\n"
"v_mfma_f32_16x16x32_bf16 a[8:11], a[172:175], v[8:11], a[8:11]\n"
"ds_read_b64_tr_b16 a[242:243], v219 offset:34576\n"
"buffer_load_dwordx4 v217, s[20:23], s50 offen offset:0 lds\n"
"v_mfma_f32_16x16x32_bf16 a[12:15], a[168:171], v[4:7], 0\n"
"ds_read_b64_tr_b16 a[244:245], v219 offset:37648\n"
"ds_read_b128 v[172:175], v224 offset:960\n"
"v_mfma_f32_16x16x32_bf16 a[12:15], a[172:175], v[12:15], a[12:15]\n"
"ds_read_b64_tr_b16 a[246:247], v219 offset:38672\n"
"v_cvt_pk_bf16_f32 v18, v180, v181\n"
"v_mfma_f32_16x16x32_bf16 a[16:19], a[176:179], v[0:3], 0\n"
"ds_read_b64_tr_b16 a[248:249], v219 offset:33560\n"
"v_cvt_pk_bf16_f32 v19, v182, v183\n"
"v_mfma_f32_16x16x32_bf16 a[16:19], a[180:183], v[8:11], a[16:19]\n"
"ds_read_b64_tr_b16 a[250:251], v219 offset:34584\n"
"v_fma_mix_f32 v184, v20, v144, v152 op_sel:[0,0,0] op_sel_hi:[1,0,0]\n"
"v_mfma_f32_16x16x32_bf16 a[20:23], a[176:179], v[4:7], 0\n"
"s_add_u32 m0, s51, 73504\n"
"ds_read_b64_tr_b16 a[252:253], v219 offset:37656\n"
"v_fma_mix_f32 v185, v20, v145, v153 op_sel:[1,0,0] op_sel_hi:[1,0,0]\n"
"v_mfma_f32_16x16x32_bf16 a[20:23], a[180:183], v[12:15], a[20:23]\n"
"ds_read_b64_tr_b16 a[254:255], v219 offset:38680\n"
"buffer_load_dwordx4 v217, s[20:23], s50 offen offset:256 lds\n"
"v_mfma_f32_16x16x32_bf16 a[24:27], a[184:187], v[0:3], 0\n"
"s_add_u32 s50, s50, 32768\n"
"ds_read_b64_tr_b16 a[160:161], v219 offset:40976\n"
"v_fma_mix_f32 v186, v21, v146, v154 op_sel:[0,0,0] op_sel_hi:[1,0,0]\n"
"v_mfma_f32_16x16x32_bf16 a[24:27], a[188:191], v[8:11], a[24:27]\n"
"ds_read_b64_tr_b16 a[162:163], v219 offset:42000\n"
"v_fma_mix_f32 v187, v21, v147, v155 op_sel:[1,0,0] op_sel_hi:[1,0,0]\n"
"v_mfma_f32_16x16x32_bf16 a[28:31], a[184:187], v[4:7], 0\n"
"ds_read_b64_tr_b16 a[164:165], v219 offset:45072\n"
"v_fma_mix_f32 v188, v22, v148, v156 op_sel:[0,0,0] op_sel_hi:[1,0,0]\n"
"v_mfma_f32_16x16x32_bf16 a[28:31], a[188:191], v[12:15], a[28:31]\n"
"s_waitcnt lgkmcnt(12)\n"
"ds_read_b64_tr_b16 a[166:167], v219 offset:46096\n"
"v_fma_mix_f32 v189, v22, v149, v157 op_sel:[1,0,0] op_sel_hi:[1,0,0]\n"
"v_mfma_f32_16x16x32_bf16 a[32:35], a[224:227], v[0:3], 0\n"
"s_add_u32 m0, s51, 66592\n"
"ds_read_b64_tr_b16 a[168:169], v219 offset:40984\n"
"v_fma_mix_f32 v190, v23, v150, v158 op_sel:[0,0,0] op_sel_hi:[1,0,0]\n"
"v_mfma_f32_16x16x32_bf16 a[32:35], a[228:231], v[8:11], a[32:35]\n"
"ds_read_b64_tr_b16 a[170:171], v219 offset:42008\n"
"buffer_load_dwordx4 v217, s[20:23], s50 offen offset:0 lds\n"
"v_mfma_f32_16x16x32_bf16 a[36:39], a[224:227], v[4:7], 0\n"
"ds_read_b64_tr_b16 a[172:173], v219 offset:45080\n"
"v_fma_mix_f32 v191, v23, v151, v159 op_sel:[1,0,0] op_sel_hi:[1,0,0]\n"
"v_mfma_f32_16x16x32_bf16 a[36:39], a[228:231], v[12:15], a[36:39]\n"
"ds_read_b64_tr_b16 a[174:175], v219 offset:46104\n"
"ds_read_b128 v[144:147], v224 offset:1024\n"
"v_mfma_f32_16x16x32_bf16 a[40:43], a[232:235], v[0:3], 0\n"
"ds_read_b64_tr_b16 a[176:177], v219 offset:41232\n"
"v_cvt_pk_bf16_f32 v20, v184, v185\n"
"v_mfma_f32_16x16x32_bf16 a[40:43], a[236:239], v[8:11], a[40:43]\n"
"ds_read_b64_tr_b16 a[178:179], v219 offset:42256\n"
"v_cvt_pk_bf16_f32 v21, v186, v187\n"
"v_mfma_f32_16x16x32_bf16 a[44:47], a[232:235], v[4:7], 0\n"
"s_add_u32 m0, s51, 74528\n"
"ds_read_b64_tr_b16 a[180:181], v219 offset:45328\n"
"ds_read_b128 v[148:151], v224 offset:1088\n"
"v_mfma_f32_16x16x32_bf16 a[44:47], a[236:239], v[12:15], a[44:47]\n"
"s_waitcnt lgkmcnt(13)\n"
"ds_read_b64_tr_b16 a[182:183], v219 offset:46352\n"
"buffer_load_dwordx4 v217, s[20:23], s50 offen offset:256 lds\n"
"v_mfma_f32_16x16x32_bf16 a[48:51], a[240:243], v[0:3], 0\n"
"s_add_u32 s50, s50, 32768\n"
"ds_read_b64_tr_b16 a[184:185], v219 offset:41240\n"
"v_cvt_pk_bf16_f32 v22, v188, v189\n"
"v_mfma_f32_16x16x32_bf16 a[48:51], a[244:247], v[8:11], a[48:51]\n"
"ds_read_b64_tr_b16 a[186:187], v219 offset:42264\n"
"ds_read_b128 v[152:155], v224 offset:1152\n"
"v_mfma_f32_16x16x32_bf16 a[52:55], a[240:243], v[4:7], 0\n"
"ds_read_b64_tr_b16 a[188:189], v219 offset:45336\n"
"v_cvt_pk_bf16_f32 v23, v190, v191\n"
"v_mfma_f32_16x16x32_bf16 a[52:55], a[244:247], v[12:15], a[52:55]\n"
"ds_read_b64_tr_b16 a[190:191], v219 offset:46360\n"
"v_fma_mix_f32 v176, v24, v160, v168 op_sel:[0,0,0] op_sel_hi:[1,0,0]\n"
"v_mfma_f32_16x16x32_bf16 a[56:59], a[248:251], v[0:3], 0\n"
"s_add_u32 m0, s51, 81960\n"
"ds_read_b64_tr_b16 a[224:225], v219 offset:41488\n"
"ds_read_b128 v[156:159], v224 offset:1216\n"
"v_mfma_f32_16x16x32_bf16 a[56:59], a[252:255], v[8:11], a[56:59]\n"
"ds_read_b64_tr_b16 a[226:227], v219 offset:42512\n"
"buffer_load_dwordx4 v217, s[20:23], s50 offen offset:0 lds\n"
"v_mfma_f32_16x16x32_bf16 a[60:63], a[248:251], v[4:7], 0\n"
"ds_read_b64_tr_b16 a[228:229], v219 offset:45584\n"
"v_fma_mix_f32 v177, v24, v161, v169 op_sel:[1,0,0] op_sel_hi:[1,0,0]\n"
"v_mfma_f32_16x16x32_bf16 a[60:63], a[252:255], v[12:15], a[60:63]\n"
"s_waitcnt lgkmcnt(14)\n"
"ds_read_b64_tr_b16 a[230:231], v219 offset:46608\n"
"v_fma_mix_f32 v178, v25, v162, v170 op_sel:[0,0,0] op_sel_hi:[1,0,0]\n"
"v_mfma_f32_16x16x32_bf16 a[64:67], a[160:163], v[0:3], 0\n"
"ds_read_b64_tr_b16 a[232:233], v219 offset:41496\n"
"v_fma_mix_f32 v179, v25, v163, v171 op_sel:[1,0,0] op_sel_hi:[1,0,0]\n"
"v_mfma_f32_16x16x32_bf16 a[64:67], a[164:167], v[8:11], a[64:67]\n"
"ds_read_b64_tr_b16 a[234:235], v219 offset:42520\n"
"v_fma_mix_f32 v180, v26, v164, v172 op_sel:[0,0,0] op_sel_hi:[1,0,0]\n"
"v_mfma_f32_16x16x32_bf16 a[68:71], a[160:163], v[4:7], 0\n"
"s_add_u32 m0, s51, 89896\n"
"ds_read_b64_tr_b16 a[236:237], v219 offset:45592\n"
"v_fma_mix_f32 v181, v26, v165, v173 op_sel:[1,0,0] op_sel_hi:[1,0,0]\n"
"v_mfma_f32_16x16x32_bf16 a[68:71], a[164:167], v[12:15], a[68:71]\n"
"ds_read_b64_tr_b16 a[238:239], v219 offset:46616\n"
"buffer_load_dwordx4 v217, s[20:23], s50 offen offset:256 lds\n"
"v_mfma_f32_16x16x32_bf16 a[72:75], a[168:171], v[0:3], 0\n"
"s_add_u32 s50, s50, 32768\n"
"ds_read_b64_tr_b16 a[240:241], v219 offset:41744\n"
"v_fma_mix_f32 v182, v27, v166, v174 op_sel:[0,0,0] op_sel_hi:[1,0,0]\n"
"v_mfma_f32_16x16x32_bf16 a[72:75], a[172:175], v[8:11], a[72:75]\n"
"ds_read_b64_tr_b16 a[242:243], v219 offset:42768\n"
"v_fma_mix_f32 v183, v27, v167, v175 op_sel:[1,0,0] op_sel_hi:[1,0,0]\n"
"v_mfma_f32_16x16x32_bf16 a[76:79], a[168:171], v[4:7], 0\n"
"ds_read_b64_tr_b16 a[244:245], v219 offset:45840\n"
"v_cvt_pk_bf16_f32 v24, v176, v177\n"
"v_mfma_f32_16x16x32_bf16 a[76:79], a[172:175], v[12:15], a[76:79]\n"
"s_waitcnt lgkmcnt(12)\n"
"ds_read_b64_tr_b16 a[246:247], v219 offset:46864\n"
"v_cvt_pk_bf16_f32 v25, v178, v179\n"
"v_mfma_f32_16x16x32_bf16 a[80:83], a[176:179], v[0:3], 0\n"
"s_add_u32 m0, s51, 82984\n"
"ds_read_b64_tr_b16 a[248:249], v219 offset:41752\n"
"v_cvt_pk_bf16_f32 v26, v180, v181\n"
"v_mfma_f32_16x16x32_bf16 a[80:83], a[180:183], v[8:11], a[80:83]\n"
"ds_read_b64_tr_b16 a[250:251], v219 offset:42776\n"
"buffer_load_dwordx4 v217, s[20:23], s50 offen offset:0 lds\n"
"v_mfma_f32_16x16x32_bf16 a[84:87], a[176:179], v[4:7], 0\n"
"ds_read_b64_tr_b16 a[252:253], v219 offset:45848\n"
"v_cvt_pk_bf16_f32 v27, v182, v183\n"
"v_mfma_f32_16x16x32_bf16 a[84:87], a[180:183], v[12:15], a[84:87]\n"
"ds_read_b64_tr_b16 a[254:255], v219 offset:46872\n"
"v_fma_mix_f32 v184, v28, v160, v168 op_sel:[0,0,0] op_sel_hi:[1,0,0]\n"
"v_mfma_f32_16x16x32_bf16 a[88:91], a[184:187], v[0:3], 0\n"
"ds_read_b64_tr_b16 a[160:161], v218 offset:0\n"
"v_fma_mix_f32 v185, v28, v161, v169 op_sel:[1,0,0] op_sel_hi:[1,0,0]\n"
"v_fma_mix_f32 v186, v29, v162, v170 op_sel:[0,0,0] op_sel_hi:[1,0,0]\n"
"v_mfma_f32_16x16x32_bf16 a[88:91], a[188:191], v[8:11], a[88:91]\n"
"ds_read_b64_tr_b16 a[162:163], v218 offset:1024\n"
"v_fma_mix_f32 v187, v29, v163, v171 op_sel:[1,0,0] op_sel_hi:[1,0,0]\n"
"v_mfma_f32_16x16x32_bf16 a[92:95], a[184:187], v[4:7], 0\n"
"s_add_u32 m0, s51, 90920\n"
"ds_read_b64_tr_b16 a[164:165], v218 offset:4096\n"
"v_fma_mix_f32 v188, v30, v164, v172 op_sel:[0,0,0] op_sel_hi:[1,0,0]\n"
"v_mfma_f32_16x16x32_bf16 a[92:95], a[188:191], v[12:15], a[92:95]\n"
"s_waitcnt lgkmcnt(11)\n"
"buffer_load_dwordx4 v217, s[20:23], s50 offen offset:256 lds\n"
"ds_read_b64_tr_b16 a[166:167], v218 offset:5120\n"
"v_mfma_f32_16x16x32_bf16 a[96:99], a[224:227], v[0:3], 0\n"
"s_add_u32 s50, s50, 425984\n"
"ds_read_b64_tr_b16 a[168:169], v218 offset:8\n"
"v_fma_mix_f32 v189, v30, v165, v173 op_sel:[1,0,0] op_sel_hi:[1,0,0]\n"
"v_mfma_f32_16x16x32_bf16 a[96:99], a[228:231], v[8:11], a[96:99]\n"
"ds_read_b64_tr_b16 a[170:171], v218 offset:1032\n"
"v_fma_mix_f32 v190, v31, v166, v174 op_sel:[0,0,0] op_sel_hi:[1,0,0]\n"
"v_mfma_f32_16x16x32_bf16 a[100:103], a[224:227], v[4:7], 0\n"
"ds_read_b64_tr_b16 a[172:173], v218 offset:4104\n"
"v_fma_mix_f32 v191, v31, v167, v175 op_sel:[1,0,0] op_sel_hi:[1,0,0]\n"
"v_mfma_f32_16x16x32_bf16 a[100:103], a[228:231], v[12:15], a[100:103]\n"
"ds_read_b64_tr_b16 a[174:175], v218 offset:5128\n"
"ds_read_b128 v[160:163], v224 offset:1280\n"
"v_mfma_f32_16x16x32_bf16 a[104:107], a[232:235], v[0:3], 0\n"
"ds_read_b64_tr_b16 a[176:177], v218 offset:256\n"
"v_cvt_pk_bf16_f32 v28, v184, v185\n"
"v_mfma_f32_16x16x32_bf16 a[104:107], a[236:239], v[8:11], a[104:107]\n"
"ds_read_b64_tr_b16 a[178:179], v218 offset:1280\n"
"v_cvt_pk_bf16_f32 v29, v186, v187\n"
"v_mfma_f32_16x16x32_bf16 a[108:111], a[232:235], v[4:7], 0\n"
"ds_read_b64_tr_b16 a[180:181], v218 offset:4352\n"
"ds_read_b128 v[164:167], v224 offset:1344\n"
"v_mfma_f32_16x16x32_bf16 a[108:111], a[236:239], v[12:15], a[108:111]\n"
"s_waitcnt lgkmcnt(13)\n"
"ds_read_b64_tr_b16 a[182:183], v218 offset:5376\n"
"v_cvt_pk_bf16_f32 v30, v188, v189\n"
"v_mfma_f32_16x16x32_bf16 a[112:115], a[240:243], v[0:3], 0\n"
"ds_read_b64_tr_b16 a[184:185], v218 offset:264\n"
"v_cvt_pk_bf16_f32 v31, v190, v191\n"
"v_mfma_f32_16x16x32_bf16 a[112:115], a[244:247], v[8:11], a[112:115]\n"
"ds_read_b64_tr_b16 a[186:187], v218 offset:1288\n"
"ds_read_b128 v[168:171], v224 offset:1408\n"
"v_mfma_f32_16x16x32_bf16 a[116:119], a[240:243], v[4:7], 0\n"
"ds_read_b64_tr_b16 a[188:189], v218 offset:4360\n"
"v_fma_mix_f32 v176, v32, v144, v152 op_sel:[0,0,0] op_sel_hi:[1,0,0]\n"
"v_mfma_f32_16x16x32_bf16 a[116:119], a[244:247], v[12:15], a[116:119]\n"
"ds_read_b64_tr_b16 a[190:191], v218 offset:5384\n"
"v_fma_mix_f32 v177, v32, v145, v153 op_sel:[1,0,0] op_sel_hi:[1,0,0]\n"
"v_mfma_f32_16x16x32_bf16 a[120:123], a[248:251], v[0:3], 0\n"
"ds_read_b64_tr_b16 a[224:225], v218 offset:512\n"
"ds_read_b128 v[172:175], v224 offset:1472\n"
"v_mfma_f32_16x16x32_bf16 a[120:123], a[252:255], v[8:11], a[120:123]\n"
"ds_read_b64_tr_b16 a[226:227], v218 offset:1536\n"
"v_fma_mix_f32 v178, v33, v146, v154 op_sel:[0,0,0] op_sel_hi:[1,0,0]\n"
"v_mfma_f32_16x16x32_bf16 a[124:127], a[248:251], v[4:7], 0\n"
"ds_read_b64_tr_b16 a[228:229], v218 offset:4608\n"
"v_fma_mix_f32 v179, v33, v147, v155 op_sel:[1,0,0] op_sel_hi:[1,0,0]\n"
"v_mfma_f32_16x16x32_bf16 a[124:127], a[252:255], v[12:15], a[124:127]\n"
"s_waitcnt lgkmcnt(14)\n"
"ds_read_b64_tr_b16 a[230:231], v218 offset:5632\n"
"v_fma_mix_f32 v180, v34, v148, v156 op_sel:[0,0,0] op_sel_hi:[1,0,0]\n"
"v_mfma_f32_16x16x32_bf16 a[0:3], a[160:163], v[16:19], a[0:3]\n"
"ds_read_b64_tr_b16 a[232:233], v218 offset:520\n"
"v_fma_mix_f32 v181, v34, v149, v157 op_sel:[1,0,0] op_sel_hi:[1,0,0]\n"
"v_mfma_f32_16x16x32_bf16 a[0:3], a[164:167], v[24:27], a[0:3]\n"
"ds_read_b64_tr_b16 a[234:235], v218 offset:1544\n"
"v_fma_mix_f32 v182, v35, v150, v158 op_sel:[0,0,0] op_sel_hi:[1,0,0]\n"
"v_mfma_f32_16x16x32_bf16 a[4:7], a[160:163], v[20:23], a[4:7]\n"
"s_waitcnt vmcnt(8)\n"
"s_barrier\n"
"ds_read_b64_tr_b16 a[236:237], v218 offset:4616\n"
"v_fma_mix_f32 v183, v35, v151, v159 op_sel:[1,0,0] op_sel_hi:[1,0,0]\n"
"v_mfma_f32_16x16x32_bf16 a[4:7], a[164:167], v[28:31], a[4:7]\n"
"ds_read_b64_tr_b16 a[238:239], v218 offset:5640\n"
"v_cvt_pk_bf16_f32 v32, v176, v177\n"
"v_mfma_f32_16x16x32_bf16 a[8:11], a[168:171], v[16:19], a[8:11]\n"
"s_add_u32 m0, s51, 98352\n"
"ds_read_b64_tr_b16 a[240:241], v218 offset:768\n"
"v_cvt_pk_bf16_f32 v33, v178, v179\n"
"v_mfma_f32_16x16x32_bf16 a[8:11], a[172:175], v[24:27], a[8:11]\n"
"ds_read_b64_tr_b16 a[242:243], v218 offset:1792\n"
"buffer_load_dwordx4 v217, s[20:23], s50 offen offset:0 lds\n"
"v_mfma_f32_16x16x32_bf16 a[12:15], a[168:171], v[20:23], a[12:15]\n"
"ds_read_b64_tr_b16 a[244:245], v218 offset:4864\n"
"v_cvt_pk_bf16_f32 v34, v180, v181\n"
"v_mfma_f32_16x16x32_bf16 a[12:15], a[172:175], v[28:31], a[12:15]\n"
"s_waitcnt lgkmcnt(12)\n"
"ds_read_b64_tr_b16 a[246:247], v218 offset:5888\n"
"v_cvt_pk_bf16_f32 v35, v182, v183\n"
"v_mfma_f32_16x16x32_bf16 a[16:19], a[176:179], v[16:19], a[16:19]\n"
"ds_read_b64_tr_b16 a[248:249], v218 offset:776\n"
"v_fma_mix_f32 v184, v36, v144, v152 op_sel:[0,0,0] op_sel_hi:[1,0,0]\n"
"v_mfma_f32_16x16x32_bf16 a[16:19], a[180:183], v[24:27], a[16:19]\n"
"ds_read_b64_tr_b16 a[250:251], v218 offset:1800\n"
"v_fma_mix_f32 v185, v36, v145, v153 op_sel:[1,0,0] op_sel_hi:[1,0,0]\n"
"v_mfma_f32_16x16x32_bf16 a[20:23], a[176:179], v[20:23], a[20:23]\n"
"s_add_u32 m0, s51, 106288\n"
"ds_read_b64_tr_b16 a[252:253], v218 offset:4872\n"
"v_fma_mix_f32 v186, v37, v146, v154 op_sel:[0,0,0] op_sel_hi:[1,0,0]\n"
"v_mfma_f32_16x16x32_bf16 a[20:23], a[180:183], v[28:31], a[20:23]\n"
"ds_read_b64_tr_b16 a[254:255], v218 offset:5896\n"
"buffer_load_dwordx4 v217, s[20:23], s50 offen offset:256 lds\n"
"v_mfma_f32_16x16x32_bf16 a[24:27], a[184:187], v[16:19], a[24:27]\n"
"s_add_u32 s50, s50, 32768\n"
"ds_read_b64_tr_b16 a[160:161], v218 offset:8192\n"
"v_fma_mix_f32 v187, v37, v147, v155 op_sel:[1,0,0] op_sel_hi:[1,0,0]\n"
"v_mfma_f32_16x16x32_bf16 a[24:27], a[188:191], v[24:27], a[24:27]\n"
"ds_read_b64_tr_b16 a[162:163], v218 offset:9216\n"
"v_fma_mix_f32 v188, v38, v148, v156 op_sel:[0,0,0] op_sel_hi:[1,0,0]\n"
"v_mfma_f32_16x16x32_bf16 a[28:31], a[184:187], v[20:23], a[28:31]\n"
"ds_read_b64_tr_b16 a[164:165], v218 offset:12288\n"
"v_fma_mix_f32 v189, v38, v149, v157 op_sel:[1,0,0] op_sel_hi:[1,0,0]\n"
"v_mfma_f32_16x16x32_bf16 a[28:31], a[188:191], v[28:31], a[28:31]\n"
"s_waitcnt lgkmcnt(11)\n"
"ds_read_b64_tr_b16 a[166:167], v218 offset:13312\n"
"v_fma_mix_f32 v190, v39, v150, v158 op_sel:[0,0,0] op_sel_hi:[1,0,0]\n"
"v_mfma_f32_16x16x32_bf16 a[32:35], a[224:227], v[16:19], a[32:35]\n"
"s_add_u32 m0, s51, 99376\n"
"ds_read_b64_tr_b16 a[168:169], v218 offset:8200\n"
"v_fma_mix_f32 v191, v39, v151, v159 op_sel:[1,0,0] op_sel_hi:[1,0,0]\n"
"v_mfma_f32_16x16x32_bf16 a[32:35], a[228:231], v[24:27], a[32:35]\n"
"ds_read_b64_tr_b16 a[170:171], v218 offset:9224\n"
"buffer_load_dwordx4 v217, s[20:23], s50 offen offset:0 lds\n"
"v_mfma_f32_16x16x32_bf16 a[36:39], a[224:227], v[20:23], a[36:39]\n"
"ds_read_b64_tr_b16 a[172:173], v218 offset:12296\n"
"ds_read_b128 v[144:147], v224 offset:1536\n"
"v_mfma_f32_16x16x32_bf16 a[36:39], a[228:231], v[28:31], a[36:39]\n"
"ds_read_b64_tr_b16 a[174:175], v218 offset:13320\n"
"v_cvt_pk_bf16_f32 v36, v184, v185\n"
"v_mfma_f32_16x16x32_bf16 a[40:43], a[232:235], v[16:19], a[40:43]\n"
"ds_read_b64_tr_b16 a[176:177], v218 offset:8448\n"
"v_cvt_pk_bf16_f32 v37, v186, v187\n"
"v_mfma_f32_16x16x32_bf16 a[40:43], a[236:239], v[24:27], a[40:43]\n"
"ds_read_b64_tr_b16 a[178:179], v218 offset:9472\n"
"ds_read_b128 v[148:151], v224 offset:1600\n"
"v_mfma_f32_16x16x32_bf16 a[44:47], a[232:235], v[20:23], a[44:47]\n"
"s_add_u32 m0, s51, 107312\n"
"ds_read_b64_tr_b16 a[180:181], v218 offset:12544\n"
"v_cvt_pk_bf16_f32 v38, v188, v189\n"
"v_mfma_f32_16x16x32_bf16 a[44:47], a[236:239], v[28:31], a[44:47]\n"
"s_waitcnt lgkmcnt(13)\n"
"ds_read_b64_tr_b16 a[182:183], v218 offset:13568\n"
"buffer_load_dwordx4 v217, s[20:23], s50 offen offset:256 lds\n"
"v_mfma_f32_16x16x32_bf16 a[48:51], a[240:243], v[16:19], a[48:51]\n"
"s_add_u32 s50, s50, 32768\n"
"ds_read_b64_tr_b16 a[184:185], v218 offset:8456\n"
"ds_read_b128 v[152:155], v224 offset:1664\n"
"v_mfma_f32_16x16x32_bf16 a[48:51], a[244:247], v[24:27], a[48:51]\n"
"ds_read_b64_tr_b16 a[186:187], v218 offset:9480\n"
"v_cvt_pk_bf16_f32 v39, v190, v191\n"
"v_mfma_f32_16x16x32_bf16 a[52:55], a[240:243], v[20:23], a[52:55]\n"
"ds_read_b64_tr_b16 a[188:189], v218 offset:12552\n"
"v_fma_mix_f32 v176, v40, v160, v168 op_sel:[0,0,0] op_sel_hi:[1,0,0]\n"
"v_mfma_f32_16x16x32_bf16 a[52:55], a[244:247], v[28:31], a[52:55]\n"
"ds_read_b64_tr_b16 a[190:191], v218 offset:13576\n"
"ds_read_b128 v[156:159], v224 offset:1728\n"
"v_mfma_f32_16x16x32_bf16 a[56:59], a[248:251], v[16:19], a[56:59]\n"
"s_add_u32 m0, s51, 114744\n"
"ds_read_b64_tr_b16 a[224:225], v218 offset:8704\n"
"v_fma_mix_f32 v177, v40, v161, v169 op_sel:[1,0,0] op_sel_hi:[1,0,0]\n"
"v_mfma_f32_16x16x32_bf16 a[56:59], a[252:255], v[24:27], a[56:59]\n"
"ds_read_b64_tr_b16 a[226:227], v218 offset:9728\n"
"buffer_load_dwordx4 v217, s[20:23], s50 offen offset:0 lds\n"
"v_mfma_f32_16x16x32_bf16 a[60:63], a[248:251], v[20:23], a[60:63]\n"
"ds_read_b64_tr_b16 a[228:229], v218 offset:12800\n"
"v_fma_mix_f32 v178, v41, v162, v170 op_sel:[0,0,0] op_sel_hi:[1,0,0]\n"
"v_mfma_f32_16x16x32_bf16 a[60:63], a[252:255], v[28:31], a[60:63]\n"
"s_waitcnt lgkmcnt(14)\n"
"ds_read_b64_tr_b16 a[230:231], v218 offset:13824\n"
"v_fma_mix_f32 v179, v41, v163, v171 op_sel:[1,0,0] op_sel_hi:[1,0,0]\n"
"v_mfma_f32_16x16x32_bf16 a[64:67], a[160:163], v[16:19], a[64:67]\n"
"ds_read_b64_tr_b16 a[232:233], v218 offset:8712\n"
"v_fma_mix_f32 v180, v42, v164, v172 op_sel:[0,0,0] op_sel_hi:[1,0,0]\n"
"v_mfma_f32_16x16x32_bf16 a[64:67], a[164:167], v[24:27], a[64:67]\n"
"ds_read_b64_tr_b16 a[234:235], v218 offset:9736\n"
"v_fma_mix_f32 v181, v42, v165, v173 op_sel:[1,0,0] op_sel_hi:[1,0,0]\n"
"v_mfma_f32_16x16x32_bf16 a[68:71], a[160:163], v[20:23], a[68:71]\n"
"s_add_u32 m0, s51, 122680\n"
"ds_read_b64_tr_b16 a[236:237], v218 offset:12808\n"
"v_fma_mix_f32 v182, v43, v166, v174 op_sel:[0,0,0] op_sel_hi:[1,0,0]\n"
"v_mfma_f32_16x16x32_bf16 a[68:71], a[164:167], v[28:31], a[68:71]\n"
"ds_read_b64_tr_b16 a[238:239], v218 offset:13832\n"
"buffer_load_dwordx4 v217, s[20:23], s50 offen offset:256 lds\n"
"v_mfma_f32_16x16x32_bf16 a[72:75], a[168:171], v[16:19], a[72:75]\n"
"s_add_u32 s50, s50, 32768\n"
"ds_read_b64_tr_b16 a[240:241], v218 offset:8960\n"
"v_fma_mix_f32 v183, v43, v167, v175 op_sel:[1,0,0] op_sel_hi:[1,0,0]\n"
"v_mfma_f32_16x16x32_bf16 a[72:75], a[172:175], v[24:27], a[72:75]\n"
"ds_read_b64_tr_b16 a[242:243], v218 offset:9984\n"
"v_cvt_pk_bf16_f32 v40, v176, v177\n"
"v_mfma_f32_16x16x32_bf16 a[76:79], a[168:171], v[20:23], a[76:79]\n"
"ds_read_b64_tr_b16 a[244:245], v218 offset:13056\n"
"v_cvt_pk_bf16_f32 v41, v178, v179\n"
"v_mfma_f32_16x16x32_bf16 a[76:79], a[172:175], v[28:31], a[76:79]\n"
"s_waitcnt lgkmcnt(11)\n"
"ds_read_b64_tr_b16 a[246:247], v218 offset:14080\n"
"v_cvt_pk_bf16_f32 v42, v180, v181\n"
"v_mfma_f32_16x16x32_bf16 a[80:83], a[176:179], v[16:19], a[80:83]\n"
"s_add_u32 m0, s51, 115768\n"
"ds_read_b64_tr_b16 a[248:249], v218 offset:8968\n"
"v_cvt_pk_bf16_f32 v43, v182, v183\n"
"v_mfma_f32_16x16x32_bf16 a[80:83], a[180:183], v[24:27], a[80:83]\n"
"ds_read_b64_tr_b16 a[250:251], v218 offset:9992\n"
"buffer_load_dwordx4 v217, s[20:23], s50 offen offset:0 lds\n"
"v_mfma_f32_16x16x32_bf16 a[84:87], a[176:179], v[20:23], a[84:87]\n"
"ds_read_b64_tr_b16 a[252:253], v218 offset:13064\n"
"v_fma_mix_f32 v184, v44, v160, v168 op_sel:[0,0,0] op_sel_hi:[1,0,0]\n"
"v_mfma_f32_16x16x32_bf16 a[84:87], a[180:183], v[28:31], a[84:87]\n"
"ds_read_b64_tr_b16 a[254:255], v218 offset:14088\n"
"v_fma_mix_f32 v185, v44, v161, v169 op_sel:[1,0,0] op_sel_hi:[1,0,0]\n"
"v_mfma_f32_16x16x32_bf16 a[88:91], a[184:187], v[16:19], a[88:91]\n"
"ds_read_b64_tr_b16 a[160:161], v218 offset:32784\n"
"v_fma_mix_f32 v186, v45, v162, v170 op_sel:[0,0,0] op_sel_hi:[1,0,0]\n"
"v_fma_mix_f32 v187, v45, v163, v171 op_sel:[1,0,0] op_sel_hi:[1,0,0]\n"
"v_mfma_f32_16x16x32_bf16 a[88:91], a[188:191], v[24:27], a[88:91]\n"
"ds_read_b64_tr_b16 a[162:163], v218 offset:33808\n"
"v_fma_mix_f32 v188, v46, v164, v172 op_sel:[0,0,0] op_sel_hi:[1,0,0]\n"
"v_mfma_f32_16x16x32_bf16 a[92:95], a[184:187], v[20:23], a[92:95]\n"
"s_add_u32 m0, s51, 123704\n"
"ds_read_b64_tr_b16 a[164:165], v218 offset:36880\n"
"v_fma_mix_f32 v189, v46, v165, v173 op_sel:[1,0,0] op_sel_hi:[1,0,0]\n"
"v_mfma_f32_16x16x32_bf16 a[92:95], a[188:191], v[28:31], a[92:95]\n"
"s_waitcnt lgkmcnt(11)\n"
"buffer_load_dwordx4 v217, s[20:23], s50 offen offset:256 lds\n"
"ds_read_b64_tr_b16 a[166:167], v218 offset:37904\n"
"v_mfma_f32_16x16x32_bf16 a[96:99], a[224:227], v[16:19], a[96:99]\n"
"s_add_u32 s50, s50, 425984\n"
"ds_read_b64_tr_b16 a[168:169], v218 offset:32792\n"
"v_fma_mix_f32 v190, v47, v166, v174 op_sel:[0,0,0] op_sel_hi:[1,0,0]\n"
"v_mfma_f32_16x16x32_bf16 a[96:99], a[228:231], v[24:27], a[96:99]\n"
"ds_read_b64_tr_b16 a[170:171], v218 offset:33816\n"
"v_fma_mix_f32 v191, v47, v167, v175 op_sel:[1,0,0] op_sel_hi:[1,0,0]\n"
"v_mfma_f32_16x16x32_bf16 a[100:103], a[224:227], v[20:23], a[100:103]\n"
"ds_read_b64_tr_b16 a[172:173], v218 offset:36888\n"
"ds_read_b128 v[160:163], v224 offset:1792\n"
"v_mfma_f32_16x16x32_bf16 a[100:103], a[228:231], v[28:31], a[100:103]\n"
"ds_read_b64_tr_b16 a[174:175], v218 offset:37912\n"
"v_cvt_pk_bf16_f32 v44, v184, v185\n"
"v_mfma_f32_16x16x32_bf16 a[104:107], a[232:235], v[16:19], a[104:107]\n"
"ds_read_b64_tr_b16 a[176:177], v218 offset:33040\n"
"v_cvt_pk_bf16_f32 v45, v186, v187\n"
"v_mfma_f32_16x16x32_bf16 a[104:107], a[236:239], v[24:27], a[104:107]\n"
"ds_read_b64_tr_b16 a[178:179], v218 offset:34064\n"
"ds_read_b128 v[164:167], v224 offset:1856\n"
"v_mfma_f32_16x16x32_bf16 a[108:111], a[232:235], v[20:23], a[108:111]\n"
"ds_read_b64_tr_b16 a[180:181], v218 offset:37136\n"
"v_cvt_pk_bf16_f32 v46, v188, v189\n"
"v_mfma_f32_16x16x32_bf16 a[108:111], a[236:239], v[28:31], a[108:111]\n"
"s_waitcnt lgkmcnt(13)\n"
"ds_read_b64_tr_b16 a[182:183], v218 offset:38160\n"
"v_cvt_pk_bf16_f32 v47, v190, v191\n"
"v_mfma_f32_16x16x32_bf16 a[112:115], a[240:243], v[16:19], a[112:115]\n"
"ds_read_b64_tr_b16 a[184:185], v218 offset:33048\n"
"ds_read_b128 v[168:171], v224 offset:1920\n"
"v_mfma_f32_16x16x32_bf16 a[112:115], a[244:247], v[24:27], a[112:115]\n"
"ds_read_b64_tr_b16 a[186:187], v218 offset:34072\n"
"v_fma_mix_f32 v176, v48, v144, v152 op_sel:[0,0,0] op_sel_hi:[1,0,0]\n"
"v_mfma_f32_16x16x32_bf16 a[116:119], a[240:243], v[20:23], a[116:119]\n"
"ds_read_b64_tr_b16 a[188:189], v218 offset:37144\n"
"v_fma_mix_f32 v177, v48, v145, v153 op_sel:[1,0,0] op_sel_hi:[1,0,0]\n"
"v_mfma_f32_16x16x32_bf16 a[116:119], a[244:247], v[28:31], a[116:119]\n"
"ds_read_b64_tr_b16 a[190:191], v218 offset:38168\n"
"ds_read_b128 v[172:175], v224 offset:1984\n"
"v_mfma_f32_16x16x32_bf16 a[120:123], a[248:251], v[16:19], a[120:123]\n"
"ds_read_b64_tr_b16 a[224:225], v218 offset:33296\n"
"v_fma_mix_f32 v178, v49, v146, v154 op_sel:[0,0,0] op_sel_hi:[1,0,0]\n"
"v_mfma_f32_16x16x32_bf16 a[120:123], a[252:255], v[24:27], a[120:123]\n"
"ds_read_b64_tr_b16 a[226:227], v218 offset:34320\n"
"v_fma_mix_f32 v179, v49, v147, v155 op_sel:[1,0,0] op_sel_hi:[1,0,0]\n"
"v_mfma_f32_16x16x32_bf16 a[124:127], a[248:251], v[20:23], a[124:127]\n"
"ds_read_b64_tr_b16 a[228:229], v218 offset:37392\n"
"v_fma_mix_f32 v180, v50, v148, v156 op_sel:[0,0,0] op_sel_hi:[1,0,0]\n"
"v_mfma_f32_16x16x32_bf16 a[124:127], a[252:255], v[28:31], a[124:127]\n"
"s_waitcnt lgkmcnt(14)\n"
"ds_read_b64_tr_b16 a[230:231], v218 offset:38416\n"
"v_fma_mix_f32 v181, v50, v149, v157 op_sel:[1,0,0] op_sel_hi:[1,0,0]\n"
"v_mfma_f32_16x16x32_bf16 a[0:3], a[160:163], v[32:35], a[0:3]\n"
"ds_read_b64_tr_b16 a[232:233], v218 offset:33304\n"
"v_fma_mix_f32 v182, v51, v150, v158 op_sel:[0,0,0] op_sel_hi:[1,0,0]\n"
"v_mfma_f32_16x16x32_bf16 a[0:3], a[164:167], v[40:43], a[0:3]\n"
"ds_read_b64_tr_b16 a[234:235], v218 offset:34328\n"
"v_fma_mix_f32 v183, v51, v151, v159 op_sel:[1,0,0] op_sel_hi:[1,0,0]\n"
"v_mfma_f32_16x16x32_bf16 a[4:7], a[160:163], v[36:39], a[4:7]\n"
"s_waitcnt vmcnt(8)\n"
"s_barrier\n"
"ds_read_b64_tr_b16 a[236:237], v218 offset:37400\n"
"v_cvt_pk_bf16_f32 v48, v176, v177\n"
"v_mfma_f32_16x16x32_bf16 a[4:7], a[164:167], v[44:47], a[4:7]\n"
"ds_read_b64_tr_b16 a[238:239], v218 offset:38424\n"
"v_cvt_pk_bf16_f32 v49, v178, v179\n"
"v_mfma_f32_16x16x32_bf16 a[8:11], a[168:171], v[32:35], a[8:11]\n"
"s_mov_b32 m0, s51\n"
"ds_read_b64_tr_b16 a[240:241], v218 offset:33552\n"
"v_cvt_pk_bf16_f32 v50, v180, v181\n"
"v_mfma_f32_16x16x32_bf16 a[8:11], a[172:175], v[40:43], a[8:11]\n"
"ds_read_b64_tr_b16 a[242:243], v218 offset:34576\n"
"buffer_load_dwordx4 v217, s[20:23], s50 offen offset:0 lds\n"
"v_mfma_f32_16x16x32_bf16 a[12:15], a[168:171], v[36:39], a[12:15]\n"
"ds_read_b64_tr_b16 a[244:245], v218 offset:37648\n"
"v_cvt_pk_bf16_f32 v51, v182, v183\n"
"v_mfma_f32_16x16x32_bf16 a[12:15], a[172:175], v[44:47], a[12:15]\n"
"s_waitcnt lgkmcnt(11)\n"
"ds_read_b64_tr_b16 a[246:247], v218 offset:38672\n"
"v_fma_mix_f32 v184, v52, v144, v152 op_sel:[0,0,0] op_sel_hi:[1,0,0]\n"
"v_mfma_f32_16x16x32_bf16 a[16:19], a[176:179], v[32:35], a[16:19]\n"
"ds_read_b64_tr_b16 a[248:249], v218 offset:33560\n"
"v_fma_mix_f32 v185, v52, v145, v153 op_sel:[1,0,0] op_sel_hi:[1,0,0]\n"
"v_mfma_f32_16x16x32_bf16 a[16:19], a[180:183], v[40:43], a[16:19]\n"
"ds_read_b64_tr_b16 a[250:251], v218 offset:34584\n"
"v_fma_mix_f32 v186, v53, v146, v154 op_sel:[0,0,0] op_sel_hi:[1,0,0]\n"
"v_mfma_f32_16x16x32_bf16 a[20:23], a[176:179], v[36:39], a[20:23]\n"
"s_add_u32 m0, s51, 7936\n"
"ds_read_b64_tr_b16 a[252:253], v218 offset:37656\n"
"v_fma_mix_f32 v187, v53, v147, v155 op_sel:[1,0,0] op_sel_hi:[1,0,0]\n"
"v_mfma_f32_16x16x32_bf16 a[20:23], a[180:183], v[44:47], a[20:23]\n"
"ds_read_b64_tr_b16 a[254:255], v218 offset:38680\n"
"buffer_load_dwordx4 v217, s[20:23], s50 offen offset:256 lds\n"
"v_mfma_f32_16x16x32_bf16 a[24:27], a[184:187], v[32:35], a[24:27]\n"
"s_add_u32 s50, s50, 32768\n"
"ds_read_b64_tr_b16 a[160:161], v218 offset:40976\n"
"v_fma_mix_f32 v188, v54, v148, v156 op_sel:[0,0,0] op_sel_hi:[1,0,0]\n"
"v_mfma_f32_16x16x32_bf16 a[24:27], a[188:191], v[40:43], a[24:27]\n"
"ds_read_b64_tr_b16 a[162:163], v218 offset:42000\n"
"v_fma_mix_f32 v189, v54, v149, v157 op_sel:[1,0,0] op_sel_hi:[1,0,0]\n"
"v_mfma_f32_16x16x32_bf16 a[28:31], a[184:187], v[36:39], a[28:31]\n"
"ds_read_b64_tr_b16 a[164:165], v218 offset:45072\n"
"v_fma_mix_f32 v190, v55, v150, v158 op_sel:[0,0,0] op_sel_hi:[1,0,0]\n"
"v_mfma_f32_16x16x32_bf16 a[28:31], a[188:191], v[44:47], a[28:31]\n"
"s_waitcnt lgkmcnt(11)\n"
"ds_read_b64_tr_b16 a[166:167], v218 offset:46096\n"
"v_fma_mix_f32 v191, v55, v151, v159 op_sel:[1,0,0] op_sel_hi:[1,0,0]\n"
"v_mfma_f32_16x16x32_bf16 a[32:35], a[224:227], v[32:35], a[32:35]\n"
"s_add_u32 m0, s51, 1024\n"
"ds_read_b64_tr_b16 a[168:169], v218 offset:40984\n"
"ds_read_b128 v[144:147], v224 offset:2048\n"
"v_mfma_f32_16x16x32_bf16 a[32:35], a[228:231], v[40:43], a[32:35]\n"
"ds_read_b64_tr_b16 a[170:171], v218 offset:42008\n"
"buffer_load_dwordx4 v217, s[20:23], s50 offen offset:0 lds\n"
"v_mfma_f32_16x16x32_bf16 a[36:39], a[224:227], v[36:39], a[36:39]\n"
"ds_read_b64_tr_b16 a[172:173], v218 offset:45080\n"
"v_cvt_pk_bf16_f32 v52, v184, v185\n"
"v_mfma_f32_16x16x32_bf16 a[36:39], a[228:231], v[44:47], a[36:39]\n"
"ds_read_b64_tr_b16 a[174:175], v218 offset:46104\n"
"ds_read_b128 v[148:151], v224 offset:2112\n"
"v_mfma_f32_16x16x32_bf16 a[40:43], a[232:235], v[32:35], a[40:43]\n"
"ds_read_b64_tr_b16 a[176:177], v218 offset:41232\n"
"v_cvt_pk_bf16_f32 v53, v186, v187\n"
"v_mfma_f32_16x16x32_bf16 a[40:43], a[236:239], v[40:43], a[40:43]\n"
"ds_read_b64_tr_b16 a[178:179], v218 offset:42256\n"
"v_cvt_pk_bf16_f32 v54, v188, v189\n"
"v_mfma_f32_16x16x32_bf16 a[44:47], a[232:235], v[36:39], a[44:47]\n"
"s_add_u32 m0, s51, 8960\n"
"ds_read_b64_tr_b16 a[180:181], v218 offset:45328\n"
"ds_read_b128 v[152:155], v224 offset:2176\n"
"v_mfma_f32_16x16x32_bf16 a[44:47], a[236:239], v[44:47], a[44:47]\n"
"s_waitcnt lgkmcnt(14)\n"
"ds_read_b64_tr_b16 a[182:183], v218 offset:46352\n"
"buffer_load_dwordx4 v217, s[20:23], s50 offen offset:256 lds\n"
"v_mfma_f32_16x16x32_bf16 a[48:51], a[240:243], v[32:35], a[48:51]\n"
"s_add_u32 s50, s50, 32768\n"
"ds_read_b64_tr_b16 a[184:185], v218 offset:41240\n"
"v_cvt_pk_bf16_f32 v55, v190, v191\n"
"v_mfma_f32_16x16x32_bf16 a[48:51], a[244:247], v[40:43], a[48:51]\n"
"ds_read_b64_tr_b16 a[186:187], v218 offset:42264\n"
"ds_read_b128 v[156:159], v224 offset:2240\n"
"v_mfma_f32_16x16x32_bf16 a[52:55], a[240:243], v[36:39], a[52:55]\n"
"ds_read_b64_tr_b16 a[188:189], v218 offset:45336\n"
"v_fma_mix_f32 v176, v56, v160, v168 op_sel:[0,0,0] op_sel_hi:[1,0,0]\n"
"v_mfma_f32_16x16x32_bf16 a[52:55], a[244:247], v[44:47], a[52:55]\n"
"ds_read_b64_tr_b16 a[190:191], v218 offset:46360\n"
"v_fma_mix_f32 v177, v56, v161, v169 op_sel:[1,0,0] op_sel_hi:[1,0,0]\n"
"v_mfma_f32_16x16x32_bf16 a[56:59], a[248:251], v[32:35], a[56:59]\n"
"s_add_u32 m0, s51, 16392\n"
"ds_read_b64_tr_b16 a[224:225], v218 offset:41488\n"
"v_fma_mix_f32 v178, v57, v162, v170 op_sel:[0,0,0] op_sel_hi:[1,0,0]\n"
"v_mfma_f32_16x16x32_bf16 a[56:59], a[252:255], v[40:43], a[56:59]\n"
"ds_read_b64_tr_b16 a[226:227], v218 offset:42512\n"
"buffer_load_dwordx4 v217, s[20:23], s50 offen offset:0 lds\n"
"v_mfma_f32_16x16x32_bf16 a[60:63], a[248:251], v[36:39], a[60:63]\n"
"ds_read_b64_tr_b16 a[228:229], v218 offset:45584\n"
"v_fma_mix_f32 v179, v57, v163, v171 op_sel:[1,0,0] op_sel_hi:[1,0,0]\n"
"v_mfma_f32_16x16x32_bf16 a[60:63], a[252:255], v[44:47], a[60:63]\n"
"s_waitcnt lgkmcnt(13)\n"
"ds_read_b64_tr_b16 a[230:231], v218 offset:46608\n"
"v_fma_mix_f32 v180, v58, v164, v172 op_sel:[0,0,0] op_sel_hi:[1,0,0]\n"
"v_mfma_f32_16x16x32_bf16 a[64:67], a[160:163], v[32:35], a[64:67]\n"
"ds_read_b64_tr_b16 a[232:233], v218 offset:41496\n"
"v_fma_mix_f32 v181, v58, v165, v173 op_sel:[1,0,0] op_sel_hi:[1,0,0]\n"
"v_mfma_f32_16x16x32_bf16 a[64:67], a[164:167], v[40:43], a[64:67]\n"
"ds_read_b64_tr_b16 a[234:235], v218 offset:42520\n"
"v_fma_mix_f32 v182, v59, v166, v174 op_sel:[0,0,0] op_sel_hi:[1,0,0]\n"
"v_mfma_f32_16x16x32_bf16 a[68:71], a[160:163], v[36:39], a[68:71]\n"
"s_add_u32 m0, s51, 24328\n"
"ds_read_b64_tr_b16 a[236:237], v218 offset:45592\n"
"v_fma_mix_f32 v183, v59, v167, v175 op_sel:[1,0,0] op_sel_hi:[1,0,0]\n"
"v_mfma_f32_16x16x32_bf16 a[68:71], a[164:167], v[44:47], a[68:71]\n"
"ds_read_b64_tr_b16 a[238:239], v218 offset:46616\n"
"buffer_load_dwordx4 v217, s[20:23], s50 offen offset:256 lds\n"
"v_mfma_f32_16x16x32_bf16 a[72:75], a[168:171], v[32:35], a[72:75]\n"
"s_add_u32 s50, s50, 32768\n"
"ds_read_b64_tr_b16 a[240:241], v218 offset:41744\n"
"v_cvt_pk_bf16_f32 v56, v176, v177\n"
"v_mfma_f32_16x16x32_bf16 a[72:75], a[172:175], v[40:43], a[72:75]\n"
"ds_read_b64_tr_b16 a[242:243], v218 offset:42768\n"
"v_cvt_pk_bf16_f32 v57, v178, v179\n"
"v_mfma_f32_16x16x32_bf16 a[76:79], a[168:171], v[36:39], a[76:79]\n"
"ds_read_b64_tr_b16 a[244:245], v218 offset:45840\n"
"v_cvt_pk_bf16_f32 v58, v180, v181\n"
"v_mfma_f32_16x16x32_bf16 a[76:79], a[172:175], v[44:47], a[76:79]\n"
"s_waitcnt lgkmcnt(11)\n"
"ds_read_b64_tr_b16 a[246:247], v218 offset:46864\n"
"v_cvt_pk_bf16_f32 v59, v182, v183\n"
"v_mfma_f32_16x16x32_bf16 a[80:83], a[176:179], v[32:35], a[80:83]\n"
"s_add_u32 m0, s51, 17416\n"
"ds_read_b64_tr_b16 a[248:249], v218 offset:41752\n"
"v_fma_mix_f32 v184, v60, v160, v168 op_sel:[0,0,0] op_sel_hi:[1,0,0]\n"
"v_mfma_f32_16x16x32_bf16 a[80:83], a[180:183], v[40:43], a[80:83]\n"
"ds_read_b64_tr_b16 a[250:251], v218 offset:42776\n"
"buffer_load_dwordx4 v217, s[20:23], s50 offen offset:0 lds\n"
"v_mfma_f32_16x16x32_bf16 a[84:87], a[176:179], v[36:39], a[84:87]\n"
"ds_read_b64_tr_b16 a[252:253], v218 offset:45848\n"
"v_fma_mix_f32 v185, v60, v161, v169 op_sel:[1,0,0] op_sel_hi:[1,0,0]\n"
"v_mfma_f32_16x16x32_bf16 a[84:87], a[180:183], v[44:47], a[84:87]\n"
"ds_read_b64_tr_b16 a[254:255], v218 offset:46872\n"
"v_fma_mix_f32 v186, v61, v162, v170 op_sel:[0,0,0] op_sel_hi:[1,0,0]\n"
"v_mfma_f32_16x16x32_bf16 a[88:91], a[184:187], v[32:35], a[88:91]\n"
"ds_read_b64_tr_b16 a[160:161], v219 offset:0\n"
"v_fma_mix_f32 v187, v61, v163, v171 op_sel:[1,0,0] op_sel_hi:[1,0,0]\n"
"v_fma_mix_f32 v188, v62, v164, v172 op_sel:[0,0,0] op_sel_hi:[1,0,0]\n"
"v_mfma_f32_16x16x32_bf16 a[88:91], a[188:191], v[40:43], a[88:91]\n"
"ds_read_b64_tr_b16 a[162:163], v219 offset:1024\n"
"v_fma_mix_f32 v189, v62, v165, v173 op_sel:[1,0,0] op_sel_hi:[1,0,0]\n"
"v_mfma_f32_16x16x32_bf16 a[92:95], a[184:187], v[36:39], a[92:95]\n"
"s_add_u32 m0, s51, 25352\n"
"ds_read_b64_tr_b16 a[164:165], v219 offset:4096\n"
"v_fma_mix_f32 v190, v63, v166, v174 op_sel:[0,0,0] op_sel_hi:[1,0,0]\n"
"v_mfma_f32_16x16x32_bf16 a[92:95], a[188:191], v[44:47], a[92:95]\n"
"s_waitcnt lgkmcnt(11)\n"
"buffer_load_dwordx4 v217, s[20:23], s50 offen offset:256 lds\n"
"ds_read_b64_tr_b16 a[166:167], v219 offset:5120\n"
"v_mfma_f32_16x16x32_bf16 a[96:99], a[224:227], v[32:35], a[96:99]\n"
"s_add_u32 s50, s50, 425984\n"
"ds_read_b64_tr_b16 a[168:169], v219 offset:8\n"
"v_fma_mix_f32 v191, v63, v167, v175 op_sel:[1,0,0] op_sel_hi:[1,0,0]\n"
"v_mfma_f32_16x16x32_bf16 a[96:99], a[228:231], v[40:43], a[96:99]\n"
"ds_read_b64_tr_b16 a[170:171], v219 offset:1032\n"
"ds_read_b128 v[160:163], v224 offset:2304\n"
"v_mfma_f32_16x16x32_bf16 a[100:103], a[224:227], v[36:39], a[100:103]\n"
"ds_read_b64_tr_b16 a[172:173], v219 offset:4104\n"
"v_cvt_pk_bf16_f32 v60, v184, v185\n"
"v_mfma_f32_16x16x32_bf16 a[100:103], a[228:231], v[44:47], a[100:103]\n"
"ds_read_b64_tr_b16 a[174:175], v219 offset:5128\n"
"v_cvt_pk_bf16_f32 v61, v186, v187\n"
"v_mfma_f32_16x16x32_bf16 a[104:107], a[232:235], v[32:35], a[104:107]\n"
"ds_read_b64_tr_b16 a[176:177], v219 offset:256\n"
"ds_read_b128 v[164:167], v224 offset:2368\n"
"v_mfma_f32_16x16x32_bf16 a[104:107], a[236:239], v[40:43], a[104:107]\n"
"ds_read_b64_tr_b16 a[178:179], v219 offset:1280\n"
"v_cvt_pk_bf16_f32 v62, v188, v189\n"
"v_mfma_f32_16x16x32_bf16 a[108:111], a[232:235], v[36:39], a[108:111]\n"
"ds_read_b64_tr_b16 a[180:181], v219 offset:4352\n"
"v_cvt_pk_bf16_f32 v63, v190, v191\n"
"v_mfma_f32_16x16x32_bf16 a[108:111], a[236:239], v[44:47], a[108:111]\n"
"s_waitcnt lgkmcnt(13)\n"
"ds_read_b64_tr_b16 a[182:183], v219 offset:5376\n"
"ds_read_b128 v[168:171], v224 offset:2432\n"
"v_mfma_f32_16x16x32_bf16 a[112:115], a[240:243], v[32:35], a[112:115]\n"
"ds_read_b64_tr_b16 a[184:185], v219 offset:264\n"
"v_fma_mix_f32 v176, v64, v144, v152 op_sel:[0,0,0] op_sel_hi:[1,0,0]\n"
"v_mfma_f32_16x16x32_bf16 a[112:115], a[244:247], v[40:43], a[112:115]\n"
"ds_read_b64_tr_b16 a[186:187], v219 offset:1288\n"
"v_fma_mix_f32 v177, v64, v145, v153 op_sel:[1,0,0] op_sel_hi:[1,0,0]\n"
"v_mfma_f32_16x16x32_bf16 a[116:119], a[240:243], v[36:39], a[116:119]\n"
"ds_read_b64_tr_b16 a[188:189], v219 offset:4360\n"
"ds_read_b128 v[172:175], v224 offset:2496\n"
"v_mfma_f32_16x16x32_bf16 a[116:119], a[244:247], v[44:47], a[116:119]\n"
"ds_read_b64_tr_b16 a[190:191], v219 offset:5384\n"
"v_fma_mix_f32 v178, v65, v146, v154 op_sel:[0,0,0] op_sel_hi:[1,0,0]\n"
"v_mfma_f32_16x16x32_bf16 a[120:123], a[248:251], v[32:35], a[120:123]\n"
"ds_read_b64_tr_b16 a[224:225], v219 offset:512\n"
"v_fma_mix_f32 v179, v65, v147, v155 op_sel:[1,0,0] op_sel_hi:[1,0,0]\n"
"v_mfma_f32_16x16x32_bf16 a[120:123], a[252:255], v[40:43], a[120:123]\n"
"ds_read_b64_tr_b16 a[226:227], v219 offset:1536\n"
"v_fma_mix_f32 v180, v66, v148, v156 op_sel:[0,0,0] op_sel_hi:[1,0,0]\n"
"v_mfma_f32_16x16x32_bf16 a[124:127], a[248:251], v[36:39], a[124:127]\n"
"ds_read_b64_tr_b16 a[228:229], v219 offset:4608\n"
"v_fma_mix_f32 v181, v66, v149, v157 op_sel:[1,0,0] op_sel_hi:[1,0,0]\n"
"v_mfma_f32_16x16x32_bf16 a[124:127], a[252:255], v[44:47], a[124:127]\n"
"s_waitcnt lgkmcnt(14)\n"
"ds_read_b64_tr_b16 a[230:231], v219 offset:5632\n"
"v_fma_mix_f32 v182, v67, v150, v158 op_sel:[0,0,0] op_sel_hi:[1,0,0]\n"
"v_mfma_f32_16x16x32_bf16 a[0:3], a[160:163], v[48:51], a[0:3]\n"
"ds_read_b64_tr_b16 a[232:233], v219 offset:520\n"
"v_fma_mix_f32 v183, v67, v151, v159 op_sel:[1,0,0] op_sel_hi:[1,0,0]\n"
"v_mfma_f32_16x16x32_bf16 a[0:3], a[164:167], v[56:59], a[0:3]\n"
"ds_read_b64_tr_b16 a[234:235], v219 offset:1544\n"
"v_cvt_pk_bf16_f32 v64, v176, v177\n"
"v_mfma_f32_16x16x32_bf16 a[4:7], a[160:163], v[52:55], a[4:7]\n"
"s_waitcnt vmcnt(8)\n"
"s_barrier\n"
"ds_read_b64_tr_b16 a[236:237], v219 offset:4616\n"
"v_cvt_pk_bf16_f32 v65, v178, v179\n"
"v_mfma_f32_16x16x32_bf16 a[4:7], a[164:167], v[60:63], a[4:7]\n"
"ds_read_b64_tr_b16 a[238:239], v219 offset:5640\n"
"v_cvt_pk_bf16_f32 v66, v180, v181\n"
"v_mfma_f32_16x16x32_bf16 a[8:11], a[168:171], v[48:51], a[8:11]\n"
"s_add_u32 m0, s51, 32784\n"
"ds_read_b64_tr_b16 a[240:241], v219 offset:768\n"
"v_cvt_pk_bf16_f32 v67, v182, v183\n"
"v_mfma_f32_16x16x32_bf16 a[8:11], a[172:175], v[56:59], a[8:11]\n"
"ds_read_b64_tr_b16 a[242:243], v219 offset:1792\n"
"buffer_load_dwordx4 v217, s[20:23], s50 offen offset:0 lds\n"
"v_mfma_f32_16x16x32_bf16 a[12:15], a[168:171], v[52:55], a[12:15]\n"
"ds_read_b64_tr_b16 a[244:245], v219 offset:4864\n"
"v_fma_mix_f32 v184, v68, v144, v152 op_sel:[0,0,0] op_sel_hi:[1,0,0]\n"
"v_mfma_f32_16x16x32_bf16 a[12:15], a[172:175], v[60:63], a[12:15]\n"
"s_waitcnt lgkmcnt(11)\n"
"ds_read_b64_tr_b16 a[246:247], v219 offset:5888\n"
"v_fma_mix_f32 v185, v68, v145, v153 op_sel:[1,0,0] op_sel_hi:[1,0,0]\n"
"v_mfma_f32_16x16x32_bf16 a[16:19], a[176:179], v[48:51], a[16:19]\n"
"ds_read_b64_tr_b16 a[248:249], v219 offset:776\n"
"v_fma_mix_f32 v186, v69, v146, v154 op_sel:[0,0,0] op_sel_hi:[1,0,0]\n"
"v_mfma_f32_16x16x32_bf16 a[16:19], a[180:183], v[56:59], a[16:19]\n"
"ds_read_b64_tr_b16 a[250:251], v219 offset:1800\n"
"v_fma_mix_f32 v187, v69, v147, v155 op_sel:[1,0,0] op_sel_hi:[1,0,0]\n"
"v_mfma_f32_16x16x32_bf16 a[20:23], a[176:179], v[52:55], a[20:23]\n"
"s_add_u32 m0, s51, 40720\n"
"ds_read_b64_tr_b16 a[252:253], v219 offset:4872\n"
"v_fma_mix_f32 v188, v70, v148, v156 op_sel:[0,0,0] op_sel_hi:[1,0,0]\n"
"v_mfma_f32_16x16x32_bf16 a[20:23], a[180:183], v[60:63], a[20:23]\n"
"ds_read_b64_tr_b16 a[254:255], v219 offset:5896\n"
"buffer_load_dwordx4 v217, s[20:23], s50 offen offset:256 lds\n"
"v_mfma_f32_16x16x32_bf16 a[24:27], a[184:187], v[48:51], a[24:27]\n"
"s_add_u32 s50, s50, 32768\n"
"ds_read_b64_tr_b16 a[160:161], v219 offset:8192\n"
"v_fma_mix_f32 v189, v70, v149, v157 op_sel:[1,0,0] op_sel_hi:[1,0,0]\n"
"v_mfma_f32_16x16x32_bf16 a[24:27], a[188:191], v[56:59], a[24:27]\n"
"ds_read_b64_tr_b16 a[162:163], v219 offset:9216\n"
"v_fma_mix_f32 v190, v71, v150, v158 op_sel:[0,0,0] op_sel_hi:[1,0,0]\n"
"v_mfma_f32_16x16x32_bf16 a[28:31], a[184:187], v[52:55], a[28:31]\n"
"ds_read_b64_tr_b16 a[164:165], v219 offset:12288\n"
"v_fma_mix_f32 v191, v71, v151, v159 op_sel:[1,0,0] op_sel_hi:[1,0,0]\n"
"v_mfma_f32_16x16x32_bf16 a[28:31], a[188:191], v[60:63], a[28:31]\n"
"s_waitcnt lgkmcnt(11)\n"
"ds_read_b64_tr_b16 a[166:167], v219 offset:13312\n"
"ds_read_b128 v[144:147], v224 offset:2560\n"
"v_mfma_f32_16x16x32_bf16 a[32:35], a[224:227], v[48:51], a[32:35]\n"
"s_add_u32 m0, s51, 33808\n"
"ds_read_b64_tr_b16 a[168:169], v219 offset:8200\n"
"v_cvt_pk_bf16_f32 v68, v184, v185\n"
"v_mfma_f32_16x16x32_bf16 a[32:35], a[228:231], v[56:59], a[32:35]\n"
"ds_read_b64_tr_b16 a[170:171], v219 offset:9224\n"
"buffer_load_dwordx4 v217, s[20:23], s50 offen offset:0 lds\n"
"v_mfma_f32_16x16x32_bf16 a[36:39], a[224:227], v[52:55], a[36:39]\n"
"ds_read_b64_tr_b16 a[172:173], v219 offset:12296\n"
"ds_read_b128 v[148:151], v224 offset:2624\n"
"v_mfma_f32_16x16x32_bf16 a[36:39], a[228:231], v[60:63], a[36:39]\n"
"ds_read_b64_tr_b16 a[174:175], v219 offset:13320\n"
"v_cvt_pk_bf16_f32 v69, v186, v187\n"
"v_mfma_f32_16x16x32_bf16 a[40:43], a[232:235], v[48:51], a[40:43]\n"
"ds_read_b64_tr_b16 a[176:177], v219 offset:8448\n"
"v_cvt_pk_bf16_f32 v70, v188, v189\n"
"v_mfma_f32_16x16x32_bf16 a[40:43], a[236:239], v[56:59], a[40:43]\n"
"ds_read_b64_tr_b16 a[178:179], v219 offset:9472\n"
"ds_read_b128 v[152:155], v224 offset:2688\n"
"v_mfma_f32_16x16x32_bf16 a[44:47], a[232:235], v[52:55], a[44:47]\n"
"s_add_u32 m0, s51, 41744\n"
"ds_read_b64_tr_b16 a[180:181], v219 offset:12544\n"
"v_cvt_pk_bf16_f32 v71, v190, v191\n"
"v_mfma_f32_16x16x32_bf16 a[44:47], a[236:239], v[60:63], a[44:47]\n"
"s_waitcnt lgkmcnt(14)\n"
"ds_read_b64_tr_b16 a[182:183], v219 offset:13568\n"
"buffer_load_dwordx4 v217, s[20:23], s50 offen offset:256 lds\n"
"v_mfma_f32_16x16x32_bf16 a[48:51], a[240:243], v[48:51], a[48:51]\n"
"s_add_u32 s50, s50, 32768\n"
"ds_read_b64_tr_b16 a[184:185], v219 offset:8456\n"
"ds_read_b128 v[156:159], v224 offset:2752\n"
"v_mfma_f32_16x16x32_bf16 a[48:51], a[244:247], v[56:59], a[48:51]\n"
"ds_read_b64_tr_b16 a[186:187], v219 offset:9480\n"
"v_fma_mix_f32 v176, v72, v160, v168 op_sel:[0,0,0] op_sel_hi:[1,0,0]\n"
"v_mfma_f32_16x16x32_bf16 a[52:55], a[240:243], v[52:55], a[52:55]\n"
"ds_read_b64_tr_b16 a[188:189], v219 offset:12552\n"
"v_fma_mix_f32 v177, v72, v161, v169 op_sel:[1,0,0] op_sel_hi:[1,0,0]\n"
"v_mfma_f32_16x16x32_bf16 a[52:55], a[244:247], v[60:63], a[52:55]\n"
"ds_read_b64_tr_b16 a[190:191], v219 offset:13576\n"
"v_fma_mix_f32 v178, v73, v162, v170 op_sel:[0,0,0] op_sel_hi:[1,0,0]\n"
"v_mfma_f32_16x16x32_bf16 a[56:59], a[248:251], v[48:51], a[56:59]\n"
"s_add_u32 m0, s51, 49176\n"
"ds_read_b64_tr_b16 a[224:225], v219 offset:8704\n"
"v_fma_mix_f32 v179, v73, v163, v171 op_sel:[1,0,0] op_sel_hi:[1,0,0]\n"
"v_mfma_f32_16x16x32_bf16 a[56:59], a[252:255], v[56:59], a[56:59]\n"
"ds_read_b64_tr_b16 a[226:227], v219 offset:9728\n"
"buffer_load_dwordx4 v217, s[20:23], s50 offen offset:0 lds\n"
"v_mfma_f32_16x16x32_bf16 a[60:63], a[248:251], v[52:55], a[60:63]\n"
"ds_read_b64_tr_b16 a[228:229], v219 offset:12800\n"
"v_fma_mix_f32 v180, v74, v164, v172 op_sel:[0,0,0] op_sel_hi:[1,0,0]\n"
"v_mfma_f32_16x16x32_bf16 a[60:63], a[252:255], v[60:63], a[60:63]\n"
"s_waitcnt lgkmcnt(13)\n"
"ds_read_b64_tr_b16 a[230:231], v219 offset:13824\n"
"v_fma_mix_f32 v181, v74, v165, v173 op_sel:[1,0,0] op_sel_hi:[1,0,0]\n"
"v_mfma_f32_16x16x32_bf16 a[64:67], a[160:163], v[48:51], a[64:67]\n"
"ds_read_b64_tr_b16 a[232:233], v219 offset:8712\n"
"v_fma_mix_f32 v182, v75, v166, v174 op_sel:[0,0,0] op_sel_hi:[1,0,0]\n"
"v_mfma_f32_16x16x32_bf16 a[64:67], a[164:167], v[56:59], a[64:67]\n"
"ds_read_b64_tr_b16 a[234:235], v219 offset:9736\n"
"v_fma_mix_f32 v183, v75, v167, v175 op_sel:[1,0,0] op_sel_hi:[1,0,0]\n"
"v_mfma_f32_16x16x32_bf16 a[68:71], a[160:163], v[52:55], a[68:71]\n"
"s_add_u32 m0, s51, 57112\n"
"ds_read_b64_tr_b16 a[236:237], v219 offset:12808\n"
"v_cvt_pk_bf16_f32 v72, v176, v177\n"
"v_mfma_f32_16x16x32_bf16 a[68:71], a[164:167], v[60:63], a[68:71]\n"
"ds_read_b64_tr_b16 a[238:239], v219 offset:13832\n"
"buffer_load_dwordx4 v217, s[20:23], s50 offen offset:256 lds\n"
"v_mfma_f32_16x16x32_bf16 a[72:75], a[168:171], v[48:51], a[72:75]\n"
"s_add_u32 s50, s50, 32768\n"
"ds_read_b64_tr_b16 a[240:241], v219 offset:8960\n"
"v_cvt_pk_bf16_f32 v73, v178, v179\n"
"v_mfma_f32_16x16x32_bf16 a[72:75], a[172:175], v[56:59], a[72:75]\n"
"ds_read_b64_tr_b16 a[242:243], v219 offset:9984\n"
"v_cvt_pk_bf16_f32 v74, v180, v181\n"
"v_mfma_f32_16x16x32_bf16 a[76:79], a[168:171], v[52:55], a[76:79]\n"
"ds_read_b64_tr_b16 a[244:245], v219 offset:13056\n"
"v_cvt_pk_bf16_f32 v75, v182, v183\n"
"v_mfma_f32_16x16x32_bf16 a[76:79], a[172:175], v[60:63], a[76:79]\n"
"s_waitcnt lgkmcnt(11)\n"
"ds_read_b64_tr_b16 a[246:247], v219 offset:14080\n"
"v_fma_mix_f32 v184, v76, v160, v168 op_sel:[0,0,0] op_sel_hi:[1,0,0]\n"
"v_mfma_f32_16x16x32_bf16 a[80:83], a[176:179], v[48:51], a[80:83]\n"
"s_add_u32 m0, s51, 50200\n"
"ds_read_b64_tr_b16 a[248:249], v219 offset:8968\n"
"v_fma_mix_f32 v185, v76, v161, v169 op_sel:[1,0,0] op_sel_hi:[1,0,0]\n"
"v_mfma_f32_16x16x32_bf16 a[80:83], a[180:183], v[56:59], a[80:83]\n"
"ds_read_b64_tr_b16 a[250:251], v219 offset:9992\n"
"buffer_load_dwordx4 v217, s[20:23], s50 offen offset:0 lds\n"
"v_mfma_f32_16x16x32_bf16 a[84:87], a[176:179], v[52:55], a[84:87]\n"
"ds_read_b64_tr_b16 a[252:253], v219 offset:13064\n"
"v_fma_mix_f32 v186, v77, v162, v170 op_sel:[0,0,0] op_sel_hi:[1,0,0]\n"
"v_mfma_f32_16x16x32_bf16 a[84:87], a[180:183], v[60:63], a[84:87]\n"
"ds_read_b64_tr_b16 a[254:255], v219 offset:14088\n"
"v_fma_mix_f32 v187, v77, v163, v171 op_sel:[1,0,0] op_sel_hi:[1,0,0]\n"
"v_mfma_f32_16x16x32_bf16 a[88:91], a[184:187], v[48:51], a[88:91]\n"
"ds_read_b64_tr_b16 a[160:161], v219 offset:32784\n"
"v_fma_mix_f32 v188, v78, v164, v172 op_sel:[0,0,0] op_sel_hi:[1,0,0]\n"
"v_fma_mix_f32 v189, v78, v165, v173 op_sel:[1,0,0] op_sel_hi:[1,0,0]\n"
"v_mfma_f32_16x16x32_bf16 a[88:91], a[188:191], v[56:59], a[88:91]\n"
"ds_read_b64_tr_b16 a[162:163], v219 offset:33808\n"
"v_fma_mix_f32 v190, v79, v166, v174 op_sel:[0,0,0] op_sel_hi:[1,0,0]\n"
"v_mfma_f32_16x16x32_bf16 a[92:95], a[184:187], v[52:55], a[92:95]\n"
"s_add_u32 m0, s51, 58136\n"
"ds_read_b64_tr_b16 a[164:165], v219 offset:36880\n"
"v_fma_mix_f32 v191, v79, v167, v175 op_sel:[1,0,0] op_sel_hi:[1,0,0]\n"
"v_mfma_f32_16x16x32_bf16 a[92:95], a[188:191], v[60:63], a[92:95]\n"
"s_waitcnt lgkmcnt(11)\n"
"buffer_load_dwordx4 v217, s[20:23], s50 offen offset:256 lds\n"
"ds_read_b64_tr_b16 a[166:167], v219 offset:37904\n"
"v_mfma_f32_16x16x32_bf16 a[96:99], a[224:227], v[48:51], a[96:99]\n"
"s_add_u32 s50, s50, 425984\n"
"ds_read_b64_tr_b16 a[168:169], v219 offset:32792\n"
"ds_read_b128 v[160:163], v224 offset:2816\n"
"v_mfma_f32_16x16x32_bf16 a[96:99], a[228:231], v[56:59], a[96:99]\n"
"ds_read_b64_tr_b16 a[170:171], v219 offset:33816\n"
"v_cvt_pk_bf16_f32 v76, v184, v185\n"
"v_mfma_f32_16x16x32_bf16 a[100:103], a[224:227], v[52:55], a[100:103]\n"
"ds_read_b64_tr_b16 a[172:173], v219 offset:36888\n"
"v_cvt_pk_bf16_f32 v77, v186, v187\n"
"v_mfma_f32_16x16x32_bf16 a[100:103], a[228:231], v[60:63], a[100:103]\n"
"ds_read_b64_tr_b16 a[174:175], v219 offset:37912\n"
"ds_read_b128 v[164:167], v224 offset:2880\n"
"v_mfma_f32_16x16x32_bf16 a[104:107], a[232:235], v[48:51], a[104:107]\n"
"ds_read_b64_tr_b16 a[176:177], v219 offset:33040\n"
"v_cvt_pk_bf16_f32 v78, v188, v189\n"
"v_mfma_f32_16x16x32_bf16 a[104:107], a[236:239], v[56:59], a[104:107]\n"
"ds_read_b64_tr_b16 a[178:179], v219 offset:34064\n"
"v_cvt_pk_bf16_f32 v79, v190, v191\n"
"v_mfma_f32_16x16x32_bf16 a[108:111], a[232:235], v[52:55], a[108:111]\n"
"ds_read_b64_tr_b16 a[180:181], v219 offset:37136\n"
"ds_read_b128 v[168:171], v224 offset:2944\n"
"v_mfma_f32_16x16x32_bf16 a[108:111], a[236:239], v[60:63], a[108:111]\n"
"s_waitcnt lgkmcnt(14)\n"
"ds_read_b64_tr_b16 a[182:183], v219 offset:38160\n"
"v_fma_mix_f32 v176, v80, v144, v152 op_sel:[0,0,0] op_sel_hi:[1,0,0]\n"
"v_mfma_f32_16x16x32_bf16 a[112:115], a[240:243], v[48:51], a[112:115]\n"
"ds_read_b64_tr_b16 a[184:185], v219 offset:33048\n"
"v_fma_mix_f32 v177, v80, v145, v153 op_sel:[1,0,0] op_sel_hi:[1,0,0]\n"
"v_mfma_f32_16x16x32_bf16 a[112:115], a[244:247], v[56:59], a[112:115]\n"
"ds_read_b64_tr_b16 a[186:187], v219 offset:34072\n"
"ds_read_b128 v[172:175], v224 offset:3008\n"
"v_mfma_f32_16x16x32_bf16 a[116:119], a[240:243], v[52:55], a[116:119]\n"
"ds_read_b64_tr_b16 a[188:189], v219 offset:37144\n"
"v_fma_mix_f32 v178, v81, v146, v154 op_sel:[0,0,0] op_sel_hi:[1,0,0]\n"
"v_mfma_f32_16x16x32_bf16 a[116:119], a[244:247], v[60:63], a[116:119]\n"
"ds_read_b64_tr_b16 a[190:191], v219 offset:38168\n"
"v_fma_mix_f32 v179, v81, v147, v155 op_sel:[1,0,0] op_sel_hi:[1,0,0]\n"
"v_mfma_f32_16x16x32_bf16 a[120:123], a[248:251], v[48:51], a[120:123]\n"
"ds_read_b64_tr_b16 a[224:225], v219 offset:33296\n"
"v_fma_mix_f32 v180, v82, v148, v156 op_sel:[0,0,0] op_sel_hi:[1,0,0]\n"
"v_mfma_f32_16x16x32_bf16 a[120:123], a[252:255], v[56:59], a[120:123]\n"
"ds_read_b64_tr_b16 a[226:227], v219 offset:34320\n"
"v_fma_mix_f32 v181, v82, v149, v157 op_sel:[1,0,0] op_sel_hi:[1,0,0]\n"
"v_mfma_f32_16x16x32_bf16 a[124:127], a[248:251], v[52:55], a[124:127]\n"
"ds_read_b64_tr_b16 a[228:229], v219 offset:37392\n"
"v_fma_mix_f32 v182, v83, v150, v158 op_sel:[0,0,0] op_sel_hi:[1,0,0]\n"
"v_mfma_f32_16x16x32_bf16 a[124:127], a[252:255], v[60:63], a[124:127]\n"
"s_waitcnt lgkmcnt(13)\n"
"ds_read_b64_tr_b16 a[230:231], v219 offset:38416\n"
"v_fma_mix_f32 v183, v83, v151, v159 op_sel:[1,0,0] op_sel_hi:[1,0,0]\n"
"v_mfma_f32_16x16x32_bf16 a[0:3], a[160:163], v[64:67], a[0:3]\n"
"ds_read_b64_tr_b16 a[232:233], v219 offset:33304\n"
"v_cvt_pk_bf16_f32 v80, v176, v177\n"
"v_mfma_f32_16x16x32_bf16 a[0:3], a[164:167], v[72:75], a[0:3]\n"
"ds_read_b64_tr_b16 a[234:235], v219 offset:34328\n"
"v_cvt_pk_bf16_f32 v81, v178, v179\n"
"v_mfma_f32_16x16x32_bf16 a[4:7], a[160:163], v[68:71], a[4:7]\n"
"s_waitcnt vmcnt(8)\n"
"s_barrier\n"
"ds_read_b64_tr_b16 a[236:237], v219 offset:37400\n"
"v_cvt_pk_bf16_f32 v82, v180, v181\n"
"v_mfma_f32_16x16x32_bf16 a[4:7], a[164:167], v[76:79], a[4:7]\n"
"ds_read_b64_tr_b16 a[238:239], v219 offset:38424\n"
"v_cvt_pk_bf16_f32 v83, v182, v183\n"
"v_mfma_f32_16x16x32_bf16 a[8:11], a[168:171], v[64:67], a[8:11]\n"
"s_add_u32 m0, s51, 65568\n"
"ds_read_b64_tr_b16 a[240:241], v219 offset:33552\n"
"v_fma_mix_f32 v184, v84, v144, v152 op_sel:[0,0,0] op_sel_hi:[1,0,0]\n"
"v_mfma_f32_16x16x32_bf16 a[8:11], a[172:175], v[72:75], a[8:11]\n"
"ds_read_b64_tr_b16 a[242:243], v219 offset:34576\n"
"buffer_load_dwordx4 v217, s[20:23], s50 offen offset:0 lds\n"
"v_mfma_f32_16x16x32_bf16 a[12:15], a[168:171], v[68:71], a[12:15]\n"
"ds_read_b64_tr_b16 a[244:245], v219 offset:37648\n"
"v_fma_mix_f32 v185, v84, v145, v153 op_sel:[1,0,0] op_sel_hi:[1,0,0]\n"
"v_mfma_f32_16x16x32_bf16 a[12:15], a[172:175], v[76:79], a[12:15]\n"
"s_waitcnt lgkmcnt(11)\n"
"ds_read_b64_tr_b16 a[246:247], v219 offset:38672\n"
"v_fma_mix_f32 v186, v85, v146, v154 op_sel:[0,0,0] op_sel_hi:[1,0,0]\n"
"v_mfma_f32_16x16x32_bf16 a[16:19], a[176:179], v[64:67], a[16:19]\n"
"ds_read_b64_tr_b16 a[248:249], v219 offset:33560\n"
"v_fma_mix_f32 v187, v85, v147, v155 op_sel:[1,0,0] op_sel_hi:[1,0,0]\n"
"v_mfma_f32_16x16x32_bf16 a[16:19], a[180:183], v[72:75], a[16:19]\n"
"ds_read_b64_tr_b16 a[250:251], v219 offset:34584\n"
"v_fma_mix_f32 v188, v86, v148, v156 op_sel:[0,0,0] op_sel_hi:[1,0,0]\n"
"v_mfma_f32_16x16x32_bf16 a[20:23], a[176:179], v[68:71], a[20:23]\n"
"s_add_u32 m0, s51, 73504\n"
"ds_read_b64_tr_b16 a[252:253], v219 offset:37656\n"
"v_fma_mix_f32 v189, v86, v149, v157 op_sel:[1,0,0] op_sel_hi:[1,0,0]\n"
"v_mfma_f32_16x16x32_bf16 a[20:23], a[180:183], v[76:79], a[20:23]\n"
"ds_read_b64_tr_b16 a[254:255], v219 offset:38680\n"
"buffer_load_dwordx4 v217, s[20:23], s50 offen offset:256 lds\n"
"v_mfma_f32_16x16x32_bf16 a[24:27], a[184:187], v[64:67], a[24:27]\n"
"s_add_u32 s50, s50, 32768\n"
"ds_read_b64_tr_b16 a[160:161], v219 offset:40976\n"
"v_fma_mix_f32 v190, v87, v150, v158 op_sel:[0,0,0] op_sel_hi:[1,0,0]\n"
"v_mfma_f32_16x16x32_bf16 a[24:27], a[188:191], v[72:75], a[24:27]\n"
"ds_read_b64_tr_b16 a[162:163], v219 offset:42000\n"
"v_fma_mix_f32 v191, v87, v151, v159 op_sel:[1,0,0] op_sel_hi:[1,0,0]\n"
"v_mfma_f32_16x16x32_bf16 a[28:31], a[184:187], v[68:71], a[28:31]\n"
"ds_read_b64_tr_b16 a[164:165], v219 offset:45072\n"
"ds_read_b128 v[144:147], v224 offset:3072\n"
"v_mfma_f32_16x16x32_bf16 a[28:31], a[188:191], v[76:79], a[28:31]\n"
"s_waitcnt lgkmcnt(12)\n"
"ds_read_b64_tr_b16 a[166:167], v219 offset:46096\n"
"v_cvt_pk_bf16_f32 v84, v184, v185\n"
"v_mfma_f32_16x16x32_bf16 a[32:35], a[224:227], v[64:67], a[32:35]\n"
"s_add_u32 m0, s51, 66592\n"
"ds_read_b64_tr_b16 a[168:169], v219 offset:40984\n"
"v_cvt_pk_bf16_f32 v85, v186, v187\n"
"v_mfma_f32_16x16x32_bf16 a[32:35], a[228:231], v[72:75], a[32:35]\n"
"ds_read_b64_tr_b16 a[170:171], v219 offset:42008\n"
"buffer_load_dwordx4 v217, s[20:23], s50 offen offset:0 lds\n"
"v_mfma_f32_16x16x32_bf16 a[36:39], a[224:227], v[68:71], a[36:39]\n"
"ds_read_b64_tr_b16 a[172:173], v219 offset:45080\n"
"ds_read_b128 v[148:151], v224 offset:3136\n"
"v_mfma_f32_16x16x32_bf16 a[36:39], a[228:231], v[76:79], a[36:39]\n"
"ds_read_b64_tr_b16 a[174:175], v219 offset:46104\n"
"v_cvt_pk_bf16_f32 v86, v188, v189\n"
"v_mfma_f32_16x16x32_bf16 a[40:43], a[232:235], v[64:67], a[40:43]\n"
"ds_read_b64_tr_b16 a[176:177], v219 offset:41232\n"
"v_cvt_pk_bf16_f32 v87, v190, v191\n"
"v_mfma_f32_16x16x32_bf16 a[40:43], a[236:239], v[72:75], a[40:43]\n"
"ds_read_b64_tr_b16 a[178:179], v219 offset:42256\n"
"ds_read_b128 v[152:155], v224 offset:3200\n"
"v_mfma_f32_16x16x32_bf16 a[44:47], a[232:235], v[68:71], a[44:47]\n"
"s_add_u32 m0, s51, 74528\n"
"ds_read_b64_tr_b16 a[180:181], v219 offset:45328\n"
"v_fma_mix_f32 v176, v88, v160, v168 op_sel:[0,0,0] op_sel_hi:[1,0,0]\n"
"v_mfma_f32_16x16x32_bf16 a[44:47], a[236:239], v[76:79], a[44:47]\n"
"s_waitcnt lgkmcnt(14)\n"
"ds_read_b64_tr_b16 a[182:183], v219 offset:46352\n"
"buffer_load_dwordx4 v217, s[20:23], s50 offen offset:256 lds\n"
"v_mfma_f32_16x16x32_bf16 a[48:51], a[240:243], v[64:67], a[48:51]\n"
"s_add_u32 s50, s50, 32768\n"
"ds_read_b64_tr_b16 a[184:185], v219 offset:41240\n"
"ds_read_b128 v[156:159], v224 offset:3264\n"
"v_mfma_f32_16x16x32_bf16 a[48:51], a[244:247], v[72:75], a[48:51]\n"
"ds_read_b64_tr_b16 a[186:187], v219 offset:42264\n"
"v_fma_mix_f32 v177, v88, v161, v169 op_sel:[1,0,0] op_sel_hi:[1,0,0]\n"
"v_mfma_f32_16x16x32_bf16 a[52:55], a[240:243], v[68:71], a[52:55]\n"
"ds_read_b64_tr_b16 a[188:189], v219 offset:45336\n"
"v_fma_mix_f32 v178, v89, v162, v170 op_sel:[0,0,0] op_sel_hi:[1,0,0]\n"
"v_mfma_f32_16x16x32_bf16 a[52:55], a[244:247], v[76:79], a[52:55]\n"
"ds_read_b64_tr_b16 a[190:191], v219 offset:46360\n"
"v_fma_mix_f32 v179, v89, v163, v171 op_sel:[1,0,0] op_sel_hi:[1,0,0]\n"
"v_mfma_f32_16x16x32_bf16 a[56:59], a[248:251], v[64:67], a[56:59]\n"
"s_add_u32 m0, s51, 81960\n"
"ds_read_b64_tr_b16 a[224:225], v219 offset:41488\n"
"v_fma_mix_f32 v180, v90, v164, v172 op_sel:[0,0,0] op_sel_hi:[1,0,0]\n"
"v_mfma_f32_16x16x32_bf16 a[56:59], a[252:255], v[72:75], a[56:59]\n"
"ds_read_b64_tr_b16 a[226:227], v219 offset:42512\n"
"buffer_load_dwordx4 v217, s[20:23], s50 offen offset:0 lds\n"
"v_mfma_f32_16x16x32_bf16 a[60:63], a[248:251], v[68:71], a[60:63]\n"
"ds_read_b64_tr_b16 a[228:229], v219 offset:45584\n"
"v_fma_mix_f32 v181, v90, v165, v173 op_sel:[1,0,0] op_sel_hi:[1,0,0]\n"
"v_mfma_f32_16x16x32_bf16 a[60:63], a[252:255], v[76:79], a[60:63]\n"
"s_waitcnt lgkmcnt(13)\n"
"ds_read_b64_tr_b16 a[230:231], v219 offset:46608\n"
"v_fma_mix_f32 v182, v91, v166, v174 op_sel:[0,0,0] op_sel_hi:[1,0,0]\n"
"v_mfma_f32_16x16x32_bf16 a[64:67], a[160:163], v[64:67], a[64:67]\n"
"ds_read_b64_tr_b16 a[232:233], v219 offset:41496\n"
"v_fma_mix_f32 v183, v91, v167, v175 op_sel:[1,0,0] op_sel_hi:[1,0,0]\n"
"v_mfma_f32_16x16x32_bf16 a[64:67], a[164:167], v[72:75], a[64:67]\n"
"ds_read_b64_tr_b16 a[234:235], v219 offset:42520\n"
"v_cvt_pk_bf16_f32 v88, v176, v177\n"
"v_mfma_f32_16x16x32_bf16 a[68:71], a[160:163], v[68:71], a[68:71]\n"
"s_add_u32 m0, s51, 89896\n"
"ds_read_b64_tr_b16 a[236:237], v219 offset:45592\n"
"v_cvt_pk_bf16_f32 v89, v178, v179\n"
"v_mfma_f32_16x16x32_bf16 a[68:71], a[164:167], v[76:79], a[68:71]\n"
"ds_read_b64_tr_b16 a[238:239], v219 offset:46616\n"
"buffer_load_dwordx4 v217, s[20:23], s50 offen offset:256 lds\n"
"v_mfma_f32_16x16x32_bf16 a[72:75], a[168:171], v[64:67], a[72:75]\n"
"s_add_u32 s50, s50, 32768\n"
"ds_read_b64_tr_b16 a[240:241], v219 offset:41744\n"
"v_cvt_pk_bf16_f32 v90, v180, v181\n"
"v_mfma_f32_16x16x32_bf16 a[72:75], a[172:175], v[72:75], a[72:75]\n"
"ds_read_b64_tr_b16 a[242:243], v219 offset:42768\n"
"v_cvt_pk_bf16_f32 v91, v182, v183\n"
"v_mfma_f32_16x16x32_bf16 a[76:79], a[168:171], v[68:71], a[76:79]\n"
"ds_read_b64_tr_b16 a[244:245], v219 offset:45840\n"
"v_fma_mix_f32 v184, v92, v160, v168 op_sel:[0,0,0] op_sel_hi:[1,0,0]\n"
"v_mfma_f32_16x16x32_bf16 a[76:79], a[172:175], v[76:79], a[76:79]\n"
"s_waitcnt lgkmcnt(11)\n"
"ds_read_b64_tr_b16 a[246:247], v219 offset:46864\n"
"v_fma_mix_f32 v185, v92, v161, v169 op_sel:[1,0,0] op_sel_hi:[1,0,0]\n"
"v_mfma_f32_16x16x32_bf16 a[80:83], a[176:179], v[64:67], a[80:83]\n"
"s_add_u32 m0, s51, 82984\n"
"ds_read_b64_tr_b16 a[248:249], v219 offset:41752\n"
"v_fma_mix_f32 v186, v93, v162, v170 op_sel:[0,0,0] op_sel_hi:[1,0,0]\n"
"v_mfma_f32_16x16x32_bf16 a[80:83], a[180:183], v[72:75], a[80:83]\n"
"ds_read_b64_tr_b16 a[250:251], v219 offset:42776\n"
"buffer_load_dwordx4 v217, s[20:23], s50 offen offset:0 lds\n"
"v_mfma_f32_16x16x32_bf16 a[84:87], a[176:179], v[68:71], a[84:87]\n"
"ds_read_b64_tr_b16 a[252:253], v219 offset:45848\n"
"v_fma_mix_f32 v187, v93, v163, v171 op_sel:[1,0,0] op_sel_hi:[1,0,0]\n"
"v_mfma_f32_16x16x32_bf16 a[84:87], a[180:183], v[76:79], a[84:87]\n"
"ds_read_b64_tr_b16 a[254:255], v219 offset:46872\n"
"v_fma_mix_f32 v188, v94, v164, v172 op_sel:[0,0,0] op_sel_hi:[1,0,0]\n"
"v_mfma_f32_16x16x32_bf16 a[88:91], a[184:187], v[64:67], a[88:91]\n"
"ds_read_b64_tr_b16 a[160:161], v218 offset:0\n"
"v_fma_mix_f32 v189, v94, v165, v173 op_sel:[1,0,0] op_sel_hi:[1,0,0]\n"
"v_fma_mix_f32 v190, v95, v166, v174 op_sel:[0,0,0] op_sel_hi:[1,0,0]\n"
"v_mfma_f32_16x16x32_bf16 a[88:91], a[188:191], v[72:75], a[88:91]\n"
"ds_read_b64_tr_b16 a[162:163], v218 offset:1024\n"
"v_fma_mix_f32 v191, v95, v167, v175 op_sel:[1,0,0] op_sel_hi:[1,0,0]\n"
"v_mfma_f32_16x16x32_bf16 a[92:95], a[184:187], v[68:71], a[92:95]\n"
"s_add_u32 m0, s51, 90920\n"
"ds_read_b64_tr_b16 a[164:165], v218 offset:4096\n"
"ds_read_b128 v[160:163], v224 offset:3328\n"
"v_mfma_f32_16x16x32_bf16 a[92:95], a[188:191], v[76:79], a[92:95]\n"
"s_waitcnt lgkmcnt(12)\n"
"buffer_load_dwordx4 v217, s[20:23], s50 offen offset:256 lds\n"
"ds_read_b64_tr_b16 a[166:167], v218 offset:5120\n"
"v_mfma_f32_16x16x32_bf16 a[96:99], a[224:227], v[64:67], a[96:99]\n"
"s_add_u32 s50, s50, 425984\n"
"ds_read_b64_tr_b16 a[168:169], v218 offset:8\n"
"v_cvt_pk_bf16_f32 v92, v184, v185\n"
"v_mfma_f32_16x16x32_bf16 a[96:99], a[228:231], v[72:75], a[96:99]\n"
"ds_read_b64_tr_b16 a[170:171], v218 offset:1032\n"
"ds_read_b128 v[164:167], v224 offset:3392\n"
"v_mfma_f32_16x16x32_bf16 a[100:103], a[224:227], v[68:71], a[100:103]\n"
"ds_read_b64_tr_b16 a[172:173], v218 offset:4104\n"
"v_cvt_pk_bf16_f32 v93, v186, v187\n"
"v_mfma_f32_16x16x32_bf16 a[100:103], a[228:231], v[76:79], a[100:103]\n"
"ds_read_b64_tr_b16 a[174:175], v218 offset:5128\n"
"v_cvt_pk_bf16_f32 v94, v188, v189\n"
"v_mfma_f32_16x16x32_bf16 a[104:107], a[232:235], v[64:67], a[104:107]\n"
"ds_read_b64_tr_b16 a[176:177], v218 offset:256\n"
"ds_read_b128 v[168:171], v224 offset:3456\n"
"v_mfma_f32_16x16x32_bf16 a[104:107], a[236:239], v[72:75], a[104:107]\n"
"ds_read_b64_tr_b16 a[178:179], v218 offset:1280\n"
"v_cvt_pk_bf16_f32 v95, v190, v191\n"
"v_mfma_f32_16x16x32_bf16 a[108:111], a[232:235], v[68:71], a[108:111]\n"
"ds_read_b64_tr_b16 a[180:181], v218 offset:4352\n"
"v_fma_mix_f32 v176, v96, v144, v152 op_sel:[0,0,0] op_sel_hi:[1,0,0]\n"
"v_mfma_f32_16x16x32_bf16 a[108:111], a[236:239], v[76:79], a[108:111]\n"
"s_waitcnt lgkmcnt(14)\n"
"ds_read_b64_tr_b16 a[182:183], v218 offset:5376\n"
"ds_read_b128 v[172:175], v224 offset:3520\n"
"v_mfma_f32_16x16x32_bf16 a[112:115], a[240:243], v[64:67], a[112:115]\n"
"ds_read_b64_tr_b16 a[184:185], v218 offset:264\n"
"v_fma_mix_f32 v177, v96, v145, v153 op_sel:[1,0,0] op_sel_hi:[1,0,0]\n"
"v_mfma_f32_16x16x32_bf16 a[112:115], a[244:247], v[72:75], a[112:115]\n"
"ds_read_b64_tr_b16 a[186:187], v218 offset:1288\n"
"v_fma_mix_f32 v178, v97, v146, v154 op_sel:[0,0,0] op_sel_hi:[1,0,0]\n"
"v_mfma_f32_16x16x32_bf16 a[116:119], a[240:243], v[68:71], a[116:119]\n"
"ds_read_b64_tr_b16 a[188:189], v218 offset:4360\n"
"v_fma_mix_f32 v179, v97, v147, v155 op_sel:[1,0,0] op_sel_hi:[1,0,0]\n"
"v_mfma_f32_16x16x32_bf16 a[116:119], a[244:247], v[76:79], a[116:119]\n"
"ds_read_b64_tr_b16 a[190:191], v218 offset:5384\n"
"v_fma_mix_f32 v180, v98, v148, v156 op_sel:[0,0,0] op_sel_hi:[1,0,0]\n"
"v_mfma_f32_16x16x32_bf16 a[120:123], a[248:251], v[64:67], a[120:123]\n"
"ds_read_b64_tr_b16 a[224:225], v218 offset:512\n"
"v_fma_mix_f32 v181, v98, v149, v157 op_sel:[1,0,0] op_sel_hi:[1,0,0]\n"
"v_mfma_f32_16x16x32_bf16 a[120:123], a[252:255], v[72:75], a[120:123]\n"
"ds_read_b64_tr_b16 a[226:227], v218 offset:1536\n"
"v_fma_mix_f32 v182, v99, v150, v158 op_sel:[0,0,0] op_sel_hi:[1,0,0]\n"
"v_mfma_f32_16x16x32_bf16 a[124:127], a[248:251], v[68:71], a[124:127]\n"
"ds_read_b64_tr_b16 a[228:229], v218 offset:4608\n"
"v_fma_mix_f32 v183, v99, v151, v159 op_sel:[1,0,0] op_sel_hi:[1,0,0]\n"
"v_mfma_f32_16x16x32_bf16 a[124:127], a[252:255], v[76:79], a[124:127]\n"
"s_waitcnt lgkmcnt(13)\n"
"ds_read_b64_tr_b16 a[230:231], v218 offset:5632\n"
"v_cvt_pk_bf16_f32 v96, v176, v177\n"
"v_mfma_f32_16x16x32_bf16 a[0:3], a[160:163], v[80:83], a[0:3]\n"
"ds_read_b64_tr_b16 a[232:233], v218 offset:520\n"
"v_cvt_pk_bf16_f32 v97, v178, v179\n"
"v_mfma_f32_16x16x32_bf16 a[0:3], a[164:167], v[88:91], a[0:3]\n"
"ds_read_b64_tr_b16 a[234:235], v218 offset:1544\n"
"v_cvt_pk_bf16_f32 v98, v180, v181\n"
"v_mfma_f32_16x16x32_bf16 a[4:7], a[160:163], v[84:87], a[4:7]\n"
"s_waitcnt vmcnt(8)\n"
"s_barrier\n"
"ds_read_b64_tr_b16 a[236:237], v218 offset:4616\n"
"v_cvt_pk_bf16_f32 v99, v182, v183\n"
"v_mfma_f32_16x16x32_bf16 a[4:7], a[164:167], v[92:95], a[4:7]\n"
"ds_read_b64_tr_b16 a[238:239], v218 offset:5640\n"
"v_fma_mix_f32 v184, v100, v144, v152 op_sel:[0,0,0] op_sel_hi:[1,0,0]\n"
"v_mfma_f32_16x16x32_bf16 a[8:11], a[168:171], v[80:83], a[8:11]\n"
"s_add_u32 m0, s51, 98352\n"
"ds_read_b64_tr_b16 a[240:241], v218 offset:768\n"
"v_fma_mix_f32 v185, v100, v145, v153 op_sel:[1,0,0] op_sel_hi:[1,0,0]\n"
"v_mfma_f32_16x16x32_bf16 a[8:11], a[172:175], v[88:91], a[8:11]\n"
"ds_read_b64_tr_b16 a[242:243], v218 offset:1792\n"
"buffer_load_dwordx4 v217, s[20:23], s50 offen offset:0 lds\n"
"v_mfma_f32_16x16x32_bf16 a[12:15], a[168:171], v[84:87], a[12:15]\n"
"ds_read_b64_tr_b16 a[244:245], v218 offset:4864\n"
"v_fma_mix_f32 v186, v101, v146, v154 op_sel:[0,0,0] op_sel_hi:[1,0,0]\n"
"v_mfma_f32_16x16x32_bf16 a[12:15], a[172:175], v[92:95], a[12:15]\n"
"s_waitcnt lgkmcnt(11)\n"
"ds_read_b64_tr_b16 a[246:247], v218 offset:5888\n"
"v_fma_mix_f32 v187, v101, v147, v155 op_sel:[1,0,0] op_sel_hi:[1,0,0]\n"
"v_mfma_f32_16x16x32_bf16 a[16:19], a[176:179], v[80:83], a[16:19]\n"
"ds_read_b64_tr_b16 a[248:249], v218 offset:776\n"
"v_fma_mix_f32 v188, v102, v148, v156 op_sel:[0,0,0] op_sel_hi:[1,0,0]\n"
"v_mfma_f32_16x16x32_bf16 a[16:19], a[180:183], v[88:91], a[16:19]\n"
"ds_read_b64_tr_b16 a[250:251], v218 offset:1800\n"
"v_fma_mix_f32 v189, v102, v149, v157 op_sel:[1,0,0] op_sel_hi:[1,0,0]\n"
"v_mfma_f32_16x16x32_bf16 a[20:23], a[176:179], v[84:87], a[20:23]\n"
"s_add_u32 m0, s51, 106288\n"
"ds_read_b64_tr_b16 a[252:253], v218 offset:4872\n"
"v_fma_mix_f32 v190, v103, v150, v158 op_sel:[0,0,0] op_sel_hi:[1,0,0]\n"
"v_mfma_f32_16x16x32_bf16 a[20:23], a[180:183], v[92:95], a[20:23]\n"
"ds_read_b64_tr_b16 a[254:255], v218 offset:5896\n"
"buffer_load_dwordx4 v217, s[20:23], s50 offen offset:256 lds\n"
"v_mfma_f32_16x16x32_bf16 a[24:27], a[184:187], v[80:83], a[24:27]\n"
"s_add_u32 s50, s50, 32768\n"
"ds_read_b64_tr_b16 a[160:161], v218 offset:8192\n"
"v_fma_mix_f32 v191, v103, v151, v159 op_sel:[1,0,0] op_sel_hi:[1,0,0]\n"
"v_mfma_f32_16x16x32_bf16 a[24:27], a[188:191], v[88:91], a[24:27]\n"
"ds_read_b64_tr_b16 a[162:163], v218 offset:9216\n"
"ds_read_b128 v[144:147], v224 offset:3584\n"
"v_mfma_f32_16x16x32_bf16 a[28:31], a[184:187], v[84:87], a[28:31]\n"
"ds_read_b64_tr_b16 a[164:165], v218 offset:12288\n"
"v_cvt_pk_bf16_f32 v100, v184, v185\n"
"v_mfma_f32_16x16x32_bf16 a[28:31], a[188:191], v[92:95], a[28:31]\n"
"s_waitcnt lgkmcnt(12)\n"
"ds_read_b64_tr_b16 a[166:167], v218 offset:13312\n"
"v_cvt_pk_bf16_f32 v101, v186, v187\n"
"v_mfma_f32_16x16x32_bf16 a[32:35], a[224:227], v[80:83], a[32:35]\n"
"s_add_u32 m0, s51, 99376\n"
"ds_read_b64_tr_b16 a[168:169], v218 offset:8200\n"
"ds_read_b128 v[148:151], v224 offset:3648\n"
"v_mfma_f32_16x16x32_bf16 a[32:35], a[228:231], v[88:91], a[32:35]\n"
"ds_read_b64_tr_b16 a[170:171], v218 offset:9224\n"
"buffer_load_dwordx4 v217, s[20:23], s50 offen offset:0 lds\n"
"v_mfma_f32_16x16x32_bf16 a[36:39], a[224:227], v[84:87], a[36:39]\n"
"ds_read_b64_tr_b16 a[172:173], v218 offset:12296\n"
"v_cvt_pk_bf16_f32 v102, v188, v189\n"
"v_mfma_f32_16x16x32_bf16 a[36:39], a[228:231], v[92:95], a[36:39]\n"
"ds_read_b64_tr_b16 a[174:175], v218 offset:13320\n"
"ds_read_b128 v[152:155], v224 offset:3712\n"
"v_mfma_f32_16x16x32_bf16 a[40:43], a[232:235], v[80:83], a[40:43]\n"
"ds_read_b64_tr_b16 a[176:177], v218 offset:8448\n"
"v_cvt_pk_bf16_f32 v103, v190, v191\n"
"v_mfma_f32_16x16x32_bf16 a[40:43], a[236:239], v[88:91], a[40:43]\n"
"ds_read_b64_tr_b16 a[178:179], v218 offset:9472\n"
"v_fma_mix_f32 v176, v104, v160, v168 op_sel:[0,0,0] op_sel_hi:[1,0,0]\n"
"v_mfma_f32_16x16x32_bf16 a[44:47], a[232:235], v[84:87], a[44:47]\n"
"s_add_u32 m0, s51, 107312\n"
"ds_read_b64_tr_b16 a[180:181], v218 offset:12544\n"
"ds_read_b128 v[156:159], v224 offset:3776\n"
"v_mfma_f32_16x16x32_bf16 a[44:47], a[236:239], v[92:95], a[44:47]\n"
"s_waitcnt lgkmcnt(14)\n"
"ds_read_b64_tr_b16 a[182:183], v218 offset:13568\n"
"buffer_load_dwordx4 v217, s[20:23], s50 offen offset:256 lds\n"
"v_mfma_f32_16x16x32_bf16 a[48:51], a[240:243], v[80:83], a[48:51]\n"
"s_add_u32 s50, s50, 32768\n"
"ds_read_b64_tr_b16 a[184:185], v218 offset:8456\n"
"v_fma_mix_f32 v177, v104, v161, v169 op_sel:[1,0,0] op_sel_hi:[1,0,0]\n"
"v_mfma_f32_16x16x32_bf16 a[48:51], a[244:247], v[88:91], a[48:51]\n"
"ds_read_b64_tr_b16 a[186:187], v218 offset:9480\n"
"v_fma_mix_f32 v178, v105, v162, v170 op_sel:[0,0,0] op_sel_hi:[1,0,0]\n"
"v_mfma_f32_16x16x32_bf16 a[52:55], a[240:243], v[84:87], a[52:55]\n"
"ds_read_b64_tr_b16 a[188:189], v218 offset:12552\n"
"v_fma_mix_f32 v179, v105, v163, v171 op_sel:[1,0,0] op_sel_hi:[1,0,0]\n"
"v_mfma_f32_16x16x32_bf16 a[52:55], a[244:247], v[92:95], a[52:55]\n"
"ds_read_b64_tr_b16 a[190:191], v218 offset:13576\n"
"v_fma_mix_f32 v180, v106, v164, v172 op_sel:[0,0,0] op_sel_hi:[1,0,0]\n"
"v_mfma_f32_16x16x32_bf16 a[56:59], a[248:251], v[80:83], a[56:59]\n"
"s_add_u32 m0, s51, 114744\n"
"ds_read_b64_tr_b16 a[224:225], v218 offset:8704\n"
"v_fma_mix_f32 v181, v106, v165, v173 op_sel:[1,0,0] op_sel_hi:[1,0,0]\n"
"v_mfma_f32_16x16x32_bf16 a[56:59], a[252:255], v[88:91], a[56:59]\n"
"ds_read_b64_tr_b16 a[226:227], v218 offset:9728\n"
"buffer_load_dwordx4 v217, s[20:23], s50 offen offset:0 lds\n"
"v_mfma_f32_16x16x32_bf16 a[60:63], a[248:251], v[84:87], a[60:63]\n"
"ds_read_b64_tr_b16 a[228:229], v218 offset:12800\n"
"v_fma_mix_f32 v182, v107, v166, v174 op_sel:[0,0,0] op_sel_hi:[1,0,0]\n"
"v_mfma_f32_16x16x32_bf16 a[60:63], a[252:255], v[92:95], a[60:63]\n"
"s_waitcnt lgkmcnt(12)\n"
"ds_read_b64_tr_b16 a[230:231], v218 offset:13824\n"
"v_fma_mix_f32 v183, v107, v167, v175 op_sel:[1,0,0] op_sel_hi:[1,0,0]\n"
"v_mfma_f32_16x16x32_bf16 a[64:67], a[160:163], v[80:83], a[64:67]\n"
"ds_read_b64_tr_b16 a[232:233], v218 offset:8712\n"
"v_cvt_pk_bf16_f32 v104, v176, v177\n"
"v_mfma_f32_16x16x32_bf16 a[64:67], a[164:167], v[88:91], a[64:67]\n"
"ds_read_b64_tr_b16 a[234:235], v218 offset:9736\n"
"v_cvt_pk_bf16_f32 v105, v178, v179\n"
"v_mfma_f32_16x16x32_bf16 a[68:71], a[160:163], v[84:87], a[68:71]\n"
"s_add_u32 m0, s51, 122680\n"
"ds_read_b64_tr_b16 a[236:237], v218 offset:12808\n"
"v_cvt_pk_bf16_f32 v106, v180, v181\n"
"v_mfma_f32_16x16x32_bf16 a[68:71], a[164:167], v[92:95], a[68:71]\n"
"ds_read_b64_tr_b16 a[238:239], v218 offset:13832\n"
"buffer_load_dwordx4 v217, s[20:23], s50 offen offset:256 lds\n"
"v_mfma_f32_16x16x32_bf16 a[72:75], a[168:171], v[80:83], a[72:75]\n"
"s_add_u32 s50, s50, 32768\n"
"ds_read_b64_tr_b16 a[240:241], v218 offset:8960\n"
"v_cvt_pk_bf16_f32 v107, v182, v183\n"
"v_mfma_f32_16x16x32_bf16 a[72:75], a[172:175], v[88:91], a[72:75]\n"
"ds_read_b64_tr_b16 a[242:243], v218 offset:9984\n"
"v_fma_mix_f32 v184, v108, v160, v168 op_sel:[0,0,0] op_sel_hi:[1,0,0]\n"
"v_mfma_f32_16x16x32_bf16 a[76:79], a[168:171], v[84:87], a[76:79]\n"
"ds_read_b64_tr_b16 a[244:245], v218 offset:13056\n"
"v_fma_mix_f32 v185, v108, v161, v169 op_sel:[1,0,0] op_sel_hi:[1,0,0]\n"
"v_mfma_f32_16x16x32_bf16 a[76:79], a[172:175], v[92:95], a[76:79]\n"
"s_waitcnt lgkmcnt(11)\n"
"ds_read_b64_tr_b16 a[246:247], v218 offset:14080\n"
"v_fma_mix_f32 v186, v109, v162, v170 op_sel:[0,0,0] op_sel_hi:[1,0,0]\n"
"v_mfma_f32_16x16x32_bf16 a[80:83], a[176:179], v[80:83], a[80:83]\n"
"s_add_u32 m0, s51, 115768\n"
"ds_read_b64_tr_b16 a[248:249], v218 offset:8968\n"
"v_fma_mix_f32 v187, v109, v163, v171 op_sel:[1,0,0] op_sel_hi:[1,0,0]\n"
"v_mfma_f32_16x16x32_bf16 a[80:83], a[180:183], v[88:91], a[80:83]\n"
"ds_read_b64_tr_b16 a[250:251], v218 offset:9992\n"
"buffer_load_dwordx4 v217, s[20:23], s50 offen offset:0 lds\n"
"v_mfma_f32_16x16x32_bf16 a[84:87], a[176:179], v[84:87], a[84:87]\n"
"ds_read_b64_tr_b16 a[252:253], v218 offset:13064\n"
"v_fma_mix_f32 v188, v110, v164, v172 op_sel:[0,0,0] op_sel_hi:[1,0,0]\n"
"v_mfma_f32_16x16x32_bf16 a[84:87], a[180:183], v[92:95], a[84:87]\n"
"ds_read_b64_tr_b16 a[254:255], v218 offset:14088\n"
"v_fma_mix_f32 v189, v110, v165, v173 op_sel:[1,0,0] op_sel_hi:[1,0,0]\n"
"v_mfma_f32_16x16x32_bf16 a[88:91], a[184:187], v[80:83], a[88:91]\n"
"ds_read_b64_tr_b16 a[160:161], v218 offset:32784\n"
"v_fma_mix_f32 v190, v111, v166, v174 op_sel:[0,0,0] op_sel_hi:[1,0,0]\n"
"v_fma_mix_f32 v191, v111, v167, v175 op_sel:[1,0,0] op_sel_hi:[1,0,0]\n"
"v_mfma_f32_16x16x32_bf16 a[88:91], a[188:191], v[88:91], a[88:91]\n"
"ds_read_b64_tr_b16 a[162:163], v218 offset:33808\n"
"ds_read_b128 v[160:163], v224 offset:3840\n"
"v_mfma_f32_16x16x32_bf16 a[92:95], a[184:187], v[84:87], a[92:95]\n"
"s_add_u32 m0, s51, 123704\n"
"ds_read_b64_tr_b16 a[164:165], v218 offset:36880\n"
"v_cvt_pk_bf16_f32 v108, v184, v185\n"
"v_mfma_f32_16x16x32_bf16 a[92:95], a[188:191], v[92:95], a[92:95]\n"
"s_waitcnt lgkmcnt(12)\n"
"buffer_load_dwordx4 v217, s[20:23], s50 offen offset:256 lds\n"
"ds_read_b64_tr_b16 a[166:167], v218 offset:37904\n"
"v_mfma_f32_16x16x32_bf16 a[96:99], a[224:227], v[80:83], a[96:99]\n"
"s_add_u32 s50, s50, 425984\n"
"ds_read_b64_tr_b16 a[168:169], v218 offset:32792\n"
"ds_read_b128 v[164:167], v224 offset:3904\n"
"v_mfma_f32_16x16x32_bf16 a[96:99], a[228:231], v[88:91], a[96:99]\n"
"ds_read_b64_tr_b16 a[170:171], v218 offset:33816\n"
"v_cvt_pk_bf16_f32 v109, v186, v187\n"
"v_mfma_f32_16x16x32_bf16 a[100:103], a[224:227], v[84:87], a[100:103]\n"
"ds_read_b64_tr_b16 a[172:173], v218 offset:36888\n"
"v_cvt_pk_bf16_f32 v110, v188, v189\n"
"v_mfma_f32_16x16x32_bf16 a[100:103], a[228:231], v[92:95], a[100:103]\n"
"ds_read_b64_tr_b16 a[174:175], v218 offset:37912\n"
"ds_read_b128 v[168:171], v224 offset:3968\n"
"v_mfma_f32_16x16x32_bf16 a[104:107], a[232:235], v[80:83], a[104:107]\n"
"ds_read_b64_tr_b16 a[176:177], v218 offset:33040\n"
"v_cvt_pk_bf16_f32 v111, v190, v191\n"
"v_mfma_f32_16x16x32_bf16 a[104:107], a[236:239], v[88:91], a[104:107]\n"
"ds_read_b64_tr_b16 a[178:179], v218 offset:34064\n"
"v_fma_mix_f32 v176, v112, v144, v152 op_sel:[0,0,0] op_sel_hi:[1,0,0]\n"
"v_mfma_f32_16x16x32_bf16 a[108:111], a[232:235], v[84:87], a[108:111]\n"
"ds_read_b64_tr_b16 a[180:181], v218 offset:37136\n"
"ds_read_b128 v[172:175], v224 offset:4032\n"
"v_mfma_f32_16x16x32_bf16 a[108:111], a[236:239], v[92:95], a[108:111]\n"
"s_waitcnt lgkmcnt(14)\n"
"ds_read_b64_tr_b16 a[182:183], v218 offset:38160\n"
"v_fma_mix_f32 v177, v112, v145, v153 op_sel:[1,0,0] op_sel_hi:[1,0,0]\n"
"v_mfma_f32_16x16x32_bf16 a[112:115], a[240:243], v[80:83], a[112:115]\n"
"ds_read_b64_tr_b16 a[184:185], v218 offset:33048\n"
"v_fma_mix_f32 v178, v113, v146, v154 op_sel:[0,0,0] op_sel_hi:[1,0,0]\n"
"v_mfma_f32_16x16x32_bf16 a[112:115], a[244:247], v[88:91], a[112:115]\n"
"ds_read_b64_tr_b16 a[186:187], v218 offset:34072\n"
"v_fma_mix_f32 v179, v113, v147, v155 op_sel:[1,0,0] op_sel_hi:[1,0,0]\n"
"v_mfma_f32_16x16x32_bf16 a[116:119], a[240:243], v[84:87], a[116:119]\n"
"ds_read_b64_tr_b16 a[188:189], v218 offset:37144\n"
"v_fma_mix_f32 v180, v114, v148, v156 op_sel:[0,0,0] op_sel_hi:[1,0,0]\n"
"v_mfma_f32_16x16x32_bf16 a[116:119], a[244:247], v[92:95], a[116:119]\n"
"ds_read_b64_tr_b16 a[190:191], v218 offset:38168\n"
"v_fma_mix_f32 v181, v114, v149, v157 op_sel:[1,0,0] op_sel_hi:[1,0,0]\n"
"v_mfma_f32_16x16x32_bf16 a[120:123], a[248:251], v[80:83], a[120:123]\n"
"ds_read_b64_tr_b16 a[224:225], v218 offset:33296\n"
"v_fma_mix_f32 v182, v115, v150, v158 op_sel:[0,0,0] op_sel_hi:[1,0,0]\n"
"v_mfma_f32_16x16x32_bf16 a[120:123], a[252:255], v[88:91], a[120:123]\n"
"ds_read_b64_tr_b16 a[226:227], v218 offset:34320\n"
"v_fma_mix_f32 v183, v115, v151, v159 op_sel:[1,0,0] op_sel_hi:[1,0,0]\n"
"v_mfma_f32_16x16x32_bf16 a[124:127], a[248:251], v[84:87], a[124:127]\n"
"ds_read_b64_tr_b16 a[228:229], v218 offset:37392\n"
"v_cvt_pk_bf16_f32 v112, v176, v177\n"
"v_mfma_f32_16x16x32_bf16 a[124:127], a[252:255], v[92:95], a[124:127]\n"
"s_waitcnt lgkmcnt(12)\n"
"ds_read_b64_tr_b16 a[230:231], v218 offset:38416\n"
"v_cvt_pk_bf16_f32 v113, v178, v179\n"
"v_mfma_f32_16x16x32_bf16 a[0:3], a[160:163], v[96:99], a[0:3]\n"
"ds_read_b64_tr_b16 a[232:233], v218 offset:33304\n"
"v_cvt_pk_bf16_f32 v114, v180, v181\n"
"v_mfma_f32_16x16x32_bf16 a[0:3], a[164:167], v[104:107], a[0:3]\n"
"ds_read_b64_tr_b16 a[234:235], v218 offset:34328\n"
"v_cvt_pk_bf16_f32 v115, v182, v183\n"
"v_mfma_f32_16x16x32_bf16 a[4:7], a[160:163], v[100:103], a[4:7]\n"
"s_waitcnt vmcnt(8)\n"
"s_barrier\n"
"ds_read_b64_tr_b16 a[236:237], v218 offset:37400\n"
"v_fma_mix_f32 v184, v116, v144, v152 op_sel:[0,0,0] op_sel_hi:[1,0,0]\n"
"v_mfma_f32_16x16x32_bf16 a[4:7], a[164:167], v[108:111], a[4:7]\n"
"ds_read_b64_tr_b16 a[238:239], v218 offset:38424\n"
"v_fma_mix_f32 v185, v116, v145, v153 op_sel:[1,0,0] op_sel_hi:[1,0,0]\n"
"v_mfma_f32_16x16x32_bf16 a[8:11], a[168:171], v[96:99], a[8:11]\n"
"s_mov_b32 m0, s86\n"
"ds_read_b64_tr_b16 a[240:241], v218 offset:33552\n"
"v_fma_mix_f32 v186, v117, v146, v154 op_sel:[0,0,0] op_sel_hi:[1,0,0]\n"
"v_mfma_f32_16x16x32_bf16 a[8:11], a[172:175], v[104:107], a[8:11]\n"
"ds_read_b64_tr_b16 a[242:243], v218 offset:34576\n"
"buffer_load_dwordx4 v246, s[8:11], s84 offen offset:0 lds\n"
"v_mfma_f32_16x16x32_bf16 a[12:15], a[168:171], v[100:103], a[12:15]\n"
"ds_read_b64_tr_b16 a[244:245], v218 offset:37648\n"
"v_fma_mix_f32 v187, v117, v147, v155 op_sel:[1,0,0] op_sel_hi:[1,0,0]\n"
"v_mfma_f32_16x16x32_bf16 a[12:15], a[172:175], v[108:111], a[12:15]\n"
"s_waitcnt lgkmcnt(11)\n"
"ds_read_b64_tr_b16 a[246:247], v218 offset:38672\n"
"v_fma_mix_f32 v188, v118, v148, v156 op_sel:[0,0,0] op_sel_hi:[1,0,0]\n"
"v_mfma_f32_16x16x32_bf16 a[16:19], a[176:179], v[96:99], a[16:19]\n"
"ds_read_b64_tr_b16 a[248:249], v218 offset:33560\n"
"v_fma_mix_f32 v189, v118, v149, v157 op_sel:[1,0,0] op_sel_hi:[1,0,0]\n"
"v_mfma_f32_16x16x32_bf16 a[16:19], a[180:183], v[104:107], a[16:19]\n"
"ds_read_b64_tr_b16 a[250:251], v218 offset:34584\n"
"v_fma_mix_f32 v190, v119, v150, v158 op_sel:[0,0,0] op_sel_hi:[1,0,0]\n"
"v_mfma_f32_16x16x32_bf16 a[20:23], a[176:179], v[100:103], a[20:23]\n"
"ds_read_b64_tr_b16 a[252:253], v218 offset:37656\n"
"v_fma_mix_f32 v191, v119, v151, v159 op_sel:[1,0,0] op_sel_hi:[1,0,0]\n"
"v_mfma_f32_16x16x32_bf16 a[20:23], a[180:183], v[108:111], a[20:23]\n"
"ds_read_b64_tr_b16 a[254:255], v218 offset:38680\n"
"buffer_load_dwordx4 v246, s[8:11], s84 offen offset:1024 lds\n"
"v_mfma_f32_16x16x32_bf16 a[24:27], a[184:187], v[96:99], a[24:27]\n"
"ds_read_b64_tr_b16 a[160:161], v218 offset:40976\n"
"ds_read_b128 v[144:147], v224 offset:4096\n"
"v_mfma_f32_16x16x32_bf16 a[24:27], a[188:191], v[104:107], a[24:27]\n"
"ds_read_b64_tr_b16 a[162:163], v218 offset:42000\n"
"v_cvt_pk_bf16_f32 v116, v184, v185\n"
"v_mfma_f32_16x16x32_bf16 a[28:31], a[184:187], v[100:103], a[28:31]\n"
"ds_read_b64_tr_b16 a[164:165], v218 offset:45072\n"
"v_cvt_pk_bf16_f32 v117, v186, v187\n"
"v_mfma_f32_16x16x32_bf16 a[28:31], a[188:191], v[108:111], a[28:31]\n"
"s_waitcnt lgkmcnt(12)\n"
"ds_read_b64_tr_b16 a[166:167], v218 offset:46096\n"
"ds_read_b128 v[148:151], v224 offset:4160\n"
"v_mfma_f32_16x16x32_bf16 a[32:35], a[224:227], v[96:99], a[32:35]\n"
"ds_read_b64_tr_b16 a[168:169], v218 offset:40984\n"
"v_cvt_pk_bf16_f32 v118, v188, v189\n"
"v_mfma_f32_16x16x32_bf16 a[32:35], a[228:231], v[104:107], a[32:35]\n"
"ds_read_b64_tr_b16 a[170:171], v218 offset:42008\n"
"buffer_load_dwordx4 v247, s[8:11], s84 offen offset:2048 lds\n"
"v_mfma_f32_16x16x32_bf16 a[36:39], a[224:227], v[100:103], a[36:39]\n"
"ds_read_b64_tr_b16 a[172:173], v218 offset:45080\n"
"ds_read_b128 v[152:155], v224 offset:4224\n"
"v_mfma_f32_16x16x32_bf16 a[36:39], a[228:231], v[108:111], a[36:39]\n"
"ds_read_b64_tr_b16 a[174:175], v218 offset:46104\n"
"v_cvt_pk_bf16_f32 v119, v190, v191\n"
"v_mfma_f32_16x16x32_bf16 a[40:43], a[232:235], v[96:99], a[40:43]\n"
"ds_read_b64_tr_b16 a[176:177], v218 offset:41232\n"
"v_fma_mix_f32 v176, v120, v160, v168 op_sel:[0,0,0] op_sel_hi:[1,0,0]\n"
"v_mfma_f32_16x16x32_bf16 a[40:43], a[236:239], v[104:107], a[40:43]\n"
"ds_read_b64_tr_b16 a[178:179], v218 offset:42256\n"
"ds_read_b128 v[156:159], v224 offset:4288\n"
"v_mfma_f32_16x16x32_bf16 a[44:47], a[232:235], v[100:103], a[44:47]\n"
"ds_read_b64_tr_b16 a[180:181], v218 offset:45328\n"
"v_fma_mix_f32 v177, v120, v161, v169 op_sel:[1,0,0] op_sel_hi:[1,0,0]\n"
"v_mfma_f32_16x16x32_bf16 a[44:47], a[236:239], v[108:111], a[44:47]\n"
"s_waitcnt lgkmcnt(14)\n"
"ds_read_b64_tr_b16 a[182:183], v218 offset:46352\n"
"buffer_load_dwordx4 v247, s[8:11], s84 offen offset:3072 lds\n"
"v_mfma_f32_16x16x32_bf16 a[48:51], a[240:243], v[96:99], a[48:51]\n"
"ds_read_b64_tr_b16 a[184:185], v218 offset:41240\n"
"v_fma_mix_f32 v178, v121, v162, v170 op_sel:[0,0,0] op_sel_hi:[1,0,0]\n"
"v_mfma_f32_16x16x32_bf16 a[48:51], a[244:247], v[104:107], a[48:51]\n"
"ds_read_b64_tr_b16 a[186:187], v218 offset:42264\n"
"v_fma_mix_f32 v179, v121, v163, v171 op_sel:[1,0,0] op_sel_hi:[1,0,0]\n"
"v_mfma_f32_16x16x32_bf16 a[52:55], a[240:243], v[100:103], a[52:55]\n"
"ds_read_b64_tr_b16 a[188:189], v218 offset:45336\n"
"v_fma_mix_f32 v180, v122, v164, v172 op_sel:[0,0,0] op_sel_hi:[1,0,0]\n"
"v_mfma_f32_16x16x32_bf16 a[52:55], a[244:247], v[108:111], a[52:55]\n"
"ds_read_b64_tr_b16 a[190:191], v218 offset:46360\n"
"v_fma_mix_f32 v181, v122, v165, v173 op_sel:[1,0,0] op_sel_hi:[1,0,0]\n"
"v_mfma_f32_16x16x32_bf16 a[56:59], a[248:251], v[96:99], a[56:59]\n"
"s_add_u32 m0, s86, 16384\n"
"ds_read_b64_tr_b16 a[224:225], v218 offset:41488\n"
"v_fma_mix_f32 v182, v123, v166, v174 op_sel:[0,0,0] op_sel_hi:[1,0,0]\n"
"v_mfma_f32_16x16x32_bf16 a[56:59], a[252:255], v[104:107], a[56:59]\n"
"ds_read_b64_tr_b16 a[226:227], v218 offset:42512\n"
"buffer_load_dwordx4 v246, s[12:15], s84 offen offset:0 lds\n"
"v_mfma_f32_16x16x32_bf16 a[60:63], a[248:251], v[100:103], a[60:63]\n"
"ds_read_b64_tr_b16 a[228:229], v218 offset:45584\n"
"v_fma_mix_f32 v183, v123, v167, v175 op_sel:[1,0,0] op_sel_hi:[1,0,0]\n"
"v_mfma_f32_16x16x32_bf16 a[60:63], a[252:255], v[108:111], a[60:63]\n"
"s_waitcnt lgkmcnt(12)\n"
"ds_read_b64_tr_b16 a[230:231], v218 offset:46608\n"
"v_cvt_pk_bf16_f32 v120, v176, v177\n"
"v_mfma_f32_16x16x32_bf16 a[64:67], a[160:163], v[96:99], a[64:67]\n"
"ds_read_b64_tr_b16 a[232:233], v218 offset:41496\n"
"v_cvt_pk_bf16_f32 v121, v178, v179\n"
"v_mfma_f32_16x16x32_bf16 a[64:67], a[164:167], v[104:107], a[64:67]\n"
"ds_read_b64_tr_b16 a[234:235], v218 offset:42520\n"
"v_cvt_pk_bf16_f32 v122, v180, v181\n"
"v_mfma_f32_16x16x32_bf16 a[68:71], a[160:163], v[100:103], a[68:71]\n"
"ds_read_b64_tr_b16 a[236:237], v218 offset:45592\n"
"v_cvt_pk_bf16_f32 v123, v182, v183\n"
"v_mfma_f32_16x16x32_bf16 a[68:71], a[164:167], v[108:111], a[68:71]\n"
"ds_read_b64_tr_b16 a[238:239], v218 offset:46616\n"
"buffer_load_dwordx4 v247, s[12:15], s84 offen offset:1024 lds\n"
"v_mfma_f32_16x16x32_bf16 a[72:75], a[168:171], v[96:99], a[72:75]\n"
"ds_read_b64_tr_b16 a[240:241], v218 offset:41744\n"
"v_fma_mix_f32 v184, v124, v160, v168 op_sel:[0,0,0] op_sel_hi:[1,0,0]\n"
"v_mfma_f32_16x16x32_bf16 a[72:75], a[172:175], v[104:107], a[72:75]\n"
"ds_read_b64_tr_b16 a[242:243], v218 offset:42768\n"
"v_fma_mix_f32 v185, v124, v161, v169 op_sel:[1,0,0] op_sel_hi:[1,0,0]\n"
"v_mfma_f32_16x16x32_bf16 a[76:79], a[168:171], v[100:103], a[76:79]\n"
"ds_read_b64_tr_b16 a[244:245], v218 offset:45840\n"
"v_fma_mix_f32 v186, v125, v162, v170 op_sel:[0,0,0] op_sel_hi:[1,0,0]\n"
"v_mfma_f32_16x16x32_bf16 a[76:79], a[172:175], v[108:111], a[76:79]\n"
"s_waitcnt lgkmcnt(11)\n"
"ds_read_b64_tr_b16 a[246:247], v218 offset:46864\n"
"v_fma_mix_f32 v187, v125, v163, v171 op_sel:[1,0,0] op_sel_hi:[1,0,0]\n"
"v_mfma_f32_16x16x32_bf16 a[80:83], a[176:179], v[96:99], a[80:83]\n"
"ds_read_b64_tr_b16 a[248:249], v218 offset:41752\n"
"v_fma_mix_f32 v188, v126, v164, v172 op_sel:[0,0,0] op_sel_hi:[1,0,0]\n"
"v_mfma_f32_16x16x32_bf16 a[80:83], a[180:183], v[104:107], a[80:83]\n"
"ds_read_b64_tr_b16 a[250:251], v218 offset:42776\n"
"buffer_load_dwordx4 v246, s[12:15], s84 offen offset:2048 lds\n"
"v_mfma_f32_16x16x32_bf16 a[84:87], a[176:179], v[100:103], a[84:87]\n"
"ds_read_b64_tr_b16 a[252:253], v218 offset:45848\n"
"v_fma_mix_f32 v189, v126, v165, v173 op_sel:[1,0,0] op_sel_hi:[1,0,0]\n"
"v_mfma_f32_16x16x32_bf16 a[84:87], a[180:183], v[108:111], a[84:87]\n"
"ds_read_b64_tr_b16 a[254:255], v218 offset:46872\n"
"v_fma_mix_f32 v190, v127, v166, v174 op_sel:[0,0,0] op_sel_hi:[1,0,0]\n"
"v_mfma_f32_16x16x32_bf16 a[88:91], a[184:187], v[96:99], a[88:91]\n"
"ds_read_b64_tr_b16 a[160:161], v219 offset:0\n"
"v_fma_mix_f32 v191, v127, v167, v175 op_sel:[1,0,0] op_sel_hi:[1,0,0]\n"
"v_cvt_pk_bf16_f32 v124, v184, v185\n"
"v_mfma_f32_16x16x32_bf16 a[88:91], a[188:191], v[104:107], a[88:91]\n"
"ds_read_b64_tr_b16 a[162:163], v219 offset:1024\n"
"ds_read_b128 v[160:163], v224 offset:4352\n"
"v_mfma_f32_16x16x32_bf16 a[92:95], a[184:187], v[100:103], a[92:95]\n"
"ds_read_b64_tr_b16 a[164:165], v219 offset:4096\n"
"v_cvt_pk_bf16_f32 v125, v186, v187\n"
"v_mfma_f32_16x16x32_bf16 a[92:95], a[188:191], v[108:111], a[92:95]\n"
"s_waitcnt lgkmcnt(12)\n"
"buffer_load_dwordx4 v247, s[12:15], s84 offen offset:3072 lds\n"
"ds_read_b64_tr_b16 a[166:167], v219 offset:5120\n"
"v_mfma_f32_16x16x32_bf16 a[96:99], a[224:227], v[96:99], a[96:99]\n"
"s_add_u32 s84, s84, 16384\n"
"ds_read_b64_tr_b16 a[168:169], v219 offset:8\n"
"ds_read_b128 v[164:167], v224 offset:4416\n"
"v_mfma_f32_16x16x32_bf16 a[96:99], a[228:231], v[104:107], a[96:99]\n"
"ds_read_b64_tr_b16 a[170:171], v219 offset:1032\n"
"v_cvt_pk_bf16_f32 v126, v188, v189\n"
"v_mfma_f32_16x16x32_bf16 a[100:103], a[224:227], v[100:103], a[100:103]\n"
"ds_read_b64_tr_b16 a[172:173], v219 offset:4104\n"
"v_cvt_pk_bf16_f32 v127, v190, v191\n"
"v_mfma_f32_16x16x32_bf16 a[100:103], a[228:231], v[108:111], a[100:103]\n"
"ds_read_b64_tr_b16 a[174:175], v219 offset:5128\n"
"ds_read_b128 v[168:171], v224 offset:4480\n"
"v_mfma_f32_16x16x32_bf16 a[104:107], a[232:235], v[96:99], a[104:107]\n"
"ds_read_b64_tr_b16 a[176:177], v219 offset:256\n"
"v_fma_mix_f32 v176, v128, v144, v152 op_sel:[0,0,0] op_sel_hi:[1,0,0]\n"
"v_mfma_f32_16x16x32_bf16 a[104:107], a[236:239], v[104:107], a[104:107]\n"
"ds_read_b64_tr_b16 a[178:179], v219 offset:1280\n"
"v_fma_mix_f32 v177, v128, v145, v153 op_sel:[1,0,0] op_sel_hi:[1,0,0]\n"
"v_mfma_f32_16x16x32_bf16 a[108:111], a[232:235], v[100:103], a[108:111]\n"
"ds_read_b64_tr_b16 a[180:181], v219 offset:4352\n"
"ds_read_b128 v[172:175], v224 offset:4544\n"
"v_mfma_f32_16x16x32_bf16 a[108:111], a[236:239], v[108:111], a[108:111]\n"
"s_waitcnt lgkmcnt(14)\n"
"ds_read_b64_tr_b16 a[182:183], v219 offset:5376\n"
"v_fma_mix_f32 v178, v129, v146, v154 op_sel:[0,0,0] op_sel_hi:[1,0,0]\n"
"v_mfma_f32_16x16x32_bf16 a[112:115], a[240:243], v[96:99], a[112:115]\n"
"ds_read_b64_tr_b16 a[184:185], v219 offset:264\n"
"v_fma_mix_f32 v179, v129, v147, v155 op_sel:[1,0,0] op_sel_hi:[1,0,0]\n"
"v_mfma_f32_16x16x32_bf16 a[112:115], a[244:247], v[104:107], a[112:115]\n"
"ds_read_b64_tr_b16 a[186:187], v219 offset:1288\n"
"v_fma_mix_f32 v180, v130, v148, v156 op_sel:[0,0,0] op_sel_hi:[1,0,0]\n"
"v_mfma_f32_16x16x32_bf16 a[116:119], a[240:243], v[100:103], a[116:119]\n"
"ds_read_b64_tr_b16 a[188:189], v219 offset:4360\n"
"v_fma_mix_f32 v181, v130, v149, v157 op_sel:[1,0,0] op_sel_hi:[1,0,0]\n"
"v_mfma_f32_16x16x32_bf16 a[116:119], a[244:247], v[108:111], a[116:119]\n"
"ds_read_b64_tr_b16 a[190:191], v219 offset:5384\n"
"v_fma_mix_f32 v182, v131, v150, v158 op_sel:[0,0,0] op_sel_hi:[1,0,0]\n"
"v_mfma_f32_16x16x32_bf16 a[120:123], a[248:251], v[96:99], a[120:123]\n"
"ds_read_b64_tr_b16 a[224:225], v219 offset:512\n"
"v_fma_mix_f32 v183, v131, v151, v159 op_sel:[1,0,0] op_sel_hi:[1,0,0]\n"
"v_mfma_f32_16x16x32_bf16 a[120:123], a[252:255], v[104:107], a[120:123]\n"
"ds_read_b64_tr_b16 a[226:227], v219 offset:1536\n"
"v_cvt_pk_bf16_f32 v128, v176, v177\n"
"v_mfma_f32_16x16x32_bf16 a[124:127], a[248:251], v[100:103], a[124:127]\n"
"ds_read_b64_tr_b16 a[228:229], v219 offset:4608\n"
"v_cvt_pk_bf16_f32 v129, v178, v179\n"
"v_mfma_f32_16x16x32_bf16 a[124:127], a[252:255], v[108:111], a[124:127]\n"
"s_waitcnt lgkmcnt(12)\n"
"ds_read_b64_tr_b16 a[230:231], v219 offset:5632\n"
"v_cvt_pk_bf16_f32 v130, v180, v181\n"
"v_mfma_f32_16x16x32_bf16 a[0:3], a[160:163], v[112:115], a[0:3]\n"
"ds_read_b64_tr_b16 a[232:233], v219 offset:520\n"
"v_cvt_pk_bf16_f32 v131, v182, v183\n"
"v_mfma_f32_16x16x32_bf16 a[0:3], a[164:167], v[120:123], a[0:3]\n"
"ds_read_b64_tr_b16 a[234:235], v219 offset:1544\n"
"v_fma_mix_f32 v184, v132, v144, v152 op_sel:[0,0,0] op_sel_hi:[1,0,0]\n"
"v_mfma_f32_16x16x32_bf16 a[4:7], a[160:163], v[116:119], a[4:7]\n"
"s_waitcnt vmcnt(8)\n"
"s_barrier\n"
"ds_read_b64_tr_b16 a[236:237], v219 offset:4616\n"
"v_fma_mix_f32 v185, v132, v145, v153 op_sel:[1,0,0] op_sel_hi:[1,0,0]\n"
"v_mfma_f32_16x16x32_bf16 a[4:7], a[164:167], v[124:127], a[4:7]\n"
"ds_read_b64_tr_b16 a[238:239], v219 offset:5640\n"
"v_fma_mix_f32 v186, v133, v146, v154 op_sel:[0,0,0] op_sel_hi:[1,0,0]\n"
"v_mfma_f32_16x16x32_bf16 a[8:11], a[168:171], v[112:115], a[8:11]\n"
"s_add_u32 m0, s86, 32768\n"
"ds_read_b64_tr_b16 a[240:241], v219 offset:768\n"
"v_fma_mix_f32 v187, v133, v147, v155 op_sel:[1,0,0] op_sel_hi:[1,0,0]\n"
"v_mfma_f32_16x16x32_bf16 a[8:11], a[172:175], v[120:123], a[8:11]\n"
"ds_read_b64_tr_b16 a[242:243], v219 offset:1792\n"
"buffer_load_dwordx4 v246, s[8:11], s84 offen offset:0 lds\n"
"v_mfma_f32_16x16x32_bf16 a[12:15], a[168:171], v[116:119], a[12:15]\n"
"ds_read_b64_tr_b16 a[244:245], v219 offset:4864\n"
"v_fma_mix_f32 v188, v134, v148, v156 op_sel:[0,0,0] op_sel_hi:[1,0,0]\n"
"v_mfma_f32_16x16x32_bf16 a[12:15], a[172:175], v[124:127], a[12:15]\n"
"s_waitcnt lgkmcnt(11)\n"
"ds_read_b64_tr_b16 a[246:247], v219 offset:5888\n"
"v_fma_mix_f32 v189, v134, v149, v157 op_sel:[1,0,0] op_sel_hi:[1,0,0]\n"
"v_mfma_f32_16x16x32_bf16 a[16:19], a[176:179], v[112:115], a[16:19]\n"
"ds_read_b64_tr_b16 a[248:249], v219 offset:776\n"
"v_fma_mix_f32 v190, v135, v150, v158 op_sel:[0,0,0] op_sel_hi:[1,0,0]\n"
"v_mfma_f32_16x16x32_bf16 a[16:19], a[180:183], v[120:123], a[16:19]\n"
"ds_read_b64_tr_b16 a[250:251], v219 offset:1800\n"
"v_fma_mix_f32 v191, v135, v151, v159 op_sel:[1,0,0] op_sel_hi:[1,0,0]\n"
"v_mfma_f32_16x16x32_bf16 a[20:23], a[176:179], v[116:119], a[20:23]\n"
"ds_read_b64_tr_b16 a[252:253], v219 offset:4872\n"
"v_cvt_pk_bf16_f32 v132, v184, v185\n"
"v_mfma_f32_16x16x32_bf16 a[20:23], a[180:183], v[124:127], a[20:23]\n"
"ds_read_b64_tr_b16 a[254:255], v219 offset:5896\n"
"buffer_load_dwordx4 v246, s[8:11], s84 offen offset:1024 lds\n"
"v_mfma_f32_16x16x32_bf16 a[24:27], a[184:187], v[112:115], a[24:27]\n"
"ds_read_b64_tr_b16 a[160:161], v219 offset:8192\n"
"v_cvt_pk_bf16_f32 v133, v186, v187\n"
"v_mfma_f32_16x16x32_bf16 a[24:27], a[188:191], v[120:123], a[24:27]\n"
"ds_read_b64_tr_b16 a[162:163], v219 offset:9216\n"
"v_cvt_pk_bf16_f32 v134, v188, v189\n"
"v_mfma_f32_16x16x32_bf16 a[28:31], a[184:187], v[116:119], a[28:31]\n"
"ds_read_b64_tr_b16 a[164:165], v219 offset:12288\n"
"v_cvt_pk_bf16_f32 v135, v190, v191\n"
"v_mfma_f32_16x16x32_bf16 a[28:31], a[188:191], v[124:127], a[28:31]\n"
"s_waitcnt lgkmcnt(11)\n"
"ds_read_b64_tr_b16 a[166:167], v219 offset:13312\n"
"v_fma_mix_f32 v176, v136, v160, v168 op_sel:[0,0,0] op_sel_hi:[1,0,0]\n"
"v_mfma_f32_16x16x32_bf16 a[32:35], a[224:227], v[112:115], a[32:35]\n"
"ds_read_b64_tr_b16 a[168:169], v219 offset:8200\n"
"v_fma_mix_f32 v177, v136, v161, v169 op_sel:[1,0,0] op_sel_hi:[1,0,0]\n"
"v_mfma_f32_16x16x32_bf16 a[32:35], a[228:231], v[120:123], a[32:35]\n"
"ds_read_b64_tr_b16 a[170:171], v219 offset:9224\n"
"buffer_load_dwordx4 v247, s[8:11], s84 offen offset:2048 lds\n"
"v_mfma_f32_16x16x32_bf16 a[36:39], a[224:227], v[116:119], a[36:39]\n"
"ds_read_b64_tr_b16 a[172:173], v219 offset:12296\n"
"v_fma_mix_f32 v178, v137, v162, v170 op_sel:[0,0,0] op_sel_hi:[1,0,0]\n"
"v_mfma_f32_16x16x32_bf16 a[36:39], a[228:231], v[124:127], a[36:39]\n"
"ds_read_b64_tr_b16 a[174:175], v219 offset:13320\n"
"v_fma_mix_f32 v179, v137, v163, v171 op_sel:[1,0,0] op_sel_hi:[1,0,0]\n"
"v_mfma_f32_16x16x32_bf16 a[40:43], a[232:235], v[112:115], a[40:43]\n"
"ds_read_b64_tr_b16 a[176:177], v219 offset:8448\n"
"v_fma_mix_f32 v180, v138, v164, v172 op_sel:[0,0,0] op_sel_hi:[1,0,0]\n"
"v_mfma_f32_16x16x32_bf16 a[40:43], a[236:239], v[120:123], a[40:43]\n"
"ds_read_b64_tr_b16 a[178:179], v219 offset:9472\n"
"v_fma_mix_f32 v181, v138, v165, v173 op_sel:[1,0,0] op_sel_hi:[1,0,0]\n"
"v_mfma_f32_16x16x32_bf16 a[44:47], a[232:235], v[116:119], a[44:47]\n"
"ds_read_b64_tr_b16 a[180:181], v219 offset:12544\n"
"v_fma_mix_f32 v182, v139, v166, v174 op_sel:[0,0,0] op_sel_hi:[1,0,0]\n"
"v_mfma_f32_16x16x32_bf16 a[44:47], a[236:239], v[124:127], a[44:47]\n"
"s_waitcnt lgkmcnt(11)\n"
"ds_read_b64_tr_b16 a[182:183], v219 offset:13568\n"
"buffer_load_dwordx4 v247, s[8:11], s84 offen offset:3072 lds\n"
"v_mfma_f32_16x16x32_bf16 a[48:51], a[240:243], v[112:115], a[48:51]\n"
"ds_read_b64_tr_b16 a[184:185], v219 offset:8456\n"
"v_fma_mix_f32 v183, v139, v167, v175 op_sel:[1,0,0] op_sel_hi:[1,0,0]\n"
"v_mfma_f32_16x16x32_bf16 a[48:51], a[244:247], v[120:123], a[48:51]\n"
"ds_read_b64_tr_b16 a[186:187], v219 offset:9480\n"
"v_cvt_pk_bf16_f32 v136, v176, v177\n"
"v_mfma_f32_16x16x32_bf16 a[52:55], a[240:243], v[116:119], a[52:55]\n"
"ds_read_b64_tr_b16 a[188:189], v219 offset:12552\n"
"v_cvt_pk_bf16_f32 v137, v178, v179\n"
"v_mfma_f32_16x16x32_bf16 a[52:55], a[244:247], v[124:127], a[52:55]\n"
"ds_read_b64_tr_b16 a[190:191], v219 offset:13576\n"
"v_cvt_pk_bf16_f32 v138, v180, v181\n"
"v_mfma_f32_16x16x32_bf16 a[56:59], a[248:251], v[112:115], a[56:59]\n"
"s_add_u32 m0, s86, 49152\n"
"ds_read_b64_tr_b16 a[224:225], v219 offset:8704\n"
"v_cvt_pk_bf16_f32 v139, v182, v183\n"
"v_mfma_f32_16x16x32_bf16 a[56:59], a[252:255], v[120:123], a[56:59]\n"
"ds_read_b64_tr_b16 a[226:227], v219 offset:9728\n"
"buffer_load_dwordx4 v246, s[12:15], s84 offen offset:0 lds\n"
"v_mfma_f32_16x16x32_bf16 a[60:63], a[248:251], v[116:119], a[60:63]\n"
"ds_read_b64_tr_b16 a[228:229], v219 offset:12800\n"
"v_fma_mix_f32 v184, v140, v160, v168 op_sel:[0,0,0] op_sel_hi:[1,0,0]\n"
"v_mfma_f32_16x16x32_bf16 a[60:63], a[252:255], v[124:127], a[60:63]\n"
"s_waitcnt lgkmcnt(11)\n"
"ds_read_b64_tr_b16 a[230:231], v219 offset:13824\n"
"v_fma_mix_f32 v185, v140, v161, v169 op_sel:[1,0,0] op_sel_hi:[1,0,0]\n"
"v_mfma_f32_16x16x32_bf16 a[64:67], a[160:163], v[112:115], a[64:67]\n"
"ds_read_b64_tr_b16 a[232:233], v219 offset:8712\n"
"v_fma_mix_f32 v186, v141, v162, v170 op_sel:[0,0,0] op_sel_hi:[1,0,0]\n"
"v_mfma_f32_16x16x32_bf16 a[64:67], a[164:167], v[120:123], a[64:67]\n"
"ds_read_b64_tr_b16 a[234:235], v219 offset:9736\n"
"v_fma_mix_f32 v187, v141, v163, v171 op_sel:[1,0,0] op_sel_hi:[1,0,0]\n"
"v_mfma_f32_16x16x32_bf16 a[68:71], a[160:163], v[116:119], a[68:71]\n"
"ds_read_b64_tr_b16 a[236:237], v219 offset:12808\n"
"v_fma_mix_f32 v188, v142, v164, v172 op_sel:[0,0,0] op_sel_hi:[1,0,0]\n"
"v_mfma_f32_16x16x32_bf16 a[68:71], a[164:167], v[124:127], a[68:71]\n"
"ds_read_b64_tr_b16 a[238:239], v219 offset:13832\n"
"buffer_load_dwordx4 v247, s[12:15], s84 offen offset:1024 lds\n"
"v_mfma_f32_16x16x32_bf16 a[72:75], a[168:171], v[112:115], a[72:75]\n"
"ds_read_b64_tr_b16 a[240:241], v219 offset:8960\n"
"v_fma_mix_f32 v189, v142, v165, v173 op_sel:[1,0,0] op_sel_hi:[1,0,0]\n"
"v_mfma_f32_16x16x32_bf16 a[72:75], a[172:175], v[120:123], a[72:75]\n"
"ds_read_b64_tr_b16 a[242:243], v219 offset:9984\n"
"v_fma_mix_f32 v190, v143, v166, v174 op_sel:[0,0,0] op_sel_hi:[1,0,0]\n"
"v_mfma_f32_16x16x32_bf16 a[76:79], a[168:171], v[116:119], a[76:79]\n"
"ds_read_b64_tr_b16 a[244:245], v219 offset:13056\n"
"v_fma_mix_f32 v191, v143, v167, v175 op_sel:[1,0,0] op_sel_hi:[1,0,0]\n"
"v_mfma_f32_16x16x32_bf16 a[76:79], a[172:175], v[124:127], a[76:79]\n"
"s_waitcnt lgkmcnt(11)\n"
"ds_read_b64_tr_b16 a[246:247], v219 offset:14080\n"
"v_cvt_pk_bf16_f32 v140, v184, v185\n"
"v_mfma_f32_16x16x32_bf16 a[80:83], a[176:179], v[112:115], a[80:83]\n"
"ds_read_b64_tr_b16 a[248:249], v219 offset:8968\n"
"v_cvt_pk_bf16_f32 v141, v186, v187\n"
"v_mfma_f32_16x16x32_bf16 a[80:83], a[180:183], v[120:123], a[80:83]\n"
"ds_read_b64_tr_b16 a[250:251], v219 offset:9992\n"
"buffer_load_dwordx4 v246, s[12:15], s84 offen offset:2048 lds\n"
"v_mfma_f32_16x16x32_bf16 a[84:87], a[176:179], v[116:119], a[84:87]\n"
"ds_read_b64_tr_b16 a[252:253], v219 offset:13064\n"
"v_cvt_pk_bf16_f32 v142, v188, v189\n"
"v_mfma_f32_16x16x32_bf16 a[84:87], a[180:183], v[124:127], a[84:87]\n"
"ds_read_b64_tr_b16 a[254:255], v219 offset:14088\n"
"v_cvt_pk_bf16_f32 v143, v190, v191\n"
"v_mfma_f32_16x16x32_bf16 a[88:91], a[184:187], v[112:115], a[88:91]\n"
"ds_read_b64_tr_b16 a[160:161], v219 offset:32784\n"
"v_mfma_f32_16x16x32_bf16 a[88:91], a[188:191], v[120:123], a[88:91]\n"
"ds_read_b64_tr_b16 a[162:163], v219 offset:33808\n"
"v_mfma_f32_16x16x32_bf16 a[92:95], a[184:187], v[116:119], a[92:95]\n"
"ds_read_b64_tr_b16 a[164:165], v219 offset:36880\n"
"v_mfma_f32_16x16x32_bf16 a[92:95], a[188:191], v[124:127], a[92:95]\n"
"s_waitcnt lgkmcnt(11)\n"
"buffer_load_dwordx4 v247, s[12:15], s84 offen offset:3072 lds\n"
"ds_read_b64_tr_b16 a[166:167], v219 offset:37904\n"
"v_mfma_f32_16x16x32_bf16 a[96:99], a[224:227], v[112:115], a[96:99]\n"
"s_add_u32 s84, s84, 16384\n"
"ds_read_b64_tr_b16 a[168:169], v219 offset:32792\n"
"v_mfma_f32_16x16x32_bf16 a[96:99], a[228:231], v[120:123], a[96:99]\n"
"ds_read_b64_tr_b16 a[170:171], v219 offset:33816\n"
"v_mfma_f32_16x16x32_bf16 a[100:103], a[224:227], v[116:119], a[100:103]\n"
"ds_read_b64_tr_b16 a[172:173], v219 offset:36888\n"
"v_mfma_f32_16x16x32_bf16 a[100:103], a[228:231], v[124:127], a[100:103]\n"
"ds_read_b64_tr_b16 a[174:175], v219 offset:37912\n"
"v_mfma_f32_16x16x32_bf16 a[104:107], a[232:235], v[112:115], a[104:107]\n"
"ds_read_b64_tr_b16 a[176:177], v219 offset:33040\n"
"v_mfma_f32_16x16x32_bf16 a[104:107], a[236:239], v[120:123], a[104:107]\n"
"ds_read_b64_tr_b16 a[178:179], v219 offset:34064\n"
"v_mfma_f32_16x16x32_bf16 a[108:111], a[232:235], v[116:119], a[108:111]\n"
"ds_read_b64_tr_b16 a[180:181], v219 offset:37136\n"
"v_mfma_f32_16x16x32_bf16 a[108:111], a[236:239], v[124:127], a[108:111]\n"
"s_waitcnt lgkmcnt(11)\n"
"ds_read_b64_tr_b16 a[182:183], v219 offset:38160\n"
"v_mfma_f32_16x16x32_bf16 a[112:115], a[240:243], v[112:115], a[112:115]\n"
"ds_read_b64_tr_b16 a[184:185], v219 offset:33048\n"
"v_mfma_f32_16x16x32_bf16 a[112:115], a[244:247], v[120:123], a[112:115]\n"
"ds_read_b64_tr_b16 a[186:187], v219 offset:34072\n"
"v_mfma_f32_16x16x32_bf16 a[116:119], a[240:243], v[116:119], a[116:119]\n"
"ds_read_b64_tr_b16 a[188:189], v219 offset:37144\n"
"v_mfma_f32_16x16x32_bf16 a[116:119], a[244:247], v[124:127], a[116:119]\n"
"ds_read_b64_tr_b16 a[190:191], v219 offset:38168\n"
"v_mfma_f32_16x16x32_bf16 a[120:123], a[248:251], v[112:115], a[120:123]\n"
"ds_read_b64_tr_b16 a[224:225], v219 offset:33296\n"
"v_mfma_f32_16x16x32_bf16 a[120:123], a[252:255], v[120:123], a[120:123]\n"
"ds_read_b64_tr_b16 a[226:227], v219 offset:34320\n"
"v_mfma_f32_16x16x32_bf16 a[124:127], a[248:251], v[116:119], a[124:127]\n"
"ds_read_b64_tr_b16 a[228:229], v219 offset:37392\n"
"v_mfma_f32_16x16x32_bf16 a[124:127], a[252:255], v[124:127], a[124:127]\n"
"s_waitcnt lgkmcnt(11)\n"
"ds_read_b64_tr_b16 a[230:231], v219 offset:38416\n"
"v_mfma_f32_16x16x32_bf16 a[0:3], a[160:163], v[128:131], a[0:3]\n"
"ds_read_b64_tr_b16 a[232:233], v219 offset:33304\n"
"v_mfma_f32_16x16x32_bf16 a[0:3], a[164:167], v[136:139], a[0:3]\n"
"ds_read_b64_tr_b16 a[234:235], v219 offset:34328\n"
"v_mfma_f32_16x16x32_bf16 a[4:7], a[160:163], v[132:135], a[4:7]\n"
"s_waitcnt vmcnt(8)\n"
"s_barrier\n"
"ds_read_b64_tr_b16 a[236:237], v219 offset:37400\n"
"v_mfma_f32_16x16x32_bf16 a[4:7], a[164:167], v[140:143], a[4:7]\n"
"ds_read_b64_tr_b16 a[238:239], v219 offset:38424\n"
"v_mfma_f32_16x16x32_bf16 a[8:11], a[168:171], v[128:131], a[8:11]\n"
"s_add_u32 m0, s86, 65536\n"
"ds_read_b64_tr_b16 a[240:241], v219 offset:33552\n"
"v_mfma_f32_16x16x32_bf16 a[8:11], a[172:175], v[136:139], a[8:11]\n"
"ds_read_b64_tr_b16 a[242:243], v219 offset:34576\n"
"buffer_load_dwordx4 v246, s[8:11], s84 offen offset:0 lds\n"
"v_mfma_f32_16x16x32_bf16 a[12:15], a[168:171], v[132:135], a[12:15]\n"
"ds_read_b64_tr_b16 a[244:245], v219 offset:37648\n"
"v_accvgpr_read_b32 v176, a0\n"
"v_mfma_f32_16x16x32_bf16 a[12:15], a[172:175], v[140:143], a[12:15]\n"
"s_waitcnt lgkmcnt(11)\n"
"ds_read_b64_tr_b16 a[246:247], v219 offset:38672\n"
"v_accvgpr_read_b32 v177, a1\n"
"v_mfma_f32_16x16x32_bf16 a[16:19], a[176:179], v[128:131], a[16:19]\n"
"ds_read_b64_tr_b16 a[248:249], v219 offset:33560\n"
"v_accvgpr_read_b32 v178, a2\n"
"v_mfma_f32_16x16x32_bf16 a[16:19], a[180:183], v[136:139], a[16:19]\n"
"ds_read_b64_tr_b16 a[250:251], v219 offset:34584\n"
"v_accvgpr_read_b32 v179, a3\n"
"v_mfma_f32_16x16x32_bf16 a[20:23], a[176:179], v[132:135], a[20:23]\n"
"ds_read_b64_tr_b16 a[252:253], v219 offset:37656\n"
"v_cvt_pk_bf16_f32 v64, v176, v177\n"
"v_mfma_f32_16x16x32_bf16 a[20:23], a[180:183], v[140:143], a[20:23]\n"
"ds_read_b64_tr_b16 a[254:255], v219 offset:38680\n"
"buffer_load_dwordx4 v246, s[8:11], s84 offen offset:1024 lds\n"
"v_mfma_f32_16x16x32_bf16 a[24:27], a[184:187], v[128:131], a[24:27]\n"
"ds_read_b64_tr_b16 a[160:161], v219 offset:40976\n"
"v_cvt_pk_bf16_f32 v65, v178, v179\n"
"v_mfma_f32_16x16x32_bf16 a[24:27], a[188:191], v[136:139], a[24:27]\n"
"ds_read_b64_tr_b16 a[162:163], v219 offset:42000\n"
"v_accvgpr_read_b32 v180, a4\n"
"v_mfma_f32_16x16x32_bf16 a[28:31], a[184:187], v[132:135], a[28:31]\n"
"ds_read_b64_tr_b16 a[164:165], v219 offset:45072\n"
"v_accvgpr_read_b32 v181, a5\n"
"v_mfma_f32_16x16x32_bf16 a[28:31], a[188:191], v[140:143], a[28:31]\n"
"s_waitcnt lgkmcnt(11)\n"
"ds_read_b64_tr_b16 a[166:167], v219 offset:46096\n"
"v_accvgpr_read_b32 v182, a6\n"
"v_mfma_f32_16x16x32_bf16 a[32:35], a[224:227], v[128:131], a[32:35]\n"
"ds_read_b64_tr_b16 a[168:169], v219 offset:40984\n"
"v_accvgpr_read_b32 v183, a7\n"
"v_mfma_f32_16x16x32_bf16 a[32:35], a[228:231], v[136:139], a[32:35]\n"
"ds_read_b64_tr_b16 a[170:171], v219 offset:42008\n"
"buffer_load_dwordx4 v247, s[8:11], s84 offen offset:2048 lds\n"
"v_mfma_f32_16x16x32_bf16 a[36:39], a[224:227], v[132:135], a[36:39]\n"
"ds_read_b64_tr_b16 a[172:173], v219 offset:45080\n"
"v_cvt_pk_bf16_f32 v96, v180, v181\n"
"v_mfma_f32_16x16x32_bf16 a[36:39], a[228:231], v[140:143], a[36:39]\n"
"ds_read_b64_tr_b16 a[174:175], v219 offset:46104\n"
"v_cvt_pk_bf16_f32 v97, v182, v183\n"
"v_mfma_f32_16x16x32_bf16 a[40:43], a[232:235], v[128:131], a[40:43]\n"
"ds_read_b64_tr_b16 a[176:177], v219 offset:41232\n"
"v_accvgpr_read_b32 v184, a8\n"
"v_mfma_f32_16x16x32_bf16 a[40:43], a[236:239], v[136:139], a[40:43]\n"
"ds_read_b64_tr_b16 a[178:179], v219 offset:42256\n"
"v_accvgpr_read_b32 v185, a9\n"
"v_mfma_f32_16x16x32_bf16 a[44:47], a[232:235], v[132:135], a[44:47]\n"
"ds_read_b64_tr_b16 a[180:181], v219 offset:45328\n"
"v_accvgpr_read_b32 v186, a10\n"
"v_mfma_f32_16x16x32_bf16 a[44:47], a[236:239], v[140:143], a[44:47]\n"
"s_waitcnt lgkmcnt(11)\n"
"ds_read_b64_tr_b16 a[182:183], v219 offset:46352\n"
"buffer_load_dwordx4 v247, s[8:11], s84 offen offset:3072 lds\n"
"v_mfma_f32_16x16x32_bf16 a[48:51], a[240:243], v[128:131], a[48:51]\n"
"ds_read_b64_tr_b16 a[184:185], v219 offset:41240\n"
"v_accvgpr_read_b32 v187, a11\n"
"v_mfma_f32_16x16x32_bf16 a[48:51], a[244:247], v[136:139], a[48:51]\n"
"ds_read_b64_tr_b16 a[186:187], v219 offset:42264\n"
"v_cvt_pk_bf16_f32 v66, v184, v185\n"
"v_mfma_f32_16x16x32_bf16 a[52:55], a[240:243], v[132:135], a[52:55]\n"
"ds_read_b64_tr_b16 a[188:189], v219 offset:45336\n"
"v_cvt_pk_bf16_f32 v67, v186, v187\n"
"v_mfma_f32_16x16x32_bf16 a[52:55], a[244:247], v[140:143], a[52:55]\n"
"ds_read_b64_tr_b16 a[190:191], v219 offset:46360\n"
"v_accvgpr_read_b32 v188, a12\n"
"v_mfma_f32_16x16x32_bf16 a[56:59], a[248:251], v[128:131], a[56:59]\n"
"s_add_u32 m0, s86, 81920\n"
"ds_read_b64_tr_b16 a[224:225], v219 offset:41488\n"
"v_accvgpr_read_b32 v189, a13\n"
"v_mfma_f32_16x16x32_bf16 a[56:59], a[252:255], v[136:139], a[56:59]\n"
"ds_read_b64_tr_b16 a[226:227], v219 offset:42512\n"
"buffer_load_dwordx4 v246, s[12:15], s84 offen offset:0 lds\n"
"v_mfma_f32_16x16x32_bf16 a[60:63], a[248:251], v[132:135], a[60:63]\n"
"ds_read_b64_tr_b16 a[228:229], v219 offset:45584\n"
"v_accvgpr_read_b32 v190, a14\n"
"v_mfma_f32_16x16x32_bf16 a[60:63], a[252:255], v[140:143], a[60:63]\n"
"s_waitcnt lgkmcnt(11)\n"
"ds_read_b64_tr_b16 a[230:231], v219 offset:46608\n"
"v_accvgpr_read_b32 v191, a15\n"
"v_mfma_f32_16x16x32_bf16 a[64:67], a[160:163], v[128:131], a[64:67]\n"
"ds_read_b64_tr_b16 a[232:233], v219 offset:41496\n"
"v_cvt_pk_bf16_f32 v98, v188, v189\n"
"v_mfma_f32_16x16x32_bf16 a[64:67], a[164:167], v[136:139], a[64:67]\n"
"ds_read_b64_tr_b16 a[234:235], v219 offset:42520\n"
"v_cvt_pk_bf16_f32 v99, v190, v191\n"
"v_mfma_f32_16x16x32_bf16 a[68:71], a[160:163], v[132:135], a[68:71]\n"
"ds_read_b64_tr_b16 a[236:237], v219 offset:45592\n"
"v_accvgpr_read_b32 v176, a16\n"
"v_mfma_f32_16x16x32_bf16 a[68:71], a[164:167], v[140:143], a[68:71]\n"
"ds_read_b64_tr_b16 a[238:239], v219 offset:46616\n"
"buffer_load_dwordx4 v247, s[12:15], s84 offen offset:1024 lds\n"
"v_mfma_f32_16x16x32_bf16 a[72:75], a[168:171], v[128:131], a[72:75]\n"
"ds_read_b64_tr_b16 a[240:241], v219 offset:41744\n"
"v_accvgpr_read_b32 v177, a17\n"
"v_mfma_f32_16x16x32_bf16 a[72:75], a[172:175], v[136:139], a[72:75]\n"
"ds_read_b64_tr_b16 a[242:243], v219 offset:42768\n"
"v_accvgpr_read_b32 v178, a18\n"
"v_mfma_f32_16x16x32_bf16 a[76:79], a[168:171], v[132:135], a[76:79]\n"
"ds_read_b64_tr_b16 a[244:245], v219 offset:45840\n"
"v_accvgpr_read_b32 v179, a19\n"
"v_mfma_f32_16x16x32_bf16 a[76:79], a[172:175], v[140:143], a[76:79]\n"
"s_waitcnt lgkmcnt(11)\n"
"ds_read_b64_tr_b16 a[246:247], v219 offset:46864\n"
"v_cvt_pk_bf16_f32 v68, v176, v177\n"
"v_mfma_f32_16x16x32_bf16 a[80:83], a[176:179], v[128:131], a[80:83]\n"
"ds_read_b64_tr_b16 a[248:249], v219 offset:41752\n"
"v_cvt_pk_bf16_f32 v69, v178, v179\n"
"v_mfma_f32_16x16x32_bf16 a[80:83], a[180:183], v[136:139], a[80:83]\n"
"ds_read_b64_tr_b16 a[250:251], v219 offset:42776\n"
"buffer_load_dwordx4 v246, s[12:15], s84 offen offset:2048 lds\n"
"v_mfma_f32_16x16x32_bf16 a[84:87], a[176:179], v[132:135], a[84:87]\n"
"ds_read_b64_tr_b16 a[252:253], v219 offset:45848\n"
"v_accvgpr_read_b32 v180, a20\n"
"v_mfma_f32_16x16x32_bf16 a[84:87], a[180:183], v[140:143], a[84:87]\n"
"ds_read_b64_tr_b16 a[254:255], v219 offset:46872\n"
"v_accvgpr_read_b32 v181, a21\n"
"v_mfma_f32_16x16x32_bf16 a[88:91], a[184:187], v[128:131], a[88:91]\n"
"ds_read_b128 a[160:163], v254 offset:0\n"
"v_accvgpr_read_b32 v182, a22\n"
"v_mfma_f32_16x16x32_bf16 a[88:91], a[188:191], v[136:139], a[88:91]\n"
"ds_read_b128 a[164:167], v254 offset:256\n"
"v_accvgpr_read_b32 v183, a23\n"
"v_mfma_f32_16x16x32_bf16 a[92:95], a[184:187], v[132:135], a[92:95]\n"
"ds_read_b128 a[168:171], v254 offset:512\n"
"v_cvt_pk_bf16_f32 v100, v180, v181\n"
"v_mfma_f32_16x16x32_bf16 a[92:95], a[188:191], v[140:143], a[92:95]\n"
"s_waitcnt lgkmcnt(11)\n"
"buffer_load_dwordx4 v247, s[12:15], s84 offen offset:3072 lds\n"
"ds_read_b128 a[172:175], v254 offset:768\n"
"v_mfma_f32_16x16x32_bf16 a[96:99], a[224:227], v[128:131], a[96:99]\n"
"s_add_u32 s84, s84, 16384\n"
"ds_read_b128 a[128:131], v240 offset:0\n"
"v_cvt_pk_bf16_f32 v101, v182, v183\n"
"v_mfma_f32_16x16x32_bf16 a[96:99], a[228:231], v[136:139], a[96:99]\n"
"ds_read_b128 a[132:135], v240 offset:256\n"
"v_accvgpr_read_b32 v184, a24\n"
"v_mfma_f32_16x16x32_bf16 a[100:103], a[224:227], v[132:135], a[100:103]\n"
"ds_read_b128 a[136:139], v240 offset:512\n"
"v_accvgpr_read_b32 v185, a25\n"
"v_mfma_f32_16x16x32_bf16 a[100:103], a[228:231], v[140:143], a[100:103]\n"
"ds_read_b128 a[140:143], v240 offset:768\n"
"v_accvgpr_read_b32 v186, a26\n"
"v_mfma_f32_16x16x32_bf16 a[104:107], a[232:235], v[128:131], a[104:107]\n"
"ds_read_b128 a[144:147], v240 offset:4096\n"
"v_accvgpr_read_b32 v187, a27\n"
"v_mfma_f32_16x16x32_bf16 a[104:107], a[236:239], v[136:139], a[104:107]\n"
"ds_read_b128 a[148:151], v240 offset:4352\n"
"v_cvt_pk_bf16_f32 v70, v184, v185\n"
"v_mfma_f32_16x16x32_bf16 a[108:111], a[232:235], v[132:135], a[108:111]\n"
"ds_read_b128 a[152:155], v240 offset:4608\n"
"v_cvt_pk_bf16_f32 v71, v186, v187\n"
"v_mfma_f32_16x16x32_bf16 a[108:111], a[236:239], v[140:143], a[108:111]\n"
"s_waitcnt lgkmcnt(11)\n"
"ds_read_b128 a[156:159], v240 offset:4864\n"
"v_accvgpr_read_b32 v188, a28\n"
"v_mfma_f32_16x16x32_bf16 a[112:115], a[240:243], v[128:131], a[112:115]\n"
"ds_read_b64_tr_b16 a[192:193], v242 offset:16384\n"
"v_accvgpr_read_b32 v189, a29\n"
"v_mfma_f32_16x16x32_bf16 a[112:115], a[244:247], v[136:139], a[112:115]\n"
"ds_read_b64_tr_b16 a[194:195], v242 offset:20480\n"
"v_accvgpr_read_b32 v190, a30\n"
"v_mfma_f32_16x16x32_bf16 a[116:119], a[240:243], v[132:135], a[116:119]\n"
"ds_read_b64_tr_b16 a[196:197], v243 offset:16384\n"
"v_accvgpr_read_b32 v191, a31\n"
"v_mfma_f32_16x16x32_bf16 a[116:119], a[244:247], v[140:143], a[116:119]\n"
"ds_read_b64_tr_b16 a[198:199], v243 offset:20480\n"
"v_cvt_pk_bf16_f32 v102, v188, v189\n"
"v_mfma_f32_16x16x32_bf16 a[120:123], a[248:251], v[128:131], a[120:123]\n"
"ds_read_b64_tr_b16 a[200:201], v242 offset:16640\n"
"v_cvt_pk_bf16_f32 v103, v190, v191\n"
"v_mfma_f32_16x16x32_bf16 a[120:123], a[252:255], v[136:139], a[120:123]\n"
"ds_read_b64_tr_b16 a[202:203], v242 offset:20736\n"
"v_accvgpr_read_b32 v176, a32\n"
"v_mfma_f32_16x16x32_bf16 a[124:127], a[248:251], v[132:135], a[124:127]\n"
"ds_read_b64_tr_b16 a[204:205], v243 offset:16640\n"
"v_accvgpr_read_b32 v177, a33\n"
"v_mfma_f32_16x16x32_bf16 a[124:127], a[252:255], v[140:143], a[124:127]\n"
"ds_read_b64_tr_b16 a[206:207], v243 offset:20736\n"
"v_accvgpr_read_b32 v178, a34\n"
"s_barrier\n"
"ds_read_b64_tr_b16 a[208:209], v242 offset:16896\n"
"v_accvgpr_read_b32 v179, a35\n"
"v_cvt_pk_bf16_f32 v72, v176, v177\n"
"v_cvt_pk_bf16_f32 v73, v178, v179\n"
"ds_read_b64_tr_b16 a[210:211], v242 offset:20992\n"
"v_accvgpr_read_b32 v180, a36\n"
"v_accvgpr_read_b32 v181, a37\n"
"v_accvgpr_read_b32 v182, a38\n"
"ds_read_b64_tr_b16 a[212:213], v243 offset:16896\n"
"v_accvgpr_read_b32 v183, a39\n"
"v_cvt_pk_bf16_f32 v104, v180, v181\n"
"v_cvt_pk_bf16_f32 v105, v182, v183\n"
"ds_read_b64_tr_b16 a[214:215], v243 offset:20992\n"
"v_accvgpr_read_b32 v184, a40\n"
"v_accvgpr_read_b32 v185, a41\n"
"v_accvgpr_read_b32 v186, a42\n"
"ds_read_b64_tr_b16 a[216:217], v242 offset:17152\n"
"v_accvgpr_read_b32 v187, a43\n"
"v_cvt_pk_bf16_f32 v74, v184, v185\n"
"v_cvt_pk_bf16_f32 v75, v186, v187\n"
"ds_read_b64_tr_b16 a[218:219], v242 offset:21248\n"
"v_accvgpr_read_b32 v188, a44\n"
"v_accvgpr_read_b32 v189, a45\n"
"v_accvgpr_read_b32 v190, a46\n"
"ds_read_b64_tr_b16 a[220:221], v243 offset:17152\n"
"v_accvgpr_read_b32 v191, a47\n"
"v_cvt_pk_bf16_f32 v106, v188, v189\n"
"v_cvt_pk_bf16_f32 v107, v190, v191\n"
"ds_read_b64_tr_b16 a[222:223], v243 offset:21248\n"
"v_accvgpr_read_b32 v176, a48\n"
"v_accvgpr_read_b32 v177, a49\n"
"v_accvgpr_read_b32 v178, a50\n"
"v_accvgpr_read_b32 v179, a51\n"
"v_cvt_pk_bf16_f32 v76, v176, v177\n"
"v_cvt_pk_bf16_f32 v77, v178, v179\n"
"v_accvgpr_read_b32 v180, a52\n"
"s_add_u32 m0, s86, 98304\n"
"v_accvgpr_read_b32 v181, a53\n"
"v_accvgpr_read_b32 v182, a54\n"
"v_accvgpr_read_b32 v183, a55\n"
"v_cvt_pk_bf16_f32 v108, v180, v181\n"
"buffer_load_dwordx4 v246, s[8:11], s84 offen offset:0 lds\n"
"v_cvt_pk_bf16_f32 v109, v182, v183\n"
"v_accvgpr_read_b32 v184, a56\n"
"v_accvgpr_read_b32 v185, a57\n"
"v_accvgpr_read_b32 v186, a58\n"
"v_accvgpr_read_b32 v187, a59\n"
"v_cvt_pk_bf16_f32 v78, v184, v185\n"
"v_cvt_pk_bf16_f32 v79, v186, v187\n"
"v_accvgpr_read_b32 v188, a60\n"
"v_accvgpr_read_b32 v189, a61\n"
"v_accvgpr_read_b32 v190, a62\n"
"v_accvgpr_read_b32 v191, a63\n"
"buffer_load_dwordx4 v246, s[8:11], s84 offen offset:1024 lds\n"
"v_cvt_pk_bf16_f32 v110, v188, v189\n"
"v_cvt_pk_bf16_f32 v111, v190, v191\n"
"v_accvgpr_read_b32 v176, a64\n"
"s_nop 1\n"
"v_accvgpr_read_b32 v177, a65\n"
"v_accvgpr_read_b32 v178, a66\n"
"v_accvgpr_read_b32 v179, a67\n"
"v_cvt_pk_bf16_f32 v80, v176, v177\n"
"s_waitcnt lgkmcnt(14)\n"
"v_mfma_f32_16x16x32_bf16 v[144:147], a[160:163], v[64:67], 0\n"
"v_cvt_pk_bf16_f32 v81, v178, v179\n"
"v_accvgpr_read_b32 v180, a68\n"
"v_mfma_f32_16x16x32_bf16 v[144:147], a[164:167], v[68:71], v[144:147]\n"
"buffer_load_dwordx4 v247, s[8:11], s84 offen offset:2048 lds\n"
"v_accvgpr_read_b32 v181, a69\n"
"v_mfma_f32_16x16x32_bf16 v[144:147], a[168:171], v[72:75], v[144:147]\n"
"v_accvgpr_read_b32 v182, a70\n"
"v_accvgpr_read_b32 v183, a71\n"
"v_mfma_f32_16x16x32_bf16 v[144:147], a[172:175], v[76:79], v[144:147]\n"
"v_cvt_pk_bf16_f32 v112, v180, v181\n"
"v_cvt_pk_bf16_f32 v113, v182, v183\n"
"v_mfma_f32_16x16x32_bf16 v[152:155], a[160:163], v[96:99], 0\n"
"buffer_load_dwordx4 v247, s[8:11], s84 offen offset:3072 lds\n"
"v_accvgpr_read_b32 v184, a72\n"
"v_mfma_f32_16x16x32_bf16 v[152:155], a[164:167], v[100:103], v[152:155]\n"
"v_accvgpr_read_b32 v185, a73\n"
"v_accvgpr_read_b32 v186, a74\n"
"v_mfma_f32_16x16x32_bf16 v[152:155], a[168:171], v[104:107], v[152:155]\n"
"s_add_u32 m0, s86, 114688\n"
"v_accvgpr_read_b32 v187, a75\n"
"v_cvt_pk_bf16_f32 v82, v184, v185\n"
"v_mfma_f32_16x16x32_bf16 v[152:155], a[172:175], v[108:111], v[152:155]\n"
"buffer_load_dwordx4 v246, s[12:15], s84 offen offset:0 lds\n"
"v_cvt_pk_bf16_f32 v83, v186, v187\n"
"v_accvgpr_read_b32 v188, a76\n"
"v_accvgpr_read_b32 v189, a77\n"
"v_accvgpr_read_b32 v190, a78\n"
"v_accvgpr_read_b32 v191, a79\n"
"v_cvt_pk_bf16_f32 v114, v188, v189\n"
"v_cvt_pk_bf16_f32 v115, v190, v191\n"
"v_accvgpr_read_b32 v176, a80\n"
"v_accvgpr_read_b32 v177, a81\n"
"buffer_load_dwordx4 v247, s[12:15], s84 offen offset:1024 lds\n"
"v_accvgpr_read_b32 v178, a82\n"
"v_accvgpr_read_b32 v179, a83\n"
"v_cvt_pk_bf16_f32 v84, v176, v177\n"
"v_cvt_pk_bf16_f32 v85, v178, v179\n"
"v_accvgpr_read_b32 v180, a84\n"
"v_accvgpr_read_b32 v181, a85\n"
"v_accvgpr_read_b32 v182, a86\n"
"v_accvgpr_read_b32 v183, a87\n"
"v_cvt_pk_bf16_f32 v116, v180, v181\n"
"v_cvt_pk_bf16_f32 v117, v182, v183\n"
"v_accvgpr_read_b32 v184, a88\n"
"buffer_load_dwordx4 v246, s[12:15], s84 offen offset:2048 lds\n"
"v_accvgpr_read_b32 v185, a89\n"
"v_accvgpr_read_b32 v186, a90\n"
"v_accvgpr_read_b32 v187, a91\n"
"v_cvt_pk_bf16_f32 v86, v184, v185\n"
"v_cvt_pk_bf16_f32 v87, v186, v187\n"
"v_accvgpr_read_b32 v188, a92\n"
"v_accvgpr_read_b32 v189, a93\n"
"v_accvgpr_read_b32 v190, a94\n"
"v_accvgpr_read_b32 v191, a95\n"
"v_cvt_pk_bf16_f32 v118, v188, v189\n"
"v_cvt_pk_bf16_f32 v119, v190, v191\n"
"buffer_load_dwordx4 v247, s[12:15], s84 offen offset:3072 lds\n"
"v_max3_f32 v144, v144, v145, v146\n"
"v_max_f32 v144, v144, v147\n"
"v_sub_f32 v24, 0xc2480000, v144\n"
"s_add_u32 s84, s84, 16384\n"
"v_sub_f32 v25, 0xc2480000, v144\n"
"v_sub_f32 v26, 0xc2480000, v144\n"
"v_sub_f32 v27, 0xc2480000, v144\n"
"v_accvgpr_read_b32 v176, a96\n"
"v_accvgpr_read_b32 v177, a97\n"
"v_accvgpr_read_b32 v178, a98\n"
"v_accvgpr_read_b32 v179, a99\n"
"v_cvt_pk_bf16_f32 v88, v176, v177\n"
"v_cvt_pk_bf16_f32 v89, v178, v179\n"
"v_accvgpr_read_b32 v180, a100\n"
"v_accvgpr_read_b32 v181, a101\n"
"v_accvgpr_read_b32 v182, a102\n"
"v_accvgpr_read_b32 v183, a103\n"
"v_cvt_pk_bf16_f32 v120, v180, v181\n"
"v_cvt_pk_bf16_f32 v121, v182, v183\n"
"v_accvgpr_read_b32 v184, a104\n"
"v_accvgpr_read_b32 v185, a105\n"
"v_accvgpr_read_b32 v186, a106\n"
"v_accvgpr_read_b32 v187, a107\n"
"v_cvt_pk_bf16_f32 v90, v184, v185\n"
"v_cvt_pk_bf16_f32 v91, v186, v187\n"
"v_accvgpr_read_b32 v188, a108\n"
"v_accvgpr_read_b32 v189, a109\n"
"v_accvgpr_read_b32 v190, a110\n"
"v_accvgpr_read_b32 v191, a111\n"
"v_cvt_pk_bf16_f32 v122, v188, v189\n"
"v_cvt_pk_bf16_f32 v123, v190, v191\n"
"v_accvgpr_read_b32 v176, a112\n"
"v_accvgpr_read_b32 v177, a113\n"
"v_accvgpr_read_b32 v178, a114\n"
"v_accvgpr_read_b32 v179, a115\n"
"v_cvt_pk_bf16_f32 v92, v176, v177\n"
"v_cvt_pk_bf16_f32 v93, v178, v179\n"
"v_accvgpr_read_b32 v180, a116\n"
"v_accvgpr_read_b32 v181, a117\n"
"v_accvgpr_read_b32 v182, a118\n"
"v_accvgpr_read_b32 v183, a119\n"
"v_cvt_pk_bf16_f32 v124, v180, v181\n"
"v_cvt_pk_bf16_f32 v125, v182, v183\n"
"v_accvgpr_read_b32 v184, a120\n"
"v_accvgpr_read_b32 v185, a121\n"
"v_accvgpr_read_b32 v186, a122\n"
"v_accvgpr_read_b32 v187, a123\n"
"v_cvt_pk_bf16_f32 v94, v184, v185\n"
"v_cvt_pk_bf16_f32 v95, v186, v187\n"
"v_accvgpr_read_b32 v188, a124\n"
"v_accvgpr_read_b32 v189, a125\n"
"v_accvgpr_read_b32 v190, a126\n"
"v_accvgpr_read_b32 v191, a127\n"
"v_cvt_pk_bf16_f32 v126, v188, v189\n"
"v_cvt_pk_bf16_f32 v127, v190, v191\n"
"s_nop 1\n"
"v_mfma_f32_16x16x32_bf16 v[148:151], a[160:163], v[80:83], 0\n"
"v_max3_f32 v152, v152, v153, v154\n"
"v_max_f32 v152, v152, v155\n"
"v_mfma_f32_16x16x32_bf16 v[148:151], a[164:167], v[84:87], v[148:151]\n"
"v_sub_f32 v32, 0xc2480000, v152\n"
"v_sub_f32 v33, 0xc2480000, v152\n"
"v_mfma_f32_16x16x32_bf16 v[148:151], a[168:171], v[88:91], v[148:151]\n"
"v_sub_f32 v34, 0xc2480000, v152\n"
"v_sub_f32 v35, 0xc2480000, v152\n"
"v_mfma_f32_16x16x32_bf16 v[148:151], a[172:175], v[92:95], v[148:151]\n"
"v_mov_b32 v40, 0x3f803f80\n"
"v_mov_b32 v41, 0x3f803f80\n"
"v_mfma_f32_16x16x32_bf16 v[156:159], a[160:163], v[112:115], 0\n"
"v_mov_b32 v42, 0x3f803f80\n"
"v_mov_b32 v43, 0x3f803f80\n"
"v_mfma_f32_16x16x32_bf16 v[156:159], a[164:167], v[116:119], v[156:159]\n"
"v_mov_b32 v20, 0\n"
"v_mov_b32 v21, 0\n"
"v_mfma_f32_16x16x32_bf16 v[156:159], a[168:171], v[120:123], v[156:159]\n"
"v_mov_b32 v22, 0\n"
"v_mov_b32 v23, 0\n"
"v_mfma_f32_16x16x32_bf16 v[156:159], a[172:175], v[124:127], v[156:159]\n"
"v_mov_b32 v44, 0\n"
"v_mov_b32 v52, 0\n"
"s_waitcnt lgkmcnt(0)\n"
"AT_main:\n"
"s_or_b32 s56, s42, s41\n"
"s_cmp_lg_u32 s56, 0\n"
"s_cbranch_scc1 AT_h1\n"
"v_lshlrev_b32 v214, 7, v248\n"
"s_mov_b32 s57, 32768\n"
"s_mov_b32 s58, 40960\n"
"s_mov_b32 s59, 49152\n"
"s_mov_b32 s60, 57344\n"
"buffer_load_dword v210, v214, s[28:31], s57 offen offset:4 sc1\n"
"buffer_load_dword v211, v214, s[28:31], s58 offen offset:4 sc1\n"
"buffer_load_dword v212, v214, s[28:31], s59 offen offset:4 sc1\n"
"buffer_load_dword v213, v214, s[28:31], s60 offen offset:4 sc1\n"
"AT_h1:\n"
"v_mfma_f32_16x16x32_bf16 v[0:3], a[128:131], v[64:67], v[24:27]\n"
"s_nop 0\n"
"ds_read_b128 a[160:163], v240 offset:8192\n"
"v_mfma_f32_16x16x32_bf16 v[0:3], a[132:135], v[68:71], v[0:3]\n"
"s_nop 0\n"
"v_mfma_f32_16x16x32_bf16 v[0:3], a[136:139], v[72:75], v[0:3]\n"
"s_nop 0\n"
"ds_read_b128 a[164:167], v240 offset:8448\n"
"v_mfma_f32_16x16x32_bf16 v[0:3], a[140:143], v[76:79], v[0:3]\n"
"v_max3_f32 v148, v148, v149, v150\n"
"v_max_f32 v148, v148, v151\n"
"v_mfma_f32_16x16x32_bf16 v[4:7], a[144:147], v[64:67], v[24:27]\n"
"ds_read_b128 a[168:171], v240 offset:8704\n"
"v_sub_f32 v28, 0xc2480000, v148\n"
"v_mfma_f32_16x16x32_bf16 v[4:7], a[148:151], v[68:71], v[4:7]\n"
"v_sub_f32 v29, 0xc2480000, v148\n"
"v_sub_f32 v30, 0xc2480000, v148\n"
"v_mfma_f32_16x16x32_bf16 v[4:7], a[152:155], v[72:75], v[4:7]\n"
"ds_read_b128 a[172:175], v240 offset:8960\n"
"v_sub_f32 v31, 0xc2480000, v148\n"
"v_mfma_f32_16x16x32_bf16 v[4:7], a[156:159], v[76:79], v[4:7]\n"
"s_nop 0\n"
"v_exp_f32 v0, v0\n"
"v_mfma_f32_16x16x32_bf16 a[96:99], v[40:43], v[20:23], 0\n"
"v_max3_f32 v156, v156, v157, v158\n"
"v_max_f32 v156, v156, v159\n"
"v_mfma_f32_16x16x32_bf16 a[100:103], v[40:43], v[20:23], 0\n"
"s_nop 0\n"
"v_exp_f32 v1, v1\n"
"v_mfma_f32_16x16x32_bf16 a[104:107], v[40:43], v[20:23], 0\n"
"ds_read_b128 a[176:179], v240 offset:12288\n"
"v_cvt_pk_bf16_f32 v16, v0, v1\n"
"v_mfma_f32_16x16x32_bf16 a[108:111], v[40:43], v[20:23], 0\n"
"s_nop 0\n"
"v_exp_f32 v2, v2\n"
"v_mfma_f32_16x16x32_bf16 a[112:115], v[40:43], v[20:23], 0\n"
"ds_read_b128 a[180:183], v240 offset:12544\n"
"v_add_f32 v44, v44, v0\n"
"v_mfma_f32_16x16x32_bf16 a[116:119], v[40:43], v[20:23], 0\n"
"s_nop 0\n"
"v_exp_f32 v3, v3\n"
"v_mfma_f32_16x16x32_bf16 a[120:123], v[40:43], v[20:23], 0\n"
"ds_read_b128 a[184:187], v240 offset:12800\n"
"v_cvt_pk_bf16_f32 v17, v2, v3\n"
"v_mfma_f32_16x16x32_bf16 a[124:127], v[40:43], v[20:23], 0\n"
"s_nop 0\n"
"v_exp_f32 v4, v4\n"
"v_mfma_f32_16x16x32_bf16 v[56:59], v[40:43], v[20:23], 0\n"
"ds_read_b128 a[188:191], v240 offset:13056\n"
"v_add_f32 v44, v44, v1\n"
"v_mfma_f32_16x16x32_bf16 v[8:11], a[128:131], v[80:83], v[28:31]\n"
"s_nop 0\n"
"v_exp_f32 v5, v5\n"
"v_mfma_f32_16x16x32_bf16 v[8:11], a[132:135], v[84:87], v[8:11]\n"
"ds_read_b64_tr_b16 a[224:225], v242 offset:24576\n"
"v_cvt_pk_bf16_f32 v18, v4, v5\n"
"v_mfma_f32_16x16x32_bf16 v[8:11], a[136:139], v[88:91], v[8:11]\n"
"s_nop 0\n"
"v_exp_f32 v6, v6\n"
"v_mfma_f32_16x16x32_bf16 v[8:11], a[140:143], v[92:95], v[8:11]\n"
"ds_read_b64_tr_b16 a[226:227], v242 offset:28672\n"
"v_add_f32 v44, v44, v2\n"
"v_mfma_f32_16x16x32_bf16 v[12:15], a[144:147], v[80:83], v[28:31]\n"
"s_nop 0\n"
"v_exp_f32 v7, v7\n"
"v_mfma_f32_16x16x32_bf16 v[12:15], a[148:151], v[84:87], v[12:15]\n"
"ds_read_b64_tr_b16 a[228:229], v243 offset:24576\n"
"v_cvt_pk_bf16_f32 v19, v6, v7\n"
"v_mfma_f32_16x16x32_bf16 v[12:15], a[152:155], v[88:91], v[12:15]\n"
"v_sub_f32 v36, 0xc2480000, v156\n"
"v_sub_f32 v37, 0xc2480000, v156\n"
"v_mfma_f32_16x16x32_bf16 v[12:15], a[156:159], v[92:95], v[12:15]\n"
"s_nop 0\n"
"v_exp_f32 v8, v8\n"
"v_mfma_f32_16x16x32_bf16 a[0:3], a[192:195], v[16:19], 0\n"
"ds_read_b64_tr_b16 a[230:231], v243 offset:28672\n"
"v_add_f32 v44, v44, v3\n"
"v_mfma_f32_16x16x32_bf16 a[4:7], a[196:199], v[16:19], 0\n"
"s_nop 0\n"
"v_exp_f32 v9, v9\n"
"v_mfma_f32_16x16x32_bf16 a[8:11], a[200:203], v[16:19], 0\n"
"ds_read_b64_tr_b16 a[232:233], v242 offset:24832\n"
"v_cvt_pk_bf16_f32 v20, v8, v9\n"
"v_mfma_f32_16x16x32_bf16 a[12:15], a[204:207], v[16:19], 0\n"
"s_nop 0\n"
"v_exp_f32 v10, v10\n"
"v_mfma_f32_16x16x32_bf16 a[16:19], a[208:211], v[16:19], 0\n"
"ds_read_b64_tr_b16 a[234:235], v242 offset:28928\n"
"v_add_f32 v44, v44, v4\n"
"v_mfma_f32_16x16x32_bf16 a[20:23], a[212:215], v[16:19], 0\n"
"s_nop 0\n"
"v_exp_f32 v11, v11\n"
"v_mfma_f32_16x16x32_bf16 a[24:27], a[216:219], v[16:19], 0\n"
"ds_read_b64_tr_b16 a[236:237], v243 offset:24832\n"
"v_cvt_pk_bf16_f32 v21, v10, v11\n"
"v_mfma_f32_16x16x32_bf16 a[28:31], a[220:223], v[16:19], 0\n"
"s_nop 0\n"
"v_exp_f32 v12, v12\n"
"v_mfma_f32_16x16x32_bf16 v[220:223], a[128:131], v[96:99], v[32:35]\n"
"ds_read_b64_tr_b16 a[238:239], v243 offset:28928\n"
"v_add_f32 v44, v44, v5\n"
"v_mfma_f32_16x16x32_bf16 v[220:223], a[132:135], v[100:103], v[220:223]\n"
"s_nop 0\n"
"v_exp_f32 v13, v13\n"
"v_mfma_f32_16x16x32_bf16 v[220:223], a[136:139], v[104:107], v[220:223]\n"
"ds_read_b64_tr_b16 a[240:241], v242 offset:25088\n"
"v_cvt_pk_bf16_f32 v22, v12, v13\n"
"v_mfma_f32_16x16x32_bf16 v[220:223], a[140:143], v[108:111], v[220:223]\n"
"s_nop 0\n"
"v_exp_f32 v14, v14\n"
"v_mfma_f32_16x16x32_bf16 v[224:227], a[144:147], v[96:99], v[32:35]\n"
"ds_read_b64_tr_b16 a[242:243], v242 offset:29184\n"
"v_add_f32 v44, v44, v6\n"
"v_mfma_f32_16x16x32_bf16 v[224:227], a[148:151], v[100:103], v[224:227]\n"
"s_nop 0\n"
"v_exp_f32 v15, v15\n"
"v_mfma_f32_16x16x32_bf16 v[224:227], a[152:155], v[104:107], v[224:227]\n"
"ds_read_b64_tr_b16 a[244:245], v243 offset:25088\n"
"v_cvt_pk_bf16_f32 v23, v14, v15\n"
"v_mfma_f32_16x16x32_bf16 v[224:227], a[156:159], v[108:111], v[224:227]\n"
"s_nop 0\n"
"v_exp_f32 v220, v220\n"
"v_mfma_f32_16x16x32_bf16 a[32:35], a[192:195], v[20:23], 0\n"
"v_sub_f32 v38, 0xc2480000, v156\n"
"v_sub_f32 v39, 0xc2480000, v156\n"
"v_mfma_f32_16x16x32_bf16 a[36:39], a[196:199], v[20:23], 0\n"
"s_and_b32 s62, s42, 7\n"
"v_exp_f32 v221, v221\n"
"v_mfma_f32_16x16x32_bf16 a[40:43], a[200:203], v[20:23], 0\n"
"s_mul_i32 s46, s62, 663552\n"
"ds_read_b64_tr_b16 a[246:247], v243 offset:29184\n"
"v_cvt_pk_bf16_f32 v16, v220, v221\n"
"v_mfma_f32_16x16x32_bf16 a[44:47], a[204:207], v[20:23], 0\n"
"s_lshr_b32 s47, s43, 2\n"
"v_exp_f32 v222, v222\n"
"v_mfma_f32_16x16x32_bf16 a[48:51], a[208:211], v[20:23], 0\n"
"s_mul_i32 s47, s47, 1179648\n"
"ds_read_b64_tr_b16 a[248:249], v242 offset:25344\n"
"v_add_f32 v44, v44, v7\n"
"v_mfma_f32_16x16x32_bf16 a[52:55], a[212:215], v[20:23], 0\n"
"s_getpc_b64 s[32:33]\n"
"AT_pc1:\n"
"s_sub_u32 s32, s32, AT_pc1-AT_start\n"
"s_subb_u32 s33, s33, 0\n"
"v_exp_f32 v223, v223\n"
"v_mfma_f32_16x16x32_bf16 a[56:59], a[216:219], v[20:23], 0\n"
"s_and_b32 s33, s33, 0xffff\n"
"ds_read_b64_tr_b16 a[250:251], v242 offset:29440\n"
"v_cvt_pk_bf16_f32 v17, v222, v223\n"
"v_mfma_f32_16x16x32_bf16 a[60:63], a[220:223], v[20:23], 0\n"
"s_mov_b32 s34, AT_kend-AT_start\n"
"v_exp_f32 v224, v224\n"
"v_mfma_f32_16x16x32_bf16 v[48:51], v[40:43], v[20:23], 0\n"
"s_mov_b32 s35, 0x20000\n"
"ds_read_b64_tr_b16 a[252:253], v243 offset:25344\n"
"v_add_f32 v52, v52, v220\n"
"v_mfma_f32_16x16x32_bf16 v[228:231], a[128:131], v[112:115], v[36:39]\n"
"s_lshr_b32 s62, s42, 3\n"
"v_exp_f32 v225, v225\n"
"v_mfma_f32_16x16x32_bf16 v[228:231], a[132:135], v[116:119], v[228:231]\n"
"s_lshl_b32 s62, s62, 2\n"
"ds_read_b64_tr_b16 a[254:255], v243 offset:29440\n"
"v_cvt_pk_bf16_f32 v18, v224, v225\n"
"v_mfma_f32_16x16x32_bf16 v[228:231], a[136:139], v[120:123], v[228:231]\n"
"s_add_u32 s62, s62, s41\n"
"v_exp_f32 v226, v226\n"
"v_mfma_f32_16x16x32_bf16 v[228:231], a[140:143], v[124:127], v[228:231]\n"
"s_and_b32 s62, s62, 31\n"
"v_add_f32 v52, v52, v221\n"
"v_add_f32 v52, v52, v222\n"
"v_mfma_f32_16x16x32_bf16 v[232:235], a[144:147], v[112:115], v[36:39]\n"
"s_lshl_b32 s62, s62, 12\n"
"v_exp_f32 v227, v227\n"
"v_mfma_f32_16x16x32_bf16 v[232:235], a[148:151], v[116:119], v[232:235]\n"
"v_cvt_pk_bf16_f32 v19, v226, v227\n"
"v_add_f32 v52, v52, v223\n"
"v_mfma_f32_16x16x32_bf16 v[232:235], a[152:155], v[120:123], v[232:235]\n"
"s_nop 0\n"
"v_lshl_add_u32 v255, v248, 6, s62\n"
"v_mfma_f32_16x16x32_bf16 v[232:235], a[156:159], v[124:127], v[232:235]\n"
"s_nop 0\n"
"v_exp_f32 v228, v228\n"
"v_mfma_f32_16x16x32_bf16 a[64:67], a[192:195], v[16:19], 0\n"
"v_add_f32 v52, v52, v224\n"
"v_add_f32 v52, v52, v225\n"
"v_mfma_f32_16x16x32_bf16 a[68:71], a[196:199], v[16:19], 0\n"
"s_nop 0\n"
"v_exp_f32 v229, v229\n"
"v_mfma_f32_16x16x32_bf16 a[72:75], a[200:203], v[16:19], 0\n"
"v_cvt_pk_bf16_f32 v20, v228, v229\n"
"v_add_f32 v52, v52, v226\n"
"v_mfma_f32_16x16x32_bf16 a[76:79], a[204:207], v[16:19], 0\n"
"s_nop 0\n"
"v_exp_f32 v230, v230\n"
"v_mfma_f32_16x16x32_bf16 a[80:83], a[208:211], v[16:19], 0\n"
"s_nop 0\n"
"v_add_f32 v52, v52, v227\n"
"v_mfma_f32_16x16x32_bf16 a[84:87], a[212:215], v[16:19], 0\n"
"s_nop 0\n"
"v_exp_f32 v231, v231\n"
"v_mfma_f32_16x16x32_bf16 a[88:91], a[216:219], v[16:19], 0\n"
"s_waitcnt vmcnt(16) lgkmcnt(0)\n"
"s_barrier\n"
"v_cvt_pk_bf16_f32 v21, v230, v231\n"
"v_mfma_f32_16x16x32_bf16 a[92:95], a[220:223], v[16:19], 0\n"
"s_nop 0\n"
"v_exp_f32 v232, v232\n"
"s_mov_b32 s87, 6\n"
".p2align 6\n"
"AT_loop:\n"
"v_mfma_f32_16x16x32_bf16 v[0:3], a[160:163], v[64:67], v[24:27]\n"
"ds_read_b128 a[128:131], v240 offset:32768\n"
"buffer_load_dword v250, v251, s[64:67], 0 offen\n"
"v_mfma_f32_16x16x32_bf16 v[0:3], a[164:167], v[68:71], v[0:3]\n"
"v_exp_f32 v233, v233\n"
"v_mfma_f32_16x16x32_bf16 v[0:3], a[168:171], v[72:75], v[0:3]\n"
"ds_read_b128 a[132:135], v240 offset:33024\n"
"buffer_load_dword v250, v255, s[32:35], 0 offen\n"
"v_cvt_pk_bf16_f32 v22, v232, v233\n"
"v_mfma_f32_16x16x32_bf16 v[0:3], a[172:175], v[76:79], v[0:3]\n"
"v_exp_f32 v234, v234\n"
"v_mfma_f32_16x16x32_bf16 v[4:7], a[176:179], v[64:67], v[24:27]\n"
"ds_read_b128 a[136:139], v240 offset:33280\n"
"v_mfma_f32_16x16x32_bf16 v[4:7], a[180:183], v[68:71], v[4:7]\n"
"v_exp_f32 v235, v235\n"
"v_mfma_f32_16x16x32_bf16 v[4:7], a[184:187], v[72:75], v[4:7]\n"
"ds_read_b128 a[140:143], v240 offset:33536\n"
"v_cvt_pk_bf16_f32 v23, v234, v235\n"
"v_mfma_f32_16x16x32_bf16 v[4:7], a[188:191], v[76:79], v[4:7]\n"
"s_mov_b32 m0, s86\n"
"v_exp_f32 v0, v0\n"
"v_mfma_f32_16x16x32_bf16 a[96:99], a[192:195], v[20:23], a[96:99]\n"
"buffer_load_dwordx4 v246, s[8:11], s84 offen offset:0 lds\n"
"v_mfma_f32_16x16x32_bf16 a[100:103], a[196:199], v[20:23], a[100:103]\n"
"v_exp_f32 v1, v1\n"
"v_mfma_f32_16x16x32_bf16 a[104:107], a[200:203], v[20:23], a[104:107]\n"
"ds_read_b128 a[144:147], v240 offset:36864\n"
"v_cvt_pk_bf16_f32 v16, v0, v1\n"
"v_mfma_f32_16x16x32_bf16 a[108:111], a[204:207], v[20:23], a[108:111]\n"
"v_exp_f32 v2, v2\n"
"v_mfma_f32_16x16x32_bf16 a[112:115], a[208:211], v[20:23], a[112:115]\n"
"ds_read_b128 a[148:151], v240 offset:37120\n"
"v_add_f32 v44, v44, v0\n"
"v_mfma_f32_16x16x32_bf16 a[116:119], a[212:215], v[20:23], a[116:119]\n"
"v_exp_f32 v3, v3\n"
"v_mfma_f32_16x16x32_bf16 a[120:123], a[216:219], v[20:23], a[120:123]\n"
"ds_read_b128 a[152:155], v240 offset:37376\n"
"v_cvt_pk_bf16_f32 v17, v2, v3\n"
"v_mfma_f32_16x16x32_bf16 a[124:127], a[220:223], v[20:23], a[124:127]\n"
"v_exp_f32 v4, v4\n"
"v_mfma_f32_16x16x32_bf16 v[56:59], v[40:43], v[20:23], v[56:59]\n"
"ds_read_b128 a[156:159], v240 offset:37632\n"
"v_add_f32 v44, v44, v1\n"
"v_mfma_f32_16x16x32_bf16 v[8:11], a[160:163], v[80:83], v[28:31]\n"
"v_exp_f32 v5, v5\n"
"v_mfma_f32_16x16x32_bf16 v[8:11], a[164:167], v[84:87], v[8:11]\n"
"ds_read_b64_tr_b16 a[192:193], v242 offset:49152\n"
"v_cvt_pk_bf16_f32 v18, v4, v5\n"
"v_mfma_f32_16x16x32_bf16 v[8:11], a[168:171], v[88:91], v[8:11]\n"
"v_exp_f32 v6, v6\n"
"v_mfma_f32_16x16x32_bf16 v[8:11], a[172:175], v[92:95], v[8:11]\n"
"ds_read_b64_tr_b16 a[194:195], v242 offset:53248\n"
"v_add_f32 v44, v44, v2\n"
"v_mfma_f32_16x16x32_bf16 v[12:15], a[176:179], v[80:83], v[28:31]\n"
"v_exp_f32 v7, v7\n"
"v_mfma_f32_16x16x32_bf16 v[12:15], a[180:183], v[84:87], v[12:15]\n"
"ds_read_b64_tr_b16 a[196:197], v243 offset:49152\n"
"v_cvt_pk_bf16_f32 v19, v6, v7\n"
"v_mfma_f32_16x16x32_bf16 v[12:15], a[184:187], v[88:91], v[12:15]\n"
"buffer_load_dwordx4 v246, s[8:11], s84 offen offset:1024 lds\n"
"v_mfma_f32_16x16x32_bf16 v[12:15], a[188:191], v[92:95], v[12:15]\n"
"s_waitcnt lgkmcnt(11)\n"
"v_exp_f32 v8, v8\n"
"v_mfma_f32_16x16x32_bf16 a[0:3], a[224:227], v[16:19], a[0:3]\n"
"ds_read_b64_tr_b16 a[198:199], v243 offset:53248\n"
"v_add_f32 v44, v44, v3\n"
"v_mfma_f32_16x16x32_bf16 a[4:7], a[228:231], v[16:19], a[4:7]\n"
"v_exp_f32 v9, v9\n"
"v_mfma_f32_16x16x32_bf16 a[8:11], a[232:235], v[16:19], a[8:11]\n"
"ds_read_b64_tr_b16 a[200:201], v242 offset:49408\n"
"v_cvt_pk_bf16_f32 v20, v8, v9\n"
"v_mfma_f32_16x16x32_bf16 a[12:15], a[236:239], v[16:19], a[12:15]\n"
"v_exp_f32 v10, v10\n"
"v_mfma_f32_16x16x32_bf16 a[16:19], a[240:243], v[16:19], a[16:19]\n"
"ds_read_b64_tr_b16 a[202:203], v242 offset:53504\n"
"v_add_f32 v44, v44, v4\n"
"v_mfma_f32_16x16x32_bf16 a[20:23], a[244:247], v[16:19], a[20:23]\n"
"v_exp_f32 v11, v11\n"
"v_mfma_f32_16x16x32_bf16 a[24:27], a[248:251], v[16:19], a[24:27]\n"
"ds_read_b64_tr_b16 a[204:205], v243 offset:49408\n"
"v_cvt_pk_bf16_f32 v21, v10, v11\n"
"v_mfma_f32_16x16x32_bf16 a[28:31], a[252:255], v[16:19], a[28:31]\n"
"v_exp_f32 v12, v12\n"
"v_mfma_f32_16x16x32_bf16 v[220:223], a[160:163], v[96:99], v[32:35]\n"
"ds_read_b64_tr_b16 a[206:207], v243 offset:53504\n"
"v_add_f32 v44, v44, v5\n"
"v_mfma_f32_16x16x32_bf16 v[220:223], a[164:167], v[100:103], v[220:223]\n"
"v_exp_f32 v13, v13\n"
"v_mfma_f32_16x16x32_bf16 v[220:223], a[168:171], v[104:107], v[220:223]\n"
"ds_read_b64_tr_b16 a[208:209], v242 offset:49664\n"
"v_cvt_pk_bf16_f32 v22, v12, v13\n"
"v_mfma_f32_16x16x32_bf16 v[220:223], a[172:175], v[108:111], v[220:223]\n"
"v_exp_f32 v14, v14\n"
"v_mfma_f32_16x16x32_bf16 v[224:227], a[176:179], v[96:99], v[32:35]\n"
"ds_read_b64_tr_b16 a[210:211], v242 offset:53760\n"
"v_add_f32 v44, v44, v6\n"
"v_mfma_f32_16x16x32_bf16 v[224:227], a[180:183], v[100:103], v[224:227]\n"
"v_exp_f32 v15, v15\n"
"v_mfma_f32_16x16x32_bf16 v[224:227], a[184:187], v[104:107], v[224:227]\n"
"ds_read_b64_tr_b16 a[212:213], v243 offset:49664\n"
"v_cvt_pk_bf16_f32 v23, v14, v15\n"
"v_mfma_f32_16x16x32_bf16 v[224:227], a[188:191], v[108:111], v[224:227]\n"
"v_exp_f32 v220, v220\n"
"v_mfma_f32_16x16x32_bf16 a[32:35], a[224:227], v[20:23], a[32:35]\n"
"buffer_load_dwordx4 v247, s[8:11], s84 offen offset:2048 lds\n"
"v_mfma_f32_16x16x32_bf16 a[36:39], a[228:231], v[20:23], a[36:39]\n"
"v_exp_f32 v221, v221\n"
"v_mfma_f32_16x16x32_bf16 a[40:43], a[232:235], v[20:23], a[40:43]\n"
"ds_read_b64_tr_b16 a[214:215], v243 offset:53760\n"
"v_cvt_pk_bf16_f32 v16, v220, v221\n"
"v_mfma_f32_16x16x32_bf16 a[44:47], a[236:239], v[20:23], a[44:47]\n"
"v_exp_f32 v222, v222\n"
"v_mfma_f32_16x16x32_bf16 a[48:51], a[240:243], v[20:23], a[48:51]\n"
"ds_read_b64_tr_b16 a[216:217], v242 offset:49920\n"
"v_add_f32 v44, v44, v7\n"
"v_mfma_f32_16x16x32_bf16 a[52:55], a[244:247], v[20:23], a[52:55]\n"
"v_exp_f32 v223, v223\n"
"v_mfma_f32_16x16x32_bf16 a[56:59], a[248:251], v[20:23], a[56:59]\n"
"ds_read_b64_tr_b16 a[218:219], v242 offset:54016\n"
"v_cvt_pk_bf16_f32 v17, v222, v223\n"
"v_mfma_f32_16x16x32_bf16 a[60:63], a[252:255], v[20:23], a[60:63]\n"
"v_exp_f32 v224, v224\n"
"v_mfma_f32_16x16x32_bf16 v[48:51], v[40:43], v[20:23], v[48:51]\n"
"ds_read_b64_tr_b16 a[220:221], v243 offset:49920\n"
"v_add_f32 v52, v52, v220\n"
"v_mfma_f32_16x16x32_bf16 v[228:231], a[160:163], v[112:115], v[36:39]\n"
"v_exp_f32 v225, v225\n"
"v_mfma_f32_16x16x32_bf16 v[228:231], a[164:167], v[116:119], v[228:231]\n"
"ds_read_b64_tr_b16 a[222:223], v243 offset:54016\n"
"v_cvt_pk_bf16_f32 v18, v224, v225\n"
"v_mfma_f32_16x16x32_bf16 v[228:231], a[168:171], v[120:123], v[228:231]\n"
"v_exp_f32 v226, v226\n"
"v_mfma_f32_16x16x32_bf16 v[228:231], a[172:175], v[124:127], v[228:231]\n"
"v_add_f32 v52, v52, v221\n"
"v_add_f32 v52, v52, v222\n"
"v_mfma_f32_16x16x32_bf16 v[232:235], a[176:179], v[112:115], v[36:39]\n"
"v_exp_f32 v227, v227\n"
"v_mfma_f32_16x16x32_bf16 v[232:235], a[180:183], v[116:119], v[232:235]\n"
"v_cvt_pk_bf16_f32 v19, v226, v227\n"
"v_add_f32 v52, v52, v223\n"
"v_mfma_f32_16x16x32_bf16 v[232:235], a[184:187], v[120:123], v[232:235]\n"
"buffer_load_dwordx4 v247, s[8:11], s84 offen offset:3072 lds\n"
"v_mfma_f32_16x16x32_bf16 v[232:235], a[188:191], v[124:127], v[232:235]\n"
"v_exp_f32 v228, v228\n"
"v_mfma_f32_16x16x32_bf16 a[64:67], a[224:227], v[16:19], a[64:67]\n"
"v_add_f32 v52, v52, v224\n"
"v_add_f32 v52, v52, v225\n"
"v_mfma_f32_16x16x32_bf16 a[68:71], a[228:231], v[16:19], a[68:71]\n"
"v_exp_f32 v229, v229\n"
"v_mfma_f32_16x16x32_bf16 a[72:75], a[232:235], v[16:19], a[72:75]\n"
"v_cvt_pk_bf16_f32 v20, v228, v229\n"
"v_add_f32 v52, v52, v226\n"
"v_mfma_f32_16x16x32_bf16 a[76:79], a[236:239], v[16:19], a[76:79]\n"
"v_exp_f32 v230, v230\n"
"v_mfma_f32_16x16x32_bf16 a[80:83], a[240:243], v[16:19], a[80:83]\n"
"v_add_f32 v52, v52, v227\n"
"v_mfma_f32_16x16x32_bf16 a[84:87], a[244:247], v[16:19], a[84:87]\n"
"v_exp_f32 v231, v231\n"
"v_mfma_f32_16x16x32_bf16 a[88:91], a[248:251], v[16:19], a[88:91]\n"
"s_waitcnt lgkmcnt(8)\n"
"v_cvt_pk_bf16_f32 v21, v230, v231\n"
"v_mfma_f32_16x16x32_bf16 a[92:95], a[252:255], v[16:19], a[92:95]\n"
"v_exp_f32 v232, v232\n"
"v_mfma_f32_16x16x32_bf16 v[0:3], a[128:131], v[64:67], v[24:27]\n"
"ds_read_b128 a[160:163], v240 offset:40960\n"
"v_mfma_f32_16x16x32_bf16 v[0:3], a[132:135], v[68:71], v[0:3]\n"
"v_exp_f32 v233, v233\n"
"v_mfma_f32_16x16x32_bf16 v[0:3], a[136:139], v[72:75], v[0:3]\n"
"ds_read_b128 a[164:167], v240 offset:41216\n"
"v_cvt_pk_bf16_f32 v22, v232, v233\n"
"v_mfma_f32_16x16x32_bf16 v[0:3], a[140:143], v[76:79], v[0:3]\n"
"v_exp_f32 v234, v234\n"
"v_mfma_f32_16x16x32_bf16 v[4:7], a[144:147], v[64:67], v[24:27]\n"
"ds_read_b128 a[168:171], v240 offset:41472\n"
"v_mfma_f32_16x16x32_bf16 v[4:7], a[148:151], v[68:71], v[4:7]\n"
"v_exp_f32 v235, v235\n"
"v_mfma_f32_16x16x32_bf16 v[4:7], a[152:155], v[72:75], v[4:7]\n"
"ds_read_b128 a[172:175], v240 offset:41728\n"
"v_cvt_pk_bf16_f32 v23, v234, v235\n"
"v_mfma_f32_16x16x32_bf16 v[4:7], a[156:159], v[76:79], v[4:7]\n"
"s_add_u32 m0, s86, 16384\n"
"v_exp_f32 v0, v0\n"
"v_mfma_f32_16x16x32_bf16 a[96:99], a[224:227], v[20:23], a[96:99]\n"
"buffer_load_dwordx4 v246, s[12:15], s84 offen offset:0 lds\n"
"v_mfma_f32_16x16x32_bf16 a[100:103], a[228:231], v[20:23], a[100:103]\n"
"v_exp_f32 v1, v1\n"
"v_mfma_f32_16x16x32_bf16 a[104:107], a[232:235], v[20:23], a[104:107]\n"
"ds_read_b128 a[176:179], v240 offset:45056\n"
"v_cvt_pk_bf16_f32 v16, v0, v1\n"
"v_mfma_f32_16x16x32_bf16 a[108:111], a[236:239], v[20:23], a[108:111]\n"
"v_exp_f32 v2, v2\n"
"v_mfma_f32_16x16x32_bf16 a[112:115], a[240:243], v[20:23], a[112:115]\n"
"ds_read_b128 a[180:183], v240 offset:45312\n"
"v_add_f32 v44, v44, v0\n"
"v_mfma_f32_16x16x32_bf16 a[116:119], a[244:247], v[20:23], a[116:119]\n"
"v_exp_f32 v3, v3\n"
"v_mfma_f32_16x16x32_bf16 a[120:123], a[248:251], v[20:23], a[120:123]\n"
"ds_read_b128 a[184:187], v240 offset:45568\n"
"v_cvt_pk_bf16_f32 v17, v2, v3\n"
"v_mfma_f32_16x16x32_bf16 a[124:127], a[252:255], v[20:23], a[124:127]\n"
"v_exp_f32 v4, v4\n"
"v_mfma_f32_16x16x32_bf16 v[56:59], v[40:43], v[20:23], v[56:59]\n"
"ds_read_b128 a[188:191], v240 offset:45824\n"
"v_add_f32 v44, v44, v1\n"
"v_mfma_f32_16x16x32_bf16 v[8:11], a[128:131], v[80:83], v[28:31]\n"
"v_exp_f32 v5, v5\n"
"v_mfma_f32_16x16x32_bf16 v[8:11], a[132:135], v[84:87], v[8:11]\n"
"ds_read_b64_tr_b16 a[224:225], v242 offset:57344\n"
"v_cvt_pk_bf16_f32 v18, v4, v5\n"
"v_mfma_f32_16x16x32_bf16 v[8:11], a[136:139], v[88:91], v[8:11]\n"
"v_exp_f32 v6, v6\n"
"v_mfma_f32_16x16x32_bf16 v[8:11], a[140:143], v[92:95], v[8:11]\n"
"ds_read_b64_tr_b16 a[226:227], v242 offset:61440\n"
"v_add_f32 v44, v44, v2\n"
"v_mfma_f32_16x16x32_bf16 v[12:15], a[144:147], v[80:83], v[28:31]\n"
"v_exp_f32 v7, v7\n"
"v_mfma_f32_16x16x32_bf16 v[12:15], a[148:151], v[84:87], v[12:15]\n"
"ds_read_b64_tr_b16 a[228:229], v243 offset:57344\n"
"v_cvt_pk_bf16_f32 v19, v6, v7\n"
"v_mfma_f32_16x16x32_bf16 v[12:15], a[152:155], v[88:91], v[12:15]\n"
"buffer_load_dwordx4 v247, s[12:15], s84 offen offset:1024 lds\n"
"v_mfma_f32_16x16x32_bf16 v[12:15], a[156:159], v[92:95], v[12:15]\n"
"s_waitcnt lgkmcnt(11)\n"
"v_exp_f32 v8, v8\n"
"v_mfma_f32_16x16x32_bf16 a[0:3], a[192:195], v[16:19], a[0:3]\n"
"ds_read_b64_tr_b16 a[230:231], v243 offset:61440\n"
"v_add_f32 v44, v44, v3\n"
"v_mfma_f32_16x16x32_bf16 a[4:7], a[196:199], v[16:19], a[4:7]\n"
"v_exp_f32 v9, v9\n"
"v_mfma_f32_16x16x32_bf16 a[8:11], a[200:203], v[16:19], a[8:11]\n"
"ds_read_b64_tr_b16 a[232:233], v242 offset:57600\n"
"v_cvt_pk_bf16_f32 v20, v8, v9\n"
"v_mfma_f32_16x16x32_bf16 a[12:15], a[204:207], v[16:19], a[12:15]\n"
"v_exp_f32 v10, v10\n"
"v_mfma_f32_16x16x32_bf16 a[16:19], a[208:211], v[16:19], a[16:19]\n"
"ds_read_b64_tr_b16 a[234:235], v242 offset:61696\n"
"v_add_f32 v44, v44, v4\n"
"v_mfma_f32_16x16x32_bf16 a[20:23], a[212:215], v[16:19], a[20:23]\n"
"v_exp_f32 v11, v11\n"
"v_mfma_f32_16x16x32_bf16 a[24:27], a[216:219], v[16:19], a[24:27]\n"
"ds_read_b64_tr_b16 a[236:237], v243 offset:57600\n"
"v_cvt_pk_bf16_f32 v21, v10, v11\n"
"v_mfma_f32_16x16x32_bf16 a[28:31], a[220:223], v[16:19], a[28:31]\n"
"v_exp_f32 v12, v12\n"
"v_mfma_f32_16x16x32_bf16 v[220:223], a[128:131], v[96:99], v[32:35]\n"
"ds_read_b64_tr_b16 a[238:239], v243 offset:61696\n"
"v_add_f32 v44, v44, v5\n"
"v_mfma_f32_16x16x32_bf16 v[220:223], a[132:135], v[100:103], v[220:223]\n"
"v_exp_f32 v13, v13\n"
"v_mfma_f32_16x16x32_bf16 v[220:223], a[136:139], v[104:107], v[220:223]\n"
"ds_read_b64_tr_b16 a[240:241], v242 offset:57856\n"
"v_cvt_pk_bf16_f32 v22, v12, v13\n"
"v_mfma_f32_16x16x32_bf16 v[220:223], a[140:143], v[108:111], v[220:223]\n"
"v_exp_f32 v14, v14\n"
"v_mfma_f32_16x16x32_bf16 v[224:227], a[144:147], v[96:99], v[32:35]\n"
"ds_read_b64_tr_b16 a[242:243], v242 offset:61952\n"
"v_add_f32 v44, v44, v6\n"
"v_mfma_f32_16x16x32_bf16 v[224:227], a[148:151], v[100:103], v[224:227]\n"
"v_exp_f32 v15, v15\n"
"v_mfma_f32_16x16x32_bf16 v[224:227], a[152:155], v[104:107], v[224:227]\n"
"ds_read_b64_tr_b16 a[244:245], v243 offset:57856\n"
"v_cvt_pk_bf16_f32 v23, v14, v15\n"
"v_mfma_f32_16x16x32_bf16 v[224:227], a[156:159], v[108:111], v[224:227]\n"
"v_exp_f32 v220, v220\n"
"v_mfma_f32_16x16x32_bf16 a[32:35], a[192:195], v[20:23], a[32:35]\n"
"buffer_load_dwordx4 v246, s[12:15], s84 offen offset:2048 lds\n"
"v_mfma_f32_16x16x32_bf16 a[36:39], a[196:199], v[20:23], a[36:39]\n"
"v_exp_f32 v221, v221\n"
"v_mfma_f32_16x16x32_bf16 a[40:43], a[200:203], v[20:23], a[40:43]\n"
"ds_read_b64_tr_b16 a[246:247], v243 offset:61952\n"
"v_cvt_pk_bf16_f32 v16, v220, v221\n"
"v_mfma_f32_16x16x32_bf16 a[44:47], a[204:207], v[20:23], a[44:47]\n"
"v_exp_f32 v222, v222\n"
"v_mfma_f32_16x16x32_bf16 a[48:51], a[208:211], v[20:23], a[48:51]\n"
"ds_read_b64_tr_b16 a[248:249], v242 offset:58112\n"
"v_add_f32 v44, v44, v7\n"
"v_mfma_f32_16x16x32_bf16 a[52:55], a[212:215], v[20:23], a[52:55]\n"
"v_exp_f32 v223, v223\n"
"v_mfma_f32_16x16x32_bf16 a[56:59], a[216:219], v[20:23], a[56:59]\n"
"ds_read_b64_tr_b16 a[250:251], v242 offset:62208\n"
"v_cvt_pk_bf16_f32 v17, v222, v223\n"
"v_mfma_f32_16x16x32_bf16 a[60:63], a[220:223], v[20:23], a[60:63]\n"
"v_exp_f32 v224, v224\n"
"v_mfma_f32_16x16x32_bf16 v[48:51], v[40:43], v[20:23], v[48:51]\n"
"ds_read_b64_tr_b16 a[252:253], v243 offset:58112\n"
"v_add_f32 v52, v52, v220\n"
"v_mfma_f32_16x16x32_bf16 v[228:231], a[128:131], v[112:115], v[36:39]\n"
"v_exp_f32 v225, v225\n"
"v_mfma_f32_16x16x32_bf16 v[228:231], a[132:135], v[116:119], v[228:231]\n"
"ds_read_b64_tr_b16 a[254:255], v243 offset:62208\n"
"v_cvt_pk_bf16_f32 v18, v224, v225\n"
"v_mfma_f32_16x16x32_bf16 v[228:231], a[136:139], v[120:123], v[228:231]\n"
"v_exp_f32 v226, v226\n"
"v_mfma_f32_16x16x32_bf16 v[228:231], a[140:143], v[124:127], v[228:231]\n"
"v_add_f32 v52, v52, v221\n"
"v_add_f32 v52, v52, v222\n"
"v_mfma_f32_16x16x32_bf16 v[232:235], a[144:147], v[112:115], v[36:39]\n"
"v_exp_f32 v227, v227\n"
"v_mfma_f32_16x16x32_bf16 v[232:235], a[148:151], v[116:119], v[232:235]\n"
"v_cvt_pk_bf16_f32 v19, v226, v227\n"
"v_add_f32 v52, v52, v223\n"
"v_mfma_f32_16x16x32_bf16 v[232:235], a[152:155], v[120:123], v[232:235]\n"
"buffer_load_dwordx4 v247, s[12:15], s84 offen offset:3072 lds\n"
"v_mfma_f32_16x16x32_bf16 v[232:235], a[156:159], v[124:127], v[232:235]\n"
"s_add_u32 s84, s84, 16384\n"
"v_exp_f32 v228, v228\n"
"v_mfma_f32_16x16x32_bf16 a[64:67], a[192:195], v[16:19], a[64:67]\n"
"v_add_f32 v52, v52, v224\n"
"v_add_f32 v52, v52, v225\n"
"v_mfma_f32_16x16x32_bf16 a[68:71], a[196:199], v[16:19], a[68:71]\n"
"v_exp_f32 v229, v229\n"
"v_mfma_f32_16x16x32_bf16 a[72:75], a[200:203], v[16:19], a[72:75]\n"
"v_cvt_pk_bf16_f32 v20, v228, v229\n"
"v_add_f32 v52, v52, v226\n"
"v_mfma_f32_16x16x32_bf16 a[76:79], a[204:207], v[16:19], a[76:79]\n"
"v_exp_f32 v230, v230\n"
"v_mfma_f32_16x16x32_bf16 a[80:83], a[208:211], v[16:19], a[80:83]\n"
"v_add_f32 v52, v52, v227\n"
"v_mfma_f32_16x16x32_bf16 a[84:87], a[212:215], v[16:19], a[84:87]\n"
"v_exp_f32 v231, v231\n"
"v_mfma_f32_16x16x32_bf16 a[88:91], a[216:219], v[16:19], a[88:91]\n"
"s_waitcnt vmcnt(18) lgkmcnt(0)\n"
"s_barrier\n"
"v_cvt_pk_bf16_f32 v21, v230, v231\n"
"v_mfma_f32_16x16x32_bf16 a[92:95], a[220:223], v[16:19], a[92:95]\n"
"v_exp_f32 v232, v232\n"
"v_mfma_f32_16x16x32_bf16 v[0:3], a[160:163], v[64:67], v[24:27]\n"
"ds_read_b128 a[128:131], v241 offset:0\n"
"buffer_load_dword v250, v251, s[68:71], 0 offen\n"
"v_mfma_f32_16x16x32_bf16 v[0:3], a[164:167], v[68:71], v[0:3]\n"
"v_exp_f32 v233, v233\n"
"v_mfma_f32_16x16x32_bf16 v[0:3], a[168:171], v[72:75], v[0:3]\n"
"ds_read_b128 a[132:135], v241 offset:256\n"
"v_cvt_pk_bf16_f32 v22, v232, v233\n"
"v_mfma_f32_16x16x32_bf16 v[0:3], a[172:175], v[76:79], v[0:3]\n"
"v_exp_f32 v234, v234\n"
"v_mfma_f32_16x16x32_bf16 v[4:7], a[176:179], v[64:67], v[24:27]\n"
"ds_read_b128 a[136:139], v241 offset:512\n"
"v_add_u32 v251, 1179648, v251\n"
"v_mfma_f32_16x16x32_bf16 v[4:7], a[180:183], v[68:71], v[4:7]\n"
"v_exp_f32 v235, v235\n"
"v_mfma_f32_16x16x32_bf16 v[4:7], a[184:187], v[72:75], v[4:7]\n"
"ds_read_b128 a[140:143], v241 offset:768\n"
"v_cvt_pk_bf16_f32 v23, v234, v235\n"
"v_mfma_f32_16x16x32_bf16 v[4:7], a[188:191], v[76:79], v[4:7]\n"
"s_add_u32 m0, s86, 32768\n"
"v_exp_f32 v0, v0\n"
"v_mfma_f32_16x16x32_bf16 a[96:99], a[192:195], v[20:23], a[96:99]\n"
"buffer_load_dwordx4 v246, s[8:11], s84 offen offset:0 lds\n"
"v_mfma_f32_16x16x32_bf16 a[100:103], a[196:199], v[20:23], a[100:103]\n"
"v_exp_f32 v1, v1\n"
"v_mfma_f32_16x16x32_bf16 a[104:107], a[200:203], v[20:23], a[104:107]\n"
"ds_read_b128 a[144:147], v241 offset:4096\n"
"v_cvt_pk_bf16_f32 v16, v0, v1\n"
"v_mfma_f32_16x16x32_bf16 a[108:111], a[204:207], v[20:23], a[108:111]\n"
"v_exp_f32 v2, v2\n"
"v_mfma_f32_16x16x32_bf16 a[112:115], a[208:211], v[20:23], a[112:115]\n"
"ds_read_b128 a[148:151], v241 offset:4352\n"
"v_add_f32 v44, v44, v0\n"
"v_mfma_f32_16x16x32_bf16 a[116:119], a[212:215], v[20:23], a[116:119]\n"
"v_exp_f32 v3, v3\n"
"v_mfma_f32_16x16x32_bf16 a[120:123], a[216:219], v[20:23], a[120:123]\n"
"ds_read_b128 a[152:155], v241 offset:4608\n"
"v_cvt_pk_bf16_f32 v17, v2, v3\n"
"v_mfma_f32_16x16x32_bf16 a[124:127], a[220:223], v[20:23], a[124:127]\n"
"v_exp_f32 v4, v4\n"
"v_mfma_f32_16x16x32_bf16 v[56:59], v[40:43], v[20:23], v[56:59]\n"
"ds_read_b128 a[156:159], v241 offset:4864\n"
"v_add_f32 v44, v44, v1\n"
"v_mfma_f32_16x16x32_bf16 v[8:11], a[160:163], v[80:83], v[28:31]\n"
"v_exp_f32 v5, v5\n"
"v_mfma_f32_16x16x32_bf16 v[8:11], a[164:167], v[84:87], v[8:11]\n"
"ds_read_b64_tr_b16 a[192:193], v244 offset:16384\n"
"v_cvt_pk_bf16_f32 v18, v4, v5\n"
"v_mfma_f32_16x16x32_bf16 v[8:11], a[168:171], v[88:91], v[8:11]\n"
"v_exp_f32 v6, v6\n"
"v_mfma_f32_16x16x32_bf16 v[8:11], a[172:175], v[92:95], v[8:11]\n"
"ds_read_b64_tr_b16 a[194:195], v244 offset:20480\n"
"v_add_f32 v44, v44, v2\n"
"v_mfma_f32_16x16x32_bf16 v[12:15], a[176:179], v[80:83], v[28:31]\n"
"v_exp_f32 v7, v7\n"
"v_mfma_f32_16x16x32_bf16 v[12:15], a[180:183], v[84:87], v[12:15]\n"
"ds_read_b64_tr_b16 a[196:197], v245 offset:16384\n"
"v_cvt_pk_bf16_f32 v19, v6, v7\n"
"v_mfma_f32_16x16x32_bf16 v[12:15], a[184:187], v[88:91], v[12:15]\n"
"buffer_load_dwordx4 v246, s[8:11], s84 offen offset:1024 lds\n"
"v_mfma_f32_16x16x32_bf16 v[12:15], a[188:191], v[92:95], v[12:15]\n"
"s_waitcnt lgkmcnt(11)\n"
"v_exp_f32 v8, v8\n"
"v_mfma_f32_16x16x32_bf16 a[0:3], a[224:227], v[16:19], a[0:3]\n"
"ds_read_b64_tr_b16 a[198:199], v245 offset:20480\n"
"v_add_f32 v44, v44, v3\n"
"v_mfma_f32_16x16x32_bf16 a[4:7], a[228:231], v[16:19], a[4:7]\n"
"v_exp_f32 v9, v9\n"
"v_mfma_f32_16x16x32_bf16 a[8:11], a[232:235], v[16:19], a[8:11]\n"
"ds_read_b64_tr_b16 a[200:201], v244 offset:16640\n"
"v_cvt_pk_bf16_f32 v20, v8, v9\n"
"v_mfma_f32_16x16x32_bf16 a[12:15], a[236:239], v[16:19], a[12:15]\n"
"v_exp_f32 v10, v10\n"
"v_mfma_f32_16x16x32_bf16 a[16:19], a[240:243], v[16:19], a[16:19]\n"
"ds_read_b64_tr_b16 a[202:203], v244 offset:20736\n"
"v_add_f32 v44, v44, v4\n"
"v_mfma_f32_16x16x32_bf16 a[20:23], a[244:247], v[16:19], a[20:23]\n"
"v_exp_f32 v11, v11\n"
"v_mfma_f32_16x16x32_bf16 a[24:27], a[248:251], v[16:19], a[24:27]\n"
"ds_read_b64_tr_b16 a[204:205], v245 offset:16640\n"
"v_cvt_pk_bf16_f32 v21, v10, v11\n"
"v_mfma_f32_16x16x32_bf16 a[28:31], a[252:255], v[16:19], a[28:31]\n"
"v_exp_f32 v12, v12\n"
"v_mfma_f32_16x16x32_bf16 v[220:223], a[160:163], v[96:99], v[32:35]\n"
"ds_read_b64_tr_b16 a[206:207], v245 offset:20736\n"
"v_add_f32 v44, v44, v5\n"
"v_mfma_f32_16x16x32_bf16 v[220:223], a[164:167], v[100:103], v[220:223]\n"
"v_exp_f32 v13, v13\n"
"v_mfma_f32_16x16x32_bf16 v[220:223], a[168:171], v[104:107], v[220:223]\n"
"ds_read_b64_tr_b16 a[208:209], v244 offset:16896\n"
"v_cvt_pk_bf16_f32 v22, v12, v13\n"
"v_mfma_f32_16x16x32_bf16 v[220:223], a[172:175], v[108:111], v[220:223]\n"
"v_exp_f32 v14, v14\n"
"v_mfma_f32_16x16x32_bf16 v[224:227], a[176:179], v[96:99], v[32:35]\n"
"ds_read_b64_tr_b16 a[210:211], v244 offset:20992\n"
"v_add_f32 v44, v44, v6\n"
"v_mfma_f32_16x16x32_bf16 v[224:227], a[180:183], v[100:103], v[224:227]\n"
"v_exp_f32 v15, v15\n"
"v_mfma_f32_16x16x32_bf16 v[224:227], a[184:187], v[104:107], v[224:227]\n"
"ds_read_b64_tr_b16 a[212:213], v245 offset:16896\n"
"v_cvt_pk_bf16_f32 v23, v14, v15\n"
"v_mfma_f32_16x16x32_bf16 v[224:227], a[188:191], v[108:111], v[224:227]\n"
"v_exp_f32 v220, v220\n"
"v_mfma_f32_16x16x32_bf16 a[32:35], a[224:227], v[20:23], a[32:35]\n"
"buffer_load_dwordx4 v247, s[8:11], s84 offen offset:2048 lds\n"
"v_mfma_f32_16x16x32_bf16 a[36:39], a[228:231], v[20:23], a[36:39]\n"
"v_exp_f32 v221, v221\n"
"v_mfma_f32_16x16x32_bf16 a[40:43], a[232:235], v[20:23], a[40:43]\n"
"ds_read_b64_tr_b16 a[214:215], v245 offset:20992\n"
"v_cvt_pk_bf16_f32 v16, v220, v221\n"
"v_mfma_f32_16x16x32_bf16 a[44:47], a[236:239], v[20:23], a[44:47]\n"
"v_exp_f32 v222, v222\n"
"v_mfma_f32_16x16x32_bf16 a[48:51], a[240:243], v[20:23], a[48:51]\n"
"ds_read_b64_tr_b16 a[216:217], v244 offset:17152\n"
"v_add_f32 v44, v44, v7\n"
"v_mfma_f32_16x16x32_bf16 a[52:55], a[244:247], v[20:23], a[52:55]\n"
"v_exp_f32 v223, v223\n"
"v_mfma_f32_16x16x32_bf16 a[56:59], a[248:251], v[20:23], a[56:59]\n"
"ds_read_b64_tr_b16 a[218:219], v244 offset:21248\n"
"v_cvt_pk_bf16_f32 v17, v222, v223\n"
"v_mfma_f32_16x16x32_bf16 a[60:63], a[252:255], v[20:23], a[60:63]\n"
"v_exp_f32 v224, v224\n"
"v_mfma_f32_16x16x32_bf16 v[48:51], v[40:43], v[20:23], v[48:51]\n"
"ds_read_b64_tr_b16 a[220:221], v245 offset:17152\n"
"v_add_f32 v52, v52, v220\n"
"v_mfma_f32_16x16x32_bf16 v[228:231], a[160:163], v[112:115], v[36:39]\n"
"v_exp_f32 v225, v225\n"
"v_mfma_f32_16x16x32_bf16 v[228:231], a[164:167], v[116:119], v[228:231]\n"
"ds_read_b64_tr_b16 a[222:223], v245 offset:21248\n"
"v_cvt_pk_bf16_f32 v18, v224, v225\n"
"v_mfma_f32_16x16x32_bf16 v[228:231], a[168:171], v[120:123], v[228:231]\n"
"v_exp_f32 v226, v226\n"
"v_mfma_f32_16x16x32_bf16 v[228:231], a[172:175], v[124:127], v[228:231]\n"
"v_add_f32 v52, v52, v221\n"
"v_add_f32 v52, v52, v222\n"
"v_mfma_f32_16x16x32_bf16 v[232:235], a[176:179], v[112:115], v[36:39]\n"
"v_exp_f32 v227, v227\n"
"v_mfma_f32_16x16x32_bf16 v[232:235], a[180:183], v[116:119], v[232:235]\n"
"v_cvt_pk_bf16_f32 v19, v226, v227\n"
"v_add_f32 v52, v52, v223\n"
"v_mfma_f32_16x16x32_bf16 v[232:235], a[184:187], v[120:123], v[232:235]\n"
"buffer_load_dwordx4 v247, s[8:11], s84 offen offset:3072 lds\n"
"v_mfma_f32_16x16x32_bf16 v[232:235], a[188:191], v[124:127], v[232:235]\n"
"v_exp_f32 v228, v228\n"
"v_mfma_f32_16x16x32_bf16 a[64:67], a[224:227], v[16:19], a[64:67]\n"
"v_add_f32 v52, v52, v224\n"
"v_add_f32 v52, v52, v225\n"
"v_mfma_f32_16x16x32_bf16 a[68:71], a[228:231], v[16:19], a[68:71]\n"
"v_exp_f32 v229, v229\n"
"v_mfma_f32_16x16x32_bf16 a[72:75], a[232:235], v[16:19], a[72:75]\n"
"v_cvt_pk_bf16_f32 v20, v228, v229\n"
"v_add_f32 v52, v52, v226\n"
"v_mfma_f32_16x16x32_bf16 a[76:79], a[236:239], v[16:19], a[76:79]\n"
"v_exp_f32 v230, v230\n"
"v_mfma_f32_16x16x32_bf16 a[80:83], a[240:243], v[16:19], a[80:83]\n"
"v_add_f32 v52, v52, v227\n"
"v_mfma_f32_16x16x32_bf16 a[84:87], a[244:247], v[16:19], a[84:87]\n"
"v_exp_f32 v231, v231\n"
"v_mfma_f32_16x16x32_bf16 a[88:91], a[248:251], v[16:19], a[88:91]\n"
"s_waitcnt lgkmcnt(8)\n"
"v_cvt_pk_bf16_f32 v21, v230, v231\n"
"v_mfma_f32_16x16x32_bf16 a[92:95], a[252:255], v[16:19], a[92:95]\n"
"v_exp_f32 v232, v232\n"
"v_mfma_f32_16x16x32_bf16 v[0:3], a[128:131], v[64:67], v[24:27]\n"
"ds_read_b128 a[160:163], v241 offset:8192\n"
"v_mfma_f32_16x16x32_bf16 v[0:3], a[132:135], v[68:71], v[0:3]\n"
"v_exp_f32 v233, v233\n"
"v_mfma_f32_16x16x32_bf16 v[0:3], a[136:139], v[72:75], v[0:3]\n"
"ds_read_b128 a[164:167], v241 offset:8448\n"
"v_cvt_pk_bf16_f32 v22, v232, v233\n"
"v_mfma_f32_16x16x32_bf16 v[0:3], a[140:143], v[76:79], v[0:3]\n"
"v_exp_f32 v234, v234\n"
"v_mfma_f32_16x16x32_bf16 v[4:7], a[144:147], v[64:67], v[24:27]\n"
"ds_read_b128 a[168:171], v241 offset:8704\n"
"v_mfma_f32_16x16x32_bf16 v[4:7], a[148:151], v[68:71], v[4:7]\n"
"v_exp_f32 v235, v235\n"
"v_mfma_f32_16x16x32_bf16 v[4:7], a[152:155], v[72:75], v[4:7]\n"
"ds_read_b128 a[172:175], v241 offset:8960\n"
"v_cvt_pk_bf16_f32 v23, v234, v235\n"
"v_mfma_f32_16x16x32_bf16 v[4:7], a[156:159], v[76:79], v[4:7]\n"
"s_add_u32 m0, s86, 49152\n"
"v_exp_f32 v0, v0\n"
"v_mfma_f32_16x16x32_bf16 a[96:99], a[224:227], v[20:23], a[96:99]\n"
"buffer_load_dwordx4 v246, s[12:15], s84 offen offset:0 lds\n"
"v_mfma_f32_16x16x32_bf16 a[100:103], a[228:231], v[20:23], a[100:103]\n"
"v_exp_f32 v1, v1\n"
"v_mfma_f32_16x16x32_bf16 a[104:107], a[232:235], v[20:23], a[104:107]\n"
"ds_read_b128 a[176:179], v241 offset:12288\n"
"v_cvt_pk_bf16_f32 v16, v0, v1\n"
"v_mfma_f32_16x16x32_bf16 a[108:111], a[236:239], v[20:23], a[108:111]\n"
"v_exp_f32 v2, v2\n"
"v_mfma_f32_16x16x32_bf16 a[112:115], a[240:243], v[20:23], a[112:115]\n"
"ds_read_b128 a[180:183], v241 offset:12544\n"
"v_add_f32 v44, v44, v0\n"
"v_mfma_f32_16x16x32_bf16 a[116:119], a[244:247], v[20:23], a[116:119]\n"
"v_exp_f32 v3, v3\n"
"v_mfma_f32_16x16x32_bf16 a[120:123], a[248:251], v[20:23], a[120:123]\n"
"ds_read_b128 a[184:187], v241 offset:12800\n"
"v_cvt_pk_bf16_f32 v17, v2, v3\n"
"v_mfma_f32_16x16x32_bf16 a[124:127], a[252:255], v[20:23], a[124:127]\n"
"v_exp_f32 v4, v4\n"
"v_mfma_f32_16x16x32_bf16 v[56:59], v[40:43], v[20:23], v[56:59]\n"
"ds_read_b128 a[188:191], v241 offset:13056\n"
"v_add_f32 v44, v44, v1\n"
"v_mfma_f32_16x16x32_bf16 v[8:11], a[128:131], v[80:83], v[28:31]\n"
"v_exp_f32 v5, v5\n"
"v_mfma_f32_16x16x32_bf16 v[8:11], a[132:135], v[84:87], v[8:11]\n"
"ds_read_b64_tr_b16 a[224:225], v244 offset:24576\n"
"v_cvt_pk_bf16_f32 v18, v4, v5\n"
"v_mfma_f32_16x16x32_bf16 v[8:11], a[136:139], v[88:91], v[8:11]\n"
"v_exp_f32 v6, v6\n"
"v_mfma_f32_16x16x32_bf16 v[8:11], a[140:143], v[92:95], v[8:11]\n"
"ds_read_b64_tr_b16 a[226:227], v244 offset:28672\n"
"v_add_f32 v44, v44, v2\n"
"v_mfma_f32_16x16x32_bf16 v[12:15], a[144:147], v[80:83], v[28:31]\n"
"v_exp_f32 v7, v7\n"
"v_mfma_f32_16x16x32_bf16 v[12:15], a[148:151], v[84:87], v[12:15]\n"
"ds_read_b64_tr_b16 a[228:229], v245 offset:24576\n"
"v_cvt_pk_bf16_f32 v19, v6, v7\n"
"v_mfma_f32_16x16x32_bf16 v[12:15], a[152:155], v[88:91], v[12:15]\n"
"buffer_load_dwordx4 v247, s[12:15], s84 offen offset:1024 lds\n"
"v_mfma_f32_16x16x32_bf16 v[12:15], a[156:159], v[92:95], v[12:15]\n"
"s_waitcnt lgkmcnt(11)\n"
"v_exp_f32 v8, v8\n"
"v_mfma_f32_16x16x32_bf16 a[0:3], a[192:195], v[16:19], a[0:3]\n"
"ds_read_b64_tr_b16 a[230:231], v245 offset:28672\n"
"v_add_f32 v44, v44, v3\n"
"v_mfma_f32_16x16x32_bf16 a[4:7], a[196:199], v[16:19], a[4:7]\n"
"v_exp_f32 v9, v9\n"
"v_mfma_f32_16x16x32_bf16 a[8:11], a[200:203], v[16:19], a[8:11]\n"
"ds_read_b64_tr_b16 a[232:233], v244 offset:24832\n"
"v_cvt_pk_bf16_f32 v20, v8, v9\n"
"v_mfma_f32_16x16x32_bf16 a[12:15], a[204:207], v[16:19], a[12:15]\n"
"v_exp_f32 v10, v10\n"
"v_mfma_f32_16x16x32_bf16 a[16:19], a[208:211], v[16:19], a[16:19]\n"
"ds_read_b64_tr_b16 a[234:235], v244 offset:28928\n"
"v_add_f32 v44, v44, v4\n"
"v_mfma_f32_16x16x32_bf16 a[20:23], a[212:215], v[16:19], a[20:23]\n"
"v_exp_f32 v11, v11\n"
"v_mfma_f32_16x16x32_bf16 a[24:27], a[216:219], v[16:19], a[24:27]\n"
"ds_read_b64_tr_b16 a[236:237], v245 offset:24832\n"
"v_cvt_pk_bf16_f32 v21, v10, v11\n"
"v_mfma_f32_16x16x32_bf16 a[28:31], a[220:223], v[16:19], a[28:31]\n"
"v_exp_f32 v12, v12\n"
"v_mfma_f32_16x16x32_bf16 v[220:223], a[128:131], v[96:99], v[32:35]\n"
"ds_read_b64_tr_b16 a[238:239], v245 offset:28928\n"
"v_add_f32 v44, v44, v5\n"
"v_mfma_f32_16x16x32_bf16 v[220:223], a[132:135], v[100:103], v[220:223]\n"
"v_exp_f32 v13, v13\n"
"v_mfma_f32_16x16x32_bf16 v[220:223], a[136:139], v[104:107], v[220:223]\n"
"ds_read_b64_tr_b16 a[240:241], v244 offset:25088\n"
"v_cvt_pk_bf16_f32 v22, v12, v13\n"
"v_mfma_f32_16x16x32_bf16 v[220:223], a[140:143], v[108:111], v[220:223]\n"
"v_exp_f32 v14, v14\n"
"v_mfma_f32_16x16x32_bf16 v[224:227], a[144:147], v[96:99], v[32:35]\n"
"ds_read_b64_tr_b16 a[242:243], v244 offset:29184\n"
"v_add_f32 v44, v44, v6\n"
"v_mfma_f32_16x16x32_bf16 v[224:227], a[148:151], v[100:103], v[224:227]\n"
"v_exp_f32 v15, v15\n"
"v_mfma_f32_16x16x32_bf16 v[224:227], a[152:155], v[104:107], v[224:227]\n"
"ds_read_b64_tr_b16 a[244:245], v245 offset:25088\n"
"v_cvt_pk_bf16_f32 v23, v14, v15\n"
"v_mfma_f32_16x16x32_bf16 v[224:227], a[156:159], v[108:111], v[224:227]\n"
"v_exp_f32 v220, v220\n"
"v_mfma_f32_16x16x32_bf16 a[32:35], a[192:195], v[20:23], a[32:35]\n"
"buffer_load_dwordx4 v246, s[12:15], s84 offen offset:2048 lds\n"
"v_mfma_f32_16x16x32_bf16 a[36:39], a[196:199], v[20:23], a[36:39]\n"
"v_exp_f32 v221, v221\n"
"v_mfma_f32_16x16x32_bf16 a[40:43], a[200:203], v[20:23], a[40:43]\n"
"ds_read_b64_tr_b16 a[246:247], v245 offset:29184\n"
"v_cvt_pk_bf16_f32 v16, v220, v221\n"
"v_mfma_f32_16x16x32_bf16 a[44:47], a[204:207], v[20:23], a[44:47]\n"
"v_exp_f32 v222, v222\n"
"v_mfma_f32_16x16x32_bf16 a[48:51], a[208:211], v[20:23], a[48:51]\n"
"ds_read_b64_tr_b16 a[248:249], v244 offset:25344\n"
"v_add_f32 v44, v44, v7\n"
"v_mfma_f32_16x16x32_bf16 a[52:55], a[212:215], v[20:23], a[52:55]\n"
"v_exp_f32 v223, v223\n"
"v_mfma_f32_16x16x32_bf16 a[56:59], a[216:219], v[20:23], a[56:59]\n"
"ds_read_b64_tr_b16 a[250:251], v244 offset:29440\n"
"v_cvt_pk_bf16_f32 v17, v222, v223\n"
"v_mfma_f32_16x16x32_bf16 a[60:63], a[220:223], v[20:23], a[60:63]\n"
"v_exp_f32 v224, v224\n"
"v_mfma_f32_16x16x32_bf16 v[48:51], v[40:43], v[20:23], v[48:51]\n"
"ds_read_b64_tr_b16 a[252:253], v245 offset:25344\n"
"v_add_f32 v52, v52, v220\n"
"v_mfma_f32_16x16x32_bf16 v[228:231], a[128:131], v[112:115], v[36:39]\n"
"v_exp_f32 v225, v225\n"
"v_mfma_f32_16x16x32_bf16 v[228:231], a[132:135], v[116:119], v[228:231]\n"
"ds_read_b64_tr_b16 a[254:255], v245 offset:29440\n"
"v_cvt_pk_bf16_f32 v18, v224, v225\n"
"v_mfma_f32_16x16x32_bf16 v[228:231], a[136:139], v[120:123], v[228:231]\n"
"v_exp_f32 v226, v226\n"
"v_mfma_f32_16x16x32_bf16 v[228:231], a[140:143], v[124:127], v[228:231]\n"
"v_add_f32 v52, v52, v221\n"
"v_add_f32 v52, v52, v222\n"
"v_mfma_f32_16x16x32_bf16 v[232:235], a[144:147], v[112:115], v[36:39]\n"
"v_exp_f32 v227, v227\n"
"v_mfma_f32_16x16x32_bf16 v[232:235], a[148:151], v[116:119], v[232:235]\n"
"v_cvt_pk_bf16_f32 v19, v226, v227\n"
"v_add_f32 v52, v52, v223\n"
"v_mfma_f32_16x16x32_bf16 v[232:235], a[152:155], v[120:123], v[232:235]\n"
"buffer_load_dwordx4 v247, s[12:15], s84 offen offset:3072 lds\n"
"v_mfma_f32_16x16x32_bf16 v[232:235], a[156:159], v[124:127], v[232:235]\n"
"s_add_u32 s84, s84, 16384\n"
"v_exp_f32 v228, v228\n"
"v_mfma_f32_16x16x32_bf16 a[64:67], a[192:195], v[16:19], a[64:67]\n"
"v_add_f32 v52, v52, v224\n"
"v_add_f32 v52, v52, v225\n"
"v_mfma_f32_16x16x32_bf16 a[68:71], a[196:199], v[16:19], a[68:71]\n"
"v_exp_f32 v229, v229\n"
"v_mfma_f32_16x16x32_bf16 a[72:75], a[200:203], v[16:19], a[72:75]\n"
"v_cvt_pk_bf16_f32 v20, v228, v229\n"
"v_add_f32 v52, v52, v226\n"
"v_mfma_f32_16x16x32_bf16 a[76:79], a[204:207], v[16:19], a[76:79]\n"
"v_exp_f32 v230, v230\n"
"v_mfma_f32_16x16x32_bf16 a[80:83], a[208:211], v[16:19], a[80:83]\n"
"v_add_f32 v52, v52, v227\n"
"v_mfma_f32_16x16x32_bf16 a[84:87], a[212:215], v[16:19], a[84:87]\n"
"v_exp_f32 v231, v231\n"
"v_mfma_f32_16x16x32_bf16 a[88:91], a[216:219], v[16:19], a[88:91]\n"
"s_waitcnt vmcnt(19) lgkmcnt(0)\n"
"s_barrier\n"
"v_cvt_pk_bf16_f32 v21, v230, v231\n"
"v_mfma_f32_16x16x32_bf16 a[92:95], a[220:223], v[16:19], a[92:95]\n"
"v_exp_f32 v232, v232\n"
"v_mfma_f32_16x16x32_bf16 v[0:3], a[160:163], v[64:67], v[24:27]\n"
"ds_read_b128 a[128:131], v241 offset:32768\n"
"buffer_load_dword v250, v252, s[72:75], s46 offen\n"
"v_mfma_f32_16x16x32_bf16 v[0:3], a[164:167], v[68:71], v[0:3]\n"
"v_exp_f32 v233, v233\n"
"v_mfma_f32_16x16x32_bf16 v[0:3], a[168:171], v[72:75], v[0:3]\n"
"ds_read_b128 a[132:135], v241 offset:33024\n"
"v_cvt_pk_bf16_f32 v22, v232, v233\n"
"v_mfma_f32_16x16x32_bf16 v[0:3], a[172:175], v[76:79], v[0:3]\n"
"v_exp_f32 v234, v234\n"
"v_mfma_f32_16x16x32_bf16 v[4:7], a[176:179], v[64:67], v[24:27]\n"
"ds_read_b128 a[136:139], v241 offset:33280\n"
"v_add_u32 v252, 147456, v252\n"
"v_mfma_f32_16x16x32_bf16 v[4:7], a[180:183], v[68:71], v[4:7]\n"
"v_exp_f32 v235, v235\n"
"v_mfma_f32_16x16x32_bf16 v[4:7], a[184:187], v[72:75], v[4:7]\n"
"ds_read_b128 a[140:143], v241 offset:33536\n"
"v_cvt_pk_bf16_f32 v23, v234, v235\n"
"v_mfma_f32_16x16x32_bf16 v[4:7], a[188:191], v[76:79], v[4:7]\n"
"s_add_u32 m0, s86, 65536\n"
"v_exp_f32 v0, v0\n"
"v_mfma_f32_16x16x32_bf16 a[96:99], a[192:195], v[20:23], a[96:99]\n"
"buffer_load_dwordx4 v246, s[8:11], s84 offen offset:0 lds\n"
"v_mfma_f32_16x16x32_bf16 a[100:103], a[196:199], v[20:23], a[100:103]\n"
"v_exp_f32 v1, v1\n"
"v_mfma_f32_16x16x32_bf16 a[104:107], a[200:203], v[20:23], a[104:107]\n"
"ds_read_b128 a[144:147], v241 offset:36864\n"
"v_cvt_pk_bf16_f32 v16, v0, v1\n"
"v_mfma_f32_16x16x32_bf16 a[108:111], a[204:207], v[20:23], a[108:111]\n"
"v_exp_f32 v2, v2\n"
"v_mfma_f32_16x16x32_bf16 a[112:115], a[208:211], v[20:23], a[112:115]\n"
"ds_read_b128 a[148:151], v241 offset:37120\n"
"v_add_f32 v44, v44, v0\n"
"v_mfma_f32_16x16x32_bf16 a[116:119], a[212:215], v[20:23], a[116:119]\n"
"v_exp_f32 v3, v3\n"
"v_mfma_f32_16x16x32_bf16 a[120:123], a[216:219], v[20:23], a[120:123]\n"
"ds_read_b128 a[152:155], v241 offset:37376\n"
"v_cvt_pk_bf16_f32 v17, v2, v3\n"
"v_mfma_f32_16x16x32_bf16 a[124:127], a[220:223], v[20:23], a[124:127]\n"
"v_exp_f32 v4, v4\n"
"v_mfma_f32_16x16x32_bf16 v[56:59], v[40:43], v[20:23], v[56:59]\n"
"ds_read_b128 a[156:159], v241 offset:37632\n"
"v_add_f32 v44, v44, v1\n"
"v_mfma_f32_16x16x32_bf16 v[8:11], a[160:163], v[80:83], v[28:31]\n"
"v_exp_f32 v5, v5\n"
"v_mfma_f32_16x16x32_bf16 v[8:11], a[164:167], v[84:87], v[8:11]\n"
"ds_read_b64_tr_b16 a[192:193], v244 offset:49152\n"
"v_cvt_pk_bf16_f32 v18, v4, v5\n"
"v_mfma_f32_16x16x32_bf16 v[8:11], a[168:171], v[88:91], v[8:11]\n"
"v_exp_f32 v6, v6\n"
"v_mfma_f32_16x16x32_bf16 v[8:11], a[172:175], v[92:95], v[8:11]\n"
"ds_read_b64_tr_b16 a[194:195], v244 offset:53248\n"
"v_add_f32 v44, v44, v2\n"
"v_mfma_f32_16x16x32_bf16 v[12:15], a[176:179], v[80:83], v[28:31]\n"
"v_exp_f32 v7, v7\n"
"v_mfma_f32_16x16x32_bf16 v[12:15], a[180:183], v[84:87], v[12:15]\n"
"ds_read_b64_tr_b16 a[196:197], v245 offset:49152\n"
"v_cvt_pk_bf16_f32 v19, v6, v7\n"
"v_mfma_f32_16x16x32_bf16 v[12:15], a[184:187], v[88:91], v[12:15]\n"
"buffer_load_dwordx4 v246, s[8:11], s84 offen offset:1024 lds\n"
"v_mfma_f32_16x16x32_bf16 v[12:15], a[188:191], v[92:95], v[12:15]\n"
"s_waitcnt lgkmcnt(11)\n"
"v_exp_f32 v8, v8\n"
"v_mfma_f32_16x16x32_bf16 a[0:3], a[224:227], v[16:19], a[0:3]\n"
"ds_read_b64_tr_b16 a[198:199], v245 offset:53248\n"
"v_add_f32 v44, v44, v3\n"
"v_mfma_f32_16x16x32_bf16 a[4:7], a[228:231], v[16:19], a[4:7]\n"
"v_exp_f32 v9, v9\n"
"v_mfma_f32_16x16x32_bf16 a[8:11], a[232:235], v[16:19], a[8:11]\n"
"ds_read_b64_tr_b16 a[200:201], v244 offset:49408\n"
"v_cvt_pk_bf16_f32 v20, v8, v9\n"
"v_mfma_f32_16x16x32_bf16 a[12:15], a[236:239], v[16:19], a[12:15]\n"
"v_exp_f32 v10, v10\n"
"v_mfma_f32_16x16x32_bf16 a[16:19], a[240:243], v[16:19], a[16:19]\n"
"ds_read_b64_tr_b16 a[202:203], v244 offset:53504\n"
"v_add_f32 v44, v44, v4\n"
"v_mfma_f32_16x16x32_bf16 a[20:23], a[244:247], v[16:19], a[20:23]\n"
"v_exp_f32 v11, v11\n"
"v_mfma_f32_16x16x32_bf16 a[24:27], a[248:251], v[16:19], a[24:27]\n"
"ds_read_b64_tr_b16 a[204:205], v245 offset:49408\n"
"v_cvt_pk_bf16_f32 v21, v10, v11\n"
"v_mfma_f32_16x16x32_bf16 a[28:31], a[252:255], v[16:19], a[28:31]\n"
"v_exp_f32 v12, v12\n"
"v_mfma_f32_16x16x32_bf16 v[220:223], a[160:163], v[96:99], v[32:35]\n"
"ds_read_b64_tr_b16 a[206:207], v245 offset:53504\n"
"v_add_f32 v44, v44, v5\n"
"v_mfma_f32_16x16x32_bf16 v[220:223], a[164:167], v[100:103], v[220:223]\n"
"v_exp_f32 v13, v13\n"
"v_mfma_f32_16x16x32_bf16 v[220:223], a[168:171], v[104:107], v[220:223]\n"
"ds_read_b64_tr_b16 a[208:209], v244 offset:49664\n"
"v_cvt_pk_bf16_f32 v22, v12, v13\n"
"v_mfma_f32_16x16x32_bf16 v[220:223], a[172:175], v[108:111], v[220:223]\n"
"v_exp_f32 v14, v14\n"
"v_mfma_f32_16x16x32_bf16 v[224:227], a[176:179], v[96:99], v[32:35]\n"
"ds_read_b64_tr_b16 a[210:211], v244 offset:53760\n"
"v_add_f32 v44, v44, v6\n"
"v_mfma_f32_16x16x32_bf16 v[224:227], a[180:183], v[100:103], v[224:227]\n"
"v_exp_f32 v15, v15\n"
"v_mfma_f32_16x16x32_bf16 v[224:227], a[184:187], v[104:107], v[224:227]\n"
"ds_read_b64_tr_b16 a[212:213], v245 offset:49664\n"
"v_cvt_pk_bf16_f32 v23, v14, v15\n"
"v_mfma_f32_16x16x32_bf16 v[224:227], a[188:191], v[108:111], v[224:227]\n"
"v_exp_f32 v220, v220\n"
"v_mfma_f32_16x16x32_bf16 a[32:35], a[224:227], v[20:23], a[32:35]\n"
"buffer_load_dwordx4 v247, s[8:11], s84 offen offset:2048 lds\n"
"v_mfma_f32_16x16x32_bf16 a[36:39], a[228:231], v[20:23], a[36:39]\n"
"v_exp_f32 v221, v221\n"
"v_mfma_f32_16x16x32_bf16 a[40:43], a[232:235], v[20:23], a[40:43]\n"
"ds_read_b64_tr_b16 a[214:215], v245 offset:53760\n"
"v_cvt_pk_bf16_f32 v16, v220, v221\n"
"v_mfma_f32_16x16x32_bf16 a[44:47], a[236:239], v[20:23], a[44:47]\n"
"v_exp_f32 v222, v222\n"
"v_mfma_f32_16x16x32_bf16 a[48:51], a[240:243], v[20:23], a[48:51]\n"
"ds_read_b64_tr_b16 a[216:217], v244 offset:49920\n"
"v_add_f32 v44, v44, v7\n"
"v_mfma_f32_16x16x32_bf16 a[52:55], a[244:247], v[20:23], a[52:55]\n"
"v_exp_f32 v223, v223\n"
"v_mfma_f32_16x16x32_bf16 a[56:59], a[248:251], v[20:23], a[56:59]\n"
"ds_read_b64_tr_b16 a[218:219], v244 offset:54016\n"
"v_cvt_pk_bf16_f32 v17, v222, v223\n"
"v_mfma_f32_16x16x32_bf16 a[60:63], a[252:255], v[20:23], a[60:63]\n"
"v_exp_f32 v224, v224\n"
"v_mfma_f32_16x16x32_bf16 v[48:51], v[40:43], v[20:23], v[48:51]\n"
"ds_read_b64_tr_b16 a[220:221], v245 offset:49920\n"
"v_add_f32 v52, v52, v220\n"
"v_mfma_f32_16x16x32_bf16 v[228:231], a[160:163], v[112:115], v[36:39]\n"
"v_exp_f32 v225, v225\n"
"v_mfma_f32_16x16x32_bf16 v[228:231], a[164:167], v[116:119], v[228:231]\n"
"ds_read_b64_tr_b16 a[222:223], v245 offset:54016\n"
"v_cvt_pk_bf16_f32 v18, v224, v225\n"
"v_mfma_f32_16x16x32_bf16 v[228:231], a[168:171], v[120:123], v[228:231]\n"
"v_exp_f32 v226, v226\n"
"v_mfma_f32_16x16x32_bf16 v[228:231], a[172:175], v[124:127], v[228:231]\n"
"v_add_f32 v52, v52, v221\n"
"v_add_f32 v52, v52, v222\n"
"v_mfma_f32_16x16x32_bf16 v[232:235], a[176:179], v[112:115], v[36:39]\n"
"v_exp_f32 v227, v227\n"
"v_mfma_f32_16x16x32_bf16 v[232:235], a[180:183], v[116:119], v[232:235]\n"
"v_cvt_pk_bf16_f32 v19, v226, v227\n"
"v_add_f32 v52, v52, v223\n"
"v_mfma_f32_16x16x32_bf16 v[232:235], a[184:187], v[120:123], v[232:235]\n"
"buffer_load_dwordx4 v247, s[8:11], s84 offen offset:3072 lds\n"
"v_mfma_f32_16x16x32_bf16 v[232:235], a[188:191], v[124:127], v[232:235]\n"
"v_exp_f32 v228, v228\n"
"v_mfma_f32_16x16x32_bf16 a[64:67], a[224:227], v[16:19], a[64:67]\n"
"v_add_f32 v52, v52, v224\n"
"v_add_f32 v52, v52, v225\n"
"v_mfma_f32_16x16x32_bf16 a[68:71], a[228:231], v[16:19], a[68:71]\n"
"v_exp_f32 v229, v229\n"
"v_mfma_f32_16x16x32_bf16 a[72:75], a[232:235], v[16:19], a[72:75]\n"
"v_cvt_pk_bf16_f32 v20, v228, v229\n"
"v_add_f32 v52, v52, v226\n"
"v_mfma_f32_16x16x32_bf16 a[76:79], a[236:239], v[16:19], a[76:79]\n"
"v_exp_f32 v230, v230\n"
"v_mfma_f32_16x16x32_bf16 a[80:83], a[240:243], v[16:19], a[80:83]\n"
"v_add_f32 v52, v52, v227\n"
"v_mfma_f32_16x16x32_bf16 a[84:87], a[244:247], v[16:19], a[84:87]\n"
"v_exp_f32 v231, v231\n"
"v_mfma_f32_16x16x32_bf16 a[88:91], a[248:251], v[16:19], a[88:91]\n"
"s_waitcnt lgkmcnt(8)\n"
"v_cvt_pk_bf16_f32 v21, v230, v231\n"
"v_mfma_f32_16x16x32_bf16 a[92:95], a[252:255], v[16:19], a[92:95]\n"
"v_exp_f32 v232, v232\n"
"v_mfma_f32_16x16x32_bf16 v[0:3], a[128:131], v[64:67], v[24:27]\n"
"ds_read_b128 a[160:163], v241 offset:40960\n"
"v_mfma_f32_16x16x32_bf16 v[0:3], a[132:135], v[68:71], v[0:3]\n"
"v_exp_f32 v233, v233\n"
"v_mfma_f32_16x16x32_bf16 v[0:3], a[136:139], v[72:75], v[0:3]\n"
"ds_read_b128 a[164:167], v241 offset:41216\n"
"v_cvt_pk_bf16_f32 v22, v232, v233\n"
"v_mfma_f32_16x16x32_bf16 v[0:3], a[140:143], v[76:79], v[0:3]\n"
"v_exp_f32 v234, v234\n"
"v_mfma_f32_16x16x32_bf16 v[4:7], a[144:147], v[64:67], v[24:27]\n"
"ds_read_b128 a[168:171], v241 offset:41472\n"
"v_mfma_f32_16x16x32_bf16 v[4:7], a[148:151], v[68:71], v[4:7]\n"
"v_exp_f32 v235, v235\n"
"v_mfma_f32_16x16x32_bf16 v[4:7], a[152:155], v[72:75], v[4:7]\n"
"ds_read_b128 a[172:175], v241 offset:41728\n"
"v_cvt_pk_bf16_f32 v23, v234, v235\n"
"v_mfma_f32_16x16x32_bf16 v[4:7], a[156:159], v[76:79], v[4:7]\n"
"s_add_u32 m0, s86, 81920\n"
"v_exp_f32 v0, v0\n"
"v_mfma_f32_16x16x32_bf16 a[96:99], a[224:227], v[20:23], a[96:99]\n"
"buffer_load_dwordx4 v246, s[12:15], s84 offen offset:0 lds\n"
"v_mfma_f32_16x16x32_bf16 a[100:103], a[228:231], v[20:23], a[100:103]\n"
"v_exp_f32 v1, v1\n"
"v_mfma_f32_16x16x32_bf16 a[104:107], a[232:235], v[20:23], a[104:107]\n"
"ds_read_b128 a[176:179], v241 offset:45056\n"
"v_cvt_pk_bf16_f32 v16, v0, v1\n"
"v_mfma_f32_16x16x32_bf16 a[108:111], a[236:239], v[20:23], a[108:111]\n"
"v_exp_f32 v2, v2\n"
"v_mfma_f32_16x16x32_bf16 a[112:115], a[240:243], v[20:23], a[112:115]\n"
"ds_read_b128 a[180:183], v241 offset:45312\n"
"v_add_f32 v44, v44, v0\n"
"v_mfma_f32_16x16x32_bf16 a[116:119], a[244:247], v[20:23], a[116:119]\n"
"v_exp_f32 v3, v3\n"
"v_mfma_f32_16x16x32_bf16 a[120:123], a[248:251], v[20:23], a[120:123]\n"
"ds_read_b128 a[184:187], v241 offset:45568\n"
"v_cvt_pk_bf16_f32 v17, v2, v3\n"
"v_mfma_f32_16x16x32_bf16 a[124:127], a[252:255], v[20:23], a[124:127]\n"
"v_exp_f32 v4, v4\n"
"v_mfma_f32_16x16x32_bf16 v[56:59], v[40:43], v[20:23], v[56:59]\n"
"ds_read_b128 a[188:191], v241 offset:45824\n"
"v_add_f32 v44, v44, v1\n"
"v_mfma_f32_16x16x32_bf16 v[8:11], a[128:131], v[80:83], v[28:31]\n"
"v_exp_f32 v5, v5\n"
"v_mfma_f32_16x16x32_bf16 v[8:11], a[132:135], v[84:87], v[8:11]\n"
"ds_read_b64_tr_b16 a[224:225], v244 offset:57344\n"
"v_cvt_pk_bf16_f32 v18, v4, v5\n"
"v_mfma_f32_16x16x32_bf16 v[8:11], a[136:139], v[88:91], v[8:11]\n"
"v_exp_f32 v6, v6\n"
"v_mfma_f32_16x16x32_bf16 v[8:11], a[140:143], v[92:95], v[8:11]\n"
"ds_read_b64_tr_b16 a[226:227], v244 offset:61440\n"
"v_add_f32 v44, v44, v2\n"
"v_mfma_f32_16x16x32_bf16 v[12:15], a[144:147], v[80:83], v[28:31]\n"
"v_exp_f32 v7, v7\n"
"v_mfma_f32_16x16x32_bf16 v[12:15], a[148:151], v[84:87], v[12:15]\n"
"ds_read_b64_tr_b16 a[228:229], v245 offset:57344\n"
"v_cvt_pk_bf16_f32 v19, v6, v7\n"
"v_mfma_f32_16x16x32_bf16 v[12:15], a[152:155], v[88:91], v[12:15]\n"
"buffer_load_dwordx4 v247, s[12:15], s84 offen offset:1024 lds\n"
"v_mfma_f32_16x16x32_bf16 v[12:15], a[156:159], v[92:95], v[12:15]\n"
"s_waitcnt lgkmcnt(11)\n"
"v_exp_f32 v8, v8\n"
"v_mfma_f32_16x16x32_bf16 a[0:3], a[192:195], v[16:19], a[0:3]\n"
"ds_read_b64_tr_b16 a[230:231], v245 offset:61440\n"
"v_add_f32 v44, v44, v3\n"
"v_mfma_f32_16x16x32_bf16 a[4:7], a[196:199], v[16:19], a[4:7]\n"
"v_exp_f32 v9, v9\n"
"v_mfma_f32_16x16x32_bf16 a[8:11], a[200:203], v[16:19], a[8:11]\n"
"ds_read_b64_tr_b16 a[232:233], v244 offset:57600\n"
"v_cvt_pk_bf16_f32 v20, v8, v9\n"
"v_mfma_f32_16x16x32_bf16 a[12:15], a[204:207], v[16:19], a[12:15]\n"
"v_exp_f32 v10, v10\n"
"v_mfma_f32_16x16x32_bf16 a[16:19], a[208:211], v[16:19], a[16:19]\n"
"ds_read_b64_tr_b16 a[234:235], v244 offset:61696\n"
"v_add_f32 v44, v44, v4\n"
"v_mfma_f32_16x16x32_bf16 a[20:23], a[212:215], v[16:19], a[20:23]\n"
"v_exp_f32 v11, v11\n"
"v_mfma_f32_16x16x32_bf16 a[24:27], a[216:219], v[16:19], a[24:27]\n"
"ds_read_b64_tr_b16 a[236:237], v245 offset:57600\n"
"v_cvt_pk_bf16_f32 v21, v10, v11\n"
"v_mfma_f32_16x16x32_bf16 a[28:31], a[220:223], v[16:19], a[28:31]\n"
"v_exp_f32 v12, v12\n"
"v_mfma_f32_16x16x32_bf16 v[220:223], a[128:131], v[96:99], v[32:35]\n"
"ds_read_b64_tr_b16 a[238:239], v245 offset:61696\n"
"v_add_f32 v44, v44, v5\n"
"v_mfma_f32_16x16x32_bf16 v[220:223], a[132:135], v[100:103], v[220:223]\n"
"v_exp_f32 v13, v13\n"
"v_mfma_f32_16x16x32_bf16 v[220:223], a[136:139], v[104:107], v[220:223]\n"
"ds_read_b64_tr_b16 a[240:241], v244 offset:57856\n"
"v_cvt_pk_bf16_f32 v22, v12, v13\n"
"v_mfma_f32_16x16x32_bf16 v[220:223], a[140:143], v[108:111], v[220:223]\n"
"v_exp_f32 v14, v14\n"
"v_mfma_f32_16x16x32_bf16 v[224:227], a[144:147], v[96:99], v[32:35]\n"
"ds_read_b64_tr_b16 a[242:243], v244 offset:61952\n"
"v_add_f32 v44, v44, v6\n"
"v_mfma_f32_16x16x32_bf16 v[224:227], a[148:151], v[100:103], v[224:227]\n"
"v_exp_f32 v15, v15\n"
"v_mfma_f32_16x16x32_bf16 v[224:227], a[152:155], v[104:107], v[224:227]\n"
"ds_read_b64_tr_b16 a[244:245], v245 offset:57856\n"
"v_cvt_pk_bf16_f32 v23, v14, v15\n"
"v_mfma_f32_16x16x32_bf16 v[224:227], a[156:159], v[108:111], v[224:227]\n"
"v_exp_f32 v220, v220\n"
"v_mfma_f32_16x16x32_bf16 a[32:35], a[192:195], v[20:23], a[32:35]\n"
"buffer_load_dwordx4 v246, s[12:15], s84 offen offset:2048 lds\n"
"v_mfma_f32_16x16x32_bf16 a[36:39], a[196:199], v[20:23], a[36:39]\n"
"v_exp_f32 v221, v221\n"
"v_mfma_f32_16x16x32_bf16 a[40:43], a[200:203], v[20:23], a[40:43]\n"
"ds_read_b64_tr_b16 a[246:247], v245 offset:61952\n"
"v_cvt_pk_bf16_f32 v16, v220, v221\n"
"v_mfma_f32_16x16x32_bf16 a[44:47], a[204:207], v[20:23], a[44:47]\n"
"v_exp_f32 v222, v222\n"
"v_mfma_f32_16x16x32_bf16 a[48:51], a[208:211], v[20:23], a[48:51]\n"
"ds_read_b64_tr_b16 a[248:249], v244 offset:58112\n"
"v_add_f32 v44, v44, v7\n"
"v_mfma_f32_16x16x32_bf16 a[52:55], a[212:215], v[20:23], a[52:55]\n"
"v_exp_f32 v223, v223\n"
"v_mfma_f32_16x16x32_bf16 a[56:59], a[216:219], v[20:23], a[56:59]\n"
"ds_read_b64_tr_b16 a[250:251], v244 offset:62208\n"
"v_cvt_pk_bf16_f32 v17, v222, v223\n"
"v_mfma_f32_16x16x32_bf16 a[60:63], a[220:223], v[20:23], a[60:63]\n"
"v_exp_f32 v224, v224\n"
"v_mfma_f32_16x16x32_bf16 v[48:51], v[40:43], v[20:23], v[48:51]\n"
"ds_read_b64_tr_b16 a[252:253], v245 offset:58112\n"
"v_add_f32 v52, v52, v220\n"
"v_mfma_f32_16x16x32_bf16 v[228:231], a[128:131], v[112:115], v[36:39]\n"
"v_exp_f32 v225, v225\n"
"v_mfma_f32_16x16x32_bf16 v[228:231], a[132:135], v[116:119], v[228:231]\n"
"ds_read_b64_tr_b16 a[254:255], v245 offset:62208\n"
"v_cvt_pk_bf16_f32 v18, v224, v225\n"
"v_mfma_f32_16x16x32_bf16 v[228:231], a[136:139], v[120:123], v[228:231]\n"
"v_exp_f32 v226, v226\n"
"v_mfma_f32_16x16x32_bf16 v[228:231], a[140:143], v[124:127], v[228:231]\n"
"v_add_f32 v52, v52, v221\n"
"v_add_f32 v52, v52, v222\n"
"v_mfma_f32_16x16x32_bf16 v[232:235], a[144:147], v[112:115], v[36:39]\n"
"v_exp_f32 v227, v227\n"
"v_mfma_f32_16x16x32_bf16 v[232:235], a[148:151], v[116:119], v[232:235]\n"
"v_cvt_pk_bf16_f32 v19, v226, v227\n"
"v_add_f32 v52, v52, v223\n"
"v_mfma_f32_16x16x32_bf16 v[232:235], a[152:155], v[120:123], v[232:235]\n"
"buffer_load_dwordx4 v247, s[12:15], s84 offen offset:3072 lds\n"
"v_mfma_f32_16x16x32_bf16 v[232:235], a[156:159], v[124:127], v[232:235]\n"
"s_add_u32 s84, s84, 16384\n"
"v_exp_f32 v228, v228\n"
"v_mfma_f32_16x16x32_bf16 a[64:67], a[192:195], v[16:19], a[64:67]\n"
"v_add_f32 v52, v52, v224\n"
"v_add_f32 v52, v52, v225\n"
"v_mfma_f32_16x16x32_bf16 a[68:71], a[196:199], v[16:19], a[68:71]\n"
"v_exp_f32 v229, v229\n"
"v_mfma_f32_16x16x32_bf16 a[72:75], a[200:203], v[16:19], a[72:75]\n"
"v_cvt_pk_bf16_f32 v20, v228, v229\n"
"v_add_f32 v52, v52, v226\n"
"v_mfma_f32_16x16x32_bf16 a[76:79], a[204:207], v[16:19], a[76:79]\n"
"v_exp_f32 v230, v230\n"
"v_mfma_f32_16x16x32_bf16 a[80:83], a[208:211], v[16:19], a[80:83]\n"
"v_add_f32 v52, v52, v227\n"
"v_mfma_f32_16x16x32_bf16 a[84:87], a[212:215], v[16:19], a[84:87]\n"
"v_exp_f32 v231, v231\n"
"v_mfma_f32_16x16x32_bf16 a[88:91], a[216:219], v[16:19], a[88:91]\n"
"s_waitcnt vmcnt(18) lgkmcnt(0)\n"
"s_barrier\n"
"v_cvt_pk_bf16_f32 v21, v230, v231\n"
"v_mfma_f32_16x16x32_bf16 a[92:95], a[220:223], v[16:19], a[92:95]\n"
"v_exp_f32 v232, v232\n"
"v_mfma_f32_16x16x32_bf16 v[0:3], a[160:163], v[64:67], v[24:27]\n"
"ds_read_b128 a[128:131], v240 offset:0\n"
"buffer_load_dword v250, v253, s[24:27], s47 offen\n"
"v_mfma_f32_16x16x32_bf16 v[0:3], a[164:167], v[68:71], v[0:3]\n"
"v_exp_f32 v233, v233\n"
"v_mfma_f32_16x16x32_bf16 v[0:3], a[168:171], v[72:75], v[0:3]\n"
"ds_read_b128 a[132:135], v240 offset:256\n"
"v_cvt_pk_bf16_f32 v22, v232, v233\n"
"v_mfma_f32_16x16x32_bf16 v[0:3], a[172:175], v[76:79], v[0:3]\n"
"v_exp_f32 v234, v234\n"
"v_mfma_f32_16x16x32_bf16 v[4:7], a[176:179], v[64:67], v[24:27]\n"
"ds_read_b128 a[136:139], v240 offset:512\n"
"v_add_u32 v253, 294912, v253\n"
"v_mfma_f32_16x16x32_bf16 v[4:7], a[180:183], v[68:71], v[4:7]\n"
"v_exp_f32 v235, v235\n"
"v_mfma_f32_16x16x32_bf16 v[4:7], a[184:187], v[72:75], v[4:7]\n"
"ds_read_b128 a[140:143], v240 offset:768\n"
"v_cvt_pk_bf16_f32 v23, v234, v235\n"
"v_mfma_f32_16x16x32_bf16 v[4:7], a[188:191], v[76:79], v[4:7]\n"
"s_add_u32 m0, s86, 98304\n"
"v_exp_f32 v0, v0\n"
"v_mfma_f32_16x16x32_bf16 a[96:99], a[192:195], v[20:23], a[96:99]\n"
"buffer_load_dwordx4 v246, s[8:11], s84 offen offset:0 lds\n"
"v_mfma_f32_16x16x32_bf16 a[100:103], a[196:199], v[20:23], a[100:103]\n"
"v_exp_f32 v1, v1\n"
"v_mfma_f32_16x16x32_bf16 a[104:107], a[200:203], v[20:23], a[104:107]\n"
"ds_read_b128 a[144:147], v240 offset:4096\n"
"v_cvt_pk_bf16_f32 v16, v0, v1\n"
"v_mfma_f32_16x16x32_bf16 a[108:111], a[204:207], v[20:23], a[108:111]\n"
"v_exp_f32 v2, v2\n"
"v_mfma_f32_16x16x32_bf16 a[112:115], a[208:211], v[20:23], a[112:115]\n"
"ds_read_b128 a[148:151], v240 offset:4352\n"
"v_add_f32 v44, v44, v0\n"
"v_mfma_f32_16x16x32_bf16 a[116:119], a[212:215], v[20:23], a[116:119]\n"
"v_exp_f32 v3, v3\n"
"v_mfma_f32_16x16x32_bf16 a[120:123], a[216:219], v[20:23], a[120:123]\n"
"ds_read_b128 a[152:155], v240 offset:4608\n"
"v_cvt_pk_bf16_f32 v17, v2, v3\n"
"v_mfma_f32_16x16x32_bf16 a[124:127], a[220:223], v[20:23], a[124:127]\n"
"v_exp_f32 v4, v4\n"
"v_mfma_f32_16x16x32_bf16 v[56:59], v[40:43], v[20:23], v[56:59]\n"
"ds_read_b128 a[156:159], v240 offset:4864\n"
"v_add_f32 v44, v44, v1\n"
"v_mfma_f32_16x16x32_bf16 v[8:11], a[160:163], v[80:83], v[28:31]\n"
"v_exp_f32 v5, v5\n"
"v_mfma_f32_16x16x32_bf16 v[8:11], a[164:167], v[84:87], v[8:11]\n"
"ds_read_b64_tr_b16 a[192:193], v242 offset:16384\n"
"v_cvt_pk_bf16_f32 v18, v4, v5\n"
"v_mfma_f32_16x16x32_bf16 v[8:11], a[168:171], v[88:91], v[8:11]\n"
"v_exp_f32 v6, v6\n"
"v_mfma_f32_16x16x32_bf16 v[8:11], a[172:175], v[92:95], v[8:11]\n"
"ds_read_b64_tr_b16 a[194:195], v242 offset:20480\n"
"v_add_f32 v44, v44, v2\n"
"v_mfma_f32_16x16x32_bf16 v[12:15], a[176:179], v[80:83], v[28:31]\n"
"v_exp_f32 v7, v7\n"
"v_mfma_f32_16x16x32_bf16 v[12:15], a[180:183], v[84:87], v[12:15]\n"
"ds_read_b64_tr_b16 a[196:197], v243 offset:16384\n"
"v_cvt_pk_bf16_f32 v19, v6, v7\n"
"v_mfma_f32_16x16x32_bf16 v[12:15], a[184:187], v[88:91], v[12:15]\n"
"buffer_load_dwordx4 v246, s[8:11], s84 offen offset:1024 lds\n"
"v_mfma_f32_16x16x32_bf16 v[12:15], a[188:191], v[92:95], v[12:15]\n"
"s_waitcnt lgkmcnt(11)\n"
"v_exp_f32 v8, v8\n"
"v_mfma_f32_16x16x32_bf16 a[0:3], a[224:227], v[16:19], a[0:3]\n"
"ds_read_b64_tr_b16 a[198:199], v243 offset:20480\n"
"v_add_f32 v44, v44, v3\n"
"v_mfma_f32_16x16x32_bf16 a[4:7], a[228:231], v[16:19], a[4:7]\n"
"v_exp_f32 v9, v9\n"
"v_mfma_f32_16x16x32_bf16 a[8:11], a[232:235], v[16:19], a[8:11]\n"
"ds_read_b64_tr_b16 a[200:201], v242 offset:16640\n"
"v_cvt_pk_bf16_f32 v20, v8, v9\n"
"v_mfma_f32_16x16x32_bf16 a[12:15], a[236:239], v[16:19], a[12:15]\n"
"v_exp_f32 v10, v10\n"
"v_mfma_f32_16x16x32_bf16 a[16:19], a[240:243], v[16:19], a[16:19]\n"
"ds_read_b64_tr_b16 a[202:203], v242 offset:20736\n"
"v_add_f32 v44, v44, v4\n"
"v_mfma_f32_16x16x32_bf16 a[20:23], a[244:247], v[16:19], a[20:23]\n"
"v_exp_f32 v11, v11\n"
"v_mfma_f32_16x16x32_bf16 a[24:27], a[248:251], v[16:19], a[24:27]\n"
"ds_read_b64_tr_b16 a[204:205], v243 offset:16640\n"
"v_cvt_pk_bf16_f32 v21, v10, v11\n"
"v_mfma_f32_16x16x32_bf16 a[28:31], a[252:255], v[16:19], a[28:31]\n"
"v_exp_f32 v12, v12\n"
"v_mfma_f32_16x16x32_bf16 v[220:223], a[160:163], v[96:99], v[32:35]\n"
"ds_read_b64_tr_b16 a[206:207], v243 offset:20736\n"
"v_add_f32 v44, v44, v5\n"
"v_mfma_f32_16x16x32_bf16 v[220:223], a[164:167], v[100:103], v[220:223]\n"
"v_exp_f32 v13, v13\n"
"v_mfma_f32_16x16x32_bf16 v[220:223], a[168:171], v[104:107], v[220:223]\n"
"ds_read_b64_tr_b16 a[208:209], v242 offset:16896\n"
"v_cvt_pk_bf16_f32 v22, v12, v13\n"
"v_mfma_f32_16x16x32_bf16 v[220:223], a[172:175], v[108:111], v[220:223]\n"
"v_exp_f32 v14, v14\n"
"v_mfma_f32_16x16x32_bf16 v[224:227], a[176:179], v[96:99], v[32:35]\n"
"ds_read_b64_tr_b16 a[210:211], v242 offset:20992\n"
"v_add_f32 v44, v44, v6\n"
"v_mfma_f32_16x16x32_bf16 v[224:227], a[180:183], v[100:103], v[224:227]\n"
"v_exp_f32 v15, v15\n"
"v_mfma_f32_16x16x32_bf16 v[224:227], a[184:187], v[104:107], v[224:227]\n"
"ds_read_b64_tr_b16 a[212:213], v243 offset:16896\n"
"v_cvt_pk_bf16_f32 v23, v14, v15\n"
"v_mfma_f32_16x16x32_bf16 v[224:227], a[188:191], v[108:111], v[224:227]\n"
"v_exp_f32 v220, v220\n"
"v_mfma_f32_16x16x32_bf16 a[32:35], a[224:227], v[20:23], a[32:35]\n"
"buffer_load_dwordx4 v247, s[8:11], s84 offen offset:2048 lds\n"
"v_mfma_f32_16x16x32_bf16 a[36:39], a[228:231], v[20:23], a[36:39]\n"
"v_exp_f32 v221, v221\n"
"v_mfma_f32_16x16x32_bf16 a[40:43], a[232:235], v[20:23], a[40:43]\n"
"ds_read_b64_tr_b16 a[214:215], v243 offset:20992\n"
"v_cvt_pk_bf16_f32 v16, v220, v221\n"
"v_mfma_f32_16x16x32_bf16 a[44:47], a[236:239], v[20:23], a[44:47]\n"
"v_exp_f32 v222, v222\n"
"v_mfma_f32_16x16x32_bf16 a[48:51], a[240:243], v[20:23], a[48:51]\n"
"ds_read_b64_tr_b16 a[216:217], v242 offset:17152\n"
"v_add_f32 v44, v44, v7\n"
"v_mfma_f32_16x16x32_bf16 a[52:55], a[244:247], v[20:23], a[52:55]\n"
"v_exp_f32 v223, v223\n"
"v_mfma_f32_16x16x32_bf16 a[56:59], a[248:251], v[20:23], a[56:59]\n"
"ds_read_b64_tr_b16 a[218:219], v242 offset:21248\n"
"v_cvt_pk_bf16_f32 v17, v222, v223\n"
"v_mfma_f32_16x16x32_bf16 a[60:63], a[252:255], v[20:23], a[60:63]\n"
"v_exp_f32 v224, v224\n"
"v_mfma_f32_16x16x32_bf16 v[48:51], v[40:43], v[20:23], v[48:51]\n"
"ds_read_b64_tr_b16 a[220:221], v243 offset:17152\n"
"v_add_f32 v52, v52, v220\n"
"v_mfma_f32_16x16x32_bf16 v[228:231], a[160:163], v[112:115], v[36:39]\n"
"v_exp_f32 v225, v225\n"
"v_mfma_f32_16x16x32_bf16 v[228:231], a[164:167], v[116:119], v[228:231]\n"
"ds_read_b64_tr_b16 a[222:223], v243 offset:21248\n"
"v_cvt_pk_bf16_f32 v18, v224, v225\n"
"v_mfma_f32_16x16x32_bf16 v[228:231], a[168:171], v[120:123], v[228:231]\n"
"v_exp_f32 v226, v226\n"
"v_mfma_f32_16x16x32_bf16 v[228:231], a[172:175], v[124:127], v[228:231]\n"
"v_add_f32 v52, v52, v221\n"
"v_add_f32 v52, v52, v222\n"
"v_mfma_f32_16x16x32_bf16 v[232:235], a[176:179], v[112:115], v[36:39]\n"
"v_exp_f32 v227, v227\n"
"v_mfma_f32_16x16x32_bf16 v[232:235], a[180:183], v[116:119], v[232:235]\n"
"v_cvt_pk_bf16_f32 v19, v226, v227\n"
"v_add_f32 v52, v52, v223\n"
"v_mfma_f32_16x16x32_bf16 v[232:235], a[184:187], v[120:123], v[232:235]\n"
"buffer_load_dwordx4 v247, s[8:11], s84 offen offset:3072 lds\n"
"v_mfma_f32_16x16x32_bf16 v[232:235], a[188:191], v[124:127], v[232:235]\n"
"v_exp_f32 v228, v228\n"
"v_mfma_f32_16x16x32_bf16 a[64:67], a[224:227], v[16:19], a[64:67]\n"
"v_add_f32 v52, v52, v224\n"
"v_add_f32 v52, v52, v225\n"
"v_mfma_f32_16x16x32_bf16 a[68:71], a[228:231], v[16:19], a[68:71]\n"
"v_exp_f32 v229, v229\n"
"v_mfma_f32_16x16x32_bf16 a[72:75], a[232:235], v[16:19], a[72:75]\n"
"v_cvt_pk_bf16_f32 v20, v228, v229\n"
"v_add_f32 v52, v52, v226\n"
"v_mfma_f32_16x16x32_bf16 a[76:79], a[236:239], v[16:19], a[76:79]\n"
"v_exp_f32 v230, v230\n"
"v_mfma_f32_16x16x32_bf16 a[80:83], a[240:243], v[16:19], a[80:83]\n"
"v_add_f32 v52, v52, v227\n"
"v_mfma_f32_16x16x32_bf16 a[84:87], a[244:247], v[16:19], a[84:87]\n"
"v_exp_f32 v231, v231\n"
"v_mfma_f32_16x16x32_bf16 a[88:91], a[248:251], v[16:19], a[88:91]\n"
"s_waitcnt lgkmcnt(8)\n"
"v_cvt_pk_bf16_f32 v21, v230, v231\n"
"v_mfma_f32_16x16x32_bf16 a[92:95], a[252:255], v[16:19], a[92:95]\n"
"v_exp_f32 v232, v232\n"
"v_mfma_f32_16x16x32_bf16 v[0:3], a[128:131], v[64:67], v[24:27]\n"
"ds_read_b128 a[160:163], v240 offset:8192\n"
"v_mfma_f32_16x16x32_bf16 v[0:3], a[132:135], v[68:71], v[0:3]\n"
"v_exp_f32 v233, v233\n"
"v_mfma_f32_16x16x32_bf16 v[0:3], a[136:139], v[72:75], v[0:3]\n"
"ds_read_b128 a[164:167], v240 offset:8448\n"
"v_cvt_pk_bf16_f32 v22, v232, v233\n"
"v_mfma_f32_16x16x32_bf16 v[0:3], a[140:143], v[76:79], v[0:3]\n"
"v_exp_f32 v234, v234\n"
"v_mfma_f32_16x16x32_bf16 v[4:7], a[144:147], v[64:67], v[24:27]\n"
"ds_read_b128 a[168:171], v240 offset:8704\n"
"v_mfma_f32_16x16x32_bf16 v[4:7], a[148:151], v[68:71], v[4:7]\n"
"v_exp_f32 v235, v235\n"
"v_mfma_f32_16x16x32_bf16 v[4:7], a[152:155], v[72:75], v[4:7]\n"
"ds_read_b128 a[172:175], v240 offset:8960\n"
"v_cvt_pk_bf16_f32 v23, v234, v235\n"
"v_mfma_f32_16x16x32_bf16 v[4:7], a[156:159], v[76:79], v[4:7]\n"
"s_add_u32 m0, s86, 114688\n"
"v_exp_f32 v0, v0\n"
"v_mfma_f32_16x16x32_bf16 a[96:99], a[224:227], v[20:23], a[96:99]\n"
"buffer_load_dwordx4 v246, s[12:15], s84 offen offset:0 lds\n"
"v_mfma_f32_16x16x32_bf16 a[100:103], a[228:231], v[20:23], a[100:103]\n"
"v_exp_f32 v1, v1\n"
"v_mfma_f32_16x16x32_bf16 a[104:107], a[232:235], v[20:23], a[104:107]\n"
"ds_read_b128 a[176:179], v240 offset:12288\n"
"v_cvt_pk_bf16_f32 v16, v0, v1\n"
"v_mfma_f32_16x16x32_bf16 a[108:111], a[236:239], v[20:23], a[108:111]\n"
"v_exp_f32 v2, v2\n"
"v_mfma_f32_16x16x32_bf16 a[112:115], a[240:243], v[20:23], a[112:115]\n"
"ds_read_b128 a[180:183], v240 offset:12544\n"
"v_add_f32 v44, v44, v0\n"
"v_mfma_f32_16x16x32_bf16 a[116:119], a[244:247], v[20:23], a[116:119]\n"
"v_exp_f32 v3, v3\n"
"v_mfma_f32_16x16x32_bf16 a[120:123], a[248:251], v[20:23], a[120:123]\n"
"ds_read_b128 a[184:187], v240 offset:12800\n"
"v_cvt_pk_bf16_f32 v17, v2, v3\n"
"v_mfma_f32_16x16x32_bf16 a[124:127], a[252:255], v[20:23], a[124:127]\n"
"v_exp_f32 v4, v4\n"
"v_mfma_f32_16x16x32_bf16 v[56:59], v[40:43], v[20:23], v[56:59]\n"
"ds_read_b128 a[188:191], v240 offset:13056\n"
"v_add_f32 v44, v44, v1\n"
"v_mfma_f32_16x16x32_bf16 v[8:11], a[128:131], v[80:83], v[28:31]\n"
"v_exp_f32 v5, v5\n"
"v_mfma_f32_16x16x32_bf16 v[8:11], a[132:135], v[84:87], v[8:11]\n"
"ds_read_b64_tr_b16 a[224:225], v242 offset:24576\n"
"v_cvt_pk_bf16_f32 v18, v4, v5\n"
"v_mfma_f32_16x16x32_bf16 v[8:11], a[136:139], v[88:91], v[8:11]\n"
"v_exp_f32 v6, v6\n"
"v_mfma_f32_16x16x32_bf16 v[8:11], a[140:143], v[92:95], v[8:11]\n"
"ds_read_b64_tr_b16 a[226:227], v242 offset:28672\n"
"v_add_f32 v44, v44, v2\n"
"v_mfma_f32_16x16x32_bf16 v[12:15], a[144:147], v[80:83], v[28:31]\n"
"v_exp_f32 v7, v7\n"
"v_mfma_f32_16x16x32_bf16 v[12:15], a[148:151], v[84:87], v[12:15]\n"
"ds_read_b64_tr_b16 a[228:229], v243 offset:24576\n"
"v_cvt_pk_bf16_f32 v19, v6, v7\n"
"v_mfma_f32_16x16x32_bf16 v[12:15], a[152:155], v[88:91], v[12:15]\n"
"buffer_load_dwordx4 v247, s[12:15], s84 offen offset:1024 lds\n"
"v_mfma_f32_16x16x32_bf16 v[12:15], a[156:159], v[92:95], v[12:15]\n"
"s_waitcnt lgkmcnt(11)\n"
"v_exp_f32 v8, v8\n"
"v_mfma_f32_16x16x32_bf16 a[0:3], a[192:195], v[16:19], a[0:3]\n"
"ds_read_b64_tr_b16 a[230:231], v243 offset:28672\n"
"v_add_f32 v44, v44, v3\n"
"v_mfma_f32_16x16x32_bf16 a[4:7], a[196:199], v[16:19], a[4:7]\n"
"v_exp_f32 v9, v9\n"
"v_mfma_f32_16x16x32_bf16 a[8:11], a[200:203], v[16:19], a[8:11]\n"
"ds_read_b64_tr_b16 a[232:233], v242 offset:24832\n"
"v_cvt_pk_bf16_f32 v20, v8, v9\n"
"v_mfma_f32_16x16x32_bf16 a[12:15], a[204:207], v[16:19], a[12:15]\n"
"v_exp_f32 v10, v10\n"
"v_mfma_f32_16x16x32_bf16 a[16:19], a[208:211], v[16:19], a[16:19]\n"
"ds_read_b64_tr_b16 a[234:235], v242 offset:28928\n"
"v_add_f32 v44, v44, v4\n"
"v_mfma_f32_16x16x32_bf16 a[20:23], a[212:215], v[16:19], a[20:23]\n"
"v_exp_f32 v11, v11\n"
"v_mfma_f32_16x16x32_bf16 a[24:27], a[216:219], v[16:19], a[24:27]\n"
"ds_read_b64_tr_b16 a[236:237], v243 offset:24832\n"
"v_cvt_pk_bf16_f32 v21, v10, v11\n"
"v_mfma_f32_16x16x32_bf16 a[28:31], a[220:223], v[16:19], a[28:31]\n"
"v_exp_f32 v12, v12\n"
"v_mfma_f32_16x16x32_bf16 v[220:223], a[128:131], v[96:99], v[32:35]\n"
"ds_read_b64_tr_b16 a[238:239], v243 offset:28928\n"
"v_add_f32 v44, v44, v5\n"
"v_mfma_f32_16x16x32_bf16 v[220:223], a[132:135], v[100:103], v[220:223]\n"
"v_exp_f32 v13, v13\n"
"v_mfma_f32_16x16x32_bf16 v[220:223], a[136:139], v[104:107], v[220:223]\n"
"ds_read_b64_tr_b16 a[240:241], v242 offset:25088\n"
"v_cvt_pk_bf16_f32 v22, v12, v13\n"
"v_mfma_f32_16x16x32_bf16 v[220:223], a[140:143], v[108:111], v[220:223]\n"
"v_exp_f32 v14, v14\n"
"v_mfma_f32_16x16x32_bf16 v[224:227], a[144:147], v[96:99], v[32:35]\n"
"ds_read_b64_tr_b16 a[242:243], v242 offset:29184\n"
"v_add_f32 v44, v44, v6\n"
"v_mfma_f32_16x16x32_bf16 v[224:227], a[148:151], v[100:103], v[224:227]\n"
"v_exp_f32 v15, v15\n"
"v_mfma_f32_16x16x32_bf16 v[224:227], a[152:155], v[104:107], v[224:227]\n"
"ds_read_b64_tr_b16 a[244:245], v243 offset:25088\n"
"v_cvt_pk_bf16_f32 v23, v14, v15\n"
"v_mfma_f32_16x16x32_bf16 v[224:227], a[156:159], v[108:111], v[224:227]\n"
"v_exp_f32 v220, v220\n"
"v_mfma_f32_16x16x32_bf16 a[32:35], a[192:195], v[20:23], a[32:35]\n"
"buffer_load_dwordx4 v246, s[12:15], s84 offen offset:2048 lds\n"
"v_mfma_f32_16x16x32_bf16 a[36:39], a[196:199], v[20:23], a[36:39]\n"
"v_exp_f32 v221, v221\n"
"v_mfma_f32_16x16x32_bf16 a[40:43], a[200:203], v[20:23], a[40:43]\n"
"ds_read_b64_tr_b16 a[246:247], v243 offset:29184\n"
"v_cvt_pk_bf16_f32 v16, v220, v221\n"
"v_mfma_f32_16x16x32_bf16 a[44:47], a[204:207], v[20:23], a[44:47]\n"
"v_exp_f32 v222, v222\n"
"v_mfma_f32_16x16x32_bf16 a[48:51], a[208:211], v[20:23], a[48:51]\n"
"ds_read_b64_tr_b16 a[248:249], v242 offset:25344\n"
"v_add_f32 v44, v44, v7\n"
"v_mfma_f32_16x16x32_bf16 a[52:55], a[212:215], v[20:23], a[52:55]\n"
"v_exp_f32 v223, v223\n"
"v_mfma_f32_16x16x32_bf16 a[56:59], a[216:219], v[20:23], a[56:59]\n"
"ds_read_b64_tr_b16 a[250:251], v242 offset:29440\n"
"v_cvt_pk_bf16_f32 v17, v222, v223\n"
"v_mfma_f32_16x16x32_bf16 a[60:63], a[220:223], v[20:23], a[60:63]\n"
"v_exp_f32 v224, v224\n"
"v_mfma_f32_16x16x32_bf16 v[48:51], v[40:43], v[20:23], v[48:51]\n"
"ds_read_b64_tr_b16 a[252:253], v243 offset:25344\n"
"v_add_f32 v52, v52, v220\n"
"v_mfma_f32_16x16x32_bf16 v[228:231], a[128:131], v[112:115], v[36:39]\n"
"v_exp_f32 v225, v225\n"
"v_mfma_f32_16x16x32_bf16 v[228:231], a[132:135], v[116:119], v[228:231]\n"
"ds_read_b64_tr_b16 a[254:255], v243 offset:29440\n"
"v_cvt_pk_bf16_f32 v18, v224, v225\n"
"v_mfma_f32_16x16x32_bf16 v[228:231], a[136:139], v[120:123], v[228:231]\n"
"v_exp_f32 v226, v226\n"
"v_mfma_f32_16x16x32_bf16 v[228:231], a[140:143], v[124:127], v[228:231]\n"
"v_add_f32 v52, v52, v221\n"
"v_add_f32 v52, v52, v222\n"
"v_mfma_f32_16x16x32_bf16 v[232:235], a[144:147], v[112:115], v[36:39]\n"
"v_exp_f32 v227, v227\n"
"v_mfma_f32_16x16x32_bf16 v[232:235], a[148:151], v[116:119], v[232:235]\n"
"v_cvt_pk_bf16_f32 v19, v226, v227\n"
"v_add_f32 v52, v52, v223\n"
"v_mfma_f32_16x16x32_bf16 v[232:235], a[152:155], v[120:123], v[232:235]\n"
"buffer_load_dwordx4 v247, s[12:15], s84 offen offset:3072 lds\n"
"v_mfma_f32_16x16x32_bf16 v[232:235], a[156:159], v[124:127], v[232:235]\n"
"s_add_u32 s84, s84, 16384\n"
"v_exp_f32 v228, v228\n"
"v_mfma_f32_16x16x32_bf16 a[64:67], a[192:195], v[16:19], a[64:67]\n"
"v_add_f32 v52, v52, v224\n"
"v_add_f32 v52, v52, v225\n"
"v_mfma_f32_16x16x32_bf16 a[68:71], a[196:199], v[16:19], a[68:71]\n"
"v_exp_f32 v229, v229\n"
"v_mfma_f32_16x16x32_bf16 a[72:75], a[200:203], v[16:19], a[72:75]\n"
"v_cvt_pk_bf16_f32 v20, v228, v229\n"
"v_add_f32 v52, v52, v226\n"
"v_mfma_f32_16x16x32_bf16 a[76:79], a[204:207], v[16:19], a[76:79]\n"
"s_sub_u32 s87, s87, 1\n"
"v_exp_f32 v230, v230\n"
"v_mfma_f32_16x16x32_bf16 a[80:83], a[208:211], v[16:19], a[80:83]\n"
"s_cmp_lg_u32 s87, 0\n"
"v_add_f32 v52, v52, v227\n"
"v_mfma_f32_16x16x32_bf16 a[84:87], a[212:215], v[16:19], a[84:87]\n"
"v_exp_f32 v231, v231\n"
"v_mfma_f32_16x16x32_bf16 a[88:91], a[216:219], v[16:19], a[88:91]\n"
"s_waitcnt vmcnt(18) lgkmcnt(0)\n"
"s_barrier\n"
"v_cvt_pk_bf16_f32 v21, v230, v231\n"
"v_mfma_f32_16x16x32_bf16 a[92:95], a[220:223], v[16:19], a[92:95]\n"
"v_exp_f32 v232, v232\n"
"s_cbranch_scc1 AT_loop\n"
"s_cmp_lg_u32 s56, 0\n"
"s_cbranch_scc1 AT_h3\n"
"s_mov_b32 s61, 0x80000\n"
"AT_h2:\n"
"v_xor_b32 v210, s85, v210\n"
"v_xor_b32 v211, s85, v211\n"
"v_xor_b32 v212, s85, v212\n"
"v_xor_b32 v213, s85, v213\n"
"v_or3_b32 v210, v210, v211, v212\n"
"v_or_b32 v210, v210, v213\n"
"v_cmp_ne_u32 vcc, 0, v210\n"
"s_sub_u32 s61, s61, 1\n"
"s_cbranch_vccz AT_h4\n"
"s_cmp_eq_u32 s61, 0\n"
"s_cbranch_scc1 AT_h4\n"
"s_sleep 2\n"
"buffer_load_dword v210, v214, s[28:31], s57 offen offset:4 sc1\n"
"buffer_load_dword v211, v214, s[28:31], s58 offen offset:4 sc1\n"
"buffer_load_dword v212, v214, s[28:31], s59 offen offset:4 sc1\n"
"buffer_load_dword v213, v214, s[28:31], s60 offen offset:4 sc1\n"
"s_waitcnt vmcnt(0)\n"
"s_branch AT_h2\n"
"AT_h4:\n"
"v_mov_b32 v210, s85\n"
"v_mov_b32 v211, 65536\n"
"s_mov_b64 exec, 1\n"
"buffer_store_dword v210, v211, s[28:31], 0 offen sc1\n"
"s_mov_b64 exec, -1\n"
"AT_h3:\n"
"v_mfma_f32_16x16x32_bf16 v[0:3], a[160:163], v[64:67], v[24:27]\n"
"ds_read_b128 a[128:131], v240 offset:32768\n"
"buffer_load_dword v250, v255, s[32:35], 0 offen\n"
"v_mfma_f32_16x16x32_bf16 v[0:3], a[164:167], v[68:71], v[0:3]\n"
"v_exp_f32 v233, v233\n"
"v_mfma_f32_16x16x32_bf16 v[0:3], a[168:171], v[72:75], v[0:3]\n"
"ds_read_b128 a[132:135], v240 offset:33024\n"
"v_cvt_pk_bf16_f32 v22, v232, v233\n"
"v_mfma_f32_16x16x32_bf16 v[0:3], a[172:175], v[76:79], v[0:3]\n"
"v_exp_f32 v234, v234\n"
"v_mfma_f32_16x16x32_bf16 v[4:7], a[176:179], v[64:67], v[24:27]\n"
"ds_read_b128 a[136:139], v240 offset:33280\n"
"v_lshlrev_b32 v216, 6, v248\n"
"v_mfma_f32_16x16x32_bf16 v[4:7], a[180:183], v[68:71], v[4:7]\n"
"s_and_b32 s48, s42, 7\n"
"v_exp_f32 v235, v235\n"
"v_mfma_f32_16x16x32_bf16 v[4:7], a[184:187], v[72:75], v[4:7]\n"
"s_mul_i32 s48, s48, 576\n"
"ds_read_b128 a[140:143], v240 offset:33536\n"
"v_cvt_pk_bf16_f32 v23, v234, v235\n"
"v_mfma_f32_16x16x32_bf16 v[4:7], a[188:191], v[76:79], v[4:7]\n"
"s_mov_b32 m0, s86\n"
"v_exp_f32 v0, v0\n"
"v_mfma_f32_16x16x32_bf16 a[96:99], a[192:195], v[20:23], a[96:99]\n"
"s_lshr_b32 s62, s42, 3\n"
"buffer_load_dwordx4 v246, s[8:11], s84 offen offset:0 lds\n"
"v_mfma_f32_16x16x32_bf16 a[100:103], a[196:199], v[20:23], a[100:103]\n"
"s_mul_i32 s62, s62, 24\n"
"v_exp_f32 v1, v1\n"
"v_mfma_f32_16x16x32_bf16 a[104:107], a[200:203], v[20:23], a[104:107]\n"
"s_add_u32 s48, s48, s62\n"
"ds_read_b128 a[144:147], v240 offset:36864\n"
"v_cvt_pk_bf16_f32 v16, v0, v1\n"
"v_mfma_f32_16x16x32_bf16 a[108:111], a[204:207], v[20:23], a[108:111]\n"
"s_cmp_lt_u32 s42, 192\n"
"s_cselect_b32 s74, s74, 0\n"
"s_cselect_b32 s66, s66, 0\n"
"s_cselect_b32 s70, s70, 0\n"
"v_exp_f32 v2, v2\n"
"v_mfma_f32_16x16x32_bf16 a[112:115], a[208:211], v[20:23], a[112:115]\n"
"s_mul_i32 s50, s48, 1152\n"
"ds_read_b128 a[148:151], v240 offset:37120\n"
"v_add_f32 v44, v44, v0\n"
"v_mfma_f32_16x16x32_bf16 a[116:119], a[212:215], v[20:23], a[116:119]\n"
"s_lshl_b32 s62, s41, 13\n"
"v_exp_f32 v3, v3\n"
"v_mfma_f32_16x16x32_bf16 a[120:123], a[216:219], v[20:23], a[120:123]\n"
"s_add_u32 s46, s50, s62\n"
"ds_read_b128 a[152:155], v240 offset:37376\n"
"v_cvt_pk_bf16_f32 v17, v2, v3\n"
"v_mfma_f32_16x16x32_bf16 a[124:127], a[220:223], v[20:23], a[124:127]\n"
"s_add_u32 s47, s46, 4096\n"
"v_exp_f32 v4, v4\n"
"v_mfma_f32_16x16x32_bf16 v[56:59], v[40:43], v[20:23], v[56:59]\n"
"ds_read_b128 a[156:159], v240 offset:37632\n"
"v_add_f32 v44, v44, v1\n"
"v_mfma_f32_16x16x32_bf16 v[8:11], a[160:163], v[80:83], v[28:31]\n"
"v_exp_f32 v5, v5\n"
"v_mfma_f32_16x16x32_bf16 v[8:11], a[164:167], v[84:87], v[8:11]\n"
"ds_read_b64_tr_b16 a[192:193], v242 offset:49152\n"
"v_cvt_pk_bf16_f32 v18, v4, v5\n"
"v_mfma_f32_16x16x32_bf16 v[8:11], a[168:171], v[88:91], v[8:11]\n"
"v_exp_f32 v6, v6\n"
"v_mfma_f32_16x16x32_bf16 v[8:11], a[172:175], v[92:95], v[8:11]\n"
"ds_read_b64_tr_b16 a[194:195], v242 offset:53248\n"
"v_add_f32 v44, v44, v2\n"
"v_mfma_f32_16x16x32_bf16 v[12:15], a[176:179], v[80:83], v[28:31]\n"
"v_exp_f32 v7, v7\n"
"v_mfma_f32_16x16x32_bf16 v[12:15], a[180:183], v[84:87], v[12:15]\n"
"ds_read_b64_tr_b16 a[196:197], v243 offset:49152\n"
"v_cvt_pk_bf16_f32 v19, v6, v7\n"
"v_mfma_f32_16x16x32_bf16 v[12:15], a[184:187], v[88:91], v[12:15]\n"
"buffer_load_dwordx4 v246, s[8:11], s84 offen offset:1024 lds\n"
"v_mfma_f32_16x16x32_bf16 v[12:15], a[188:191], v[92:95], v[12:15]\n"
"s_waitcnt lgkmcnt(11)\n"
"v_exp_f32 v8, v8\n"
"v_mfma_f32_16x16x32_bf16 a[0:3], a[224:227], v[16:19], a[0:3]\n"
"ds_read_b64_tr_b16 a[198:199], v243 offset:53248\n"
"buffer_load_dword v250, v216, s[72:75], s46 offen\n"
"v_add_f32 v44, v44, v3\n"
"v_mfma_f32_16x16x32_bf16 a[4:7], a[228:231], v[16:19], a[4:7]\n"
"v_exp_f32 v9, v9\n"
"v_mfma_f32_16x16x32_bf16 a[8:11], a[232:235], v[16:19], a[8:11]\n"
"ds_read_b64_tr_b16 a[200:201], v242 offset:49408\n"
"v_cvt_pk_bf16_f32 v20, v8, v9\n"
"v_mfma_f32_16x16x32_bf16 a[12:15], a[236:239], v[16:19], a[12:15]\n"
"v_exp_f32 v10, v10\n"
"v_mfma_f32_16x16x32_bf16 a[16:19], a[240:243], v[16:19], a[16:19]\n"
"ds_read_b64_tr_b16 a[202:203], v242 offset:53504\n"
"v_add_f32 v44, v44, v4\n"
"v_mfma_f32_16x16x32_bf16 a[20:23], a[244:247], v[16:19], a[20:23]\n"
"v_exp_f32 v11, v11\n"
"v_mfma_f32_16x16x32_bf16 a[24:27], a[248:251], v[16:19], a[24:27]\n"
"ds_read_b64_tr_b16 a[204:205], v243 offset:49408\n"
"v_cvt_pk_bf16_f32 v21, v10, v11\n"
"v_mfma_f32_16x16x32_bf16 a[28:31], a[252:255], v[16:19], a[28:31]\n"
"v_exp_f32 v12, v12\n"
"v_mfma_f32_16x16x32_bf16 v[220:223], a[160:163], v[96:99], v[32:35]\n"
"ds_read_b64_tr_b16 a[206:207], v243 offset:53504\n"
"v_add_f32 v44, v44, v5\n"
"v_mfma_f32_16x16x32_bf16 v[220:223], a[164:167], v[100:103], v[220:223]\n"
"v_exp_f32 v13, v13\n"
"v_mfma_f32_16x16x32_bf16 v[220:223], a[168:171], v[104:107], v[220:223]\n"
"ds_read_b64_tr_b16 a[208:209], v242 offset:49664\n"
"v_cvt_pk_bf16_f32 v22, v12, v13\n"
"v_mfma_f32_16x16x32_bf16 v[220:223], a[172:175], v[108:111], v[220:223]\n"
"v_exp_f32 v14, v14\n"
"v_mfma_f32_16x16x32_bf16 v[224:227], a[176:179], v[96:99], v[32:35]\n"
"ds_read_b64_tr_b16 a[210:211], v242 offset:53760\n"
"v_add_f32 v44, v44, v6\n"
"v_mfma_f32_16x16x32_bf16 v[224:227], a[180:183], v[100:103], v[224:227]\n"
"v_exp_f32 v15, v15\n"
"v_mfma_f32_16x16x32_bf16 v[224:227], a[184:187], v[104:107], v[224:227]\n"
"ds_read_b64_tr_b16 a[212:213], v243 offset:49664\n"
"v_cvt_pk_bf16_f32 v23, v14, v15\n"
"v_mfma_f32_16x16x32_bf16 v[224:227], a[188:191], v[108:111], v[224:227]\n"
"v_exp_f32 v220, v220\n"
"v_mfma_f32_16x16x32_bf16 a[32:35], a[224:227], v[20:23], a[32:35]\n"
"buffer_load_dwordx4 v247, s[8:11], s84 offen offset:2048 lds\n"
"v_mfma_f32_16x16x32_bf16 a[36:39], a[228:231], v[20:23], a[36:39]\n"
"v_exp_f32 v221, v221\n"
"v_mfma_f32_16x16x32_bf16 a[40:43], a[232:235], v[20:23], a[40:43]\n"
"ds_read_b64_tr_b16 a[214:215], v243 offset:53760\n"
"v_cvt_pk_bf16_f32 v16, v220, v221\n"
"v_mfma_f32_16x16x32_bf16 a[44:47], a[236:239], v[20:23], a[44:47]\n"
"v_exp_f32 v222, v222\n"
"v_mfma_f32_16x16x32_bf16 a[48:51], a[240:243], v[20:23], a[48:51]\n"
"ds_read_b64_tr_b16 a[216:217], v242 offset:49920\n"
"v_add_f32 v44, v44, v7\n"
"v_mfma_f32_16x16x32_bf16 a[52:55], a[244:247], v[20:23], a[52:55]\n"
"v_exp_f32 v223, v223\n"
"v_mfma_f32_16x16x32_bf16 a[56:59], a[248:251], v[20:23], a[56:59]\n"
"ds_read_b64_tr_b16 a[218:219], v242 offset:54016\n"
"v_cvt_pk_bf16_f32 v17, v222, v223\n"
"v_mfma_f32_16x16x32_bf16 a[60:63], a[252:255], v[20:23], a[60:63]\n"
"v_exp_f32 v224, v224\n"
"v_mfma_f32_16x16x32_bf16 v[48:51], v[40:43], v[20:23], v[48:51]\n"
"ds_read_b64_tr_b16 a[220:221], v243 offset:49920\n"
"buffer_load_dword v250, v216, s[72:75], s47 offen\n"
"v_add_f32 v52, v52, v220\n"
"v_mfma_f32_16x16x32_bf16 v[228:231], a[160:163], v[112:115], v[36:39]\n"
"v_exp_f32 v225, v225\n"
"v_mfma_f32_16x16x32_bf16 v[228:231], a[164:167], v[116:119], v[228:231]\n"
"ds_read_b64_tr_b16 a[222:223], v243 offset:54016\n"
"v_cvt_pk_bf16_f32 v18, v224, v225\n"
"v_mfma_f32_16x16x32_bf16 v[228:231], a[168:171], v[120:123], v[228:231]\n"
"v_exp_f32 v226, v226\n"
"v_mfma_f32_16x16x32_bf16 v[228:231], a[172:175], v[124:127], v[228:231]\n"
"v_add_f32 v52, v52, v221\n"
"v_add_f32 v52, v52, v222\n"
"v_mfma_f32_16x16x32_bf16 v[232:235], a[176:179], v[112:115], v[36:39]\n"
"v_exp_f32 v227, v227\n"
"v_mfma_f32_16x16x32_bf16 v[232:235], a[180:183], v[116:119], v[232:235]\n"
"v_cvt_pk_bf16_f32 v19, v226, v227\n"
"v_add_f32 v52, v52, v223\n"
"v_mfma_f32_16x16x32_bf16 v[232:235], a[184:187], v[120:123], v[232:235]\n"
"buffer_load_dwordx4 v247, s[8:11], s84 offen offset:3072 lds\n"
"v_lshrrev_b32 v251, 2, v248\n"
"v_mfma_f32_16x16x32_bf16 v[232:235], a[188:191], v[124:127], v[232:235]\n"
"v_exp_f32 v228, v228\n"
"v_mfma_f32_16x16x32_bf16 a[64:67], a[224:227], v[16:19], a[64:67]\n"
"v_add_f32 v52, v52, v224\n"
"v_add_f32 v52, v52, v225\n"
"v_mfma_f32_16x16x32_bf16 a[68:71], a[228:231], v[16:19], a[68:71]\n"
"v_exp_f32 v229, v229\n"
"v_mfma_f32_16x16x32_bf16 a[72:75], a[232:235], v[16:19], a[72:75]\n"
"v_cvt_pk_bf16_f32 v20, v228, v229\n"
"v_add_f32 v52, v52, v226\n"
"v_mfma_f32_16x16x32_bf16 a[76:79], a[236:239], v[16:19], a[76:79]\n"
"v_exp_f32 v230, v230\n"
"v_mfma_f32_16x16x32_bf16 a[80:83], a[240:243], v[16:19], a[80:83]\n"
"v_add_f32 v52, v52, v227\n"
"v_mul_u32_u24 v251, 9216, v251\n"
"v_mfma_f32_16x16x32_bf16 a[84:87], a[244:247], v[16:19], a[84:87]\n"
"v_exp_f32 v231, v231\n"
"v_mfma_f32_16x16x32_bf16 a[88:91], a[248:251], v[16:19], a[88:91]\n"
"s_waitcnt lgkmcnt(8)\n"
"v_cvt_pk_bf16_f32 v21, v230, v231\n"
"v_and_b32 v217, 3, v248\n"
"v_mfma_f32_16x16x32_bf16 a[92:95], a[252:255], v[16:19], a[92:95]\n"
"v_exp_f32 v232, v232\n"
"v_mfma_f32_16x16x32_bf16 v[0:3], a[128:131], v[64:67], v[24:27]\n"
"ds_read_b128 a[160:163], v240 offset:40960\n"
"v_add_u32 v255, 1, v217\n"
"v_mfma_f32_16x16x32_bf16 v[0:3], a[132:135], v[68:71], v[0:3]\n"
"v_exp_f32 v233, v233\n"
"v_mfma_f32_16x16x32_bf16 v[0:3], a[136:139], v[72:75], v[0:3]\n"
"ds_read_b128 a[164:167], v240 offset:41216\n"
"v_cvt_pk_bf16_f32 v22, v232, v233\n"
"v_mfma_f32_16x16x32_bf16 v[0:3], a[140:143], v[76:79], v[0:3]\n"
"v_exp_f32 v234, v234\n"
"v_mfma_f32_16x16x32_bf16 v[4:7], a[144:147], v[64:67], v[24:27]\n"
"ds_read_b128 a[168:171], v240 offset:41472\n"
"v_and_b32 v255, 4, v255\n"
"v_mfma_f32_16x16x32_bf16 v[4:7], a[148:151], v[68:71], v[4:7]\n"
"v_exp_f32 v235, v235\n"
"v_mfma_f32_16x16x32_bf16 v[4:7], a[152:155], v[72:75], v[4:7]\n"
"ds_read_b128 a[172:175], v240 offset:41728\n"
"v_cvt_pk_bf16_f32 v23, v234, v235\n"
"v_mfma_f32_16x16x32_bf16 v[4:7], a[156:159], v[76:79], v[4:7]\n"
"s_add_u32 m0, s86, 16384\n"
"v_exp_f32 v0, v0\n"
"v_mfma_f32_16x16x32_bf16 a[96:99], a[224:227], v[20:23], a[96:99]\n"
"buffer_load_dwordx4 v246, s[12:15], s84 offen offset:0 lds\n"
"v_lshlrev_b32 v255, 26, v255\n"
"v_mfma_f32_16x16x32_bf16 a[100:103], a[228:231], v[20:23], a[100:103]\n"
"v_exp_f32 v1, v1\n"
"v_mfma_f32_16x16x32_bf16 a[104:107], a[232:235], v[20:23], a[104:107]\n"
"ds_read_b128 a[176:179], v240 offset:45056\n"
"v_cvt_pk_bf16_f32 v16, v0, v1\n"
"v_mfma_f32_16x16x32_bf16 a[108:111], a[236:239], v[20:23], a[108:111]\n"
"v_exp_f32 v2, v2\n"
"v_mfma_f32_16x16x32_bf16 a[112:115], a[240:243], v[20:23], a[112:115]\n"
"ds_read_b128 a[180:183], v240 offset:45312\n"
"v_add_f32 v44, v44, v0\n"
"v_mfma_f32_16x16x32_bf16 a[116:119], a[244:247], v[20:23], a[116:119]\n"
"v_exp_f32 v3, v3\n"
"v_mfma_f32_16x16x32_bf16 a[120:123], a[248:251], v[20:23], a[120:123]\n"
"ds_read_b128 a[184:187], v240 offset:45568\n"
"v_cvt_pk_bf16_f32 v17, v2, v3\n"
"v_mfma_f32_16x16x32_bf16 a[124:127], a[252:255], v[20:23], a[124:127]\n"
"v_exp_f32 v4, v4\n"
"v_mfma_f32_16x16x32_bf16 v[56:59], v[40:43], v[20:23], v[56:59]\n"
"ds_read_b128 a[188:191], v240 offset:45824\n"
"v_add_f32 v44, v44, v1\n"
"v_mfma_f32_16x16x32_bf16 v[8:11], a[128:131], v[80:83], v[28:31]\n"
"v_exp_f32 v5, v5\n"
"v_mfma_f32_16x16x32_bf16 v[8:11], a[132:135], v[84:87], v[8:11]\n"
"ds_read_b64_tr_b16 a[224:225], v242 offset:57344\n"
"v_cvt_pk_bf16_f32 v18, v4, v5\n"
"v_mfma_f32_16x16x32_bf16 v[8:11], a[136:139], v[88:91], v[8:11]\n"
"v_exp_f32 v6, v6\n"
"v_mfma_f32_16x16x32_bf16 v[8:11], a[140:143], v[92:95], v[8:11]\n"
"ds_read_b64_tr_b16 a[226:227], v242 offset:61440\n"
"v_add_f32 v44, v44, v2\n"
"v_mfma_f32_16x16x32_bf16 v[12:15], a[144:147], v[80:83], v[28:31]\n"
"v_exp_f32 v7, v7\n"
"v_mfma_f32_16x16x32_bf16 v[12:15], a[148:151], v[84:87], v[12:15]\n"
"ds_read_b64_tr_b16 a[228:229], v243 offset:57344\n"
"v_cvt_pk_bf16_f32 v19, v6, v7\n"
"v_mfma_f32_16x16x32_bf16 v[12:15], a[152:155], v[88:91], v[12:15]\n"
"buffer_load_dwordx4 v247, s[12:15], s84 offen offset:1024 lds\n"
"v_lshl_add_u32 v217, v217, 4, v255\n"
"v_mfma_f32_16x16x32_bf16 v[12:15], a[156:159], v[92:95], v[12:15]\n"
"s_waitcnt lgkmcnt(11)\n"
"v_exp_f32 v8, v8\n"
"v_mfma_f32_16x16x32_bf16 a[0:3], a[192:195], v[16:19], a[0:3]\n"
"ds_read_b64_tr_b16 a[230:231], v243 offset:61440\n"
"v_add_f32 v44, v44, v3\n"
"v_mfma_f32_16x16x32_bf16 a[4:7], a[196:199], v[16:19], a[4:7]\n"
"v_exp_f32 v9, v9\n"
"v_mfma_f32_16x16x32_bf16 a[8:11], a[200:203], v[16:19], a[8:11]\n"
"ds_read_b64_tr_b16 a[232:233], v242 offset:57600\n"
"v_cvt_pk_bf16_f32 v20, v8, v9\n"
"v_mfma_f32_16x16x32_bf16 a[12:15], a[204:207], v[16:19], a[12:15]\n"
"v_exp_f32 v10, v10\n"
"v_mfma_f32_16x16x32_bf16 a[16:19], a[208:211], v[16:19], a[16:19]\n"
"ds_read_b64_tr_b16 a[234:235], v242 offset:61696\n"
"v_add_f32 v44, v44, v4\n"
"v_mfma_f32_16x16x32_bf16 a[20:23], a[212:215], v[16:19], a[20:23]\n"
"v_exp_f32 v11, v11\n"
"v_mfma_f32_16x16x32_bf16 a[24:27], a[216:219], v[16:19], a[24:27]\n"
"ds_read_b64_tr_b16 a[236:237], v243 offset:57600\n"
"v_cvt_pk_bf16_f32 v21, v10, v11\n"
"v_mfma_f32_16x16x32_bf16 a[28:31], a[220:223], v[16:19], a[28:31]\n"
"v_exp_f32 v12, v12\n"
"v_mfma_f32_16x16x32_bf16 v[220:223], a[128:131], v[96:99], v[32:35]\n"
"ds_read_b64_tr_b16 a[238:239], v243 offset:61696\n"
"v_add_f32 v44, v44, v5\n"
"v_mfma_f32_16x16x32_bf16 v[220:223], a[132:135], v[100:103], v[220:223]\n"
"v_exp_f32 v13, v13\n"
"v_mfma_f32_16x16x32_bf16 v[220:223], a[136:139], v[104:107], v[220:223]\n"
"ds_read_b64_tr_b16 a[240:241], v242 offset:57856\n"
"v_cvt_pk_bf16_f32 v22, v12, v13\n"
"v_mfma_f32_16x16x32_bf16 v[220:223], a[140:143], v[108:111], v[220:223]\n"
"v_exp_f32 v14, v14\n"
"v_mfma_f32_16x16x32_bf16 v[224:227], a[144:147], v[96:99], v[32:35]\n"
"ds_read_b64_tr_b16 a[242:243], v242 offset:61952\n"
"v_add_f32 v44, v44, v6\n"
"v_mfma_f32_16x16x32_bf16 v[224:227], a[148:151], v[100:103], v[224:227]\n"
"v_exp_f32 v15, v15\n"
"v_mfma_f32_16x16x32_bf16 v[224:227], a[152:155], v[104:107], v[224:227]\n"
"ds_read_b64_tr_b16 a[244:245], v243 offset:57856\n"
"v_cvt_pk_bf16_f32 v23, v14, v15\n"
"v_mfma_f32_16x16x32_bf16 v[224:227], a[156:159], v[108:111], v[224:227]\n"
"v_exp_f32 v220, v220\n"
"v_mfma_f32_16x16x32_bf16 a[32:35], a[192:195], v[20:23], a[32:35]\n"
"buffer_load_dwordx4 v246, s[12:15], s84 offen offset:2048 lds\n"
"v_lshl_add_u32 v252, v251, 1, v217\n"
"v_mfma_f32_16x16x32_bf16 a[36:39], a[196:199], v[20:23], a[36:39]\n"
"v_exp_f32 v221, v221\n"
"v_mfma_f32_16x16x32_bf16 a[40:43], a[200:203], v[20:23], a[40:43]\n"
"ds_read_b64_tr_b16 a[246:247], v243 offset:61952\n"
"v_cvt_pk_bf16_f32 v16, v220, v221\n"
"v_mfma_f32_16x16x32_bf16 a[44:47], a[204:207], v[20:23], a[44:47]\n"
"v_exp_f32 v222, v222\n"
"v_mfma_f32_16x16x32_bf16 a[48:51], a[208:211], v[20:23], a[48:51]\n"
"ds_read_b64_tr_b16 a[248:249], v242 offset:58112\n"
"v_add_f32 v44, v44, v7\n"
"v_mfma_f32_16x16x32_bf16 a[52:55], a[212:215], v[20:23], a[52:55]\n"
"v_exp_f32 v223, v223\n"
"v_mfma_f32_16x16x32_bf16 a[56:59], a[216:219], v[20:23], a[56:59]\n"
"ds_read_b64_tr_b16 a[250:251], v242 offset:62208\n"
"v_cvt_pk_bf16_f32 v17, v222, v223\n"
"v_mfma_f32_16x16x32_bf16 a[60:63], a[220:223], v[20:23], a[60:63]\n"
"v_exp_f32 v224, v224\n"
"v_mfma_f32_16x16x32_bf16 v[48:51], v[40:43], v[20:23], v[48:51]\n"
"ds_read_b64_tr_b16 a[252:253], v243 offset:58112\n"
"v_add_f32 v52, v52, v220\n"
"v_mfma_f32_16x16x32_bf16 v[228:231], a[128:131], v[112:115], v[36:39]\n"
"v_exp_f32 v225, v225\n"
"v_mfma_f32_16x16x32_bf16 v[228:231], a[132:135], v[116:119], v[228:231]\n"
"ds_read_b64_tr_b16 a[254:255], v243 offset:62208\n"
"v_cvt_pk_bf16_f32 v18, v224, v225\n"
"v_mfma_f32_16x16x32_bf16 v[228:231], a[136:139], v[120:123], v[228:231]\n"
"v_exp_f32 v226, v226\n"
"v_mfma_f32_16x16x32_bf16 v[228:231], a[140:143], v[124:127], v[228:231]\n"
"v_add_f32 v52, v52, v221\n"
"v_add_f32 v52, v52, v222\n"
"v_mfma_f32_16x16x32_bf16 v[232:235], a[144:147], v[112:115], v[36:39]\n"
"v_exp_f32 v227, v227\n"
"v_mfma_f32_16x16x32_bf16 v[232:235], a[148:151], v[116:119], v[232:235]\n"
"v_cvt_pk_bf16_f32 v19, v226, v227\n"
"v_add_f32 v52, v52, v223\n"
"v_mfma_f32_16x16x32_bf16 v[232:235], a[152:155], v[120:123], v[232:235]\n"
"buffer_load_dwordx4 v247, s[12:15], s84 offen offset:3072 lds\n"
"v_add_u32 v251, v251, v217\n"
"v_mfma_f32_16x16x32_bf16 v[232:235], a[156:159], v[124:127], v[232:235]\n"
"s_add_u32 s84, s84, 16384\n"
"v_exp_f32 v228, v228\n"
"v_mfma_f32_16x16x32_bf16 a[64:67], a[192:195], v[16:19], a[64:67]\n"
"s_mul_i32 s62, s41, 1327104\n"
"v_add_f32 v52, v52, v224\n"
"v_add_f32 v52, v52, v225\n"
"v_mfma_f32_16x16x32_bf16 a[68:71], a[196:199], v[16:19], a[68:71]\n"
"s_lshl_b32 s49, s48, 1\n"
"v_exp_f32 v229, v229\n"
"v_mfma_f32_16x16x32_bf16 a[72:75], a[200:203], v[16:19], a[72:75]\n"
"s_add_u32 s49, s49, s62\n"
"v_cvt_pk_bf16_f32 v20, v228, v229\n"
"v_add_f32 v52, v52, v226\n"
"v_mfma_f32_16x16x32_bf16 a[76:79], a[204:207], v[16:19], a[76:79]\n"
"v_exp_f32 v230, v230\n"
"v_mfma_f32_16x16x32_bf16 a[80:83], a[208:211], v[16:19], a[80:83]\n"
"buffer_load_dwordx4 v[128:131], v252, s[64:67], s49 offen\n"
"v_add_f32 v52, v52, v227\n"
"v_mfma_f32_16x16x32_bf16 a[84:87], a[212:215], v[16:19], a[84:87]\n"
"v_exp_f32 v231, v231\n"
"v_mfma_f32_16x16x32_bf16 a[88:91], a[216:219], v[16:19], a[88:91]\n"
"s_waitcnt vmcnt(20) lgkmcnt(0)\n"
"s_barrier\n"
"v_cvt_pk_bf16_f32 v21, v230, v231\n"
"v_bfe_u32 v217, v248, 1, 2\n"
"v_mfma_f32_16x16x32_bf16 a[92:95], a[220:223], v[16:19], a[92:95]\n"
"v_exp_f32 v232, v232\n"
"v_mfma_f32_16x16x32_bf16 v[0:3], a[160:163], v[64:67], v[24:27]\n"
"ds_read_b128 a[128:131], v241 offset:0\n"
"v_mul_u32_u24 v210, 1152, v217\n"
"v_mfma_f32_16x16x32_bf16 v[0:3], a[164:167], v[68:71], v[0:3]\n"
"v_exp_f32 v233, v233\n"
"v_mfma_f32_16x16x32_bf16 v[0:3], a[168:171], v[72:75], v[0:3]\n"
"ds_read_b128 a[132:135], v241 offset:256\n"
"v_cvt_pk_bf16_f32 v22, v232, v233\n"
"v_mfma_f32_16x16x32_bf16 v[0:3], a[172:175], v[76:79], v[0:3]\n"
"v_exp_f32 v234, v234\n"
"v_mfma_f32_16x16x32_bf16 v[4:7], a[176:179], v[64:67], v[24:27]\n"
"ds_read_b128 a[136:139], v241 offset:512\n"
"v_and_b32 v217, 1, v248\n"
"v_mfma_f32_16x16x32_bf16 v[4:7], a[180:183], v[68:71], v[4:7]\n"
"v_exp_f32 v235, v235\n"
"v_mfma_f32_16x16x32_bf16 v[4:7], a[184:187], v[72:75], v[4:7]\n"
"ds_read_b128 a[140:143], v241 offset:768\n"
"v_cvt_pk_bf16_f32 v23, v234, v235\n"
"v_mfma_f32_16x16x32_bf16 v[4:7], a[188:191], v[76:79], v[4:7]\n"
"s_add_u32 m0, s86, 32768\n"
"v_exp_f32 v0, v0\n"
"v_mfma_f32_16x16x32_bf16 a[96:99], a[192:195], v[20:23], a[96:99]\n"
"buffer_load_dwordx4 v246, s[8:11], s84 offen offset:0 lds\n"
"v_lshl_add_u32 v210, v217, 4, v210\n"
"v_mfma_f32_16x16x32_bf16 a[100:103], a[196:199], v[20:23], a[100:103]\n"
"v_exp_f32 v1, v1\n"
"v_mfma_f32_16x16x32_bf16 a[104:107], a[200:203], v[20:23], a[104:107]\n"
"ds_read_b128 a[144:147], v241 offset:4096\n"
"v_cvt_pk_bf16_f32 v16, v0, v1\n"
"v_mfma_f32_16x16x32_bf16 a[108:111], a[204:207], v[20:23], a[108:111]\n"
"v_exp_f32 v2, v2\n"
"v_mfma_f32_16x16x32_bf16 a[112:115], a[208:211], v[20:23], a[112:115]\n"
"ds_read_b128 a[148:151], v241 offset:4352\n"
"v_add_f32 v44, v44, v0\n"
"v_mfma_f32_16x16x32_bf16 a[116:119], a[212:215], v[20:23], a[116:119]\n"
"v_exp_f32 v3, v3\n"
"v_mfma_f32_16x16x32_bf16 a[120:123], a[216:219], v[20:23], a[120:123]\n"
"ds_read_b128 a[152:155], v241 offset:4608\n"
"v_cvt_pk_bf16_f32 v17, v2, v3\n"
"v_mfma_f32_16x16x32_bf16 a[124:127], a[220:223], v[20:23], a[124:127]\n"
"v_exp_f32 v4, v4\n"
"v_mfma_f32_16x16x32_bf16 v[56:59], v[40:43], v[20:23], v[56:59]\n"
"ds_read_b128 a[156:159], v241 offset:4864\n"
"v_add_f32 v44, v44, v1\n"
"v_mfma_f32_16x16x32_bf16 v[8:11], a[160:163], v[80:83], v[28:31]\n"
"v_exp_f32 v5, v5\n"
"v_mfma_f32_16x16x32_bf16 v[8:11], a[164:167], v[84:87], v[8:11]\n"
"ds_read_b64_tr_b16 a[192:193], v244 offset:16384\n"
"v_cvt_pk_bf16_f32 v18, v4, v5\n"
"v_mfma_f32_16x16x32_bf16 v[8:11], a[168:171], v[88:91], v[8:11]\n"
"v_exp_f32 v6, v6\n"
"v_mfma_f32_16x16x32_bf16 v[8:11], a[172:175], v[92:95], v[8:11]\n"
"ds_read_b64_tr_b16 a[194:195], v244 offset:20480\n"
"v_add_f32 v44, v44, v2\n"
"v_mfma_f32_16x16x32_bf16 v[12:15], a[176:179], v[80:83], v[28:31]\n"
"v_exp_f32 v7, v7\n"
"v_mfma_f32_16x16x32_bf16 v[12:15], a[180:183], v[84:87], v[12:15]\n"
"ds_read_b64_tr_b16 a[196:197], v245 offset:16384\n"
"v_cvt_pk_bf16_f32 v19, v6, v7\n"
"v_mfma_f32_16x16x32_bf16 v[12:15], a[184:187], v[88:91], v[12:15]\n"
"buffer_load_dwordx4 v246, s[8:11], s84 offen offset:1024 lds\n"
"v_lshrrev_b32 v217, 3, v248\n"
"v_mfma_f32_16x16x32_bf16 v[12:15], a[188:191], v[92:95], v[12:15]\n"
"s_waitcnt lgkmcnt(11)\n"
"v_exp_f32 v8, v8\n"
"v_mfma_f32_16x16x32_bf16 a[0:3], a[224:227], v[16:19], a[0:3]\n"
"ds_read_b64_tr_b16 a[198:199], v245 offset:20480\n"
"v_add_f32 v44, v44, v3\n"
"v_mfma_f32_16x16x32_bf16 a[4:7], a[228:231], v[16:19], a[4:7]\n"
"v_exp_f32 v9, v9\n"
"v_mfma_f32_16x16x32_bf16 a[8:11], a[232:235], v[16:19], a[8:11]\n"
"ds_read_b64_tr_b16 a[200:201], v244 offset:16640\n"
"v_cvt_pk_bf16_f32 v20, v8, v9\n"
"v_mfma_f32_16x16x32_bf16 a[12:15], a[236:239], v[16:19], a[12:15]\n"
"v_exp_f32 v10, v10\n"
"v_mfma_f32_16x16x32_bf16 a[16:19], a[240:243], v[16:19], a[16:19]\n"
"ds_read_b64_tr_b16 a[202:203], v244 offset:20736\n"
"v_add_f32 v44, v44, v4\n"
"v_mfma_f32_16x16x32_bf16 a[20:23], a[244:247], v[16:19], a[20:23]\n"
"v_exp_f32 v11, v11\n"
"v_mfma_f32_16x16x32_bf16 a[24:27], a[248:251], v[16:19], a[24:27]\n"
"ds_read_b64_tr_b16 a[204:205], v245 offset:16640\n"
"v_cvt_pk_bf16_f32 v21, v10, v11\n"
"v_mfma_f32_16x16x32_bf16 a[28:31], a[252:255], v[16:19], a[28:31]\n"
"v_exp_f32 v12, v12\n"
"v_mfma_f32_16x16x32_bf16 v[220:223], a[160:163], v[96:99], v[32:35]\n"
"ds_read_b64_tr_b16 a[206:207], v245 offset:20736\n"
"v_add_f32 v44, v44, v5\n"
"v_mfma_f32_16x16x32_bf16 v[220:223], a[164:167], v[100:103], v[220:223]\n"
"v_exp_f32 v13, v13\n"
"v_mfma_f32_16x16x32_bf16 v[220:223], a[168:171], v[104:107], v[220:223]\n"
"ds_read_b64_tr_b16 a[208:209], v244 offset:16896\n"
"v_cvt_pk_bf16_f32 v22, v12, v13\n"
"v_mfma_f32_16x16x32_bf16 v[220:223], a[172:175], v[108:111], v[220:223]\n"
"v_exp_f32 v14, v14\n"
"v_mfma_f32_16x16x32_bf16 v[224:227], a[176:179], v[96:99], v[32:35]\n"
"ds_read_b64_tr_b16 a[210:211], v244 offset:20992\n"
"v_add_f32 v44, v44, v6\n"
"v_mfma_f32_16x16x32_bf16 v[224:227], a[180:183], v[100:103], v[224:227]\n"
"v_exp_f32 v15, v15\n"
"v_mfma_f32_16x16x32_bf16 v[224:227], a[184:187], v[104:107], v[224:227]\n"
"ds_read_b64_tr_b16 a[212:213], v245 offset:16896\n"
"v_cvt_pk_bf16_f32 v23, v14, v15\n"
"v_mfma_f32_16x16x32_bf16 v[224:227], a[188:191], v[108:111], v[224:227]\n"
"v_exp_f32 v220, v220\n"
"v_mfma_f32_16x16x32_bf16 a[32:35], a[224:227], v[20:23], a[32:35]\n"
"buffer_load_dwordx4 v247, s[8:11], s84 offen offset:2048 lds\n"
"v_lshl_add_u32 v210, v217, 5, v210\n"
"v_mfma_f32_16x16x32_bf16 a[36:39], a[228:231], v[20:23], a[36:39]\n"
"v_exp_f32 v221, v221\n"
"v_mfma_f32_16x16x32_bf16 a[40:43], a[232:235], v[20:23], a[40:43]\n"
"ds_read_b64_tr_b16 a[214:215], v245 offset:20992\n"
"v_cvt_pk_bf16_f32 v16, v220, v221\n"
"v_mfma_f32_16x16x32_bf16 a[44:47], a[236:239], v[20:23], a[44:47]\n"
"v_exp_f32 v222, v222\n"
"v_mfma_f32_16x16x32_bf16 a[48:51], a[240:243], v[20:23], a[48:51]\n"
"ds_read_b64_tr_b16 a[216:217], v244 offset:17152\n"
"v_add_f32 v44, v44, v7\n"
"v_mfma_f32_16x16x32_bf16 a[52:55], a[244:247], v[20:23], a[52:55]\n"
"v_exp_f32 v223, v223\n"
"v_mfma_f32_16x16x32_bf16 a[56:59], a[248:251], v[20:23], a[56:59]\n"
"ds_read_b64_tr_b16 a[218:219], v244 offset:21248\n"
"v_cvt_pk_bf16_f32 v17, v222, v223\n"
"v_mfma_f32_16x16x32_bf16 a[60:63], a[252:255], v[20:23], a[60:63]\n"
"v_exp_f32 v224, v224\n"
"v_mfma_f32_16x16x32_bf16 v[48:51], v[40:43], v[20:23], v[48:51]\n"
"ds_read_b64_tr_b16 a[220:221], v245 offset:17152\n"
"v_add_f32 v52, v52, v220\n"
"v_mfma_f32_16x16x32_bf16 v[228:231], a[160:163], v[112:115], v[36:39]\n"
"v_exp_f32 v225, v225\n"
"v_mfma_f32_16x16x32_bf16 v[228:231], a[164:167], v[116:119], v[228:231]\n"
"ds_read_b64_tr_b16 a[222:223], v245 offset:21248\n"
"v_cvt_pk_bf16_f32 v18, v224, v225\n"
"v_mfma_f32_16x16x32_bf16 v[228:231], a[168:171], v[120:123], v[228:231]\n"
"v_exp_f32 v226, v226\n"
"v_mfma_f32_16x16x32_bf16 v[228:231], a[172:175], v[124:127], v[228:231]\n"
"v_add_f32 v52, v52, v221\n"
"v_add_f32 v52, v52, v222\n"
"v_mfma_f32_16x16x32_bf16 v[232:235], a[176:179], v[112:115], v[36:39]\n"
"v_exp_f32 v227, v227\n"
"v_mfma_f32_16x16x32_bf16 v[232:235], a[180:183], v[116:119], v[232:235]\n"
"v_cvt_pk_bf16_f32 v19, v226, v227\n"
"v_add_f32 v52, v52, v223\n"
"v_mfma_f32_16x16x32_bf16 v[232:235], a[184:187], v[120:123], v[232:235]\n"
"buffer_load_dwordx4 v247, s[8:11], s84 offen offset:3072 lds\n"
"v_mov_b32 v249, 4576\n"
"v_mfma_f32_16x16x32_bf16 v[232:235], a[188:191], v[124:127], v[232:235]\n"
"v_exp_f32 v228, v228\n"
"v_mfma_f32_16x16x32_bf16 a[64:67], a[224:227], v[16:19], a[64:67]\n"
"v_add_f32 v52, v52, v224\n"
"v_add_f32 v52, v52, v225\n"
"v_mfma_f32_16x16x32_bf16 a[68:71], a[228:231], v[16:19], a[68:71]\n"
"v_exp_f32 v229, v229\n"
"v_mfma_f32_16x16x32_bf16 a[72:75], a[232:235], v[16:19], a[72:75]\n"
"v_cvt_pk_bf16_f32 v20, v228, v229\n"
"v_add_f32 v52, v52, v226\n"
"v_mfma_f32_16x16x32_bf16 a[76:79], a[236:239], v[16:19], a[76:79]\n"
"v_exp_f32 v230, v230\n"
"v_mfma_f32_16x16x32_bf16 a[80:83], a[240:243], v[16:19], a[80:83]\n"
"v_add_f32 v52, v52, v227\n"
"v_mov_b32 v206, 256\n"
"v_mfma_f32_16x16x32_bf16 a[84:87], a[244:247], v[16:19], a[84:87]\n"
"s_mov_b32 vcc_lo, 255\n"
"s_mov_b32 vcc_hi, 0\n"
"v_exp_f32 v231, v231\n"
"v_mfma_f32_16x16x32_bf16 a[88:91], a[248:251], v[16:19], a[88:91]\n"
"s_waitcnt lgkmcnt(8)\n"
"v_cvt_pk_bf16_f32 v21, v230, v231\n"
"v_cndmask_b32 v217, v249, v206, vcc\n"
"v_mfma_f32_16x16x32_bf16 a[92:95], a[252:255], v[16:19], a[92:95]\n"
"v_exp_f32 v232, v232\n"
"v_mfma_f32_16x16x32_bf16 v[0:3], a[128:131], v[64:67], v[24:27]\n"
"ds_read_b128 a[160:163], v241 offset:8192\n"
"v_add_u32 v211, v210, v217\n"
"v_mfma_f32_16x16x32_bf16 v[0:3], a[132:135], v[68:71], v[0:3]\n"
"s_mov_b32 vcc_lo, 65280\n"
"s_mov_b32 vcc_hi, 0\n"
"v_exp_f32 v233, v233\n"
"v_mfma_f32_16x16x32_bf16 v[0:3], a[136:139], v[72:75], v[0:3]\n"
"ds_read_b128 a[164:167], v241 offset:8448\n"
"v_cvt_pk_bf16_f32 v22, v232, v233\n"
"v_mfma_f32_16x16x32_bf16 v[0:3], a[140:143], v[76:79], v[0:3]\n"
"v_exp_f32 v234, v234\n"
"v_mfma_f32_16x16x32_bf16 v[4:7], a[144:147], v[64:67], v[24:27]\n"
"ds_read_b128 a[168:171], v241 offset:8704\n"
"v_cndmask_b32 v217, v249, v206, vcc\n"
"v_mfma_f32_16x16x32_bf16 v[4:7], a[148:151], v[68:71], v[4:7]\n"
"v_exp_f32 v235, v235\n"
"v_mfma_f32_16x16x32_bf16 v[4:7], a[152:155], v[72:75], v[4:7]\n"
"ds_read_b128 a[172:175], v241 offset:8960\n"
"v_cvt_pk_bf16_f32 v23, v234, v235\n"
"v_mfma_f32_16x16x32_bf16 v[4:7], a[156:159], v[76:79], v[4:7]\n"
"s_add_u32 m0, s86, 49152\n"
"v_exp_f32 v0, v0\n"
"v_mfma_f32_16x16x32_bf16 a[96:99], a[224:227], v[20:23], a[96:99]\n"
"buffer_load_dwordx4 v246, s[12:15], s84 offen offset:0 lds\n"
"v_add_u32 v212, v211, v217\n"
"v_mfma_f32_16x16x32_bf16 a[100:103], a[228:231], v[20:23], a[100:103]\n"
"s_mov_b32 vcc_lo, 16711680\n"
"s_mov_b32 vcc_hi, 0\n"
"v_exp_f32 v1, v1\n"
"v_mfma_f32_16x16x32_bf16 a[104:107], a[232:235], v[20:23], a[104:107]\n"
"ds_read_b128 a[176:179], v241 offset:12288\n"
"v_cvt_pk_bf16_f32 v16, v0, v1\n"
"v_mfma_f32_16x16x32_bf16 a[108:111], a[236:239], v[20:23], a[108:111]\n"
"v_exp_f32 v2, v2\n"
"v_mfma_f32_16x16x32_bf16 a[112:115], a[240:243], v[20:23], a[112:115]\n"
"ds_read_b128 a[180:183], v241 offset:12544\n"
"v_add_f32 v44, v44, v0\n"
"v_mfma_f32_16x16x32_bf16 a[116:119], a[244:247], v[20:23], a[116:119]\n"
"v_exp_f32 v3, v3\n"
"v_mfma_f32_16x16x32_bf16 a[120:123], a[248:251], v[20:23], a[120:123]\n"
"ds_read_b128 a[184:187], v241 offset:12800\n"
"v_cvt_pk_bf16_f32 v17, v2, v3\n"
"v_mfma_f32_16x16x32_bf16 a[124:127], a[252:255], v[20:23], a[124:127]\n"
"v_exp_f32 v4, v4\n"
"v_mfma_f32_16x16x32_bf16 v[56:59], v[40:43], v[20:23], v[56:59]\n"
"ds_read_b128 a[188:191], v241 offset:13056\n"
"v_add_f32 v44, v44, v1\n"
"v_mfma_f32_16x16x32_bf16 v[8:11], a[128:131], v[80:83], v[28:31]\n"
"v_exp_f32 v5, v5\n"
"v_mfma_f32_16x16x32_bf16 v[8:11], a[132:135], v[84:87], v[8:11]\n"
"ds_read_b64_tr_b16 a[224:225], v244 offset:24576\n"
"v_cvt_pk_bf16_f32 v18, v4, v5\n"
"v_mfma_f32_16x16x32_bf16 v[8:11], a[136:139], v[88:91], v[8:11]\n"
"v_exp_f32 v6, v6\n"
"v_mfma_f32_16x16x32_bf16 v[8:11], a[140:143], v[92:95], v[8:11]\n"
"ds_read_b64_tr_b16 a[226:227], v244 offset:28672\n"
"v_add_f32 v44, v44, v2\n"
"v_mfma_f32_16x16x32_bf16 v[12:15], a[144:147], v[80:83], v[28:31]\n"
"v_exp_f32 v7, v7\n"
"v_mfma_f32_16x16x32_bf16 v[12:15], a[148:151], v[84:87], v[12:15]\n"
"ds_read_b64_tr_b16 a[228:229], v245 offset:24576\n"
"v_cvt_pk_bf16_f32 v19, v6, v7\n"
"v_mfma_f32_16x16x32_bf16 v[12:15], a[152:155], v[88:91], v[12:15]\n"
"buffer_load_dwordx4 v247, s[12:15], s84 offen offset:1024 lds\n"
"v_cndmask_b32 v217, v249, v206, vcc\n"
"v_mfma_f32_16x16x32_bf16 v[12:15], a[156:159], v[92:95], v[12:15]\n"
"s_waitcnt lgkmcnt(11)\n"
"v_exp_f32 v8, v8\n"
"v_mfma_f32_16x16x32_bf16 a[0:3], a[192:195], v[16:19], a[0:3]\n"
"ds_read_b64_tr_b16 a[230:231], v245 offset:28672\n"
"buffer_load_dwordx4 v[164:167], v252, s[68:71], s49 offen\n"
"v_add_f32 v44, v44, v3\n"
"v_mfma_f32_16x16x32_bf16 a[4:7], a[196:199], v[16:19], a[4:7]\n"
"v_exp_f32 v9, v9\n"
"v_mfma_f32_16x16x32_bf16 a[8:11], a[200:203], v[16:19], a[8:11]\n"
"ds_read_b64_tr_b16 a[232:233], v244 offset:24832\n"
"v_cvt_pk_bf16_f32 v20, v8, v9\n"
"v_mfma_f32_16x16x32_bf16 a[12:15], a[204:207], v[16:19], a[12:15]\n"
"v_exp_f32 v10, v10\n"
"v_mfma_f32_16x16x32_bf16 a[16:19], a[208:211], v[16:19], a[16:19]\n"
"ds_read_b64_tr_b16 a[234:235], v244 offset:28928\n"
"v_add_f32 v44, v44, v4\n"
"v_mfma_f32_16x16x32_bf16 a[20:23], a[212:215], v[16:19], a[20:23]\n"
"v_exp_f32 v11, v11\n"
"v_mfma_f32_16x16x32_bf16 a[24:27], a[216:219], v[16:19], a[24:27]\n"
"ds_read_b64_tr_b16 a[236:237], v245 offset:24832\n"
"v_cvt_pk_bf16_f32 v21, v10, v11\n"
"v_mfma_f32_16x16x32_bf16 a[28:31], a[220:223], v[16:19], a[28:31]\n"
"v_exp_f32 v12, v12\n"
"v_mfma_f32_16x16x32_bf16 v[220:223], a[128:131], v[96:99], v[32:35]\n"
"ds_read_b64_tr_b16 a[238:239], v245 offset:28928\n"
"v_add_f32 v44, v44, v5\n"
"v_mfma_f32_16x16x32_bf16 v[220:223], a[132:135], v[100:103], v[220:223]\n"
"v_exp_f32 v13, v13\n"
"v_mfma_f32_16x16x32_bf16 v[220:223], a[136:139], v[104:107], v[220:223]\n"
"ds_read_b64_tr_b16 a[240:241], v244 offset:25088\n"
"v_cvt_pk_bf16_f32 v22, v12, v13\n"
"v_mfma_f32_16x16x32_bf16 v[220:223], a[140:143], v[108:111], v[220:223]\n"
"v_exp_f32 v14, v14\n"
"v_mfma_f32_16x16x32_bf16 v[224:227], a[144:147], v[96:99], v[32:35]\n"
"ds_read_b64_tr_b16 a[242:243], v244 offset:29184\n"
"v_add_f32 v44, v44, v6\n"
"v_mfma_f32_16x16x32_bf16 v[224:227], a[148:151], v[100:103], v[224:227]\n"
"v_exp_f32 v15, v15\n"
"v_mfma_f32_16x16x32_bf16 v[224:227], a[152:155], v[104:107], v[224:227]\n"
"ds_read_b64_tr_b16 a[244:245], v245 offset:25088\n"
"v_cvt_pk_bf16_f32 v23, v14, v15\n"
"v_mfma_f32_16x16x32_bf16 v[224:227], a[156:159], v[108:111], v[224:227]\n"
"v_exp_f32 v220, v220\n"
"v_mfma_f32_16x16x32_bf16 a[32:35], a[192:195], v[20:23], a[32:35]\n"
"buffer_load_dwordx4 v246, s[12:15], s84 offen offset:2048 lds\n"
"v_add_u32 v213, v212, v217\n"
"v_mfma_f32_16x16x32_bf16 a[36:39], a[196:199], v[20:23], a[36:39]\n"
"s_mov_b32 vcc_lo, 4278190080\n"
"s_mov_b32 vcc_hi, 0\n"
"v_exp_f32 v221, v221\n"
"v_mfma_f32_16x16x32_bf16 a[40:43], a[200:203], v[20:23], a[40:43]\n"
"ds_read_b64_tr_b16 a[246:247], v245 offset:29184\n"
"v_cvt_pk_bf16_f32 v16, v220, v221\n"
"v_mfma_f32_16x16x32_bf16 a[44:47], a[204:207], v[20:23], a[44:47]\n"
"v_exp_f32 v222, v222\n"
"v_mfma_f32_16x16x32_bf16 a[48:51], a[208:211], v[20:23], a[48:51]\n"
"ds_read_b64_tr_b16 a[248:249], v244 offset:25344\n"
"v_add_f32 v44, v44, v7\n"
"v_mfma_f32_16x16x32_bf16 a[52:55], a[212:215], v[20:23], a[52:55]\n"
"v_exp_f32 v223, v223\n"
"v_mfma_f32_16x16x32_bf16 a[56:59], a[216:219], v[20:23], a[56:59]\n"
"ds_read_b64_tr_b16 a[250:251], v244 offset:29440\n"
"v_cvt_pk_bf16_f32 v17, v222, v223\n"
"v_mfma_f32_16x16x32_bf16 a[60:63], a[220:223], v[20:23], a[60:63]\n"
"v_exp_f32 v224, v224\n"
"v_mfma_f32_16x16x32_bf16 v[48:51], v[40:43], v[20:23], v[48:51]\n"
"ds_read_b64_tr_b16 a[252:253], v245 offset:25344\n"
"v_add_f32 v52, v52, v220\n"
"v_mfma_f32_16x16x32_bf16 v[228:231], a[128:131], v[112:115], v[36:39]\n"
"v_exp_f32 v225, v225\n"
"v_mfma_f32_16x16x32_bf16 v[228:231], a[132:135], v[116:119], v[228:231]\n"
"ds_read_b64_tr_b16 a[254:255], v245 offset:29440\n"
"v_cvt_pk_bf16_f32 v18, v224, v225\n"
"v_mfma_f32_16x16x32_bf16 v[228:231], a[136:139], v[120:123], v[228:231]\n"
"v_exp_f32 v226, v226\n"
"v_mfma_f32_16x16x32_bf16 v[228:231], a[140:143], v[124:127], v[228:231]\n"
"v_add_f32 v52, v52, v221\n"
"v_add_f32 v52, v52, v222\n"
"v_mfma_f32_16x16x32_bf16 v[232:235], a[144:147], v[112:115], v[36:39]\n"
"v_exp_f32 v227, v227\n"
"v_mfma_f32_16x16x32_bf16 v[232:235], a[148:151], v[116:119], v[232:235]\n"
"v_cvt_pk_bf16_f32 v19, v226, v227\n"
"v_add_f32 v52, v52, v223\n"
"v_mfma_f32_16x16x32_bf16 v[232:235], a[152:155], v[120:123], v[232:235]\n"
"buffer_load_dwordx4 v247, s[12:15], s84 offen offset:3072 lds\n"
"v_cndmask_b32 v217, v249, v206, vcc\n"
"v_mfma_f32_16x16x32_bf16 v[232:235], a[156:159], v[124:127], v[232:235]\n"
"s_add_u32 s84, s84, 16384\n"
"v_exp_f32 v228, v228\n"
"v_mfma_f32_16x16x32_bf16 a[64:67], a[192:195], v[16:19], a[64:67]\n"
"v_add_f32 v52, v52, v224\n"
"v_add_f32 v52, v52, v225\n"
"v_mfma_f32_16x16x32_bf16 a[68:71], a[196:199], v[16:19], a[68:71]\n"
"v_exp_f32 v229, v229\n"
"v_mfma_f32_16x16x32_bf16 a[72:75], a[200:203], v[16:19], a[72:75]\n"
"v_cvt_pk_bf16_f32 v20, v228, v229\n"
"v_add_f32 v52, v52, v226\n"
"v_mfma_f32_16x16x32_bf16 a[76:79], a[204:207], v[16:19], a[76:79]\n"
"v_exp_f32 v230, v230\n"
"v_mfma_f32_16x16x32_bf16 a[80:83], a[208:211], v[16:19], a[80:83]\n"
"v_add_f32 v52, v52, v227\n"
"v_add_u32 v214, v213, v217\n"
"v_mfma_f32_16x16x32_bf16 a[84:87], a[212:215], v[16:19], a[84:87]\n"
"s_mov_b32 vcc_lo, 0\n"
"s_mov_b32 vcc_hi, 255\n"
"v_exp_f32 v231, v231\n"
"v_mfma_f32_16x16x32_bf16 a[88:91], a[216:219], v[16:19], a[88:91]\n"
"s_waitcnt vmcnt(21) lgkmcnt(0)\n"
"s_barrier\n"
"v_cvt_pk_bf16_f32 v21, v230, v231\n"
"v_cndmask_b32 v217, v249, v206, vcc\n"
"v_mfma_f32_16x16x32_bf16 a[92:95], a[220:223], v[16:19], a[92:95]\n"
"v_exp_f32 v232, v232\n"
"v_mfma_f32_16x16x32_bf16 v[0:3], a[160:163], v[64:67], v[24:27]\n"
"ds_read_b128 a[128:131], v241 offset:32768\n"
"v_add_u32 v236, v214, v217\n"
"v_mfma_f32_16x16x32_bf16 v[0:3], a[164:167], v[68:71], v[0:3]\n"
"s_mov_b32 vcc_lo, 0\n"
"s_mov_b32 vcc_hi, 65280\n"
"v_exp_f32 v233, v233\n"
"v_mfma_f32_16x16x32_bf16 v[0:3], a[168:171], v[72:75], v[0:3]\n"
"ds_read_b128 a[132:135], v241 offset:33024\n"
"v_cvt_pk_bf16_f32 v22, v232, v233\n"
"v_mfma_f32_16x16x32_bf16 v[0:3], a[172:175], v[76:79], v[0:3]\n"
"v_exp_f32 v234, v234\n"
"v_mfma_f32_16x16x32_bf16 v[4:7], a[176:179], v[64:67], v[24:27]\n"
"ds_read_b128 a[136:139], v241 offset:33280\n"
"v_cndmask_b32 v217, v249, v206, vcc\n"
"v_mfma_f32_16x16x32_bf16 v[4:7], a[180:183], v[68:71], v[4:7]\n"
"v_exp_f32 v235, v235\n"
"v_mfma_f32_16x16x32_bf16 v[4:7], a[184:187], v[72:75], v[4:7]\n"
"ds_read_b128 a[140:143], v241 offset:33536\n"
"v_cvt_pk_bf16_f32 v23, v234, v235\n"
"v_mfma_f32_16x16x32_bf16 v[4:7], a[188:191], v[76:79], v[4:7]\n"
"s_add_u32 m0, s86, 65536\n"
"v_exp_f32 v0, v0\n"
"v_mfma_f32_16x16x32_bf16 a[96:99], a[192:195], v[20:23], a[96:99]\n"
"buffer_load_dwordx4 v246, s[8:11], s84 offen offset:0 lds\n"
"v_add_u32 v237, v236, v217\n"
"v_mfma_f32_16x16x32_bf16 a[100:103], a[196:199], v[20:23], a[100:103]\n"
"s_mov_b32 vcc_lo, 0\n"
"s_mov_b32 vcc_hi, 16711680\n"
"v_exp_f32 v1, v1\n"
"v_mfma_f32_16x16x32_bf16 a[104:107], a[200:203], v[20:23], a[104:107]\n"
"ds_read_b128 a[144:147], v241 offset:36864\n"
"v_cvt_pk_bf16_f32 v16, v0, v1\n"
"v_mfma_f32_16x16x32_bf16 a[108:111], a[204:207], v[20:23], a[108:111]\n"
"v_exp_f32 v2, v2\n"
"v_mfma_f32_16x16x32_bf16 a[112:115], a[208:211], v[20:23], a[112:115]\n"
"ds_read_b128 a[148:151], v241 offset:37120\n"
"v_add_f32 v44, v44, v0\n"
"v_mfma_f32_16x16x32_bf16 a[116:119], a[212:215], v[20:23], a[116:119]\n"
"v_exp_f32 v3, v3\n"
"v_mfma_f32_16x16x32_bf16 a[120:123], a[216:219], v[20:23], a[120:123]\n"
"ds_read_b128 a[152:155], v241 offset:37376\n"
"v_cvt_pk_bf16_f32 v17, v2, v3\n"
"v_mfma_f32_16x16x32_bf16 a[124:127], a[220:223], v[20:23], a[124:127]\n"
"v_exp_f32 v4, v4\n"
"v_mfma_f32_16x16x32_bf16 v[56:59], v[40:43], v[20:23], v[56:59]\n"
"ds_read_b128 a[156:159], v241 offset:37632\n"
"v_add_f32 v44, v44, v1\n"
"v_mfma_f32_16x16x32_bf16 v[8:11], a[160:163], v[80:83], v[28:31]\n"
"v_exp_f32 v5, v5\n"
"v_mfma_f32_16x16x32_bf16 v[8:11], a[164:167], v[84:87], v[8:11]\n"
"ds_read_b64_tr_b16 a[192:193], v244 offset:49152\n"
"v_cvt_pk_bf16_f32 v18, v4, v5\n"
"v_mfma_f32_16x16x32_bf16 v[8:11], a[168:171], v[88:91], v[8:11]\n"
"v_exp_f32 v6, v6\n"
"v_mfma_f32_16x16x32_bf16 v[8:11], a[172:175], v[92:95], v[8:11]\n"
"ds_read_b64_tr_b16 a[194:195], v244 offset:53248\n"
"v_add_f32 v44, v44, v2\n"
"v_mfma_f32_16x16x32_bf16 v[12:15], a[176:179], v[80:83], v[28:31]\n"
"v_exp_f32 v7, v7\n"
"v_mfma_f32_16x16x32_bf16 v[12:15], a[180:183], v[84:87], v[12:15]\n"
"ds_read_b64_tr_b16 a[196:197], v245 offset:49152\n"
"v_cvt_pk_bf16_f32 v19, v6, v7\n"
"v_mfma_f32_16x16x32_bf16 v[12:15], a[184:187], v[88:91], v[12:15]\n"
"buffer_load_dwordx4 v246, s[8:11], s84 offen offset:1024 lds\n"
"v_cndmask_b32 v217, v249, v206, vcc\n"
"v_mfma_f32_16x16x32_bf16 v[12:15], a[188:191], v[92:95], v[12:15]\n"
"s_waitcnt lgkmcnt(11)\n"
"v_exp_f32 v8, v8\n"
"v_mfma_f32_16x16x32_bf16 a[0:3], a[224:227], v[16:19], a[0:3]\n"
"ds_read_b64_tr_b16 a[198:199], v245 offset:53248\n"
"v_add_f32 v44, v44, v3\n"
"v_mfma_f32_16x16x32_bf16 a[4:7], a[228:231], v[16:19], a[4:7]\n"
"v_exp_f32 v9, v9\n"
"v_mfma_f32_16x16x32_bf16 a[8:11], a[232:235], v[16:19], a[8:11]\n"
"ds_read_b64_tr_b16 a[200:201], v244 offset:49408\n"
"v_cvt_pk_bf16_f32 v20, v8, v9\n"
"v_mfma_f32_16x16x32_bf16 a[12:15], a[236:239], v[16:19], a[12:15]\n"
"v_exp_f32 v10, v10\n"
"v_mfma_f32_16x16x32_bf16 a[16:19], a[240:243], v[16:19], a[16:19]\n"
"ds_read_b64_tr_b16 a[202:203], v244 offset:53504\n"
"v_add_f32 v44, v44, v4\n"
"v_mfma_f32_16x16x32_bf16 a[20:23], a[244:247], v[16:19], a[20:23]\n"
"v_exp_f32 v11, v11\n"
"v_mfma_f32_16x16x32_bf16 a[24:27], a[248:251], v[16:19], a[24:27]\n"
"ds_read_b64_tr_b16 a[204:205], v245 offset:49408\n"
"v_cvt_pk_bf16_f32 v21, v10, v11\n"
"v_mfma_f32_16x16x32_bf16 a[28:31], a[252:255], v[16:19], a[28:31]\n"
"v_exp_f32 v12, v12\n"
"v_mfma_f32_16x16x32_bf16 v[220:223], a[160:163], v[96:99], v[32:35]\n"
"ds_read_b64_tr_b16 a[206:207], v245 offset:53504\n"
"v_add_f32 v44, v44, v5\n"
"v_mfma_f32_16x16x32_bf16 v[220:223], a[164:167], v[100:103], v[220:223]\n"
"v_exp_f32 v13, v13\n"
"v_mfma_f32_16x16x32_bf16 v[220:223], a[168:171], v[104:107], v[220:223]\n"
"ds_read_b64_tr_b16 a[208:209], v244 offset:49664\n"
"v_cvt_pk_bf16_f32 v22, v12, v13\n"
"v_mfma_f32_16x16x32_bf16 v[220:223], a[172:175], v[108:111], v[220:223]\n"
"v_exp_f32 v14, v14\n"
"v_mfma_f32_16x16x32_bf16 v[224:227], a[176:179], v[96:99], v[32:35]\n"
"ds_read_b64_tr_b16 a[210:211], v244 offset:53760\n"
"v_add_f32 v44, v44, v6\n"
"v_mfma_f32_16x16x32_bf16 v[224:227], a[180:183], v[100:103], v[224:227]\n"
"v_exp_f32 v15, v15\n"
"v_mfma_f32_16x16x32_bf16 v[224:227], a[184:187], v[104:107], v[224:227]\n"
"ds_read_b64_tr_b16 a[212:213], v245 offset:49664\n"
"v_cvt_pk_bf16_f32 v23, v14, v15\n"
"v_mfma_f32_16x16x32_bf16 v[224:227], a[188:191], v[108:111], v[224:227]\n"
"v_exp_f32 v220, v220\n"
"v_mfma_f32_16x16x32_bf16 a[32:35], a[224:227], v[20:23], a[32:35]\n"
"buffer_load_dwordx4 v247, s[8:11], s84 offen offset:2048 lds\n"
"v_add_u32 v238, v237, v217\n"
"v_mfma_f32_16x16x32_bf16 a[36:39], a[228:231], v[20:23], a[36:39]\n"
"s_mov_b32 vcc_lo, 0\n"
"s_mov_b32 vcc_hi, 4278190080\n"
"v_exp_f32 v221, v221\n"
"v_mfma_f32_16x16x32_bf16 a[40:43], a[232:235], v[20:23], a[40:43]\n"
"ds_read_b64_tr_b16 a[214:215], v245 offset:53760\n"
"v_cvt_pk_bf16_f32 v16, v220, v221\n"
"v_mfma_f32_16x16x32_bf16 a[44:47], a[236:239], v[20:23], a[44:47]\n"
"v_exp_f32 v222, v222\n"
"v_mfma_f32_16x16x32_bf16 a[48:51], a[240:243], v[20:23], a[48:51]\n"
"ds_read_b64_tr_b16 a[216:217], v244 offset:49920\n"
"v_add_f32 v44, v44, v7\n"
"v_mfma_f32_16x16x32_bf16 a[52:55], a[244:247], v[20:23], a[52:55]\n"
"v_exp_f32 v223, v223\n"
"v_mfma_f32_16x16x32_bf16 a[56:59], a[248:251], v[20:23], a[56:59]\n"
"ds_read_b64_tr_b16 a[218:219], v244 offset:54016\n"
"v_cvt_pk_bf16_f32 v17, v222, v223\n"
"v_mfma_f32_16x16x32_bf16 a[60:63], a[252:255], v[20:23], a[60:63]\n"
"v_exp_f32 v224, v224\n"
"v_mfma_f32_16x16x32_bf16 v[48:51], v[40:43], v[20:23], v[48:51]\n"
"ds_read_b64_tr_b16 a[220:221], v245 offset:49920\n"
"v_add_f32 v52, v52, v220\n"
"v_mfma_f32_16x16x32_bf16 v[228:231], a[160:163], v[112:115], v[36:39]\n"
"v_exp_f32 v225, v225\n"
"v_mfma_f32_16x16x32_bf16 v[228:231], a[164:167], v[116:119], v[228:231]\n"
"ds_read_b64_tr_b16 a[222:223], v245 offset:54016\n"
"v_cvt_pk_bf16_f32 v18, v224, v225\n"
"v_mfma_f32_16x16x32_bf16 v[228:231], a[168:171], v[120:123], v[228:231]\n"
"v_exp_f32 v226, v226\n"
"v_mfma_f32_16x16x32_bf16 v[228:231], a[172:175], v[124:127], v[228:231]\n"
"v_add_f32 v52, v52, v221\n"
"v_add_f32 v52, v52, v222\n"
"v_mfma_f32_16x16x32_bf16 v[232:235], a[176:179], v[112:115], v[36:39]\n"
"v_exp_f32 v227, v227\n"
"v_mfma_f32_16x16x32_bf16 v[232:235], a[180:183], v[116:119], v[232:235]\n"
"v_cvt_pk_bf16_f32 v19, v226, v227\n"
"v_add_f32 v52, v52, v223\n"
"v_mfma_f32_16x16x32_bf16 v[232:235], a[184:187], v[120:123], v[232:235]\n"
"buffer_load_dwordx4 v247, s[8:11], s84 offen offset:3072 lds\n"
"v_cndmask_b32 v217, v249, v206, vcc\n"
"v_mfma_f32_16x16x32_bf16 v[232:235], a[188:191], v[124:127], v[232:235]\n"
"v_exp_f32 v228, v228\n"
"v_mfma_f32_16x16x32_bf16 a[64:67], a[224:227], v[16:19], a[64:67]\n"
"v_add_f32 v52, v52, v224\n"
"v_add_f32 v52, v52, v225\n"
"v_mfma_f32_16x16x32_bf16 a[68:71], a[228:231], v[16:19], a[68:71]\n"
"v_exp_f32 v229, v229\n"
"v_mfma_f32_16x16x32_bf16 a[72:75], a[232:235], v[16:19], a[72:75]\n"
"v_cvt_pk_bf16_f32 v20, v228, v229\n"
"v_add_f32 v52, v52, v226\n"
"v_mfma_f32_16x16x32_bf16 a[76:79], a[236:239], v[16:19], a[76:79]\n"
"v_exp_f32 v230, v230\n"
"v_mfma_f32_16x16x32_bf16 a[80:83], a[240:243], v[16:19], a[80:83]\n"
"v_add_f32 v52, v52, v227\n"
"v_add_u32 v239, v238, v217\n"
"v_mfma_f32_16x16x32_bf16 a[84:87], a[244:247], v[16:19], a[84:87]\n"
"s_add_u32 s49, s49, 9216\n"
"v_exp_f32 v231, v231\n"
"v_mfma_f32_16x16x32_bf16 a[88:91], a[248:251], v[16:19], a[88:91]\n"
"s_waitcnt lgkmcnt(8)\n"
"buffer_load_dwordx4 v[132:135], v252, s[64:67], s49 offen\n"
"v_cvt_pk_bf16_f32 v21, v230, v231\n"
"v_mfma_f32_16x16x32_bf16 a[92:95], a[252:255], v[16:19], a[92:95]\n"
"s_and_b32 s56, s43, 3\n"
"v_exp_f32 v232, v232\n"
"v_mfma_f32_16x16x32_bf16 v[0:3], a[128:131], v[64:67], v[24:27]\n"
"s_lshr_b32 s57, s43, 2\n"
"ds_read_b128 a[160:163], v241 offset:40960\n"
"v_mfma_f32_16x16x32_bf16 v[0:3], a[132:135], v[68:71], v[0:3]\n"
"s_mul_i32 s57, s57, 1179648\n"
"v_exp_f32 v233, v233\n"
"v_mfma_f32_16x16x32_bf16 v[0:3], a[136:139], v[72:75], v[0:3]\n"
"s_mul_i32 s62, s56, 288\n"
"ds_read_b128 a[164:167], v241 offset:41216\n"
"v_cvt_pk_bf16_f32 v22, v232, v233\n"
"v_mfma_f32_16x16x32_bf16 v[0:3], a[140:143], v[76:79], v[0:3]\n"
"s_add_u32 s57, s57, s62\n"
"v_exp_f32 v234, v234\n"
"v_mfma_f32_16x16x32_bf16 v[4:7], a[144:147], v[64:67], v[24:27]\n"
"s_lshl_b32 s58, s56, 3\n"
"ds_read_b128 a[168:171], v241 offset:41472\n"
"v_mfma_f32_16x16x32_bf16 v[4:7], a[148:151], v[68:71], v[4:7]\n"
"s_add_u32 s58, s58, s41\n"
"v_exp_f32 v235, v235\n"
"v_mfma_f32_16x16x32_bf16 v[4:7], a[152:155], v[72:75], v[4:7]\n"
"s_add_u32 s58, s58, 0\n"
"ds_read_b128 a[172:175], v241 offset:41728\n"
"v_cvt_pk_bf16_f32 v23, v234, v235\n"
"v_mfma_f32_16x16x32_bf16 v[4:7], a[156:159], v[76:79], v[4:7]\n"
"s_add_u32 m0, s86, 81920\n"
"v_exp_f32 v0, v0\n"
"v_mfma_f32_16x16x32_bf16 a[96:99], a[224:227], v[20:23], a[96:99]\n"
"s_mul_i32 s58, s58, 36864\n"
"buffer_load_dwordx4 v246, s[12:15], s84 offen offset:0 lds\n"
"v_mfma_f32_16x16x32_bf16 a[100:103], a[228:231], v[20:23], a[100:103]\n"
"s_add_u32 s20, s58, s57\n"
"v_exp_f32 v1, v1\n"
"v_mfma_f32_16x16x32_bf16 a[104:107], a[232:235], v[20:23], a[104:107]\n"
"s_lshl_b32 s58, s56, 3\n"
"ds_read_b128 a[176:179], v241 offset:45056\n"
"v_cvt_pk_bf16_f32 v16, v0, v1\n"
"v_mfma_f32_16x16x32_bf16 a[108:111], a[236:239], v[20:23], a[108:111]\n"
"s_add_u32 s58, s58, s41\n"
"v_exp_f32 v2, v2\n"
"v_mfma_f32_16x16x32_bf16 a[112:115], a[240:243], v[20:23], a[112:115]\n"
"s_add_u32 s58, s58, 4\n"
"ds_read_b128 a[180:183], v241 offset:45312\n"
"v_add_f32 v44, v44, v0\n"
"v_mfma_f32_16x16x32_bf16 a[116:119], a[244:247], v[20:23], a[116:119]\n"
"s_mul_i32 s58, s58, 36864\n"
"v_exp_f32 v3, v3\n"
"v_mfma_f32_16x16x32_bf16 a[120:123], a[248:251], v[20:23], a[120:123]\n"
"s_add_u32 s21, s58, s57\n"
"ds_read_b128 a[184:187], v241 offset:45568\n"
"v_cvt_pk_bf16_f32 v17, v2, v3\n"
"v_mfma_f32_16x16x32_bf16 a[124:127], a[252:255], v[20:23], a[124:127]\n"
"s_add_u32 s58, s41, 0\n"
"v_exp_f32 v4, v4\n"
"v_mfma_f32_16x16x32_bf16 v[56:59], v[40:43], v[20:23], v[56:59]\n"
"ds_read_b128 a[188:191], v241 offset:45824\n"
"v_add_f32 v44, v44, v1\n"
"v_mfma_f32_16x16x32_bf16 v[8:11], a[128:131], v[80:83], v[28:31]\n"
"v_exp_f32 v5, v5\n"
"v_mfma_f32_16x16x32_bf16 v[8:11], a[132:135], v[84:87], v[8:11]\n"
"ds_read_b64_tr_b16 a[224:225], v244 offset:57344\n"
"v_cvt_pk_bf16_f32 v18, v4, v5\n"
"v_mfma_f32_16x16x32_bf16 v[8:11], a[136:139], v[88:91], v[8:11]\n"
"v_exp_f32 v6, v6\n"
"v_mfma_f32_16x16x32_bf16 v[8:11], a[140:143], v[92:95], v[8:11]\n"
"ds_read_b64_tr_b16 a[226:227], v244 offset:61440\n"
"v_add_f32 v44, v44, v2\n"
"v_mfma_f32_16x16x32_bf16 v[12:15], a[144:147], v[80:83], v[28:31]\n"
"v_exp_f32 v7, v7\n"
"v_mfma_f32_16x16x32_bf16 v[12:15], a[148:151], v[84:87], v[12:15]\n"
"ds_read_b64_tr_b16 a[228:229], v245 offset:57344\n"
"buffer_load_dwordx4 v[168:171], v252, s[68:71], s49 offen\n"
"v_cvt_pk_bf16_f32 v19, v6, v7\n"
"v_mfma_f32_16x16x32_bf16 v[12:15], a[152:155], v[88:91], v[12:15]\n"
"s_mul_i32 s59, s58, 43\n"
"buffer_load_dwordx4 v247, s[12:15], s84 offen offset:1024 lds\n"
"v_mfma_f32_16x16x32_bf16 v[12:15], a[156:159], v[92:95], v[12:15]\n"
"s_waitcnt lgkmcnt(11)\n"
"v_exp_f32 v8, v8\n"
"v_mfma_f32_16x16x32_bf16 a[0:3], a[192:195], v[16:19], a[0:3]\n"
"s_lshr_b32 s59, s59, 7\n"
"ds_read_b64_tr_b16 a[230:231], v245 offset:61440\n"
"v_add_f32 v44, v44, v3\n"
"v_mfma_f32_16x16x32_bf16 a[4:7], a[196:199], v[16:19], a[4:7]\n"
"s_mul_i32 s60, s59, 3\n"
"v_exp_f32 v9, v9\n"
"v_mfma_f32_16x16x32_bf16 a[8:11], a[200:203], v[16:19], a[8:11]\n"
"s_sub_u32 s58, s58, s60\n"
"ds_read_b64_tr_b16 a[232:233], v244 offset:57600\n"
"v_cvt_pk_bf16_f32 v20, v8, v9\n"
"v_mfma_f32_16x16x32_bf16 a[12:15], a[204:207], v[16:19], a[12:15]\n"
"s_add_u32 s58, s58, s56\n"
"v_exp_f32 v10, v10\n"
"v_mfma_f32_16x16x32_bf16 a[16:19], a[208:211], v[16:19], a[16:19]\n"
"s_add_u32 s58, s58, 1\n"
"ds_read_b64_tr_b16 a[234:235], v244 offset:61696\n"
"v_add_f32 v44, v44, v4\n"
"v_mfma_f32_16x16x32_bf16 a[20:23], a[212:215], v[16:19], a[20:23]\n"
"s_and_b32 s58, s58, 3\n"
"v_exp_f32 v11, v11\n"
"v_mfma_f32_16x16x32_bf16 a[24:27], a[216:219], v[16:19], a[24:27]\n"
"s_lshl_b32 s58, s58, 3\n"
"ds_read_b64_tr_b16 a[236:237], v245 offset:57600\n"
"v_cvt_pk_bf16_f32 v21, v10, v11\n"
"v_mfma_f32_16x16x32_bf16 a[28:31], a[220:223], v[16:19], a[28:31]\n"
"s_add_u32 s58, s58, s59\n"
"v_exp_f32 v12, v12\n"
"v_mfma_f32_16x16x32_bf16 v[220:223], a[128:131], v[96:99], v[32:35]\n"
"s_mul_i32 s58, s58, 36864\n"
"ds_read_b64_tr_b16 a[238:239], v245 offset:61696\n"
"v_add_f32 v44, v44, v5\n"
"v_mfma_f32_16x16x32_bf16 v[220:223], a[132:135], v[100:103], v[220:223]\n"
"s_add_u32 s22, s58, s57\n"
"v_exp_f32 v13, v13\n"
"v_mfma_f32_16x16x32_bf16 v[220:223], a[136:139], v[104:107], v[220:223]\n"
"s_add_u32 s58, s41, 4\n"
"ds_read_b64_tr_b16 a[240:241], v244 offset:57856\n"
"v_cvt_pk_bf16_f32 v22, v12, v13\n"
"v_mfma_f32_16x16x32_bf16 v[220:223], a[140:143], v[108:111], v[220:223]\n"
"s_mul_i32 s59, s58, 43\n"
"v_exp_f32 v14, v14\n"
"v_mfma_f32_16x16x32_bf16 v[224:227], a[144:147], v[96:99], v[32:35]\n"
"s_lshr_b32 s59, s59, 7\n"
"ds_read_b64_tr_b16 a[242:243], v244 offset:61952\n"
"v_add_f32 v44, v44, v6\n"
"v_mfma_f32_16x16x32_bf16 v[224:227], a[148:151], v[100:103], v[224:227]\n"
"s_mul_i32 s60, s59, 3\n"
"v_exp_f32 v15, v15\n"
"v_mfma_f32_16x16x32_bf16 v[224:227], a[152:155], v[104:107], v[224:227]\n"
"s_sub_u32 s58, s58, s60\n"
"ds_read_b64_tr_b16 a[244:245], v245 offset:57856\n"
"v_cvt_pk_bf16_f32 v23, v14, v15\n"
"v_mfma_f32_16x16x32_bf16 v[224:227], a[156:159], v[108:111], v[224:227]\n"
"s_add_u32 s49, s49, 285696\n"
"v_exp_f32 v220, v220\n"
"v_mfma_f32_16x16x32_bf16 a[32:35], a[192:195], v[20:23], a[32:35]\n"
"buffer_load_dwordx4 v246, s[12:15], s84 offen offset:2048 lds\n"
"v_mfma_f32_16x16x32_bf16 a[36:39], a[196:199], v[20:23], a[36:39]\n"
"v_exp_f32 v221, v221\n"
"v_mfma_f32_16x16x32_bf16 a[40:43], a[200:203], v[20:23], a[40:43]\n"
"ds_read_b64_tr_b16 a[246:247], v245 offset:61952\n"
"v_cvt_pk_bf16_f32 v16, v220, v221\n"
"v_mfma_f32_16x16x32_bf16 a[44:47], a[204:207], v[20:23], a[44:47]\n"
"v_exp_f32 v222, v222\n"
"v_mfma_f32_16x16x32_bf16 a[48:51], a[208:211], v[20:23], a[48:51]\n"
"ds_read_b64_tr_b16 a[248:249], v244 offset:58112\n"
"v_add_f32 v44, v44, v7\n"
"v_mfma_f32_16x16x32_bf16 a[52:55], a[212:215], v[20:23], a[52:55]\n"
"v_exp_f32 v223, v223\n"
"v_mfma_f32_16x16x32_bf16 a[56:59], a[216:219], v[20:23], a[56:59]\n"
"ds_read_b64_tr_b16 a[250:251], v244 offset:62208\n"
"buffer_load_dwordx4 v[136:139], v252, s[64:67], s49 offen\n"
"v_cvt_pk_bf16_f32 v17, v222, v223\n"
"v_mfma_f32_16x16x32_bf16 a[60:63], a[220:223], v[20:23], a[60:63]\n"
"s_add_u32 s58, s58, s56\n"
"v_exp_f32 v224, v224\n"
"v_mfma_f32_16x16x32_bf16 v[48:51], v[40:43], v[20:23], v[48:51]\n"
"s_add_u32 s58, s58, 1\n"
"ds_read_b64_tr_b16 a[252:253], v245 offset:58112\n"
"v_add_f32 v52, v52, v220\n"
"v_mfma_f32_16x16x32_bf16 v[228:231], a[128:131], v[112:115], v[36:39]\n"
"s_and_b32 s58, s58, 3\n"
"v_exp_f32 v225, v225\n"
"v_mfma_f32_16x16x32_bf16 v[228:231], a[132:135], v[116:119], v[228:231]\n"
"s_lshl_b32 s58, s58, 3\n"
"ds_read_b64_tr_b16 a[254:255], v245 offset:62208\n"
"v_cvt_pk_bf16_f32 v18, v224, v225\n"
"v_mfma_f32_16x16x32_bf16 v[228:231], a[136:139], v[120:123], v[228:231]\n"
"s_add_u32 s58, s58, s59\n"
"v_exp_f32 v226, v226\n"
"v_mfma_f32_16x16x32_bf16 v[228:231], a[140:143], v[124:127], v[228:231]\n"
"s_mul_i32 s58, s58, 36864\n"
"v_add_f32 v52, v52, v221\n"
"v_add_f32 v52, v52, v222\n"
"v_mfma_f32_16x16x32_bf16 v[232:235], a[144:147], v[112:115], v[36:39]\n"
"s_add_u32 s23, s58, s57\n"
"v_exp_f32 v227, v227\n"
"v_mfma_f32_16x16x32_bf16 v[232:235], a[148:151], v[116:119], v[232:235]\n"
"s_add_u32 s58, s41, 8\n"
"v_cvt_pk_bf16_f32 v19, v226, v227\n"
"v_add_f32 v52, v52, v223\n"
"v_mfma_f32_16x16x32_bf16 v[232:235], a[152:155], v[120:123], v[232:235]\n"
"s_mul_i32 s59, s58, 43\n"
"buffer_load_dwordx4 v247, s[12:15], s84 offen offset:3072 lds\n"
"v_mfma_f32_16x16x32_bf16 v[232:235], a[156:159], v[124:127], v[232:235]\n"
"s_add_u32 s84, s84, 16384\n"
"v_exp_f32 v228, v228\n"
"v_mfma_f32_16x16x32_bf16 a[64:67], a[192:195], v[16:19], a[64:67]\n"
"s_lshr_b32 s59, s59, 7\n"
"v_add_f32 v52, v52, v224\n"
"v_add_f32 v52, v52, v225\n"
"v_mfma_f32_16x16x32_bf16 a[68:71], a[196:199], v[16:19], a[68:71]\n"
"s_mul_i32 s60, s59, 3\n"
"v_exp_f32 v229, v229\n"
"v_mfma_f32_16x16x32_bf16 a[72:75], a[200:203], v[16:19], a[72:75]\n"
"s_sub_u32 s58, s58, s60\n"
"v_cvt_pk_bf16_f32 v20, v228, v229\n"
"v_add_f32 v52, v52, v226\n"
"v_mfma_f32_16x16x32_bf16 a[76:79], a[204:207], v[16:19], a[76:79]\n"
"s_add_u32 s58, s58, s56\n"
"v_exp_f32 v230, v230\n"
"v_mfma_f32_16x16x32_bf16 a[80:83], a[208:211], v[16:19], a[80:83]\n"
"s_add_u32 s58, s58, 1\n"
"v_add_f32 v52, v52, v227\n"
"v_mfma_f32_16x16x32_bf16 a[84:87], a[212:215], v[16:19], a[84:87]\n"
"s_and_b32 s58, s58, 3\n"
"v_exp_f32 v231, v231\n"
"v_mfma_f32_16x16x32_bf16 a[88:91], a[216:219], v[16:19], a[88:91]\n"
"s_waitcnt vmcnt(21) lgkmcnt(0)\n"
"s_barrier\n"
"v_cvt_pk_bf16_f32 v21, v230, v231\n"
"v_mfma_f32_16x16x32_bf16 a[92:95], a[220:223], v[16:19], a[92:95]\n"
"s_lshl_b32 s58, s58, 3\n"
"v_exp_f32 v232, v232\n"
"v_mfma_f32_16x16x32_bf16 v[0:3], a[160:163], v[64:67], v[24:27]\n"
"ds_read_b128 a[128:131], v240 offset:0\n"
"v_mfma_f32_16x16x32_bf16 v[0:3], a[164:167], v[68:71], v[0:3]\n"
"v_exp_f32 v233, v233\n"
"v_mfma_f32_16x16x32_bf16 v[0:3], a[168:171], v[72:75], v[0:3]\n"
"ds_read_b128 a[132:135], v240 offset:256\n"
"v_cvt_pk_bf16_f32 v22, v232, v233\n"
"v_mfma_f32_16x16x32_bf16 v[0:3], a[172:175], v[76:79], v[0:3]\n"
"v_exp_f32 v234, v234\n"
"v_mfma_f32_16x16x32_bf16 v[4:7], a[176:179], v[64:67], v[24:27]\n"
"ds_read_b128 a[136:139], v240 offset:512\n"
"v_mfma_f32_16x16x32_bf16 v[4:7], a[180:183], v[68:71], v[4:7]\n"
"v_exp_f32 v235, v235\n"
"v_mfma_f32_16x16x32_bf16 v[4:7], a[184:187], v[72:75], v[4:7]\n"
"ds_read_b128 a[140:143], v240 offset:768\n"
"buffer_load_dwordx4 v[172:175], v252, s[68:71], s49 offen\n"
"v_cvt_pk_bf16_f32 v23, v234, v235\n"
"v_mfma_f32_16x16x32_bf16 v[4:7], a[188:191], v[76:79], v[4:7]\n"
"s_add_u32 m0, s86, 98304\n"
"v_exp_f32 v0, v0\n"
"v_mfma_f32_16x16x32_bf16 a[96:99], a[192:195], v[20:23], a[96:99]\n"
"s_add_u32 s58, s58, s59\n"
"buffer_load_dwordx4 v246, s[8:11], s84 offen offset:0 lds\n"
"v_mfma_f32_16x16x32_bf16 a[100:103], a[196:199], v[20:23], a[100:103]\n"
"s_mul_i32 s58, s58, 36864\n"
"v_exp_f32 v1, v1\n"
"v_mfma_f32_16x16x32_bf16 a[104:107], a[200:203], v[20:23], a[104:107]\n"
"s_add_u32 s36, s58, s57\n"
"ds_read_b128 a[144:147], v240 offset:4096\n"
"v_cvt_pk_bf16_f32 v16, v0, v1\n"
"v_mfma_f32_16x16x32_bf16 a[108:111], a[204:207], v[20:23], a[108:111]\n"
"s_add_u32 s58, s41, 12\n"
"v_exp_f32 v2, v2\n"
"v_mfma_f32_16x16x32_bf16 a[112:115], a[208:211], v[20:23], a[112:115]\n"
"s_mul_i32 s59, s58, 43\n"
"ds_read_b128 a[148:151], v240 offset:4352\n"
"v_add_f32 v44, v44, v0\n"
"v_mfma_f32_16x16x32_bf16 a[116:119], a[212:215], v[20:23], a[116:119]\n"
"s_lshr_b32 s59, s59, 7\n"
"v_exp_f32 v3, v3\n"
"v_mfma_f32_16x16x32_bf16 a[120:123], a[216:219], v[20:23], a[120:123]\n"
"s_mul_i32 s60, s59, 3\n"
"ds_read_b128 a[152:155], v240 offset:4608\n"
"v_cvt_pk_bf16_f32 v17, v2, v3\n"
"v_mfma_f32_16x16x32_bf16 a[124:127], a[220:223], v[20:23], a[124:127]\n"
"s_sub_u32 s58, s58, s60\n"
"v_exp_f32 v4, v4\n"
"v_mfma_f32_16x16x32_bf16 v[56:59], v[40:43], v[20:23], v[56:59]\n"
"s_add_u32 s58, s58, s56\n"
"ds_read_b128 a[156:159], v240 offset:4864\n"
"v_add_f32 v44, v44, v1\n"
"v_mfma_f32_16x16x32_bf16 v[8:11], a[160:163], v[80:83], v[28:31]\n"
"s_add_u32 s58, s58, 1\n"
"v_exp_f32 v5, v5\n"
"v_mfma_f32_16x16x32_bf16 v[8:11], a[164:167], v[84:87], v[8:11]\n"
"s_and_b32 s58, s58, 3\n"
"ds_read_b64_tr_b16 a[192:193], v242 offset:16384\n"
"v_cvt_pk_bf16_f32 v18, v4, v5\n"
"v_mfma_f32_16x16x32_bf16 v[8:11], a[168:171], v[88:91], v[8:11]\n"
"s_lshl_b32 s58, s58, 3\n"
"v_exp_f32 v6, v6\n"
"v_mfma_f32_16x16x32_bf16 v[8:11], a[172:175], v[92:95], v[8:11]\n"
"s_add_u32 s58, s58, s59\n"
"ds_read_b64_tr_b16 a[194:195], v242 offset:20480\n"
"v_add_f32 v44, v44, v2\n"
"v_mfma_f32_16x16x32_bf16 v[12:15], a[176:179], v[80:83], v[28:31]\n"
"s_mul_i32 s58, s58, 36864\n"
"v_exp_f32 v7, v7\n"
"v_mfma_f32_16x16x32_bf16 v[12:15], a[180:183], v[84:87], v[12:15]\n"
"s_add_u32 s37, s58, s57\n"
"ds_read_b64_tr_b16 a[196:197], v243 offset:16384\n"
"v_cvt_pk_bf16_f32 v19, v6, v7\n"
"v_mfma_f32_16x16x32_bf16 v[12:15], a[184:187], v[88:91], v[12:15]\n"
"s_add_u32 s58, s41, 16\n"
"buffer_load_dwordx4 v246, s[8:11], s84 offen offset:1024 lds\n"
"v_mfma_f32_16x16x32_bf16 v[12:15], a[188:191], v[92:95], v[12:15]\n"
"s_waitcnt lgkmcnt(11)\n"
"v_exp_f32 v8, v8\n"
"v_mfma_f32_16x16x32_bf16 a[0:3], a[224:227], v[16:19], a[0:3]\n"
"s_add_u32 s49, s49, 9216\n"
"ds_read_b64_tr_b16 a[198:199], v243 offset:20480\n"
"v_add_f32 v44, v44, v3\n"
"v_mfma_f32_16x16x32_bf16 a[4:7], a[228:231], v[16:19], a[4:7]\n"
"v_exp_f32 v9, v9\n"
"v_mfma_f32_16x16x32_bf16 a[8:11], a[232:235], v[16:19], a[8:11]\n"
"ds_read_b64_tr_b16 a[200:201], v242 offset:16640\n"
"v_cvt_pk_bf16_f32 v20, v8, v9\n"
"v_mfma_f32_16x16x32_bf16 a[12:15], a[236:239], v[16:19], a[12:15]\n"
"v_exp_f32 v10, v10\n"
"v_mfma_f32_16x16x32_bf16 a[16:19], a[240:243], v[16:19], a[16:19]\n"
"ds_read_b64_tr_b16 a[202:203], v242 offset:20736\n"
"v_add_f32 v44, v44, v4\n"
"v_mfma_f32_16x16x32_bf16 a[20:23], a[244:247], v[16:19], a[20:23]\n"
"v_exp_f32 v11, v11\n"
"v_mfma_f32_16x16x32_bf16 a[24:27], a[248:251], v[16:19], a[24:27]\n"
"ds_read_b64_tr_b16 a[204:205], v243 offset:16640\n"
"buffer_load_dwordx4 v[140:143], v252, s[64:67], s49 offen\n"
"v_cvt_pk_bf16_f32 v21, v10, v11\n"
"v_mfma_f32_16x16x32_bf16 a[28:31], a[252:255], v[16:19], a[28:31]\n"
"s_mul_i32 s59, s58, 43\n"
"v_exp_f32 v12, v12\n"
"v_mfma_f32_16x16x32_bf16 v[220:223], a[160:163], v[96:99], v[32:35]\n"
"s_lshr_b32 s59, s59, 7\n"
"ds_read_b64_tr_b16 a[206:207], v243 offset:20736\n"
"v_add_f32 v44, v44, v5\n"
"v_mfma_f32_16x16x32_bf16 v[220:223], a[164:167], v[100:103], v[220:223]\n"
"s_mul_i32 s60, s59, 3\n"
"v_exp_f32 v13, v13\n"
"v_mfma_f32_16x16x32_bf16 v[220:223], a[168:171], v[104:107], v[220:223]\n"
"s_sub_u32 s58, s58, s60\n"
"ds_read_b64_tr_b16 a[208:209], v242 offset:16896\n"
"v_cvt_pk_bf16_f32 v22, v12, v13\n"
"v_mfma_f32_16x16x32_bf16 v[220:223], a[172:175], v[108:111], v[220:223]\n"
"s_add_u32 s58, s58, s56\n"
"v_exp_f32 v14, v14\n"
"v_mfma_f32_16x16x32_bf16 v[224:227], a[176:179], v[96:99], v[32:35]\n"
"s_add_u32 s58, s58, 1\n"
"ds_read_b64_tr_b16 a[210:211], v242 offset:20992\n"
"v_add_f32 v44, v44, v6\n"
"v_mfma_f32_16x16x32_bf16 v[224:227], a[180:183], v[100:103], v[224:227]\n"
"s_and_b32 s58, s58, 3\n"
"v_exp_f32 v15, v15\n"
"v_mfma_f32_16x16x32_bf16 v[224:227], a[184:187], v[104:107], v[224:227]\n"
"s_lshl_b32 s58, s58, 3\n"
"ds_read_b64_tr_b16 a[212:213], v243 offset:16896\n"
"v_cvt_pk_bf16_f32 v23, v14, v15\n"
"v_mfma_f32_16x16x32_bf16 v[224:227], a[188:191], v[108:111], v[224:227]\n"
"s_add_u32 s58, s58, s59\n"
"v_exp_f32 v220, v220\n"
"v_mfma_f32_16x16x32_bf16 a[32:35], a[224:227], v[20:23], a[32:35]\n"
"s_mul_i32 s58, s58, 36864\n"
"buffer_load_dwordx4 v247, s[8:11], s84 offen offset:2048 lds\n"
"v_mfma_f32_16x16x32_bf16 a[36:39], a[228:231], v[20:23], a[36:39]\n"
"s_add_u32 s38, s58, s57\n"
"v_exp_f32 v221, v221\n"
"v_mfma_f32_16x16x32_bf16 a[40:43], a[232:235], v[20:23], a[40:43]\n"
"s_add_u32 s58, s41, 20\n"
"ds_read_b64_tr_b16 a[214:215], v243 offset:20992\n"
"v_cvt_pk_bf16_f32 v16, v220, v221\n"
"v_mfma_f32_16x16x32_bf16 a[44:47], a[236:239], v[20:23], a[44:47]\n"
"s_mul_i32 s59, s58, 43\n"
"v_exp_f32 v222, v222\n"
"v_mfma_f32_16x16x32_bf16 a[48:51], a[240:243], v[20:23], a[48:51]\n"
"s_lshr_b32 s59, s59, 7\n"
"ds_read_b64_tr_b16 a[216:217], v242 offset:17152\n"
"v_add_f32 v44, v44, v7\n"
"v_mfma_f32_16x16x32_bf16 a[52:55], a[244:247], v[20:23], a[52:55]\n"
"s_mul_i32 s60, s59, 3\n"
"v_exp_f32 v223, v223\n"
"v_mfma_f32_16x16x32_bf16 a[56:59], a[248:251], v[20:23], a[56:59]\n"
"s_sub_u32 s58, s58, s60\n"
"ds_read_b64_tr_b16 a[218:219], v242 offset:21248\n"
"v_cvt_pk_bf16_f32 v17, v222, v223\n"
"v_mfma_f32_16x16x32_bf16 a[60:63], a[252:255], v[20:23], a[60:63]\n"
"v_exp_f32 v224, v224\n"
"v_mfma_f32_16x16x32_bf16 v[48:51], v[40:43], v[20:23], v[48:51]\n"
"ds_read_b64_tr_b16 a[220:221], v243 offset:17152\n"
"v_add_f32 v52, v52, v220\n"
"v_mfma_f32_16x16x32_bf16 v[228:231], a[160:163], v[112:115], v[36:39]\n"
"v_exp_f32 v225, v225\n"
"v_mfma_f32_16x16x32_bf16 v[228:231], a[164:167], v[116:119], v[228:231]\n"
"ds_read_b64_tr_b16 a[222:223], v243 offset:21248\n"
"v_cvt_pk_bf16_f32 v18, v224, v225\n"
"v_mfma_f32_16x16x32_bf16 v[228:231], a[168:171], v[120:123], v[228:231]\n"
"v_exp_f32 v226, v226\n"
"v_mfma_f32_16x16x32_bf16 v[228:231], a[172:175], v[124:127], v[228:231]\n"
"v_add_f32 v52, v52, v221\n"
"v_add_f32 v52, v52, v222\n"
"v_mfma_f32_16x16x32_bf16 v[232:235], a[176:179], v[112:115], v[36:39]\n"
"v_exp_f32 v227, v227\n"
"v_mfma_f32_16x16x32_bf16 v[232:235], a[180:183], v[116:119], v[232:235]\n"
"buffer_load_dwordx4 v[176:179], v252, s[68:71], s49 offen\n"
"v_cvt_pk_bf16_f32 v19, v226, v227\n"
"v_add_f32 v52, v52, v223\n"
"v_mfma_f32_16x16x32_bf16 v[232:235], a[184:187], v[120:123], v[232:235]\n"
"s_add_u32 s58, s58, s56\n"
"buffer_load_dwordx4 v247, s[8:11], s84 offen offset:3072 lds\n"
"v_mfma_f32_16x16x32_bf16 v[232:235], a[188:191], v[124:127], v[232:235]\n"
"s_add_u32 s58, s58, 1\n"
"v_exp_f32 v228, v228\n"
"v_mfma_f32_16x16x32_bf16 a[64:67], a[224:227], v[16:19], a[64:67]\n"
"s_and_b32 s58, s58, 3\n"
"v_add_f32 v52, v52, v224\n"
"v_add_f32 v52, v52, v225\n"
"v_mfma_f32_16x16x32_bf16 a[68:71], a[228:231], v[16:19], a[68:71]\n"
"s_lshl_b32 s58, s58, 3\n"
"v_exp_f32 v229, v229\n"
"v_mfma_f32_16x16x32_bf16 a[72:75], a[232:235], v[16:19], a[72:75]\n"
"s_add_u32 s58, s58, s59\n"
"v_cvt_pk_bf16_f32 v20, v228, v229\n"
"v_add_f32 v52, v52, v226\n"
"v_mfma_f32_16x16x32_bf16 a[76:79], a[236:239], v[16:19], a[76:79]\n"
"s_mul_i32 s58, s58, 36864\n"
"v_exp_f32 v230, v230\n"
"v_mfma_f32_16x16x32_bf16 a[80:83], a[240:243], v[16:19], a[80:83]\n"
"s_add_u32 s39, s58, s57\n"
"v_add_f32 v52, v52, v227\n"
"v_mfma_f32_16x16x32_bf16 a[84:87], a[244:247], v[16:19], a[84:87]\n"
"s_mul_i32 s58, s41, 9216\n"
"v_exp_f32 v231, v231\n"
"v_mfma_f32_16x16x32_bf16 a[88:91], a[248:251], v[16:19], a[88:91]\n"
"s_waitcnt lgkmcnt(8)\n"
"v_cvt_pk_bf16_f32 v21, v230, v231\n"
"v_mfma_f32_16x16x32_bf16 a[92:95], a[252:255], v[16:19], a[92:95]\n"
"s_add_u32 s58, s58, s40\n"
"v_exp_f32 v232, v232\n"
"v_mfma_f32_16x16x32_bf16 v[0:3], a[128:131], v[64:67], v[24:27]\n"
"s_sub_u32 s53, s58, 27648\n"
"ds_read_b128 a[160:163], v240 offset:8192\n"
"v_mfma_f32_16x16x32_bf16 v[0:3], a[132:135], v[68:71], v[0:3]\n"
"s_add_u32 s58, s58, 131072\n"
"v_exp_f32 v233, v233\n"
"v_mfma_f32_16x16x32_bf16 v[0:3], a[136:139], v[72:75], v[0:3]\n"
"s_cmp_lt_u32 s41, 3\n"
"s_cselect_b32 s51, s58, s40\n"
"ds_read_b128 a[164:167], v240 offset:8448\n"
"v_cvt_pk_bf16_f32 v22, v232, v233\n"
"v_mfma_f32_16x16x32_bf16 v[0:3], a[140:143], v[76:79], v[0:3]\n"
"s_nop 0\n"
"v_exp_f32 v234, v234\n"
"v_mfma_f32_16x16x32_bf16 v[4:7], a[144:147], v[64:67], v[24:27]\n"
"s_and_b32 s58, s56, 1\n"
"ds_read_b128 a[168:171], v240 offset:8704\n"
"v_mfma_f32_16x16x32_bf16 v[4:7], a[148:151], v[68:71], v[4:7]\n"
"s_lshl_b32 s59, s58, 7\n"
"v_exp_f32 v235, v235\n"
"v_mfma_f32_16x16x32_bf16 v[4:7], a[152:155], v[72:75], v[4:7]\n"
"s_add_u32 s59, s59, s40\n"
"ds_read_b128 a[172:175], v240 offset:8960\n"
"v_cvt_pk_bf16_f32 v23, v234, v235\n"
"v_mfma_f32_16x16x32_bf16 v[4:7], a[156:159], v[76:79], v[4:7]\n"
"s_add_u32 m0, s86, 114688\n"
"v_exp_f32 v0, v0\n"
"v_mfma_f32_16x16x32_bf16 a[96:99], a[224:227], v[20:23], a[96:99]\n"
"s_add_u32 s49, s49, 285696\n"
"buffer_load_dwordx4 v246, s[12:15], s84 offen offset:0 lds\n"
"v_mfma_f32_16x16x32_bf16 a[100:103], a[228:231], v[20:23], a[100:103]\n"
"v_exp_f32 v1, v1\n"
"v_mfma_f32_16x16x32_bf16 a[104:107], a[232:235], v[20:23], a[104:107]\n"
"ds_read_b128 a[176:179], v240 offset:12288\n"
"v_cvt_pk_bf16_f32 v16, v0, v1\n"
"v_mfma_f32_16x16x32_bf16 a[108:111], a[236:239], v[20:23], a[108:111]\n"
"v_exp_f32 v2, v2\n"
"v_mfma_f32_16x16x32_bf16 a[112:115], a[240:243], v[20:23], a[112:115]\n"
"ds_read_b128 a[180:183], v240 offset:12544\n"
"v_add_f32 v44, v44, v0\n"
"v_mfma_f32_16x16x32_bf16 a[116:119], a[244:247], v[20:23], a[116:119]\n"
"v_exp_f32 v3, v3\n"
"v_mfma_f32_16x16x32_bf16 a[120:123], a[248:251], v[20:23], a[120:123]\n"
"ds_read_b128 a[184:187], v240 offset:12800\n"
"buffer_load_dwordx4 v[144:147], v252, s[64:67], s49 offen\n"
"v_cvt_pk_bf16_f32 v17, v2, v3\n"
"v_mfma_f32_16x16x32_bf16 a[124:127], a[252:255], v[20:23], a[124:127]\n"
"s_xor_b32 s60, s58, 1\n"
"v_exp_f32 v4, v4\n"
"v_mfma_f32_16x16x32_bf16 v[56:59], v[40:43], v[20:23], v[56:59]\n"
"s_lshl_b32 s60, s60, 10\n"
"ds_read_b128 a[188:191], v240 offset:13056\n"
"v_add_f32 v44, v44, v1\n"
"v_mfma_f32_16x16x32_bf16 v[8:11], a[128:131], v[80:83], v[28:31]\n"
"s_add_u32 s60, s60, s40\n"
"v_exp_f32 v5, v5\n"
"v_mfma_f32_16x16x32_bf16 v[8:11], a[132:135], v[84:87], v[8:11]\n"
"ds_read_b64_tr_b16 a[224:225], v242 offset:24576\n"
"v_cvt_pk_bf16_f32 v18, v4, v5\n"
"v_mfma_f32_16x16x32_bf16 v[8:11], a[136:139], v[88:91], v[8:11]\n"
"v_exp_f32 v6, v6\n"
"v_mfma_f32_16x16x32_bf16 v[8:11], a[140:143], v[92:95], v[8:11]\n"
"ds_read_b64_tr_b16 a[226:227], v242 offset:28672\n"
"v_add_f32 v44, v44, v2\n"
"v_mfma_f32_16x16x32_bf16 v[12:15], a[144:147], v[80:83], v[28:31]\n"
"v_exp_f32 v7, v7\n"
"v_mfma_f32_16x16x32_bf16 v[12:15], a[148:151], v[84:87], v[12:15]\n"
"ds_read_b64_tr_b16 a[228:229], v243 offset:24576\n"
"v_cvt_pk_bf16_f32 v19, v6, v7\n"
"v_mfma_f32_16x16x32_bf16 v[12:15], a[152:155], v[88:91], v[12:15]\n"
"buffer_load_dwordx4 v247, s[12:15], s84 offen offset:1024 lds\n"
"v_lshrrev_b32 v217, 4, v248\n"
"v_mfma_f32_16x16x32_bf16 v[12:15], a[156:159], v[92:95], v[12:15]\n"
"s_waitcnt lgkmcnt(11)\n"
"v_exp_f32 v8, v8\n"
"v_mfma_f32_16x16x32_bf16 a[0:3], a[192:195], v[16:19], a[0:3]\n"
"ds_read_b64_tr_b16 a[230:231], v243 offset:28672\n"
"v_add_f32 v44, v44, v3\n"
"v_mfma_f32_16x16x32_bf16 a[4:7], a[196:199], v[16:19], a[4:7]\n"
"v_exp_f32 v9, v9\n"
"v_mfma_f32_16x16x32_bf16 a[8:11], a[200:203], v[16:19], a[8:11]\n"
"ds_read_b64_tr_b16 a[232:233], v242 offset:24832\n"
"v_cvt_pk_bf16_f32 v20, v8, v9\n"
"v_mfma_f32_16x16x32_bf16 a[12:15], a[204:207], v[16:19], a[12:15]\n"
"v_exp_f32 v10, v10\n"
"v_mfma_f32_16x16x32_bf16 a[16:19], a[208:211], v[16:19], a[16:19]\n"
"ds_read_b64_tr_b16 a[234:235], v242 offset:28928\n"
"v_add_f32 v44, v44, v4\n"
"v_mfma_f32_16x16x32_bf16 a[20:23], a[212:215], v[16:19], a[20:23]\n"
"v_exp_f32 v11, v11\n"
"v_mfma_f32_16x16x32_bf16 a[24:27], a[216:219], v[16:19], a[24:27]\n"
"ds_read_b64_tr_b16 a[236:237], v243 offset:24832\n"
"v_cvt_pk_bf16_f32 v21, v10, v11\n"
"v_mfma_f32_16x16x32_bf16 a[28:31], a[220:223], v[16:19], a[28:31]\n"
"v_exp_f32 v12, v12\n"
"v_mfma_f32_16x16x32_bf16 v[220:223], a[128:131], v[96:99], v[32:35]\n"
"ds_read_b64_tr_b16 a[238:239], v243 offset:28928\n"
"v_add_f32 v44, v44, v5\n"
"v_mfma_f32_16x16x32_bf16 v[220:223], a[132:135], v[100:103], v[220:223]\n"
"v_exp_f32 v13, v13\n"
"v_mfma_f32_16x16x32_bf16 v[220:223], a[136:139], v[104:107], v[220:223]\n"
"ds_read_b64_tr_b16 a[240:241], v242 offset:25088\n"
"v_cvt_pk_bf16_f32 v22, v12, v13\n"
"v_mfma_f32_16x16x32_bf16 v[220:223], a[140:143], v[108:111], v[220:223]\n"
"v_exp_f32 v14, v14\n"
"v_mfma_f32_16x16x32_bf16 v[224:227], a[144:147], v[96:99], v[32:35]\n"
"ds_read_b64_tr_b16 a[242:243], v242 offset:29184\n"
"v_add_f32 v44, v44, v6\n"
"v_mfma_f32_16x16x32_bf16 v[224:227], a[148:151], v[100:103], v[224:227]\n"
"v_exp_f32 v15, v15\n"
"v_mfma_f32_16x16x32_bf16 v[224:227], a[152:155], v[104:107], v[224:227]\n"
"ds_read_b64_tr_b16 a[244:245], v243 offset:25088\n"
"v_cvt_pk_bf16_f32 v23, v14, v15\n"
"v_mfma_f32_16x16x32_bf16 v[224:227], a[156:159], v[108:111], v[224:227]\n"
"v_exp_f32 v220, v220\n"
"v_mfma_f32_16x16x32_bf16 a[32:35], a[192:195], v[20:23], a[32:35]\n"
"buffer_load_dwordx4 v246, s[12:15], s84 offen offset:2048 lds\n"
"v_mul_u32_u24 v203, 1152, v217\n"
"v_mfma_f32_16x16x32_bf16 a[36:39], a[196:199], v[20:23], a[36:39]\n"
"v_exp_f32 v221, v221\n"
"v_mfma_f32_16x16x32_bf16 a[40:43], a[200:203], v[20:23], a[40:43]\n"
"ds_read_b64_tr_b16 a[246:247], v243 offset:29184\n"
"v_cvt_pk_bf16_f32 v16, v220, v221\n"
"v_mfma_f32_16x16x32_bf16 a[44:47], a[204:207], v[20:23], a[44:47]\n"
"v_exp_f32 v222, v222\n"
"v_mfma_f32_16x16x32_bf16 a[48:51], a[208:211], v[20:23], a[48:51]\n"
"ds_read_b64_tr_b16 a[248:249], v242 offset:25344\n"
"v_add_f32 v44, v44, v7\n"
"v_mfma_f32_16x16x32_bf16 a[52:55], a[212:215], v[20:23], a[52:55]\n"
"v_exp_f32 v223, v223\n"
"v_mfma_f32_16x16x32_bf16 a[56:59], a[216:219], v[20:23], a[56:59]\n"
"ds_read_b64_tr_b16 a[250:251], v242 offset:29440\n"
"v_cvt_pk_bf16_f32 v17, v222, v223\n"
"v_mfma_f32_16x16x32_bf16 a[60:63], a[220:223], v[20:23], a[60:63]\n"
"v_exp_f32 v224, v224\n"
"v_mfma_f32_16x16x32_bf16 v[48:51], v[40:43], v[20:23], v[48:51]\n"
"ds_read_b64_tr_b16 a[252:253], v243 offset:25344\n"
"v_add_f32 v52, v52, v220\n"
"v_mfma_f32_16x16x32_bf16 v[228:231], a[128:131], v[112:115], v[36:39]\n"
"v_exp_f32 v225, v225\n"
"v_mfma_f32_16x16x32_bf16 v[228:231], a[132:135], v[116:119], v[228:231]\n"
"ds_read_b64_tr_b16 a[254:255], v243 offset:29440\n"
"v_cvt_pk_bf16_f32 v18, v224, v225\n"
"v_mfma_f32_16x16x32_bf16 v[228:231], a[136:139], v[120:123], v[228:231]\n"
"v_exp_f32 v226, v226\n"
"v_mfma_f32_16x16x32_bf16 v[228:231], a[140:143], v[124:127], v[228:231]\n"
"v_add_f32 v52, v52, v221\n"
"v_add_f32 v52, v52, v222\n"
"v_mfma_f32_16x16x32_bf16 v[232:235], a[144:147], v[112:115], v[36:39]\n"
"v_exp_f32 v227, v227\n"
"v_mfma_f32_16x16x32_bf16 v[232:235], a[148:151], v[116:119], v[232:235]\n"
"v_cvt_pk_bf16_f32 v19, v226, v227\n"
"v_add_f32 v52, v52, v223\n"
"v_mfma_f32_16x16x32_bf16 v[232:235], a[152:155], v[120:123], v[232:235]\n"
"buffer_load_dwordx4 v247, s[12:15], s84 offen offset:3072 lds\n"
"v_and_b32 v217, 15, v248\n"
"v_mfma_f32_16x16x32_bf16 v[232:235], a[156:159], v[124:127], v[232:235]\n"
"s_add_u32 s84, s84, 16384\n"
"v_exp_f32 v228, v228\n"
"v_mfma_f32_16x16x32_bf16 a[64:67], a[192:195], v[16:19], a[64:67]\n"
"v_add_f32 v52, v52, v224\n"
"v_add_f32 v52, v52, v225\n"
"v_mfma_f32_16x16x32_bf16 a[68:71], a[196:199], v[16:19], a[68:71]\n"
"v_exp_f32 v229, v229\n"
"v_mfma_f32_16x16x32_bf16 a[72:75], a[200:203], v[16:19], a[72:75]\n"
"v_cvt_pk_bf16_f32 v20, v228, v229\n"
"v_add_f32 v52, v52, v226\n"
"v_mfma_f32_16x16x32_bf16 a[76:79], a[204:207], v[16:19], a[76:79]\n"
"v_exp_f32 v230, v230\n"
"v_mfma_f32_16x16x32_bf16 a[80:83], a[208:211], v[16:19], a[80:83]\n"
"v_add_f32 v52, v52, v227\n"
"v_lshl_add_u32 v203, v217, 3, v203\n"
"v_mfma_f32_16x16x32_bf16 a[84:87], a[212:215], v[16:19], a[84:87]\n"
"v_exp_f32 v231, v231\n"
"v_mfma_f32_16x16x32_bf16 a[88:91], a[216:219], v[16:19], a[88:91]\n"
"s_waitcnt vmcnt(23) lgkmcnt(0)\n"
"s_barrier\n"
"v_cvt_pk_bf16_f32 v21, v230, v231\n"
"v_add_u32 v200, s59, v203\n"
"v_mfma_f32_16x16x32_bf16 a[92:95], a[220:223], v[16:19], a[92:95]\n"
"v_exp_f32 v232, v232\n"
"v_mfma_f32_16x16x32_bf16 v[0:3], a[160:163], v[64:67], v[24:27]\n"
"ds_read_b128 a[128:131], v240 offset:32768\n"
"v_add_u32 v203, s60, v203\n"
"v_mfma_f32_16x16x32_bf16 v[0:3], a[164:167], v[68:71], v[0:3]\n"
"v_exp_f32 v233, v233\n"
"v_mfma_f32_16x16x32_bf16 v[0:3], a[168:171], v[72:75], v[0:3]\n"
"ds_read_b128 a[132:135], v240 offset:33024\n"
"v_cvt_pk_bf16_f32 v22, v232, v233\n"
"v_mfma_f32_16x16x32_bf16 v[0:3], a[172:175], v[76:79], v[0:3]\n"
"v_exp_f32 v234, v234\n"
"v_mfma_f32_16x16x32_bf16 v[4:7], a[176:179], v[64:67], v[24:27]\n"
"ds_read_b128 a[136:139], v240 offset:33280\n"
"v_add_u32 v201, 65536, v200\n"
"v_mfma_f32_16x16x32_bf16 v[4:7], a[180:183], v[68:71], v[4:7]\n"
"v_exp_f32 v235, v235\n"
"v_mfma_f32_16x16x32_bf16 v[4:7], a[184:187], v[72:75], v[4:7]\n"
"ds_read_b128 a[140:143], v240 offset:33536\n"
"v_cvt_pk_bf16_f32 v23, v234, v235\n"
"v_mfma_f32_16x16x32_bf16 v[4:7], a[188:191], v[76:79], v[4:7]\n"
"s_mov_b32 m0, s51\n"
"v_exp_f32 v0, v0\n"
"v_mfma_f32_16x16x32_bf16 a[96:99], a[192:195], v[20:23], a[96:99]\n"
"buffer_load_dwordx4 v210, s[24:27], s20 offen lds\n"
"v_add_u32 v204, 65536, v203\n"
"v_mfma_f32_16x16x32_bf16 a[100:103], a[196:199], v[20:23], a[100:103]\n"
"v_exp_f32 v1, v1\n"
"v_mfma_f32_16x16x32_bf16 a[104:107], a[200:203], v[20:23], a[104:107]\n"
"ds_read_b128 a[144:147], v240 offset:36864\n"
"v_cvt_pk_bf16_f32 v16, v0, v1\n"
"v_mfma_f32_16x16x32_bf16 a[108:111], a[204:207], v[20:23], a[108:111]\n"
"v_exp_f32 v2, v2\n"
"v_mfma_f32_16x16x32_bf16 a[112:115], a[208:211], v[20:23], a[112:115]\n"
"ds_read_b128 a[148:151], v240 offset:37120\n"
"v_add_f32 v44, v44, v0\n"
"v_mfma_f32_16x16x32_bf16 a[116:119], a[212:215], v[20:23], a[116:119]\n"
"v_exp_f32 v3, v3\n"
"v_mfma_f32_16x16x32_bf16 a[120:123], a[216:219], v[20:23], a[120:123]\n"
"ds_read_b128 a[152:155], v240 offset:37376\n"
"v_cvt_pk_bf16_f32 v17, v2, v3\n"
"v_mfma_f32_16x16x32_bf16 a[124:127], a[220:223], v[20:23], a[124:127]\n"
"v_exp_f32 v4, v4\n"
"v_mfma_f32_16x16x32_bf16 v[56:59], v[40:43], v[20:23], v[56:59]\n"
"ds_read_b128 a[156:159], v240 offset:37632\n"
"v_add_f32 v44, v44, v1\n"
"v_mfma_f32_16x16x32_bf16 v[8:11], a[160:163], v[80:83], v[28:31]\n"
"v_exp_f32 v5, v5\n"
"v_mfma_f32_16x16x32_bf16 v[8:11], a[164:167], v[84:87], v[8:11]\n"
"ds_read_b64_tr_b16 a[192:193], v242 offset:49152\n"
"v_cvt_pk_bf16_f32 v18, v4, v5\n"
"v_mfma_f32_16x16x32_bf16 v[8:11], a[168:171], v[88:91], v[8:11]\n"
"v_exp_f32 v6, v6\n"
"v_mfma_f32_16x16x32_bf16 v[8:11], a[172:175], v[92:95], v[8:11]\n"
"ds_read_b64_tr_b16 a[194:195], v242 offset:53248\n"
"v_add_f32 v44, v44, v2\n"
"v_mfma_f32_16x16x32_bf16 v[12:15], a[176:179], v[80:83], v[28:31]\n"
"v_exp_f32 v7, v7\n"
"v_mfma_f32_16x16x32_bf16 v[12:15], a[180:183], v[84:87], v[12:15]\n"
"s_add_u32 m0, s51, 1024\n"
"ds_read_b64_tr_b16 a[196:197], v243 offset:49152\n"
"v_cvt_pk_bf16_f32 v19, v6, v7\n"
"v_mfma_f32_16x16x32_bf16 v[12:15], a[184:187], v[88:91], v[12:15]\n"
"buffer_load_dwordx4 v211, s[24:27], s20 offen lds\n"
"v_add_u32 v202, 131072, v200\n"
"v_mfma_f32_16x16x32_bf16 v[12:15], a[188:191], v[92:95], v[12:15]\n"
"s_waitcnt lgkmcnt(11)\n"
"v_exp_f32 v8, v8\n"
"v_mfma_f32_16x16x32_bf16 a[0:3], a[224:227], v[16:19], a[0:3]\n"
"ds_read_b64_tr_b16 a[198:199], v243 offset:53248\n"
"v_add_f32 v44, v44, v3\n"
"v_mfma_f32_16x16x32_bf16 a[4:7], a[228:231], v[16:19], a[4:7]\n"
"v_exp_f32 v9, v9\n"
"v_mfma_f32_16x16x32_bf16 a[8:11], a[232:235], v[16:19], a[8:11]\n"
"ds_read_b64_tr_b16 a[200:201], v242 offset:49408\n"
"v_cvt_pk_bf16_f32 v20, v8, v9\n"
"v_mfma_f32_16x16x32_bf16 a[12:15], a[236:239], v[16:19], a[12:15]\n"
"v_exp_f32 v10, v10\n"
"v_mfma_f32_16x16x32_bf16 a[16:19], a[240:243], v[16:19], a[16:19]\n"
"ds_read_b64_tr_b16 a[202:203], v242 offset:53504\n"
"v_add_f32 v44, v44, v4\n"
"v_mfma_f32_16x16x32_bf16 a[20:23], a[244:247], v[16:19], a[20:23]\n"
"v_exp_f32 v11, v11\n"
"v_mfma_f32_16x16x32_bf16 a[24:27], a[248:251], v[16:19], a[24:27]\n"
"ds_read_b64_tr_b16 a[204:205], v243 offset:49408\n"
"v_cvt_pk_bf16_f32 v21, v10, v11\n"
"v_mfma_f32_16x16x32_bf16 a[28:31], a[252:255], v[16:19], a[28:31]\n"
"v_exp_f32 v12, v12\n"
"v_mfma_f32_16x16x32_bf16 v[220:223], a[160:163], v[96:99], v[32:35]\n"
"ds_read_b64_tr_b16 a[206:207], v243 offset:53504\n"
"v_add_f32 v44, v44, v5\n"
"v_mfma_f32_16x16x32_bf16 v[220:223], a[164:167], v[100:103], v[220:223]\n"
"v_exp_f32 v13, v13\n"
"v_mfma_f32_16x16x32_bf16 v[220:223], a[168:171], v[104:107], v[220:223]\n"
"ds_read_b64_tr_b16 a[208:209], v242 offset:49664\n"
"v_cvt_pk_bf16_f32 v22, v12, v13\n"
"v_mfma_f32_16x16x32_bf16 v[220:223], a[172:175], v[108:111], v[220:223]\n"
"v_exp_f32 v14, v14\n"
"v_mfma_f32_16x16x32_bf16 v[224:227], a[176:179], v[96:99], v[32:35]\n"
"ds_read_b64_tr_b16 a[210:211], v242 offset:53760\n"
"v_add_f32 v44, v44, v6\n"
"v_mfma_f32_16x16x32_bf16 v[224:227], a[180:183], v[100:103], v[224:227]\n"
"v_exp_f32 v15, v15\n"
"v_mfma_f32_16x16x32_bf16 v[224:227], a[184:187], v[104:107], v[224:227]\n"
"ds_read_b64_tr_b16 a[212:213], v243 offset:49664\n"
"v_cvt_pk_bf16_f32 v23, v14, v15\n"
"v_mfma_f32_16x16x32_bf16 v[224:227], a[188:191], v[108:111], v[224:227]\n"
"s_add_u32 m0, s51, 2048\n"
"v_exp_f32 v220, v220\n"
"v_mfma_f32_16x16x32_bf16 a[32:35], a[224:227], v[20:23], a[32:35]\n"
"buffer_load_dwordx4 v212, s[24:27], s20 offen lds\n"
"v_add_u32 v205, 131072, v203\n"
"v_mfma_f32_16x16x32_bf16 a[36:39], a[228:231], v[20:23], a[36:39]\n"
"v_exp_f32 v221, v221\n"
"v_mfma_f32_16x16x32_bf16 a[40:43], a[232:235], v[20:23], a[40:43]\n"
"ds_read_b64_tr_b16 a[214:215], v243 offset:53760\n"
"v_cvt_pk_bf16_f32 v16, v220, v221\n"
"v_mfma_f32_16x16x32_bf16 a[44:47], a[236:239], v[20:23], a[44:47]\n"
"v_exp_f32 v222, v222\n"
"v_mfma_f32_16x16x32_bf16 a[48:51], a[240:243], v[20:23], a[48:51]\n"
"ds_read_b64_tr_b16 a[216:217], v242 offset:49920\n"
"v_add_f32 v44, v44, v7\n"
"v_mfma_f32_16x16x32_bf16 a[52:55], a[244:247], v[20:23], a[52:55]\n"
"v_exp_f32 v223, v223\n"
"v_mfma_f32_16x16x32_bf16 a[56:59], a[248:251], v[20:23], a[56:59]\n"
"ds_read_b64_tr_b16 a[218:219], v242 offset:54016\n"
"v_cvt_pk_bf16_f32 v17, v222, v223\n"
"v_mfma_f32_16x16x32_bf16 a[60:63], a[252:255], v[20:23], a[60:63]\n"
"v_exp_f32 v224, v224\n"
"v_mfma_f32_16x16x32_bf16 v[48:51], v[40:43], v[20:23], v[48:51]\n"
"ds_read_b64_tr_b16 a[220:221], v243 offset:49920\n"
"v_add_f32 v52, v52, v220\n"
"v_mfma_f32_16x16x32_bf16 v[228:231], a[160:163], v[112:115], v[36:39]\n"
"v_exp_f32 v225, v225\n"
"v_mfma_f32_16x16x32_bf16 v[228:231], a[164:167], v[116:119], v[228:231]\n"
"ds_read_b64_tr_b16 a[222:223], v243 offset:54016\n"
"v_cvt_pk_bf16_f32 v18, v224, v225\n"
"v_mfma_f32_16x16x32_bf16 v[228:231], a[168:171], v[120:123], v[228:231]\n"
"v_exp_f32 v226, v226\n"
"v_mfma_f32_16x16x32_bf16 v[228:231], a[172:175], v[124:127], v[228:231]\n"
"v_add_f32 v52, v52, v221\n"
"v_add_f32 v52, v52, v222\n"
"v_mfma_f32_16x16x32_bf16 v[232:235], a[176:179], v[112:115], v[36:39]\n"
"v_exp_f32 v227, v227\n"
"v_mfma_f32_16x16x32_bf16 v[232:235], a[180:183], v[116:119], v[232:235]\n"
"s_add_u32 m0, s51, 3072\n"
"v_cvt_pk_bf16_f32 v19, v226, v227\n"
"v_add_f32 v52, v52, v223\n"
"v_mfma_f32_16x16x32_bf16 v[232:235], a[184:187], v[120:123], v[232:235]\n"
"buffer_load_dwordx4 v213, s[24:27], s20 offen lds\n"
"v_lshlrev_b32 v207, 4, v248\n"
"v_mfma_f32_16x16x32_bf16 v[232:235], a[188:191], v[124:127], v[232:235]\n"
"v_exp_f32 v228, v228\n"
"v_mfma_f32_16x16x32_bf16 a[64:67], a[224:227], v[16:19], a[64:67]\n"
"v_add_f32 v52, v52, v224\n"
"v_add_f32 v52, v52, v225\n"
"v_mfma_f32_16x16x32_bf16 a[68:71], a[228:231], v[16:19], a[68:71]\n"
"v_exp_f32 v229, v229\n"
"v_mfma_f32_16x16x32_bf16 a[72:75], a[232:235], v[16:19], a[72:75]\n"
"v_cvt_pk_bf16_f32 v20, v228, v229\n"
"v_add_f32 v52, v52, v226\n"
"v_mfma_f32_16x16x32_bf16 a[76:79], a[236:239], v[16:19], a[76:79]\n"
"v_exp_f32 v230, v230\n"
"v_mfma_f32_16x16x32_bf16 a[80:83], a[240:243], v[16:19], a[80:83]\n"
"v_add_f32 v52, v52, v227\n"
"v_add_u32 v208, 18432, v207\n"
"v_mfma_f32_16x16x32_bf16 a[84:87], a[244:247], v[16:19], a[84:87]\n"
"s_add_u32 s62, s40, 163776\n"
"v_exp_f32 v231, v231\n"
"v_mfma_f32_16x16x32_bf16 a[88:91], a[248:251], v[16:19], a[88:91]\n"
"s_waitcnt lgkmcnt(8)\n"
"buffer_load_dwordx4 v[180:183], v252, s[68:71], s49 offen\n"
"v_cvt_pk_bf16_f32 v21, v230, v231\n"
"v_mfma_f32_16x16x32_bf16 a[92:95], a[252:255], v[16:19], a[92:95]\n"
"v_exp_f32 v232, v232\n"
"v_mfma_f32_16x16x32_bf16 v[0:3], a[128:131], v[64:67], v[24:27]\n"
"ds_read_b128 a[160:163], v240 offset:40960\n"
"v_and_b32 v217, 3, v248\n"
"v_mfma_f32_16x16x32_bf16 v[0:3], a[132:135], v[68:71], v[0:3]\n"
"v_exp_f32 v233, v233\n"
"v_mfma_f32_16x16x32_bf16 v[0:3], a[136:139], v[72:75], v[0:3]\n"
"ds_read_b128 a[164:167], v240 offset:41216\n"
"v_cvt_pk_bf16_f32 v22, v232, v233\n"
"v_mfma_f32_16x16x32_bf16 v[0:3], a[140:143], v[76:79], v[0:3]\n"
"v_exp_f32 v234, v234\n"
"v_mfma_f32_16x16x32_bf16 v[4:7], a[144:147], v[64:67], v[24:27]\n"
"ds_read_b128 a[168:171], v240 offset:41472\n"
"v_lshl_add_u32 v209, v217, 2, s62\n"
"v_mfma_f32_16x16x32_bf16 v[4:7], a[148:151], v[68:71], v[4:7]\n"
"s_lshl_b32 s55, s44, 4\n"
"v_exp_f32 v235, v235\n"
"v_mfma_f32_16x16x32_bf16 v[4:7], a[152:155], v[72:75], v[4:7]\n"
"s_add_u32 s55, s55, s43\n"
"ds_read_b128 a[172:175], v240 offset:41728\n"
"v_cvt_pk_bf16_f32 v23, v234, v235\n"
"v_mfma_f32_16x16x32_bf16 v[4:7], a[156:159], v[76:79], v[4:7]\n"
"s_add_u32 m0, s51, 4096\n"
"v_exp_f32 v0, v0\n"
"v_mfma_f32_16x16x32_bf16 a[96:99], a[224:227], v[20:23], a[96:99]\n"
"s_lshl_b32 s52, s55, 2\n"
"buffer_load_dwordx4 v214, s[24:27], s20 offen lds\n"
"v_mfma_f32_16x16x32_bf16 a[100:103], a[228:231], v[20:23], a[100:103]\n"
"s_add_u32 s52, s52, s41\n"
"v_exp_f32 v1, v1\n"
"v_mfma_f32_16x16x32_bf16 a[104:107], a[232:235], v[20:23], a[104:107]\n"
"s_lshl_b32 s52, s52, 14\n"
"ds_read_b128 a[176:179], v240 offset:45056\n"
"v_cvt_pk_bf16_f32 v16, v0, v1\n"
"v_mfma_f32_16x16x32_bf16 a[108:111], a[236:239], v[20:23], a[108:111]\n"
"s_add_u32 s52, s52, 9592832\n"
"v_exp_f32 v2, v2\n"
"v_mfma_f32_16x16x32_bf16 a[112:115], a[240:243], v[20:23], a[112:115]\n"
"s_add_u32 s49, s49, 9216\n"
"ds_read_b128 a[180:183], v240 offset:45312\n"
"v_add_f32 v44, v44, v0\n"
"v_mfma_f32_16x16x32_bf16 a[116:119], a[244:247], v[20:23], a[116:119]\n"
"v_exp_f32 v3, v3\n"
"v_mfma_f32_16x16x32_bf16 a[120:123], a[248:251], v[20:23], a[120:123]\n"
"ds_read_b128 a[184:187], v240 offset:45568\n"
"v_cvt_pk_bf16_f32 v17, v2, v3\n"
"v_mfma_f32_16x16x32_bf16 a[124:127], a[252:255], v[20:23], a[124:127]\n"
"v_exp_f32 v4, v4\n"
"v_mfma_f32_16x16x32_bf16 v[56:59], v[40:43], v[20:23], v[56:59]\n"
"ds_read_b128 a[188:191], v240 offset:45824\n"
"v_add_f32 v44, v44, v1\n"
"v_mfma_f32_16x16x32_bf16 v[8:11], a[128:131], v[80:83], v[28:31]\n"
"v_exp_f32 v5, v5\n"
"v_mfma_f32_16x16x32_bf16 v[8:11], a[132:135], v[84:87], v[8:11]\n"
"ds_read_b64_tr_b16 a[224:225], v242 offset:57344\n"
"v_cvt_pk_bf16_f32 v18, v4, v5\n"
"v_mfma_f32_16x16x32_bf16 v[8:11], a[136:139], v[88:91], v[8:11]\n"
"v_exp_f32 v6, v6\n"
"v_mfma_f32_16x16x32_bf16 v[8:11], a[140:143], v[92:95], v[8:11]\n"
"ds_read_b64_tr_b16 a[226:227], v242 offset:61440\n"
"v_add_f32 v44, v44, v2\n"
"v_mfma_f32_16x16x32_bf16 v[12:15], a[144:147], v[80:83], v[28:31]\n"
"v_exp_f32 v7, v7\n"
"v_mfma_f32_16x16x32_bf16 v[12:15], a[148:151], v[84:87], v[12:15]\n"
"s_add_u32 m0, s51, 5120\n"
"ds_read_b64_tr_b16 a[228:229], v243 offset:57344\n"
"buffer_load_dwordx4 v[148:151], v252, s[64:67], s49 offen\n"
"v_cvt_pk_bf16_f32 v19, v6, v7\n"
"v_mfma_f32_16x16x32_bf16 v[12:15], a[152:155], v[88:91], v[12:15]\n"
"buffer_load_dwordx4 v236, s[24:27], s20 offen lds\n"
"v_mfma_f32_16x16x32_bf16 v[12:15], a[156:159], v[92:95], v[12:15]\n"
"s_waitcnt lgkmcnt(11)\n"
"v_exp_f32 v8, v8\n"
"v_mfma_f32_16x16x32_bf16 a[0:3], a[192:195], v[16:19], a[0:3]\n"
"ds_read_b64_tr_b16 a[230:231], v243 offset:61440\n"
"v_add_f32 v44, v44, v3\n"
"v_mfma_f32_16x16x32_bf16 a[4:7], a[196:199], v[16:19], a[4:7]\n"
"v_exp_f32 v9, v9\n"
"v_mfma_f32_16x16x32_bf16 a[8:11], a[200:203], v[16:19], a[8:11]\n"
"ds_read_b64_tr_b16 a[232:233], v242 offset:57600\n"
"v_cvt_pk_bf16_f32 v20, v8, v9\n"
"v_mfma_f32_16x16x32_bf16 a[12:15], a[204:207], v[16:19], a[12:15]\n"
"v_exp_f32 v10, v10\n"
"v_mfma_f32_16x16x32_bf16 a[16:19], a[208:211], v[16:19], a[16:19]\n"
"ds_read_b64_tr_b16 a[234:235], v242 offset:61696\n"
"v_add_f32 v44, v44, v4\n"
"v_mfma_f32_16x16x32_bf16 a[20:23], a[212:215], v[16:19], a[20:23]\n"
"v_exp_f32 v11, v11\n"
"v_mfma_f32_16x16x32_bf16 a[24:27], a[216:219], v[16:19], a[24:27]\n"
"ds_read_b64_tr_b16 a[236:237], v243 offset:57600\n"
"v_cvt_pk_bf16_f32 v21, v10, v11\n"
"v_mfma_f32_16x16x32_bf16 a[28:31], a[220:223], v[16:19], a[28:31]\n"
"v_exp_f32 v12, v12\n"
"v_mfma_f32_16x16x32_bf16 v[220:223], a[128:131], v[96:99], v[32:35]\n"
"ds_read_b64_tr_b16 a[238:239], v243 offset:61696\n"
"v_add_f32 v44, v44, v5\n"
"v_mfma_f32_16x16x32_bf16 v[220:223], a[132:135], v[100:103], v[220:223]\n"
"v_exp_f32 v13, v13\n"
"v_mfma_f32_16x16x32_bf16 v[220:223], a[136:139], v[104:107], v[220:223]\n"
"ds_read_b64_tr_b16 a[240:241], v242 offset:57856\n"
"v_cvt_pk_bf16_f32 v22, v12, v13\n"
"v_mfma_f32_16x16x32_bf16 v[220:223], a[140:143], v[108:111], v[220:223]\n"
"v_exp_f32 v14, v14\n"
"v_mfma_f32_16x16x32_bf16 v[224:227], a[144:147], v[96:99], v[32:35]\n"
"ds_read_b64_tr_b16 a[242:243], v242 offset:61952\n"
"v_add_f32 v44, v44, v6\n"
"v_mfma_f32_16x16x32_bf16 v[224:227], a[148:151], v[100:103], v[224:227]\n"
"v_exp_f32 v15, v15\n"
"v_mfma_f32_16x16x32_bf16 v[224:227], a[152:155], v[104:107], v[224:227]\n"
"ds_read_b64_tr_b16 a[244:245], v243 offset:57856\n"
"v_cvt_pk_bf16_f32 v23, v14, v15\n"
"v_mfma_f32_16x16x32_bf16 v[224:227], a[156:159], v[108:111], v[224:227]\n"
"s_add_u32 m0, s51, 6144\n"
"v_exp_f32 v220, v220\n"
"v_mfma_f32_16x16x32_bf16 a[32:35], a[192:195], v[20:23], a[32:35]\n"
"buffer_load_dwordx4 v237, s[24:27], s20 offen lds\n"
"v_mfma_f32_16x16x32_bf16 a[36:39], a[196:199], v[20:23], a[36:39]\n"
"v_exp_f32 v221, v221\n"
"v_mfma_f32_16x16x32_bf16 a[40:43], a[200:203], v[20:23], a[40:43]\n"
"ds_read_b64_tr_b16 a[246:247], v243 offset:61952\n"
"v_cvt_pk_bf16_f32 v16, v220, v221\n"
"v_mfma_f32_16x16x32_bf16 a[44:47], a[204:207], v[20:23], a[44:47]\n"
"v_exp_f32 v222, v222\n"
"v_mfma_f32_16x16x32_bf16 a[48:51], a[208:211], v[20:23], a[48:51]\n"
"ds_read_b64_tr_b16 a[248:249], v242 offset:58112\n"
"v_add_f32 v44, v44, v7\n"
"v_mfma_f32_16x16x32_bf16 a[52:55], a[212:215], v[20:23], a[52:55]\n"
"v_exp_f32 v223, v223\n"
"v_mfma_f32_16x16x32_bf16 a[56:59], a[216:219], v[20:23], a[56:59]\n"
"ds_read_b64_tr_b16 a[250:251], v242 offset:62208\n"
"buffer_load_dwordx4 v[184:187], v252, s[68:71], s49 offen\n"
"v_cvt_pk_bf16_f32 v17, v222, v223\n"
"v_mfma_f32_16x16x32_bf16 a[60:63], a[220:223], v[20:23], a[60:63]\n"
"s_add_u32 s49, s49, 285696\n"
"v_exp_f32 v224, v224\n"
"v_mfma_f32_16x16x32_bf16 v[48:51], v[40:43], v[20:23], v[48:51]\n"
"ds_read_b64_tr_b16 a[252:253], v243 offset:58112\n"
"v_add_f32 v52, v52, v220\n"
"v_mfma_f32_16x16x32_bf16 v[228:231], a[128:131], v[112:115], v[36:39]\n"
"v_exp_f32 v225, v225\n"
"v_mfma_f32_16x16x32_bf16 v[228:231], a[132:135], v[116:119], v[228:231]\n"
"ds_read_b64_tr_b16 a[254:255], v243 offset:62208\n"
"v_cvt_pk_bf16_f32 v18, v224, v225\n"
"v_mfma_f32_16x16x32_bf16 v[228:231], a[136:139], v[120:123], v[228:231]\n"
"v_exp_f32 v226, v226\n"
"v_mfma_f32_16x16x32_bf16 v[228:231], a[140:143], v[124:127], v[228:231]\n"
"v_add_f32 v52, v52, v221\n"
"v_add_f32 v52, v52, v222\n"
"v_mfma_f32_16x16x32_bf16 v[232:235], a[144:147], v[112:115], v[36:39]\n"
"v_exp_f32 v227, v227\n"
"v_mfma_f32_16x16x32_bf16 v[232:235], a[148:151], v[116:119], v[232:235]\n"
"s_add_u32 m0, s51, 7168\n"
"v_cvt_pk_bf16_f32 v19, v226, v227\n"
"v_add_f32 v52, v52, v223\n"
"v_mfma_f32_16x16x32_bf16 v[232:235], a[152:155], v[120:123], v[232:235]\n"
"buffer_load_dwordx4 v238, s[24:27], s20 offen lds\n"
"v_mfma_f32_16x16x32_bf16 v[232:235], a[156:159], v[124:127], v[232:235]\n"
"v_exp_f32 v228, v228\n"
"v_mfma_f32_16x16x32_bf16 a[64:67], a[192:195], v[16:19], a[64:67]\n"
"v_add_f32 v52, v52, v224\n"
"v_add_f32 v52, v52, v225\n"
"v_mfma_f32_16x16x32_bf16 a[68:71], a[196:199], v[16:19], a[68:71]\n"
"v_exp_f32 v229, v229\n"
"v_mfma_f32_16x16x32_bf16 a[72:75], a[200:203], v[16:19], a[72:75]\n"
"v_cvt_pk_bf16_f32 v20, v228, v229\n"
"v_add_f32 v52, v52, v226\n"
"v_mfma_f32_16x16x32_bf16 a[76:79], a[204:207], v[16:19], a[76:79]\n"
"v_exp_f32 v230, v230\n"
"v_mfma_f32_16x16x32_bf16 a[80:83], a[208:211], v[16:19], a[80:83]\n"
"v_add_f32 v52, v52, v227\n"
"v_mfma_f32_16x16x32_bf16 a[84:87], a[212:215], v[16:19], a[84:87]\n"
"v_exp_f32 v231, v231\n"
"v_mfma_f32_16x16x32_bf16 a[88:91], a[216:219], v[16:19], a[88:91]\n"
"s_waitcnt vmcnt(23) lgkmcnt(0)\n"
"s_barrier\n"
"v_cvt_pk_bf16_f32 v21, v230, v231\n"
"v_mfma_f32_16x16x32_bf16 a[92:95], a[220:223], v[16:19], a[92:95]\n"
"v_exp_f32 v232, v232\n"
"v_mfma_f32_16x16x32_bf16 v[0:3], a[160:163], v[64:67], v[24:27]\n"
"ds_read_b128 a[128:131], v241 offset:0\n"
"v_mfma_f32_16x16x32_bf16 v[0:3], a[164:167], v[68:71], v[0:3]\n"
"v_exp_f32 v233, v233\n"
"v_mfma_f32_16x16x32_bf16 v[0:3], a[168:171], v[72:75], v[0:3]\n"
"ds_read_b128 a[132:135], v241 offset:256\n"
"v_cvt_pk_bf16_f32 v22, v232, v233\n"
"v_mfma_f32_16x16x32_bf16 v[0:3], a[172:175], v[76:79], v[0:3]\n"
"v_exp_f32 v234, v234\n"
"v_mfma_f32_16x16x32_bf16 v[4:7], a[176:179], v[64:67], v[24:27]\n"
"ds_read_b128 a[136:139], v241 offset:512\n"
"v_mfma_f32_16x16x32_bf16 v[4:7], a[180:183], v[68:71], v[4:7]\n"
"v_exp_f32 v235, v235\n"
"v_mfma_f32_16x16x32_bf16 v[4:7], a[184:187], v[72:75], v[4:7]\n"
"ds_read_b128 a[140:143], v241 offset:768\n"
"buffer_load_dwordx4 v[152:155], v252, s[64:67], s49 offen\n"
"v_cvt_pk_bf16_f32 v23, v234, v235\n"
"v_mfma_f32_16x16x32_bf16 v[4:7], a[188:191], v[76:79], v[4:7]\n"
"s_add_u32 m0, s51, 8192\n"
"v_exp_f32 v0, v0\n"
"v_mfma_f32_16x16x32_bf16 a[96:99], a[192:195], v[20:23], a[96:99]\n"
"buffer_load_dwordx4 v239, s[24:27], s20 offen lds\n"
"v_mfma_f32_16x16x32_bf16 a[100:103], a[196:199], v[20:23], a[100:103]\n"
"v_exp_f32 v1, v1\n"
"v_mfma_f32_16x16x32_bf16 a[104:107], a[200:203], v[20:23], a[104:107]\n"
"ds_read_b128 a[144:147], v241 offset:4096\n"
"v_cvt_pk_bf16_f32 v16, v0, v1\n"
"v_mfma_f32_16x16x32_bf16 a[108:111], a[204:207], v[20:23], a[108:111]\n"
"v_exp_f32 v2, v2\n"
"v_mfma_f32_16x16x32_bf16 a[112:115], a[208:211], v[20:23], a[112:115]\n"
"ds_read_b128 a[148:151], v241 offset:4352\n"
"v_add_f32 v44, v44, v0\n"
"v_mfma_f32_16x16x32_bf16 a[116:119], a[212:215], v[20:23], a[116:119]\n"
"v_exp_f32 v3, v3\n"
"v_mfma_f32_16x16x32_bf16 a[120:123], a[216:219], v[20:23], a[120:123]\n"
"ds_read_b128 a[152:155], v241 offset:4608\n"
"v_cvt_pk_bf16_f32 v17, v2, v3\n"
"v_mfma_f32_16x16x32_bf16 a[124:127], a[220:223], v[20:23], a[124:127]\n"
"v_exp_f32 v4, v4\n"
"v_mfma_f32_16x16x32_bf16 v[56:59], v[40:43], v[20:23], v[56:59]\n"
"ds_read_b128 a[156:159], v241 offset:4864\n"
"v_add_f32 v44, v44, v1\n"
"v_mfma_f32_16x16x32_bf16 v[8:11], a[160:163], v[80:83], v[28:31]\n"
"v_exp_f32 v5, v5\n"
"v_mfma_f32_16x16x32_bf16 v[8:11], a[164:167], v[84:87], v[8:11]\n"
"ds_read_b64_tr_b16 a[192:193], v244 offset:16384\n"
"v_cvt_pk_bf16_f32 v18, v4, v5\n"
"v_mfma_f32_16x16x32_bf16 v[8:11], a[168:171], v[88:91], v[8:11]\n"
"v_exp_f32 v6, v6\n"
"v_mfma_f32_16x16x32_bf16 v[8:11], a[172:175], v[92:95], v[8:11]\n"
"ds_read_b64_tr_b16 a[194:195], v244 offset:20480\n"
"v_add_f32 v44, v44, v2\n"
"v_mfma_f32_16x16x32_bf16 v[12:15], a[176:179], v[80:83], v[28:31]\n"
"v_exp_f32 v7, v7\n"
"v_mfma_f32_16x16x32_bf16 v[12:15], a[180:183], v[84:87], v[12:15]\n"
"s_add_u32 m0, s53, 36864\n"
"ds_read_b64_tr_b16 a[196:197], v245 offset:16384\n"
"v_cvt_pk_bf16_f32 v19, v6, v7\n"
"v_mfma_f32_16x16x32_bf16 v[12:15], a[184:187], v[88:91], v[12:15]\n"
"buffer_load_dwordx4 v210, s[24:27], s21 offen lds\n"
"v_mfma_f32_16x16x32_bf16 v[12:15], a[188:191], v[92:95], v[12:15]\n"
"s_waitcnt lgkmcnt(11)\n"
"v_exp_f32 v8, v8\n"
"v_mfma_f32_16x16x32_bf16 a[0:3], a[224:227], v[16:19], a[0:3]\n"
"ds_read_b64_tr_b16 a[198:199], v245 offset:20480\n"
"v_add_f32 v44, v44, v3\n"
"v_mfma_f32_16x16x32_bf16 a[4:7], a[228:231], v[16:19], a[4:7]\n"
"v_exp_f32 v9, v9\n"
"v_mfma_f32_16x16x32_bf16 a[8:11], a[232:235], v[16:19], a[8:11]\n"
"ds_read_b64_tr_b16 a[200:201], v244 offset:16640\n"
"v_cvt_pk_bf16_f32 v20, v8, v9\n"
"v_mfma_f32_16x16x32_bf16 a[12:15], a[236:239], v[16:19], a[12:15]\n"
"v_exp_f32 v10, v10\n"
"v_mfma_f32_16x16x32_bf16 a[16:19], a[240:243], v[16:19], a[16:19]\n"
"ds_read_b64_tr_b16 a[202:203], v244 offset:20736\n"
"v_add_f32 v44, v44, v4\n"
"v_mfma_f32_16x16x32_bf16 a[20:23], a[244:247], v[16:19], a[20:23]\n"
"v_exp_f32 v11, v11\n"
"v_mfma_f32_16x16x32_bf16 a[24:27], a[248:251], v[16:19], a[24:27]\n"
"ds_read_b64_tr_b16 a[204:205], v245 offset:16640\n"
"buffer_load_dwordx4 v[188:191], v252, s[68:71], s49 offen\n"
"v_cvt_pk_bf16_f32 v21, v10, v11\n"
"v_mfma_f32_16x16x32_bf16 a[28:31], a[252:255], v[16:19], a[28:31]\n"
"s_add_u32 s49, s49, 9216\n"
"v_exp_f32 v12, v12\n"
"v_mfma_f32_16x16x32_bf16 v[220:223], a[160:163], v[96:99], v[32:35]\n"
"ds_read_b64_tr_b16 a[206:207], v245 offset:20736\n"
"v_add_f32 v44, v44, v5\n"
"v_mfma_f32_16x16x32_bf16 v[220:223], a[164:167], v[100:103], v[220:223]\n"
"v_exp_f32 v13, v13\n"
"v_mfma_f32_16x16x32_bf16 v[220:223], a[168:171], v[104:107], v[220:223]\n"
"ds_read_b64_tr_b16 a[208:209], v244 offset:16896\n"
"v_cvt_pk_bf16_f32 v22, v12, v13\n"
"v_mfma_f32_16x16x32_bf16 v[220:223], a[172:175], v[108:111], v[220:223]\n"
"v_exp_f32 v14, v14\n"
"v_mfma_f32_16x16x32_bf16 v[224:227], a[176:179], v[96:99], v[32:35]\n"
"ds_read_b64_tr_b16 a[210:211], v244 offset:20992\n"
"v_add_f32 v44, v44, v6\n"
"v_mfma_f32_16x16x32_bf16 v[224:227], a[180:183], v[100:103], v[224:227]\n"
"v_exp_f32 v15, v15\n"
"v_mfma_f32_16x16x32_bf16 v[224:227], a[184:187], v[104:107], v[224:227]\n"
"ds_read_b64_tr_b16 a[212:213], v245 offset:16896\n"
"v_cvt_pk_bf16_f32 v23, v14, v15\n"
"v_mfma_f32_16x16x32_bf16 v[224:227], a[188:191], v[108:111], v[224:227]\n"
"s_add_u32 m0, s53, 37888\n"
"v_exp_f32 v220, v220\n"
"v_mfma_f32_16x16x32_bf16 a[32:35], a[224:227], v[20:23], a[32:35]\n"
"buffer_load_dwordx4 v211, s[24:27], s21 offen lds\n"
"v_mfma_f32_16x16x32_bf16 a[36:39], a[228:231], v[20:23], a[36:39]\n"
"v_exp_f32 v221, v221\n"
"v_mfma_f32_16x16x32_bf16 a[40:43], a[232:235], v[20:23], a[40:43]\n"
"ds_read_b64_tr_b16 a[214:215], v245 offset:20992\n"
"v_cvt_pk_bf16_f32 v16, v220, v221\n"
"v_mfma_f32_16x16x32_bf16 a[44:47], a[236:239], v[20:23], a[44:47]\n"
"v_exp_f32 v222, v222\n"
"v_mfma_f32_16x16x32_bf16 a[48:51], a[240:243], v[20:23], a[48:51]\n"
"ds_read_b64_tr_b16 a[216:217], v244 offset:17152\n"
"v_add_f32 v44, v44, v7\n"
"v_mfma_f32_16x16x32_bf16 a[52:55], a[244:247], v[20:23], a[52:55]\n"
"v_exp_f32 v223, v223\n"
"v_mfma_f32_16x16x32_bf16 a[56:59], a[248:251], v[20:23], a[56:59]\n"
"ds_read_b64_tr_b16 a[218:219], v244 offset:21248\n"
"v_cvt_pk_bf16_f32 v17, v222, v223\n"
"v_mfma_f32_16x16x32_bf16 a[60:63], a[252:255], v[20:23], a[60:63]\n"
"v_exp_f32 v224, v224\n"
"v_mfma_f32_16x16x32_bf16 v[48:51], v[40:43], v[20:23], v[48:51]\n"
"ds_read_b64_tr_b16 a[220:221], v245 offset:17152\n"
"v_add_f32 v52, v52, v220\n"
"v_mfma_f32_16x16x32_bf16 v[228:231], a[160:163], v[112:115], v[36:39]\n"
"v_exp_f32 v225, v225\n"
"v_mfma_f32_16x16x32_bf16 v[228:231], a[164:167], v[116:119], v[228:231]\n"
"ds_read_b64_tr_b16 a[222:223], v245 offset:21248\n"
"v_cvt_pk_bf16_f32 v18, v224, v225\n"
"v_mfma_f32_16x16x32_bf16 v[228:231], a[168:171], v[120:123], v[228:231]\n"
"v_exp_f32 v226, v226\n"
"v_mfma_f32_16x16x32_bf16 v[228:231], a[172:175], v[124:127], v[228:231]\n"
"v_add_f32 v52, v52, v221\n"
"v_add_f32 v52, v52, v222\n"
"v_mfma_f32_16x16x32_bf16 v[232:235], a[176:179], v[112:115], v[36:39]\n"
"v_exp_f32 v227, v227\n"
"v_mfma_f32_16x16x32_bf16 v[232:235], a[180:183], v[116:119], v[232:235]\n"
"s_add_u32 m0, s53, 38912\n"
"buffer_load_dwordx4 v[156:159], v252, s[64:67], s49 offen\n"
"v_cvt_pk_bf16_f32 v19, v226, v227\n"
"v_add_f32 v52, v52, v223\n"
"v_mfma_f32_16x16x32_bf16 v[232:235], a[184:187], v[120:123], v[232:235]\n"
"buffer_load_dwordx4 v212, s[24:27], s21 offen lds\n"
"v_mfma_f32_16x16x32_bf16 v[232:235], a[188:191], v[124:127], v[232:235]\n"
"v_exp_f32 v228, v228\n"
"v_mfma_f32_16x16x32_bf16 a[64:67], a[224:227], v[16:19], a[64:67]\n"
"v_add_f32 v52, v52, v224\n"
"v_add_f32 v52, v52, v225\n"
"v_mfma_f32_16x16x32_bf16 a[68:71], a[228:231], v[16:19], a[68:71]\n"
"v_exp_f32 v229, v229\n"
"v_mfma_f32_16x16x32_bf16 a[72:75], a[232:235], v[16:19], a[72:75]\n"
"v_cvt_pk_bf16_f32 v20, v228, v229\n"
"v_add_f32 v52, v52, v226\n"
"v_mfma_f32_16x16x32_bf16 a[76:79], a[236:239], v[16:19], a[76:79]\n"
"v_exp_f32 v230, v230\n"
"v_mfma_f32_16x16x32_bf16 a[80:83], a[240:243], v[16:19], a[80:83]\n"
"v_add_f32 v52, v52, v227\n"
"v_mfma_f32_16x16x32_bf16 a[84:87], a[244:247], v[16:19], a[84:87]\n"
"v_exp_f32 v231, v231\n"
"v_mfma_f32_16x16x32_bf16 a[88:91], a[248:251], v[16:19], a[88:91]\n"
"s_waitcnt lgkmcnt(8)\n"
"v_cvt_pk_bf16_f32 v21, v230, v231\n"
"v_mfma_f32_16x16x32_bf16 a[92:95], a[252:255], v[16:19], a[92:95]\n"
"v_exp_f32 v232, v232\n"
"v_mfma_f32_16x16x32_bf16 v[0:3], a[128:131], v[64:67], v[24:27]\n"
"ds_read_b128 a[160:163], v241 offset:8192\n"
"v_mfma_f32_16x16x32_bf16 v[0:3], a[132:135], v[68:71], v[0:3]\n"
"v_exp_f32 v233, v233\n"
"v_mfma_f32_16x16x32_bf16 v[0:3], a[136:139], v[72:75], v[0:3]\n"
"ds_read_b128 a[164:167], v241 offset:8448\n"
"v_cvt_pk_bf16_f32 v22, v232, v233\n"
"v_mfma_f32_16x16x32_bf16 v[0:3], a[140:143], v[76:79], v[0:3]\n"
"v_exp_f32 v234, v234\n"
"v_mfma_f32_16x16x32_bf16 v[4:7], a[144:147], v[64:67], v[24:27]\n"
"ds_read_b128 a[168:171], v241 offset:8704\n"
"v_mfma_f32_16x16x32_bf16 v[4:7], a[148:151], v[68:71], v[4:7]\n"
"v_exp_f32 v235, v235\n"
"v_mfma_f32_16x16x32_bf16 v[4:7], a[152:155], v[72:75], v[4:7]\n"
"ds_read_b128 a[172:175], v241 offset:8960\n"
"v_cvt_pk_bf16_f32 v23, v234, v235\n"
"v_mfma_f32_16x16x32_bf16 v[4:7], a[156:159], v[76:79], v[4:7]\n"
"s_add_u32 m0, s53, 39936\n"
"v_exp_f32 v0, v0\n"
"v_mfma_f32_16x16x32_bf16 a[96:99], a[224:227], v[20:23], a[96:99]\n"
"buffer_load_dwordx4 v213, s[24:27], s21 offen lds\n"
"v_mfma_f32_16x16x32_bf16 a[100:103], a[228:231], v[20:23], a[100:103]\n"
"v_exp_f32 v1, v1\n"
"v_mfma_f32_16x16x32_bf16 a[104:107], a[232:235], v[20:23], a[104:107]\n"
"ds_read_b128 a[176:179], v241 offset:12288\n"
"v_cvt_pk_bf16_f32 v16, v0, v1\n"
"v_mfma_f32_16x16x32_bf16 a[108:111], a[236:239], v[20:23], a[108:111]\n"
"v_exp_f32 v2, v2\n"
"v_mfma_f32_16x16x32_bf16 a[112:115], a[240:243], v[20:23], a[112:115]\n"
"ds_read_b128 a[180:183], v241 offset:12544\n"
"v_add_f32 v44, v44, v0\n"
"v_mfma_f32_16x16x32_bf16 a[116:119], a[244:247], v[20:23], a[116:119]\n"
"v_exp_f32 v3, v3\n"
"v_mfma_f32_16x16x32_bf16 a[120:123], a[248:251], v[20:23], a[120:123]\n"
"ds_read_b128 a[184:187], v241 offset:12800\n"
"buffer_load_dwordx4 v[192:195], v252, s[68:71], s49 offen\n"
"v_cvt_pk_bf16_f32 v17, v2, v3\n"
"v_mfma_f32_16x16x32_bf16 a[124:127], a[252:255], v[20:23], a[124:127]\n"
"s_add_u32 s49, s49, 285696\n"
"v_exp_f32 v4, v4\n"
"v_mfma_f32_16x16x32_bf16 v[56:59], v[40:43], v[20:23], v[56:59]\n"
"ds_read_b128 a[188:191], v241 offset:13056\n"
"v_add_f32 v44, v44, v1\n"
"v_mfma_f32_16x16x32_bf16 v[8:11], a[128:131], v[80:83], v[28:31]\n"
"v_exp_f32 v5, v5\n"
"v_mfma_f32_16x16x32_bf16 v[8:11], a[132:135], v[84:87], v[8:11]\n"
"ds_read_b64_tr_b16 a[224:225], v244 offset:24576\n"
"v_cvt_pk_bf16_f32 v18, v4, v5\n"
"v_mfma_f32_16x16x32_bf16 v[8:11], a[136:139], v[88:91], v[8:11]\n"
"v_exp_f32 v6, v6\n"
"v_mfma_f32_16x16x32_bf16 v[8:11], a[140:143], v[92:95], v[8:11]\n"
"ds_read_b64_tr_b16 a[226:227], v244 offset:28672\n"
"v_add_f32 v44, v44, v2\n"
"v_mfma_f32_16x16x32_bf16 v[12:15], a[144:147], v[80:83], v[28:31]\n"
"v_exp_f32 v7, v7\n"
"v_mfma_f32_16x16x32_bf16 v[12:15], a[148:151], v[84:87], v[12:15]\n"
"s_add_u32 m0, s53, 40960\n"
"ds_read_b64_tr_b16 a[228:229], v245 offset:24576\n"
"v_cvt_pk_bf16_f32 v19, v6, v7\n"
"v_mfma_f32_16x16x32_bf16 v[12:15], a[152:155], v[88:91], v[12:15]\n"
"buffer_load_dwordx4 v214, s[24:27], s21 offen lds\n"
"v_mfma_f32_16x16x32_bf16 v[12:15], a[156:159], v[92:95], v[12:15]\n"
"s_waitcnt lgkmcnt(11)\n"
"v_exp_f32 v8, v8\n"
"v_mfma_f32_16x16x32_bf16 a[0:3], a[192:195], v[16:19], a[0:3]\n"
"ds_read_b64_tr_b16 a[230:231], v245 offset:28672\n"
"v_add_f32 v44, v44, v3\n"
"v_mfma_f32_16x16x32_bf16 a[4:7], a[196:199], v[16:19], a[4:7]\n"
"v_exp_f32 v9, v9\n"
"v_mfma_f32_16x16x32_bf16 a[8:11], a[200:203], v[16:19], a[8:11]\n"
"ds_read_b64_tr_b16 a[232:233], v244 offset:24832\n"
"v_cvt_pk_bf16_f32 v20, v8, v9\n"
"v_mfma_f32_16x16x32_bf16 a[12:15], a[204:207], v[16:19], a[12:15]\n"
"v_exp_f32 v10, v10\n"
"v_mfma_f32_16x16x32_bf16 a[16:19], a[208:211], v[16:19], a[16:19]\n"
"ds_read_b64_tr_b16 a[234:235], v244 offset:28928\n"
"v_add_f32 v44, v44, v4\n"
"v_mfma_f32_16x16x32_bf16 a[20:23], a[212:215], v[16:19], a[20:23]\n"
"v_exp_f32 v11, v11\n"
"v_mfma_f32_16x16x32_bf16 a[24:27], a[216:219], v[16:19], a[24:27]\n"
"ds_read_b64_tr_b16 a[236:237], v245 offset:24832\n"
"v_cvt_pk_bf16_f32 v21, v10, v11\n"
"v_mfma_f32_16x16x32_bf16 a[28:31], a[220:223], v[16:19], a[28:31]\n"
"v_exp_f32 v12, v12\n"
"v_mfma_f32_16x16x32_bf16 v[220:223], a[128:131], v[96:99], v[32:35]\n"
"ds_read_b64_tr_b16 a[238:239], v245 offset:28928\n"
"v_add_f32 v44, v44, v5\n"
"v_mfma_f32_16x16x32_bf16 v[220:223], a[132:135], v[100:103], v[220:223]\n"
"v_exp_f32 v13, v13\n"
"v_mfma_f32_16x16x32_bf16 v[220:223], a[136:139], v[104:107], v[220:223]\n"
"ds_read_b64_tr_b16 a[240:241], v244 offset:25088\n"
"v_cvt_pk_bf16_f32 v22, v12, v13\n"
"v_mfma_f32_16x16x32_bf16 v[220:223], a[140:143], v[108:111], v[220:223]\n"
"v_exp_f32 v14, v14\n"
"v_mfma_f32_16x16x32_bf16 v[224:227], a[144:147], v[96:99], v[32:35]\n"
"ds_read_b64_tr_b16 a[242:243], v244 offset:29184\n"
"v_add_f32 v44, v44, v6\n"
"v_mfma_f32_16x16x32_bf16 v[224:227], a[148:151], v[100:103], v[224:227]\n"
"v_exp_f32 v15, v15\n"
"v_mfma_f32_16x16x32_bf16 v[224:227], a[152:155], v[104:107], v[224:227]\n"
"ds_read_b64_tr_b16 a[244:245], v245 offset:25088\n"
"buffer_load_dwordx4 v[160:163], v251, s[64:67], s49 offen\n"
"v_cvt_pk_bf16_f32 v23, v14, v15\n"
"v_mfma_f32_16x16x32_bf16 v[224:227], a[156:159], v[108:111], v[224:227]\n"
"s_add_u32 m0, s53, 41984\n"
"v_exp_f32 v220, v220\n"
"v_mfma_f32_16x16x32_bf16 a[32:35], a[192:195], v[20:23], a[32:35]\n"
"buffer_load_dwordx4 v236, s[24:27], s21 offen lds\n"
"v_mfma_f32_16x16x32_bf16 a[36:39], a[196:199], v[20:23], a[36:39]\n"
"v_exp_f32 v221, v221\n"
"v_mfma_f32_16x16x32_bf16 a[40:43], a[200:203], v[20:23], a[40:43]\n"
"ds_read_b64_tr_b16 a[246:247], v245 offset:29184\n"
"v_cvt_pk_bf16_f32 v16, v220, v221\n"
"v_mfma_f32_16x16x32_bf16 a[44:47], a[204:207], v[20:23], a[44:47]\n"
"v_exp_f32 v222, v222\n"
"v_mfma_f32_16x16x32_bf16 a[48:51], a[208:211], v[20:23], a[48:51]\n"
"ds_read_b64_tr_b16 a[248:249], v244 offset:25344\n"
"v_add_f32 v44, v44, v7\n"
"v_mfma_f32_16x16x32_bf16 a[52:55], a[212:215], v[20:23], a[52:55]\n"
"v_exp_f32 v223, v223\n"
"v_mfma_f32_16x16x32_bf16 a[56:59], a[216:219], v[20:23], a[56:59]\n"
"ds_read_b64_tr_b16 a[250:251], v244 offset:29440\n"
"v_cvt_pk_bf16_f32 v17, v222, v223\n"
"v_mfma_f32_16x16x32_bf16 a[60:63], a[220:223], v[20:23], a[60:63]\n"
"v_exp_f32 v224, v224\n"
"v_mfma_f32_16x16x32_bf16 v[48:51], v[40:43], v[20:23], v[48:51]\n"
"ds_read_b64_tr_b16 a[252:253], v245 offset:25344\n"
"v_add_f32 v52, v52, v220\n"
"v_mfma_f32_16x16x32_bf16 v[228:231], a[128:131], v[112:115], v[36:39]\n"
"v_exp_f32 v225, v225\n"
"v_mfma_f32_16x16x32_bf16 v[228:231], a[132:135], v[116:119], v[228:231]\n"
"ds_read_b64_tr_b16 a[254:255], v245 offset:29440\n"
"v_cvt_pk_bf16_f32 v18, v224, v225\n"
"v_mfma_f32_16x16x32_bf16 v[228:231], a[136:139], v[120:123], v[228:231]\n"
"v_exp_f32 v226, v226\n"
"v_mfma_f32_16x16x32_bf16 v[228:231], a[140:143], v[124:127], v[228:231]\n"
"v_add_f32 v52, v52, v221\n"
"v_add_f32 v52, v52, v222\n"
"v_mfma_f32_16x16x32_bf16 v[232:235], a[144:147], v[112:115], v[36:39]\n"
"v_exp_f32 v227, v227\n"
"v_mfma_f32_16x16x32_bf16 v[232:235], a[148:151], v[116:119], v[232:235]\n"
"s_add_u32 m0, s53, 43008\n"
"v_cvt_pk_bf16_f32 v19, v226, v227\n"
"v_add_f32 v52, v52, v223\n"
"v_mfma_f32_16x16x32_bf16 v[232:235], a[152:155], v[120:123], v[232:235]\n"
"buffer_load_dwordx4 v237, s[24:27], s21 offen lds\n"
"v_mfma_f32_16x16x32_bf16 v[232:235], a[156:159], v[124:127], v[232:235]\n"
"v_exp_f32 v228, v228\n"
"v_mfma_f32_16x16x32_bf16 a[64:67], a[192:195], v[16:19], a[64:67]\n"
"v_add_f32 v52, v52, v224\n"
"v_add_f32 v52, v52, v225\n"
"v_mfma_f32_16x16x32_bf16 a[68:71], a[196:199], v[16:19], a[68:71]\n"
"v_exp_f32 v229, v229\n"
"v_mfma_f32_16x16x32_bf16 a[72:75], a[200:203], v[16:19], a[72:75]\n"
"v_cvt_pk_bf16_f32 v20, v228, v229\n"
"v_add_f32 v52, v52, v226\n"
"v_mfma_f32_16x16x32_bf16 a[76:79], a[204:207], v[16:19], a[76:79]\n"
"v_exp_f32 v230, v230\n"
"v_mfma_f32_16x16x32_bf16 a[80:83], a[208:211], v[16:19], a[80:83]\n"
"v_add_f32 v52, v52, v227\n"
"v_mfma_f32_16x16x32_bf16 a[84:87], a[212:215], v[16:19], a[84:87]\n"
"v_exp_f32 v231, v231\n"
"v_mfma_f32_16x16x32_bf16 a[88:91], a[216:219], v[16:19], a[88:91]\n"
"s_waitcnt vmcnt(24) lgkmcnt(0)\n"
"s_barrier\n"
"buffer_load_dwordx4 v[196:199], v251, s[68:71], s49 offen\n"
"v_cvt_pk_bf16_f32 v21, v230, v231\n"
"v_mfma_f32_16x16x32_bf16 a[92:95], a[220:223], v[16:19], a[92:95]\n"
"s_lshl_b32 s62, s41, 6\n"
"v_exp_f32 v232, v232\n"
"v_mfma_f32_16x16x32_bf16 v[0:3], a[160:163], v[64:67], v[24:27]\n"
"ds_read_b128 a[128:131], v241 offset:32768\n"
"v_add_u32 v253, s62, v248\n"
"v_mfma_f32_16x16x32_bf16 v[0:3], a[164:167], v[68:71], v[0:3]\n"
"v_exp_f32 v233, v233\n"
"v_mfma_f32_16x16x32_bf16 v[0:3], a[168:171], v[72:75], v[0:3]\n"
"ds_read_b128 a[132:135], v241 offset:33024\n"
"v_cvt_pk_bf16_f32 v22, v232, v233\n"
"v_mfma_f32_16x16x32_bf16 v[0:3], a[172:175], v[76:79], v[0:3]\n"
"v_exp_f32 v234, v234\n"
"v_mfma_f32_16x16x32_bf16 v[4:7], a[176:179], v[64:67], v[24:27]\n"
"ds_read_b128 a[136:139], v241 offset:33280\n"
"v_mov_b32 v255, 0x10000000\n"
"v_mfma_f32_16x16x32_bf16 v[4:7], a[180:183], v[68:71], v[4:7]\n"
"v_exp_f32 v235, v235\n"
"v_mfma_f32_16x16x32_bf16 v[4:7], a[184:187], v[72:75], v[4:7]\n"
"ds_read_b128 a[140:143], v241 offset:33536\n"
"v_cvt_pk_bf16_f32 v23, v234, v235\n"
"v_mfma_f32_16x16x32_bf16 v[4:7], a[188:191], v[76:79], v[4:7]\n"
"s_add_u32 m0, s53, 44032\n"
"v_exp_f32 v0, v0\n"
"v_mfma_f32_16x16x32_bf16 a[96:99], a[192:195], v[20:23], a[96:99]\n"
"buffer_load_dwordx4 v238, s[24:27], s21 offen lds\n"
"v_cmp_gt_u32 vcc, 144, v253\n"
"v_mfma_f32_16x16x32_bf16 a[100:103], a[196:199], v[20:23], a[100:103]\n"
"v_exp_f32 v1, v1\n"
"v_mfma_f32_16x16x32_bf16 a[104:107], a[200:203], v[20:23], a[104:107]\n"
"ds_read_b128 a[144:147], v241 offset:36864\n"
"v_cvt_pk_bf16_f32 v16, v0, v1\n"
"v_mfma_f32_16x16x32_bf16 a[108:111], a[204:207], v[20:23], a[108:111]\n"
"v_exp_f32 v2, v2\n"
"v_mfma_f32_16x16x32_bf16 a[112:115], a[208:211], v[20:23], a[112:115]\n"
"ds_read_b128 a[148:151], v241 offset:37120\n"
"v_add_f32 v44, v44, v0\n"
"v_mfma_f32_16x16x32_bf16 a[116:119], a[212:215], v[20:23], a[116:119]\n"
"v_exp_f32 v3, v3\n"
"v_mfma_f32_16x16x32_bf16 a[120:123], a[216:219], v[20:23], a[120:123]\n"
"ds_read_b128 a[152:155], v241 offset:37376\n"
"v_cvt_pk_bf16_f32 v17, v2, v3\n"
"v_mfma_f32_16x16x32_bf16 a[124:127], a[220:223], v[20:23], a[124:127]\n"
"v_exp_f32 v4, v4\n"
"v_mfma_f32_16x16x32_bf16 v[56:59], v[40:43], v[20:23], v[56:59]\n"
"ds_read_b128 a[156:159], v241 offset:37632\n"
"v_add_f32 v44, v44, v1\n"
"v_mfma_f32_16x16x32_bf16 v[8:11], a[160:163], v[80:83], v[28:31]\n"
"v_exp_f32 v5, v5\n"
"v_mfma_f32_16x16x32_bf16 v[8:11], a[164:167], v[84:87], v[8:11]\n"
"ds_read_b64_tr_b16 a[192:193], v244 offset:49152\n"
"v_cvt_pk_bf16_f32 v18, v4, v5\n"
"v_mfma_f32_16x16x32_bf16 v[8:11], a[168:171], v[88:91], v[8:11]\n"
"v_exp_f32 v6, v6\n"
"v_mfma_f32_16x16x32_bf16 v[8:11], a[172:175], v[92:95], v[8:11]\n"
"ds_read_b64_tr_b16 a[194:195], v244 offset:53248\n"
"v_add_f32 v44, v44, v2\n"
"v_mfma_f32_16x16x32_bf16 v[12:15], a[176:179], v[80:83], v[28:31]\n"
"v_exp_f32 v7, v7\n"
"v_mfma_f32_16x16x32_bf16 v[12:15], a[180:183], v[84:87], v[12:15]\n"
"s_add_u32 m0, s53, 45056\n"
"ds_read_b64_tr_b16 a[196:197], v245 offset:49152\n"
"v_cvt_pk_bf16_f32 v19, v6, v7\n"
"v_mfma_f32_16x16x32_bf16 v[12:15], a[184:187], v[88:91], v[12:15]\n"
"buffer_load_dwordx4 v239, s[24:27], s21 offen lds\n"
"v_lshlrev_b32 v254, 3, v253\n"
"v_mfma_f32_16x16x32_bf16 v[12:15], a[188:191], v[92:95], v[12:15]\n"
"s_waitcnt lgkmcnt(11)\n"
"v_exp_f32 v8, v8\n"
"v_mfma_f32_16x16x32_bf16 a[0:3], a[224:227], v[16:19], a[0:3]\n"
"s_nop 0\n"
"ds_read_b64_tr_b16 a[198:199], v245 offset:53248\n"
"v_add_f32 v44, v44, v3\n"
"v_mfma_f32_16x16x32_bf16 a[4:7], a[228:231], v[16:19], a[4:7]\n"
"v_exp_f32 v9, v9\n"
"v_mfma_f32_16x16x32_bf16 a[8:11], a[232:235], v[16:19], a[8:11]\n"
"ds_read_b64_tr_b16 a[200:201], v244 offset:49408\n"
"v_cvt_pk_bf16_f32 v20, v8, v9\n"
"v_mfma_f32_16x16x32_bf16 a[12:15], a[236:239], v[16:19], a[12:15]\n"
"v_exp_f32 v10, v10\n"
"v_mfma_f32_16x16x32_bf16 a[16:19], a[240:243], v[16:19], a[16:19]\n"
"ds_read_b64_tr_b16 a[202:203], v244 offset:53504\n"
"v_add_f32 v44, v44, v4\n"
"v_mfma_f32_16x16x32_bf16 a[20:23], a[244:247], v[16:19], a[20:23]\n"
"v_exp_f32 v11, v11\n"
"v_mfma_f32_16x16x32_bf16 a[24:27], a[248:251], v[16:19], a[24:27]\n"
"ds_read_b64_tr_b16 a[204:205], v245 offset:49408\n"
"v_cvt_pk_bf16_f32 v21, v10, v11\n"
"v_mfma_f32_16x16x32_bf16 a[28:31], a[252:255], v[16:19], a[28:31]\n"
"v_exp_f32 v12, v12\n"
"v_mfma_f32_16x16x32_bf16 v[220:223], a[160:163], v[96:99], v[32:35]\n"
"ds_read_b64_tr_b16 a[206:207], v245 offset:53504\n"
"v_add_f32 v44, v44, v5\n"
"v_mfma_f32_16x16x32_bf16 v[220:223], a[164:167], v[100:103], v[220:223]\n"
"v_exp_f32 v13, v13\n"
"v_mfma_f32_16x16x32_bf16 v[220:223], a[168:171], v[104:107], v[220:223]\n"
"ds_read_b64_tr_b16 a[208:209], v244 offset:49664\n"
"v_cvt_pk_bf16_f32 v22, v12, v13\n"
"v_mfma_f32_16x16x32_bf16 v[220:223], a[172:175], v[108:111], v[220:223]\n"
"v_exp_f32 v14, v14\n"
"v_mfma_f32_16x16x32_bf16 v[224:227], a[176:179], v[96:99], v[32:35]\n"
"ds_read_b64_tr_b16 a[210:211], v244 offset:53760\n"
"v_add_f32 v44, v44, v6\n"
"v_mfma_f32_16x16x32_bf16 v[224:227], a[180:183], v[100:103], v[224:227]\n"
"v_exp_f32 v15, v15\n"
"v_mfma_f32_16x16x32_bf16 v[224:227], a[184:187], v[104:107], v[224:227]\n"
"ds_read_b64_tr_b16 a[212:213], v245 offset:49664\n"
"v_cvt_pk_bf16_f32 v23, v14, v15\n"
"v_mfma_f32_16x16x32_bf16 v[224:227], a[188:191], v[108:111], v[224:227]\n"
"s_add_u32 m0, s53, 73728\n"
"v_exp_f32 v220, v220\n"
"v_mfma_f32_16x16x32_bf16 a[32:35], a[224:227], v[20:23], a[32:35]\n"
"buffer_load_dwordx4 v210, s[24:27], s22 offen lds\n"
"v_cndmask_b32 v254, v255, v254, vcc\n"
"v_mfma_f32_16x16x32_bf16 a[36:39], a[228:231], v[20:23], a[36:39]\n"
"v_exp_f32 v221, v221\n"
"v_mfma_f32_16x16x32_bf16 a[40:43], a[232:235], v[20:23], a[40:43]\n"
"ds_read_b64_tr_b16 a[214:215], v245 offset:53760\n"
"v_cvt_pk_bf16_f32 v16, v220, v221\n"
"v_mfma_f32_16x16x32_bf16 a[44:47], a[236:239], v[20:23], a[44:47]\n"
"v_exp_f32 v222, v222\n"
"v_mfma_f32_16x16x32_bf16 a[48:51], a[240:243], v[20:23], a[48:51]\n"
"ds_read_b64_tr_b16 a[216:217], v244 offset:49920\n"
"v_add_f32 v44, v44, v7\n"
"v_mfma_f32_16x16x32_bf16 a[52:55], a[244:247], v[20:23], a[52:55]\n"
"v_exp_f32 v223, v223\n"
"v_mfma_f32_16x16x32_bf16 a[56:59], a[248:251], v[20:23], a[56:59]\n"
"ds_read_b64_tr_b16 a[218:219], v244 offset:54016\n"
"v_cvt_pk_bf16_f32 v17, v222, v223\n"
"v_mfma_f32_16x16x32_bf16 a[60:63], a[252:255], v[20:23], a[60:63]\n"
"v_exp_f32 v224, v224\n"
"v_mfma_f32_16x16x32_bf16 v[48:51], v[40:43], v[20:23], v[48:51]\n"
"ds_read_b64_tr_b16 a[220:221], v245 offset:49920\n"
"v_add_f32 v52, v52, v220\n"
"v_mfma_f32_16x16x32_bf16 v[228:231], a[160:163], v[112:115], v[36:39]\n"
"v_exp_f32 v225, v225\n"
"v_mfma_f32_16x16x32_bf16 v[228:231], a[164:167], v[116:119], v[228:231]\n"
"ds_read_b64_tr_b16 a[222:223], v245 offset:54016\n"
"v_cvt_pk_bf16_f32 v18, v224, v225\n"
"v_mfma_f32_16x16x32_bf16 v[228:231], a[168:171], v[120:123], v[228:231]\n"
"v_exp_f32 v226, v226\n"
"v_mfma_f32_16x16x32_bf16 v[228:231], a[172:175], v[124:127], v[228:231]\n"
"v_add_f32 v52, v52, v221\n"
"v_add_f32 v52, v52, v222\n"
"v_mfma_f32_16x16x32_bf16 v[232:235], a[176:179], v[112:115], v[36:39]\n"
"v_exp_f32 v227, v227\n"
"v_mfma_f32_16x16x32_bf16 v[232:235], a[180:183], v[116:119], v[232:235]\n"
"s_add_u32 m0, s53, 74752\n"
"v_cvt_pk_bf16_f32 v19, v226, v227\n"
"v_add_f32 v52, v52, v223\n"
"v_mfma_f32_16x16x32_bf16 v[232:235], a[184:187], v[120:123], v[232:235]\n"
"buffer_load_dwordx4 v211, s[24:27], s22 offen lds\n"
"v_lshlrev_b32 v216, 6, v248\n"
"v_mfma_f32_16x16x32_bf16 v[232:235], a[188:191], v[124:127], v[232:235]\n"
"v_exp_f32 v228, v228\n"
"v_mfma_f32_16x16x32_bf16 a[64:67], a[224:227], v[16:19], a[64:67]\n"
"v_add_f32 v52, v52, v224\n"
"v_add_f32 v52, v52, v225\n"
"v_mfma_f32_16x16x32_bf16 a[68:71], a[228:231], v[16:19], a[68:71]\n"
"v_exp_f32 v229, v229\n"
"v_mfma_f32_16x16x32_bf16 a[72:75], a[232:235], v[16:19], a[72:75]\n"
"v_cvt_pk_bf16_f32 v20, v228, v229\n"
"v_add_f32 v52, v52, v226\n"
"v_mfma_f32_16x16x32_bf16 a[76:79], a[236:239], v[16:19], a[76:79]\n"
"v_exp_f32 v230, v230\n"
"v_mfma_f32_16x16x32_bf16 a[80:83], a[240:243], v[16:19], a[80:83]\n"
"v_add_f32 v52, v52, v227\n"
"v_lshrrev_b32 v215, 3, v248\n"
"v_mfma_f32_16x16x32_bf16 a[84:87], a[244:247], v[16:19], a[84:87]\n"
"v_exp_f32 v231, v231\n"
"v_mfma_f32_16x16x32_bf16 a[88:91], a[248:251], v[16:19], a[88:91]\n"
"s_waitcnt lgkmcnt(8)\n"
"v_cvt_pk_bf16_f32 v21, v230, v231\n"
"v_mul_u32_u24 v215, 1152, v215\n"
"v_mfma_f32_16x16x32_bf16 a[92:95], a[252:255], v[16:19], a[92:95]\n"
"v_exp_f32 v232, v232\n"
"v_mfma_f32_16x16x32_bf16 v[0:3], a[128:131], v[64:67], v[24:27]\n"
"ds_read_b128 a[160:163], v241 offset:40960\n"
"v_and_b32 v217, 7, v248\n"
"v_mfma_f32_16x16x32_bf16 v[0:3], a[132:135], v[68:71], v[0:3]\n"
"v_exp_f32 v233, v233\n"
"v_mfma_f32_16x16x32_bf16 v[0:3], a[136:139], v[72:75], v[0:3]\n"
"ds_read_b128 a[164:167], v241 offset:41216\n"
"v_cvt_pk_bf16_f32 v22, v232, v233\n"
"v_mfma_f32_16x16x32_bf16 v[0:3], a[140:143], v[76:79], v[0:3]\n"
"v_exp_f32 v234, v234\n"
"v_mfma_f32_16x16x32_bf16 v[4:7], a[144:147], v[64:67], v[24:27]\n"
"ds_read_b128 a[168:171], v241 offset:41472\n"
"v_lshl_add_u32 v215, v217, 7, v215\n"
"v_mfma_f32_16x16x32_bf16 v[4:7], a[148:151], v[68:71], v[4:7]\n"
"s_and_b32 s46, s44, 7\n"
"v_exp_f32 v235, v235\n"
"v_mfma_f32_16x16x32_bf16 v[4:7], a[152:155], v[72:75], v[4:7]\n"
"s_lshl_b32 s46, s46, 2\n"
"ds_read_b128 a[172:175], v241 offset:41728\n"
"v_cvt_pk_bf16_f32 v23, v234, v235\n"
"v_mfma_f32_16x16x32_bf16 v[4:7], a[156:159], v[76:79], v[4:7]\n"
"s_add_u32 m0, s53, 75776\n"
"v_exp_f32 v0, v0\n"
"v_mfma_f32_16x16x32_bf16 a[96:99], a[224:227], v[20:23], a[96:99]\n"
"s_add_u32 s46, s46, s41\n"
"buffer_load_dwordx4 v212, s[24:27], s22 offen lds\n"
"v_mfma_f32_16x16x32_bf16 a[100:103], a[228:231], v[20:23], a[100:103]\n"
"s_mul_i32 s46, s46, 9216\n"
"v_exp_f32 v1, v1\n"
"v_mfma_f32_16x16x32_bf16 a[104:107], a[232:235], v[20:23], a[104:107]\n"
"s_mul_i32 s62, s43, 294912\n"
"ds_read_b128 a[176:179], v241 offset:45056\n"
"v_cvt_pk_bf16_f32 v16, v0, v1\n"
"v_mfma_f32_16x16x32_bf16 a[108:111], a[236:239], v[20:23], a[108:111]\n"
"s_add_u32 s46, s46, s62\n"
"v_exp_f32 v2, v2\n"
"v_mfma_f32_16x16x32_bf16 a[112:115], a[240:243], v[20:23], a[112:115]\n"
"ds_read_b128 a[180:183], v241 offset:45312\n"
"buffer_load_dword v250, v215, s[24:27], s46 offen offset:128\n"
"v_add_f32 v44, v44, v0\n"
"v_mfma_f32_16x16x32_bf16 a[116:119], a[244:247], v[20:23], a[116:119]\n"
"v_exp_f32 v3, v3\n"
"v_mfma_f32_16x16x32_bf16 a[120:123], a[248:251], v[20:23], a[120:123]\n"
"ds_read_b128 a[184:187], v241 offset:45568\n"
"v_cvt_pk_bf16_f32 v17, v2, v3\n"
"v_mfma_f32_16x16x32_bf16 a[124:127], a[252:255], v[20:23], a[124:127]\n"
"v_exp_f32 v4, v4\n"
"v_mfma_f32_16x16x32_bf16 v[56:59], v[40:43], v[20:23], v[56:59]\n"
"ds_read_b128 a[188:191], v241 offset:45824\n"
"v_add_f32 v44, v44, v1\n"
"v_mfma_f32_16x16x32_bf16 v[8:11], a[128:131], v[80:83], v[28:31]\n"
"v_exp_f32 v5, v5\n"
"v_mfma_f32_16x16x32_bf16 v[8:11], a[132:135], v[84:87], v[8:11]\n"
"ds_read_b64_tr_b16 a[224:225], v244 offset:57344\n"
"v_cvt_pk_bf16_f32 v18, v4, v5\n"
"v_mfma_f32_16x16x32_bf16 v[8:11], a[136:139], v[88:91], v[8:11]\n"
"v_exp_f32 v6, v6\n"
"v_mfma_f32_16x16x32_bf16 v[8:11], a[140:143], v[92:95], v[8:11]\n"
"ds_read_b64_tr_b16 a[226:227], v244 offset:61440\n"
"v_add_f32 v44, v44, v2\n"
"v_mfma_f32_16x16x32_bf16 v[12:15], a[144:147], v[80:83], v[28:31]\n"
"v_exp_f32 v7, v7\n"
"v_mfma_f32_16x16x32_bf16 v[12:15], a[148:151], v[84:87], v[12:15]\n"
"s_add_u32 m0, s53, 76800\n"
"ds_read_b64_tr_b16 a[228:229], v245 offset:57344\n"
"v_cvt_pk_bf16_f32 v19, v6, v7\n"
"v_mfma_f32_16x16x32_bf16 v[12:15], a[152:155], v[88:91], v[12:15]\n"
"buffer_load_dwordx4 v213, s[24:27], s22 offen lds\n"
"v_mfma_f32_16x16x32_bf16 v[12:15], a[156:159], v[92:95], v[12:15]\n"
"s_waitcnt lgkmcnt(11)\n"
"v_exp_f32 v8, v8\n"
"v_mfma_f32_16x16x32_bf16 a[0:3], a[192:195], v[16:19], a[0:3]\n"
"ds_read_b64_tr_b16 a[230:231], v245 offset:61440\n"
"v_add_f32 v44, v44, v3\n"
"v_mfma_f32_16x16x32_bf16 a[4:7], a[196:199], v[16:19], a[4:7]\n"
"v_exp_f32 v9, v9\n"
"v_mfma_f32_16x16x32_bf16 a[8:11], a[200:203], v[16:19], a[8:11]\n"
"ds_read_b64_tr_b16 a[232:233], v244 offset:57600\n"
"v_cvt_pk_bf16_f32 v20, v8, v9\n"
"v_mfma_f32_16x16x32_bf16 a[12:15], a[204:207], v[16:19], a[12:15]\n"
"v_exp_f32 v10, v10\n"
"v_mfma_f32_16x16x32_bf16 a[16:19], a[208:211], v[16:19], a[16:19]\n"
"ds_read_b64_tr_b16 a[234:235], v244 offset:61696\n"
"v_add_f32 v44, v44, v4\n"
"v_mfma_f32_16x16x32_bf16 a[20:23], a[212:215], v[16:19], a[20:23]\n"
"v_exp_f32 v11, v11\n"
"v_mfma_f32_16x16x32_bf16 a[24:27], a[216:219], v[16:19], a[24:27]\n"
"ds_read_b64_tr_b16 a[236:237], v245 offset:57600\n"
"v_cvt_pk_bf16_f32 v21, v10, v11\n"
"v_mfma_f32_16x16x32_bf16 a[28:31], a[220:223], v[16:19], a[28:31]\n"
"v_exp_f32 v12, v12\n"
"v_mfma_f32_16x16x32_bf16 v[220:223], a[128:131], v[96:99], v[32:35]\n"
"ds_read_b64_tr_b16 a[238:239], v245 offset:61696\n"
"v_add_f32 v44, v44, v5\n"
"v_mfma_f32_16x16x32_bf16 v[220:223], a[132:135], v[100:103], v[220:223]\n"
"v_exp_f32 v13, v13\n"
"v_mfma_f32_16x16x32_bf16 v[220:223], a[136:139], v[104:107], v[220:223]\n"
"ds_read_b64_tr_b16 a[240:241], v244 offset:57856\n"
"v_cvt_pk_bf16_f32 v22, v12, v13\n"
"v_mfma_f32_16x16x32_bf16 v[220:223], a[140:143], v[108:111], v[220:223]\n"
"v_exp_f32 v14, v14\n"
"v_mfma_f32_16x16x32_bf16 v[224:227], a[144:147], v[96:99], v[32:35]\n"
"ds_read_b64_tr_b16 a[242:243], v244 offset:61952\n"
"buffer_load_dword v250, v215, s[24:27], s46 offen offset:192\n"
"v_add_f32 v44, v44, v6\n"
"v_mfma_f32_16x16x32_bf16 v[224:227], a[148:151], v[100:103], v[224:227]\n"
"v_exp_f32 v15, v15\n"
"v_mfma_f32_16x16x32_bf16 v[224:227], a[152:155], v[104:107], v[224:227]\n"
"ds_read_b64_tr_b16 a[244:245], v245 offset:57856\n"
"v_cvt_pk_bf16_f32 v23, v14, v15\n"
"v_mfma_f32_16x16x32_bf16 v[224:227], a[156:159], v[108:111], v[224:227]\n"
"s_add_u32 m0, s53, 77824\n"
"v_exp_f32 v220, v220\n"
"v_mfma_f32_16x16x32_bf16 a[32:35], a[192:195], v[20:23], a[32:35]\n"
"buffer_load_dwordx4 v214, s[24:27], s22 offen lds\n"
"v_mfma_f32_16x16x32_bf16 a[36:39], a[196:199], v[20:23], a[36:39]\n"
"v_exp_f32 v221, v221\n"
"v_mfma_f32_16x16x32_bf16 a[40:43], a[200:203], v[20:23], a[40:43]\n"
"ds_read_b64_tr_b16 a[246:247], v245 offset:61952\n"
"v_cvt_pk_bf16_f32 v16, v220, v221\n"
"v_mfma_f32_16x16x32_bf16 a[44:47], a[204:207], v[20:23], a[44:47]\n"
"v_exp_f32 v222, v222\n"
"v_mfma_f32_16x16x32_bf16 a[48:51], a[208:211], v[20:23], a[48:51]\n"
"ds_read_b64_tr_b16 a[248:249], v244 offset:58112\n"
"v_add_f32 v44, v44, v7\n"
"v_mfma_f32_16x16x32_bf16 a[52:55], a[212:215], v[20:23], a[52:55]\n"
"v_exp_f32 v223, v223\n"
"v_mfma_f32_16x16x32_bf16 a[56:59], a[216:219], v[20:23], a[56:59]\n"
"ds_read_b64_tr_b16 a[250:251], v244 offset:62208\n"
"v_cvt_pk_bf16_f32 v17, v222, v223\n"
"v_mfma_f32_16x16x32_bf16 a[60:63], a[220:223], v[20:23], a[60:63]\n"
"v_exp_f32 v224, v224\n"
"v_mfma_f32_16x16x32_bf16 v[48:51], v[40:43], v[20:23], v[48:51]\n"
"ds_read_b64_tr_b16 a[252:253], v245 offset:58112\n"
"v_add_f32 v52, v52, v220\n"
"v_mfma_f32_16x16x32_bf16 v[228:231], a[128:131], v[112:115], v[36:39]\n"
"v_exp_f32 v225, v225\n"
"v_mfma_f32_16x16x32_bf16 v[228:231], a[132:135], v[116:119], v[228:231]\n"
"ds_read_b64_tr_b16 a[254:255], v245 offset:62208\n"
"v_cvt_pk_bf16_f32 v18, v224, v225\n"
"v_mfma_f32_16x16x32_bf16 v[228:231], a[136:139], v[120:123], v[228:231]\n"
"v_exp_f32 v226, v226\n"
"v_mfma_f32_16x16x32_bf16 v[228:231], a[140:143], v[124:127], v[228:231]\n"
"v_add_f32 v52, v52, v221\n"
"v_add_f32 v52, v52, v222\n"
"v_mfma_f32_16x16x32_bf16 v[232:235], a[144:147], v[112:115], v[36:39]\n"
"v_exp_f32 v227, v227\n"
"v_mfma_f32_16x16x32_bf16 v[232:235], a[148:151], v[116:119], v[232:235]\n"
"s_add_u32 m0, s53, 78848\n"
"v_cvt_pk_bf16_f32 v19, v226, v227\n"
"v_add_f32 v52, v52, v223\n"
"v_mfma_f32_16x16x32_bf16 v[232:235], a[152:155], v[120:123], v[232:235]\n"
"buffer_load_dwordx4 v236, s[24:27], s22 offen lds\n"
"v_mfma_f32_16x16x32_bf16 v[232:235], a[156:159], v[124:127], v[232:235]\n"
"v_exp_f32 v228, v228\n"
"v_mfma_f32_16x16x32_bf16 a[64:67], a[192:195], v[16:19], a[64:67]\n"
"v_add_f32 v52, v52, v224\n"
"v_add_f32 v52, v52, v225\n"
"v_mfma_f32_16x16x32_bf16 a[68:71], a[196:199], v[16:19], a[68:71]\n"
"v_exp_f32 v229, v229\n"
"v_mfma_f32_16x16x32_bf16 a[72:75], a[200:203], v[16:19], a[72:75]\n"
"v_cvt_pk_bf16_f32 v20, v228, v229\n"
"v_add_f32 v52, v52, v226\n"
"v_mfma_f32_16x16x32_bf16 a[76:79], a[204:207], v[16:19], a[76:79]\n"
"v_exp_f32 v230, v230\n"
"v_mfma_f32_16x16x32_bf16 a[80:83], a[208:211], v[16:19], a[80:83]\n"
"v_add_f32 v52, v52, v227\n"
"v_mfma_f32_16x16x32_bf16 a[84:87], a[212:215], v[16:19], a[84:87]\n"
"v_exp_f32 v231, v231\n"
"v_mfma_f32_16x16x32_bf16 a[88:91], a[216:219], v[16:19], a[88:91]\n"
"s_waitcnt vmcnt(22) lgkmcnt(0)\n"
"s_barrier\n"
"v_cvt_pk_bf16_f32 v21, v230, v231\n"
"v_mfma_f32_16x16x32_bf16 a[92:95], a[220:223], v[16:19], a[92:95]\n"
"v_exp_f32 v232, v232\n"
"v_mfma_f32_16x16x32_bf16 v[0:3], a[160:163], v[64:67], v[24:27]\n"
"ds_read_b64_tr_b16 a[128:129], v202 offset:0\n"
"v_mfma_f32_16x16x32_bf16 v[0:3], a[164:167], v[68:71], v[0:3]\n"
"v_exp_f32 v233, v233\n"
"v_mfma_f32_16x16x32_bf16 v[0:3], a[168:171], v[72:75], v[0:3]\n"
"ds_read_b64_tr_b16 a[130:131], v202 offset:4608\n"
"v_cvt_pk_bf16_f32 v22, v232, v233\n"
"v_mfma_f32_16x16x32_bf16 v[0:3], a[172:175], v[76:79], v[0:3]\n"
"v_exp_f32 v234, v234\n"
"v_mfma_f32_16x16x32_bf16 v[4:7], a[176:179], v[64:67], v[24:27]\n"
"ds_read_b64_tr_b16 a[132:133], v202 offset:128\n"
"v_mfma_f32_16x16x32_bf16 v[4:7], a[180:183], v[68:71], v[4:7]\n"
"v_exp_f32 v235, v235\n"
"v_mfma_f32_16x16x32_bf16 v[4:7], a[184:187], v[72:75], v[4:7]\n"
"ds_read_b64_tr_b16 a[134:135], v202 offset:4736\n"
"v_cvt_pk_bf16_f32 v23, v234, v235\n"
"v_mfma_f32_16x16x32_bf16 v[4:7], a[188:191], v[76:79], v[4:7]\n"
"s_add_u32 m0, s53, 79872\n"
"v_exp_f32 v0, v0\n"
"v_mfma_f32_16x16x32_bf16 a[96:99], a[192:195], v[20:23], a[96:99]\n"
"buffer_load_dwordx4 v237, s[24:27], s22 offen lds\n"
"v_mfma_f32_16x16x32_bf16 a[100:103], a[196:199], v[20:23], a[100:103]\n"
"v_exp_f32 v1, v1\n"
"v_mfma_f32_16x16x32_bf16 a[104:107], a[200:203], v[20:23], a[104:107]\n"
"ds_read_b64_tr_b16 a[136:137], v202 offset:256\n"
"v_cvt_pk_bf16_f32 v16, v0, v1\n"
"v_mfma_f32_16x16x32_bf16 a[108:111], a[204:207], v[20:23], a[108:111]\n"
"v_exp_f32 v2, v2\n"
"v_mfma_f32_16x16x32_bf16 a[112:115], a[208:211], v[20:23], a[112:115]\n"
"ds_read_b64_tr_b16 a[138:139], v202 offset:4864\n"
"v_add_f32 v44, v44, v0\n"
"v_mfma_f32_16x16x32_bf16 a[116:119], a[212:215], v[20:23], a[116:119]\n"
"v_exp_f32 v3, v3\n"
"v_mfma_f32_16x16x32_bf16 a[120:123], a[216:219], v[20:23], a[120:123]\n"
"ds_read_b64_tr_b16 a[140:141], v202 offset:384\n"
"v_cvt_pk_bf16_f32 v17, v2, v3\n"
"v_mfma_f32_16x16x32_bf16 a[124:127], a[220:223], v[20:23], a[124:127]\n"
"v_exp_f32 v4, v4\n"
"v_mfma_f32_16x16x32_bf16 v[56:59], v[40:43], v[20:23], v[56:59]\n"
"ds_read_b64_tr_b16 a[142:143], v202 offset:4992\n"
"v_add_f32 v44, v44, v1\n"
"v_mfma_f32_16x16x32_bf16 v[8:11], a[160:163], v[80:83], v[28:31]\n"
"v_exp_f32 v5, v5\n"
"v_mfma_f32_16x16x32_bf16 v[8:11], a[164:167], v[84:87], v[8:11]\n"
"ds_read_b64_tr_b16 a[144:145], v202 offset:512\n"
"v_cvt_pk_bf16_f32 v18, v4, v5\n"
"v_mfma_f32_16x16x32_bf16 v[8:11], a[168:171], v[88:91], v[8:11]\n"
"v_exp_f32 v6, v6\n"
"v_mfma_f32_16x16x32_bf16 v[8:11], a[172:175], v[92:95], v[8:11]\n"
"ds_read_b64_tr_b16 a[146:147], v202 offset:5120\n"
"v_add_f32 v44, v44, v2\n"
"v_mfma_f32_16x16x32_bf16 v[12:15], a[176:179], v[80:83], v[28:31]\n"
"v_exp_f32 v7, v7\n"
"v_mfma_f32_16x16x32_bf16 v[12:15], a[180:183], v[84:87], v[12:15]\n"
"s_add_u32 m0, s53, 80896\n"
"ds_read_b64_tr_b16 a[148:149], v202 offset:640\n"
"v_cvt_pk_bf16_f32 v19, v6, v7\n"
"v_mfma_f32_16x16x32_bf16 v[12:15], a[184:187], v[88:91], v[12:15]\n"
"buffer_load_dwordx4 v238, s[24:27], s22 offen lds\n"
"v_mfma_f32_16x16x32_bf16 v[12:15], a[188:191], v[92:95], v[12:15]\n"
"v_exp_f32 v8, v8\n"
"v_mfma_f32_16x16x32_bf16 a[0:3], a[224:227], v[16:19], a[0:3]\n"
"ds_read_b64_tr_b16 a[150:151], v202 offset:5248\n"
"v_add_f32 v44, v44, v3\n"
"v_mfma_f32_16x16x32_bf16 a[4:7], a[228:231], v[16:19], a[4:7]\n"
"v_exp_f32 v9, v9\n"
"v_mfma_f32_16x16x32_bf16 a[8:11], a[232:235], v[16:19], a[8:11]\n"
"ds_read_b64_tr_b16 a[152:153], v202 offset:768\n"
"v_cvt_pk_bf16_f32 v20, v8, v9\n"
"v_mfma_f32_16x16x32_bf16 a[12:15], a[236:239], v[16:19], a[12:15]\n"
"v_exp_f32 v10, v10\n"
"v_mfma_f32_16x16x32_bf16 a[16:19], a[240:243], v[16:19], a[16:19]\n"
"ds_read_b64_tr_b16 a[154:155], v202 offset:5376\n"
"v_add_f32 v44, v44, v4\n"
"v_mfma_f32_16x16x32_bf16 a[20:23], a[244:247], v[16:19], a[20:23]\n"
"v_exp_f32 v11, v11\n"
"v_mfma_f32_16x16x32_bf16 a[24:27], a[248:251], v[16:19], a[24:27]\n"
"ds_read_b64_tr_b16 a[156:157], v202 offset:896\n"
"v_cvt_pk_bf16_f32 v21, v10, v11\n"
"v_mfma_f32_16x16x32_bf16 a[28:31], a[252:255], v[16:19], a[28:31]\n"
"v_exp_f32 v12, v12\n"
"v_mfma_f32_16x16x32_bf16 v[220:223], a[160:163], v[96:99], v[32:35]\n"
"ds_read_b64_tr_b16 a[158:159], v202 offset:5504\n"
"v_add_f32 v44, v44, v5\n"
"v_mfma_f32_16x16x32_bf16 v[220:223], a[164:167], v[100:103], v[220:223]\n"
"v_exp_f32 v13, v13\n"
"v_mfma_f32_16x16x32_bf16 v[220:223], a[168:171], v[104:107], v[220:223]\n"
"ds_read_b64_tr_b16 a[192:193], v205 offset:0\n"
"v_cvt_pk_bf16_f32 v22, v12, v13\n"
"v_mfma_f32_16x16x32_bf16 v[220:223], a[172:175], v[108:111], v[220:223]\n"
"v_exp_f32 v14, v14\n"
"v_mfma_f32_16x16x32_bf16 v[224:227], a[176:179], v[96:99], v[32:35]\n"
"ds_read_b64_tr_b16 a[194:195], v205 offset:4608\n"
"v_add_f32 v44, v44, v6\n"
"v_mfma_f32_16x16x32_bf16 v[224:227], a[180:183], v[100:103], v[224:227]\n"
"v_exp_f32 v15, v15\n"
"v_mfma_f32_16x16x32_bf16 v[224:227], a[184:187], v[104:107], v[224:227]\n"
"ds_read_b64_tr_b16 a[196:197], v202 offset:9216\n"
"v_cvt_pk_bf16_f32 v23, v14, v15\n"
"v_mfma_f32_16x16x32_bf16 v[224:227], a[188:191], v[108:111], v[224:227]\n"
"s_add_u32 m0, s53, 81920\n"
"v_exp_f32 v220, v220\n"
"v_mfma_f32_16x16x32_bf16 a[32:35], a[224:227], v[20:23], a[32:35]\n"
"buffer_load_dwordx4 v239, s[24:27], s22 offen lds\n"
"v_mfma_f32_16x16x32_bf16 a[36:39], a[228:231], v[20:23], a[36:39]\n"
"v_exp_f32 v221, v221\n"
"v_mfma_f32_16x16x32_bf16 a[40:43], a[232:235], v[20:23], a[40:43]\n"
"ds_read_b64_tr_b16 a[198:199], v202 offset:13824\n"
"v_cvt_pk_bf16_f32 v16, v220, v221\n"
"v_mfma_f32_16x16x32_bf16 a[44:47], a[236:239], v[20:23], a[44:47]\n"
"v_exp_f32 v222, v222\n"
"v_mfma_f32_16x16x32_bf16 a[48:51], a[240:243], v[20:23], a[48:51]\n"
"v_add_f32 v44, v44, v7\n"
"v_mfma_f32_16x16x32_bf16 a[52:55], a[244:247], v[20:23], a[52:55]\n"
"v_exp_f32 v223, v223\n"
"v_mfma_f32_16x16x32_bf16 a[56:59], a[248:251], v[20:23], a[56:59]\n"
"v_cvt_pk_bf16_f32 v17, v222, v223\n"
"v_mfma_f32_16x16x32_bf16 a[60:63], a[252:255], v[20:23], a[60:63]\n"
"v_exp_f32 v224, v224\n"
"v_mfma_f32_16x16x32_bf16 v[48:51], v[40:43], v[20:23], v[48:51]\n"
"v_add_f32 v52, v52, v220\n"
"v_mfma_f32_16x16x32_bf16 v[228:231], a[160:163], v[112:115], v[36:39]\n"
"v_exp_f32 v225, v225\n"
"v_mfma_f32_16x16x32_bf16 v[228:231], a[164:167], v[116:119], v[228:231]\n"
"v_cvt_pk_bf16_f32 v18, v224, v225\n"
"v_mfma_f32_16x16x32_bf16 v[228:231], a[168:171], v[120:123], v[228:231]\n"
"v_exp_f32 v226, v226\n"
"v_mfma_f32_16x16x32_bf16 v[228:231], a[172:175], v[124:127], v[228:231]\n"
"v_add_f32 v52, v52, v221\n"
"v_add_f32 v52, v52, v222\n"
"v_mfma_f32_16x16x32_bf16 v[232:235], a[176:179], v[112:115], v[36:39]\n"
"v_exp_f32 v227, v227\n"
"v_mfma_f32_16x16x32_bf16 v[232:235], a[180:183], v[116:119], v[232:235]\n"
"s_add_u32 m0, s53, 110592\n"
"v_cvt_pk_bf16_f32 v19, v226, v227\n"
"v_add_f32 v52, v52, v223\n"
"v_mfma_f32_16x16x32_bf16 v[232:235], a[184:187], v[120:123], v[232:235]\n"
"buffer_load_dwordx4 v210, s[24:27], s23 offen lds\n"
"v_mfma_f32_16x16x32_bf16 v[232:235], a[188:191], v[124:127], v[232:235]\n"
"v_exp_f32 v228, v228\n"
"v_mfma_f32_16x16x32_bf16 a[64:67], a[224:227], v[16:19], a[64:67]\n"
"v_add_f32 v52, v52, v224\n"
"v_add_f32 v52, v52, v225\n"
"v_mfma_f32_16x16x32_bf16 a[68:71], a[228:231], v[16:19], a[68:71]\n"
"v_exp_f32 v229, v229\n"
"v_mfma_f32_16x16x32_bf16 a[72:75], a[232:235], v[16:19], a[72:75]\n"
"v_cvt_pk_bf16_f32 v20, v228, v229\n"
"v_add_f32 v52, v52, v226\n"
"v_mfma_f32_16x16x32_bf16 a[76:79], a[236:239], v[16:19], a[76:79]\n"
"v_exp_f32 v230, v230\n"
"v_mfma_f32_16x16x32_bf16 a[80:83], a[240:243], v[16:19], a[80:83]\n"
"v_add_f32 v52, v52, v227\n"
"v_mfma_f32_16x16x32_bf16 a[84:87], a[244:247], v[16:19], a[84:87]\n"
"v_exp_f32 v231, v231\n"
"v_mfma_f32_16x16x32_bf16 a[88:91], a[248:251], v[16:19], a[88:91]\n"
"v_cvt_pk_bf16_f32 v21, v230, v231\n"
"v_mfma_f32_16x16x32_bf16 a[92:95], a[252:255], v[16:19], a[92:95]\n"
"v_exp_f32 v232, v232\n"
"v_exp_f32 v233, v233\n"
"s_nop 0\n"
"v_cvt_pk_bf16_f32 v22, v232, v233\n"
"s_nop 0\n"
"v_exp_f32 v234, v234\n"
"s_nop 0\n"
"v_exp_f32 v235, v235\n"
"s_nop 0\n"
"v_cvt_pk_bf16_f32 v23, v234, v235\n"
"s_nop 0\n"
"s_nop 1\n"
"v_mfma_f32_16x16x32_bf16 a[96:99], a[224:227], v[20:23], a[96:99]\n"
"v_mfma_f32_16x16x32_bf16 a[100:103], a[228:231], v[20:23], a[100:103]\n"
"v_mfma_f32_16x16x32_bf16 a[104:107], a[232:235], v[20:23], a[104:107]\n"
"v_mfma_f32_16x16x32_bf16 a[108:111], a[236:239], v[20:23], a[108:111]\n"
"v_mfma_f32_16x16x32_bf16 a[112:115], a[240:243], v[20:23], a[112:115]\n"
"v_mfma_f32_16x16x32_bf16 a[116:119], a[244:247], v[20:23], a[116:119]\n"
"v_mfma_f32_16x16x32_bf16 a[120:123], a[248:251], v[20:23], a[120:123]\n"
"v_mfma_f32_16x16x32_bf16 a[124:127], a[252:255], v[20:23], a[124:127]\n"
"v_mfma_f32_16x16x32_bf16 v[56:59], v[40:43], v[20:23], v[56:59]\n"
"s_nop 7\n"
"v_mov_b32 v0, v44\n"
"v_mov_b32 v1, v52\n"
"s_nop 0\n"
"v_permlane16_swap_b32 v44, v0\n"
"v_permlane16_swap_b32 v52, v1\n"
"v_add_f32 v44, v44, v0\n"
"v_add_f32 v52, v52, v1\n"
"v_mov_b32 v0, v44\n"
"v_mov_b32 v1, v52\n"
"s_nop 0\n"
"v_permlane32_swap_b32 v44, v0\n"
"v_permlane32_swap_b32 v52, v1\n"
"v_add_f32 v44, v44, v0\n"
"v_add_f32 v52, v52, v1\n"
"v_mov_b32 v219, 0x5f800000\n"
"v_cmp_nle_f32 s[56:57], v44, v219\n"
"v_cmp_nle_f32 s[58:59], v48, v219\n"
"v_cmp_nle_f32 s[60:61], v52, v219\n"
"v_cmp_nle_f32 s[62:63], v56, v219\n"
"v_rcp_f32 v218, v44\n"
"v_rcp_f32 v220, v48\n"
"v_rcp_f32 v222, v52\n"
"v_rcp_f32 v224, v56\n"
"s_or_b64 s[56:57], s[56:57], s[58:59]\n"
"s_or_b64 s[60:61], s[60:61], s[62:63]\n"
"s_or_b64 s[56:57], s[56:57], s[60:61]\n"
"s_cselect_b32 s56, 1, 0\n"
"s_cmp_eq_u32 s45, 0\n"
"s_cselect_b32 s56, s56, 0\n"
"s_lshl_b32 s57, s41, 2\n"
"s_add_u32 s57, s57, s40\n"
"s_add_u32 s57, s57, 163776\n"
"v_mov_b32 v234, s56\n"
"v_mov_b32 v235, s57\n"
"ds_write_b32 v235, v234\n"
"v_accvgpr_read_b32 v226, a0\n"
"v_accvgpr_read_b32 v227, a1\n"
"v_accvgpr_read_b32 v228, a2\n"
"v_accvgpr_read_b32 v229, a3\n"
"v_accvgpr_read_b32 v230, a4\n"
"v_accvgpr_read_b32 v231, a5\n"
"v_accvgpr_read_b32 v232, a6\n"
"v_accvgpr_read_b32 v233, a7\n"
"v_pk_mul_f32 v[226:227], v[226:227], v[218:219] op_sel_hi:[1,0]\n"
"v_pk_mul_f32 v[228:229], v[228:229], v[218:219] op_sel_hi:[1,0]\n"
"v_pk_mul_f32 v[230:231], v[230:231], v[218:219] op_sel_hi:[1,0]\n"
"v_pk_mul_f32 v[232:233], v[232:233], v[218:219] op_sel_hi:[1,0]\n"
"v_cvt_pk_bf16_f32 v0, v226, v227\n"
"v_cvt_pk_bf16_f32 v1, v228, v229\n"
"v_cvt_pk_bf16_f32 v2, v230, v231\n"
"v_cvt_pk_bf16_f32 v3, v232, v233\n"
"v_accvgpr_read_b32 v226, a64\n"
"v_accvgpr_read_b32 v227, a65\n"
"v_accvgpr_read_b32 v228, a66\n"
"v_accvgpr_read_b32 v229, a67\n"
"v_accvgpr_read_b32 v230, a68\n"
"v_accvgpr_read_b32 v231, a69\n"
"v_accvgpr_read_b32 v232, a70\n"
"v_accvgpr_read_b32 v233, a71\n"
"v_pk_mul_f32 v[226:227], v[226:227], v[222:223] op_sel_hi:[1,0]\n"
"v_pk_mul_f32 v[228:229], v[228:229], v[222:223] op_sel_hi:[1,0]\n"
"v_pk_mul_f32 v[230:231], v[230:231], v[222:223] op_sel_hi:[1,0]\n"
"v_pk_mul_f32 v[232:233], v[232:233], v[222:223] op_sel_hi:[1,0]\n"
"v_cvt_pk_bf16_f32 v32, v226, v227\n"
"v_cvt_pk_bf16_f32 v33, v228, v229\n"
"v_cvt_pk_bf16_f32 v34, v230, v231\n"
"v_cvt_pk_bf16_f32 v35, v232, v233\n"
"buffer_store_dwordx4 v[0:3], v207, s[28:31], s52 offen offset:0 sc1\n"
"v_accvgpr_read_b32 v226, a8\n"
"v_accvgpr_read_b32 v227, a9\n"
"v_accvgpr_read_b32 v228, a10\n"
"v_accvgpr_read_b32 v229, a11\n"
"v_accvgpr_read_b32 v230, a12\n"
"v_accvgpr_read_b32 v231, a13\n"
"v_accvgpr_read_b32 v232, a14\n"
"v_accvgpr_read_b32 v233, a15\n"
"v_pk_mul_f32 v[226:227], v[226:227], v[218:219] op_sel_hi:[1,0]\n"
"v_pk_mul_f32 v[228:229], v[228:229], v[218:219] op_sel_hi:[1,0]\n"
"v_pk_mul_f32 v[230:231], v[230:231], v[218:219] op_sel_hi:[1,0]\n"
"v_pk_mul_f32 v[232:233], v[232:233], v[218:219] op_sel_hi:[1,0]\n"
"v_cvt_pk_bf16_f32 v4, v226, v227\n"
"v_cvt_pk_bf16_f32 v5, v228, v229\n"
"v_cvt_pk_bf16_f32 v6, v230, v231\n"
"v_cvt_pk_bf16_f32 v7, v232, v233\n"
"v_accvgpr_read_b32 v226, a72\n"
"v_accvgpr_read_b32 v227, a73\n"
"v_accvgpr_read_b32 v228, a74\n"
"v_accvgpr_read_b32 v229, a75\n"
"v_accvgpr_read_b32 v230, a76\n"
"v_accvgpr_read_b32 v231, a77\n"
"v_accvgpr_read_b32 v232, a78\n"
"v_accvgpr_read_b32 v233, a79\n"
"v_pk_mul_f32 v[226:227], v[226:227], v[222:223] op_sel_hi:[1,0]\n"
"v_pk_mul_f32 v[228:229], v[228:229], v[222:223] op_sel_hi:[1,0]\n"
"v_pk_mul_f32 v[230:231], v[230:231], v[222:223] op_sel_hi:[1,0]\n"
"v_pk_mul_f32 v[232:233], v[232:233], v[222:223] op_sel_hi:[1,0]\n"
"v_cvt_pk_bf16_f32 v36, v226, v227\n"
"v_cvt_pk_bf16_f32 v37, v228, v229\n"
"v_cvt_pk_bf16_f32 v38, v230, v231\n"
"v_cvt_pk_bf16_f32 v39, v232, v233\n"
"buffer_store_dwordx4 v[32:35], v207, s[28:31], s52 offen offset:1024 sc1\n"
"v_accvgpr_read_b32 v226, a16\n"
"v_accvgpr_read_b32 v227, a17\n"
"v_accvgpr_read_b32 v228, a18\n"
"v_accvgpr_read_b32 v229, a19\n"
"v_accvgpr_read_b32 v230, a20\n"
"v_accvgpr_read_b32 v231, a21\n"
"v_accvgpr_read_b32 v232, a22\n"
"v_accvgpr_read_b32 v233, a23\n"
"v_pk_mul_f32 v[226:227], v[226:227], v[218:219] op_sel_hi:[1,0]\n"
"v_pk_mul_f32 v[228:229], v[228:229], v[218:219] op_sel_hi:[1,0]\n"
"v_pk_mul_f32 v[230:231], v[230:231], v[218:219] op_sel_hi:[1,0]\n"
"v_pk_mul_f32 v[232:233], v[232:233], v[218:219] op_sel_hi:[1,0]\n"
"v_cvt_pk_bf16_f32 v8, v226, v227\n"
"v_cvt_pk_bf16_f32 v9, v228, v229\n"
"v_cvt_pk_bf16_f32 v10, v230, v231\n"
"v_cvt_pk_bf16_f32 v11, v232, v233\n"
"v_accvgpr_read_b32 v226, a80\n"
"v_accvgpr_read_b32 v227, a81\n"
"v_accvgpr_read_b32 v228, a82\n"
"v_accvgpr_read_b32 v229, a83\n"
"v_accvgpr_read_b32 v230, a84\n"
"v_accvgpr_read_b32 v231, a85\n"
"v_accvgpr_read_b32 v232, a86\n"
"v_accvgpr_read_b32 v233, a87\n"
"v_pk_mul_f32 v[226:227], v[226:227], v[222:223] op_sel_hi:[1,0]\n"
"v_pk_mul_f32 v[228:229], v[228:229], v[222:223] op_sel_hi:[1,0]\n"
"v_pk_mul_f32 v[230:231], v[230:231], v[222:223] op_sel_hi:[1,0]\n"
"v_pk_mul_f32 v[232:233], v[232:233], v[222:223] op_sel_hi:[1,0]\n"
"v_cvt_pk_bf16_f32 v40, v226, v227\n"
"v_cvt_pk_bf16_f32 v41, v228, v229\n"
"v_cvt_pk_bf16_f32 v42, v230, v231\n"
"v_cvt_pk_bf16_f32 v43, v232, v233\n"
"buffer_store_dwordx4 v[4:7], v207, s[28:31], s52 offen offset:2048 sc1\n"
"v_accvgpr_read_b32 v226, a24\n"
"v_accvgpr_read_b32 v227, a25\n"
"v_accvgpr_read_b32 v228, a26\n"
"v_accvgpr_read_b32 v229, a27\n"
"v_accvgpr_read_b32 v230, a28\n"
"v_accvgpr_read_b32 v231, a29\n"
"v_accvgpr_read_b32 v232, a30\n"
"v_accvgpr_read_b32 v233, a31\n"
"v_pk_mul_f32 v[226:227], v[226:227], v[218:219] op_sel_hi:[1,0]\n"
"v_pk_mul_f32 v[228:229], v[228:229], v[218:219] op_sel_hi:[1,0]\n"
"v_pk_mul_f32 v[230:231], v[230:231], v[218:219] op_sel_hi:[1,0]\n"
"v_pk_mul_f32 v[232:233], v[232:233], v[218:219] op_sel_hi:[1,0]\n"
"v_cvt_pk_bf16_f32 v12, v226, v227\n"
"v_cvt_pk_bf16_f32 v13, v228, v229\n"
"v_cvt_pk_bf16_f32 v14, v230, v231\n"
"v_cvt_pk_bf16_f32 v15, v232, v233\n"
"v_accvgpr_read_b32 v226, a88\n"
"v_accvgpr_read_b32 v227, a89\n"
"v_accvgpr_read_b32 v228, a90\n"
"v_accvgpr_read_b32 v229, a91\n"
"v_accvgpr_read_b32 v230, a92\n"
"v_accvgpr_read_b32 v231, a93\n"
"v_accvgpr_read_b32 v232, a94\n"
"v_accvgpr_read_b32 v233, a95\n"
"v_pk_mul_f32 v[226:227], v[226:227], v[222:223] op_sel_hi:[1,0]\n"
"v_pk_mul_f32 v[228:229], v[228:229], v[222:223] op_sel_hi:[1,0]\n"
"v_pk_mul_f32 v[230:231], v[230:231], v[222:223] op_sel_hi:[1,0]\n"
"v_pk_mul_f32 v[232:233], v[232:233], v[222:223] op_sel_hi:[1,0]\n"
"v_cvt_pk_bf16_f32 v44, v226, v227\n"
"v_cvt_pk_bf16_f32 v45, v228, v229\n"
"v_cvt_pk_bf16_f32 v46, v230, v231\n"
"v_cvt_pk_bf16_f32 v47, v232, v233\n"
"buffer_store_dwordx4 v[36:39], v207, s[28:31], s52 offen offset:3072 sc1\n"
"s_add_u32 s52, s52, 4096\n"
"s_waitcnt vmcnt(16) lgkmcnt(0)\n"
"s_barrier\n"
"ds_read_b32 v234, v209\n"
"v_accvgpr_read_b32 v226, a32\n"
"v_accvgpr_read_b32 v227, a33\n"
"v_accvgpr_read_b32 v228, a34\n"
"v_accvgpr_read_b32 v229, a35\n"
"v_accvgpr_read_b32 v230, a36\n"
"v_accvgpr_read_b32 v231, a37\n"
"v_accvgpr_read_b32 v232, a38\n"
"v_accvgpr_read_b32 v233, a39\n"
"v_pk_mul_f32 v[226:227], v[226:227], v[220:221] op_sel_hi:[1,0]\n"
"v_pk_mul_f32 v[228:229], v[228:229], v[220:221] op_sel_hi:[1,0]\n"
"v_pk_mul_f32 v[230:231], v[230:231], v[220:221] op_sel_hi:[1,0]\n"
"v_pk_mul_f32 v[232:233], v[232:233], v[220:221] op_sel_hi:[1,0]\n"
"v_cvt_pk_bf16_f32 v16, v226, v227\n"
"v_cvt_pk_bf16_f32 v17, v228, v229\n"
"v_cvt_pk_bf16_f32 v18, v230, v231\n"
"v_cvt_pk_bf16_f32 v19, v232, v233\n"
"v_accvgpr_read_b32 v226, a96\n"
"v_accvgpr_read_b32 v227, a97\n"
"v_accvgpr_read_b32 v228, a98\n"
"v_accvgpr_read_b32 v229, a99\n"
"v_accvgpr_read_b32 v230, a100\n"
"v_accvgpr_read_b32 v231, a101\n"
"v_accvgpr_read_b32 v232, a102\n"
"v_accvgpr_read_b32 v233, a103\n"
"v_pk_mul_f32 v[226:227], v[226:227], v[224:225] op_sel_hi:[1,0]\n"
"v_pk_mul_f32 v[228:229], v[228:229], v[224:225] op_sel_hi:[1,0]\n"
"v_pk_mul_f32 v[230:231], v[230:231], v[224:225] op_sel_hi:[1,0]\n"
"v_pk_mul_f32 v[232:233], v[232:233], v[224:225] op_sel_hi:[1,0]\n"
"v_cvt_pk_bf16_f32 v48, v226, v227\n"
"v_cvt_pk_bf16_f32 v49, v228, v229\n"
"v_cvt_pk_bf16_f32 v50, v230, v231\n"
"v_cvt_pk_bf16_f32 v51, v232, v233\n"
"buffer_store_dwordx4 v[8:11], v207, s[28:31], s52 offen offset:0 sc1\n"
"v_accvgpr_read_b32 v226, a40\n"
"v_accvgpr_read_b32 v227, a41\n"
"v_accvgpr_read_b32 v228, a42\n"
"v_accvgpr_read_b32 v229, a43\n"
"v_accvgpr_read_b32 v230, a44\n"
"v_accvgpr_read_b32 v231, a45\n"
"v_accvgpr_read_b32 v232, a46\n"
"v_accvgpr_read_b32 v233, a47\n"
"v_pk_mul_f32 v[226:227], v[226:227], v[220:221] op_sel_hi:[1,0]\n"
"v_pk_mul_f32 v[228:229], v[228:229], v[220:221] op_sel_hi:[1,0]\n"
"v_pk_mul_f32 v[230:231], v[230:231], v[220:221] op_sel_hi:[1,0]\n"
"v_pk_mul_f32 v[232:233], v[232:233], v[220:221] op_sel_hi:[1,0]\n"
"v_cvt_pk_bf16_f32 v20, v226, v227\n"
"v_cvt_pk_bf16_f32 v21, v228, v229\n"
"v_cvt_pk_bf16_f32 v22, v230, v231\n"
"v_cvt_pk_bf16_f32 v23, v232, v233\n"
"v_accvgpr_read_b32 v226, a104\n"
"v_accvgpr_read_b32 v227, a105\n"
"v_accvgpr_read_b32 v228, a106\n"
"v_accvgpr_read_b32 v229, a107\n"
"v_accvgpr_read_b32 v230, a108\n"
"v_accvgpr_read_b32 v231, a109\n"
"v_accvgpr_read_b32 v232, a110\n"
"v_accvgpr_read_b32 v233, a111\n"
"v_pk_mul_f32 v[226:227], v[226:227], v[224:225] op_sel_hi:[1,0]\n"
"v_pk_mul_f32 v[228:229], v[228:229], v[224:225] op_sel_hi:[1,0]\n"
"v_pk_mul_f32 v[230:231], v[230:231], v[224:225] op_sel_hi:[1,0]\n"
"v_pk_mul_f32 v[232:233], v[232:233], v[224:225] op_sel_hi:[1,0]\n"
"v_cvt_pk_bf16_f32 v52, v226, v227\n"
"v_cvt_pk_bf16_f32 v53, v228, v229\n"
"v_cvt_pk_bf16_f32 v54, v230, v231\n"
"v_cvt_pk_bf16_f32 v55, v232, v233\n"
"buffer_store_dwordx4 v[40:43], v207, s[28:31], s52 offen offset:1024 sc1\n"
"v_accvgpr_read_b32 v226, a48\n"
"v_accvgpr_read_b32 v227, a49\n"
"v_accvgpr_read_b32 v228, a50\n"
"v_accvgpr_read_b32 v229, a51\n"
"v_accvgpr_read_b32 v230, a52\n"
"v_accvgpr_read_b32 v231, a53\n"
"v_accvgpr_read_b32 v232, a54\n"
"v_accvgpr_read_b32 v233, a55\n"
"v_pk_mul_f32 v[226:227], v[226:227], v[220:221] op_sel_hi:[1,0]\n"
"v_pk_mul_f32 v[228:229], v[228:229], v[220:221] op_sel_hi:[1,0]\n"
"v_pk_mul_f32 v[230:231], v[230:231], v[220:221] op_sel_hi:[1,0]\n"
"v_pk_mul_f32 v[232:233], v[232:233], v[220:221] op_sel_hi:[1,0]\n"
"v_cvt_pk_bf16_f32 v24, v226, v227\n"
"v_cvt_pk_bf16_f32 v25, v228, v229\n"
"v_cvt_pk_bf16_f32 v26, v230, v231\n"
"v_cvt_pk_bf16_f32 v27, v232, v233\n"
"v_accvgpr_read_b32 v226, a112\n"
"v_accvgpr_read_b32 v227, a113\n"
"v_accvgpr_read_b32 v228, a114\n"
"v_accvgpr_read_b32 v229, a115\n"
"v_accvgpr_read_b32 v230, a116\n"
"v_accvgpr_read_b32 v231, a117\n"
"v_accvgpr_read_b32 v232, a118\n"
"v_accvgpr_read_b32 v233, a119\n"
"v_pk_mul_f32 v[226:227], v[226:227], v[224:225] op_sel_hi:[1,0]\n"
"v_pk_mul_f32 v[228:229], v[228:229], v[224:225] op_sel_hi:[1,0]\n"
"v_pk_mul_f32 v[230:231], v[230:231], v[224:225] op_sel_hi:[1,0]\n"
"v_pk_mul_f32 v[232:233], v[232:233], v[224:225] op_sel_hi:[1,0]\n"
"v_cvt_pk_bf16_f32 v56, v226, v227\n"
"v_cvt_pk_bf16_f32 v57, v228, v229\n"
"v_cvt_pk_bf16_f32 v58, v230, v231\n"
"v_cvt_pk_bf16_f32 v59, v232, v233\n"
"buffer_store_dwordx4 v[12:15], v207, s[28:31], s52 offen offset:2048 sc1\n"
"v_accvgpr_read_b32 v226, a56\n"
"v_accvgpr_read_b32 v227, a57\n"
"v_accvgpr_read_b32 v228, a58\n"
"v_accvgpr_read_b32 v229, a59\n"
"v_accvgpr_read_b32 v230, a60\n"
"v_accvgpr_read_b32 v231, a61\n"
"v_accvgpr_read_b32 v232, a62\n"
"v_accvgpr_read_b32 v233, a63\n"
"v_pk_mul_f32 v[226:227], v[226:227], v[220:221] op_sel_hi:[1,0]\n"
"v_pk_mul_f32 v[228:229], v[228:229], v[220:221] op_sel_hi:[1,0]\n"
"v_pk_mul_f32 v[230:231], v[230:231], v[220:221] op_sel_hi:[1,0]\n"
"v_pk_mul_f32 v[232:233], v[232:233], v[220:221] op_sel_hi:[1,0]\n"
"v_cvt_pk_bf16_f32 v28, v226, v227\n"
"v_cvt_pk_bf16_f32 v29, v228, v229\n"
"v_cvt_pk_bf16_f32 v30, v230, v231\n"
"v_cvt_pk_bf16_f32 v31, v232, v233\n"
"v_accvgpr_read_b32 v226, a120\n"
"v_accvgpr_read_b32 v227, a121\n"
"v_accvgpr_read_b32 v228, a122\n"
"v_accvgpr_read_b32 v229, a123\n"
"v_accvgpr_read_b32 v230, a124\n"
"v_accvgpr_read_b32 v231, a125\n"
"v_accvgpr_read_b32 v232, a126\n"
"v_accvgpr_read_b32 v233, a127\n"
"v_pk_mul_f32 v[226:227], v[226:227], v[224:225] op_sel_hi:[1,0]\n"
"v_pk_mul_f32 v[228:229], v[228:229], v[224:225] op_sel_hi:[1,0]\n"
"v_pk_mul_f32 v[230:231], v[230:231], v[224:225] op_sel_hi:[1,0]\n"
"v_pk_mul_f32 v[232:233], v[232:233], v[224:225] op_sel_hi:[1,0]\n"
"v_cvt_pk_bf16_f32 v60, v226, v227\n"
"v_cvt_pk_bf16_f32 v61, v228, v229\n"
"v_cvt_pk_bf16_f32 v62, v230, v231\n"
"v_cvt_pk_bf16_f32 v63, v232, v233\n"
"buffer_store_dwordx4 v[44:47], v207, s[28:31], s52 offen offset:3072 sc1\n"
"s_add_u32 s52, s52, 4096\n"
"s_waitcnt lgkmcnt(0)\n"
"v_cmp_ne_u32 vcc, 0, v234\n"
"s_nop 0\n"
"s_cbranch_vccnz AT_fb\n"
"v_mfma_f32_16x16x32_bf16 v[64:67], a[128:131], v[0:3], 0\n"
"ds_read_b64_tr_b16 a[200:201], v202 offset:9344\n"
"v_mfma_f32_16x16x32_bf16 v[68:71], a[128:131], v[32:35], 0\n"
"ds_read_b64_tr_b16 a[202:203], v202 offset:13952\n"
"buffer_store_dwordx4 v[16:19], v207, s[28:31], s52 offen offset:0 sc1\n"
"v_mfma_f32_16x16x32_bf16 v[72:75], a[132:135], v[0:3], 0\n"
"ds_read_b64_tr_b16 a[204:205], v202 offset:9472\n"
"v_mfma_f32_16x16x32_bf16 v[76:79], a[132:135], v[32:35], 0\n"
"s_add_u32 m0, s53, 111616\n"
"ds_read_b64_tr_b16 a[206:207], v202 offset:14080\n"
"v_mfma_f32_16x16x32_bf16 v[80:83], a[136:139], v[0:3], 0\n"
"s_lshl_b32 s54, s44, 2\n"
"ds_read_b64_tr_b16 a[208:209], v202 offset:9600\n"
"buffer_load_dwordx4 v211, s[24:27], s23 offen lds\n"
"v_mfma_f32_16x16x32_bf16 v[84:87], a[136:139], v[32:35], 0\n"
"s_add_u32 s54, s54, s41\n"
"ds_read_b64_tr_b16 a[210:211], v202 offset:14208\n"
"v_mfma_f32_16x16x32_bf16 v[88:91], a[140:143], v[0:3], 0\n"
"s_mul_i32 s54, s54, 36864\n"
"ds_read_b64_tr_b16 a[212:213], v202 offset:9728\n"
"v_mfma_f32_16x16x32_bf16 v[92:95], a[140:143], v[32:35], 0\n"
"s_lshr_b32 s62, s43, 2\n"
"ds_read_b64_tr_b16 a[214:215], v202 offset:14336\n"
"v_mfma_f32_16x16x32_bf16 v[96:99], a[144:147], v[0:3], 0\n"
"s_mul_i32 s62, s62, 2359296\n"
"ds_read_b64_tr_b16 a[216:217], v202 offset:9856\n"
"v_mfma_f32_16x16x32_bf16 v[100:103], a[144:147], v[32:35], 0\n"
"s_add_u32 s54, s54, s62\n"
"ds_read_b64_tr_b16 a[218:219], v202 offset:14464\n"
"v_mfma_f32_16x16x32_bf16 v[104:107], a[148:151], v[0:3], 0\n"
"s_add_u32 m0, s53, 112640\n"
"ds_read_b64_tr_b16 a[220:221], v202 offset:9984\n"
"buffer_store_dwordx4 v[48:51], v207, s[28:31], s52 offen offset:1024 sc1\n"
"v_mfma_f32_16x16x32_bf16 v[108:111], a[148:151], v[32:35], 0\n"
"s_add_u32 s54, s54, 139264\n"
"ds_read_b64_tr_b16 a[222:223], v202 offset:14592\n"
"buffer_load_dwordx4 v212, s[24:27], s23 offen lds\n"
"v_mfma_f32_16x16x32_bf16 v[112:115], a[152:155], v[0:3], 0\n"
"s_bfe_u32 s62, s43, 0x10001\n"
"ds_read_b64_tr_b16 a[128:129], v202 offset:10112\n"
"v_mfma_f32_16x16x32_bf16 v[116:119], a[152:155], v[32:35], 0\n"
"s_mul_i32 s62, s62, 9216\n"
"ds_read_b64_tr_b16 a[130:131], v202 offset:14720\n"
"v_mfma_f32_16x16x32_bf16 v[120:123], a[156:159], v[0:3], 0\n"
"s_add_u32 s54, s54, s62\n"
"ds_read_b64_tr_b16 a[132:133], v205 offset:9216\n"
"v_mfma_f32_16x16x32_bf16 v[124:127], a[156:159], v[32:35], 0\n"
"s_and_b32 s62, s43, 1\n"
"ds_read_b64_tr_b16 a[134:135], v205 offset:13824\n"
"v_mfma_f32_16x16x32_bf16 v[226:229], a[192:195], v[0:3], 0\n"
"s_add_u32 m0, s53, 113664\n"
"ds_read_b64_tr_b16 a[136:137], v202 offset:18432\n"
"v_mfma_f32_16x16x32_bf16 v[230:233], a[192:195], v[32:35], 0\n"
"s_lshl_b32 s49, s62, 3\n"
"ds_read_b64_tr_b16 a[138:139], v202 offset:23040\n"
"buffer_load_dwordx4 v213, s[24:27], s23 offen lds\n"
"v_mfma_f32_16x16x32_bf16 v[64:67], a[196:199], v[4:7], v[64:67]\n"
"s_add_u32 s49, s49, s54\n"
"ds_read_b64_tr_b16 a[140:141], v202 offset:18560\n"
"v_mfma_f32_16x16x32_bf16 v[68:71], a[196:199], v[36:39], v[68:71]\n"
"s_waitcnt lgkmcnt(11)\n"
"s_add_u32 s49, s49, 4096\n"
"ds_read_b64_tr_b16 a[142:143], v202 offset:23168\n"
"buffer_store_dwordx4 v[20:23], v207, s[28:31], s52 offen offset:2048 sc1\n"
"v_mfma_f32_16x16x32_bf16 v[72:75], a[200:203], v[4:7], v[72:75]\n"
"s_mul_i32 s62, s62, 5120\n"
"ds_read_b64_tr_b16 a[144:145], v202 offset:18688\n"
"v_mfma_f32_16x16x32_bf16 v[76:79], a[200:203], v[36:39], v[76:79]\n"
"s_add_u32 s54, s54, s62\n"
"ds_read_b64_tr_b16 a[146:147], v202 offset:23296\n"
"v_mfma_f32_16x16x32_bf16 v[80:83], a[204:207], v[4:7], v[80:83]\n"
"s_add_u32 m0, s53, 114688\n"
"ds_read_b64_tr_b16 a[148:149], v202 offset:18816\n"
"v_mfma_f32_16x16x32_bf16 v[84:87], a[204:207], v[36:39], v[84:87]\n"
"s_mul_i32 s46, s55, 9216\n"
"ds_read_b64_tr_b16 a[150:151], v202 offset:23424\n"
"buffer_load_dwordx4 v214, s[24:27], s23 offen lds\n"
"v_mfma_f32_16x16x32_bf16 v[88:91], a[208:211], v[4:7], v[88:91]\n"
"s_lshr_b32 s47, s55, 1\n"
"ds_read_b64_tr_b16 a[152:153], v202 offset:18944\n"
"v_mfma_f32_16x16x32_bf16 v[92:95], a[208:211], v[36:39], v[92:95]\n"
"s_mul_i32 s47, s47, 18432\n"
"ds_read_b64_tr_b16 a[154:155], v202 offset:23552\n"
"v_mfma_f32_16x16x32_bf16 v[96:99], a[212:215], v[4:7], v[96:99]\n"
"s_and_b32 s62, s55, 1\n"
"ds_read_b64_tr_b16 a[156:157], v202 offset:19072\n"
"v_mfma_f32_16x16x32_bf16 v[100:103], a[212:215], v[36:39], v[100:103]\n"
"s_waitcnt lgkmcnt(11)\n"
"s_lshl_b32 s62, s62, 7\n"
"ds_read_b64_tr_b16 a[158:159], v202 offset:23680\n"
"v_mfma_f32_16x16x32_bf16 v[104:107], a[216:219], v[4:7], v[104:107]\n"
"s_add_u32 m0, s53, 115712\n"
"ds_read_b64_tr_b16 a[192:193], v202 offset:19200\n"
"buffer_store_dwordx4 v[52:55], v207, s[28:31], s52 offen offset:3072 sc1\n"
"v_mfma_f32_16x16x32_bf16 v[108:111], a[216:219], v[36:39], v[108:111]\n"
"s_add_u32 s52, s52, 4096\n"
"ds_read_b64_tr_b16 a[194:195], v202 offset:23808\n"
"buffer_load_dwordx4 v236, s[24:27], s23 offen lds\n"
"v_mfma_f32_16x16x32_bf16 v[112:115], a[220:223], v[4:7], v[112:115]\n"
"s_add_u32 s47, s47, s62\n"
"ds_read_b64_tr_b16 a[196:197], v202 offset:19328\n"
"v_mfma_f32_16x16x32_bf16 v[116:119], a[220:223], v[36:39], v[116:119]\n"
"s_add_u32 s47, s47, 139264\n"
"ds_read_b64_tr_b16 a[198:199], v202 offset:23936\n"
"v_mfma_f32_16x16x32_bf16 v[120:123], a[128:131], v[4:7], v[120:123]\n"
"s_mul_i32 s48, s55, 73728\n"
"ds_read_b64_tr_b16 a[200:201], v205 offset:18432\n"
"v_mfma_f32_16x16x32_bf16 v[124:127], a[128:131], v[36:39], v[124:127]\n"
"s_add_u32 s48, s48, 37888000\n"
"ds_read_b64_tr_b16 a[202:203], v205 offset:23040\n"
"v_mfma_f32_16x16x32_bf16 v[226:229], a[132:135], v[4:7], v[226:229]\n"
"s_add_u32 m0, s53, 116736\n"
"ds_read_b64_tr_b16 a[204:205], v200 offset:0\n"
"v_mfma_f32_16x16x32_bf16 v[230:233], a[132:135], v[36:39], v[230:233]\n"
"s_waitcnt lgkmcnt(11)\n"
"s_add_u32 s62, s40, 119808\n"
"ds_read_b64_tr_b16 a[206:207], v200 offset:4608\n"
"buffer_load_dwordx4 v237, s[24:27], s23 offen lds\n"
"v_mfma_f32_16x16x32_bf16 v[64:67], a[136:139], v[8:11], v[64:67]\n"
"s_mov_b32 s56, 41984\n"
"ds_read_b64_tr_b16 a[208:209], v200 offset:128\n"
"v_mfma_f32_16x16x32_bf16 v[68:71], a[136:139], v[40:43], v[68:71]\n"
"s_mov_b32 s57, 46592\n"
"ds_read_b64_tr_b16 a[210:211], v200 offset:4736\n"
"buffer_store_dwordx4 v[24:27], v207, s[28:31], s52 offen offset:0 sc1\n"
"v_mfma_f32_16x16x32_bf16 v[72:75], a[140:143], v[8:11], v[72:75]\n"
"s_mov_b32 s58, 51200\n"
"ds_read_b64_tr_b16 a[212:213], v200 offset:256\n"
"v_mfma_f32_16x16x32_bf16 v[76:79], a[140:143], v[40:43], v[76:79]\n"
"s_mov_b32 s59, 55808\n"
"ds_read_b64_tr_b16 a[214:215], v200 offset:4864\n"
"v_mfma_f32_16x16x32_bf16 v[80:83], a[144:147], v[8:11], v[80:83]\n"
"s_add_u32 m0, s53, 117760\n"
"ds_read_b64_tr_b16 a[216:217], v200 offset:384\n"
"v_and_b32 v206, 7, v248\n"
"v_mfma_f32_16x16x32_bf16 v[84:87], a[144:147], v[40:43], v[84:87]\n"
"ds_read_b64_tr_b16 a[218:219], v200 offset:4992\n"
"buffer_load_dwordx4 v238, s[24:27], s23 offen lds\n"
"v_mfma_f32_16x16x32_bf16 v[88:91], a[148:151], v[8:11], v[88:91]\n"
"ds_read_b64_tr_b16 a[220:221], v200 offset:512\n"
"v_lshrrev_b32 v255, 5, v253\n"
"v_mfma_f32_16x16x32_bf16 v[92:95], a[148:151], v[40:43], v[92:95]\n"
"s_waitcnt lgkmcnt(11)\n"
"ds_read_b64_tr_b16 a[222:223], v200 offset:5120\n"
"v_bfe_u32 v246, v253, 3, 2\n"
"v_mfma_f32_16x16x32_bf16 v[96:99], a[152:155], v[8:11], v[96:99]\n"
"ds_read_b64_tr_b16 a[128:129], v200 offset:640\n"
"v_lshlrev_b32 v241, 10, v255\n"
"v_mfma_f32_16x16x32_bf16 v[100:103], a[152:155], v[40:43], v[100:103]\n"
"ds_read_b64_tr_b16 a[130:131], v200 offset:5248\n"
"v_lshl_add_u32 v241, v246, 8, v241\n"
"v_mfma_f32_16x16x32_bf16 v[104:107], a[156:159], v[8:11], v[104:107]\n"
"s_add_u32 m0, s53, 118784\n"
"ds_read_b64_tr_b16 a[132:133], v200 offset:768\n"
"buffer_store_dwordx4 v[56:59], v207, s[28:31], s52 offen offset:1024 sc1\n"
"v_mfma_f32_16x16x32_bf16 v[108:111], a[156:159], v[40:43], v[108:111]\n"
"ds_read_b64_tr_b16 a[134:135], v200 offset:5376\n"
"buffer_load_dwordx4 v239, s[24:27], s23 offen lds\n"
"v_mfma_f32_16x16x32_bf16 v[112:115], a[192:195], v[8:11], v[112:115]\n"
"s_and_b32 s60, s55, 4294967292\n"
"ds_read_b64_tr_b16 a[136:137], v200 offset:896\n"
"v_lshl_add_u32 v241, v206, 4, v241\n"
"v_mfma_f32_16x16x32_bf16 v[116:119], a[192:195], v[40:43], v[116:119]\n"
"s_add_u32 s61, s43, 1\n"
"ds_read_b64_tr_b16 a[138:139], v200 offset:5504\n"
"v_add_u32 v242, 6144, v241\n"
"v_mfma_f32_16x16x32_bf16 v[120:123], a[196:199], v[8:11], v[120:123]\n"
"s_and_b32 s61, s61, 3\n"
"ds_read_b64_tr_b16 a[140:141], v203 offset:0\n"
"v_add_u32 v243, 12288, v241\n"
"v_mfma_f32_16x16x32_bf16 v[124:127], a[196:199], v[40:43], v[124:127]\n"
"s_waitcnt lgkmcnt(11)\n"
"s_add_u32 s61, s61, s60\n"
"ds_read_b64_tr_b16 a[142:143], v203 offset:4608\n"
"v_lshlrev_b32 v255, 5, v255\n"
"v_mfma_f32_16x16x32_bf16 v[226:229], a[200:203], v[8:11], v[226:229]\n"
"s_lshl_b32 s61, s61, 2\n"
"ds_read_b64_tr_b16 a[144:145], v200 offset:9216\n"
"v_lshl_add_u32 v255, v246, 2, v255\n"
"v_mfma_f32_16x16x32_bf16 v[230:233], a[200:203], v[40:43], v[230:233]\n"
"s_add_u32 s61, s61, s41\n"
"ds_read_b64_tr_b16 a[146:147], v200 offset:13824\n"
"v_lshl_add_u32 v244, v255, 2, s62\n"
"v_mfma_f32_16x16x32_bf16 v[64:67], a[204:207], v[12:15], v[64:67]\n"
"s_lshl_b32 s61, s61, 14\n"
"ds_read_b64_tr_b16 a[148:149], v200 offset:9344\n"
"v_mul_u32_u24 v206, 1152, v206\n"
"v_mfma_f32_16x16x32_bf16 v[68:71], a[204:207], v[44:47], v[68:71]\n"
"s_add_u32 s20, s61, 9592832\n"
"ds_read_b64_tr_b16 a[150:151], v200 offset:13952\n"
"buffer_store_dwordx4 v[28:31], v207, s[28:31], s52 offen offset:2048 sc1\n"
"v_mfma_f32_16x16x32_bf16 v[72:75], a[208:211], v[12:15], v[72:75]\n"
"s_add_u32 s61, s43, 2\n"
"ds_read_b64_tr_b16 a[152:153], v200 offset:9472\n"
"v_lshl_add_u32 v240, v255, 1, v206\n"
"v_mfma_f32_16x16x32_bf16 v[76:79], a[208:211], v[44:47], v[76:79]\n"
"s_and_b32 s61, s61, 3\n"
"ds_read_b64_tr_b16 a[154:155], v200 offset:14080\n"
"v_add_u32 v245, v206, v255\n"
"v_mfma_f32_16x16x32_bf16 v[80:83], a[212:215], v[12:15], v[80:83]\n"
"s_add_u32 s61, s61, s60\n"
"ds_read_b64_tr_b16 a[156:157], v200 offset:9600\n"
"v_lshlrev_b32 v245, 3, v245\n"
"v_mfma_f32_16x16x32_bf16 v[84:87], a[212:215], v[44:47], v[84:87]\n"
"s_waitcnt lgkmcnt(11)\n"
"s_lshl_b32 s61, s61, 2\n"
"ds_read_b64_tr_b16 a[158:159], v200 offset:14208\n"
"v_lshlrev_b32 v246, 2, v254\n"
"v_mfma_f32_16x16x32_bf16 v[88:91], a[216:219], v[12:15], v[88:91]\n"
"s_add_u32 s61, s61, s41\n"
"ds_read_b64_tr_b16 a[192:193], v200 offset:9728\n"
"v_lshl_add_u32 v247, v253, 4, s62\n"
"v_mfma_f32_16x16x32_bf16 v[92:95], a[216:219], v[44:47], v[92:95]\n"
"s_lshl_b32 s61, s61, 14\n"
"ds_read_b64_tr_b16 a[194:195], v200 offset:14336\n"
"buffer_load_dwordx4 a[160:163], v246, s[28:31], s56 offen offset:0 sc1\n"
"v_mfma_f32_16x16x32_bf16 v[96:99], a[220:223], v[12:15], v[96:99]\n"
"s_add_u32 s21, s61, 9592832\n"
"ds_read_b64_tr_b16 a[196:197], v200 offset:9856\n"
"v_mfma_f32_16x16x32_bf16 v[100:103], a[220:223], v[44:47], v[100:103]\n"
"s_add_u32 s61, s43, 3\n"
"ds_read_b64_tr_b16 a[198:199], v200 offset:14464\n"
"v_mfma_f32_16x16x32_bf16 v[104:107], a[128:131], v[12:15], v[104:107]\n"
"s_and_b32 s61, s61, 3\n"
"ds_read_b64_tr_b16 a[200:201], v200 offset:9984\n"
"buffer_store_dwordx4 v[60:63], v207, s[28:31], s52 offen offset:3072 sc1\n"
"v_mfma_f32_16x16x32_bf16 v[108:111], a[128:131], v[44:47], v[108:111]\n"
"s_add_u32 s52, s52, 4096\n"
"ds_read_b64_tr_b16 a[202:203], v200 offset:14592\n"
"buffer_load_dwordx4 a[164:167], v246, s[28:31], s56 offen offset:16 sc1\n"
"v_mfma_f32_16x16x32_bf16 v[112:115], a[132:135], v[12:15], v[112:115]\n"
"s_add_u32 s61, s61, s60\n"
"ds_read_b64_tr_b16 a[204:205], v200 offset:10112\n"
"v_mfma_f32_16x16x32_bf16 v[116:119], a[132:135], v[44:47], v[116:119]\n"
"s_waitcnt lgkmcnt(11)\n"
"s_lshl_b32 s61, s61, 2\n"
"ds_read_b64_tr_b16 a[206:207], v200 offset:14720\n"
"v_mfma_f32_16x16x32_bf16 v[120:123], a[136:139], v[12:15], v[120:123]\n"
"s_add_u32 s61, s61, s41\n"
"ds_read_b64_tr_b16 a[208:209], v203 offset:9216\n"
"v_mfma_f32_16x16x32_bf16 v[124:127], a[136:139], v[44:47], v[124:127]\n"
"s_lshl_b32 s61, s61, 14\n"
"ds_read_b64_tr_b16 a[210:211], v203 offset:13824\n"
"buffer_load_dwordx4 a[168:171], v246, s[28:31], s57 offen offset:0 sc1\n"
"v_mfma_f32_16x16x32_bf16 v[226:229], a[140:143], v[12:15], v[226:229]\n"
"s_add_u32 s22, s61, 9592832\n"
"ds_read_b64_tr_b16 a[212:213], v200 offset:18432\n"
"v_mfma_f32_16x16x32_bf16 v[230:233], a[140:143], v[44:47], v[230:233]\n"
"s_lshl_b32 s23, s60, 6\n"
"ds_read_b64_tr_b16 a[214:215], v200 offset:23040\n"
"v_mfma_f32_16x16x32_bf16 v[64:67], a[144:147], v[16:19], v[64:67]\n"
"s_add_u32 s23, s23, 9576448\n"
"ds_read_b64_tr_b16 a[216:217], v200 offset:18560\n"
"v_mfma_f32_16x16x32_bf16 v[68:71], a[144:147], v[48:51], v[68:71]\n"
"s_lshl_b32 s61, s55, 6\n"
"ds_read_b64_tr_b16 a[218:219], v200 offset:23168\n"
"buffer_load_dwordx4 a[172:175], v246, s[28:31], s57 offen offset:16 sc1\n"
"v_mfma_f32_16x16x32_bf16 v[72:75], a[148:151], v[16:19], v[72:75]\n"
"s_waitcnt vmcnt(29)\n"
"s_barrier\n"
"ds_read_b64_tr_b16 a[220:221], v200 offset:18688\n"
"v_mfma_f32_16x16x32_bf16 v[76:79], a[148:151], v[48:51], v[76:79]\n"
"s_waitcnt lgkmcnt(11)\n"
"ds_read_b64_tr_b16 a[222:223], v200 offset:23296\n"
"v_mfma_f32_16x16x32_bf16 v[80:83], a[152:155], v[16:19], v[80:83]\n"
"s_mov_b32 m0, s51\n"
"ds_read_b64_tr_b16 a[128:129], v200 offset:18816\n"
"v_mfma_f32_16x16x32_bf16 v[84:87], a[152:155], v[48:51], v[84:87]\n"
"ds_read_b64_tr_b16 a[130:131], v200 offset:23424\n"
"buffer_load_dwordx4 v210, s[24:27], s36 offen lds\n"
"v_mfma_f32_16x16x32_bf16 v[88:91], a[156:159], v[16:19], v[88:91]\n"
"ds_read_b64_tr_b16 a[132:133], v200 offset:18944\n"
"buffer_load_dwordx4 a[176:179], v246, s[28:31], s58 offen offset:0 sc1\n"
"v_mfma_f32_16x16x32_bf16 v[92:95], a[156:159], v[48:51], v[92:95]\n"
"ds_read_b64_tr_b16 a[134:135], v200 offset:23552\n"
"v_mfma_f32_16x16x32_bf16 v[96:99], a[192:195], v[16:19], v[96:99]\n"
"ds_read_b64_tr_b16 a[136:137], v200 offset:19072\n"
"v_mfma_f32_16x16x32_bf16 v[100:103], a[192:195], v[48:51], v[100:103]\n"
"ds_read_b64_tr_b16 a[138:139], v200 offset:23680\n"
"v_mfma_f32_16x16x32_bf16 v[104:107], a[196:199], v[16:19], v[104:107]\n"
"s_add_u32 m0, s51, 1024\n"
"ds_read_b64_tr_b16 a[140:141], v200 offset:19200\n"
"buffer_load_dwordx4 a[180:183], v246, s[28:31], s58 offen offset:16 sc1\n"
"v_mfma_f32_16x16x32_bf16 v[108:111], a[196:199], v[48:51], v[108:111]\n"
"s_waitcnt lgkmcnt(11)\n"
"ds_read_b64_tr_b16 a[142:143], v200 offset:23808\n"
"buffer_load_dwordx4 v211, s[24:27], s36 offen lds\n"
"v_mfma_f32_16x16x32_bf16 v[112:115], a[200:203], v[16:19], v[112:115]\n"
"ds_read_b64_tr_b16 a[144:145], v200 offset:19328\n"
"v_mfma_f32_16x16x32_bf16 v[116:119], a[200:203], v[48:51], v[116:119]\n"
"ds_read_b64_tr_b16 a[146:147], v200 offset:23936\n"
"v_mfma_f32_16x16x32_bf16 v[120:123], a[204:207], v[16:19], v[120:123]\n"
"ds_read_b64_tr_b16 a[148:149], v203 offset:18432\n"
"buffer_load_dwordx4 a[184:187], v246, s[28:31], s59 offen offset:0 sc1\n"
"v_mfma_f32_16x16x32_bf16 v[124:127], a[204:207], v[48:51], v[124:127]\n"
"ds_read_b64_tr_b16 a[150:151], v203 offset:23040\n"
"v_mfma_f32_16x16x32_bf16 v[226:229], a[208:211], v[16:19], v[226:229]\n"
"s_add_u32 m0, s51, 2048\n"
"ds_read_b64_tr_b16 a[152:153], v200 offset:27648\n"
"v_mfma_f32_16x16x32_bf16 v[230:233], a[208:211], v[48:51], v[230:233]\n"
"ds_read_b64_tr_b16 a[154:155], v200 offset:32256\n"
"buffer_load_dwordx4 v212, s[24:27], s36 offen lds\n"
"v_mfma_f32_16x16x32_bf16 v[64:67], a[212:215], v[20:23], v[64:67]\n"
"ds_read_b64_tr_b16 a[156:157], v200 offset:27776\n"
"buffer_load_dwordx4 a[188:191], v246, s[28:31], s59 offen offset:16 sc1\n"
"v_mfma_f32_16x16x32_bf16 v[68:71], a[212:215], v[52:55], v[68:71]\n"
"s_waitcnt lgkmcnt(11)\n"
"ds_read_b64_tr_b16 a[158:159], v200 offset:32384\n"
"v_mfma_f32_16x16x32_bf16 v[72:75], a[216:219], v[20:23], v[72:75]\n"
"ds_read_b64_tr_b16 a[192:193], v200 offset:27904\n"
"v_mfma_f32_16x16x32_bf16 v[76:79], a[216:219], v[52:55], v[76:79]\n"
"ds_read_b64_tr_b16 a[194:195], v200 offset:32512\n"
"v_mfma_f32_16x16x32_bf16 v[80:83], a[220:223], v[20:23], v[80:83]\n"
"s_add_u32 m0, s51, 3072\n"
"ds_read_b64_tr_b16 a[196:197], v200 offset:28032\n"
"buffer_load_dwordx2 a[224:225], v254, s[80:83], 0 offen\n"
"v_mfma_f32_16x16x32_bf16 v[84:87], a[220:223], v[52:55], v[84:87]\n"
"ds_read_b64_tr_b16 a[198:199], v200 offset:32640\n"
"buffer_load_dwordx4 v213, s[24:27], s36 offen lds\n"
"v_mfma_f32_16x16x32_bf16 v[88:91], a[128:131], v[20:23], v[88:91]\n"
"ds_read_b64_tr_b16 a[200:201], v200 offset:28160\n"
"v_mfma_f32_16x16x32_bf16 v[92:95], a[128:131], v[52:55], v[92:95]\n"
"ds_read_b64_tr_b16 a[202:203], v200 offset:32768\n"
"v_mfma_f32_16x16x32_bf16 v[96:99], a[132:135], v[20:23], v[96:99]\n"
"ds_read_b64_tr_b16 a[204:205], v200 offset:28288\n"
"v_mfma_f32_16x16x32_bf16 v[100:103], a[132:135], v[52:55], v[100:103]\n"
"s_waitcnt lgkmcnt(11)\n"
"ds_read_b64_tr_b16 a[206:207], v200 offset:32896\n"
"v_mfma_f32_16x16x32_bf16 v[104:107], a[136:139], v[20:23], v[104:107]\n"
"s_add_u32 m0, s51, 4096\n"
"ds_read_b64_tr_b16 a[208:209], v200 offset:28416\n"
"v_mfma_f32_16x16x32_bf16 v[108:111], a[136:139], v[52:55], v[108:111]\n"
"ds_read_b64_tr_b16 a[210:211], v200 offset:33024\n"
"buffer_load_dwordx4 v214, s[24:27], s36 offen lds\n"
"v_mfma_f32_16x16x32_bf16 v[112:115], a[140:143], v[20:23], v[112:115]\n"
"ds_read_b64_tr_b16 a[212:213], v200 offset:28544\n"
"v_mfma_f32_16x16x32_bf16 v[116:119], a[140:143], v[52:55], v[116:119]\n"
"ds_read_b64_tr_b16 a[214:215], v200 offset:33152\n"
"v_mfma_f32_16x16x32_bf16 v[120:123], a[144:147], v[20:23], v[120:123]\n"
"ds_read_b64_tr_b16 a[216:217], v203 offset:27648\n"
"v_mfma_f32_16x16x32_bf16 v[124:127], a[144:147], v[52:55], v[124:127]\n"
"ds_read_b64_tr_b16 a[218:219], v203 offset:32256\n"
"v_mfma_f32_16x16x32_bf16 v[226:229], a[148:151], v[20:23], v[226:229]\n"
"s_add_u32 m0, s51, 5120\n"
"ds_read_b64_tr_b16 a[220:221], v200 offset:36864\n"
"v_mfma_f32_16x16x32_bf16 v[230:233], a[148:151], v[52:55], v[230:233]\n"
"s_waitcnt lgkmcnt(11)\n"
"ds_read_b64_tr_b16 a[222:223], v200 offset:41472\n"
"buffer_load_dwordx4 v236, s[24:27], s36 offen lds\n"
"v_mfma_f32_16x16x32_bf16 v[64:67], a[152:155], v[24:27], v[64:67]\n"
"ds_read_b64_tr_b16 a[128:129], v200 offset:36992\n"
"v_mfma_f32_16x16x32_bf16 v[68:71], a[152:155], v[56:59], v[68:71]\n"
"ds_read_b64_tr_b16 a[130:131], v200 offset:41600\n"
"v_mfma_f32_16x16x32_bf16 v[72:75], a[156:159], v[24:27], v[72:75]\n"
"ds_read_b64_tr_b16 a[132:133], v200 offset:37120\n"
"v_mfma_f32_16x16x32_bf16 v[76:79], a[156:159], v[56:59], v[76:79]\n"
"ds_read_b64_tr_b16 a[134:135], v200 offset:41728\n"
"v_mfma_f32_16x16x32_bf16 v[80:83], a[192:195], v[24:27], v[80:83]\n"
"s_add_u32 m0, s51, 6144\n"
"ds_read_b64_tr_b16 a[136:137], v200 offset:37248\n"
"v_mfma_f32_16x16x32_bf16 v[84:87], a[192:195], v[56:59], v[84:87]\n"
"ds_read_b64_tr_b16 a[138:139], v200 offset:41856\n"
"buffer_load_dwordx4 v237, s[24:27], s36 offen lds\n"
"v_mfma_f32_16x16x32_bf16 v[88:91], a[196:199], v[24:27], v[88:91]\n"
"ds_read_b64_tr_b16 a[140:141], v200 offset:37376\n"
"v_mfma_f32_16x16x32_bf16 v[92:95], a[196:199], v[56:59], v[92:95]\n"
"s_waitcnt lgkmcnt(11)\n"
"ds_read_b64_tr_b16 a[142:143], v200 offset:41984\n"
"v_mfma_f32_16x16x32_bf16 v[96:99], a[200:203], v[24:27], v[96:99]\n"
"ds_read_b64_tr_b16 a[144:145], v200 offset:37504\n"
"v_mfma_f32_16x16x32_bf16 v[100:103], a[200:203], v[56:59], v[100:103]\n"
"ds_read_b64_tr_b16 a[146:147], v200 offset:42112\n"
"v_mfma_f32_16x16x32_bf16 v[104:107], a[204:207], v[24:27], v[104:107]\n"
"s_add_u32 m0, s51, 7168\n"
"ds_read_b64_tr_b16 a[148:149], v200 offset:37632\n"
"v_mfma_f32_16x16x32_bf16 v[108:111], a[204:207], v[56:59], v[108:111]\n"
"ds_read_b64_tr_b16 a[150:151], v200 offset:42240\n"
"buffer_load_dwordx4 v238, s[24:27], s36 offen lds\n"
"v_mfma_f32_16x16x32_bf16 v[112:115], a[208:211], v[24:27], v[112:115]\n"
"ds_read_b64_tr_b16 a[152:153], v200 offset:37760\n"
"v_mfma_f32_16x16x32_bf16 v[116:119], a[208:211], v[56:59], v[116:119]\n"
"ds_read_b64_tr_b16 a[154:155], v200 offset:42368\n"
"v_mfma_f32_16x16x32_bf16 v[120:123], a[212:215], v[24:27], v[120:123]\n"
"ds_read_b64_tr_b16 a[156:157], v203 offset:36864\n"
"v_mfma_f32_16x16x32_bf16 v[124:127], a[212:215], v[56:59], v[124:127]\n"
"s_waitcnt lgkmcnt(11)\n"
"ds_read_b64_tr_b16 a[158:159], v203 offset:41472\n"
"v_mfma_f32_16x16x32_bf16 v[226:229], a[216:219], v[24:27], v[226:229]\n"
"s_add_u32 m0, s51, 8192\n"
"ds_read_b64_tr_b16 a[192:193], v200 offset:46080\n"
"v_mfma_f32_16x16x32_bf16 v[230:233], a[216:219], v[56:59], v[230:233]\n"
"ds_read_b64_tr_b16 a[194:195], v200 offset:50688\n"
"buffer_load_dwordx4 v239, s[24:27], s36 offen lds\n"
"v_mfma_f32_16x16x32_bf16 v[64:67], a[220:223], v[28:31], v[64:67]\n"
"s_add_u32 s36, s61, 9576448\n"
"ds_read_b64_tr_b16 a[196:197], v200 offset:46208\n"
"v_mfma_f32_16x16x32_bf16 v[68:71], a[220:223], v[60:63], v[68:71]\n"
"ds_read_b64_tr_b16 a[198:199], v200 offset:50816\n"
"v_mfma_f32_16x16x32_bf16 v[72:75], a[128:131], v[28:31], v[72:75]\n"
"ds_read_b64_tr_b16 a[200:201], v200 offset:46336\n"
"v_mfma_f32_16x16x32_bf16 v[76:79], a[128:131], v[60:63], v[76:79]\n"
"ds_read_b64_tr_b16 a[202:203], v200 offset:50944\n"
"v_mfma_f32_16x16x32_bf16 v[80:83], a[132:135], v[28:31], v[80:83]\n"
"ds_read_b64_tr_b16 a[204:205], v200 offset:46464\n"
"v_mfma_f32_16x16x32_bf16 v[84:87], a[132:135], v[60:63], v[84:87]\n"
"s_waitcnt lgkmcnt(11)\n"
"ds_read_b64_tr_b16 a[206:207], v200 offset:51072\n"
"v_mfma_f32_16x16x32_bf16 v[88:91], a[136:139], v[28:31], v[88:91]\n"
"ds_read_b64_tr_b16 a[208:209], v200 offset:46592\n"
"v_mfma_f32_16x16x32_bf16 v[92:95], a[136:139], v[60:63], v[92:95]\n"
"ds_read_b64_tr_b16 a[210:211], v200 offset:51200\n"
"v_mfma_f32_16x16x32_bf16 v[96:99], a[140:143], v[28:31], v[96:99]\n"
"ds_read_b64_tr_b16 a[212:213], v200 offset:46720\n"
"v_mfma_f32_16x16x32_bf16 v[100:103], a[140:143], v[60:63], v[100:103]\n"
"ds_read_b64_tr_b16 a[214:215], v200 offset:51328\n"
"v_mfma_f32_16x16x32_bf16 v[104:107], a[144:147], v[28:31], v[104:107]\n"
"ds_read_b64_tr_b16 a[216:217], v200 offset:46848\n"
"v_mfma_f32_16x16x32_bf16 v[108:111], a[144:147], v[60:63], v[108:111]\n"
"ds_read_b64_tr_b16 a[218:219], v200 offset:51456\n"
"v_mfma_f32_16x16x32_bf16 v[112:115], a[148:151], v[28:31], v[112:115]\n"
"ds_read_b64_tr_b16 a[220:221], v200 offset:46976\n"
"v_mfma_f32_16x16x32_bf16 v[116:119], a[148:151], v[60:63], v[116:119]\n"
"s_waitcnt lgkmcnt(11)\n"
"ds_read_b64_tr_b16 a[222:223], v200 offset:51584\n"
"v_mfma_f32_16x16x32_bf16 v[120:123], a[152:155], v[28:31], v[120:123]\n"
"ds_read_b64_tr_b16 a[128:129], v203 offset:46080\n"
"v_mfma_f32_16x16x32_bf16 v[124:127], a[152:155], v[60:63], v[124:127]\n"
"ds_read_b64_tr_b16 a[130:131], v203 offset:50688\n"
"v_mfma_f32_16x16x32_bf16 v[226:229], a[156:159], v[28:31], v[226:229]\n"
"ds_read_b64_tr_b16 a[132:133], v200 offset:55296\n"
"v_mfma_f32_16x16x32_bf16 v[230:233], a[156:159], v[60:63], v[230:233]\n"
"ds_read_b64_tr_b16 a[134:135], v200 offset:59904\n"
"v_mov_b32 v62, s85\n"
"v_mov_b32 v63, 0\n"
"v_and_b32 v61, 3, v248\n"
"v_lshlrev_b32 v61, 6, v61\n"
"s_waitcnt vmcnt(0)\n"
"s_barrier\n"
"s_cmp_lg_u32 s41, 3\n"
"s_cbranch_scc1 AT_x0\n"
"s_mov_b64 exec, 1\n"
"buffer_store_dword v62, v63, s[28:31], s36 offen sc1\n"
"s_mov_b64 exec, -1\n"
"AT_x0:\n"
"s_add_u32 m0, s53, 36864\n"
"s_nop 0\n"
"buffer_load_dwordx4 v210, s[24:27], s37 offen lds\n"
"s_add_u32 m0, s53, 37888\n"
"s_nop 0\n"
"buffer_load_dwordx4 v211, s[24:27], s37 offen lds\n"
"s_add_u32 m0, s53, 38912\n"
"s_nop 0\n"
"buffer_load_dwordx4 v212, s[24:27], s37 offen lds\n"
"s_add_u32 m0, s53, 39936\n"
"s_nop 0\n"
"buffer_load_dwordx4 v213, s[24:27], s37 offen lds\n"
"s_add_u32 m0, s53, 40960\n"
"s_nop 0\n"
"buffer_load_dwordx4 v214, s[24:27], s37 offen lds\n"
"s_add_u32 m0, s53, 41984\n"
"s_nop 0\n"
"buffer_load_dwordx4 v236, s[24:27], s37 offen lds\n"
"s_add_u32 m0, s53, 43008\n"
"s_nop 0\n"
"buffer_load_dwordx4 v237, s[24:27], s37 offen lds\n"
"s_add_u32 m0, s53, 44032\n"
"s_nop 0\n"
"buffer_load_dwordx4 v238, s[24:27], s37 offen lds\n"
"s_add_u32 m0, s53, 45056\n"
"s_nop 0\n"
"buffer_load_dwordx4 v239, s[24:27], s37 offen lds\n"
"s_mov_b32 s87, 0x80000\n"
"v_accvgpr_read_b32 v26, a160\n"
"v_accvgpr_read_b32 v27, a161\n"
"v_accvgpr_read_b32 v28, a162\n"
"v_accvgpr_read_b32 v29, a163\n"
"v_accvgpr_read_b32 v30, a164\n"
"v_accvgpr_read_b32 v31, a165\n"
"v_accvgpr_read_b32 v32, a166\n"
"v_accvgpr_read_b32 v33, a167\n"
"v_accvgpr_read_b32 v34, a168\n"
"v_accvgpr_read_b32 v35, a169\n"
"v_accvgpr_read_b32 v36, a170\n"
"v_accvgpr_read_b32 v37, a171\n"
"v_accvgpr_read_b32 v38, a172\n"
"v_accvgpr_read_b32 v39, a173\n"
"v_accvgpr_read_b32 v40, a174\n"
"v_accvgpr_read_b32 v41, a175\n"
"v_accvgpr_read_b32 v42, a176\n"
"v_accvgpr_read_b32 v43, a177\n"
"v_accvgpr_read_b32 v44, a178\n"
"v_accvgpr_read_b32 v45, a179\n"
"v_accvgpr_read_b32 v46, a180\n"
"v_accvgpr_read_b32 v47, a181\n"
"v_accvgpr_read_b32 v48, a182\n"
"v_accvgpr_read_b32 v49, a183\n"
"v_accvgpr_read_b32 v50, a184\n"
"v_accvgpr_read_b32 v51, a185\n"
"v_accvgpr_read_b32 v52, a186\n"
"v_accvgpr_read_b32 v53, a187\n"
"v_accvgpr_read_b32 v54, a188\n"
"v_accvgpr_read_b32 v55, a189\n"
"v_accvgpr_read_b32 v56, a190\n"
"v_accvgpr_read_b32 v57, a191\n"
"v_accvgpr_read_b32 v58, a224\n"
"v_accvgpr_read_b32 v59, a225\n"
"AT_tchk:\n"
"v_xor_b32 v24, s85, v27\n"
"v_xor_b32 v25, s85, v29\n"
"v_bitop3_b32 v24, v31, s85, v24 bitop3:0xbe\n"
"v_bitop3_b32 v25, v33, s85, v25 bitop3:0xbe\n"
"v_bitop3_b32 v24, v35, s85, v24 bitop3:0xbe\n"
"v_bitop3_b32 v25, v37, s85, v25 bitop3:0xbe\n"
"v_bitop3_b32 v24, v39, s85, v24 bitop3:0xbe\n"
"v_bitop3_b32 v25, v41, s85, v25 bitop3:0xbe\n"
"v_bitop3_b32 v24, v43, s85, v24 bitop3:0xbe\n"
"v_bitop3_b32 v25, v45, s85, v25 bitop3:0xbe\n"
"v_bitop3_b32 v24, v47, s85, v24 bitop3:0xbe\n"
"v_bitop3_b32 v25, v49, s85, v25 bitop3:0xbe\n"
"v_bitop3_b32 v24, v51, s85, v24 bitop3:0xbe\n"
"v_bitop3_b32 v25, v53, s85, v25 bitop3:0xbe\n"
"v_bitop3_b32 v24, v55, s85, v24 bitop3:0xbe\n"
"v_bitop3_b32 v25, v57, s85, v25 bitop3:0xbe\n"
"v_or_b32 v24, v24, v25\n"
"v_cmp_ne_u32 s[62:63], 0, v24\n"
"v_cmp_gt_u32 vcc, 144, v253\n"
"s_nop 1\n"
"s_and_b64 s[62:63], vcc, s[62:63]\n"
"s_cmp_eq_u64 s[62:63], 0\n"
"s_cbranch_scc1 AT_tok\n"
"s_sub_u32 s87, s87, 1\n"
"s_cmp_eq_u32 s87, 0\n"
"s_cbranch_scc1 AT_tok\n"
"s_sleep 2\n"
"buffer_load_dwordx4 v[26:29], v246, s[28:31], s56 offen offset:0 sc1\n"
"buffer_load_dwordx4 v[30:33], v246, s[28:31], s56 offen offset:16 sc1\n"
"buffer_load_dwordx4 v[34:37], v246, s[28:31], s57 offen offset:0 sc1\n"
"buffer_load_dwordx4 v[38:41], v246, s[28:31], s57 offen offset:16 sc1\n"
"buffer_load_dwordx4 v[42:45], v246, s[28:31], s58 offen offset:0 sc1\n"
"buffer_load_dwordx4 v[46:49], v246, s[28:31], s58 offen offset:16 sc1\n"
"buffer_load_dwordx4 v[50:53], v246, s[28:31], s59 offen offset:0 sc1\n"
"buffer_load_dwordx4 v[54:57], v246, s[28:31], s59 offen offset:16 sc1\n"
"s_waitcnt vmcnt(0)\n"
"s_branch AT_tchk\n"
"AT_tok:\n"
"s_mov_b32 s56, s20\n"
"s_mov_b32 s57, s21\n"
"s_mov_b32 s58, s22\n"
"s_mov_b32 s87, 0x80000\n"
"AT_x1:\n"
"buffer_load_dword v60, v61, s[28:31], s23 offen sc1\n"
"s_waitcnt vmcnt(0)\n"
"v_cmp_ne_u32 vcc, s85, v60\n"
"s_sub_u32 s87, s87, 1\n"
"s_cbranch_vccz AT_x2\n"
"s_cmp_eq_u32 s87, 0\n"
"s_cbranch_scc1 AT_x2\n"
"s_sleep 1\n"
"s_branch AT_x1\n"
"AT_x2:\n"
"buffer_load_dwordx4 a[0:3], v207, s[28:31], s56 offen offset:0 sc1\n"
"buffer_load_dwordx4 a[4:7], v207, s[28:31], s56 offen offset:1024 sc1\n"
"buffer_load_dwordx4 a[8:11], v207, s[28:31], s57 offen offset:0 sc1\n"
"buffer_load_dwordx4 a[12:15], v207, s[28:31], s57 offen offset:1024 sc1\n"
"buffer_load_dwordx4 a[16:19], v207, s[28:31], s58 offen offset:0 sc1\n"
"buffer_load_dwordx4 a[20:23], v207, s[28:31], s58 offen offset:1024 sc1\n"
"v_cmp_gt_u32 vcc, 144, v253\n"
"v_lshlrev_b32 v0, 16, v58\n"
"v_and_b32 v1, 0xffff0000, v58\n"
"v_lshlrev_b32 v2, 16, v59\n"
"v_and_b32 v3, 0xffff0000, v59\n"
"v_mov_b32 v4, v26\n"
"v_add_f32 v12, 1.0, v42\n"
"v_mov_b32 v16, v34\n"
"v_mov_b32 v20, v50\n"
"v_mul_f32 v8, v12, v0\n"
"v_mov_b32 v5, v28\n"
"v_add_f32 v13, 1.0, v44\n"
"v_mov_b32 v17, v36\n"
"v_mov_b32 v21, v52\n"
"v_mul_f32 v9, v13, v1\n"
"v_mov_b32 v6, v30\n"
"v_add_f32 v14, 1.0, v46\n"
"v_mov_b32 v18, v38\n"
"v_mov_b32 v22, v54\n"
"v_mul_f32 v10, v14, v2\n"
"v_mov_b32 v7, v32\n"
"v_add_f32 v15, 1.0, v48\n"
"v_mov_b32 v19, v40\n"
"v_mov_b32 v23, v56\n"
"v_mul_f32 v11, v15, v3\n"
"s_mov_b64 exec, vcc\n"
"ds_write_b128 v247, v[4:7] offset:0\n"
"ds_write_b128 v247, v[8:11] offset:2304\n"
"ds_write_b128 v247, v[12:15] offset:4608\n"
"ds_write_b128 v247, v[16:19] offset:6912\n"
"ds_write_b128 v247, v[20:23] offset:38912\n"
"s_mov_b64 exec, -1\n"
"v_add_f32 v24, v8, v9\n"
"v_mul_f32 v25, v16, v0\n"
"v_add_f32 v24, v24, v10\n"
"v_fmac_f32 v25, v17, v1\n"
"v_add_f32 v24, v24, v11\n"
"v_fmac_f32 v25, v18, v2\n"
"s_nop 0\n"
"v_fmac_f32 v25, v19, v3\n"
"s_nop 1\n"
"v_add_f32_dpp v24, v24, v24 row_ror:1 row_mask:0xf bank_mask:0xf\n"
"v_add_f32_dpp v25, v25, v25 row_ror:1 row_mask:0xf bank_mask:0xf\n"
"s_nop 0\n"
"v_add_f32_dpp v24, v24, v24 row_ror:2 row_mask:0xf bank_mask:0xf\n"
"v_add_f32_dpp v25, v25, v25 row_ror:2 row_mask:0xf bank_mask:0xf\n"
"s_nop 0\n"
"v_add_f32_dpp v24, v24, v24 row_ror:4 row_mask:0xf bank_mask:0xf\n"
"v_add_f32_dpp v25, v25, v25 row_ror:4 row_mask:0xf bank_mask:0xf\n"
"s_nop 0\n"
"v_add_f32_dpp v24, v24, v24 row_ror:8 row_mask:0xf bank_mask:0xf\n"
"v_add_f32_dpp v25, v25, v25 row_ror:8 row_mask:0xf bank_mask:0xf\n"
"s_nop 0\n"
"v_mov_b32 v26, v24\n"
"v_mov_b32 v27, v25\n"
"s_nop 0\n"
"v_permlane16_swap_b32 v24, v26\n"
"v_permlane16_swap_b32 v25, v27\n"
"v_add_f32 v24, v24, v26\n"
"v_add_f32 v25, v25, v27\n"
"v_mov_b32 v26, v24\n"
"v_mov_b32 v27, v25\n"
"s_nop 0\n"
"v_permlane32_swap_b32 v24, v26\n"
"v_permlane32_swap_b32 v25, v27\n"
"v_add_f32 v24, v24, v26\n"
"v_add_f32 v25, v25, v27\n"
"s_lshl_b32 s62, s41, 3\n"
"s_add_u32 s62, s62, s40\n"
"v_mov_b32 v26, s62\n"
"v_mov_b32 v27, s40\n"
"v_mov_b32 v29, 0\n"
"v_add_u32 v26, 161536, v26\n"
"v_add_u32 v27, 161024, v27\n"
"s_mov_b64 exec, 1\n"
"ds_write_b64 v26, v[24:25]\n"
"ds_write_b32 v27, v29 offset:640\n"
"s_mov_b64 exec, -1\n"
"s_waitcnt vmcnt(4)\n"
"v_mfma_f32_16x16x32_bf16 v[64:67], a[192:195], a[0:3], v[64:67]\n"
"ds_read_b64_tr_b16 a[136:137], v200 offset:55424\n"
"buffer_load_dwordx4 a[24:27], v207, s[28:31], s56 offen offset:2048 sc1\n"
"v_mfma_f32_16x16x32_bf16 v[68:71], a[192:195], a[4:7], v[68:71]\n"
"ds_read_b64_tr_b16 a[138:139], v200 offset:60032\n"
"v_mfma_f32_16x16x32_bf16 v[72:75], a[196:199], a[0:3], v[72:75]\n"
"ds_read_b64_tr_b16 a[140:141], v200 offset:55552\n"
"v_mfma_f32_16x16x32_bf16 v[76:79], a[196:199], a[4:7], v[76:79]\n"
"s_waitcnt lgkmcnt(14)\n"
"ds_read_b64_tr_b16 a[142:143], v200 offset:60160\n"
"v_mfma_f32_16x16x32_bf16 v[80:83], a[200:203], a[0:3], v[80:83]\n"
"ds_read_b64_tr_b16 a[144:145], v200 offset:55680\n"
"v_mfma_f32_16x16x32_bf16 v[84:87], a[200:203], a[4:7], v[84:87]\n"
"ds_read_b64_tr_b16 a[146:147], v200 offset:60288\n"
"buffer_load_dwordx4 a[28:31], v207, s[28:31], s56 offen offset:3072 sc1\n"
"v_mfma_f32_16x16x32_bf16 v[88:91], a[204:207], a[0:3], v[88:91]\n"
"s_add_u32 s56, s56, 4096\n"
"ds_read_b64_tr_b16 a[148:149], v200 offset:55808\n"
"v_mfma_f32_16x16x32_bf16 v[92:95], a[204:207], a[4:7], v[92:95]\n"
"ds_read_b64_tr_b16 a[150:151], v200 offset:60416\n"
"v_mfma_f32_16x16x32_bf16 v[96:99], a[208:211], a[0:3], v[96:99]\n"
"ds_read_b64_tr_b16 a[152:153], v200 offset:55936\n"
"v_mfma_f32_16x16x32_bf16 v[100:103], a[208:211], a[4:7], v[100:103]\n"
"ds_read_b64_tr_b16 a[154:155], v200 offset:60544\n"
"v_mfma_f32_16x16x32_bf16 v[104:107], a[212:215], a[0:3], v[104:107]\n"
"ds_read_b64_tr_b16 a[156:157], v200 offset:56064\n"
"buffer_load_dwordx4 a[32:35], v207, s[28:31], s57 offen offset:2048 sc1\n"
"v_mfma_f32_16x16x32_bf16 v[108:111], a[212:215], a[4:7], v[108:111]\n"
"ds_read_b64_tr_b16 a[158:159], v200 offset:60672\n"
"v_mfma_f32_16x16x32_bf16 v[112:115], a[216:219], a[0:3], v[112:115]\n"
"ds_read_b64_tr_b16 a[192:193], v200 offset:56192\n"
"v_mfma_f32_16x16x32_bf16 v[116:119], a[216:219], a[4:7], v[116:119]\n"
"ds_read_b64_tr_b16 a[194:195], v200 offset:60800\n"
"v_mfma_f32_16x16x32_bf16 v[120:123], a[220:223], a[0:3], v[120:123]\n"
"ds_read_b64_tr_b16 a[196:197], v203 offset:55296\n"
"v_mfma_f32_16x16x32_bf16 v[124:127], a[220:223], a[4:7], v[124:127]\n"
"s_waitcnt lgkmcnt(11)\n"
"ds_read_b64_tr_b16 a[198:199], v203 offset:59904\n"
"buffer_load_dwordx4 a[36:39], v207, s[28:31], s57 offen offset:3072 sc1\n"
"v_mfma_f32_16x16x32_bf16 v[226:229], a[128:131], a[0:3], v[226:229]\n"
"s_add_u32 s57, s57, 4096\n"
"ds_read_b64_tr_b16 a[200:201], v200 offset:64512\n"
"v_mfma_f32_16x16x32_bf16 v[230:233], a[128:131], a[4:7], v[230:233]\n"
"s_waitcnt vmcnt(6)\n"
"ds_read_b64_tr_b16 a[202:203], v201 offset:3584\n"
"v_mfma_f32_16x16x32_bf16 v[64:67], a[132:135], a[8:11], v[64:67]\n"
"ds_read_b64_tr_b16 a[204:205], v200 offset:64640\n"
"v_mfma_f32_16x16x32_bf16 v[68:71], a[132:135], a[12:15], v[68:71]\n"
"ds_read_b64_tr_b16 a[206:207], v201 offset:3712\n"
"v_mfma_f32_16x16x32_bf16 v[72:75], a[136:139], a[8:11], v[72:75]\n"
"ds_read_b64_tr_b16 a[208:209], v200 offset:64768\n"
"buffer_load_dwordx4 a[40:43], v207, s[28:31], s58 offen offset:2048 sc1\n"
"v_mfma_f32_16x16x32_bf16 v[76:79], a[136:139], a[12:15], v[76:79]\n"
"ds_read_b64_tr_b16 a[210:211], v201 offset:3840\n"
"v_mfma_f32_16x16x32_bf16 v[80:83], a[140:143], a[8:11], v[80:83]\n"
"ds_read_b64_tr_b16 a[212:213], v200 offset:64896\n"
"v_mfma_f32_16x16x32_bf16 v[84:87], a[140:143], a[12:15], v[84:87]\n"
"s_waitcnt lgkmcnt(11)\n"
"ds_read_b64_tr_b16 a[214:215], v201 offset:3968\n"
"v_mfma_f32_16x16x32_bf16 v[88:91], a[144:147], a[8:11], v[88:91]\n"
"ds_read_b64_tr_b16 a[216:217], v200 offset:65024\n"
"v_mfma_f32_16x16x32_bf16 v[92:95], a[144:147], a[12:15], v[92:95]\n"
"ds_read_b64_tr_b16 a[218:219], v201 offset:4096\n"
"buffer_load_dwordx4 a[44:47], v207, s[28:31], s58 offen offset:3072 sc1\n"
"v_mfma_f32_16x16x32_bf16 v[96:99], a[148:151], a[8:11], v[96:99]\n"
"s_add_u32 s58, s58, 4096\n"
"ds_read_b64_tr_b16 a[220:221], v200 offset:65152\n"
"v_mfma_f32_16x16x32_bf16 v[100:103], a[148:151], a[12:15], v[100:103]\n"
"ds_read_b64_tr_b16 a[222:223], v201 offset:4224\n"
"v_mfma_f32_16x16x32_bf16 v[104:107], a[152:155], a[8:11], v[104:107]\n"
"ds_read_b64_tr_b16 a[128:129], v200 offset:65280\n"
"v_mfma_f32_16x16x32_bf16 v[108:111], a[152:155], a[12:15], v[108:111]\n"
"ds_read_b64_tr_b16 a[130:131], v201 offset:4352\n"
"v_mfma_f32_16x16x32_bf16 v[112:115], a[156:159], a[8:11], v[112:115]\n"
"ds_read_b64_tr_b16 a[132:133], v200 offset:65408\n"
"buffer_load_dwordx4 a[48:51], v207, s[28:31], s56 offen offset:0 sc1\n"
"v_mfma_f32_16x16x32_bf16 v[116:119], a[156:159], a[12:15], v[116:119]\n"
"s_waitcnt lgkmcnt(11)\n"
"ds_read_b64_tr_b16 a[134:135], v201 offset:4480\n"
"v_mfma_f32_16x16x32_bf16 v[120:123], a[192:195], a[8:11], v[120:123]\n"
"ds_read_b64_tr_b16 a[136:137], v203 offset:64512\n"
"v_mfma_f32_16x16x32_bf16 v[124:127], a[192:195], a[12:15], v[124:127]\n"
"ds_read_b64_tr_b16 a[138:139], v204 offset:3584\n"
"v_mfma_f32_16x16x32_bf16 v[226:229], a[196:199], a[8:11], v[226:229]\n"
"ds_read_b64_tr_b16 a[140:141], v201 offset:8192\n"
"v_mfma_f32_16x16x32_bf16 v[230:233], a[196:199], a[12:15], v[230:233]\n"
"s_waitcnt vmcnt(7)\n"
"ds_read_b64_tr_b16 a[142:143], v201 offset:12800\n"
"buffer_load_dwordx4 a[52:55], v207, s[28:31], s56 offen offset:1024 sc1\n"
"v_mfma_f32_16x16x32_bf16 v[64:67], a[200:203], a[16:19], v[64:67]\n"
"ds_read_b64_tr_b16 a[144:145], v201 offset:8320\n"
"v_mfma_f32_16x16x32_bf16 v[68:71], a[200:203], a[20:23], v[68:71]\n"
"ds_read_b64_tr_b16 a[146:147], v201 offset:12928\n"
"v_mfma_f32_16x16x32_bf16 v[72:75], a[204:207], a[16:19], v[72:75]\n"
"ds_read_b64_tr_b16 a[148:149], v201 offset:8448\n"
"v_mfma_f32_16x16x32_bf16 v[76:79], a[204:207], a[20:23], v[76:79]\n"
"s_waitcnt lgkmcnt(11)\n"
"ds_read_b64_tr_b16 a[150:151], v201 offset:13056\n"
"v_mfma_f32_16x16x32_bf16 v[80:83], a[208:211], a[16:19], v[80:83]\n"
"ds_read_b64_tr_b16 a[152:153], v201 offset:8576\n"
"buffer_load_dwordx4 a[56:59], v207, s[28:31], s57 offen offset:0 sc1\n"
"v_mfma_f32_16x16x32_bf16 v[84:87], a[208:211], a[20:23], v[84:87]\n"
"ds_read_b64_tr_b16 a[154:155], v201 offset:13184\n"
"v_mfma_f32_16x16x32_bf16 v[88:91], a[212:215], a[16:19], v[88:91]\n"
"ds_read_b64_tr_b16 a[156:157], v201 offset:8704\n"
"v_mfma_f32_16x16x32_bf16 v[92:95], a[212:215], a[20:23], v[92:95]\n"
"ds_read_b64_tr_b16 a[158:159], v201 offset:13312\n"
"v_mfma_f32_16x16x32_bf16 v[96:99], a[216:219], a[16:19], v[96:99]\n"
"ds_read_b64_tr_b16 a[192:193], v201 offset:8832\n"
"v_mfma_f32_16x16x32_bf16 v[100:103], a[216:219], a[20:23], v[100:103]\n"
"ds_read_b64_tr_b16 a[194:195], v201 offset:13440\n"
"buffer_load_dwordx4 a[60:63], v207, s[28:31], s57 offen offset:1024 sc1\n"
"v_mfma_f32_16x16x32_bf16 v[104:107], a[220:223], a[16:19], v[104:107]\n"
"ds_read_b64_tr_b16 a[196:197], v201 offset:8960\n"
"v_mfma_f32_16x16x32_bf16 v[108:111], a[220:223], a[20:23], v[108:111]\n"
"s_waitcnt lgkmcnt(11)\n"
"ds_read_b64_tr_b16 a[198:199], v201 offset:13568\n"
"v_mfma_f32_16x16x32_bf16 v[112:115], a[128:131], a[16:19], v[112:115]\n"
"ds_read_b64_tr_b16 a[200:201], v201 offset:9088\n"
"v_mfma_f32_16x16x32_bf16 v[116:119], a[128:131], a[20:23], v[116:119]\n"
"ds_read_b64_tr_b16 a[202:203], v201 offset:13696\n"
"v_mfma_f32_16x16x32_bf16 v[120:123], a[132:135], a[16:19], v[120:123]\n"
"ds_read_b64_tr_b16 a[204:205], v204 offset:8192\n"
"buffer_load_dwordx4 a[64:67], v207, s[28:31], s58 offen offset:0 sc1\n"
"v_mfma_f32_16x16x32_bf16 v[124:127], a[132:135], a[20:23], v[124:127]\n"
"ds_read_b64_tr_b16 a[206:207], v204 offset:12800\n"
"v_mfma_f32_16x16x32_bf16 v[226:229], a[136:139], a[16:19], v[226:229]\n"
"ds_read_b64_tr_b16 a[208:209], v201 offset:17408\n"
"v_mfma_f32_16x16x32_bf16 v[230:233], a[136:139], a[20:23], v[230:233]\n"
"s_waitcnt vmcnt(9)\n"
"ds_read_b64_tr_b16 a[210:211], v201 offset:22016\n"
"v_mfma_f32_16x16x32_bf16 v[64:67], a[140:143], a[24:27], v[64:67]\n"
"ds_read_b64_tr_b16 a[212:213], v201 offset:17536\n"
"v_mfma_f32_16x16x32_bf16 v[68:71], a[140:143], a[28:31], v[68:71]\n"
"s_waitcnt lgkmcnt(11)\n"
"ds_read_b64_tr_b16 a[214:215], v201 offset:22144\n"
"buffer_load_dwordx4 a[68:71], v207, s[28:31], s58 offen offset:1024 sc1\n"
"v_mfma_f32_16x16x32_bf16 v[72:75], a[144:147], a[24:27], v[72:75]\n"
"ds_read_b64_tr_b16 a[216:217], v201 offset:17664\n"
"v_mfma_f32_16x16x32_bf16 v[76:79], a[144:147], a[28:31], v[76:79]\n"
"ds_read_b64_tr_b16 a[218:219], v201 offset:22272\n"
"v_mfma_f32_16x16x32_bf16 v[80:83], a[148:151], a[24:27], v[80:83]\n"
"ds_read_b64_tr_b16 a[220:221], v201 offset:17792\n"
"v_mfma_f32_16x16x32_bf16 v[84:87], a[148:151], a[28:31], v[84:87]\n"
"ds_read_b64_tr_b16 a[222:223], v201 offset:22400\n"
"v_mfma_f32_16x16x32_bf16 v[88:91], a[152:155], a[24:27], v[88:91]\n"
"ds_read_b64_tr_b16 a[128:129], v201 offset:17920\n"
"buffer_load_dwordx4 a[72:75], v207, s[28:31], s56 offen offset:2048 sc1\n"
"v_mfma_f32_16x16x32_bf16 v[92:95], a[152:155], a[28:31], v[92:95]\n"
"ds_read_b64_tr_b16 a[130:131], v201 offset:22528\n"
"v_mfma_f32_16x16x32_bf16 v[96:99], a[156:159], a[24:27], v[96:99]\n"
"ds_read_b64_tr_b16 a[132:133], v201 offset:18048\n"
"v_mfma_f32_16x16x32_bf16 v[100:103], a[156:159], a[28:31], v[100:103]\n"
"s_waitcnt lgkmcnt(11)\n"
"ds_read_b64_tr_b16 a[134:135], v201 offset:22656\n"
"v_mfma_f32_16x16x32_bf16 v[104:107], a[192:195], a[24:27], v[104:107]\n"
"ds_read_b64_tr_b16 a[136:137], v201 offset:18176\n"
"v_mfma_f32_16x16x32_bf16 v[108:111], a[192:195], a[28:31], v[108:111]\n"
"ds_read_b64_tr_b16 a[138:139], v201 offset:22784\n"
"v_mfma_f32_16x16x32_bf16 v[112:115], a[196:199], a[24:27], v[112:115]\n"
"ds_read_b64_tr_b16 a[140:141], v201 offset:18304\n"
"v_mfma_f32_16x16x32_bf16 v[116:119], a[196:199], a[28:31], v[116:119]\n"
"ds_read_b64_tr_b16 a[142:143], v201 offset:22912\n"
"v_mfma_f32_16x16x32_bf16 v[120:123], a[200:203], a[24:27], v[120:123]\n"
"ds_read_b64_tr_b16 a[144:145], v204 offset:17408\n"
"v_mfma_f32_16x16x32_bf16 v[124:127], a[200:203], a[28:31], v[124:127]\n"
"ds_read_b64_tr_b16 a[146:147], v204 offset:22016\n"
"buffer_load_dwordx4 a[76:79], v207, s[28:31], s56 offen offset:3072 sc1\n"
"v_mfma_f32_16x16x32_bf16 v[226:229], a[204:207], a[24:27], v[226:229]\n"
"s_add_u32 s56, s56, 4096\n"
"ds_read_b64_tr_b16 a[148:149], v201 offset:26624\n"
"v_mfma_f32_16x16x32_bf16 v[230:233], a[204:207], a[28:31], v[230:233]\n"
"s_waitcnt lgkmcnt(11)\n"
"s_waitcnt vmcnt(10)\n"
"ds_read_b64_tr_b16 a[150:151], v201 offset:31232\n"
"v_mfma_f32_16x16x32_bf16 v[64:67], a[208:211], a[32:35], v[64:67]\n"
"ds_read_b64_tr_b16 a[152:153], v201 offset:26752\n"
"v_mfma_f32_16x16x32_bf16 v[68:71], a[208:211], a[36:39], v[68:71]\n"
"ds_read_b64_tr_b16 a[154:155], v201 offset:31360\n"
"v_mfma_f32_16x16x32_bf16 v[72:75], a[212:215], a[32:35], v[72:75]\n"
"s_waitcnt vmcnt(30)\n"
"s_barrier\n"
"ds_read_b64_tr_b16 a[156:157], v201 offset:26880\n"
"v_mfma_f32_16x16x32_bf16 v[76:79], a[212:215], a[36:39], v[76:79]\n"
"ds_read_b64_tr_b16 a[158:159], v201 offset:31488\n"
"v_mfma_f32_16x16x32_bf16 v[80:83], a[216:219], a[32:35], v[80:83]\n"
"s_add_u32 m0, s53, 73728\n"
"ds_read_b64_tr_b16 a[192:193], v201 offset:27008\n"
"v_mfma_f32_16x16x32_bf16 v[84:87], a[216:219], a[36:39], v[84:87]\n"
"ds_read_b64_tr_b16 a[194:195], v201 offset:31616\n"
"buffer_load_dwordx4 v210, s[24:27], s38 offen lds\n"
"v_mfma_f32_16x16x32_bf16 v[88:91], a[220:223], a[32:35], v[88:91]\n"
"ds_read_b64_tr_b16 a[196:197], v201 offset:27136\n"
"buffer_load_dwordx4 a[80:83], v207, s[28:31], s57 offen offset:2048 sc1\n"
"v_mfma_f32_16x16x32_bf16 v[92:95], a[220:223], a[36:39], v[92:95]\n"
"s_waitcnt lgkmcnt(11)\n"
"ds_read_b64_tr_b16 a[198:199], v201 offset:31744\n"
"v_mfma_f32_16x16x32_bf16 v[96:99], a[128:131], a[32:35], v[96:99]\n"
"ds_read_b64_tr_b16 a[200:201], v201 offset:27264\n"
"v_mfma_f32_16x16x32_bf16 v[100:103], a[128:131], a[36:39], v[100:103]\n"
"ds_read_b64_tr_b16 a[202:203], v201 offset:31872\n"
"v_mfma_f32_16x16x32_bf16 v[104:107], a[132:135], a[32:35], v[104:107]\n"
"ds_read_b64_tr_b16 a[204:205], v201 offset:27392\n"
"v_mfma_f32_16x16x32_bf16 v[108:111], a[132:135], a[36:39], v[108:111]\n"
"ds_read_b64_tr_b16 a[206:207], v201 offset:32000\n"
"v_mfma_f32_16x16x32_bf16 v[112:115], a[136:139], a[32:35], v[112:115]\n"
"ds_read_b64_tr_b16 a[208:209], v201 offset:27520\n"
"v_mfma_f32_16x16x32_bf16 v[116:119], a[136:139], a[36:39], v[116:119]\n"
"ds_read_b64_tr_b16 a[210:211], v201 offset:32128\n"
"v_mfma_f32_16x16x32_bf16 v[120:123], a[140:143], a[32:35], v[120:123]\n"
"ds_read_b64_tr_b16 a[212:213], v204 offset:26624\n"
"v_mfma_f32_16x16x32_bf16 v[124:127], a[140:143], a[36:39], v[124:127]\n"
"s_waitcnt lgkmcnt(11)\n"
"s_add_u32 m0, s53, 74752\n"
"ds_read_b64_tr_b16 a[214:215], v204 offset:31232\n"
"buffer_load_dwordx4 a[84:87], v207, s[28:31], s57 offen offset:3072 sc1\n"
"v_mfma_f32_16x16x32_bf16 v[226:229], a[144:147], a[32:35], v[226:229]\n"
"s_add_u32 s57, s57, 4096\n"
"ds_read_b64_tr_b16 a[216:217], v201 offset:35840\n"
"buffer_load_dwordx4 v211, s[24:27], s38 offen lds\n"
"v_mfma_f32_16x16x32_bf16 v[230:233], a[144:147], a[36:39], v[230:233]\n"
"s_waitcnt vmcnt(12)\n"
"ds_read_b64_tr_b16 a[218:219], v201 offset:40448\n"
"v_mfma_f32_16x16x32_bf16 v[64:67], a[148:151], a[40:43], v[64:67]\n"
"ds_read_b64_tr_b16 a[220:221], v201 offset:35968\n"
"v_mfma_f32_16x16x32_bf16 v[68:71], a[148:151], a[44:47], v[68:71]\n"
"ds_read_b64_tr_b16 a[222:223], v201 offset:40576\n"
"v_mfma_f32_16x16x32_bf16 v[72:75], a[152:155], a[40:43], v[72:75]\n"
"ds_read_b64_tr_b16 a[128:129], v201 offset:36096\n"
"v_mfma_f32_16x16x32_bf16 v[76:79], a[152:155], a[44:47], v[76:79]\n"
"ds_read_b64_tr_b16 a[130:131], v201 offset:40704\n"
"v_mfma_f32_16x16x32_bf16 v[80:83], a[156:159], a[40:43], v[80:83]\n"
"ds_read_b64_tr_b16 a[132:133], v201 offset:36224\n"
"v_mfma_f32_16x16x32_bf16 v[84:87], a[156:159], a[44:47], v[84:87]\n"
"s_waitcnt lgkmcnt(11)\n"
"ds_read_b64_tr_b16 a[134:135], v201 offset:40832\n"
"v_mfma_f32_16x16x32_bf16 v[88:91], a[192:195], a[40:43], v[88:91]\n"
"ds_read_b64_tr_b16 a[136:137], v201 offset:36352\n"
"buffer_load_dwordx4 a[88:91], v207, s[28:31], s58 offen offset:2048 sc1\n"
"v_mfma_f32_16x16x32_bf16 v[92:95], a[192:195], a[44:47], v[92:95]\n"
"ds_read_b64_tr_b16 a[138:139], v201 offset:40960\n"
"v_mfma_f32_16x16x32_bf16 v[96:99], a[196:199], a[40:43], v[96:99]\n"
"s_add_u32 m0, s53, 75776\n"
"ds_read_b64_tr_b16 a[140:141], v201 offset:36480\n"
"v_mfma_f32_16x16x32_bf16 v[100:103], a[196:199], a[44:47], v[100:103]\n"
"ds_read_b64_tr_b16 a[142:143], v201 offset:41088\n"
"buffer_load_dwordx4 v212, s[24:27], s38 offen lds\n"
"v_mfma_f32_16x16x32_bf16 v[104:107], a[200:203], a[40:43], v[104:107]\n"
"ds_read_b64_tr_b16 a[144:145], v201 offset:36608\n"
"v_mfma_f32_16x16x32_bf16 v[108:111], a[200:203], a[44:47], v[108:111]\n"
"ds_read_b64_tr_b16 a[146:147], v201 offset:41216\n"
"v_mfma_f32_16x16x32_bf16 v[112:115], a[204:207], a[40:43], v[112:115]\n"
"ds_read_b64_tr_b16 a[148:149], v201 offset:36736\n"
"v_mfma_f32_16x16x32_bf16 v[116:119], a[204:207], a[44:47], v[116:119]\n"
"s_waitcnt lgkmcnt(11)\n"
"ds_read_b64_tr_b16 a[150:151], v201 offset:41344\n"
"v_mfma_f32_16x16x32_bf16 v[120:123], a[208:211], a[40:43], v[120:123]\n"
"ds_read_b64_tr_b16 a[152:153], v204 offset:35840\n"
"v_mfma_f32_16x16x32_bf16 v[124:127], a[208:211], a[44:47], v[124:127]\n"
"ds_read_b64_tr_b16 a[154:155], v204 offset:40448\n"
"buffer_load_dwordx4 a[92:95], v207, s[28:31], s58 offen offset:3072 sc1\n"
"v_mfma_f32_16x16x32_bf16 v[226:229], a[212:215], a[40:43], v[226:229]\n"
"s_add_u32 s58, s58, 4096\n"
"ds_read_b64_tr_b16 a[156:157], v201 offset:45056\n"
"v_mfma_f32_16x16x32_bf16 v[230:233], a[212:215], a[44:47], v[230:233]\n"
"s_waitcnt vmcnt(13)\n"
"ds_read_b64_tr_b16 a[158:159], v201 offset:49664\n"
"v_mfma_f32_16x16x32_bf16 v[64:67], a[216:219], a[48:51], v[64:67]\n"
"ds_read_b64_tr_b16 a[192:193], v201 offset:45184\n"
"v_mfma_f32_16x16x32_bf16 v[68:71], a[216:219], a[52:55], v[68:71]\n"
"s_add_u32 m0, s53, 76800\n"
"ds_read_b64_tr_b16 a[194:195], v201 offset:49792\n"
"v_mfma_f32_16x16x32_bf16 v[72:75], a[220:223], a[48:51], v[72:75]\n"
"ds_read_b64_tr_b16 a[196:197], v201 offset:45312\n"
"buffer_load_dwordx4 v213, s[24:27], s38 offen lds\n"
"v_mfma_f32_16x16x32_bf16 v[76:79], a[220:223], a[52:55], v[76:79]\n"
"s_waitcnt lgkmcnt(11)\n"
"ds_read_b64_tr_b16 a[198:199], v201 offset:49920\n"
"v_mfma_f32_16x16x32_bf16 v[80:83], a[128:131], a[48:51], v[80:83]\n"
"ds_read_b64_tr_b16 a[200:201], v201 offset:45440\n"
"v_mfma_f32_16x16x32_bf16 v[84:87], a[128:131], a[52:55], v[84:87]\n"
"ds_read_b64_tr_b16 a[202:203], v201 offset:50048\n"
"v_mfma_f32_16x16x32_bf16 v[88:91], a[132:135], a[48:51], v[88:91]\n"
"ds_read_b64_tr_b16 a[204:205], v201 offset:45568\n"
"buffer_load_dwordx4 a[96:99], v207, s[28:31], s56 offen offset:0 sc1\n"
"v_mfma_f32_16x16x32_bf16 v[92:95], a[132:135], a[52:55], v[92:95]\n"
"ds_read_b64_tr_b16 a[206:207], v201 offset:50176\n"
"v_mfma_f32_16x16x32_bf16 v[96:99], a[136:139], a[48:51], v[96:99]\n"
"ds_read_b64_tr_b16 a[208:209], v201 offset:45696\n"
"v_mfma_f32_16x16x32_bf16 v[100:103], a[136:139], a[52:55], v[100:103]\n"
"ds_read_b64_tr_b16 a[210:211], v201 offset:50304\n"
"v_mfma_f32_16x16x32_bf16 v[104:107], a[140:143], a[48:51], v[104:107]\n"
"ds_read_b64_tr_b16 a[212:213], v201 offset:45824\n"
"v_mfma_f32_16x16x32_bf16 v[108:111], a[140:143], a[52:55], v[108:111]\n"
"s_waitcnt lgkmcnt(11)\n"
"ds_read_b64_tr_b16 a[214:215], v201 offset:50432\n"
"v_mfma_f32_16x16x32_bf16 v[112:115], a[144:147], a[48:51], v[112:115]\n"
"s_add_u32 m0, s53, 77824\n"
"ds_read_b64_tr_b16 a[216:217], v201 offset:45952\n"
"v_mfma_f32_16x16x32_bf16 v[116:119], a[144:147], a[52:55], v[116:119]\n"
"ds_read_b64_tr_b16 a[218:219], v201 offset:50560\n"
"buffer_load_dwordx4 v214, s[24:27], s38 offen lds\n"
"v_mfma_f32_16x16x32_bf16 v[120:123], a[148:151], a[48:51], v[120:123]\n"
"ds_read_b64_tr_b16 a[220:221], v204 offset:45056\n"
"v_mfma_f32_16x16x32_bf16 v[124:127], a[148:151], a[52:55], v[124:127]\n"
"ds_read_b64_tr_b16 a[222:223], v204 offset:49664\n"
"buffer_load_dwordx4 a[100:103], v207, s[28:31], s56 offen offset:1024 sc1\n"
"v_mfma_f32_16x16x32_bf16 v[226:229], a[152:155], a[48:51], v[226:229]\n"
"ds_read_b64_tr_b16 a[128:129], v202 offset:0\n"
"v_mfma_f32_16x16x32_bf16 v[230:233], a[152:155], a[52:55], v[230:233]\n"
"s_waitcnt vmcnt(15)\n"
"ds_read_b64_tr_b16 a[130:131], v202 offset:4608\n"
"v_mfma_f32_16x16x32_bf16 v[64:67], a[156:159], a[56:59], v[64:67]\n"
"ds_read_b64_tr_b16 a[132:133], v202 offset:128\n"
"v_mfma_f32_16x16x32_bf16 v[68:71], a[156:159], a[60:63], v[68:71]\n"
"s_waitcnt lgkmcnt(11)\n"
"ds_read_b64_tr_b16 a[134:135], v202 offset:4736\n"
"v_mfma_f32_16x16x32_bf16 v[72:75], a[192:195], a[56:59], v[72:75]\n"
"ds_read_b64_tr_b16 a[136:137], v202 offset:256\n"
"v_mfma_f32_16x16x32_bf16 v[76:79], a[192:195], a[60:63], v[76:79]\n"
"ds_read_b64_tr_b16 a[138:139], v202 offset:4864\n"
"v_mfma_f32_16x16x32_bf16 v[80:83], a[196:199], a[56:59], v[80:83]\n"
"ds_read_b64_tr_b16 a[140:141], v202 offset:384\n"
"v_mfma_f32_16x16x32_bf16 v[84:87], a[196:199], a[60:63], v[84:87]\n"
"ds_read_b64_tr_b16 a[142:143], v202 offset:4992\n"
"v_mfma_f32_16x16x32_bf16 v[88:91], a[200:203], a[56:59], v[88:91]\n"
"s_add_u32 m0, s53, 78848\n"
"ds_read_b64_tr_b16 a[144:145], v202 offset:512\n"
"buffer_load_dwordx4 a[104:107], v207, s[28:31], s57 offen offset:0 sc1\n"
"v_mfma_f32_16x16x32_bf16 v[92:95], a[200:203], a[60:63], v[92:95]\n"
"ds_read_b64_tr_b16 a[146:147], v202 offset:5120\n"
"buffer_load_dwordx4 v236, s[24:27], s38 offen lds\n"
"v_mfma_f32_16x16x32_bf16 v[96:99], a[204:207], a[56:59], v[96:99]\n"
"ds_read_b64_tr_b16 a[148:149], v202 offset:640\n"
"v_mfma_f32_16x16x32_bf16 v[100:103], a[204:207], a[60:63], v[100:103]\n"
"s_waitcnt lgkmcnt(11)\n"
"ds_read_b64_tr_b16 a[150:151], v202 offset:5248\n"
"v_mfma_f32_16x16x32_bf16 v[104:107], a[208:211], a[56:59], v[104:107]\n"
"ds_read_b64_tr_b16 a[152:153], v202 offset:768\n"
"v_mfma_f32_16x16x32_bf16 v[108:111], a[208:211], a[60:63], v[108:111]\n"
"ds_read_b64_tr_b16 a[154:155], v202 offset:5376\n"
"v_mfma_f32_16x16x32_bf16 v[112:115], a[212:215], a[56:59], v[112:115]\n"
"ds_read_b64_tr_b16 a[156:157], v202 offset:896\n"
"v_mfma_f32_16x16x32_bf16 v[116:119], a[212:215], a[60:63], v[116:119]\n"
"ds_read_b64_tr_b16 a[158:159], v202 offset:5504\n"
"v_mfma_f32_16x16x32_bf16 v[120:123], a[216:219], a[56:59], v[120:123]\n"
"ds_read_b64_tr_b16 a[192:193], v205 offset:0\n"
"v_mfma_f32_16x16x32_bf16 v[124:127], a[216:219], a[60:63], v[124:127]\n"
"ds_read_b64_tr_b16 a[194:195], v205 offset:4608\n"
"buffer_load_dwordx4 a[108:111], v207, s[28:31], s57 offen offset:1024 sc1\n"
"v_mfma_f32_16x16x32_bf16 v[226:229], a[220:223], a[56:59], v[226:229]\n"
"ds_read_b64_tr_b16 a[196:197], v202 offset:9216\n"
"v_mfma_f32_16x16x32_bf16 v[230:233], a[220:223], a[60:63], v[230:233]\n"
"s_waitcnt lgkmcnt(11)\n"
"s_waitcnt vmcnt(16)\n"
"ds_read_b64_tr_b16 a[198:199], v202 offset:13824\n"
"v_mfma_f32_16x16x32_bf16 v[64:67], a[128:131], a[64:67], v[64:67]\n"
"s_add_u32 m0, s53, 79872\n"
"ds_read_b64_tr_b16 a[200:201], v202 offset:9344\n"
"v_mfma_f32_16x16x32_bf16 v[68:71], a[128:131], a[68:71], v[68:71]\n"
"ds_read_b64_tr_b16 a[202:203], v202 offset:13952\n"
"buffer_load_dwordx4 v237, s[24:27], s38 offen lds\n"
"v_mfma_f32_16x16x32_bf16 v[72:75], a[132:135], a[64:67], v[72:75]\n"
"s_waitcnt vmcnt(36)\n"
"s_barrier\n"
"ds_read_b64_tr_b16 a[204:205], v202 offset:9472\n"
"v_mfma_f32_16x16x32_bf16 v[76:79], a[132:135], a[68:71], v[76:79]\n"
"ds_read_b64_tr_b16 a[206:207], v202 offset:14080\n"
"v_mfma_f32_16x16x32_bf16 v[80:83], a[136:139], a[64:67], v[80:83]\n"
"ds_read_b64_tr_b16 a[208:209], v202 offset:9600\n"
"v_mfma_f32_16x16x32_bf16 v[84:87], a[136:139], a[68:71], v[84:87]\n"
"ds_read_b64_tr_b16 a[210:211], v202 offset:14208\n"
"v_mfma_f32_16x16x32_bf16 v[88:91], a[140:143], a[64:67], v[88:91]\n"
"ds_read_b64_tr_b16 a[212:213], v202 offset:9728\n"
"buffer_load_dwordx4 a[112:115], v207, s[28:31], s58 offen offset:0 sc1\n"
"v_mfma_f32_16x16x32_bf16 v[92:95], a[140:143], a[68:71], v[92:95]\n"
"s_waitcnt lgkmcnt(11)\n"
"ds_read_b64_tr_b16 a[214:215], v202 offset:14336\n"
"v_mfma_f32_16x16x32_bf16 v[96:99], a[144:147], a[64:67], v[96:99]\n"
"ds_read_b64_tr_b16 a[216:217], v202 offset:9856\n"
"v_mfma_f32_16x16x32_bf16 v[100:103], a[144:147], a[68:71], v[100:103]\n"
"ds_read_b64_tr_b16 a[218:219], v202 offset:14464\n"
"v_mfma_f32_16x16x32_bf16 v[104:107], a[148:151], a[64:67], v[104:107]\n"
"ds_read_b64_tr_b16 a[220:221], v202 offset:9984\n"
"v_mfma_f32_16x16x32_bf16 v[108:111], a[148:151], a[68:71], v[108:111]\n"
"s_add_u32 m0, s53, 80896\n"
"ds_read_b64_tr_b16 a[222:223], v202 offset:14592\n"
"v_mfma_f32_16x16x32_bf16 v[112:115], a[152:155], a[64:67], v[112:115]\n"
"ds_read_b64_tr_b16 a[128:129], v202 offset:10112\n"
"buffer_load_dwordx4 v238, s[24:27], s38 offen lds\n"
"v_mfma_f32_16x16x32_bf16 v[116:119], a[152:155], a[68:71], v[116:119]\n"
"ds_read_b64_tr_b16 a[130:131], v202 offset:14720\n"
"v_mfma_f32_16x16x32_bf16 v[120:123], a[156:159], a[64:67], v[120:123]\n"
"ds_read_b64_tr_b16 a[132:133], v205 offset:9216\n"
"v_mfma_f32_16x16x32_bf16 v[124:127], a[156:159], a[68:71], v[124:127]\n"
"s_waitcnt lgkmcnt(11)\n"
"ds_read_b64_tr_b16 a[134:135], v205 offset:13824\n"
"buffer_load_dwordx4 a[116:119], v207, s[28:31], s58 offen offset:1024 sc1\n"
"v_mfma_f32_16x16x32_bf16 v[226:229], a[192:195], a[64:67], v[226:229]\n"
"ds_read_b64_tr_b16 a[136:137], v202 offset:18432\n"
"v_mfma_f32_16x16x32_bf16 v[230:233], a[192:195], a[68:71], v[230:233]\n"
"s_waitcnt vmcnt(18)\n"
"ds_read_b64_tr_b16 a[138:139], v202 offset:23040\n"
"v_mfma_f32_16x16x32_bf16 v[64:67], a[196:199], a[72:75], v[64:67]\n"
"ds_read_b64_tr_b16 a[140:141], v202 offset:18560\n"
"v_mfma_f32_16x16x32_bf16 v[68:71], a[196:199], a[76:79], v[68:71]\n"
"ds_read_b64_tr_b16 a[142:143], v202 offset:23168\n"
"v_mfma_f32_16x16x32_bf16 v[72:75], a[200:203], a[72:75], v[72:75]\n"
"ds_read_b64_tr_b16 a[144:145], v202 offset:18688\n"
"v_mfma_f32_16x16x32_bf16 v[76:79], a[200:203], a[76:79], v[76:79]\n"
"ds_read_b64_tr_b16 a[146:147], v202 offset:23296\n"
"v_mfma_f32_16x16x32_bf16 v[80:83], a[204:207], a[72:75], v[80:83]\n"
"s_add_u32 m0, s53, 81920\n"
"ds_read_b64_tr_b16 a[148:149], v202 offset:18816\n"
"v_mfma_f32_16x16x32_bf16 v[84:87], a[204:207], a[76:79], v[84:87]\n"
"s_waitcnt lgkmcnt(11)\n"
"ds_read_b64_tr_b16 a[150:151], v202 offset:23424\n"
"buffer_load_dwordx4 v239, s[24:27], s38 offen lds\n"
"v_mfma_f32_16x16x32_bf16 v[88:91], a[208:211], a[72:75], v[88:91]\n"
"ds_read_b64_tr_b16 a[152:153], v202 offset:18944\n"
"buffer_load_dwordx4 a[120:123], v207, s[28:31], s56 offen offset:2048 sc1\n"
"v_mfma_f32_16x16x32_bf16 v[92:95], a[208:211], a[76:79], v[92:95]\n"
"ds_read_b64_tr_b16 a[154:155], v202 offset:23552\n"
"v_mfma_f32_16x16x32_bf16 v[96:99], a[212:215], a[72:75], v[96:99]\n"
"ds_read_b64_tr_b16 a[156:157], v202 offset:19072\n"
"v_mfma_f32_16x16x32_bf16 v[100:103], a[212:215], a[76:79], v[100:103]\n"
"ds_read_b64_tr_b16 a[158:159], v202 offset:23680\n"
"v_mfma_f32_16x16x32_bf16 v[104:107], a[216:219], a[72:75], v[104:107]\n"
"ds_read_b64_tr_b16 a[192:193], v202 offset:19200\n"
"v_mfma_f32_16x16x32_bf16 v[108:111], a[216:219], a[76:79], v[108:111]\n"
"ds_read_b64_tr_b16 a[194:195], v202 offset:23808\n"
"v_mfma_f32_16x16x32_bf16 v[112:115], a[220:223], a[72:75], v[112:115]\n"
"ds_read_b64_tr_b16 a[196:197], v202 offset:19328\n"
"v_mfma_f32_16x16x32_bf16 v[116:119], a[220:223], a[76:79], v[116:119]\n"
"s_waitcnt lgkmcnt(11)\n"
"ds_read_b64_tr_b16 a[198:199], v202 offset:23936\n"
"v_mfma_f32_16x16x32_bf16 v[120:123], a[128:131], a[72:75], v[120:123]\n"
"ds_read_b64_tr_b16 a[200:201], v205 offset:18432\n"
"v_mfma_f32_16x16x32_bf16 v[124:127], a[128:131], a[76:79], v[124:127]\n"
"s_add_u32 m0, s53, 110592\n"
"ds_read_b64_tr_b16 a[202:203], v205 offset:23040\n"
"buffer_load_dwordx4 a[124:127], v207, s[28:31], s56 offen offset:3072 sc1\n"
"v_mfma_f32_16x16x32_bf16 v[226:229], a[132:135], a[72:75], v[226:229]\n"
"s_add_u32 s56, s56, 4096\n"
"ds_read_b64_tr_b16 a[204:205], v200 offset:0\n"
"buffer_load_dwordx4 v210, s[24:27], s39 offen lds\n"
"v_mfma_f32_16x16x32_bf16 v[230:233], a[132:135], a[76:79], v[230:233]\n"
"s_waitcnt vmcnt(19)\n"
"ds_read_b64_tr_b16 a[206:207], v200 offset:4608\n"
"v_mfma_f32_16x16x32_bf16 v[64:67], a[136:139], a[80:83], v[64:67]\n"
"ds_read_b64_tr_b16 a[208:209], v200 offset:128\n"
"v_mfma_f32_16x16x32_bf16 v[68:71], a[136:139], a[84:87], v[68:71]\n"
"ds_read_b64_tr_b16 a[210:211], v200 offset:4736\n"
"v_mfma_f32_16x16x32_bf16 v[72:75], a[140:143], a[80:83], v[72:75]\n"
"ds_read_b64_tr_b16 a[212:213], v200 offset:256\n"
"v_mfma_f32_16x16x32_bf16 v[76:79], a[140:143], a[84:87], v[76:79]\n"
"s_waitcnt lgkmcnt(11)\n"
"ds_read_b64_tr_b16 a[214:215], v200 offset:4864\n"
"v_mfma_f32_16x16x32_bf16 v[80:83], a[144:147], a[80:83], v[80:83]\n"
"ds_read_b64_tr_b16 a[216:217], v200 offset:384\n"
"v_mfma_f32_16x16x32_bf16 v[84:87], a[144:147], a[84:87], v[84:87]\n"
"ds_read_b64_tr_b16 a[218:219], v200 offset:4992\n"
"v_mfma_f32_16x16x32_bf16 v[88:91], a[148:151], a[80:83], v[88:91]\n"
"ds_read_b64_tr_b16 a[220:221], v200 offset:512\n"
"buffer_load_dwordx4 v[0:3], v207, s[28:31], s57 offen offset:2048 sc1\n"
"v_mfma_f32_16x16x32_bf16 v[92:95], a[148:151], a[84:87], v[92:95]\n"
"ds_read_b64_tr_b16 a[222:223], v200 offset:5120\n"
"v_mfma_f32_16x16x32_bf16 v[96:99], a[152:155], a[80:83], v[96:99]\n"
"s_add_u32 m0, s53, 111616\n"
"ds_read_b64_tr_b16 a[128:129], v200 offset:640\n"
"v_mfma_f32_16x16x32_bf16 v[100:103], a[152:155], a[84:87], v[100:103]\n"
"ds_read_b64_tr_b16 a[130:131], v200 offset:5248\n"
"buffer_load_dwordx4 v211, s[24:27], s39 offen lds\n"
"v_mfma_f32_16x16x32_bf16 v[104:107], a[156:159], a[80:83], v[104:107]\n"
"ds_read_b64_tr_b16 a[132:133], v200 offset:768\n"
"v_mfma_f32_16x16x32_bf16 v[108:111], a[156:159], a[84:87], v[108:111]\n"
"s_waitcnt lgkmcnt(11)\n"
"ds_read_b64_tr_b16 a[134:135], v200 offset:5376\n"
"v_mfma_f32_16x16x32_bf16 v[112:115], a[192:195], a[80:83], v[112:115]\n"
"ds_read_b64_tr_b16 a[136:137], v200 offset:896\n"
"v_mfma_f32_16x16x32_bf16 v[116:119], a[192:195], a[84:87], v[116:119]\n"
"ds_read_b64_tr_b16 a[138:139], v200 offset:5504\n"
"v_mfma_f32_16x16x32_bf16 v[120:123], a[196:199], a[80:83], v[120:123]\n"
"ds_read_b64_tr_b16 a[140:141], v203 offset:0\n"
"v_mfma_f32_16x16x32_bf16 v[124:127], a[196:199], a[84:87], v[124:127]\n"
"ds_read_b64_tr_b16 a[142:143], v203 offset:4608\n"
"buffer_load_dwordx4 v[4:7], v207, s[28:31], s57 offen offset:3072 sc1\n"
"v_mfma_f32_16x16x32_bf16 v[226:229], a[200:203], a[80:83], v[226:229]\n"
"s_add_u32 s57, s57, 4096\n"
"ds_read_b64_tr_b16 a[144:145], v200 offset:9216\n"
"v_mfma_f32_16x16x32_bf16 v[230:233], a[200:203], a[84:87], v[230:233]\n"
"s_waitcnt vmcnt(18)\n"
"ds_read_b64_tr_b16 a[146:147], v200 offset:13824\n"
"v_mfma_f32_16x16x32_bf16 v[64:67], a[204:207], a[88:91], v[64:67]\n"
"ds_read_b64_tr_b16 a[148:149], v200 offset:9344\n"
"v_mfma_f32_16x16x32_bf16 v[68:71], a[204:207], a[92:95], v[68:71]\n"
"s_waitcnt lgkmcnt(11)\n"
"s_add_u32 m0, s53, 112640\n"
"ds_read_b64_tr_b16 a[150:151], v200 offset:13952\n"
"v_mfma_f32_16x16x32_bf16 v[72:75], a[208:211], a[88:91], v[72:75]\n"
"ds_read_b64_tr_b16 a[152:153], v200 offset:9472\n"
"buffer_load_dwordx4 v212, s[24:27], s39 offen lds\n"
"v_mfma_f32_16x16x32_bf16 v[76:79], a[208:211], a[92:95], v[76:79]\n"
"ds_read_b64_tr_b16 a[154:155], v200 offset:14080\n"
"v_mfma_f32_16x16x32_bf16 v[80:83], a[212:215], a[88:91], v[80:83]\n"
"ds_read_b64_tr_b16 a[156:157], v200 offset:9600\n"
"v_mfma_f32_16x16x32_bf16 v[84:87], a[212:215], a[92:95], v[84:87]\n"
"ds_read_b64_tr_b16 a[158:159], v200 offset:14208\n"
"v_mfma_f32_16x16x32_bf16 v[88:91], a[216:219], a[88:91], v[88:91]\n"
"ds_read_b64_tr_b16 a[192:193], v200 offset:9728\n"
"buffer_load_dwordx4 v[8:11], v207, s[28:31], s58 offen offset:2048 sc1\n"
"v_mfma_f32_16x16x32_bf16 v[92:95], a[216:219], a[92:95], v[92:95]\n"
"ds_read_b64_tr_b16 a[194:195], v200 offset:14336\n"
"v_mfma_f32_16x16x32_bf16 v[96:99], a[220:223], a[88:91], v[96:99]\n"
"ds_read_b64_tr_b16 a[196:197], v200 offset:9856\n"
"v_mfma_f32_16x16x32_bf16 v[100:103], a[220:223], a[92:95], v[100:103]\n"
"s_waitcnt lgkmcnt(11)\n"
"ds_read_b64_tr_b16 a[198:199], v200 offset:14464\n"
"v_mfma_f32_16x16x32_bf16 v[104:107], a[128:131], a[88:91], v[104:107]\n"
"ds_read_b64_tr_b16 a[200:201], v200 offset:9984\n"
"v_mfma_f32_16x16x32_bf16 v[108:111], a[128:131], a[92:95], v[108:111]\n"
"ds_read_b64_tr_b16 a[202:203], v200 offset:14592\n"
"v_mfma_f32_16x16x32_bf16 v[112:115], a[132:135], a[88:91], v[112:115]\n"
"s_add_u32 m0, s53, 113664\n"
"ds_read_b64_tr_b16 a[204:205], v200 offset:10112\n"
"v_mfma_f32_16x16x32_bf16 v[116:119], a[132:135], a[92:95], v[116:119]\n"
"ds_read_b64_tr_b16 a[206:207], v200 offset:14720\n"
"buffer_load_dwordx4 v213, s[24:27], s39 offen lds\n"
"v_mfma_f32_16x16x32_bf16 v[120:123], a[136:139], a[88:91], v[120:123]\n"
"ds_read_b64_tr_b16 a[208:209], v203 offset:9216\n"
"v_mfma_f32_16x16x32_bf16 v[124:127], a[136:139], a[92:95], v[124:127]\n"
"ds_read_b64_tr_b16 a[210:211], v203 offset:13824\n"
"buffer_load_dwordx4 v[12:15], v207, s[28:31], s58 offen offset:3072 sc1\n"
"v_mfma_f32_16x16x32_bf16 v[226:229], a[140:143], a[88:91], v[226:229]\n"
"s_add_u32 s58, s58, 4096\n"
"ds_read_b64_tr_b16 a[212:213], v200 offset:18432\n"
"v_mfma_f32_16x16x32_bf16 v[230:233], a[140:143], a[92:95], v[230:233]\n"
"s_waitcnt lgkmcnt(11)\n"
"s_waitcnt vmcnt(18)\n"
"ds_read_b64_tr_b16 a[214:215], v200 offset:23040\n"
"v_mfma_f32_16x16x32_bf16 v[64:67], a[144:147], a[96:99], v[64:67]\n"
"ds_read_b64_tr_b16 a[216:217], v200 offset:18560\n"
"v_mfma_f32_16x16x32_bf16 v[68:71], a[144:147], a[100:103], v[68:71]\n"
"ds_read_b64_tr_b16 a[218:219], v200 offset:23168\n"
"v_mfma_f32_16x16x32_bf16 v[72:75], a[148:151], a[96:99], v[72:75]\n"
"ds_read_b64_tr_b16 a[220:221], v200 offset:18688\n"
"v_mfma_f32_16x16x32_bf16 v[76:79], a[148:151], a[100:103], v[76:79]\n"
"ds_read_b64_tr_b16 a[222:223], v200 offset:23296\n"
"v_mfma_f32_16x16x32_bf16 v[80:83], a[152:155], a[96:99], v[80:83]\n"
"ds_read_b64_tr_b16 a[128:129], v200 offset:18816\n"
"v_mfma_f32_16x16x32_bf16 v[84:87], a[152:155], a[100:103], v[84:87]\n"
"ds_read_b64_tr_b16 a[130:131], v200 offset:23424\n"
"v_mfma_f32_16x16x32_bf16 v[88:91], a[156:159], a[96:99], v[88:91]\n"
"s_add_u32 m0, s53, 114688\n"
"ds_read_b64_tr_b16 a[132:133], v200 offset:18944\n"
"buffer_load_dwordx4 v[16:19], v207, s[28:31], s56 offen offset:0 sc1\n"
"v_mfma_f32_16x16x32_bf16 v[92:95], a[156:159], a[100:103], v[92:95]\n"
"s_waitcnt lgkmcnt(11)\n"
"ds_read_b64_tr_b16 a[134:135], v200 offset:23552\n"
"buffer_load_dwordx4 v214, s[24:27], s39 offen lds\n"
"v_mfma_f32_16x16x32_bf16 v[96:99], a[192:195], a[96:99], v[96:99]\n"
"ds_read_b64_tr_b16 a[136:137], v200 offset:19072\n"
"v_mfma_f32_16x16x32_bf16 v[100:103], a[192:195], a[100:103], v[100:103]\n"
"ds_read_b64_tr_b16 a[138:139], v200 offset:23680\n"
"v_mfma_f32_16x16x32_bf16 v[104:107], a[196:199], a[96:99], v[104:107]\n"
"ds_read_b64_tr_b16 a[140:141], v200 offset:19200\n"
"v_mfma_f32_16x16x32_bf16 v[108:111], a[196:199], a[100:103], v[108:111]\n"
"ds_read_b64_tr_b16 a[142:143], v200 offset:23808\n"
"v_mfma_f32_16x16x32_bf16 v[112:115], a[200:203], a[96:99], v[112:115]\n"
"ds_read_b64_tr_b16 a[144:145], v200 offset:19328\n"
"v_mfma_f32_16x16x32_bf16 v[116:119], a[200:203], a[100:103], v[116:119]\n"
"ds_read_b64_tr_b16 a[146:147], v200 offset:23936\n"
"v_mfma_f32_16x16x32_bf16 v[120:123], a[204:207], a[96:99], v[120:123]\n"
"ds_read_b64_tr_b16 a[148:149], v203 offset:18432\n"
"v_mfma_f32_16x16x32_bf16 v[124:127], a[204:207], a[100:103], v[124:127]\n"
"s_waitcnt lgkmcnt(11)\n"
"ds_read_b64_tr_b16 a[150:151], v203 offset:23040\n"
"buffer_load_dwordx4 v[20:23], v207, s[28:31], s56 offen offset:1024 sc1\n"
"v_mfma_f32_16x16x32_bf16 v[226:229], a[208:211], a[96:99], v[226:229]\n"
"ds_read_b64_tr_b16 a[152:153], v200 offset:27648\n"
"v_mfma_f32_16x16x32_bf16 v[230:233], a[208:211], a[100:103], v[230:233]\n"
"s_waitcnt vmcnt(18)\n"
"ds_read_b64_tr_b16 a[154:155], v200 offset:32256\n"
"v_mfma_f32_16x16x32_bf16 v[64:67], a[212:215], a[104:107], v[64:67]\n"
"s_add_u32 m0, s53, 115712\n"
"ds_read_b64_tr_b16 a[156:157], v200 offset:27776\n"
"v_mfma_f32_16x16x32_bf16 v[68:71], a[212:215], a[108:111], v[68:71]\n"
"ds_read_b64_tr_b16 a[158:159], v200 offset:32384\n"
"buffer_load_dwordx4 v236, s[24:27], s39 offen lds\n"
"v_mfma_f32_16x16x32_bf16 v[72:75], a[216:219], a[104:107], v[72:75]\n"
"ds_read_b64_tr_b16 a[192:193], v200 offset:27904\n"
"v_mfma_f32_16x16x32_bf16 v[76:79], a[216:219], a[108:111], v[76:79]\n"
"ds_read_b64_tr_b16 a[194:195], v200 offset:32512\n"
"v_mfma_f32_16x16x32_bf16 v[80:83], a[220:223], a[104:107], v[80:83]\n"
"ds_read_b64_tr_b16 a[196:197], v200 offset:28032\n"
"v_mfma_f32_16x16x32_bf16 v[84:87], a[220:223], a[108:111], v[84:87]\n"
"s_waitcnt lgkmcnt(11)\n"
"ds_read_b64_tr_b16 a[198:199], v200 offset:32640\n"
"v_mfma_f32_16x16x32_bf16 v[88:91], a[128:131], a[104:107], v[88:91]\n"
"ds_read_b64_tr_b16 a[200:201], v200 offset:28160\n"
"buffer_load_dwordx4 v[24:27], v207, s[28:31], s57 offen offset:0 sc1\n"
"v_mfma_f32_16x16x32_bf16 v[92:95], a[128:131], a[108:111], v[92:95]\n"
"ds_read_b64_tr_b16 a[202:203], v200 offset:32768\n"
"v_mfma_f32_16x16x32_bf16 v[96:99], a[132:135], a[104:107], v[96:99]\n"
"ds_read_b64_tr_b16 a[204:205], v200 offset:28288\n"
"v_mfma_f32_16x16x32_bf16 v[100:103], a[132:135], a[108:111], v[100:103]\n"
"ds_read_b64_tr_b16 a[206:207], v200 offset:32896\n"
"v_mfma_f32_16x16x32_bf16 v[104:107], a[136:139], a[104:107], v[104:107]\n"
"ds_read_b64_tr_b16 a[208:209], v200 offset:28416\n"
"v_mfma_f32_16x16x32_bf16 v[108:111], a[136:139], a[108:111], v[108:111]\n"
"s_add_u32 m0, s53, 116736\n"
"ds_read_b64_tr_b16 a[210:211], v200 offset:33024\n"
"v_mfma_f32_16x16x32_bf16 v[112:115], a[140:143], a[104:107], v[112:115]\n"
"ds_read_b64_tr_b16 a[212:213], v200 offset:28544\n"
"buffer_load_dwordx4 v237, s[24:27], s39 offen lds\n"
"v_mfma_f32_16x16x32_bf16 v[116:119], a[140:143], a[108:111], v[116:119]\n"
"s_waitcnt lgkmcnt(11)\n"
"ds_read_b64_tr_b16 a[214:215], v200 offset:33152\n"
"v_mfma_f32_16x16x32_bf16 v[120:123], a[144:147], a[104:107], v[120:123]\n"
"ds_read_b64_tr_b16 a[216:217], v203 offset:27648\n"
"v_mfma_f32_16x16x32_bf16 v[124:127], a[144:147], a[108:111], v[124:127]\n"
"ds_read_b64_tr_b16 a[218:219], v203 offset:32256\n"
"buffer_load_dwordx4 v[28:31], v207, s[28:31], s57 offen offset:1024 sc1\n"
"v_mfma_f32_16x16x32_bf16 v[226:229], a[148:151], a[104:107], v[226:229]\n"
"ds_read_b64_tr_b16 a[220:221], v200 offset:36864\n"
"v_mfma_f32_16x16x32_bf16 v[230:233], a[148:151], a[108:111], v[230:233]\n"
"s_waitcnt vmcnt(18)\n"
"ds_read_b64_tr_b16 a[222:223], v200 offset:41472\n"
"v_mfma_f32_16x16x32_bf16 v[64:67], a[152:155], a[112:115], v[64:67]\n"
"ds_read_b64_tr_b16 a[128:129], v200 offset:36992\n"
"v_mfma_f32_16x16x32_bf16 v[68:71], a[152:155], a[116:119], v[68:71]\n"
"ds_read_b64_tr_b16 a[130:131], v200 offset:41600\n"
"v_mfma_f32_16x16x32_bf16 v[72:75], a[156:159], a[112:115], v[72:75]\n"
"ds_read_b64_tr_b16 a[132:133], v200 offset:37120\n"
"v_mfma_f32_16x16x32_bf16 v[76:79], a[156:159], a[116:119], v[76:79]\n"
"s_waitcnt lgkmcnt(11)\n"
"ds_read_b64_tr_b16 a[134:135], v200 offset:41728\n"
"v_mfma_f32_16x16x32_bf16 v[80:83], a[192:195], a[112:115], v[80:83]\n"
"s_add_u32 m0, s53, 117760\n"
"ds_read_b64_tr_b16 a[136:137], v200 offset:37248\n"
"v_mfma_f32_16x16x32_bf16 v[84:87], a[192:195], a[116:119], v[84:87]\n"
"ds_read_b64_tr_b16 a[138:139], v200 offset:41856\n"
"buffer_load_dwordx4 v238, s[24:27], s39 offen lds\n"
"v_mfma_f32_16x16x32_bf16 v[88:91], a[196:199], a[112:115], v[88:91]\n"
"ds_read_b64_tr_b16 a[140:141], v200 offset:37376\n"
"buffer_load_dwordx4 v[32:35], v207, s[28:31], s58 offen offset:0 sc1\n"
"v_mfma_f32_16x16x32_bf16 v[92:95], a[196:199], a[116:119], v[92:95]\n"
"ds_read_b64_tr_b16 a[142:143], v200 offset:41984\n"
"v_mfma_f32_16x16x32_bf16 v[96:99], a[200:203], a[112:115], v[96:99]\n"
"ds_read_b64_tr_b16 a[144:145], v200 offset:37504\n"
"v_mfma_f32_16x16x32_bf16 v[100:103], a[200:203], a[116:119], v[100:103]\n"
"ds_read_b64_tr_b16 a[146:147], v200 offset:42112\n"
"v_mfma_f32_16x16x32_bf16 v[104:107], a[204:207], a[112:115], v[104:107]\n"
"ds_read_b64_tr_b16 a[148:149], v200 offset:37632\n"
"v_mfma_f32_16x16x32_bf16 v[108:111], a[204:207], a[116:119], v[108:111]\n"
"s_waitcnt lgkmcnt(11)\n"
"ds_read_b64_tr_b16 a[150:151], v200 offset:42240\n"
"v_mfma_f32_16x16x32_bf16 v[112:115], a[208:211], a[112:115], v[112:115]\n"
"s_waitcnt vmcnt(19)\n"
"s_barrier\n"
"ds_read_b64_tr_b16 a[152:153], v200 offset:37760\n"
"v_mfma_f32_16x16x32_bf16 v[116:119], a[208:211], a[116:119], v[116:119]\n"
"ds_read_b64_tr_b16 a[154:155], v200 offset:42368\n"
"v_mfma_f32_16x16x32_bf16 v[120:123], a[212:215], a[112:115], v[120:123]\n"
"ds_read_b64_tr_b16 a[156:157], v203 offset:36864\n"
"v_mfma_f32_16x16x32_bf16 v[124:127], a[212:215], a[116:119], v[124:127]\n"
"s_add_u32 m0, s53, 118784\n"
"ds_read_b64_tr_b16 a[158:159], v203 offset:41472\n"
"buffer_load_dwordx4 v[36:39], v207, s[28:31], s58 offen offset:1024 sc1\n"
"v_mfma_f32_16x16x32_bf16 v[226:229], a[216:219], a[112:115], v[226:229]\n"
"ds_read_b64_tr_b16 a[192:193], v200 offset:46080\n"
"buffer_load_dwordx4 v239, s[24:27], s39 offen lds\n"
"v_mfma_f32_16x16x32_bf16 v[230:233], a[216:219], a[116:119], v[230:233]\n"
"s_waitcnt vmcnt(19)\n"
"ds_read_b64_tr_b16 a[194:195], v200 offset:50688\n"
"v_mfma_f32_16x16x32_bf16 v[64:67], a[220:223], a[120:123], v[64:67]\n"
"ds_read_b64_tr_b16 a[196:197], v200 offset:46208\n"
"v_mfma_f32_16x16x32_bf16 v[68:71], a[220:223], a[124:127], v[68:71]\n"
"s_waitcnt lgkmcnt(11)\n"
"ds_read_b64_tr_b16 a[198:199], v200 offset:50816\n"
"v_mfma_f32_16x16x32_bf16 v[72:75], a[128:131], a[120:123], v[72:75]\n"
"ds_read_b64_tr_b16 a[200:201], v200 offset:46336\n"
"v_mfma_f32_16x16x32_bf16 v[76:79], a[128:131], a[124:127], v[76:79]\n"
"ds_read_b64_tr_b16 a[202:203], v200 offset:50944\n"
"v_mfma_f32_16x16x32_bf16 v[80:83], a[132:135], a[120:123], v[80:83]\n"
"ds_read_b64_tr_b16 a[204:205], v200 offset:46464\n"
"v_mfma_f32_16x16x32_bf16 v[84:87], a[132:135], a[124:127], v[84:87]\n"
"ds_read_b64_tr_b16 a[206:207], v200 offset:51072\n"
"v_mfma_f32_16x16x32_bf16 v[88:91], a[136:139], a[120:123], v[88:91]\n"
"ds_read_b64_tr_b16 a[208:209], v200 offset:46592\n"
"buffer_load_dwordx4 v[40:43], v207, s[28:31], s56 offen offset:2048 sc1\n"
"v_mfma_f32_16x16x32_bf16 v[92:95], a[136:139], a[124:127], v[92:95]\n"
"ds_read_b64_tr_b16 a[210:211], v200 offset:51200\n"
"v_mfma_f32_16x16x32_bf16 v[96:99], a[140:143], a[120:123], v[96:99]\n"
"ds_read_b64_tr_b16 a[212:213], v200 offset:46720\n"
"v_mfma_f32_16x16x32_bf16 v[100:103], a[140:143], a[124:127], v[100:103]\n"
"s_waitcnt lgkmcnt(11)\n"
"ds_read_b64_tr_b16 a[214:215], v200 offset:51328\n"
"v_mfma_f32_16x16x32_bf16 v[104:107], a[144:147], a[120:123], v[104:107]\n"
"ds_read_b64_tr_b16 a[216:217], v200 offset:46848\n"
"v_mfma_f32_16x16x32_bf16 v[108:111], a[144:147], a[124:127], v[108:111]\n"
"ds_read_b64_tr_b16 a[218:219], v200 offset:51456\n"
"v_mfma_f32_16x16x32_bf16 v[112:115], a[148:151], a[120:123], v[112:115]\n"
"ds_read_b64_tr_b16 a[220:221], v200 offset:46976\n"
"v_mfma_f32_16x16x32_bf16 v[116:119], a[148:151], a[124:127], v[116:119]\n"
"ds_read_b64_tr_b16 a[222:223], v200 offset:51584\n"
"v_mfma_f32_16x16x32_bf16 v[120:123], a[152:155], a[120:123], v[120:123]\n"
"ds_read_b64_tr_b16 a[128:129], v203 offset:46080\n"
"v_mfma_f32_16x16x32_bf16 v[124:127], a[152:155], a[124:127], v[124:127]\n"
"ds_read_b64_tr_b16 a[130:131], v203 offset:50688\n"
"buffer_load_dwordx4 v[44:47], v207, s[28:31], s56 offen offset:3072 sc1\n"
"v_mfma_f32_16x16x32_bf16 v[226:229], a[156:159], a[120:123], v[226:229]\n"
"s_add_u32 s56, s56, 4096\n"
"ds_read_b64_tr_b16 a[132:133], v200 offset:55296\n"
"v_mfma_f32_16x16x32_bf16 v[230:233], a[156:159], a[124:127], v[230:233]\n"
"s_waitcnt lgkmcnt(11)\n"
"s_waitcnt vmcnt(17)\n"
"ds_read_b64_tr_b16 a[134:135], v200 offset:59904\n"
"v_mfma_f32_16x16x32_bf16 v[64:67], a[192:195], v[0:3], v[64:67]\n"
"ds_read_b64_tr_b16 a[136:137], v200 offset:55424\n"
"v_mfma_f32_16x16x32_bf16 v[68:71], a[192:195], v[4:7], v[68:71]\n"
"ds_read_b64_tr_b16 a[138:139], v200 offset:60032\n"
"v_mfma_f32_16x16x32_bf16 v[72:75], a[196:199], v[0:3], v[72:75]\n"
"ds_read_b64_tr_b16 a[140:141], v200 offset:55552\n"
"v_accvgpr_write_b32 a48, v128\n"
"v_mfma_f32_16x16x32_bf16 v[76:79], a[196:199], v[4:7], v[76:79]\n"
"ds_read_b64_tr_b16 a[142:143], v200 offset:60160\n"
"v_accvgpr_write_b32 a49, v129\n"
"v_mfma_f32_16x16x32_bf16 v[80:83], a[200:203], v[0:3], v[80:83]\n"
"ds_read_b64_tr_b16 a[144:145], v200 offset:55680\n"
"v_accvgpr_write_b32 a50, v130\n"
"v_mfma_f32_16x16x32_bf16 v[84:87], a[200:203], v[4:7], v[84:87]\n"
"ds_read_b64_tr_b16 a[146:147], v200 offset:60288\n"
"v_accvgpr_write_b32 a51, v131\n"
"v_mfma_f32_16x16x32_bf16 v[88:91], a[204:207], v[0:3], v[88:91]\n"
"ds_read_b64_tr_b16 a[148:149], v200 offset:55808\n"
"buffer_load_dwordx4 v[48:51], v207, s[28:31], s57 offen offset:2048 sc1\n"
"v_mfma_f32_16x16x32_bf16 v[92:95], a[204:207], v[4:7], v[92:95]\n"
"s_waitcnt lgkmcnt(11)\n"
"ds_read_b64_tr_b16 a[150:151], v200 offset:60416\n"
"v_accvgpr_write_b32 a52, v132\n"
"v_mfma_f32_16x16x32_bf16 v[96:99], a[208:211], v[0:3], v[96:99]\n"
"ds_read_b64_tr_b16 a[152:153], v200 offset:55936\n"
"v_accvgpr_write_b32 a53, v133\n"
"v_mfma_f32_16x16x32_bf16 v[100:103], a[208:211], v[4:7], v[100:103]\n"
"ds_read_b64_tr_b16 a[154:155], v200 offset:60544\n"
"v_accvgpr_write_b32 a54, v134\n"
"v_mfma_f32_16x16x32_bf16 v[104:107], a[212:215], v[0:3], v[104:107]\n"
"ds_read_b64_tr_b16 a[156:157], v200 offset:56064\n"
"v_accvgpr_write_b32 a55, v135\n"
"v_mfma_f32_16x16x32_bf16 v[108:111], a[212:215], v[4:7], v[108:111]\n"
"ds_read_b64_tr_b16 a[158:159], v200 offset:60672\n"
"v_accvgpr_write_b32 a56, v136\n"
"v_mfma_f32_16x16x32_bf16 v[112:115], a[216:219], v[0:3], v[112:115]\n"
"ds_read_b64_tr_b16 a[192:193], v200 offset:56192\n"
"v_accvgpr_write_b32 a57, v137\n"
"v_mfma_f32_16x16x32_bf16 v[116:119], a[216:219], v[4:7], v[116:119]\n"
"ds_read_b64_tr_b16 a[194:195], v200 offset:60800\n"
"v_accvgpr_write_b32 a58, v138\n"
"v_mfma_f32_16x16x32_bf16 v[120:123], a[220:223], v[0:3], v[120:123]\n"
"ds_read_b64_tr_b16 a[196:197], v203 offset:55296\n"
"v_accvgpr_write_b32 a59, v139\n"
"v_mfma_f32_16x16x32_bf16 v[124:127], a[220:223], v[4:7], v[124:127]\n"
"s_waitcnt lgkmcnt(11)\n"
"ds_read_b64_tr_b16 a[198:199], v203 offset:59904\n"
"buffer_load_dwordx4 v[52:55], v207, s[28:31], s57 offen offset:3072 sc1\n"
"v_mfma_f32_16x16x32_bf16 v[226:229], a[128:131], v[0:3], v[226:229]\n"
"s_add_u32 s57, s57, 4096\n"
"ds_read_b64_tr_b16 a[200:201], v200 offset:64512\n"
"v_accvgpr_write_b32 a60, v140\n"
"v_mfma_f32_16x16x32_bf16 v[230:233], a[128:131], v[4:7], v[230:233]\n"
"s_waitcnt vmcnt(15)\n"
"ds_read_b64_tr_b16 a[202:203], v201 offset:3584\n"
"v_accvgpr_write_b32 a61, v141\n"
"v_mfma_f32_16x16x32_bf16 v[64:67], a[132:135], v[8:11], v[64:67]\n"
"ds_read_b64_tr_b16 a[204:205], v200 offset:64640\n"
"v_accvgpr_write_b32 a62, v142\n"
"v_mfma_f32_16x16x32_bf16 v[68:71], a[132:135], v[12:15], v[68:71]\n"
"ds_read_b64_tr_b16 a[206:207], v201 offset:3712\n"
"v_accvgpr_write_b32 a63, v143\n"
"v_mfma_f32_16x16x32_bf16 v[72:75], a[136:139], v[8:11], v[72:75]\n"
"ds_read_b64_tr_b16 a[208:209], v200 offset:64768\n"
"v_accvgpr_write_b32 a64, v144\n"
"v_mfma_f32_16x16x32_bf16 v[76:79], a[136:139], v[12:15], v[76:79]\n"
"ds_read_b64_tr_b16 a[210:211], v201 offset:3840\n"
"v_accvgpr_write_b32 a65, v145\n"
"v_mfma_f32_16x16x32_bf16 v[80:83], a[140:143], v[8:11], v[80:83]\n"
"ds_read_b64_tr_b16 a[212:213], v200 offset:64896\n"
"v_accvgpr_write_b32 a66, v146\n"
"v_mfma_f32_16x16x32_bf16 v[84:87], a[140:143], v[12:15], v[84:87]\n"
"s_waitcnt lgkmcnt(11)\n"
"ds_read_b64_tr_b16 a[214:215], v201 offset:3968\n"
"v_accvgpr_write_b32 a67, v147\n"
"v_mfma_f32_16x16x32_bf16 v[88:91], a[144:147], v[8:11], v[88:91]\n"
"ds_read_b64_tr_b16 a[216:217], v200 offset:65024\n"
"buffer_load_dwordx4 v[56:59], v207, s[28:31], s58 offen offset:2048 sc1\n"
"v_mfma_f32_16x16x32_bf16 v[92:95], a[144:147], v[12:15], v[92:95]\n"
"ds_read_b64_tr_b16 a[218:219], v201 offset:4096\n"
"v_accvgpr_write_b32 a68, v148\n"
"v_mfma_f32_16x16x32_bf16 v[96:99], a[148:151], v[8:11], v[96:99]\n"
"ds_read_b64_tr_b16 a[220:221], v200 offset:65152\n"
"v_accvgpr_write_b32 a69, v149\n"
"v_mfma_f32_16x16x32_bf16 v[100:103], a[148:151], v[12:15], v[100:103]\n"
"ds_read_b64_tr_b16 a[222:223], v201 offset:4224\n"
"v_accvgpr_write_b32 a70, v150\n"
"v_mfma_f32_16x16x32_bf16 v[104:107], a[152:155], v[8:11], v[104:107]\n"
"ds_read_b64_tr_b16 a[128:129], v200 offset:65280\n"
"v_accvgpr_write_b32 a71, v151\n"
"v_mfma_f32_16x16x32_bf16 v[108:111], a[152:155], v[12:15], v[108:111]\n"
"ds_read_b64_tr_b16 a[130:131], v201 offset:4352\n"
"v_accvgpr_write_b32 a72, v152\n"
"v_mfma_f32_16x16x32_bf16 v[112:115], a[156:159], v[8:11], v[112:115]\n"
"ds_read_b64_tr_b16 a[132:133], v200 offset:65408\n"
"v_accvgpr_write_b32 a73, v153\n"
"v_mfma_f32_16x16x32_bf16 v[116:119], a[156:159], v[12:15], v[116:119]\n"
"s_waitcnt lgkmcnt(11)\n"
"ds_read_b64_tr_b16 a[134:135], v201 offset:4480\n"
"v_accvgpr_write_b32 a74, v154\n"
"v_mfma_f32_16x16x32_bf16 v[120:123], a[192:195], v[8:11], v[120:123]\n"
"ds_read_b64_tr_b16 a[136:137], v203 offset:64512\n"
"v_accvgpr_write_b32 a75, v155\n"
"v_mfma_f32_16x16x32_bf16 v[124:127], a[192:195], v[12:15], v[124:127]\n"
"ds_read_b64_tr_b16 a[138:139], v204 offset:3584\n"
"buffer_load_dwordx4 v[60:63], v207, s[28:31], s58 offen offset:3072 sc1\n"
"v_mfma_f32_16x16x32_bf16 v[226:229], a[196:199], v[8:11], v[226:229]\n"
"s_add_u32 s58, s58, 4096\n"
"ds_read_b64_tr_b16 a[140:141], v201 offset:8192\n"
"v_accvgpr_write_b32 a76, v156\n"
"v_mfma_f32_16x16x32_bf16 v[230:233], a[196:199], v[12:15], v[230:233]\n"
"s_waitcnt vmcnt(14)\n"
"ds_read_b64_tr_b16 a[142:143], v201 offset:12800\n"
"v_accvgpr_write_b32 a77, v157\n"
"v_mfma_f32_16x16x32_bf16 v[64:67], a[200:203], v[16:19], v[64:67]\n"
"ds_read_b64_tr_b16 a[144:145], v201 offset:8320\n"
"v_accvgpr_write_b32 a78, v158\n"
"v_mfma_f32_16x16x32_bf16 v[68:71], a[200:203], v[20:23], v[68:71]\n"
"ds_read_b64_tr_b16 a[146:147], v201 offset:12928\n"
"v_accvgpr_write_b32 a79, v159\n"
"v_mfma_f32_16x16x32_bf16 v[72:75], a[204:207], v[16:19], v[72:75]\n"
"ds_read_b64_tr_b16 a[148:149], v201 offset:8448\n"
"v_accvgpr_write_b32 a80, v160\n"
"v_mfma_f32_16x16x32_bf16 v[76:79], a[204:207], v[20:23], v[76:79]\n"
"s_waitcnt lgkmcnt(11)\n"
"ds_read_b64_tr_b16 a[150:151], v201 offset:13056\n"
"v_accvgpr_write_b32 a81, v161\n"
"v_mfma_f32_16x16x32_bf16 v[80:83], a[208:211], v[16:19], v[80:83]\n"
"ds_read_b64_tr_b16 a[152:153], v201 offset:8576\n"
"v_accvgpr_write_b32 a82, v162\n"
"v_mfma_f32_16x16x32_bf16 v[84:87], a[208:211], v[20:23], v[84:87]\n"
"ds_read_b64_tr_b16 a[154:155], v201 offset:13184\n"
"v_accvgpr_write_b32 a83, v163\n"
"v_mfma_f32_16x16x32_bf16 v[88:91], a[212:215], v[16:19], v[88:91]\n"
"ds_read_b64_tr_b16 a[156:157], v201 offset:8704\n"
"v_accvgpr_write_b32 a84, v164\n"
"v_mfma_f32_16x16x32_bf16 v[92:95], a[212:215], v[20:23], v[92:95]\n"
"ds_read_b64_tr_b16 a[158:159], v201 offset:13312\n"
"v_accvgpr_write_b32 a85, v165\n"
"v_mfma_f32_16x16x32_bf16 v[96:99], a[216:219], v[16:19], v[96:99]\n"
"ds_read_b64_tr_b16 a[192:193], v201 offset:8832\n"
"v_accvgpr_write_b32 a86, v166\n"
"v_mfma_f32_16x16x32_bf16 v[100:103], a[216:219], v[20:23], v[100:103]\n"
"ds_read_b64_tr_b16 a[194:195], v201 offset:13440\n"
"v_accvgpr_write_b32 a87, v167\n"
"v_mfma_f32_16x16x32_bf16 v[104:107], a[220:223], v[16:19], v[104:107]\n"
"ds_read_b64_tr_b16 a[196:197], v201 offset:8960\n"
"v_accvgpr_write_b32 a88, v168\n"
"v_mfma_f32_16x16x32_bf16 v[108:111], a[220:223], v[20:23], v[108:111]\n"
"s_waitcnt lgkmcnt(11)\n"
"ds_read_b64_tr_b16 a[198:199], v201 offset:13568\n"
"v_accvgpr_write_b32 a89, v169\n"
"v_mfma_f32_16x16x32_bf16 v[112:115], a[128:131], v[16:19], v[112:115]\n"
"s_waitcnt vmcnt(6)\n"
"s_barrier\n"
"ds_read_b64_tr_b16 a[200:201], v201 offset:9088\n"
"v_accvgpr_write_b32 a90, v170\n"
"v_mfma_f32_16x16x32_bf16 v[116:119], a[128:131], v[20:23], v[116:119]\n"
"ds_read_b64_tr_b16 a[202:203], v201 offset:13696\n"
"v_accvgpr_write_b32 a91, v171\n"
"v_mfma_f32_16x16x32_bf16 v[120:123], a[132:135], v[16:19], v[120:123]\n"
"ds_read_b64_tr_b16 a[204:205], v204 offset:8192\n"
"v_accvgpr_write_b32 a92, v172\n"
"v_mfma_f32_16x16x32_bf16 v[124:127], a[132:135], v[20:23], v[124:127]\n"
"ds_read_b64_tr_b16 a[206:207], v204 offset:12800\n"
"v_accvgpr_write_b32 a93, v173\n"
"v_mfma_f32_16x16x32_bf16 v[226:229], a[136:139], v[16:19], v[226:229]\n"
"ds_read_b64_tr_b16 a[208:209], v204 offset:17408\n"
"v_accvgpr_write_b32 a94, v174\n"
"v_mfma_f32_16x16x32_bf16 v[230:233], a[136:139], v[20:23], v[230:233]\n"
"s_waitcnt vmcnt(10)\n"
"ds_read_b64_tr_b16 a[210:211], v204 offset:22016\n"
"v_accvgpr_write_b32 a95, v175\n"
"v_mfma_f32_16x16x32_bf16 v[64:67], a[140:143], v[24:27], v[64:67]\n"
"ds_read_b64_tr_b16 a[212:213], v204 offset:26624\n"
"v_accvgpr_write_b32 a96, v176\n"
"v_mfma_f32_16x16x32_bf16 v[68:71], a[140:143], v[28:31], v[68:71]\n"
"s_waitcnt lgkmcnt(11)\n"
"ds_read_b64_tr_b16 a[214:215], v204 offset:31232\n"
"v_accvgpr_write_b32 a97, v177\n"
"v_mfma_f32_16x16x32_bf16 v[72:75], a[144:147], v[24:27], v[72:75]\n"
"ds_read_b64_tr_b16 a[216:217], v204 offset:35840\n"
"v_accvgpr_write_b32 a98, v178\n"
"v_mfma_f32_16x16x32_bf16 v[76:79], a[144:147], v[28:31], v[76:79]\n"
"ds_read_b64_tr_b16 a[218:219], v204 offset:40448\n"
"v_accvgpr_write_b32 a99, v179\n"
"v_mfma_f32_16x16x32_bf16 v[80:83], a[148:151], v[24:27], v[80:83]\n"
"ds_read_b64_tr_b16 a[220:221], v204 offset:45056\n"
"v_accvgpr_write_b32 a100, v180\n"
"v_mfma_f32_16x16x32_bf16 v[84:87], a[148:151], v[28:31], v[84:87]\n"
"ds_read_b64_tr_b16 a[222:223], v204 offset:49664\n"
"v_accvgpr_write_b32 a101, v181\n"
"v_mfma_f32_16x16x32_bf16 v[88:91], a[152:155], v[24:27], v[88:91]\n"
"ds_read_b64_tr_b16 a[128:129], v201 offset:17408\n"
"v_accvgpr_write_b32 a102, v182\n"
"v_mfma_f32_16x16x32_bf16 v[92:95], a[152:155], v[28:31], v[92:95]\n"
"ds_read_b64_tr_b16 a[130:131], v201 offset:22016\n"
"v_accvgpr_write_b32 a103, v183\n"
"v_mfma_f32_16x16x32_bf16 v[96:99], a[156:159], v[24:27], v[96:99]\n"
"ds_read_b64_tr_b16 a[132:133], v201 offset:26624\n"
"v_accvgpr_write_b32 a104, v184\n"
"v_mfma_f32_16x16x32_bf16 v[100:103], a[156:159], v[28:31], v[100:103]\n"
"s_waitcnt lgkmcnt(11)\n"
"ds_read_b64_tr_b16 a[134:135], v201 offset:31232\n"
"v_accvgpr_write_b32 a105, v185\n"
"v_mfma_f32_16x16x32_bf16 v[104:107], a[192:195], v[24:27], v[104:107]\n"
"ds_read_b64_tr_b16 a[136:137], v201 offset:35840\n"
"v_accvgpr_write_b32 a106, v186\n"
"v_mfma_f32_16x16x32_bf16 v[108:111], a[192:195], v[28:31], v[108:111]\n"
"ds_read_b64_tr_b16 a[138:139], v201 offset:40448\n"
"v_accvgpr_write_b32 a107, v187\n"
"v_mfma_f32_16x16x32_bf16 v[112:115], a[196:199], v[24:27], v[112:115]\n"
"ds_read_b64_tr_b16 a[140:141], v201 offset:45056\n"
"v_accvgpr_write_b32 a108, v188\n"
"v_mfma_f32_16x16x32_bf16 v[116:119], a[196:199], v[28:31], v[116:119]\n"
"ds_read_b64_tr_b16 a[142:143], v201 offset:49664\n"
"v_accvgpr_write_b32 a109, v189\n"
"v_mfma_f32_16x16x32_bf16 v[120:123], a[200:203], v[24:27], v[120:123]\n"
"ds_read_b64_tr_b16 a[144:145], v201 offset:17536\n"
"v_accvgpr_write_b32 a110, v190\n"
"v_mfma_f32_16x16x32_bf16 v[124:127], a[200:203], v[28:31], v[124:127]\n"
"ds_read_b64_tr_b16 a[146:147], v201 offset:22144\n"
"v_accvgpr_write_b32 a111, v191\n"
"v_mfma_f32_16x16x32_bf16 v[226:229], a[204:207], v[24:27], v[226:229]\n"
"ds_read_b64_tr_b16 a[148:149], v201 offset:26752\n"
"v_accvgpr_write_b32 a112, v192\n"
"v_mfma_f32_16x16x32_bf16 v[230:233], a[204:207], v[28:31], v[230:233]\n"
"s_waitcnt lgkmcnt(11)\n"
"s_waitcnt vmcnt(7)\n"
"ds_read_b64_tr_b16 a[150:151], v201 offset:31360\n"
"v_accvgpr_write_b32 a113, v193\n"
"v_mfma_f32_16x16x32_bf16 v[226:229], a[208:211], v[32:35], v[226:229]\n"
"ds_read_b64_tr_b16 a[152:153], v201 offset:35968\n"
"v_accvgpr_write_b32 a114, v194\n"
"v_mfma_f32_16x16x32_bf16 v[230:233], a[208:211], v[36:39], v[230:233]\n"
"s_waitcnt vmcnt(4)\n"
"ds_read_b64_tr_b16 a[154:155], v201 offset:40576\n"
"v_accvgpr_write_b32 a115, v195\n"
"v_mfma_f32_16x16x32_bf16 v[226:229], a[212:215], v[40:43], v[226:229]\n"
"ds_read_b64_tr_b16 a[156:157], v201 offset:45184\n"
"v_accvgpr_write_b32 a116, v196\n"
"v_mfma_f32_16x16x32_bf16 v[230:233], a[212:215], v[44:47], v[230:233]\n"
"s_waitcnt vmcnt(2)\n"
"ds_read_b64_tr_b16 a[158:159], v201 offset:49792\n"
"v_accvgpr_write_b32 a117, v197\n"
"v_mfma_f32_16x16x32_bf16 v[226:229], a[216:219], v[48:51], v[226:229]\n"
"ds_read_b64_tr_b16 a[192:193], v201 offset:17664\n"
"v_accvgpr_write_b32 a118, v198\n"
"v_mfma_f32_16x16x32_bf16 v[230:233], a[216:219], v[52:55], v[230:233]\n"
"s_waitcnt vmcnt(0)\n"
"ds_read_b64_tr_b16 a[194:195], v201 offset:22272\n"
"v_accvgpr_write_b32 a119, v199\n"
"v_mfma_f32_16x16x32_bf16 v[226:229], a[220:223], v[56:59], v[226:229]\n"
"ds_read_b64_tr_b16 a[196:197], v201 offset:26880\n"
"v_mfma_f32_16x16x32_bf16 v[230:233], a[220:223], v[60:63], v[230:233]\n"
"s_waitcnt lgkmcnt(11)\n"
"ds_read_b64_tr_b16 a[198:199], v201 offset:31488\n"
"v_mfma_f32_16x16x32_bf16 v[64:67], a[128:131], v[32:35], v[64:67]\n"
"ds_read_b64_tr_b16 a[200:201], v201 offset:36096\n"
"v_mfma_f32_16x16x32_bf16 v[68:71], a[128:131], v[36:39], v[68:71]\n"
"ds_read_b64_tr_b16 a[202:203], v201 offset:40704\n"
"v_mfma_f32_16x16x32_bf16 v[64:67], a[132:135], v[40:43], v[64:67]\n"
"ds_read_b64_tr_b16 a[204:205], v201 offset:45312\n"
"v_mfma_f32_16x16x32_bf16 v[68:71], a[132:135], v[44:47], v[68:71]\n"
"ds_read_b64_tr_b16 a[206:207], v201 offset:49920\n"
"v_cvt_pk_bf16_f32 v218, v226, v227\n"
"v_mfma_f32_16x16x32_bf16 v[64:67], a[136:139], v[48:51], v[64:67]\n"
"ds_read_b64_tr_b16 a[208:209], v201 offset:17792\n"
"v_cvt_pk_bf16_f32 v219, v228, v229\n"
"v_mfma_f32_16x16x32_bf16 v[68:71], a[136:139], v[52:55], v[68:71]\n"
"ds_read_b64_tr_b16 a[210:211], v201 offset:22400\n"
"v_cvt_pk_bf16_f32 v222, v230, v231\n"
"v_mfma_f32_16x16x32_bf16 v[64:67], a[140:143], v[56:59], v[64:67]\n"
"ds_read_b64_tr_b16 a[212:213], v201 offset:27008\n"
"v_cvt_pk_bf16_f32 v223, v232, v233\n"
"v_mfma_f32_16x16x32_bf16 v[68:71], a[140:143], v[60:63], v[68:71]\n"
"s_waitcnt lgkmcnt(11)\n"
"ds_read_b64_tr_b16 a[214:215], v201 offset:31616\n"
"v_mfma_f32_16x16x32_bf16 v[72:75], a[144:147], v[32:35], v[72:75]\n"
"ds_read_b64_tr_b16 a[216:217], v201 offset:36224\n"
"buffer_store_dwordx2 v[218:219], v207, s[28:31], s49 offen sc1\n"
"v_mfma_f32_16x16x32_bf16 v[76:79], a[144:147], v[36:39], v[76:79]\n"
"ds_read_b64_tr_b16 a[218:219], v201 offset:40832\n"
"v_mfma_f32_16x16x32_bf16 v[72:75], a[148:151], v[40:43], v[72:75]\n"
"ds_read_b64_tr_b16 a[220:221], v201 offset:45440\n"
"buffer_store_dwordx2 v[222:223], v208, s[28:31], s49 offen sc1\n"
"v_mfma_f32_16x16x32_bf16 v[76:79], a[148:151], v[44:47], v[76:79]\n"
"ds_read_b64_tr_b16 a[222:223], v201 offset:50048\n"
"v_cvt_pk_bf16_f32 v218, v64, v65\n"
"v_mfma_f32_16x16x32_bf16 v[72:75], a[152:155], v[48:51], v[72:75]\n"
"ds_read_b64_tr_b16 a[128:129], v201 offset:17920\n"
"v_cvt_pk_bf16_f32 v219, v66, v67\n"
"v_mfma_f32_16x16x32_bf16 v[76:79], a[152:155], v[52:55], v[76:79]\n"
"ds_read_b64_tr_b16 a[130:131], v201 offset:22528\n"
"v_cvt_pk_bf16_f32 v222, v68, v69\n"
"v_mfma_f32_16x16x32_bf16 v[72:75], a[156:159], v[56:59], v[72:75]\n"
"ds_read_b64_tr_b16 a[132:133], v201 offset:27136\n"
"v_cvt_pk_bf16_f32 v223, v70, v71\n"
"v_mfma_f32_16x16x32_bf16 v[76:79], a[156:159], v[60:63], v[76:79]\n"
"s_waitcnt lgkmcnt(11)\n"
"ds_read_b64_tr_b16 a[134:135], v201 offset:31744\n"
"v_mfma_f32_16x16x32_bf16 v[80:83], a[192:195], v[32:35], v[80:83]\n"
"ds_read_b64_tr_b16 a[136:137], v201 offset:36352\n"
"v_mfma_f32_16x16x32_bf16 v[84:87], a[192:195], v[36:39], v[84:87]\n"
"ds_read_b64_tr_b16 a[138:139], v201 offset:40960\n"
"v_mfma_f32_16x16x32_bf16 v[80:83], a[196:199], v[40:43], v[80:83]\n"
"ds_read_b64_tr_b16 a[140:141], v201 offset:45568\n"
"v_mfma_f32_16x16x32_bf16 v[84:87], a[196:199], v[44:47], v[84:87]\n"
"ds_read_b64_tr_b16 a[142:143], v201 offset:50176\n"
"v_cvt_pk_bf16_f32 v220, v72, v73\n"
"v_mfma_f32_16x16x32_bf16 v[80:83], a[200:203], v[48:51], v[80:83]\n"
"ds_read_b64_tr_b16 a[144:145], v201 offset:18048\n"
"v_cvt_pk_bf16_f32 v221, v74, v75\n"
"v_mfma_f32_16x16x32_bf16 v[84:87], a[200:203], v[52:55], v[84:87]\n"
"ds_read_b64_tr_b16 a[146:147], v201 offset:22656\n"
"v_cvt_pk_bf16_f32 v224, v76, v77\n"
"v_mfma_f32_16x16x32_bf16 v[80:83], a[204:207], v[56:59], v[80:83]\n"
"ds_read_b64_tr_b16 a[148:149], v201 offset:27264\n"
"v_cvt_pk_bf16_f32 v225, v78, v79\n"
"v_mfma_f32_16x16x32_bf16 v[84:87], a[204:207], v[60:63], v[84:87]\n"
"s_waitcnt lgkmcnt(11)\n"
"ds_read_b64_tr_b16 a[150:151], v201 offset:31872\n"
"v_mfma_f32_16x16x32_bf16 v[88:91], a[208:211], v[32:35], v[88:91]\n"
"ds_read_b64_tr_b16 a[152:153], v201 offset:36480\n"
"buffer_store_dwordx4 v[218:221], v207, s[28:31], s54 offen offset:0 sc1\n"
"v_mfma_f32_16x16x32_bf16 v[92:95], a[208:211], v[36:39], v[92:95]\n"
"ds_read_b64_tr_b16 a[154:155], v201 offset:41088\n"
"v_mfma_f32_16x16x32_bf16 v[88:91], a[212:215], v[40:43], v[88:91]\n"
"ds_read_b64_tr_b16 a[156:157], v201 offset:45696\n"
"buffer_store_dwordx4 v[222:225], v208, s[28:31], s54 offen offset:0 sc1\n"
"v_mfma_f32_16x16x32_bf16 v[92:95], a[212:215], v[44:47], v[92:95]\n"
"ds_read_b64_tr_b16 a[158:159], v201 offset:50304\n"
"v_cvt_pk_bf16_f32 v218, v80, v81\n"
"v_mfma_f32_16x16x32_bf16 v[88:91], a[216:219], v[48:51], v[88:91]\n"
"ds_read_b64_tr_b16 a[192:193], v201 offset:18176\n"
"v_cvt_pk_bf16_f32 v219, v82, v83\n"
"v_mfma_f32_16x16x32_bf16 v[92:95], a[216:219], v[52:55], v[92:95]\n"
"ds_read_b64_tr_b16 a[194:195], v201 offset:22784\n"
"v_cvt_pk_bf16_f32 v222, v84, v85\n"
"v_mfma_f32_16x16x32_bf16 v[88:91], a[220:223], v[56:59], v[88:91]\n"
"ds_read_b64_tr_b16 a[196:197], v201 offset:27392\n"
"v_cvt_pk_bf16_f32 v223, v86, v87\n"
"v_mfma_f32_16x16x32_bf16 v[92:95], a[220:223], v[60:63], v[92:95]\n"
"s_waitcnt lgkmcnt(11)\n"
"ds_read_b64_tr_b16 a[198:199], v201 offset:32000\n"
"v_mfma_f32_16x16x32_bf16 v[96:99], a[128:131], v[32:35], v[96:99]\n"
"ds_read_b64_tr_b16 a[200:201], v201 offset:36608\n"
"v_mfma_f32_16x16x32_bf16 v[100:103], a[128:131], v[36:39], v[100:103]\n"
"ds_read_b64_tr_b16 a[202:203], v201 offset:41216\n"
"v_mfma_f32_16x16x32_bf16 v[96:99], a[132:135], v[40:43], v[96:99]\n"
"ds_read_b64_tr_b16 a[204:205], v201 offset:45824\n"
"v_mfma_f32_16x16x32_bf16 v[100:103], a[132:135], v[44:47], v[100:103]\n"
"ds_read_b64_tr_b16 a[206:207], v201 offset:50432\n"
"v_cvt_pk_bf16_f32 v220, v88, v89\n"
"v_mfma_f32_16x16x32_bf16 v[96:99], a[136:139], v[48:51], v[96:99]\n"
"ds_read_b64_tr_b16 a[208:209], v201 offset:18304\n"
"v_cvt_pk_bf16_f32 v221, v90, v91\n"
"v_mfma_f32_16x16x32_bf16 v[100:103], a[136:139], v[52:55], v[100:103]\n"
"ds_read_b64_tr_b16 a[210:211], v201 offset:22912\n"
"v_cvt_pk_bf16_f32 v224, v92, v93\n"
"v_mfma_f32_16x16x32_bf16 v[96:99], a[140:143], v[56:59], v[96:99]\n"
"ds_read_b64_tr_b16 a[212:213], v201 offset:27520\n"
"v_cvt_pk_bf16_f32 v225, v94, v95\n"
"v_mfma_f32_16x16x32_bf16 v[100:103], a[140:143], v[60:63], v[100:103]\n"
"s_waitcnt lgkmcnt(11)\n"
"ds_read_b64_tr_b16 a[214:215], v201 offset:32128\n"
"v_mfma_f32_16x16x32_bf16 v[104:107], a[144:147], v[32:35], v[104:107]\n"
"ds_read_b64_tr_b16 a[216:217], v201 offset:36736\n"
"buffer_store_dwordx4 v[218:221], v207, s[28:31], s54 offen offset:1024 sc1\n"
"v_mfma_f32_16x16x32_bf16 v[108:111], a[144:147], v[36:39], v[108:111]\n"
"ds_read_b64_tr_b16 a[218:219], v201 offset:41344\n"
"v_mfma_f32_16x16x32_bf16 v[104:107], a[148:151], v[40:43], v[104:107]\n"
"ds_read_b64_tr_b16 a[220:221], v201 offset:45952\n"
"buffer_store_dwordx4 v[222:225], v208, s[28:31], s54 offen offset:1024 sc1\n"
"v_mfma_f32_16x16x32_bf16 v[108:111], a[148:151], v[44:47], v[108:111]\n"
"ds_read_b64_tr_b16 a[222:223], v201 offset:50560\n"
"v_cvt_pk_bf16_f32 v218, v96, v97\n"
"v_mfma_f32_16x16x32_bf16 v[104:107], a[152:155], v[48:51], v[104:107]\n"
"v_cvt_pk_bf16_f32 v219, v98, v99\n"
"v_mfma_f32_16x16x32_bf16 v[108:111], a[152:155], v[52:55], v[108:111]\n"
"v_cvt_pk_bf16_f32 v222, v100, v101\n"
"v_mfma_f32_16x16x32_bf16 v[104:107], a[156:159], v[56:59], v[104:107]\n"
"v_cvt_pk_bf16_f32 v223, v102, v103\n"
"v_mfma_f32_16x16x32_bf16 v[108:111], a[156:159], v[60:63], v[108:111]\n"
"s_waitcnt lgkmcnt(8)\n"
"v_mfma_f32_16x16x32_bf16 v[112:115], a[192:195], v[32:35], v[112:115]\n"
"s_nop 0\n"
"v_mfma_f32_16x16x32_bf16 v[116:119], a[192:195], v[36:39], v[116:119]\n"
"s_nop 0\n"
"v_mfma_f32_16x16x32_bf16 v[112:115], a[196:199], v[40:43], v[112:115]\n"
"s_nop 0\n"
"v_mfma_f32_16x16x32_bf16 v[116:119], a[196:199], v[44:47], v[116:119]\n"
"v_cvt_pk_bf16_f32 v220, v104, v105\n"
"v_mfma_f32_16x16x32_bf16 v[112:115], a[200:203], v[48:51], v[112:115]\n"
"v_cvt_pk_bf16_f32 v221, v106, v107\n"
"v_mfma_f32_16x16x32_bf16 v[116:119], a[200:203], v[52:55], v[116:119]\n"
"v_cvt_pk_bf16_f32 v224, v108, v109\n"
"v_mfma_f32_16x16x32_bf16 v[112:115], a[204:207], v[56:59], v[112:115]\n"
"v_cvt_pk_bf16_f32 v225, v110, v111\n"
"v_mfma_f32_16x16x32_bf16 v[116:119], a[204:207], v[60:63], v[116:119]\n"
"s_waitcnt lgkmcnt(0)\n"
"v_mfma_f32_16x16x32_bf16 v[120:123], a[208:211], v[32:35], v[120:123]\n"
"buffer_store_dwordx4 v[218:221], v207, s[28:31], s54 offen offset:2048 sc1\n"
"v_mfma_f32_16x16x32_bf16 v[124:127], a[208:211], v[36:39], v[124:127]\n"
"s_nop 0\n"
"v_mfma_f32_16x16x32_bf16 v[120:123], a[212:215], v[40:43], v[120:123]\n"
"buffer_store_dwordx4 v[222:225], v208, s[28:31], s54 offen offset:2048 sc1\n"
"v_mfma_f32_16x16x32_bf16 v[124:127], a[212:215], v[44:47], v[124:127]\n"
"v_cvt_pk_bf16_f32 v218, v112, v113\n"
"v_mfma_f32_16x16x32_bf16 v[120:123], a[216:219], v[48:51], v[120:123]\n"
"v_cvt_pk_bf16_f32 v219, v114, v115\n"
"v_mfma_f32_16x16x32_bf16 v[124:127], a[216:219], v[52:55], v[124:127]\n"
"v_cvt_pk_bf16_f32 v222, v116, v117\n"
"v_mfma_f32_16x16x32_bf16 v[120:123], a[220:223], v[56:59], v[120:123]\n"
"v_cvt_pk_bf16_f32 v223, v118, v119\n"
"v_mfma_f32_16x16x32_bf16 v[124:127], a[220:223], v[60:63], v[124:127]\n"
"s_nop 0\n"
"s_nop 1\n"
"s_nop 1\n"
"s_nop 1\n"
"s_nop 1\n"
"v_cvt_pk_bf16_f32 v220, v120, v121\n"
"s_nop 1\n"
"v_cvt_pk_bf16_f32 v221, v122, v123\n"
"s_nop 1\n"
"v_cvt_pk_bf16_f32 v224, v124, v125\n"
"s_nop 1\n"
"v_cvt_pk_bf16_f32 v225, v126, v127\n"
"s_nop 1\n"
"s_nop 1\n"
"buffer_store_dwordx4 v[218:221], v207, s[28:31], s54 offen offset:3072 sc1\n"
"s_nop 1\n"
"s_nop 1\n"
"buffer_store_dwordx4 v[222:225], v208, s[28:31], s54 offen offset:3072 sc1\n"
"s_nop 1\n"
"s_add_u32 s56, s40, 161024\n"
"s_nop 1\n"
"v_mov_b32 v227, s56\n"
"v_mov_b32 v224, 0x3f80\n"
"v_mov_b32 v225, 0x3f800000\n"
"v_and_b32 v226, 7, v248\n"
"v_lshl_add_u32 v226, v226, 4, v227\n"
"s_waitcnt vmcnt(0) lgkmcnt(0)\n"
"s_barrier\n"
"s_mov_b32 s14, 0xfffff800\n"
"s_cmp_lg_u32 s41, 3\n"
"s_cbranch_scc1 AT_A0\n"
"v_and_b32 v2, 15, v248\n"
"v_lshlrev_b32 v2, 6, v2\n"
"s_lshl_b32 s57, s44, 10\n"
"s_mov_b32 s87, 0x80000\n"
"AT_S1:\n"
"buffer_load_dword v3, v2, s[28:31], s57 offen sc1\n"
"s_waitcnt vmcnt(0)\n"
"v_cmp_eq_u32 vcc, s85, v3\n"
"s_nop 1\n"
"s_lshr_b32 s58, vcc_lo, 1\n"
"s_and_b32 s58, s58, vcc_lo\n"
"s_lshr_b32 s59, s58, 2\n"
"s_and_b32 s58, s58, s59\n"
"s_and_b32 s58, s58, 0x1111\n"
"s_sub_u32 s87, s87, 1\n"
"s_cmp_eq_u32 s87, 0\n"
"s_cselect_b32 s58, 0x1111, s58\n"
"v_mov_b32 v4, s58\n"
"ds_write_b32 v227, v4 offset:640\n"
"s_cmp_eq_u32 s58, 0x1111\n"
"s_cbranch_scc1 AT_S1x\n"
"s_sleep 2\n"
"s_branch AT_S1\n"
"AT_S1x:\n"
"s_mov_b64 s[8:9], 0\n"
"v_mov_b32 v224, 0xffff0000\n"
"v_accvgpr_read_b32 v1, a48\n"
"v_accvgpr_read_b32 v3, a49\n"
"v_accvgpr_read_b32 v5, a50\n"
"v_accvgpr_read_b32 v7, a51\n"
"v_lshlrev_b32 v0, 16, v1\n"
"v_and_b32 v1, v224, v1\n"
"v_lshlrev_b32 v2, 16, v3\n"
"v_and_b32 v3, v224, v3\n"
"v_lshlrev_b32 v4, 16, v5\n"
"v_and_b32 v5, v224, v5\n"
"v_lshlrev_b32 v6, 16, v7\n"
"v_and_b32 v7, v224, v7\n"
"v_accvgpr_read_b32 v9, a84\n"
"v_accvgpr_read_b32 v11, a85\n"
"v_accvgpr_read_b32 v13, a86\n"
"v_accvgpr_read_b32 v15, a87\n"
"v_lshlrev_b32 v8, 16, v9\n"
"v_and_b32 v9, v224, v9\n"
"v_lshlrev_b32 v10, 16, v11\n"
"v_and_b32 v11, v224, v11\n"
"v_lshlrev_b32 v12, 16, v13\n"
"v_and_b32 v13, v224, v13\n"
"v_lshlrev_b32 v14, 16, v15\n"
"v_and_b32 v15, v224, v15\n"
"v_accvgpr_read_b32 v17, a52\n"
"v_accvgpr_read_b32 v19, a53\n"
"v_accvgpr_read_b32 v21, a54\n"
"v_accvgpr_read_b32 v23, a55\n"
"v_lshlrev_b32 v16, 16, v17\n"
"v_and_b32 v17, v224, v17\n"
"v_lshlrev_b32 v18, 16, v19\n"
"v_and_b32 v19, v224, v19\n"
"v_lshlrev_b32 v20, 16, v21\n"
"v_and_b32 v21, v224, v21\n"
"v_lshlrev_b32 v22, 16, v23\n"
"v_and_b32 v23, v224, v23\n"
"v_accvgpr_read_b32 v25, a88\n"
"v_accvgpr_read_b32 v27, a89\n"
"v_accvgpr_read_b32 v29, a90\n"
"v_accvgpr_read_b32 v31, a91\n"
"v_lshlrev_b32 v24, 16, v25\n"
"v_and_b32 v25, v224, v25\n"
"v_lshlrev_b32 v26, 16, v27\n"
"v_and_b32 v27, v224, v27\n"
"v_lshlrev_b32 v28, 16, v29\n"
"v_and_b32 v29, v224, v29\n"
"v_lshlrev_b32 v30, 16, v31\n"
"v_and_b32 v31, v224, v31\n"
"v_accvgpr_read_b32 v33, a56\n"
"v_accvgpr_read_b32 v35, a57\n"
"v_accvgpr_read_b32 v37, a58\n"
"v_accvgpr_read_b32 v39, a59\n"
"v_lshlrev_b32 v32, 16, v33\n"
"v_and_b32 v33, v224, v33\n"
"v_lshlrev_b32 v34, 16, v35\n"
"v_and_b32 v35, v224, v35\n"
"v_lshlrev_b32 v36, 16, v37\n"
"v_and_b32 v37, v224, v37\n"
"v_lshlrev_b32 v38, 16, v39\n"
"v_and_b32 v39, v224, v39\n"
"v_accvgpr_read_b32 v41, a92\n"
"v_accvgpr_read_b32 v43, a93\n"
"v_accvgpr_read_b32 v45, a94\n"
"v_accvgpr_read_b32 v47, a95\n"
"v_lshlrev_b32 v40, 16, v41\n"
"v_and_b32 v41, v224, v41\n"
"v_lshlrev_b32 v42, 16, v43\n"
"v_and_b32 v43, v224, v43\n"
"v_lshlrev_b32 v44, 16, v45\n"
"v_and_b32 v45, v224, v45\n"
"v_lshlrev_b32 v46, 16, v47\n"
"v_and_b32 v47, v224, v47\n"
"v_accvgpr_read_b32 v49, a60\n"
"v_accvgpr_read_b32 v51, a61\n"
"v_accvgpr_read_b32 v53, a62\n"
"v_accvgpr_read_b32 v55, a63\n"
"v_lshlrev_b32 v48, 16, v49\n"
"v_and_b32 v49, v224, v49\n"
"v_lshlrev_b32 v50, 16, v51\n"
"v_and_b32 v51, v224, v51\n"
"v_lshlrev_b32 v52, 16, v53\n"
"v_and_b32 v53, v224, v53\n"
"v_lshlrev_b32 v54, 16, v55\n"
"v_and_b32 v55, v224, v55\n"
"v_accvgpr_read_b32 v57, a96\n"
"v_accvgpr_read_b32 v59, a97\n"
"v_accvgpr_read_b32 v61, a98\n"
"v_accvgpr_read_b32 v63, a99\n"
"v_lshlrev_b32 v56, 16, v57\n"
"v_and_b32 v57, v224, v57\n"
"v_lshlrev_b32 v58, 16, v59\n"
"v_and_b32 v59, v224, v59\n"
"v_lshlrev_b32 v60, 16, v61\n"
"v_and_b32 v61, v224, v61\n"
"v_lshlrev_b32 v62, 16, v63\n"
"v_and_b32 v63, v224, v63\n"
"v_accvgpr_read_b32 v65, a64\n"
"v_accvgpr_read_b32 v67, a65\n"
"v_accvgpr_read_b32 v69, a66\n"
"v_accvgpr_read_b32 v71, a67\n"
"v_lshlrev_b32 v64, 16, v65\n"
"v_and_b32 v65, v224, v65\n"
"v_lshlrev_b32 v66, 16, v67\n"
"v_and_b32 v67, v224, v67\n"
"v_lshlrev_b32 v68, 16, v69\n"
"v_and_b32 v69, v224, v69\n"
"v_lshlrev_b32 v70, 16, v71\n"
"v_and_b32 v71, v224, v71\n"
"v_accvgpr_read_b32 v73, a100\n"
"v_accvgpr_read_b32 v75, a101\n"
"v_accvgpr_read_b32 v77, a102\n"
"v_accvgpr_read_b32 v79, a103\n"
"v_lshlrev_b32 v72, 16, v73\n"
"v_and_b32 v73, v224, v73\n"
"v_lshlrev_b32 v74, 16, v75\n"
"v_and_b32 v75, v224, v75\n"
"v_lshlrev_b32 v76, 16, v77\n"
"v_and_b32 v77, v224, v77\n"
"v_lshlrev_b32 v78, 16, v79\n"
"v_and_b32 v79, v224, v79\n"
"v_accvgpr_read_b32 v81, a68\n"
"v_accvgpr_read_b32 v83, a69\n"
"v_accvgpr_read_b32 v85, a70\n"
"v_accvgpr_read_b32 v87, a71\n"
"v_lshlrev_b32 v80, 16, v81\n"
"v_and_b32 v81, v224, v81\n"
"v_lshlrev_b32 v82, 16, v83\n"
"v_and_b32 v83, v224, v83\n"
"v_lshlrev_b32 v84, 16, v85\n"
"v_and_b32 v85, v224, v85\n"
"v_lshlrev_b32 v86, 16, v87\n"
"v_and_b32 v87, v224, v87\n"
"v_accvgpr_read_b32 v89, a104\n"
"v_accvgpr_read_b32 v91, a105\n"
"v_accvgpr_read_b32 v93, a106\n"
"v_accvgpr_read_b32 v95, a107\n"
"v_lshlrev_b32 v88, 16, v89\n"
"v_and_b32 v89, v224, v89\n"
"v_lshlrev_b32 v90, 16, v91\n"
"v_and_b32 v91, v224, v91\n"
"v_lshlrev_b32 v92, 16, v93\n"
"v_and_b32 v93, v224, v93\n"
"v_lshlrev_b32 v94, 16, v95\n"
"v_and_b32 v95, v224, v95\n"
"v_accvgpr_read_b32 v97, a72\n"
"v_accvgpr_read_b32 v99, a73\n"
"v_accvgpr_read_b32 v101, a74\n"
"v_accvgpr_read_b32 v103, a75\n"
"v_lshlrev_b32 v96, 16, v97\n"
"v_and_b32 v97, v224, v97\n"
"v_lshlrev_b32 v98, 16, v99\n"
"v_and_b32 v99, v224, v99\n"
"v_lshlrev_b32 v100, 16, v101\n"
"v_and_b32 v101, v224, v101\n"
"v_lshlrev_b32 v102, 16, v103\n"
"v_and_b32 v103, v224, v103\n"
"v_accvgpr_read_b32 v105, a108\n"
"v_accvgpr_read_b32 v107, a109\n"
"v_accvgpr_read_b32 v109, a110\n"
"v_accvgpr_read_b32 v111, a111\n"
"v_lshlrev_b32 v104, 16, v105\n"
"v_and_b32 v105, v224, v105\n"
"v_lshlrev_b32 v106, 16, v107\n"
"v_and_b32 v107, v224, v107\n"
"v_lshlrev_b32 v108, 16, v109\n"
"v_and_b32 v109, v224, v109\n"
"v_lshlrev_b32 v110, 16, v111\n"
"v_and_b32 v111, v224, v111\n"
"v_accvgpr_read_b32 v113, a76\n"
"v_accvgpr_read_b32 v115, a77\n"
"v_accvgpr_read_b32 v117, a78\n"
"v_accvgpr_read_b32 v119, a79\n"
"v_lshlrev_b32 v112, 16, v113\n"
"v_and_b32 v113, v224, v113\n"
"v_lshlrev_b32 v114, 16, v115\n"
"v_and_b32 v115, v224, v115\n"
"v_lshlrev_b32 v116, 16, v117\n"
"v_and_b32 v117, v224, v117\n"
"v_lshlrev_b32 v118, 16, v119\n"
"v_and_b32 v119, v224, v119\n"
"v_accvgpr_read_b32 v121, a112\n"
"v_accvgpr_read_b32 v123, a113\n"
"v_accvgpr_read_b32 v125, a114\n"
"v_accvgpr_read_b32 v127, a115\n"
"v_lshlrev_b32 v120, 16, v121\n"
"v_and_b32 v121, v224, v121\n"
"v_lshlrev_b32 v122, 16, v123\n"
"v_and_b32 v123, v224, v123\n"
"v_lshlrev_b32 v124, 16, v125\n"
"v_and_b32 v125, v224, v125\n"
"v_lshlrev_b32 v126, 16, v127\n"
"v_and_b32 v127, v224, v127\n"
"v_accvgpr_read_b32 v129, a80\n"
"v_accvgpr_read_b32 v131, a81\n"
"v_accvgpr_read_b32 v133, a82\n"
"v_accvgpr_read_b32 v135, a83\n"
"v_lshlrev_b32 v128, 16, v129\n"
"v_and_b32 v129, v224, v129\n"
"v_lshlrev_b32 v130, 16, v131\n"
"v_and_b32 v131, v224, v131\n"
"v_lshlrev_b32 v132, 16, v133\n"
"v_and_b32 v133, v224, v133\n"
"v_lshlrev_b32 v134, 16, v135\n"
"v_and_b32 v135, v224, v135\n"
"v_accvgpr_read_b32 v137, a116\n"
"v_accvgpr_read_b32 v139, a117\n"
"v_accvgpr_read_b32 v141, a118\n"
"v_accvgpr_read_b32 v143, a119\n"
"v_lshlrev_b32 v136, 16, v137\n"
"v_and_b32 v137, v224, v137\n"
"v_lshlrev_b32 v138, 16, v139\n"
"v_and_b32 v139, v224, v139\n"
"v_lshlrev_b32 v140, 16, v141\n"
"v_and_b32 v141, v224, v141\n"
"v_lshlrev_b32 v142, 16, v143\n"
"v_and_b32 v143, v224, v143\n"
"v_lshrrev_b32 v242, 2, v248\n"
"v_lshlrev_b32 v241, 4, v242\n"
"v_lshlrev_b32 v242, 3, v242\n"
"v_lshrrev_b32 v243, 5, v254\n"
"v_mul_u32_u24 v243, 24576, v243\n"
"v_and_b32 v247, 3, v248\n"
"v_lshl_add_u32 v243, v247, 5, v243\n"
"v_and_b32 v244, 0x10000000, v254\n"
"v_or_b32 v243, v244, v243\n"
"v_lshrrev_b32 v244, 5, v248\n"
"v_mul_u32_u24 v244, 96, v244\n"
"v_and_b32 v246, 16, v248\n"
"v_subrev_u32 v252, 65536, v227\n"
"v_add3_u32 v244, v244, v246, v252\n"
"v_lshl_add_u32 v244, v247, 5, v244\n"
"s_mul_i32 s63, s41, 384\n"
"v_add_u32 v244, s63, v244\n"
"v_lshl_add_u32 v249, v248, 2, v252\n"
"s_lshl_b32 s63, s41, 8\n"
"v_add_u32 v251, s63, v249\n"
"v_add_u32 v252, s63, v252\n"
"s_branch AT_B2\n"
"AT_A0:\n"
"s_cmp_lg_u32 s41, 0\n"
"s_cbranch_scc1 AT_A0b\n"
"s_lshl_b32 s56, s55, 6\n"
"v_mov_b32 v0, s85\n"
"v_mov_b32 v1, 0\n"
"s_mov_b64 exec, 1\n"
"buffer_store_dword v0, v1, s[28:31], s56 offen sc1\n"
"s_mov_b64 exec, -1\n"
"AT_A0b:\n"
"buffer_load_dwordx2 v[228:229], v240, s[16:19], s46 offen offset:0\n"
"buffer_load_dwordx2 v[230:231], v240, s[16:19], s46 offen offset:32\n"
"buffer_load_dwordx2 v[232:233], v240, s[16:19], s46 offen offset:384\n"
"buffer_load_dwordx2 v[234:235], v240, s[16:19], s46 offen offset:416\n"
"buffer_load_dwordx2 v[236:237], v240, s[16:19], s46 offen offset:768\n"
"buffer_load_dwordx2 v[238:239], v240, s[16:19], s46 offen offset:800\n"
"buffer_load_dwordx2 a[0:1], v254, s[72:75], s50 offen offset:0\n"
"buffer_load_dwordx2 a[2:3], v254, s[72:75], s50 offen offset:1152\n"
"buffer_load_dwordx2 a[4:5], v254, s[72:75], s50 offen offset:2304\n"
"s_add_u32 s50, s50, 3456\n"
"buffer_load_dwordx2 a[6:7], v254, s[72:75], s50 offen offset:0\n"
"buffer_load_dwordx2 a[8:9], v254, s[72:75], s50 offen offset:1152\n"
"buffer_load_dwordx2 a[10:11], v254, s[72:75], s50 offen offset:2304\n"
"s_add_u32 s50, s50, 3456\n"
"buffer_load_dwordx2 a[12:13], v254, s[72:75], s50 offen offset:0\n"
"buffer_load_dwordx2 a[14:15], v254, s[72:75], s50 offen offset:1152\n"
"buffer_load_dwordx2 a[16:17], v254, s[72:75], s50 offen offset:2304\n"
"s_add_u32 s50, s50, 3456\n"
"buffer_load_dwordx2 a[18:19], v254, s[72:75], s50 offen offset:0\n"
"buffer_load_dwordx2 a[20:21], v254, s[72:75], s50 offen offset:1152\n"
"buffer_load_dwordx2 a[22:23], v254, s[72:75], s50 offen offset:2304\n"
"s_add_u32 s50, s50, 3456\n"
"buffer_load_dwordx2 a[24:25], v254, s[72:75], s50 offen offset:0\n"
"buffer_load_dwordx2 a[26:27], v254, s[72:75], s50 offen offset:1152\n"
"buffer_load_dwordx2 a[28:29], v254, s[72:75], s50 offen offset:2304\n"
"s_add_u32 s50, s50, 3456\n"
"buffer_load_dwordx2 a[30:31], v254, s[72:75], s50 offen offset:0\n"
"buffer_load_dwordx2 a[32:33], v254, s[72:75], s50 offen offset:1152\n"
"buffer_load_dwordx2 a[34:35], v254, s[72:75], s50 offen offset:2304\n"
"s_add_u32 s50, s50, 3456\n"
"buffer_load_dwordx2 a[36:37], v254, s[72:75], s50 offen offset:0\n"
"buffer_load_dwordx2 a[38:39], v254, s[72:75], s50 offen offset:1152\n"
"buffer_load_dwordx2 a[40:41], v254, s[72:75], s50 offen offset:2304\n"
"s_add_u32 s50, s50, 3456\n"
"buffer_load_dwordx2 a[42:43], v254, s[72:75], s50 offen offset:0\n"
"buffer_load_dwordx2 a[44:45], v254, s[72:75], s50 offen offset:1152\n"
"buffer_load_dwordx2 a[46:47], v254, s[72:75], s50 offen offset:2304\n"
"s_add_u32 s50, s50, 3456\n"
"ds_read_b128 v[200:203], v244 offset:0\n"
"ds_read_b128 v[204:207], v244 offset:64\n"
"ds_read_b128 v[208:211], v244 offset:768\n"
"ds_read_b128 v[212:215], v244 offset:832\n"
"ds_read_b128 v[216:219], v244 offset:1536\n"
"ds_read_b128 v[220:223], v244 offset:1600\n"
"s_mov_b32 s52, 0\n"
"AT_A1:\n"
"ds_read_b32 v192, v227 offset:640\n"
"s_waitcnt lgkmcnt(0)\n"
"v_readfirstlane_b32 s53, v192\n"
"s_nop 0\n"
"s_andn2_b32 s53, s53, s52\n"
"s_cmp_lg_u32 s53, 0\n"
"s_cbranch_scc1 AT_A2\n"
"s_sleep 1\n"
"s_branch AT_A1\n"
"AT_A2:\n"
"s_bcnt1_i32_b32 s54, s53\n"
"s_bitcmp1_b32 s53, 0\n"
"s_cbranch_scc0 AT_Ai0\n"
"s_mov_b32 s56, s47\n"
"buffer_load_dwordx4 v[12:15], v241, s[28:31], s56 offen sc1\n"
"buffer_load_dwordx4 v[28:31], v242, s[28:31], s56 offen sc1\n"
"buffer_load_dwordx4 v[44:47], v243, s[28:31], s56 offen sc1\n"
"AT_Ai0:\n"
"s_bitcmp1_b32 s53, 4\n"
"s_cbranch_scc0 AT_Ai1\n"
"s_add_u32 s56, s47, 2359296\n"
"buffer_load_dwordx4 v[60:63], v241, s[28:31], s56 offen sc1\n"
"buffer_load_dwordx4 v[76:79], v242, s[28:31], s56 offen sc1\n"
"buffer_load_dwordx4 v[92:95], v243, s[28:31], s56 offen sc1\n"
"AT_Ai1:\n"
"s_bitcmp1_b32 s53, 8\n"
"s_cbranch_scc0 AT_Ai2\n"
"s_add_u32 s56, s47, 4718592\n"
"buffer_load_dwordx4 v[108:111], v241, s[28:31], s56 offen sc1\n"
"buffer_load_dwordx4 v[124:127], v242, s[28:31], s56 offen sc1\n"
"buffer_load_dwordx4 v[140:143], v243, s[28:31], s56 offen sc1\n"
"AT_Ai2:\n"
"s_bitcmp1_b32 s53, 12\n"
"s_cbranch_scc0 AT_Ai3\n"
"s_add_u32 s56, s47, 7077888\n"
"buffer_load_dwordx4 v[156:159], v241, s[28:31], s56 offen sc1\n"
"buffer_load_dwordx4 v[172:175], v242, s[28:31], s56 offen sc1\n"
"buffer_load_dwordx4 v[188:191], v243, s[28:31], s56 offen sc1\n"
"AT_Ai3:\n"
"s_bitcmp1_b32 s53, 0\n"
"s_cbranch_scc0 AT_Ar0\n"
"s_sub_u32 s54, s54, 1\n"
"s_cmp_eq_u32 s54, 0\n"
"s_cbranch_scc1 AT_Aw0_0\n"
"s_cmp_eq_u32 s54, 1\n"
"s_cbranch_scc1 AT_Aw0_1\n"
"s_cmp_eq_u32 s54, 2\n"
"s_cbranch_scc1 AT_Aw0_2\n"
"s_waitcnt vmcnt(9)\n"
"s_branch AT_Ax0\n"
"AT_Aw0_2:\n"
"s_waitcnt vmcnt(6)\n"
"s_branch AT_Ax0\n"
"AT_Aw0_1:\n"
"s_waitcnt vmcnt(3)\n"
"s_branch AT_Ax0\n"
"AT_Aw0_0:\n"
"s_waitcnt vmcnt(0)\n"
"AT_Ax0:\n"
"v_dot2_f32_bf16 v0, v12, v224, 0\n"
"v_dot2_f32_bf16 v1, v12, v225, 0\n"
"v_dot2_f32_bf16 v2, v13, v224, 0\n"
"v_dot2_f32_bf16 v3, v13, v225, 0\n"
"v_dot2_f32_bf16 v4, v14, v224, 0\n"
"v_dot2_f32_bf16 v5, v14, v225, 0\n"
"v_dot2_f32_bf16 v6, v15, v224, 0\n"
"v_dot2_f32_bf16 v7, v15, v225, 0\n"
"v_dot2_f32_bf16 v16, v28, v224, 0\n"
"v_dot2_f32_bf16 v17, v28, v225, 0\n"
"v_dot2_f32_bf16 v18, v29, v224, 0\n"
"v_dot2_f32_bf16 v19, v29, v225, 0\n"
"v_dot2_f32_bf16 v20, v30, v224, 0\n"
"v_dot2_f32_bf16 v21, v30, v225, 0\n"
"v_dot2_f32_bf16 v22, v31, v224, 0\n"
"v_dot2_f32_bf16 v23, v31, v225, 0\n"
"v_dot2_f32_bf16 v32, v44, v224, 0\n"
"v_dot2_f32_bf16 v33, v44, v225, 0\n"
"v_dot2_f32_bf16 v34, v45, v224, 0\n"
"v_dot2_f32_bf16 v35, v45, v225, 0\n"
"v_dot2_f32_bf16 v36, v46, v224, 0\n"
"v_dot2_f32_bf16 v37, v46, v225, 0\n"
"v_dot2_f32_bf16 v38, v47, v224, 0\n"
"v_dot2_f32_bf16 v39, v47, v225, 0\n"
"AT_Ar0:\n"
"s_bitcmp1_b32 s53, 4\n"
"s_cbranch_scc0 AT_Ar1\n"
"s_sub_u32 s54, s54, 1\n"
"s_cmp_eq_u32 s54, 0\n"
"s_cbranch_scc1 AT_Aw1_0\n"
"s_cmp_eq_u32 s54, 1\n"
"s_cbranch_scc1 AT_Aw1_1\n"
"s_waitcnt vmcnt(6)\n"
"s_branch AT_Ax1\n"
"AT_Aw1_1:\n"
"s_waitcnt vmcnt(3)\n"
"s_branch AT_Ax1\n"
"AT_Aw1_0:\n"
"s_waitcnt vmcnt(0)\n"
"AT_Ax1:\n"
"v_dot2_f32_bf16 v48, v60, v224, 0\n"
"v_dot2_f32_bf16 v49, v60, v225, 0\n"
"v_dot2_f32_bf16 v50, v61, v224, 0\n"
"v_dot2_f32_bf16 v51, v61, v225, 0\n"
"v_dot2_f32_bf16 v52, v62, v224, 0\n"
"v_dot2_f32_bf16 v53, v62, v225, 0\n"
"v_dot2_f32_bf16 v54, v63, v224, 0\n"
"v_dot2_f32_bf16 v55, v63, v225, 0\n"
"v_dot2_f32_bf16 v64, v76, v224, 0\n"
"v_dot2_f32_bf16 v65, v76, v225, 0\n"
"v_dot2_f32_bf16 v66, v77, v224, 0\n"
"v_dot2_f32_bf16 v67, v77, v225, 0\n"
"v_dot2_f32_bf16 v68, v78, v224, 0\n"
"v_dot2_f32_bf16 v69, v78, v225, 0\n"
"v_dot2_f32_bf16 v70, v79, v224, 0\n"
"v_dot2_f32_bf16 v71, v79, v225, 0\n"
"v_dot2_f32_bf16 v80, v92, v224, 0\n"
"v_dot2_f32_bf16 v81, v92, v225, 0\n"
"v_dot2_f32_bf16 v82, v93, v224, 0\n"
"v_dot2_f32_bf16 v83, v93, v225, 0\n"
"v_dot2_f32_bf16 v84, v94, v224, 0\n"
"v_dot2_f32_bf16 v85, v94, v225, 0\n"
"v_dot2_f32_bf16 v86, v95, v224, 0\n"
"v_dot2_f32_bf16 v87, v95, v225, 0\n"
"AT_Ar1:\n"
"s_bitcmp1_b32 s53, 8\n"
"s_cbranch_scc0 AT_Ar2\n"
"s_sub_u32 s54, s54, 1\n"
"s_cmp_eq_u32 s54, 0\n"
"s_cbranch_scc1 AT_Aw2_0\n"
"s_waitcnt vmcnt(3)\n"
"s_branch AT_Ax2\n"
"AT_Aw2_0:\n"
"s_waitcnt vmcnt(0)\n"
"AT_Ax2:\n"
"v_dot2_f32_bf16 v96, v108, v224, 0\n"
"v_dot2_f32_bf16 v97, v108, v225, 0\n"
"v_dot2_f32_bf16 v98, v109, v224, 0\n"
"v_dot2_f32_bf16 v99, v109, v225, 0\n"
"v_dot2_f32_bf16 v100, v110, v224, 0\n"
"v_dot2_f32_bf16 v101, v110, v225, 0\n"
"v_dot2_f32_bf16 v102, v111, v224, 0\n"
"v_dot2_f32_bf16 v103, v111, v225, 0\n"
"v_dot2_f32_bf16 v112, v124, v224, 0\n"
"v_dot2_f32_bf16 v113, v124, v225, 0\n"
"v_dot2_f32_bf16 v114, v125, v224, 0\n"
"v_dot2_f32_bf16 v115, v125, v225, 0\n"
"v_dot2_f32_bf16 v116, v126, v224, 0\n"
"v_dot2_f32_bf16 v117, v126, v225, 0\n"
"v_dot2_f32_bf16 v118, v127, v224, 0\n"
"v_dot2_f32_bf16 v119, v127, v225, 0\n"
"v_dot2_f32_bf16 v128, v140, v224, 0\n"
"v_dot2_f32_bf16 v129, v140, v225, 0\n"
"v_dot2_f32_bf16 v130, v141, v224, 0\n"
"v_dot2_f32_bf16 v131, v141, v225, 0\n"
"v_dot2_f32_bf16 v132, v142, v224, 0\n"
"v_dot2_f32_bf16 v133, v142, v225, 0\n"
"v_dot2_f32_bf16 v134, v143, v224, 0\n"
"v_dot2_f32_bf16 v135, v143, v225, 0\n"
"AT_Ar2:\n"
"s_bitcmp1_b32 s53, 12\n"
"s_cbranch_scc0 AT_Ar3\n"
"s_sub_u32 s54, s54, 1\n"
"s_waitcnt vmcnt(0)\n"
"AT_Ax3:\n"
"v_dot2_f32_bf16 v144, v156, v224, 0\n"
"v_dot2_f32_bf16 v145, v156, v225, 0\n"
"v_dot2_f32_bf16 v146, v157, v224, 0\n"
"v_dot2_f32_bf16 v147, v157, v225, 0\n"
"v_dot2_f32_bf16 v148, v158, v224, 0\n"
"v_dot2_f32_bf16 v149, v158, v225, 0\n"
"v_dot2_f32_bf16 v150, v159, v224, 0\n"
"v_dot2_f32_bf16 v151, v159, v225, 0\n"
"v_dot2_f32_bf16 v160, v172, v224, 0\n"
"v_dot2_f32_bf16 v161, v172, v225, 0\n"
"v_dot2_f32_bf16 v162, v173, v224, 0\n"
"v_dot2_f32_bf16 v163, v173, v225, 0\n"
"v_dot2_f32_bf16 v164, v174, v224, 0\n"
"v_dot2_f32_bf16 v165, v174, v225, 0\n"
"v_dot2_f32_bf16 v166, v175, v224, 0\n"
"v_dot2_f32_bf16 v167, v175, v225, 0\n"
"v_dot2_f32_bf16 v176, v188, v224, 0\n"
"v_dot2_f32_bf16 v177, v188, v225, 0\n"
"v_dot2_f32_bf16 v178, v189, v224, 0\n"
"v_dot2_f32_bf16 v179, v189, v225, 0\n"
"v_dot2_f32_bf16 v180, v190, v224, 0\n"
"v_dot2_f32_bf16 v181, v190, v225, 0\n"
"v_dot2_f32_bf16 v182, v191, v224, 0\n"
"v_dot2_f32_bf16 v183, v191, v225, 0\n"
"AT_Ar3:\n"
"s_or_b32 s52, s52, s53\n"
"s_cmp_lg_u32 s52, 0x1111\n"
"s_cbranch_scc1 AT_A1\n"
"ds_read_b128 v[8:11], v244 offset:2304\n"
"ds_read_b128 v[12:15], v244 offset:2368\n"
"ds_read_b128 v[24:27], v244 offset:3072\n"
"ds_read_b128 v[28:31], v244 offset:3136\n"
"ds_read_b128 v[40:43], v244 offset:3840\n"
"ds_read_b128 v[44:47], v244 offset:3904\n"
"v_pk_add_f32 v[0:1], v[0:1], v[48:49]\n"
"v_pk_add_f32 v[96:97], v[96:97], v[144:145]\n"
"v_pk_add_f32 v[2:3], v[2:3], v[50:51]\n"
"v_pk_add_f32 v[98:99], v[98:99], v[146:147]\n"
"v_pk_add_f32 v[4:5], v[4:5], v[52:53]\n"
"v_pk_add_f32 v[100:101], v[100:101], v[148:149]\n"
"v_pk_add_f32 v[6:7], v[6:7], v[54:55]\n"
"v_pk_add_f32 v[102:103], v[102:103], v[150:151]\n"
"v_pk_add_f32 v[16:17], v[16:17], v[64:65]\n"
"v_pk_add_f32 v[112:113], v[112:113], v[160:161]\n"
"v_pk_add_f32 v[18:19], v[18:19], v[66:67]\n"
"v_pk_add_f32 v[114:115], v[114:115], v[162:163]\n"
"v_pk_add_f32 v[20:21], v[20:21], v[68:69]\n"
"v_pk_add_f32 v[116:117], v[116:117], v[164:165]\n"
"v_pk_add_f32 v[22:23], v[22:23], v[70:71]\n"
"v_pk_add_f32 v[118:119], v[118:119], v[166:167]\n"
"v_pk_add_f32 v[32:33], v[32:33], v[80:81]\n"
"v_pk_add_f32 v[128:129], v[128:129], v[176:177]\n"
"v_pk_add_f32 v[34:35], v[34:35], v[82:83]\n"
"v_pk_add_f32 v[130:131], v[130:131], v[178:179]\n"
"v_pk_add_f32 v[36:37], v[36:37], v[84:85]\n"
"v_pk_add_f32 v[132:133], v[132:133], v[180:181]\n"
"v_pk_add_f32 v[38:39], v[38:39], v[86:87]\n"
"v_pk_add_f32 v[134:135], v[134:135], v[182:183]\n"
"v_pk_add_f32 v[0:1], v[0:1], v[96:97]\n"
"v_pk_add_f32 v[2:3], v[2:3], v[98:99]\n"
"v_pk_add_f32 v[4:5], v[4:5], v[100:101]\n"
"v_pk_add_f32 v[6:7], v[6:7], v[102:103]\n"
"v_pk_add_f32 v[16:17], v[16:17], v[112:113]\n"
"v_pk_add_f32 v[18:19], v[18:19], v[114:115]\n"
"v_pk_add_f32 v[20:21], v[20:21], v[116:117]\n"
"v_pk_add_f32 v[22:23], v[22:23], v[118:119]\n"
"v_pk_add_f32 v[32:33], v[32:33], v[128:129]\n"
"v_pk_add_f32 v[34:35], v[34:35], v[130:131]\n"
"v_pk_add_f32 v[36:37], v[36:37], v[132:133]\n"
"v_pk_add_f32 v[38:39], v[38:39], v[134:135]\n"
"v_pk_mul_f32 v[0:1], v[0:1], v[200:201]\n"
"v_pk_mul_f32 v[2:3], v[2:3], v[202:203]\n"
"v_pk_mul_f32 v[4:5], v[4:5], v[204:205]\n"
"v_pk_mul_f32 v[6:7], v[6:7], v[206:207]\n"
"v_pk_mul_f32 v[16:17], v[16:17], v[208:209]\n"
"v_pk_mul_f32 v[18:19], v[18:19], v[210:211]\n"
"v_pk_mul_f32 v[20:21], v[20:21], v[212:213]\n"
"v_pk_mul_f32 v[22:23], v[22:23], v[214:215]\n"
"v_pk_mul_f32 v[32:33], v[32:33], v[216:217]\n"
"v_pk_mul_f32 v[34:35], v[34:35], v[218:219]\n"
"v_pk_mul_f32 v[36:37], v[36:37], v[220:221]\n"
"v_pk_mul_f32 v[38:39], v[38:39], v[222:223]\n"
"v_dot2_f32_bf16 v0, v228, v224, v0\n"
"v_dot2_f32_bf16 v1, v228, v225, v1\n"
"v_dot2_f32_bf16 v2, v229, v224, v2\n"
"v_dot2_f32_bf16 v3, v229, v225, v3\n"
"v_dot2_f32_bf16 v4, v230, v224, v4\n"
"v_dot2_f32_bf16 v5, v230, v225, v5\n"
"v_dot2_f32_bf16 v6, v231, v224, v6\n"
"v_dot2_f32_bf16 v7, v231, v225, v7\n"
"v_dot2_f32_bf16 v16, v232, v224, v16\n"
"v_dot2_f32_bf16 v17, v232, v225, v17\n"
"v_dot2_f32_bf16 v18, v233, v224, v18\n"
"v_dot2_f32_bf16 v19, v233, v225, v19\n"
"v_dot2_f32_bf16 v20, v234, v224, v20\n"
"v_dot2_f32_bf16 v21, v234, v225, v21\n"
"v_dot2_f32_bf16 v22, v235, v224, v22\n"
"v_dot2_f32_bf16 v23, v235, v225, v23\n"
"v_dot2_f32_bf16 v32, v236, v224, v32\n"
"v_dot2_f32_bf16 v33, v236, v225, v33\n"
"v_dot2_f32_bf16 v34, v237, v224, v34\n"
"v_dot2_f32_bf16 v35, v237, v225, v35\n"
"v_dot2_f32_bf16 v36, v238, v224, v36\n"
"v_dot2_f32_bf16 v37, v238, v225, v37\n"
"v_dot2_f32_bf16 v38, v239, v224, v38\n"
"v_dot2_f32_bf16 v39, v239, v225, v39\n"
"ds_read_b128 v[56:59], v244 offset:4608\n"
"ds_read_b128 v[60:63], v244 offset:4672\n"
"ds_read_b128 v[72:75], v244 offset:5376\n"
"ds_read_b128 v[76:79], v244 offset:5440\n"
"ds_read_b128 v[88:91], v244 offset:6144\n"
"ds_read_b128 v[92:95], v244 offset:6208\n"
"ds_read_b128 v[104:107], v244 offset:6912\n"
"ds_read_b128 v[108:111], v244 offset:6976\n"
"ds_read_b128 v[120:123], v244 offset:7680\n"
"ds_read_b128 v[124:127], v244 offset:7744\n"
"ds_read_b128 v[136:139], v244 offset:8448\n"
"ds_read_b128 v[140:143], v244 offset:8512\n"
"ds_read_b128 v[152:155], v244 offset:38912\n"
"ds_read_b128 v[156:159], v244 offset:38976\n"
"ds_read_b128 v[168:171], v244 offset:39680\n"
"ds_read_b128 v[172:175], v244 offset:39744\n"
"ds_read_b128 v[184:187], v244 offset:40448\n"
"ds_read_b128 v[188:191], v244 offset:40512\n"
"v_pk_mul_f32 v[194:195], v[0:1], v[0:1]\n"
"s_waitcnt lgkmcnt(14)\n"
"v_pk_mul_f32 v[196:197], v[0:1], v[8:9]\n"
"v_pk_add_f32 v[192:193], v[0:1], v[2:3]\n"
"v_pk_fma_f32 v[194:195], v[2:3], v[2:3], v[194:195]\n"
"v_pk_fma_f32 v[196:197], v[2:3], v[10:11], v[196:197]\n"
"v_pk_add_f32 v[192:193], v[192:193], v[4:5]\n"
"v_pk_fma_f32 v[194:195], v[4:5], v[4:5], v[194:195]\n"
"v_pk_fma_f32 v[196:197], v[4:5], v[12:13], v[196:197]\n"
"v_pk_add_f32 v[192:193], v[192:193], v[6:7]\n"
"v_pk_fma_f32 v[194:195], v[6:7], v[6:7], v[194:195]\n"
"v_pk_fma_f32 v[196:197], v[6:7], v[14:15], v[196:197]\n"
"v_pk_add_f32 v[192:193], v[192:193], v[16:17]\n"
"v_pk_fma_f32 v[194:195], v[16:17], v[16:17], v[194:195]\n"
"v_pk_fma_f32 v[196:197], v[16:17], v[24:25], v[196:197]\n"
"v_pk_add_f32 v[192:193], v[192:193], v[18:19]\n"
"v_pk_fma_f32 v[194:195], v[18:19], v[18:19], v[194:195]\n"
"v_pk_fma_f32 v[196:197], v[18:19], v[26:27], v[196:197]\n"
"v_pk_add_f32 v[192:193], v[192:193], v[20:21]\n"
"v_pk_fma_f32 v[194:195], v[20:21], v[20:21], v[194:195]\n"
"v_pk_fma_f32 v[196:197], v[20:21], v[28:29], v[196:197]\n"
"v_pk_add_f32 v[192:193], v[192:193], v[22:23]\n"
"v_pk_fma_f32 v[194:195], v[22:23], v[22:23], v[194:195]\n"
"v_pk_fma_f32 v[196:197], v[22:23], v[30:31], v[196:197]\n"
"v_pk_add_f32 v[192:193], v[192:193], v[32:33]\n"
"v_pk_fma_f32 v[194:195], v[32:33], v[32:33], v[194:195]\n"
"v_pk_fma_f32 v[196:197], v[32:33], v[40:41], v[196:197]\n"
"v_pk_add_f32 v[192:193], v[192:193], v[34:35]\n"
"v_pk_fma_f32 v[194:195], v[34:35], v[34:35], v[194:195]\n"
"v_pk_fma_f32 v[196:197], v[34:35], v[42:43], v[196:197]\n"
"v_pk_add_f32 v[192:193], v[192:193], v[36:37]\n"
"v_pk_fma_f32 v[194:195], v[36:37], v[36:37], v[194:195]\n"
"v_pk_fma_f32 v[196:197], v[36:37], v[44:45], v[196:197]\n"
"v_pk_add_f32 v[192:193], v[192:193], v[38:39]\n"
"v_pk_fma_f32 v[194:195], v[38:39], v[38:39], v[194:195]\n"
"v_pk_fma_f32 v[196:197], v[38:39], v[46:47], v[196:197]\n"
"v_add_f32 v192, v192, v193\n"
"v_add_f32 v193, v194, v195\n"
"v_add_f32 v194, v196, v197\n"
"s_lshl_b32 s56, s41, 7\n"
"v_add_u32 v199, s56, v226\n"
"v_add_f32_dpp v192, v192, v192 row_ror:8 row_mask:0xf bank_mask:0xf\n"
"v_add_f32_dpp v193, v193, v193 row_ror:8 row_mask:0xf bank_mask:0xf\n"
"v_add_f32_dpp v194, v194, v194 row_ror:8 row_mask:0xf bank_mask:0xf\n"
"v_mov_b32 v196, v192\n"
"v_mov_b32 v197, v193\n"
"v_mov_b32 v198, v194\n"
"v_permlane16_swap_b32 v192, v196\n"
"v_permlane16_swap_b32 v193, v197\n"
"v_permlane16_swap_b32 v194, v198\n"
"v_add_f32 v192, v192, v196\n"
"v_add_f32 v193, v193, v197\n"
"v_add_f32 v194, v194, v198\n"
"v_mov_b32 v196, v192\n"
"v_mov_b32 v197, v193\n"
"v_mov_b32 v198, v194\n"
"v_permlane32_swap_b32 v192, v196\n"
"v_permlane32_swap_b32 v193, v197\n"
"v_permlane32_swap_b32 v194, v198\n"
"v_add_f32 v192, v192, v196\n"
"v_add_f32 v193, v193, v197\n"
"v_add_f32 v194, v194, v198\n"
"s_mov_b64 exec, 0xff\n"
"ds_write_b128 v199, v[192:195]\n"
"s_mov_b64 exec, -1\n"
"v_mov_b32 v49, s85\n"
"v_mov_b32 v51, s85\n"
"v_mov_b32 v53, s85\n"
"v_mov_b32 v55, s85\n"
"v_mov_b32 v97, s85\n"
"v_mov_b32 v99, s85\n"
"v_mov_b32 v101, s85\n"
"v_mov_b32 v103, s85\n"
"v_mov_b32 v65, s85\n"
"v_mov_b32 v67, s85\n"
"v_mov_b32 v69, s85\n"
"v_mov_b32 v71, s85\n"
"v_mov_b32 v113, s85\n"
"v_mov_b32 v115, s85\n"
"v_mov_b32 v117, s85\n"
"v_mov_b32 v119, s85\n"
"v_mov_b32 v81, s85\n"
"v_mov_b32 v83, s85\n"
"v_mov_b32 v85, s85\n"
"v_mov_b32 v87, s85\n"
"v_mov_b32 v129, s85\n"
"v_mov_b32 v131, s85\n"
"v_mov_b32 v133, s85\n"
"v_mov_b32 v135, s85\n"
"AT_B2:\n"
"s_waitcnt lgkmcnt(0)\n"
"s_barrier\n"
"ds_read_b128 v[200:203], v226 offset:0\n"
"ds_read_b128 v[204:207], v226 offset:128\n"
"ds_read_b128 v[208:211], v226 offset:256\n"
"ds_read_b128 v[212:215], v227 offset:512\n"
"ds_read_b128 v[216:219], v227 offset:528\n"
"s_lshl_b32 s56, s55, 3\n"
"s_sub_u32 s56, 2047, s56\n"
"v_and_b32 v199, 7, v248\n"
"v_sub_u32 v199, s56, v199\n"
"s_waitcnt lgkmcnt(3)\n"
"v_add_f32 v200, v200, v204\n"
"v_add_f32 v201, v201, v205\n"
"v_add_f32 v202, v202, v206\n"
"s_waitcnt lgkmcnt(2)\n"
"v_add_f32 v200, v200, v208\n"
"v_add_f32 v201, v201, v209\n"
"v_add_f32 v202, v202, v210\n"
"v_mul_f32 v200, 0x3ae38e39, v200\n"
"v_mul_f32 v201, 0x3ae38e39, v201\n"
"s_waitcnt lgkmcnt(1)\n"
"v_add_f32 v212, v212, v214\n"
"v_fma_f32 v201, -v200, v200, v201\n"
"v_add_f32 v213, v213, v215\n"
"v_max_f32 v201, 0, v201\n"
"s_waitcnt lgkmcnt(0)\n"
"v_add_f32 v212, v212, v216\n"
"v_add_f32 v201, 0x3727c5ac, v201\n"
"v_add_f32 v213, v213, v217\n"
"v_rsq_f32 v192, v201\n"
"v_fma_f32 v202, -v200, v212, v202\n"
"v_fma_f32 v196, v192, v202, v213\n"
"v_mul_f32 v194, v200, v192\n"
"v_ashrrev_i32 v203, 31, v196\n"
"v_sub_f32 v194, 0, v194\n"
"v_or_b32 v203, 0x80000000, v203\n"
"v_xor_b32 v196, v196, v203\n"
"v_and_or_b32 v196, v196, s14, v199\n"
"s_nop 1\n"
"v_mov_b32_dpp v203, v196 row_ror:1 row_mask:0xf bank_mask:0xf\n"
"v_min_u32 v198, v196, v203\n"
"v_max_u32 v197, v196, v203\n"
"s_nop 0\n"
"v_mov_b32_dpp v204, v198 row_ror:2 row_mask:0xf bank_mask:0xf\n"
"v_mov_b32_dpp v203, v197 row_ror:2 row_mask:0xf bank_mask:0xf\n"
"v_min_u32 v205, v197, v203\n"
"v_max_u32 v197, v197, v203\n"
"v_max3_u32 v198, v198, v204, v205\n"
"s_nop 0\n"
"v_mov_b32_dpp v203, v197 row_ror:4 row_mask:0xf bank_mask:0xf\n"
"v_mov_b32_dpp v204, v198 row_ror:4 row_mask:0xf bank_mask:0xf\n"
"v_min_u32 v205, v197, v203\n"
"v_max_u32 v197, v197, v203\n"
"v_max3_u32 v198, v198, v204, v205\n"
"s_cmp_lg_u32 s41, 3\n"
"s_cbranch_scc1 AT_A3\n"
"v_mov_b32 v144, v197\n"
"v_mov_b32 v145, s85\n"
"v_mov_b32 v146, v198\n"
"v_mov_b32 v147, s85\n"
"s_lshl_b32 s56, s55, 4\n"
"v_mov_b32 v148, 16384\n"
"s_mov_b64 exec, 1\n"
"buffer_store_dwordx4 v[144:147], v148, s[28:31], s56 offen sc1\n"
"s_mov_b64 exec, -1\n"
"v_lshlrev_b32 v149, 4, v248\n"
"s_mov_b32 s57, 16384\n"
"s_mov_b32 s87, 0x80000\n"
"AT_S2:\n"
"buffer_load_dwordx4 v[152:155], v149, s[28:31], s57 offen offset:0 sc1\n"
"buffer_load_dwordx4 v[156:159], v149, s[28:31], s57 offen offset:1024 sc1\n"
"buffer_load_dwordx4 v[160:163], v149, s[28:31], s57 offen offset:2048 sc1\n"
"buffer_load_dwordx4 v[164:167], v149, s[28:31], s57 offen offset:3072 sc1\n"
"s_waitcnt vmcnt(0)\n"
"v_xor_b32 v150, s85, v153\n"
"v_xor_b32 v151, s85, v155\n"
"v_bitop3_b32 v150, v157, s85, v150 bitop3:0xbe\n"
"v_bitop3_b32 v151, v159, s85, v151 bitop3:0xbe\n"
"v_bitop3_b32 v150, v161, s85, v150 bitop3:0xbe\n"
"v_bitop3_b32 v151, v163, s85, v151 bitop3:0xbe\n"
"v_bitop3_b32 v150, v165, s85, v150 bitop3:0xbe\n"
"v_bitop3_b32 v151, v167, s85, v151 bitop3:0xbe\n"
"v_or_b32 v150, v150, v151\n"
"v_cmp_ne_u32 vcc, 0, v150\n"
"s_nop 0\n"
"s_cbranch_vccz AT_S3\n"
"s_sub_u32 s87, s87, 1\n"
"s_cmp_eq_u32 s87, 0\n"
"s_cbranch_scc1 AT_S3\n"
"s_sleep 1\n"
"s_branch AT_S2\n"
"AT_S3:\n"
"v_min_u32 v205, v152, v156\n"
"v_max_u32 v152, v152, v156\n"
"v_max3_u32 v154, v154, v158, v205\n"
"v_min_u32 v205, v160, v164\n"
"v_max_u32 v160, v160, v164\n"
"v_max3_u32 v162, v162, v166, v205\n"
"v_min_u32 v205, v152, v160\n"
"v_max_u32 v152, v152, v160\n"
"v_max3_u32 v154, v154, v162, v205\n"
"s_nop 0\n"
"v_mov_b32_dpp v156, v152 row_ror:1 row_mask:0xf bank_mask:0xf\n"
"v_mov_b32_dpp v158, v154 row_ror:1 row_mask:0xf bank_mask:0xf\n"
"v_min_u32 v205, v152, v156\n"
"v_max_u32 v152, v152, v156\n"
"v_max3_u32 v154, v154, v158, v205\n"
"s_nop 0\n"
"v_mov_b32_dpp v156, v152 row_ror:2 row_mask:0xf bank_mask:0xf\n"
"v_mov_b32_dpp v158, v154 row_ror:2 row_mask:0xf bank_mask:0xf\n"
"v_min_u32 v205, v152, v156\n"
"v_max_u32 v152, v152, v156\n"
"v_max3_u32 v154, v154, v158, v205\n"
"s_nop 0\n"
"v_mov_b32_dpp v156, v152 row_ror:4 row_mask:0xf bank_mask:0xf\n"
"v_mov_b32_dpp v158, v154 row_ror:4 row_mask:0xf bank_mask:0xf\n"
"v_min_u32 v205, v152, v156\n"
"v_max_u32 v152, v152, v156\n"
"v_max3_u32 v154, v154, v158, v205\n"
"s_nop 0\n"
"v_mov_b32_dpp v156, v152 row_ror:8 row_mask:0xf bank_mask:0xf\n"
"v_mov_b32_dpp v158, v154 row_ror:8 row_mask:0xf bank_mask:0xf\n"
"v_min_u32 v205, v152, v156\n"
"v_max_u32 v152, v152, v156\n"
"v_max3_u32 v154, v154, v158, v205\n"
"v_mov_b32 v156, v152\n"
"v_mov_b32 v158, v154\n"
"s_nop 0\n"
"v_permlane16_swap_b32 v152, v156\n"
"v_permlane16_swap_b32 v154, v158\n"
"v_min_u32 v205, v152, v156\n"
"v_max_u32 v152, v152, v156\n"
"v_max3_u32 v154, v154, v158, v205\n"
"v_mov_b32 v156, v152\n"
"v_mov_b32 v158, v154\n"
"s_nop 0\n"
"v_permlane32_swap_b32 v152, v156\n"
"v_permlane32_swap_b32 v154, v158\n"
"v_min_u32 v205, v152, v156\n"
"v_max_u32 v152, v152, v156\n"
"v_max3_u32 v154, v154, v158, v205\n"
"v_mov_b32 v153, v154\n"
"s_mov_b64 exec, 1\n"
"ds_write_b64 v227, v[152:153] offset:656\n"
"s_mov_b64 exec, -1\n"
"s_branch AT_B3\n"
"AT_A3:\n"
"v_cmp_ge_u32 s[8:9], v196, v198\n"
"v_pk_fma_f32 v[144:145], v[0:1], v[192:193], v[194:195] op_sel_hi:[1,0,0]\n"
"v_pk_fma_f32 v[146:147], v[2:3], v[192:193], v[194:195] op_sel_hi:[1,0,0]\n"
"v_pk_fma_f32 v[148:149], v[4:5], v[192:193], v[194:195] op_sel_hi:[1,0,0]\n"
"v_pk_fma_f32 v[150:151], v[6:7], v[192:193], v[194:195] op_sel_hi:[1,0,0]\n"
"v_pk_fma_f32 v[160:161], v[16:17], v[192:193], v[194:195] op_sel_hi:[1,0,0]\n"
"v_pk_fma_f32 v[162:163], v[18:19], v[192:193], v[194:195] op_sel_hi:[1,0,0]\n"
"v_pk_fma_f32 v[164:165], v[20:21], v[192:193], v[194:195] op_sel_hi:[1,0,0]\n"
"v_pk_fma_f32 v[166:167], v[22:23], v[192:193], v[194:195] op_sel_hi:[1,0,0]\n"
"v_pk_fma_f32 v[176:177], v[32:33], v[192:193], v[194:195] op_sel_hi:[1,0,0]\n"
"v_pk_fma_f32 v[178:179], v[34:35], v[192:193], v[194:195] op_sel_hi:[1,0,0]\n"
"v_pk_fma_f32 v[180:181], v[36:37], v[192:193], v[194:195] op_sel_hi:[1,0,0]\n"
"v_pk_fma_f32 v[182:183], v[38:39], v[192:193], v[194:195] op_sel_hi:[1,0,0]\n"
"v_fma_f32 v48, v144, v56, v104\n"
"v_fma_f32 v50, v145, v57, v105\n"
"v_fma_f32 v52, v146, v58, v106\n"
"v_fma_f32 v54, v147, v59, v107\n"
"v_fma_f32 v96, v148, v60, v108\n"
"v_fma_f32 v98, v149, v61, v109\n"
"v_fma_f32 v100, v150, v62, v110\n"
"v_fma_f32 v102, v151, v63, v111\n"
"v_fma_f32 v64, v160, v72, v120\n"
"v_fma_f32 v66, v161, v73, v121\n"
"v_fma_f32 v68, v162, v74, v122\n"
"v_fma_f32 v70, v163, v75, v123\n"
"v_fma_f32 v112, v164, v76, v124\n"
"v_fma_f32 v114, v165, v77, v125\n"
"v_fma_f32 v116, v166, v78, v126\n"
"v_fma_f32 v118, v167, v79, v127\n"
"v_fma_f32 v80, v176, v88, v136\n"
"v_fma_f32 v82, v177, v89, v137\n"
"v_fma_f32 v84, v178, v90, v138\n"
"v_fma_f32 v86, v179, v91, v139\n"
"v_fma_f32 v128, v180, v92, v140\n"
"v_fma_f32 v130, v181, v93, v141\n"
"v_fma_f32 v132, v182, v94, v142\n"
"v_fma_f32 v134, v183, v95, v143\n"
"s_mov_b64 exec, s[8:9]\n"
"buffer_store_dwordx4 v[48:51], v245, s[28:31], s48 offen offset:0 sc1\n"
"buffer_store_dwordx4 v[52:55], v245, s[28:31], s48 offen offset:16 sc1\n"
"buffer_store_dwordx4 v[96:99], v245, s[28:31], s48 offen offset:128 sc1\n"
"buffer_store_dwordx4 v[100:103], v245, s[28:31], s48 offen offset:144 sc1\n"
"buffer_store_dwordx4 v[64:67], v245, s[28:31], s48 offen offset:1536 sc1\n"
"buffer_store_dwordx4 v[68:71], v245, s[28:31], s48 offen offset:1552 sc1\n"
"buffer_store_dwordx4 v[112:115], v245, s[28:31], s48 offen offset:1664 sc1\n"
"buffer_store_dwordx4 v[116:119], v245, s[28:31], s48 offen offset:1680 sc1\n"
"buffer_store_dwordx4 v[80:83], v245, s[28:31], s48 offen offset:3072 sc1\n"
"buffer_store_dwordx4 v[84:87], v245, s[28:31], s48 offen offset:3088 sc1\n"
"buffer_store_dwordx4 v[128:131], v245, s[28:31], s48 offen offset:3200 sc1\n"
"buffer_store_dwordx4 v[132:135], v245, s[28:31], s48 offen offset:3216 sc1\n"
"s_mov_b64 exec, -1\n"
"v_fma_f32 v200, v152, v48, v0\n"
"v_fma_f32 v201, v153, v50, v1\n"
"v_fma_f32 v202, v154, v52, v2\n"
"v_fma_f32 v203, v155, v54, v3\n"
"v_fma_f32 v204, v156, v96, v4\n"
"v_fma_f32 v205, v157, v98, v5\n"
"v_fma_f32 v206, v158, v100, v6\n"
"v_fma_f32 v207, v159, v102, v7\n"
"v_fma_f32 v208, v168, v64, v16\n"
"v_fma_f32 v209, v169, v66, v17\n"
"v_fma_f32 v210, v170, v68, v18\n"
"v_fma_f32 v211, v171, v70, v19\n"
"v_fma_f32 v212, v172, v112, v20\n"
"v_fma_f32 v213, v173, v114, v21\n"
"v_fma_f32 v214, v174, v116, v22\n"
"v_fma_f32 v215, v175, v118, v23\n"
"v_fma_f32 v216, v184, v80, v32\n"
"v_fma_f32 v217, v185, v82, v33\n"
"v_fma_f32 v218, v186, v84, v34\n"
"v_fma_f32 v219, v187, v86, v35\n"
"v_fma_f32 v220, v188, v128, v36\n"
"v_fma_f32 v221, v189, v130, v37\n"
"v_fma_f32 v222, v190, v132, v38\n"
"v_fma_f32 v223, v191, v134, v39\n"
"v_cvt_pk_bf16_f32 v228, v200, v201\n"
"v_cvt_pk_bf16_f32 v229, v202, v203\n"
"v_cvt_pk_bf16_f32 v230, v204, v205\n"
"v_cvt_pk_bf16_f32 v231, v206, v207\n"
"v_cvt_pk_bf16_f32 v232, v208, v209\n"
"v_cvt_pk_bf16_f32 v233, v210, v211\n"
"v_cvt_pk_bf16_f32 v234, v212, v213\n"
"v_cvt_pk_bf16_f32 v235, v214, v215\n"
"v_cvt_pk_bf16_f32 v236, v216, v217\n"
"v_cvt_pk_bf16_f32 v237, v218, v219\n"
"v_cvt_pk_bf16_f32 v238, v220, v221\n"
"v_cvt_pk_bf16_f32 v239, v222, v223\n"
"s_not_b64 exec, s[8:9]\n"
"buffer_store_dwordx2 v[228:229], v240, s[76:79], s46 offen offset:0\n"
"buffer_store_dwordx2 v[230:231], v240, s[76:79], s46 offen offset:32\n"
"buffer_store_dwordx2 v[232:233], v240, s[76:79], s46 offen offset:384\n"
"buffer_store_dwordx2 v[234:235], v240, s[76:79], s46 offen offset:416\n"
"buffer_store_dwordx2 v[236:237], v240, s[76:79], s46 offen offset:768\n"
"buffer_store_dwordx2 v[238:239], v240, s[76:79], s46 offen offset:800\n"
"s_mov_b64 exec, -1\n"
"v_mov_b32 v225, v196\n"
"v_mov_b32 v224, 0xffff0000\n"
"v_accvgpr_read_b32 v1, a48\n"
"v_accvgpr_read_b32 v3, a49\n"
"v_accvgpr_read_b32 v5, a50\n"
"v_accvgpr_read_b32 v7, a51\n"
"v_lshlrev_b32 v0, 16, v1\n"
"v_and_b32 v1, v224, v1\n"
"v_lshlrev_b32 v2, 16, v3\n"
"v_and_b32 v3, v224, v3\n"
"v_lshlrev_b32 v4, 16, v5\n"
"v_and_b32 v5, v224, v5\n"
"v_lshlrev_b32 v6, 16, v7\n"
"v_and_b32 v7, v224, v7\n"
"v_accvgpr_read_b32 v9, a84\n"
"v_accvgpr_read_b32 v11, a85\n"
"v_accvgpr_read_b32 v13, a86\n"
"v_accvgpr_read_b32 v15, a87\n"
"v_lshlrev_b32 v8, 16, v9\n"
"v_and_b32 v9, v224, v9\n"
"v_lshlrev_b32 v10, 16, v11\n"
"v_and_b32 v11, v224, v11\n"
"v_lshlrev_b32 v12, 16, v13\n"
"v_and_b32 v13, v224, v13\n"
"v_lshlrev_b32 v14, 16, v15\n"
"v_and_b32 v15, v224, v15\n"
"v_accvgpr_read_b32 v17, a52\n"
"v_accvgpr_read_b32 v19, a53\n"
"v_accvgpr_read_b32 v21, a54\n"
"v_accvgpr_read_b32 v23, a55\n"
"v_lshlrev_b32 v16, 16, v17\n"
"v_and_b32 v17, v224, v17\n"
"v_lshlrev_b32 v18, 16, v19\n"
"v_and_b32 v19, v224, v19\n"
"v_lshlrev_b32 v20, 16, v21\n"
"v_and_b32 v21, v224, v21\n"
"v_lshlrev_b32 v22, 16, v23\n"
"v_and_b32 v23, v224, v23\n"
"v_accvgpr_read_b32 v25, a88\n"
"v_accvgpr_read_b32 v27, a89\n"
"v_accvgpr_read_b32 v29, a90\n"
"v_accvgpr_read_b32 v31, a91\n"
"v_lshlrev_b32 v24, 16, v25\n"
"v_and_b32 v25, v224, v25\n"
"v_lshlrev_b32 v26, 16, v27\n"
"v_and_b32 v27, v224, v27\n"
"v_lshlrev_b32 v28, 16, v29\n"
"v_and_b32 v29, v224, v29\n"
"v_lshlrev_b32 v30, 16, v31\n"
"v_and_b32 v31, v224, v31\n"
"v_accvgpr_read_b32 v33, a56\n"
"v_accvgpr_read_b32 v35, a57\n"
"v_accvgpr_read_b32 v37, a58\n"
"v_accvgpr_read_b32 v39, a59\n"
"v_lshlrev_b32 v32, 16, v33\n"
"v_and_b32 v33, v224, v33\n"
"v_lshlrev_b32 v34, 16, v35\n"
"v_and_b32 v35, v224, v35\n"
"v_lshlrev_b32 v36, 16, v37\n"
"v_and_b32 v37, v224, v37\n"
"v_lshlrev_b32 v38, 16, v39\n"
"v_and_b32 v39, v224, v39\n"
"v_accvgpr_read_b32 v41, a92\n"
"v_accvgpr_read_b32 v43, a93\n"
"v_accvgpr_read_b32 v45, a94\n"
"v_accvgpr_read_b32 v47, a95\n"
"v_lshlrev_b32 v40, 16, v41\n"
"v_and_b32 v41, v224, v41\n"
"v_lshlrev_b32 v42, 16, v43\n"
"v_and_b32 v43, v224, v43\n"
"v_lshlrev_b32 v44, 16, v45\n"
"v_and_b32 v45, v224, v45\n"
"v_lshlrev_b32 v46, 16, v47\n"
"v_and_b32 v47, v224, v47\n"
"v_accvgpr_read_b32 v49, a60\n"
"v_accvgpr_read_b32 v51, a61\n"
"v_accvgpr_read_b32 v53, a62\n"
"v_accvgpr_read_b32 v55, a63\n"
"v_lshlrev_b32 v48, 16, v49\n"
"v_and_b32 v49, v224, v49\n"
"v_lshlrev_b32 v50, 16, v51\n"
"v_and_b32 v51, v224, v51\n"
"v_lshlrev_b32 v52, 16, v53\n"
"v_and_b32 v53, v224, v53\n"
"v_lshlrev_b32 v54, 16, v55\n"
"v_and_b32 v55, v224, v55\n"
"v_accvgpr_read_b32 v57, a96\n"
"v_accvgpr_read_b32 v59, a97\n"
"v_accvgpr_read_b32 v61, a98\n"
"v_accvgpr_read_b32 v63, a99\n"
"v_lshlrev_b32 v56, 16, v57\n"
"v_and_b32 v57, v224, v57\n"
"v_lshlrev_b32 v58, 16, v59\n"
"v_and_b32 v59, v224, v59\n"
"v_lshlrev_b32 v60, 16, v61\n"
"v_and_b32 v61, v224, v61\n"
"v_lshlrev_b32 v62, 16, v63\n"
"v_and_b32 v63, v224, v63\n"
"v_accvgpr_read_b32 v65, a64\n"
"v_accvgpr_read_b32 v67, a65\n"
"v_accvgpr_read_b32 v69, a66\n"
"v_accvgpr_read_b32 v71, a67\n"
"v_lshlrev_b32 v64, 16, v65\n"
"v_and_b32 v65, v224, v65\n"
"v_lshlrev_b32 v66, 16, v67\n"
"v_and_b32 v67, v224, v67\n"
"v_lshlrev_b32 v68, 16, v69\n"
"v_and_b32 v69, v224, v69\n"
"v_lshlrev_b32 v70, 16, v71\n"
"v_and_b32 v71, v224, v71\n"
"v_accvgpr_read_b32 v73, a100\n"
"v_accvgpr_read_b32 v75, a101\n"
"v_accvgpr_read_b32 v77, a102\n"
"v_accvgpr_read_b32 v79, a103\n"
"v_lshlrev_b32 v72, 16, v73\n"
"v_and_b32 v73, v224, v73\n"
"v_lshlrev_b32 v74, 16, v75\n"
"v_and_b32 v75, v224, v75\n"
"v_lshlrev_b32 v76, 16, v77\n"
"v_and_b32 v77, v224, v77\n"
"v_lshlrev_b32 v78, 16, v79\n"
"v_and_b32 v79, v224, v79\n"
"v_accvgpr_read_b32 v81, a68\n"
"v_accvgpr_read_b32 v83, a69\n"
"v_accvgpr_read_b32 v85, a70\n"
"v_accvgpr_read_b32 v87, a71\n"
"v_lshlrev_b32 v80, 16, v81\n"
"v_and_b32 v81, v224, v81\n"
"v_lshlrev_b32 v82, 16, v83\n"
"v_and_b32 v83, v224, v83\n"
"v_lshlrev_b32 v84, 16, v85\n"
"v_and_b32 v85, v224, v85\n"
"v_lshlrev_b32 v86, 16, v87\n"
"v_and_b32 v87, v224, v87\n"
"v_accvgpr_read_b32 v89, a104\n"
"v_accvgpr_read_b32 v91, a105\n"
"v_accvgpr_read_b32 v93, a106\n"
"v_accvgpr_read_b32 v95, a107\n"
"v_lshlrev_b32 v88, 16, v89\n"
"v_and_b32 v89, v224, v89\n"
"v_lshlrev_b32 v90, 16, v91\n"
"v_and_b32 v91, v224, v91\n"
"v_lshlrev_b32 v92, 16, v93\n"
"v_and_b32 v93, v224, v93\n"
"v_lshlrev_b32 v94, 16, v95\n"
"v_and_b32 v95, v224, v95\n"
"v_accvgpr_read_b32 v97, a72\n"
"v_accvgpr_read_b32 v99, a73\n"
"v_accvgpr_read_b32 v101, a74\n"
"v_accvgpr_read_b32 v103, a75\n"
"v_lshlrev_b32 v96, 16, v97\n"
"v_and_b32 v97, v224, v97\n"
"v_lshlrev_b32 v98, 16, v99\n"
"v_and_b32 v99, v224, v99\n"
"v_lshlrev_b32 v100, 16, v101\n"
"v_and_b32 v101, v224, v101\n"
"v_lshlrev_b32 v102, 16, v103\n"
"v_and_b32 v103, v224, v103\n"
"v_accvgpr_read_b32 v105, a108\n"
"v_accvgpr_read_b32 v107, a109\n"
"v_accvgpr_read_b32 v109, a110\n"
"v_accvgpr_read_b32 v111, a111\n"
"v_lshlrev_b32 v104, 16, v105\n"
"v_and_b32 v105, v224, v105\n"
"v_lshlrev_b32 v106, 16, v107\n"
"v_and_b32 v107, v224, v107\n"
"v_lshlrev_b32 v108, 16, v109\n"
"v_and_b32 v109, v224, v109\n"
"v_lshlrev_b32 v110, 16, v111\n"
"v_and_b32 v111, v224, v111\n"
"v_accvgpr_read_b32 v113, a76\n"
"v_accvgpr_read_b32 v115, a77\n"
"v_accvgpr_read_b32 v117, a78\n"
"v_accvgpr_read_b32 v119, a79\n"
"v_lshlrev_b32 v112, 16, v113\n"
"v_and_b32 v113, v224, v113\n"
"v_lshlrev_b32 v114, 16, v115\n"
"v_and_b32 v115, v224, v115\n"
"v_lshlrev_b32 v116, 16, v117\n"
"v_and_b32 v117, v224, v117\n"
"v_lshlrev_b32 v118, 16, v119\n"
"v_and_b32 v119, v224, v119\n"
"v_accvgpr_read_b32 v121, a112\n"
"v_accvgpr_read_b32 v123, a113\n"
"v_accvgpr_read_b32 v125, a114\n"
"v_accvgpr_read_b32 v127, a115\n"
"v_lshlrev_b32 v120, 16, v121\n"
"v_and_b32 v121, v224, v121\n"
"v_lshlrev_b32 v122, 16, v123\n"
"v_and_b32 v123, v224, v123\n"
"v_lshlrev_b32 v124, 16, v125\n"
"v_and_b32 v125, v224, v125\n"
"v_lshlrev_b32 v126, 16, v127\n"
"v_and_b32 v127, v224, v127\n"
"v_accvgpr_read_b32 v129, a80\n"
"v_accvgpr_read_b32 v131, a81\n"
"v_accvgpr_read_b32 v133, a82\n"
"v_accvgpr_read_b32 v135, a83\n"
"v_lshlrev_b32 v128, 16, v129\n"
"v_and_b32 v129, v224, v129\n"
"v_lshlrev_b32 v130, 16, v131\n"
"v_and_b32 v131, v224, v131\n"
"v_lshlrev_b32 v132, 16, v133\n"
"v_and_b32 v133, v224, v133\n"
"v_lshlrev_b32 v134, 16, v135\n"
"v_and_b32 v135, v224, v135\n"
"v_accvgpr_read_b32 v137, a116\n"
"v_accvgpr_read_b32 v139, a117\n"
"v_accvgpr_read_b32 v141, a118\n"
"v_accvgpr_read_b32 v143, a119\n"
"v_lshlrev_b32 v136, 16, v137\n"
"v_and_b32 v137, v224, v137\n"
"v_lshlrev_b32 v138, 16, v139\n"
"v_and_b32 v139, v224, v139\n"
"v_lshlrev_b32 v140, 16, v141\n"
"v_and_b32 v141, v224, v141\n"
"v_lshlrev_b32 v142, 16, v143\n"
"v_and_b32 v143, v224, v143\n"
"v_lshrrev_b32 v242, 2, v248\n"
"v_lshlrev_b32 v241, 4, v242\n"
"v_lshlrev_b32 v242, 3, v242\n"
"v_lshrrev_b32 v243, 5, v254\n"
"v_mul_u32_u24 v243, 24576, v243\n"
"v_and_b32 v247, 3, v248\n"
"v_lshl_add_u32 v243, v247, 5, v243\n"
"v_and_b32 v244, 0x10000000, v254\n"
"v_or_b32 v243, v244, v243\n"
"v_lshrrev_b32 v244, 5, v248\n"
"v_mul_u32_u24 v244, 96, v244\n"
"v_and_b32 v246, 16, v248\n"
"v_subrev_u32 v252, 65536, v227\n"
"v_add3_u32 v244, v244, v246, v252\n"
"v_lshl_add_u32 v244, v247, 5, v244\n"
"s_mul_i32 s63, s41, 384\n"
"v_add_u32 v244, s63, v244\n"
"v_lshl_add_u32 v249, v248, 2, v252\n"
"s_lshl_b32 s63, s41, 8\n"
"v_add_u32 v251, s63, v249\n"
"v_add_u32 v252, s63, v252\n"
"AT_B3:\n"
"s_waitcnt lgkmcnt(0)\n"
"s_barrier\n"
"ds_read_b64 v[246:247], v227 offset:656\n"
"s_mul_i32 s58, s41, 1152\n"
"s_add_u32 s58, s58, 37888000\n"
"s_waitcnt lgkmcnt(0)\n"
"v_readfirstlane_b32 s10, v246\n"
"v_readfirstlane_b32 s11, v247\n"
"s_nop 0\n"
"s_andn2_b32 s12, 0x7ff, s10\n"
"s_andn2_b32 s13, 0x7ff, s11\n"
"s_mul_i32 s56, s12, 9216\n"
"s_mul_i32 s57, s13, 9216\n"
"s_add_u32 s56, s56, s58\n"
"s_add_u32 s57, s57, s58\n"
"s_cmp_ge_u32 s42, 192\n"
"s_cbranch_scc1 AT_A4\n"
"buffer_load_dwordx4 v[144:147], v241, s[28:31], s56 offen offset:0 sc1\n"
"buffer_load_dwordx4 v[148:151], v241, s[28:31], s56 offen offset:256 sc1\n"
"buffer_load_dwordx4 v[152:155], v241, s[28:31], s56 offen offset:512 sc1\n"
"buffer_load_dwordx4 v[156:159], v241, s[28:31], s56 offen offset:768 sc1\n"
"buffer_load_dwordx2 v[160:161], v242, s[28:31], s56 offen offset:1024 sc1\n"
"buffer_load_dwordx4 v[162:165], v241, s[28:31], s57 offen offset:0 sc1\n"
"buffer_load_dwordx4 v[166:169], v241, s[28:31], s57 offen offset:256 sc1\n"
"buffer_load_dwordx4 v[170:173], v241, s[28:31], s57 offen offset:512 sc1\n"
"buffer_load_dwordx4 v[174:177], v241, s[28:31], s57 offen offset:768 sc1\n"
"buffer_load_dwordx2 v[178:179], v242, s[28:31], s57 offen offset:1024 sc1\n"
"AT_A4:\n"
"v_cmp_eq_u32 vcc, s10, v225\n"
"v_cmp_eq_u32 s[58:59], s11, v225\n"
"s_nop 0\n"
"s_or_b64 s[58:59], s[58:59], vcc\n"
"s_andn2_b64 exec, s[8:9], s[58:59]\n"
"buffer_store_dwordx2 v[228:229], v240, s[76:79], s46 offen offset:0\n"
"buffer_store_dwordx2 v[230:231], v240, s[76:79], s46 offen offset:32\n"
"buffer_store_dwordx2 v[232:233], v240, s[76:79], s46 offen offset:384\n"
"buffer_store_dwordx2 v[234:235], v240, s[76:79], s46 offen offset:416\n"
"buffer_store_dwordx2 v[236:237], v240, s[76:79], s46 offen offset:768\n"
"buffer_store_dwordx2 v[238:239], v240, s[76:79], s46 offen offset:800\n"
"s_and_b64 exec, s[8:9], s[58:59]\n"
"s_cbranch_execz AT_A5\n"
"s_add_u32 s60, s48, 4608\n"
"v_mov_b32 v181, s85\n"
"v_mov_b32 v183, s85\n"
"v_mov_b32 v185, s85\n"
"v_mov_b32 v187, s85\n"
"v_mov_b32 v189, s85\n"
"v_mov_b32 v191, s85\n"
"v_mov_b32 v193, s85\n"
"v_mov_b32 v195, s85\n"
"v_mov_b32 v180, v200\n"
"v_mov_b32 v182, v201\n"
"buffer_store_dwordx4 v[180:183], v245, s[28:31], s60 offen offset:0 sc1\n"
"v_mov_b32 v184, v202\n"
"v_mov_b32 v186, v203\n"
"buffer_store_dwordx4 v[184:187], v245, s[28:31], s60 offen offset:16 sc1\n"
"v_mov_b32 v188, v204\n"
"v_mov_b32 v190, v205\n"
"buffer_store_dwordx4 v[188:191], v245, s[28:31], s60 offen offset:128 sc1\n"
"v_mov_b32 v192, v206\n"
"v_mov_b32 v194, v207\n"
"buffer_store_dwordx4 v[192:195], v245, s[28:31], s60 offen offset:144 sc1\n"
"v_mov_b32 v180, v208\n"
"v_mov_b32 v182, v209\n"
"buffer_store_dwordx4 v[180:183], v245, s[28:31], s60 offen offset:1536 sc1\n"
"v_mov_b32 v184, v210\n"
"v_mov_b32 v186, v211\n"
"buffer_store_dwordx4 v[184:187], v245, s[28:31], s60 offen offset:1552 sc1\n"
"v_mov_b32 v188, v212\n"
"v_mov_b32 v190, v213\n"
"buffer_store_dwordx4 v[188:191], v245, s[28:31], s60 offen offset:1664 sc1\n"
"v_mov_b32 v192, v214\n"
"v_mov_b32 v194, v215\n"
"buffer_store_dwordx4 v[192:195], v245, s[28:31], s60 offen offset:1680 sc1\n"
"v_mov_b32 v180, v216\n"
"v_mov_b32 v182, v217\n"
"buffer_store_dwordx4 v[180:183], v245, s[28:31], s60 offen offset:3072 sc1\n"
"v_mov_b32 v184, v218\n"
"v_mov_b32 v186, v219\n"
"buffer_store_dwordx4 v[184:187], v245, s[28:31], s60 offen offset:3088 sc1\n"
"v_mov_b32 v188, v220\n"
"v_mov_b32 v190, v221\n"
"buffer_store_dwordx4 v[188:191], v245, s[28:31], s60 offen offset:3200 sc1\n"
"v_mov_b32 v192, v222\n"
"v_mov_b32 v194, v223\n"
"buffer_store_dwordx4 v[192:195], v245, s[28:31], s60 offen offset:3216 sc1\n"
"AT_A5:\n"
"s_mov_b64 exec, -1\n"
"s_cmp_ge_u32 s42, 192\n"
"s_cbranch_scc1 AT_F0\n"
"s_mov_b32 s87, 0x80000\n"
"s_waitcnt vmcnt(6)\n"
"AT_A6:\n"
"v_xor_b32 v212, s85, v145\n"
"v_xor_b32 v213, s85, v147\n"
"v_bitop3_b32 v212, v149, s85, v212 bitop3:0xbe\n"
"v_bitop3_b32 v213, v151, s85, v213 bitop3:0xbe\n"
"v_bitop3_b32 v212, v153, s85, v212 bitop3:0xbe\n"
"v_bitop3_b32 v213, v155, s85, v213 bitop3:0xbe\n"
"v_bitop3_b32 v212, v157, s85, v212 bitop3:0xbe\n"
"v_bitop3_b32 v213, v159, s85, v213 bitop3:0xbe\n"
"v_bitop3_b32 v212, v161, s85, v212 bitop3:0xbe\n"
"v_bitop3_b32 v213, v163, s85, v213 bitop3:0xbe\n"
"v_bitop3_b32 v212, v165, s85, v212 bitop3:0xbe\n"
"v_bitop3_b32 v213, v167, s85, v213 bitop3:0xbe\n"
"v_bitop3_b32 v212, v169, s85, v212 bitop3:0xbe\n"
"v_bitop3_b32 v213, v171, s85, v213 bitop3:0xbe\n"
"v_bitop3_b32 v212, v173, s85, v212 bitop3:0xbe\n"
"v_bitop3_b32 v213, v175, s85, v213 bitop3:0xbe\n"
"v_bitop3_b32 v212, v177, s85, v212 bitop3:0xbe\n"
"v_bitop3_b32 v213, v179, s85, v213 bitop3:0xbe\n"
"v_or_b32 v212, v212, v213\n"
"v_cmp_ne_u32 vcc, 0, v212\n"
"s_nop 0\n"
"s_cbranch_vccz AT_A7\n"
"s_sub_u32 s87, s87, 1\n"
"s_cmp_eq_u32 s87, 0\n"
"s_cbranch_scc1 AT_A7\n"
"s_sleep 1\n"
"buffer_load_dwordx4 v[144:147], v241, s[28:31], s56 offen offset:0 sc1\n"
"buffer_load_dwordx4 v[148:151], v241, s[28:31], s56 offen offset:256 sc1\n"
"buffer_load_dwordx4 v[152:155], v241, s[28:31], s56 offen offset:512 sc1\n"
"buffer_load_dwordx4 v[156:159], v241, s[28:31], s56 offen offset:768 sc1\n"
"buffer_load_dwordx2 v[160:161], v242, s[28:31], s56 offen offset:1024 sc1\n"
"buffer_load_dwordx4 v[162:165], v241, s[28:31], s57 offen offset:0 sc1\n"
"buffer_load_dwordx4 v[166:169], v241, s[28:31], s57 offen offset:256 sc1\n"
"buffer_load_dwordx4 v[170:173], v241, s[28:31], s57 offen offset:512 sc1\n"
"buffer_load_dwordx4 v[174:177], v241, s[28:31], s57 offen offset:768 sc1\n"
"buffer_load_dwordx2 v[178:179], v242, s[28:31], s57 offen offset:1024 sc1\n"
"s_waitcnt vmcnt(0)\n"
"s_branch AT_A6\n"
"AT_A7:\n"
"v_pk_mul_f32 v[180:181], v[0:1], v[144:145] op_sel_hi:[1,0]\n"
"v_pk_mul_f32 v[182:183], v[2:3], v[144:145] op_sel_hi:[1,0]\n"
"v_pk_mul_f32 v[184:185], v[4:5], v[144:145] op_sel_hi:[1,0]\n"
"v_pk_mul_f32 v[186:187], v[6:7], v[144:145] op_sel_hi:[1,0]\n"
"v_pk_mul_f32 v[196:197], v[8:9], v[144:145] op_sel_hi:[1,0]\n"
"v_pk_mul_f32 v[198:199], v[10:11], v[144:145] op_sel_hi:[1,0]\n"
"v_pk_mul_f32 v[200:201], v[12:13], v[144:145] op_sel_hi:[1,0]\n"
"v_pk_mul_f32 v[202:203], v[14:15], v[144:145] op_sel_hi:[1,0]\n"
"v_pk_mul_f32 v[188:189], v[0:1], v[162:163] op_sel_hi:[1,0]\n"
"v_pk_mul_f32 v[190:191], v[2:3], v[162:163] op_sel_hi:[1,0]\n"
"v_pk_mul_f32 v[192:193], v[4:5], v[162:163] op_sel_hi:[1,0]\n"
"v_pk_mul_f32 v[194:195], v[6:7], v[162:163] op_sel_hi:[1,0]\n"
"v_pk_mul_f32 v[204:205], v[8:9], v[162:163] op_sel_hi:[1,0]\n"
"v_pk_mul_f32 v[206:207], v[10:11], v[162:163] op_sel_hi:[1,0]\n"
"v_pk_mul_f32 v[208:209], v[12:13], v[162:163] op_sel_hi:[1,0]\n"
"v_pk_mul_f32 v[210:211], v[14:15], v[162:163] op_sel_hi:[1,0]\n"
"v_pk_fma_f32 v[180:181], v[16:17], v[146:147], v[180:181] op_sel_hi:[1,0,1]\n"
"v_pk_fma_f32 v[182:183], v[18:19], v[146:147], v[182:183] op_sel_hi:[1,0,1]\n"
"v_pk_fma_f32 v[184:185], v[20:21], v[146:147], v[184:185] op_sel_hi:[1,0,1]\n"
"v_pk_fma_f32 v[186:187], v[22:23], v[146:147], v[186:187] op_sel_hi:[1,0,1]\n"
"v_pk_fma_f32 v[196:197], v[24:25], v[146:147], v[196:197] op_sel_hi:[1,0,1]\n"
"v_pk_fma_f32 v[198:199], v[26:27], v[146:147], v[198:199] op_sel_hi:[1,0,1]\n"
"v_pk_fma_f32 v[200:201], v[28:29], v[146:147], v[200:201] op_sel_hi:[1,0,1]\n"
"v_pk_fma_f32 v[202:203], v[30:31], v[146:147], v[202:203] op_sel_hi:[1,0,1]\n"
"v_pk_fma_f32 v[188:189], v[16:17], v[164:165], v[188:189] op_sel_hi:[1,0,1]\n"
"v_pk_fma_f32 v[190:191], v[18:19], v[164:165], v[190:191] op_sel_hi:[1,0,1]\n"
"v_pk_fma_f32 v[192:193], v[20:21], v[164:165], v[192:193] op_sel_hi:[1,0,1]\n"
"v_pk_fma_f32 v[194:195], v[22:23], v[164:165], v[194:195] op_sel_hi:[1,0,1]\n"
"v_pk_fma_f32 v[204:205], v[24:25], v[164:165], v[204:205] op_sel_hi:[1,0,1]\n"
"v_pk_fma_f32 v[206:207], v[26:27], v[164:165], v[206:207] op_sel_hi:[1,0,1]\n"
"v_pk_fma_f32 v[208:209], v[28:29], v[164:165], v[208:209] op_sel_hi:[1,0,1]\n"
"v_pk_fma_f32 v[210:211], v[30:31], v[164:165], v[210:211] op_sel_hi:[1,0,1]\n"
"v_pk_fma_f32 v[180:181], v[32:33], v[148:149], v[180:181] op_sel_hi:[1,0,1]\n"
"v_pk_fma_f32 v[182:183], v[34:35], v[148:149], v[182:183] op_sel_hi:[1,0,1]\n"
"v_pk_fma_f32 v[184:185], v[36:37], v[148:149], v[184:185] op_sel_hi:[1,0,1]\n"
"v_pk_fma_f32 v[186:187], v[38:39], v[148:149], v[186:187] op_sel_hi:[1,0,1]\n"
"v_pk_fma_f32 v[196:197], v[40:41], v[148:149], v[196:197] op_sel_hi:[1,0,1]\n"
"v_pk_fma_f32 v[198:199], v[42:43], v[148:149], v[198:199] op_sel_hi:[1,0,1]\n"
"v_pk_fma_f32 v[200:201], v[44:45], v[148:149], v[200:201] op_sel_hi:[1,0,1]\n"
"v_pk_fma_f32 v[202:203], v[46:47], v[148:149], v[202:203] op_sel_hi:[1,0,1]\n"
"v_pk_fma_f32 v[188:189], v[32:33], v[166:167], v[188:189] op_sel_hi:[1,0,1]\n"
"v_pk_fma_f32 v[190:191], v[34:35], v[166:167], v[190:191] op_sel_hi:[1,0,1]\n"
"v_pk_fma_f32 v[192:193], v[36:37], v[166:167], v[192:193] op_sel_hi:[1,0,1]\n"
"v_pk_fma_f32 v[194:195], v[38:39], v[166:167], v[194:195] op_sel_hi:[1,0,1]\n"
"v_pk_fma_f32 v[204:205], v[40:41], v[166:167], v[204:205] op_sel_hi:[1,0,1]\n"
"v_pk_fma_f32 v[206:207], v[42:43], v[166:167], v[206:207] op_sel_hi:[1,0,1]\n"
"v_pk_fma_f32 v[208:209], v[44:45], v[166:167], v[208:209] op_sel_hi:[1,0,1]\n"
"v_pk_fma_f32 v[210:211], v[46:47], v[166:167], v[210:211] op_sel_hi:[1,0,1]\n"
"v_pk_fma_f32 v[180:181], v[48:49], v[150:151], v[180:181] op_sel_hi:[1,0,1]\n"
"v_pk_fma_f32 v[182:183], v[50:51], v[150:151], v[182:183] op_sel_hi:[1,0,1]\n"
"v_pk_fma_f32 v[184:185], v[52:53], v[150:151], v[184:185] op_sel_hi:[1,0,1]\n"
"v_pk_fma_f32 v[186:187], v[54:55], v[150:151], v[186:187] op_sel_hi:[1,0,1]\n"
"v_pk_fma_f32 v[196:197], v[56:57], v[150:151], v[196:197] op_sel_hi:[1,0,1]\n"
"v_pk_fma_f32 v[198:199], v[58:59], v[150:151], v[198:199] op_sel_hi:[1,0,1]\n"
"v_pk_fma_f32 v[200:201], v[60:61], v[150:151], v[200:201] op_sel_hi:[1,0,1]\n"
"v_pk_fma_f32 v[202:203], v[62:63], v[150:151], v[202:203] op_sel_hi:[1,0,1]\n"
"v_pk_fma_f32 v[188:189], v[48:49], v[168:169], v[188:189] op_sel_hi:[1,0,1]\n"
"v_pk_fma_f32 v[190:191], v[50:51], v[168:169], v[190:191] op_sel_hi:[1,0,1]\n"
"v_pk_fma_f32 v[192:193], v[52:53], v[168:169], v[192:193] op_sel_hi:[1,0,1]\n"
"v_pk_fma_f32 v[194:195], v[54:55], v[168:169], v[194:195] op_sel_hi:[1,0,1]\n"
"v_pk_fma_f32 v[204:205], v[56:57], v[168:169], v[204:205] op_sel_hi:[1,0,1]\n"
"v_pk_fma_f32 v[206:207], v[58:59], v[168:169], v[206:207] op_sel_hi:[1,0,1]\n"
"v_pk_fma_f32 v[208:209], v[60:61], v[168:169], v[208:209] op_sel_hi:[1,0,1]\n"
"v_pk_fma_f32 v[210:211], v[62:63], v[168:169], v[210:211] op_sel_hi:[1,0,1]\n"
"v_pk_fma_f32 v[180:181], v[64:65], v[152:153], v[180:181] op_sel_hi:[1,0,1]\n"
"v_pk_fma_f32 v[182:183], v[66:67], v[152:153], v[182:183] op_sel_hi:[1,0,1]\n"
"v_pk_fma_f32 v[184:185], v[68:69], v[152:153], v[184:185] op_sel_hi:[1,0,1]\n"
"v_pk_fma_f32 v[186:187], v[70:71], v[152:153], v[186:187] op_sel_hi:[1,0,1]\n"
"v_pk_fma_f32 v[196:197], v[72:73], v[152:153], v[196:197] op_sel_hi:[1,0,1]\n"
"v_pk_fma_f32 v[198:199], v[74:75], v[152:153], v[198:199] op_sel_hi:[1,0,1]\n"
"v_pk_fma_f32 v[200:201], v[76:77], v[152:153], v[200:201] op_sel_hi:[1,0,1]\n"
"v_pk_fma_f32 v[202:203], v[78:79], v[152:153], v[202:203] op_sel_hi:[1,0,1]\n"
"v_pk_fma_f32 v[188:189], v[64:65], v[170:171], v[188:189] op_sel_hi:[1,0,1]\n"
"v_pk_fma_f32 v[190:191], v[66:67], v[170:171], v[190:191] op_sel_hi:[1,0,1]\n"
"v_pk_fma_f32 v[192:193], v[68:69], v[170:171], v[192:193] op_sel_hi:[1,0,1]\n"
"v_pk_fma_f32 v[194:195], v[70:71], v[170:171], v[194:195] op_sel_hi:[1,0,1]\n"
"v_pk_fma_f32 v[204:205], v[72:73], v[170:171], v[204:205] op_sel_hi:[1,0,1]\n"
"v_pk_fma_f32 v[206:207], v[74:75], v[170:171], v[206:207] op_sel_hi:[1,0,1]\n"
"v_pk_fma_f32 v[208:209], v[76:77], v[170:171], v[208:209] op_sel_hi:[1,0,1]\n"
"v_pk_fma_f32 v[210:211], v[78:79], v[170:171], v[210:211] op_sel_hi:[1,0,1]\n"
"v_pk_fma_f32 v[180:181], v[80:81], v[154:155], v[180:181] op_sel_hi:[1,0,1]\n"
"v_pk_fma_f32 v[182:183], v[82:83], v[154:155], v[182:183] op_sel_hi:[1,0,1]\n"
"v_pk_fma_f32 v[184:185], v[84:85], v[154:155], v[184:185] op_sel_hi:[1,0,1]\n"
"v_pk_fma_f32 v[186:187], v[86:87], v[154:155], v[186:187] op_sel_hi:[1,0,1]\n"
"v_pk_fma_f32 v[196:197], v[88:89], v[154:155], v[196:197] op_sel_hi:[1,0,1]\n"
"v_pk_fma_f32 v[198:199], v[90:91], v[154:155], v[198:199] op_sel_hi:[1,0,1]\n"
"v_pk_fma_f32 v[200:201], v[92:93], v[154:155], v[200:201] op_sel_hi:[1,0,1]\n"
"v_pk_fma_f32 v[202:203], v[94:95], v[154:155], v[202:203] op_sel_hi:[1,0,1]\n"
"v_pk_fma_f32 v[188:189], v[80:81], v[172:173], v[188:189] op_sel_hi:[1,0,1]\n"
"v_pk_fma_f32 v[190:191], v[82:83], v[172:173], v[190:191] op_sel_hi:[1,0,1]\n"
"v_pk_fma_f32 v[192:193], v[84:85], v[172:173], v[192:193] op_sel_hi:[1,0,1]\n"
"v_pk_fma_f32 v[194:195], v[86:87], v[172:173], v[194:195] op_sel_hi:[1,0,1]\n"
"v_pk_fma_f32 v[204:205], v[88:89], v[172:173], v[204:205] op_sel_hi:[1,0,1]\n"
"v_pk_fma_f32 v[206:207], v[90:91], v[172:173], v[206:207] op_sel_hi:[1,0,1]\n"
"v_pk_fma_f32 v[208:209], v[92:93], v[172:173], v[208:209] op_sel_hi:[1,0,1]\n"
"v_pk_fma_f32 v[210:211], v[94:95], v[172:173], v[210:211] op_sel_hi:[1,0,1]\n"
"v_pk_fma_f32 v[180:181], v[96:97], v[156:157], v[180:181] op_sel_hi:[1,0,1]\n"
"v_pk_fma_f32 v[182:183], v[98:99], v[156:157], v[182:183] op_sel_hi:[1,0,1]\n"
"v_pk_fma_f32 v[184:185], v[100:101], v[156:157], v[184:185] op_sel_hi:[1,0,1]\n"
"v_pk_fma_f32 v[186:187], v[102:103], v[156:157], v[186:187] op_sel_hi:[1,0,1]\n"
"v_pk_fma_f32 v[196:197], v[104:105], v[156:157], v[196:197] op_sel_hi:[1,0,1]\n"
"v_pk_fma_f32 v[198:199], v[106:107], v[156:157], v[198:199] op_sel_hi:[1,0,1]\n"
"v_pk_fma_f32 v[200:201], v[108:109], v[156:157], v[200:201] op_sel_hi:[1,0,1]\n"
"v_pk_fma_f32 v[202:203], v[110:111], v[156:157], v[202:203] op_sel_hi:[1,0,1]\n"
"v_pk_fma_f32 v[188:189], v[96:97], v[174:175], v[188:189] op_sel_hi:[1,0,1]\n"
"v_pk_fma_f32 v[190:191], v[98:99], v[174:175], v[190:191] op_sel_hi:[1,0,1]\n"
"v_pk_fma_f32 v[192:193], v[100:101], v[174:175], v[192:193] op_sel_hi:[1,0,1]\n"
"v_pk_fma_f32 v[194:195], v[102:103], v[174:175], v[194:195] op_sel_hi:[1,0,1]\n"
"v_pk_fma_f32 v[204:205], v[104:105], v[174:175], v[204:205] op_sel_hi:[1,0,1]\n"
"v_pk_fma_f32 v[206:207], v[106:107], v[174:175], v[206:207] op_sel_hi:[1,0,1]\n"
"v_pk_fma_f32 v[208:209], v[108:109], v[174:175], v[208:209] op_sel_hi:[1,0,1]\n"
"v_pk_fma_f32 v[210:211], v[110:111], v[174:175], v[210:211] op_sel_hi:[1,0,1]\n"
"v_pk_fma_f32 v[180:181], v[112:113], v[158:159], v[180:181] op_sel_hi:[1,0,1]\n"
"v_pk_fma_f32 v[182:183], v[114:115], v[158:159], v[182:183] op_sel_hi:[1,0,1]\n"
"v_pk_fma_f32 v[184:185], v[116:117], v[158:159], v[184:185] op_sel_hi:[1,0,1]\n"
"v_pk_fma_f32 v[186:187], v[118:119], v[158:159], v[186:187] op_sel_hi:[1,0,1]\n"
"v_pk_fma_f32 v[196:197], v[120:121], v[158:159], v[196:197] op_sel_hi:[1,0,1]\n"
"v_pk_fma_f32 v[198:199], v[122:123], v[158:159], v[198:199] op_sel_hi:[1,0,1]\n"
"v_pk_fma_f32 v[200:201], v[124:125], v[158:159], v[200:201] op_sel_hi:[1,0,1]\n"
"v_pk_fma_f32 v[202:203], v[126:127], v[158:159], v[202:203] op_sel_hi:[1,0,1]\n"
"v_pk_fma_f32 v[188:189], v[112:113], v[176:177], v[188:189] op_sel_hi:[1,0,1]\n"
"v_pk_fma_f32 v[190:191], v[114:115], v[176:177], v[190:191] op_sel_hi:[1,0,1]\n"
"v_pk_fma_f32 v[192:193], v[116:117], v[176:177], v[192:193] op_sel_hi:[1,0,1]\n"
"v_pk_fma_f32 v[194:195], v[118:119], v[176:177], v[194:195] op_sel_hi:[1,0,1]\n"
"v_pk_fma_f32 v[204:205], v[120:121], v[176:177], v[204:205] op_sel_hi:[1,0,1]\n"
"v_pk_fma_f32 v[206:207], v[122:123], v[176:177], v[206:207] op_sel_hi:[1,0,1]\n"
"v_pk_fma_f32 v[208:209], v[124:125], v[176:177], v[208:209] op_sel_hi:[1,0,1]\n"
"v_pk_fma_f32 v[210:211], v[126:127], v[176:177], v[210:211] op_sel_hi:[1,0,1]\n"
"v_pk_fma_f32 v[180:181], v[128:129], v[160:161], v[180:181] op_sel_hi:[1,0,1]\n"
"v_pk_fma_f32 v[182:183], v[130:131], v[160:161], v[182:183] op_sel_hi:[1,0,1]\n"
"v_pk_fma_f32 v[184:185], v[132:133], v[160:161], v[184:185] op_sel_hi:[1,0,1]\n"
"v_pk_fma_f32 v[186:187], v[134:135], v[160:161], v[186:187] op_sel_hi:[1,0,1]\n"
"v_pk_fma_f32 v[196:197], v[136:137], v[160:161], v[196:197] op_sel_hi:[1,0,1]\n"
"v_pk_fma_f32 v[198:199], v[138:139], v[160:161], v[198:199] op_sel_hi:[1,0,1]\n"
"v_pk_fma_f32 v[200:201], v[140:141], v[160:161], v[200:201] op_sel_hi:[1,0,1]\n"
"v_pk_fma_f32 v[202:203], v[142:143], v[160:161], v[202:203] op_sel_hi:[1,0,1]\n"
"v_pk_fma_f32 v[188:189], v[128:129], v[178:179], v[188:189] op_sel_hi:[1,0,1]\n"
"v_pk_fma_f32 v[190:191], v[130:131], v[178:179], v[190:191] op_sel_hi:[1,0,1]\n"
"v_pk_fma_f32 v[192:193], v[132:133], v[178:179], v[192:193] op_sel_hi:[1,0,1]\n"
"v_pk_fma_f32 v[194:195], v[134:135], v[178:179], v[194:195] op_sel_hi:[1,0,1]\n"
"v_pk_fma_f32 v[204:205], v[136:137], v[178:179], v[204:205] op_sel_hi:[1,0,1]\n"
"v_pk_fma_f32 v[206:207], v[138:139], v[178:179], v[206:207] op_sel_hi:[1,0,1]\n"
"v_pk_fma_f32 v[208:209], v[140:141], v[178:179], v[208:209] op_sel_hi:[1,0,1]\n"
"v_pk_fma_f32 v[210:211], v[142:143], v[178:179], v[210:211] op_sel_hi:[1,0,1]\n"
"v_accvgpr_read_b32 v1, a0\n"
"v_accvgpr_read_b32 v3, a1\n"
"v_permlane32_swap_b32 v180, v188\n"
"v_permlane32_swap_b32 v181, v189\n"
"v_permlane32_swap_b32 v182, v190\n"
"v_permlane32_swap_b32 v183, v191\n"
"v_permlane32_swap_b32 v184, v192\n"
"v_permlane32_swap_b32 v185, v193\n"
"v_permlane32_swap_b32 v186, v194\n"
"v_permlane32_swap_b32 v187, v195\n"
"v_permlane32_swap_b32 v196, v204\n"
"v_permlane32_swap_b32 v197, v205\n"
"v_permlane32_swap_b32 v198, v206\n"
"v_permlane32_swap_b32 v199, v207\n"
"v_permlane32_swap_b32 v200, v208\n"
"v_permlane32_swap_b32 v201, v209\n"
"v_permlane32_swap_b32 v202, v210\n"
"v_permlane32_swap_b32 v203, v211\n"
"v_pk_add_f32 v[180:181], v[180:181], v[188:189]\n"
"v_pk_add_f32 v[182:183], v[182:183], v[190:191]\n"
"v_pk_add_f32 v[184:185], v[184:185], v[192:193]\n"
"v_pk_add_f32 v[186:187], v[186:187], v[194:195]\n"
"v_pk_add_f32 v[196:197], v[196:197], v[204:205]\n"
"v_pk_add_f32 v[198:199], v[198:199], v[206:207]\n"
"v_pk_add_f32 v[200:201], v[200:201], v[208:209]\n"
"v_pk_add_f32 v[202:203], v[202:203], v[210:211]\n"
"v_lshlrev_b32 v0, 16, v1\n"
"v_lshlrev_b32 v2, 16, v3\n"
"v_permlane16_swap_b32 v180, v184\n"
"v_permlane16_swap_b32 v181, v185\n"
"v_permlane16_swap_b32 v182, v186\n"
"v_permlane16_swap_b32 v183, v187\n"
"v_permlane16_swap_b32 v196, v200\n"
"v_permlane16_swap_b32 v197, v201\n"
"v_permlane16_swap_b32 v198, v202\n"
"v_permlane16_swap_b32 v199, v203\n"
"v_pk_add_f32 v[180:181], v[180:181], v[184:185]\n"
"v_pk_add_f32 v[182:183], v[182:183], v[186:187]\n"
"v_pk_add_f32 v[196:197], v[196:197], v[200:201]\n"
"v_pk_add_f32 v[198:199], v[198:199], v[202:203]\n"
"v_and_b32 v1, v224, v1\n"
"v_and_b32 v3, v224, v3\n"
"v_add_f32_dpp v180, v180, v180 row_ror:4 row_mask:0xf bank_mask:0xf\n"
"v_add_f32_dpp v181, v181, v181 row_ror:4 row_mask:0xf bank_mask:0xf\n"
"v_add_f32_dpp v182, v182, v182 row_ror:4 row_mask:0xf bank_mask:0xf\n"
"v_add_f32_dpp v183, v183, v183 row_ror:4 row_mask:0xf bank_mask:0xf\n"
"v_add_f32_dpp v196, v196, v196 row_ror:4 row_mask:0xf bank_mask:0xf\n"
"v_add_f32_dpp v197, v197, v197 row_ror:4 row_mask:0xf bank_mask:0xf\n"
"v_add_f32_dpp v198, v198, v198 row_ror:4 row_mask:0xf bank_mask:0xf\n"
"v_add_f32_dpp v199, v199, v199 row_ror:4 row_mask:0xf bank_mask:0xf\n"
"v_add_f32_dpp v180, v180, v180 row_ror:8 row_mask:0xf bank_mask:0xf\n"
"v_add_f32_dpp v181, v181, v181 row_ror:8 row_mask:0xf bank_mask:0xf\n"
"v_add_f32_dpp v182, v182, v182 row_ror:8 row_mask:0xf bank_mask:0xf\n"
"v_add_f32_dpp v183, v183, v183 row_ror:8 row_mask:0xf bank_mask:0xf\n"
"v_add_f32_dpp v196, v196, v196 row_ror:8 row_mask:0xf bank_mask:0xf\n"
"v_add_f32_dpp v197, v197, v197 row_ror:8 row_mask:0xf bank_mask:0xf\n"
"v_add_f32_dpp v198, v198, v198 row_ror:8 row_mask:0xf bank_mask:0xf\n"
"v_add_f32_dpp v199, v199, v199 row_ror:8 row_mask:0xf bank_mask:0xf\n"
"s_mov_b32 exec_lo, 0x00070007\n"
"s_mov_b32 exec_hi, 0x00070007\n"
"ds_write_b128 v244, v[180:183] offset:2048\n"
"ds_write_b128 v244, v[196:199] offset:2240\n"
"s_mov_b64 exec, -1\n"
"v_accvgpr_read_b32 v5, a2\n"
"v_accvgpr_read_b32 v7, a3\n"
"v_lshlrev_b32 v4, 16, v5\n"
"v_lshlrev_b32 v6, 16, v7\n"
"v_and_b32 v5, v224, v5\n"
"v_and_b32 v7, v224, v7\n"
"v_accvgpr_read_b32 v9, a4\n"
"v_accvgpr_read_b32 v11, a5\n"
"v_lshlrev_b32 v8, 16, v9\n"
"v_lshlrev_b32 v10, 16, v11\n"
"v_and_b32 v9, v224, v9\n"
"v_and_b32 v11, v224, v11\n"
"v_accvgpr_read_b32 v13, a6\n"
"v_accvgpr_read_b32 v15, a7\n"
"v_lshlrev_b32 v12, 16, v13\n"
"v_lshlrev_b32 v14, 16, v15\n"
"v_and_b32 v13, v224, v13\n"
"v_and_b32 v15, v224, v15\n"
"v_accvgpr_read_b32 v17, a8\n"
"v_accvgpr_read_b32 v19, a9\n"
"v_lshlrev_b32 v16, 16, v17\n"
"v_lshlrev_b32 v18, 16, v19\n"
"v_and_b32 v17, v224, v17\n"
"v_and_b32 v19, v224, v19\n"
"v_accvgpr_read_b32 v21, a10\n"
"v_accvgpr_read_b32 v23, a11\n"
"v_lshlrev_b32 v20, 16, v21\n"
"v_lshlrev_b32 v22, 16, v23\n"
"v_and_b32 v21, v224, v21\n"
"v_and_b32 v23, v224, v23\n"
"s_waitcnt lgkmcnt(0)\n"
"s_barrier\n"
"s_cmp_eq_u32 s41, 3\n"
"s_cbranch_scc1 AT_F1\n"
"ds_read_b32 v180, v249 offset:2048\n"
"ds_read_b32 v181, v249 offset:2240\n"
"ds_read_b32 v182, v249 offset:2432\n"
"ds_read_b32 v183, v249 offset:2624\n"
"ds_read_b32 v184, v249 offset:2816\n"
"ds_read_b32 v185, v249 offset:3008\n"
"ds_read_b32 v186, v249 offset:3200\n"
"ds_read_b32 v187, v249 offset:3392\n"
"v_accvgpr_read_b32 v25, a12\n"
"v_accvgpr_read_b32 v27, a13\n"
"v_lshlrev_b32 v24, 16, v25\n"
"v_lshlrev_b32 v26, 16, v27\n"
"v_and_b32 v25, v224, v25\n"
"v_and_b32 v27, v224, v27\n"
"v_accvgpr_read_b32 v29, a14\n"
"v_accvgpr_read_b32 v31, a15\n"
"v_lshlrev_b32 v28, 16, v29\n"
"v_lshlrev_b32 v30, 16, v31\n"
"v_and_b32 v29, v224, v29\n"
"v_and_b32 v31, v224, v31\n"
"v_accvgpr_read_b32 v33, a16\n"
"v_accvgpr_read_b32 v35, a17\n"
"v_lshlrev_b32 v32, 16, v33\n"
"v_lshlrev_b32 v34, 16, v35\n"
"v_and_b32 v33, v224, v33\n"
"v_and_b32 v35, v224, v35\n"
"v_accvgpr_read_b32 v37, a18\n"
"v_accvgpr_read_b32 v39, a19\n"
"v_lshlrev_b32 v36, 16, v37\n"
"v_lshlrev_b32 v38, 16, v39\n"
"v_and_b32 v37, v224, v37\n"
"v_and_b32 v39, v224, v39\n"
"s_waitcnt lgkmcnt(5)\n"
"v_add_f32 v180, v180, v182\n"
"s_waitcnt lgkmcnt(1)\n"
"v_add_f32 v184, v184, v186\n"
"v_add_f32 v181, v181, v183\n"
"v_add_f32 v180, v180, v184\n"
"s_waitcnt lgkmcnt(0)\n"
"v_add_f32 v185, v185, v187\n"
"v_mul_f32 v182, 0xbfb8aa3b, v180\n"
"v_add_f32 v181, v181, v185\n"
"v_exp_f32 v182, v182\n"
"v_mul_f32 v180, v180, v181\n"
"v_add_f32 v182, 1.0, v182\n"
"v_rcp_f32 v182, v182\n"
"v_accvgpr_read_b32 v41, a20\n"
"v_mul_f32 v180, v180, v182\n"
"ds_write_b32 v251, v180 offset:4096\n"
"ds_read_b128 v[100:103], v252 offset:4096\n"
"ds_read_b128 v[124:127], v252 offset:4192\n"
"ds_read_b128 v[104:107], v252 offset:4112\n"
"ds_read_b128 v[128:131], v252 offset:4208\n"
"ds_read_b128 v[108:111], v252 offset:4128\n"
"ds_read_b128 v[132:135], v252 offset:4224\n"
"ds_read_b128 v[112:115], v252 offset:4144\n"
"ds_read_b128 v[136:139], v252 offset:4240\n"
"ds_read_b128 v[116:119], v252 offset:4160\n"
"ds_read_b128 v[140:143], v252 offset:4256\n"
"ds_read_b128 v[120:123], v252 offset:4176\n"
"ds_read_b128 v[144:147], v252 offset:4272\n"
"s_lshl_b32 s58, s42, 7\n"
"s_add_u32 s58, s58, 56762368\n"
"s_add_u32 s59, s58, 884736\n"
"v_mov_b32 v161, s85\n"
"v_mov_b32 v163, s85\n"
"v_mov_b32 v165, s85\n"
"v_mov_b32 v167, s85\n"
"v_mov_b32 v169, s85\n"
"v_mov_b32 v171, s85\n"
"v_mov_b32 v173, s85\n"
"v_mov_b32 v175, s85\n"
"v_accvgpr_read_b32 v43, a21\n"
"v_lshlrev_b32 v40, 16, v41\n"
"v_lshlrev_b32 v42, 16, v43\n"
"v_and_b32 v41, v224, v41\n"
"v_and_b32 v43, v224, v43\n"
"v_accvgpr_read_b32 v45, a22\n"
"v_accvgpr_read_b32 v47, a23\n"
"v_lshlrev_b32 v44, 16, v45\n"
"v_lshlrev_b32 v46, 16, v47\n"
"v_and_b32 v45, v224, v45\n"
"v_and_b32 v47, v224, v47\n"
"v_accvgpr_read_b32 v49, a24\n"
"v_accvgpr_read_b32 v51, a25\n"
"v_lshlrev_b32 v48, 16, v49\n"
"v_lshlrev_b32 v50, 16, v51\n"
"v_and_b32 v49, v224, v49\n"
"v_and_b32 v51, v224, v51\n"
"v_accvgpr_read_b32 v53, a26\n"
"v_accvgpr_read_b32 v55, a27\n"
"v_lshlrev_b32 v52, 16, v53\n"
"v_lshlrev_b32 v54, 16, v55\n"
"v_and_b32 v53, v224, v53\n"
"v_and_b32 v55, v224, v55\n"
"v_accvgpr_read_b32 v57, a28\n"
"v_accvgpr_read_b32 v59, a29\n"
"v_lshlrev_b32 v56, 16, v57\n"
"v_lshlrev_b32 v58, 16, v59\n"
"v_and_b32 v57, v224, v57\n"
"v_and_b32 v59, v224, v59\n"
"v_accvgpr_read_b32 v61, a30\n"
"v_accvgpr_read_b32 v63, a31\n"
"v_lshlrev_b32 v60, 16, v61\n"
"v_lshlrev_b32 v62, 16, v63\n"
"v_and_b32 v61, v224, v61\n"
"v_and_b32 v63, v224, v63\n"
"v_accvgpr_read_b32 v65, a32\n"
"v_accvgpr_read_b32 v67, a33\n"
"v_lshlrev_b32 v64, 16, v65\n"
"v_lshlrev_b32 v66, 16, v67\n"
"v_and_b32 v65, v224, v65\n"
"v_and_b32 v67, v224, v67\n"
"v_accvgpr_read_b32 v69, a34\n"
"v_accvgpr_read_b32 v71, a35\n"
"v_lshlrev_b32 v68, 16, v69\n"
"v_lshlrev_b32 v70, 16, v71\n"
"v_and_b32 v69, v224, v69\n"
"v_and_b32 v71, v224, v71\n"
"v_accvgpr_read_b32 v73, a36\n"
"v_accvgpr_read_b32 v75, a37\n"
"v_lshlrev_b32 v72, 16, v73\n"
"v_lshlrev_b32 v74, 16, v75\n"
"v_and_b32 v73, v224, v73\n"
"v_and_b32 v75, v224, v75\n"
"v_accvgpr_read_b32 v77, a38\n"
"v_accvgpr_read_b32 v79, a39\n"
"v_lshlrev_b32 v76, 16, v77\n"
"v_lshlrev_b32 v78, 16, v79\n"
"v_and_b32 v77, v224, v77\n"
"v_and_b32 v79, v224, v79\n"
"v_accvgpr_read_b32 v81, a40\n"
"v_accvgpr_read_b32 v83, a41\n"
"v_lshlrev_b32 v80, 16, v81\n"
"v_lshlrev_b32 v82, 16, v83\n"
"v_and_b32 v81, v224, v81\n"
"v_and_b32 v83, v224, v83\n"
"v_accvgpr_read_b32 v85, a42\n"
"v_accvgpr_read_b32 v87, a43\n"
"v_lshlrev_b32 v84, 16, v85\n"
"v_lshlrev_b32 v86, 16, v87\n"
"v_and_b32 v85, v224, v85\n"
"v_and_b32 v87, v224, v87\n"
"v_accvgpr_read_b32 v89, a44\n"
"v_accvgpr_read_b32 v91, a45\n"
"v_lshlrev_b32 v88, 16, v89\n"
"v_lshlrev_b32 v90, 16, v91\n"
"v_and_b32 v89, v224, v89\n"
"v_and_b32 v91, v224, v91\n"
"v_accvgpr_read_b32 v93, a46\n"
"v_accvgpr_read_b32 v95, a47\n"
"v_lshlrev_b32 v92, 16, v93\n"
"v_lshlrev_b32 v94, 16, v95\n"
"v_and_b32 v93, v224, v93\n"
"v_and_b32 v95, v224, v95\n"
"s_waitcnt lgkmcnt(11)\n"
"v_pk_mul_f32 v[150:151], v[0:1], v[100:101] op_sel:[0,0] op_sel_hi:[1,0]\n"
"v_pk_mul_f32 v[152:153], v[2:3], v[100:101] op_sel:[0,0] op_sel_hi:[1,0]\n"
"s_waitcnt lgkmcnt(10)\n"
"v_pk_mul_f32 v[154:155], v[0:1], v[124:125] op_sel:[0,0] op_sel_hi:[1,0]\n"
"v_pk_mul_f32 v[156:157], v[2:3], v[124:125] op_sel:[0,0] op_sel_hi:[1,0]\n"
"v_pk_fma_f32 v[150:151], v[4:5], v[100:101], v[150:151] op_sel:[0,1,0] op_sel_hi:[1,1,1]\n"
"v_pk_fma_f32 v[152:153], v[6:7], v[100:101], v[152:153] op_sel:[0,1,0] op_sel_hi:[1,1,1]\n"
"v_pk_fma_f32 v[154:155], v[4:5], v[124:125], v[154:155] op_sel:[0,1,0] op_sel_hi:[1,1,1]\n"
"v_pk_fma_f32 v[156:157], v[6:7], v[124:125], v[156:157] op_sel:[0,1,0] op_sel_hi:[1,1,1]\n"
"v_pk_fma_f32 v[150:151], v[8:9], v[102:103], v[150:151] op_sel:[0,0,0] op_sel_hi:[1,0,1]\n"
"v_pk_fma_f32 v[152:153], v[10:11], v[102:103], v[152:153] op_sel:[0,0,0] op_sel_hi:[1,0,1]\n"
"v_pk_fma_f32 v[154:155], v[8:9], v[126:127], v[154:155] op_sel:[0,0,0] op_sel_hi:[1,0,1]\n"
"v_pk_fma_f32 v[156:157], v[10:11], v[126:127], v[156:157] op_sel:[0,0,0] op_sel_hi:[1,0,1]\n"
"v_pk_fma_f32 v[150:151], v[12:13], v[102:103], v[150:151] op_sel:[0,1,0] op_sel_hi:[1,1,1]\n"
"v_pk_fma_f32 v[152:153], v[14:15], v[102:103], v[152:153] op_sel:[0,1,0] op_sel_hi:[1,1,1]\n"
"v_pk_fma_f32 v[154:155], v[12:13], v[126:127], v[154:155] op_sel:[0,1,0] op_sel_hi:[1,1,1]\n"
"v_pk_fma_f32 v[156:157], v[14:15], v[126:127], v[156:157] op_sel:[0,1,0] op_sel_hi:[1,1,1]\n"
"s_waitcnt lgkmcnt(9)\n"
"v_pk_fma_f32 v[150:151], v[16:17], v[104:105], v[150:151] op_sel:[0,0,0] op_sel_hi:[1,0,1]\n"
"v_pk_fma_f32 v[152:153], v[18:19], v[104:105], v[152:153] op_sel:[0,0,0] op_sel_hi:[1,0,1]\n"
"s_waitcnt lgkmcnt(8)\n"
"v_pk_fma_f32 v[154:155], v[16:17], v[128:129], v[154:155] op_sel:[0,0,0] op_sel_hi:[1,0,1]\n"
"v_pk_fma_f32 v[156:157], v[18:19], v[128:129], v[156:157] op_sel:[0,0,0] op_sel_hi:[1,0,1]\n"
"v_pk_fma_f32 v[150:151], v[20:21], v[104:105], v[150:151] op_sel:[0,1,0] op_sel_hi:[1,1,1]\n"
"v_pk_fma_f32 v[152:153], v[22:23], v[104:105], v[152:153] op_sel:[0,1,0] op_sel_hi:[1,1,1]\n"
"v_pk_fma_f32 v[154:155], v[20:21], v[128:129], v[154:155] op_sel:[0,1,0] op_sel_hi:[1,1,1]\n"
"v_pk_fma_f32 v[156:157], v[22:23], v[128:129], v[156:157] op_sel:[0,1,0] op_sel_hi:[1,1,1]\n"
"v_pk_fma_f32 v[150:151], v[24:25], v[106:107], v[150:151] op_sel:[0,0,0] op_sel_hi:[1,0,1]\n"
"v_pk_fma_f32 v[152:153], v[26:27], v[106:107], v[152:153] op_sel:[0,0,0] op_sel_hi:[1,0,1]\n"
"v_pk_fma_f32 v[154:155], v[24:25], v[130:131], v[154:155] op_sel:[0,0,0] op_sel_hi:[1,0,1]\n"
"v_pk_fma_f32 v[156:157], v[26:27], v[130:131], v[156:157] op_sel:[0,0,0] op_sel_hi:[1,0,1]\n"
"v_pk_fma_f32 v[150:151], v[28:29], v[106:107], v[150:151] op_sel:[0,1,0] op_sel_hi:[1,1,1]\n"
"v_pk_fma_f32 v[152:153], v[30:31], v[106:107], v[152:153] op_sel:[0,1,0] op_sel_hi:[1,1,1]\n"
"v_pk_fma_f32 v[154:155], v[28:29], v[130:131], v[154:155] op_sel:[0,1,0] op_sel_hi:[1,1,1]\n"
"v_pk_fma_f32 v[156:157], v[30:31], v[130:131], v[156:157] op_sel:[0,1,0] op_sel_hi:[1,1,1]\n"
"s_waitcnt lgkmcnt(7)\n"
"v_pk_fma_f32 v[150:151], v[32:33], v[108:109], v[150:151] op_sel:[0,0,0] op_sel_hi:[1,0,1]\n"
"v_pk_fma_f32 v[152:153], v[34:35], v[108:109], v[152:153] op_sel:[0,0,0] op_sel_hi:[1,0,1]\n"
"s_waitcnt lgkmcnt(6)\n"
"v_pk_fma_f32 v[154:155], v[32:33], v[132:133], v[154:155] op_sel:[0,0,0] op_sel_hi:[1,0,1]\n"
"v_pk_fma_f32 v[156:157], v[34:35], v[132:133], v[156:157] op_sel:[0,0,0] op_sel_hi:[1,0,1]\n"
"v_pk_fma_f32 v[150:151], v[36:37], v[108:109], v[150:151] op_sel:[0,1,0] op_sel_hi:[1,1,1]\n"
"v_pk_fma_f32 v[152:153], v[38:39], v[108:109], v[152:153] op_sel:[0,1,0] op_sel_hi:[1,1,1]\n"
"v_pk_fma_f32 v[154:155], v[36:37], v[132:133], v[154:155] op_sel:[0,1,0] op_sel_hi:[1,1,1]\n"
"v_pk_fma_f32 v[156:157], v[38:39], v[132:133], v[156:157] op_sel:[0,1,0] op_sel_hi:[1,1,1]\n"
"v_pk_fma_f32 v[150:151], v[40:41], v[110:111], v[150:151] op_sel:[0,0,0] op_sel_hi:[1,0,1]\n"
"v_pk_fma_f32 v[152:153], v[42:43], v[110:111], v[152:153] op_sel:[0,0,0] op_sel_hi:[1,0,1]\n"
"v_pk_fma_f32 v[154:155], v[40:41], v[134:135], v[154:155] op_sel:[0,0,0] op_sel_hi:[1,0,1]\n"
"v_pk_fma_f32 v[156:157], v[42:43], v[134:135], v[156:157] op_sel:[0,0,0] op_sel_hi:[1,0,1]\n"
"v_pk_fma_f32 v[150:151], v[44:45], v[110:111], v[150:151] op_sel:[0,1,0] op_sel_hi:[1,1,1]\n"
"v_pk_fma_f32 v[152:153], v[46:47], v[110:111], v[152:153] op_sel:[0,1,0] op_sel_hi:[1,1,1]\n"
"v_pk_fma_f32 v[154:155], v[44:45], v[134:135], v[154:155] op_sel:[0,1,0] op_sel_hi:[1,1,1]\n"
"v_pk_fma_f32 v[156:157], v[46:47], v[134:135], v[156:157] op_sel:[0,1,0] op_sel_hi:[1,1,1]\n"
"s_waitcnt lgkmcnt(5)\n"
"v_pk_fma_f32 v[150:151], v[48:49], v[112:113], v[150:151] op_sel:[0,0,0] op_sel_hi:[1,0,1]\n"
"v_pk_fma_f32 v[152:153], v[50:51], v[112:113], v[152:153] op_sel:[0,0,0] op_sel_hi:[1,0,1]\n"
"s_waitcnt lgkmcnt(4)\n"
"v_pk_fma_f32 v[154:155], v[48:49], v[136:137], v[154:155] op_sel:[0,0,0] op_sel_hi:[1,0,1]\n"
"v_pk_fma_f32 v[156:157], v[50:51], v[136:137], v[156:157] op_sel:[0,0,0] op_sel_hi:[1,0,1]\n"
"v_pk_fma_f32 v[150:151], v[52:53], v[112:113], v[150:151] op_sel:[0,1,0] op_sel_hi:[1,1,1]\n"
"v_pk_fma_f32 v[152:153], v[54:55], v[112:113], v[152:153] op_sel:[0,1,0] op_sel_hi:[1,1,1]\n"
"v_pk_fma_f32 v[154:155], v[52:53], v[136:137], v[154:155] op_sel:[0,1,0] op_sel_hi:[1,1,1]\n"
"v_pk_fma_f32 v[156:157], v[54:55], v[136:137], v[156:157] op_sel:[0,1,0] op_sel_hi:[1,1,1]\n"
"v_pk_fma_f32 v[150:151], v[56:57], v[114:115], v[150:151] op_sel:[0,0,0] op_sel_hi:[1,0,1]\n"
"v_pk_fma_f32 v[152:153], v[58:59], v[114:115], v[152:153] op_sel:[0,0,0] op_sel_hi:[1,0,1]\n"
"v_pk_fma_f32 v[154:155], v[56:57], v[138:139], v[154:155] op_sel:[0,0,0] op_sel_hi:[1,0,1]\n"
"v_pk_fma_f32 v[156:157], v[58:59], v[138:139], v[156:157] op_sel:[0,0,0] op_sel_hi:[1,0,1]\n"
"v_pk_fma_f32 v[150:151], v[60:61], v[114:115], v[150:151] op_sel:[0,1,0] op_sel_hi:[1,1,1]\n"
"v_pk_fma_f32 v[152:153], v[62:63], v[114:115], v[152:153] op_sel:[0,1,0] op_sel_hi:[1,1,1]\n"
"v_pk_fma_f32 v[154:155], v[60:61], v[138:139], v[154:155] op_sel:[0,1,0] op_sel_hi:[1,1,1]\n"
"v_pk_fma_f32 v[156:157], v[62:63], v[138:139], v[156:157] op_sel:[0,1,0] op_sel_hi:[1,1,1]\n"
"s_waitcnt lgkmcnt(3)\n"
"v_pk_fma_f32 v[150:151], v[64:65], v[116:117], v[150:151] op_sel:[0,0,0] op_sel_hi:[1,0,1]\n"
"v_pk_fma_f32 v[152:153], v[66:67], v[116:117], v[152:153] op_sel:[0,0,0] op_sel_hi:[1,0,1]\n"
"s_waitcnt lgkmcnt(2)\n"
"v_pk_fma_f32 v[154:155], v[64:65], v[140:141], v[154:155] op_sel:[0,0,0] op_sel_hi:[1,0,1]\n"
"v_pk_fma_f32 v[156:157], v[66:67], v[140:141], v[156:157] op_sel:[0,0,0] op_sel_hi:[1,0,1]\n"
"v_pk_fma_f32 v[150:151], v[68:69], v[116:117], v[150:151] op_sel:[0,1,0] op_sel_hi:[1,1,1]\n"
"v_pk_fma_f32 v[152:153], v[70:71], v[116:117], v[152:153] op_sel:[0,1,0] op_sel_hi:[1,1,1]\n"
"v_pk_fma_f32 v[154:155], v[68:69], v[140:141], v[154:155] op_sel:[0,1,0] op_sel_hi:[1,1,1]\n"
"v_pk_fma_f32 v[156:157], v[70:71], v[140:141], v[156:157] op_sel:[0,1,0] op_sel_hi:[1,1,1]\n"
"v_pk_fma_f32 v[150:151], v[72:73], v[118:119], v[150:151] op_sel:[0,0,0] op_sel_hi:[1,0,1]\n"
"v_pk_fma_f32 v[152:153], v[74:75], v[118:119], v[152:153] op_sel:[0,0,0] op_sel_hi:[1,0,1]\n"
"v_pk_fma_f32 v[154:155], v[72:73], v[142:143], v[154:155] op_sel:[0,0,0] op_sel_hi:[1,0,1]\n"
"v_pk_fma_f32 v[156:157], v[74:75], v[142:143], v[156:157] op_sel:[0,0,0] op_sel_hi:[1,0,1]\n"
"v_pk_fma_f32 v[150:151], v[76:77], v[118:119], v[150:151] op_sel:[0,1,0] op_sel_hi:[1,1,1]\n"
"v_pk_fma_f32 v[152:153], v[78:79], v[118:119], v[152:153] op_sel:[0,1,0] op_sel_hi:[1,1,1]\n"
"v_pk_fma_f32 v[154:155], v[76:77], v[142:143], v[154:155] op_sel:[0,1,0] op_sel_hi:[1,1,1]\n"
"v_pk_fma_f32 v[156:157], v[78:79], v[142:143], v[156:157] op_sel:[0,1,0] op_sel_hi:[1,1,1]\n"
"s_waitcnt lgkmcnt(1)\n"
"v_pk_fma_f32 v[150:151], v[80:81], v[120:121], v[150:151] op_sel:[0,0,0] op_sel_hi:[1,0,1]\n"
"v_pk_fma_f32 v[152:153], v[82:83], v[120:121], v[152:153] op_sel:[0,0,0] op_sel_hi:[1,0,1]\n"
"s_waitcnt lgkmcnt(0)\n"
"v_pk_fma_f32 v[154:155], v[80:81], v[144:145], v[154:155] op_sel:[0,0,0] op_sel_hi:[1,0,1]\n"
"v_pk_fma_f32 v[156:157], v[82:83], v[144:145], v[156:157] op_sel:[0,0,0] op_sel_hi:[1,0,1]\n"
"v_pk_fma_f32 v[150:151], v[84:85], v[120:121], v[150:151] op_sel:[0,1,0] op_sel_hi:[1,1,1]\n"
"v_pk_fma_f32 v[152:153], v[86:87], v[120:121], v[152:153] op_sel:[0,1,0] op_sel_hi:[1,1,1]\n"
"v_pk_fma_f32 v[154:155], v[84:85], v[144:145], v[154:155] op_sel:[0,1,0] op_sel_hi:[1,1,1]\n"
"v_pk_fma_f32 v[156:157], v[86:87], v[144:145], v[156:157] op_sel:[0,1,0] op_sel_hi:[1,1,1]\n"
"v_pk_fma_f32 v[150:151], v[88:89], v[122:123], v[150:151] op_sel:[0,0,0] op_sel_hi:[1,0,1]\n"
"v_pk_fma_f32 v[152:153], v[90:91], v[122:123], v[152:153] op_sel:[0,0,0] op_sel_hi:[1,0,1]\n"
"v_pk_fma_f32 v[154:155], v[88:89], v[146:147], v[154:155] op_sel:[0,0,0] op_sel_hi:[1,0,1]\n"
"v_pk_fma_f32 v[156:157], v[90:91], v[146:147], v[156:157] op_sel:[0,0,0] op_sel_hi:[1,0,1]\n"
"v_pk_fma_f32 v[150:151], v[92:93], v[122:123], v[150:151] op_sel:[0,1,0] op_sel_hi:[1,1,1]\n"
"v_pk_fma_f32 v[152:153], v[94:95], v[122:123], v[152:153] op_sel:[0,1,0] op_sel_hi:[1,1,1]\n"
"v_pk_fma_f32 v[154:155], v[92:93], v[146:147], v[154:155] op_sel:[0,1,0] op_sel_hi:[1,1,1]\n"
"v_pk_fma_f32 v[156:157], v[94:95], v[146:147], v[156:157] op_sel:[0,1,0] op_sel_hi:[1,1,1]\n"
"v_mov_b32 v160, v150\n"
"v_mov_b32 v162, v151\n"
"v_mov_b32 v164, v152\n"
"v_mov_b32 v166, v153\n"
"v_mov_b32 v168, v154\n"
"v_mov_b32 v170, v155\n"
"v_mov_b32 v172, v156\n"
"v_mov_b32 v174, v157\n"
"buffer_store_dwordx4 v[160:163], v243, s[28:31], s58 offen offset:0 sc1\n"
"buffer_store_dwordx4 v[164:167], v243, s[28:31], s58 offen offset:16 sc1\n"
"buffer_store_dwordx4 v[168:171], v243, s[28:31], s59 offen offset:0 sc1\n"
"buffer_store_dwordx4 v[172:175], v243, s[28:31], s59 offen offset:16 sc1\n"
"s_branch AT_F1\n"
"AT_F0:\n"
"s_sleep 30\n"
"AT_F1:\n"
"s_cmp_lt_u32 s42, 184\n"
"s_cbranch_scc1 AT_end\n"
"s_sub_u32 s56, s42, 184\n"
"s_cmp_ge_u32 s56, 36\n"
"s_cselect_b32 s57, 36, 0\n"
"s_cselect_b32 s59, s13, s12\n"
"s_cselect_b32 s60, s11, s10\n"
"s_sub_u32 s58, s56, s57\n"
"s_lshl_b32 s58, s58, 4\n"
"s_mul_i32 s56, s56, 24576\n"
"s_add_u32 s56, s56, 56762368\n"
"s_mul_i32 s61, s59, 1152\n"
"s_add_u32 s61, s61, s58\n"
"s_lshl_b32 s61, s61, 3\n"
"s_add_u32 s61, s61, 37892608\n"
"v_lshlrev_b32 v30, 4, v253\n"
"v_add_u32 v31, 4096, v30\n"
"v_add_u32 v32, 8192, v30\n"
"v_add_u32 v33, 12288, v30\n"
"v_add_u32 v34, 16384, v30\n"
"v_add_u32 v35, 20480, v30\n"
"v_and_b32 v36, 15, v248\n"
"v_lshlrev_b32 v37, 3, v36\n"
"s_lshl_b32 s62, s58, 2\n"
"s_add_u32 s62, s62, s40\n"
"s_add_u32 s62, s62, 158720\n"
"v_lshl_add_u32 v38, v36, 2, s62\n"
"ds_read_b32 v39, v38\n"
"s_mov_b32 s87, 0x80000\n"
"AT_F2:\n"
"buffer_load_dwordx4 v[0:3], v30, s[28:31], s56 offen sc1\n"
"buffer_load_dwordx4 v[4:7], v31, s[28:31], s56 offen sc1\n"
"buffer_load_dwordx4 v[8:11], v32, s[28:31], s56 offen sc1\n"
"buffer_load_dwordx4 v[12:15], v33, s[28:31], s56 offen sc1\n"
"buffer_load_dwordx4 v[16:19], v34, s[28:31], s56 offen sc1\n"
"buffer_load_dwordx4 v[20:23], v35, s[28:31], s56 offen sc1\n"
"buffer_load_dwordx2 v[24:25], v37, s[28:31], s61 offen sc1\n"
"s_and_b32 s62, s60, s14\n"
"s_ashr_i32 s63, s62, 31\n"
"s_not_b32 s63, s63\n"
"s_and_b32 s63, s63, s14\n"
"s_or_b32 s63, s63, 0x80000000\n"
"s_xor_b32 s62, s62, s63\n"
"v_mov_b32 v40, s62\n"
"v_mul_f32 v40, 0xbfb8aa3b, v40\n"
"v_exp_f32 v40, v40\n"
"s_nop 0\n"
"v_add_f32 v40, 1.0, v40\n"
"v_rcp_f32 v40, v40\n"
"s_waitcnt vmcnt(0) lgkmcnt(0)\n"
"v_xor_b32 v26, s85, v1\n"
"v_xor_b32 v27, s85, v3\n"
"v_bitop3_b32 v26, v5, s85, v26 bitop3:0xbe\n"
"v_bitop3_b32 v27, v7, s85, v27 bitop3:0xbe\n"
"v_bitop3_b32 v26, v9, s85, v26 bitop3:0xbe\n"
"v_bitop3_b32 v27, v11, s85, v27 bitop3:0xbe\n"
"v_bitop3_b32 v26, v13, s85, v26 bitop3:0xbe\n"
"v_bitop3_b32 v27, v15, s85, v27 bitop3:0xbe\n"
"v_bitop3_b32 v26, v17, s85, v26 bitop3:0xbe\n"
"v_bitop3_b32 v27, v19, s85, v27 bitop3:0xbe\n"
"v_bitop3_b32 v26, v21, s85, v26 bitop3:0xbe\n"
"v_bitop3_b32 v27, v23, s85, v27 bitop3:0xbe\n"
"v_bitop3_b32 v26, v25, s85, v26 bitop3:0xbe\n"
"v_or_b32 v26, v26, v27\n"
"v_cmp_ne_u32 vcc, 0, v26\n"
"s_nop 0\n"
"s_cbranch_vccz AT_F3\n"
"s_sub_u32 s87, s87, 1\n"
"s_cmp_eq_u32 s87, 0\n"
"s_cbranch_scc1 AT_F3\n"
"s_sleep 2\n"
"s_branch AT_F2\n"
"AT_F3:\n"
"v_add_f32 v0, v0, v4\n"
"v_add_f32 v2, v2, v6\n"
"v_add_f32 v8, v8, v12\n"
"v_add_f32 v10, v10, v14\n"
"v_add_f32 v16, v16, v20\n"
"v_add_f32 v18, v18, v22\n"
"v_add_f32 v0, v0, v8\n"
"v_add_f32 v2, v2, v10\n"
"v_add_f32 v0, v0, v16\n"
"v_add_f32 v1, v2, v18\n"
"v_mul_f32 v40, v40, v39\n"
"s_lshl_b32 s62, s41, 6\n"
"v_add3_u32 v28, v37, v227, s62\n"
"v_add_f32_dpp v0, v0, v0 row_ror:8 row_mask:0xf bank_mask:0xf\n"
"v_add_f32_dpp v1, v1, v1 row_ror:8 row_mask:0xf bank_mask:0xf\n"
"s_nop 0\n"
"v_mov_b32 v2, v0\n"
"v_mov_b32 v3, v1\n"
"s_nop 0\n"
"v_permlane16_swap_b32 v0, v2\n"
"v_permlane16_swap_b32 v1, v3\n"
"v_add_f32 v0, v0, v2\n"
"v_add_f32 v1, v1, v3\n"
"v_mov_b32 v2, v0\n"
"v_mov_b32 v3, v1\n"
"s_nop 0\n"
"v_permlane32_swap_b32 v0, v2\n"
"v_permlane32_swap_b32 v1, v3\n"
"v_add_f32 v0, v0, v2\n"
"v_add_f32 v1, v1, v3\n"
"s_mov_b64 exec, 0xff\n"
"ds_write_b64 v28, v[0:1] offset:1024\n"
"s_mov_b64 exec, -1\n"
"v_lshl_add_u32 v28, v36, 2, v227\n"
"s_waitcnt lgkmcnt(0)\n"
"s_barrier\n"
"s_cmp_lg_u32 s41, 0\n"
"s_cbranch_scc1 AT_end\n"
"ds_read_b32 v0, v28 offset:1024\n"
"ds_read_b32 v1, v28 offset:1088\n"
"ds_read_b32 v2, v28 offset:1152\n"
"ds_read_b32 v3, v28 offset:1216\n"
"s_mul_i32 s61, s59, 576\n"
"s_add_u32 s61, s61, s58\n"
"s_lshl_b32 s61, s61, 1\n"
"v_lshlrev_b32 v5, 1, v36\n"
"s_waitcnt lgkmcnt(0)\n"
"v_add_f32 v0, v0, v1\n"
"v_add_f32 v2, v2, v3\n"
"v_add_f32 v0, v0, v2\n"
"v_fma_f32 v0, v0, v40, v24\n"
"v_cvt_pk_bf16_f32 v0, v0, v0\n"
"s_mov_b64 exec, 0xffff\n"
"buffer_store_short v0, v5, s[76:79], s61 offen\n"
"s_branch AT_end\n"
"AT_fb:\n"
"s_waitcnt vmcnt(0) lgkmcnt(0)\n"
"s_barrier\n"
"s_mov_b32 s45, 1\n"
"s_mov_b32 s87, 32\n"
"s_lshl_b32 s84, s41, 12\n"
"s_lshl_b32 s56, s43, 19\n"
"s_add_u32 s84, s84, s56\n"
"v_mov_b32 v160, 0xff800000\n"
"v_mov_b32 v161, 0xff800000\n"
"v_mov_b32 v162, 0xff800000\n"
"v_mov_b32 v163, 0xff800000\n"
"AT_fbl:\n"
"s_mov_b32 m0, s86\n"
"s_nop 0\n"
"buffer_load_dwordx4 v246, s[8:11], s84 offen offset:0 lds\n"
"buffer_load_dwordx4 v246, s[8:11], s84 offen offset:1024 lds\n"
"buffer_load_dwordx4 v247, s[8:11], s84 offen offset:2048 lds\n"
"buffer_load_dwordx4 v247, s[8:11], s84 offen offset:3072 lds\n"
"s_add_u32 s84, s84, 16384\n"
"s_waitcnt vmcnt(0)\n"
"s_barrier\n"
"ds_read_b128 a[128:131], v240 offset:0\n"
"ds_read_b128 a[132:135], v240 offset:256\n"
"ds_read_b128 a[136:139], v240 offset:512\n"
"ds_read_b128 a[140:143], v240 offset:768\n"
"ds_read_b128 a[144:147], v240 offset:4096\n"
"ds_read_b128 a[148:151], v240 offset:4352\n"
"ds_read_b128 a[152:155], v240 offset:4608\n"
"ds_read_b128 a[156:159], v240 offset:4864\n"
"s_waitcnt lgkmcnt(0)\n"
"v_mfma_f32_16x16x32_bf16 v[128:131], a[128:131], v[64:67], 0\n"
"v_mfma_f32_16x16x32_bf16 v[128:131], a[132:135], v[68:71], v[128:131]\n"
"v_mfma_f32_16x16x32_bf16 v[128:131], a[136:139], v[72:75], v[128:131]\n"
"v_mfma_f32_16x16x32_bf16 v[128:131], a[140:143], v[76:79], v[128:131]\n"
"v_mfma_f32_16x16x32_bf16 v[132:135], a[144:147], v[64:67], 0\n"
"v_mfma_f32_16x16x32_bf16 v[132:135], a[148:151], v[68:71], v[132:135]\n"
"v_mfma_f32_16x16x32_bf16 v[132:135], a[152:155], v[72:75], v[132:135]\n"
"v_mfma_f32_16x16x32_bf16 v[132:135], a[156:159], v[76:79], v[132:135]\n"
"v_mfma_f32_16x16x32_bf16 v[136:139], a[128:131], v[80:83], 0\n"
"v_mfma_f32_16x16x32_bf16 v[136:139], a[132:135], v[84:87], v[136:139]\n"
"v_mfma_f32_16x16x32_bf16 v[136:139], a[136:139], v[88:91], v[136:139]\n"
"v_mfma_f32_16x16x32_bf16 v[136:139], a[140:143], v[92:95], v[136:139]\n"
"v_mfma_f32_16x16x32_bf16 v[140:143], a[144:147], v[80:83], 0\n"
"v_mfma_f32_16x16x32_bf16 v[140:143], a[148:151], v[84:87], v[140:143]\n"
"v_mfma_f32_16x16x32_bf16 v[140:143], a[152:155], v[88:91], v[140:143]\n"
"v_mfma_f32_16x16x32_bf16 v[140:143], a[156:159], v[92:95], v[140:143]\n"
"v_mfma_f32_16x16x32_bf16 v[144:147], a[128:131], v[96:99], 0\n"
"v_mfma_f32_16x16x32_bf16 v[144:147], a[132:135], v[100:103], v[144:147]\n"
"v_mfma_f32_16x16x32_bf16 v[144:147], a[136:139], v[104:107], v[144:147]\n"
"v_mfma_f32_16x16x32_bf16 v[144:147], a[140:143], v[108:111], v[144:147]\n"
"v_mfma_f32_16x16x32_bf16 v[148:151], a[144:147], v[96:99], 0\n"
"v_mfma_f32_16x16x32_bf16 v[148:151], a[148:151], v[100:103], v[148:151]\n"
"v_mfma_f32_16x16x32_bf16 v[148:151], a[152:155], v[104:107], v[148:151]\n"
"v_mfma_f32_16x16x32_bf16 v[148:151], a[156:159], v[108:111], v[148:151]\n"
"v_mfma_f32_16x16x32_bf16 v[152:155], a[128:131], v[112:115], 0\n"
"v_mfma_f32_16x16x32_bf16 v[152:155], a[132:135], v[116:119], v[152:155]\n"
"v_mfma_f32_16x16x32_bf16 v[152:155], a[136:139], v[120:123], v[152:155]\n"
"v_mfma_f32_16x16x32_bf16 v[152:155], a[140:143], v[124:127], v[152:155]\n"
"v_mfma_f32_16x16x32_bf16 v[156:159], a[144:147], v[112:115], 0\n"
"v_mfma_f32_16x16x32_bf16 v[156:159], a[148:151], v[116:119], v[156:159]\n"
"v_mfma_f32_16x16x32_bf16 v[156:159], a[152:155], v[120:123], v[156:159]\n"
"v_mfma_f32_16x16x32_bf16 v[156:159], a[156:159], v[124:127], v[156:159]\n"
"s_nop 7\n"
"v_max3_f32 v160, v160, v128, v129\n"
"v_max3_f32 v160, v160, v130, v131\n"
"v_max3_f32 v160, v160, v132, v133\n"
"v_max3_f32 v160, v160, v134, v135\n"
"v_max3_f32 v161, v161, v136, v137\n"
"v_max3_f32 v161, v161, v138, v139\n"
"v_max3_f32 v161, v161, v140, v141\n"
"v_max3_f32 v161, v161, v142, v143\n"
"v_max3_f32 v162, v162, v144, v145\n"
"v_max3_f32 v162, v162, v146, v147\n"
"v_max3_f32 v162, v162, v148, v149\n"
"v_max3_f32 v162, v162, v150, v151\n"
"v_max3_f32 v163, v163, v152, v153\n"
"v_max3_f32 v163, v163, v154, v155\n"
"v_max3_f32 v163, v163, v156, v157\n"
"v_max3_f32 v163, v163, v158, v159\n"
"ds_read_b128 a[128:131], v240 offset:8192\n"
"ds_read_b128 a[132:135], v240 offset:8448\n"
"ds_read_b128 a[136:139], v240 offset:8704\n"
"ds_read_b128 a[140:143], v240 offset:8960\n"
"ds_read_b128 a[144:147], v240 offset:12288\n"
"ds_read_b128 a[148:151], v240 offset:12544\n"
"ds_read_b128 a[152:155], v240 offset:12800\n"
"ds_read_b128 a[156:159], v240 offset:13056\n"
"s_waitcnt lgkmcnt(0)\n"
"v_mfma_f32_16x16x32_bf16 v[128:131], a[128:131], v[64:67], 0\n"
"v_mfma_f32_16x16x32_bf16 v[128:131], a[132:135], v[68:71], v[128:131]\n"
"v_mfma_f32_16x16x32_bf16 v[128:131], a[136:139], v[72:75], v[128:131]\n"
"v_mfma_f32_16x16x32_bf16 v[128:131], a[140:143], v[76:79], v[128:131]\n"
"v_mfma_f32_16x16x32_bf16 v[132:135], a[144:147], v[64:67], 0\n"
"v_mfma_f32_16x16x32_bf16 v[132:135], a[148:151], v[68:71], v[132:135]\n"
"v_mfma_f32_16x16x32_bf16 v[132:135], a[152:155], v[72:75], v[132:135]\n"
"v_mfma_f32_16x16x32_bf16 v[132:135], a[156:159], v[76:79], v[132:135]\n"
"v_mfma_f32_16x16x32_bf16 v[136:139], a[128:131], v[80:83], 0\n"
"v_mfma_f32_16x16x32_bf16 v[136:139], a[132:135], v[84:87], v[136:139]\n"
"v_mfma_f32_16x16x32_bf16 v[136:139], a[136:139], v[88:91], v[136:139]\n"
"v_mfma_f32_16x16x32_bf16 v[136:139], a[140:143], v[92:95], v[136:139]\n"
"v_mfma_f32_16x16x32_bf16 v[140:143], a[144:147], v[80:83], 0\n"
"v_mfma_f32_16x16x32_bf16 v[140:143], a[148:151], v[84:87], v[140:143]\n"
"v_mfma_f32_16x16x32_bf16 v[140:143], a[152:155], v[88:91], v[140:143]\n"
"v_mfma_f32_16x16x32_bf16 v[140:143], a[156:159], v[92:95], v[140:143]\n"
"v_mfma_f32_16x16x32_bf16 v[144:147], a[128:131], v[96:99], 0\n"
"v_mfma_f32_16x16x32_bf16 v[144:147], a[132:135], v[100:103], v[144:147]\n"
"v_mfma_f32_16x16x32_bf16 v[144:147], a[136:139], v[104:107], v[144:147]\n"
"v_mfma_f32_16x16x32_bf16 v[144:147], a[140:143], v[108:111], v[144:147]\n"
"v_mfma_f32_16x16x32_bf16 v[148:151], a[144:147], v[96:99], 0\n"
"v_mfma_f32_16x16x32_bf16 v[148:151], a[148:151], v[100:103], v[148:151]\n"
"v_mfma_f32_16x16x32_bf16 v[148:151], a[152:155], v[104:107], v[148:151]\n"
"v_mfma_f32_16x16x32_bf16 v[148:151], a[156:159], v[108:111], v[148:151]\n"
"v_mfma_f32_16x16x32_bf16 v[152:155], a[128:131], v[112:115], 0\n"
"v_mfma_f32_16x16x32_bf16 v[152:155], a[132:135], v[116:119], v[152:155]\n"
"v_mfma_f32_16x16x32_bf16 v[152:155], a[136:139], v[120:123], v[152:155]\n"
"v_mfma_f32_16x16x32_bf16 v[152:155], a[140:143], v[124:127], v[152:155]\n"
"v_mfma_f32_16x16x32_bf16 v[156:159], a[144:147], v[112:115], 0\n"
"v_mfma_f32_16x16x32_bf16 v[156:159], a[148:151], v[116:119], v[156:159]\n"
"v_mfma_f32_16x16x32_bf16 v[156:159], a[152:155], v[120:123], v[156:159]\n"
"v_mfma_f32_16x16x32_bf16 v[156:159], a[156:159], v[124:127], v[156:159]\n"
"s_nop 7\n"
"v_max3_f32 v160, v160, v128, v129\n"
"v_max3_f32 v160, v160, v130, v131\n"
"v_max3_f32 v160, v160, v132, v133\n"
"v_max3_f32 v160, v160, v134, v135\n"
"v_max3_f32 v161, v161, v136, v137\n"
"v_max3_f32 v161, v161, v138, v139\n"
"v_max3_f32 v161, v161, v140, v141\n"
"v_max3_f32 v161, v161, v142, v143\n"
"v_max3_f32 v162, v162, v144, v145\n"
"v_max3_f32 v162, v162, v146, v147\n"
"v_max3_f32 v162, v162, v148, v149\n"
"v_max3_f32 v162, v162, v150, v151\n"
"v_max3_f32 v163, v163, v152, v153\n"
"v_max3_f32 v163, v163, v154, v155\n"
"v_max3_f32 v163, v163, v156, v157\n"
"v_max3_f32 v163, v163, v158, v159\n"
"s_barrier\n"
"s_sub_u32 s87, s87, 1\n"
"s_cmp_lg_u32 s87, 0\n"
"s_cbranch_scc1 AT_fbl\n"
"v_mov_b32 v164, v160\n"
"s_nop 1\n"
"v_permlane32_swap_b32 v160, v164\n"
"v_max_f32 v160, v160, v164\n"
"v_mov_b32 v164, v160\n"
"s_nop 1\n"
"v_permlane16_swap_b32 v160, v164\n"
"v_max_f32 v160, v160, v164\n"
"v_sub_f32 v24, 0, v160\n"
"v_sub_f32 v25, 0, v160\n"
"v_sub_f32 v26, 0, v160\n"
"v_sub_f32 v27, 0, v160\n"
"v_mov_b32 v164, v161\n"
"s_nop 1\n"
"v_permlane32_swap_b32 v161, v164\n"
"v_max_f32 v161, v161, v164\n"
"v_mov_b32 v164, v161\n"
"s_nop 1\n"
"v_permlane16_swap_b32 v161, v164\n"
"v_max_f32 v161, v161, v164\n"
"v_sub_f32 v28, 0, v161\n"
"v_sub_f32 v29, 0, v161\n"
"v_sub_f32 v30, 0, v161\n"
"v_sub_f32 v31, 0, v161\n"
"v_mov_b32 v164, v162\n"
"s_nop 1\n"
"v_permlane32_swap_b32 v162, v164\n"
"v_max_f32 v162, v162, v164\n"
"v_mov_b32 v164, v162\n"
"s_nop 1\n"
"v_permlane16_swap_b32 v162, v164\n"
"v_max_f32 v162, v162, v164\n"
"v_sub_f32 v32, 0, v162\n"
"v_sub_f32 v33, 0, v162\n"
"v_sub_f32 v34, 0, v162\n"
"v_sub_f32 v35, 0, v162\n"
"v_mov_b32 v164, v163\n"
"s_nop 1\n"
"v_permlane32_swap_b32 v163, v164\n"
"v_max_f32 v163, v163, v164\n"
"v_mov_b32 v164, v163\n"
"s_nop 1\n"
"v_permlane16_swap_b32 v163, v164\n"
"v_max_f32 v163, v163, v164\n"
"v_sub_f32 v36, 0, v163\n"
"v_sub_f32 v37, 0, v163\n"
"v_sub_f32 v38, 0, v163\n"
"v_sub_f32 v39, 0, v163\n"
"v_mov_b32 v40, 0x3f803f80\n"
"v_mov_b32 v41, 0x3f803f80\n"
"v_mov_b32 v42, 0x3f803f80\n"
"v_mov_b32 v43, 0x3f803f80\n"
"v_mov_b32 v20, 0\n"
"v_mov_b32 v21, 0\n"
"v_mov_b32 v22, 0\n"
"v_mov_b32 v23, 0\n"
"v_mov_b32 v44, 0\n"
"v_mov_b32 v52, 0\n"
"s_lshl_b32 s84, s41, 12\n"
"s_lshl_b32 s56, s43, 19\n"
"s_add_u32 s84, s84, s56\n"
"v_mov_b32 v251, 0x40000000\n"
"v_mov_b32 v252, 0x40000000\n"
"v_mov_b32 v253, 0x40000000\n"
"v_add_f32 v148, 0xc2480000, v161\n"
"v_add_f32 v149, 0xc2480000, v161\n"
"v_add_f32 v150, 0xc2480000, v161\n"
"v_add_f32 v151, 0xc2480000, v161\n"
"v_add_f32 v156, 0xc2480000, v163\n"
"v_add_f32 v157, 0xc2480000, v163\n"
"v_add_f32 v158, 0xc2480000, v163\n"
"v_add_f32 v159, 0xc2480000, v163\n"
"s_mov_b32 m0, s86\n"
"s_nop 0\n"
"buffer_load_dwordx4 v246, s[8:11], s84 offen offset:0 lds\n"
"buffer_load_dwordx4 v246, s[8:11], s84 offen offset:1024 lds\n"
"buffer_load_dwordx4 v247, s[8:11], s84 offen offset:2048 lds\n"
"buffer_load_dwordx4 v247, s[8:11], s84 offen offset:3072 lds\n"
"s_add_u32 m0, s86, 16384\n"
"s_nop 0\n"
"buffer_load_dwordx4 v246, s[12:15], s84 offen offset:0 lds\n"
"buffer_load_dwordx4 v247, s[12:15], s84 offen offset:1024 lds\n"
"buffer_load_dwordx4 v246, s[12:15], s84 offen offset:2048 lds\n"
"buffer_load_dwordx4 v247, s[12:15], s84 offen offset:3072 lds\n"
"s_add_u32 s84, s84, 16384\n"
"s_add_u32 m0, s86, 32768\n"
"s_nop 0\n"
"buffer_load_dwordx4 v246, s[8:11], s84 offen offset:0 lds\n"
"buffer_load_dwordx4 v246, s[8:11], s84 offen offset:1024 lds\n"
"buffer_load_dwordx4 v247, s[8:11], s84 offen offset:2048 lds\n"
"buffer_load_dwordx4 v247, s[8:11], s84 offen offset:3072 lds\n"
"s_add_u32 m0, s86, 49152\n"
"s_nop 0\n"
"buffer_load_dwordx4 v246, s[12:15], s84 offen offset:0 lds\n"
"buffer_load_dwordx4 v247, s[12:15], s84 offen offset:1024 lds\n"
"buffer_load_dwordx4 v246, s[12:15], s84 offen offset:2048 lds\n"
"buffer_load_dwordx4 v247, s[12:15], s84 offen offset:3072 lds\n"
"s_add_u32 s84, s84, 16384\n"
"s_add_u32 m0, s86, 65536\n"
"s_nop 0\n"
"buffer_load_dwordx4 v246, s[8:11], s84 offen offset:0 lds\n"
"buffer_load_dwordx4 v246, s[8:11], s84 offen offset:1024 lds\n"
"buffer_load_dwordx4 v247, s[8:11], s84 offen offset:2048 lds\n"
"buffer_load_dwordx4 v247, s[8:11], s84 offen offset:3072 lds\n"
"s_add_u32 m0, s86, 81920\n"
"s_nop 0\n"
"buffer_load_dwordx4 v246, s[12:15], s84 offen offset:0 lds\n"
"buffer_load_dwordx4 v247, s[12:15], s84 offen offset:1024 lds\n"
"buffer_load_dwordx4 v246, s[12:15], s84 offen offset:2048 lds\n"
"buffer_load_dwordx4 v247, s[12:15], s84 offen offset:3072 lds\n"
"s_add_u32 s84, s84, 16384\n"
"s_add_u32 m0, s86, 98304\n"
"s_nop 0\n"
"buffer_load_dwordx4 v246, s[8:11], s84 offen offset:0 lds\n"
"buffer_load_dwordx4 v246, s[8:11], s84 offen offset:1024 lds\n"
"buffer_load_dwordx4 v247, s[8:11], s84 offen offset:2048 lds\n"
"buffer_load_dwordx4 v247, s[8:11], s84 offen offset:3072 lds\n"
"s_add_u32 m0, s86, 114688\n"
"s_nop 0\n"
"buffer_load_dwordx4 v246, s[12:15], s84 offen offset:0 lds\n"
"buffer_load_dwordx4 v247, s[12:15], s84 offen offset:1024 lds\n"
"buffer_load_dwordx4 v246, s[12:15], s84 offen offset:2048 lds\n"
"buffer_load_dwordx4 v247, s[12:15], s84 offen offset:3072 lds\n"
"s_add_u32 s84, s84, 16384\n"
"s_waitcnt vmcnt(24)\n"
"s_barrier\n"
"ds_read_b128 a[128:131], v240 offset:0\n"
"ds_read_b128 a[132:135], v240 offset:256\n"
"ds_read_b128 a[136:139], v240 offset:512\n"
"ds_read_b128 a[140:143], v240 offset:768\n"
"ds_read_b128 a[144:147], v240 offset:4096\n"
"ds_read_b128 a[148:151], v240 offset:4352\n"
"ds_read_b128 a[152:155], v240 offset:4608\n"
"ds_read_b128 a[156:159], v240 offset:4864\n"
"ds_read_b64_tr_b16 a[192:193], v242 offset:16384\n"
"ds_read_b64_tr_b16 a[194:195], v242 offset:20480\n"
"ds_read_b64_tr_b16 a[196:197], v243 offset:16384\n"
"ds_read_b64_tr_b16 a[198:199], v243 offset:20480\n"
"ds_read_b64_tr_b16 a[200:201], v242 offset:16640\n"
"ds_read_b64_tr_b16 a[202:203], v242 offset:20736\n"
"ds_read_b64_tr_b16 a[204:205], v243 offset:16640\n"
"ds_read_b64_tr_b16 a[206:207], v243 offset:20736\n"
"ds_read_b64_tr_b16 a[208:209], v242 offset:16896\n"
"ds_read_b64_tr_b16 a[210:211], v242 offset:20992\n"
"ds_read_b64_tr_b16 a[212:213], v243 offset:16896\n"
"ds_read_b64_tr_b16 a[214:215], v243 offset:20992\n"
"ds_read_b64_tr_b16 a[216:217], v242 offset:17152\n"
"ds_read_b64_tr_b16 a[218:219], v242 offset:21248\n"
"ds_read_b64_tr_b16 a[220:221], v243 offset:17152\n"
"ds_read_b64_tr_b16 a[222:223], v243 offset:21248\n"
"s_waitcnt lgkmcnt(0)\n"
"s_branch AT_main\n"
"AT_end:\n"
"AT_kend:\n"
  :: [kp] "s"(__builtin_amdgcn_kernarg_segment_ptr()), [blk] "s"(blockIdx.x), [wid] "s"(wid), [lds] "s"(ldsb) : "v0", "v1", "v2", "v3", "v4", "v5", "v6", "v7", "v8", "v9", "v10", "v11", "v12", "v13", "v14", "v15", "v16", "v17", "v18", "v19", "v20", "v21", "v22", "v23", "v24", "v25", "v26", "v27", "v28", "v29", "v30", "v31", "v32", "v33", "v34", "v35", "v36", "v37", "v38", "v39", "v40", "v41", "v42", "v43", "v44", "v45", "v46", "v47", "v48", "v49", "v50", "v51", "v52", "v53", "v54", "v55", "v56", "v57", "v58", "v59", "v60", "v61", "v62", "v63", "v64", "v65", "v66", "v67", "v68", "v69", "v70", "v71", "v72", "v73", "v74", "v75", "v76", "v77", "v78", "v79", "v80", "v81", "v82", "v83", "v84", "v85", "v86", "v87", "v88", "v89", "v90", "v91", "v92", "v93", "v94", "v95", "v96", "v97", "v98", "v99", "v100", "v101", "v102", "v103", "v104", "v105", "v106", "v107", "v108", "v109", "v110", "v111", "v112", "v113", "v114", "v115", "v116", "v117", "v118", "v119", "v120", "v121", "v122", "v123", "v124", "v125", "v126", "v127", "v128", "v129", "v130", "v131", "v132", "v133", "v134", "v135", "v136", "v137", "v138", "v139", "v140", "v141", "v142", "v143", "v144", "v145", "v146", "v147", "v148", "v149", "v150", "v151", "v152", "v153", "v154", "v155", "v156", "v157", "v158", "v159", "v160", "v161", "v162", "v163", "v164", "v165", "v166", "v167", "v168", "v169", "v170", "v171", "v172", "v173", "v174", "v175", "v176", "v177", "v178", "v179", "v180", "v181", "v182", "v183", "v184", "v185", "v186", "v187", "v188", "v189", "v190", "v191", "v192", "v193", "v194", "v195", "v196", "v197", "v198", "v199", "v200", "v201", "v202", "v203", "v204", "v205", "v206", "v207", "v208", "v209", "v210", "v211", "v212", "v213", "v214", "v215", "v216", "v217", "v218", "v219", "v220", "v221", "v222", "v223", "v224", "v225", "v226", "v227", "v228", "v229", "v230", "v231", "v232", "v233", "v234", "v235", "v236", "v237", "v238", "v239", "v240", "v241", "v242", "v243", "v244", "v245", "v246", "v247", "v248", "v249", "v250", "v251", "v252", "v253", "v254", "v255", "a0", "a1", "a2", "a3", "a4", "a5", "a6", "a7", "a8", "a9", "a10", "a11", "a12", "a13", "a14", "a15", "a16", "a17", "a18", "a19", "a20", "a21", "a22", "a23", "a24", "a25", "a26", "a27", "a28", "a29", "a30", "a31", "a32", "a33", "a34", "a35", "a36", "a37", "a38", "a39", "a40", "a41", "a42", "a43", "a44", "a45", "a46", "a47", "a48", "a49", "a50", "a51", "a52", "a53", "a54", "a55", "a56", "a57", "a58", "a59", "a60", "a61", "a62", "a63", "a64", "a65", "a66", "a67", "a68", "a69", "a70", "a71", "a72", "a73", "a74", "a75", "a76", "a77", "a78", "a79", "a80", "a81", "a82", "a83", "a84", "a85", "a86", "a87", "a88", "a89", "a90", "a91", "a92", "a93", "a94", "a95", "a96", "a97", "a98", "a99", "a100", "a101", "a102", "a103", "a104", "a105", "a106", "a107", "a108", "a109", "a110", "a111", "a112", "a113", "a114", "a115", "a116", "a117", "a118", "a119", "a120", "a121", "a122", "a123", "a124", "a125", "a126", "a127", "a128", "a129", "a130", "a131", "a132", "a133", "a134", "a135", "a136", "a137", "a138", "a139", "a140", "a141", "a142", "a143", "a144", "a145", "a146", "a147", "a148", "a149", "a150", "a151", "a152", "a153", "a154", "a155", "a156", "a157", "a158", "a159", "a160", "a161", "a162", "a163", "a164", "a165", "a166", "a167", "a168", "a169", "a170", "a171", "a172", "a173", "a174", "a175", "a176", "a177", "a178", "a179", "a180", "a181", "a182", "a183", "a184", "a185", "a186", "a187", "a188", "a189", "a190", "a191", "a192", "a193", "a194", "a195", "a196", "a197", "a198", "a199", "a200", "a201", "a202", "a203", "a204", "a205", "a206", "a207", "a208", "a209", "a210", "a211", "a212", "a213", "a214", "a215", "a216", "a217", "a218", "a219", "a220", "a221", "a222", "a223", "a224", "a225", "a226", "a227", "a228", "a229", "a230", "a231", "a232", "a233", "a234", "a235", "a236", "a237", "a238", "a239", "a240", "a241", "a242", "a243", "a244", "a245", "a246", "a247", "a248", "a249", "a250", "a251", "a252", "a253", "a254", "a255", "s8", "s9", "s10", "s11", "s12", "s13", "s14", "s15", "s16", "s17", "s18", "s19", "s20", "s21", "s22", "s23", "s24", "s25", "s26", "s27", "s28", "s29", "s30", "s31", "s32", "s33", "s34", "s35", "s36", "s37", "s38", "s39", "s40", "s41", "s42", "s43", "s44", "s45", "s46", "s47", "s48", "s49", "s50", "s51", "s52", "s53", "s54", "s55", "s56", "s57", "s58", "s59", "s60", "s61", "s62", "s63", "s64", "s65", "s66", "s67", "s68", "s69", "s70", "s71", "s72", "s73", "s74", "s75", "s76", "s77", "s78", "s79", "s80", "s81", "s82", "s83", "s84", "s85", "s86", "s87", "s88", "s89", "s90", "s91", "vcc", "scc", "memory");
}

extern "C" void kernel_launch(void* const* d_in, const int* in_sizes, int n_in, void* d_out, int out_size, void* d_ws, size_t ws_size,
                              hipStream_t stream) {
  const unsigned F = 0x20000u;
  KArgs a;
  a.k = {d_in[1], HKV * SQ * 256u, F}; a.v = {d_in[2], HKV * SQ * 256u, F}; a.x = {d_in[0], SQ * DM * 2u, F}; a.wq = {d_in[3], DM * 4096u * 2u, F};
  a.wo = {d_in[4], DM * 4096u * 2u, F}; a.ws = {d_ws, (unsigned)WS_END, F}; a.c = {d_in[10], DM * 2u, F}; a.wmod = {d_in[11], DM * 6u * DM * 2u, F};
  a.wg = {d_in[7], DM * FF * 2u, F}; a.wu = {d_in[8], DM * FF * 2u, F}; a.wd = {d_in[9], DM * FF * 2u, F}; a.out = {d_out, SQ * DM * 2u, F}; a.wr = {d_in[6], DM * 2u, F};
  (void)hipFuncSetAttribute((const void*)k_main, hipFuncAttributeMaxDynamicSharedMemorySize, 163840);
  k_main<<<256, 256, 163840, stream>>>(a);
}
```

```cpp
#include <hip/hip_runtime.h>
#include <stdint.h>


constexpr int SQ = 2048, DM = 576, HKV = 16, FF = 4608;

constexpr size_t WS_FLAG = 0;
constexpr size_t WS_R    = 20480;
constexpr size_t WS_GRAN = 32768;
constexpr size_t WS_EPOCH = 65536;
constexpr size_t WS_ST   = 69632;
constexpr size_t WS_PART = 139264;
constexpr size_t WS_ROW  = WS_PART + (size_t)HKV * SQ * DM * 2;
constexpr size_t WS_Y    = WS_ROW + (size_t)SQ * DM * 16;
constexpr size_t WS_END  = WS_Y + 72 * 192 * 128;

struct Buf { const void* p; unsigned bytes; unsigned fmt; };
struct KArgs { Buf k, v, x, wq, wo, ws, c, wmod, wg, wu, wd, out, wr; };

__global__ __launch_bounds__(256, 1) void k_main(KArgs args) {
  extern __shared__ __attribute__((aligned(16))) char lds[];
  const unsigned wid = __builtin_amdgcn_readfirstlane(threadIdx.x >> 6);
  const unsigned ldsb = (unsigned)(size_t)lds;
  asm volatile(
"AT_start:\n"
"s_load_dwordx16 s[24:39], %[kp], 64\n"
"s_load_dwordx16 s[8:23], %[kp], 0\n"
"s_load_dwordx16 s[64:79], %[kp], 128\n"
"s_load_dwordx4 s[80:83], %[kp], 192\n"
"s_mov_b32 s42, %[blk]\n"
"s_mov_b32 s41, %[wid]\n"
"s_mov_b32 s40, %[lds]\n"
"s_mov_b32 s45, 0\n"
"v_mbcnt_lo_u32_b32 v248, -1, 0\n"
"v_mbcnt_hi_u32_b32 v248, -1, v248\n"
"s_and_b32 s56, s42, 3\n"
"s_lshr_b32 s57, s42, 3\n"
"s_and_b32 s58, s57, 3\n"
"s_lshl_b32 s56, s56, 2\n"
"s_add_u32 s43, s56, s58\n"
"s_lshr_b32 s44, s57, 2\n"
"s_and_b32 s56, s42, 7\n"
"s_lshr_b32 s56, s56, 2\n"
"s_lshl_b32 s56, s56, 3\n"
"s_add_u32 s44, s44, s56\n"
"s_lshr_b32 s56, s42, 5\n"
"s_lshl_b32 s56, s56, 3\n"
"s_and_b32 s58, s42, 7\n"
"s_add_u32 s56, s56, s58\n"
"s_lshl_b32 s56, s56, 2\n"
"s_and_b32 s57, s57, 3\n"
"s_add_u32 s55, s56, s57\n"
"v_lshrrev_b32 v226, 4, v248\n"
"v_bfe_u32 v227, v248, 2, 2\n"
"v_and_b32 v228, 3, v248\n"
"v_and_b32 v229, 15, v248\n"
"v_lshrrev_b32 v230, 3, v248\n"
"v_and_b32 v231, 7, v248\n"
"s_lshl_b32 s56, s41, 6\n"
"v_add_u32 v234, s56, v248\n"
"v_lshrrev_b32 v232, 1, v234\n"
"v_and_b32 v233, 1, v234\n"
"v_lshlrev_b32 v236, 1, v232\n"
"v_mul_u32_u24 v232, 6912, v232\n"
"v_lshl_add_u32 v235, v233, 4, v232\n"
"s_lshl_b32 s56, s55, 5\n"
"s_add_u32 s57, s56, 884736\n"
"s_add_u32 s58, s56, 1769472\n"
"s_add_u32 s59, s56, 2654208\n"
"s_add_u32 s60, s56, 3538944\n"
"v_lshrrev_b32 v232, 1, v230\n"
"v_xor_b32 v232, v232, v231\n"
"v_mul_u32_u24 v233, 1152, v230\n"
"v_lshl_add_u32 v222, v232, 4, v233\n"
"v_xor_b32 v232, 4, v232\n"
"v_lshl_add_u32 v223, v232, 4, v233\n"
"s_lshl_b32 s61, s44, 2\n"
"s_add_u32 s61, s61, s41\n"
"s_mul_i32 s46, s61, 36864\n"
"s_add_u32 s47, s46, 9216\n"
"s_add_u32 s48, s46, 18432\n"
"s_add_u32 s49, s46, 27648\n"
"s_mul_i32 s63, s41, 36864\n"
"s_add_u32 s63, s63, s40\n"
"s_getpc_b64 s[84:85]\n"
"AT_pc0:\n"
"s_sub_u32 s84, s84, AT_pc0-AT_start\n"
"s_subb_u32 s85, s85, 0\n"
"s_and_b32 s85, s85, 0xffff\n"
"s_mov_b32 s86, AT_kend-AT_start\n"
"s_mov_b32 s87, 0x20000\n"
"s_cmp_ge_u32 s42, 224\n"
"s_cselect_b32 s86, s86, 0\n"
"s_lshr_b32 s52, s42, 3\n"
"s_lshl_b32 s52, s52, 2\n"
"s_add_u32 s52, s52, s41\n"
"s_and_b32 s52, s52, 15\n"
"s_lshl_b32 s52, s52, 12\n"
"v_lshlrev_b32 v214, 6, v248\n"
"buffer_load_dword v250, v214, s[84:87], s52 offen\n"
"s_add_u32 s52, s52, 65536\n"
"buffer_load_dword v250, v214, s[84:87], s52 offen\n"
"s_waitcnt lgkmcnt(0)\n"
"s_load_dword s85, s[28:29], 65536\n"
"s_cmp_lt_u32 s55, 216\n"
"s_cselect_b32 s38, s38, 0\n"
"buffer_load_ushort v164, v236, s[32:35], 0 offen offset:0\n"
"buffer_load_ushort v165, v236, s[32:35], 0 offen offset:256\n"
"buffer_load_ushort v166, v236, s[32:35], 0 offen offset:512\n"
"buffer_load_ushort v167, v236, s[32:35], 0 offen offset:768\n"
"buffer_load_ushort v168, v236, s[32:35], 0 offen offset:1024\n"
"buffer_load_dwordx4 v[144:147], v235, s[36:39], s56 offen\n"
"buffer_load_dwordx4 v[148:151], v235, s[36:39], s57 offen\n"
"buffer_load_dwordx4 v[152:155], v235, s[36:39], s58 offen\n"
"buffer_load_dwordx4 v[156:159], v235, s[36:39], s59 offen\n"
"buffer_load_dwordx4 v[160:163], v235, s[36:39], s60 offen\n"
"s_mov_b32 m0, s63\n"
"s_nop 0\n"
"buffer_load_dwordx4 v222, s[16:19], s46 offen offset:0 lds\n"
"s_add_u32 m0, s63, 1024\n"
"s_nop 0\n"
"buffer_load_dwordx4 v223, s[16:19], s47 offen offset:0 lds\n"
"s_add_u32 m0, s63, 2048\n"
"s_nop 0\n"
"buffer_load_dwordx4 v222, s[16:19], s48 offen offset:0 lds\n"
"s_add_u32 m0, s63, 3072\n"
"s_nop 0\n"
"buffer_load_dwordx4 v223, s[16:19], s49 offen offset:0 lds\n"
"s_add_u32 m0, s63, 3968\n"
"s_nop 0\n"
"buffer_load_dwordx4 v222, s[16:19], s46 offen offset:128 lds\n"
"s_add_u32 m0, s63, 4992\n"
"s_nop 0\n"
"buffer_load_dwordx4 v223, s[16:19], s47 offen offset:128 lds\n"
"s_add_u32 m0, s63, 6016\n"
"s_nop 0\n"
"buffer_load_dwordx4 v222, s[16:19], s48 offen offset:128 lds\n"
"s_add_u32 m0, s63, 7040\n"
"s_nop 0\n"
"buffer_load_dwordx4 v223, s[16:19], s49 offen offset:128 lds\n"
"s_lshl_b32 s56, s41, 12\n"
"s_add_u32 s86, s40, s56\n"
"s_lshl_b32 s84, s41, 12\n"
"s_lshl_b32 s56, s43, 19\n"
"s_add_u32 s84, s84, s56\n"
"s_lshl_b32 s50, s41, 17\n"
"s_lshl_b32 s56, s43, 9\n"
"s_add_u32 s50, s50, s56\n"
"s_lshl_b32 s56, s41, 11\n"
"s_add_u32 s51, s40, s56\n"
"s_add_u32 m0, s63, 7936\n"
"s_nop 0\n"
"buffer_load_dwordx4 v222, s[16:19], s46 offen offset:256 lds\n"
"s_mov_b32 s61, 32768\n"
"s_mov_b32 s62, 37376\n"
"v_lshlrev_b32 v232, 13, v227\n"
"v_lshl_add_u32 v232, v226, 6, v232\n"
"v_lshl_add_u32 v217, v228, 4, v232\n"
"v_and_b32 v232, 2, v227\n"
"v_xor_b32 v232, v232, v226\n"
"v_lshlrev_b32 v233, 10, v227\n"
"v_lshl_add_u32 v233, v228, 6, v233\n"
"v_lshl_add_u32 v233, v232, 4, v233\n"
"s_add_u32 m0, s63, 8960\n"
"s_nop 0\n"
"buffer_load_dwordx4 v223, s[16:19], s47 offen offset:256 lds\n"
"v_add_u32 v240, s40, v233\n"
"v_add_u32 v241, 0x10000, v240\n"
"v_lshlrev_b32 v233, 10, v226\n"
"v_lshl_add_u32 v233, v227, 6, v233\n"
"v_lshl_add_u32 v233, v228, 3, v233\n"
"v_add_u32 v233, s40, v233\n"
"v_and_b32 v232, 1, v226\n"
"v_lshl_add_u32 v242, v232, 5, v233\n"
"v_xor_b32 v243, 32, v242\n"
"v_add_u32 v244, 0x10000, v242\n"
"s_add_u32 m0, s63, 9984\n"
"s_nop 0\n"
"buffer_load_dwordx4 v222, s[16:19], s48 offen offset:256 lds\n"
"v_add_u32 v245, 0x10000, v243\n"
"v_lshlrev_b32 v233, 8, v227\n"
"v_lshl_add_u32 v233, v226, 6, v233\n"
"v_lshl_add_u32 v246, v228, 4, v233\n"
"v_xor_b32 v247, 32, v246\n"
"v_mul_u32_u24 v233, 16392, v232\n"
"v_lshrrev_b32 v232, 1, v226\n"
"v_lshl_add_u32 v233, v232, 11, v233\n"
"v_lshl_add_u32 v233, v227, 6, v233\n"
"v_lshl_add_u32 v233, v228, 4, v233\n"
"s_add_u32 m0, s63, 11008\n"
"s_nop 0\n"
"buffer_load_dwordx4 v223, s[16:19], s49 offen offset:256 lds\n"
"v_add_u32 v218, s40, v233\n"
"v_add_u32 v219, 65568, v218\n"
"v_lshrrev_b32 v232, 1, v229\n"
"v_xor_b32 v232, v232, v226\n"
"v_lshlrev_b32 v233, 7, v229\n"
"v_lshl_add_u32 v233, v232, 4, v233\n"
"v_add_u32 v220, s63, v233\n"
"v_xor_b32 v221, 64, v220\n"
"s_add_u32 s56, s40, 148480\n"
"v_lshl_add_u32 v224, v226, 4, s56\n"
"s_add_u32 m0, s63, 11904\n"
"s_nop 0\n"
"buffer_load_dwordx4 v222, s[16:19], s46 offen offset:384 lds\n"
"v_lshrrev_b32 v232, 3, v234\n"
"v_lshlrev_b32 v233, 8, v232\n"
"v_and_b32 v232, 1, v234\n"
"v_lshl_add_u32 v233, v232, 6, v233\n"
"v_bfe_u32 v232, v234, 1, 2\n"
"v_lshl_add_u32 v233, v232, 4, v233\n"
"v_add_u32 v216, s56, v233\n"
"v_lshlrev_b32 v225, 5, v234\n"
"v_mov_b32 v232, 0x40000000\n"
"v_cmp_gt_u32 vcc, 144, v234\n"
"s_nop 1\n"
"v_cndmask_b32 v225, v232, v225, vcc\n"
"s_lshl_b32 s56, s41, 8\n"
"s_add_u32 s57, s40, 147456\n"
"s_add_u32 s56, s56, s57\n"
"v_and_b32 v232, 1, v248\n"
"v_lshlrev_b32 v232, 5, v232\n"
"v_lshl_add_u32 v232, v226, 6, v232\n"
"v_add_u32 v238, s56, v232\n"
"v_lshlrev_b32 v232, 8, v226\n"
"s_add_u32 m0, s63, 12928\n"
"s_nop 0\n"
"buffer_load_dwordx4 v223, s[16:19], s47 offen offset:384 lds\n"
"v_lshl_add_u32 v232, v229, 2, v232\n"
"v_add_u32 v239, s57, v232\n"
"v_lshlrev_b32 v232, 6, v228\n"
"v_lshl_add_u32 v232, v226, 4, v232\n"
"v_add_u32 v254, s40, v232\n"
"s_lshr_b32 s57, s42, 3\n"
"s_lshl_b32 s57, s57, 2\n"
"s_add_u32 s57, s57, s41\n"
"s_sub_u32 s57, s57, 128\n"
"s_and_b32 s58, s42, 7\n"
"s_add_u32 m0, s63, 13952\n"
"s_nop 0\n"
"buffer_load_dwordx4 v222, s[16:19], s48 offen offset:384 lds\n"
"s_mul_i32 s59, s57, 9216\n"
"s_mul_i32 s60, s58, 1152\n"
"s_add_u32 s59, s59, s60\n"
"v_lshlrev_b32 v252, 6, v248\n"
"v_add_u32 v253, 0x40000000, v252\n"
"v_cmp_gt_u32 vcc, 18, v248\n"
"s_nop 1\n"
"v_cndmask_b32 v251, v253, v252, vcc\n"
"v_cmp_gt_u32 vcc, 36, v248\n"
"s_nop 1\n"
"v_cndmask_b32 v253, v253, v252, vcc\n"
"s_mul_i32 s60, s57, 1152\n"
"s_lshl_b32 s57, s60, 1\n"
"v_add_u32 v252, s60, v251\n"
"v_add_u32 v251, s59, v251\n"
"v_add_u32 v253, s57, v253\n"
"v_mov_b32 v237, 0xffff0000\n"
"v_mov_b32 v176, 0\n"
"v_mov_b32 v177, 0\n"
"v_mov_b32 v178, 0\n"
"s_add_u32 m0, s63, 14976\n"
"s_nop 0\n"
"buffer_load_dwordx4 v223, s[16:19], s49 offen offset:384 lds\n"
"v_mov_b32 v179, 0\n"
"v_mov_b32 v180, 0\n"
"v_mov_b32 v181, 0\n"
"v_mov_b32 v182, 0\n"
"v_mov_b32 v183, 0\n"
"v_mov_b32 v170, 0x3f803f80\n"
"v_mov_b32 v171, 0x3f803f80\n"
"v_mov_b32 v172, 0x3f803f80\n"
"v_mov_b32 v173, 0x3f803f80\n"
"s_waitcnt vmcnt(21)\n"
"v_lshlrev_b32 v164, 16, v164\n"
"v_mul_f32 v184, 0xbfb8aa3b, v164\n"
"v_lshlrev_b32 v165, 16, v165\n"
"v_mul_f32 v185, 0xbfb8aa3b, v165\n"
"v_lshlrev_b32 v166, 16, v166\n"
"v_mul_f32 v186, 0xbfb8aa3b, v166\n"
"v_lshlrev_b32 v167, 16, v167\n"
"v_mul_f32 v187, 0xbfb8aa3b, v167\n"
"v_lshlrev_b32 v168, 16, v168\n"
"v_mul_f32 v188, 0xbfb8aa3b, v168\n"
"v_exp_f32 v184, v184\n"
"s_add_u32 m0, s63, 15872\n"
"s_nop 0\n"
"buffer_load_dwordx4 v222, s[16:19], s46 offen offset:512 lds\n"
"v_exp_f32 v185, v185\n"
"v_exp_f32 v186, v186\n"
"v_exp_f32 v187, v187\n"
"v_exp_f32 v188, v188\n"
"v_add_f32 v184, 1.0, v184\n"
"v_add_f32 v185, 1.0, v185\n"
"v_add_f32 v186, 1.0, v186\n"
"v_add_f32 v187, 1.0, v187\n"
"v_add_f32 v188, 1.0, v188\n"
"v_rcp_f32 v184, v184\n"
"v_rcp_f32 v185, v185\n"
"v_rcp_f32 v186, v186\n"
"s_add_u32 m0, s63, 16896\n"
"s_nop 0\n"
"buffer_load_dwordx4 v223, s[16:19], s47 offen offset:512 lds\n"
"v_rcp_f32 v187, v187\n"
"v_rcp_f32 v188, v188\n"
"v_mul_f32 v164, v164, v184\n"
"v_mul_f32 v165, v165, v185\n"
"v_mul_f32 v166, v166, v186\n"
"v_mul_f32 v167, v167, v187\n"
"v_mul_f32 v168, v168, v188\n"
"v_cvt_pk_bf16_f32 v164, v164, v164\n"
"v_and_b32 v164, v237, v164\n"
"v_cvt_pk_bf16_f32 v165, v165, v165\n"
"v_and_b32 v165, v237, v165\n"
"v_cvt_pk_bf16_f32 v166, v166, v166\n"
"s_add_u32 m0, s63, 17920\n"
"s_nop 0\n"
"buffer_load_dwordx4 v222, s[16:19], s48 offen offset:512 lds\n"
"v_and_b32 v166, v237, v166\n"
"v_cvt_pk_bf16_f32 v167, v167, v167\n"
"v_and_b32 v167, v237, v167\n"
"v_cvt_pk_bf16_f32 v168, v168, v168\n"
"v_and_b32 v168, v237, v168\n"
"s_waitcnt vmcnt(23)\n"
"v_lshlrev_b32 v184, 16, v144\n"
"v_and_b32 v185, v237, v144\n"
"v_lshlrev_b32 v186, 16, v145\n"
"v_and_b32 v187, v237, v145\n"
"v_lshlrev_b32 v188, 16, v146\n"
"v_and_b32 v189, v237, v146\n"
"s_add_u32 m0, s63, 18944\n"
"s_nop 0\n"
"buffer_load_dwordx4 v223, s[16:19], s49 offen offset:512 lds\n"
"v_lshlrev_b32 v190, 16, v147\n"
"v_and_b32 v191, v237, v147\n"
"v_fmac_f32 v176, v164, v184\n"
"v_fmac_f32 v177, v164, v185\n"
"v_fmac_f32 v178, v164, v186\n"
"v_fmac_f32 v179, v164, v187\n"
"v_fmac_f32 v180, v164, v188\n"
"v_fmac_f32 v181, v164, v189\n"
"v_fmac_f32 v182, v164, v190\n"
"v_fmac_f32 v183, v164, v191\n"
"s_waitcnt vmcnt(23)\n"
"v_lshlrev_b32 v184, 16, v148\n"
"s_add_u32 m0, s63, 19840\n"
"s_nop 0\n"
"buffer_load_dwordx4 v222, s[16:19], s46 offen offset:640 lds\n"
"v_and_b32 v185, v237, v148\n"
"v_lshlrev_b32 v186, 16, v149\n"
"v_and_b32 v187, v237, v149\n"
"v_lshlrev_b32 v188, 16, v150\n"
"v_and_b32 v189, v237, v150\n"
"v_lshlrev_b32 v190, 16, v151\n"
"v_and_b32 v191, v237, v151\n"
"v_fmac_f32 v176, v165, v184\n"
"v_fmac_f32 v177, v165, v185\n"
"v_fmac_f32 v178, v165, v186\n"
"v_fmac_f32 v179, v165, v187\n"
"v_fmac_f32 v180, v165, v188\n"
"s_add_u32 m0, s63, 20864\n"
"s_nop 0\n"
"buffer_load_dwordx4 v223, s[16:19], s47 offen offset:640 lds\n"
"v_fmac_f32 v181, v165, v189\n"
"v_fmac_f32 v182, v165, v190\n"
"v_fmac_f32 v183, v165, v191\n"
"s_waitcnt vmcnt(24)\n"
"v_lshlrev_b32 v184, 16, v152\n"
"v_and_b32 v185, v237, v152\n"
"v_lshlrev_b32 v186, 16, v153\n"
"v_and_b32 v187, v237, v153\n"
"v_lshlrev_b32 v188, 16, v154\n"
"v_and_b32 v189, v237, v154\n"
"v_lshlrev_b32 v190, 16, v155\n"
"v_and_b32 v191, v237, v155\n"
"s_add_u32 m0, s63, 21888\n"
"s_nop 0\n"
"buffer_load_dwordx4 v222, s[16:19], s48 offen offset:640 lds\n"
"v_fmac_f32 v176, v166, v184\n"
"v_fmac_f32 v177, v166, v185\n"
"v_fmac_f32 v178, v166, v186\n"
"v_fmac_f32 v179, v166, v187\n"
"v_fmac_f32 v180, v166, v188\n"
"v_fmac_f32 v181, v166, v189\n"
"v_fmac_f32 v182, v166, v190\n"
"v_fmac_f32 v183, v166, v191\n"
"s_waitcnt vmcnt(24)\n"
"v_lshlrev_b32 v184, 16, v156\n"
"v_and_b32 v185, v237, v156\n"
"v_lshlrev_b32 v186, 16, v157\n"
"s_add_u32 m0, s63, 22912\n"
"s_nop 0\n"
"buffer_load_dwordx4 v223, s[16:19], s49 offen offset:640 lds\n"
"v_and_b32 v187, v237, v157\n"
"v_lshlrev_b32 v188, 16, v158\n"
"v_and_b32 v189, v237, v158\n"
"v_lshlrev_b32 v190, 16, v159\n"
"v_and_b32 v191, v237, v159\n"
"v_fmac_f32 v176, v167, v184\n"
"v_fmac_f32 v177, v167, v185\n"
"v_fmac_f32 v178, v167, v186\n"
"v_fmac_f32 v179, v167, v187\n"
"v_fmac_f32 v180, v167, v188\n"
"v_fmac_f32 v181, v167, v189\n"
"v_fmac_f32 v182, v167, v190\n"
"s_add_u32 m0, s63, 23808\n"
"s_nop 0\n"
"buffer_load_dwordx4 v222, s[16:19], s46 offen offset:768 lds\n"
"v_fmac_f32 v183, v167, v191\n"
"s_waitcnt vmcnt(25)\n"
"v_lshlrev_b32 v184, 16, v160\n"
"v_and_b32 v185, v237, v160\n"
"v_lshlrev_b32 v186, 16, v161\n"
"v_and_b32 v187, v237, v161\n"
"v_lshlrev_b32 v188, 16, v162\n"
"v_and_b32 v189, v237, v162\n"
"v_lshlrev_b32 v190, 16, v163\n"
"v_and_b32 v191, v237, v163\n"
"v_fmac_f32 v176, v168, v184\n"
"v_fmac_f32 v177, v168, v185\n"
"s_add_u32 m0, s63, 24832\n"
"s_nop 0\n"
"buffer_load_dwordx4 v223, s[16:19], s47 offen offset:768 lds\n"
"v_fmac_f32 v178, v168, v186\n"
"v_fmac_f32 v179, v168, v187\n"
"v_fmac_f32 v180, v168, v188\n"
"v_fmac_f32 v181, v168, v189\n"
"v_fmac_f32 v182, v168, v190\n"
"v_fmac_f32 v183, v168, v191\n"
"v_add_f32_dpp v176, v176, v176 row_ror:2 row_mask:0xf bank_mask:0xf\n"
"v_add_f32_dpp v177, v177, v177 row_ror:2 row_mask:0xf bank_mask:0xf\n"
"v_add_f32_dpp v178, v178, v178 row_ror:2 row_mask:0xf bank_mask:0xf\n"
"v_add_f32_dpp v179, v179, v179 row_ror:2 row_mask:0xf bank_mask:0xf\n"
"v_add_f32_dpp v180, v180, v180 row_ror:2 row_mask:0xf bank_mask:0xf\n"
"v_add_f32_dpp v181, v181, v181 row_ror:2 row_mask:0xf bank_mask:0xf\n"
"s_add_u32 m0, s63, 25856\n"
"s_nop 0\n"
"buffer_load_dwordx4 v222, s[16:19], s48 offen offset:768 lds\n"
"v_add_f32_dpp v182, v182, v182 row_ror:2 row_mask:0xf bank_mask:0xf\n"
"v_add_f32_dpp v183, v183, v183 row_ror:2 row_mask:0xf bank_mask:0xf\n"
"v_add_f32_dpp v176, v176, v176 row_ror:4 row_mask:0xf bank_mask:0xf\n"
"v_add_f32_dpp v177, v177, v177 row_ror:4 row_mask:0xf bank_mask:0xf\n"
"v_add_f32_dpp v178, v178, v178 row_ror:4 row_mask:0xf bank_mask:0xf\n"
"v_add_f32_dpp v179, v179, v179 row_ror:4 row_mask:0xf bank_mask:0xf\n"
"v_add_f32_dpp v180, v180, v180 row_ror:4 row_mask:0xf bank_mask:0xf\n"
"v_add_f32_dpp v181, v181, v181 row_ror:4 row_mask:0xf bank_mask:0xf\n"
"v_add_f32_dpp v182, v182, v182 row_ror:4 row_mask:0xf bank_mask:0xf\n"
"v_add_f32_dpp v183, v183, v183 row_ror:4 row_mask:0xf bank_mask:0xf\n"
"v_add_f32_dpp v176, v176, v176 row_ror:8 row_mask:0xf bank_mask:0xf\n"
"v_add_f32_dpp v177, v177, v177 row_ror:8 row_mask:0xf bank_mask:0xf\n"
"s_add_u32 m0, s63, 26880\n"
"s_nop 0\n"
"buffer_load_dwordx4 v223, s[16:19], s49 offen offset:768 lds\n"
"v_add_f32_dpp v178, v178, v178 row_ror:8 row_mask:0xf bank_mask:0xf\n"
"v_add_f32_dpp v179, v179, v179 row_ror:8 row_mask:0xf bank_mask:0xf\n"
"v_add_f32_dpp v180, v180, v180 row_ror:8 row_mask:0xf bank_mask:0xf\n"
"v_add_f32_dpp v181, v181, v181 row_ror:8 row_mask:0xf bank_mask:0xf\n"
"v_add_f32_dpp v182, v182, v182 row_ror:8 row_mask:0xf bank_mask:0xf\n"
"v_add_f32_dpp v183, v183, v183 row_ror:8 row_mask:0xf bank_mask:0xf\n"
"s_mov_b32 exec_lo, 0x00030003\n"
"s_mov_b32 exec_hi, 0x00030003\n"
"ds_write_b128 v238, v[176:179]\n"
"ds_write_b128 v238, v[180:183] offset:16\n"
"s_mov_b64 exec, -1\n"
"s_waitcnt lgkmcnt(0)\n"
"s_barrier\n"
"ds_read_b32 v184, v239 offset:0\n"
"ds_read_b32 v185, v239 offset:64\n"
"ds_read_b32 v186, v239 offset:128\n"
"ds_read_b32 v187, v239 offset:192\n"
"s_add_u32 s85, s85, 1\n"
"s_lshl_b32 s56, s55, 7\n"
"s_add_u32 s56, s56, 32768\n"
"s_lshl_b32 s58, s41, 2\n"
"s_lshl_b32 s58, 15, s58\n"
"v_lshlrev_b32 v190, 3, v248\n"
"v_mov_b32 v189, s85\n"
"s_waitcnt lgkmcnt(2)\n"
"v_add_f32 v184, v184, v185\n"
"s_waitcnt lgkmcnt(0)\n"
"v_add_f32 v186, v186, v187\n"
"v_add_f32 v184, v184, v186\n"
"v_mov_b32 v185, v184\n"
"s_nop 1\n"
"v_permlane32_swap_b32 v184, v185\n"
"v_add_f32 v184, v184, v185\n"
"v_mov_b32 v185, v184\n"
"s_nop 1\n"
"v_permlane16_swap_b32 v184, v185\n"
"v_add_f32 v184, v184, v185\n"
"v_cvt_pk_bf16_f32 v188, v184, v184\n"
"v_and_b32 v188, v237, v188\n"
"s_mov_b32 exec_lo, s58\n"
"s_mov_b32 exec_hi, 0\n"
"buffer_store_dwordx2 v[188:189], v190, s[28:31], s56 offen sc1\n"
"s_mov_b64 exec, -1\n"
"s_waitcnt vmcnt(25)\n"
"ds_read_b128 v[0:3], v220 offset:0\n"
"ds_read_b128 v[4:7], v220 offset:2048\n"
"ds_read_b128 v[8:11], v221 offset:0\n"
"ds_read_b128 v[12:15], v221 offset:2048\n"
"s_waitcnt vmcnt(21)\n"
"ds_read_b128 v[16:19], v220 offset:4096\n"
"ds_read_b128 v[20:23], v220 offset:6144\n"
"ds_read_b128 v[24:27], v221 offset:4096\n"
"ds_read_b128 v[28:31], v221 offset:6144\n"
"s_waitcnt lgkmcnt(7)\n"
"v_mfma_f32_16x16x32_bf16 v[192:195], v[170:173], v[0:3], 0\n"
"v_mfma_f32_16x16x32_bf16 v[200:203], v[0:3], v[0:3], 0\n"
"s_waitcnt lgkmcnt(6)\n"
"v_mfma_f32_16x16x32_bf16 v[196:199], v[170:173], v[4:7], 0\n"
"v_mfma_f32_16x16x32_bf16 v[204:207], v[4:7], v[4:7], 0\n"
"s_waitcnt lgkmcnt(5)\n"
"v_mfma_f32_16x16x32_bf16 v[192:195], v[170:173], v[8:11], v[192:195]\n"
"v_mfma_f32_16x16x32_bf16 v[200:203], v[8:11], v[8:11], v[200:203]\n"
"s_waitcnt lgkmcnt(4)\n"
"v_mfma_f32_16x16x32_bf16 v[196:199], v[170:173], v[12:15], v[196:199]\n"
"v_mfma_f32_16x16x32_bf16 v[204:207], v[12:15], v[12:15], v[204:207]\n"
"s_waitcnt lgkmcnt(3)\n"
"s_waitcnt vmcnt(17)\n"
"ds_read_b128 v[32:35], v220 offset:8192\n"
"ds_read_b128 v[36:39], v220 offset:10240\n"
"ds_read_b128 v[40:43], v221 offset:8192\n"
"ds_read_b128 v[44:47], v221 offset:10240\n"
"v_mfma_f32_16x16x32_bf16 v[192:195], v[170:173], v[16:19], v[192:195]\n"
"v_mfma_f32_16x16x32_bf16 v[200:203], v[16:19], v[16:19], v[200:203]\n"
"s_waitcnt lgkmcnt(6)\n"
"v_mfma_f32_16x16x32_bf16 v[196:199], v[170:173], v[20:23], v[196:199]\n"
"v_mfma_f32_16x16x32_bf16 v[204:207], v[20:23], v[20:23], v[204:207]\n"
"s_waitcnt lgkmcnt(4)\n"
"v_mfma_f32_16x16x32_bf16 v[192:195], v[170:173], v[24:27], v[192:195]\n"
"v_mfma_f32_16x16x32_bf16 v[200:203], v[24:27], v[24:27], v[200:203]\n"
"v_mfma_f32_16x16x32_bf16 v[196:199], v[170:173], v[28:31], v[196:199]\n"
"v_mfma_f32_16x16x32_bf16 v[204:207], v[28:31], v[28:31], v[204:207]\n"
"s_waitcnt lgkmcnt(3)\n"
"s_waitcnt vmcnt(13)\n"
"ds_read_b128 v[48:51], v220 offset:12288\n"
"ds_read_b128 v[52:55], v220 offset:14336\n"
"ds_read_b128 v[56:59], v221 offset:12288\n"
"ds_read_b128 v[60:63], v221 offset:14336\n"
"v_mfma_f32_16x16x32_bf16 v[192:195], v[170:173], v[32:35], v[192:195]\n"
"v_mfma_f32_16x16x32_bf16 v[200:203], v[32:35], v[32:35], v[200:203]\n"
"s_waitcnt lgkmcnt(6)\n"
"v_mfma_f32_16x16x32_bf16 v[196:199], v[170:173], v[36:39], v[196:199]\n"
"v_mfma_f32_16x16x32_bf16 v[204:207], v[36:39], v[36:39], v[204:207]\n"
"s_waitcnt lgkmcnt(4)\n"
"v_mfma_f32_16x16x32_bf16 v[192:195], v[170:173], v[40:43], v[192:195]\n"
"v_mfma_f32_16x16x32_bf16 v[200:203], v[40:43], v[40:43], v[200:203]\n"
"v_mfma_f32_16x16x32_bf16 v[196:199], v[170:173], v[44:47], v[196:199]\n"
"v_mfma_f32_16x16x32_bf16 v[204:207], v[44:47], v[44:47], v[204:207]\n"
"s_waitcnt lgkmcnt(3)\n"
"s_add_u32 m0, s63, 27776\n"
"s_nop 0\n"
"buffer_load_dwordx4 v222, s[16:19], s46 offen offset:896 lds\n"
"s_add_u32 m0, s63, 28800\n"
"s_nop 0\n"
"buffer_load_dwordx4 v223, s[16:19], s47 offen offset:896 lds\n"
"s_add_u32 m0, s63, 29824\n"
"s_nop 0\n"
"buffer_load_dwordx4 v222, s[16:19], s48 offen offset:896 lds\n"
"s_add_u32 m0, s63, 30848\n"
"s_nop 0\n"
"buffer_load_dwordx4 v223, s[16:19], s49 offen offset:896 lds\n"
"s_waitcnt vmcnt(13)\n"
"ds_read_b128 v[64:67], v220 offset:16384\n"
"ds_read_b128 v[68:71], v220 offset:18432\n"
"ds_read_b128 v[72:75], v221 offset:16384\n"
"ds_read_b128 v[76:79], v221 offset:18432\n"
"v_mfma_f32_16x16x32_bf16 v[192:195], v[170:173], v[48:51], v[192:195]\n"
"v_mfma_f32_16x16x32_bf16 v[200:203], v[48:51], v[48:51], v[200:203]\n"
"s_waitcnt lgkmcnt(6)\n"
"v_mfma_f32_16x16x32_bf16 v[196:199], v[170:173], v[52:55], v[196:199]\n"
"v_mfma_f32_16x16x32_bf16 v[204:207], v[52:55], v[52:55], v[204:207]\n"
"s_waitcnt lgkmcnt(4)\n"
"v_mfma_f32_16x16x32_bf16 v[192:195], v[170:173], v[56:59], v[192:195]\n"
"v_mfma_f32_16x16x32_bf16 v[200:203], v[56:59], v[56:59], v[200:203]\n"
"v_mfma_f32_16x16x32_bf16 v[196:199], v[170:173], v[60:63], v[196:199]\n"
"v_mfma_f32_16x16x32_bf16 v[204:207], v[60:63], v[60:63], v[204:207]\n"
"s_waitcnt lgkmcnt(3)\n"
"s_add_u32 m0, s63, 31744\n"
"s_nop 0\n"
"buffer_load_dwordx4 v222, s[16:19], s46 offen offset:1024 lds\n"
"s_add_u32 m0, s63, 32768\n"
"s_nop 0\n"
"buffer_load_dwordx4 v223, s[16:19], s47 offen offset:1024 lds\n"
"s_add_u32 m0, s63, 33792\n"
"s_nop 0\n"
"buffer_load_dwordx4 v222, s[16:19], s48 offen offset:1024 lds\n"
"s_add_u32 m0, s63, 34816\n"
"s_nop 0\n"
"buffer_load_dwordx4 v223, s[16:19], s49 offen offset:1024 lds\n"
"s_waitcnt vmcnt(13)\n"
"ds_read_b128 v[80:83], v220 offset:20480\n"
"ds_read_b128 v[84:87], v220 offset:22528\n"
"ds_read_b128 v[88:91], v221 offset:20480\n"
"ds_read_b128 v[92:95], v221 offset:22528\n"
"v_mfma_f32_16x16x32_bf16 v[192:195], v[170:173], v[64:67], v[192:195]\n"
"v_mfma_f32_16x16x32_bf16 v[200:203], v[64:67], v[64:67], v[200:203]\n"
"s_waitcnt lgkmcnt(6)\n"
"v_mfma_f32_16x16x32_bf16 v[196:199], v[170:173], v[68:71], v[196:199]\n"
"v_mfma_f32_16x16x32_bf16 v[204:207], v[68:71], v[68:71], v[204:207]\n"
"s_waitcnt lgkmcnt(4)\n"
"v_mfma_f32_16x16x32_bf16 v[192:195], v[170:173], v[72:75], v[192:195]\n"
"v_mfma_f32_16x16x32_bf16 v[200:203], v[72:75], v[72:75], v[200:203]\n"
"v_mfma_f32_16x16x32_bf16 v[196:199], v[170:173], v[76:79], v[196:199]\n"
"v_mfma_f32_16x16x32_bf16 v[204:207], v[76:79], v[76:79], v[204:207]\n"
"s_waitcnt lgkmcnt(3)\n"
"s_waitcnt vmcnt(9)\n"
"ds_read_b128 v[96:99], v220 offset:24576\n"
"ds_read_b128 v[100:103], v220 offset:26624\n"
"ds_read_b128 v[104:107], v221 offset:24576\n"
"ds_read_b128 v[108:111], v221 offset:26624\n"
"v_mfma_f32_16x16x32_bf16 v[192:195], v[170:173], v[80:83], v[192:195]\n"
"v_mfma_f32_16x16x32_bf16 v[200:203], v[80:83], v[80:83], v[200:203]\n"
"s_waitcnt lgkmcnt(6)\n"
"v_mfma_f32_16x16x32_bf16 v[196:199], v[170:173], v[84:87], v[196:199]\n"
"v_mfma_f32_16x16x32_bf16 v[204:207], v[84:87], v[84:87], v[204:207]\n"
"s_waitcnt lgkmcnt(4)\n"
"v_mfma_f32_16x16x32_bf16 v[192:195], v[170:173], v[88:91], v[192:195]\n"
"v_mfma_f32_16x16x32_bf16 v[200:203], v[88:91], v[88:91], v[200:203]\n"
"v_mfma_f32_16x16x32_bf16 v[196:199], v[170:173], v[92:95], v[196:199]\n"
"v_mfma_f32_16x16x32_bf16 v[204:207], v[92:95], v[92:95], v[204:207]\n"
"s_waitcnt lgkmcnt(3)\n"
"s_waitcnt vmcnt(4)\n"
"ds_read_b128 v[112:115], v220 offset:28672\n"
"ds_read_b128 v[116:119], v220 offset:30720\n"
"ds_read_b128 v[120:123], v221 offset:28672\n"
"ds_read_b128 v[124:127], v221 offset:30720\n"
"v_mfma_f32_16x16x32_bf16 v[192:195], v[170:173], v[96:99], v[192:195]\n"
"v_mfma_f32_16x16x32_bf16 v[200:203], v[96:99], v[96:99], v[200:203]\n"
"s_waitcnt lgkmcnt(6)\n"
"v_mfma_f32_16x16x32_bf16 v[196:199], v[170:173], v[100:103], v[196:199]\n"
"v_mfma_f32_16x16x32_bf16 v[204:207], v[100:103], v[100:103], v[204:207]\n"
"s_waitcnt lgkmcnt(4)\n"
"v_mfma_f32_16x16x32_bf16 v[192:195], v[170:173], v[104:107], v[192:195]\n"
"v_mfma_f32_16x16x32_bf16 v[200:203], v[104:107], v[104:107], v[200:203]\n"
"v_mfma_f32_16x16x32_bf16 v[196:199], v[170:173], v[108:111], v[196:199]\n"
"v_mfma_f32_16x16x32_bf16 v[204:207], v[108:111], v[108:111], v[204:207]\n"
"s_waitcnt lgkmcnt(3)\n"
"s_waitcnt vmcnt(0)\n"
"ds_read_b128 v[128:131], v220 offset:32768\n"
"ds_read_b128 v[132:135], v220 offset:34816\n"
"ds_read_b128 v[136:139], v221 offset:32768\n"
"ds_read_b128 v[140:143], v221 offset:34816\n"
"v_mfma_f32_16x16x32_bf16 v[192:195], v[170:173], v[112:115], v[192:195]\n"
"v_mfma_f32_16x16x32_bf16 v[200:203], v[112:115], v[112:115], v[200:203]\n"
"s_waitcnt lgkmcnt(6)\n"
"v_mfma_f32_16x16x32_bf16 v[196:199], v[170:173], v[116:119], v[196:199]\n"
"v_mfma_f32_16x16x32_bf16 v[204:207], v[116:119], v[116:119], v[204:207]\n"
"s_waitcnt lgkmcnt(4)\n"
"v_mfma_f32_16x16x32_bf16 v[192:195], v[170:173], v[120:123], v[192:195]\n"
"v_mfma_f32_16x16x32_bf16 v[200:203], v[120:123], v[120:123], v[200:203]\n"
"v_mfma_f32_16x16x32_bf16 v[196:199], v[170:173], v[124:127], v[196:199]\n"
"v_mfma_f32_16x16x32_bf16 v[204:207], v[124:127], v[124:127], v[204:207]\n"
"s_waitcnt lgkmcnt(0)\n"
"s_barrier\n"
"v_mfma_f32_16x16x32_bf16 v[192:195], v[170:173], v[128:131], v[192:195]\n"
"v_mfma_f32_16x16x32_bf16 v[200:203], v[128:131], v[128:131], v[200:203]\n"
"v_mfma_f32_16x16x32_bf16 v[196:199], v[170:173], v[132:135], v[196:199]\n"
"v_mfma_f32_16x16x32_bf16 v[204:207], v[132:135], v[132:135], v[204:207]\n"
"v_mfma_f32_16x16x32_bf16 v[192:195], v[170:173], v[136:139], v[192:195]\n"
"v_mfma_f32_16x16x32_bf16 v[200:203], v[136:139], v[136:139], v[200:203]\n"
"v_mfma_f32_16x16x32_bf16 v[196:199], v[170:173], v[140:143], v[196:199]\n"
"v_mfma_f32_16x16x32_bf16 v[204:207], v[140:143], v[140:143], v[204:207]\n"
"s_add_u32 m0, s51, 98352\n"
"s_nop 0\n"
"buffer_load_dwordx4 v217, s[20:23], s50 offen offset:0 lds\n"
"s_add_u32 m0, s51, 106288\n"
"s_nop 0\n"
"buffer_load_dwordx4 v217, s[20:23], s50 offen offset:256 lds\n"
"s_add_u32 s50, s50, 32768\n"
"v_cmp_eq_u32 s[56:57], 1, v228\n"
"v_cmp_eq_u32 s[58:59], 2, v228\n"
"v_cmp_eq_u32 vcc, 3, v228\n"
"s_nop 4\n"
"v_cndmask_b32 v200, v200, v201, s[56:57]\n"
"v_cndmask_b32 v200, v200, v202, s[58:59]\n"
"v_cndmask_b32 v200, v200, v203, vcc\n"
"v_cndmask_b32 v204, v204, v205, s[56:57]\n"
"v_cndmask_b32 v204, v204, v206, s[58:59]\n"
"v_cndmask_b32 v204, v204, v207, vcc\n"
"v_cmp_eq_u32 vcc, v226, v227\n"
"v_mov_b32 v208, v192\n"
"v_mov_b32 v212, v196\n"
"v_cndmask_b32 v210, 0, v200, vcc\n"
"v_cndmask_b32 v214, 0, v204, vcc\n"
"v_mov_b32 v192, v210\n"
"v_mov_b32 v193, v214\n"
"s_nop 0\n"
"v_permlane32_swap_b32 v210, v192\n"
"v_permlane32_swap_b32 v214, v193\n"
"v_add_f32 v210, v210, v192\n"
"v_add_f32 v214, v214, v193\n"
"v_mov_b32 v192, v210\n"
"v_mov_b32 v193, v214\n"
"s_nop 0\n"
"v_permlane16_swap_b32 v210, v192\n"
"v_permlane16_swap_b32 v214, v193\n"
"v_add_f32 v210, v210, v192\n"
"v_add_f32 v214, v214, v193\n"
"v_mul_f32 v208, 0x3ae38e39, v208\n"
"v_mul_f32 v210, 0x3ae38e39, v210\n"
"v_fma_f32 v210, -v208, v208, v210\n"
"v_max_f32 v210, 0, v210\n"
"v_add_f32 v210, 0x3727c5ac, v210\n"
"v_mul_f32 v212, 0x3ae38e39, v212\n"
"v_mul_f32 v214, 0x3ae38e39, v214\n"
"v_fma_f32 v214, -v212, v212, v214\n"
"v_max_f32 v214, 0, v214\n"
"v_add_f32 v214, 0x3727c5ac, v214\n"
"v_rsq_f32 v209, v210\n"
"v_rsq_f32 v213, v214\n"
"s_nop 0\n"
"v_mul_f32 v210, v208, v209\n"
"v_sub_f32 v210, 0, v210\n"
"v_mov_b32 v208, v209\n"
"v_mul_f32 v214, v212, v213\n"
"v_sub_f32 v214, 0, v214\n"
"v_mov_b32 v212, v213\n"
"v_lshlrev_b32 v176, 16, v0\n"
"v_and_b32 v177, v237, v0\n"
"v_lshlrev_b32 v178, 16, v1\n"
"v_and_b32 v179, v237, v1\n"
"v_lshlrev_b32 v180, 16, v2\n"
"v_and_b32 v181, v237, v2\n"
"v_lshlrev_b32 v182, 16, v3\n"
"v_and_b32 v183, v237, v3\n"
"v_pk_fma_f32 v[176:177], v[176:177], v[208:209], v[210:211] op_sel_hi:[1,0,0]\n"
"v_pk_fma_f32 v[178:179], v[178:179], v[208:209], v[210:211] op_sel_hi:[1,0,0]\n"
"v_pk_fma_f32 v[180:181], v[180:181], v[208:209], v[210:211] op_sel_hi:[1,0,0]\n"
"v_pk_fma_f32 v[182:183], v[182:183], v[208:209], v[210:211] op_sel_hi:[1,0,0]\n"
"v_cvt_pk_f16_f32 v0, v176, v177\n"
"v_cvt_pk_f16_f32 v1, v178, v179\n"
"v_cvt_pk_f16_f32 v2, v180, v181\n"
"v_cvt_pk_f16_f32 v3, v182, v183\n"
"v_lshlrev_b32 v184, 16, v4\n"
"v_and_b32 v185, v237, v4\n"
"v_lshlrev_b32 v186, 16, v5\n"
"v_and_b32 v187, v237, v5\n"
"v_lshlrev_b32 v188, 16, v6\n"
"s_add_u32 m0, s51, 99376\n"
"s_nop 0\n"
"buffer_load_dwordx4 v217, s[20:23], s50 offen offset:0 lds\n"
"v_and_b32 v189, v237, v6\n"
"v_lshlrev_b32 v190, 16, v7\n"
"v_and_b32 v191, v237, v7\n"
"v_pk_fma_f32 v[184:185], v[184:185], v[212:213], v[214:215] op_sel_hi:[1,0,0]\n"
"v_pk_fma_f32 v[186:187], v[186:187], v[212:213], v[214:215] op_sel_hi:[1,0,0]\n"
"v_pk_fma_f32 v[188:189], v[188:189], v[212:213], v[214:215] op_sel_hi:[1,0,0]\n"
"v_pk_fma_f32 v[190:191], v[190:191], v[212:213], v[214:215] op_sel_hi:[1,0,0]\n"
"v_cvt_pk_f16_f32 v4, v184, v185\n"
"v_cvt_pk_f16_f32 v5, v186, v187\n"
"v_cvt_pk_f16_f32 v6, v188, v189\n"
"v_cvt_pk_f16_f32 v7, v190, v191\n"
"v_lshlrev_b32 v176, 16, v8\n"
"v_and_b32 v177, v237, v8\n"
"v_lshlrev_b32 v178, 16, v9\n"
"v_and_b32 v179, v237, v9\n"
"v_lshlrev_b32 v180, 16, v10\n"
"v_and_b32 v181, v237, v10\n"
"v_lshlrev_b32 v182, 16, v11\n"
"v_and_b32 v183, v237, v11\n"
"v_pk_fma_f32 v[176:177], v[176:177], v[208:209], v[210:211] op_sel_hi:[1,0,0]\n"
"v_pk_fma_f32 v[178:179], v[178:179], v[208:209], v[210:211] op_sel_hi:[1,0,0]\n"
"s_add_u32 m0, s51, 107312\n"
"s_nop 0\n"
"buffer_load_dwordx4 v217, s[20:23], s50 offen offset:256 lds\n"
"s_add_u32 s50, s50, 32768\n"
"v_pk_fma_f32 v[180:181], v[180:181], v[208:209], v[210:211] op_sel_hi:[1,0,0]\n"
"v_pk_fma_f32 v[182:183], v[182:183], v[208:209], v[210:211] op_sel_hi:[1,0,0]\n"
"v_cvt_pk_f16_f32 v8, v176, v177\n"
"v_cvt_pk_f16_f32 v9, v178, v179\n"
"v_cvt_pk_f16_f32 v10, v180, v181\n"
"v_cvt_pk_f16_f32 v11, v182, v183\n"
"v_lshlrev_b32 v184, 16, v12\n"
"v_and_b32 v185, v237, v12\n"
"v_lshlrev_b32 v186, 16, v13\n"
"v_and_b32 v187, v237, v13\n"
"v_lshlrev_b32 v188, 16, v14\n"
"v_and_b32 v189, v237, v14\n"
"v_lshlrev_b32 v190, 16, v15\n"
"v_and_b32 v191, v237, v15\n"
"v_pk_fma_f32 v[184:185], v[184:185], v[212:213], v[214:215] op_sel_hi:[1,0,0]\n"
"v_pk_fma_f32 v[186:187], v[186:187], v[212:213], v[214:215] op_sel_hi:[1,0,0]\n"
"v_pk_fma_f32 v[188:189], v[188:189], v[212:213], v[214:215] op_sel_hi:[1,0,0]\n"
"v_pk_fma_f32 v[190:191], v[190:191], v[212:213], v[214:215] op_sel_hi:[1,0,0]\n"
"v_cvt_pk_f16_f32 v12, v184, v185\n"
"v_cvt_pk_f16_f32 v13, v186, v187\n"
"v_cvt_pk_f16_f32 v14, v188, v189\n"
"s_add_u32 m0, s51, 114744\n"
"s_nop 0\n"
"buffer_load_dwordx4 v217, s[20:23], s50 offen offset:0 lds\n"
"v_cvt_pk_f16_f32 v15, v190, v191\n"
"v_lshlrev_b32 v176, 16, v16\n"
"v_and_b32 v177, v237, v16\n"
"v_lshlrev_b32 v178, 16, v17\n"
"v_and_b32 v179, v237, v17\n"
"v_lshlrev_b32 v180, 16, v18\n"
"v_and_b32 v181, v237, v18\n"
"v_lshlrev_b32 v182, 16, v19\n"
"v_and_b32 v183, v237, v19\n"
"v_pk_fma_f32 v[176:177], v[176:177], v[208:209], v[210:211] op_sel_hi:[1,0,0]\n"
"v_pk_fma_f32 v[178:179], v[178:179], v[208:209], v[210:211] op_sel_hi:[1,0,0]\n"
"v_pk_fma_f32 v[180:181], v[180:181], v[208:209], v[210:211] op_sel_hi:[1,0,0]\n"
"v_pk_fma_f32 v[182:183], v[182:183], v[208:209], v[210:211] op_sel_hi:[1,0,0]\n"
"v_cvt_pk_f16_f32 v16, v176, v177\n"
"v_cvt_pk_f16_f32 v17, v178, v179\n"
"v_cvt_pk_f16_f32 v18, v180, v181\n"
"v_cvt_pk_f16_f32 v19, v182, v183\n"
"v_lshlrev_b32 v184, 16, v20\n"
"v_and_b32 v185, v237, v20\n"
"v_lshlrev_b32 v186, 16, v21\n"
"v_and_b32 v187, v237, v21\n"
"v_lshlrev_b32 v188, 16, v22\n"
"s_add_u32 m0, s51, 122680\n"
"s_nop 0\n"
"buffer_load_dwordx4 v217, s[20:23], s50 offen offset:256 lds\n"
"s_add_u32 s50, s50, 32768\n"
"v_and_b32 v189, v237, v22\n"
"v_lshlrev_b32 v190, 16, v23\n"
"v_and_b32 v191, v237, v23\n"
"v_pk_fma_f32 v[184:185], v[184:185], v[212:213], v[214:215] op_sel_hi:[1,0,0]\n"
"v_pk_fma_f32 v[186:187], v[186:187], v[212:213], v[214:215] op_sel_hi:[1,0,0]\n"
"v_pk_fma_f32 v[188:189], v[188:189], v[212:213], v[214:215] op_sel_hi:[1,0,0]\n"
"v_pk_fma_f32 v[190:191], v[190:191], v[212:213], v[214:215] op_sel_hi:[1,0,0]\n"
"v_cvt_pk_f16_f32 v20, v184, v185\n"
"v_cvt_pk_f16_f32 v21, v186, v187\n"
"v_cvt_pk_f16_f32 v22, v188, v189\n"
"v_cvt_pk_f16_f32 v23, v190, v191\n"
"v_lshlrev_b32 v176, 16, v24\n"
"v_and_b32 v177, v237, v24\n"
"v_lshlrev_b32 v178, 16, v25\n"
"v_and_b32 v179, v237, v25\n"
"v_lshlrev_b32 v180, 16, v26\n"
"v_and_b32 v181, v237, v26\n"
"v_lshlrev_b32 v182, 16, v27\n"
"v_and_b32 v183, v237, v27\n"
"v_pk_fma_f32 v[176:177], v[176:177], v[208:209], v[210:211] op_sel_hi:[1,0,0]\n"
"v_pk_fma_f32 v[178:179], v[178:179], v[208:209], v[210:211] op_sel_hi:[1,0,0]\n"
"s_add_u32 m0, s51, 115768\n"
"s_nop 0\n"
"buffer_load_dwordx4 v217, s[20:23], s50 offen offset:0 lds\n"
"v_pk_fma_f32 v[180:181], v[180:181], v[208:209], v[210:211] op_sel_hi:[1,0,0]\n"
"v_pk_fma_f32 v[182:183], v[182:183], v[208:209], v[210:211] op_sel_hi:[1,0,0]\n"
"v_cvt_pk_f16_f32 v24, v176, v177\n"
"v_cvt_pk_f16_f32 v25, v178, v179\n"
"v_cvt_pk_f16_f32 v26, v180, v181\n"
"v_cvt_pk_f16_f32 v27, v182, v183\n"
"v_lshlrev_b32 v184, 16, v28\n"
"v_and_b32 v185, v237, v28\n"
"v_lshlrev_b32 v186, 16, v29\n"
"v_and_b32 v187, v237, v29\n"
"v_lshlrev_b32 v188, 16, v30\n"
"v_and_b32 v189, v237, v30\n"
"v_lshlrev_b32 v190, 16, v31\n"
"v_and_b32 v191, v237, v31\n"
"v_pk_fma_f32 v[184:185], v[184:185], v[212:213], v[214:215] op_sel_hi:[1,0,0]\n"
"v_pk_fma_f32 v[186:187], v[186:187], v[212:213], v[214:215] op_sel_hi:[1,0,0]\n"
"v_pk_fma_f32 v[188:189], v[188:189], v[212:213], v[214:215] op_sel_hi:[1,0,0]\n"
"v_pk_fma_f32 v[190:191], v[190:191], v[212:213], v[214:215] op_sel_hi:[1,0,0]\n"
"v_cvt_pk_f16_f32 v28, v184, v185\n"
"v_cvt_pk_f16_f32 v29, v186, v187\n"
"v_cvt_pk_f16_f32 v30, v188, v189\n"
"s_add_u32 m0, s51, 123704\n"
"s_nop 0\n"
"buffer_load_dwordx4 v217, s[20:23], s50 offen offset:256 lds\n"
"s_add_u32 s50, s50, 425984\n"
"v_cvt_pk_f16_f32 v31, v190, v191\n"
"buffer_load_dwordx4 v[144:147], v225, s[28:31], s61 offen offset:0 sc1\n"
"buffer_load_dwordx4 v[148:151], v225, s[28:31], s61 offen offset:16 sc1\n"
"buffer_load_dwordx4 v[152:155], v225, s[28:31], s62 offen offset:0 sc1\n"
"buffer_load_dwordx4 v[156:159], v225, s[28:31], s62 offen offset:16 sc1\n"
"v_lshlrev_b32 v176, 16, v32\n"
"v_and_b32 v177, v237, v32\n"
"v_lshlrev_b32 v178, 16, v33\n"
"v_and_b32 v179, v237, v33\n"
"v_lshlrev_b32 v180, 16, v34\n"
"v_and_b32 v181, v237, v34\n"
"v_lshlrev_b32 v182, 16, v35\n"
"v_and_b32 v183, v237, v35\n"
"v_pk_fma_f32 v[176:177], v[176:177], v[208:209], v[210:211] op_sel_hi:[1,0,0]\n"
"v_pk_fma_f32 v[178:179], v[178:179], v[208:209], v[210:211] op_sel_hi:[1,0,0]\n"
"v_pk_fma_f32 v[180:181], v[180:181], v[208:209], v[210:211] op_sel_hi:[1,0,0]\n"
"v_pk_fma_f32 v[182:183], v[182:183], v[208:209], v[210:211] op_sel_hi:[1,0,0]\n"
"v_cvt_pk_f16_f32 v32, v176, v177\n"
"v_cvt_pk_f16_f32 v33, v178, v179\n"
"v_cvt_pk_f16_f32 v34, v180, v181\n"
"v_cvt_pk_f16_f32 v35, v182, v183\n"
"v_lshlrev_b32 v184, 16, v36\n"
"v_and_b32 v185, v237, v36\n"
"v_lshlrev_b32 v186, 16, v37\n"
"v_and_b32 v187, v237, v37\n"
"v_lshlrev_b32 v188, 16, v38\n"
"s_mov_b32 m0, s51\n"
"s_nop 0\n"
"buffer_load_dwordx4 v217, s[20:23], s50 offen offset:0 lds\n"
"v_and_b32 v189, v237, v38\n"
"v_lshlrev_b32 v190, 16, v39\n"
"v_and_b32 v191, v237, v39\n"
"v_pk_fma_f32 v[184:185], v[184:185], v[212:213], v[214:215] op_sel_hi:[1,0,0]\n"
"v_pk_fma_f32 v[186:187], v[186:187], v[212:213], v[214:215] op_sel_hi:[1,0,0]\n"
"v_pk_fma_f32 v[188:189], v[188:189], v[212:213], v[214:215] op_sel_hi:[1,0,0]\n"
"v_pk_fma_f32 v[190:191], v[190:191], v[212:213], v[214:215] op_sel_hi:[1,0,0]\n"
"v_cvt_pk_f16_f32 v36, v184, v185\n"
"v_cvt_pk_f16_f32 v37, v186, v187\n"
"v_cvt_pk_f16_f32 v38, v188, v189\n"
"v_cvt_pk_f16_f32 v39, v190, v191\n"
"v_lshlrev_b32 v176, 16, v40\n"
"v_and_b32 v177, v237, v40\n"
"v_lshlrev_b32 v178, 16, v41\n"
"v_and_b32 v179, v237, v41\n"
"v_lshlrev_b32 v180, 16, v42\n"
"v_and_b32 v181, v237, v42\n"
"v_lshlrev_b32 v182, 16, v43\n"
"v_and_b32 v183, v237, v43\n"
"v_pk_fma_f32 v[176:177], v[176:177], v[208:209], v[210:211] op_sel_hi:[1,0,0]\n"
"v_pk_fma_f32 v[178:179], v[178:179], v[208:209], v[210:211] op_sel_hi:[1,0,0]\n"
"s_add_u32 m0, s51, 7936\n"
"s_nop 0\n"
"buffer_load_dwordx4 v217, s[20:23], s50 offen offset:256 lds\n"
"s_add_u32 s50, s50, 32768\n"
"v_pk_fma_f32 v[180:181], v[180:181], v[208:209], v[210:211] op_sel_hi:[1,0,0]\n"
"v_pk_fma_f32 v[182:183], v[182:183], v[208:209], v[210:211] op_sel_hi:[1,0,0]\n"
"v_cvt_pk_f16_f32 v40, v176, v177\n"
"v_cvt_pk_f16_f32 v41, v178, v179\n"
"v_cvt_pk_f16_f32 v42, v180, v181\n"
"v_cvt_pk_f16_f32 v43, v182, v183\n"
"v_lshlrev_b32 v184, 16, v44\n"
"v_and_b32 v185, v237, v44\n"
"v_lshlrev_b32 v186, 16, v45\n"
"v_and_b32 v187, v237, v45\n"
"v_lshlrev_b32 v188, 16, v46\n"
"v_and_b32 v189, v237, v46\n"
"v_lshlrev_b32 v190, 16, v47\n"
"v_and_b32 v191, v237, v47\n"
"v_pk_fma_f32 v[184:185], v[184:185], v[212:213], v[214:215] op_sel_hi:[1,0,0]\n"
"v_pk_fma_f32 v[186:187], v[186:187], v[212:213], v[214:215] op_sel_hi:[1,0,0]\n"
"v_pk_fma_f32 v[188:189], v[188:189], v[212:213], v[214:215] op_sel_hi:[1,0,0]\n"
"v_pk_fma_f32 v[190:191], v[190:191], v[212:213], v[214:215] op_sel_hi:[1,0,0]\n"
"v_cvt_pk_f16_f32 v44, v184, v185\n"
"v_cvt_pk_f16_f32 v45, v186, v187\n"
"v_cvt_pk_f16_f32 v46, v188, v189\n"
"s_add_u32 m0, s51, 1024\n"
"s_nop 0\n"
"buffer_load_dwordx4 v217, s[20:23], s50 offen offset:0 lds\n"
"v_cvt_pk_f16_f32 v47, v190, v191\n"
"v_lshlrev_b32 v176, 16, v48\n"
"v_and_b32 v177, v237, v48\n"
"v_lshlrev_b32 v178, 16, v49\n"
"v_and_b32 v179, v237, v49\n"
"v_lshlrev_b32 v180, 16, v50\n"
"v_and_b32 v181, v237, v50\n"
"v_lshlrev_b32 v182, 16, v51\n"
"v_and_b32 v183, v237, v51\n"
"v_pk_fma_f32 v[176:177], v[176:177], v[208:209], v[210:211] op_sel_hi:[1,0,0]\n"
"v_pk_fma_f32 v[178:179], v[178:179], v[208:209], v[210:211] op_sel_hi:[1,0,0]\n"
"v_pk_fma_f32 v[180:181], v[180:181], v[208:209], v[210:211] op_sel_hi:[1,0,0]\n"
"v_pk_fma_f32 v[182:183], v[182:183], v[208:209], v[210:211] op_sel_hi:[1,0,0]\n"
"v_cvt_pk_f16_f32 v48, v176, v177\n"
"v_cvt_pk_f16_f32 v49, v178, v179\n"
"v_cvt_pk_f16_f32 v50, v180, v181\n"
"v_cvt_pk_f16_f32 v51, v182, v183\n"
"v_lshlrev_b32 v184, 16, v52\n"
"v_and_b32 v185, v237, v52\n"
"v_lshlrev_b32 v186, 16, v53\n"
"v_and_b32 v187, v237, v53\n"
"v_lshlrev_b32 v188, 16, v54\n"
"s_add_u32 m0, s51, 8960\n"
"s_nop 0\n"
"buffer_load_dwordx4 v217, s[20:23], s50 offen offset:256 lds\n"
"s_add_u32 s50, s50, 32768\n"
"v_and_b32 v189, v237, v54\n"
"v_lshlrev_b32 v190, 16, v55\n"
"v_and_b32 v191, v237, v55\n"
"v_pk_fma_f32 v[184:185], v[184:185], v[212:213], v[214:215] op_sel_hi:[1,0,0]\n"
"v_pk_fma_f32 v[186:187], v[186:187], v[212:213], v[214:215] op_sel_hi:[1,0,0]\n"
"v_pk_fma_f32 v[188:189], v[188:189], v[212:213], v[214:215] op_sel_hi:[1,0,0]\n"
"v_pk_fma_f32 v[190:191], v[190:191], v[212:213], v[214:215] op_sel_hi:[1,0,0]\n"
"v_cvt_pk_f16_f32 v52, v184, v185\n"
"v_cvt_pk_f16_f32 v53, v186, v187\n"
"v_cvt_pk_f16_f32 v54, v188, v189\n"
"v_cvt_pk_f16_f32 v55, v190, v191\n"
"v_lshlrev_b32 v176, 16, v56\n"
"v_and_b32 v177, v237, v56\n"
"v_lshlrev_b32 v178, 16, v57\n"
"v_and_b32 v179, v237, v57\n"
"v_lshlrev_b32 v180, 16, v58\n"
"v_and_b32 v181, v237, v58\n"
"v_lshlrev_b32 v182, 16, v59\n"
"v_and_b32 v183, v237, v59\n"
"v_pk_fma_f32 v[176:177], v[176:177], v[208:209], v[210:211] op_sel_hi:[1,0,0]\n"
"v_pk_fma_f32 v[178:179], v[178:179], v[208:209], v[210:211] op_sel_hi:[1,0,0]\n"
"s_add_u32 m0, s51, 16392\n"
"s_nop 0\n"
"buffer_load_dwordx4 v217, s[20:23], s50 offen offset:0 lds\n"
"v_pk_fma_f32 v[180:181], v[180:181], v[208:209], v[210:211] op_sel_hi:[1,0,0]\n"
"v_pk_fma_f32 v[182:183], v[182:183], v[208:209], v[210:211] op_sel_hi:[1,0,0]\n"
"v_cvt_pk_f16_f32 v56, v176, v177\n"
"v_cvt_pk_f16_f32 v57, v178, v179\n"
"v_cvt_pk_f16_f32 v58, v180, v181\n"
"v_cvt_pk_f16_f32 v59, v182, v183\n"
"v_lshlrev_b32 v184, 16, v60\n"
"v_and_b32 v185, v237, v60\n"
"v_lshlrev_b32 v186, 16, v61\n"
"v_and_b32 v187, v237, v61\n"
"v_lshlrev_b32 v188, 16, v62\n"
"v_and_b32 v189, v237, v62\n"
"v_lshlrev_b32 v190, 16, v63\n"
"v_and_b32 v191, v237, v63\n"
"v_pk_fma_f32 v[184:185], v[184:185], v[212:213], v[214:215] op_sel_hi:[1,0,0]\n"
"v_pk_fma_f32 v[186:187], v[186:187], v[212:213], v[214:215] op_sel_hi:[1,0,0]\n"
"v_pk_fma_f32 v[188:189], v[188:189], v[212:213], v[214:215] op_sel_hi:[1,0,0]\n"
"v_pk_fma_f32 v[190:191], v[190:191], v[212:213], v[214:215] op_sel_hi:[1,0,0]\n"
"v_cvt_pk_f16_f32 v60, v184, v185\n"
"v_cvt_pk_f16_f32 v61, v186, v187\n"
"v_cvt_pk_f16_f32 v62, v188, v189\n"
"s_add_u32 m0, s51, 24328\n"
"s_nop 0\n"
"buffer_load_dwordx4 v217, s[20:23], s50 offen offset:256 lds\n"
"s_add_u32 s50, s50, 32768\n"
"v_cvt_pk_f16_f32 v63, v190, v191\n"
"v_lshlrev_b32 v176, 16, v64\n"
"v_and_b32 v177, v237, v64\n"
"v_lshlrev_b32 v178, 16, v65\n"
"v_and_b32 v179, v237, v65\n"
"v_lshlrev_b32 v180, 16, v66\n"
"v_and_b32 v181, v237, v66\n"
"v_lshlrev_b32 v182, 16, v67\n"
"v_and_b32 v183, v237, v67\n"
"v_pk_fma_f32 v[176:177], v[176:177], v[208:209], v[210:211] op_sel_hi:[1,0,0]\n"
"v_pk_fma_f32 v[178:179], v[178:179], v[208:209], v[210:211] op_sel_hi:[1,0,0]\n"
"v_pk_fma_f32 v[180:181], v[180:181], v[208:209], v[210:211] op_sel_hi:[1,0,0]\n"
"v_pk_fma_f32 v[182:183], v[182:183], v[208:209], v[210:211] op_sel_hi:[1,0,0]\n"
"v_cvt_pk_f16_f32 v64, v176, v177\n"
"v_cvt_pk_f16_f32 v65, v178, v179\n"
"v_cvt_pk_f16_f32 v66, v180, v181\n"
"v_cvt_pk_f16_f32 v67, v182, v183\n"
"v_lshlrev_b32 v184, 16, v68\n"
"v_and_b32 v185, v237, v68\n"
"v_lshlrev_b32 v186, 16, v69\n"
"v_and_b32 v187, v237, v69\n"
"v_lshlrev_b32 v188, 16, v70\n"
"s_add_u32 m0, s51, 17416\n"
"s_nop 0\n"
"buffer_load_dwordx4 v217, s[20:23], s50 offen offset:0 lds\n"
"v_and_b32 v189, v237, v70\n"
"v_lshlrev_b32 v190, 16, v71\n"
"v_and_b32 v191, v237, v71\n"
"v_pk_fma_f32 v[184:185], v[184:185], v[212:213], v[214:215] op_sel_hi:[1,0,0]\n"
"v_pk_fma_f32 v[186:187], v[186:187], v[212:213], v[214:215] op_sel_hi:[1,0,0]\n"
"v_pk_fma_f32 v[188:189], v[188:189], v[212:213], v[214:215] op_sel_hi:[1,0,0]\n"
"v_pk_fma_f32 v[190:191], v[190:191], v[212:213], v[214:215] op_sel_hi:[1,0,0]\n"
"v_cvt_pk_f16_f32 v68, v184, v185\n"
"v_cvt_pk_f16_f32 v69, v186, v187\n"
"v_cvt_pk_f16_f32 v70, v188, v189\n"
"v_cvt_pk_f16_f32 v71, v190, v191\n"
"v_lshlrev_b32 v176, 16, v72\n"
"v_and_b32 v177, v237, v72\n"
"v_lshlrev_b32 v178, 16, v73\n"
"v_and_b32 v179, v237, v73\n"
"v_lshlrev_b32 v180, 16, v74\n"
"v_and_b32 v181, v237, v74\n"
"v_lshlrev_b32 v182, 16, v75\n"
"v_and_b32 v183, v237, v75\n"
"v_pk_fma_f32 v[176:177], v[176:177], v[208:209], v[210:211] op_sel_hi:[1,0,0]\n"
"v_pk_fma_f32 v[178:179], v[178:179], v[208:209], v[210:211] op_sel_hi:[1,0,0]\n"
"s_add_u32 m0, s51, 25352\n"
"s_nop 0\n"
"buffer_load_dwordx4 v217, s[20:23], s50 offen offset:256 lds\n"
"s_add_u32 s50, s50, 425984\n"
"v_pk_fma_f32 v[180:181], v[180:181], v[208:209], v[210:211] op_sel_hi:[1,0,0]\n"
"v_pk_fma_f32 v[182:183], v[182:183], v[208:209], v[210:211] op_sel_hi:[1,0,0]\n"
"v_cvt_pk_f16_f32 v72, v176, v177\n"
"v_cvt_pk_f16_f32 v73, v178, v179\n"
"v_cvt_pk_f16_f32 v74, v180, v181\n"
"v_cvt_pk_f16_f32 v75, v182, v183\n"
"v_lshlrev_b32 v184, 16, v76\n"
"v_and_b32 v185, v237, v76\n"
"v_lshlrev_b32 v186, 16, v77\n"
"v_and_b32 v187, v237, v77\n"
"v_lshlrev_b32 v188, 16, v78\n"
"v_and_b32 v189, v237, v78\n"
"v_lshlrev_b32 v190, 16, v79\n"
"v_and_b32 v191, v237, v79\n"
"v_pk_fma_f32 v[184:185], v[184:185], v[212:213], v[214:215] op_sel_hi:[1,0,0]\n"
"v_pk_fma_f32 v[186:187], v[186:187], v[212:213], v[214:215] op_sel_hi:[1,0,0]\n"
"v_pk_fma_f32 v[188:189], v[188:189], v[212:213], v[214:215] op_sel_hi:[1,0,0]\n"
"v_pk_fma_f32 v[190:191], v[190:191], v[212:213], v[214:215] op_sel_hi:[1,0,0]\n"
"v_cvt_pk_f16_f32 v76, v184, v185\n"
"v_cvt_pk_f16_f32 v77, v186, v187\n"
"v_cvt_pk_f16_f32 v78, v188, v189\n"
"s_add_u32 m0, s51, 32784\n"
"s_nop 0\n"
"buffer_load_dwordx4 v217, s[20:23], s50 offen offset:0 lds\n"
"v_cvt_pk_f16_f32 v79, v190, v191\n"
"v_lshlrev_b32 v176, 16, v80\n"
"v_and_b32 v177, v237, v80\n"
"v_lshlrev_b32 v178, 16, v81\n"
"v_and_b32 v179, v237, v81\n"
"v_lshlrev_b32 v180, 16, v82\n"
"v_and_b32 v181, v237, v82\n"
"v_lshlrev_b32 v182, 16, v83\n"
"v_and_b32 v183, v237, v83\n"
"v_pk_fma_f32 v[176:177], v[176:177], v[208:209], v[210:211] op_sel_hi:[1,0,0]\n"
"v_pk_fma_f32 v[178:179], v[178:179], v[208:209], v[210:211] op_sel_hi:[1,0,0]\n"
"v_pk_fma_f32 v[180:181], v[180:181], v[208:209], v[210:211] op_sel_hi:[1,0,0]\n"
"v_pk_fma_f32 v[182:183], v[182:183], v[208:209], v[210:211] op_sel_hi:[1,0,0]\n"
"v_cvt_pk_f16_f32 v80, v176, v177\n"
"v_cvt_pk_f16_f32 v81, v178, v179\n"
"v_cvt_pk_f16_f32 v82, v180, v181\n"
"v_cvt_pk_f16_f32 v83, v182, v183\n"
"v_lshlrev_b32 v184, 16, v84\n"
"v_and_b32 v185, v237, v84\n"
"v_lshlrev_b32 v186, 16, v85\n"
"v_and_b32 v187, v237, v85\n"
"v_lshlrev_b32 v188, 16, v86\n"
"s_add_u32 m0, s51, 40720\n"
"s_nop 0\n"
"buffer_load_dwordx4 v217, s[20:23], s50 offen offset:256 lds\n"
"s_add_u32 s50, s50, 32768\n"
"v_and_b32 v189, v237, v86\n"
"v_lshlrev_b32 v190, 16, v87\n"
"v_and_b32 v191, v237, v87\n"
"v_pk_fma_f32 v[184:185], v[184:185], v[212:213], v[214:215] op_sel_hi:[1,0,0]\n"
"v_pk_fma_f32 v[186:187], v[186:187], v[212:213], v[214:215] op_sel_hi:[1,0,0]\n"
"v_pk_fma_f32 v[188:189], v[188:189], v[212:213], v[214:215] op_sel_hi:[1,0,0]\n"
"v_pk_fma_f32 v[190:191], v[190:191], v[212:213], v[214:215] op_sel_hi:[1,0,0]\n"
"v_cvt_pk_f16_f32 v84, v184, v185\n"
"v_cvt_pk_f16_f32 v85, v186, v187\n"
"v_cvt_pk_f16_f32 v86, v188, v189\n"
"v_cvt_pk_f16_f32 v87, v190, v191\n"
"v_lshlrev_b32 v176, 16, v88\n"
"v_and_b32 v177, v237, v88\n"
"v_lshlrev_b32 v178, 16, v89\n"
"v_and_b32 v179, v237, v89\n"
"v_lshlrev_b32 v180, 16, v90\n"
"v_and_b32 v181, v237, v90\n"
"v_lshlrev_b32 v182, 16, v91\n"
"v_and_b32 v183, v237, v91\n"
"v_pk_fma_f32 v[176:177], v[176:177], v[208:209], v[210:211] op_sel_hi:[1,0,0]\n"
"v_pk_fma_f32 v[178:179], v[178:179], v[208:209], v[210:211] op_sel_hi:[1,0,0]\n"
"s_add_u32 m0, s51, 33808\n"
"s_nop 0\n"
"buffer_load_dwordx4 v217, s[20:23], s50 offen offset:0 lds\n"
"v_pk_fma_f32 v[180:181], v[180:181], v[208:209], v[210:211] op_sel_hi:[1,0,0]\n"
"v_pk_fma_f32 v[182:183], v[182:183], v[208:209], v[210:211] op_sel_hi:[1,0,0]\n"
"v_cvt_pk_f16_f32 v88, v176, v177\n"
"v_cvt_pk_f16_f32 v89, v178, v179\n"
"v_cvt_pk_f16_f32 v90, v180, v181\n"
"v_cvt_pk_f16_f32 v91, v182, v183\n"
"v_lshlrev_b32 v184, 16, v92\n"
"v_and_b32 v185, v237, v92\n"
"v_lshlrev_b32 v186, 16, v93\n"
"v_and_b32 v187, v237, v93\n"
"v_lshlrev_b32 v188, 16, v94\n"
"v_and_b32 v189, v237, v94\n"
"v_lshlrev_b32 v190, 16, v95\n"
"v_and_b32 v191, v237, v95\n"
"v_pk_fma_f32 v[184:185], v[184:185], v[212:213], v[214:215] op_sel_hi:[1,0,0]\n"
"v_pk_fma_f32 v[186:187], v[186:187], v[212:213], v[214:215] op_sel_hi:[1,0,0]\n"
"v_pk_fma_f32 v[188:189], v[188:189], v[212:213], v[214:215] op_sel_hi:[1,0,0]\n"
"v_pk_fma_f32 v[190:191], v[190:191], v[212:213], v[214:215] op_sel_hi:[1,0,0]\n"
"v_cvt_pk_f16_f32 v92, v184, v185\n"
"v_cvt_pk_f16_f32 v93, v186, v187\n"
"v_cvt_pk_f16_f32 v94, v188, v189\n"
"s_add_u32 m0, s51, 41744\n"
"s_nop 0\n"
"buffer_load_dwordx4 v217, s[20:23], s50 offen offset:256 lds\n"
"s_add_u32 s50, s50, 32768\n"
"v_cvt_pk_f16_f32 v95, v190, v191\n"
"v_lshlrev_b32 v176, 16, v96\n"
"v_and_b32 v177, v237, v96\n"
"v_lshlrev_b32 v178, 16, v97\n"
"v_and_b32 v179, v237, v97\n"
"v_lshlrev_b32 v180, 16, v98\n"
"v_and_b32 v181, v237, v98\n"
"v_lshlrev_b32 v182, 16, v99\n"
"v_and_b32 v183, v237, v99\n"
"v_pk_fma_f32 v[176:177], v[176:177], v[208:209], v[210:211] op_sel_hi:[1,0,0]\n"
"v_pk_fma_f32 v[178:179], v[178:179], v[208:209], v[210:211] op_sel_hi:[1,0,0]\n"
"v_pk_fma_f32 v[180:181], v[180:181], v[208:209], v[210:211] op_sel_hi:[1,0,0]\n"
"v_pk_fma_f32 v[182:183], v[182:183], v[208:209], v[210:211] op_sel_hi:[1,0,0]\n"
"v_cvt_pk_f16_f32 v96, v176, v177\n"
"v_cvt_pk_f16_f32 v97, v178, v179\n"
"v_cvt_pk_f16_f32 v98, v180, v181\n"
"v_cvt_pk_f16_f32 v99, v182, v183\n"
"v_lshlrev_b32 v184, 16, v100\n"
"v_and_b32 v185, v237, v100\n"
"v_lshlrev_b32 v186, 16, v101\n"
"v_and_b32 v187, v237, v101\n"
"v_lshlrev_b32 v188, 16, v102\n"
"s_add_u32 m0, s51, 49176\n"
"s_nop 0\n"
"buffer_load_dwordx4 v217, s[20:23], s50 offen offset:0 lds\n"
"v_and_b32 v189, v237, v102\n"
"v_lshlrev_b32 v190, 16, v103\n"
"v_and_b32 v191, v237, v103\n"
"v_pk_fma_f32 v[184:185], v[184:185], v[212:213], v[214:215] op_sel_hi:[1,0,0]\n"
"v_pk_fma_f32 v[186:187], v[186:187], v[212:213], v[214:215] op_sel_hi:[1,0,0]\n"
"v_pk_fma_f32 v[188:189], v[188:189], v[212:213], v[214:215] op_sel_hi:[1,0,0]\n"
"v_pk_fma_f32 v[190:191], v[190:191], v[212:213], v[214:215] op_sel_hi:[1,0,0]\n"
"v_cvt_pk_f16_f32 v100, v184, v185\n"
"v_cvt_pk_f16_f32 v101, v186, v187\n"
"v_cvt_pk_f16_f32 v102, v188, v189\n"
"v_cvt_pk_f16_f32 v103, v190, v191\n"
"v_lshlrev_b32 v176, 16, v104\n"
"v_and_b32 v177, v237, v104\n"
"v_lshlrev_b32 v178, 16, v105\n"
"v_and_b32 v179, v237, v105\n"
"v_lshlrev_b32 v180, 16, v106\n"
"v_and_b32 v181, v237, v106\n"
"v_lshlrev_b32 v182, 16, v107\n"
"v_and_b32 v183, v237, v107\n"
"v_pk_fma_f32 v[176:177], v[176:177], v[208:209], v[210:211] op_sel_hi:[1,0,0]\n"
"v_pk_fma_f32 v[178:179], v[178:179], v[208:209], v[210:211] op_sel_hi:[1,0,0]\n"
"s_add_u32 m0, s51, 57112\n"
"s_nop 0\n"
"buffer_load_dwordx4 v217, s[20:23], s50 offen offset:256 lds\n"
"s_add_u32 s50, s50, 32768\n"
"v_pk_fma_f32 v[180:181], v[180:181], v[208:209], v[210:211] op_sel_hi:[1,0,0]\n"
"v_pk_fma_f32 v[182:183], v[182:183], v[208:209], v[210:211] op_sel_hi:[1,0,0]\n"
"v_cvt_pk_f16_f32 v104, v176, v177\n"
"v_cvt_pk_f16_f32 v105, v178, v179\n"
"v_cvt_pk_f16_f32 v106, v180, v181\n"
"v_cvt_pk_f16_f32 v107, v182, v183\n"
"v_lshlrev_b32 v184, 16, v108\n"
"v_and_b32 v185, v237, v108\n"
"v_lshlrev_b32 v186, 16, v109\n"
"v_and_b32 v187, v237, v109\n"
"v_lshlrev_b32 v188, 16, v110\n"
"v_and_b32 v189, v237, v110\n"
"v_lshlrev_b32 v190, 16, v111\n"
"v_and_b32 v191, v237, v111\n"
"v_pk_fma_f32 v[184:185], v[184:185], v[212:213], v[214:215] op_sel_hi:[1,0,0]\n"
"v_pk_fma_f32 v[186:187], v[186:187], v[212:213], v[214:215] op_sel_hi:[1,0,0]\n"
"v_pk_fma_f32 v[188:189], v[188:189], v[212:213], v[214:215] op_sel_hi:[1,0,0]\n"
"v_pk_fma_f32 v[190:191], v[190:191], v[212:213], v[214:215] op_sel_hi:[1,0,0]\n"
"v_cvt_pk_f16_f32 v108, v184, v185\n"
"v_cvt_pk_f16_f32 v109, v186, v187\n"
"v_cvt_pk_f16_f32 v110, v188, v189\n"
"s_add_u32 m0, s51, 50200\n"
"s_nop 0\n"
"buffer_load_dwordx4 v217, s[20:23], s50 offen offset:0 lds\n"
"v_cvt_pk_f16_f32 v111, v190, v191\n"
"s_mov_b32 s87, 0x80000\n"
"s_waitcnt vmcnt(15)\n"
"AT_gchk:\n"
"v_xor_b32 v192, s85, v145\n"
"v_xor_b32 v193, s85, v147\n"
"v_bitop3_b32 v192, v149, s85, v192 bitop3:0xbe\n"
"v_bitop3_b32 v193, v151, s85, v193 bitop3:0xbe\n"
"v_bitop3_b32 v192, v153, s85, v192 bitop3:0xbe\n"
"v_bitop3_b32 v193, v155, s85, v193 bitop3:0xbe\n"
"v_bitop3_b32 v192, v157, s85, v192 bitop3:0xbe\n"
"v_bitop3_b32 v193, v159, s85, v193 bitop3:0xbe\n"
"v_or_b32 v192, v192, v193\n"
"v_cmp_ne_u32 s[56:57], 0, v192\n"
"v_cmp_gt_u32 vcc, 144, v234\n"
"s_nop 1\n"
"s_and_b64 vcc, vcc, s[56:57]\n"
"s_cbranch_vccz AT_gok\n"
"s_sub_u32 s87, s87, 1\n"
"s_cmp_eq_u32 s87, 0\n"
"s_cbranch_scc1 AT_gok\n"
"s_sleep 2\n"
"buffer_load_dwordx4 v[144:147], v225, s[28:31], s61 offen offset:0 sc1\n"
"buffer_load_dwordx4 v[148:151], v225, s[28:31], s61 offen offset:16 sc1\n"
"buffer_load_dwordx4 v[152:155], v225, s[28:31], s62 offen offset:0 sc1\n"
"buffer_load_dwordx4 v[156:159], v225, s[28:31], s62 offen offset:16 sc1\n"
"s_waitcnt vmcnt(0)\n"
"s_branch AT_gchk\n"
"AT_gok:\n"
"v_mov_b32 v236, 0x3e0293ee\n"
"v_mul_f32 v192, v236, v144\n"
"v_fma_f32 v196, v152, v236, v236\n"
"v_mul_f32 v193, v236, v146\n"
"v_fma_f32 v197, v154, v236, v236\n"
"v_mul_f32 v194, v236, v148\n"
"v_fma_f32 v198, v156, v236, v236\n"
"v_mul_f32 v195, v236, v150\n"
"v_fma_f32 v199, v158, v236, v236\n"
"ds_write_b128 v216, v[196:199]\n"
"ds_write_b128 v216, v[192:195] offset:128\n"
"v_lshlrev_b32 v176, 16, v112\n"
"v_and_b32 v177, v237, v112\n"
"v_lshlrev_b32 v178, 16, v113\n"
"v_and_b32 v179, v237, v113\n"
"v_lshlrev_b32 v180, 16, v114\n"
"v_and_b32 v181, v237, v114\n"
"v_lshlrev_b32 v182, 16, v115\n"
"v_and_b32 v183, v237, v115\n"
"v_pk_fma_f32 v[176:177], v[176:177], v[208:209], v[210:211] op_sel_hi:[1,0,0]\n"
"v_pk_fma_f32 v[178:179], v[178:179], v[208:209], v[210:211] op_sel_hi:[1,0,0]\n"
"v_pk_fma_f32 v[180:181], v[180:181], v[208:209], v[210:211] op_sel_hi:[1,0,0]\n"
"v_pk_fma_f32 v[182:183], v[182:183], v[208:209], v[210:211] op_sel_hi:[1,0,0]\n"
"v_cvt_pk_f16_f32 v112, v176, v177\n"
"v_cvt_pk_f16_f32 v113, v178, v179\n"
"v_cvt_pk_f16_f32 v114, v180, v181\n"
"v_cvt_pk_f16_f32 v115, v182, v183\n"
"v_lshlrev_b32 v184, 16, v116\n"
"v_and_b32 v185, v237, v116\n"
"v_lshlrev_b32 v186, 16, v117\n"
"v_and_b32 v187, v237, v117\n"
"v_lshlrev_b32 v188, 16, v118\n"
"s_add_u32 m0, s51, 58136\n"
"s_nop 0\n"
"buffer_load_dwordx4 v217, s[20:23], s50 offen offset:256 lds\n"
"s_add_u32 s50, s50, 425984\n"
"v_and_b32 v189, v237, v118\n"
"v_lshlrev_b32 v190, 16, v119\n"
"v_and_b32 v191, v237, v119\n"
"v_pk_fma_f32 v[184:185], v[184:185], v[212:213], v[214:215] op_sel_hi:[1,0,0]\n"
"v_pk_fma_f32 v[186:187], v[186:187], v[212:213], v[214:215] op_sel_hi:[1,0,0]\n"
"v_pk_fma_f32 v[188:189], v[188:189], v[212:213], v[214:215] op_sel_hi:[1,0,0]\n"
"v_pk_fma_f32 v[190:191], v[190:191], v[212:213], v[214:215] op_sel_hi:[1,0,0]\n"
"v_cvt_pk_f16_f32 v116, v184, v185\n"
"v_cvt_pk_f16_f32 v117, v186, v187\n"
"v_cvt_pk_f16_f32 v118, v188, v189\n"
"v_cvt_pk_f16_f32 v119, v190, v191\n"
"v_lshlrev_b32 v176, 16, v120\n"
"v_and_b32 v177, v237, v120\n"
"v_lshlrev_b32 v178, 16, v121\n"
"v_and_b32 v179, v237, v121\n"
"v_lshlrev_b32 v180, 16, v122\n"
"v_and_b32 v181, v237, v122\n"
"v_lshlrev_b32 v182, 16, v123\n"
"v_and_b32 v183, v237, v123\n"
"v_pk_fma_f32 v[176:177], v[176:177], v[208:209], v[210:211] op_sel_hi:[1,0,0]\n"
"v_pk_fma_f32 v[178:179], v[178:179], v[208:209], v[210:211] op_sel_hi:[1,0,0]\n"
"v_pk_fma_f32 v[180:181], v[180:181], v[208:209], v[210:211] op_sel_hi:[1,0,0]\n"
"v_pk_fma_f32 v[182:183], v[182:183], v[208:209], v[210:211] op_sel_hi:[1,0,0]\n"
"v_cvt_pk_f16_f32 v120, v176, v177\n"
"v_cvt_pk_f16_f32 v121, v178, v179\n"
"v_cvt_pk_f16_f32 v122, v180, v181\n"
"v_cvt_pk_f16_f32 v123, v182, v183\n"
"v_lshlrev_b32 v184, 16, v124\n"
"v_and_b32 v185, v237, v124\n"
"v_lshlrev_b32 v186, 16, v125\n"
"v_and_b32 v187, v237, v125\n"
"v_lshlrev_b32 v188, 16, v126\n"
"v_and_b32 v189, v237, v126\n"
"v_lshlrev_b32 v190, 16, v127\n"
"v_and_b32 v191, v237, v127\n"
"v_pk_fma_f32 v[184:185], v[184:185], v[212:213], v[214:215] op_sel_hi:[1,0,0]\n"
"v_pk_fma_f32 v[186:187], v[186:187], v[212:213], v[214:215] op_sel_hi:[1,0,0]\n"
"v_pk_fma_f32 v[188:189], v[188:189], v[212:213], v[214:215] op_sel_hi:[1,0,0]\n"
"v_pk_fma_f32 v[190:191], v[190:191], v[212:213], v[214:215] op_sel_hi:[1,0,0]\n"
"v_cvt_pk_f16_f32 v124, v184, v185\n"
"v_cvt_pk_f16_f32 v125, v186, v187\n"
"v_cvt_pk_f16_f32 v126, v188, v189\n"
"v_cvt_pk_f16_f32 v127, v190, v191\n"
"s_waitcnt vmcnt(20) lgkmcnt(0)\n"
"s_barrier\n"
"ds_read_b128 v[144:147], v224 offset:0\n"
"ds_read_b128 v[148:151], v224 offset:64\n"
"ds_read_b128 v[152:155], v224 offset:128\n"
"ds_read_b128 v[156:159], v224 offset:192\n"
"ds_read_b128 v[160:163], v224 offset:256\n"
"ds_read_b128 v[164:167], v224 offset:320\n"
"ds_read_b128 v[168:171], v224 offset:384\n"
"ds_read_b128 v[172:175], v224 offset:448\n"
"v_lshlrev_b32 v176, 16, v128\n"
"v_and_b32 v177, v237, v128\n"
"v_lshlrev_b32 v178, 16, v129\n"
"v_and_b32 v179, v237, v129\n"
"v_lshlrev_b32 v180, 16, v130\n"
"v_and_b32 v181, v237, v130\n"
"v_lshlrev_b32 v182, 16, v131\n"
"v_and_b32 v183, v237, v131\n"
"v_pk_fma_f32 v[176:177], v[176:177], v[208:209], v[210:211] op_sel_hi:[1,0,0]\n"
"v_pk_fma_f32 v[178:179], v[178:179], v[208:209], v[210:211] op_sel_hi:[1,0,0]\n"
"v_pk_fma_f32 v[180:181], v[180:181], v[208:209], v[210:211] op_sel_hi:[1,0,0]\n"
"v_pk_fma_f32 v[182:183], v[182:183], v[208:209], v[210:211] op_sel_hi:[1,0,0]\n"
"v_cvt_pk_f16_f32 v128, v176, v177\n"
"v_cvt_pk_f16_f32 v129, v178, v179\n"
"v_cvt_pk_f16_f32 v130, v180, v181\n"
"v_cvt_pk_f16_f32 v131, v182, v183\n"
"v_lshlrev_b32 v184, 16, v132\n"
"v_and_b32 v185, v237, v132\n"
"v_lshlrev_b32 v186, 16, v133\n"
"v_and_b32 v187, v237, v133\n"
"v_lshlrev_b32 v188, 16, v134\n"
"v_and_b32 v189, v237, v134\n"
"v_lshlrev_b32 v190, 16, v135\n"
"v_and_b32 v191, v237, v135\n"
"v_pk_fma_f32 v[184:185], v[184:185], v[212:213], v[214:215] op_sel_hi:[1,0,0]\n"
"v_pk_fma_f32 v[186:187], v[186:187], v[212:213], v[214:215] op_sel_hi:[1,0,0]\n"
"v_pk_fma_f32 v[188:189], v[188:189], v[212:213], v[214:215] op_sel_hi:[1,0,0]\n"
"v_pk_fma_f32 v[190:191], v[190:191], v[212:213], v[214:215] op_sel_hi:[1,0,0]\n"
"v_cvt_pk_f16_f32 v132, v184, v185\n"
"v_cvt_pk_f16_f32 v133, v186, v187\n"
"v_cvt_pk_f16_f32 v134, v188, v189\n"
"v_cvt_pk_f16_f32 v135, v190, v191\n"
"v_lshlrev_b32 v176, 16, v136\n"
"v_and_b32 v177, v237, v136\n"
"v_lshlrev_b32 v178, 16, v137\n"
"v_and_b32 v179, v237, v137\n"
"v_lshlrev_b32 v180, 16, v138\n"
"v_and_b32 v181, v237, v138\n"
"v_lshlrev_b32 v182, 16, v139\n"
"v_and_b32 v183, v237, v139\n"
"v_pk_fma_f32 v[176:177], v[176:177], v[208:209], v[210:211] op_sel_hi:[1,0,0]\n"
"v_pk_fma_f32 v[178:179], v[178:179], v[208:209], v[210:211] op_sel_hi:[1,0,0]\n"
"v_pk_fma_f32 v[180:181], v[180:181], v[208:209], v[210:211] op_sel_hi:[1,0,0]\n"
"v_pk_fma_f32 v[182:183], v[182:183], v[208:209], v[210:211] op_sel_hi:[1,0,0]\n"
"v_cvt_pk_f16_f32 v136, v176, v177\n"
"v_cvt_pk_f16_f32 v137, v178, v179\n"
"v_cvt_pk_f16_f32 v138, v180, v181\n"
"v_cvt_pk_f16_f32 v139, v182, v183\n"
"v_lshlrev_b32 v184, 16, v140\n"
"v_and_b32 v185, v237, v140\n"
"v_lshlrev_b32 v186, 16, v141\n"
"v_and_b32 v187, v237, v141\n"
"v_lshlrev_b32 v188, 16, v142\n"
"v_and_b32 v189, v237, v142\n"
"v_lshlrev_b32 v190, 16, v143\n"
"v_and_b32 v191, v237, v143\n"
"v_pk_fma_f32 v[184:185], v[184:185], v[212:213], v[214:215] op_sel_hi:[1,0,0]\n"
"v_pk_fma_f32 v[186:187], v[186:187], v[212:213], v[214:215] op_sel_hi:[1,0,0]\n"
"v_pk_fma_f32 v[188:189], v[188:189], v[212:213], v[214:215] op_sel_hi:[1,0,0]\n"
"v_pk_fma_f32 v[190:191], v[190:191], v[212:213], v[214:215] op_sel_hi:[1,0,0]\n"
"v_cvt_pk_f16_f32 v140, v184, v185\n"
"v_cvt_pk_f16_f32 v141, v186, v187\n"
"v_cvt_pk_f16_f32 v142, v188, v189\n"
"v_cvt_pk_f16_f32 v143, v190, v191\n"
"ds_read_b64_tr_b16 a[160:161], v219 offset:32784\n"
"s_waitcnt lgkmcnt(6)\n"
"v_fma_mix_f32 v176, v0, v144, v152 op_sel:[0,0,0] op_sel_hi:[1,0,0]\n"
"v_fma_mix_f32 v177, v0, v145, v153 op_sel:[1,0,0] op_sel_hi:[1,0,0]\n"
"v_fma_mix_f32 v178, v1, v146, v154 op_sel:[0,0,0] op_sel_hi:[1,0,0]\n"
"ds_read_b64_tr_b16 a[162:163], v219 offset:33808\n"
"v_fma_mix_f32 v179, v1, v147, v155 op_sel:[1,0,0] op_sel_hi:[1,0,0]\n"
"s_waitcnt lgkmcnt(6)\n"
"v_fma_mix_f32 v180, v2, v148, v156 op_sel:[0,0,0] op_sel_hi:[1,0,0]\n"
"v_fma_mix_f32 v181, v2, v149, v157 op_sel:[1,0,0] op_sel_hi:[1,0,0]\n"
"ds_read_b64_tr_b16 a[164:165], v219 offset:36880\n"
"v_fma_mix_f32 v182, v3, v150, v158 op_sel:[0,0,0] op_sel_hi:[1,0,0]\n"
"v_fma_mix_f32 v183, v3, v151, v159 op_sel:[1,0,0] op_sel_hi:[1,0,0]\n"
"v_cvt_pk_bf16_f32 v0, v176, v177\n"
"ds_read_b64_tr_b16 a[166:167], v219 offset:37904\n"
"v_cvt_pk_bf16_f32 v1, v178, v179\n"
"v_cvt_pk_bf16_f32 v2, v180, v181\n"
"v_cvt_pk_bf16_f32 v3, v182, v183\n"
"ds_read_b64_tr_b16 a[168:169], v219 offset:32792\n"
"v_fma_mix_f32 v184, v4, v144, v152 op_sel:[0,0,0] op_sel_hi:[1,0,0]\n"
"v_fma_mix_f32 v185, v4, v145, v153 op_sel:[1,0,0] op_sel_hi:[1,0,0]\n"
"v_fma_mix_f32 v186, v5, v146, v154 op_sel:[0,0,0] op_sel_hi:[1,0,0]\n"
"ds_read_b64_tr_b16 a[170:171], v219 offset:33816\n"
"v_fma_mix_f32 v187, v5, v147, v155 op_sel:[1,0,0] op_sel_hi:[1,0,0]\n"
"v_fma_mix_f32 v188, v6, v148, v156 op_sel:[0,0,0] op_sel_hi:[1,0,0]\n"
"v_fma_mix_f32 v189, v6, v149, v157 op_sel:[1,0,0] op_sel_hi:[1,0,0]\n"
"ds_read_b64_tr_b16 a[172:173], v219 offset:36888\n"
"v_fma_mix_f32 v190, v7, v150, v158 op_sel:[0,0,0] op_sel_hi:[1,0,0]\n"
"v_fma_mix_f32 v191, v7, v151, v159 op_sel:[1,0,0] op_sel_hi:[1,0,0]\n"
"v_cvt_pk_bf16_f32 v4, v184, v185\n"
"ds_read_b64_tr_b16 a[174:175], v219 offset:37912\n"
"ds_read_b128 v[144:147], v224 offset:512\n"
"v_cvt_pk_bf16_f32 v5, v186, v187\n"
"v_cvt_pk_bf16_f32 v6, v188, v189\n"
"ds_read_b64_tr_b16 a[176:177], v219 offset:33040\n"
"v_cvt_pk_bf16_f32 v7, v190, v191\n"
"s_waitcnt lgkmcnt(11)\n"
"v_fma_mix_f32 v176, v8, v160, v168 op_sel:[0,0,0] op_sel_hi:[1,0,0]\n"
"v_fma_mix_f32 v177, v8, v161, v169 op_sel:[1,0,0] op_sel_hi:[1,0,0]\n"
"ds_read_b64_tr_b16 a[178:179], v219 offset:34064\n"
"v_fma_mix_f32 v178, v9, v162, v170 op_sel:[0,0,0] op_sel_hi:[1,0,0]\n"
"v_fma_mix_f32 v179, v9, v163, v171 op_sel:[1,0,0] op_sel_hi:[1,0,0]\n"
"s_waitcnt lgkmcnt(11)\n"
"v_fma_mix_f32 v180, v10, v164, v172 op_sel:[0,0,0] op_sel_hi:[1,0,0]\n"
"ds_read_b64_tr_b16 a[180:181], v219 offset:37136\n"
"ds_read_b128 v[148:151], v224 offset:576\n"
"v_fma_mix_f32 v181, v10, v165, v173 op_sel:[1,0,0] op_sel_hi:[1,0,0]\n"
"v_fma_mix_f32 v182, v11, v166, v174 op_sel:[0,0,0] op_sel_hi:[1,0,0]\n"
"ds_read_b64_tr_b16 a[182:183], v219 offset:38160\n"
"v_fma_mix_f32 v183, v11, v167, v175 op_sel:[1,0,0] op_sel_hi:[1,0,0]\n"
"v_cvt_pk_bf16_f32 v8, v176, v177\n"
"v_cvt_pk_bf16_f32 v9, v178, v179\n"
"ds_read_b64_tr_b16 a[184:185], v219 offset:33048\n"
"v_cvt_pk_bf16_f32 v10, v180, v181\n"
"v_cvt_pk_bf16_f32 v11, v182, v183\n"
"v_fma_mix_f32 v184, v12, v160, v168 op_sel:[0,0,0] op_sel_hi:[1,0,0]\n"
"ds_read_b64_tr_b16 a[186:187], v219 offset:34072\n"
"ds_read_b128 v[152:155], v224 offset:640\n"
"v_fma_mix_f32 v185, v12, v161, v169 op_sel:[1,0,0] op_sel_hi:[1,0,0]\n"
"v_fma_mix_f32 v186, v13, v162, v170 op_sel:[0,0,0] op_sel_hi:[1,0,0]\n"
"ds_read_b64_tr_b16 a[188:189], v219 offset:37144\n"
"v_fma_mix_f32 v187, v13, v163, v171 op_sel:[1,0,0] op_sel_hi:[1,0,0]\n"
"v_fma_mix_f32 v188, v14, v164, v172 op_sel:[0,0,0] op_sel_hi:[1,0,0]\n"
"v_fma_mix_f32 v189, v14, v165, v173 op_sel:[1,0,0] op_sel_hi:[1,0,0]\n"
"ds_read_b64_tr_b16 a[190:191], v219 offset:38168\n"
"v_fma_mix_f32 v190, v15, v166, v174 op_sel:[0,0,0] op_sel_hi:[1,0,0]\n"
"v_fma_mix_f32 v191, v15, v167, v175 op_sel:[1,0,0] op_sel_hi:[1,0,0]\n"
"v_cvt_pk_bf16_f32 v12, v184, v185\n"
"ds_read_b64_tr_b16 a[224:225], v219 offset:33296\n"
"ds_read_b128 v[156:159], v224 offset:704\n"
"ds_read_b128 v[160:163], v224 offset:768\n"
"v_cvt_pk_bf16_f32 v13, v186, v187\n"
"ds_read_b64_tr_b16 a[226:227], v219 offset:34320\n"
"v_cvt_pk_bf16_f32 v14, v188, v189\n"
"v_cvt_pk_bf16_f32 v15, v190, v191\n"
"s_waitcnt lgkmcnt(6)\n"
"v_fma_mix_f32 v176, v16, v144, v152 op_sel:[0,0,0] op_sel_hi:[1,0,0]\n"
"ds_read_b64_tr_b16 a[228:229], v219 offset:37392\n"
"v_fma_mix_f32 v177, v16, v145, v153 op_sel:[1,0,0] op_sel_hi:[1,0,0]\n"
"v_fma_mix_f32 v178, v17, v146, v154 op_sel:[0,0,0] op_sel_hi:[1,0,0]\n"
"v_fma_mix_f32 v179, v17, v147, v155 op_sel:[1,0,0] op_sel_hi:[1,0,0]\n"
"ds_read_b64_tr_b16 a[230:231], v219 offset:38416\n"
"ds_read_b128 v[164:167], v224 offset:832\n"
"s_waitcnt lgkmcnt(5)\n"
"v_fma_mix_f32 v180, v18, v148, v156 op_sel:[0,0,0] op_sel_hi:[1,0,0]\n"
"v_fma_mix_f32 v181, v18, v149, v157 op_sel:[1,0,0] op_sel_hi:[1,0,0]\n"
"v_mfma_f32_16x16x32_bf16 a[0:3], a[160:163], v[0:3], 0\n"
"ds_read_b64_tr_b16 a[232:233], v219 offset:33304\n"
"v_fma_mix_f32 v182, v19, v150, v158 op_sel:[0,0,0] op_sel_hi:[1,0,0]\n"
"v_mfma_f32_16x16x32_bf16 a[0:3], a[164:167], v[8:11], a[0:3]\n"
"ds_read_b64_tr_b16 a[234:235], v219 offset:34328\n"
"v_fma_mix_f32 v183, v19, v151, v159 op_sel:[1,0,0] op_sel_hi:[1,0,0]\n"
"v_mfma_f32_16x16x32_bf16 a[4:7], a[160:163], v[4:7], 0\n"
"s_waitcnt vmcnt(8)\n"
"s_barrier\n"
"ds_read_b64_tr_b16 a[236:237], v219 offset:37400\n"
"ds_read_b128 v[168:171], v224 offset:896\n"
"v_mfma_f32_16x16x32_bf16 a[4:7], a[164:167], v[12:15], a[4:7]\n"
"ds_read_b64_tr_b16 a[238:239], v219 offset:38424\n"
"v_cvt_pk_bf16_f32 v16, v176, v177\n"
"v_mfma_f32_16x16x32_bf16 a[8:11], a[168:171], v[0:3], 0\n"
"s_add_u32 m0, s51, 65568\n"
"ds_read_b64_tr_b16 a[240:241], v219 offset:33552\n"
"v_cvt_pk_bf16_f32 v17, v178, v179\n"
"v_mfma_f32_16x16x32_bf16 a[8:11], a[172:175], v[8:11], a[8:11]\n"
"ds_read_b64_tr_b16 a[242:243], v219 offset:34576\n"
"buffer_load_dwordx4 v217, s[20:23], s50 offen offset:0 lds\n"
"v_mfma_f32_16x16x32_bf16 a[12:15], a[168:171], v[4:7], 0\n"
"ds_read_b64_tr_b16 a[244:245], v219 offset:37648\n"
"ds_read_b128 v[172:175], v224 offset:960\n"
"v_mfma_f32_16x16x32_bf16 a[12:15], a[172:175], v[12:15], a[12:15]\n"
"ds_read_b64_tr_b16 a[246:247], v219 offset:38672\n"
"v_cvt_pk_bf16_f32 v18, v180, v181\n"
"v_mfma_f32_16x16x32_bf16 a[16:19], a[176:179], v[0:3], 0\n"
"ds_read_b64_tr_b16 a[248:249], v219 offset:33560\n"
"v_cvt_pk_bf16_f32 v19, v182, v183\n"
"v_mfma_f32_16x16x32_bf16 a[16:19], a[180:183], v[8:11], a[16:19]\n"
"ds_read_b64_tr_b16 a[250:251], v219 offset:34584\n"
"v_fma_mix_f32 v184, v20, v144, v152 op_sel:[0,0,0] op_sel_hi:[1,0,0]\n"
"v_mfma_f32_16x16x32_bf16 a[20:23], a[176:179], v[4:7], 0\n"
"s_add_u32 m0, s51, 73504\n"
"ds_read_b64_tr_b16 a[252:253], v219 offset:37656\n"
"v_fma_mix_f32 v185, v20, v145, v153 op_sel:[1,0,0] op_sel_hi:[1,0,0]\n"
"v_mfma_f32_16x16x32_bf16 a[20:23], a[180:183], v[12:15], a[20:23]\n"
"ds_read_b64_tr_b16 a[254:255], v219 offset:38680\n"
"buffer_load_dwordx4 v217, s[20:23], s50 offen offset:256 lds\n"
"v_mfma_f32_16x16x32_bf16 a[24:27], a[184:187], v[0:3], 0\n"
"s_add_u32 s50, s50, 32768\n"
"ds_read_b64_tr_b16 a[160:161], v219 offset:40976\n"
"v_fma_mix_f32 v186, v21, v146, v154 op_sel:[0,0,0] op_sel_hi:[1,0,0]\n"
"v_mfma_f32_16x16x32_bf16 a[24:27], a[188:191], v[8:11], a[24:27]\n"
"ds_read_b64_tr_b16 a[162:163], v219 offset:42000\n"
"v_fma_mix_f32 v187, v21, v147, v155 op_sel:[1,0,0] op_sel_hi:[1,0,0]\n"
"v_mfma_f32_16x16x32_bf16 a[28:31], a[184:187], v[4:7], 0\n"
"ds_read_b64_tr_b16 a[164:165], v219 offset:45072\n"
"v_fma_mix_f32 v188, v22, v148, v156 op_sel:[0,0,0] op_sel_hi:[1,0,0]\n"
"v_mfma_f32_16x16x32_bf16 a[28:31], a[188:191], v[12:15], a[28:31]\n"
"s_waitcnt lgkmcnt(12)\n"
"ds_read_b64_tr_b16 a[166:167], v219 offset:46096\n"
"v_fma_mix_f32 v189, v22, v149, v157 op_sel:[1,0,0] op_sel_hi:[1,0,0]\n"
"v_mfma_f32_16x16x32_bf16 a[32:35], a[224:227], v[0:3], 0\n"
"s_add_u32 m0, s51, 66592\n"
"ds_read_b64_tr_b16 a[168:169], v219 offset:40984\n"
"v_fma_mix_f32 v190, v23, v150, v158 op_sel:[0,0,0] op_sel_hi:[1,0,0]\n"
"v_mfma_f32_16x16x32_bf16 a[32:35], a[228:231], v[8:11], a[32:35]\n"
"ds_read_b64_tr_b16 a[170:171], v219 offset:42008\n"
"buffer_load_dwordx4 v217, s[20:23], s50 offen offset:0 lds\n"
"v_mfma_f32_16x16x32_bf16 a[36:39], a[224:227], v[4:7], 0\n"
"ds_read_b64_tr_b16 a[172:173], v219 offset:45080\n"
"v_fma_mix_f32 v191, v23, v151, v159 op_sel:[1,0,0] op_sel_hi:[1,0,0]\n"
"v_mfma_f32_16x16x32_bf16 a[36:39], a[228:231], v[12:15], a[36:39]\n"
"ds_read_b64_tr_b16 a[174:175], v219 offset:46104\n"
"ds_read_b128 v[144:147], v224 offset:1024\n"
"v_mfma_f32_16x16x32_bf16 a[40:43], a[232:235], v[0:3], 0\n"
"ds_read_b64_tr_b16 a[176:177], v219 offset:41232\n"
"v_cvt_pk_bf16_f32 v20, v184, v185\n"
"v_mfma_f32_16x16x32_bf16 a[40:43], a[236:239], v[8:11], a[40:43]\n"
"ds_read_b64_tr_b16 a[178:179], v219 offset:42256\n"
"v_cvt_pk_bf16_f32 v21, v186, v187\n"
"v_mfma_f32_16x16x32_bf16 a[44:47], a[232:235], v[4:7], 0\n"
"s_add_u32 m0, s51, 74528\n"
"ds_read_b64_tr_b16 a[180:181], v219 offset:45328\n"
"ds_read_b128 v[148:151], v224 offset:1088\n"
"v_mfma_f32_16x16x32_bf16 a[44:47], a[236:239], v[12:15], a[44:47]\n"
"s_waitcnt lgkmcnt(13)\n"
"ds_read_b64_tr_b16 a[182:183], v219 offset:46352\n"
"buffer_load_dwordx4 v217, s[20:23], s50 offen offset:256 lds\n"
"v_mfma_f32_16x16x32_bf16 a[48:51], a[240:243], v[0:3], 0\n"
"s_add_u32 s50, s50, 32768\n"
"ds_read_b64_tr_b16 a[184:185], v219 offset:41240\n"
"v_cvt_pk_bf16_f32 v22, v188, v189\n"
"v_mfma_f32_16x16x32_bf16 a[48:51], a[244:247], v[8:11], a[48:51]\n"
"ds_read_b64_tr_b16 a[186:187], v219 offset:42264\n"
"ds_read_b128 v[152:155], v224 offset:1152\n"
"v_mfma_f32_16x16x32_bf16 a[52:55], a[240:243], v[4:7], 0\n"
"ds_read_b64_tr_b16 a[188:189], v219 offset:45336\n"
"v_cvt_pk_bf16_f32 v23, v190, v191\n"
"v_mfma_f32_16x16x32_bf16 a[52:55], a[244:247], v[12:15], a[52:55]\n"
"ds_read_b64_tr_b16 a[190:191], v219 offset:46360\n"
"v_fma_mix_f32 v176, v24, v160, v168 op_sel:[0,0,0] op_sel_hi:[1,0,0]\n"
"v_mfma_f32_16x16x32_bf16 a[56:59], a[248:251], v[0:3], 0\n"
"s_add_u32 m0, s51, 81960\n"
"ds_read_b64_tr_b16 a[224:225], v219 offset:41488\n"
"ds_read_b128 v[156:159], v224 offset:1216\n"
"v_mfma_f32_16x16x32_bf16 a[56:59], a[252:255], v[8:11], a[56:59]\n"
"ds_read_b64_tr_b16 a[226:227], v219 offset:42512\n"
"buffer_load_dwordx4 v217, s[20:23], s50 offen offset:0 lds\n"
"v_mfma_f32_16x16x32_bf16 a[60:63], a[248:251], v[4:7], 0\n"
"ds_read_b64_tr_b16 a[228:229], v219 offset:45584\n"
"v_fma_mix_f32 v177, v24, v161, v169 op_sel:[1,0,0] op_sel_hi:[1,0,0]\n"
"v_mfma_f32_16x16x32_bf16 a[60:63], a[252:255], v[12:15], a[60:63]\n"
"s_waitcnt lgkmcnt(14)\n"
"ds_read_b64_tr_b16 a[230:231], v219 offset:46608\n"
"v_fma_mix_f32 v178, v25, v162, v170 op_sel:[0,0,0] op_sel_hi:[1,0,0]\n"
"v_mfma_f32_16x16x32_bf16 a[64:67], a[160:163], v[0:3], 0\n"
"ds_read_b64_tr_b16 a[232:233], v219 offset:41496\n"
"v_fma_mix_f32 v179, v25, v163, v171 op_sel:[1,0,0] op_sel_hi:[1,0,0]\n"
"v_mfma_f32_16x16x32_bf16 a[64:67], a[164:167], v[8:11], a[64:67]\n"
"ds_read_b64_tr_b16 a[234:235], v219 offset:42520\n"
"v_fma_mix_f32 v180, v26, v164, v172 op_sel:[0,0,0] op_sel_hi:[1,0,0]\n"
"v_mfma_f32_16x16x32_bf16 a[68:71], a[160:163], v[4:7], 0\n"
"s_add_u32 m0, s51, 89896\n"
"ds_read_b64_tr_b16 a[236:237], v219 offset:45592\n"
"v_fma_mix_f32 v181, v26, v165, v173 op_sel:[1,0,0] op_sel_hi:[1,0,0]\n"
"v_mfma_f32_16x16x32_bf16 a[68:71], a[164:167], v[12:15], a[68:71]\n"
"ds_read_b64_tr_b16 a[238:239], v219 offset:46616\n"
"buffer_load_dwordx4 v217, s[20:23], s50 offen offset:256 lds\n"
"v_mfma_f32_16x16x32_bf16 a[72:75], a[168:171], v[0:3], 0\n"
"s_add_u32 s50, s50, 32768\n"
"ds_read_b64_tr_b16 a[240:241], v219 offset:41744\n"
"v_fma_mix_f32 v182, v27, v166, v174 op_sel:[0,0,0] op_sel_hi:[1,0,0]\n"
"v_mfma_f32_16x16x32_bf16 a[72:75], a[172:175], v[8:11], a[72:75]\n"
"ds_read_b64_tr_b16 a[242:243], v219 offset:42768\n"
"v_fma_mix_f32 v183, v27, v167, v175 op_sel:[1,0,0] op_sel_hi:[1,0,0]\n"
"v_mfma_f32_16x16x32_bf16 a[76:79], a[168:171], v[4:7], 0\n"
"ds_read_b64_tr_b16 a[244:245], v219 offset:45840\n"
"v_cvt_pk_bf16_f32 v24, v176, v177\n"
"v_mfma_f32_16x16x32_bf16 a[76:79], a[172:175], v[12:15], a[76:79]\n"
"s_waitcnt lgkmcnt(12)\n"
"ds_read_b64_tr_b16 a[246:247], v219 offset:46864\n"
"v_cvt_pk_bf16_f32 v25, v178, v179\n"
"v_mfma_f32_16x16x32_bf16 a[80:83], a[176:179], v[0:3], 0\n"
"s_add_u32 m0, s51, 82984\n"
"ds_read_b64_tr_b16 a[248:249], v219 offset:41752\n"
"v_cvt_pk_bf16_f32 v26, v180, v181\n"
"v_mfma_f32_16x16x32_bf16 a[80:83], a[180:183], v[8:11], a[80:83]\n"
"ds_read_b64_tr_b16 a[250:251], v219 offset:42776\n"
"buffer_load_dwordx4 v217, s[20:23], s50 offen offset:0 lds\n"
"v_mfma_f32_16x16x32_bf16 a[84:87], a[176:179], v[4:7], 0\n"
"ds_read_b64_tr_b16 a[252:253], v219 offset:45848\n"
"v_cvt_pk_bf16_f32 v27, v182, v183\n"
"v_mfma_f32_16x16x32_bf16 a[84:87], a[180:183], v[12:15], a[84:87]\n"
"ds_read_b64_tr_b16 a[254:255], v219 offset:46872\n"
"v_fma_mix_f32 v184, v28, v160, v168 op_sel:[0,0,0] op_sel_hi:[1,0,0]\n"
"v_mfma_f32_16x16x32_bf16 a[88:91], a[184:187], v[0:3], 0\n"
"ds_read_b64_tr_b16 a[160:161], v218 offset:0\n"
"v_fma_mix_f32 v185, v28, v161, v169 op_sel:[1,0,0] op_sel_hi:[1,0,0]\n"
"v_fma_mix_f32 v186, v29, v162, v170 op_sel:[0,0,0] op_sel_hi:[1,0,0]\n"
"v_mfma_f32_16x16x32_bf16 a[88:91], a[188:191], v[8:11], a[88:91]\n"
"ds_read_b64_tr_b16 a[162:163], v218 offset:1024\n"
"v_fma_mix_f32 v187, v29, v163, v171 op_sel:[1,0,0] op_sel_hi:[1,0,0]\n"
"v_mfma_f32_16x16x32_bf16 a[92:95], a[184:187], v[4:7], 0\n"
"s_add_u32 m0, s51, 90920\n"
"ds_read_b64_tr_b16 a[164:165], v218 offset:4096\n"
"v_fma_mix_f32 v188, v30, v164, v172 op_sel:[0,0,0] op_sel_hi:[1,0,0]\n"
"v_mfma_f32_16x16x32_bf16 a[92:95], a[188:191], v[12:15], a[92:95]\n"
"s_waitcnt lgkmcnt(11)\n"
"buffer_load_dwordx4 v217, s[20:23], s50 offen offset:256 lds\n"
"ds_read_b64_tr_b16 a[166:167], v218 offset:5120\n"
"v_mfma_f32_16x16x32_bf16 a[96:99], a[224:227], v[0:3], 0\n"
"s_add_u32 s50, s50, 425984\n"
"ds_read_b64_tr_b16 a[168:169], v218 offset:8\n"
"v_fma_mix_f32 v189, v30, v165, v173 op_sel:[1,0,0] op_sel_hi:[1,0,0]\n"
"v_mfma_f32_16x16x32_bf16 a[96:99], a[228:231], v[8:11], a[96:99]\n"
"ds_read_b64_tr_b16 a[170:171], v218 offset:1032\n"
"v_fma_mix_f32 v190, v31, v166, v174 op_sel:[0,0,0] op_sel_hi:[1,0,0]\n"
"v_mfma_f32_16x16x32_bf16 a[100:103], a[224:227], v[4:7], 0\n"
"ds_read_b64_tr_b16 a[172:173], v218 offset:4104\n"
"v_fma_mix_f32 v191, v31, v167, v175 op_sel:[1,0,0] op_sel_hi:[1,0,0]\n"
"v_mfma_f32_16x16x32_bf16 a[100:103], a[228:231], v[12:15], a[100:103]\n"
"ds_read_b64_tr_b16 a[174:175], v218 offset:5128\n"
"ds_read_b128 v[160:163], v224 offset:1280\n"
"v_mfma_f32_16x16x32_bf16 a[104:107], a[232:235], v[0:3], 0\n"
"ds_read_b64_tr_b16 a[176:177], v218 offset:256\n"
"v_cvt_pk_bf16_f32 v28, v184, v185\n"
"v_mfma_f32_16x16x32_bf16 a[104:107], a[236:239], v[8:11], a[104:107]\n"
"ds_read_b64_tr_b16 a[178:179], v218 offset:1280\n"
"v_cvt_pk_bf16_f32 v29, v186, v187\n"
"v_mfma_f32_16x16x32_bf16 a[108:111], a[232:235], v[4:7], 0\n"
"ds_read_b64_tr_b16 a[180:181], v218 offset:4352\n"
"ds_read_b128 v[164:167], v224 offset:1344\n"
"v_mfma_f32_16x16x32_bf16 a[108:111], a[236:239], v[12:15], a[108:111]\n"
"s_waitcnt lgkmcnt(13)\n"
"ds_read_b64_tr_b16 a[182:183], v218 offset:5376\n"
"v_cvt_pk_bf16_f32 v30, v188, v189\n"
"v_mfma_f32_16x16x32_bf16 a[112:115], a[240:243], v[0:3], 0\n"
"ds_read_b64_tr_b16 a[184:185], v218 offset:264\n"
"v_cvt_pk_bf16_f32 v31, v190, v191\n"
"v_mfma_f32_16x16x32_bf16 a[112:115], a[244:247], v[8:11], a[112:115]\n"
"ds_read_b64_tr_b16 a[186:187], v218 offset:1288\n"
"ds_read_b128 v[168:171], v224 offset:1408\n"
"v_mfma_f32_16x16x32_bf16 a[116:119], a[240:243], v[4:7], 0\n"
"ds_read_b64_tr_b16 a[188:189], v218 offset:4360\n"
"v_fma_mix_f32 v176, v32, v144, v152 op_sel:[0,0,0] op_sel_hi:[1,0,0]\n"
"v_mfma_f32_16x16x32_bf16 a[116:119], a[244:247], v[12:15], a[116:119]\n"
"ds_read_b64_tr_b16 a[190:191], v218 offset:5384\n"
"v_fma_mix_f32 v177, v32, v145, v153 op_sel:[1,0,0] op_sel_hi:[1,0,0]\n"
"v_mfma_f32_16x16x32_bf16 a[120:123], a[248:251], v[0:3], 0\n"
"ds_read_b64_tr_b16 a[224:225], v218 offset:512\n"
"ds_read_b128 v[172:175], v224 offset:1472\n"
"v_mfma_f32_16x16x32_bf16 a[120:123], a[252:255], v[8:11], a[120:123]\n"
"ds_read_b64_tr_b16 a[226:227], v218 offset:1536\n"
"v_fma_mix_f32 v178, v33, v146, v154 op_sel:[0,0,0] op_sel_hi:[1,0,0]\n"
"v_mfma_f32_16x16x32_bf16 a[124:127], a[248:251], v[4:7], 0\n"
"ds_read_b64_tr_b16 a[228:229], v218 offset:4608\n"
"v_fma_mix_f32 v179, v33, v147, v155 op_sel:[1,0,0] op_sel_hi:[1,0,0]\n"
"v_mfma_f32_16x16x32_bf16 a[124:127], a[252:255], v[12:15], a[124:127]\n"
"s_waitcnt lgkmcnt(14)\n"
"ds_read_b64_tr_b16 a[230:231], v218 offset:5632\n"
"v_fma_mix_f32 v180, v34, v148, v156 op_sel:[0,0,0] op_sel_hi:[1,0,0]\n"
"v_mfma_f32_16x16x32_bf16 a[0:3], a[160:163], v[16:19], a[0:3]\n"
"ds_read_b64_tr_b16 a[232:233], v218 offset:520\n"
"v_fma_mix_f32 v181, v34, v149, v157 op_sel:[1,0,0] op_sel_hi:[1,0,0]\n"
"v_mfma_f32_16x16x32_bf16 a[0:3], a[164:167], v[24:27], a[0:3]\n"
"ds_read_b64_tr_b16 a[234:235], v218 offset:1544\n"
"v_fma_mix_f32 v182, v35, v150, v158 op_sel:[0,0,0] op_sel_hi:[1,0,0]\n"
"v_mfma_f32_16x16x32_bf16 a[4:7], a[160:163], v[20:23], a[4:7]\n"
"s_waitcnt vmcnt(8)\n"
"s_barrier\n"
"ds_read_b64_tr_b16 a[236:237], v218 offset:4616\n"
"v_fma_mix_f32 v183, v35, v151, v159 op_sel:[1,0,0] op_sel_hi:[1,0,0]\n"
"v_mfma_f32_16x16x32_bf16 a[4:7], a[164:167], v[28:31], a[4:7]\n"
"ds_read_b64_tr_b16 a[238:239], v218 offset:5640\n"
"v_cvt_pk_bf16_f32 v32, v176, v177\n"
"v_mfma_f32_16x16x32_bf16 a[8:11], a[168:171], v[16:19], a[8:11]\n"
"s_add_u32 m0, s51, 98352\n"
"ds_read_b64_tr_b16 a[240:241], v218 offset:768\n"
"v_cvt_pk_bf16_f32 v33, v178, v179\n"
"v_mfma_f32_16x16x32_bf16 a[8:11], a[172:175], v[24:27], a[8:11]\n"
"ds_read_b64_tr_b16 a[242:243], v218 offset:1792\n"
"buffer_load_dwordx4 v217, s[20:23], s50 offen offset:0 lds\n"
"v_mfma_f32_16x16x32_bf16 a[12:15], a[168:171], v[20:23], a[12:15]\n"
"ds_read_b64_tr_b16 a[244:245], v218 offset:4864\n"
"v_cvt_pk_bf16_f32 v34, v180, v181\n"
"v_mfma_f32_16x16x32_bf16 a[12:15], a[172:175], v[28:31], a[12:15]\n"
"s_waitcnt lgkmcnt(12)\n"
"ds_read_b64_tr_b16 a[246:247], v218 offset:5888\n"
"v_cvt_pk_bf16_f32 v35, v182, v183\n"
"v_mfma_f32_16x16x32_bf16 a[16:19], a[176:179], v[16:19], a[16:19]\n"
"ds_read_b64_tr_b16 a[248:249], v218 offset:776\n"
"v_fma_mix_f32 v184, v36, v144, v152 op_sel:[0,0,0] op_sel_hi:[1,0,0]\n"
"v_mfma_f32_16x16x32_bf16 a[16:19], a[180:183], v[24:27], a[16:19]\n"
"ds_read_b64_tr_b16 a[250:251], v218 offset:1800\n"
"v_fma_mix_f32 v185, v36, v145, v153 op_sel:[1,0,0] op_sel_hi:[1,0,0]\n"
"v_mfma_f32_16x16x32_bf16 a[20:23], a[176:179], v[20:23], a[20:23]\n"
"s_add_u32 m0, s51, 106288\n"
"ds_read_b64_tr_b16 a[252:253], v218 offset:4872\n"
"v_fma_mix_f32 v186, v37, v146, v154 op_sel:[0,0,0] op_sel_hi:[1,0,0]\n"
"v_mfma_f32_16x16x32_bf16 a[20:23], a[180:183], v[28:31], a[20:23]\n"
"ds_read_b64_tr_b16 a[254:255], v218 offset:5896\n"
"buffer_load_dwordx4 v217, s[20:23], s50 offen offset:256 lds\n"
"v_mfma_f32_16x16x32_bf16 a[24:27], a[184:187], v[16:19], a[24:27]\n"
"s_add_u32 s50, s50, 32768\n"
"ds_read_b64_tr_b16 a[160:161], v218 offset:8192\n"
"v_fma_mix_f32 v187, v37, v147, v155 op_sel:[1,0,0] op_sel_hi:[1,0,0]\n"
"v_mfma_f32_16x16x32_bf16 a[24:27], a[188:191], v[24:27], a[24:27]\n"
"ds_read_b64_tr_b16 a[162:163], v218 offset:9216\n"
"v_fma_mix_f32 v188, v38, v148, v156 op_sel:[0,0,0] op_sel_hi:[1,0,0]\n"
"v_mfma_f32_16x16x32_bf16 a[28:31], a[184:187], v[20:23], a[28:31]\n"
"ds_read_b64_tr_b16 a[164:165], v218 offset:12288\n"
"v_fma_mix_f32 v189, v38, v149, v157 op_sel:[1,0,0] op_sel_hi:[1,0,0]\n"
"v_mfma_f32_16x16x32_bf16 a[28:31], a[188:191], v[28:31], a[28:31]\n"
"s_waitcnt lgkmcnt(11)\n"
"ds_read_b64_tr_b16 a[166:167], v218 offset:13312\n"
"v_fma_mix_f32 v190, v39, v150, v158 op_sel:[0,0,0] op_sel_hi:[1,0,0]\n"
"v_mfma_f32_16x16x32_bf16 a[32:35], a[224:227], v[16:19], a[32:35]\n"
"s_add_u32 m0, s51, 99376\n"
"ds_read_b64_tr_b16 a[168:169], v218 offset:8200\n"
"v_fma_mix_f32 v191, v39, v151, v159 op_sel:[1,0,0] op_sel_hi:[1,0,0]\n"
"v_mfma_f32_16x16x32_bf16 a[32:35], a[228:231], v[24:27], a[32:35]\n"
"s_and_b32 s62, s42, 7\n"
"ds_read_b64_tr_b16 a[170:171], v218 offset:9224\n"
"buffer_load_dwordx4 v217, s[20:23], s50 offen offset:0 lds\n"
"v_mfma_f32_16x16x32_bf16 a[36:39], a[224:227], v[20:23], a[36:39]\n"
"ds_read_b64_tr_b16 a[172:173], v218 offset:12296\n"
"ds_read_b128 v[144:147], v224 offset:1536\n"
"v_mfma_f32_16x16x32_bf16 a[36:39], a[228:231], v[28:31], a[36:39]\n"
"s_mul_i32 s46, s62, 663552\n"
"ds_read_b64_tr_b16 a[174:175], v218 offset:13320\n"
"v_cvt_pk_bf16_f32 v36, v184, v185\n"
"v_mfma_f32_16x16x32_bf16 a[40:43], a[232:235], v[16:19], a[40:43]\n"
"ds_read_b64_tr_b16 a[176:177], v218 offset:8448\n"
"v_cvt_pk_bf16_f32 v37, v186, v187\n"
"v_mfma_f32_16x16x32_bf16 a[40:43], a[236:239], v[24:27], a[40:43]\n"
"s_lshr_b32 s47, s43, 2\n"
"ds_read_b64_tr_b16 a[178:179], v218 offset:9472\n"
"ds_read_b128 v[148:151], v224 offset:1600\n"
"v_mfma_f32_16x16x32_bf16 a[44:47], a[232:235], v[20:23], a[44:47]\n"
"s_add_u32 m0, s51, 107312\n"
"ds_read_b64_tr_b16 a[180:181], v218 offset:12544\n"
"v_cvt_pk_bf16_f32 v38, v188, v189\n"
"v_mfma_f32_16x16x32_bf16 a[44:47], a[236:239], v[28:31], a[44:47]\n"
"s_waitcnt lgkmcnt(13)\n"
"s_mul_i32 s47, s47, 1179648\n"
"ds_read_b64_tr_b16 a[182:183], v218 offset:13568\n"
"buffer_load_dwordx4 v217, s[20:23], s50 offen offset:256 lds\n"
"v_mfma_f32_16x16x32_bf16 a[48:51], a[240:243], v[16:19], a[48:51]\n"
"s_add_u32 s50, s50, 32768\n"
"ds_read_b64_tr_b16 a[184:185], v218 offset:8456\n"
"ds_read_b128 v[152:155], v224 offset:1664\n"
"v_mfma_f32_16x16x32_bf16 a[48:51], a[244:247], v[24:27], a[48:51]\n"
"s_getpc_b64 s[32:33]\n"
"AT_pc1:\n"
"s_sub_u32 s32, s32, AT_pc1-AT_start\n"
"s_subb_u32 s33, s33, 0\n"
"ds_read_b64_tr_b16 a[186:187], v218 offset:9480\n"
"v_cvt_pk_bf16_f32 v39, v190, v191\n"
"v_mfma_f32_16x16x32_bf16 a[52:55], a[240:243], v[20:23], a[52:55]\n"
"ds_read_b64_tr_b16 a[188:189], v218 offset:12552\n"
"v_fma_mix_f32 v176, v40, v160, v168 op_sel:[0,0,0] op_sel_hi:[1,0,0]\n"
"v_mfma_f32_16x16x32_bf16 a[52:55], a[244:247], v[28:31], a[52:55]\n"
"s_and_b32 s33, s33, 0xffff\n"
"ds_read_b64_tr_b16 a[190:191], v218 offset:13576\n"
"ds_read_b128 v[156:159], v224 offset:1728\n"
"v_mfma_f32_16x16x32_bf16 a[56:59], a[248:251], v[16:19], a[56:59]\n"
"s_add_u32 m0, s51, 114744\n"
"ds_read_b64_tr_b16 a[224:225], v218 offset:8704\n"
"v_fma_mix_f32 v177, v40, v161, v169 op_sel:[1,0,0] op_sel_hi:[1,0,0]\n"
"v_mfma_f32_16x16x32_bf16 a[56:59], a[252:255], v[24:27], a[56:59]\n"
"s_mov_b32 s34, AT_kend-AT_start\n"
"ds_read_b64_tr_b16 a[226:227], v218 offset:9728\n"
"buffer_load_dwordx4 v217, s[20:23], s50 offen offset:0 lds\n"
"v_mfma_f32_16x16x32_bf16 a[60:63], a[248:251], v[20:23], a[60:63]\n"
"ds_read_b64_tr_b16 a[228:229], v218 offset:12800\n"
"v_fma_mix_f32 v178, v41, v162, v170 op_sel:[0,0,0] op_sel_hi:[1,0,0]\n"
"v_mfma_f32_16x16x32_bf16 a[60:63], a[252:255], v[28:31], a[60:63]\n"
"s_waitcnt lgkmcnt(14)\n"
"s_mov_b32 s35, 0x20000\n"
"ds_read_b64_tr_b16 a[230:231], v218 offset:13824\n"
"v_fma_mix_f32 v179, v41, v163, v171 op_sel:[1,0,0] op_sel_hi:[1,0,0]\n"
"v_mfma_f32_16x16x32_bf16 a[64:67], a[160:163], v[16:19], a[64:67]\n"
"ds_read_b64_tr_b16 a[232:233], v218 offset:8712\n"
"v_fma_mix_f32 v180, v42, v164, v172 op_sel:[0,0,0] op_sel_hi:[1,0,0]\n"
"v_mfma_f32_16x16x32_bf16 a[64:67], a[164:167], v[24:27], a[64:67]\n"
"s_lshr_b32 s62, s42, 3\n"
"ds_read_b64_tr_b16 a[234:235], v218 offset:9736\n"
"v_fma_mix_f32 v181, v42, v165, v173 op_sel:[1,0,0] op_sel_hi:[1,0,0]\n"
"v_mfma_f32_16x16x32_bf16 a[68:71], a[160:163], v[20:23], a[68:71]\n"
"s_add_u32 m0, s51, 122680\n"
"ds_read_b64_tr_b16 a[236:237], v218 offset:12808\n"
"v_fma_mix_f32 v182, v43, v166, v174 op_sel:[0,0,0] op_sel_hi:[1,0,0]\n"
"v_mfma_f32_16x16x32_bf16 a[68:71], a[164:167], v[28:31], a[68:71]\n"
"s_lshl_b32 s62, s62, 2\n"
"ds_read_b64_tr_b16 a[238:239], v218 offset:13832\n"
"buffer_load_dwordx4 v217, s[20:23], s50 offen offset:256 lds\n"
"v_mfma_f32_16x16x32_bf16 a[72:75], a[168:171], v[16:19], a[72:75]\n"
"s_add_u32 s50, s50, 32768\n"
"ds_read_b64_tr_b16 a[240:241], v218 offset:8960\n"
"v_fma_mix_f32 v183, v43, v167, v175 op_sel:[1,0,0] op_sel_hi:[1,0,0]\n"
"v_mfma_f32_16x16x32_bf16 a[72:75], a[172:175], v[24:27], a[72:75]\n"
"s_add_u32 s62, s62, s41\n"
"ds_read_b64_tr_b16 a[242:243], v218 offset:9984\n"
"v_cvt_pk_bf16_f32 v40, v176, v177\n"
"v_mfma_f32_16x16x32_bf16 a[76:79], a[168:171], v[20:23], a[76:79]\n"
"ds_read_b64_tr_b16 a[244:245], v218 offset:13056\n"
"v_cvt_pk_bf16_f32 v41, v178, v179\n"
"v_mfma_f32_16x16x32_bf16 a[76:79], a[172:175], v[28:31], a[76:79]\n"
"s_waitcnt lgkmcnt(11)\n"
"s_and_b32 s62, s62, 31\n"
"ds_read_b64_tr_b16 a[246:247], v218 offset:14080\n"
"v_cvt_pk_bf16_f32 v42, v180, v181\n"
"v_mfma_f32_16x16x32_bf16 a[80:83], a[176:179], v[16:19], a[80:83]\n"
"s_add_u32 m0, s51, 115768\n"
"ds_read_b64_tr_b16 a[248:249], v218 offset:8968\n"
"v_cvt_pk_bf16_f32 v43, v182, v183\n"
"v_mfma_f32_16x16x32_bf16 a[80:83], a[180:183], v[24:27], a[80:83]\n"
"s_lshl_b32 s48, s62, 12\n"
"ds_read_b64_tr_b16 a[250:251], v218 offset:9992\n"
"buffer_load_dwordx4 v217, s[20:23], s50 offen offset:0 lds\n"
"v_mfma_f32_16x16x32_bf16 a[84:87], a[176:179], v[20:23], a[84:87]\n"
"ds_read_b64_tr_b16 a[252:253], v218 offset:13064\n"
"v_fma_mix_f32 v184, v44, v160, v168 op_sel:[0,0,0] op_sel_hi:[1,0,0]\n"
"v_mfma_f32_16x16x32_bf16 a[84:87], a[180:183], v[28:31], a[84:87]\n"
"s_lshl_b32 s54, s44, 2\n"
"ds_read_b64_tr_b16 a[254:255], v218 offset:14088\n"
"v_fma_mix_f32 v185, v44, v161, v169 op_sel:[1,0,0] op_sel_hi:[1,0,0]\n"
"v_mfma_f32_16x16x32_bf16 a[88:91], a[184:187], v[16:19], a[88:91]\n"
"ds_read_b64_tr_b16 a[160:161], v218 offset:32784\n"
"v_fma_mix_f32 v186, v45, v162, v170 op_sel:[0,0,0] op_sel_hi:[1,0,0]\n"
"v_fma_mix_f32 v187, v45, v163, v171 op_sel:[1,0,0] op_sel_hi:[1,0,0]\n"
"v_mfma_f32_16x16x32_bf16 a[88:91], a[188:191], v[24:27], a[88:91]\n"
"s_add_u32 s54, s54, s41\n"
"ds_read_b64_tr_b16 a[162:163], v218 offset:33808\n"
"v_fma_mix_f32 v188, v46, v164, v172 op_sel:[0,0,0] op_sel_hi:[1,0,0]\n"
"v_mfma_f32_16x16x32_bf16 a[92:95], a[184:187], v[20:23], a[92:95]\n"
"s_add_u32 m0, s51, 123704\n"
"ds_read_b64_tr_b16 a[164:165], v218 offset:36880\n"
"v_fma_mix_f32 v189, v46, v165, v173 op_sel:[1,0,0] op_sel_hi:[1,0,0]\n"
"v_mfma_f32_16x16x32_bf16 a[92:95], a[188:191], v[28:31], a[92:95]\n"
"s_waitcnt lgkmcnt(11)\n"
"s_mul_i32 s54, s54, 36864\n"
"buffer_load_dwordx4 v217, s[20:23], s50 offen offset:256 lds\n"
"ds_read_b64_tr_b16 a[166:167], v218 offset:37904\n"
"v_mfma_f32_16x16x32_bf16 a[96:99], a[224:227], v[16:19], a[96:99]\n"
"s_add_u32 s50, s50, 425984\n"
"ds_read_b64_tr_b16 a[168:169], v218 offset:32792\n"
"v_fma_mix_f32 v190, v47, v166, v174 op_sel:[0,0,0] op_sel_hi:[1,0,0]\n"
"v_mfma_f32_16x16x32_bf16 a[96:99], a[228:231], v[24:27], a[96:99]\n"
"s_lshr_b32 s62, s43, 2\n"
"ds_read_b64_tr_b16 a[170:171], v218 offset:33816\n"
"v_fma_mix_f32 v191, v47, v167, v175 op_sel:[1,0,0] op_sel_hi:[1,0,0]\n"
"v_mfma_f32_16x16x32_bf16 a[100:103], a[224:227], v[20:23], a[100:103]\n"
"ds_read_b64_tr_b16 a[172:173], v218 offset:36888\n"
"ds_read_b128 v[160:163], v224 offset:1792\n"
"v_mfma_f32_16x16x32_bf16 a[100:103], a[228:231], v[28:31], a[100:103]\n"
"s_mul_i32 s62, s62, 2359296\n"
"ds_read_b64_tr_b16 a[174:175], v218 offset:37912\n"
"v_cvt_pk_bf16_f32 v44, v184, v185\n"
"v_mfma_f32_16x16x32_bf16 a[104:107], a[232:235], v[16:19], a[104:107]\n"
"ds_read_b64_tr_b16 a[176:177], v218 offset:33040\n"
"v_cvt_pk_bf16_f32 v45, v186, v187\n"
"v_mfma_f32_16x16x32_bf16 a[104:107], a[236:239], v[24:27], a[104:107]\n"
"s_add_u32 s54, s54, s62\n"
"ds_read_b64_tr_b16 a[178:179], v218 offset:34064\n"
"ds_read_b128 v[164:167], v224 offset:1856\n"
"v_mfma_f32_16x16x32_bf16 a[108:111], a[232:235], v[20:23], a[108:111]\n"
"ds_read_b64_tr_b16 a[180:181], v218 offset:37136\n"
"v_cvt_pk_bf16_f32 v46, v188, v189\n"
"v_mfma_f32_16x16x32_bf16 a[108:111], a[236:239], v[28:31], a[108:111]\n"
"s_waitcnt lgkmcnt(13)\n"
"s_add_u32 s54, s54, 139264\n"
"ds_read_b64_tr_b16 a[182:183], v218 offset:38160\n"
"v_cvt_pk_bf16_f32 v47, v190, v191\n"
"v_mfma_f32_16x16x32_bf16 a[112:115], a[240:243], v[16:19], a[112:115]\n"
"ds_read_b64_tr_b16 a[184:185], v218 offset:33048\n"
"ds_read_b128 v[168:171], v224 offset:1920\n"
"v_mfma_f32_16x16x32_bf16 a[112:115], a[244:247], v[24:27], a[112:115]\n"
"s_bfe_u32 s62, s43, 0x10001\n"
"ds_read_b64_tr_b16 a[186:187], v218 offset:34072\n"
"v_fma_mix_f32 v176, v48, v144, v152 op_sel:[0,0,0] op_sel_hi:[1,0,0]\n"
"v_mfma_f32_16x16x32_bf16 a[116:119], a[240:243], v[20:23], a[116:119]\n"
"ds_read_b64_tr_b16 a[188:189], v218 offset:37144\n"
"v_fma_mix_f32 v177, v48, v145, v153 op_sel:[1,0,0] op_sel_hi:[1,0,0]\n"
"v_mfma_f32_16x16x32_bf16 a[116:119], a[244:247], v[28:31], a[116:119]\n"
"s_mul_i32 s62, s62, 9216\n"
"ds_read_b64_tr_b16 a[190:191], v218 offset:38168\n"
"ds_read_b128 v[172:175], v224 offset:1984\n"
"v_mfma_f32_16x16x32_bf16 a[120:123], a[248:251], v[16:19], a[120:123]\n"
"ds_read_b64_tr_b16 a[224:225], v218 offset:33296\n"
"v_fma_mix_f32 v178, v49, v146, v154 op_sel:[0,0,0] op_sel_hi:[1,0,0]\n"
"v_mfma_f32_16x16x32_bf16 a[120:123], a[252:255], v[24:27], a[120:123]\n"
"s_add_u32 s54, s54, s62\n"
"ds_read_b64_tr_b16 a[226:227], v218 offset:34320\n"
"v_fma_mix_f32 v179, v49, v147, v155 op_sel:[1,0,0] op_sel_hi:[1,0,0]\n"
"v_mfma_f32_16x16x32_bf16 a[124:127], a[248:251], v[20:23], a[124:127]\n"
"ds_read_b64_tr_b16 a[228:229], v218 offset:37392\n"
"v_fma_mix_f32 v180, v50, v148, v156 op_sel:[0,0,0] op_sel_hi:[1,0,0]\n"
"v_mfma_f32_16x16x32_bf16 a[124:127], a[252:255], v[28:31], a[124:127]\n"
"s_waitcnt lgkmcnt(14)\n"
"s_and_b32 s62, s43, 1\n"
"ds_read_b64_tr_b16 a[230:231], v218 offset:38416\n"
"v_fma_mix_f32 v181, v50, v149, v157 op_sel:[1,0,0] op_sel_hi:[1,0,0]\n"
"v_mfma_f32_16x16x32_bf16 a[0:3], a[160:163], v[32:35], a[0:3]\n"
"ds_read_b64_tr_b16 a[232:233], v218 offset:33304\n"
"v_fma_mix_f32 v182, v51, v150, v158 op_sel:[0,0,0] op_sel_hi:[1,0,0]\n"
"v_mfma_f32_16x16x32_bf16 a[0:3], a[164:167], v[40:43], a[0:3]\n"
"s_lshl_b32 s49, s62, 3\n"
"ds_read_b64_tr_b16 a[234:235], v218 offset:34328\n"
"v_fma_mix_f32 v183, v51, v151, v159 op_sel:[1,0,0] op_sel_hi:[1,0,0]\n"
"v_mfma_f32_16x16x32_bf16 a[4:7], a[160:163], v[36:39], a[4:7]\n"
"s_waitcnt vmcnt(8)\n"
"s_barrier\n"
"ds_read_b64_tr_b16 a[236:237], v218 offset:37400\n"
"v_cvt_pk_bf16_f32 v48, v176, v177\n"
"v_mfma_f32_16x16x32_bf16 a[4:7], a[164:167], v[44:47], a[4:7]\n"
"s_add_u32 s49, s49, s54\n"
"ds_read_b64_tr_b16 a[238:239], v218 offset:38424\n"
"v_cvt_pk_bf16_f32 v49, v178, v179\n"
"v_mfma_f32_16x16x32_bf16 a[8:11], a[168:171], v[32:35], a[8:11]\n"
"s_mov_b32 m0, s51\n"
"ds_read_b64_tr_b16 a[240:241], v218 offset:33552\n"
"v_cvt_pk_bf16_f32 v50, v180, v181\n"
"v_mfma_f32_16x16x32_bf16 a[8:11], a[172:175], v[40:43], a[8:11]\n"
"s_add_u32 s49, s49, 4096\n"
"ds_read_b64_tr_b16 a[242:243], v218 offset:34576\n"
"buffer_load_dwordx4 v217, s[20:23], s50 offen offset:0 lds\n"
"v_mfma_f32_16x16x32_bf16 a[12:15], a[168:171], v[36:39], a[12:15]\n"
"ds_read_b64_tr_b16 a[244:245], v218 offset:37648\n"
"v_cvt_pk_bf16_f32 v51, v182, v183\n"
"v_mfma_f32_16x16x32_bf16 a[12:15], a[172:175], v[44:47], a[12:15]\n"
"s_waitcnt lgkmcnt(11)\n"
"s_mul_i32 s62, s62, 5120\n"
"ds_read_b64_tr_b16 a[246:247], v218 offset:38672\n"
"v_fma_mix_f32 v184, v52, v144, v152 op_sel:[0,0,0] op_sel_hi:[1,0,0]\n"
"v_mfma_f32_16x16x32_bf16 a[16:19], a[176:179], v[32:35], a[16:19]\n"
"ds_read_b64_tr_b16 a[248:249], v218 offset:33560\n"
"v_fma_mix_f32 v185, v52, v145, v153 op_sel:[1,0,0] op_sel_hi:[1,0,0]\n"
"v_mfma_f32_16x16x32_bf16 a[16:19], a[180:183], v[40:43], a[16:19]\n"
"s_add_u32 s54, s54, s62\n"
"ds_read_b64_tr_b16 a[250:251], v218 offset:34584\n"
"v_fma_mix_f32 v186, v53, v146, v154 op_sel:[0,0,0] op_sel_hi:[1,0,0]\n"
"v_mfma_f32_16x16x32_bf16 a[20:23], a[176:179], v[36:39], a[20:23]\n"
"s_add_u32 m0, s51, 7936\n"
"ds_read_b64_tr_b16 a[252:253], v218 offset:37656\n"
"v_fma_mix_f32 v187, v53, v147, v155 op_sel:[1,0,0] op_sel_hi:[1,0,0]\n"
"v_mfma_f32_16x16x32_bf16 a[20:23], a[180:183], v[44:47], a[20:23]\n"
"s_and_b32 s62, s43, 1\n"
"ds_read_b64_tr_b16 a[254:255], v218 offset:38680\n"
"buffer_load_dwordx4 v217, s[20:23], s50 offen offset:256 lds\n"
"v_mfma_f32_16x16x32_bf16 a[24:27], a[184:187], v[32:35], a[24:27]\n"
"s_add_u32 s50, s50, 32768\n"
"ds_read_b64_tr_b16 a[160:161], v218 offset:40976\n"
"v_fma_mix_f32 v188, v54, v148, v156 op_sel:[0,0,0] op_sel_hi:[1,0,0]\n"
"v_mfma_f32_16x16x32_bf16 a[24:27], a[188:191], v[40:43], a[24:27]\n"
"s_lshl_b32 s62, s62, 10\n"
"ds_read_b64_tr_b16 a[162:163], v218 offset:42000\n"
"v_fma_mix_f32 v189, v54, v149, v157 op_sel:[1,0,0] op_sel_hi:[1,0,0]\n"
"v_mfma_f32_16x16x32_bf16 a[28:31], a[184:187], v[36:39], a[28:31]\n"
"ds_read_b64_tr_b16 a[164:165], v218 offset:45072\n"
"v_fma_mix_f32 v190, v55, v150, v158 op_sel:[0,0,0] op_sel_hi:[1,0,0]\n"
"v_mfma_f32_16x16x32_bf16 a[28:31], a[188:191], v[44:47], a[28:31]\n"
"s_waitcnt lgkmcnt(11)\n"
"s_sub_u32 s54, s54, s62\n"
"ds_read_b64_tr_b16 a[166:167], v218 offset:46096\n"
"v_fma_mix_f32 v191, v55, v151, v159 op_sel:[1,0,0] op_sel_hi:[1,0,0]\n"
"v_mfma_f32_16x16x32_bf16 a[32:35], a[224:227], v[32:35], a[32:35]\n"
"s_add_u32 m0, s51, 1024\n"
"ds_read_b64_tr_b16 a[168:169], v218 offset:40984\n"
"ds_read_b128 v[144:147], v224 offset:2048\n"
"v_mfma_f32_16x16x32_bf16 a[32:35], a[228:231], v[40:43], a[32:35]\n"
"s_add_u32 s49, s54, 4096\n"
"ds_read_b64_tr_b16 a[170:171], v218 offset:42008\n"
"buffer_load_dwordx4 v217, s[20:23], s50 offen offset:0 lds\n"
"v_mfma_f32_16x16x32_bf16 a[36:39], a[224:227], v[36:39], a[36:39]\n"
"ds_read_b64_tr_b16 a[172:173], v218 offset:45080\n"
"v_cvt_pk_bf16_f32 v52, v184, v185\n"
"v_mfma_f32_16x16x32_bf16 a[36:39], a[228:231], v[44:47], a[36:39]\n"
"ds_read_b64_tr_b16 a[174:175], v218 offset:46104\n"
"ds_read_b128 v[148:151], v224 offset:2112\n"
"v_mfma_f32_16x16x32_bf16 a[40:43], a[232:235], v[32:35], a[40:43]\n"
"ds_read_b64_tr_b16 a[176:177], v218 offset:41232\n"
"v_cvt_pk_bf16_f32 v53, v186, v187\n"
"v_mfma_f32_16x16x32_bf16 a[40:43], a[236:239], v[40:43], a[40:43]\n"
"ds_read_b64_tr_b16 a[178:179], v218 offset:42256\n"
"v_cvt_pk_bf16_f32 v54, v188, v189\n"
"v_mfma_f32_16x16x32_bf16 a[44:47], a[232:235], v[36:39], a[44:47]\n"
"s_add_u32 m0, s51, 8960\n"
"ds_read_b64_tr_b16 a[180:181], v218 offset:45328\n"
"ds_read_b128 v[152:155], v224 offset:2176\n"
"v_mfma_f32_16x16x32_bf16 a[44:47], a[236:239], v[44:47], a[44:47]\n"
"s_waitcnt lgkmcnt(14)\n"
"ds_read_b64_tr_b16 a[182:183], v218 offset:46352\n"
"buffer_load_dwordx4 v217, s[20:23], s50 offen offset:256 lds\n"
"v_mfma_f32_16x16x32_bf16 a[48:51], a[240:243], v[32:35], a[48:51]\n"
"s_add_u32 s50, s50, 32768\n"
"ds_read_b64_tr_b16 a[184:185], v218 offset:41240\n"
"v_cvt_pk_bf16_f32 v55, v190, v191\n"
"v_mfma_f32_16x16x32_bf16 a[48:51], a[244:247], v[40:43], a[48:51]\n"
"ds_read_b64_tr_b16 a[186:187], v218 offset:42264\n"
"ds_read_b128 v[156:159], v224 offset:2240\n"
"v_mfma_f32_16x16x32_bf16 a[52:55], a[240:243], v[36:39], a[52:55]\n"
"ds_read_b64_tr_b16 a[188:189], v218 offset:45336\n"
"v_fma_mix_f32 v176, v56, v160, v168 op_sel:[0,0,0] op_sel_hi:[1,0,0]\n"
"v_mfma_f32_16x16x32_bf16 a[52:55], a[244:247], v[44:47], a[52:55]\n"
"ds_read_b64_tr_b16 a[190:191], v218 offset:46360\n"
"v_fma_mix_f32 v177, v56, v161, v169 op_sel:[1,0,0] op_sel_hi:[1,0,0]\n"
"v_mfma_f32_16x16x32_bf16 a[56:59], a[248:251], v[32:35], a[56:59]\n"
"s_add_u32 m0, s51, 16392\n"
"ds_read_b64_tr_b16 a[224:225], v218 offset:41488\n"
"v_fma_mix_f32 v178, v57, v162, v170 op_sel:[0,0,0] op_sel_hi:[1,0,0]\n"
"v_mfma_f32_16x16x32_bf16 a[56:59], a[252:255], v[40:43], a[56:59]\n"
"ds_read_b64_tr_b16 a[226:227], v218 offset:42512\n"
"buffer_load_dwordx4 v217, s[20:23], s50 offen offset:0 lds\n"
"v_mfma_f32_16x16x32_bf16 a[60:63], a[248:251], v[36:39], a[60:63]\n"
"ds_read_b64_tr_b16 a[228:229], v218 offset:45584\n"
"v_fma_mix_f32 v179, v57, v163, v171 op_sel:[1,0,0] op_sel_hi:[1,0,0]\n"
"v_mfma_f32_16x16x32_bf16 a[60:63], a[252:255], v[44:47], a[60:63]\n"
"s_waitcnt lgkmcnt(13)\n"
"ds_read_b64_tr_b16 a[230:231], v218 offset:46608\n"
"v_fma_mix_f32 v180, v58, v164, v172 op_sel:[0,0,0] op_sel_hi:[1,0,0]\n"
"v_mfma_f32_16x16x32_bf16 a[64:67], a[160:163], v[32:35], a[64:67]\n"
"ds_read_b64_tr_b16 a[232:233], v218 offset:41496\n"
"v_fma_mix_f32 v181, v58, v165, v173 op_sel:[1,0,0] op_sel_hi:[1,0,0]\n"
"v_mfma_f32_16x16x32_bf16 a[64:67], a[164:167], v[40:43], a[64:67]\n"
"ds_read_b64_tr_b16 a[234:235], v218 offset:42520\n"
"v_fma_mix_f32 v182, v59, v166, v174 op_sel:[0,0,0] op_sel_hi:[1,0,0]\n"
"v_mfma_f32_16x16x32_bf16 a[68:71], a[160:163], v[36:39], a[68:71]\n"
"s_add_u32 m0, s51, 24328\n"
"ds_read_b64_tr_b16 a[236:237], v218 offset:45592\n"
"v_fma_mix_f32 v183, v59, v167, v175 op_sel:[1,0,0] op_sel_hi:[1,0,0]\n"
"v_mfma_f32_16x16x32_bf16 a[68:71], a[164:167], v[44:47], a[68:71]\n"
"ds_read_b64_tr_b16 a[238:239], v218 offset:46616\n"
"buffer_load_dwordx4 v217, s[20:23], s50 offen offset:256 lds\n"
"v_mfma_f32_16x16x32_bf16 a[72:75], a[168:171], v[32:35], a[72:75]\n"
"s_add_u32 s50, s50, 32768\n"
"ds_read_b64_tr_b16 a[240:241], v218 offset:41744\n"
"v_cvt_pk_bf16_f32 v56, v176, v177\n"
"v_mfma_f32_16x16x32_bf16 a[72:75], a[172:175], v[40:43], a[72:75]\n"
"ds_read_b64_tr_b16 a[242:243], v218 offset:42768\n"
"v_cvt_pk_bf16_f32 v57, v178, v179\n"
"v_mfma_f32_16x16x32_bf16 a[76:79], a[168:171], v[36:39], a[76:79]\n"
"ds_read_b64_tr_b16 a[244:245], v218 offset:45840\n"
"v_cvt_pk_bf16_f32 v58, v180, v181\n"
"v_mfma_f32_16x16x32_bf16 a[76:79], a[172:175], v[44:47], a[76:79]\n"
"s_waitcnt lgkmcnt(11)\n"
"ds_read_b64_tr_b16 a[246:247], v218 offset:46864\n"
"v_cvt_pk_bf16_f32 v59, v182, v183\n"
"v_mfma_f32_16x16x32_bf16 a[80:83], a[176:179], v[32:35], a[80:83]\n"
"s_add_u32 m0, s51, 17416\n"
"ds_read_b64_tr_b16 a[248:249], v218 offset:41752\n"
"v_fma_mix_f32 v184, v60, v160, v168 op_sel:[0,0,0] op_sel_hi:[1,0,0]\n"
"v_mfma_f32_16x16x32_bf16 a[80:83], a[180:183], v[40:43], a[80:83]\n"
"ds_read_b64_tr_b16 a[250:251], v218 offset:42776\n"
"buffer_load_dwordx4 v217, s[20:23], s50 offen offset:0 lds\n"
"v_mfma_f32_16x16x32_bf16 a[84:87], a[176:179], v[36:39], a[84:87]\n"
"ds_read_b64_tr_b16 a[252:253], v218 offset:45848\n"
"v_fma_mix_f32 v185, v60, v161, v169 op_sel:[1,0,0] op_sel_hi:[1,0,0]\n"
"v_mfma_f32_16x16x32_bf16 a[84:87], a[180:183], v[44:47], a[84:87]\n"
"ds_read_b64_tr_b16 a[254:255], v218 offset:46872\n"
"v_fma_mix_f32 v186, v61, v162, v170 op_sel:[0,0,0] op_sel_hi:[1,0,0]\n"
"v_mfma_f32_16x16x32_bf16 a[88:91], a[184:187], v[32:35], a[88:91]\n"
"ds_read_b64_tr_b16 a[160:161], v219 offset:0\n"
"v_fma_mix_f32 v187, v61, v163, v171 op_sel:[1,0,0] op_sel_hi:[1,0,0]\n"
"v_fma_mix_f32 v188, v62, v164, v172 op_sel:[0,0,0] op_sel_hi:[1,0,0]\n"
"v_mfma_f32_16x16x32_bf16 a[88:91], a[188:191], v[40:43], a[88:91]\n"
"ds_read_b64_tr_b16 a[162:163], v219 offset:1024\n"
"v_fma_mix_f32 v189, v62, v165, v173 op_sel:[1,0,0] op_sel_hi:[1,0,0]\n"
"v_mfma_f32_16x16x32_bf16 a[92:95], a[184:187], v[36:39], a[92:95]\n"
"s_add_u32 m0, s51, 25352\n"
"ds_read_b64_tr_b16 a[164:165], v219 offset:4096\n"
"v_fma_mix_f32 v190, v63, v166, v174 op_sel:[0,0,0] op_sel_hi:[1,0,0]\n"
"v_mfma_f32_16x16x32_bf16 a[92:95], a[188:191], v[44:47], a[92:95]\n"
"s_waitcnt lgkmcnt(11)\n"
"buffer_load_dwordx4 v217, s[20:23], s50 offen offset:256 lds\n"
"ds_read_b64_tr_b16 a[166:167], v219 offset:5120\n"
"v_mfma_f32_16x16x32_bf16 a[96:99], a[224:227], v[32:35], a[96:99]\n"
"s_add_u32 s50, s50, 425984\n"
"ds_read_b64_tr_b16 a[168:169], v219 offset:8\n"
"v_fma_mix_f32 v191, v63, v167, v175 op_sel:[1,0,0] op_sel_hi:[1,0,0]\n"
"v_mfma_f32_16x16x32_bf16 a[96:99], a[228:231], v[40:43], a[96:99]\n"
"ds_read_b64_tr_b16 a[170:171], v219 offset:1032\n"
"ds_read_b128 v[160:163], v224 offset:2304\n"
"v_mfma_f32_16x16x32_bf16 a[100:103], a[224:227], v[36:39], a[100:103]\n"
"ds_read_b64_tr_b16 a[172:173], v219 offset:4104\n"
"v_cvt_pk_bf16_f32 v60, v184, v185\n"
"v_mfma_f32_16x16x32_bf16 a[100:103], a[228:231], v[44:47], a[100:103]\n"
"ds_read_b64_tr_b16 a[174:175], v219 offset:5128\n"
"v_cvt_pk_bf16_f32 v61, v186, v187\n"
"v_mfma_f32_16x16x32_bf16 a[104:107], a[232:235], v[32:35], a[104:107]\n"
"ds_read_b64_tr_b16 a[176:177], v219 offset:256\n"
"ds_read_b128 v[164:167], v224 offset:2368\n"
"v_mfma_f32_16x16x32_bf16 a[104:107], a[236:239], v[40:43], a[104:107]\n"
"ds_read_b64_tr_b16 a[178:179], v219 offset:1280\n"
"v_cvt_pk_bf16_f32 v62, v188, v189\n"
"v_mfma_f32_16x16x32_bf16 a[108:111], a[232:235], v[36:39], a[108:111]\n"
"ds_read_b64_tr_b16 a[180:181], v219 offset:4352\n"
"v_cvt_pk_bf16_f32 v63, v190, v191\n"
"v_mfma_f32_16x16x32_bf16 a[108:111], a[236:239], v[44:47], a[108:111]\n"
"s_waitcnt lgkmcnt(13)\n"
"ds_read_b64_tr_b16 a[182:183], v219 offset:5376\n"
"ds_read_b128 v[168:171], v224 offset:2432\n"
"v_mfma_f32_16x16x32_bf16 a[112:115], a[240:243], v[32:35], a[112:115]\n"
"ds_read_b64_tr_b16 a[184:185], v219 offset:264\n"
"v_fma_mix_f32 v176, v64, v144, v152 op_sel:[0,0,0] op_sel_hi:[1,0,0]\n"
"v_mfma_f32_16x16x32_bf16 a[112:115], a[244:247], v[40:43], a[112:115]\n"
"ds_read_b64_tr_b16 a[186:187], v219 offset:1288\n"
"v_fma_mix_f32 v177, v64, v145, v153 op_sel:[1,0,0] op_sel_hi:[1,0,0]\n"
"v_mfma_f32_16x16x32_bf16 a[116:119], a[240:243], v[36:39], a[116:119]\n"
"ds_read_b64_tr_b16 a[188:189], v219 offset:4360\n"
"ds_read_b128 v[172:175], v224 offset:2496\n"
"v_mfma_f32_16x16x32_bf16 a[116:119], a[244:247], v[44:47], a[116:119]\n"
"ds_read_b64_tr_b16 a[190:191], v219 offset:5384\n"
"v_fma_mix_f32 v178, v65, v146, v154 op_sel:[0,0,0] op_sel_hi:[1,0,0]\n"
"v_mfma_f32_16x16x32_bf16 a[120:123], a[248:251], v[32:35], a[120:123]\n"
"ds_read_b64_tr_b16 a[224:225], v219 offset:512\n"
"v_fma_mix_f32 v179, v65, v147, v155 op_sel:[1,0,0] op_sel_hi:[1,0,0]\n"
"v_mfma_f32_16x16x32_bf16 a[120:123], a[252:255], v[40:43], a[120:123]\n"
"ds_read_b64_tr_b16 a[226:227], v219 offset:1536\n"
"v_fma_mix_f32 v180, v66, v148, v156 op_sel:[0,0,0] op_sel_hi:[1,0,0]\n"
"v_mfma_f32_16x16x32_bf16 a[124:127], a[248:251], v[36:39], a[124:127]\n"
"ds_read_b64_tr_b16 a[228:229], v219 offset:4608\n"
"v_fma_mix_f32 v181, v66, v149, v157 op_sel:[1,0,0] op_sel_hi:[1,0,0]\n"
"v_mfma_f32_16x16x32_bf16 a[124:127], a[252:255], v[44:47], a[124:127]\n"
"s_waitcnt lgkmcnt(14)\n"
"ds_read_b64_tr_b16 a[230:231], v219 offset:5632\n"
"v_fma_mix_f32 v182, v67, v150, v158 op_sel:[0,0,0] op_sel_hi:[1,0,0]\n"
"v_mfma_f32_16x16x32_bf16 a[0:3], a[160:163], v[48:51], a[0:3]\n"
"ds_read_b64_tr_b16 a[232:233], v219 offset:520\n"
"v_fma_mix_f32 v183, v67, v151, v159 op_sel:[1,0,0] op_sel_hi:[1,0,0]\n"
"v_mfma_f32_16x16x32_bf16 a[0:3], a[164:167], v[56:59], a[0:3]\n"
"ds_read_b64_tr_b16 a[234:235], v219 offset:1544\n"
"v_cvt_pk_bf16_f32 v64, v176, v177\n"
"v_mfma_f32_16x16x32_bf16 a[4:7], a[160:163], v[52:55], a[4:7]\n"
"s_waitcnt vmcnt(8)\n"
"s_barrier\n"
"ds_read_b64_tr_b16 a[236:237], v219 offset:4616\n"
"v_cvt_pk_bf16_f32 v65, v178, v179\n"
"v_mfma_f32_16x16x32_bf16 a[4:7], a[164:167], v[60:63], a[4:7]\n"
"ds_read_b64_tr_b16 a[238:239], v219 offset:5640\n"
"v_cvt_pk_bf16_f32 v66, v180, v181\n"
"v_mfma_f32_16x16x32_bf16 a[8:11], a[168:171], v[48:51], a[8:11]\n"
"s_add_u32 m0, s51, 32784\n"
"ds_read_b64_tr_b16 a[240:241], v219 offset:768\n"
"v_cvt_pk_bf16_f32 v67, v182, v183\n"
"v_mfma_f32_16x16x32_bf16 a[8:11], a[172:175], v[56:59], a[8:11]\n"
"ds_read_b64_tr_b16 a[242:243], v219 offset:1792\n"
"buffer_load_dwordx4 v217, s[20:23], s50 offen offset:0 lds\n"
"v_mfma_f32_16x16x32_bf16 a[12:15], a[168:171], v[52:55], a[12:15]\n"
"ds_read_b64_tr_b16 a[244:245], v219 offset:4864\n"
"v_fma_mix_f32 v184, v68, v144, v152 op_sel:[0,0,0] op_sel_hi:[1,0,0]\n"
"v_mfma_f32_16x16x32_bf16 a[12:15], a[172:175], v[60:63], a[12:15]\n"
"s_waitcnt lgkmcnt(11)\n"
"ds_read_b64_tr_b16 a[246:247], v219 offset:5888\n"
"v_fma_mix_f32 v185, v68, v145, v153 op_sel:[1,0,0] op_sel_hi:[1,0,0]\n"
"v_mfma_f32_16x16x32_bf16 a[16:19], a[176:179], v[48:51], a[16:19]\n"
"ds_read_b64_tr_b16 a[248:249], v219 offset:776\n"
"v_fma_mix_f32 v186, v69, v146, v154 op_sel:[0,0,0] op_sel_hi:[1,0,0]\n"
"v_mfma_f32_16x16x32_bf16 a[16:19], a[180:183], v[56:59], a[16:19]\n"
"ds_read_b64_tr_b16 a[250:251], v219 offset:1800\n"
"v_fma_mix_f32 v187, v69, v147, v155 op_sel:[1,0,0] op_sel_hi:[1,0,0]\n"
"v_mfma_f32_16x16x32_bf16 a[20:23], a[176:179], v[52:55], a[20:23]\n"
"s_add_u32 m0, s51, 40720\n"
"ds_read_b64_tr_b16 a[252:253], v219 offset:4872\n"
"v_fma_mix_f32 v188, v70, v148, v156 op_sel:[0,0,0] op_sel_hi:[1,0,0]\n"
"v_mfma_f32_16x16x32_bf16 a[20:23], a[180:183], v[60:63], a[20:23]\n"
"ds_read_b64_tr_b16 a[254:255], v219 offset:5896\n"
"buffer_load_dwordx4 v217, s[20:23], s50 offen offset:256 lds\n"
"v_mfma_f32_16x16x32_bf16 a[24:27], a[184:187], v[48:51], a[24:27]\n"
"s_add_u32 s50, s50, 32768\n"
"ds_read_b64_tr_b16 a[160:161], v219 offset:8192\n"
"v_fma_mix_f32 v189, v70, v149, v157 op_sel:[1,0,0] op_sel_hi:[1,0,0]\n"
"v_mfma_f32_16x16x32_bf16 a[24:27], a[188:191], v[56:59], a[24:27]\n"
"ds_read_b64_tr_b16 a[162:163], v219 offset:9216\n"
"v_fma_mix_f32 v190, v71, v150, v158 op_sel:[0,0,0] op_sel_hi:[1,0,0]\n"
"v_mfma_f32_16x16x32_bf16 a[28:31], a[184:187], v[52:55], a[28:31]\n"
"ds_read_b64_tr_b16 a[164:165], v219 offset:12288\n"
"v_fma_mix_f32 v191, v71, v151, v159 op_sel:[1,0,0] op_sel_hi:[1,0,0]\n"
"v_mfma_f32_16x16x32_bf16 a[28:31], a[188:191], v[60:63], a[28:31]\n"
"s_waitcnt lgkmcnt(11)\n"
"ds_read_b64_tr_b16 a[166:167], v219 offset:13312\n"
"ds_read_b128 v[144:147], v224 offset:2560\n"
"v_mfma_f32_16x16x32_bf16 a[32:35], a[224:227], v[48:51], a[32:35]\n"
"s_add_u32 m0, s51, 33808\n"
"ds_read_b64_tr_b16 a[168:169], v219 offset:8200\n"
"v_cvt_pk_bf16_f32 v68, v184, v185\n"
"v_mfma_f32_16x16x32_bf16 a[32:35], a[228:231], v[56:59], a[32:35]\n"
"ds_read_b64_tr_b16 a[170:171], v219 offset:9224\n"
"buffer_load_dwordx4 v217, s[20:23], s50 offen offset:0 lds\n"
"v_mfma_f32_16x16x32_bf16 a[36:39], a[224:227], v[52:55], a[36:39]\n"
"ds_read_b64_tr_b16 a[172:173], v219 offset:12296\n"
"ds_read_b128 v[148:151], v224 offset:2624\n"
"v_mfma_f32_16x16x32_bf16 a[36:39], a[228:231], v[60:63], a[36:39]\n"
"ds_read_b64_tr_b16 a[174:175], v219 offset:13320\n"
"v_cvt_pk_bf16_f32 v69, v186, v187\n"
"v_mfma_f32_16x16x32_bf16 a[40:43], a[232:235], v[48:51], a[40:43]\n"
"ds_read_b64_tr_b16 a[176:177], v219 offset:8448\n"
"v_cvt_pk_bf16_f32 v70, v188, v189\n"
"v_mfma_f32_16x16x32_bf16 a[40:43], a[236:239], v[56:59], a[40:43]\n"
"ds_read_b64_tr_b16 a[178:179], v219 offset:9472\n"
"ds_read_b128 v[152:155], v224 offset:2688\n"
"v_mfma_f32_16x16x32_bf16 a[44:47], a[232:235], v[52:55], a[44:47]\n"
"s_add_u32 m0, s51, 41744\n"
"ds_read_b64_tr_b16 a[180:181], v219 offset:12544\n"
"v_cvt_pk_bf16_f32 v71, v190, v191\n"
"v_mfma_f32_16x16x32_bf16 a[44:47], a[236:239], v[60:63], a[44:47]\n"
"s_waitcnt lgkmcnt(14)\n"
"ds_read_b64_tr_b16 a[182:183], v219 offset:13568\n"
"buffer_load_dwordx4 v217, s[20:23], s50 offen offset:256 lds\n"
"v_mfma_f32_16x16x32_bf16 a[48:51], a[240:243], v[48:51], a[48:51]\n"
"s_add_u32 s50, s50, 32768\n"
"ds_read_b64_tr_b16 a[184:185], v219 offset:8456\n"
"ds_read_b128 v[156:159], v224 offset:2752\n"
"v_mfma_f32_16x16x32_bf16 a[48:51], a[244:247], v[56:59], a[48:51]\n"
"ds_read_b64_tr_b16 a[186:187], v219 offset:9480\n"
"v_fma_mix_f32 v176, v72, v160, v168 op_sel:[0,0,0] op_sel_hi:[1,0,0]\n"
"v_mfma_f32_16x16x32_bf16 a[52:55], a[240:243], v[52:55], a[52:55]\n"
"ds_read_b64_tr_b16 a[188:189], v219 offset:12552\n"
"v_fma_mix_f32 v177, v72, v161, v169 op_sel:[1,0,0] op_sel_hi:[1,0,0]\n"
"v_mfma_f32_16x16x32_bf16 a[52:55], a[244:247], v[60:63], a[52:55]\n"
"ds_read_b64_tr_b16 a[190:191], v219 offset:13576\n"
"v_fma_mix_f32 v178, v73, v162, v170 op_sel:[0,0,0] op_sel_hi:[1,0,0]\n"
"v_mfma_f32_16x16x32_bf16 a[56:59], a[248:251], v[48:51], a[56:59]\n"
"s_add_u32 m0, s51, 49176\n"
"ds_read_b64_tr_b16 a[224:225], v219 offset:8704\n"
"v_fma_mix_f32 v179, v73, v163, v171 op_sel:[1,0,0] op_sel_hi:[1,0,0]\n"
"v_mfma_f32_16x16x32_bf16 a[56:59], a[252:255], v[56:59], a[56:59]\n"
"ds_read_b64_tr_b16 a[226:227], v219 offset:9728\n"
"buffer_load_dwordx4 v217, s[20:23], s50 offen offset:0 lds\n"
"v_mfma_f32_16x16x32_bf16 a[60:63], a[248:251], v[52:55], a[60:63]\n"
"ds_read_b64_tr_b16 a[228:229], v219 offset:12800\n"
"v_fma_mix_f32 v180, v74, v164, v172 op_sel:[0,0,0] op_sel_hi:[1,0,0]\n"
"v_mfma_f32_16x16x32_bf16 a[60:63], a[252:255], v[60:63], a[60:63]\n"
"s_waitcnt lgkmcnt(13)\n"
"ds_read_b64_tr_b16 a[230:231], v219 offset:13824\n"
"v_fma_mix_f32 v181, v74, v165, v173 op_sel:[1,0,0] op_sel_hi:[1,0,0]\n"
"v_mfma_f32_16x16x32_bf16 a[64:67], a[160:163], v[48:51], a[64:67]\n"
"ds_read_b64_tr_b16 a[232:233], v219 offset:8712\n"
"v_fma_mix_f32 v182, v75, v166, v174 op_sel:[0,0,0] op_sel_hi:[1,0,0]\n"
"v_mfma_f32_16x16x32_bf16 a[64:67], a[164:167], v[56:59], a[64:67]\n"
"ds_read_b64_tr_b16 a[234:235], v219 offset:9736\n"
"v_fma_mix_f32 v183, v75, v167, v175 op_sel:[1,0,0] op_sel_hi:[1,0,0]\n"
"v_mfma_f32_16x16x32_bf16 a[68:71], a[160:163], v[52:55], a[68:71]\n"
"s_add_u32 m0, s51, 57112\n"
"ds_read_b64_tr_b16 a[236:237], v219 offset:12808\n"
"v_cvt_pk_bf16_f32 v72, v176, v177\n"
"v_mfma_f32_16x16x32_bf16 a[68:71], a[164:167], v[60:63], a[68:71]\n"
"ds_read_b64_tr_b16 a[238:239], v219 offset:13832\n"
"buffer_load_dwordx4 v217, s[20:23], s50 offen offset:256 lds\n"
"v_mfma_f32_16x16x32_bf16 a[72:75], a[168:171], v[48:51], a[72:75]\n"
"s_add_u32 s50, s50, 32768\n"
"ds_read_b64_tr_b16 a[240:241], v219 offset:8960\n"
"v_cvt_pk_bf16_f32 v73, v178, v179\n"
"v_mfma_f32_16x16x32_bf16 a[72:75], a[172:175], v[56:59], a[72:75]\n"
"ds_read_b64_tr_b16 a[242:243], v219 offset:9984\n"
"v_cvt_pk_bf16_f32 v74, v180, v181\n"
"v_mfma_f32_16x16x32_bf16 a[76:79], a[168:171], v[52:55], a[76:79]\n"
"ds_read_b64_tr_b16 a[244:245], v219 offset:13056\n"
"v_cvt_pk_bf16_f32 v75, v182, v183\n"
"v_mfma_f32_16x16x32_bf16 a[76:79], a[172:175], v[60:63], a[76:79]\n"
"s_waitcnt lgkmcnt(11)\n"
"ds_read_b64_tr_b16 a[246:247], v219 offset:14080\n"
"v_fma_mix_f32 v184, v76, v160, v168 op_sel:[0,0,0] op_sel_hi:[1,0,0]\n"
"v_mfma_f32_16x16x32_bf16 a[80:83], a[176:179], v[48:51], a[80:83]\n"
"s_add_u32 m0, s51, 50200\n"
"ds_read_b64_tr_b16 a[248:249], v219 offset:8968\n"
"v_fma_mix_f32 v185, v76, v161, v169 op_sel:[1,0,0] op_sel_hi:[1,0,0]\n"
"v_mfma_f32_16x16x32_bf16 a[80:83], a[180:183], v[56:59], a[80:83]\n"
"ds_read_b64_tr_b16 a[250:251], v219 offset:9992\n"
"buffer_load_dwordx4 v217, s[20:23], s50 offen offset:0 lds\n"
"v_mfma_f32_16x16x32_bf16 a[84:87], a[176:179], v[52:55], a[84:87]\n"
"ds_read_b64_tr_b16 a[252:253], v219 offset:13064\n"
"v_fma_mix_f32 v186, v77, v162, v170 op_sel:[0,0,0] op_sel_hi:[1,0,0]\n"
"v_mfma_f32_16x16x32_bf16 a[84:87], a[180:183], v[60:63], a[84:87]\n"
"ds_read_b64_tr_b16 a[254:255], v219 offset:14088\n"
"v_fma_mix_f32 v187, v77, v163, v171 op_sel:[1,0,0] op_sel_hi:[1,0,0]\n"
"v_mfma_f32_16x16x32_bf16 a[88:91], a[184:187], v[48:51], a[88:91]\n"
"ds_read_b64_tr_b16 a[160:161], v219 offset:32784\n"
"v_fma_mix_f32 v188, v78, v164, v172 op_sel:[0,0,0] op_sel_hi:[1,0,0]\n"
"v_fma_mix_f32 v189, v78, v165, v173 op_sel:[1,0,0] op_sel_hi:[1,0,0]\n"
"v_mfma_f32_16x16x32_bf16 a[88:91], a[188:191], v[56:59], a[88:91]\n"
"ds_read_b64_tr_b16 a[162:163], v219 offset:33808\n"
"v_fma_mix_f32 v190, v79, v166, v174 op_sel:[0,0,0] op_sel_hi:[1,0,0]\n"
"v_mfma_f32_16x16x32_bf16 a[92:95], a[184:187], v[52:55], a[92:95]\n"
"s_add_u32 m0, s51, 58136\n"
"ds_read_b64_tr_b16 a[164:165], v219 offset:36880\n"
"v_fma_mix_f32 v191, v79, v167, v175 op_sel:[1,0,0] op_sel_hi:[1,0,0]\n"
"v_mfma_f32_16x16x32_bf16 a[92:95], a[188:191], v[60:63], a[92:95]\n"
"s_waitcnt lgkmcnt(11)\n"
"buffer_load_dwordx4 v217, s[20:23], s50 offen offset:256 lds\n"
"ds_read_b64_tr_b16 a[166:167], v219 offset:37904\n"
"v_mfma_f32_16x16x32_bf16 a[96:99], a[224:227], v[48:51], a[96:99]\n"
"s_add_u32 s50, s50, 425984\n"
"ds_read_b64_tr_b16 a[168:169], v219 offset:32792\n"
"ds_read_b128 v[160:163], v224 offset:2816\n"
"v_mfma_f32_16x16x32_bf16 a[96:99], a[228:231], v[56:59], a[96:99]\n"
"ds_read_b64_tr_b16 a[170:171], v219 offset:33816\n"
"v_cvt_pk_bf16_f32 v76, v184, v185\n"
"v_mfma_f32_16x16x32_bf16 a[100:103], a[224:227], v[52:55], a[100:103]\n"
"ds_read_b64_tr_b16 a[172:173], v219 offset:36888\n"
"v_cvt_pk_bf16_f32 v77, v186, v187\n"
"v_mfma_f32_16x16x32_bf16 a[100:103], a[228:231], v[60:63], a[100:103]\n"
"ds_read_b64_tr_b16 a[174:175], v219 offset:37912\n"
"ds_read_b128 v[164:167], v224 offset:2880\n"
"v_mfma_f32_16x16x32_bf16 a[104:107], a[232:235], v[48:51], a[104:107]\n"
"ds_read_b64_tr_b16 a[176:177], v219 offset:33040\n"
"v_cvt_pk_bf16_f32 v78, v188, v189\n"
"v_mfma_f32_16x16x32_bf16 a[104:107], a[236:239], v[56:59], a[104:107]\n"
"ds_read_b64_tr_b16 a[178:179], v219 offset:34064\n"
"v_cvt_pk_bf16_f32 v79, v190, v191\n"
"v_mfma_f32_16x16x32_bf16 a[108:111], a[232:235], v[52:55], a[108:111]\n"
"ds_read_b64_tr_b16 a[180:181], v219 offset:37136\n"
"ds_read_b128 v[168:171], v224 offset:2944\n"
"v_mfma_f32_16x16x32_bf16 a[108:111], a[236:239], v[60:63], a[108:111]\n"
"s_waitcnt lgkmcnt(14)\n"
"ds_read_b64_tr_b16 a[182:183], v219 offset:38160\n"
"v_fma_mix_f32 v176, v80, v144, v152 op_sel:[0,0,0] op_sel_hi:[1,0,0]\n"
"v_mfma_f32_16x16x32_bf16 a[112:115], a[240:243], v[48:51], a[112:115]\n"
"ds_read_b64_tr_b16 a[184:185], v219 offset:33048\n"
"v_fma_mix_f32 v177, v80, v145, v153 op_sel:[1,0,0] op_sel_hi:[1,0,0]\n"
"v_mfma_f32_16x16x32_bf16 a[112:115], a[244:247], v[56:59], a[112:115]\n"
"ds_read_b64_tr_b16 a[186:187], v219 offset:34072\n"
"ds_read_b128 v[172:175], v224 offset:3008\n"
"v_mfma_f32_16x16x32_bf16 a[116:119], a[240:243], v[52:55], a[116:119]\n"
"ds_read_b64_tr_b16 a[188:189], v219 offset:37144\n"
"v_fma_mix_f32 v178, v81, v146, v154 op_sel:[0,0,0] op_sel_hi:[1,0,0]\n"
"v_mfma_f32_16x16x32_bf16 a[116:119], a[244:247], v[60:63], a[116:119]\n"
"ds_read_b64_tr_b16 a[190:191], v219 offset:38168\n"
"v_fma_mix_f32 v179, v81, v147, v155 op_sel:[1,0,0] op_sel_hi:[1,0,0]\n"
"v_mfma_f32_16x16x32_bf16 a[120:123], a[248:251], v[48:51], a[120:123]\n"
"ds_read_b64_tr_b16 a[224:225], v219 offset:33296\n"
"v_fma_mix_f32 v180, v82, v148, v156 op_sel:[0,0,0] op_sel_hi:[1,0,0]\n"
"v_mfma_f32_16x16x32_bf16 a[120:123], a[252:255], v[56:59], a[120:123]\n"
"ds_read_b64_tr_b16 a[226:227], v219 offset:34320\n"
"v_fma_mix_f32 v181, v82, v149, v157 op_sel:[1,0,0] op_sel_hi:[1,0,0]\n"
"v_mfma_f32_16x16x32_bf16 a[124:127], a[248:251], v[52:55], a[124:127]\n"
"ds_read_b64_tr_b16 a[228:229], v219 offset:37392\n"
"v_fma_mix_f32 v182, v83, v150, v158 op_sel:[0,0,0] op_sel_hi:[1,0,0]\n"
"v_mfma_f32_16x16x32_bf16 a[124:127], a[252:255], v[60:63], a[124:127]\n"
"s_waitcnt lgkmcnt(13)\n"
"ds_read_b64_tr_b16 a[230:231], v219 offset:38416\n"
"v_fma_mix_f32 v183, v83, v151, v159 op_sel:[1,0,0] op_sel_hi:[1,0,0]\n"
"v_mfma_f32_16x16x32_bf16 a[0:3], a[160:163], v[64:67], a[0:3]\n"
"ds_read_b64_tr_b16 a[232:233], v219 offset:33304\n"
"v_cvt_pk_bf16_f32 v80, v176, v177\n"
"v_mfma_f32_16x16x32_bf16 a[0:3], a[164:167], v[72:75], a[0:3]\n"
"ds_read_b64_tr_b16 a[234:235], v219 offset:34328\n"
"v_cvt_pk_bf16_f32 v81, v178, v179\n"
"v_mfma_f32_16x16x32_bf16 a[4:7], a[160:163], v[68:71], a[4:7]\n"
"s_waitcnt vmcnt(8)\n"
"s_barrier\n"
"ds_read_b64_tr_b16 a[236:237], v219 offset:37400\n"
"v_cvt_pk_bf16_f32 v82, v180, v181\n"
"v_mfma_f32_16x16x32_bf16 a[4:7], a[164:167], v[76:79], a[4:7]\n"
"ds_read_b64_tr_b16 a[238:239], v219 offset:38424\n"
"v_cvt_pk_bf16_f32 v83, v182, v183\n"
"v_mfma_f32_16x16x32_bf16 a[8:11], a[168:171], v[64:67], a[8:11]\n"
"s_add_u32 m0, s51, 65568\n"
"ds_read_b64_tr_b16 a[240:241], v219 offset:33552\n"
"v_fma_mix_f32 v184, v84, v144, v152 op_sel:[0,0,0] op_sel_hi:[1,0,0]\n"
"v_mfma_f32_16x16x32_bf16 a[8:11], a[172:175], v[72:75], a[8:11]\n"
"ds_read_b64_tr_b16 a[242:243], v219 offset:34576\n"
"buffer_load_dwordx4 v217, s[20:23], s50 offen offset:0 lds\n"
"v_mfma_f32_16x16x32_bf16 a[12:15], a[168:171], v[68:71], a[12:15]\n"
"ds_read_b64_tr_b16 a[244:245], v219 offset:37648\n"
"v_fma_mix_f32 v185, v84, v145, v153 op_sel:[1,0,0] op_sel_hi:[1,0,0]\n"
"v_mfma_f32_16x16x32_bf16 a[12:15], a[172:175], v[76:79], a[12:15]\n"
"s_waitcnt lgkmcnt(11)\n"
"ds_read_b64_tr_b16 a[246:247], v219 offset:38672\n"
"v_fma_mix_f32 v186, v85, v146, v154 op_sel:[0,0,0] op_sel_hi:[1,0,0]\n"
"v_mfma_f32_16x16x32_bf16 a[16:19], a[176:179], v[64:67], a[16:19]\n"
"ds_read_b64_tr_b16 a[248:249], v219 offset:33560\n"
"v_fma_mix_f32 v187, v85, v147, v155 op_sel:[1,0,0] op_sel_hi:[1,0,0]\n"
"v_mfma_f32_16x16x32_bf16 a[16:19], a[180:183], v[72:75], a[16:19]\n"
"ds_read_b64_tr_b16 a[250:251], v219 offset:34584\n"
"v_fma_mix_f32 v188, v86, v148, v156 op_sel:[0,0,0] op_sel_hi:[1,0,0]\n"
"v_mfma_f32_16x16x32_bf16 a[20:23], a[176:179], v[68:71], a[20:23]\n"
"s_add_u32 m0, s51, 73504\n"
"ds_read_b64_tr_b16 a[252:253], v219 offset:37656\n"
"v_fma_mix_f32 v189, v86, v149, v157 op_sel:[1,0,0] op_sel_hi:[1,0,0]\n"
"v_mfma_f32_16x16x32_bf16 a[20:23], a[180:183], v[76:79], a[20:23]\n"
"ds_read_b64_tr_b16 a[254:255], v219 offset:38680\n"
"buffer_load_dwordx4 v217, s[20:23], s50 offen offset:256 lds\n"
"v_mfma_f32_16x16x32_bf16 a[24:27], a[184:187], v[64:67], a[24:27]\n"
"s_add_u32 s50, s50, 32768\n"
"ds_read_b64_tr_b16 a[160:161], v219 offset:40976\n"
"v_fma_mix_f32 v190, v87, v150, v158 op_sel:[0,0,0] op_sel_hi:[1,0,0]\n"
"v_mfma_f32_16x16x32_bf16 a[24:27], a[188:191], v[72:75], a[24:27]\n"
"ds_read_b64_tr_b16 a[162:163], v219 offset:42000\n"
"v_fma_mix_f32 v191, v87, v151, v159 op_sel:[1,0,0] op_sel_hi:[1,0,0]\n"
"v_mfma_f32_16x16x32_bf16 a[28:31], a[184:187], v[68:71], a[28:31]\n"
"ds_read_b64_tr_b16 a[164:165], v219 offset:45072\n"
"ds_read_b128 v[144:147], v224 offset:3072\n"
"v_mfma_f32_16x16x32_bf16 a[28:31], a[188:191], v[76:79], a[28:31]\n"
"s_waitcnt lgkmcnt(12)\n"
"ds_read_b64_tr_b16 a[166:167], v219 offset:46096\n"
"v_cvt_pk_bf16_f32 v84, v184, v185\n"
"v_mfma_f32_16x16x32_bf16 a[32:35], a[224:227], v[64:67], a[32:35]\n"
"s_add_u32 m0, s51, 66592\n"
"ds_read_b64_tr_b16 a[168:169], v219 offset:40984\n"
"v_cvt_pk_bf16_f32 v85, v186, v187\n"
"v_mfma_f32_16x16x32_bf16 a[32:35], a[228:231], v[72:75], a[32:35]\n"
"ds_read_b64_tr_b16 a[170:171], v219 offset:42008\n"
"buffer_load_dwordx4 v217, s[20:23], s50 offen offset:0 lds\n"
"v_mfma_f32_16x16x32_bf16 a[36:39], a[224:227], v[68:71], a[36:39]\n"
"ds_read_b64_tr_b16 a[172:173], v219 offset:45080\n"
"ds_read_b128 v[148:151], v224 offset:3136\n"
"v_mfma_f32_16x16x32_bf16 a[36:39], a[228:231], v[76:79], a[36:39]\n"
"ds_read_b64_tr_b16 a[174:175], v219 offset:46104\n"
"v_cvt_pk_bf16_f32 v86, v188, v189\n"
"v_mfma_f32_16x16x32_bf16 a[40:43], a[232:235], v[64:67], a[40:43]\n"
"ds_read_b64_tr_b16 a[176:177], v219 offset:41232\n"
"v_cvt_pk_bf16_f32 v87, v190, v191\n"
"v_mfma_f32_16x16x32_bf16 a[40:43], a[236:239], v[72:75], a[40:43]\n"
"ds_read_b64_tr_b16 a[178:179], v219 offset:42256\n"
"ds_read_b128 v[152:155], v224 offset:3200\n"
"v_mfma_f32_16x16x32_bf16 a[44:47], a[232:235], v[68:71], a[44:47]\n"
"s_add_u32 m0, s51, 74528\n"
"ds_read_b64_tr_b16 a[180:181], v219 offset:45328\n"
"v_fma_mix_f32 v176, v88, v160, v168 op_sel:[0,0,0] op_sel_hi:[1,0,0]\n"
"v_mfma_f32_16x16x32_bf16 a[44:47], a[236:239], v[76:79], a[44:47]\n"
"s_waitcnt lgkmcnt(14)\n"
"ds_read_b64_tr_b16 a[182:183], v219 offset:46352\n"
"buffer_load_dwordx4 v217, s[20:23], s50 offen offset:256 lds\n"
"v_mfma_f32_16x16x32_bf16 a[48:51], a[240:243], v[64:67], a[48:51]\n"
"s_add_u32 s50, s50, 32768\n"
"ds_read_b64_tr_b16 a[184:185], v219 offset:41240\n"
"ds_read_b128 v[156:159], v224 offset:3264\n"
"v_mfma_f32_16x16x32_bf16 a[48:51], a[244:247], v[72:75], a[48:51]\n"
"ds_read_b64_tr_b16 a[186:187], v219 offset:42264\n"
"v_fma_mix_f32 v177, v88, v161, v169 op_sel:[1,0,0] op_sel_hi:[1,0,0]\n"
"v_mfma_f32_16x16x32_bf16 a[52:55], a[240:243], v[68:71], a[52:55]\n"
"ds_read_b64_tr_b16 a[188:189], v219 offset:45336\n"
"v_fma_mix_f32 v178, v89, v162, v170 op_sel:[0,0,0] op_sel_hi:[1,0,0]\n"
"v_mfma_f32_16x16x32_bf16 a[52:55], a[244:247], v[76:79], a[52:55]\n"
"ds_read_b64_tr_b16 a[190:191], v219 offset:46360\n"
"v_fma_mix_f32 v179, v89, v163, v171 op_sel:[1,0,0] op_sel_hi:[1,0,0]\n"
"v_mfma_f32_16x16x32_bf16 a[56:59], a[248:251], v[64:67], a[56:59]\n"
"s_add_u32 m0, s51, 81960\n"
"ds_read_b64_tr_b16 a[224:225], v219 offset:41488\n"
"v_fma_mix_f32 v180, v90, v164, v172 op_sel:[0,0,0] op_sel_hi:[1,0,0]\n"
"v_mfma_f32_16x16x32_bf16 a[56:59], a[252:255], v[72:75], a[56:59]\n"
"ds_read_b64_tr_b16 a[226:227], v219 offset:42512\n"
"buffer_load_dwordx4 v217, s[20:23], s50 offen offset:0 lds\n"
"v_mfma_f32_16x16x32_bf16 a[60:63], a[248:251], v[68:71], a[60:63]\n"
"ds_read_b64_tr_b16 a[228:229], v219 offset:45584\n"
"v_fma_mix_f32 v181, v90, v165, v173 op_sel:[1,0,0] op_sel_hi:[1,0,0]\n"
"v_mfma_f32_16x16x32_bf16 a[60:63], a[252:255], v[76:79], a[60:63]\n"
"s_waitcnt lgkmcnt(13)\n"
"ds_read_b64_tr_b16 a[230:231], v219 offset:46608\n"
"v_fma_mix_f32 v182, v91, v166, v174 op_sel:[0,0,0] op_sel_hi:[1,0,0]\n"
"v_mfma_f32_16x16x32_bf16 a[64:67], a[160:163], v[64:67], a[64:67]\n"
"ds_read_b64_tr_b16 a[232:233], v219 offset:41496\n"
"v_fma_mix_f32 v183, v91, v167, v175 op_sel:[1,0,0] op_sel_hi:[1,0,0]\n"
"v_mfma_f32_16x16x32_bf16 a[64:67], a[164:167], v[72:75], a[64:67]\n"
"ds_read_b64_tr_b16 a[234:235], v219 offset:42520\n"
"v_cvt_pk_bf16_f32 v88, v176, v177\n"
"v_mfma_f32_16x16x32_bf16 a[68:71], a[160:163], v[68:71], a[68:71]\n"
"s_add_u32 m0, s51, 89896\n"
"ds_read_b64_tr_b16 a[236:237], v219 offset:45592\n"
"v_cvt_pk_bf16_f32 v89, v178, v179\n"
"v_mfma_f32_16x16x32_bf16 a[68:71], a[164:167], v[76:79], a[68:71]\n"
"ds_read_b64_tr_b16 a[238:239], v219 offset:46616\n"
"buffer_load_dwordx4 v217, s[20:23], s50 offen offset:256 lds\n"
"v_mfma_f32_16x16x32_bf16 a[72:75], a[168:171], v[64:67], a[72:75]\n"
"s_add_u32 s50, s50, 32768\n"
"ds_read_b64_tr_b16 a[240:241], v219 offset:41744\n"
"v_cvt_pk_bf16_f32 v90, v180, v181\n"
"v_mfma_f32_16x16x32_bf16 a[72:75], a[172:175], v[72:75], a[72:75]\n"
"ds_read_b64_tr_b16 a[242:243], v219 offset:42768\n"
"v_cvt_pk_bf16_f32 v91, v182, v183\n"
"v_mfma_f32_16x16x32_bf16 a[76:79], a[168:171], v[68:71], a[76:79]\n"
"ds_read_b64_tr_b16 a[244:245], v219 offset:45840\n"
"v_fma_mix_f32 v184, v92, v160, v168 op_sel:[0,0,0] op_sel_hi:[1,0,0]\n"
"v_mfma_f32_16x16x32_bf16 a[76:79], a[172:175], v[76:79], a[76:79]\n"
"s_waitcnt lgkmcnt(11)\n"
"ds_read_b64_tr_b16 a[246:247], v219 offset:46864\n"
"v_fma_mix_f32 v185, v92, v161, v169 op_sel:[1,0,0] op_sel_hi:[1,0,0]\n"
"v_mfma_f32_16x16x32_bf16 a[80:83], a[176:179], v[64:67], a[80:83]\n"
"s_add_u32 m0, s51, 82984\n"
"ds_read_b64_tr_b16 a[248:249], v219 offset:41752\n"
"v_fma_mix_f32 v186, v93, v162, v170 op_sel:[0,0,0] op_sel_hi:[1,0,0]\n"
"v_mfma_f32_16x16x32_bf16 a[80:83], a[180:183], v[72:75], a[80:83]\n"
"ds_read_b64_tr_b16 a[250:251], v219 offset:42776\n"
"buffer_load_dwordx4 v217, s[20:23], s50 offen offset:0 lds\n"
"v_mfma_f32_16x16x32_bf16 a[84:87], a[176:179], v[68:71], a[84:87]\n"
"ds_read_b64_tr_b16 a[252:253], v219 offset:45848\n"
"v_fma_mix_f32 v187, v93, v163, v171 op_sel:[1,0,0] op_sel_hi:[1,0,0]\n"
"v_mfma_f32_16x16x32_bf16 a[84:87], a[180:183], v[76:79], a[84:87]\n"
"ds_read_b64_tr_b16 a[254:255], v219 offset:46872\n"
"v_fma_mix_f32 v188, v94, v164, v172 op_sel:[0,0,0] op_sel_hi:[1,0,0]\n"
"v_mfma_f32_16x16x32_bf16 a[88:91], a[184:187], v[64:67], a[88:91]\n"
"ds_read_b64_tr_b16 a[160:161], v218 offset:0\n"
"v_fma_mix_f32 v189, v94, v165, v173 op_sel:[1,0,0] op_sel_hi:[1,0,0]\n"
"v_fma_mix_f32 v190, v95, v166, v174 op_sel:[0,0,0] op_sel_hi:[1,0,0]\n"
"v_mfma_f32_16x16x32_bf16 a[88:91], a[188:191], v[72:75], a[88:91]\n"
"ds_read_b64_tr_b16 a[162:163], v218 offset:1024\n"
"v_fma_mix_f32 v191, v95, v167, v175 op_sel:[1,0,0] op_sel_hi:[1,0,0]\n"
"v_mfma_f32_16x16x32_bf16 a[92:95], a[184:187], v[68:71], a[92:95]\n"
"s_add_u32 m0, s51, 90920\n"
"ds_read_b64_tr_b16 a[164:165], v218 offset:4096\n"
"ds_read_b128 v[160:163], v224 offset:3328\n"
"v_mfma_f32_16x16x32_bf16 a[92:95], a[188:191], v[76:79], a[92:95]\n"
"s_waitcnt lgkmcnt(12)\n"
"buffer_load_dwordx4 v217, s[20:23], s50 offen offset:256 lds\n"
"ds_read_b64_tr_b16 a[166:167], v218 offset:5120\n"
"v_mfma_f32_16x16x32_bf16 a[96:99], a[224:227], v[64:67], a[96:99]\n"
"s_add_u32 s50, s50, 425984\n"
"ds_read_b64_tr_b16 a[168:169], v218 offset:8\n"
"v_cvt_pk_bf16_f32 v92, v184, v185\n"
"v_mfma_f32_16x16x32_bf16 a[96:99], a[228:231], v[72:75], a[96:99]\n"
"ds_read_b64_tr_b16 a[170:171], v218 offset:1032\n"
"ds_read_b128 v[164:167], v224 offset:3392\n"
"v_mfma_f32_16x16x32_bf16 a[100:103], a[224:227], v[68:71], a[100:103]\n"
"ds_read_b64_tr_b16 a[172:173], v218 offset:4104\n"
"v_cvt_pk_bf16_f32 v93, v186, v187\n"
"v_mfma_f32_16x16x32_bf16 a[100:103], a[228:231], v[76:79], a[100:103]\n"
"ds_read_b64_tr_b16 a[174:175], v218 offset:5128\n"
"v_cvt_pk_bf16_f32 v94, v188, v189\n"
"v_mfma_f32_16x16x32_bf16 a[104:107], a[232:235], v[64:67], a[104:107]\n"
"ds_read_b64_tr_b16 a[176:177], v218 offset:256\n"
"ds_read_b128 v[168:171], v224 offset:3456\n"
"v_mfma_f32_16x16x32_bf16 a[104:107], a[236:239], v[72:75], a[104:107]\n"
"ds_read_b64_tr_b16 a[178:179], v218 offset:1280\n"
"v_cvt_pk_bf16_f32 v95, v190, v191\n"
"v_mfma_f32_16x16x32_bf16 a[108:111], a[232:235], v[68:71], a[108:111]\n"
"ds_read_b64_tr_b16 a[180:181], v218 offset:4352\n"
"v_fma_mix_f32 v176, v96, v144, v152 op_sel:[0,0,0] op_sel_hi:[1,0,0]\n"
"v_mfma_f32_16x16x32_bf16 a[108:111], a[236:239], v[76:79], a[108:111]\n"
"s_waitcnt lgkmcnt(14)\n"
"ds_read_b64_tr_b16 a[182:183], v218 offset:5376\n"
"ds_read_b128 v[172:175], v224 offset:3520\n"
"v_mfma_f32_16x16x32_bf16 a[112:115], a[240:243], v[64:67], a[112:115]\n"
"ds_read_b64_tr_b16 a[184:185], v218 offset:264\n"
"v_fma_mix_f32 v177, v96, v145, v153 op_sel:[1,0,0] op_sel_hi:[1,0,0]\n"
"v_mfma_f32_16x16x32_bf16 a[112:115], a[244:247], v[72:75], a[112:115]\n"
"ds_read_b64_tr_b16 a[186:187], v218 offset:1288\n"
"v_fma_mix_f32 v178, v97, v146, v154 op_sel:[0,0,0] op_sel_hi:[1,0,0]\n"
"v_mfma_f32_16x16x32_bf16 a[116:119], a[240:243], v[68:71], a[116:119]\n"
"ds_read_b64_tr_b16 a[188:189], v218 offset:4360\n"
"v_fma_mix_f32 v179, v97, v147, v155 op_sel:[1,0,0] op_sel_hi:[1,0,0]\n"
"v_mfma_f32_16x16x32_bf16 a[116:119], a[244:247], v[76:79], a[116:119]\n"
"ds_read_b64_tr_b16 a[190:191], v218 offset:5384\n"
"v_fma_mix_f32 v180, v98, v148, v156 op_sel:[0,0,0] op_sel_hi:[1,0,0]\n"
"v_mfma_f32_16x16x32_bf16 a[120:123], a[248:251], v[64:67], a[120:123]\n"
"ds_read_b64_tr_b16 a[224:225], v218 offset:512\n"
"v_fma_mix_f32 v181, v98, v149, v157 op_sel:[1,0,0] op_sel_hi:[1,0,0]\n"
"v_mfma_f32_16x16x32_bf16 a[120:123], a[252:255], v[72:75], a[120:123]\n"
"ds_read_b64_tr_b16 a[226:227], v218 offset:1536\n"
"v_fma_mix_f32 v182, v99, v150, v158 op_sel:[0,0,0] op_sel_hi:[1,0,0]\n"
"v_mfma_f32_16x16x32_bf16 a[124:127], a[248:251], v[68:71], a[124:127]\n"
"ds_read_b64_tr_b16 a[228:229], v218 offset:4608\n"
"v_fma_mix_f32 v183, v99, v151, v159 op_sel:[1,0,0] op_sel_hi:[1,0,0]\n"
"v_mfma_f32_16x16x32_bf16 a[124:127], a[252:255], v[76:79], a[124:127]\n"
"s_waitcnt lgkmcnt(13)\n"
"ds_read_b64_tr_b16 a[230:231], v218 offset:5632\n"
"v_cvt_pk_bf16_f32 v96, v176, v177\n"
"v_mfma_f32_16x16x32_bf16 a[0:3], a[160:163], v[80:83], a[0:3]\n"
"ds_read_b64_tr_b16 a[232:233], v218 offset:520\n"
"v_cvt_pk_bf16_f32 v97, v178, v179\n"
"v_mfma_f32_16x16x32_bf16 a[0:3], a[164:167], v[88:91], a[0:3]\n"
"ds_read_b64_tr_b16 a[234:235], v218 offset:1544\n"
"v_cvt_pk_bf16_f32 v98, v180, v181\n"
"v_mfma_f32_16x16x32_bf16 a[4:7], a[160:163], v[84:87], a[4:7]\n"
"s_waitcnt vmcnt(8)\n"
"s_barrier\n"
"ds_read_b64_tr_b16 a[236:237], v218 offset:4616\n"
"v_cvt_pk_bf16_f32 v99, v182, v183\n"
"v_mfma_f32_16x16x32_bf16 a[4:7], a[164:167], v[92:95], a[4:7]\n"
"ds_read_b64_tr_b16 a[238:239], v218 offset:5640\n"
"v_fma_mix_f32 v184, v100, v144, v152 op_sel:[0,0,0] op_sel_hi:[1,0,0]\n"
"v_mfma_f32_16x16x32_bf16 a[8:11], a[168:171], v[80:83], a[8:11]\n"
"s_add_u32 m0, s51, 98352\n"
"ds_read_b64_tr_b16 a[240:241], v218 offset:768\n"
"v_fma_mix_f32 v185, v100, v145, v153 op_sel:[1,0,0] op_sel_hi:[1,0,0]\n"
"v_mfma_f32_16x16x32_bf16 a[8:11], a[172:175], v[88:91], a[8:11]\n"
"ds_read_b64_tr_b16 a[242:243], v218 offset:1792\n"
"buffer_load_dwordx4 v217, s[20:23], s50 offen offset:0 lds\n"
"v_mfma_f32_16x16x32_bf16 a[12:15], a[168:171], v[84:87], a[12:15]\n"
"ds_read_b64_tr_b16 a[244:245], v218 offset:4864\n"
"v_fma_mix_f32 v186, v101, v146, v154 op_sel:[0,0,0] op_sel_hi:[1,0,0]\n"
"v_mfma_f32_16x16x32_bf16 a[12:15], a[172:175], v[92:95], a[12:15]\n"
"s_waitcnt lgkmcnt(11)\n"
"ds_read_b64_tr_b16 a[246:247], v218 offset:5888\n"
"v_fma_mix_f32 v187, v101, v147, v155 op_sel:[1,0,0] op_sel_hi:[1,0,0]\n"
"v_mfma_f32_16x16x32_bf16 a[16:19], a[176:179], v[80:83], a[16:19]\n"
"ds_read_b64_tr_b16 a[248:249], v218 offset:776\n"
"v_fma_mix_f32 v188, v102, v148, v156 op_sel:[0,0,0] op_sel_hi:[1,0,0]\n"
"v_mfma_f32_16x16x32_bf16 a[16:19], a[180:183], v[88:91], a[16:19]\n"
"ds_read_b64_tr_b16 a[250:251], v218 offset:1800\n"
"v_fma_mix_f32 v189, v102, v149, v157 op_sel:[1,0,0] op_sel_hi:[1,0,0]\n"
"v_mfma_f32_16x16x32_bf16 a[20:23], a[176:179], v[84:87], a[20:23]\n"
"s_add_u32 m0, s51, 106288\n"
"ds_read_b64_tr_b16 a[252:253], v218 offset:4872\n"
"v_fma_mix_f32 v190, v103, v150, v158 op_sel:[0,0,0] op_sel_hi:[1,0,0]\n"
"v_mfma_f32_16x16x32_bf16 a[20:23], a[180:183], v[92:95], a[20:23]\n"
"ds_read_b64_tr_b16 a[254:255], v218 offset:5896\n"
"buffer_load_dwordx4 v217, s[20:23], s50 offen offset:256 lds\n"
"v_mfma_f32_16x16x32_bf16 a[24:27], a[184:187], v[80:83], a[24:27]\n"
"s_add_u32 s50, s50, 32768\n"
"ds_read_b64_tr_b16 a[160:161], v218 offset:8192\n"
"v_fma_mix_f32 v191, v103, v151, v159 op_sel:[1,0,0] op_sel_hi:[1,0,0]\n"
"v_mfma_f32_16x16x32_bf16 a[24:27], a[188:191], v[88:91], a[24:27]\n"
"ds_read_b64_tr_b16 a[162:163], v218 offset:9216\n"
"ds_read_b128 v[144:147], v224 offset:3584\n"
"v_mfma_f32_16x16x32_bf16 a[28:31], a[184:187], v[84:87], a[28:31]\n"
"ds_read_b64_tr_b16 a[164:165], v218 offset:12288\n"
"v_cvt_pk_bf16_f32 v100, v184, v185\n"
"v_mfma_f32_16x16x32_bf16 a[28:31], a[188:191], v[92:95], a[28:31]\n"
"s_waitcnt lgkmcnt(12)\n"
"ds_read_b64_tr_b16 a[166:167], v218 offset:13312\n"
"v_cvt_pk_bf16_f32 v101, v186, v187\n"
"v_mfma_f32_16x16x32_bf16 a[32:35], a[224:227], v[80:83], a[32:35]\n"
"s_add_u32 m0, s51, 99376\n"
"ds_read_b64_tr_b16 a[168:169], v218 offset:8200\n"
"ds_read_b128 v[148:151], v224 offset:3648\n"
"v_mfma_f32_16x16x32_bf16 a[32:35], a[228:231], v[88:91], a[32:35]\n"
"ds_read_b64_tr_b16 a[170:171], v218 offset:9224\n"
"buffer_load_dwordx4 v217, s[20:23], s50 offen offset:0 lds\n"
"v_mfma_f32_16x16x32_bf16 a[36:39], a[224:227], v[84:87], a[36:39]\n"
"ds_read_b64_tr_b16 a[172:173], v218 offset:12296\n"
"v_cvt_pk_bf16_f32 v102, v188, v189\n"
"v_mfma_f32_16x16x32_bf16 a[36:39], a[228:231], v[92:95], a[36:39]\n"
"ds_read_b64_tr_b16 a[174:175], v218 offset:13320\n"
"ds_read_b128 v[152:155], v224 offset:3712\n"
"v_mfma_f32_16x16x32_bf16 a[40:43], a[232:235], v[80:83], a[40:43]\n"
"ds_read_b64_tr_b16 a[176:177], v218 offset:8448\n"
"v_cvt_pk_bf16_f32 v103, v190, v191\n"
"v_mfma_f32_16x16x32_bf16 a[40:43], a[236:239], v[88:91], a[40:43]\n"
"ds_read_b64_tr_b16 a[178:179], v218 offset:9472\n"
"v_fma_mix_f32 v176, v104, v160, v168 op_sel:[0,0,0] op_sel_hi:[1,0,0]\n"
"v_mfma_f32_16x16x32_bf16 a[44:47], a[232:235], v[84:87], a[44:47]\n"
"s_add_u32 m0, s51, 107312\n"
"ds_read_b64_tr_b16 a[180:181], v218 offset:12544\n"
"ds_read_b128 v[156:159], v224 offset:3776\n"
"v_mfma_f32_16x16x32_bf16 a[44:47], a[236:239], v[92:95], a[44:47]\n"
"s_waitcnt lgkmcnt(14)\n"
"ds_read_b64_tr_b16 a[182:183], v218 offset:13568\n"
"buffer_load_dwordx4 v217, s[20:23], s50 offen offset:256 lds\n"
"v_mfma_f32_16x16x32_bf16 a[48:51], a[240:243], v[80:83], a[48:51]\n"
"s_add_u32 s50, s50, 32768\n"
"ds_read_b64_tr_b16 a[184:185], v218 offset:8456\n"
"v_fma_mix_f32 v177, v104, v161, v169 op_sel:[1,0,0] op_sel_hi:[1,0,0]\n"
"v_mfma_f32_16x16x32_bf16 a[48:51], a[244:247], v[88:91], a[48:51]\n"
"ds_read_b64_tr_b16 a[186:187], v218 offset:9480\n"
"v_fma_mix_f32 v178, v105, v162, v170 op_sel:[0,0,0] op_sel_hi:[1,0,0]\n"
"v_mfma_f32_16x16x32_bf16 a[52:55], a[240:243], v[84:87], a[52:55]\n"
"ds_read_b64_tr_b16 a[188:189], v218 offset:12552\n"
"v_fma_mix_f32 v179, v105, v163, v171 op_sel:[1,0,0] op_sel_hi:[1,0,0]\n"
"v_mfma_f32_16x16x32_bf16 a[52:55], a[244:247], v[92:95], a[52:55]\n"
"ds_read_b64_tr_b16 a[190:191], v218 offset:13576\n"
"v_fma_mix_f32 v180, v106, v164, v172 op_sel:[0,0,0] op_sel_hi:[1,0,0]\n"
"v_mfma_f32_16x16x32_bf16 a[56:59], a[248:251], v[80:83], a[56:59]\n"
"s_add_u32 m0, s51, 114744\n"
"ds_read_b64_tr_b16 a[224:225], v218 offset:8704\n"
"v_fma_mix_f32 v181, v106, v165, v173 op_sel:[1,0,0] op_sel_hi:[1,0,0]\n"
"v_mfma_f32_16x16x32_bf16 a[56:59], a[252:255], v[88:91], a[56:59]\n"
"ds_read_b64_tr_b16 a[226:227], v218 offset:9728\n"
"buffer_load_dwordx4 v217, s[20:23], s50 offen offset:0 lds\n"
"v_mfma_f32_16x16x32_bf16 a[60:63], a[248:251], v[84:87], a[60:63]\n"
"ds_read_b64_tr_b16 a[228:229], v218 offset:12800\n"
"v_fma_mix_f32 v182, v107, v166, v174 op_sel:[0,0,0] op_sel_hi:[1,0,0]\n"
"v_mfma_f32_16x16x32_bf16 a[60:63], a[252:255], v[92:95], a[60:63]\n"
"s_waitcnt lgkmcnt(12)\n"
"ds_read_b64_tr_b16 a[230:231], v218 offset:13824\n"
"v_fma_mix_f32 v183, v107, v167, v175 op_sel:[1,0,0] op_sel_hi:[1,0,0]\n"
"v_mfma_f32_16x16x32_bf16 a[64:67], a[160:163], v[80:83], a[64:67]\n"
"ds_read_b64_tr_b16 a[232:233], v218 offset:8712\n"
"v_cvt_pk_bf16_f32 v104, v176, v177\n"
"v_mfma_f32_16x16x32_bf16 a[64:67], a[164:167], v[88:91], a[64:67]\n"
"ds_read_b64_tr_b16 a[234:235], v218 offset:9736\n"
"v_cvt_pk_bf16_f32 v105, v178, v179\n"
"v_mfma_f32_16x16x32_bf16 a[68:71], a[160:163], v[84:87], a[68:71]\n"
"s_add_u32 m0, s51, 122680\n"
"ds_read_b64_tr_b16 a[236:237], v218 offset:12808\n"
"v_cvt_pk_bf16_f32 v106, v180, v181\n"
"v_mfma_f32_16x16x32_bf16 a[68:71], a[164:167], v[92:95], a[68:71]\n"
"ds_read_b64_tr_b16 a[238:239], v218 offset:13832\n"
"buffer_load_dwordx4 v217, s[20:23], s50 offen offset:256 lds\n"
"v_mfma_f32_16x16x32_bf16 a[72:75], a[168:171], v[80:83], a[72:75]\n"
"s_add_u32 s50, s50, 32768\n"
"ds_read_b64_tr_b16 a[240:241], v218 offset:8960\n"
"v_cvt_pk_bf16_f32 v107, v182, v183\n"
"v_mfma_f32_16x16x32_bf16 a[72:75], a[172:175], v[88:91], a[72:75]\n"
"ds_read_b64_tr_b16 a[242:243], v218 offset:9984\n"
"v_fma_mix_f32 v184, v108, v160, v168 op_sel:[0,0,0] op_sel_hi:[1,0,0]\n"
"v_mfma_f32_16x16x32_bf16 a[76:79], a[168:171], v[84:87], a[76:79]\n"
"ds_read_b64_tr_b16 a[244:245], v218 offset:13056\n"
"v_fma_mix_f32 v185, v108, v161, v169 op_sel:[1,0,0] op_sel_hi:[1,0,0]\n"
"v_mfma_f32_16x16x32_bf16 a[76:79], a[172:175], v[92:95], a[76:79]\n"
"s_waitcnt lgkmcnt(11)\n"
"ds_read_b64_tr_b16 a[246:247], v218 offset:14080\n"
"v_fma_mix_f32 v186, v109, v162, v170 op_sel:[0,0,0] op_sel_hi:[1,0,0]\n"
"v_mfma_f32_16x16x32_bf16 a[80:83], a[176:179], v[80:83], a[80:83]\n"
"s_add_u32 m0, s51, 115768\n"
"ds_read_b64_tr_b16 a[248:249], v218 offset:8968\n"
"v_fma_mix_f32 v187, v109, v163, v171 op_sel:[1,0,0] op_sel_hi:[1,0,0]\n"
"v_mfma_f32_16x16x32_bf16 a[80:83], a[180:183], v[88:91], a[80:83]\n"
"ds_read_b64_tr_b16 a[250:251], v218 offset:9992\n"
"buffer_load_dwordx4 v217, s[20:23], s50 offen offset:0 lds\n"
"v_mfma_f32_16x16x32_bf16 a[84:87], a[176:179], v[84:87], a[84:87]\n"
"ds_read_b64_tr_b16 a[252:253], v218 offset:13064\n"
"v_fma_mix_f32 v188, v110, v164, v172 op_sel:[0,0,0] op_sel_hi:[1,0,0]\n"
"v_mfma_f32_16x16x32_bf16 a[84:87], a[180:183], v[92:95], a[84:87]\n"
"ds_read_b64_tr_b16 a[254:255], v218 offset:14088\n"
"v_fma_mix_f32 v189, v110, v165, v173 op_sel:[1,0,0] op_sel_hi:[1,0,0]\n"
"v_mfma_f32_16x16x32_bf16 a[88:91], a[184:187], v[80:83], a[88:91]\n"
"ds_read_b64_tr_b16 a[160:161], v218 offset:32784\n"
"v_fma_mix_f32 v190, v111, v166, v174 op_sel:[0,0,0] op_sel_hi:[1,0,0]\n"
"v_fma_mix_f32 v191, v111, v167, v175 op_sel:[1,0,0] op_sel_hi:[1,0,0]\n"
"v_mfma_f32_16x16x32_bf16 a[88:91], a[188:191], v[88:91], a[88:91]\n"
"ds_read_b64_tr_b16 a[162:163], v218 offset:33808\n"
"ds_read_b128 v[160:163], v224 offset:3840\n"
"v_mfma_f32_16x16x32_bf16 a[92:95], a[184:187], v[84:87], a[92:95]\n"
"s_add_u32 m0, s51, 123704\n"
"ds_read_b64_tr_b16 a[164:165], v218 offset:36880\n"
"v_cvt_pk_bf16_f32 v108, v184, v185\n"
"v_mfma_f32_16x16x32_bf16 a[92:95], a[188:191], v[92:95], a[92:95]\n"
"s_waitcnt lgkmcnt(12)\n"
"buffer_load_dwordx4 v217, s[20:23], s50 offen offset:256 lds\n"
"ds_read_b64_tr_b16 a[166:167], v218 offset:37904\n"
"v_mfma_f32_16x16x32_bf16 a[96:99], a[224:227], v[80:83], a[96:99]\n"
"s_add_u32 s50, s50, 425984\n"
"ds_read_b64_tr_b16 a[168:169], v218 offset:32792\n"
"ds_read_b128 v[164:167], v224 offset:3904\n"
"v_mfma_f32_16x16x32_bf16 a[96:99], a[228:231], v[88:91], a[96:99]\n"
"ds_read_b64_tr_b16 a[170:171], v218 offset:33816\n"
"v_cvt_pk_bf16_f32 v109, v186, v187\n"
"v_mfma_f32_16x16x32_bf16 a[100:103], a[224:227], v[84:87], a[100:103]\n"
"ds_read_b64_tr_b16 a[172:173], v218 offset:36888\n"
"v_cvt_pk_bf16_f32 v110, v188, v189\n"
"v_mfma_f32_16x16x32_bf16 a[100:103], a[228:231], v[92:95], a[100:103]\n"
"ds_read_b64_tr_b16 a[174:175], v218 offset:37912\n"
"ds_read_b128 v[168:171], v224 offset:3968\n"
"v_mfma_f32_16x16x32_bf16 a[104:107], a[232:235], v[80:83], a[104:107]\n"
"ds_read_b64_tr_b16 a[176:177], v218 offset:33040\n"
"v_cvt_pk_bf16_f32 v111, v190, v191\n"
"v_mfma_f32_16x16x32_bf16 a[104:107], a[236:239], v[88:91], a[104:107]\n"
"ds_read_b64_tr_b16 a[178:179], v218 offset:34064\n"
"v_fma_mix_f32 v176, v112, v144, v152 op_sel:[0,0,0] op_sel_hi:[1,0,0]\n"
"v_mfma_f32_16x16x32_bf16 a[108:111], a[232:235], v[84:87], a[108:111]\n"
"ds_read_b64_tr_b16 a[180:181], v218 offset:37136\n"
"ds_read_b128 v[172:175], v224 offset:4032\n"
"v_mfma_f32_16x16x32_bf16 a[108:111], a[236:239], v[92:95], a[108:111]\n"
"s_waitcnt lgkmcnt(14)\n"
"ds_read_b64_tr_b16 a[182:183], v218 offset:38160\n"
"v_fma_mix_f32 v177, v112, v145, v153 op_sel:[1,0,0] op_sel_hi:[1,0,0]\n"
"v_mfma_f32_16x16x32_bf16 a[112:115], a[240:243], v[80:83], a[112:115]\n"
"ds_read_b64_tr_b16 a[184:185], v218 offset:33048\n"
"v_fma_mix_f32 v178, v113, v146, v154 op_sel:[0,0,0] op_sel_hi:[1,0,0]\n"
"v_mfma_f32_16x16x32_bf16 a[112:115], a[244:247], v[88:91], a[112:115]\n"
"ds_read_b64_tr_b16 a[186:187], v218 offset:34072\n"
"v_fma_mix_f32 v179, v113, v147, v155 op_sel:[1,0,0] op_sel_hi:[1,0,0]\n"
"v_mfma_f32_16x16x32_bf16 a[116:119], a[240:243], v[84:87], a[116:119]\n"
"ds_read_b64_tr_b16 a[188:189], v218 offset:37144\n"
"v_fma_mix_f32 v180, v114, v148, v156 op_sel:[0,0,0] op_sel_hi:[1,0,0]\n"
"v_mfma_f32_16x16x32_bf16 a[116:119], a[244:247], v[92:95], a[116:119]\n"
"ds_read_b64_tr_b16 a[190:191], v218 offset:38168\n"
"v_fma_mix_f32 v181, v114, v149, v157 op_sel:[1,0,0] op_sel_hi:[1,0,0]\n"
"v_mfma_f32_16x16x32_bf16 a[120:123], a[248:251], v[80:83], a[120:123]\n"
"ds_read_b64_tr_b16 a[224:225], v218 offset:33296\n"
"v_fma_mix_f32 v182, v115, v150, v158 op_sel:[0,0,0] op_sel_hi:[1,0,0]\n"
"v_mfma_f32_16x16x32_bf16 a[120:123], a[252:255], v[88:91], a[120:123]\n"
"ds_read_b64_tr_b16 a[226:227], v218 offset:34320\n"
"v_fma_mix_f32 v183, v115, v151, v159 op_sel:[1,0,0] op_sel_hi:[1,0,0]\n"
"v_mfma_f32_16x16x32_bf16 a[124:127], a[248:251], v[84:87], a[124:127]\n"
"ds_read_b64_tr_b16 a[228:229], v218 offset:37392\n"
"v_cvt_pk_bf16_f32 v112, v176, v177\n"
"v_mfma_f32_16x16x32_bf16 a[124:127], a[252:255], v[92:95], a[124:127]\n"
"s_waitcnt lgkmcnt(12)\n"
"ds_read_b64_tr_b16 a[230:231], v218 offset:38416\n"
"v_cvt_pk_bf16_f32 v113, v178, v179\n"
"v_mfma_f32_16x16x32_bf16 a[0:3], a[160:163], v[96:99], a[0:3]\n"
"ds_read_b64_tr_b16 a[232:233], v218 offset:33304\n"
"v_cvt_pk_bf16_f32 v114, v180, v181\n"
"v_mfma_f32_16x16x32_bf16 a[0:3], a[164:167], v[104:107], a[0:3]\n"
"ds_read_b64_tr_b16 a[234:235], v218 offset:34328\n"
"v_cvt_pk_bf16_f32 v115, v182, v183\n"
"v_mfma_f32_16x16x32_bf16 a[4:7], a[160:163], v[100:103], a[4:7]\n"
"s_waitcnt vmcnt(8)\n"
"s_barrier\n"
"ds_read_b64_tr_b16 a[236:237], v218 offset:37400\n"
"v_fma_mix_f32 v184, v116, v144, v152 op_sel:[0,0,0] op_sel_hi:[1,0,0]\n"
"v_mfma_f32_16x16x32_bf16 a[4:7], a[164:167], v[108:111], a[4:7]\n"
"ds_read_b64_tr_b16 a[238:239], v218 offset:38424\n"
"v_fma_mix_f32 v185, v116, v145, v153 op_sel:[1,0,0] op_sel_hi:[1,0,0]\n"
"v_mfma_f32_16x16x32_bf16 a[8:11], a[168:171], v[96:99], a[8:11]\n"
"s_mov_b32 m0, s86\n"
"ds_read_b64_tr_b16 a[240:241], v218 offset:33552\n"
"v_fma_mix_f32 v186, v117, v146, v154 op_sel:[0,0,0] op_sel_hi:[1,0,0]\n"
"v_mfma_f32_16x16x32_bf16 a[8:11], a[172:175], v[104:107], a[8:11]\n"
"ds_read_b64_tr_b16 a[242:243], v218 offset:34576\n"
"buffer_load_dwordx4 v246, s[8:11], s84 offen offset:0 lds\n"
"v_mfma_f32_16x16x32_bf16 a[12:15], a[168:171], v[100:103], a[12:15]\n"
"ds_read_b64_tr_b16 a[244:245], v218 offset:37648\n"
"v_fma_mix_f32 v187, v117, v147, v155 op_sel:[1,0,0] op_sel_hi:[1,0,0]\n"
"v_mfma_f32_16x16x32_bf16 a[12:15], a[172:175], v[108:111], a[12:15]\n"
"s_waitcnt lgkmcnt(11)\n"
"ds_read_b64_tr_b16 a[246:247], v218 offset:38672\n"
"v_fma_mix_f32 v188, v118, v148, v156 op_sel:[0,0,0] op_sel_hi:[1,0,0]\n"
"v_mfma_f32_16x16x32_bf16 a[16:19], a[176:179], v[96:99], a[16:19]\n"
"ds_read_b64_tr_b16 a[248:249], v218 offset:33560\n"
"v_fma_mix_f32 v189, v118, v149, v157 op_sel:[1,0,0] op_sel_hi:[1,0,0]\n"
"v_mfma_f32_16x16x32_bf16 a[16:19], a[180:183], v[104:107], a[16:19]\n"
"ds_read_b64_tr_b16 a[250:251], v218 offset:34584\n"
"v_fma_mix_f32 v190, v119, v150, v158 op_sel:[0,0,0] op_sel_hi:[1,0,0]\n"
"v_mfma_f32_16x16x32_bf16 a[20:23], a[176:179], v[100:103], a[20:23]\n"
"ds_read_b64_tr_b16 a[252:253], v218 offset:37656\n"
"v_fma_mix_f32 v191, v119, v151, v159 op_sel:[1,0,0] op_sel_hi:[1,0,0]\n"
"v_mfma_f32_16x16x32_bf16 a[20:23], a[180:183], v[108:111], a[20:23]\n"
"ds_read_b64_tr_b16 a[254:255], v218 offset:38680\n"
"buffer_load_dwordx4 v246, s[8:11], s84 offen offset:1024 lds\n"
"v_mfma_f32_16x16x32_bf16 a[24:27], a[184:187], v[96:99], a[24:27]\n"
"ds_read_b64_tr_b16 a[160:161], v218 offset:40976\n"
"ds_read_b128 v[144:147], v224 offset:4096\n"
"v_mfma_f32_16x16x32_bf16 a[24:27], a[188:191], v[104:107], a[24:27]\n"
"ds_read_b64_tr_b16 a[162:163], v218 offset:42000\n"
"v_cvt_pk_bf16_f32 v116, v184, v185\n"
"v_mfma_f32_16x16x32_bf16 a[28:31], a[184:187], v[100:103], a[28:31]\n"
"ds_read_b64_tr_b16 a[164:165], v218 offset:45072\n"
"v_cvt_pk_bf16_f32 v117, v186, v187\n"
"v_mfma_f32_16x16x32_bf16 a[28:31], a[188:191], v[108:111], a[28:31]\n"
"s_waitcnt lgkmcnt(12)\n"
"ds_read_b64_tr_b16 a[166:167], v218 offset:46096\n"
"ds_read_b128 v[148:151], v224 offset:4160\n"
"v_mfma_f32_16x16x32_bf16 a[32:35], a[224:227], v[96:99], a[32:35]\n"
"ds_read_b64_tr_b16 a[168:169], v218 offset:40984\n"
"v_cvt_pk_bf16_f32 v118, v188, v189\n"
"v_mfma_f32_16x16x32_bf16 a[32:35], a[228:231], v[104:107], a[32:35]\n"
"ds_read_b64_tr_b16 a[170:171], v218 offset:42008\n"
"buffer_load_dwordx4 v247, s[8:11], s84 offen offset:2048 lds\n"
"v_mfma_f32_16x16x32_bf16 a[36:39], a[224:227], v[100:103], a[36:39]\n"
"ds_read_b64_tr_b16 a[172:173], v218 offset:45080\n"
"ds_read_b128 v[152:155], v224 offset:4224\n"
"v_mfma_f32_16x16x32_bf16 a[36:39], a[228:231], v[108:111], a[36:39]\n"
"ds_read_b64_tr_b16 a[174:175], v218 offset:46104\n"
"v_cvt_pk_bf16_f32 v119, v190, v191\n"
"v_mfma_f32_16x16x32_bf16 a[40:43], a[232:235], v[96:99], a[40:43]\n"
"ds_read_b64_tr_b16 a[176:177], v218 offset:41232\n"
"v_fma_mix_f32 v176, v120, v160, v168 op_sel:[0,0,0] op_sel_hi:[1,0,0]\n"
"v_mfma_f32_16x16x32_bf16 a[40:43], a[236:239], v[104:107], a[40:43]\n"
"ds_read_b64_tr_b16 a[178:179], v218 offset:42256\n"
"ds_read_b128 v[156:159], v224 offset:4288\n"
"v_mfma_f32_16x16x32_bf16 a[44:47], a[232:235], v[100:103], a[44:47]\n"
"ds_read_b64_tr_b16 a[180:181], v218 offset:45328\n"
"v_fma_mix_f32 v177, v120, v161, v169 op_sel:[1,0,0] op_sel_hi:[1,0,0]\n"
"v_mfma_f32_16x16x32_bf16 a[44:47], a[236:239], v[108:111], a[44:47]\n"
"s_waitcnt lgkmcnt(14)\n"
"ds_read_b64_tr_b16 a[182:183], v218 offset:46352\n"
"buffer_load_dwordx4 v247, s[8:11], s84 offen offset:3072 lds\n"
"v_mfma_f32_16x16x32_bf16 a[48:51], a[240:243], v[96:99], a[48:51]\n"
"ds_read_b64_tr_b16 a[184:185], v218 offset:41240\n"
"v_fma_mix_f32 v178, v121, v162, v170 op_sel:[0,0,0] op_sel_hi:[1,0,0]\n"
"v_mfma_f32_16x16x32_bf16 a[48:51], a[244:247], v[104:107], a[48:51]\n"
"ds_read_b64_tr_b16 a[186:187], v218 offset:42264\n"
"v_fma_mix_f32 v179, v121, v163, v171 op_sel:[1,0,0] op_sel_hi:[1,0,0]\n"
"v_mfma_f32_16x16x32_bf16 a[52:55], a[240:243], v[100:103], a[52:55]\n"
"ds_read_b64_tr_b16 a[188:189], v218 offset:45336\n"
"v_fma_mix_f32 v180, v122, v164, v172 op_sel:[0,0,0] op_sel_hi:[1,0,0]\n"
"v_mfma_f32_16x16x32_bf16 a[52:55], a[244:247], v[108:111], a[52:55]\n"
"ds_read_b64_tr_b16 a[190:191], v218 offset:46360\n"
"v_fma_mix_f32 v181, v122, v165, v173 op_sel:[1,0,0] op_sel_hi:[1,0,0]\n"
"v_mfma_f32_16x16x32_bf16 a[56:59], a[248:251], v[96:99], a[56:59]\n"
"s_add_u32 m0, s86, 16384\n"
"ds_read_b64_tr_b16 a[224:225], v218 offset:41488\n"
"v_fma_mix_f32 v182, v123, v166, v174 op_sel:[0,0,0] op_sel_hi:[1,0,0]\n"
"v_mfma_f32_16x16x32_bf16 a[56:59], a[252:255], v[104:107], a[56:59]\n"
"ds_read_b64_tr_b16 a[226:227], v218 offset:42512\n"
"buffer_load_dwordx4 v246, s[12:15], s84 offen offset:0 lds\n"
"v_mfma_f32_16x16x32_bf16 a[60:63], a[248:251], v[100:103], a[60:63]\n"
"ds_read_b64_tr_b16 a[228:229], v218 offset:45584\n"
"v_fma_mix_f32 v183, v123, v167, v175 op_sel:[1,0,0] op_sel_hi:[1,0,0]\n"
"v_mfma_f32_16x16x32_bf16 a[60:63], a[252:255], v[108:111], a[60:63]\n"
"s_waitcnt lgkmcnt(12)\n"
"ds_read_b64_tr_b16 a[230:231], v218 offset:46608\n"
"v_cvt_pk_bf16_f32 v120, v176, v177\n"
"v_mfma_f32_16x16x32_bf16 a[64:67], a[160:163], v[96:99], a[64:67]\n"
"ds_read_b64_tr_b16 a[232:233], v218 offset:41496\n"
"v_cvt_pk_bf16_f32 v121, v178, v179\n"
"v_mfma_f32_16x16x32_bf16 a[64:67], a[164:167], v[104:107], a[64:67]\n"
"ds_read_b64_tr_b16 a[234:235], v218 offset:42520\n"
"v_cvt_pk_bf16_f32 v122, v180, v181\n"
"v_mfma_f32_16x16x32_bf16 a[68:71], a[160:163], v[100:103], a[68:71]\n"
"ds_read_b64_tr_b16 a[236:237], v218 offset:45592\n"
"v_cvt_pk_bf16_f32 v123, v182, v183\n"
"v_mfma_f32_16x16x32_bf16 a[68:71], a[164:167], v[108:111], a[68:71]\n"
"ds_read_b64_tr_b16 a[238:239], v218 offset:46616\n"
"buffer_load_dwordx4 v247, s[12:15], s84 offen offset:1024 lds\n"
"v_mfma_f32_16x16x32_bf16 a[72:75], a[168:171], v[96:99], a[72:75]\n"
"ds_read_b64_tr_b16 a[240:241], v218 offset:41744\n"
"v_fma_mix_f32 v184, v124, v160, v168 op_sel:[0,0,0] op_sel_hi:[1,0,0]\n"
"v_mfma_f32_16x16x32_bf16 a[72:75], a[172:175], v[104:107], a[72:75]\n"
"ds_read_b64_tr_b16 a[242:243], v218 offset:42768\n"
"v_fma_mix_f32 v185, v124, v161, v169 op_sel:[1,0,0] op_sel_hi:[1,0,0]\n"
"v_mfma_f32_16x16x32_bf16 a[76:79], a[168:171], v[100:103], a[76:79]\n"
"ds_read_b64_tr_b16 a[244:245], v218 offset:45840\n"
"v_fma_mix_f32 v186, v125, v162, v170 op_sel:[0,0,0] op_sel_hi:[1,0,0]\n"
"v_mfma_f32_16x16x32_bf16 a[76:79], a[172:175], v[108:111], a[76:79]\n"
"s_waitcnt lgkmcnt(11)\n"
"ds_read_b64_tr_b16 a[246:247], v218 offset:46864\n"
"v_fma_mix_f32 v187, v125, v163, v171 op_sel:[1,0,0] op_sel_hi:[1,0,0]\n"
"v_mfma_f32_16x16x32_bf16 a[80:83], a[176:179], v[96:99], a[80:83]\n"
"ds_read_b64_tr_b16 a[248:249], v218 offset:41752\n"
"v_fma_mix_f32 v188, v126, v164, v172 op_sel:[0,0,0] op_sel_hi:[1,0,0]\n"
"v_mfma_f32_16x16x32_bf16 a[80:83], a[180:183], v[104:107], a[80:83]\n"
"ds_read_b64_tr_b16 a[250:251], v218 offset:42776\n"
"buffer_load_dwordx4 v246, s[12:15], s84 offen offset:2048 lds\n"
"v_mfma_f32_16x16x32_bf16 a[84:87], a[176:179], v[100:103], a[84:87]\n"
"ds_read_b64_tr_b16 a[252:253], v218 offset:45848\n"
"v_fma_mix_f32 v189, v126, v165, v173 op_sel:[1,0,0] op_sel_hi:[1,0,0]\n"
"v_mfma_f32_16x16x32_bf16 a[84:87], a[180:183], v[108:111], a[84:87]\n"
"ds_read_b64_tr_b16 a[254:255], v218 offset:46872\n"
"v_fma_mix_f32 v190, v127, v166, v174 op_sel:[0,0,0] op_sel_hi:[1,0,0]\n"
"v_mfma_f32_16x16x32_bf16 a[88:91], a[184:187], v[96:99], a[88:91]\n"
"ds_read_b64_tr_b16 a[160:161], v219 offset:0\n"
"v_fma_mix_f32 v191, v127, v167, v175 op_sel:[1,0,0] op_sel_hi:[1,0,0]\n"
"v_cvt_pk_bf16_f32 v124, v184, v185\n"
"v_mfma_f32_16x16x32_bf16 a[88:91], a[188:191], v[104:107], a[88:91]\n"
"ds_read_b64_tr_b16 a[162:163], v219 offset:1024\n"
"ds_read_b128 v[160:163], v224 offset:4352\n"
"v_mfma_f32_16x16x32_bf16 a[92:95], a[184:187], v[100:103], a[92:95]\n"
"ds_read_b64_tr_b16 a[164:165], v219 offset:4096\n"
"v_cvt_pk_bf16_f32 v125, v186, v187\n"
"v_mfma_f32_16x16x32_bf16 a[92:95], a[188:191], v[108:111], a[92:95]\n"
"s_waitcnt lgkmcnt(12)\n"
"buffer_load_dwordx4 v247, s[12:15], s84 offen offset:3072 lds\n"
"ds_read_b64_tr_b16 a[166:167], v219 offset:5120\n"
"v_mfma_f32_16x16x32_bf16 a[96:99], a[224:227], v[96:99], a[96:99]\n"
"s_add_u32 s84, s84, 16384\n"
"ds_read_b64_tr_b16 a[168:169], v219 offset:8\n"
"ds_read_b128 v[164:167], v224 offset:4416\n"
"v_mfma_f32_16x16x32_bf16 a[96:99], a[228:231], v[104:107], a[96:99]\n"
"ds_read_b64_tr_b16 a[170:171], v219 offset:1032\n"
"v_cvt_pk_bf16_f32 v126, v188, v189\n"
"v_mfma_f32_16x16x32_bf16 a[100:103], a[224:227], v[100:103], a[100:103]\n"
"ds_read_b64_tr_b16 a[172:173], v219 offset:4104\n"
"v_cvt_pk_bf16_f32 v127, v190, v191\n"
"v_mfma_f32_16x16x32_bf16 a[100:103], a[228:231], v[108:111], a[100:103]\n"
"ds_read_b64_tr_b16 a[174:175], v219 offset:5128\n"
"ds_read_b128 v[168:171], v224 offset:4480\n"
"v_mfma_f32_16x16x32_bf16 a[104:107], a[232:235], v[96:99], a[104:107]\n"
"ds_read_b64_tr_b16 a[176:177], v219 offset:256\n"
"v_fma_mix_f32 v176, v128, v144, v152 op_sel:[0,0,0] op_sel_hi:[1,0,0]\n"
"v_mfma_f32_16x16x32_bf16 a[104:107], a[236:239], v[104:107], a[104:107]\n"
"ds_read_b64_tr_b16 a[178:179], v219 offset:1280\n"
"v_fma_mix_f32 v177, v128, v145, v153 op_sel:[1,0,0] op_sel_hi:[1,0,0]\n"
"v_mfma_f32_16x16x32_bf16 a[108:111], a[232:235], v[100:103], a[108:111]\n"
"ds_read_b64_tr_b16 a[180:181], v219 offset:4352\n"
"ds_read_b128 v[172:175], v224 offset:4544\n"
"v_mfma_f32_16x16x32_bf16 a[108:111], a[236:239], v[108:111], a[108:111]\n"
"s_waitcnt lgkmcnt(14)\n"
"ds_read_b64_tr_b16 a[182:183], v219 offset:5376\n"
"v_fma_mix_f32 v178, v129, v146, v154 op_sel:[0,0,0] op_sel_hi:[1,0,0]\n"
"v_mfma_f32_16x16x32_bf16 a[112:115], a[240:243], v[96:99], a[112:115]\n"
"ds_read_b64_tr_b16 a[184:185], v219 offset:264\n"
"v_fma_mix_f32 v179, v129, v147, v155 op_sel:[1,0,0] op_sel_hi:[1,0,0]\n"
"v_mfma_f32_16x16x32_bf16 a[112:115], a[244:247], v[104:107], a[112:115]\n"
"ds_read_b64_tr_b16 a[186:187], v219 offset:1288\n"
"v_fma_mix_f32 v180, v130, v148, v156 op_sel:[0,0,0] op_sel_hi:[1,0,0]\n"
"v_mfma_f32_16x16x32_bf16 a[116:119], a[240:243], v[100:103], a[116:119]\n"
"ds_read_b64_tr_b16 a[188:189], v219 offset:4360\n"
"v_fma_mix_f32 v181, v130, v149, v157 op_sel:[1,0,0] op_sel_hi:[1,0,0]\n"
"v_mfma_f32_16x16x32_bf16 a[116:119], a[244:247], v[108:111], a[116:119]\n"
"ds_read_b64_tr_b16 a[190:191], v219 offset:5384\n"
"v_fma_mix_f32 v182, v131, v150, v158 op_sel:[0,0,0] op_sel_hi:[1,0,0]\n"
"v_mfma_f32_16x16x32_bf16 a[120:123], a[248:251], v[96:99], a[120:123]\n"
"ds_read_b64_tr_b16 a[224:225], v219 offset:512\n"
"v_fma_mix_f32 v183, v131, v151, v159 op_sel:[1,0,0] op_sel_hi:[1,0,0]\n"
"v_mfma_f32_16x16x32_bf16 a[120:123], a[252:255], v[104:107], a[120:123]\n"
"ds_read_b64_tr_b16 a[226:227], v219 offset:1536\n"
"v_cvt_pk_bf16_f32 v128, v176, v177\n"
"v_mfma_f32_16x16x32_bf16 a[124:127], a[248:251], v[100:103], a[124:127]\n"
"ds_read_b64_tr_b16 a[228:229], v219 offset:4608\n"
"v_cvt_pk_bf16_f32 v129, v178, v179\n"
"v_mfma_f32_16x16x32_bf16 a[124:127], a[252:255], v[108:111], a[124:127]\n"
"s_waitcnt lgkmcnt(12)\n"
"ds_read_b64_tr_b16 a[230:231], v219 offset:5632\n"
"v_cvt_pk_bf16_f32 v130, v180, v181\n"
"v_mfma_f32_16x16x32_bf16 a[0:3], a[160:163], v[112:115], a[0:3]\n"
"ds_read_b64_tr_b16 a[232:233], v219 offset:520\n"
"v_cvt_pk_bf16_f32 v131, v182, v183\n"
"v_mfma_f32_16x16x32_bf16 a[0:3], a[164:167], v[120:123], a[0:3]\n"
"ds_read_b64_tr_b16 a[234:235], v219 offset:1544\n"
"v_fma_mix_f32 v184, v132, v144, v152 op_sel:[0,0,0] op_sel_hi:[1,0,0]\n"
"v_mfma_f32_16x16x32_bf16 a[4:7], a[160:163], v[116:119], a[4:7]\n"
"s_waitcnt vmcnt(8)\n"
"s_barrier\n"
"ds_read_b64_tr_b16 a[236:237], v219 offset:4616\n"
"v_fma_mix_f32 v185, v132, v145, v153 op_sel:[1,0,0] op_sel_hi:[1,0,0]\n"
"v_mfma_f32_16x16x32_bf16 a[4:7], a[164:167], v[124:127], a[4:7]\n"
"ds_read_b64_tr_b16 a[238:239], v219 offset:5640\n"
"v_fma_mix_f32 v186, v133, v146, v154 op_sel:[0,0,0] op_sel_hi:[1,0,0]\n"
"v_mfma_f32_16x16x32_bf16 a[8:11], a[168:171], v[112:115], a[8:11]\n"
"s_add_u32 m0, s86, 32768\n"
"ds_read_b64_tr_b16 a[240:241], v219 offset:768\n"
"v_fma_mix_f32 v187, v133, v147, v155 op_sel:[1,0,0] op_sel_hi:[1,0,0]\n"
"v_mfma_f32_16x16x32_bf16 a[8:11], a[172:175], v[120:123], a[8:11]\n"
"ds_read_b64_tr_b16 a[242:243], v219 offset:1792\n"
"buffer_load_dwordx4 v246, s[8:11], s84 offen offset:0 lds\n"
"v_mfma_f32_16x16x32_bf16 a[12:15], a[168:171], v[116:119], a[12:15]\n"
"ds_read_b64_tr_b16 a[244:245], v219 offset:4864\n"
"v_fma_mix_f32 v188, v134, v148, v156 op_sel:[0,0,0] op_sel_hi:[1,0,0]\n"
"v_mfma_f32_16x16x32_bf16 a[12:15], a[172:175], v[124:127], a[12:15]\n"
"s_waitcnt lgkmcnt(11)\n"
"ds_read_b64_tr_b16 a[246:247], v219 offset:5888\n"
"v_fma_mix_f32 v189, v134, v149, v157 op_sel:[1,0,0] op_sel_hi:[1,0,0]\n"
"v_mfma_f32_16x16x32_bf16 a[16:19], a[176:179], v[112:115], a[16:19]\n"
"ds_read_b64_tr_b16 a[248:249], v219 offset:776\n"
"v_fma_mix_f32 v190, v135, v150, v158 op_sel:[0,0,0] op_sel_hi:[1,0,0]\n"
"v_mfma_f32_16x16x32_bf16 a[16:19], a[180:183], v[120:123], a[16:19]\n"
"ds_read_b64_tr_b16 a[250:251], v219 offset:1800\n"
"v_fma_mix_f32 v191, v135, v151, v159 op_sel:[1,0,0] op_sel_hi:[1,0,0]\n"
"v_mfma_f32_16x16x32_bf16 a[20:23], a[176:179], v[116:119], a[20:23]\n"
"ds_read_b64_tr_b16 a[252:253], v219 offset:4872\n"
"v_cvt_pk_bf16_f32 v132, v184, v185\n"
"v_mfma_f32_16x16x32_bf16 a[20:23], a[180:183], v[124:127], a[20:23]\n"
"ds_read_b64_tr_b16 a[254:255], v219 offset:5896\n"
"buffer_load_dwordx4 v246, s[8:11], s84 offen offset:1024 lds\n"
"v_mfma_f32_16x16x32_bf16 a[24:27], a[184:187], v[112:115], a[24:27]\n"
"ds_read_b64_tr_b16 a[160:161], v219 offset:8192\n"
"v_cvt_pk_bf16_f32 v133, v186, v187\n"
"v_mfma_f32_16x16x32_bf16 a[24:27], a[188:191], v[120:123], a[24:27]\n"
"ds_read_b64_tr_b16 a[162:163], v219 offset:9216\n"
"v_cvt_pk_bf16_f32 v134, v188, v189\n"
"v_mfma_f32_16x16x32_bf16 a[28:31], a[184:187], v[116:119], a[28:31]\n"
"ds_read_b64_tr_b16 a[164:165], v219 offset:12288\n"
"v_cvt_pk_bf16_f32 v135, v190, v191\n"
"v_mfma_f32_16x16x32_bf16 a[28:31], a[188:191], v[124:127], a[28:31]\n"
"s_waitcnt lgkmcnt(11)\n"
"ds_read_b64_tr_b16 a[166:167], v219 offset:13312\n"
"v_fma_mix_f32 v176, v136, v160, v168 op_sel:[0,0,0] op_sel_hi:[1,0,0]\n"
"v_mfma_f32_16x16x32_bf16 a[32:35], a[224:227], v[112:115], a[32:35]\n"
"ds_read_b64_tr_b16 a[168:169], v219 offset:8200\n"
"v_fma_mix_f32 v177, v136, v161, v169 op_sel:[1,0,0] op_sel_hi:[1,0,0]\n"
"v_mfma_f32_16x16x32_bf16 a[32:35], a[228:231], v[120:123], a[32:35]\n"
"ds_read_b64_tr_b16 a[170:171], v219 offset:9224\n"
"buffer_load_dwordx4 v247, s[8:11], s84 offen offset:2048 lds\n"
"v_mfma_f32_16x16x32_bf16 a[36:39], a[224:227], v[116:119], a[36:39]\n"
"ds_read_b64_tr_b16 a[172:173], v219 offset:12296\n"
"v_fma_mix_f32 v178, v137, v162, v170 op_sel:[0,0,0] op_sel_hi:[1,0,0]\n"
"v_mfma_f32_16x16x32_bf16 a[36:39], a[228:231], v[124:127], a[36:39]\n"
"ds_read_b64_tr_b16 a[174:175], v219 offset:13320\n"
"v_fma_mix_f32 v179, v137, v163, v171 op_sel:[1,0,0] op_sel_hi:[1,0,0]\n"
"v_mfma_f32_16x16x32_bf16 a[40:43], a[232:235], v[112:115], a[40:43]\n"
"ds_read_b64_tr_b16 a[176:177], v219 offset:8448\n"
"v_fma_mix_f32 v180, v138, v164, v172 op_sel:[0,0,0] op_sel_hi:[1,0,0]\n"
"v_mfma_f32_16x16x32_bf16 a[40:43], a[236:239], v[120:123], a[40:43]\n"
"ds_read_b64_tr_b16 a[178:179], v219 offset:9472\n"
"v_fma_mix_f32 v181, v138, v165, v173 op_sel:[1,0,0] op_sel_hi:[1,0,0]\n"
"v_mfma_f32_16x16x32_bf16 a[44:47], a[232:235], v[116:119], a[44:47]\n"
"ds_read_b64_tr_b16 a[180:181], v219 offset:12544\n"
"v_fma_mix_f32 v182, v139, v166, v174 op_sel:[0,0,0] op_sel_hi:[1,0,0]\n"
"v_mfma_f32_16x16x32_bf16 a[44:47], a[236:239], v[124:127], a[44:47]\n"
"s_waitcnt lgkmcnt(11)\n"
"ds_read_b64_tr_b16 a[182:183], v219 offset:13568\n"
"buffer_load_dwordx4 v247, s[8:11], s84 offen offset:3072 lds\n"
"v_mfma_f32_16x16x32_bf16 a[48:51], a[240:243], v[112:115], a[48:51]\n"
"ds_read_b64_tr_b16 a[184:185], v219 offset:8456\n"
"v_fma_mix_f32 v183, v139, v167, v175 op_sel:[1,0,0] op_sel_hi:[1,0,0]\n"
"v_mfma_f32_16x16x32_bf16 a[48:51], a[244:247], v[120:123], a[48:51]\n"
"ds_read_b64_tr_b16 a[186:187], v219 offset:9480\n"
"v_cvt_pk_bf16_f32 v136, v176, v177\n"
"v_mfma_f32_16x16x32_bf16 a[52:55], a[240:243], v[116:119], a[52:55]\n"
"ds_read_b64_tr_b16 a[188:189], v219 offset:12552\n"
"v_cvt_pk_bf16_f32 v137, v178, v179\n"
"v_mfma_f32_16x16x32_bf16 a[52:55], a[244:247], v[124:127], a[52:55]\n"
"ds_read_b64_tr_b16 a[190:191], v219 offset:13576\n"
"v_cvt_pk_bf16_f32 v138, v180, v181\n"
"v_mfma_f32_16x16x32_bf16 a[56:59], a[248:251], v[112:115], a[56:59]\n"
"s_add_u32 m0, s86, 49152\n"
"ds_read_b64_tr_b16 a[224:225], v219 offset:8704\n"
"v_cvt_pk_bf16_f32 v139, v182, v183\n"
"v_mfma_f32_16x16x32_bf16 a[56:59], a[252:255], v[120:123], a[56:59]\n"
"ds_read_b64_tr_b16 a[226:227], v219 offset:9728\n"
"buffer_load_dwordx4 v246, s[12:15], s84 offen offset:0 lds\n"
"v_mfma_f32_16x16x32_bf16 a[60:63], a[248:251], v[116:119], a[60:63]\n"
"ds_read_b64_tr_b16 a[228:229], v219 offset:12800\n"
"v_fma_mix_f32 v184, v140, v160, v168 op_sel:[0,0,0] op_sel_hi:[1,0,0]\n"
"v_mfma_f32_16x16x32_bf16 a[60:63], a[252:255], v[124:127], a[60:63]\n"
"s_waitcnt lgkmcnt(11)\n"
"ds_read_b64_tr_b16 a[230:231], v219 offset:13824\n"
"v_fma_mix_f32 v185, v140, v161, v169 op_sel:[1,0,0] op_sel_hi:[1,0,0]\n"
"v_mfma_f32_16x16x32_bf16 a[64:67], a[160:163], v[112:115], a[64:67]\n"
"ds_read_b64_tr_b16 a[232:233], v219 offset:8712\n"
"v_fma_mix_f32 v186, v141, v162, v170 op_sel:[0,0,0] op_sel_hi:[1,0,0]\n"
"v_mfma_f32_16x16x32_bf16 a[64:67], a[164:167], v[120:123], a[64:67]\n"
"ds_read_b64_tr_b16 a[234:235], v219 offset:9736\n"
"v_fma_mix_f32 v187, v141, v163, v171 op_sel:[1,0,0] op_sel_hi:[1,0,0]\n"
"v_mfma_f32_16x16x32_bf16 a[68:71], a[160:163], v[116:119], a[68:71]\n"
"ds_read_b64_tr_b16 a[236:237], v219 offset:12808\n"
"v_fma_mix_f32 v188, v142, v164, v172 op_sel:[0,0,0] op_sel_hi:[1,0,0]\n"
"v_mfma_f32_16x16x32_bf16 a[68:71], a[164:167], v[124:127], a[68:71]\n"
"ds_read_b64_tr_b16 a[238:239], v219 offset:13832\n"
"buffer_load_dwordx4 v247, s[12:15], s84 offen offset:1024 lds\n"
"v_mfma_f32_16x16x32_bf16 a[72:75], a[168:171], v[112:115], a[72:75]\n"
"ds_read_b64_tr_b16 a[240:241], v219 offset:8960\n"
"v_fma_mix_f32 v189, v142, v165, v173 op_sel:[1,0,0] op_sel_hi:[1,0,0]\n"
"v_mfma_f32_16x16x32_bf16 a[72:75], a[172:175], v[120:123], a[72:75]\n"
"ds_read_b64_tr_b16 a[242:243], v219 offset:9984\n"
"v_fma_mix_f32 v190, v143, v166, v174 op_sel:[0,0,0] op_sel_hi:[1,0,0]\n"
"v_mfma_f32_16x16x32_bf16 a[76:79], a[168:171], v[116:119], a[76:79]\n"
"ds_read_b64_tr_b16 a[244:245], v219 offset:13056\n"
"v_fma_mix_f32 v191, v143, v167, v175 op_sel:[1,0,0] op_sel_hi:[1,0,0]\n"
"v_mfma_f32_16x16x32_bf16 a[76:79], a[172:175], v[124:127], a[76:79]\n"
"s_waitcnt lgkmcnt(11)\n"
"ds_read_b64_tr_b16 a[246:247], v219 offset:14080\n"
"v_cvt_pk_bf16_f32 v140, v184, v185\n"
"v_mfma_f32_16x16x32_bf16 a[80:83], a[176:179], v[112:115], a[80:83]\n"
"ds_read_b64_tr_b16 a[248:249], v219 offset:8968\n"
"v_cvt_pk_bf16_f32 v141, v186, v187\n"
"v_mfma_f32_16x16x32_bf16 a[80:83], a[180:183], v[120:123], a[80:83]\n"
"ds_read_b64_tr_b16 a[250:251], v219 offset:9992\n"
"buffer_load_dwordx4 v246, s[12:15], s84 offen offset:2048 lds\n"
"v_mfma_f32_16x16x32_bf16 a[84:87], a[176:179], v[116:119], a[84:87]\n"
"ds_read_b64_tr_b16 a[252:253], v219 offset:13064\n"
"v_cvt_pk_bf16_f32 v142, v188, v189\n"
"v_mfma_f32_16x16x32_bf16 a[84:87], a[180:183], v[124:127], a[84:87]\n"
"ds_read_b64_tr_b16 a[254:255], v219 offset:14088\n"
"v_cvt_pk_bf16_f32 v143, v190, v191\n"
"v_mfma_f32_16x16x32_bf16 a[88:91], a[184:187], v[112:115], a[88:91]\n"
"ds_read_b64_tr_b16 a[160:161], v219 offset:32784\n"
"v_mfma_f32_16x16x32_bf16 a[88:91], a[188:191], v[120:123], a[88:91]\n"
"ds_read_b64_tr_b16 a[162:163], v219 offset:33808\n"
"v_mfma_f32_16x16x32_bf16 a[92:95], a[184:187], v[116:119], a[92:95]\n"
"ds_read_b64_tr_b16 a[164:165], v219 offset:36880\n"
"v_mfma_f32_16x16x32_bf16 a[92:95], a[188:191], v[124:127], a[92:95]\n"
"s_waitcnt lgkmcnt(11)\n"
"buffer_load_dwordx4 v247, s[12:15], s84 offen offset:3072 lds\n"
"ds_read_b64_tr_b16 a[166:167], v219 offset:37904\n"
"v_mfma_f32_16x16x32_bf16 a[96:99], a[224:227], v[112:115], a[96:99]\n"
"s_add_u32 s84, s84, 16384\n"
"ds_read_b64_tr_b16 a[168:169], v219 offset:32792\n"
"v_mfma_f32_16x16x32_bf16 a[96:99], a[228:231], v[120:123], a[96:99]\n"
"ds_read_b64_tr_b16 a[170:171], v219 offset:33816\n"
"v_mfma_f32_16x16x32_bf16 a[100:103], a[224:227], v[116:119], a[100:103]\n"
"ds_read_b64_tr_b16 a[172:173], v219 offset:36888\n"
"v_mfma_f32_16x16x32_bf16 a[100:103], a[228:231], v[124:127], a[100:103]\n"
"ds_read_b64_tr_b16 a[174:175], v219 offset:37912\n"
"v_mfma_f32_16x16x32_bf16 a[104:107], a[232:235], v[112:115], a[104:107]\n"
"ds_read_b64_tr_b16 a[176:177], v219 offset:33040\n"
"v_mfma_f32_16x16x32_bf16 a[104:107], a[236:239], v[120:123], a[104:107]\n"
"ds_read_b64_tr_b16 a[178:179], v219 offset:34064\n"
"v_mfma_f32_16x16x32_bf16 a[108:111], a[232:235], v[116:119], a[108:111]\n"
"ds_read_b64_tr_b16 a[180:181], v219 offset:37136\n"
"v_mfma_f32_16x16x32_bf16 a[108:111], a[236:239], v[124:127], a[108:111]\n"
"s_waitcnt lgkmcnt(11)\n"
"ds_read_b64_tr_b16 a[182:183], v219 offset:38160\n"
"v_mfma_f32_16x16x32_bf16 a[112:115], a[240:243], v[112:115], a[112:115]\n"
"ds_read_b64_tr_b16 a[184:185], v219 offset:33048\n"
"v_mfma_f32_16x16x32_bf16 a[112:115], a[244:247], v[120:123], a[112:115]\n"
"ds_read_b64_tr_b16 a[186:187], v219 offset:34072\n"
"v_mfma_f32_16x16x32_bf16 a[116:119], a[240:243], v[116:119], a[116:119]\n"
"ds_read_b64_tr_b16 a[188:189], v219 offset:37144\n"
"v_mfma_f32_16x16x32_bf16 a[116:119], a[244:247], v[124:127], a[116:119]\n"
"ds_read_b64_tr_b16 a[190:191], v219 offset:38168\n"
"v_mfma_f32_16x16x32_bf16 a[120:123], a[248:251], v[112:115], a[120:123]\n"
"ds_read_b64_tr_b16 a[224:225], v219 offset:33296\n"
"v_mfma_f32_16x16x32_bf16 a[120:123], a[252:255], v[120:123], a[120:123]\n"
"ds_read_b64_tr_b16 a[226:227], v219 offset:34320\n"
"v_mfma_f32_16x16x32_bf16 a[124:127], a[248:251], v[116:119], a[124:127]\n"
"ds_read_b64_tr_b16 a[228:229], v219 offset:37392\n"
"v_mfma_f32_16x16x32_bf16 a[124:127], a[252:255], v[124:127], a[124:127]\n"
"s_waitcnt lgkmcnt(11)\n"
"ds_read_b64_tr_b16 a[230:231], v219 offset:38416\n"
"v_mfma_f32_16x16x32_bf16 a[0:3], a[160:163], v[128:131], a[0:3]\n"
"ds_read_b64_tr_b16 a[232:233], v219 offset:33304\n"
"v_mfma_f32_16x16x32_bf16 a[0:3], a[164:167], v[136:139], a[0:3]\n"
"ds_read_b64_tr_b16 a[234:235], v219 offset:34328\n"
"v_mfma_f32_16x16x32_bf16 a[4:7], a[160:163], v[132:135], a[4:7]\n"
"s_waitcnt vmcnt(8)\n"
"s_barrier\n"
"ds_read_b64_tr_b16 a[236:237], v219 offset:37400\n"
"v_mfma_f32_16x16x32_bf16 a[4:7], a[164:167], v[140:143], a[4:7]\n"
"ds_read_b64_tr_b16 a[238:239], v219 offset:38424\n"
"v_mfma_f32_16x16x32_bf16 a[8:11], a[168:171], v[128:131], a[8:11]\n"
"s_add_u32 m0, s86, 65536\n"
"ds_read_b64_tr_b16 a[240:241], v219 offset:33552\n"
"v_mfma_f32_16x16x32_bf16 a[8:11], a[172:175], v[136:139], a[8:11]\n"
"ds_read_b64_tr_b16 a[242:243], v219 offset:34576\n"
"buffer_load_dwordx4 v246, s[8:11], s84 offen offset:0 lds\n"
"v_mfma_f32_16x16x32_bf16 a[12:15], a[168:171], v[132:135], a[12:15]\n"
"ds_read_b64_tr_b16 a[244:245], v219 offset:37648\n"
"v_accvgpr_read_b32 v176, a0\n"
"v_mfma_f32_16x16x32_bf16 a[12:15], a[172:175], v[140:143], a[12:15]\n"
"s_waitcnt lgkmcnt(11)\n"
"ds_read_b64_tr_b16 a[246:247], v219 offset:38672\n"
"v_accvgpr_read_b32 v177, a1\n"
"v_mfma_f32_16x16x32_bf16 a[16:19], a[176:179], v[128:131], a[16:19]\n"
"ds_read_b64_tr_b16 a[248:249], v219 offset:33560\n"
"v_accvgpr_read_b32 v178, a2\n"
"v_mfma_f32_16x16x32_bf16 a[16:19], a[180:183], v[136:139], a[16:19]\n"
"ds_read_b64_tr_b16 a[250:251], v219 offset:34584\n"
"v_accvgpr_read_b32 v179, a3\n"
"v_mfma_f32_16x16x32_bf16 a[20:23], a[176:179], v[132:135], a[20:23]\n"
"ds_read_b64_tr_b16 a[252:253], v219 offset:37656\n"
"v_cvt_pk_bf16_f32 v64, v176, v177\n"
"v_mfma_f32_16x16x32_bf16 a[20:23], a[180:183], v[140:143], a[20:23]\n"
"ds_read_b64_tr_b16 a[254:255], v219 offset:38680\n"
"buffer_load_dwordx4 v246, s[8:11], s84 offen offset:1024 lds\n"
"v_mfma_f32_16x16x32_bf16 a[24:27], a[184:187], v[128:131], a[24:27]\n"
"ds_read_b64_tr_b16 a[160:161], v219 offset:40976\n"
"v_cvt_pk_bf16_f32 v65, v178, v179\n"
"v_mfma_f32_16x16x32_bf16 a[24:27], a[188:191], v[136:139], a[24:27]\n"
"ds_read_b64_tr_b16 a[162:163], v219 offset:42000\n"
"v_accvgpr_read_b32 v180, a4\n"
"v_mfma_f32_16x16x32_bf16 a[28:31], a[184:187], v[132:135], a[28:31]\n"
"ds_read_b64_tr_b16 a[164:165], v219 offset:45072\n"
"v_accvgpr_read_b32 v181, a5\n"
"v_mfma_f32_16x16x32_bf16 a[28:31], a[188:191], v[140:143], a[28:31]\n"
"s_waitcnt lgkmcnt(11)\n"
"ds_read_b64_tr_b16 a[166:167], v219 offset:46096\n"
"v_accvgpr_read_b32 v182, a6\n"
"v_mfma_f32_16x16x32_bf16 a[32:35], a[224:227], v[128:131], a[32:35]\n"
"ds_read_b64_tr_b16 a[168:169], v219 offset:40984\n"
"v_accvgpr_read_b32 v183, a7\n"
"v_mfma_f32_16x16x32_bf16 a[32:35], a[228:231], v[136:139], a[32:35]\n"
"ds_read_b64_tr_b16 a[170:171], v219 offset:42008\n"
"buffer_load_dwordx4 v247, s[8:11], s84 offen offset:2048 lds\n"
"v_mfma_f32_16x16x32_bf16 a[36:39], a[224:227], v[132:135], a[36:39]\n"
"ds_read_b64_tr_b16 a[172:173], v219 offset:45080\n"
"v_cvt_pk_bf16_f32 v96, v180, v181\n"
"v_mfma_f32_16x16x32_bf16 a[36:39], a[228:231], v[140:143], a[36:39]\n"
"ds_read_b64_tr_b16 a[174:175], v219 offset:46104\n"
"v_cvt_pk_bf16_f32 v97, v182, v183\n"
"v_mfma_f32_16x16x32_bf16 a[40:43], a[232:235], v[128:131], a[40:43]\n"
"ds_read_b64_tr_b16 a[176:177], v219 offset:41232\n"
"v_accvgpr_read_b32 v184, a8\n"
"v_mfma_f32_16x16x32_bf16 a[40:43], a[236:239], v[136:139], a[40:43]\n"
"ds_read_b64_tr_b16 a[178:179], v219 offset:42256\n"
"v_accvgpr_read_b32 v185, a9\n"
"v_mfma_f32_16x16x32_bf16 a[44:47], a[232:235], v[132:135], a[44:47]\n"
"ds_read_b64_tr_b16 a[180:181], v219 offset:45328\n"
"v_accvgpr_read_b32 v186, a10\n"
"v_mfma_f32_16x16x32_bf16 a[44:47], a[236:239], v[140:143], a[44:47]\n"
"s_waitcnt lgkmcnt(11)\n"
"ds_read_b64_tr_b16 a[182:183], v219 offset:46352\n"
"buffer_load_dwordx4 v247, s[8:11], s84 offen offset:3072 lds\n"
"v_mfma_f32_16x16x32_bf16 a[48:51], a[240:243], v[128:131], a[48:51]\n"
"ds_read_b64_tr_b16 a[184:185], v219 offset:41240\n"
"v_accvgpr_read_b32 v187, a11\n"
"v_mfma_f32_16x16x32_bf16 a[48:51], a[244:247], v[136:139], a[48:51]\n"
"ds_read_b64_tr_b16 a[186:187], v219 offset:42264\n"
"v_cvt_pk_bf16_f32 v66, v184, v185\n"
"v_mfma_f32_16x16x32_bf16 a[52:55], a[240:243], v[132:135], a[52:55]\n"
"ds_read_b64_tr_b16 a[188:189], v219 offset:45336\n"
"v_cvt_pk_bf16_f32 v67, v186, v187\n"
"v_mfma_f32_16x16x32_bf16 a[52:55], a[244:247], v[140:143], a[52:55]\n"
"ds_read_b64_tr_b16 a[190:191], v219 offset:46360\n"
"v_accvgpr_read_b32 v188, a12\n"
"v_mfma_f32_16x16x32_bf16 a[56:59], a[248:251], v[128:131], a[56:59]\n"
"s_add_u32 m0, s86, 81920\n"
"ds_read_b64_tr_b16 a[224:225], v219 offset:41488\n"
"v_accvgpr_read_b32 v189, a13\n"
"v_mfma_f32_16x16x32_bf16 a[56:59], a[252:255], v[136:139], a[56:59]\n"
"ds_read_b64_tr_b16 a[226:227], v219 offset:42512\n"
"buffer_load_dwordx4 v246, s[12:15], s84 offen offset:0 lds\n"
"v_mfma_f32_16x16x32_bf16 a[60:63], a[248:251], v[132:135], a[60:63]\n"
"ds_read_b64_tr_b16 a[228:229], v219 offset:45584\n"
"v_accvgpr_read_b32 v190, a14\n"
"v_mfma_f32_16x16x32_bf16 a[60:63], a[252:255], v[140:143], a[60:63]\n"
"s_waitcnt lgkmcnt(11)\n"
"ds_read_b64_tr_b16 a[230:231], v219 offset:46608\n"
"v_accvgpr_read_b32 v191, a15\n"
"v_mfma_f32_16x16x32_bf16 a[64:67], a[160:163], v[128:131], a[64:67]\n"
"ds_read_b64_tr_b16 a[232:233], v219 offset:41496\n"
"v_cvt_pk_bf16_f32 v98, v188, v189\n"
"v_mfma_f32_16x16x32_bf16 a[64:67], a[164:167], v[136:139], a[64:67]\n"
"ds_read_b64_tr_b16 a[234:235], v219 offset:42520\n"
"v_cvt_pk_bf16_f32 v99, v190, v191\n"
"v_mfma_f32_16x16x32_bf16 a[68:71], a[160:163], v[132:135], a[68:71]\n"
"ds_read_b64_tr_b16 a[236:237], v219 offset:45592\n"
"v_accvgpr_read_b32 v176, a16\n"
"v_mfma_f32_16x16x32_bf16 a[68:71], a[164:167], v[140:143], a[68:71]\n"
"ds_read_b64_tr_b16 a[238:239], v219 offset:46616\n"
"buffer_load_dwordx4 v247, s[12:15], s84 offen offset:1024 lds\n"
"v_mfma_f32_16x16x32_bf16 a[72:75], a[168:171], v[128:131], a[72:75]\n"
"ds_read_b64_tr_b16 a[240:241], v219 offset:41744\n"
"v_accvgpr_read_b32 v177, a17\n"
"v_mfma_f32_16x16x32_bf16 a[72:75], a[172:175], v[136:139], a[72:75]\n"
"ds_read_b64_tr_b16 a[242:243], v219 offset:42768\n"
"v_accvgpr_read_b32 v178, a18\n"
"v_mfma_f32_16x16x32_bf16 a[76:79], a[168:171], v[132:135], a[76:79]\n"
"ds_read_b64_tr_b16 a[244:245], v219 offset:45840\n"
"v_accvgpr_read_b32 v179, a19\n"
"v_mfma_f32_16x16x32_bf16 a[76:79], a[172:175], v[140:143], a[76:79]\n"
"s_waitcnt lgkmcnt(11)\n"
"ds_read_b64_tr_b16 a[246:247], v219 offset:46864\n"
"v_cvt_pk_bf16_f32 v68, v176, v177\n"
"v_mfma_f32_16x16x32_bf16 a[80:83], a[176:179], v[128:131], a[80:83]\n"
"ds_read_b64_tr_b16 a[248:249], v219 offset:41752\n"
"v_cvt_pk_bf16_f32 v69, v178, v179\n"
"v_mfma_f32_16x16x32_bf16 a[80:83], a[180:183], v[136:139], a[80:83]\n"
"ds_read_b64_tr_b16 a[250:251], v219 offset:42776\n"
"buffer_load_dwordx4 v246, s[12:15], s84 offen offset:2048 lds\n"
"v_mfma_f32_16x16x32_bf16 a[84:87], a[176:179], v[132:135], a[84:87]\n"
"ds_read_b64_tr_b16 a[252:253], v219 offset:45848\n"
"v_accvgpr_read_b32 v180, a20\n"
"v_mfma_f32_16x16x32_bf16 a[84:87], a[180:183], v[140:143], a[84:87]\n"
"ds_read_b64_tr_b16 a[254:255], v219 offset:46872\n"
"v_accvgpr_read_b32 v181, a21\n"
"v_mfma_f32_16x16x32_bf16 a[88:91], a[184:187], v[128:131], a[88:91]\n"
"ds_read_b128 a[160:163], v254 offset:0\n"
"v_accvgpr_read_b32 v182, a22\n"
"v_mfma_f32_16x16x32_bf16 a[88:91], a[188:191], v[136:139], a[88:91]\n"
"ds_read_b128 a[164:167], v254 offset:256\n"
"v_accvgpr_read_b32 v183, a23\n"
"v_mfma_f32_16x16x32_bf16 a[92:95], a[184:187], v[132:135], a[92:95]\n"
"ds_read_b128 a[168:171], v254 offset:512\n"
"v_cvt_pk_bf16_f32 v100, v180, v181\n"
"v_mfma_f32_16x16x32_bf16 a[92:95], a[188:191], v[140:143], a[92:95]\n"
"s_waitcnt lgkmcnt(11)\n"
"buffer_load_dwordx4 v247, s[12:15], s84 offen offset:3072 lds\n"
"ds_read_b128 a[172:175], v254 offset:768\n"
"v_mfma_f32_16x16x32_bf16 a[96:99], a[224:227], v[128:131], a[96:99]\n"
"s_add_u32 s84, s84, 16384\n"
"ds_read_b128 a[128:131], v240 offset:0\n"
"v_cvt_pk_bf16_f32 v101, v182, v183\n"
"v_mfma_f32_16x16x32_bf16 a[96:99], a[228:231], v[136:139], a[96:99]\n"
"ds_read_b128 a[132:135], v240 offset:256\n"
"v_accvgpr_read_b32 v184, a24\n"
"v_mfma_f32_16x16x32_bf16 a[100:103], a[224:227], v[132:135], a[100:103]\n"
"ds_read_b128 a[136:139], v240 offset:512\n"
"v_accvgpr_read_b32 v185, a25\n"
"v_mfma_f32_16x16x32_bf16 a[100:103], a[228:231], v[140:143], a[100:103]\n"
"ds_read_b128 a[140:143], v240 offset:768\n"
"v_accvgpr_read_b32 v186, a26\n"
"v_mfma_f32_16x16x32_bf16 a[104:107], a[232:235], v[128:131], a[104:107]\n"
"ds_read_b128 a[144:147], v240 offset:4096\n"
"v_accvgpr_read_b32 v187, a27\n"
"v_mfma_f32_16x16x32_bf16 a[104:107], a[236:239], v[136:139], a[104:107]\n"
"ds_read_b128 a[148:151], v240 offset:4352\n"
"v_cvt_pk_bf16_f32 v70, v184, v185\n"
"v_mfma_f32_16x16x32_bf16 a[108:111], a[232:235], v[132:135], a[108:111]\n"
"ds_read_b128 a[152:155], v240 offset:4608\n"
"v_cvt_pk_bf16_f32 v71, v186, v187\n"
"v_mfma_f32_16x16x32_bf16 a[108:111], a[236:239], v[140:143], a[108:111]\n"
"s_waitcnt lgkmcnt(11)\n"
"ds_read_b128 a[156:159], v240 offset:4864\n"
"v_accvgpr_read_b32 v188, a28\n"
"v_mfma_f32_16x16x32_bf16 a[112:115], a[240:243], v[128:131], a[112:115]\n"
"ds_read_b64_tr_b16 a[192:193], v242 offset:16384\n"
"v_accvgpr_read_b32 v189, a29\n"
"v_mfma_f32_16x16x32_bf16 a[112:115], a[244:247], v[136:139], a[112:115]\n"
"ds_read_b64_tr_b16 a[194:195], v242 offset:20480\n"
"v_accvgpr_read_b32 v190, a30\n"
"v_mfma_f32_16x16x32_bf16 a[116:119], a[240:243], v[132:135], a[116:119]\n"
"ds_read_b64_tr_b16 a[196:197], v243 offset:16384\n"
"v_accvgpr_read_b32 v191, a31\n"
"v_mfma_f32_16x16x32_bf16 a[116:119], a[244:247], v[140:143], a[116:119]\n"
"ds_read_b64_tr_b16 a[198:199], v243 offset:20480\n"
"v_cvt_pk_bf16_f32 v102, v188, v189\n"
"v_mfma_f32_16x16x32_bf16 a[120:123], a[248:251], v[128:131], a[120:123]\n"
"ds_read_b64_tr_b16 a[200:201], v242 offset:16640\n"
"v_cvt_pk_bf16_f32 v103, v190, v191\n"
"v_mfma_f32_16x16x32_bf16 a[120:123], a[252:255], v[136:139], a[120:123]\n"
"ds_read_b64_tr_b16 a[202:203], v242 offset:20736\n"
"v_accvgpr_read_b32 v176, a32\n"
"v_mfma_f32_16x16x32_bf16 a[124:127], a[248:251], v[132:135], a[124:127]\n"
"ds_read_b64_tr_b16 a[204:205], v243 offset:16640\n"
"v_accvgpr_read_b32 v177, a33\n"
"v_mfma_f32_16x16x32_bf16 a[124:127], a[252:255], v[140:143], a[124:127]\n"
"ds_read_b64_tr_b16 a[206:207], v243 offset:20736\n"
"v_accvgpr_read_b32 v178, a34\n"
"s_barrier\n"
"ds_read_b64_tr_b16 a[208:209], v242 offset:16896\n"
"v_accvgpr_read_b32 v179, a35\n"
"v_cvt_pk_bf16_f32 v72, v176, v177\n"
"v_cvt_pk_bf16_f32 v73, v178, v179\n"
"ds_read_b64_tr_b16 a[210:211], v242 offset:20992\n"
"v_accvgpr_read_b32 v180, a36\n"
"v_accvgpr_read_b32 v181, a37\n"
"v_accvgpr_read_b32 v182, a38\n"
"ds_read_b64_tr_b16 a[212:213], v243 offset:16896\n"
"v_accvgpr_read_b32 v183, a39\n"
"v_cvt_pk_bf16_f32 v104, v180, v181\n"
"v_cvt_pk_bf16_f32 v105, v182, v183\n"
"ds_read_b64_tr_b16 a[214:215], v243 offset:20992\n"
"v_accvgpr_read_b32 v184, a40\n"
"v_accvgpr_read_b32 v185, a41\n"
"v_accvgpr_read_b32 v186, a42\n"
"ds_read_b64_tr_b16 a[216:217], v242 offset:17152\n"
"v_accvgpr_read_b32 v187, a43\n"
"v_cvt_pk_bf16_f32 v74, v184, v185\n"
"v_cvt_pk_bf16_f32 v75, v186, v187\n"
"ds_read_b64_tr_b16 a[218:219], v242 offset:21248\n"
"v_accvgpr_read_b32 v188, a44\n"
"v_accvgpr_read_b32 v189, a45\n"
"v_accvgpr_read_b32 v190, a46\n"
"ds_read_b64_tr_b16 a[220:221], v243 offset:17152\n"
"v_accvgpr_read_b32 v191, a47\n"
"v_cvt_pk_bf16_f32 v106, v188, v189\n"
"v_cvt_pk_bf16_f32 v107, v190, v191\n"
"ds_read_b64_tr_b16 a[222:223], v243 offset:21248\n"
"v_accvgpr_read_b32 v176, a48\n"
"v_accvgpr_read_b32 v177, a49\n"
"v_accvgpr_read_b32 v178, a50\n"
"v_accvgpr_read_b32 v179, a51\n"
"v_cvt_pk_bf16_f32 v76, v176, v177\n"
"v_cvt_pk_bf16_f32 v77, v178, v179\n"
"v_accvgpr_read_b32 v180, a52\n"
"s_add_u32 m0, s86, 98304\n"
"v_accvgpr_read_b32 v181, a53\n"
"v_accvgpr_read_b32 v182, a54\n"
"v_accvgpr_read_b32 v183, a55\n"
"v_cvt_pk_bf16_f32 v108, v180, v181\n"
"buffer_load_dwordx4 v246, s[8:11], s84 offen offset:0 lds\n"
"v_cvt_pk_bf16_f32 v109, v182, v183\n"
"v_accvgpr_read_b32 v184, a56\n"
"v_accvgpr_read_b32 v185, a57\n"
"v_accvgpr_read_b32 v186, a58\n"
"v_accvgpr_read_b32 v187, a59\n"
"v_cvt_pk_bf16_f32 v78, v184, v185\n"
"v_cvt_pk_bf16_f32 v79, v186, v187\n"
"v_accvgpr_read_b32 v188, a60\n"
"v_accvgpr_read_b32 v189, a61\n"
"v_accvgpr_read_b32 v190, a62\n"
"v_accvgpr_read_b32 v191, a63\n"
"buffer_load_dwordx4 v246, s[8:11], s84 offen offset:1024 lds\n"
"v_cvt_pk_bf16_f32 v110, v188, v189\n"
"v_cvt_pk_bf16_f32 v111, v190, v191\n"
"v_accvgpr_read_b32 v176, a64\n"
"s_nop 1\n"
"v_accvgpr_read_b32 v177, a65\n"
"v_accvgpr_read_b32 v178, a66\n"
"v_accvgpr_read_b32 v179, a67\n"
"v_cvt_pk_bf16_f32 v80, v176, v177\n"
"s_waitcnt lgkmcnt(14)\n"
"v_mfma_f32_16x16x32_bf16 v[144:147], a[160:163], v[64:67], 0\n"
"v_cvt_pk_bf16_f32 v81, v178, v179\n"
"v_accvgpr_read_b32 v180, a68\n"
"v_mfma_f32_16x16x32_bf16 v[144:147], a[164:167], v[68:71], v[144:147]\n"
"buffer_load_dwordx4 v247, s[8:11], s84 offen offset:2048 lds\n"
"v_accvgpr_read_b32 v181, a69\n"
"v_mfma_f32_16x16x32_bf16 v[144:147], a[168:171], v[72:75], v[144:147]\n"
"v_accvgpr_read_b32 v182, a70\n"
"v_accvgpr_read_b32 v183, a71\n"
"v_mfma_f32_16x16x32_bf16 v[144:147], a[172:175], v[76:79], v[144:147]\n"
"v_cvt_pk_bf16_f32 v112, v180, v181\n"
"v_cvt_pk_bf16_f32 v113, v182, v183\n"
"v_mfma_f32_16x16x32_bf16 v[152:155], a[160:163], v[96:99], 0\n"
"buffer_load_dwordx4 v247, s[8:11], s84 offen offset:3072 lds\n"
"v_accvgpr_read_b32 v184, a72\n"
"v_mfma_f32_16x16x32_bf16 v[152:155], a[164:167], v[100:103], v[152:155]\n"
"v_accvgpr_read_b32 v185, a73\n"
"v_accvgpr_read_b32 v186, a74\n"
"v_mfma_f32_16x16x32_bf16 v[152:155], a[168:171], v[104:107], v[152:155]\n"
"s_add_u32 m0, s86, 114688\n"
"v_accvgpr_read_b32 v187, a75\n"
"v_cvt_pk_bf16_f32 v82, v184, v185\n"
"v_mfma_f32_16x16x32_bf16 v[152:155], a[172:175], v[108:111], v[152:155]\n"
"buffer_load_dwordx4 v246, s[12:15], s84 offen offset:0 lds\n"
"v_cvt_pk_bf16_f32 v83, v186, v187\n"
"v_accvgpr_read_b32 v188, a76\n"
"v_accvgpr_read_b32 v189, a77\n"
"v_accvgpr_read_b32 v190, a78\n"
"v_accvgpr_read_b32 v191, a79\n"
"v_cvt_pk_bf16_f32 v114, v188, v189\n"
"v_cvt_pk_bf16_f32 v115, v190, v191\n"
"v_accvgpr_read_b32 v176, a80\n"
"v_accvgpr_read_b32 v177, a81\n"
"buffer_load_dwordx4 v247, s[12:15], s84 offen offset:1024 lds\n"
"v_accvgpr_read_b32 v178, a82\n"
"v_accvgpr_read_b32 v179, a83\n"
"v_cvt_pk_bf16_f32 v84, v176, v177\n"
"v_cvt_pk_bf16_f32 v85, v178, v179\n"
"v_accvgpr_read_b32 v180, a84\n"
"v_accvgpr_read_b32 v181, a85\n"
"v_accvgpr_read_b32 v182, a86\n"
"v_accvgpr_read_b32 v183, a87\n"
"v_cvt_pk_bf16_f32 v116, v180, v181\n"
"v_cvt_pk_bf16_f32 v117, v182, v183\n"
"v_accvgpr_read_b32 v184, a88\n"
"buffer_load_dwordx4 v246, s[12:15], s84 offen offset:2048 lds\n"
"v_accvgpr_read_b32 v185, a89\n"
"v_accvgpr_read_b32 v186, a90\n"
"v_accvgpr_read_b32 v187, a91\n"
"v_cvt_pk_bf16_f32 v86, v184, v185\n"
"v_cvt_pk_bf16_f32 v87, v186, v187\n"
"v_accvgpr_read_b32 v188, a92\n"
"v_accvgpr_read_b32 v189, a93\n"
"v_accvgpr_read_b32 v190, a94\n"
"v_accvgpr_read_b32 v191, a95\n"
"v_cvt_pk_bf16_f32 v118, v188, v189\n"
"v_cvt_pk_bf16_f32 v119, v190, v191\n"
"buffer_load_dwordx4 v247, s[12:15], s84 offen offset:3072 lds\n"
"v_max3_f32 v144, v144, v145, v146\n"
"v_max_f32 v144, v144, v147\n"
"v_sub_f32 v24, 0xc2480000, v144\n"
"s_add_u32 s84, s84, 16384\n"
"v_sub_f32 v25, 0xc2480000, v144\n"
"v_sub_f32 v26, 0xc2480000, v144\n"
"v_sub_f32 v27, 0xc2480000, v144\n"
"v_accvgpr_read_b32 v176, a96\n"
"v_accvgpr_read_b32 v177, a97\n"
"v_accvgpr_read_b32 v178, a98\n"
"v_accvgpr_read_b32 v179, a99\n"
"v_cvt_pk_bf16_f32 v88, v176, v177\n"
"v_cvt_pk_bf16_f32 v89, v178, v179\n"
"v_accvgpr_read_b32 v180, a100\n"
"v_accvgpr_read_b32 v181, a101\n"
"v_accvgpr_read_b32 v182, a102\n"
"v_accvgpr_read_b32 v183, a103\n"
"v_cvt_pk_bf16_f32 v120, v180, v181\n"
"v_cvt_pk_bf16_f32 v121, v182, v183\n"
"v_accvgpr_read_b32 v184, a104\n"
"v_accvgpr_read_b32 v185, a105\n"
"v_accvgpr_read_b32 v186, a106\n"
"v_accvgpr_read_b32 v187, a107\n"
"v_cvt_pk_bf16_f32 v90, v184, v185\n"
"v_cvt_pk_bf16_f32 v91, v186, v187\n"
"v_accvgpr_read_b32 v188, a108\n"
"v_accvgpr_read_b32 v189, a109\n"
"v_accvgpr_read_b32 v190, a110\n"
"v_accvgpr_read_b32 v191, a111\n"
"v_cvt_pk_bf16_f32 v122, v188, v189\n"
"v_cvt_pk_bf16_f32 v123, v190, v191\n"
"v_accvgpr_read_b32 v176, a112\n"
"v_accvgpr_read_b32 v177, a113\n"
"v_accvgpr_read_b32 v178, a114\n"
"v_accvgpr_read_b32 v179, a115\n"
"v_cvt_pk_bf16_f32 v92, v176, v177\n"
"v_cvt_pk_bf16_f32 v93, v178, v179\n"
"v_accvgpr_read_b32 v180, a116\n"
"v_accvgpr_read_b32 v181, a117\n"
"v_accvgpr_read_b32 v182, a118\n"
"v_accvgpr_read_b32 v183, a119\n"
"v_cvt_pk_bf16_f32 v124, v180, v181\n"
"v_cvt_pk_bf16_f32 v125, v182, v183\n"
"v_accvgpr_read_b32 v184, a120\n"
"v_accvgpr_read_b32 v185, a121\n"
"v_accvgpr_read_b32 v186, a122\n"
"v_accvgpr_read_b32 v187, a123\n"
"v_cvt_pk_bf16_f32 v94, v184, v185\n"
"v_cvt_pk_bf16_f32 v95, v186, v187\n"
"v_accvgpr_read_b32 v188, a124\n"
"v_accvgpr_read_b32 v189, a125\n"
"v_accvgpr_read_b32 v190, a126\n"
"v_accvgpr_read_b32 v191, a127\n"
"v_cvt_pk_bf16_f32 v126, v188, v189\n"
"v_cvt_pk_bf16_f32 v127, v190, v191\n"
"s_nop 1\n"
"v_mfma_f32_16x16x32_bf16 v[148:151], a[160:163], v[80:83], 0\n"
"v_max3_f32 v152, v152, v153, v154\n"
"v_max_f32 v152, v152, v155\n"
"v_mfma_f32_16x16x32_bf16 v[148:151], a[164:167], v[84:87], v[148:151]\n"
"v_sub_f32 v32, 0xc2480000, v152\n"
"v_sub_f32 v33, 0xc2480000, v152\n"
"v_mfma_f32_16x16x32_bf16 v[148:151], a[168:171], v[88:91], v[148:151]\n"
"v_sub_f32 v34, 0xc2480000, v152\n"
"v_sub_f32 v35, 0xc2480000, v152\n"
"v_mfma_f32_16x16x32_bf16 v[148:151], a[172:175], v[92:95], v[148:151]\n"
"v_mov_b32 v40, 0x3f803f80\n"
"v_mov_b32 v41, 0x3f803f80\n"
"v_mfma_f32_16x16x32_bf16 v[156:159], a[160:163], v[112:115], 0\n"
"v_mov_b32 v42, 0x3f803f80\n"
"v_mov_b32 v43, 0x3f803f80\n"
"v_mfma_f32_16x16x32_bf16 v[156:159], a[164:167], v[116:119], v[156:159]\n"
"v_mov_b32 v20, 0\n"
"v_mov_b32 v21, 0\n"
"v_mfma_f32_16x16x32_bf16 v[156:159], a[168:171], v[120:123], v[156:159]\n"
"v_mov_b32 v22, 0\n"
"v_mov_b32 v23, 0\n"
"v_mfma_f32_16x16x32_bf16 v[156:159], a[172:175], v[124:127], v[156:159]\n"
"v_mov_b32 v44, 0\n"
"v_mov_b32 v52, 0\n"
"v_mov_b32 v216, -1\n"
"v_mov_b32 v217, -1\n"
"v_mov_b32 v218, -1\n"
"v_mov_b32 v219, -1\n"
"v_lshl_add_u32 v255, v248, 6, s48\n"
"v_lshlrev_b32 v207, 4, v248\n"
"v_add_u32 v208, 18432, v207\n"
"s_waitcnt lgkmcnt(0)\n"
"AT_main:\n"
"s_or_b32 s56, s42, s41\n"
"s_cmp_lg_u32 s56, 0\n"
"s_cbranch_scc1 AT_h1\n"
"v_lshlrev_b32 v214, 7, v248\n"
"s_mov_b32 s57, 32768\n"
"s_mov_b32 s58, 40960\n"
"s_mov_b32 s59, 49152\n"
"s_mov_b32 s60, 57344\n"
"buffer_load_dword v210, v214, s[28:31], s57 offen offset:4 sc1\n"
"buffer_load_dword v211, v214, s[28:31], s58 offen offset:4 sc1\n"
"buffer_load_dword v212, v214, s[28:31], s59 offen offset:4 sc1\n"
"buffer_load_dword v213, v214, s[28:31], s60 offen offset:4 sc1\n"
"AT_h1:\n"
"v_mfma_f32_16x16x32_bf16 v[0:3], a[128:131], v[64:67], v[24:27]\n"
"s_nop 0\n"
"ds_read_b128 a[160:163], v240 offset:8192\n"
"v_mfma_f32_16x16x32_bf16 v[0:3], a[132:135], v[68:71], v[0:3]\n"
"s_nop 0\n"
"v_mfma_f32_16x16x32_bf16 v[0:3], a[136:139], v[72:75], v[0:3]\n"
"s_nop 0\n"
"ds_read_b128 a[164:167], v240 offset:8448\n"
"v_mfma_f32_16x16x32_bf16 v[0:3], a[140:143], v[76:79], v[0:3]\n"
"v_max3_f32 v148, v148, v149, v150\n"
"v_max_f32 v148, v148, v151\n"
"v_mfma_f32_16x16x32_bf16 v[4:7], a[144:147], v[64:67], v[24:27]\n"
"ds_read_b128 a[168:171], v240 offset:8704\n"
"v_sub_f32 v28, 0xc2480000, v148\n"
"v_mfma_f32_16x16x32_bf16 v[4:7], a[148:151], v[68:71], v[4:7]\n"
"v_sub_f32 v29, 0xc2480000, v148\n"
"v_sub_f32 v30, 0xc2480000, v148\n"
"v_mfma_f32_16x16x32_bf16 v[4:7], a[152:155], v[72:75], v[4:7]\n"
"ds_read_b128 a[172:175], v240 offset:8960\n"
"v_sub_f32 v31, 0xc2480000, v148\n"
"v_mfma_f32_16x16x32_bf16 v[4:7], a[156:159], v[76:79], v[4:7]\n"
"s_nop 0\n"
"v_exp_f32 v0, v0\n"
"v_mfma_f32_16x16x32_bf16 a[96:99], v[40:43], v[20:23], 0\n"
"v_max3_f32 v156, v156, v157, v158\n"
"v_max_f32 v156, v156, v159\n"
"v_mfma_f32_16x16x32_bf16 a[100:103], v[40:43], v[20:23], 0\n"
"s_nop 0\n"
"v_exp_f32 v1, v1\n"
"v_mfma_f32_16x16x32_bf16 a[104:107], v[40:43], v[20:23], 0\n"
"ds_read_b128 a[176:179], v240 offset:12288\n"
"v_cvt_pk_bf16_f32 v16, v0, v1\n"
"v_mfma_f32_16x16x32_bf16 a[108:111], v[40:43], v[20:23], 0\n"
"s_nop 0\n"
"v_exp_f32 v2, v2\n"
"v_mfma_f32_16x16x32_bf16 a[112:115], v[40:43], v[20:23], 0\n"
"ds_read_b128 a[180:183], v240 offset:12544\n"
"v_add_f32 v44, v44, v0\n"
"v_mfma_f32_16x16x32_bf16 a[116:119], v[40:43], v[20:23], 0\n"
"s_nop 0\n"
"v_exp_f32 v3, v3\n"
"v_mfma_f32_16x16x32_bf16 a[120:123], v[40:43], v[20:23], 0\n"
"ds_read_b128 a[184:187], v240 offset:12800\n"
"v_cvt_pk_bf16_f32 v17, v2, v3\n"
"v_mfma_f32_16x16x32_bf16 a[124:127], v[40:43], v[20:23], 0\n"
"s_nop 0\n"
"v_exp_f32 v4, v4\n"
"v_mfma_f32_16x16x32_bf16 v[56:59], v[40:43], v[20:23], 0\n"
"ds_read_b128 a[188:191], v240 offset:13056\n"
"v_add_f32 v44, v44, v1\n"
"v_mfma_f32_16x16x32_bf16 v[8:11], a[128:131], v[80:83], v[28:31]\n"
"s_nop 0\n"
"v_exp_f32 v5, v5\n"
"v_mfma_f32_16x16x32_bf16 v[8:11], a[132:135], v[84:87], v[8:11]\n"
"ds_read_b64_tr_b16 a[224:225], v242 offset:24576\n"
"v_cvt_pk_bf16_f32 v18, v4, v5\n"
"v_mfma_f32_16x16x32_bf16 v[8:11], a[136:139], v[88:91], v[8:11]\n"
"s_nop 0\n"
"v_exp_f32 v6, v6\n"
"v_mfma_f32_16x16x32_bf16 v[8:11], a[140:143], v[92:95], v[8:11]\n"
"ds_read_b64_tr_b16 a[226:227], v242 offset:28672\n"
"v_add_f32 v44, v44, v2\n"
"v_mfma_f32_16x16x32_bf16 v[12:15], a[144:147], v[80:83], v[28:31]\n"
"s_nop 0\n"
"v_exp_f32 v7, v7\n"
"v_mfma_f32_16x16x32_bf16 v[12:15], a[148:151], v[84:87], v[12:15]\n"
"ds_read_b64_tr_b16 a[228:229], v243 offset:24576\n"
"v_cvt_pk_bf16_f32 v19, v6, v7\n"
"v_mfma_f32_16x16x32_bf16 v[12:15], a[152:155], v[88:91], v[12:15]\n"
"v_sub_f32 v36, 0xc2480000, v156\n"
"v_sub_f32 v37, 0xc2480000, v156\n"
"v_mfma_f32_16x16x32_bf16 v[12:15], a[156:159], v[92:95], v[12:15]\n"
"s_nop 0\n"
"v_exp_f32 v8, v8\n"
"v_mfma_f32_16x16x32_bf16 a[0:3], a[192:195], v[16:19], 0\n"
"ds_read_b64_tr_b16 a[230:231], v243 offset:28672\n"
"v_add_f32 v44, v44, v3\n"
"v_mfma_f32_16x16x32_bf16 a[4:7], a[196:199], v[16:19], 0\n"
"s_nop 0\n"
"v_exp_f32 v9, v9\n"
"v_mfma_f32_16x16x32_bf16 a[8:11], a[200:203], v[16:19], 0\n"
"ds_read_b64_tr_b16 a[232:233], v242 offset:24832\n"
"v_cvt_pk_bf16_f32 v20, v8, v9\n"
"v_mfma_f32_16x16x32_bf16 a[12:15], a[204:207], v[16:19], 0\n"
"s_nop 0\n"
"v_exp_f32 v10, v10\n"
"v_mfma_f32_16x16x32_bf16 a[16:19], a[208:211], v[16:19], 0\n"
"ds_read_b64_tr_b16 a[234:235], v242 offset:28928\n"
"v_add_f32 v44, v44, v4\n"
"v_mfma_f32_16x16x32_bf16 a[20:23], a[212:215], v[16:19], 0\n"
"s_nop 0\n"
"v_exp_f32 v11, v11\n"
"v_mfma_f32_16x16x32_bf16 a[24:27], a[216:219], v[16:19], 0\n"
"ds_read_b64_tr_b16 a[236:237], v243 offset:24832\n"
"v_cvt_pk_bf16_f32 v21, v10, v11\n"
"v_mfma_f32_16x16x32_bf16 a[28:31], a[220:223], v[16:19], 0\n"
"s_nop 0\n"
"v_exp_f32 v12, v12\n"
"v_mfma_f32_16x16x32_bf16 v[220:223], a[128:131], v[96:99], v[32:35]\n"
"ds_read_b64_tr_b16 a[238:239], v243 offset:28928\n"
"v_add_f32 v44, v44, v5\n"
"v_mfma_f32_16x16x32_bf16 v[220:223], a[132:135], v[100:103], v[220:223]\n"
"s_nop 0\n"
"v_exp_f32 v13, v13\n"
"v_mfma_f32_16x16x32_bf16 v[220:223], a[136:139], v[104:107], v[220:223]\n"
"ds_read_b64_tr_b16 a[240:241], v242 offset:25088\n"
"v_cvt_pk_bf16_f32 v22, v12, v13\n"
"v_mfma_f32_16x16x32_bf16 v[220:223], a[140:143], v[108:111], v[220:223]\n"
"s_nop 0\n"
"v_exp_f32 v14, v14\n"
"v_mfma_f32_16x16x32_bf16 v[224:227], a[144:147], v[96:99], v[32:35]\n"
"ds_read_b64_tr_b16 a[242:243], v242 offset:29184\n"
"v_add_f32 v44, v44, v6\n"
"v_mfma_f32_16x16x32_bf16 v[224:227], a[148:151], v[100:103], v[224:227]\n"
"s_nop 0\n"
"v_exp_f32 v15, v15\n"
"v_mfma_f32_16x16x32_bf16 v[224:227], a[152:155], v[104:107], v[224:227]\n"
"ds_read_b64_tr_b16 a[244:245], v243 offset:25088\n"
"v_cvt_pk_bf16_f32 v23, v14, v15\n"
"v_mfma_f32_16x16x32_bf16 v[224:227], a[156:159], v[108:111], v[224:227]\n"
"s_nop 0\n"
"v_exp_f32 v220, v220\n"
"v_mfma_f32_16x16x32_bf16 a[32:35], a[192:195], v[20:23], 0\n"
"v_sub_f32 v38, 0xc2480000, v156\n"
"v_sub_f32 v39, 0xc2480000, v156\n"
"v_mfma_f32_16x16x32_bf16 a[36:39], a[196:199], v[20:23], 0\n"
"s_nop 0\n"
"v_exp_f32 v221, v221\n"
"v_mfma_f32_16x16x32_bf16 a[40:43], a[200:203], v[20:23], 0\n"
"ds_read_b64_tr_b16 a[246:247], v243 offset:29184\n"
"v_cvt_pk_bf16_f32 v16, v220, v221\n"
"v_mfma_f32_16x16x32_bf16 a[44:47], a[204:207], v[20:23], 0\n"
"s_nop 0\n"
"v_exp_f32 v222, v222\n"
"v_mfma_f32_16x16x32_bf16 a[48:51], a[208:211], v[20:23], 0\n"
"ds_read_b64_tr_b16 a[248:249], v242 offset:25344\n"
"v_add_f32 v44, v44, v7\n"
"v_mfma_f32_16x16x32_bf16 a[52:55], a[212:215], v[20:23], 0\n"
"s_nop 0\n"
"v_exp_f32 v223, v223\n"
"v_mfma_f32_16x16x32_bf16 a[56:59], a[216:219], v[20:23], 0\n"
"ds_read_b64_tr_b16 a[250:251], v242 offset:29440\n"
"v_cvt_pk_bf16_f32 v17, v222, v223\n"
"v_mfma_f32_16x16x32_bf16 a[60:63], a[220:223], v[20:23], 0\n"
"s_nop 0\n"
"v_exp_f32 v224, v224\n"
"v_mfma_f32_16x16x32_bf16 v[48:51], v[40:43], v[20:23], 0\n"
"ds_read_b64_tr_b16 a[252:253], v243 offset:25344\n"
"v_add_f32 v52, v52, v220\n"
"v_mfma_f32_16x16x32_bf16 v[228:231], a[128:131], v[112:115], v[36:39]\n"
"s_nop 0\n"
"v_exp_f32 v225, v225\n"
"v_mfma_f32_16x16x32_bf16 v[228:231], a[132:135], v[116:119], v[228:231]\n"
"ds_read_b64_tr_b16 a[254:255], v243 offset:29440\n"
"v_cvt_pk_bf16_f32 v18, v224, v225\n"
"v_mfma_f32_16x16x32_bf16 v[228:231], a[136:139], v[120:123], v[228:231]\n"
"s_nop 0\n"
"v_exp_f32 v226, v226\n"
"v_mfma_f32_16x16x32_bf16 v[228:231], a[140:143], v[124:127], v[228:231]\n"
"v_add_f32 v52, v52, v221\n"
"v_add_f32 v52, v52, v222\n"
"v_mfma_f32_16x16x32_bf16 v[232:235], a[144:147], v[112:115], v[36:39]\n"
"s_nop 0\n"
"v_exp_f32 v227, v227\n"
"v_mfma_f32_16x16x32_bf16 v[232:235], a[148:151], v[116:119], v[232:235]\n"
"v_cvt_pk_bf16_f32 v19, v226, v227\n"
"v_add_f32 v52, v52, v223\n"
"v_mfma_f32_16x16x32_bf16 v[232:235], a[152:155], v[120:123], v[232:235]\n"
"s_nop 0\n"
"v_mfma_f32_16x16x32_bf16 v[232:235], a[156:159], v[124:127], v[232:235]\n"
"s_nop 0\n"
"v_exp_f32 v228, v228\n"
"v_mfma_f32_16x16x32_bf16 a[64:67], a[192:195], v[16:19], 0\n"
"v_add_f32 v52, v52, v224\n"
"v_add_f32 v52, v52, v225\n"
"v_mfma_f32_16x16x32_bf16 a[68:71], a[196:199], v[16:19], 0\n"
"s_nop 0\n"
"v_exp_f32 v229, v229\n"
"v_mfma_f32_16x16x32_bf16 a[72:75], a[200:203], v[16:19], 0\n"
"v_cvt_pk_bf16_f32 v20, v228, v229\n"
"v_add_f32 v52, v52, v226\n"
"v_mfma_f32_16x16x32_bf16 a[76:79], a[204:207], v[16:19], 0\n"
"s_nop 0\n"
"v_exp_f32 v230, v230\n"
"v_mfma_f32_16x16x32_bf16 a[80:83], a[208:211], v[16:19], 0\n"
"s_nop 0\n"
"v_add_f32 v52, v52, v227\n"
"v_mfma_f32_16x16x32_bf16 a[84:87], a[212:215], v[16:19], 0\n"
"s_nop 0\n"
"v_exp_f32 v231, v231\n"
"v_mfma_f32_16x16x32_bf16 a[88:91], a[216:219], v[16:19], 0\n"
"s_waitcnt vmcnt(16) lgkmcnt(0)\n"
"s_barrier\n"
"v_cvt_pk_bf16_f32 v21, v230, v231\n"
"v_mfma_f32_16x16x32_bf16 a[92:95], a[220:223], v[16:19], 0\n"
"s_nop 0\n"
"v_exp_f32 v232, v232\n"
"s_mov_b32 s87, 6\n"
".p2align 6\n"
"AT_loop:\n"
"v_mfma_f32_16x16x32_bf16 v[0:3], a[160:163], v[64:67], v[24:27]\n"
"ds_read_b128 a[128:131], v240 offset:32768\n"
"buffer_load_dword v250, v251, s[64:67], 0 offen\n"
"v_mfma_f32_16x16x32_bf16 v[0:3], a[164:167], v[68:71], v[0:3]\n"
"v_exp_f32 v233, v233\n"
"v_mfma_f32_16x16x32_bf16 v[0:3], a[168:171], v[72:75], v[0:3]\n"
"ds_read_b128 a[132:135], v240 offset:33024\n"
"v_cvt_pk_bf16_f32 v22, v232, v233\n"
"v_mfma_f32_16x16x32_bf16 v[0:3], a[172:175], v[76:79], v[0:3]\n"
"v_exp_f32 v234, v234\n"
"v_mfma_f32_16x16x32_bf16 v[4:7], a[176:179], v[64:67], v[24:27]\n"
"ds_read_b128 a[136:139], v240 offset:33280\n"
"v_mfma_f32_16x16x32_bf16 v[4:7], a[180:183], v[68:71], v[4:7]\n"
"v_exp_f32 v235, v235\n"
"v_mfma_f32_16x16x32_bf16 v[4:7], a[184:187], v[72:75], v[4:7]\n"
"ds_read_b128 a[140:143], v240 offset:33536\n"
"v_cvt_pk_bf16_f32 v23, v234, v235\n"
"v_mfma_f32_16x16x32_bf16 v[4:7], a[188:191], v[76:79], v[4:7]\n"
"s_mov_b32 m0, s86\n"
"v_exp_f32 v0, v0\n"
"v_mfma_f32_16x16x32_bf16 a[96:99], a[192:195], v[20:23], a[96:99]\n"
"buffer_load_dwordx4 v246, s[8:11], s84 offen offset:0 lds\n"
"v_mfma_f32_16x16x32_bf16 a[100:103], a[196:199], v[20:23], a[100:103]\n"
"v_exp_f32 v1, v1\n"
"v_mfma_f32_16x16x32_bf16 a[104:107], a[200:203], v[20:23], a[104:107]\n"
"ds_read_b128 a[144:147], v240 offset:36864\n"
"v_cvt_pk_bf16_f32 v16, v0, v1\n"
"v_mfma_f32_16x16x32_bf16 a[108:111], a[204:207], v[20:23], a[108:111]\n"
"v_exp_f32 v2, v2\n"
"v_mfma_f32_16x16x32_bf16 a[112:115], a[208:211], v[20:23], a[112:115]\n"
"ds_read_b128 a[148:151], v240 offset:37120\n"
"v_add_f32 v44, v44, v0\n"
"v_mfma_f32_16x16x32_bf16 a[116:119], a[212:215], v[20:23], a[116:119]\n"
"v_exp_f32 v3, v3\n"
"v_mfma_f32_16x16x32_bf16 a[120:123], a[216:219], v[20:23], a[120:123]\n"
"ds_read_b128 a[152:155], v240 offset:37376\n"
"v_cvt_pk_bf16_f32 v17, v2, v3\n"
"v_mfma_f32_16x16x32_bf16 a[124:127], a[220:223], v[20:23], a[124:127]\n"
"v_exp_f32 v4, v4\n"
"v_mfma_f32_16x16x32_bf16 v[56:59], v[40:43], v[20:23], v[56:59]\n"
"ds_read_b128 a[156:159], v240 offset:37632\n"
"v_add_f32 v44, v44, v1\n"
"v_mfma_f32_16x16x32_bf16 v[8:11], a[160:163], v[80:83], v[28:31]\n"
"v_exp_f32 v5, v5\n"
"v_mfma_f32_16x16x32_bf16 v[8:11], a[164:167], v[84:87], v[8:11]\n"
"ds_read_b64_tr_b16 a[192:193], v242 offset:49152\n"
"v_cvt_pk_bf16_f32 v18, v4, v5\n"
"v_mfma_f32_16x16x32_bf16 v[8:11], a[168:171], v[88:91], v[8:11]\n"
"v_exp_f32 v6, v6\n"
"v_mfma_f32_16x16x32_bf16 v[8:11], a[172:175], v[92:95], v[8:11]\n"
"ds_read_b64_tr_b16 a[194:195], v242 offset:53248\n"
"v_add_f32 v44, v44, v2\n"
"v_mfma_f32_16x16x32_bf16 v[12:15], a[176:179], v[80:83], v[28:31]\n"
"v_exp_f32 v7, v7\n"
"v_mfma_f32_16x16x32_bf16 v[12:15], a[180:183], v[84:87], v[12:15]\n"
"ds_read_b64_tr_b16 a[196:197], v243 offset:49152\n"
"v_cvt_pk_bf16_f32 v19, v6, v7\n"
"v_mfma_f32_16x16x32_bf16 v[12:15], a[184:187], v[88:91], v[12:15]\n"
"buffer_load_dwordx4 v246, s[8:11], s84 offen offset:1024 lds\n"
"v_mfma_f32_16x16x32_bf16 v[12:15], a[188:191], v[92:95], v[12:15]\n"
"s_waitcnt lgkmcnt(11)\n"
"v_exp_f32 v8, v8\n"
"v_mfma_f32_16x16x32_bf16 a[0:3], a[224:227], v[16:19], a[0:3]\n"
"ds_read_b64_tr_b16 a[198:199], v243 offset:53248\n"
"v_add_f32 v44, v44, v3\n"
"v_mfma_f32_16x16x32_bf16 a[4:7], a[228:231], v[16:19], a[4:7]\n"
"v_exp_f32 v9, v9\n"
"v_mfma_f32_16x16x32_bf16 a[8:11], a[232:235], v[16:19], a[8:11]\n"
"ds_read_b64_tr_b16 a[200:201], v242 offset:49408\n"
"v_cvt_pk_bf16_f32 v20, v8, v9\n"
"v_mfma_f32_16x16x32_bf16 a[12:15], a[236:239], v[16:19], a[12:15]\n"
"v_exp_f32 v10, v10\n"
"v_mfma_f32_16x16x32_bf16 a[16:19], a[240:243], v[16:19], a[16:19]\n"
"ds_read_b64_tr_b16 a[202:203], v242 offset:53504\n"
"v_add_f32 v44, v44, v4\n"
"v_mfma_f32_16x16x32_bf16 a[20:23], a[244:247], v[16:19], a[20:23]\n"
"v_exp_f32 v11, v11\n"
"v_mfma_f32_16x16x32_bf16 a[24:27], a[248:251], v[16:19], a[24:27]\n"
"ds_read_b64_tr_b16 a[204:205], v243 offset:49408\n"
"buffer_load_dword v250, v255, s[32:35], 0 offen\n"
"v_cvt_pk_bf16_f32 v21, v10, v11\n"
"v_mfma_f32_16x16x32_bf16 a[28:31], a[252:255], v[16:19], a[28:31]\n"
"v_exp_f32 v12, v12\n"
"v_mfma_f32_16x16x32_bf16 v[220:223], a[160:163], v[96:99], v[32:35]\n"
"ds_read_b64_tr_b16 a[206:207], v243 offset:53504\n"
"v_add_f32 v44, v44, v5\n"
"v_mfma_f32_16x16x32_bf16 v[220:223], a[164:167], v[100:103], v[220:223]\n"
"v_exp_f32 v13, v13\n"
"v_mfma_f32_16x16x32_bf16 v[220:223], a[168:171], v[104:107], v[220:223]\n"
"ds_read_b64_tr_b16 a[208:209], v242 offset:49664\n"
"v_cvt_pk_bf16_f32 v22, v12, v13\n"
"v_mfma_f32_16x16x32_bf16 v[220:223], a[172:175], v[108:111], v[220:223]\n"
"v_exp_f32 v14, v14\n"
"v_mfma_f32_16x16x32_bf16 v[224:227], a[176:179], v[96:99], v[32:35]\n"
"ds_read_b64_tr_b16 a[210:211], v242 offset:53760\n"
"v_add_f32 v44, v44, v6\n"
"v_mfma_f32_16x16x32_bf16 v[224:227], a[180:183], v[100:103], v[224:227]\n"
"v_exp_f32 v15, v15\n"
"v_mfma_f32_16x16x32_bf16 v[224:227], a[184:187], v[104:107], v[224:227]\n"
"ds_read_b64_tr_b16 a[212:213], v243 offset:49664\n"
"v_cvt_pk_bf16_f32 v23, v14, v15\n"
"v_mfma_f32_16x16x32_bf16 v[224:227], a[188:191], v[108:111], v[224:227]\n"
"v_exp_f32 v220, v220\n"
"v_mfma_f32_16x16x32_bf16 a[32:35], a[224:227], v[20:23], a[32:35]\n"
"buffer_load_dwordx4 v247, s[8:11], s84 offen offset:2048 lds\n"
"v_mfma_f32_16x16x32_bf16 a[36:39], a[228:231], v[20:23], a[36:39]\n"
"v_exp_f32 v221, v221\n"
"v_mfma_f32_16x16x32_bf16 a[40:43], a[232:235], v[20:23], a[40:43]\n"
"ds_read_b64_tr_b16 a[214:215], v243 offset:53760\n"
"v_cvt_pk_bf16_f32 v16, v220, v221\n"
"v_mfma_f32_16x16x32_bf16 a[44:47], a[236:239], v[20:23], a[44:47]\n"
"v_exp_f32 v222, v222\n"
"v_mfma_f32_16x16x32_bf16 a[48:51], a[240:243], v[20:23], a[48:51]\n"
"ds_read_b64_tr_b16 a[216:217], v242 offset:49920\n"
"v_add_f32 v44, v44, v7\n"
"v_mfma_f32_16x16x32_bf16 a[52:55], a[244:247], v[20:23], a[52:55]\n"
"v_exp_f32 v223, v223\n"
"v_mfma_f32_16x16x32_bf16 a[56:59], a[248:251], v[20:23], a[56:59]\n"
"ds_read_b64_tr_b16 a[218:219], v242 offset:54016\n"
"v_cvt_pk_bf16_f32 v17, v222, v223\n"
"v_mfma_f32_16x16x32_bf16 a[60:63], a[252:255], v[20:23], a[60:63]\n"
"v_exp_f32 v224, v224\n"
"v_mfma_f32_16x16x32_bf16 v[48:51], v[40:43], v[20:23], v[48:51]\n"
"ds_read_b64_tr_b16 a[220:221], v243 offset:49920\n"
"v_add_f32 v52, v52, v220\n"
"v_mfma_f32_16x16x32_bf16 v[228:231], a[160:163], v[112:115], v[36:39]\n"
"v_exp_f32 v225, v225\n"
"v_mfma_f32_16x16x32_bf16 v[228:231], a[164:167], v[116:119], v[228:231]\n"
"ds_read_b64_tr_b16 a[222:223], v243 offset:54016\n"
"v_cvt_pk_bf16_f32 v18, v224, v225\n"
"v_mfma_f32_16x16x32_bf16 v[228:231], a[168:171], v[120:123], v[228:231]\n"
"v_exp_f32 v226, v226\n"
"v_mfma_f32_16x16x32_bf16 v[228:231], a[172:175], v[124:127], v[228:231]\n"
"v_add_f32 v52, v52, v221\n"
"v_add_f32 v52, v52, v222\n"
"v_mfma_f32_16x16x32_bf16 v[232:235], a[176:179], v[112:115], v[36:39]\n"
"v_exp_f32 v227, v227\n"
"v_mfma_f32_16x16x32_bf16 v[232:235], a[180:183], v[116:119], v[232:235]\n"
"v_cvt_pk_bf16_f32 v19, v226, v227\n"
"v_add_f32 v52, v52, v223\n"
"v_mfma_f32_16x16x32_bf16 v[232:235], a[184:187], v[120:123], v[232:235]\n"
"buffer_load_dwordx4 v247, s[8:11], s84 offen offset:3072 lds\n"
"v_mfma_f32_16x16x32_bf16 v[232:235], a[188:191], v[124:127], v[232:235]\n"
"v_exp_f32 v228, v228\n"
"v_mfma_f32_16x16x32_bf16 a[64:67], a[224:227], v[16:19], a[64:67]\n"
"v_add_f32 v52, v52, v224\n"
"v_add_f32 v52, v52, v225\n"
"v_mfma_f32_16x16x32_bf16 a[68:71], a[228:231], v[16:19], a[68:71]\n"
"v_exp_f32 v229, v229\n"
"v_mfma_f32_16x16x32_bf16 a[72:75], a[232:235], v[16:19], a[72:75]\n"
"v_cvt_pk_bf16_f32 v20, v228, v229\n"
"v_add_f32 v52, v52, v226\n"
"v_mfma_f32_16x16x32_bf16 a[76:79], a[236:239], v[16:19], a[76:79]\n"
"v_exp_f32 v230, v230\n"
"v_mfma_f32_16x16x32_bf16 a[80:83], a[240:243], v[16:19], a[80:83]\n"
"v_add_f32 v52, v52, v227\n"
"v_mfma_f32_16x16x32_bf16 a[84:87], a[244:247], v[16:19], a[84:87]\n"
"v_exp_f32 v231, v231\n"
"v_mfma_f32_16x16x32_bf16 a[88:91], a[248:251], v[16:19], a[88:91]\n"
"s_waitcnt lgkmcnt(8)\n"
"v_cvt_pk_bf16_f32 v21, v230, v231\n"
"v_mfma_f32_16x16x32_bf16 a[92:95], a[252:255], v[16:19], a[92:95]\n"
"v_exp_f32 v232, v232\n"
"v_mfma_f32_16x16x32_bf16 v[0:3], a[128:131], v[64:67], v[24:27]\n"
"ds_read_b128 a[160:163], v240 offset:40960\n"
"v_mfma_f32_16x16x32_bf16 v[0:3], a[132:135], v[68:71], v[0:3]\n"
"v_exp_f32 v233, v233\n"
"v_mfma_f32_16x16x32_bf16 v[0:3], a[136:139], v[72:75], v[0:3]\n"
"ds_read_b128 a[164:167], v240 offset:41216\n"
"v_cvt_pk_bf16_f32 v22, v232, v233\n"
"v_mfma_f32_16x16x32_bf16 v[0:3], a[140:143], v[76:79], v[0:3]\n"
"v_exp_f32 v234, v234\n"
"v_mfma_f32_16x16x32_bf16 v[4:7], a[144:147], v[64:67], v[24:27]\n"
"ds_read_b128 a[168:171], v240 offset:41472\n"
"v_mfma_f32_16x16x32_bf16 v[4:7], a[148:151], v[68:71], v[4:7]\n"
"v_exp_f32 v235, v235\n"
"v_mfma_f32_16x16x32_bf16 v[4:7], a[152:155], v[72:75], v[4:7]\n"
"ds_read_b128 a[172:175], v240 offset:41728\n"
"v_cvt_pk_bf16_f32 v23, v234, v235\n"
"v_mfma_f32_16x16x32_bf16 v[4:7], a[156:159], v[76:79], v[4:7]\n"
"s_add_u32 m0, s86, 16384\n"
"v_exp_f32 v0, v0\n"
"v_mfma_f32_16x16x32_bf16 a[96:99], a[224:227], v[20:23], a[96:99]\n"
"buffer_load_dwordx4 v246, s[12:15], s84 offen offset:0 lds\n"
"v_mfma_f32_16x16x32_bf16 a[100:103], a[228:231], v[20:23], a[100:103]\n"
"v_exp_f32 v1, v1\n"
"v_mfma_f32_16x16x32_bf16 a[104:107], a[232:235], v[20:23], a[104:107]\n"
"ds_read_b128 a[176:179], v240 offset:45056\n"
"v_cvt_pk_bf16_f32 v16, v0, v1\n"
"v_mfma_f32_16x16x32_bf16 a[108:111], a[236:239], v[20:23], a[108:111]\n"
"v_exp_f32 v2, v2\n"
"v_mfma_f32_16x16x32_bf16 a[112:115], a[240:243], v[20:23], a[112:115]\n"
"ds_read_b128 a[180:183], v240 offset:45312\n"
"v_add_f32 v44, v44, v0\n"
"v_mfma_f32_16x16x32_bf16 a[116:119], a[244:247], v[20:23], a[116:119]\n"
"v_exp_f32 v3, v3\n"
"v_mfma_f32_16x16x32_bf16 a[120:123], a[248:251], v[20:23], a[120:123]\n"
"ds_read_b128 a[184:187], v240 offset:45568\n"
"v_cvt_pk_bf16_f32 v17, v2, v3\n"
"v_mfma_f32_16x16x32_bf16 a[124:127], a[252:255], v[20:23], a[124:127]\n"
"v_exp_f32 v4, v4\n"
"v_mfma_f32_16x16x32_bf16 v[56:59], v[40:43], v[20:23], v[56:59]\n"
"ds_read_b128 a[188:191], v240 offset:45824\n"
"v_add_f32 v44, v44, v1\n"
"v_mfma_f32_16x16x32_bf16 v[8:11], a[128:131], v[80:83], v[28:31]\n"
"v_exp_f32 v5, v5\n"
"v_mfma_f32_16x16x32_bf16 v[8:11], a[132:135], v[84:87], v[8:11]\n"
"ds_read_b64_tr_b16 a[224:225], v242 offset:57344\n"
"v_cvt_pk_bf16_f32 v18, v4, v5\n"
"v_mfma_f32_16x16x32_bf16 v[8:11], a[136:139], v[88:91], v[8:11]\n"
"v_exp_f32 v6, v6\n"
"v_mfma_f32_16x16x32_bf16 v[8:11], a[140:143], v[92:95], v[8:11]\n"
"ds_read_b64_tr_b16 a[226:227], v242 offset:61440\n"
"v_add_f32 v44, v44, v2\n"
"v_mfma_f32_16x16x32_bf16 v[12:15], a[144:147], v[80:83], v[28:31]\n"
"v_exp_f32 v7, v7\n"
"v_mfma_f32_16x16x32_bf16 v[12:15], a[148:151], v[84:87], v[12:15]\n"
"ds_read_b64_tr_b16 a[228:229], v243 offset:57344\n"
"v_cvt_pk_bf16_f32 v19, v6, v7\n"
"v_mfma_f32_16x16x32_bf16 v[12:15], a[152:155], v[88:91], v[12:15]\n"
"buffer_load_dwordx4 v247, s[12:15], s84 offen offset:1024 lds\n"
"v_mfma_f32_16x16x32_bf16 v[12:15], a[156:159], v[92:95], v[12:15]\n"
"s_waitcnt lgkmcnt(11)\n"
"v_exp_f32 v8, v8\n"
"v_mfma_f32_16x16x32_bf16 a[0:3], a[192:195], v[16:19], a[0:3]\n"
"ds_read_b64_tr_b16 a[230:231], v243 offset:61440\n"
"v_add_f32 v44, v44, v3\n"
"v_mfma_f32_16x16x32_bf16 a[4:7], a[196:199], v[16:19], a[4:7]\n"
"v_exp_f32 v9, v9\n"
"v_mfma_f32_16x16x32_bf16 a[8:11], a[200:203], v[16:19], a[8:11]\n"
"ds_read_b64_tr_b16 a[232:233], v242 offset:57600\n"
"v_cvt_pk_bf16_f32 v20, v8, v9\n"
"v_mfma_f32_16x16x32_bf16 a[12:15], a[204:207], v[16:19], a[12:15]\n"
"v_exp_f32 v10, v10\n"
"v_mfma_f32_16x16x32_bf16 a[16:19], a[208:211], v[16:19], a[16:19]\n"
"ds_read_b64_tr_b16 a[234:235], v242 offset:61696\n"
"v_add_f32 v44, v44, v4\n"
"v_mfma_f32_16x16x32_bf16 a[20:23], a[212:215], v[16:19], a[20:23]\n"
"v_exp_f32 v11, v11\n"
"v_mfma_f32_16x16x32_bf16 a[24:27], a[216:219], v[16:19], a[24:27]\n"
"ds_read_b64_tr_b16 a[236:237], v243 offset:57600\n"
"v_cvt_pk_bf16_f32 v21, v10, v11\n"
"v_mfma_f32_16x16x32_bf16 a[28:31], a[220:223], v[16:19], a[28:31]\n"
"v_exp_f32 v12, v12\n"
"v_mfma_f32_16x16x32_bf16 v[220:223], a[128:131], v[96:99], v[32:35]\n"
"ds_read_b64_tr_b16 a[238:239], v243 offset:61696\n"
"v_add_f32 v44, v44, v5\n"
"v_mfma_f32_16x16x32_bf16 v[220:223], a[132:135], v[100:103], v[220:223]\n"
"v_exp_f32 v13, v13\n"
"v_mfma_f32_16x16x32_bf16 v[220:223], a[136:139], v[104:107], v[220:223]\n"
"ds_read_b64_tr_b16 a[240:241], v242 offset:57856\n"
"v_cvt_pk_bf16_f32 v22, v12, v13\n"
"v_mfma_f32_16x16x32_bf16 v[220:223], a[140:143], v[108:111], v[220:223]\n"
"v_exp_f32 v14, v14\n"
"v_mfma_f32_16x16x32_bf16 v[224:227], a[144:147], v[96:99], v[32:35]\n"
"ds_read_b64_tr_b16 a[242:243], v242 offset:61952\n"
"v_add_f32 v44, v44, v6\n"
"v_mfma_f32_16x16x32_bf16 v[224:227], a[148:151], v[100:103], v[224:227]\n"
"v_exp_f32 v15, v15\n"
"v_mfma_f32_16x16x32_bf16 v[224:227], a[152:155], v[104:107], v[224:227]\n"
"ds_read_b64_tr_b16 a[244:245], v243 offset:57856\n"
"v_cvt_pk_bf16_f32 v23, v14, v15\n"
"v_mfma_f32_16x16x32_bf16 v[224:227], a[156:159], v[108:111], v[224:227]\n"
"v_exp_f32 v220, v220\n"
"v_mfma_f32_16x16x32_bf16 a[32:35], a[192:195], v[20:23], a[32:35]\n"
"buffer_load_dwordx4 v246, s[12:15], s84 offen offset:2048 lds\n"
"v_mfma_f32_16x16x32_bf16 a[36:39], a[196:199], v[20:23], a[36:39]\n"
"v_exp_f32 v221, v221\n"
"v_mfma_f32_16x16x32_bf16 a[40:43], a[200:203], v[20:23], a[40:43]\n"
"ds_read_b64_tr_b16 a[246:247], v243 offset:61952\n"
"v_cvt_pk_bf16_f32 v16, v220, v221\n"
"v_mfma_f32_16x16x32_bf16 a[44:47], a[204:207], v[20:23], a[44:47]\n"
"v_exp_f32 v222, v222\n"
"v_mfma_f32_16x16x32_bf16 a[48:51], a[208:211], v[20:23], a[48:51]\n"
"ds_read_b64_tr_b16 a[248:249], v242 offset:58112\n"
"v_add_f32 v44, v44, v7\n"
"v_mfma_f32_16x16x32_bf16 a[52:55], a[212:215], v[20:23], a[52:55]\n"
"v_exp_f32 v223, v223\n"
"v_mfma_f32_16x16x32_bf16 a[56:59], a[216:219], v[20:23], a[56:59]\n"
"ds_read_b64_tr_b16 a[250:251], v242 offset:62208\n"
"v_cvt_pk_bf16_f32 v17, v222, v223\n"
"v_mfma_f32_16x16x32_bf16 a[60:63], a[220:223], v[20:23], a[60:63]\n"
"v_exp_f32 v224, v224\n"
"v_mfma_f32_16x16x32_bf16 v[48:51], v[40:43], v[20:23], v[48:51]\n"
"ds_read_b64_tr_b16 a[252:253], v243 offset:58112\n"
"v_add_f32 v52, v52, v220\n"
"v_mfma_f32_16x16x32_bf16 v[228:231], a[128:131], v[112:115], v[36:39]\n"
"v_exp_f32 v225, v225\n"
"v_mfma_f32_16x16x32_bf16 v[228:231], a[132:135], v[116:119], v[228:231]\n"
"ds_read_b64_tr_b16 a[254:255], v243 offset:62208\n"
"v_cvt_pk_bf16_f32 v18, v224, v225\n"
"v_mfma_f32_16x16x32_bf16 v[228:231], a[136:139], v[120:123], v[228:231]\n"
"v_exp_f32 v226, v226\n"
"v_mfma_f32_16x16x32_bf16 v[228:231], a[140:143], v[124:127], v[228:231]\n"
"v_add_f32 v52, v52, v221\n"
"v_add_f32 v52, v52, v222\n"
"v_mfma_f32_16x16x32_bf16 v[232:235], a[144:147], v[112:115], v[36:39]\n"
"v_exp_f32 v227, v227\n"
"v_mfma_f32_16x16x32_bf16 v[232:235], a[148:151], v[116:119], v[232:235]\n"
"v_cvt_pk_bf16_f32 v19, v226, v227\n"
"v_add_f32 v52, v52, v223\n"
"v_mfma_f32_16x16x32_bf16 v[232:235], a[152:155], v[120:123], v[232:235]\n"
"buffer_load_dwordx4 v247, s[12:15], s84 offen offset:3072 lds\n"
"v_mfma_f32_16x16x32_bf16 v[232:235], a[156:159], v[124:127], v[232:235]\n"
"s_add_u32 s84, s84, 16384\n"
"v_exp_f32 v228, v228\n"
"v_mfma_f32_16x16x32_bf16 a[64:67], a[192:195], v[16:19], a[64:67]\n"
"v_add_f32 v52, v52, v224\n"
"v_add_f32 v52, v52, v225\n"
"v_mfma_f32_16x16x32_bf16 a[68:71], a[196:199], v[16:19], a[68:71]\n"
"v_exp_f32 v229, v229\n"
"v_mfma_f32_16x16x32_bf16 a[72:75], a[200:203], v[16:19], a[72:75]\n"
"v_cvt_pk_bf16_f32 v20, v228, v229\n"
"v_add_f32 v52, v52, v226\n"
"v_mfma_f32_16x16x32_bf16 a[76:79], a[204:207], v[16:19], a[76:79]\n"
"v_exp_f32 v230, v230\n"
"v_mfma_f32_16x16x32_bf16 a[80:83], a[208:211], v[16:19], a[80:83]\n"
"v_add_f32 v52, v52, v227\n"
"v_mfma_f32_16x16x32_bf16 a[84:87], a[212:215], v[16:19], a[84:87]\n"
"v_exp_f32 v231, v231\n"
"v_mfma_f32_16x16x32_bf16 a[88:91], a[216:219], v[16:19], a[88:91]\n"
"s_waitcnt vmcnt(18) lgkmcnt(0)\n"
"s_barrier\n"
"v_cvt_pk_bf16_f32 v21, v230, v231\n"
"v_mfma_f32_16x16x32_bf16 a[92:95], a[220:223], v[16:19], a[92:95]\n"
"v_exp_f32 v232, v232\n"
"v_mfma_f32_16x16x32_bf16 v[0:3], a[160:163], v[64:67], v[24:27]\n"
"ds_read_b128 a[128:131], v241 offset:0\n"
"buffer_store_dwordx4 v[216:219], v207, s[28:31], s54 offen sc1\n"
"v_mfma_f32_16x16x32_bf16 v[0:3], a[164:167], v[68:71], v[0:3]\n"
"v_exp_f32 v233, v233\n"
"v_mfma_f32_16x16x32_bf16 v[0:3], a[168:171], v[72:75], v[0:3]\n"
"ds_read_b128 a[132:135], v241 offset:256\n"
"v_cvt_pk_bf16_f32 v22, v232, v233\n"
"v_mfma_f32_16x16x32_bf16 v[0:3], a[172:175], v[76:79], v[0:3]\n"
"v_exp_f32 v234, v234\n"
"v_mfma_f32_16x16x32_bf16 v[4:7], a[176:179], v[64:67], v[24:27]\n"
"ds_read_b128 a[136:139], v241 offset:512\n"
"v_mfma_f32_16x16x32_bf16 v[4:7], a[180:183], v[68:71], v[4:7]\n"
"v_exp_f32 v235, v235\n"
"v_mfma_f32_16x16x32_bf16 v[4:7], a[184:187], v[72:75], v[4:7]\n"
"ds_read_b128 a[140:143], v241 offset:768\n"
"v_cvt_pk_bf16_f32 v23, v234, v235\n"
"v_mfma_f32_16x16x32_bf16 v[4:7], a[188:191], v[76:79], v[4:7]\n"
"s_add_u32 m0, s86, 32768\n"
"v_exp_f32 v0, v0\n"
"v_mfma_f32_16x16x32_bf16 a[96:99], a[192:195], v[20:23], a[96:99]\n"
"buffer_load_dwordx4 v246, s[8:11], s84 offen offset:0 lds\n"
"v_mfma_f32_16x16x32_bf16 a[100:103], a[196:199], v[20:23], a[100:103]\n"
"v_exp_f32 v1, v1\n"
"v_mfma_f32_16x16x32_bf16 a[104:107], a[200:203], v[20:23], a[104:107]\n"
"ds_read_b128 a[144:147], v241 offset:4096\n"
"v_cvt_pk_bf16_f32 v16, v0, v1\n"
"v_mfma_f32_16x16x32_bf16 a[108:111], a[204:207], v[20:23], a[108:111]\n"
"v_exp_f32 v2, v2\n"
"v_mfma_f32_16x16x32_bf16 a[112:115], a[208:211], v[20:23], a[112:115]\n"
"ds_read_b128 a[148:151], v241 offset:4352\n"
"v_add_f32 v44, v44, v0\n"
"v_mfma_f32_16x16x32_bf16 a[116:119], a[212:215], v[20:23], a[116:119]\n"
"v_exp_f32 v3, v3\n"
"v_mfma_f32_16x16x32_bf16 a[120:123], a[216:219], v[20:23], a[120:123]\n"
"ds_read_b128 a[152:155], v241 offset:4608\n"
"v_cvt_pk_bf16_f32 v17, v2, v3\n"
"v_mfma_f32_16x16x32_bf16 a[124:127], a[220:223], v[20:23], a[124:127]\n"
"v_exp_f32 v4, v4\n"
"v_mfma_f32_16x16x32_bf16 v[56:59], v[40:43], v[20:23], v[56:59]\n"
"ds_read_b128 a[156:159], v241 offset:4864\n"
"v_add_f32 v44, v44, v1\n"
"v_mfma_f32_16x16x32_bf16 v[8:11], a[160:163], v[80:83], v[28:31]\n"
"v_exp_f32 v5, v5\n"
"v_mfma_f32_16x16x32_bf16 v[8:11], a[164:167], v[84:87], v[8:11]\n"
"ds_read_b64_tr_b16 a[192:193], v244 offset:16384\n"
"v_cvt_pk_bf16_f32 v18, v4, v5\n"
"v_mfma_f32_16x16x32_bf16 v[8:11], a[168:171], v[88:91], v[8:11]\n"
"v_exp_f32 v6, v6\n"
"v_mfma_f32_16x16x32_bf16 v[8:11], a[172:175], v[92:95], v[8:11]\n"
"ds_read_b64_tr_b16 a[194:195], v244 offset:20480\n"
"v_add_f32 v44, v44, v2\n"
"v_mfma_f32_16x16x32_bf16 v[12:15], a[176:179], v[80:83], v[28:31]\n"
"v_exp_f32 v7, v7\n"
"v_mfma_f32_16x16x32_bf16 v[12:15], a[180:183], v[84:87], v[12:15]\n"
"ds_read_b64_tr_b16 a[196:197], v245 offset:16384\n"
"v_cvt_pk_bf16_f32 v19, v6, v7\n"
"v_mfma_f32_16x16x32_bf16 v[12:15], a[184:187], v[88:91], v[12:15]\n"
"buffer_load_dwordx4 v246, s[8:11], s84 offen offset:1024 lds\n"
"v_mfma_f32_16x16x32_bf16 v[12:15], a[188:191], v[92:95], v[12:15]\n"
"s_waitcnt lgkmcnt(11)\n"
"v_exp_f32 v8, v8\n"
"v_mfma_f32_16x16x32_bf16 a[0:3], a[224:227], v[16:19], a[0:3]\n"
"ds_read_b64_tr_b16 a[198:199], v245 offset:20480\n"
"v_add_f32 v44, v44, v3\n"
"v_mfma_f32_16x16x32_bf16 a[4:7], a[228:231], v[16:19], a[4:7]\n"
"v_exp_f32 v9, v9\n"
"v_mfma_f32_16x16x32_bf16 a[8:11], a[232:235], v[16:19], a[8:11]\n"
"ds_read_b64_tr_b16 a[200:201], v244 offset:16640\n"
"v_cvt_pk_bf16_f32 v20, v8, v9\n"
"v_mfma_f32_16x16x32_bf16 a[12:15], a[236:239], v[16:19], a[12:15]\n"
"v_exp_f32 v10, v10\n"
"v_mfma_f32_16x16x32_bf16 a[16:19], a[240:243], v[16:19], a[16:19]\n"
"ds_read_b64_tr_b16 a[202:203], v244 offset:20736\n"
"v_add_f32 v44, v44, v4\n"
"v_mfma_f32_16x16x32_bf16 a[20:23], a[244:247], v[16:19], a[20:23]\n"
"v_exp_f32 v11, v11\n"
"v_mfma_f32_16x16x32_bf16 a[24:27], a[248:251], v[16:19], a[24:27]\n"
"ds_read_b64_tr_b16 a[204:205], v245 offset:16640\n"
"buffer_load_dword v250, v251, s[68:71], 0 offen\n"
"v_cvt_pk_bf16_f32 v21, v10, v11\n"
"v_mfma_f32_16x16x32_bf16 a[28:31], a[252:255], v[16:19], a[28:31]\n"
"v_exp_f32 v12, v12\n"
"v_mfma_f32_16x16x32_bf16 v[220:223], a[160:163], v[96:99], v[32:35]\n"
"ds_read_b64_tr_b16 a[206:207], v245 offset:20736\n"
"v_add_f32 v44, v44, v5\n"
"v_mfma_f32_16x16x32_bf16 v[220:223], a[164:167], v[100:103], v[220:223]\n"
"v_exp_f32 v13, v13\n"
"v_mfma_f32_16x16x32_bf16 v[220:223], a[168:171], v[104:107], v[220:223]\n"
"ds_read_b64_tr_b16 a[208:209], v244 offset:16896\n"
"v_cvt_pk_bf16_f32 v22, v12, v13\n"
"v_mfma_f32_16x16x32_bf16 v[220:223], a[172:175], v[108:111], v[220:223]\n"
"v_exp_f32 v14, v14\n"
"v_mfma_f32_16x16x32_bf16 v[224:227], a[176:179], v[96:99], v[32:35]\n"
"ds_read_b64_tr_b16 a[210:211], v244 offset:20992\n"
"v_add_f32 v44, v44, v6\n"
"v_mfma_f32_16x16x32_bf16 v[224:227], a[180:183], v[100:103], v[224:227]\n"
"v_exp_f32 v15, v15\n"
"v_mfma_f32_16x16x32_bf16 v[224:227], a[184:187], v[104:107], v[224:227]\n"
"ds_read_b64_tr_b16 a[212:213], v245 offset:16896\n"
"v_cvt_pk_bf16_f32 v23, v14, v15\n"
"v_mfma_f32_16x16x32_bf16 v[224:227], a[188:191], v[108:111], v[224:227]\n"
"v_exp_f32 v220, v220\n"
"v_mfma_f32_16x16x32_bf16 a[32:35], a[224:227], v[20:23], a[32:35]\n"
"buffer_load_dwordx4 v247, s[8:11], s84 offen offset:2048 lds\n"
"v_add_u32 v251, 1179648, v251\n"
"v_mfma_f32_16x16x32_bf16 a[36:39], a[228:231], v[20:23], a[36:39]\n"
"v_exp_f32 v221, v221\n"
"v_mfma_f32_16x16x32_bf16 a[40:43], a[232:235], v[20:23], a[40:43]\n"
"ds_read_b64_tr_b16 a[214:215], v245 offset:20992\n"
"v_cvt_pk_bf16_f32 v16, v220, v221\n"
"v_mfma_f32_16x16x32_bf16 a[44:47], a[236:239], v[20:23], a[44:47]\n"
"v_exp_f32 v222, v222\n"
"v_mfma_f32_16x16x32_bf16 a[48:51], a[240:243], v[20:23], a[48:51]\n"
"ds_read_b64_tr_b16 a[216:217], v244 offset:17152\n"
"v_add_f32 v44, v44, v7\n"
"v_mfma_f32_16x16x32_bf16 a[52:55], a[244:247], v[20:23], a[52:55]\n"
"v_exp_f32 v223, v223\n"
"v_mfma_f32_16x16x32_bf16 a[56:59], a[248:251], v[20:23], a[56:59]\n"
"ds_read_b64_tr_b16 a[218:219], v244 offset:21248\n"
"v_cvt_pk_bf16_f32 v17, v222, v223\n"
"v_mfma_f32_16x16x32_bf16 a[60:63], a[252:255], v[20:23], a[60:63]\n"
"v_exp_f32 v224, v224\n"
"v_mfma_f32_16x16x32_bf16 v[48:51], v[40:43], v[20:23], v[48:51]\n"
"ds_read_b64_tr_b16 a[220:221], v245 offset:17152\n"
"v_add_f32 v52, v52, v220\n"
"v_mfma_f32_16x16x32_bf16 v[228:231], a[160:163], v[112:115], v[36:39]\n"
"v_exp_f32 v225, v225\n"
"v_mfma_f32_16x16x32_bf16 v[228:231], a[164:167], v[116:119], v[228:231]\n"
"ds_read_b64_tr_b16 a[222:223], v245 offset:21248\n"
"v_cvt_pk_bf16_f32 v18, v224, v225\n"
"v_mfma_f32_16x16x32_bf16 v[228:231], a[168:171], v[120:123], v[228:231]\n"
"v_exp_f32 v226, v226\n"
"v_mfma_f32_16x16x32_bf16 v[228:231], a[172:175], v[124:127], v[228:231]\n"
"v_add_f32 v52, v52, v221\n"
"v_add_f32 v52, v52, v222\n"
"v_mfma_f32_16x16x32_bf16 v[232:235], a[176:179], v[112:115], v[36:39]\n"
"v_exp_f32 v227, v227\n"
"v_mfma_f32_16x16x32_bf16 v[232:235], a[180:183], v[116:119], v[232:235]\n"
"v_cvt_pk_bf16_f32 v19, v226, v227\n"
"v_add_f32 v52, v52, v223\n"
"v_mfma_f32_16x16x32_bf16 v[232:235], a[184:187], v[120:123], v[232:235]\n"
"buffer_load_dwordx4 v247, s[8:11], s84 offen offset:3072 lds\n"
"v_mfma_f32_16x16x32_bf16 v[232:235], a[188:191], v[124:127], v[232:235]\n"
"v_exp_f32 v228, v228\n"
"v_mfma_f32_16x16x32_bf16 a[64:67], a[224:227], v[16:19], a[64:67]\n"
"v_add_f32 v52, v52, v224\n"
"v_add_f32 v52, v52, v225\n"
"v_mfma_f32_16x16x32_bf16 a[68:71], a[228:231], v[16:19], a[68:71]\n"
"v_exp_f32 v229, v229\n"
"v_mfma_f32_16x16x32_bf16 a[72:75], a[232:235], v[16:19], a[72:75]\n"
"v_cvt_pk_bf16_f32 v20, v228, v229\n"
"v_add_f32 v52, v52, v226\n"
"v_mfma_f32_16x16x32_bf16 a[76:79], a[236:239], v[16:19], a[76:79]\n"
"v_exp_f32 v230, v230\n"
"v_mfma_f32_16x16x32_bf16 a[80:83], a[240:243], v[16:19], a[80:83]\n"
"v_add_f32 v52, v52, v227\n"
"v_mfma_f32_16x16x32_bf16 a[84:87], a[244:247], v[16:19], a[84:87]\n"
"v_exp_f32 v231, v231\n"
"v_mfma_f32_16x16x32_bf16 a[88:91], a[248:251], v[16:19], a[88:91]\n"
"s_waitcnt lgkmcnt(8)\n"
"v_cvt_pk_bf16_f32 v21, v230, v231\n"
"v_mfma_f32_16x16x32_bf16 a[92:95], a[252:255], v[16:19], a[92:95]\n"
"v_exp_f32 v232, v232\n"
"v_mfma_f32_16x16x32_bf16 v[0:3], a[128:131], v[64:67], v[24:27]\n"
"ds_read_b128 a[160:163], v241 offset:8192\n"
"v_mfma_f32_16x16x32_bf16 v[0:3], a[132:135], v[68:71], v[0:3]\n"
"v_exp_f32 v233, v233\n"
"v_mfma_f32_16x16x32_bf16 v[0:3], a[136:139], v[72:75], v[0:3]\n"
"ds_read_b128 a[164:167], v241 offset:8448\n"
"v_cvt_pk_bf16_f32 v22, v232, v233\n"
"v_mfma_f32_16x16x32_bf16 v[0:3], a[140:143], v[76:79], v[0:3]\n"
"v_exp_f32 v234, v234\n"
"v_mfma_f32_16x16x32_bf16 v[4:7], a[144:147], v[64:67], v[24:27]\n"
"ds_read_b128 a[168:171], v241 offset:8704\n"
"v_mfma_f32_16x16x32_bf16 v[4:7], a[148:151], v[68:71], v[4:7]\n"
"v_exp_f32 v235, v235\n"
"v_mfma_f32_16x16x32_bf16 v[4:7], a[152:155], v[72:75], v[4:7]\n"
"ds_read_b128 a[172:175], v241 offset:8960\n"
"v_cvt_pk_bf16_f32 v23, v234, v235\n"
"v_mfma_f32_16x16x32_bf16 v[4:7], a[156:159], v[76:79], v[4:7]\n"
"s_add_u32 m0, s86, 49152\n"
"v_exp_f32 v0, v0\n"
"v_mfma_f32_16x16x32_bf16 a[96:99], a[224:227], v[20:23], a[96:99]\n"
"buffer_load_dwordx4 v246, s[12:15], s84 offen offset:0 lds\n"
"v_mfma_f32_16x16x32_bf16 a[100:103], a[228:231], v[20:23], a[100:103]\n"
"v_exp_f32 v1, v1\n"
"v_mfma_f32_16x16x32_bf16 a[104:107], a[232:235], v[20:23], a[104:107]\n"
"ds_read_b128 a[176:179], v241 offset:12288\n"
"v_cvt_pk_bf16_f32 v16, v0, v1\n"
"v_mfma_f32_16x16x32_bf16 a[108:111], a[236:239], v[20:23], a[108:111]\n"
"v_exp_f32 v2, v2\n"
"v_mfma_f32_16x16x32_bf16 a[112:115], a[240:243], v[20:23], a[112:115]\n"
"ds_read_b128 a[180:183], v241 offset:12544\n"
"v_add_f32 v44, v44, v0\n"
"v_mfma_f32_16x16x32_bf16 a[116:119], a[244:247], v[20:23], a[116:119]\n"
"v_exp_f32 v3, v3\n"
"v_mfma_f32_16x16x32_bf16 a[120:123], a[248:251], v[20:23], a[120:123]\n"
"ds_read_b128 a[184:187], v241 offset:12800\n"
"v_cvt_pk_bf16_f32 v17, v2, v3\n"
"v_mfma_f32_16x16x32_bf16 a[124:127], a[252:255], v[20:23], a[124:127]\n"
"v_exp_f32 v4, v4\n"
"v_mfma_f32_16x16x32_bf16 v[56:59], v[40:43], v[20:23], v[56:59]\n"
"ds_read_b128 a[188:191], v241 offset:13056\n"
"v_add_f32 v44, v44, v1\n"
"v_mfma_f32_16x16x32_bf16 v[8:11], a[128:131], v[80:83], v[28:31]\n"
"v_exp_f32 v5, v5\n"
"v_mfma_f32_16x16x32_bf16 v[8:11], a[132:135], v[84:87], v[8:11]\n"
"ds_read_b64_tr_b16 a[224:225], v244 offset:24576\n"
"v_cvt_pk_bf16_f32 v18, v4, v5\n"
"v_mfma_f32_16x16x32_bf16 v[8:11], a[136:139], v[88:91], v[8:11]\n"
"v_exp_f32 v6, v6\n"
"v_mfma_f32_16x16x32_bf16 v[8:11], a[140:143], v[92:95], v[8:11]\n"
"ds_read_b64_tr_b16 a[226:227], v244 offset:28672\n"
"v_add_f32 v44, v44, v2\n"
"v_mfma_f32_16x16x32_bf16 v[12:15], a[144:147], v[80:83], v[28:31]\n"
"v_exp_f32 v7, v7\n"
"v_mfma_f32_16x16x32_bf16 v[12:15], a[148:151], v[84:87], v[12:15]\n"
"ds_read_b64_tr_b16 a[228:229], v245 offset:24576\n"
"v_cvt_pk_bf16_f32 v19, v6, v7\n"
"v_mfma_f32_16x16x32_bf16 v[12:15], a[152:155], v[88:91], v[12:15]\n"
"buffer_load_dwordx4 v247, s[12:15], s84 offen offset:1024 lds\n"
"v_mfma_f32_16x16x32_bf16 v[12:15], a[156:159], v[92:95], v[12:15]\n"
"s_waitcnt lgkmcnt(11)\n"
"v_exp_f32 v8, v8\n"
"v_mfma_f32_16x16x32_bf16 a[0:3], a[192:195], v[16:19], a[0:3]\n"
"ds_read_b64_tr_b16 a[230:231], v245 offset:28672\n"
"v_add_f32 v44, v44, v3\n"
"v_mfma_f32_16x16x32_bf16 a[4:7], a[196:199], v[16:19], a[4:7]\n"
"v_exp_f32 v9, v9\n"
"v_mfma_f32_16x16x32_bf16 a[8:11], a[200:203], v[16:19], a[8:11]\n"
"ds_read_b64_tr_b16 a[232:233], v244 offset:24832\n"
"v_cvt_pk_bf16_f32 v20, v8, v9\n"
"v_mfma_f32_16x16x32_bf16 a[12:15], a[204:207], v[16:19], a[12:15]\n"
"v_exp_f32 v10, v10\n"
"v_mfma_f32_16x16x32_bf16 a[16:19], a[208:211], v[16:19], a[16:19]\n"
"ds_read_b64_tr_b16 a[234:235], v244 offset:28928\n"
"v_add_f32 v44, v44, v4\n"
"v_mfma_f32_16x16x32_bf16 a[20:23], a[212:215], v[16:19], a[20:23]\n"
"v_exp_f32 v11, v11\n"
"v_mfma_f32_16x16x32_bf16 a[24:27], a[216:219], v[16:19], a[24:27]\n"
"ds_read_b64_tr_b16 a[236:237], v245 offset:24832\n"
"v_cvt_pk_bf16_f32 v21, v10, v11\n"
"v_mfma_f32_16x16x32_bf16 a[28:31], a[220:223], v[16:19], a[28:31]\n"
"v_exp_f32 v12, v12\n"
"v_mfma_f32_16x16x32_bf16 v[220:223], a[128:131], v[96:99], v[32:35]\n"
"ds_read_b64_tr_b16 a[238:239], v245 offset:28928\n"
"v_add_f32 v44, v44, v5\n"
"v_mfma_f32_16x16x32_bf16 v[220:223], a[132:135], v[100:103], v[220:223]\n"
"v_exp_f32 v13, v13\n"
"v_mfma_f32_16x16x32_bf16 v[220:223], a[136:139], v[104:107], v[220:223]\n"
"ds_read_b64_tr_b16 a[240:241], v244 offset:25088\n"
"v_cvt_pk_bf16_f32 v22, v12, v13\n"
"v_mfma_f32_16x16x32_bf16 v[220:223], a[140:143], v[108:111], v[220:223]\n"
"v_exp_f32 v14, v14\n"
"v_mfma_f32_16x16x32_bf16 v[224:227], a[144:147], v[96:99], v[32:35]\n"
"ds_read_b64_tr_b16 a[242:243], v244 offset:29184\n"
"v_add_f32 v44, v44, v6\n"
"v_mfma_f32_16x16x32_bf16 v[224:227], a[148:151], v[100:103], v[224:227]\n"
"v_exp_f32 v15, v15\n"
"v_mfma_f32_16x16x32_bf16 v[224:227], a[152:155], v[104:107], v[224:227]\n"
"ds_read_b64_tr_b16 a[244:245], v245 offset:25088\n"
"v_cvt_pk_bf16_f32 v23, v14, v15\n"
"v_mfma_f32_16x16x32_bf16 v[224:227], a[156:159], v[108:111], v[224:227]\n"
"v_exp_f32 v220, v220\n"
"v_mfma_f32_16x16x32_bf16 a[32:35], a[192:195], v[20:23], a[32:35]\n"
"buffer_load_dwordx4 v246, s[12:15], s84 offen offset:2048 lds\n"
"v_mfma_f32_16x16x32_bf16 a[36:39], a[196:199], v[20:23], a[36:39]\n"
"v_exp_f32 v221, v221\n"
"v_mfma_f32_16x16x32_bf16 a[40:43], a[200:203], v[20:23], a[40:43]\n"
"ds_read_b64_tr_b16 a[246:247], v245 offset:29184\n"
"v_cvt_pk_bf16_f32 v16, v220, v221\n"
"v_mfma_f32_16x16x32_bf16 a[44:47], a[204:207], v[20:23], a[44:47]\n"
"v_exp_f32 v222, v222\n"
"v_mfma_f32_16x16x32_bf16 a[48:51], a[208:211], v[20:23], a[48:51]\n"
"ds_read_b64_tr_b16 a[248:249], v244 offset:25344\n"
"v_add_f32 v44, v44, v7\n"
"v_mfma_f32_16x16x32_bf16 a[52:55], a[212:215], v[20:23], a[52:55]\n"
"v_exp_f32 v223, v223\n"
"v_mfma_f32_16x16x32_bf16 a[56:59], a[216:219], v[20:23], a[56:59]\n"
"ds_read_b64_tr_b16 a[250:251], v244 offset:29440\n"
"v_cvt_pk_bf16_f32 v17, v222, v223\n"
"v_mfma_f32_16x16x32_bf16 a[60:63], a[220:223], v[20:23], a[60:63]\n"
"v_exp_f32 v224, v224\n"
"v_mfma_f32_16x16x32_bf16 v[48:51], v[40:43], v[20:23], v[48:51]\n"
"ds_read_b64_tr_b16 a[252:253], v245 offset:25344\n"
"v_add_f32 v52, v52, v220\n"
"v_mfma_f32_16x16x32_bf16 v[228:231], a[128:131], v[112:115], v[36:39]\n"
"v_exp_f32 v225, v225\n"
"v_mfma_f32_16x16x32_bf16 v[228:231], a[132:135], v[116:119], v[228:231]\n"
"ds_read_b64_tr_b16 a[254:255], v245 offset:29440\n"
"v_cvt_pk_bf16_f32 v18, v224, v225\n"
"v_mfma_f32_16x16x32_bf16 v[228:231], a[136:139], v[120:123], v[228:231]\n"
"v_exp_f32 v226, v226\n"
"v_mfma_f32_16x16x32_bf16 v[228:231], a[140:143], v[124:127], v[228:231]\n"
"v_add_f32 v52, v52, v221\n"
"v_add_f32 v52, v52, v222\n"
"v_mfma_f32_16x16x32_bf16 v[232:235], a[144:147], v[112:115], v[36:39]\n"
"v_exp_f32 v227, v227\n"
"v_mfma_f32_16x16x32_bf16 v[232:235], a[148:151], v[116:119], v[232:235]\n"
"v_cvt_pk_bf16_f32 v19, v226, v227\n"
"v_add_f32 v52, v52, v223\n"
"v_mfma_f32_16x16x32_bf16 v[232:235], a[152:155], v[120:123], v[232:235]\n"
"buffer_load_dwordx4 v247, s[12:15], s84 offen offset:3072 lds\n"
"v_mfma_f32_16x16x32_bf16 v[232:235], a[156:159], v[124:127], v[232:235]\n"
"s_add_u32 s84, s84, 16384\n"
"v_exp_f32 v228, v228\n"
"v_mfma_f32_16x16x32_bf16 a[64:67], a[192:195], v[16:19], a[64:67]\n"
"v_add_f32 v52, v52, v224\n"
"v_add_f32 v52, v52, v225\n"
"v_mfma_f32_16x16x32_bf16 a[68:71], a[196:199], v[16:19], a[68:71]\n"
"v_exp_f32 v229, v229\n"
"v_mfma_f32_16x16x32_bf16 a[72:75], a[200:203], v[16:19], a[72:75]\n"
"v_cvt_pk_bf16_f32 v20, v228, v229\n"
"v_add_f32 v52, v52, v226\n"
"v_mfma_f32_16x16x32_bf16 a[76:79], a[204:207], v[16:19], a[76:79]\n"
"v_exp_f32 v230, v230\n"
"v_mfma_f32_16x16x32_bf16 a[80:83], a[208:211], v[16:19], a[80:83]\n"
"v_add_f32 v52, v52, v227\n"
"v_mfma_f32_16x16x32_bf16 a[84:87], a[212:215], v[16:19], a[84:87]\n"
"v_exp_f32 v231, v231\n"
"v_mfma_f32_16x16x32_bf16 a[88:91], a[216:219], v[16:19], a[88:91]\n"
"s_waitcnt vmcnt(20) lgkmcnt(0)\n"
"s_barrier\n"
"v_cvt_pk_bf16_f32 v21, v230, v231\n"
"v_mfma_f32_16x16x32_bf16 a[92:95], a[220:223], v[16:19], a[92:95]\n"
"v_exp_f32 v232, v232\n"
"v_mfma_f32_16x16x32_bf16 v[0:3], a[160:163], v[64:67], v[24:27]\n"
"ds_read_b128 a[128:131], v241 offset:32768\n"
"buffer_load_dword v250, v252, s[72:75], s46 offen\n"
"v_mfma_f32_16x16x32_bf16 v[0:3], a[164:167], v[68:71], v[0:3]\n"
"v_exp_f32 v233, v233\n"
"v_mfma_f32_16x16x32_bf16 v[0:3], a[168:171], v[72:75], v[0:3]\n"
"ds_read_b128 a[132:135], v241 offset:33024\n"
"v_cvt_pk_bf16_f32 v22, v232, v233\n"
"v_mfma_f32_16x16x32_bf16 v[0:3], a[172:175], v[76:79], v[0:3]\n"
"v_exp_f32 v234, v234\n"
"v_mfma_f32_16x16x32_bf16 v[4:7], a[176:179], v[64:67], v[24:27]\n"
"ds_read_b128 a[136:139], v241 offset:33280\n"
"v_add_u32 v252, 147456, v252\n"
"v_mfma_f32_16x16x32_bf16 v[4:7], a[180:183], v[68:71], v[4:7]\n"
"v_exp_f32 v235, v235\n"
"v_mfma_f32_16x16x32_bf16 v[4:7], a[184:187], v[72:75], v[4:7]\n"
"ds_read_b128 a[140:143], v241 offset:33536\n"
"v_cvt_pk_bf16_f32 v23, v234, v235\n"
"v_mfma_f32_16x16x32_bf16 v[4:7], a[188:191], v[76:79], v[4:7]\n"
"s_add_u32 m0, s86, 65536\n"
"v_exp_f32 v0, v0\n"
"v_mfma_f32_16x16x32_bf16 a[96:99], a[192:195], v[20:23], a[96:99]\n"
"buffer_load_dwordx4 v246, s[8:11], s84 offen offset:0 lds\n"
"v_mfma_f32_16x16x32_bf16 a[100:103], a[196:199], v[20:23], a[100:103]\n"
"v_exp_f32 v1, v1\n"
"v_mfma_f32_16x16x32_bf16 a[104:107], a[200:203], v[20:23], a[104:107]\n"
"ds_read_b128 a[144:147], v241 offset:36864\n"
"v_cvt_pk_bf16_f32 v16, v0, v1\n"
"v_mfma_f32_16x16x32_bf16 a[108:111], a[204:207], v[20:23], a[108:111]\n"
"v_exp_f32 v2, v2\n"
"v_mfma_f32_16x16x32_bf16 a[112:115], a[208:211], v[20:23], a[112:115]\n"
"ds_read_b128 a[148:151], v241 offset:37120\n"
"v_add_f32 v44, v44, v0\n"
"v_mfma_f32_16x16x32_bf16 a[116:119], a[212:215], v[20:23], a[116:119]\n"
"v_exp_f32 v3, v3\n"
"v_mfma_f32_16x16x32_bf16 a[120:123], a[216:219], v[20:23], a[120:123]\n"
"ds_read_b128 a[152:155], v241 offset:37376\n"
"v_cvt_pk_bf16_f32 v17, v2, v3\n"
"v_mfma_f32_16x16x32_bf16 a[124:127], a[220:223], v[20:23], a[124:127]\n"
"v_exp_f32 v4, v4\n"
"v_mfma_f32_16x16x32_bf16 v[56:59], v[40:43], v[20:23], v[56:59]\n"
"ds_read_b128 a[156:159], v241 offset:37632\n"
"v_add_f32 v44, v44, v1\n"
"v_mfma_f32_16x16x32_bf16 v[8:11], a[160:163], v[80:83], v[28:31]\n"
"v_exp_f32 v5, v5\n"
"v_mfma_f32_16x16x32_bf16 v[8:11], a[164:167], v[84:87], v[8:11]\n"
"ds_read_b64_tr_b16 a[192:193], v244 offset:49152\n"
"v_cvt_pk_bf16_f32 v18, v4, v5\n"
"v_mfma_f32_16x16x32_bf16 v[8:11], a[168:171], v[88:91], v[8:11]\n"
"v_exp_f32 v6, v6\n"
"v_mfma_f32_16x16x32_bf16 v[8:11], a[172:175], v[92:95], v[8:11]\n"
"ds_read_b64_tr_b16 a[194:195], v244 offset:53248\n"
"v_add_f32 v44, v44, v2\n"
"v_mfma_f32_16x16x32_bf16 v[12:15], a[176:179], v[80:83], v[28:31]\n"
"v_exp_f32 v7, v7\n"
"v_mfma_f32_16x16x32_bf16 v[12:15], a[180:183], v[84:87], v[12:15]\n"
"ds_read_b64_tr_b16 a[196:197], v245 offset:49152\n"
"v_cvt_pk_bf16_f32 v19, v6, v7\n"
"v_mfma_f32_16x16x32_bf16 v[12:15], a[184:187], v[88:91], v[12:15]\n"
"buffer_load_dwordx4 v246, s[8:11], s84 offen offset:1024 lds\n"
"v_mfma_f32_16x16x32_bf16 v[12:15], a[188:191], v[92:95], v[12:15]\n"
"s_waitcnt lgkmcnt(11)\n"
"v_exp_f32 v8, v8\n"
"v_mfma_f32_16x16x32_bf16 a[0:3], a[224:227], v[16:19], a[0:3]\n"
"ds_read_b64_tr_b16 a[198:199], v245 offset:53248\n"
"v_add_f32 v44, v44, v3\n"
"v_mfma_f32_16x16x32_bf16 a[4:7], a[228:231], v[16:19], a[4:7]\n"
"v_exp_f32 v9, v9\n"
"v_mfma_f32_16x16x32_bf16 a[8:11], a[232:235], v[16:19], a[8:11]\n"
"ds_read_b64_tr_b16 a[200:201], v244 offset:49408\n"
"v_cvt_pk_bf16_f32 v20, v8, v9\n"
"v_mfma_f32_16x16x32_bf16 a[12:15], a[236:239], v[16:19], a[12:15]\n"
"v_exp_f32 v10, v10\n"
"v_mfma_f32_16x16x32_bf16 a[16:19], a[240:243], v[16:19], a[16:19]\n"
"ds_read_b64_tr_b16 a[202:203], v244 offset:53504\n"
"v_add_f32 v44, v44, v4\n"
"v_mfma_f32_16x16x32_bf16 a[20:23], a[244:247], v[16:19], a[20:23]\n"
"v_exp_f32 v11, v11\n"
"v_mfma_f32_16x16x32_bf16 a[24:27], a[248:251], v[16:19], a[24:27]\n"
"ds_read_b64_tr_b16 a[204:205], v245 offset:49408\n"
"v_cvt_pk_bf16_f32 v21, v10, v11\n"
"v_mfma_f32_16x16x32_bf16 a[28:31], a[252:255], v[16:19], a[28:31]\n"
"v_exp_f32 v12, v12\n"
"v_mfma_f32_16x16x32_bf16 v[220:223], a[160:163], v[96:99], v[32:35]\n"
"ds_read_b64_tr_b16 a[206:207], v245 offset:53504\n"
"v_add_f32 v44, v44, v5\n"
"v_mfma_f32_16x16x32_bf16 v[220:223], a[164:167], v[100:103], v[220:223]\n"
"v_exp_f32 v13, v13\n"
"v_mfma_f32_16x16x32_bf16 v[220:223], a[168:171], v[104:107], v[220:223]\n"
"ds_read_b64_tr_b16 a[208:209], v244 offset:49664\n"
"v_cvt_pk_bf16_f32 v22, v12, v13\n"
"v_mfma_f32_16x16x32_bf16 v[220:223], a[172:175], v[108:111], v[220:223]\n"
"v_exp_f32 v14, v14\n"
"v_mfma_f32_16x16x32_bf16 v[224:227], a[176:179], v[96:99], v[32:35]\n"
"ds_read_b64_tr_b16 a[210:211], v244 offset:53760\n"
"v_add_f32 v44, v44, v6\n"
"v_mfma_f32_16x16x32_bf16 v[224:227], a[180:183], v[100:103], v[224:227]\n"
"v_exp_f32 v15, v15\n"
"v_mfma_f32_16x16x32_bf16 v[224:227], a[184:187], v[104:107], v[224:227]\n"
"ds_read_b64_tr_b16 a[212:213], v245 offset:49664\n"
"v_cvt_pk_bf16_f32 v23, v14, v15\n"
"v_mfma_f32_16x16x32_bf16 v[224:227], a[188:191], v[108:111], v[224:227]\n"
"v_exp_f32 v220, v220\n"
"v_mfma_f32_16x16x32_bf16 a[32:35], a[224:227], v[20:23], a[32:35]\n"
"buffer_load_dwordx4 v247, s[8:11], s84 offen offset:2048 lds\n"
"v_mfma_f32_16x16x32_bf16 a[36:39], a[228:231], v[20:23], a[36:39]\n"
"v_exp_f32 v221, v221\n"
"v_mfma_f32_16x16x32_bf16 a[40:43], a[232:235], v[20:23], a[40:43]\n"
"ds_read_b64_tr_b16 a[214:215], v245 offset:53760\n"
"v_cvt_pk_bf16_f32 v16, v220, v221\n"
"v_mfma_f32_16x16x32_bf16 a[44:47], a[236:239], v[20:23], a[44:47]\n"
"v_exp_f32 v222, v222\n"
"v_mfma_f32_16x16x32_bf16 a[48:51], a[240:243], v[20:23], a[48:51]\n"
"ds_read_b64_tr_b16 a[216:217], v244 offset:49920\n"
"v_add_f32 v44, v44, v7\n"
"v_mfma_f32_16x16x32_bf16 a[52:55], a[244:247], v[20:23], a[52:55]\n"
"v_exp_f32 v223, v223\n"
"v_mfma_f32_16x16x32_bf16 a[56:59], a[248:251], v[20:23], a[56:59]\n"
"ds_read_b64_tr_b16 a[218:219], v244 offset:54016\n"
"v_cvt_pk_bf16_f32 v17, v222, v223\n"
"v_mfma_f32_16x16x32_bf16 a[60:63], a[252:255], v[20:23], a[60:63]\n"
"v_exp_f32 v224, v224\n"
"v_mfma_f32_16x16x32_bf16 v[48:51], v[40:43], v[20:23], v[48:51]\n"
"ds_read_b64_tr_b16 a[220:221], v245 offset:49920\n"
"v_add_f32 v52, v52, v220\n"
"v_mfma_f32_16x16x32_bf16 v[228:231], a[160:163], v[112:115], v[36:39]\n"
"v_exp_f32 v225, v225\n"
"v_mfma_f32_16x16x32_bf16 v[228:231], a[164:167], v[116:119], v[228:231]\n"
"ds_read_b64_tr_b16 a[222:223], v245 offset:54016\n"
"v_cvt_pk_bf16_f32 v18, v224, v225\n"
"v_mfma_f32_16x16x32_bf16 v[228:231], a[168:171], v[120:123], v[228:231]\n"
"v_exp_f32 v226, v226\n"
"v_mfma_f32_16x16x32_bf16 v[228:231], a[172:175], v[124:127], v[228:231]\n"
"v_add_f32 v52, v52, v221\n"
"v_add_f32 v52, v52, v222\n"
"v_mfma_f32_16x16x32_bf16 v[232:235], a[176:179], v[112:115], v[36:39]\n"
"v_exp_f32 v227, v227\n"
"v_mfma_f32_16x16x32_bf16 v[232:235], a[180:183], v[116:119], v[232:235]\n"
"v_cvt_pk_bf16_f32 v19, v226, v227\n"
"v_add_f32 v52, v52, v223\n"
"v_mfma_f32_16x16x32_bf16 v[232:235], a[184:187], v[120:123], v[232:235]\n"
"buffer_load_dwordx4 v247, s[8:11], s84 offen offset:3072 lds\n"
"v_mfma_f32_16x16x32_bf16 v[232:235], a[188:191], v[124:127], v[232:235]\n"
"v_exp_f32 v228, v228\n"
"v_mfma_f32_16x16x32_bf16 a[64:67], a[224:227], v[16:19], a[64:67]\n"
"v_add_f32 v52, v52, v224\n"
"v_add_f32 v52, v52, v225\n"
"v_mfma_f32_16x16x32_bf16 a[68:71], a[228:231], v[16:19], a[68:71]\n"
"v_exp_f32 v229, v229\n"
"v_mfma_f32_16x16x32_bf16 a[72:75], a[232:235], v[16:19], a[72:75]\n"
"v_cvt_pk_bf16_f32 v20, v228, v229\n"
"v_add_f32 v52, v52, v226\n"
"v_mfma_f32_16x16x32_bf16 a[76:79], a[236:239], v[16:19], a[76:79]\n"
"v_exp_f32 v230, v230\n"
"v_mfma_f32_16x16x32_bf16 a[80:83], a[240:243], v[16:19], a[80:83]\n"
"v_add_f32 v52, v52, v227\n"
"v_mfma_f32_16x16x32_bf16 a[84:87], a[244:247], v[16:19], a[84:87]\n"
"v_exp_f32 v231, v231\n"
"v_mfma_f32_16x16x32_bf16 a[88:91], a[248:251], v[16:19], a[88:91]\n"
"s_waitcnt lgkmcnt(8)\n"
"v_cvt_pk_bf16_f32 v21, v230, v231\n"
"v_mfma_f32_16x16x32_bf16 a[92:95], a[252:255], v[16:19], a[92:95]\n"
"v_exp_f32 v232, v232\n"
"v_mfma_f32_16x16x32_bf16 v[0:3], a[128:131], v[64:67], v[24:27]\n"
"ds_read_b128 a[160:163], v241 offset:40960\n"
"v_mfma_f32_16x16x32_bf16 v[0:3], a[132:135], v[68:71], v[0:3]\n"
"v_exp_f32 v233, v233\n"
"v_mfma_f32_16x16x32_bf16 v[0:3], a[136:139], v[72:75], v[0:3]\n"
"ds_read_b128 a[164:167], v241 offset:41216\n"
"v_cvt_pk_bf16_f32 v22, v232, v233\n"
"v_mfma_f32_16x16x32_bf16 v[0:3], a[140:143], v[76:79], v[0:3]\n"
"v_exp_f32 v234, v234\n"
"v_mfma_f32_16x16x32_bf16 v[4:7], a[144:147], v[64:67], v[24:27]\n"
"ds_read_b128 a[168:171], v241 offset:41472\n"
"v_mfma_f32_16x16x32_bf16 v[4:7], a[148:151], v[68:71], v[4:7]\n"
"v_exp_f32 v235, v235\n"
"v_mfma_f32_16x16x32_bf16 v[4:7], a[152:155], v[72:75], v[4:7]\n"
"ds_read_b128 a[172:175], v241 offset:41728\n"
"v_cvt_pk_bf16_f32 v23, v234, v235\n"
"v_mfma_f32_16x16x32_bf16 v[4:7], a[156:159], v[76:79], v[4:7]\n"
"s_add_u32 m0, s86, 81920\n"
"v_exp_f32 v0, v0\n"
"v_mfma_f32_16x16x32_bf16 a[96:99], a[224:227], v[20:23], a[96:99]\n"
"buffer_load_dwordx4 v246, s[12:15], s84 offen offset:0 lds\n"
"v_mfma_f32_16x16x32_bf16 a[100:103], a[228:231], v[20:23], a[100:103]\n"
"v_exp_f32 v1, v1\n"
"v_mfma_f32_16x16x32_bf16 a[104:107], a[232:235], v[20:23], a[104:107]\n"
"ds_read_b128 a[176:179], v241 offset:45056\n"
"v_cvt_pk_bf16_f32 v16, v0, v1\n"
"v_mfma_f32_16x16x32_bf16 a[108:111], a[236:239], v[20:23], a[108:111]\n"
"v_exp_f32 v2, v2\n"
"v_mfma_f32_16x16x32_bf16 a[112:115], a[240:243], v[20:23], a[112:115]\n"
"ds_read_b128 a[180:183], v241 offset:45312\n"
"v_add_f32 v44, v44, v0\n"
"v_mfma_f32_16x16x32_bf16 a[116:119], a[244:247], v[20:23], a[116:119]\n"
"v_exp_f32 v3, v3\n"
"v_mfma_f32_16x16x32_bf16 a[120:123], a[248:251], v[20:23], a[120:123]\n"
"ds_read_b128 a[184:187], v241 offset:45568\n"
"v_cvt_pk_bf16_f32 v17, v2, v3\n"
"v_mfma_f32_16x16x32_bf16 a[124:127], a[252:255], v[20:23], a[124:127]\n"
"v_exp_f32 v4, v4\n"
"v_mfma_f32_16x16x32_bf16 v[56:59], v[40:43], v[20:23], v[56:59]\n"
"ds_read_b128 a[188:191], v241 offset:45824\n"
"v_add_f32 v44, v44, v1\n"
"v_mfma_f32_16x16x32_bf16 v[8:11], a[128:131], v[80:83], v[28:31]\n"
"v_exp_f32 v5, v5\n"
"v_mfma_f32_16x16x32_bf16 v[8:11], a[132:135], v[84:87], v[8:11]\n"
"ds_read_b64_tr_b16 a[224:225], v244 offset:57344\n"
"v_cvt_pk_bf16_f32 v18, v4, v5\n"
"v_mfma_f32_16x16x32_bf16 v[8:11], a[136:139], v[88:91], v[8:11]\n"
"v_exp_f32 v6, v6\n"
"v_mfma_f32_16x16x32_bf16 v[8:11], a[140:143], v[92:95], v[8:11]\n"
"ds_read_b64_tr_b16 a[226:227], v244 offset:61440\n"
"v_add_f32 v44, v44, v2\n"
"v_mfma_f32_16x16x32_bf16 v[12:15], a[144:147], v[80:83], v[28:31]\n"
"v_exp_f32 v7, v7\n"
"v_mfma_f32_16x16x32_bf16 v[12:15], a[148:151], v[84:87], v[12:15]\n"
"ds_read_b64_tr_b16 a[228:229], v245 offset:57344\n"
"v_cvt_pk_bf16_f32 v19, v6, v7\n"
"v_mfma_f32_16x16x32_bf16 v[12:15], a[152:155], v[88:91], v[12:15]\n"
"buffer_load_dwordx4 v247, s[12:15], s84 offen offset:1024 lds\n"
"v_mfma_f32_16x16x32_bf16 v[12:15], a[156:159], v[92:95], v[12:15]\n"
"s_waitcnt lgkmcnt(11)\n"
"v_exp_f32 v8, v8\n"
"v_mfma_f32_16x16x32_bf16 a[0:3], a[192:195], v[16:19], a[0:3]\n"
"ds_read_b64_tr_b16 a[230:231], v245 offset:61440\n"
"v_add_f32 v44, v44, v3\n"
"v_mfma_f32_16x16x32_bf16 a[4:7], a[196:199], v[16:19], a[4:7]\n"
"v_exp_f32 v9, v9\n"
"v_mfma_f32_16x16x32_bf16 a[8:11], a[200:203], v[16:19], a[8:11]\n"
"ds_read_b64_tr_b16 a[232:233], v244 offset:57600\n"
"v_cvt_pk_bf16_f32 v20, v8, v9\n"
"v_mfma_f32_16x16x32_bf16 a[12:15], a[204:207], v[16:19], a[12:15]\n"
"v_exp_f32 v10, v10\n"
"v_mfma_f32_16x16x32_bf16 a[16:19], a[208:211], v[16:19], a[16:19]\n"
"ds_read_b64_tr_b16 a[234:235], v244 offset:61696\n"
"v_add_f32 v44, v44, v4\n"
"v_mfma_f32_16x16x32_bf16 a[20:23], a[212:215], v[16:19], a[20:23]\n"
"v_exp_f32 v11, v11\n"
"v_mfma_f32_16x16x32_bf16 a[24:27], a[216:219], v[16:19], a[24:27]\n"
"ds_read_b64_tr_b16 a[236:237], v245 offset:57600\n"
"v_cvt_pk_bf16_f32 v21, v10, v11\n"
"v_mfma_f32_16x16x32_bf16 a[28:31], a[220:223], v[16:19], a[28:31]\n"
"v_exp_f32 v12, v12\n"
"v_mfma_f32_16x16x32_bf16 v[220:223], a[128:131], v[96:99], v[32:35]\n"
"ds_read_b64_tr_b16 a[238:239], v245 offset:61696\n"
"v_add_f32 v44, v44, v5\n"
"v_mfma_f32_16x16x32_bf16 v[220:223], a[132:135], v[100:103], v[220:223]\n"
"v_exp_f32 v13, v13\n"
"v_mfma_f32_16x16x32_bf16 v[220:223], a[136:139], v[104:107], v[220:223]\n"
"ds_read_b64_tr_b16 a[240:241], v244 offset:57856\n"
"v_cvt_pk_bf16_f32 v22, v12, v13\n"
"v_mfma_f32_16x16x32_bf16 v[220:223], a[140:143], v[108:111], v[220:223]\n"
"v_exp_f32 v14, v14\n"
"v_mfma_f32_16x16x32_bf16 v[224:227], a[144:147], v[96:99], v[32:35]\n"
"ds_read_b64_tr_b16 a[242:243], v244 offset:61952\n"
"v_add_f32 v44, v44, v6\n"
"v_mfma_f32_16x16x32_bf16 v[224:227], a[148:151], v[100:103], v[224:227]\n"
"v_exp_f32 v15, v15\n"
"v_mfma_f32_16x16x32_bf16 v[224:227], a[152:155], v[104:107], v[224:227]\n"
"ds_read_b64_tr_b16 a[244:245], v245 offset:57856\n"
"v_cvt_pk_bf16_f32 v23, v14, v15\n"
"v_mfma_f32_16x16x32_bf16 v[224:227], a[156:159], v[108:111], v[224:227]\n"
"v_exp_f32 v220, v220\n"
"v_mfma_f32_16x16x32_bf16 a[32:35], a[192:195], v[20:23], a[32:35]\n"
"buffer_load_dwordx4 v246, s[12:15], s84 offen offset:2048 lds\n"
"v_mfma_f32_16x16x32_bf16 a[36:39], a[196:199], v[20:23], a[36:39]\n"
"v_exp_f32 v221, v221\n"
"v_mfma_f32_16x16x32_bf16 a[40:43], a[200:203], v[20:23], a[40:43]\n"
"ds_read_b64_tr_b16 a[246:247], v245 offset:61952\n"
"v_cvt_pk_bf16_f32 v16, v220, v221\n"
"v_mfma_f32_16x16x32_bf16 a[44:47], a[204:207], v[20:23], a[44:47]\n"
"v_exp_f32 v222, v222\n"
"v_mfma_f32_16x16x32_bf16 a[48:51], a[208:211], v[20:23], a[48:51]\n"
"ds_read_b64_tr_b16 a[248:249], v244 offset:58112\n"
"v_add_f32 v44, v44, v7\n"
"v_mfma_f32_16x16x32_bf16 a[52:55], a[212:215], v[20:23], a[52:55]\n"
"v_exp_f32 v223, v223\n"
"v_mfma_f32_16x16x32_bf16 a[56:59], a[216:219], v[20:23], a[56:59]\n"
"ds_read_b64_tr_b16 a[250:251], v244 offset:62208\n"
"v_cvt_pk_bf16_f32 v17, v222, v223\n"
"v_mfma_f32_16x16x32_bf16 a[60:63], a[220:223], v[20:23], a[60:63]\n"
"v_exp_f32 v224, v224\n"
"v_mfma_f32_16x16x32_bf16 v[48:51], v[40:43], v[20:23], v[48:51]\n"
"ds_read_b64_tr_b16 a[252:253], v245 offset:58112\n"
"v_add_f32 v52, v52, v220\n"
"v_mfma_f32_16x16x32_bf16 v[228:231], a[128:131], v[112:115], v[36:39]\n"
"v_exp_f32 v225, v225\n"
"v_mfma_f32_16x16x32_bf16 v[228:231], a[132:135], v[116:119], v[228:231]\n"
"ds_read_b64_tr_b16 a[254:255], v245 offset:62208\n"
"v_cvt_pk_bf16_f32 v18, v224, v225\n"
"v_mfma_f32_16x16x32_bf16 v[228:231], a[136:139], v[120:123], v[228:231]\n"
"v_exp_f32 v226, v226\n"
"v_mfma_f32_16x16x32_bf16 v[228:231], a[140:143], v[124:127], v[228:231]\n"
"v_add_f32 v52, v52, v221\n"
"v_add_f32 v52, v52, v222\n"
"v_mfma_f32_16x16x32_bf16 v[232:235], a[144:147], v[112:115], v[36:39]\n"
"v_exp_f32 v227, v227\n"
"v_mfma_f32_16x16x32_bf16 v[232:235], a[148:151], v[116:119], v[232:235]\n"
"v_cvt_pk_bf16_f32 v19, v226, v227\n"
"v_add_f32 v52, v52, v223\n"
"v_mfma_f32_16x16x32_bf16 v[232:235], a[152:155], v[120:123], v[232:235]\n"
"buffer_load_dwordx4 v247, s[12:15], s84 offen offset:3072 lds\n"
"v_mfma_f32_16x16x32_bf16 v[232:235], a[156:159], v[124:127], v[232:235]\n"
"s_add_u32 s84, s84, 16384\n"
"v_exp_f32 v228, v228\n"
"v_mfma_f32_16x16x32_bf16 a[64:67], a[192:195], v[16:19], a[64:67]\n"
"v_add_f32 v52, v52, v224\n"
"v_add_f32 v52, v52, v225\n"
"v_mfma_f32_16x16x32_bf16 a[68:71], a[196:199], v[16:19], a[68:71]\n"
"v_exp_f32 v229, v229\n"
"v_mfma_f32_16x16x32_bf16 a[72:75], a[200:203], v[16:19], a[72:75]\n"
"v_cvt_pk_bf16_f32 v20, v228, v229\n"
"v_add_f32 v52, v52, v226\n"
"v_mfma_f32_16x16x32_bf16 a[76:79], a[204:207], v[16:19], a[76:79]\n"
"v_exp_f32 v230, v230\n"
"v_mfma_f32_16x16x32_bf16 a[80:83], a[208:211], v[16:19], a[80:83]\n"
"v_add_f32 v52, v52, v227\n"
"v_mfma_f32_16x16x32_bf16 a[84:87], a[212:215], v[16:19], a[84:87]\n"
"v_exp_f32 v231, v231\n"
"v_mfma_f32_16x16x32_bf16 a[88:91], a[216:219], v[16:19], a[88:91]\n"
"s_waitcnt vmcnt(19) lgkmcnt(0)\n"
"s_barrier\n"
"v_cvt_pk_bf16_f32 v21, v230, v231\n"
"v_mfma_f32_16x16x32_bf16 a[92:95], a[220:223], v[16:19], a[92:95]\n"
"v_exp_f32 v232, v232\n"
"v_mfma_f32_16x16x32_bf16 v[0:3], a[160:163], v[64:67], v[24:27]\n"
"ds_read_b128 a[128:131], v240 offset:0\n"
"buffer_store_dwordx4 v[216:219], v208, s[28:31], s54 offen sc1\n"
"v_mfma_f32_16x16x32_bf16 v[0:3], a[164:167], v[68:71], v[0:3]\n"
"s_add_u32 s54, s54, 1024\n"
"v_exp_f32 v233, v233\n"
"v_mfma_f32_16x16x32_bf16 v[0:3], a[168:171], v[72:75], v[0:3]\n"
"s_min_u32 s54, s54, s49\n"
"ds_read_b128 a[132:135], v240 offset:256\n"
"v_cvt_pk_bf16_f32 v22, v232, v233\n"
"v_mfma_f32_16x16x32_bf16 v[0:3], a[172:175], v[76:79], v[0:3]\n"
"v_exp_f32 v234, v234\n"
"v_mfma_f32_16x16x32_bf16 v[4:7], a[176:179], v[64:67], v[24:27]\n"
"ds_read_b128 a[136:139], v240 offset:512\n"
"v_mfma_f32_16x16x32_bf16 v[4:7], a[180:183], v[68:71], v[4:7]\n"
"v_exp_f32 v235, v235\n"
"v_mfma_f32_16x16x32_bf16 v[4:7], a[184:187], v[72:75], v[4:7]\n"
"ds_read_b128 a[140:143], v240 offset:768\n"
"v_cvt_pk_bf16_f32 v23, v234, v235\n"
"v_mfma_f32_16x16x32_bf16 v[4:7], a[188:191], v[76:79], v[4:7]\n"
"s_add_u32 m0, s86, 98304\n"
"v_exp_f32 v0, v0\n"
"v_mfma_f32_16x16x32_bf16 a[96:99], a[192:195], v[20:23], a[96:99]\n"
"buffer_load_dwordx4 v246, s[8:11], s84 offen offset:0 lds\n"
"v_mfma_f32_16x16x32_bf16 a[100:103], a[196:199], v[20:23], a[100:103]\n"
"v_exp_f32 v1, v1\n"
"v_mfma_f32_16x16x32_bf16 a[104:107], a[200:203], v[20:23], a[104:107]\n"
"ds_read_b128 a[144:147], v240 offset:4096\n"
"v_cvt_pk_bf16_f32 v16, v0, v1\n"
"v_mfma_f32_16x16x32_bf16 a[108:111], a[204:207], v[20:23], a[108:111]\n"
"v_exp_f32 v2, v2\n"
"v_mfma_f32_16x16x32_bf16 a[112:115], a[208:211], v[20:23], a[112:115]\n"
"ds_read_b128 a[148:151], v240 offset:4352\n"
"v_add_f32 v44, v44, v0\n"
"v_mfma_f32_16x16x32_bf16 a[116:119], a[212:215], v[20:23], a[116:119]\n"
"v_exp_f32 v3, v3\n"
"v_mfma_f32_16x16x32_bf16 a[120:123], a[216:219], v[20:23], a[120:123]\n"
"ds_read_b128 a[152:155], v240 offset:4608\n"
"v_cvt_pk_bf16_f32 v17, v2, v3\n"
"v_mfma_f32_16x16x32_bf16 a[124:127], a[220:223], v[20:23], a[124:127]\n"
"v_exp_f32 v4, v4\n"
"v_mfma_f32_16x16x32_bf16 v[56:59], v[40:43], v[20:23], v[56:59]\n"
"ds_read_b128 a[156:159], v240 offset:4864\n"
"v_add_f32 v44, v44, v1\n"
"v_mfma_f32_16x16x32_bf16 v[8:11], a[160:163], v[80:83], v[28:31]\n"
"v_exp_f32 v5, v5\n"
"v_mfma_f32_16x16x32_bf16 v[8:11], a[164:167], v[84:87], v[8:11]\n"
"ds_read_b64_tr_b16 a[192:193], v242 offset:16384\n"
"v_cvt_pk_bf16_f32 v18, v4, v5\n"
"v_mfma_f32_16x16x32_bf16 v[8:11], a[168:171], v[88:91], v[8:11]\n"
"v_exp_f32 v6, v6\n"
"v_mfma_f32_16x16x32_bf16 v[8:11], a[172:175], v[92:95], v[8:11]\n"
"ds_read_b64_tr_b16 a[194:195], v242 offset:20480\n"
"v_add_f32 v44, v44, v2\n"
"v_mfma_f32_16x16x32_bf16 v[12:15], a[176:179], v[80:83], v[28:31]\n"
"v_exp_f32 v7, v7\n"
"v_mfma_f32_16x16x32_bf16 v[12:15], a[180:183], v[84:87], v[12:15]\n"
"ds_read_b64_tr_b16 a[196:197], v243 offset:16384\n"
"v_cvt_pk_bf16_f32 v19, v6, v7\n"
"v_mfma_f32_16x16x32_bf16 v[12:15], a[184:187], v[88:91], v[12:15]\n"
"buffer_load_dwordx4 v246, s[8:11], s84 offen offset:1024 lds\n"
"v_mfma_f32_16x16x32_bf16 v[12:15], a[188:191], v[92:95], v[12:15]\n"
"s_waitcnt lgkmcnt(11)\n"
"v_exp_f32 v8, v8\n"
"v_mfma_f32_16x16x32_bf16 a[0:3], a[224:227], v[16:19], a[0:3]\n"
"ds_read_b64_tr_b16 a[198:199], v243 offset:20480\n"
"v_add_f32 v44, v44, v3\n"
"v_mfma_f32_16x16x32_bf16 a[4:7], a[228:231], v[16:19], a[4:7]\n"
"v_exp_f32 v9, v9\n"
"v_mfma_f32_16x16x32_bf16 a[8:11], a[232:235], v[16:19], a[8:11]\n"
"ds_read_b64_tr_b16 a[200:201], v242 offset:16640\n"
"v_cvt_pk_bf16_f32 v20, v8, v9\n"
"v_mfma_f32_16x16x32_bf16 a[12:15], a[236:239], v[16:19], a[12:15]\n"
"v_exp_f32 v10, v10\n"
"v_mfma_f32_16x16x32_bf16 a[16:19], a[240:243], v[16:19], a[16:19]\n"
"ds_read_b64_tr_b16 a[202:203], v242 offset:20736\n"
"v_add_f32 v44, v44, v4\n"
"v_mfma_f32_16x16x32_bf16 a[20:23], a[244:247], v[16:19], a[20:23]\n"
"v_exp_f32 v11, v11\n"
"v_mfma_f32_16x16x32_bf16 a[24:27], a[248:251], v[16:19], a[24:27]\n"
"ds_read_b64_tr_b16 a[204:205], v243 offset:16640\n"
"buffer_load_dword v250, v253, s[24:27], s47 offen\n"
"v_cvt_pk_bf16_f32 v21, v10, v11\n"
"v_mfma_f32_16x16x32_bf16 a[28:31], a[252:255], v[16:19], a[28:31]\n"
"v_exp_f32 v12, v12\n"
"v_mfma_f32_16x16x32_bf16 v[220:223], a[160:163], v[96:99], v[32:35]\n"
"ds_read_b64_tr_b16 a[206:207], v243 offset:20736\n"
"v_add_f32 v44, v44, v5\n"
"v_mfma_f32_16x16x32_bf16 v[220:223], a[164:167], v[100:103], v[220:223]\n"
"v_exp_f32 v13, v13\n"
"v_mfma_f32_16x16x32_bf16 v[220:223], a[168:171], v[104:107], v[220:223]\n"
"ds_read_b64_tr_b16 a[208:209], v242 offset:16896\n"
"v_cvt_pk_bf16_f32 v22, v12, v13\n"
"v_mfma_f32_16x16x32_bf16 v[220:223], a[172:175], v[108:111], v[220:223]\n"
"v_exp_f32 v14, v14\n"
"v_mfma_f32_16x16x32_bf16 v[224:227], a[176:179], v[96:99], v[32:35]\n"
"ds_read_b64_tr_b16 a[210:211], v242 offset:20992\n"
"v_add_f32 v44, v44, v6\n"
"v_mfma_f32_16x16x32_bf16 v[224:227], a[180:183], v[100:103], v[224:227]\n"
"v_exp_f32 v15, v15\n"
"v_mfma_f32_16x16x32_bf16 v[224:227], a[184:187], v[104:107], v[224:227]\n"
"ds_read_b64_tr_b16 a[212:213], v243 offset:16896\n"
"v_cvt_pk_bf16_f32 v23, v14, v15\n"
"v_mfma_f32_16x16x32_bf16 v[224:227], a[188:191], v[108:111], v[224:227]\n"
"v_exp_f32 v220, v220\n"
"v_mfma_f32_16x16x32_bf16 a[32:35], a[224:227], v[20:23], a[32:35]\n"
"buffer_load_dwordx4 v247, s[8:11], s84 offen offset:2048 lds\n"
"v_add_u32 v253, 294912, v253\n"
"v_mfma_f32_16x16x32_bf16 a[36:39], a[228:231], v[20:23], a[36:39]\n"
"v_exp_f32 v221, v221\n"
"v_mfma_f32_16x16x32_bf16 a[40:43], a[232:235], v[20:23], a[40:43]\n"
"ds_read_b64_tr_b16 a[214:215], v243 offset:20992\n"
"v_cvt_pk_bf16_f32 v16, v220, v221\n"
"v_mfma_f32_16x16x32_bf16 a[44:47], a[236:239], v[20:23], a[44:47]\n"
"v_exp_f32 v222, v222\n"
"v_mfma_f32_16x16x32_bf16 a[48:51], a[240:243], v[20:23], a[48:51]\n"
"ds_read_b64_tr_b16 a[216:217], v242 offset:17152\n"
"v_add_f32 v44, v44, v7\n"
"v_mfma_f32_16x16x32_bf16 a[52:55], a[244:247], v[20:23], a[52:55]\n"
"v_exp_f32 v223, v223\n"
"v_mfma_f32_16x16x32_bf16 a[56:59], a[248:251], v[20:23], a[56:59]\n"
"ds_read_b64_tr_b16 a[218:219], v242 offset:21248\n"
"v_cvt_pk_bf16_f32 v17, v222, v223\n"
"v_mfma_f32_16x16x32_bf16 a[60:63], a[252:255], v[20:23], a[60:63]\n"
"v_exp_f32 v224, v224\n"
"v_mfma_f32_16x16x32_bf16 v[48:51], v[40:43], v[20:23], v[48:51]\n"
"ds_read_b64_tr_b16 a[220:221], v243 offset:17152\n"
"v_add_f32 v52, v52, v220\n"
"v_mfma_f32_16x16x32_bf16 v[228:231], a[160:163], v[112:115], v[36:39]\n"
"v_exp_f32 v225, v225\n"
"v_mfma_f32_16x16x32_bf16 v[228:231], a[164:167], v[116:119], v[228:231]\n"
"ds_read_b64_tr_b16 a[222:223], v243 offset:21248\n"
"v_cvt_pk_bf16_f32 v18, v224, v225\n"
"v_mfma_f32_16x16x32_bf16 v[228:231], a[168:171], v[120:123], v[228:231]\n"
"v_exp_f32 v226, v226\n"
"v_mfma_f32_16x16x32_bf16 v[228:231], a[172:175], v[124:127], v[228:231]\n"
"v_add_f32 v52, v52, v221\n"
"v_add_f32 v52, v52, v222\n"
"v_mfma_f32_16x16x32_bf16 v[232:235], a[176:179], v[112:115], v[36:39]\n"
"v_exp_f32 v227, v227\n"
"v_mfma_f32_16x16x32_bf16 v[232:235], a[180:183], v[116:119], v[232:235]\n"
"v_cvt_pk_bf16_f32 v19, v226, v227\n"
"v_add_f32 v52, v52, v223\n"
"v_mfma_f32_16x16x32_bf16 v[232:235], a[184:187], v[120:123], v[232:235]\n"
"buffer_load_dwordx4 v247, s[8:11], s84 offen offset:3072 lds\n"
"v_mfma_f32_16x16x32_bf16 v[232:235], a[188:191], v[124:127], v[232:235]\n"
"v_exp_f32 v228, v228\n"
"v_mfma_f32_16x16x32_bf16 a[64:67], a[224:227], v[16:19], a[64:67]\n"
"v_add_f32 v52, v52, v224\n"
"v_add_f32 v52, v52, v225\n"
"v_mfma_f32_16x16x32_bf16 a[68:71], a[228:231], v[16:19], a[68:71]\n"
"v_exp_f32 v229, v229\n"
"v_mfma_f32_16x16x32_bf16 a[72:75], a[232:235], v[16:19], a[72:75]\n"
"v_cvt_pk_bf16_f32 v20, v228, v229\n"
"v_add_f32 v52, v52, v226\n"
"v_mfma_f32_16x16x32_bf16 a[76:79], a[236:239], v[16:19], a[76:79]\n"
"v_exp_f32 v230, v230\n"
"v_mfma_f32_16x16x32_bf16 a[80:83], a[240:243], v[16:19], a[80:83]\n"
"v_add_f32 v52, v52, v227\n"
"v_mfma_f32_16x16x32_bf16 a[84:87], a[244:247], v[16:19], a[84:87]\n"
"v_exp_f32 v231, v231\n"
"v_mfma_f32_16x16x32_bf16 a[88:91], a[248:251], v[16:19], a[88:91]\n"
"s_waitcnt lgkmcnt(8)\n"
"v_cvt_pk_bf16_f32 v21, v230, v231\n"
"v_mfma_f32_16x16x32_bf16 a[92:95], a[252:255], v[16:19], a[92:95]\n"
"v_exp_f32 v232, v232\n"
"v_mfma_f32_16x16x32_bf16 v[0:3], a[128:131], v[64:67], v[24:27]\n"
"ds_read_b128 a[160:163], v240 offset:8192\n"
"v_mfma_f32_16x16x32_bf16 v[0:3], a[132:135], v[68:71], v[0:3]\n"
"v_exp_f32 v233, v233\n"
"v_mfma_f32_16x16x32_bf16 v[0:3], a[136:139], v[72:75], v[0:3]\n"
"ds_read_b128 a[164:167], v240 offset:8448\n"
"v_cvt_pk_bf16_f32 v22, v232, v233\n"
"v_mfma_f32_16x16x32_bf16 v[0:3], a[140:143], v[76:79], v[0:3]\n"
"v_exp_f32 v234, v234\n"
"v_mfma_f32_16x16x32_bf16 v[4:7], a[144:147], v[64:67], v[24:27]\n"
"ds_read_b128 a[168:171], v240 offset:8704\n"
"v_mfma_f32_16x16x32_bf16 v[4:7], a[148:151], v[68:71], v[4:7]\n"
"v_exp_f32 v235, v235\n"
"v_mfma_f32_16x16x32_bf16 v[4:7], a[152:155], v[72:75], v[4:7]\n"
"ds_read_b128 a[172:175], v240 offset:8960\n"
"v_cvt_pk_bf16_f32 v23, v234, v235\n"
"v_mfma_f32_16x16x32_bf16 v[4:7], a[156:159], v[76:79], v[4:7]\n"
"s_add_u32 m0, s86, 114688\n"
"v_exp_f32 v0, v0\n"
"v_mfma_f32_16x16x32_bf16 a[96:99], a[224:227], v[20:23], a[96:99]\n"
"buffer_load_dwordx4 v246, s[12:15], s84 offen offset:0 lds\n"
"v_mfma_f32_16x16x32_bf16 a[100:103], a[228:231], v[20:23], a[100:103]\n"
"v_exp_f32 v1, v1\n"
"v_mfma_f32_16x16x32_bf16 a[104:107], a[232:235], v[20:23], a[104:107]\n"
"ds_read_b128 a[176:179], v240 offset:12288\n"
"v_cvt_pk_bf16_f32 v16, v0, v1\n"
"v_mfma_f32_16x16x32_bf16 a[108:111], a[236:239], v[20:23], a[108:111]\n"
"v_exp_f32 v2, v2\n"
"v_mfma_f32_16x16x32_bf16 a[112:115], a[240:243], v[20:23], a[112:115]\n"
"ds_read_b128 a[180:183], v240 offset:12544\n"
"v_add_f32 v44, v44, v0\n"
"v_mfma_f32_16x16x32_bf16 a[116:119], a[244:247], v[20:23], a[116:119]\n"
"v_exp_f32 v3, v3\n"
"v_mfma_f32_16x16x32_bf16 a[120:123], a[248:251], v[20:23], a[120:123]\n"
"ds_read_b128 a[184:187], v240 offset:12800\n"
"v_cvt_pk_bf16_f32 v17, v2, v3\n"
"v_mfma_f32_16x16x32_bf16 a[124:127], a[252:255], v[20:23], a[124:127]\n"
"v_exp_f32 v4, v4\n"
"v_mfma_f32_16x16x32_bf16 v[56:59], v[40:43], v[20:23], v[56:59]\n"
"ds_read_b128 a[188:191], v240 offset:13056\n"
"v_add_f32 v44, v44, v1\n"
"v_mfma_f32_16x16x32_bf16 v[8:11], a[128:131], v[80:83], v[28:31]\n"
"v_exp_f32 v5, v5\n"
"v_mfma_f32_16x16x32_bf16 v[8:11], a[132:135], v[84:87], v[8:11]\n"
"ds_read_b64_tr_b16 a[224:225], v242 offset:24576\n"
"v_cvt_pk_bf16_f32 v18, v4, v5\n"
"v_mfma_f32_16x16x32_bf16 v[8:11], a[136:139], v[88:91], v[8:11]\n"
"v_exp_f32 v6, v6\n"
"v_mfma_f32_16x16x32_bf16 v[8:11], a[140:143], v[92:95], v[8:11]\n"
"ds_read_b64_tr_b16 a[226:227], v242 offset:28672\n"
"v_add_f32 v44, v44, v2\n"
"v_mfma_f32_16x16x32_bf16 v[12:15], a[144:147], v[80:83], v[28:31]\n"
"v_exp_f32 v7, v7\n"
"v_mfma_f32_16x16x32_bf16 v[12:15], a[148:151], v[84:87], v[12:15]\n"
"ds_read_b64_tr_b16 a[228:229], v243 offset:24576\n"
"v_cvt_pk_bf16_f32 v19, v6, v7\n"
"v_mfma_f32_16x16x32_bf16 v[12:15], a[152:155], v[88:91], v[12:15]\n"
"buffer_load_dwordx4 v247, s[12:15], s84 offen offset:1024 lds\n"
"v_mfma_f32_16x16x32_bf16 v[12:15], a[156:159], v[92:95], v[12:15]\n"
"s_waitcnt lgkmcnt(11)\n"
"v_exp_f32 v8, v8\n"
"v_mfma_f32_16x16x32_bf16 a[0:3], a[192:195], v[16:19], a[0:3]\n"
"ds_read_b64_tr_b16 a[230:231], v243 offset:28672\n"
"v_add_f32 v44, v44, v3\n"
"v_mfma_f32_16x16x32_bf16 a[4:7], a[196:199], v[16:19], a[4:7]\n"
"v_exp_f32 v9, v9\n"
"v_mfma_f32_16x16x32_bf16 a[8:11], a[200:203], v[16:19], a[8:11]\n"
"ds_read_b64_tr_b16 a[232:233], v242 offset:24832\n"
"v_cvt_pk_bf16_f32 v20, v8, v9\n"
"v_mfma_f32_16x16x32_bf16 a[12:15], a[204:207], v[16:19], a[12:15]\n"
"v_exp_f32 v10, v10\n"
"v_mfma_f32_16x16x32_bf16 a[16:19], a[208:211], v[16:19], a[16:19]\n"
"ds_read_b64_tr_b16 a[234:235], v242 offset:28928\n"
"v_add_f32 v44, v44, v4\n"
"v_mfma_f32_16x16x32_bf16 a[20:23], a[212:215], v[16:19], a[20:23]\n"
"v_exp_f32 v11, v11\n"
"v_mfma_f32_16x16x32_bf16 a[24:27], a[216:219], v[16:19], a[24:27]\n"
"ds_read_b64_tr_b16 a[236:237], v243 offset:24832\n"
"v_cvt_pk_bf16_f32 v21, v10, v11\n"
"v_mfma_f32_16x16x32_bf16 a[28:31], a[220:223], v[16:19], a[28:31]\n"
"v_exp_f32 v12, v12\n"
"v_mfma_f32_16x16x32_bf16 v[220:223], a[128:131], v[96:99], v[32:35]\n"
"ds_read_b64_tr_b16 a[238:239], v243 offset:28928\n"
"v_add_f32 v44, v44, v5\n"
"v_mfma_f32_16x16x32_bf16 v[220:223], a[132:135], v[100:103], v[220:223]\n"
"v_exp_f32 v13, v13\n"
"v_mfma_f32_16x16x32_bf16 v[220:223], a[136:139], v[104:107], v[220:223]\n"
"ds_read_b64_tr_b16 a[240:241], v242 offset:25088\n"
"v_cvt_pk_bf16_f32 v22, v12, v13\n"
"v_mfma_f32_16x16x32_bf16 v[220:223], a[140:143], v[108:111], v[220:223]\n"
"v_exp_f32 v14, v14\n"
"v_mfma_f32_16x16x32_bf16 v[224:227], a[144:147], v[96:99], v[32:35]\n"
"ds_read_b64_tr_b16 a[242:243], v242 offset:29184\n"
"v_add_f32 v44, v44, v6\n"
"v_mfma_f32_16x16x32_bf16 v[224:227], a[148:151], v[100:103], v[224:227]\n"
"v_exp_f32 v15, v15\n"
"v_mfma_f32_16x16x32_bf16 v[224:227], a[152:155], v[104:107], v[224:227]\n"
"ds_read_b64_tr_b16 a[244:245], v243 offset:25088\n"
"v_cvt_pk_bf16_f32 v23, v14, v15\n"
"v_mfma_f32_16x16x32_bf16 v[224:227], a[156:159], v[108:111], v[224:227]\n"
"v_exp_f32 v220, v220\n"
"v_mfma_f32_16x16x32_bf16 a[32:35], a[192:195], v[20:23], a[32:35]\n"
"buffer_load_dwordx4 v246, s[12:15], s84 offen offset:2048 lds\n"
"v_mfma_f32_16x16x32_bf16 a[36:39], a[196:199], v[20:23], a[36:39]\n"
"v_exp_f32 v221, v221\n"
"v_mfma_f32_16x16x32_bf16 a[40:43], a[200:203], v[20:23], a[40:43]\n"
"ds_read_b64_tr_b16 a[246:247], v243 offset:29184\n"
"v_cvt_pk_bf16_f32 v16, v220, v221\n"
"v_mfma_f32_16x16x32_bf16 a[44:47], a[204:207], v[20:23], a[44:47]\n"
"v_exp_f32 v222, v222\n"
"v_mfma_f32_16x16x32_bf16 a[48:51], a[208:211], v[20:23], a[48:51]\n"
"ds_read_b64_tr_b16 a[248:249], v242 offset:25344\n"
"v_add_f32 v44, v44, v7\n"
"v_mfma_f32_16x16x32_bf16 a[52:55], a[212:215], v[20:23], a[52:55]\n"
"v_exp_f32 v223, v223\n"
"v_mfma_f32_16x16x32_bf16 a[56:59], a[216:219], v[20:23], a[56:59]\n"
"ds_read_b64_tr_b16 a[250:251], v242 offset:29440\n"
"v_cvt_pk_bf16_f32 v17, v222, v223\n"
"v_mfma_f32_16x16x32_bf16 a[60:63], a[220:223], v[20:23], a[60:63]\n"
"v_exp_f32 v224, v224\n"
"v_mfma_f32_16x16x32_bf16 v[48:51], v[40:43], v[20:23], v[48:51]\n"
"ds_read_b64_tr_b16 a[252:253], v243 offset:25344\n"
"v_add_f32 v52, v52, v220\n"
"v_mfma_f32_16x16x32_bf16 v[228:231], a[128:131], v[112:115], v[36:39]\n"
"v_exp_f32 v225, v225\n"
"v_mfma_f32_16x16x32_bf16 v[228:231], a[132:135], v[116:119], v[228:231]\n"
"ds_read_b64_tr_b16 a[254:255], v243 offset:29440\n"
"v_cvt_pk_bf16_f32 v18, v224, v225\n"
"v_mfma_f32_16x16x32_bf16 v[228:231], a[136:139], v[120:123], v[228:231]\n"
"v_exp_f32 v226, v226\n"
"v_mfma_f32_16x16x32_bf16 v[228:231], a[140:143], v[124:127], v[228:231]\n"
"v_add_f32 v52, v52, v221\n"
"v_add_f32 v52, v52, v222\n"
"v_mfma_f32_16x16x32_bf16 v[232:235], a[144:147], v[112:115], v[36:39]\n"
"v_exp_f32 v227, v227\n"
"v_mfma_f32_16x16x32_bf16 v[232:235], a[148:151], v[116:119], v[232:235]\n"
"v_cvt_pk_bf16_f32 v19, v226, v227\n"
"v_add_f32 v52, v52, v223\n"
"v_mfma_f32_16x16x32_bf16 v[232:235], a[152:155], v[120:123], v[232:235]\n"
"buffer_load_dwordx4 v247, s[12:15], s84 offen offset:3072 lds\n"
"v_mfma_f32_16x16x32_bf16 v[232:235], a[156:159], v[124:127], v[232:235]\n"
"s_add_u32 s84, s84, 16384\n"
"v_exp_f32 v228, v228\n"
"v_mfma_f32_16x16x32_bf16 a[64:67], a[192:195], v[16:19], a[64:67]\n"
"v_add_f32 v52, v52, v224\n"
"v_add_f32 v52, v52, v225\n"
"v_mfma_f32_16x16x32_bf16 a[68:71], a[196:199], v[16:19], a[68:71]\n"
"v_exp_f32 v229, v229\n"
"v_mfma_f32_16x16x32_bf16 a[72:75], a[200:203], v[16:19], a[72:75]\n"
"v_cvt_pk_bf16_f32 v20, v228, v229\n"
"v_add_f32 v52, v52, v226\n"
"v_mfma_f32_16x16x32_bf16 a[76:79], a[204:207], v[16:19], a[76:79]\n"
"s_sub_u32 s87, s87, 1\n"
"v_exp_f32 v230, v230\n"
"v_mfma_f32_16x16x32_bf16 a[80:83], a[208:211], v[16:19], a[80:83]\n"
"s_cmp_lg_u32 s87, 0\n"
"v_add_f32 v52, v52, v227\n"
"v_mfma_f32_16x16x32_bf16 a[84:87], a[212:215], v[16:19], a[84:87]\n"
"v_exp_f32 v231, v231\n"
"v_mfma_f32_16x16x32_bf16 a[88:91], a[216:219], v[16:19], a[88:91]\n"
"s_waitcnt vmcnt(19) lgkmcnt(0)\n"
"s_barrier\n"
"v_cvt_pk_bf16_f32 v21, v230, v231\n"
"v_mfma_f32_16x16x32_bf16 a[92:95], a[220:223], v[16:19], a[92:95]\n"
"v_exp_f32 v232, v232\n"
"s_cbranch_scc1 AT_loop\n"
"s_cmp_lg_u32 s56, 0\n"
"s_cbranch_scc1 AT_h3\n"
"s_mov_b32 s61, 0x80000\n"
"AT_h2:\n"
"v_xor_b32 v210, s85, v210\n"
"v_xor_b32 v211, s85, v211\n"
"v_xor_b32 v212, s85, v212\n"
"v_xor_b32 v213, s85, v213\n"
"v_or3_b32 v210, v210, v211, v212\n"
"v_or_b32 v210, v210, v213\n"
"v_cmp_ne_u32 vcc, 0, v210\n"
"s_sub_u32 s61, s61, 1\n"
"s_cbranch_vccz AT_h4\n"
"s_cmp_eq_u32 s61, 0\n"
"s_cbranch_scc1 AT_h4\n"
"s_sleep 2\n"
"buffer_load_dword v210, v214, s[28:31], s57 offen offset:4 sc1\n"
"buffer_load_dword v211, v214, s[28:31], s58 offen offset:4 sc1\n"
"buffer_load_dword v212, v214, s[28:31], s59 offen offset:4 sc1\n"
"buffer_load_dword v213, v214, s[28:31], s60 offen offset:4 sc1\n"
"s_waitcnt vmcnt(0)\n"
"s_branch AT_h2\n"
"AT_h4:\n"
"v_mov_b32 v210, s85\n"
"v_mov_b32 v211, 65536\n"
"s_mov_b64 exec, 1\n"
"buffer_store_dword v210, v211, s[28:31], 0 offen sc1\n"
"s_mov_b64 exec, -1\n"
"AT_h3:\n"
"v_mfma_f32_16x16x32_bf16 v[0:3], a[160:163], v[64:67], v[24:27]\n"
"ds_read_b128 a[128:131], v240 offset:32768\n"
"buffer_load_dword v250, v255, s[32:35], 0 offen\n"
"v_mfma_f32_16x16x32_bf16 v[0:3], a[164:167], v[68:71], v[0:3]\n"
"v_exp_f32 v233, v233\n"
"v_mfma_f32_16x16x32_bf16 v[0:3], a[168:171], v[72:75], v[0:3]\n"
"ds_read_b128 a[132:135], v240 offset:33024\n"
"v_cvt_pk_bf16_f32 v22, v232, v233\n"
"v_mfma_f32_16x16x32_bf16 v[0:3], a[172:175], v[76:79], v[0:3]\n"
"v_exp_f32 v234, v234\n"
"v_mfma_f32_16x16x32_bf16 v[4:7], a[176:179], v[64:67], v[24:27]\n"
"ds_read_b128 a[136:139], v240 offset:33280\n"
"v_lshlrev_b32 v216, 6, v248\n"
"v_mfma_f32_16x16x32_bf16 v[4:7], a[180:183], v[68:71], v[4:7]\n"
"s_and_b32 s48, s42, 7\n"
"v_exp_f32 v235, v235\n"
"v_mfma_f32_16x16x32_bf16 v[4:7], a[184:187], v[72:75], v[4:7]\n"
"s_mul_i32 s48, s48, 576\n"
"ds_read_b128 a[140:143], v240 offset:33536\n"
"v_cvt_pk_bf16_f32 v23, v234, v235\n"
"v_mfma_f32_16x16x32_bf16 v[4:7], a[188:191], v[76:79], v[4:7]\n"
"s_mov_b32 m0, s86\n"
"v_exp_f32 v0, v0\n"
"v_mfma_f32_16x16x32_bf16 a[96:99], a[192:195], v[20:23], a[96:99]\n"
"s_lshr_b32 s62, s42, 3\n"
"buffer_load_dwordx4 v246, s[8:11], s84 offen offset:0 lds\n"
"v_mfma_f32_16x16x32_bf16 a[100:103], a[196:199], v[20:23], a[100:103]\n"
"s_mul_i32 s62, s62, 24\n"
"v_exp_f32 v1, v1\n"
"v_mfma_f32_16x16x32_bf16 a[104:107], a[200:203], v[20:23], a[104:107]\n"
"s_add_u32 s48, s48, s62\n"
"ds_read_b128 a[144:147], v240 offset:36864\n"
"v_cvt_pk_bf16_f32 v16, v0, v1\n"
"v_mfma_f32_16x16x32_bf16 a[108:111], a[204:207], v[20:23], a[108:111]\n"
"s_cmp_lt_u32 s42, 192\n"
"s_cselect_b32 s74, s74, 0\n"
"s_cselect_b32 s66, s66, 0\n"
"s_cselect_b32 s70, s70, 0\n"
"v_exp_f32 v2, v2\n"
"v_mfma_f32_16x16x32_bf16 a[112:115], a[208:211], v[20:23], a[112:115]\n"
"s_mul_i32 s50, s48, 1152\n"
"ds_read_b128 a[148:151], v240 offset:37120\n"
"v_add_f32 v44, v44, v0\n"
"v_mfma_f32_16x16x32_bf16 a[116:119], a[212:215], v[20:23], a[116:119]\n"
"s_lshl_b32 s62, s41, 13\n"
"v_exp_f32 v3, v3\n"
"v_mfma_f32_16x16x32_bf16 a[120:123], a[216:219], v[20:23], a[120:123]\n"
"s_add_u32 s46, s50, s62\n"
"ds_read_b128 a[152:155], v240 offset:37376\n"
"v_cvt_pk_bf16_f32 v17, v2, v3\n"
"v_mfma_f32_16x16x32_bf16 a[124:127], a[220:223], v[20:23], a[124:127]\n"
"s_add_u32 s47, s46, 4096\n"
"v_exp_f32 v4, v4\n"
"v_mfma_f32_16x16x32_bf16 v[56:59], v[40:43], v[20:23], v[56:59]\n"
"ds_read_b128 a[156:159], v240 offset:37632\n"
"v_add_f32 v44, v44, v1\n"
"v_mfma_f32_16x16x32_bf16 v[8:11], a[160:163], v[80:83], v[28:31]\n"
"v_exp_f32 v5, v5\n"
"v_mfma_f32_16x16x32_bf16 v[8:11], a[164:167], v[84:87], v[8:11]\n"
"ds_read_b64_tr_b16 a[192:193], v242 offset:49152\n"
"v_cvt_pk_bf16_f32 v18, v4, v5\n"
"v_mfma_f32_16x16x32_bf16 v[8:11], a[168:171], v[88:91], v[8:11]\n"
"v_exp_f32 v6, v6\n"
"v_mfma_f32_16x16x32_bf16 v[8:11], a[172:175], v[92:95], v[8:11]\n"
"ds_read_b64_tr_b16 a[194:195], v242 offset:53248\n"
"v_add_f32 v44, v44, v2\n"
"v_mfma_f32_16x16x32_bf16 v[12:15], a[176:179], v[80:83], v[28:31]\n"
"v_exp_f32 v7, v7\n"
"v_mfma_f32_16x16x32_bf16 v[12:15], a[180:183], v[84:87], v[12:15]\n"
"ds_read_b64_tr_b16 a[196:197], v243 offset:49152\n"
"v_cvt_pk_bf16_f32 v19, v6, v7\n"
"v_mfma_f32_16x16x32_bf16 v[12:15], a[184:187], v[88:91], v[12:15]\n"
"buffer_load_dwordx4 v246, s[8:11], s84 offen offset:1024 lds\n"
"v_mfma_f32_16x16x32_bf16 v[12:15], a[188:191], v[92:95], v[12:15]\n"
"s_waitcnt lgkmcnt(11)\n"
"v_exp_f32 v8, v8\n"
"v_mfma_f32_16x16x32_bf16 a[0:3], a[224:227], v[16:19], a[0:3]\n"
"ds_read_b64_tr_b16 a[198:199], v243 offset:53248\n"
"buffer_load_dword v250, v216, s[72:75], s46 offen\n"
"v_add_f32 v44, v44, v3\n"
"v_mfma_f32_16x16x32_bf16 a[4:7], a[228:231], v[16:19], a[4:7]\n"
"v_exp_f32 v9, v9\n"
"v_mfma_f32_16x16x32_bf16 a[8:11], a[232:235], v[16:19], a[8:11]\n"
"ds_read_b64_tr_b16 a[200:201], v242 offset:49408\n"
"v_cvt_pk_bf16_f32 v20, v8, v9\n"
"v_mfma_f32_16x16x32_bf16 a[12:15], a[236:239], v[16:19], a[12:15]\n"
"v_exp_f32 v10, v10\n"
"v_mfma_f32_16x16x32_bf16 a[16:19], a[240:243], v[16:19], a[16:19]\n"
"ds_read_b64_tr_b16 a[202:203], v242 offset:53504\n"
"v_add_f32 v44, v44, v4\n"
"v_mfma_f32_16x16x32_bf16 a[20:23], a[244:247], v[16:19], a[20:23]\n"
"v_exp_f32 v11, v11\n"
"v_mfma_f32_16x16x32_bf16 a[24:27], a[248:251], v[16:19], a[24:27]\n"
"ds_read_b64_tr_b16 a[204:205], v243 offset:49408\n"
"v_cvt_pk_bf16_f32 v21, v10, v11\n"
"v_mfma_f32_16x16x32_bf16 a[28:31], a[252:255], v[16:19], a[28:31]\n"
"v_exp_f32 v12, v12\n"
"v_mfma_f32_16x16x32_bf16 v[220:223], a[160:163], v[96:99], v[32:35]\n"
"ds_read_b64_tr_b16 a[206:207], v243 offset:53504\n"
"v_add_f32 v44, v44, v5\n"
"v_mfma_f32_16x16x32_bf16 v[220:223], a[164:167], v[100:103], v[220:223]\n"
"v_exp_f32 v13, v13\n"
"v_mfma_f32_16x16x32_bf16 v[220:223], a[168:171], v[104:107], v[220:223]\n"
"ds_read_b64_tr_b16 a[208:209], v242 offset:49664\n"
"v_cvt_pk_bf16_f32 v22, v12, v13\n"
"v_mfma_f32_16x16x32_bf16 v[220:223], a[172:175], v[108:111], v[220:223]\n"
"v_exp_f32 v14, v14\n"
"v_mfma_f32_16x16x32_bf16 v[224:227], a[176:179], v[96:99], v[32:35]\n"
"ds_read_b64_tr_b16 a[210:211], v242 offset:53760\n"
"v_add_f32 v44, v44, v6\n"
"v_mfma_f32_16x16x32_bf16 v[224:227], a[180:183], v[100:103], v[224:227]\n"
"v_exp_f32 v15, v15\n"
"v_mfma_f32_16x16x32_bf16 v[224:227], a[184:187], v[104:107], v[224:227]\n"
"ds_read_b64_tr_b16 a[212:213], v243 offset:49664\n"
"v_cvt_pk_bf16_f32 v23, v14, v15\n"
"v_mfma_f32_16x16x32_bf16 v[224:227], a[188:191], v[108:111], v[224:227]\n"
"v_exp_f32 v220, v220\n"
"v_mfma_f32_16x16x32_bf16 a[32:35], a[224:227], v[20:23], a[32:35]\n"
"buffer_load_dwordx4 v247, s[8:11], s84 offen offset:2048 lds\n"
"v_mfma_f32_16x16x32_bf16 a[36:39], a[228:231], v[20:23], a[36:39]\n"
"v_exp_f32 v221, v221\n"
"v_mfma_f32_16x16x32_bf16 a[40:43], a[232:235], v[20:23], a[40:43]\n"
"ds_read_b64_tr_b16 a[214:215], v243 offset:53760\n"
"v_cvt_pk_bf16_f32 v16, v220, v221\n"
"v_mfma_f32_16x16x32_bf16 a[44:47], a[236:239], v[20:23], a[44:47]\n"
"v_exp_f32 v222, v222\n"
"v_mfma_f32_16x16x32_bf16 a[48:51], a[240:243], v[20:23], a[48:51]\n"
"ds_read_b64_tr_b16 a[216:217], v242 offset:49920\n"
"v_add_f32 v44, v44, v7\n"
"v_mfma_f32_16x16x32_bf16 a[52:55], a[244:247], v[20:23], a[52:55]\n"
"v_exp_f32 v223, v223\n"
"v_mfma_f32_16x16x32_bf16 a[56:59], a[248:251], v[20:23], a[56:59]\n"
"ds_read_b64_tr_b16 a[218:219], v242 offset:54016\n"
"v_cvt_pk_bf16_f32 v17, v222, v223\n"
"v_mfma_f32_16x16x32_bf16 a[60:63], a[252:255], v[20:23], a[60:63]\n"
"v_exp_f32 v224, v224\n"
"v_mfma_f32_16x16x32_bf16 v[48:51], v[40:43], v[20:23], v[48:51]\n"
"ds_read_b64_tr_b16 a[220:221], v243 offset:49920\n"
"buffer_load_dword v250, v216, s[72:75], s47 offen\n"
"v_add_f32 v52, v52, v220\n"
"v_mfma_f32_16x16x32_bf16 v[228:231], a[160:163], v[112:115], v[36:39]\n"
"v_exp_f32 v225, v225\n"
"v_mfma_f32_16x16x32_bf16 v[228:231], a[164:167], v[116:119], v[228:231]\n"
"ds_read_b64_tr_b16 a[222:223], v243 offset:54016\n"
"v_cvt_pk_bf16_f32 v18, v224, v225\n"
"v_mfma_f32_16x16x32_bf16 v[228:231], a[168:171], v[120:123], v[228:231]\n"
"v_exp_f32 v226, v226\n"
"v_mfma_f32_16x16x32_bf16 v[228:231], a[172:175], v[124:127], v[228:231]\n"
"v_add_f32 v52, v52, v221\n"
"v_add_f32 v52, v52, v222\n"
"v_mfma_f32_16x16x32_bf16 v[232:235], a[176:179], v[112:115], v[36:39]\n"
"v_exp_f32 v227, v227\n"
"v_mfma_f32_16x16x32_bf16 v[232:235], a[180:183], v[116:119], v[232:235]\n"
"v_cvt_pk_bf16_f32 v19, v226, v227\n"
"v_add_f32 v52, v52, v223\n"
"v_mfma_f32_16x16x32_bf16 v[232:235], a[184:187], v[120:123], v[232:235]\n"
"buffer_load_dwordx4 v247, s[8:11], s84 offen offset:3072 lds\n"
"v_lshrrev_b32 v251, 2, v248\n"
"v_mfma_f32_16x16x32_bf16 v[232:235], a[188:191], v[124:127], v[232:235]\n"
"v_exp_f32 v228, v228\n"
"v_mfma_f32_16x16x32_bf16 a[64:67], a[224:227], v[16:19], a[64:67]\n"
"v_add_f32 v52, v52, v224\n"
"v_add_f32 v52, v52, v225\n"
"v_mfma_f32_16x16x32_bf16 a[68:71], a[228:231], v[16:19], a[68:71]\n"
"v_exp_f32 v229, v229\n"
"v_mfma_f32_16x16x32_bf16 a[72:75], a[232:235], v[16:19], a[72:75]\n"
"v_cvt_pk_bf16_f32 v20, v228, v229\n"
"v_add_f32 v52, v52, v226\n"
"v_mfma_f32_16x16x32_bf16 a[76:79], a[236:239], v[16:19], a[76:79]\n"
"v_exp_f32 v230, v230\n"
"v_mfma_f32_16x16x32_bf16 a[80:83], a[240:243], v[16:19], a[80:83]\n"
"v_add_f32 v52, v52, v227\n"
"v_mul_u32_u24 v251, 9216, v251\n"
"v_mfma_f32_16x16x32_bf16 a[84:87], a[244:247], v[16:19], a[84:87]\n"
"v_exp_f32 v231, v231\n"
"v_mfma_f32_16x16x32_bf16 a[88:91], a[248:251], v[16:19], a[88:91]\n"
"s_waitcnt lgkmcnt(8)\n"
"v_cvt_pk_bf16_f32 v21, v230, v231\n"
"v_and_b32 v217, 3, v248\n"
"v_mfma_f32_16x16x32_bf16 a[92:95], a[252:255], v[16:19], a[92:95]\n"
"v_exp_f32 v232, v232\n"
"v_mfma_f32_16x16x32_bf16 v[0:3], a[128:131], v[64:67], v[24:27]\n"
"ds_read_b128 a[160:163], v240 offset:40960\n"
"v_add_u32 v255, 1, v217\n"
"v_mfma_f32_16x16x32_bf16 v[0:3], a[132:135], v[68:71], v[0:3]\n"
"v_exp_f32 v233, v233\n"
"v_mfma_f32_16x16x32_bf16 v[0:3], a[136:139], v[72:75], v[0:3]\n"
"ds_read_b128 a[164:167], v240 offset:41216\n"
"v_cvt_pk_bf16_f32 v22, v232, v233\n"
"v_mfma_f32_16x16x32_bf16 v[0:3], a[140:143], v[76:79], v[0:3]\n"
"v_exp_f32 v234, v234\n"
"v_mfma_f32_16x16x32_bf16 v[4:7], a[144:147], v[64:67], v[24:27]\n"
"ds_read_b128 a[168:171], v240 offset:41472\n"
"v_and_b32 v255, 4, v255\n"
"v_mfma_f32_16x16x32_bf16 v[4:7], a[148:151], v[68:71], v[4:7]\n"
"v_exp_f32 v235, v235\n"
"v_mfma_f32_16x16x32_bf16 v[4:7], a[152:155], v[72:75], v[4:7]\n"
"ds_read_b128 a[172:175], v240 offset:41728\n"
"v_cvt_pk_bf16_f32 v23, v234, v235\n"
"v_mfma_f32_16x16x32_bf16 v[4:7], a[156:159], v[76:79], v[4:7]\n"
"s_add_u32 m0, s86, 16384\n"
"v_exp_f32 v0, v0\n"
"v_mfma_f32_16x16x32_bf16 a[96:99], a[224:227], v[20:23], a[96:99]\n"
"buffer_load_dwordx4 v246, s[12:15], s84 offen offset:0 lds\n"
"v_lshlrev_b32 v255, 26, v255\n"
"v_mfma_f32_16x16x32_bf16 a[100:103], a[228:231], v[20:23], a[100:103]\n"
"v_exp_f32 v1, v1\n"
"v_mfma_f32_16x16x32_bf16 a[104:107], a[232:235], v[20:23], a[104:107]\n"
"ds_read_b128 a[176:179], v240 offset:45056\n"
"v_cvt_pk_bf16_f32 v16, v0, v1\n"
"v_mfma_f32_16x16x32_bf16 a[108:111], a[236:239], v[20:23], a[108:111]\n"
"v_exp_f32 v2, v2\n"
"v_mfma_f32_16x16x32_bf16 a[112:115], a[240:243], v[20:23], a[112:115]\n"
"ds_read_b128 a[180:183], v240 offset:45312\n"
"v_add_f32 v44, v44, v0\n"
"v_mfma_f32_16x16x32_bf16 a[116:119], a[244:247], v[20:23], a[116:119]\n"
"v_exp_f32 v3, v3\n"
"v_mfma_f32_16x16x32_bf16 a[120:123], a[248:251], v[20:23], a[120:123]\n"
"ds_read_b128 a[184:187], v240 offset:45568\n"
"v_cvt_pk_bf16_f32 v17, v2, v3\n"
"v_mfma_f32_16x16x32_bf16 a[124:127], a[252:255], v[20:23], a[124:127]\n"
"v_exp_f32 v4, v4\n"
"v_mfma_f32_16x16x32_bf16 v[56:59], v[40:43], v[20:23], v[56:59]\n"
"ds_read_b128 a[188:191], v240 offset:45824\n"
"v_add_f32 v44, v44, v1\n"
"v_mfma_f32_16x16x32_bf16 v[8:11], a[128:131], v[80:83], v[28:31]\n"
"v_exp_f32 v5, v5\n"
"v_mfma_f32_16x16x32_bf16 v[8:11], a[132:135], v[84:87], v[8:11]\n"
"ds_read_b64_tr_b16 a[224:225], v242 offset:57344\n"
"v_cvt_pk_bf16_f32 v18, v4, v5\n"
"v_mfma_f32_16x16x32_bf16 v[8:11], a[136:139], v[88:91], v[8:11]\n"
"v_exp_f32 v6, v6\n"
"v_mfma_f32_16x16x32_bf16 v[8:11], a[140:143], v[92:95], v[8:11]\n"
"ds_read_b64_tr_b16 a[226:227], v242 offset:61440\n"
"v_add_f32 v44, v44, v2\n"
"v_mfma_f32_16x16x32_bf16 v[12:15], a[144:147], v[80:83], v[28:31]\n"
"v_exp_f32 v7, v7\n"
"v_mfma_f32_16x16x32_bf16 v[12:15], a[148:151], v[84:87], v[12:15]\n"
"ds_read_b64_tr_b16 a[228:229], v243 offset:57344\n"
"v_cvt_pk_bf16_f32 v19, v6, v7\n"
"v_mfma_f32_16x16x32_bf16 v[12:15], a[152:155], v[88:91], v[12:15]\n"
"buffer_load_dwordx4 v247, s[12:15], s84 offen offset:1024 lds\n"
"v_lshl_add_u32 v217, v217, 4, v255\n"
"v_mfma_f32_16x16x32_bf16 v[12:15], a[156:159], v[92:95], v[12:15]\n"
"s_waitcnt lgkmcnt(11)\n"
"v_exp_f32 v8, v8\n"
"v_mfma_f32_16x16x32_bf16 a[0:3], a[192:195], v[16:19], a[0:3]\n"
"ds_read_b64_tr_b16 a[230:231], v243 offset:61440\n"
"v_add_f32 v44, v44, v3\n"
"v_mfma_f32_16x16x32_bf16 a[4:7], a[196:199], v[16:19], a[4:7]\n"
"v_exp_f32 v9, v9\n"
"v_mfma_f32_16x16x32_bf16 a[8:11], a[200:203], v[16:19], a[8:11]\n"
"ds_read_b64_tr_b16 a[232:233], v242 offset:57600\n"
"v_cvt_pk_bf16_f32 v20, v8, v9\n"
"v_mfma_f32_16x16x32_bf16 a[12:15], a[204:207], v[16:19], a[12:15]\n"
"v_exp_f32 v10, v10\n"
"v_mfma_f32_16x16x32_bf16 a[16:19], a[208:211], v[16:19], a[16:19]\n"
"ds_read_b64_tr_b16 a[234:235], v242 offset:61696\n"
"v_add_f32 v44, v44, v4\n"
"v_mfma_f32_16x16x32_bf16 a[20:23], a[212:215], v[16:19], a[20:23]\n"
"v_exp_f32 v11, v11\n"
"v_mfma_f32_16x16x32_bf16 a[24:27], a[216:219], v[16:19], a[24:27]\n"
"ds_read_b64_tr_b16 a[236:237], v243 offset:57600\n"
"v_cvt_pk_bf16_f32 v21, v10, v11\n"
"v_mfma_f32_16x16x32_bf16 a[28:31], a[220:223], v[16:19], a[28:31]\n"
"v_exp_f32 v12, v12\n"
"v_mfma_f32_16x16x32_bf16 v[220:223], a[128:131], v[96:99], v[32:35]\n"
"ds_read_b64_tr_b16 a[238:239], v243 offset:61696\n"
"v_add_f32 v44, v44, v5\n"
"v_mfma_f32_16x16x32_bf16 v[220:223], a[132:135], v[100:103], v[220:223]\n"
"v_exp_f32 v13, v13\n"
"v_mfma_f32_16x16x32_bf16 v[220:223], a[136:139], v[104:107], v[220:223]\n"
"ds_read_b64_tr_b16 a[240:241], v242 offset:57856\n"
"v_cvt_pk_bf16_f32 v22, v12, v13\n"
"v_mfma_f32_16x16x32_bf16 v[220:223], a[140:143], v[108:111], v[220:223]\n"
"v_exp_f32 v14, v14\n"
"v_mfma_f32_16x16x32_bf16 v[224:227], a[144:147], v[96:99], v[32:35]\n"
"ds_read_b64_tr_b16 a[242:243], v242 offset:61952\n"
"v_add_f32 v44, v44, v6\n"
"v_mfma_f32_16x16x32_bf16 v[224:227], a[148:151], v[100:103], v[224:227]\n"
"v_exp_f32 v15, v15\n"
"v_mfma_f32_16x16x32_bf16 v[224:227], a[152:155], v[104:107], v[224:227]\n"
"ds_read_b64_tr_b16 a[244:245], v243 offset:57856\n"
"v_cvt_pk_bf16_f32 v23, v14, v15\n"
"v_mfma_f32_16x16x32_bf16 v[224:227], a[156:159], v[108:111], v[224:227]\n"
"v_exp_f32 v220, v220\n"
"v_mfma_f32_16x16x32_bf16 a[32:35], a[192:195], v[20:23], a[32:35]\n"
"buffer_load_dwordx4 v246, s[12:15], s84 offen offset:2048 lds\n"
"v_lshl_add_u32 v252, v251, 1, v217\n"
"v_mfma_f32_16x16x32_bf16 a[36:39], a[196:199], v[20:23], a[36:39]\n"
"v_exp_f32 v221, v221\n"
"v_mfma_f32_16x16x32_bf16 a[40:43], a[200:203], v[20:23], a[40:43]\n"
"ds_read_b64_tr_b16 a[246:247], v243 offset:61952\n"
"v_cvt_pk_bf16_f32 v16, v220, v221\n"
"v_mfma_f32_16x16x32_bf16 a[44:47], a[204:207], v[20:23], a[44:47]\n"
"v_exp_f32 v222, v222\n"
"v_mfma_f32_16x16x32_bf16 a[48:51], a[208:211], v[20:23], a[48:51]\n"
"ds_read_b64_tr_b16 a[248:249], v242 offset:58112\n"
"v_add_f32 v44, v44, v7\n"
"v_mfma_f32_16x16x32_bf16 a[52:55], a[212:215], v[20:23], a[52:55]\n"
"v_exp_f32 v223, v223\n"
"v_mfma_f32_16x16x32_bf16 a[56:59], a[216:219], v[20:23], a[56:59]\n"
"ds_read_b64_tr_b16 a[250:251], v242 offset:62208\n"
"v_cvt_pk_bf16_f32 v17, v222, v223\n"
"v_mfma_f32_16x16x32_bf16 a[60:63], a[220:223], v[20:23], a[60:63]\n"
"v_exp_f32 v224, v224\n"
"v_mfma_f32_16x16x32_bf16 v[48:51], v[40:43], v[20:23], v[48:51]\n"
"ds_read_b64_tr_b16 a[252:253], v243 offset:58112\n"
"v_add_f32 v52, v52, v220\n"
"v_mfma_f32_16x16x32_bf16 v[228:231], a[128:131], v[112:115], v[36:39]\n"
"v_exp_f32 v225, v225\n"
"v_mfma_f32_16x16x32_bf16 v[228:231], a[132:135], v[116:119], v[228:231]\n"
"ds_read_b64_tr_b16 a[254:255], v243 offset:62208\n"
"v_cvt_pk_bf16_f32 v18, v224, v225\n"
"v_mfma_f32_16x16x32_bf16 v[228:231], a[136:139], v[120:123], v[228:231]\n"
"v_exp_f32 v226, v226\n"
"v_mfma_f32_16x16x32_bf16 v[228:231], a[140:143], v[124:127], v[228:231]\n"
"v_add_f32 v52, v52, v221\n"
"v_add_f32 v52, v52, v222\n"
"v_mfma_f32_16x16x32_bf16 v[232:235], a[144:147], v[112:115], v[36:39]\n"
"v_exp_f32 v227, v227\n"
"v_mfma_f32_16x16x32_bf16 v[232:235], a[148:151], v[116:119], v[232:235]\n"
"v_cvt_pk_bf16_f32 v19, v226, v227\n"
"v_add_f32 v52, v52, v223\n"
"v_mfma_f32_16x16x32_bf16 v[232:235], a[152:155], v[120:123], v[232:235]\n"
"buffer_load_dwordx4 v247, s[12:15], s84 offen offset:3072 lds\n"
"v_add_u32 v251, v251, v217\n"
"v_mfma_f32_16x16x32_bf16 v[232:235], a[156:159], v[124:127], v[232:235]\n"
"s_add_u32 s84, s84, 16384\n"
"v_exp_f32 v228, v228\n"
"v_mfma_f32_16x16x32_bf16 a[64:67], a[192:195], v[16:19], a[64:67]\n"
"s_mul_i32 s62, s41, 1327104\n"
"v_add_f32 v52, v52, v224\n"
"v_add_f32 v52, v52, v225\n"
"v_mfma_f32_16x16x32_bf16 a[68:71], a[196:199], v[16:19], a[68:71]\n"
"s_lshl_b32 s49, s48, 1\n"
"v_exp_f32 v229, v229\n"
"v_mfma_f32_16x16x32_bf16 a[72:75], a[200:203], v[16:19], a[72:75]\n"
"s_add_u32 s49, s49, s62\n"
"v_cvt_pk_bf16_f32 v20, v228, v229\n"
"v_add_f32 v52, v52, v226\n"
"v_mfma_f32_16x16x32_bf16 a[76:79], a[204:207], v[16:19], a[76:79]\n"
"v_exp_f32 v230, v230\n"
"v_mfma_f32_16x16x32_bf16 a[80:83], a[208:211], v[16:19], a[80:83]\n"
"buffer_load_dwordx4 v[128:131], v252, s[64:67], s49 offen\n"
"v_add_f32 v52, v52, v227\n"
"v_mfma_f32_16x16x32_bf16 a[84:87], a[212:215], v[16:19], a[84:87]\n"
"v_exp_f32 v231, v231\n"
"v_mfma_f32_16x16x32_bf16 a[88:91], a[216:219], v[16:19], a[88:91]\n"
"s_waitcnt vmcnt(20) lgkmcnt(0)\n"
"s_barrier\n"
"v_cvt_pk_bf16_f32 v21, v230, v231\n"
"v_mul_u32_u24 v249, 3641, v248\n"
"v_mfma_f32_16x16x32_bf16 a[92:95], a[220:223], v[16:19], a[92:95]\n"
"s_movk_i32 s60, 864\n"
"v_exp_f32 v232, v232\n"
"v_mfma_f32_16x16x32_bf16 v[0:3], a[160:163], v[64:67], v[24:27]\n"
"ds_read_b128 a[128:131], v241 offset:0\n"
"v_add_u32 v206, 4096, v207\n"
"v_mfma_f32_16x16x32_bf16 v[0:3], a[164:167], v[68:71], v[0:3]\n"
"v_exp_f32 v233, v233\n"
"v_mfma_f32_16x16x32_bf16 v[0:3], a[168:171], v[72:75], v[0:3]\n"
"ds_read_b128 a[132:135], v241 offset:256\n"
"v_cvt_pk_bf16_f32 v22, v232, v233\n"
"v_mfma_f32_16x16x32_bf16 v[0:3], a[172:175], v[76:79], v[0:3]\n"
"v_exp_f32 v234, v234\n"
"v_mfma_f32_16x16x32_bf16 v[4:7], a[176:179], v[64:67], v[24:27]\n"
"ds_read_b128 a[136:139], v241 offset:512\n"
"v_lshrrev_b32 v217, 16, v249\n"
"v_mfma_f32_16x16x32_bf16 v[4:7], a[180:183], v[68:71], v[4:7]\n"
"v_exp_f32 v235, v235\n"
"v_mfma_f32_16x16x32_bf16 v[4:7], a[184:187], v[72:75], v[4:7]\n"
"ds_read_b128 a[140:143], v241 offset:768\n"
"v_cvt_pk_bf16_f32 v23, v234, v235\n"
"v_mfma_f32_16x16x32_bf16 v[4:7], a[188:191], v[76:79], v[4:7]\n"
"s_add_u32 m0, s86, 32768\n"
"v_exp_f32 v0, v0\n"
"v_mfma_f32_16x16x32_bf16 a[96:99], a[192:195], v[20:23], a[96:99]\n"
"buffer_load_dwordx4 v246, s[8:11], s84 offen offset:0 lds\n"
"v_mad_u32_u24 v210, v217, s60, v207\n"
"v_mfma_f32_16x16x32_bf16 a[100:103], a[196:199], v[20:23], a[100:103]\n"
"v_exp_f32 v1, v1\n"
"v_mfma_f32_16x16x32_bf16 a[104:107], a[200:203], v[20:23], a[104:107]\n"
"ds_read_b128 a[144:147], v241 offset:4096\n"
"v_cvt_pk_bf16_f32 v16, v0, v1\n"
"v_mfma_f32_16x16x32_bf16 a[108:111], a[204:207], v[20:23], a[108:111]\n"
"v_exp_f32 v2, v2\n"
"v_mfma_f32_16x16x32_bf16 a[112:115], a[208:211], v[20:23], a[112:115]\n"
"ds_read_b128 a[148:151], v241 offset:4352\n"
"v_add_f32 v44, v44, v0\n"
"v_mfma_f32_16x16x32_bf16 a[116:119], a[212:215], v[20:23], a[116:119]\n"
"v_exp_f32 v3, v3\n"
"v_mfma_f32_16x16x32_bf16 a[120:123], a[216:219], v[20:23], a[120:123]\n"
"ds_read_b128 a[152:155], v241 offset:4608\n"
"v_cvt_pk_bf16_f32 v17, v2, v3\n"
"v_mfma_f32_16x16x32_bf16 a[124:127], a[220:223], v[20:23], a[124:127]\n"
"v_exp_f32 v4, v4\n"
"v_mfma_f32_16x16x32_bf16 v[56:59], v[40:43], v[20:23], v[56:59]\n"
"ds_read_b128 a[156:159], v241 offset:4864\n"
"v_add_f32 v44, v44, v1\n"
"v_mfma_f32_16x16x32_bf16 v[8:11], a[160:163], v[80:83], v[28:31]\n"
"v_exp_f32 v5, v5\n"
"v_mfma_f32_16x16x32_bf16 v[8:11], a[164:167], v[84:87], v[8:11]\n"
"ds_read_b64_tr_b16 a[192:193], v244 offset:16384\n"
"v_cvt_pk_bf16_f32 v18, v4, v5\n"
"v_mfma_f32_16x16x32_bf16 v[8:11], a[168:171], v[88:91], v[8:11]\n"
"v_exp_f32 v6, v6\n"
"v_mfma_f32_16x16x32_bf16 v[8:11], a[172:175], v[92:95], v[8:11]\n"
"ds_read_b64_tr_b16 a[194:195], v244 offset:20480\n"
"v_add_f32 v44, v44, v2\n"
"v_mfma_f32_16x16x32_bf16 v[12:15], a[176:179], v[80:83], v[28:31]\n"
"v_exp_f32 v7, v7\n"
"v_mfma_f32_16x16x32_bf16 v[12:15], a[180:183], v[84:87], v[12:15]\n"
"ds_read_b64_tr_b16 a[196:197], v245 offset:16384\n"
"v_cvt_pk_bf16_f32 v19, v6, v7\n"
"v_mfma_f32_16x16x32_bf16 v[12:15], a[184:187], v[88:91], v[12:15]\n"
"buffer_load_dwordx4 v246, s[8:11], s84 offen offset:1024 lds\n"
"v_add_u32 v249, 233024, v249\n"
"v_mfma_f32_16x16x32_bf16 v[12:15], a[188:191], v[92:95], v[12:15]\n"
"s_waitcnt lgkmcnt(11)\n"
"v_exp_f32 v8, v8\n"
"v_mfma_f32_16x16x32_bf16 a[0:3], a[224:227], v[16:19], a[0:3]\n"
"ds_read_b64_tr_b16 a[198:199], v245 offset:20480\n"
"v_add_f32 v44, v44, v3\n"
"v_mfma_f32_16x16x32_bf16 a[4:7], a[228:231], v[16:19], a[4:7]\n"
"v_exp_f32 v9, v9\n"
"v_mfma_f32_16x16x32_bf16 a[8:11], a[232:235], v[16:19], a[8:11]\n"
"ds_read_b64_tr_b16 a[200:201], v244 offset:16640\n"
"v_cvt_pk_bf16_f32 v20, v8, v9\n"
"v_mfma_f32_16x16x32_bf16 a[12:15], a[236:239], v[16:19], a[12:15]\n"
"v_exp_f32 v10, v10\n"
"v_mfma_f32_16x16x32_bf16 a[16:19], a[240:243], v[16:19], a[16:19]\n"
"ds_read_b64_tr_b16 a[202:203], v244 offset:20736\n"
"v_add_f32 v44, v44, v4\n"
"v_mfma_f32_16x16x32_bf16 a[20:23], a[244:247], v[16:19], a[20:23]\n"
"v_exp_f32 v11, v11\n"
"v_mfma_f32_16x16x32_bf16 a[24:27], a[248:251], v[16:19], a[24:27]\n"
"ds_read_b64_tr_b16 a[204:205], v245 offset:16640\n"
"v_cvt_pk_bf16_f32 v21, v10, v11\n"
"v_mfma_f32_16x16x32_bf16 a[28:31], a[252:255], v[16:19], a[28:31]\n"
"v_exp_f32 v12, v12\n"
"v_mfma_f32_16x16x32_bf16 v[220:223], a[160:163], v[96:99], v[32:35]\n"
"ds_read_b64_tr_b16 a[206:207], v245 offset:20736\n"
"v_add_f32 v44, v44, v5\n"
"v_mfma_f32_16x16x32_bf16 v[220:223], a[164:167], v[100:103], v[220:223]\n"
"v_exp_f32 v13, v13\n"
"v_mfma_f32_16x16x32_bf16 v[220:223], a[168:171], v[104:107], v[220:223]\n"
"ds_read_b64_tr_b16 a[208:209], v244 offset:16896\n"
"v_cvt_pk_bf16_f32 v22, v12, v13\n"
"v_mfma_f32_16x16x32_bf16 v[220:223], a[172:175], v[108:111], v[220:223]\n"
"v_exp_f32 v14, v14\n"
"v_mfma_f32_16x16x32_bf16 v[224:227], a[176:179], v[96:99], v[32:35]\n"
"ds_read_b64_tr_b16 a[210:211], v244 offset:20992\n"
"v_add_f32 v44, v44, v6\n"
"v_mfma_f32_16x16x32_bf16 v[224:227], a[180:183], v[100:103], v[224:227]\n"
"v_exp_f32 v15, v15\n"
"v_mfma_f32_16x16x32_bf16 v[224:227], a[184:187], v[104:107], v[224:227]\n"
"ds_read_b64_tr_b16 a[212:213], v245 offset:16896\n"
"v_cvt_pk_bf16_f32 v23, v14, v15\n"
"v_mfma_f32_16x16x32_bf16 v[224:227], a[188:191], v[108:111], v[224:227]\n"
"v_exp_f32 v220, v220\n"
"v_mfma_f32_16x16x32_bf16 a[32:35], a[224:227], v[20:23], a[32:35]\n"
"buffer_load_dwordx4 v247, s[8:11], s84 offen offset:2048 lds\n"
"v_lshrrev_b32 v217, 16, v249\n"
"v_mfma_f32_16x16x32_bf16 a[36:39], a[228:231], v[20:23], a[36:39]\n"
"v_exp_f32 v221, v221\n"
"v_mfma_f32_16x16x32_bf16 a[40:43], a[232:235], v[20:23], a[40:43]\n"
"ds_read_b64_tr_b16 a[214:215], v245 offset:20992\n"
"v_cvt_pk_bf16_f32 v16, v220, v221\n"
"v_mfma_f32_16x16x32_bf16 a[44:47], a[236:239], v[20:23], a[44:47]\n"
"v_exp_f32 v222, v222\n"
"v_mfma_f32_16x16x32_bf16 a[48:51], a[240:243], v[20:23], a[48:51]\n"
"ds_read_b64_tr_b16 a[216:217], v244 offset:17152\n"
"v_add_f32 v44, v44, v7\n"
"v_mfma_f32_16x16x32_bf16 a[52:55], a[244:247], v[20:23], a[52:55]\n"
"v_exp_f32 v223, v223\n"
"v_mfma_f32_16x16x32_bf16 a[56:59], a[248:251], v[20:23], a[56:59]\n"
"ds_read_b64_tr_b16 a[218:219], v244 offset:21248\n"
"v_cvt_pk_bf16_f32 v17, v222, v223\n"
"v_mfma_f32_16x16x32_bf16 a[60:63], a[252:255], v[20:23], a[60:63]\n"
"v_exp_f32 v224, v224\n"
"v_mfma_f32_16x16x32_bf16 v[48:51], v[40:43], v[20:23], v[48:51]\n"
"ds_read_b64_tr_b16 a[220:221], v245 offset:17152\n"
"v_add_f32 v52, v52, v220\n"
"v_mfma_f32_16x16x32_bf16 v[228:231], a[160:163], v[112:115], v[36:39]\n"
"v_exp_f32 v225, v225\n"
"v_mfma_f32_16x16x32_bf16 v[228:231], a[164:167], v[116:119], v[228:231]\n"
"ds_read_b64_tr_b16 a[222:223], v245 offset:21248\n"
"v_cvt_pk_bf16_f32 v18, v224, v225\n"
"v_mfma_f32_16x16x32_bf16 v[228:231], a[168:171], v[120:123], v[228:231]\n"
"v_exp_f32 v226, v226\n"
"v_mfma_f32_16x16x32_bf16 v[228:231], a[172:175], v[124:127], v[228:231]\n"
"v_add_f32 v52, v52, v221\n"
"v_add_f32 v52, v52, v222\n"
"v_mfma_f32_16x16x32_bf16 v[232:235], a[176:179], v[112:115], v[36:39]\n"
"v_exp_f32 v227, v227\n"
"v_mfma_f32_16x16x32_bf16 v[232:235], a[180:183], v[116:119], v[232:235]\n"
"v_cvt_pk_bf16_f32 v19, v226, v227\n"
"v_add_f32 v52, v52, v223\n"
"v_mfma_f32_16x16x32_bf16 v[232:235], a[184:187], v[120:123], v[232:235]\n"
"buffer_load_dwordx4 v247, s[8:11], s84 offen offset:3072 lds\n"
"v_mad_u32_u24 v211, v217, s60, v207\n"
"v_mfma_f32_16x16x32_bf16 v[232:235], a[188:191], v[124:127], v[232:235]\n"
"v_exp_f32 v228, v228\n"
"v_mfma_f32_16x16x32_bf16 a[64:67], a[224:227], v[16:19], a[64:67]\n"
"v_add_f32 v52, v52, v224\n"
"v_add_f32 v52, v52, v225\n"
"v_mfma_f32_16x16x32_bf16 a[68:71], a[228:231], v[16:19], a[68:71]\n"
"v_exp_f32 v229, v229\n"
"v_mfma_f32_16x16x32_bf16 a[72:75], a[232:235], v[16:19], a[72:75]\n"
"v_cvt_pk_bf16_f32 v20, v228, v229\n"
"v_add_f32 v52, v52, v226\n"
"v_mfma_f32_16x16x32_bf16 a[76:79], a[236:239], v[16:19], a[76:79]\n"
"v_exp_f32 v230, v230\n"
"v_mfma_f32_16x16x32_bf16 a[80:83], a[240:243], v[16:19], a[80:83]\n"
"v_add_f32 v52, v52, v227\n"
"v_add_u32 v249, 233024, v249\n"
"v_mfma_f32_16x16x32_bf16 a[84:87], a[244:247], v[16:19], a[84:87]\n"
"v_exp_f32 v231, v231\n"
"v_mfma_f32_16x16x32_bf16 a[88:91], a[248:251], v[16:19], a[88:91]\n"
"s_waitcnt lgkmcnt(8)\n"
"v_cvt_pk_bf16_f32 v21, v230, v231\n"
"v_lshrrev_b32 v217, 16, v249\n"
"v_mfma_f32_16x16x32_bf16 a[92:95], a[252:255], v[16:19], a[92:95]\n"
"v_exp_f32 v232, v232\n"
"v_mfma_f32_16x16x32_bf16 v[0:3], a[128:131], v[64:67], v[24:27]\n"
"ds_read_b128 a[160:163], v241 offset:8192\n"
"v_mad_u32_u24 v212, v217, s60, v207\n"
"v_mfma_f32_16x16x32_bf16 v[0:3], a[132:135], v[68:71], v[0:3]\n"
"v_exp_f32 v233, v233\n"
"v_mfma_f32_16x16x32_bf16 v[0:3], a[136:139], v[72:75], v[0:3]\n"
"ds_read_b128 a[164:167], v241 offset:8448\n"
"v_cvt_pk_bf16_f32 v22, v232, v233\n"
"v_mfma_f32_16x16x32_bf16 v[0:3], a[140:143], v[76:79], v[0:3]\n"
"v_exp_f32 v234, v234\n"
"v_mfma_f32_16x16x32_bf16 v[4:7], a[144:147], v[64:67], v[24:27]\n"
"ds_read_b128 a[168:171], v241 offset:8704\n"
"v_add_u32 v249, 233024, v249\n"
"v_mfma_f32_16x16x32_bf16 v[4:7], a[148:151], v[68:71], v[4:7]\n"
"v_exp_f32 v235, v235\n"
"v_mfma_f32_16x16x32_bf16 v[4:7], a[152:155], v[72:75], v[4:7]\n"
"ds_read_b128 a[172:175], v241 offset:8960\n"
"v_cvt_pk_bf16_f32 v23, v234, v235\n"
"v_mfma_f32_16x16x32_bf16 v[4:7], a[156:159], v[76:79], v[4:7]\n"
"s_add_u32 m0, s86, 49152\n"
"v_exp_f32 v0, v0\n"
"v_mfma_f32_16x16x32_bf16 a[96:99], a[224:227], v[20:23], a[96:99]\n"
"buffer_load_dwordx4 v246, s[12:15], s84 offen offset:0 lds\n"
"v_lshrrev_b32 v217, 16, v249\n"
"v_mfma_f32_16x16x32_bf16 a[100:103], a[228:231], v[20:23], a[100:103]\n"
"v_exp_f32 v1, v1\n"
"v_mfma_f32_16x16x32_bf16 a[104:107], a[232:235], v[20:23], a[104:107]\n"
"ds_read_b128 a[176:179], v241 offset:12288\n"
"v_cvt_pk_bf16_f32 v16, v0, v1\n"
"v_mfma_f32_16x16x32_bf16 a[108:111], a[236:239], v[20:23], a[108:111]\n"
"v_exp_f32 v2, v2\n"
"v_mfma_f32_16x16x32_bf16 a[112:115], a[240:243], v[20:23], a[112:115]\n"
"ds_read_b128 a[180:183], v241 offset:12544\n"
"v_add_f32 v44, v44, v0\n"
"v_mfma_f32_16x16x32_bf16 a[116:119], a[244:247], v[20:23], a[116:119]\n"
"v_exp_f32 v3, v3\n"
"v_mfma_f32_16x16x32_bf16 a[120:123], a[248:251], v[20:23], a[120:123]\n"
"ds_read_b128 a[184:187], v241 offset:12800\n"
"v_cvt_pk_bf16_f32 v17, v2, v3\n"
"v_mfma_f32_16x16x32_bf16 a[124:127], a[252:255], v[20:23], a[124:127]\n"
"v_exp_f32 v4, v4\n"
"v_mfma_f32_16x16x32_bf16 v[56:59], v[40:43], v[20:23], v[56:59]\n"
"ds_read_b128 a[188:191], v241 offset:13056\n"
"v_add_f32 v44, v44, v1\n"
"v_mfma_f32_16x16x32_bf16 v[8:11], a[128:131], v[80:83], v[28:31]\n"
"v_exp_f32 v5, v5\n"
"v_mfma_f32_16x16x32_bf16 v[8:11], a[132:135], v[84:87], v[8:11]\n"
"ds_read_b64_tr_b16 a[224:225], v244 offset:24576\n"
"v_cvt_pk_bf16_f32 v18, v4, v5\n"
"v_mfma_f32_16x16x32_bf16 v[8:11], a[136:139], v[88:91], v[8:11]\n"
"v_exp_f32 v6, v6\n"
"v_mfma_f32_16x16x32_bf16 v[8:11], a[140:143], v[92:95], v[8:11]\n"
"ds_read_b64_tr_b16 a[226:227], v244 offset:28672\n"
"v_add_f32 v44, v44, v2\n"
"v_mfma_f32_16x16x32_bf16 v[12:15], a[144:147], v[80:83], v[28:31]\n"
"v_exp_f32 v7, v7\n"
"v_mfma_f32_16x16x32_bf16 v[12:15], a[148:151], v[84:87], v[12:15]\n"
"ds_read_b64_tr_b16 a[228:229], v245 offset:24576\n"
"v_cvt_pk_bf16_f32 v19, v6, v7\n"
"v_mfma_f32_16x16x32_bf16 v[12:15], a[152:155], v[88:91], v[12:15]\n"
"buffer_load_dwordx4 v247, s[12:15], s84 offen offset:1024 lds\n"
"v_mad_u32_u24 v213, v217, s60, v207\n"
"v_mfma_f32_16x16x32_bf16 v[12:15], a[156:159], v[92:95], v[12:15]\n"
"s_waitcnt lgkmcnt(11)\n"
"v_exp_f32 v8, v8\n"
"v_mfma_f32_16x16x32_bf16 a[0:3], a[192:195], v[16:19], a[0:3]\n"
"ds_read_b64_tr_b16 a[230:231], v245 offset:28672\n"
"v_add_f32 v44, v44, v3\n"
"v_mfma_f32_16x16x32_bf16 a[4:7], a[196:199], v[16:19], a[4:7]\n"
"v_exp_f32 v9, v9\n"
"v_mfma_f32_16x16x32_bf16 a[8:11], a[200:203], v[16:19], a[8:11]\n"
"ds_read_b64_tr_b16 a[232:233], v244 offset:24832\n"
"v_cvt_pk_bf16_f32 v20, v8, v9\n"
"v_mfma_f32_16x16x32_bf16 a[12:15], a[204:207], v[16:19], a[12:15]\n"
"v_exp_f32 v10, v10\n"
"v_mfma_f32_16x16x32_bf16 a[16:19], a[208:211], v[16:19], a[16:19]\n"
"ds_read_b64_tr_b16 a[234:235], v244 offset:28928\n"
"v_add_f32 v44, v44, v4\n"
"v_mfma_f32_16x16x32_bf16 a[20:23], a[212:215], v[16:19], a[20:23]\n"
"v_exp_f32 v11, v11\n"
"v_mfma_f32_16x16x32_bf16 a[24:27], a[216:219], v[16:19], a[24:27]\n"
"ds_read_b64_tr_b16 a[236:237], v245 offset:24832\n"
"v_cvt_pk_bf16_f32 v21, v10, v11\n"
"v_mfma_f32_16x16x32_bf16 a[28:31], a[220:223], v[16:19], a[28:31]\n"
"v_exp_f32 v12, v12\n"
"v_mfma_f32_16x16x32_bf16 v[220:223], a[128:131], v[96:99], v[32:35]\n"
"ds_read_b64_tr_b16 a[238:239], v245 offset:28928\n"
"v_add_f32 v44, v44, v5\n"
"v_mfma_f32_16x16x32_bf16 v[220:223], a[132:135], v[100:103], v[220:223]\n"
"v_exp_f32 v13, v13\n"
"v_mfma_f32_16x16x32_bf16 v[220:223], a[136:139], v[104:107], v[220:223]\n"
"ds_read_b64_tr_b16 a[240:241], v244 offset:25088\n"
"v_cvt_pk_bf16_f32 v22, v12, v13\n"
"v_mfma_f32_16x16x32_bf16 v[220:223], a[140:143], v[108:111], v[220:223]\n"
"v_exp_f32 v14, v14\n"
"v_mfma_f32_16x16x32_bf16 v[224:227], a[144:147], v[96:99], v[32:35]\n"
"ds_read_b64_tr_b16 a[242:243], v244 offset:29184\n"
"v_add_f32 v44, v44, v6\n"
"v_mfma_f32_16x16x32_bf16 v[224:227], a[148:151], v[100:103], v[224:227]\n"
"v_exp_f32 v15, v15\n"
"v_mfma_f32_16x16x32_bf16 v[224:227], a[152:155], v[104:107], v[224:227]\n"
"ds_read_b64_tr_b16 a[244:245], v245 offset:25088\n"
"v_cvt_pk_bf16_f32 v23, v14, v15\n"
"v_mfma_f32_16x16x32_bf16 v[224:227], a[156:159], v[108:111], v[224:227]\n"
"v_exp_f32 v220, v220\n"
"v_mfma_f32_16x16x32_bf16 a[32:35], a[192:195], v[20:23], a[32:35]\n"
"buffer_load_dwordx4 v246, s[12:15], s84 offen offset:2048 lds\n"
"v_add_u32 v249, 233024, v249\n"
"v_mfma_f32_16x16x32_bf16 a[36:39], a[196:199], v[20:23], a[36:39]\n"
"v_exp_f32 v221, v221\n"
"v_mfma_f32_16x16x32_bf16 a[40:43], a[200:203], v[20:23], a[40:43]\n"
"ds_read_b64_tr_b16 a[246:247], v245 offset:29184\n"
"v_cvt_pk_bf16_f32 v16, v220, v221\n"
"v_mfma_f32_16x16x32_bf16 a[44:47], a[204:207], v[20:23], a[44:47]\n"
"v_exp_f32 v222, v222\n"
"v_mfma_f32_16x16x32_bf16 a[48:51], a[208:211], v[20:23], a[48:51]\n"
"ds_read_b64_tr_b16 a[248:249], v244 offset:25344\n"
"v_add_f32 v44, v44, v7\n"
"v_mfma_f32_16x16x32_bf16 a[52:55], a[212:215], v[20:23], a[52:55]\n"
"v_exp_f32 v223, v223\n"
"v_mfma_f32_16x16x32_bf16 a[56:59], a[216:219], v[20:23], a[56:59]\n"
"ds_read_b64_tr_b16 a[250:251], v244 offset:29440\n"
"v_cvt_pk_bf16_f32 v17, v222, v223\n"
"v_mfma_f32_16x16x32_bf16 a[60:63], a[220:223], v[20:23], a[60:63]\n"
"v_exp_f32 v224, v224\n"
"v_mfma_f32_16x16x32_bf16 v[48:51], v[40:43], v[20:23], v[48:51]\n"
"ds_read_b64_tr_b16 a[252:253], v245 offset:25344\n"
"v_add_f32 v52, v52, v220\n"
"v_mfma_f32_16x16x32_bf16 v[228:231], a[128:131], v[112:115], v[36:39]\n"
"v_exp_f32 v225, v225\n"
"v_mfma_f32_16x16x32_bf16 v[228:231], a[132:135], v[116:119], v[228:231]\n"
"ds_read_b64_tr_b16 a[254:255], v245 offset:29440\n"
"v_cvt_pk_bf16_f32 v18, v224, v225\n"
"v_mfma_f32_16x16x32_bf16 v[228:231], a[136:139], v[120:123], v[228:231]\n"
"v_exp_f32 v226, v226\n"
"v_mfma_f32_16x16x32_bf16 v[228:231], a[140:143], v[124:127], v[228:231]\n"
"v_add_f32 v52, v52, v221\n"
"v_add_f32 v52, v52, v222\n"
"v_mfma_f32_16x16x32_bf16 v[232:235], a[144:147], v[112:115], v[36:39]\n"
"v_exp_f32 v227, v227\n"
"v_mfma_f32_16x16x32_bf16 v[232:235], a[148:151], v[116:119], v[232:235]\n"
"v_cvt_pk_bf16_f32 v19, v226, v227\n"
"v_add_f32 v52, v52, v223\n"
"v_mfma_f32_16x16x32_bf16 v[232:235], a[152:155], v[120:123], v[232:235]\n"
"buffer_load_dwordx4 v247, s[12:15], s84 offen offset:3072 lds\n"
"v_lshrrev_b32 v217, 16, v249\n"
"v_mfma_f32_16x16x32_bf16 v[232:235], a[156:159], v[124:127], v[232:235]\n"
"s_add_u32 s84, s84, 16384\n"
"v_exp_f32 v228, v228\n"
"v_mfma_f32_16x16x32_bf16 a[64:67], a[192:195], v[16:19], a[64:67]\n"
"buffer_load_dwordx4 v[164:167], v252, s[68:71], s49 offen\n"
"v_add_f32 v52, v52, v224\n"
"v_add_f32 v52, v52, v225\n"
"v_mfma_f32_16x16x32_bf16 a[68:71], a[196:199], v[16:19], a[68:71]\n"
"v_exp_f32 v229, v229\n"
"v_mfma_f32_16x16x32_bf16 a[72:75], a[200:203], v[16:19], a[72:75]\n"
"v_cvt_pk_bf16_f32 v20, v228, v229\n"
"v_add_f32 v52, v52, v226\n"
"v_mfma_f32_16x16x32_bf16 a[76:79], a[204:207], v[16:19], a[76:79]\n"
"v_exp_f32 v230, v230\n"
"v_mfma_f32_16x16x32_bf16 a[80:83], a[208:211], v[16:19], a[80:83]\n"
"v_add_f32 v52, v52, v227\n"
"v_mad_u32_u24 v214, v217, s60, v206\n"
"v_mfma_f32_16x16x32_bf16 a[84:87], a[212:215], v[16:19], a[84:87]\n"
"v_exp_f32 v231, v231\n"
"v_mfma_f32_16x16x32_bf16 a[88:91], a[216:219], v[16:19], a[88:91]\n"
"s_waitcnt vmcnt(21) lgkmcnt(0)\n"
"s_barrier\n"
"v_cvt_pk_bf16_f32 v21, v230, v231\n"
"v_add_u32 v249, 233024, v249\n"
"v_mfma_f32_16x16x32_bf16 a[92:95], a[220:223], v[16:19], a[92:95]\n"
"v_exp_f32 v232, v232\n"
"v_mfma_f32_16x16x32_bf16 v[0:3], a[160:163], v[64:67], v[24:27]\n"
"ds_read_b128 a[128:131], v241 offset:32768\n"
"v_lshrrev_b32 v217, 16, v249\n"
"v_mfma_f32_16x16x32_bf16 v[0:3], a[164:167], v[68:71], v[0:3]\n"
"v_exp_f32 v233, v233\n"
"v_mfma_f32_16x16x32_bf16 v[0:3], a[168:171], v[72:75], v[0:3]\n"
"ds_read_b128 a[132:135], v241 offset:33024\n"
"v_cvt_pk_bf16_f32 v22, v232, v233\n"
"v_mfma_f32_16x16x32_bf16 v[0:3], a[172:175], v[76:79], v[0:3]\n"
"v_exp_f32 v234, v234\n"
"v_mfma_f32_16x16x32_bf16 v[4:7], a[176:179], v[64:67], v[24:27]\n"
"ds_read_b128 a[136:139], v241 offset:33280\n"
"v_mad_u32_u24 v236, v217, s60, v206\n"
"v_mfma_f32_16x16x32_bf16 v[4:7], a[180:183], v[68:71], v[4:7]\n"
"v_exp_f32 v235, v235\n"
"v_mfma_f32_16x16x32_bf16 v[4:7], a[184:187], v[72:75], v[4:7]\n"
"ds_read_b128 a[140:143], v241 offset:33536\n"
"v_cvt_pk_bf16_f32 v23, v234, v235\n"
"v_mfma_f32_16x16x32_bf16 v[4:7], a[188:191], v[76:79], v[4:7]\n"
"s_add_u32 m0, s86, 65536\n"
"v_exp_f32 v0, v0\n"
"v_mfma_f32_16x16x32_bf16 a[96:99], a[192:195], v[20:23], a[96:99]\n"
"buffer_load_dwordx4 v246, s[8:11], s84 offen offset:0 lds\n"
"v_add_u32 v249, 233024, v249\n"
"v_mfma_f32_16x16x32_bf16 a[100:103], a[196:199], v[20:23], a[100:103]\n"
"v_exp_f32 v1, v1\n"
"v_mfma_f32_16x16x32_bf16 a[104:107], a[200:203], v[20:23], a[104:107]\n"
"ds_read_b128 a[144:147], v241 offset:36864\n"
"v_cvt_pk_bf16_f32 v16, v0, v1\n"
"v_mfma_f32_16x16x32_bf16 a[108:111], a[204:207], v[20:23], a[108:111]\n"
"v_exp_f32 v2, v2\n"
"v_mfma_f32_16x16x32_bf16 a[112:115], a[208:211], v[20:23], a[112:115]\n"
"ds_read_b128 a[148:151], v241 offset:37120\n"
"v_add_f32 v44, v44, v0\n"
"v_mfma_f32_16x16x32_bf16 a[116:119], a[212:215], v[20:23], a[116:119]\n"
"v_exp_f32 v3, v3\n"
"v_mfma_f32_16x16x32_bf16 a[120:123], a[216:219], v[20:23], a[120:123]\n"
"ds_read_b128 a[152:155], v241 offset:37376\n"
"v_cvt_pk_bf16_f32 v17, v2, v3\n"
"v_mfma_f32_16x16x32_bf16 a[124:127], a[220:223], v[20:23], a[124:127]\n"
"v_exp_f32 v4, v4\n"
"v_mfma_f32_16x16x32_bf16 v[56:59], v[40:43], v[20:23], v[56:59]\n"
"ds_read_b128 a[156:159], v241 offset:37632\n"
"v_add_f32 v44, v44, v1\n"
"v_mfma_f32_16x16x32_bf16 v[8:11], a[160:163], v[80:83], v[28:31]\n"
"v_exp_f32 v5, v5\n"
"v_mfma_f32_16x16x32_bf16 v[8:11], a[164:167], v[84:87], v[8:11]\n"
"ds_read_b64_tr_b16 a[192:193], v244 offset:49152\n"
"v_cvt_pk_bf16_f32 v18, v4, v5\n"
"v_mfma_f32_16x16x32_bf16 v[8:11], a[168:171], v[88:91], v[8:11]\n"
"v_exp_f32 v6, v6\n"
"v_mfma_f32_16x16x32_bf16 v[8:11], a[172:175], v[92:95], v[8:11]\n"
"ds_read_b64_tr_b16 a[194:195], v244 offset:53248\n"
"v_add_f32 v44, v44, v2\n"
"v_mfma_f32_16x16x32_bf16 v[12:15], a[176:179], v[80:83], v[28:31]\n"
"v_exp_f32 v7, v7\n"
"v_mfma_f32_16x16x32_bf16 v[12:15], a[180:183], v[84:87], v[12:15]\n"
"ds_read_b64_tr_b16 a[196:197], v245 offset:49152\n"
"v_cvt_pk_bf16_f32 v19, v6, v7\n"
"v_mfma_f32_16x16x32_bf16 v[12:15], a[184:187], v[88:91], v[12:15]\n"
"buffer_load_dwordx4 v246, s[8:11], s84 offen offset:1024 lds\n"
"v_lshrrev_b32 v217, 16, v249\n"
"v_mfma_f32_16x16x32_bf16 v[12:15], a[188:191], v[92:95], v[12:15]\n"
"s_waitcnt lgkmcnt(11)\n"
"v_exp_f32 v8, v8\n"
"v_mfma_f32_16x16x32_bf16 a[0:3], a[224:227], v[16:19], a[0:3]\n"
"ds_read_b64_tr_b16 a[198:199], v245 offset:53248\n"
"v_add_f32 v44, v44, v3\n"
"v_mfma_f32_16x16x32_bf16 a[4:7], a[228:231], v[16:19], a[4:7]\n"
"v_exp_f32 v9, v9\n"
"v_mfma_f32_16x16x32_bf16 a[8:11], a[232:235], v[16:19], a[8:11]\n"
"ds_read_b64_tr_b16 a[200:201], v244 offset:49408\n"
"v_cvt_pk_bf16_f32 v20, v8, v9\n"
"v_mfma_f32_16x16x32_bf16 a[12:15], a[236:239], v[16:19], a[12:15]\n"
"v_exp_f32 v10, v10\n"
"v_mfma_f32_16x16x32_bf16 a[16:19], a[240:243], v[16:19], a[16:19]\n"
"ds_read_b64_tr_b16 a[202:203], v244 offset:53504\n"
"v_add_f32 v44, v44, v4\n"
"v_mfma_f32_16x16x32_bf16 a[20:23], a[244:247], v[16:19], a[20:23]\n"
"v_exp_f32 v11, v11\n"
"v_mfma_f32_16x16x32_bf16 a[24:27], a[248:251], v[16:19], a[24:27]\n"
"ds_read_b64_tr_b16 a[204:205], v245 offset:49408\n"
"v_cvt_pk_bf16_f32 v21, v10, v11\n"
"v_mfma_f32_16x16x32_bf16 a[28:31], a[252:255], v[16:19], a[28:31]\n"
"v_exp_f32 v12, v12\n"
"v_mfma_f32_16x16x32_bf16 v[220:223], a[160:163], v[96:99], v[32:35]\n"
"ds_read_b64_tr_b16 a[206:207], v245 offset:53504\n"
"v_add_f32 v44, v44, v5\n"
"v_mfma_f32_16x16x32_bf16 v[220:223], a[164:167], v[100:103], v[220:223]\n"
"v_exp_f32 v13, v13\n"
"v_mfma_f32_16x16x32_bf16 v[220:223], a[168:171], v[104:107], v[220:223]\n"
"ds_read_b64_tr_b16 a[208:209], v244 offset:49664\n"
"v_cvt_pk_bf16_f32 v22, v12, v13\n"
"v_mfma_f32_16x16x32_bf16 v[220:223], a[172:175], v[108:111], v[220:223]\n"
"v_exp_f32 v14, v14\n"
"v_mfma_f32_16x16x32_bf16 v[224:227], a[176:179], v[96:99], v[32:35]\n"
"ds_read_b64_tr_b16 a[210:211], v244 offset:53760\n"
"v_add_f32 v44, v44, v6\n"
"v_mfma_f32_16x16x32_bf16 v[224:227], a[180:183], v[100:103], v[224:227]\n"
"v_exp_f32 v15, v15\n"
"v_mfma_f32_16x16x32_bf16 v[224:227], a[184:187], v[104:107], v[224:227]\n"
"ds_read_b64_tr_b16 a[212:213], v245 offset:49664\n"
"v_cvt_pk_bf16_f32 v23, v14, v15\n"
"v_mfma_f32_16x16x32_bf16 v[224:227], a[188:191], v[108:111], v[224:227]\n"
"v_exp_f32 v220, v220\n"
"v_mfma_f32_16x16x32_bf16 a[32:35], a[224:227], v[20:23], a[32:35]\n"
"buffer_load_dwordx4 v247, s[8:11], s84 offen offset:2048 lds\n"
"v_mad_u32_u24 v237, v217, s60, v206\n"
"v_mfma_f32_16x16x32_bf16 a[36:39], a[228:231], v[20:23], a[36:39]\n"
"v_exp_f32 v221, v221\n"
"v_mfma_f32_16x16x32_bf16 a[40:43], a[232:235], v[20:23], a[40:43]\n"
"ds_read_b64_tr_b16 a[214:215], v245 offset:53760\n"
"v_cvt_pk_bf16_f32 v16, v220, v221\n"
"v_mfma_f32_16x16x32_bf16 a[44:47], a[236:239], v[20:23], a[44:47]\n"
"v_exp_f32 v222, v222\n"
"v_mfma_f32_16x16x32_bf16 a[48:51], a[240:243], v[20:23], a[48:51]\n"
"ds_read_b64_tr_b16 a[216:217], v244 offset:49920\n"
"v_add_f32 v44, v44, v7\n"
"v_mfma_f32_16x16x32_bf16 a[52:55], a[244:247], v[20:23], a[52:55]\n"
"v_exp_f32 v223, v223\n"
"v_mfma_f32_16x16x32_bf16 a[56:59], a[248:251], v[20:23], a[56:59]\n"
"ds_read_b64_tr_b16 a[218:219], v244 offset:54016\n"
"v_cvt_pk_bf16_f32 v17, v222, v223\n"
"v_mfma_f32_16x16x32_bf16 a[60:63], a[252:255], v[20:23], a[60:63]\n"
"v_exp_f32 v224, v224\n"
"v_mfma_f32_16x16x32_bf16 v[48:51], v[40:43], v[20:23], v[48:51]\n"
"ds_read_b64_tr_b16 a[220:221], v245 offset:49920\n"
"v_add_f32 v52, v52, v220\n"
"v_mfma_f32_16x16x32_bf16 v[228:231], a[160:163], v[112:115], v[36:39]\n"
"v_exp_f32 v225, v225\n"
"v_mfma_f32_16x16x32_bf16 v[228:231], a[164:167], v[116:119], v[228:231]\n"
"ds_read_b64_tr_b16 a[222:223], v245 offset:54016\n"
"v_cvt_pk_bf16_f32 v18, v224, v225\n"
"v_mfma_f32_16x16x32_bf16 v[228:231], a[168:171], v[120:123], v[228:231]\n"
"v_exp_f32 v226, v226\n"
"v_mfma_f32_16x16x32_bf16 v[228:231], a[172:175], v[124:127], v[228:231]\n"
"v_add_f32 v52, v52, v221\n"
"v_add_f32 v52, v52, v222\n"
"v_mfma_f32_16x16x32_bf16 v[232:235], a[176:179], v[112:115], v[36:39]\n"
"v_exp_f32 v227, v227\n"
"v_mfma_f32_16x16x32_bf16 v[232:235], a[180:183], v[116:119], v[232:235]\n"
"v_cvt_pk_bf16_f32 v19, v226, v227\n"
"v_add_f32 v52, v52, v223\n"
"v_mfma_f32_16x16x32_bf16 v[232:235], a[184:187], v[120:123], v[232:235]\n"
"buffer_load_dwordx4 v247, s[8:11], s84 offen offset:3072 lds\n"
"v_add_u32 v249, 233024, v249\n"
"v_mfma_f32_16x16x32_bf16 v[232:235], a[188:191], v[124:127], v[232:235]\n"
"v_exp_f32 v228, v228\n"
"v_mfma_f32_16x16x32_bf16 a[64:67], a[224:227], v[16:19], a[64:67]\n"
"v_add_f32 v52, v52, v224\n"
"v_add_f32 v52, v52, v225\n"
"v_mfma_f32_16x16x32_bf16 a[68:71], a[228:231], v[16:19], a[68:71]\n"
"v_exp_f32 v229, v229\n"
"v_mfma_f32_16x16x32_bf16 a[72:75], a[232:235], v[16:19], a[72:75]\n"
"v_cvt_pk_bf16_f32 v20, v228, v229\n"
"v_add_f32 v52, v52, v226\n"
"v_mfma_f32_16x16x32_bf16 a[76:79], a[236:239], v[16:19], a[76:79]\n"
"v_exp_f32 v230, v230\n"
"v_mfma_f32_16x16x32_bf16 a[80:83], a[240:243], v[16:19], a[80:83]\n"
"v_add_f32 v52, v52, v227\n"
"v_lshrrev_b32 v217, 16, v249\n"
"v_mfma_f32_16x16x32_bf16 a[84:87], a[244:247], v[16:19], a[84:87]\n"
"v_exp_f32 v231, v231\n"
"v_mfma_f32_16x16x32_bf16 a[88:91], a[248:251], v[16:19], a[88:91]\n"
"s_waitcnt lgkmcnt(8)\n"
"v_cvt_pk_bf16_f32 v21, v230, v231\n"
"v_mad_u32_u24 v238, v217, s60, v206\n"
"v_mfma_f32_16x16x32_bf16 a[92:95], a[252:255], v[16:19], a[92:95]\n"
"v_exp_f32 v232, v232\n"
"v_mfma_f32_16x16x32_bf16 v[0:3], a[128:131], v[64:67], v[24:27]\n"
"ds_read_b128 a[160:163], v241 offset:40960\n"
"v_add_u32 v206, 4096, v206\n"
"v_mfma_f32_16x16x32_bf16 v[0:3], a[132:135], v[68:71], v[0:3]\n"
"v_exp_f32 v233, v233\n"
"v_mfma_f32_16x16x32_bf16 v[0:3], a[136:139], v[72:75], v[0:3]\n"
"ds_read_b128 a[164:167], v241 offset:41216\n"
"v_cvt_pk_bf16_f32 v22, v232, v233\n"
"v_mfma_f32_16x16x32_bf16 v[0:3], a[140:143], v[76:79], v[0:3]\n"
"v_exp_f32 v234, v234\n"
"v_mfma_f32_16x16x32_bf16 v[4:7], a[144:147], v[64:67], v[24:27]\n"
"ds_read_b128 a[168:171], v241 offset:41472\n"
"v_add_u32 v249, 233024, v249\n"
"v_mfma_f32_16x16x32_bf16 v[4:7], a[148:151], v[68:71], v[4:7]\n"
"v_exp_f32 v235, v235\n"
"v_mfma_f32_16x16x32_bf16 v[4:7], a[152:155], v[72:75], v[4:7]\n"
"ds_read_b128 a[172:175], v241 offset:41728\n"
"v_cvt_pk_bf16_f32 v23, v234, v235\n"
"v_mfma_f32_16x16x32_bf16 v[4:7], a[156:159], v[76:79], v[4:7]\n"
"s_add_u32 m0, s86, 81920\n"
"v_exp_f32 v0, v0\n"
"v_mfma_f32_16x16x32_bf16 a[96:99], a[224:227], v[20:23], a[96:99]\n"
"buffer_load_dwordx4 v246, s[12:15], s84 offen offset:0 lds\n"
"v_lshrrev_b32 v217, 16, v249\n"
"v_mfma_f32_16x16x32_bf16 a[100:103], a[228:231], v[20:23], a[100:103]\n"
"v_exp_f32 v1, v1\n"
"v_mfma_f32_16x16x32_bf16 a[104:107], a[232:235], v[20:23], a[104:107]\n"
"ds_read_b128 a[176:179], v241 offset:45056\n"
"v_cvt_pk_bf16_f32 v16, v0, v1\n"
"v_mfma_f32_16x16x32_bf16 a[108:111], a[236:239], v[20:23], a[108:111]\n"
"v_exp_f32 v2, v2\n"
"v_mfma_f32_16x16x32_bf16 a[112:115], a[240:243], v[20:23], a[112:115]\n"
"ds_read_b128 a[180:183], v241 offset:45312\n"
"v_add_f32 v44, v44, v0\n"
"v_mfma_f32_16x16x32_bf16 a[116:119], a[244:247], v[20:23], a[116:119]\n"
"v_exp_f32 v3, v3\n"
"v_mfma_f32_16x16x32_bf16 a[120:123], a[248:251], v[20:23], a[120:123]\n"
"ds_read_b128 a[184:187], v241 offset:45568\n"
"v_cvt_pk_bf16_f32 v17, v2, v3\n"
"v_mfma_f32_16x16x32_bf16 a[124:127], a[252:255], v[20:23], a[124:127]\n"
"v_exp_f32 v4, v4\n"
"v_mfma_f32_16x16x32_bf16 v[56:59], v[40:43], v[20:23], v[56:59]\n"
"ds_read_b128 a[188:191], v241 offset:45824\n"
"v_add_f32 v44, v44, v1\n"
"v_mfma_f32_16x16x32_bf16 v[8:11], a[128:131], v[80:83], v[28:31]\n"
"v_exp_f32 v5, v5\n"
"v_mfma_f32_16x16x32_bf16 v[8:11], a[132:135], v[84:87], v[8:11]\n"
"ds_read_b64_tr_b16 a[224:225], v244 offset:57344\n"
"v_cvt_pk_bf16_f32 v18, v4, v5\n"
"v_mfma_f32_16x16x32_bf16 v[8:11], a[136:139], v[88:91], v[8:11]\n"
"v_exp_f32 v6, v6\n"
"v_mfma_f32_16x16x32_bf16 v[8:11], a[140:143], v[92:95], v[8:11]\n"
"ds_read_b64_tr_b16 a[226:227], v244 offset:61440\n"
"v_add_f32 v44, v44, v2\n"
"v_mfma_f32_16x16x32_bf16 v[12:15], a[144:147], v[80:83], v[28:31]\n"
"v_exp_f32 v7, v7\n"
"v_mfma_f32_16x16x32_bf16 v[12:15], a[148:151], v[84:87], v[12:15]\n"
"ds_read_b64_tr_b16 a[228:229], v245 offset:57344\n"
"v_cvt_pk_bf16_f32 v19, v6, v7\n"
"v_mfma_f32_16x16x32_bf16 v[12:15], a[152:155], v[88:91], v[12:15]\n"
"buffer_load_dwordx4 v247, s[12:15], s84 offen offset:1024 lds\n"
"v_mad_u32_u24 v239, v217, s60, v206\n"
"v_mfma_f32_16x16x32_bf16 v[12:15], a[156:159], v[92:95], v[12:15]\n"
"s_waitcnt lgkmcnt(11)\n"
"v_exp_f32 v8, v8\n"
"v_mfma_f32_16x16x32_bf16 a[0:3], a[192:195], v[16:19], a[0:3]\n"
"s_and_b32 s56, s43, 3\n"
"ds_read_b64_tr_b16 a[230:231], v245 offset:61440\n"
"v_add_f32 v44, v44, v3\n"
"v_mfma_f32_16x16x32_bf16 a[4:7], a[196:199], v[16:19], a[4:7]\n"
"s_lshr_b32 s57, s43, 2\n"
"v_exp_f32 v9, v9\n"
"v_mfma_f32_16x16x32_bf16 a[8:11], a[200:203], v[16:19], a[8:11]\n"
"s_add_u32 s49, s49, 9216\n"
"ds_read_b64_tr_b16 a[232:233], v244 offset:57600\n"
"v_cvt_pk_bf16_f32 v20, v8, v9\n"
"v_mfma_f32_16x16x32_bf16 a[12:15], a[204:207], v[16:19], a[12:15]\n"
"v_exp_f32 v10, v10\n"
"v_mfma_f32_16x16x32_bf16 a[16:19], a[208:211], v[16:19], a[16:19]\n"
"ds_read_b64_tr_b16 a[234:235], v244 offset:61696\n"
"buffer_load_dwordx4 v[132:135], v252, s[64:67], s49 offen\n"
"v_add_f32 v44, v44, v4\n"
"v_mfma_f32_16x16x32_bf16 a[20:23], a[212:215], v[16:19], a[20:23]\n"
"s_mul_i32 s57, s57, 1179648\n"
"v_exp_f32 v11, v11\n"
"v_mfma_f32_16x16x32_bf16 a[24:27], a[216:219], v[16:19], a[24:27]\n"
"s_mul_i32 s62, s56, 288\n"
"ds_read_b64_tr_b16 a[236:237], v245 offset:57600\n"
"v_cvt_pk_bf16_f32 v21, v10, v11\n"
"v_mfma_f32_16x16x32_bf16 a[28:31], a[220:223], v[16:19], a[28:31]\n"
"s_add_u32 s57, s57, s62\n"
"v_exp_f32 v12, v12\n"
"v_mfma_f32_16x16x32_bf16 v[220:223], a[128:131], v[96:99], v[32:35]\n"
"s_lshl_b32 s58, s56, 3\n"
"ds_read_b64_tr_b16 a[238:239], v245 offset:61696\n"
"v_add_f32 v44, v44, v5\n"
"v_mfma_f32_16x16x32_bf16 v[220:223], a[132:135], v[100:103], v[220:223]\n"
"s_add_u32 s58, s58, s41\n"
"v_exp_f32 v13, v13\n"
"v_mfma_f32_16x16x32_bf16 v[220:223], a[136:139], v[104:107], v[220:223]\n"
"s_add_u32 s58, s58, 0\n"
"ds_read_b64_tr_b16 a[240:241], v244 offset:57856\n"
"v_cvt_pk_bf16_f32 v22, v12, v13\n"
"v_mfma_f32_16x16x32_bf16 v[220:223], a[140:143], v[108:111], v[220:223]\n"
"s_mul_i32 s58, s58, 36864\n"
"v_exp_f32 v14, v14\n"
"v_mfma_f32_16x16x32_bf16 v[224:227], a[144:147], v[96:99], v[32:35]\n"
"s_add_u32 s20, s58, s57\n"
"ds_read_b64_tr_b16 a[242:243], v244 offset:61952\n"
"v_add_f32 v44, v44, v6\n"
"v_mfma_f32_16x16x32_bf16 v[224:227], a[148:151], v[100:103], v[224:227]\n"
"s_lshl_b32 s58, s56, 3\n"
"v_exp_f32 v15, v15\n"
"v_mfma_f32_16x16x32_bf16 v[224:227], a[152:155], v[104:107], v[224:227]\n"
"s_add_u32 s58, s58, s41\n"
"ds_read_b64_tr_b16 a[244:245], v245 offset:57856\n"
"v_cvt_pk_bf16_f32 v23, v14, v15\n"
"v_mfma_f32_16x16x32_bf16 v[224:227], a[156:159], v[108:111], v[224:227]\n"
"s_add_u32 s58, s58, 4\n"
"v_exp_f32 v220, v220\n"
"v_mfma_f32_16x16x32_bf16 a[32:35], a[192:195], v[20:23], a[32:35]\n"
"s_mul_i32 s58, s58, 36864\n"
"buffer_load_dwordx4 v246, s[12:15], s84 offen offset:2048 lds\n"
"v_mfma_f32_16x16x32_bf16 a[36:39], a[196:199], v[20:23], a[36:39]\n"
"s_add_u32 s21, s58, s57\n"
"v_exp_f32 v221, v221\n"
"v_mfma_f32_16x16x32_bf16 a[40:43], a[200:203], v[20:23], a[40:43]\n"
"s_add_u32 s58, s41, 0\n"
"ds_read_b64_tr_b16 a[246:247], v245 offset:61952\n"
"v_cvt_pk_bf16_f32 v16, v220, v221\n"
"v_mfma_f32_16x16x32_bf16 a[44:47], a[204:207], v[20:23], a[44:47]\n"
"s_mul_i32 s59, s58, 43\n"
"v_exp_f32 v222, v222\n"
"v_mfma_f32_16x16x32_bf16 a[48:51], a[208:211], v[20:23], a[48:51]\n"
"s_lshr_b32 s59, s59, 7\n"
"ds_read_b64_tr_b16 a[248:249], v244 offset:58112\n"
"v_add_f32 v44, v44, v7\n"
"v_mfma_f32_16x16x32_bf16 a[52:55], a[212:215], v[20:23], a[52:55]\n"
"v_exp_f32 v223, v223\n"
"v_mfma_f32_16x16x32_bf16 a[56:59], a[216:219], v[20:23], a[56:59]\n"
"ds_read_b64_tr_b16 a[250:251], v244 offset:62208\n"
"v_cvt_pk_bf16_f32 v17, v222, v223\n"
"v_mfma_f32_16x16x32_bf16 a[60:63], a[220:223], v[20:23], a[60:63]\n"
"v_exp_f32 v224, v224\n"
"v_mfma_f32_16x16x32_bf16 v[48:51], v[40:43], v[20:23], v[48:51]\n"
"ds_read_b64_tr_b16 a[252:253], v245 offset:58112\n"
"v_add_f32 v52, v52, v220\n"
"v_mfma_f32_16x16x32_bf16 v[228:231], a[128:131], v[112:115], v[36:39]\n"
"v_exp_f32 v225, v225\n"
"v_mfma_f32_16x16x32_bf16 v[228:231], a[132:135], v[116:119], v[228:231]\n"
"ds_read_b64_tr_b16 a[254:255], v245 offset:62208\n"
"v_cvt_pk_bf16_f32 v18, v224, v225\n"
"v_mfma_f32_16x16x32_bf16 v[228:231], a[136:139], v[120:123], v[228:231]\n"
"v_exp_f32 v226, v226\n"
"v_mfma_f32_16x16x32_bf16 v[228:231], a[140:143], v[124:127], v[228:231]\n"
"buffer_load_dwordx4 v[168:171], v252, s[68:71], s49 offen\n"
"v_add_f32 v52, v52, v221\n"
"v_add_f32 v52, v52, v222\n"
"v_mfma_f32_16x16x32_bf16 v[232:235], a[144:147], v[112:115], v[36:39]\n"
"s_mul_i32 s60, s59, 3\n"
"v_exp_f32 v227, v227\n"
"v_mfma_f32_16x16x32_bf16 v[232:235], a[148:151], v[116:119], v[232:235]\n"
"s_sub_u32 s58, s58, s60\n"
"v_cvt_pk_bf16_f32 v19, v226, v227\n"
"v_add_f32 v52, v52, v223\n"
"v_mfma_f32_16x16x32_bf16 v[232:235], a[152:155], v[120:123], v[232:235]\n"
"s_add_u32 s58, s58, s56\n"
"buffer_load_dwordx4 v247, s[12:15], s84 offen offset:3072 lds\n"
"v_mfma_f32_16x16x32_bf16 v[232:235], a[156:159], v[124:127], v[232:235]\n"
"s_add_u32 s84, s84, 16384\n"
"v_exp_f32 v228, v228\n"
"v_mfma_f32_16x16x32_bf16 a[64:67], a[192:195], v[16:19], a[64:67]\n"
"s_add_u32 s58, s58, 1\n"
"v_add_f32 v52, v52, v224\n"
"v_add_f32 v52, v52, v225\n"
"v_mfma_f32_16x16x32_bf16 a[68:71], a[196:199], v[16:19], a[68:71]\n"
"s_and_b32 s58, s58, 3\n"
"v_exp_f32 v229, v229\n"
"v_mfma_f32_16x16x32_bf16 a[72:75], a[200:203], v[16:19], a[72:75]\n"
"s_lshl_b32 s58, s58, 3\n"
"v_cvt_pk_bf16_f32 v20, v228, v229\n"
"v_add_f32 v52, v52, v226\n"
"v_mfma_f32_16x16x32_bf16 a[76:79], a[204:207], v[16:19], a[76:79]\n"
"s_add_u32 s58, s58, s59\n"
"v_exp_f32 v230, v230\n"
"v_mfma_f32_16x16x32_bf16 a[80:83], a[208:211], v[16:19], a[80:83]\n"
"s_mul_i32 s58, s58, 36864\n"
"v_add_f32 v52, v52, v227\n"
"v_mfma_f32_16x16x32_bf16 a[84:87], a[212:215], v[16:19], a[84:87]\n"
"s_add_u32 s22, s58, s57\n"
"v_exp_f32 v231, v231\n"
"v_mfma_f32_16x16x32_bf16 a[88:91], a[216:219], v[16:19], a[88:91]\n"
"s_waitcnt vmcnt(20) lgkmcnt(0)\n"
"s_barrier\n"
"v_cvt_pk_bf16_f32 v21, v230, v231\n"
"v_mfma_f32_16x16x32_bf16 a[92:95], a[220:223], v[16:19], a[92:95]\n"
"s_add_u32 s58, s41, 4\n"
"v_exp_f32 v232, v232\n"
"v_mfma_f32_16x16x32_bf16 v[0:3], a[160:163], v[64:67], v[24:27]\n"
"s_mul_i32 s59, s58, 43\n"
"ds_read_b128 a[128:131], v240 offset:0\n"
"v_mfma_f32_16x16x32_bf16 v[0:3], a[164:167], v[68:71], v[0:3]\n"
"s_lshr_b32 s59, s59, 7\n"
"v_exp_f32 v233, v233\n"
"v_mfma_f32_16x16x32_bf16 v[0:3], a[168:171], v[72:75], v[0:3]\n"
"s_mul_i32 s60, s59, 3\n"
"ds_read_b128 a[132:135], v240 offset:256\n"
"v_cvt_pk_bf16_f32 v22, v232, v233\n"
"v_mfma_f32_16x16x32_bf16 v[0:3], a[172:175], v[76:79], v[0:3]\n"
"s_sub_u32 s58, s58, s60\n"
"v_exp_f32 v234, v234\n"
"v_mfma_f32_16x16x32_bf16 v[4:7], a[176:179], v[64:67], v[24:27]\n"
"s_add_u32 s58, s58, s56\n"
"ds_read_b128 a[136:139], v240 offset:512\n"
"v_mfma_f32_16x16x32_bf16 v[4:7], a[180:183], v[68:71], v[4:7]\n"
"s_add_u32 s58, s58, 1\n"
"v_exp_f32 v235, v235\n"
"v_mfma_f32_16x16x32_bf16 v[4:7], a[184:187], v[72:75], v[4:7]\n"
"s_add_u32 s49, s49, 285696\n"
"ds_read_b128 a[140:143], v240 offset:768\n"
"v_cvt_pk_bf16_f32 v23, v234, v235\n"
"v_mfma_f32_16x16x32_bf16 v[4:7], a[188:191], v[76:79], v[4:7]\n"
"s_add_u32 m0, s86, 98304\n"
"v_exp_f32 v0, v0\n"
"v_mfma_f32_16x16x32_bf16 a[96:99], a[192:195], v[20:23], a[96:99]\n"
"buffer_load_dwordx4 v246, s[8:11], s84 offen offset:0 lds\n"
"v_mfma_f32_16x16x32_bf16 a[100:103], a[196:199], v[20:23], a[100:103]\n"
"v_exp_f32 v1, v1\n"
"v_mfma_f32_16x16x32_bf16 a[104:107], a[200:203], v[20:23], a[104:107]\n"
"ds_read_b128 a[144:147], v240 offset:4096\n"
"v_cvt_pk_bf16_f32 v16, v0, v1\n"
"v_mfma_f32_16x16x32_bf16 a[108:111], a[204:207], v[20:23], a[108:111]\n"
"v_exp_f32 v2, v2\n"
"v_mfma_f32_16x16x32_bf16 a[112:115], a[208:211], v[20:23], a[112:115]\n"
"ds_read_b128 a[148:151], v240 offset:4352\n"
"buffer_load_dwordx4 v[136:139], v252, s[64:67], s49 offen\n"
"v_add_f32 v44, v44, v0\n"
"v_mfma_f32_16x16x32_bf16 a[116:119], a[212:215], v[20:23], a[116:119]\n"
"s_and_b32 s58, s58, 3\n"
"v_exp_f32 v3, v3\n"
"v_mfma_f32_16x16x32_bf16 a[120:123], a[216:219], v[20:23], a[120:123]\n"
"s_lshl_b32 s58, s58, 3\n"
"ds_read_b128 a[152:155], v240 offset:4608\n"
"v_cvt_pk_bf16_f32 v17, v2, v3\n"
"v_mfma_f32_16x16x32_bf16 a[124:127], a[220:223], v[20:23], a[124:127]\n"
"s_add_u32 s58, s58, s59\n"
"v_exp_f32 v4, v4\n"
"v_mfma_f32_16x16x32_bf16 v[56:59], v[40:43], v[20:23], v[56:59]\n"
"s_mul_i32 s58, s58, 36864\n"
"ds_read_b128 a[156:159], v240 offset:4864\n"
"v_add_f32 v44, v44, v1\n"
"v_mfma_f32_16x16x32_bf16 v[8:11], a[160:163], v[80:83], v[28:31]\n"
"s_add_u32 s23, s58, s57\n"
"v_exp_f32 v5, v5\n"
"v_mfma_f32_16x16x32_bf16 v[8:11], a[164:167], v[84:87], v[8:11]\n"
"s_add_u32 s58, s41, 8\n"
"ds_read_b64_tr_b16 a[192:193], v242 offset:16384\n"
"v_cvt_pk_bf16_f32 v18, v4, v5\n"
"v_mfma_f32_16x16x32_bf16 v[8:11], a[168:171], v[88:91], v[8:11]\n"
"s_mul_i32 s59, s58, 43\n"
"v_exp_f32 v6, v6\n"
"v_mfma_f32_16x16x32_bf16 v[8:11], a[172:175], v[92:95], v[8:11]\n"
"s_lshr_b32 s59, s59, 7\n"
"ds_read_b64_tr_b16 a[194:195], v242 offset:20480\n"
"v_add_f32 v44, v44, v2\n"
"v_mfma_f32_16x16x32_bf16 v[12:15], a[176:179], v[80:83], v[28:31]\n"
"s_mul_i32 s60, s59, 3\n"
"v_exp_f32 v7, v7\n"
"v_mfma_f32_16x16x32_bf16 v[12:15], a[180:183], v[84:87], v[12:15]\n"
"s_sub_u32 s58, s58, s60\n"
"ds_read_b64_tr_b16 a[196:197], v243 offset:16384\n"
"v_cvt_pk_bf16_f32 v19, v6, v7\n"
"v_mfma_f32_16x16x32_bf16 v[12:15], a[184:187], v[88:91], v[12:15]\n"
"s_add_u32 s58, s58, s56\n"
"buffer_load_dwordx4 v246, s[8:11], s84 offen offset:1024 lds\n"
"v_mfma_f32_16x16x32_bf16 v[12:15], a[188:191], v[92:95], v[12:15]\n"
"s_waitcnt lgkmcnt(11)\n"
"v_exp_f32 v8, v8\n"
"v_mfma_f32_16x16x32_bf16 a[0:3], a[224:227], v[16:19], a[0:3]\n"
"s_add_u32 s58, s58, 1\n"
"ds_read_b64_tr_b16 a[198:199], v243 offset:20480\n"
"v_add_f32 v44, v44, v3\n"
"v_mfma_f32_16x16x32_bf16 a[4:7], a[228:231], v[16:19], a[4:7]\n"
"s_and_b32 s58, s58, 3\n"
"v_exp_f32 v9, v9\n"
"v_mfma_f32_16x16x32_bf16 a[8:11], a[232:235], v[16:19], a[8:11]\n"
"s_lshl_b32 s58, s58, 3\n"
"ds_read_b64_tr_b16 a[200:201], v242 offset:16640\n"
"v_cvt_pk_bf16_f32 v20, v8, v9\n"
"v_mfma_f32_16x16x32_bf16 a[12:15], a[236:239], v[16:19], a[12:15]\n"
"s_add_u32 s58, s58, s59\n"
"v_exp_f32 v10, v10\n"
"v_mfma_f32_16x16x32_bf16 a[16:19], a[240:243], v[16:19], a[16:19]\n"
"s_mul_i32 s58, s58, 36864\n"
"ds_read_b64_tr_b16 a[202:203], v242 offset:20736\n"
"v_add_f32 v44, v44, v4\n"
"v_mfma_f32_16x16x32_bf16 a[20:23], a[244:247], v[16:19], a[20:23]\n"
"v_exp_f32 v11, v11\n"
"v_mfma_f32_16x16x32_bf16 a[24:27], a[248:251], v[16:19], a[24:27]\n"
"ds_read_b64_tr_b16 a[204:205], v243 offset:16640\n"
"v_cvt_pk_bf16_f32 v21, v10, v11\n"
"v_mfma_f32_16x16x32_bf16 a[28:31], a[252:255], v[16:19], a[28:31]\n"
"v_exp_f32 v12, v12\n"
"v_mfma_f32_16x16x32_bf16 v[220:223], a[160:163], v[96:99], v[32:35]\n"
"ds_read_b64_tr_b16 a[206:207], v243 offset:20736\n"
"v_add_f32 v44, v44, v5\n"
"v_mfma_f32_16x16x32_bf16 v[220:223], a[164:167], v[100:103], v[220:223]\n"
"v_exp_f32 v13, v13\n"
"v_mfma_f32_16x16x32_bf16 v[220:223], a[168:171], v[104:107], v[220:223]\n"
"ds_read_b64_tr_b16 a[208:209], v242 offset:16896\n"
"v_cvt_pk_bf16_f32 v22, v12, v13\n"
"v_mfma_f32_16x16x32_bf16 v[220:223], a[172:175], v[108:111], v[220:223]\n"
"v_exp_f32 v14, v14\n"
"v_mfma_f32_16x16x32_bf16 v[224:227], a[176:179], v[96:99], v[32:35]\n"
"ds_read_b64_tr_b16 a[210:211], v242 offset:20992\n"
"buffer_load_dwordx4 v[172:175], v252, s[68:71], s49 offen\n"
"v_add_f32 v44, v44, v6\n"
"v_mfma_f32_16x16x32_bf16 v[224:227], a[180:183], v[100:103], v[224:227]\n"
"s_add_u32 s36, s58, s57\n"
"v_exp_f32 v15, v15\n"
"v_mfma_f32_16x16x32_bf16 v[224:227], a[184:187], v[104:107], v[224:227]\n"
"s_add_u32 s58, s41, 12\n"
"ds_read_b64_tr_b16 a[212:213], v243 offset:16896\n"
"v_cvt_pk_bf16_f32 v23, v14, v15\n"
"v_mfma_f32_16x16x32_bf16 v[224:227], a[188:191], v[108:111], v[224:227]\n"
"s_mul_i32 s59, s58, 43\n"
"v_exp_f32 v220, v220\n"
"v_mfma_f32_16x16x32_bf16 a[32:35], a[224:227], v[20:23], a[32:35]\n"
"s_lshr_b32 s59, s59, 7\n"
"buffer_load_dwordx4 v247, s[8:11], s84 offen offset:2048 lds\n"
"v_mfma_f32_16x16x32_bf16 a[36:39], a[228:231], v[20:23], a[36:39]\n"
"s_mul_i32 s60, s59, 3\n"
"v_exp_f32 v221, v221\n"
"v_mfma_f32_16x16x32_bf16 a[40:43], a[232:235], v[20:23], a[40:43]\n"
"s_sub_u32 s58, s58, s60\n"
"ds_read_b64_tr_b16 a[214:215], v243 offset:20992\n"
"v_cvt_pk_bf16_f32 v16, v220, v221\n"
"v_mfma_f32_16x16x32_bf16 a[44:47], a[236:239], v[20:23], a[44:47]\n"
"s_add_u32 s58, s58, s56\n"
"v_exp_f32 v222, v222\n"
"v_mfma_f32_16x16x32_bf16 a[48:51], a[240:243], v[20:23], a[48:51]\n"
"s_add_u32 s58, s58, 1\n"
"ds_read_b64_tr_b16 a[216:217], v242 offset:17152\n"
"v_add_f32 v44, v44, v7\n"
"v_mfma_f32_16x16x32_bf16 a[52:55], a[244:247], v[20:23], a[52:55]\n"
"s_and_b32 s58, s58, 3\n"
"v_exp_f32 v223, v223\n"
"v_mfma_f32_16x16x32_bf16 a[56:59], a[248:251], v[20:23], a[56:59]\n"
"s_lshl_b32 s58, s58, 3\n"
"ds_read_b64_tr_b16 a[218:219], v242 offset:21248\n"
"v_cvt_pk_bf16_f32 v17, v222, v223\n"
"v_mfma_f32_16x16x32_bf16 a[60:63], a[252:255], v[20:23], a[60:63]\n"
"s_add_u32 s58, s58, s59\n"
"v_exp_f32 v224, v224\n"
"v_mfma_f32_16x16x32_bf16 v[48:51], v[40:43], v[20:23], v[48:51]\n"
"s_mul_i32 s58, s58, 36864\n"
"ds_read_b64_tr_b16 a[220:221], v243 offset:17152\n"
"v_add_f32 v52, v52, v220\n"
"v_mfma_f32_16x16x32_bf16 v[228:231], a[160:163], v[112:115], v[36:39]\n"
"s_add_u32 s37, s58, s57\n"
"v_exp_f32 v225, v225\n"
"v_mfma_f32_16x16x32_bf16 v[228:231], a[164:167], v[116:119], v[228:231]\n"
"s_add_u32 s58, s41, 16\n"
"ds_read_b64_tr_b16 a[222:223], v243 offset:21248\n"
"v_cvt_pk_bf16_f32 v18, v224, v225\n"
"v_mfma_f32_16x16x32_bf16 v[228:231], a[168:171], v[120:123], v[228:231]\n"
"s_mul_i32 s59, s58, 43\n"
"v_exp_f32 v226, v226\n"
"v_mfma_f32_16x16x32_bf16 v[228:231], a[172:175], v[124:127], v[228:231]\n"
"s_lshr_b32 s59, s59, 7\n"
"v_add_f32 v52, v52, v221\n"
"v_add_f32 v52, v52, v222\n"
"v_mfma_f32_16x16x32_bf16 v[232:235], a[176:179], v[112:115], v[36:39]\n"
"s_add_u32 s49, s49, 9216\n"
"v_exp_f32 v227, v227\n"
"v_mfma_f32_16x16x32_bf16 v[232:235], a[180:183], v[116:119], v[232:235]\n"
"v_cvt_pk_bf16_f32 v19, v226, v227\n"
"v_add_f32 v52, v52, v223\n"
"v_mfma_f32_16x16x32_bf16 v[232:235], a[184:187], v[120:123], v[232:235]\n"
"buffer_load_dwordx4 v247, s[8:11], s84 offen offset:3072 lds\n"
"v_mfma_f32_16x16x32_bf16 v[232:235], a[188:191], v[124:127], v[232:235]\n"
"v_exp_f32 v228, v228\n"
"v_mfma_f32_16x16x32_bf16 a[64:67], a[224:227], v[16:19], a[64:67]\n"
"v_add_f32 v52, v52, v224\n"
"v_add_f32 v52, v52, v225\n"
"v_mfma_f32_16x16x32_bf16 a[68:71], a[228:231], v[16:19], a[68:71]\n"
"v_exp_f32 v229, v229\n"
"v_mfma_f32_16x16x32_bf16 a[72:75], a[232:235], v[16:19], a[72:75]\n"
"v_cvt_pk_bf16_f32 v20, v228, v229\n"
"v_add_f32 v52, v52, v226\n"
"v_mfma_f32_16x16x32_bf16 a[76:79], a[236:239], v[16:19], a[76:79]\n"
"v_exp_f32 v230, v230\n"
"v_mfma_f32_16x16x32_bf16 a[80:83], a[240:243], v[16:19], a[80:83]\n"
"buffer_load_dwordx4 v[140:143], v252, s[64:67], s49 offen\n"
"v_add_f32 v52, v52, v227\n"
"v_mfma_f32_16x16x32_bf16 a[84:87], a[244:247], v[16:19], a[84:87]\n"
"s_mul_i32 s60, s59, 3\n"
"v_exp_f32 v231, v231\n"
"v_mfma_f32_16x16x32_bf16 a[88:91], a[248:251], v[16:19], a[88:91]\n"
"s_waitcnt lgkmcnt(8)\n"
"v_cvt_pk_bf16_f32 v21, v230, v231\n"
"v_mfma_f32_16x16x32_bf16 a[92:95], a[252:255], v[16:19], a[92:95]\n"
"s_sub_u32 s58, s58, s60\n"
"v_exp_f32 v232, v232\n"
"v_mfma_f32_16x16x32_bf16 v[0:3], a[128:131], v[64:67], v[24:27]\n"
"s_add_u32 s58, s58, s56\n"
"ds_read_b128 a[160:163], v240 offset:8192\n"
"v_mfma_f32_16x16x32_bf16 v[0:3], a[132:135], v[68:71], v[0:3]\n"
"s_add_u32 s58, s58, 1\n"
"v_exp_f32 v233, v233\n"
"v_mfma_f32_16x16x32_bf16 v[0:3], a[136:139], v[72:75], v[0:3]\n"
"s_and_b32 s58, s58, 3\n"
"ds_read_b128 a[164:167], v240 offset:8448\n"
"v_cvt_pk_bf16_f32 v22, v232, v233\n"
"v_mfma_f32_16x16x32_bf16 v[0:3], a[140:143], v[76:79], v[0:3]\n"
"s_lshl_b32 s58, s58, 3\n"
"v_exp_f32 v234, v234\n"
"v_mfma_f32_16x16x32_bf16 v[4:7], a[144:147], v[64:67], v[24:27]\n"
"s_add_u32 s58, s58, s59\n"
"ds_read_b128 a[168:171], v240 offset:8704\n"
"v_mfma_f32_16x16x32_bf16 v[4:7], a[148:151], v[68:71], v[4:7]\n"
"s_mul_i32 s58, s58, 36864\n"
"v_exp_f32 v235, v235\n"
"v_mfma_f32_16x16x32_bf16 v[4:7], a[152:155], v[72:75], v[4:7]\n"
"s_add_u32 s38, s58, s57\n"
"ds_read_b128 a[172:175], v240 offset:8960\n"
"v_cvt_pk_bf16_f32 v23, v234, v235\n"
"v_mfma_f32_16x16x32_bf16 v[4:7], a[156:159], v[76:79], v[4:7]\n"
"s_add_u32 m0, s86, 114688\n"
"v_exp_f32 v0, v0\n"
"v_mfma_f32_16x16x32_bf16 a[96:99], a[224:227], v[20:23], a[96:99]\n"
"s_add_u32 s58, s41, 20\n"
"buffer_load_dwordx4 v246, s[12:15], s84 offen offset:0 lds\n"
"v_mfma_f32_16x16x32_bf16 a[100:103], a[228:231], v[20:23], a[100:103]\n"
"s_mul_i32 s59, s58, 43\n"
"v_exp_f32 v1, v1\n"
"v_mfma_f32_16x16x32_bf16 a[104:107], a[232:235], v[20:23], a[104:107]\n"
"s_lshr_b32 s59, s59, 7\n"
"ds_read_b128 a[176:179], v240 offset:12288\n"
"v_cvt_pk_bf16_f32 v16, v0, v1\n"
"v_mfma_f32_16x16x32_bf16 a[108:111], a[236:239], v[20:23], a[108:111]\n"
"s_mul_i32 s60, s59, 3\n"
"v_exp_f32 v2, v2\n"
"v_mfma_f32_16x16x32_bf16 a[112:115], a[240:243], v[20:23], a[112:115]\n"
"s_sub_u32 s58, s58, s60\n"
"ds_read_b128 a[180:183], v240 offset:12544\n"
"v_add_f32 v44, v44, v0\n"
"v_mfma_f32_16x16x32_bf16 a[116:119], a[244:247], v[20:23], a[116:119]\n"
"s_add_u32 s58, s58, s56\n"
"v_exp_f32 v3, v3\n"
"v_mfma_f32_16x16x32_bf16 a[120:123], a[248:251], v[20:23], a[120:123]\n"
"s_add_u32 s58, s58, 1\n"
"ds_read_b128 a[184:187], v240 offset:12800\n"
"v_cvt_pk_bf16_f32 v17, v2, v3\n"
"v_mfma_f32_16x16x32_bf16 a[124:127], a[252:255], v[20:23], a[124:127]\n"
"v_exp_f32 v4, v4\n"
"v_mfma_f32_16x16x32_bf16 v[56:59], v[40:43], v[20:23], v[56:59]\n"
"ds_read_b128 a[188:191], v240 offset:13056\n"
"v_add_f32 v44, v44, v1\n"
"v_mfma_f32_16x16x32_bf16 v[8:11], a[128:131], v[80:83], v[28:31]\n"
"v_exp_f32 v5, v5\n"
"v_mfma_f32_16x16x32_bf16 v[8:11], a[132:135], v[84:87], v[8:11]\n"
"ds_read_b64_tr_b16 a[224:225], v242 offset:24576\n"
"v_cvt_pk_bf16_f32 v18, v4, v5\n"
"v_mfma_f32_16x16x32_bf16 v[8:11], a[136:139], v[88:91], v[8:11]\n"
"v_exp_f32 v6, v6\n"
"v_mfma_f32_16x16x32_bf16 v[8:11], a[140:143], v[92:95], v[8:11]\n"
"ds_read_b64_tr_b16 a[226:227], v242 offset:28672\n"
"buffer_load_dwordx4 v[176:179], v252, s[68:71], s49 offen\n"
"v_add_f32 v44, v44, v2\n"
"v_mfma_f32_16x16x32_bf16 v[12:15], a[144:147], v[80:83], v[28:31]\n"
"s_and_b32 s58, s58, 3\n"
"v_exp_f32 v7, v7\n"
"v_mfma_f32_16x16x32_bf16 v[12:15], a[148:151], v[84:87], v[12:15]\n"
"s_lshl_b32 s58, s58, 3\n"
"ds_read_b64_tr_b16 a[228:229], v243 offset:24576\n"
"v_cvt_pk_bf16_f32 v19, v6, v7\n"
"v_mfma_f32_16x16x32_bf16 v[12:15], a[152:155], v[88:91], v[12:15]\n"
"s_add_u32 s58, s58, s59\n"
"buffer_load_dwordx4 v247, s[12:15], s84 offen offset:1024 lds\n"
"v_mfma_f32_16x16x32_bf16 v[12:15], a[156:159], v[92:95], v[12:15]\n"
"s_waitcnt lgkmcnt(11)\n"
"v_exp_f32 v8, v8\n"
"v_mfma_f32_16x16x32_bf16 a[0:3], a[192:195], v[16:19], a[0:3]\n"
"s_mul_i32 s58, s58, 36864\n"
"ds_read_b64_tr_b16 a[230:231], v243 offset:28672\n"
"v_add_f32 v44, v44, v3\n"
"v_mfma_f32_16x16x32_bf16 a[4:7], a[196:199], v[16:19], a[4:7]\n"
"s_add_u32 s39, s58, s57\n"
"v_exp_f32 v9, v9\n"
"v_mfma_f32_16x16x32_bf16 a[8:11], a[200:203], v[16:19], a[8:11]\n"
"s_mul_i32 s58, s41, 9216\n"
"ds_read_b64_tr_b16 a[232:233], v242 offset:24832\n"
"v_cvt_pk_bf16_f32 v20, v8, v9\n"
"v_mfma_f32_16x16x32_bf16 a[12:15], a[204:207], v[16:19], a[12:15]\n"
"s_add_u32 s58, s58, s40\n"
"v_exp_f32 v10, v10\n"
"v_mfma_f32_16x16x32_bf16 a[16:19], a[208:211], v[16:19], a[16:19]\n"
"s_sub_u32 s53, s58, 27648\n"
"ds_read_b64_tr_b16 a[234:235], v242 offset:28928\n"
"v_add_f32 v44, v44, v4\n"
"v_mfma_f32_16x16x32_bf16 a[20:23], a[212:215], v[16:19], a[20:23]\n"
"s_add_u32 s58, s58, 131072\n"
"v_exp_f32 v11, v11\n"
"v_mfma_f32_16x16x32_bf16 a[24:27], a[216:219], v[16:19], a[24:27]\n"
"s_cmp_lt_u32 s41, 3\n"
"s_cselect_b32 s51, s58, s40\n"
"ds_read_b64_tr_b16 a[236:237], v243 offset:24832\n"
"v_cvt_pk_bf16_f32 v21, v10, v11\n"
"v_mfma_f32_16x16x32_bf16 a[28:31], a[220:223], v[16:19], a[28:31]\n"
"s_nop 0\n"
"v_exp_f32 v12, v12\n"
"v_mfma_f32_16x16x32_bf16 v[220:223], a[128:131], v[96:99], v[32:35]\n"
"s_and_b32 s58, s56, 1\n"
"ds_read_b64_tr_b16 a[238:239], v243 offset:28928\n"
"v_add_f32 v44, v44, v5\n"
"v_mfma_f32_16x16x32_bf16 v[220:223], a[132:135], v[100:103], v[220:223]\n"
"s_lshl_b32 s59, s58, 5\n"
"v_exp_f32 v13, v13\n"
"v_mfma_f32_16x16x32_bf16 v[220:223], a[136:139], v[104:107], v[220:223]\n"
"s_add_u32 s59, s59, s40\n"
"ds_read_b64_tr_b16 a[240:241], v242 offset:25088\n"
"v_cvt_pk_bf16_f32 v22, v12, v13\n"
"v_mfma_f32_16x16x32_bf16 v[220:223], a[140:143], v[108:111], v[220:223]\n"
"s_xor_b32 s60, s58, 1\n"
"v_exp_f32 v14, v14\n"
"v_mfma_f32_16x16x32_bf16 v[224:227], a[144:147], v[96:99], v[32:35]\n"
"s_lshl_b32 s60, s60, 8\n"
"ds_read_b64_tr_b16 a[242:243], v242 offset:29184\n"
"v_add_f32 v44, v44, v6\n"
"v_mfma_f32_16x16x32_bf16 v[224:227], a[148:151], v[100:103], v[224:227]\n"
"s_add_u32 s49, s49, 285696\n"
"v_exp_f32 v15, v15\n"
"v_mfma_f32_16x16x32_bf16 v[224:227], a[152:155], v[104:107], v[224:227]\n"
"ds_read_b64_tr_b16 a[244:245], v243 offset:25088\n"
"v_cvt_pk_bf16_f32 v23, v14, v15\n"
"v_mfma_f32_16x16x32_bf16 v[224:227], a[156:159], v[108:111], v[224:227]\n"
"v_exp_f32 v220, v220\n"
"v_mfma_f32_16x16x32_bf16 a[32:35], a[192:195], v[20:23], a[32:35]\n"
"buffer_load_dwordx4 v246, s[12:15], s84 offen offset:2048 lds\n"
"v_mfma_f32_16x16x32_bf16 a[36:39], a[196:199], v[20:23], a[36:39]\n"
"v_exp_f32 v221, v221\n"
"v_mfma_f32_16x16x32_bf16 a[40:43], a[200:203], v[20:23], a[40:43]\n"
"ds_read_b64_tr_b16 a[246:247], v243 offset:29184\n"
"v_cvt_pk_bf16_f32 v16, v220, v221\n"
"v_mfma_f32_16x16x32_bf16 a[44:47], a[204:207], v[20:23], a[44:47]\n"
"v_exp_f32 v222, v222\n"
"v_mfma_f32_16x16x32_bf16 a[48:51], a[208:211], v[20:23], a[48:51]\n"
"ds_read_b64_tr_b16 a[248:249], v242 offset:25344\n"
"buffer_load_dwordx4 v[144:147], v252, s[64:67], s49 offen\n"
"v_add_f32 v44, v44, v7\n"
"v_mfma_f32_16x16x32_bf16 a[52:55], a[212:215], v[20:23], a[52:55]\n"
"s_add_u32 s60, s60, s40\n"
"v_exp_f32 v223, v223\n"
"v_mfma_f32_16x16x32_bf16 a[56:59], a[216:219], v[20:23], a[56:59]\n"
"ds_read_b64_tr_b16 a[250:251], v242 offset:29440\n"
"v_cvt_pk_bf16_f32 v17, v222, v223\n"
"v_mfma_f32_16x16x32_bf16 a[60:63], a[220:223], v[20:23], a[60:63]\n"
"v_exp_f32 v224, v224\n"
"v_mfma_f32_16x16x32_bf16 v[48:51], v[40:43], v[20:23], v[48:51]\n"
"ds_read_b64_tr_b16 a[252:253], v243 offset:25344\n"
"v_add_f32 v52, v52, v220\n"
"v_mfma_f32_16x16x32_bf16 v[228:231], a[128:131], v[112:115], v[36:39]\n"
"v_exp_f32 v225, v225\n"
"v_mfma_f32_16x16x32_bf16 v[228:231], a[132:135], v[116:119], v[228:231]\n"
"ds_read_b64_tr_b16 a[254:255], v243 offset:29440\n"
"v_cvt_pk_bf16_f32 v18, v224, v225\n"
"v_mfma_f32_16x16x32_bf16 v[228:231], a[136:139], v[120:123], v[228:231]\n"
"v_exp_f32 v226, v226\n"
"v_mfma_f32_16x16x32_bf16 v[228:231], a[140:143], v[124:127], v[228:231]\n"
"v_add_f32 v52, v52, v221\n"
"v_add_f32 v52, v52, v222\n"
"v_mfma_f32_16x16x32_bf16 v[232:235], a[144:147], v[112:115], v[36:39]\n"
"v_exp_f32 v227, v227\n"
"v_mfma_f32_16x16x32_bf16 v[232:235], a[148:151], v[116:119], v[232:235]\n"
"v_cvt_pk_bf16_f32 v19, v226, v227\n"
"v_add_f32 v52, v52, v223\n"
"v_mfma_f32_16x16x32_bf16 v[232:235], a[152:155], v[120:123], v[232:235]\n"
"buffer_load_dwordx4 v247, s[12:15], s84 offen offset:3072 lds\n"
"v_lshrrev_b32 v217, 2, v248\n"
"v_mfma_f32_16x16x32_bf16 v[232:235], a[156:159], v[124:127], v[232:235]\n"
"s_add_u32 s84, s84, 16384\n"
"v_exp_f32 v228, v228\n"
"v_mfma_f32_16x16x32_bf16 a[64:67], a[192:195], v[16:19], a[64:67]\n"
"v_add_f32 v52, v52, v224\n"
"v_add_f32 v52, v52, v225\n"
"v_mfma_f32_16x16x32_bf16 a[68:71], a[196:199], v[16:19], a[68:71]\n"
"v_exp_f32 v229, v229\n"
"v_mfma_f32_16x16x32_bf16 a[72:75], a[200:203], v[16:19], a[72:75]\n"
"v_cvt_pk_bf16_f32 v20, v228, v229\n"
"v_add_f32 v52, v52, v226\n"
"v_mfma_f32_16x16x32_bf16 a[76:79], a[204:207], v[16:19], a[76:79]\n"
"v_exp_f32 v230, v230\n"
"v_mfma_f32_16x16x32_bf16 a[80:83], a[208:211], v[16:19], a[80:83]\n"
"v_add_f32 v52, v52, v227\n"
"v_mul_u32_u24 v203, 288, v217\n"
"v_mfma_f32_16x16x32_bf16 a[84:87], a[212:215], v[16:19], a[84:87]\n"
"v_exp_f32 v231, v231\n"
"v_mfma_f32_16x16x32_bf16 a[88:91], a[216:219], v[16:19], a[88:91]\n"
"s_waitcnt vmcnt(24) lgkmcnt(0)\n"
"s_barrier\n"
"v_cvt_pk_bf16_f32 v21, v230, v231\n"
"v_and_b32 v217, 3, v248\n"
"v_mfma_f32_16x16x32_bf16 a[92:95], a[220:223], v[16:19], a[92:95]\n"
"v_exp_f32 v232, v232\n"
"v_mfma_f32_16x16x32_bf16 v[0:3], a[160:163], v[64:67], v[24:27]\n"
"ds_read_b128 a[128:131], v240 offset:32768\n"
"v_lshl_add_u32 v203, v217, 3, v203\n"
"v_mfma_f32_16x16x32_bf16 v[0:3], a[164:167], v[68:71], v[0:3]\n"
"v_exp_f32 v233, v233\n"
"v_mfma_f32_16x16x32_bf16 v[0:3], a[168:171], v[72:75], v[0:3]\n"
"ds_read_b128 a[132:135], v240 offset:33024\n"
"v_cvt_pk_bf16_f32 v22, v232, v233\n"
"v_mfma_f32_16x16x32_bf16 v[0:3], a[172:175], v[76:79], v[0:3]\n"
"v_exp_f32 v234, v234\n"
"v_mfma_f32_16x16x32_bf16 v[4:7], a[176:179], v[64:67], v[24:27]\n"
"ds_read_b128 a[136:139], v240 offset:33280\n"
"v_add_u32 v200, s59, v203\n"
"v_mfma_f32_16x16x32_bf16 v[4:7], a[180:183], v[68:71], v[4:7]\n"
"v_exp_f32 v235, v235\n"
"v_mfma_f32_16x16x32_bf16 v[4:7], a[184:187], v[72:75], v[4:7]\n"
"ds_read_b128 a[140:143], v240 offset:33536\n"
"v_cvt_pk_bf16_f32 v23, v234, v235\n"
"v_mfma_f32_16x16x32_bf16 v[4:7], a[188:191], v[76:79], v[4:7]\n"
"s_mov_b32 m0, s51\n"
"v_exp_f32 v0, v0\n"
"v_mfma_f32_16x16x32_bf16 a[96:99], a[192:195], v[20:23], a[96:99]\n"
"buffer_load_dwordx4 v210, s[24:27], s20 offen offset:0 lds\n"
"v_add_u32 v203, s60, v203\n"
"v_mfma_f32_16x16x32_bf16 a[100:103], a[196:199], v[20:23], a[100:103]\n"
"v_exp_f32 v1, v1\n"
"v_mfma_f32_16x16x32_bf16 a[104:107], a[200:203], v[20:23], a[104:107]\n"
"ds_read_b128 a[144:147], v240 offset:36864\n"
"v_cvt_pk_bf16_f32 v16, v0, v1\n"
"v_mfma_f32_16x16x32_bf16 a[108:111], a[204:207], v[20:23], a[108:111]\n"
"v_exp_f32 v2, v2\n"
"v_mfma_f32_16x16x32_bf16 a[112:115], a[208:211], v[20:23], a[112:115]\n"
"ds_read_b128 a[148:151], v240 offset:37120\n"
"v_add_f32 v44, v44, v0\n"
"v_mfma_f32_16x16x32_bf16 a[116:119], a[212:215], v[20:23], a[116:119]\n"
"v_exp_f32 v3, v3\n"
"v_mfma_f32_16x16x32_bf16 a[120:123], a[216:219], v[20:23], a[120:123]\n"
"ds_read_b128 a[152:155], v240 offset:37376\n"
"v_cvt_pk_bf16_f32 v17, v2, v3\n"
"v_mfma_f32_16x16x32_bf16 a[124:127], a[220:223], v[20:23], a[124:127]\n"
"v_exp_f32 v4, v4\n"
"v_mfma_f32_16x16x32_bf16 v[56:59], v[40:43], v[20:23], v[56:59]\n"
"ds_read_b128 a[156:159], v240 offset:37632\n"
"v_add_f32 v44, v44, v1\n"
"v_mfma_f32_16x16x32_bf16 v[8:11], a[160:163], v[80:83], v[28:31]\n"
"v_exp_f32 v5, v5\n"
"v_mfma_f32_16x16x32_bf16 v[8:11], a[164:167], v[84:87], v[8:11]\n"
"ds_read_b64_tr_b16 a[192:193], v242 offset:49152\n"
"v_cvt_pk_bf16_f32 v18, v4, v5\n"
"v_mfma_f32_16x16x32_bf16 v[8:11], a[168:171], v[88:91], v[8:11]\n"
"v_exp_f32 v6, v6\n"
"v_mfma_f32_16x16x32_bf16 v[8:11], a[172:175], v[92:95], v[8:11]\n"
"ds_read_b64_tr_b16 a[194:195], v242 offset:53248\n"
"v_add_f32 v44, v44, v2\n"
"v_mfma_f32_16x16x32_bf16 v[12:15], a[176:179], v[80:83], v[28:31]\n"
"v_exp_f32 v7, v7\n"
"v_mfma_f32_16x16x32_bf16 v[12:15], a[180:183], v[84:87], v[12:15]\n"
"s_mov_b32 m0, s51\n"
"ds_read_b64_tr_b16 a[196:197], v243 offset:49152\n"
"v_cvt_pk_bf16_f32 v19, v6, v7\n"
"v_mfma_f32_16x16x32_bf16 v[12:15], a[184:187], v[88:91], v[12:15]\n"
"buffer_load_dwordx4 v211, s[24:27], s20 offen offset:1024 lds\n"
"v_add_u32 v201, 65536, v200\n"
"v_mfma_f32_16x16x32_bf16 v[12:15], a[188:191], v[92:95], v[12:15]\n"
"s_waitcnt lgkmcnt(11)\n"
"v_exp_f32 v8, v8\n"
"v_mfma_f32_16x16x32_bf16 a[0:3], a[224:227], v[16:19], a[0:3]\n"
"ds_read_b64_tr_b16 a[198:199], v243 offset:53248\n"
"v_add_f32 v44, v44, v3\n"
"v_mfma_f32_16x16x32_bf16 a[4:7], a[228:231], v[16:19], a[4:7]\n"
"v_exp_f32 v9, v9\n"
"v_mfma_f32_16x16x32_bf16 a[8:11], a[232:235], v[16:19], a[8:11]\n"
"ds_read_b64_tr_b16 a[200:201], v242 offset:49408\n"
"v_cvt_pk_bf16_f32 v20, v8, v9\n"
"v_mfma_f32_16x16x32_bf16 a[12:15], a[236:239], v[16:19], a[12:15]\n"
"v_exp_f32 v10, v10\n"
"v_mfma_f32_16x16x32_bf16 a[16:19], a[240:243], v[16:19], a[16:19]\n"
"ds_read_b64_tr_b16 a[202:203], v242 offset:53504\n"
"v_add_f32 v44, v44, v4\n"
"v_mfma_f32_16x16x32_bf16 a[20:23], a[244:247], v[16:19], a[20:23]\n"
"v_exp_f32 v11, v11\n"
"v_mfma_f32_16x16x32_bf16 a[24:27], a[248:251], v[16:19], a[24:27]\n"
"ds_read_b64_tr_b16 a[204:205], v243 offset:49408\n"
"v_cvt_pk_bf16_f32 v21, v10, v11\n"
"v_mfma_f32_16x16x32_bf16 a[28:31], a[252:255], v[16:19], a[28:31]\n"
"v_exp_f32 v12, v12\n"
"v_mfma_f32_16x16x32_bf16 v[220:223], a[160:163], v[96:99], v[32:35]\n"
"ds_read_b64_tr_b16 a[206:207], v243 offset:53504\n"
"v_add_f32 v44, v44, v5\n"
"v_mfma_f32_16x16x32_bf16 v[220:223], a[164:167], v[100:103], v[220:223]\n"
"v_exp_f32 v13, v13\n"
"v_mfma_f32_16x16x32_bf16 v[220:223], a[168:171], v[104:107], v[220:223]\n"
"ds_read_b64_tr_b16 a[208:209], v242 offset:49664\n"
"v_cvt_pk_bf16_f32 v22, v12, v13\n"
"v_mfma_f32_16x16x32_bf16 v[220:223], a[172:175], v[108:111], v[220:223]\n"
"v_exp_f32 v14, v14\n"
"v_mfma_f32_16x16x32_bf16 v[224:227], a[176:179], v[96:99], v[32:35]\n"
"ds_read_b64_tr_b16 a[210:211], v242 offset:53760\n"
"v_add_f32 v44, v44, v6\n"
"v_mfma_f32_16x16x32_bf16 v[224:227], a[180:183], v[100:103], v[224:227]\n"
"v_exp_f32 v15, v15\n"
"v_mfma_f32_16x16x32_bf16 v[224:227], a[184:187], v[104:107], v[224:227]\n"
"ds_read_b64_tr_b16 a[212:213], v243 offset:49664\n"
"v_cvt_pk_bf16_f32 v23, v14, v15\n"
"v_mfma_f32_16x16x32_bf16 v[224:227], a[188:191], v[108:111], v[224:227]\n"
"s_mov_b32 m0, s51\n"
"v_exp_f32 v220, v220\n"
"v_mfma_f32_16x16x32_bf16 a[32:35], a[224:227], v[20:23], a[32:35]\n"
"buffer_load_dwordx4 v212, s[24:27], s20 offen offset:2048 lds\n"
"v_add_u32 v204, 65536, v203\n"
"v_mfma_f32_16x16x32_bf16 a[36:39], a[228:231], v[20:23], a[36:39]\n"
"v_exp_f32 v221, v221\n"
"v_mfma_f32_16x16x32_bf16 a[40:43], a[232:235], v[20:23], a[40:43]\n"
"ds_read_b64_tr_b16 a[214:215], v243 offset:53760\n"
"v_cvt_pk_bf16_f32 v16, v220, v221\n"
"v_mfma_f32_16x16x32_bf16 a[44:47], a[236:239], v[20:23], a[44:47]\n"
"v_exp_f32 v222, v222\n"
"v_mfma_f32_16x16x32_bf16 a[48:51], a[240:243], v[20:23], a[48:51]\n"
"ds_read_b64_tr_b16 a[216:217], v242 offset:49920\n"
"v_add_f32 v44, v44, v7\n"
"v_mfma_f32_16x16x32_bf16 a[52:55], a[244:247], v[20:23], a[52:55]\n"
"v_exp_f32 v223, v223\n"
"v_mfma_f32_16x16x32_bf16 a[56:59], a[248:251], v[20:23], a[56:59]\n"
"ds_read_b64_tr_b16 a[218:219], v242 offset:54016\n"
"v_cvt_pk_bf16_f32 v17, v222, v223\n"
"v_mfma_f32_16x16x32_bf16 a[60:63], a[252:255], v[20:23], a[60:63]\n"
"v_exp_f32 v224, v224\n"
"v_mfma_f32_16x16x32_bf16 v[48:51], v[40:43], v[20:23], v[48:51]\n"
"ds_read_b64_tr_b16 a[220:221], v243 offset:49920\n"
"v_add_f32 v52, v52, v220\n"
"v_mfma_f32_16x16x32_bf16 v[228:231], a[160:163], v[112:115], v[36:39]\n"
"v_exp_f32 v225, v225\n"
"v_mfma_f32_16x16x32_bf16 v[228:231], a[164:167], v[116:119], v[228:231]\n"
"ds_read_b64_tr_b16 a[222:223], v243 offset:54016\n"
"v_cvt_pk_bf16_f32 v18, v224, v225\n"
"v_mfma_f32_16x16x32_bf16 v[228:231], a[168:171], v[120:123], v[228:231]\n"
"v_exp_f32 v226, v226\n"
"v_mfma_f32_16x16x32_bf16 v[228:231], a[172:175], v[124:127], v[228:231]\n"
"v_add_f32 v52, v52, v221\n"
"v_add_f32 v52, v52, v222\n"
"v_mfma_f32_16x16x32_bf16 v[232:235], a[176:179], v[112:115], v[36:39]\n"
"v_exp_f32 v227, v227\n"
"v_mfma_f32_16x16x32_bf16 v[232:235], a[180:183], v[116:119], v[232:235]\n"
"s_mov_b32 m0, s51\n"
"v_cvt_pk_bf16_f32 v19, v226, v227\n"
"v_add_f32 v52, v52, v223\n"
"v_mfma_f32_16x16x32_bf16 v[232:235], a[184:187], v[120:123], v[232:235]\n"
"buffer_load_dwordx4 v213, s[24:27], s20 offen offset:3072 lds\n"
"v_add_u32 v202, 131072, v200\n"
"v_mfma_f32_16x16x32_bf16 v[232:235], a[188:191], v[124:127], v[232:235]\n"
"v_exp_f32 v228, v228\n"
"v_mfma_f32_16x16x32_bf16 a[64:67], a[224:227], v[16:19], a[64:67]\n"
"v_add_f32 v52, v52, v224\n"
"v_add_f32 v52, v52, v225\n"
"v_mfma_f32_16x16x32_bf16 a[68:71], a[228:231], v[16:19], a[68:71]\n"
"v_exp_f32 v229, v229\n"
"v_mfma_f32_16x16x32_bf16 a[72:75], a[232:235], v[16:19], a[72:75]\n"
"v_cvt_pk_bf16_f32 v20, v228, v229\n"
"v_add_f32 v52, v52, v226\n"
"v_mfma_f32_16x16x32_bf16 a[76:79], a[236:239], v[16:19], a[76:79]\n"
"v_exp_f32 v230, v230\n"
"v_mfma_f32_16x16x32_bf16 a[80:83], a[240:243], v[16:19], a[80:83]\n"
"v_add_f32 v52, v52, v227\n"
"v_add_u32 v205, 131072, v203\n"
"v_mfma_f32_16x16x32_bf16 a[84:87], a[244:247], v[16:19], a[84:87]\n"
"s_add_u32 s62, s40, 163776\n"
"v_exp_f32 v231, v231\n"
"v_mfma_f32_16x16x32_bf16 a[88:91], a[248:251], v[16:19], a[88:91]\n"
"s_waitcnt lgkmcnt(8)\n"
"v_cvt_pk_bf16_f32 v21, v230, v231\n"
"v_lshl_add_u32 v209, v217, 2, s62\n"
"v_mfma_f32_16x16x32_bf16 a[92:95], a[252:255], v[16:19], a[92:95]\n"
"s_lshl_b32 s55, s44, 4\n"
"v_exp_f32 v232, v232\n"
"v_mfma_f32_16x16x32_bf16 v[0:3], a[128:131], v[64:67], v[24:27]\n"
"s_add_u32 s55, s55, s43\n"
"ds_read_b128 a[160:163], v240 offset:40960\n"
"v_mfma_f32_16x16x32_bf16 v[0:3], a[132:135], v[68:71], v[0:3]\n"
"s_lshl_b32 s52, s55, 2\n"
"v_exp_f32 v233, v233\n"
"v_mfma_f32_16x16x32_bf16 v[0:3], a[136:139], v[72:75], v[0:3]\n"
"ds_read_b128 a[164:167], v240 offset:41216\n"
"buffer_load_dwordx4 v[180:183], v252, s[68:71], s49 offen\n"
"v_cvt_pk_bf16_f32 v22, v232, v233\n"
"v_mfma_f32_16x16x32_bf16 v[0:3], a[140:143], v[76:79], v[0:3]\n"
"s_add_u32 s52, s52, s41\n"
"v_exp_f32 v234, v234\n"
"v_mfma_f32_16x16x32_bf16 v[4:7], a[144:147], v[64:67], v[24:27]\n"
"s_lshl_b32 s52, s52, 14\n"
"ds_read_b128 a[168:171], v240 offset:41472\n"
"v_mfma_f32_16x16x32_bf16 v[4:7], a[148:151], v[68:71], v[4:7]\n"
"s_add_u32 s52, s52, 9592832\n"
"v_exp_f32 v235, v235\n"
"v_mfma_f32_16x16x32_bf16 v[4:7], a[152:155], v[72:75], v[4:7]\n"
"s_add_u32 s49, s49, 9216\n"
"ds_read_b128 a[172:175], v240 offset:41728\n"
"v_cvt_pk_bf16_f32 v23, v234, v235\n"
"v_mfma_f32_16x16x32_bf16 v[4:7], a[156:159], v[76:79], v[4:7]\n"
"s_add_u32 m0, s51, 4096\n"
"v_exp_f32 v0, v0\n"
"v_mfma_f32_16x16x32_bf16 a[96:99], a[224:227], v[20:23], a[96:99]\n"
"buffer_load_dwordx4 v214, s[24:27], s20 offen offset:0 lds\n"
"v_mfma_f32_16x16x32_bf16 a[100:103], a[228:231], v[20:23], a[100:103]\n"
"v_exp_f32 v1, v1\n"
"v_mfma_f32_16x16x32_bf16 a[104:107], a[232:235], v[20:23], a[104:107]\n"
"ds_read_b128 a[176:179], v240 offset:45056\n"
"v_cvt_pk_bf16_f32 v16, v0, v1\n"
"v_mfma_f32_16x16x32_bf16 a[108:111], a[236:239], v[20:23], a[108:111]\n"
"v_exp_f32 v2, v2\n"
"v_mfma_f32_16x16x32_bf16 a[112:115], a[240:243], v[20:23], a[112:115]\n"
"ds_read_b128 a[180:183], v240 offset:45312\n"
"v_add_f32 v44, v44, v0\n"
"v_mfma_f32_16x16x32_bf16 a[116:119], a[244:247], v[20:23], a[116:119]\n"
"v_exp_f32 v3, v3\n"
"v_mfma_f32_16x16x32_bf16 a[120:123], a[248:251], v[20:23], a[120:123]\n"
"ds_read_b128 a[184:187], v240 offset:45568\n"
"v_cvt_pk_bf16_f32 v17, v2, v3\n"
"v_mfma_f32_16x16x32_bf16 a[124:127], a[252:255], v[20:23], a[124:127]\n"
"v_exp_f32 v4, v4\n"
"v_mfma_f32_16x16x32_bf16 v[56:59], v[40:43], v[20:23], v[56:59]\n"
"ds_read_b128 a[188:191], v240 offset:45824\n"
"v_add_f32 v44, v44, v1\n"
"v_mfma_f32_16x16x32_bf16 v[8:11], a[128:131], v[80:83], v[28:31]\n"
"v_exp_f32 v5, v5\n"
"v_mfma_f32_16x16x32_bf16 v[8:11], a[132:135], v[84:87], v[8:11]\n"
"ds_read_b64_tr_b16 a[224:225], v242 offset:57344\n"
"v_cvt_pk_bf16_f32 v18, v4, v5\n"
"v_mfma_f32_16x16x32_bf16 v[8:11], a[136:139], v[88:91], v[8:11]\n"
"v_exp_f32 v6, v6\n"
"v_mfma_f32_16x16x32_bf16 v[8:11], a[140:143], v[92:95], v[8:11]\n"
"ds_read_b64_tr_b16 a[226:227], v242 offset:61440\n"
"v_add_f32 v44, v44, v2\n"
"v_mfma_f32_16x16x32_bf16 v[12:15], a[144:147], v[80:83], v[28:31]\n"
"v_exp_f32 v7, v7\n"
"v_mfma_f32_16x16x32_bf16 v[12:15], a[148:151], v[84:87], v[12:15]\n"
"s_add_u32 m0, s51, 4096\n"
"ds_read_b64_tr_b16 a[228:229], v243 offset:57344\n"
"v_cvt_pk_bf16_f32 v19, v6, v7\n"
"v_mfma_f32_16x16x32_bf16 v[12:15], a[152:155], v[88:91], v[12:15]\n"
"buffer_load_dwordx4 v236, s[24:27], s20 offen offset:1024 lds\n"
"v_mfma_f32_16x16x32_bf16 v[12:15], a[156:159], v[92:95], v[12:15]\n"
"s_waitcnt lgkmcnt(11)\n"
"v_exp_f32 v8, v8\n"
"v_mfma_f32_16x16x32_bf16 a[0:3], a[192:195], v[16:19], a[0:3]\n"
"ds_read_b64_tr_b16 a[230:231], v243 offset:61440\n"
"v_add_f32 v44, v44, v3\n"
"v_mfma_f32_16x16x32_bf16 a[4:7], a[196:199], v[16:19], a[4:7]\n"
"v_exp_f32 v9, v9\n"
"v_mfma_f32_16x16x32_bf16 a[8:11], a[200:203], v[16:19], a[8:11]\n"
"ds_read_b64_tr_b16 a[232:233], v242 offset:57600\n"
"buffer_load_dwordx4 v[148:151], v252, s[64:67], s49 offen\n"
"v_cvt_pk_bf16_f32 v20, v8, v9\n"
"v_mfma_f32_16x16x32_bf16 a[12:15], a[204:207], v[16:19], a[12:15]\n"
"v_exp_f32 v10, v10\n"
"v_mfma_f32_16x16x32_bf16 a[16:19], a[208:211], v[16:19], a[16:19]\n"
"ds_read_b64_tr_b16 a[234:235], v242 offset:61696\n"
"v_add_f32 v44, v44, v4\n"
"v_mfma_f32_16x16x32_bf16 a[20:23], a[212:215], v[16:19], a[20:23]\n"
"s_add_u32 m0, s51, 4096\n"
"v_exp_f32 v11, v11\n"
"v_mfma_f32_16x16x32_bf16 a[24:27], a[216:219], v[16:19], a[24:27]\n"
"buffer_load_dwordx4 v237, s[24:27], s20 offen offset:2048 lds\n"
"ds_read_b64_tr_b16 a[236:237], v243 offset:57600\n"
"v_cvt_pk_bf16_f32 v21, v10, v11\n"
"v_mfma_f32_16x16x32_bf16 a[28:31], a[220:223], v[16:19], a[28:31]\n"
"v_exp_f32 v12, v12\n"
"v_mfma_f32_16x16x32_bf16 v[220:223], a[128:131], v[96:99], v[32:35]\n"
"ds_read_b64_tr_b16 a[238:239], v243 offset:61696\n"
"v_add_f32 v44, v44, v5\n"
"v_mfma_f32_16x16x32_bf16 v[220:223], a[132:135], v[100:103], v[220:223]\n"
"v_exp_f32 v13, v13\n"
"v_mfma_f32_16x16x32_bf16 v[220:223], a[136:139], v[104:107], v[220:223]\n"
"ds_read_b64_tr_b16 a[240:241], v242 offset:57856\n"
"v_cvt_pk_bf16_f32 v22, v12, v13\n"
"v_mfma_f32_16x16x32_bf16 v[220:223], a[140:143], v[108:111], v[220:223]\n"
"v_exp_f32 v14, v14\n"
"v_mfma_f32_16x16x32_bf16 v[224:227], a[144:147], v[96:99], v[32:35]\n"
"ds_read_b64_tr_b16 a[242:243], v242 offset:61952\n"
"v_add_f32 v44, v44, v6\n"
"v_mfma_f32_16x16x32_bf16 v[224:227], a[148:151], v[100:103], v[224:227]\n"
"v_exp_f32 v15, v15\n"
"v_mfma_f32_16x16x32_bf16 v[224:227], a[152:155], v[104:107], v[224:227]\n"
"ds_read_b64_tr_b16 a[244:245], v243 offset:57856\n"
"v_cvt_pk_bf16_f32 v23, v14, v15\n"
"v_mfma_f32_16x16x32_bf16 v[224:227], a[156:159], v[108:111], v[224:227]\n"
"s_add_u32 m0, s51, 4096\n"
"v_exp_f32 v220, v220\n"
"v_mfma_f32_16x16x32_bf16 a[32:35], a[192:195], v[20:23], a[32:35]\n"
"buffer_load_dwordx4 v238, s[24:27], s20 offen offset:3072 lds\n"
"v_mfma_f32_16x16x32_bf16 a[36:39], a[196:199], v[20:23], a[36:39]\n"
"v_exp_f32 v221, v221\n"
"v_mfma_f32_16x16x32_bf16 a[40:43], a[200:203], v[20:23], a[40:43]\n"
"ds_read_b64_tr_b16 a[246:247], v243 offset:61952\n"
"v_cvt_pk_bf16_f32 v16, v220, v221\n"
"v_mfma_f32_16x16x32_bf16 a[44:47], a[204:207], v[20:23], a[44:47]\n"
"v_exp_f32 v222, v222\n"
"v_mfma_f32_16x16x32_bf16 a[48:51], a[208:211], v[20:23], a[48:51]\n"
"ds_read_b64_tr_b16 a[248:249], v242 offset:58112\n"
"v_add_f32 v44, v44, v7\n"
"v_mfma_f32_16x16x32_bf16 a[52:55], a[212:215], v[20:23], a[52:55]\n"
"v_exp_f32 v223, v223\n"
"v_mfma_f32_16x16x32_bf16 a[56:59], a[216:219], v[20:23], a[56:59]\n"
"ds_read_b64_tr_b16 a[250:251], v242 offset:62208\n"
"v_cvt_pk_bf16_f32 v17, v222, v223\n"
"v_mfma_f32_16x16x32_bf16 a[60:63], a[220:223], v[20:23], a[60:63]\n"
"v_exp_f32 v224, v224\n"
"v_mfma_f32_16x16x32_bf16 v[48:51], v[40:43], v[20:23], v[48:51]\n"
"ds_read_b64_tr_b16 a[252:253], v243 offset:58112\n"
"v_add_f32 v52, v52, v220\n"
"v_mfma_f32_16x16x32_bf16 v[228:231], a[128:131], v[112:115], v[36:39]\n"
"v_exp_f32 v225, v225\n"
"v_mfma_f32_16x16x32_bf16 v[228:231], a[132:135], v[116:119], v[228:231]\n"
"ds_read_b64_tr_b16 a[254:255], v243 offset:62208\n"
"buffer_load_dwordx4 v[184:187], v252, s[68:71], s49 offen\n"
"v_cvt_pk_bf16_f32 v18, v224, v225\n"
"v_mfma_f32_16x16x32_bf16 v[228:231], a[136:139], v[120:123], v[228:231]\n"
"s_add_u32 s49, s49, 285696\n"
"v_exp_f32 v226, v226\n"
"v_mfma_f32_16x16x32_bf16 v[228:231], a[140:143], v[124:127], v[228:231]\n"
"v_add_f32 v52, v52, v221\n"
"v_add_f32 v52, v52, v222\n"
"v_mfma_f32_16x16x32_bf16 v[232:235], a[144:147], v[112:115], v[36:39]\n"
"v_exp_f32 v227, v227\n"
"v_mfma_f32_16x16x32_bf16 v[232:235], a[148:151], v[116:119], v[232:235]\n"
"s_add_u32 m0, s51, 8192\n"
"v_cvt_pk_bf16_f32 v19, v226, v227\n"
"v_add_f32 v52, v52, v223\n"
"v_mfma_f32_16x16x32_bf16 v[232:235], a[152:155], v[120:123], v[232:235]\n"
"buffer_load_dwordx4 v239, s[24:27], s20 offen offset:0 lds\n"
"v_mfma_f32_16x16x32_bf16 v[232:235], a[156:159], v[124:127], v[232:235]\n"
"v_exp_f32 v228, v228\n"
"v_mfma_f32_16x16x32_bf16 a[64:67], a[192:195], v[16:19], a[64:67]\n"
"v_add_f32 v52, v52, v224\n"
"v_add_f32 v52, v52, v225\n"
"v_mfma_f32_16x16x32_bf16 a[68:71], a[196:199], v[16:19], a[68:71]\n"
"v_exp_f32 v229, v229\n"
"v_mfma_f32_16x16x32_bf16 a[72:75], a[200:203], v[16:19], a[72:75]\n"
"v_cvt_pk_bf16_f32 v20, v228, v229\n"
"v_add_f32 v52, v52, v226\n"
"v_mfma_f32_16x16x32_bf16 a[76:79], a[204:207], v[16:19], a[76:79]\n"
"v_exp_f32 v230, v230\n"
"v_mfma_f32_16x16x32_bf16 a[80:83], a[208:211], v[16:19], a[80:83]\n"
"v_add_f32 v52, v52, v227\n"
"v_mfma_f32_16x16x32_bf16 a[84:87], a[212:215], v[16:19], a[84:87]\n"
"v_exp_f32 v231, v231\n"
"v_mfma_f32_16x16x32_bf16 a[88:91], a[216:219], v[16:19], a[88:91]\n"
"s_waitcnt vmcnt(25) lgkmcnt(0)\n"
"s_barrier\n"
"v_cvt_pk_bf16_f32 v21, v230, v231\n"
"v_mfma_f32_16x16x32_bf16 a[92:95], a[220:223], v[16:19], a[92:95]\n"
"v_exp_f32 v232, v232\n"
"v_mfma_f32_16x16x32_bf16 v[0:3], a[160:163], v[64:67], v[24:27]\n"
"ds_read_b128 a[128:131], v241 offset:0\n"
"v_mfma_f32_16x16x32_bf16 v[0:3], a[164:167], v[68:71], v[0:3]\n"
"v_exp_f32 v233, v233\n"
"v_mfma_f32_16x16x32_bf16 v[0:3], a[168:171], v[72:75], v[0:3]\n"
"ds_read_b128 a[132:135], v241 offset:256\n"
"v_cvt_pk_bf16_f32 v22, v232, v233\n"
"v_mfma_f32_16x16x32_bf16 v[0:3], a[172:175], v[76:79], v[0:3]\n"
"v_exp_f32 v234, v234\n"
"v_mfma_f32_16x16x32_bf16 v[4:7], a[176:179], v[64:67], v[24:27]\n"
"ds_read_b128 a[136:139], v241 offset:512\n"
"v_mfma_f32_16x16x32_bf16 v[4:7], a[180:183], v[68:71], v[4:7]\n"
"v_exp_f32 v235, v235\n"
"v_mfma_f32_16x16x32_bf16 v[4:7], a[184:187], v[72:75], v[4:7]\n"
"ds_read_b128 a[140:143], v241 offset:768\n"
"v_cvt_pk_bf16_f32 v23, v234, v235\n"
"v_mfma_f32_16x16x32_bf16 v[4:7], a[188:191], v[76:79], v[4:7]\n"
"s_add_u32 m0, s53, 36864\n"
"v_exp_f32 v0, v0\n"
"v_mfma_f32_16x16x32_bf16 a[96:99], a[192:195], v[20:23], a[96:99]\n"
"buffer_load_dwordx4 v210, s[24:27], s21 offen offset:0 lds\n"
"v_mfma_f32_16x16x32_bf16 a[100:103], a[196:199], v[20:23], a[100:103]\n"
"v_exp_f32 v1, v1\n"
"v_mfma_f32_16x16x32_bf16 a[104:107], a[200:203], v[20:23], a[104:107]\n"
"ds_read_b128 a[144:147], v241 offset:4096\n"
"buffer_load_dwordx4 v[152:155], v252, s[64:67], s49 offen\n"
"v_cvt_pk_bf16_f32 v16, v0, v1\n"
"v_mfma_f32_16x16x32_bf16 a[108:111], a[204:207], v[20:23], a[108:111]\n"
"v_exp_f32 v2, v2\n"
"v_mfma_f32_16x16x32_bf16 a[112:115], a[208:211], v[20:23], a[112:115]\n"
"ds_read_b128 a[148:151], v241 offset:4352\n"
"v_add_f32 v44, v44, v0\n"
"v_mfma_f32_16x16x32_bf16 a[116:119], a[212:215], v[20:23], a[116:119]\n"
"v_exp_f32 v3, v3\n"
"v_mfma_f32_16x16x32_bf16 a[120:123], a[216:219], v[20:23], a[120:123]\n"
"ds_read_b128 a[152:155], v241 offset:4608\n"
"v_cvt_pk_bf16_f32 v17, v2, v3\n"
"v_mfma_f32_16x16x32_bf16 a[124:127], a[220:223], v[20:23], a[124:127]\n"
"v_exp_f32 v4, v4\n"
"v_mfma_f32_16x16x32_bf16 v[56:59], v[40:43], v[20:23], v[56:59]\n"
"ds_read_b128 a[156:159], v241 offset:4864\n"
"v_add_f32 v44, v44, v1\n"
"v_mfma_f32_16x16x32_bf16 v[8:11], a[160:163], v[80:83], v[28:31]\n"
"v_exp_f32 v5, v5\n"
"v_mfma_f32_16x16x32_bf16 v[8:11], a[164:167], v[84:87], v[8:11]\n"
"ds_read_b64_tr_b16 a[192:193], v244 offset:16384\n"
"v_cvt_pk_bf16_f32 v18, v4, v5\n"
"v_mfma_f32_16x16x32_bf16 v[8:11], a[168:171], v[88:91], v[8:11]\n"
"v_exp_f32 v6, v6\n"
"v_mfma_f32_16x16x32_bf16 v[8:11], a[172:175], v[92:95], v[8:11]\n"
"ds_read_b64_tr_b16 a[194:195], v244 offset:20480\n"
"v_add_f32 v44, v44, v2\n"
"v_mfma_f32_16x16x32_bf16 v[12:15], a[176:179], v[80:83], v[28:31]\n"
"v_exp_f32 v7, v7\n"
"v_mfma_f32_16x16x32_bf16 v[12:15], a[180:183], v[84:87], v[12:15]\n"
"s_add_u32 m0, s53, 36864\n"
"ds_read_b64_tr_b16 a[196:197], v245 offset:16384\n"
"v_cvt_pk_bf16_f32 v19, v6, v7\n"
"v_mfma_f32_16x16x32_bf16 v[12:15], a[184:187], v[88:91], v[12:15]\n"
"buffer_load_dwordx4 v211, s[24:27], s21 offen offset:1024 lds\n"
"v_mfma_f32_16x16x32_bf16 v[12:15], a[188:191], v[92:95], v[12:15]\n"
"s_waitcnt lgkmcnt(11)\n"
"v_exp_f32 v8, v8\n"
"v_mfma_f32_16x16x32_bf16 a[0:3], a[224:227], v[16:19], a[0:3]\n"
"ds_read_b64_tr_b16 a[198:199], v245 offset:20480\n"
"v_add_f32 v44, v44, v3\n"
"v_mfma_f32_16x16x32_bf16 a[4:7], a[228:231], v[16:19], a[4:7]\n"
"v_exp_f32 v9, v9\n"
"v_mfma_f32_16x16x32_bf16 a[8:11], a[232:235], v[16:19], a[8:11]\n"
"ds_read_b64_tr_b16 a[200:201], v244 offset:16640\n"
"v_cvt_pk_bf16_f32 v20, v8, v9\n"
"v_mfma_f32_16x16x32_bf16 a[12:15], a[236:239], v[16:19], a[12:15]\n"
"v_exp_f32 v10, v10\n"
"v_mfma_f32_16x16x32_bf16 a[16:19], a[240:243], v[16:19], a[16:19]\n"
"ds_read_b64_tr_b16 a[202:203], v244 offset:20736\n"
"v_add_f32 v44, v44, v4\n"
"v_mfma_f32_16x16x32_bf16 a[20:23], a[244:247], v[16:19], a[20:23]\n"
"v_exp_f32 v11, v11\n"
"v_mfma_f32_16x16x32_bf16 a[24:27], a[248:251], v[16:19], a[24:27]\n"
"ds_read_b64_tr_b16 a[204:205], v245 offset:16640\n"
"v_cvt_pk_bf16_f32 v21, v10, v11\n"
"v_mfma_f32_16x16x32_bf16 a[28:31], a[252:255], v[16:19], a[28:31]\n"
"v_exp_f32 v12, v12\n"
"v_mfma_f32_16x16x32_bf16 v[220:223], a[160:163], v[96:99], v[32:35]\n"
"ds_read_b64_tr_b16 a[206:207], v245 offset:20736\n"
"v_add_f32 v44, v44, v5\n"
"v_mfma_f32_16x16x32_bf16 v[220:223], a[164:167], v[100:103], v[220:223]\n"
"v_exp_f32 v13, v13\n"
"v_mfma_f32_16x16x32_bf16 v[220:223], a[168:171], v[104:107], v[220:223]\n"
"ds_read_b64_tr_b16 a[208:209], v244 offset:16896\n"
"buffer_load_dwordx4 v[188:191], v252, s[68:71], s49 offen\n"
"v_cvt_pk_bf16_f32 v22, v12, v13\n"
"v_mfma_f32_16x16x32_bf16 v[220:223], a[172:175], v[108:111], v[220:223]\n"
"s_add_u32 s49, s49, 9216\n"
"v_exp_f32 v14, v14\n"
"v_mfma_f32_16x16x32_bf16 v[224:227], a[176:179], v[96:99], v[32:35]\n"
"ds_read_b64_tr_b16 a[210:211], v244 offset:20992\n"
"v_add_f32 v44, v44, v6\n"
"v_mfma_f32_16x16x32_bf16 v[224:227], a[180:183], v[100:103], v[224:227]\n"
"v_exp_f32 v15, v15\n"
"v_mfma_f32_16x16x32_bf16 v[224:227], a[184:187], v[104:107], v[224:227]\n"
"ds_read_b64_tr_b16 a[212:213], v245 offset:16896\n"
"v_cvt_pk_bf16_f32 v23, v14, v15\n"
"v_mfma_f32_16x16x32_bf16 v[224:227], a[188:191], v[108:111], v[224:227]\n"
"s_add_u32 m0, s53, 36864\n"
"v_exp_f32 v220, v220\n"
"v_mfma_f32_16x16x32_bf16 a[32:35], a[224:227], v[20:23], a[32:35]\n"
"buffer_load_dwordx4 v212, s[24:27], s21 offen offset:2048 lds\n"
"v_mfma_f32_16x16x32_bf16 a[36:39], a[228:231], v[20:23], a[36:39]\n"
"v_exp_f32 v221, v221\n"
"v_mfma_f32_16x16x32_bf16 a[40:43], a[232:235], v[20:23], a[40:43]\n"
"ds_read_b64_tr_b16 a[214:215], v245 offset:20992\n"
"v_cvt_pk_bf16_f32 v16, v220, v221\n"
"v_mfma_f32_16x16x32_bf16 a[44:47], a[236:239], v[20:23], a[44:47]\n"
"v_exp_f32 v222, v222\n"
"v_mfma_f32_16x16x32_bf16 a[48:51], a[240:243], v[20:23], a[48:51]\n"
"ds_read_b64_tr_b16 a[216:217], v244 offset:17152\n"
"v_add_f32 v44, v44, v7\n"
"v_mfma_f32_16x16x32_bf16 a[52:55], a[244:247], v[20:23], a[52:55]\n"
"v_exp_f32 v223, v223\n"
"v_mfma_f32_16x16x32_bf16 a[56:59], a[248:251], v[20:23], a[56:59]\n"
"ds_read_b64_tr_b16 a[218:219], v244 offset:21248\n"
"v_cvt_pk_bf16_f32 v17, v222, v223\n"
"v_mfma_f32_16x16x32_bf16 a[60:63], a[252:255], v[20:23], a[60:63]\n"
"v_exp_f32 v224, v224\n"
"v_mfma_f32_16x16x32_bf16 v[48:51], v[40:43], v[20:23], v[48:51]\n"
"ds_read_b64_tr_b16 a[220:221], v245 offset:17152\n"
"v_add_f32 v52, v52, v220\n"
"v_mfma_f32_16x16x32_bf16 v[228:231], a[160:163], v[112:115], v[36:39]\n"
"v_exp_f32 v225, v225\n"
"v_mfma_f32_16x16x32_bf16 v[228:231], a[164:167], v[116:119], v[228:231]\n"
"ds_read_b64_tr_b16 a[222:223], v245 offset:21248\n"
"v_cvt_pk_bf16_f32 v18, v224, v225\n"
"v_mfma_f32_16x16x32_bf16 v[228:231], a[168:171], v[120:123], v[228:231]\n"
"v_exp_f32 v226, v226\n"
"v_mfma_f32_16x16x32_bf16 v[228:231], a[172:175], v[124:127], v[228:231]\n"
"v_add_f32 v52, v52, v221\n"
"v_add_f32 v52, v52, v222\n"
"v_mfma_f32_16x16x32_bf16 v[232:235], a[176:179], v[112:115], v[36:39]\n"
"v_exp_f32 v227, v227\n"
"v_mfma_f32_16x16x32_bf16 v[232:235], a[180:183], v[116:119], v[232:235]\n"
"s_add_u32 m0, s53, 36864\n"
"v_cvt_pk_bf16_f32 v19, v226, v227\n"
"v_add_f32 v52, v52, v223\n"
"v_mfma_f32_16x16x32_bf16 v[232:235], a[184:187], v[120:123], v[232:235]\n"
"buffer_load_dwordx4 v213, s[24:27], s21 offen offset:3072 lds\n"
"v_mfma_f32_16x16x32_bf16 v[232:235], a[188:191], v[124:127], v[232:235]\n"
"v_exp_f32 v228, v228\n"
"v_mfma_f32_16x16x32_bf16 a[64:67], a[224:227], v[16:19], a[64:67]\n"
"v_add_f32 v52, v52, v224\n"
"v_add_f32 v52, v52, v225\n"
"v_mfma_f32_16x16x32_bf16 a[68:71], a[228:231], v[16:19], a[68:71]\n"
"v_exp_f32 v229, v229\n"
"v_mfma_f32_16x16x32_bf16 a[72:75], a[232:235], v[16:19], a[72:75]\n"
"buffer_load_dwordx4 v[156:159], v252, s[64:67], s49 offen\n"
"v_cvt_pk_bf16_f32 v20, v228, v229\n"
"v_add_f32 v52, v52, v226\n"
"v_mfma_f32_16x16x32_bf16 a[76:79], a[236:239], v[16:19], a[76:79]\n"
"v_exp_f32 v230, v230\n"
"v_mfma_f32_16x16x32_bf16 a[80:83], a[240:243], v[16:19], a[80:83]\n"
"v_add_f32 v52, v52, v227\n"
"v_mfma_f32_16x16x32_bf16 a[84:87], a[244:247], v[16:19], a[84:87]\n"
"v_exp_f32 v231, v231\n"
"v_mfma_f32_16x16x32_bf16 a[88:91], a[248:251], v[16:19], a[88:91]\n"
"s_waitcnt lgkmcnt(8)\n"
"v_cvt_pk_bf16_f32 v21, v230, v231\n"
"v_mfma_f32_16x16x32_bf16 a[92:95], a[252:255], v[16:19], a[92:95]\n"
"v_exp_f32 v232, v232\n"
"v_mfma_f32_16x16x32_bf16 v[0:3], a[128:131], v[64:67], v[24:27]\n"
"ds_read_b128 a[160:163], v241 offset:8192\n"
"v_mfma_f32_16x16x32_bf16 v[0:3], a[132:135], v[68:71], v[0:3]\n"
"v_exp_f32 v233, v233\n"
"v_mfma_f32_16x16x32_bf16 v[0:3], a[136:139], v[72:75], v[0:3]\n"
"ds_read_b128 a[164:167], v241 offset:8448\n"
"v_cvt_pk_bf16_f32 v22, v232, v233\n"
"v_mfma_f32_16x16x32_bf16 v[0:3], a[140:143], v[76:79], v[0:3]\n"
"v_exp_f32 v234, v234\n"
"v_mfma_f32_16x16x32_bf16 v[4:7], a[144:147], v[64:67], v[24:27]\n"
"ds_read_b128 a[168:171], v241 offset:8704\n"
"v_mfma_f32_16x16x32_bf16 v[4:7], a[148:151], v[68:71], v[4:7]\n"
"v_exp_f32 v235, v235\n"
"v_mfma_f32_16x16x32_bf16 v[4:7], a[152:155], v[72:75], v[4:7]\n"
"ds_read_b128 a[172:175], v241 offset:8960\n"
"v_cvt_pk_bf16_f32 v23, v234, v235\n"
"v_mfma_f32_16x16x32_bf16 v[4:7], a[156:159], v[76:79], v[4:7]\n"
"s_add_u32 m0, s53, 40960\n"
"v_exp_f32 v0, v0\n"
"v_mfma_f32_16x16x32_bf16 a[96:99], a[224:227], v[20:23], a[96:99]\n"
"buffer_load_dwordx4 v214, s[24:27], s21 offen offset:0 lds\n"
"v_mfma_f32_16x16x32_bf16 a[100:103], a[228:231], v[20:23], a[100:103]\n"
"v_exp_f32 v1, v1\n"
"v_mfma_f32_16x16x32_bf16 a[104:107], a[232:235], v[20:23], a[104:107]\n"
"ds_read_b128 a[176:179], v241 offset:12288\n"
"v_cvt_pk_bf16_f32 v16, v0, v1\n"
"v_mfma_f32_16x16x32_bf16 a[108:111], a[236:239], v[20:23], a[108:111]\n"
"v_exp_f32 v2, v2\n"
"v_mfma_f32_16x16x32_bf16 a[112:115], a[240:243], v[20:23], a[112:115]\n"
"ds_read_b128 a[180:183], v241 offset:12544\n"
"v_add_f32 v44, v44, v0\n"
"v_mfma_f32_16x16x32_bf16 a[116:119], a[244:247], v[20:23], a[116:119]\n"
"v_exp_f32 v3, v3\n"
"v_mfma_f32_16x16x32_bf16 a[120:123], a[248:251], v[20:23], a[120:123]\n"
"ds_read_b128 a[184:187], v241 offset:12800\n"
"v_cvt_pk_bf16_f32 v17, v2, v3\n"
"v_mfma_f32_16x16x32_bf16 a[124:127], a[252:255], v[20:23], a[124:127]\n"
"v_exp_f32 v4, v4\n"
"v_mfma_f32_16x16x32_bf16 v[56:59], v[40:43], v[20:23], v[56:59]\n"
"ds_read_b128 a[188:191], v241 offset:13056\n"
"v_add_f32 v44, v44, v1\n"
"v_mfma_f32_16x16x32_bf16 v[8:11], a[128:131], v[80:83], v[28:31]\n"
"v_exp_f32 v5, v5\n"
"v_mfma_f32_16x16x32_bf16 v[8:11], a[132:135], v[84:87], v[8:11]\n"
"ds_read_b64_tr_b16 a[224:225], v244 offset:24576\n"
"buffer_load_dwordx4 v[192:195], v252, s[68:71], s49 offen\n"
"v_cvt_pk_bf16_f32 v18, v4, v5\n"
"v_mfma_f32_16x16x32_bf16 v[8:11], a[136:139], v[88:91], v[8:11]\n"
"s_add_u32 s49, s49, 285696\n"
"v_exp_f32 v6, v6\n"
"v_mfma_f32_16x16x32_bf16 v[8:11], a[140:143], v[92:95], v[8:11]\n"
"ds_read_b64_tr_b16 a[226:227], v244 offset:28672\n"
"v_add_f32 v44, v44, v2\n"
"v_mfma_f32_16x16x32_bf16 v[12:15], a[144:147], v[80:83], v[28:31]\n"
"v_exp_f32 v7, v7\n"
"v_mfma_f32_16x16x32_bf16 v[12:15], a[148:151], v[84:87], v[12:15]\n"
"s_add_u32 m0, s53, 40960\n"
"ds_read_b64_tr_b16 a[228:229], v245 offset:24576\n"
"v_cvt_pk_bf16_f32 v19, v6, v7\n"
"v_mfma_f32_16x16x32_bf16 v[12:15], a[152:155], v[88:91], v[12:15]\n"
"buffer_load_dwordx4 v236, s[24:27], s21 offen offset:1024 lds\n"
"v_mfma_f32_16x16x32_bf16 v[12:15], a[156:159], v[92:95], v[12:15]\n"
"s_waitcnt lgkmcnt(11)\n"
"v_exp_f32 v8, v8\n"
"v_mfma_f32_16x16x32_bf16 a[0:3], a[192:195], v[16:19], a[0:3]\n"
"ds_read_b64_tr_b16 a[230:231], v245 offset:28672\n"
"v_add_f32 v44, v44, v3\n"
"v_mfma_f32_16x16x32_bf16 a[4:7], a[196:199], v[16:19], a[4:7]\n"
"v_exp_f32 v9, v9\n"
"v_mfma_f32_16x16x32_bf16 a[8:11], a[200:203], v[16:19], a[8:11]\n"
"ds_read_b64_tr_b16 a[232:233], v244 offset:24832\n"
"v_cvt_pk_bf16_f32 v20, v8, v9\n"
"v_mfma_f32_16x16x32_bf16 a[12:15], a[204:207], v[16:19], a[12:15]\n"
"v_exp_f32 v10, v10\n"
"v_mfma_f32_16x16x32_bf16 a[16:19], a[208:211], v[16:19], a[16:19]\n"
"ds_read_b64_tr_b16 a[234:235], v244 offset:28928\n"
"v_add_f32 v44, v44, v4\n"
"v_mfma_f32_16x16x32_bf16 a[20:23], a[212:215], v[16:19], a[20:23]\n"
"s_add_u32 m0, s53, 40960\n"
"v_exp_f32 v11, v11\n"
"v_mfma_f32_16x16x32_bf16 a[24:27], a[216:219], v[16:19], a[24:27]\n"
"buffer_load_dwordx4 v237, s[24:27], s21 offen offset:2048 lds\n"
"ds_read_b64_tr_b16 a[236:237], v245 offset:24832\n"
"v_cvt_pk_bf16_f32 v21, v10, v11\n"
"v_mfma_f32_16x16x32_bf16 a[28:31], a[220:223], v[16:19], a[28:31]\n"
"v_exp_f32 v12, v12\n"
"v_mfma_f32_16x16x32_bf16 v[220:223], a[128:131], v[96:99], v[32:35]\n"
"ds_read_b64_tr_b16 a[238:239], v245 offset:28928\n"
"v_add_f32 v44, v44, v5\n"
"v_mfma_f32_16x16x32_bf16 v[220:223], a[132:135], v[100:103], v[220:223]\n"
"v_exp_f32 v13, v13\n"
"v_mfma_f32_16x16x32_bf16 v[220:223], a[136:139], v[104:107], v[220:223]\n"
"ds_read_b64_tr_b16 a[240:241], v244 offset:25088\n"
"v_cvt_pk_bf16_f32 v22, v12, v13\n"
"v_mfma_f32_16x16x32_bf16 v[220:223], a[140:143], v[108:111], v[220:223]\n"
"v_exp_f32 v14, v14\n"
"v_mfma_f32_16x16x32_bf16 v[224:227], a[144:147], v[96:99], v[32:35]\n"
"ds_read_b64_tr_b16 a[242:243], v244 offset:29184\n"
"v_add_f32 v44, v44, v6\n"
"v_mfma_f32_16x16x32_bf16 v[224:227], a[148:151], v[100:103], v[224:227]\n"
"v_exp_f32 v15, v15\n"
"v_mfma_f32_16x16x32_bf16 v[224:227], a[152:155], v[104:107], v[224:227]\n"
"ds_read_b64_tr_b16 a[244:245], v245 offset:25088\n"
"v_cvt_pk_bf16_f32 v23, v14, v15\n"
"v_mfma_f32_16x16x32_bf16 v[224:227], a[156:159], v[108:111], v[224:227]\n"
"s_add_u32 m0, s53, 40960\n"
"v_exp_f32 v220, v220\n"
"v_mfma_f32_16x16x32_bf16 a[32:35], a[192:195], v[20:23], a[32:35]\n"
"buffer_load_dwordx4 v238, s[24:27], s21 offen offset:3072 lds\n"
"v_mfma_f32_16x16x32_bf16 a[36:39], a[196:199], v[20:23], a[36:39]\n"
"v_exp_f32 v221, v221\n"
"v_mfma_f32_16x16x32_bf16 a[40:43], a[200:203], v[20:23], a[40:43]\n"
"ds_read_b64_tr_b16 a[246:247], v245 offset:29184\n"
"buffer_load_dwordx4 v[160:163], v251, s[64:67], s49 offen\n"
"v_cvt_pk_bf16_f32 v16, v220, v221\n"
"v_mfma_f32_16x16x32_bf16 a[44:47], a[204:207], v[20:23], a[44:47]\n"
"v_exp_f32 v222, v222\n"
"v_mfma_f32_16x16x32_bf16 a[48:51], a[208:211], v[20:23], a[48:51]\n"
"ds_read_b64_tr_b16 a[248:249], v244 offset:25344\n"
"v_add_f32 v44, v44, v7\n"
"v_mfma_f32_16x16x32_bf16 a[52:55], a[212:215], v[20:23], a[52:55]\n"
"v_exp_f32 v223, v223\n"
"v_mfma_f32_16x16x32_bf16 a[56:59], a[216:219], v[20:23], a[56:59]\n"
"ds_read_b64_tr_b16 a[250:251], v244 offset:29440\n"
"v_cvt_pk_bf16_f32 v17, v222, v223\n"
"v_mfma_f32_16x16x32_bf16 a[60:63], a[220:223], v[20:23], a[60:63]\n"
"v_exp_f32 v224, v224\n"
"v_mfma_f32_16x16x32_bf16 v[48:51], v[40:43], v[20:23], v[48:51]\n"
"ds_read_b64_tr_b16 a[252:253], v245 offset:25344\n"
"v_add_f32 v52, v52, v220\n"
"v_mfma_f32_16x16x32_bf16 v[228:231], a[128:131], v[112:115], v[36:39]\n"
"v_exp_f32 v225, v225\n"
"v_mfma_f32_16x16x32_bf16 v[228:231], a[132:135], v[116:119], v[228:231]\n"
"ds_read_b64_tr_b16 a[254:255], v245 offset:29440\n"
"v_cvt_pk_bf16_f32 v18, v224, v225\n"
"v_mfma_f32_16x16x32_bf16 v[228:231], a[136:139], v[120:123], v[228:231]\n"
"v_exp_f32 v226, v226\n"
"v_mfma_f32_16x16x32_bf16 v[228:231], a[140:143], v[124:127], v[228:231]\n"
"v_add_f32 v52, v52, v221\n"
"v_add_f32 v52, v52, v222\n"
"v_mfma_f32_16x16x32_bf16 v[232:235], a[144:147], v[112:115], v[36:39]\n"
"v_exp_f32 v227, v227\n"
"v_mfma_f32_16x16x32_bf16 v[232:235], a[148:151], v[116:119], v[232:235]\n"
"s_add_u32 m0, s53, 45056\n"
"v_cvt_pk_bf16_f32 v19, v226, v227\n"
"v_add_f32 v52, v52, v223\n"
"v_mfma_f32_16x16x32_bf16 v[232:235], a[152:155], v[120:123], v[232:235]\n"
"buffer_load_dwordx4 v239, s[24:27], s21 offen offset:0 lds\n"
"v_mfma_f32_16x16x32_bf16 v[232:235], a[156:159], v[124:127], v[232:235]\n"
"v_exp_f32 v228, v228\n"
"v_mfma_f32_16x16x32_bf16 a[64:67], a[192:195], v[16:19], a[64:67]\n"
"v_add_f32 v52, v52, v224\n"
"v_add_f32 v52, v52, v225\n"
"v_mfma_f32_16x16x32_bf16 a[68:71], a[196:199], v[16:19], a[68:71]\n"
"v_exp_f32 v229, v229\n"
"v_mfma_f32_16x16x32_bf16 a[72:75], a[200:203], v[16:19], a[72:75]\n"
"v_cvt_pk_bf16_f32 v20, v228, v229\n"
"v_add_f32 v52, v52, v226\n"
"v_mfma_f32_16x16x32_bf16 a[76:79], a[204:207], v[16:19], a[76:79]\n"
"v_exp_f32 v230, v230\n"
"v_mfma_f32_16x16x32_bf16 a[80:83], a[208:211], v[16:19], a[80:83]\n"
"v_add_f32 v52, v52, v227\n"
"v_mfma_f32_16x16x32_bf16 a[84:87], a[212:215], v[16:19], a[84:87]\n"
"v_exp_f32 v231, v231\n"
"v_mfma_f32_16x16x32_bf16 a[88:91], a[216:219], v[16:19], a[88:91]\n"
"s_waitcnt vmcnt(26) lgkmcnt(0)\n"
"s_barrier\n"
"v_cvt_pk_bf16_f32 v21, v230, v231\n"
"v_mfma_f32_16x16x32_bf16 a[92:95], a[220:223], v[16:19], a[92:95]\n"
"v_exp_f32 v232, v232\n"
"v_mfma_f32_16x16x32_bf16 v[0:3], a[160:163], v[64:67], v[24:27]\n"
"ds_read_b128 a[128:131], v241 offset:32768\n"
"v_mfma_f32_16x16x32_bf16 v[0:3], a[164:167], v[68:71], v[0:3]\n"
"v_exp_f32 v233, v233\n"
"v_mfma_f32_16x16x32_bf16 v[0:3], a[168:171], v[72:75], v[0:3]\n"
"ds_read_b128 a[132:135], v241 offset:33024\n"
"buffer_load_dwordx4 v[196:199], v251, s[68:71], s49 offen\n"
"v_cvt_pk_bf16_f32 v22, v232, v233\n"
"v_mfma_f32_16x16x32_bf16 v[0:3], a[172:175], v[76:79], v[0:3]\n"
"s_lshl_b32 s62, s41, 6\n"
"v_exp_f32 v234, v234\n"
"v_mfma_f32_16x16x32_bf16 v[4:7], a[176:179], v[64:67], v[24:27]\n"
"ds_read_b128 a[136:139], v241 offset:33280\n"
"v_add_u32 v253, s62, v248\n"
"v_mfma_f32_16x16x32_bf16 v[4:7], a[180:183], v[68:71], v[4:7]\n"
"v_exp_f32 v235, v235\n"
"v_mfma_f32_16x16x32_bf16 v[4:7], a[184:187], v[72:75], v[4:7]\n"
"ds_read_b128 a[140:143], v241 offset:33536\n"
"v_cvt_pk_bf16_f32 v23, v234, v235\n"
"v_mfma_f32_16x16x32_bf16 v[4:7], a[188:191], v[76:79], v[4:7]\n"
"s_add_u32 m0, s53, 73728\n"
"v_exp_f32 v0, v0\n"
"v_mfma_f32_16x16x32_bf16 a[96:99], a[192:195], v[20:23], a[96:99]\n"
"buffer_load_dwordx4 v210, s[24:27], s22 offen offset:0 lds\n"
"v_mov_b32 v255, 0x10000000\n"
"v_mfma_f32_16x16x32_bf16 a[100:103], a[196:199], v[20:23], a[100:103]\n"
"v_exp_f32 v1, v1\n"
"v_mfma_f32_16x16x32_bf16 a[104:107], a[200:203], v[20:23], a[104:107]\n"
"ds_read_b128 a[144:147], v241 offset:36864\n"
"v_cvt_pk_bf16_f32 v16, v0, v1\n"
"v_mfma_f32_16x16x32_bf16 a[108:111], a[204:207], v[20:23], a[108:111]\n"
"v_exp_f32 v2, v2\n"
"v_mfma_f32_16x16x32_bf16 a[112:115], a[208:211], v[20:23], a[112:115]\n"
"ds_read_b128 a[148:151], v241 offset:37120\n"
"v_add_f32 v44, v44, v0\n"
"v_mfma_f32_16x16x32_bf16 a[116:119], a[212:215], v[20:23], a[116:119]\n"
"v_exp_f32 v3, v3\n"
"v_mfma_f32_16x16x32_bf16 a[120:123], a[216:219], v[20:23], a[120:123]\n"
"ds_read_b128 a[152:155], v241 offset:37376\n"
"v_cvt_pk_bf16_f32 v17, v2, v3\n"
"v_mfma_f32_16x16x32_bf16 a[124:127], a[220:223], v[20:23], a[124:127]\n"
"v_exp_f32 v4, v4\n"
"v_mfma_f32_16x16x32_bf16 v[56:59], v[40:43], v[20:23], v[56:59]\n"
"ds_read_b128 a[156:159], v241 offset:37632\n"
"v_add_f32 v44, v44, v1\n"
"v_mfma_f32_16x16x32_bf16 v[8:11], a[160:163], v[80:83], v[28:31]\n"
"v_exp_f32 v5, v5\n"
"v_mfma_f32_16x16x32_bf16 v[8:11], a[164:167], v[84:87], v[8:11]\n"
"ds_read_b64_tr_b16 a[192:193], v244 offset:49152\n"
"v_cvt_pk_bf16_f32 v18, v4, v5\n"
"v_mfma_f32_16x16x32_bf16 v[8:11], a[168:171], v[88:91], v[8:11]\n"
"v_exp_f32 v6, v6\n"
"v_mfma_f32_16x16x32_bf16 v[8:11], a[172:175], v[92:95], v[8:11]\n"
"ds_read_b64_tr_b16 a[194:195], v244 offset:53248\n"
"v_add_f32 v44, v44, v2\n"
"v_mfma_f32_16x16x32_bf16 v[12:15], a[176:179], v[80:83], v[28:31]\n"
"v_exp_f32 v7, v7\n"
"v_mfma_f32_16x16x32_bf16 v[12:15], a[180:183], v[84:87], v[12:15]\n"
"s_add_u32 m0, s53, 73728\n"
"ds_read_b64_tr_b16 a[196:197], v245 offset:49152\n"
"v_cvt_pk_bf16_f32 v19, v6, v7\n"
"v_mfma_f32_16x16x32_bf16 v[12:15], a[184:187], v[88:91], v[12:15]\n"
"buffer_load_dwordx4 v211, s[24:27], s22 offen offset:1024 lds\n"
"v_cmp_gt_u32 vcc, 144, v253\n"
"v_mfma_f32_16x16x32_bf16 v[12:15], a[188:191], v[92:95], v[12:15]\n"
"s_waitcnt lgkmcnt(11)\n"
"v_exp_f32 v8, v8\n"
"v_mfma_f32_16x16x32_bf16 a[0:3], a[224:227], v[16:19], a[0:3]\n"
"ds_read_b64_tr_b16 a[198:199], v245 offset:53248\n"
"v_add_f32 v44, v44, v3\n"
"v_mfma_f32_16x16x32_bf16 a[4:7], a[228:231], v[16:19], a[4:7]\n"
"v_exp_f32 v9, v9\n"
"v_mfma_f32_16x16x32_bf16 a[8:11], a[232:235], v[16:19], a[8:11]\n"
"ds_read_b64_tr_b16 a[200:201], v244 offset:49408\n"
"v_cvt_pk_bf16_f32 v20, v8, v9\n"
"v_mfma_f32_16x16x32_bf16 a[12:15], a[236:239], v[16:19], a[12:15]\n"
"v_exp_f32 v10, v10\n"
"v_mfma_f32_16x16x32_bf16 a[16:19], a[240:243], v[16:19], a[16:19]\n"
"ds_read_b64_tr_b16 a[202:203], v244 offset:53504\n"
"v_add_f32 v44, v44, v4\n"
"v_mfma_f32_16x16x32_bf16 a[20:23], a[244:247], v[16:19], a[20:23]\n"
"v_exp_f32 v11, v11\n"
"v_mfma_f32_16x16x32_bf16 a[24:27], a[248:251], v[16:19], a[24:27]\n"
"ds_read_b64_tr_b16 a[204:205], v245 offset:49408\n"
"v_cvt_pk_bf16_f32 v21, v10, v11\n"
"v_mfma_f32_16x16x32_bf16 a[28:31], a[252:255], v[16:19], a[28:31]\n"
"v_exp_f32 v12, v12\n"
"v_mfma_f32_16x16x32_bf16 v[220:223], a[160:163], v[96:99], v[32:35]\n"
"ds_read_b64_tr_b16 a[206:207], v245 offset:53504\n"
"v_add_f32 v44, v44, v5\n"
"v_mfma_f32_16x16x32_bf16 v[220:223], a[164:167], v[100:103], v[220:223]\n"
"v_exp_f32 v13, v13\n"
"v_mfma_f32_16x16x32_bf16 v[220:223], a[168:171], v[104:107], v[220:223]\n"
"ds_read_b64_tr_b16 a[208:209], v244 offset:49664\n"
"v_cvt_pk_bf16_f32 v22, v12, v13\n"
"v_mfma_f32_16x16x32_bf16 v[220:223], a[172:175], v[108:111], v[220:223]\n"
"v_exp_f32 v14, v14\n"
"v_mfma_f32_16x16x32_bf16 v[224:227], a[176:179], v[96:99], v[32:35]\n"
"ds_read_b64_tr_b16 a[210:211], v244 offset:53760\n"
"v_add_f32 v44, v44, v6\n"
"v_mfma_f32_16x16x32_bf16 v[224:227], a[180:183], v[100:103], v[224:227]\n"
"v_exp_f32 v15, v15\n"
"v_mfma_f32_16x16x32_bf16 v[224:227], a[184:187], v[104:107], v[224:227]\n"
"ds_read_b64_tr_b16 a[212:213], v245 offset:49664\n"
"v_cvt_pk_bf16_f32 v23, v14, v15\n"
"v_mfma_f32_16x16x32_bf16 v[224:227], a[188:191], v[108:111], v[224:227]\n"
"s_add_u32 m0, s53, 73728\n"
"v_exp_f32 v220, v220\n"
"v_mfma_f32_16x16x32_bf16 a[32:35], a[224:227], v[20:23], a[32:35]\n"
"buffer_load_dwordx4 v212, s[24:27], s22 offen offset:2048 lds\n"
"v_lshlrev_b32 v254, 3, v253\n"
"v_mfma_f32_16x16x32_bf16 a[36:39], a[228:231], v[20:23], a[36:39]\n"
"s_nop 0\n"
"v_exp_f32 v221, v221\n"
"v_mfma_f32_16x16x32_bf16 a[40:43], a[232:235], v[20:23], a[40:43]\n"
"ds_read_b64_tr_b16 a[214:215], v245 offset:53760\n"
"v_cvt_pk_bf16_f32 v16, v220, v221\n"
"v_mfma_f32_16x16x32_bf16 a[44:47], a[236:239], v[20:23], a[44:47]\n"
"v_exp_f32 v222, v222\n"
"v_mfma_f32_16x16x32_bf16 a[48:51], a[240:243], v[20:23], a[48:51]\n"
"ds_read_b64_tr_b16 a[216:217], v244 offset:49920\n"
"v_add_f32 v44, v44, v7\n"
"v_mfma_f32_16x16x32_bf16 a[52:55], a[244:247], v[20:23], a[52:55]\n"
"v_exp_f32 v223, v223\n"
"v_mfma_f32_16x16x32_bf16 a[56:59], a[248:251], v[20:23], a[56:59]\n"
"ds_read_b64_tr_b16 a[218:219], v244 offset:54016\n"
"v_cvt_pk_bf16_f32 v17, v222, v223\n"
"v_mfma_f32_16x16x32_bf16 a[60:63], a[252:255], v[20:23], a[60:63]\n"
"v_exp_f32 v224, v224\n"
"v_mfma_f32_16x16x32_bf16 v[48:51], v[40:43], v[20:23], v[48:51]\n"
"ds_read_b64_tr_b16 a[220:221], v245 offset:49920\n"
"v_add_f32 v52, v52, v220\n"
"v_mfma_f32_16x16x32_bf16 v[228:231], a[160:163], v[112:115], v[36:39]\n"
"v_exp_f32 v225, v225\n"
"v_mfma_f32_16x16x32_bf16 v[228:231], a[164:167], v[116:119], v[228:231]\n"
"ds_read_b64_tr_b16 a[222:223], v245 offset:54016\n"
"v_cvt_pk_bf16_f32 v18, v224, v225\n"
"v_mfma_f32_16x16x32_bf16 v[228:231], a[168:171], v[120:123], v[228:231]\n"
"v_exp_f32 v226, v226\n"
"v_mfma_f32_16x16x32_bf16 v[228:231], a[172:175], v[124:127], v[228:231]\n"
"v_add_f32 v52, v52, v221\n"
"v_add_f32 v52, v52, v222\n"
"v_mfma_f32_16x16x32_bf16 v[232:235], a[176:179], v[112:115], v[36:39]\n"
"v_exp_f32 v227, v227\n"
"v_mfma_f32_16x16x32_bf16 v[232:235], a[180:183], v[116:119], v[232:235]\n"
"s_add_u32 m0, s53, 73728\n"
"v_cvt_pk_bf16_f32 v19, v226, v227\n"
"v_add_f32 v52, v52, v223\n"
"v_mfma_f32_16x16x32_bf16 v[232:235], a[184:187], v[120:123], v[232:235]\n"
"buffer_load_dwordx4 v213, s[24:27], s22 offen offset:3072 lds\n"
"v_cndmask_b32 v254, v255, v254, vcc\n"
"v_mfma_f32_16x16x32_bf16 v[232:235], a[188:191], v[124:127], v[232:235]\n"
"v_exp_f32 v228, v228\n"
"v_mfma_f32_16x16x32_bf16 a[64:67], a[224:227], v[16:19], a[64:67]\n"
"v_add_f32 v52, v52, v224\n"
"v_add_f32 v52, v52, v225\n"
"v_mfma_f32_16x16x32_bf16 a[68:71], a[228:231], v[16:19], a[68:71]\n"
"v_exp_f32 v229, v229\n"
"v_mfma_f32_16x16x32_bf16 a[72:75], a[232:235], v[16:19], a[72:75]\n"
"v_cvt_pk_bf16_f32 v20, v228, v229\n"
"v_add_f32 v52, v52, v226\n"
"v_mfma_f32_16x16x32_bf16 a[76:79], a[236:239], v[16:19], a[76:79]\n"
"v_exp_f32 v230, v230\n"
"v_mfma_f32_16x16x32_bf16 a[80:83], a[240:243], v[16:19], a[80:83]\n"
"v_add_f32 v52, v52, v227\n"
"v_lshlrev_b32 v216, 6, v248\n"
"v_mfma_f32_16x16x32_bf16 a[84:87], a[244:247], v[16:19], a[84:87]\n"
"v_exp_f32 v231, v231\n"
"v_mfma_f32_16x16x32_bf16 a[88:91], a[248:251], v[16:19], a[88:91]\n"
"s_waitcnt lgkmcnt(8)\n"
"v_cvt_pk_bf16_f32 v21, v230, v231\n"
"v_lshrrev_b32 v215, 3, v248\n"
"v_mfma_f32_16x16x32_bf16 a[92:95], a[252:255], v[16:19], a[92:95]\n"
"v_exp_f32 v232, v232\n"
"v_mfma_f32_16x16x32_bf16 v[0:3], a[128:131], v[64:67], v[24:27]\n"
"ds_read_b128 a[160:163], v241 offset:40960\n"
"v_mul_u32_u24 v215, 1152, v215\n"
"v_mfma_f32_16x16x32_bf16 v[0:3], a[132:135], v[68:71], v[0:3]\n"
"v_exp_f32 v233, v233\n"
"v_mfma_f32_16x16x32_bf16 v[0:3], a[136:139], v[72:75], v[0:3]\n"
"ds_read_b128 a[164:167], v241 offset:41216\n"
"v_cvt_pk_bf16_f32 v22, v232, v233\n"
"v_mfma_f32_16x16x32_bf16 v[0:3], a[140:143], v[76:79], v[0:3]\n"
"v_exp_f32 v234, v234\n"
"v_mfma_f32_16x16x32_bf16 v[4:7], a[144:147], v[64:67], v[24:27]\n"
"ds_read_b128 a[168:171], v241 offset:41472\n"
"v_and_b32 v217, 7, v248\n"
"v_mfma_f32_16x16x32_bf16 v[4:7], a[148:151], v[68:71], v[4:7]\n"
"v_exp_f32 v235, v235\n"
"v_mfma_f32_16x16x32_bf16 v[4:7], a[152:155], v[72:75], v[4:7]\n"
"ds_read_b128 a[172:175], v241 offset:41728\n"
"v_cvt_pk_bf16_f32 v23, v234, v235\n"
"v_mfma_f32_16x16x32_bf16 v[4:7], a[156:159], v[76:79], v[4:7]\n"
"s_add_u32 m0, s53, 77824\n"
"v_exp_f32 v0, v0\n"
"v_mfma_f32_16x16x32_bf16 a[96:99], a[224:227], v[20:23], a[96:99]\n"
"buffer_load_dwordx4 v214, s[24:27], s22 offen offset:0 lds\n"
"v_lshl_add_u32 v215, v217, 7, v215\n"
"v_mfma_f32_16x16x32_bf16 a[100:103], a[228:231], v[20:23], a[100:103]\n"
"s_and_b32 s46, s44, 7\n"
"v_exp_f32 v1, v1\n"
"v_mfma_f32_16x16x32_bf16 a[104:107], a[232:235], v[20:23], a[104:107]\n"
"s_lshl_b32 s46, s46, 2\n"
"ds_read_b128 a[176:179], v241 offset:45056\n"
"v_cvt_pk_bf16_f32 v16, v0, v1\n"
"v_mfma_f32_16x16x32_bf16 a[108:111], a[236:239], v[20:23], a[108:111]\n"
"s_add_u32 s46, s46, s41\n"
"v_exp_f32 v2, v2\n"
"v_mfma_f32_16x16x32_bf16 a[112:115], a[240:243], v[20:23], a[112:115]\n"
"s_mul_i32 s46, s46, 9216\n"
"ds_read_b128 a[180:183], v241 offset:45312\n"
"v_add_f32 v44, v44, v0\n"
"v_mfma_f32_16x16x32_bf16 a[116:119], a[244:247], v[20:23], a[116:119]\n"
"s_mul_i32 s62, s43, 294912\n"
"v_exp_f32 v3, v3\n"
"v_mfma_f32_16x16x32_bf16 a[120:123], a[248:251], v[20:23], a[120:123]\n"
"s_add_u32 s46, s46, s62\n"
"ds_read_b128 a[184:187], v241 offset:45568\n"
"v_cvt_pk_bf16_f32 v17, v2, v3\n"
"v_mfma_f32_16x16x32_bf16 a[124:127], a[252:255], v[20:23], a[124:127]\n"
"v_exp_f32 v4, v4\n"
"v_mfma_f32_16x16x32_bf16 v[56:59], v[40:43], v[20:23], v[56:59]\n"
"ds_read_b128 a[188:191], v241 offset:45824\n"
"buffer_load_dword v250, v215, s[24:27], s46 offen offset:128\n"
"v_add_f32 v44, v44, v1\n"
"v_mfma_f32_16x16x32_bf16 v[8:11], a[128:131], v[80:83], v[28:31]\n"
"v_exp_f32 v5, v5\n"
"v_mfma_f32_16x16x32_bf16 v[8:11], a[132:135], v[84:87], v[8:11]\n"
"ds_read_b64_tr_b16 a[224:225], v244 offset:57344\n"
"v_cvt_pk_bf16_f32 v18, v4, v5\n"
"v_mfma_f32_16x16x32_bf16 v[8:11], a[136:139], v[88:91], v[8:11]\n"
"v_exp_f32 v6, v6\n"
"v_mfma_f32_16x16x32_bf16 v[8:11], a[140:143], v[92:95], v[8:11]\n"
"ds_read_b64_tr_b16 a[226:227], v244 offset:61440\n"
"v_add_f32 v44, v44, v2\n"
"v_mfma_f32_16x16x32_bf16 v[12:15], a[144:147], v[80:83], v[28:31]\n"
"v_exp_f32 v7, v7\n"
"v_mfma_f32_16x16x32_bf16 v[12:15], a[148:151], v[84:87], v[12:15]\n"
"s_add_u32 m0, s53, 77824\n"
"ds_read_b64_tr_b16 a[228:229], v245 offset:57344\n"
"v_cvt_pk_bf16_f32 v19, v6, v7\n"
"v_mfma_f32_16x16x32_bf16 v[12:15], a[152:155], v[88:91], v[12:15]\n"
"buffer_load_dwordx4 v236, s[24:27], s22 offen offset:1024 lds\n"
"v_mfma_f32_16x16x32_bf16 v[12:15], a[156:159], v[92:95], v[12:15]\n"
"s_waitcnt lgkmcnt(11)\n"
"v_exp_f32 v8, v8\n"
"v_mfma_f32_16x16x32_bf16 a[0:3], a[192:195], v[16:19], a[0:3]\n"
"ds_read_b64_tr_b16 a[230:231], v245 offset:61440\n"
"v_add_f32 v44, v44, v3\n"
"v_mfma_f32_16x16x32_bf16 a[4:7], a[196:199], v[16:19], a[4:7]\n"
"v_exp_f32 v9, v9\n"
"v_mfma_f32_16x16x32_bf16 a[8:11], a[200:203], v[16:19], a[8:11]\n"
"ds_read_b64_tr_b16 a[232:233], v244 offset:57600\n"
"v_cvt_pk_bf16_f32 v20, v8, v9\n"
"v_mfma_f32_16x16x32_bf16 a[12:15], a[204:207], v[16:19], a[12:15]\n"
"v_exp_f32 v10, v10\n"
"v_mfma_f32_16x16x32_bf16 a[16:19], a[208:211], v[16:19], a[16:19]\n"
"ds_read_b64_tr_b16 a[234:235], v244 offset:61696\n"
"v_add_f32 v44, v44, v4\n"
"v_mfma_f32_16x16x32_bf16 a[20:23], a[212:215], v[16:19], a[20:23]\n"
"s_add_u32 m0, s53, 77824\n"
"v_exp_f32 v11, v11\n"
"v_mfma_f32_16x16x32_bf16 a[24:27], a[216:219], v[16:19], a[24:27]\n"
"buffer_load_dwordx4 v237, s[24:27], s22 offen offset:2048 lds\n"
"ds_read_b64_tr_b16 a[236:237], v245 offset:57600\n"
"v_cvt_pk_bf16_f32 v21, v10, v11\n"
"v_mfma_f32_16x16x32_bf16 a[28:31], a[220:223], v[16:19], a[28:31]\n"
"v_exp_f32 v12, v12\n"
"v_mfma_f32_16x16x32_bf16 v[220:223], a[128:131], v[96:99], v[32:35]\n"
"ds_read_b64_tr_b16 a[238:239], v245 offset:61696\n"
"v_add_f32 v44, v44, v5\n"
"v_mfma_f32_16x16x32_bf16 v[220:223], a[132:135], v[100:103], v[220:223]\n"
"v_exp_f32 v13, v13\n"
"v_mfma_f32_16x16x32_bf16 v[220:223], a[136:139], v[104:107], v[220:223]\n"
"ds_read_b64_tr_b16 a[240:241], v244 offset:57856\n"
"v_cvt_pk_bf16_f32 v22, v12, v13\n"
"v_mfma_f32_16x16x32_bf16 v[220:223], a[140:143], v[108:111], v[220:223]\n"
"v_exp_f32 v14, v14\n"
"v_mfma_f32_16x16x32_bf16 v[224:227], a[144:147], v[96:99], v[32:35]\n"
"ds_read_b64_tr_b16 a[242:243], v244 offset:61952\n"
"v_add_f32 v44, v44, v6\n"
"v_mfma_f32_16x16x32_bf16 v[224:227], a[148:151], v[100:103], v[224:227]\n"
"v_exp_f32 v15, v15\n"
"v_mfma_f32_16x16x32_bf16 v[224:227], a[152:155], v[104:107], v[224:227]\n"
"ds_read_b64_tr_b16 a[244:245], v245 offset:57856\n"
"v_cvt_pk_bf16_f32 v23, v14, v15\n"
"v_mfma_f32_16x16x32_bf16 v[224:227], a[156:159], v[108:111], v[224:227]\n"
"s_add_u32 m0, s53, 77824\n"
"v_exp_f32 v220, v220\n"
"v_mfma_f32_16x16x32_bf16 a[32:35], a[192:195], v[20:23], a[32:35]\n"
"buffer_load_dwordx4 v238, s[24:27], s22 offen offset:3072 lds\n"
"v_mfma_f32_16x16x32_bf16 a[36:39], a[196:199], v[20:23], a[36:39]\n"
"v_exp_f32 v221, v221\n"
"v_mfma_f32_16x16x32_bf16 a[40:43], a[200:203], v[20:23], a[40:43]\n"
"ds_read_b64_tr_b16 a[246:247], v245 offset:61952\n"
"buffer_load_dword v250, v215, s[24:27], s46 offen offset:192\n"
"v_cvt_pk_bf16_f32 v16, v220, v221\n"
"v_mfma_f32_16x16x32_bf16 a[44:47], a[204:207], v[20:23], a[44:47]\n"
"v_exp_f32 v222, v222\n"
"v_mfma_f32_16x16x32_bf16 a[48:51], a[208:211], v[20:23], a[48:51]\n"
"ds_read_b64_tr_b16 a[248:249], v244 offset:58112\n"
"v_add_f32 v44, v44, v7\n"
"v_mfma_f32_16x16x32_bf16 a[52:55], a[212:215], v[20:23], a[52:55]\n"
"v_exp_f32 v223, v223\n"
"v_mfma_f32_16x16x32_bf16 a[56:59], a[216:219], v[20:23], a[56:59]\n"
"ds_read_b64_tr_b16 a[250:251], v244 offset:62208\n"
"v_cvt_pk_bf16_f32 v17, v222, v223\n"
"v_mfma_f32_16x16x32_bf16 a[60:63], a[220:223], v[20:23], a[60:63]\n"
"v_exp_f32 v224, v224\n"
"v_mfma_f32_16x16x32_bf16 v[48:51], v[40:43], v[20:23], v[48:51]\n"
"ds_read_b64_tr_b16 a[252:253], v245 offset:58112\n"
"v_add_f32 v52, v52, v220\n"
"v_mfma_f32_16x16x32_bf16 v[228:231], a[128:131], v[112:115], v[36:39]\n"
"v_exp_f32 v225, v225\n"
"v_mfma_f32_16x16x32_bf16 v[228:231], a[132:135], v[116:119], v[228:231]\n"
"ds_read_b64_tr_b16 a[254:255], v245 offset:62208\n"
"v_cvt_pk_bf16_f32 v18, v224, v225\n"
"v_mfma_f32_16x16x32_bf16 v[228:231], a[136:139], v[120:123], v[228:231]\n"
"v_exp_f32 v226, v226\n"
"v_mfma_f32_16x16x32_bf16 v[228:231], a[140:143], v[124:127], v[228:231]\n"
"v_add_f32 v52, v52, v221\n"
"v_add_f32 v52, v52, v222\n"
"v_mfma_f32_16x16x32_bf16 v[232:235], a[144:147], v[112:115], v[36:39]\n"
"v_exp_f32 v227, v227\n"
"v_mfma_f32_16x16x32_bf16 v[232:235], a[148:151], v[116:119], v[232:235]\n"
"s_add_u32 m0, s53, 81920\n"
"v_cvt_pk_bf16_f32 v19, v226, v227\n"
"v_add_f32 v52, v52, v223\n"
"v_mfma_f32_16x16x32_bf16 v[232:235], a[152:155], v[120:123], v[232:235]\n"
"buffer_load_dwordx4 v239, s[24:27], s22 offen offset:0 lds\n"
"v_mfma_f32_16x16x32_bf16 v[232:235], a[156:159], v[124:127], v[232:235]\n"
"v_exp_f32 v228, v228\n"
"v_mfma_f32_16x16x32_bf16 a[64:67], a[192:195], v[16:19], a[64:67]\n"
"v_add_f32 v52, v52, v224\n"
"v_add_f32 v52, v52, v225\n"
"v_mfma_f32_16x16x32_bf16 a[68:71], a[196:199], v[16:19], a[68:71]\n"
"v_exp_f32 v229, v229\n"
"v_mfma_f32_16x16x32_bf16 a[72:75], a[200:203], v[16:19], a[72:75]\n"
"v_cvt_pk_bf16_f32 v20, v228, v229\n"
"v_add_f32 v52, v52, v226\n"
"v_mfma_f32_16x16x32_bf16 a[76:79], a[204:207], v[16:19], a[76:79]\n"
"v_exp_f32 v230, v230\n"
"v_mfma_f32_16x16x32_bf16 a[80:83], a[208:211], v[16:19], a[80:83]\n"
"v_add_f32 v52, v52, v227\n"
"v_mfma_f32_16x16x32_bf16 a[84:87], a[212:215], v[16:19], a[84:87]\n"
"v_exp_f32 v231, v231\n"
"v_mfma_f32_16x16x32_bf16 a[88:91], a[216:219], v[16:19], a[88:91]\n"
"s_waitcnt vmcnt(26) lgkmcnt(0)\n"
"s_barrier\n"
"v_cvt_pk_bf16_f32 v21, v230, v231\n"
"v_mfma_f32_16x16x32_bf16 a[92:95], a[220:223], v[16:19], a[92:95]\n"
"v_exp_f32 v232, v232\n"
"v_mfma_f32_16x16x32_bf16 v[0:3], a[160:163], v[64:67], v[24:27]\n"
"ds_read_b64_tr_b16 a[128:129], v202 offset:0\n"
"v_mfma_f32_16x16x32_bf16 v[0:3], a[164:167], v[68:71], v[0:3]\n"
"s_add_u32 m0, s53, 110592\n"
"v_exp_f32 v233, v233\n"
"v_mfma_f32_16x16x32_bf16 v[0:3], a[168:171], v[72:75], v[0:3]\n"
"buffer_load_dwordx4 v210, s[24:27], s23 offen offset:0 lds\n"
"ds_read_b64_tr_b16 a[130:131], v202 offset:4608\n"
"v_cvt_pk_bf16_f32 v22, v232, v233\n"
"v_mfma_f32_16x16x32_bf16 v[0:3], a[172:175], v[76:79], v[0:3]\n"
"v_exp_f32 v234, v234\n"
"v_mfma_f32_16x16x32_bf16 v[4:7], a[176:179], v[64:67], v[24:27]\n"
"ds_read_b64_tr_b16 a[132:133], v202 offset:32\n"
"v_mfma_f32_16x16x32_bf16 v[4:7], a[180:183], v[68:71], v[4:7]\n"
"v_exp_f32 v235, v235\n"
"v_mfma_f32_16x16x32_bf16 v[4:7], a[184:187], v[72:75], v[4:7]\n"
"ds_read_b64_tr_b16 a[134:135], v202 offset:4640\n"
"v_cvt_pk_bf16_f32 v23, v234, v235\n"
"v_mfma_f32_16x16x32_bf16 v[4:7], a[188:191], v[76:79], v[4:7]\n"
"s_add_u32 m0, s53, 110592\n"
"v_exp_f32 v0, v0\n"
"v_mfma_f32_16x16x32_bf16 a[96:99], a[192:195], v[20:23], a[96:99]\n"
"buffer_load_dwordx4 v211, s[24:27], s23 offen offset:1024 lds\n"
"v_mfma_f32_16x16x32_bf16 a[100:103], a[196:199], v[20:23], a[100:103]\n"
"v_exp_f32 v1, v1\n"
"v_mfma_f32_16x16x32_bf16 a[104:107], a[200:203], v[20:23], a[104:107]\n"
"ds_read_b64_tr_b16 a[136:137], v202 offset:64\n"
"v_cvt_pk_bf16_f32 v16, v0, v1\n"
"v_mfma_f32_16x16x32_bf16 a[108:111], a[204:207], v[20:23], a[108:111]\n"
"v_exp_f32 v2, v2\n"
"v_mfma_f32_16x16x32_bf16 a[112:115], a[208:211], v[20:23], a[112:115]\n"
"ds_read_b64_tr_b16 a[138:139], v202 offset:4672\n"
"v_add_f32 v44, v44, v0\n"
"v_mfma_f32_16x16x32_bf16 a[116:119], a[212:215], v[20:23], a[116:119]\n"
"v_exp_f32 v3, v3\n"
"v_mfma_f32_16x16x32_bf16 a[120:123], a[216:219], v[20:23], a[120:123]\n"
"ds_read_b64_tr_b16 a[140:141], v202 offset:96\n"
"v_cvt_pk_bf16_f32 v17, v2, v3\n"
"v_mfma_f32_16x16x32_bf16 a[124:127], a[220:223], v[20:23], a[124:127]\n"
"s_add_u32 m0, s53, 110592\n"
"v_exp_f32 v4, v4\n"
"v_mfma_f32_16x16x32_bf16 v[56:59], v[40:43], v[20:23], v[56:59]\n"
"buffer_load_dwordx4 v212, s[24:27], s23 offen offset:2048 lds\n"
"ds_read_b64_tr_b16 a[142:143], v202 offset:4704\n"
"v_add_f32 v44, v44, v1\n"
"v_mfma_f32_16x16x32_bf16 v[8:11], a[160:163], v[80:83], v[28:31]\n"
"v_exp_f32 v5, v5\n"
"v_mfma_f32_16x16x32_bf16 v[8:11], a[164:167], v[84:87], v[8:11]\n"
"ds_read_b64_tr_b16 a[144:145], v202 offset:128\n"
"v_cvt_pk_bf16_f32 v18, v4, v5\n"
"v_mfma_f32_16x16x32_bf16 v[8:11], a[168:171], v[88:91], v[8:11]\n"
"v_exp_f32 v6, v6\n"
"v_mfma_f32_16x16x32_bf16 v[8:11], a[172:175], v[92:95], v[8:11]\n"
"ds_read_b64_tr_b16 a[146:147], v202 offset:4736\n"
"v_add_f32 v44, v44, v2\n"
"v_mfma_f32_16x16x32_bf16 v[12:15], a[176:179], v[80:83], v[28:31]\n"
"v_exp_f32 v7, v7\n"
"v_mfma_f32_16x16x32_bf16 v[12:15], a[180:183], v[84:87], v[12:15]\n"
"s_add_u32 m0, s53, 110592\n"
"ds_read_b64_tr_b16 a[148:149], v202 offset:160\n"
"v_cvt_pk_bf16_f32 v19, v6, v7\n"
"v_mfma_f32_16x16x32_bf16 v[12:15], a[184:187], v[88:91], v[12:15]\n"
"buffer_load_dwordx4 v213, s[24:27], s23 offen offset:3072 lds\n"
"v_mfma_f32_16x16x32_bf16 v[12:15], a[188:191], v[92:95], v[12:15]\n"
"v_exp_f32 v8, v8\n"
"v_mfma_f32_16x16x32_bf16 a[0:3], a[224:227], v[16:19], a[0:3]\n"
"ds_read_b64_tr_b16 a[150:151], v202 offset:4768\n"
"v_add_f32 v44, v44, v3\n"
"v_mfma_f32_16x16x32_bf16 a[4:7], a[228:231], v[16:19], a[4:7]\n"
"v_exp_f32 v9, v9\n"
"v_mfma_f32_16x16x32_bf16 a[8:11], a[232:235], v[16:19], a[8:11]\n"
"ds_read_b64_tr_b16 a[152:153], v202 offset:192\n"
"v_cvt_pk_bf16_f32 v20, v8, v9\n"
"v_mfma_f32_16x16x32_bf16 a[12:15], a[236:239], v[16:19], a[12:15]\n"
"v_exp_f32 v10, v10\n"
"v_mfma_f32_16x16x32_bf16 a[16:19], a[240:243], v[16:19], a[16:19]\n"
"ds_read_b64_tr_b16 a[154:155], v202 offset:4800\n"
"v_add_f32 v44, v44, v4\n"
"v_mfma_f32_16x16x32_bf16 a[20:23], a[244:247], v[16:19], a[20:23]\n"
"s_add_u32 m0, s53, 114688\n"
"v_exp_f32 v11, v11\n"
"v_mfma_f32_16x16x32_bf16 a[24:27], a[248:251], v[16:19], a[24:27]\n"
"buffer_load_dwordx4 v214, s[24:27], s23 offen offset:0 lds\n"
"ds_read_b64_tr_b16 a[156:157], v202 offset:224\n"
"v_cvt_pk_bf16_f32 v21, v10, v11\n"
"v_mfma_f32_16x16x32_bf16 a[28:31], a[252:255], v[16:19], a[28:31]\n"
"v_exp_f32 v12, v12\n"
"v_mfma_f32_16x16x32_bf16 v[220:223], a[160:163], v[96:99], v[32:35]\n"
"ds_read_b64_tr_b16 a[158:159], v202 offset:4832\n"
"v_add_f32 v44, v44, v5\n"
"v_mfma_f32_16x16x32_bf16 v[220:223], a[164:167], v[100:103], v[220:223]\n"
"v_exp_f32 v13, v13\n"
"v_mfma_f32_16x16x32_bf16 v[220:223], a[168:171], v[104:107], v[220:223]\n"
"ds_read_b64_tr_b16 a[192:193], v205 offset:0\n"
"v_cvt_pk_bf16_f32 v22, v12, v13\n"
"v_mfma_f32_16x16x32_bf16 v[220:223], a[172:175], v[108:111], v[220:223]\n"
"v_exp_f32 v14, v14\n"
"v_mfma_f32_16x16x32_bf16 v[224:227], a[176:179], v[96:99], v[32:35]\n"
"ds_read_b64_tr_b16 a[194:195], v205 offset:4608\n"
"v_add_f32 v44, v44, v6\n"
"v_mfma_f32_16x16x32_bf16 v[224:227], a[180:183], v[100:103], v[224:227]\n"
"v_exp_f32 v15, v15\n"
"v_mfma_f32_16x16x32_bf16 v[224:227], a[184:187], v[104:107], v[224:227]\n"
"ds_read_b64_tr_b16 a[196:197], v202 offset:9216\n"
"v_cvt_pk_bf16_f32 v23, v14, v15\n"
"v_mfma_f32_16x16x32_bf16 v[224:227], a[188:191], v[108:111], v[224:227]\n"
"s_add_u32 m0, s53, 114688\n"
"v_exp_f32 v220, v220\n"
"v_mfma_f32_16x16x32_bf16 a[32:35], a[224:227], v[20:23], a[32:35]\n"
"buffer_load_dwordx4 v236, s[24:27], s23 offen offset:1024 lds\n"
"v_mfma_f32_16x16x32_bf16 a[36:39], a[228:231], v[20:23], a[36:39]\n"
"v_exp_f32 v221, v221\n"
"v_mfma_f32_16x16x32_bf16 a[40:43], a[232:235], v[20:23], a[40:43]\n"
"ds_read_b64_tr_b16 a[198:199], v202 offset:13824\n"
"v_cvt_pk_bf16_f32 v16, v220, v221\n"
"v_mfma_f32_16x16x32_bf16 a[44:47], a[236:239], v[20:23], a[44:47]\n"
"s_add_u32 m0, s53, 114688\n"
"v_exp_f32 v222, v222\n"
"v_mfma_f32_16x16x32_bf16 a[48:51], a[240:243], v[20:23], a[48:51]\n"
"buffer_load_dwordx4 v237, s[24:27], s23 offen offset:2048 lds\n"
"v_add_f32 v44, v44, v7\n"
"v_mfma_f32_16x16x32_bf16 a[52:55], a[244:247], v[20:23], a[52:55]\n"
"v_exp_f32 v223, v223\n"
"v_mfma_f32_16x16x32_bf16 a[56:59], a[248:251], v[20:23], a[56:59]\n"
"v_cvt_pk_bf16_f32 v17, v222, v223\n"
"v_mfma_f32_16x16x32_bf16 a[60:63], a[252:255], v[20:23], a[60:63]\n"
"s_add_u32 m0, s53, 114688\n"
"v_exp_f32 v224, v224\n"
"v_mfma_f32_16x16x32_bf16 v[48:51], v[40:43], v[20:23], v[48:51]\n"
"buffer_load_dwordx4 v238, s[24:27], s23 offen offset:3072 lds\n"
"v_add_f32 v52, v52, v220\n"
"v_mfma_f32_16x16x32_bf16 v[228:231], a[160:163], v[112:115], v[36:39]\n"
"v_exp_f32 v225, v225\n"
"v_mfma_f32_16x16x32_bf16 v[228:231], a[164:167], v[116:119], v[228:231]\n"
"v_cvt_pk_bf16_f32 v18, v224, v225\n"
"v_mfma_f32_16x16x32_bf16 v[228:231], a[168:171], v[120:123], v[228:231]\n"
"v_exp_f32 v226, v226\n"
"v_mfma_f32_16x16x32_bf16 v[228:231], a[172:175], v[124:127], v[228:231]\n"
"v_add_f32 v52, v52, v221\n"
"v_add_f32 v52, v52, v222\n"
"v_mfma_f32_16x16x32_bf16 v[232:235], a[176:179], v[112:115], v[36:39]\n"
"v_exp_f32 v227, v227\n"
"v_mfma_f32_16x16x32_bf16 v[232:235], a[180:183], v[116:119], v[232:235]\n"
"s_add_u32 m0, s53, 118784\n"
"v_cvt_pk_bf16_f32 v19, v226, v227\n"
"v_add_f32 v52, v52, v223\n"
"v_mfma_f32_16x16x32_bf16 v[232:235], a[184:187], v[120:123], v[232:235]\n"
"buffer_load_dwordx4 v239, s[24:27], s23 offen offset:0 lds\n"
"v_mfma_f32_16x16x32_bf16 v[232:235], a[188:191], v[124:127], v[232:235]\n"
"v_exp_f32 v228, v228\n"
"v_mfma_f32_16x16x32_bf16 a[64:67], a[224:227], v[16:19], a[64:67]\n"
"v_add_f32 v52, v52, v224\n"
"v_add_f32 v52, v52, v225\n"
"v_mfma_f32_16x16x32_bf16 a[68:71], a[228:231], v[16:19], a[68:71]\n"
"v_exp_f32 v229, v229\n"
"v_mfma_f32_16x16x32_bf16 a[72:75], a[232:235], v[16:19], a[72:75]\n"
"v_cvt_pk_bf16_f32 v20, v228, v229\n"
"v_add_f32 v52, v52, v226\n"
"v_mfma_f32_16x16x32_bf16 a[76:79], a[236:239], v[16:19], a[76:79]\n"
"v_exp_f32 v230, v230\n"
"v_mfma_f32_16x16x32_bf16 a[80:83], a[240:243], v[16:19], a[80:83]\n"
"v_add_f32 v52, v52, v227\n"
"v_mfma_f32_16x16x32_bf16 a[84:87], a[244:247], v[16:19], a[84:87]\n"
"v_exp_f32 v231, v231\n"
"v_mfma_f32_16x16x32_bf16 a[88:91], a[248:251], v[16:19], a[88:91]\n"
"v_cvt_pk_bf16_f32 v21, v230, v231\n"
"v_mfma_f32_16x16x32_bf16 a[92:95], a[252:255], v[16:19], a[92:95]\n"
"v_exp_f32 v232, v232\n"
"v_exp_f32 v233, v233\n"
"s_nop 0\n"
"v_cvt_pk_bf16_f32 v22, v232, v233\n"
"s_nop 0\n"
"v_exp_f32 v234, v234\n"
"s_nop 0\n"
"v_exp_f32 v235, v235\n"
"s_nop 0\n"
"v_cvt_pk_bf16_f32 v23, v234, v235\n"
"s_nop 0\n"
"s_nop 1\n"
"v_mfma_f32_16x16x32_bf16 a[96:99], a[224:227], v[20:23], a[96:99]\n"
"v_mfma_f32_16x16x32_bf16 a[100:103], a[228:231], v[20:23], a[100:103]\n"
"v_mfma_f32_16x16x32_bf16 a[104:107], a[232:235], v[20:23], a[104:107]\n"
"v_mfma_f32_16x16x32_bf16 a[108:111], a[236:239], v[20:23], a[108:111]\n"
"v_mfma_f32_16x16x32_bf16 a[112:115], a[240:243], v[20:23], a[112:115]\n"
"v_mfma_f32_16x16x32_bf16 a[116:119], a[244:247], v[20:23], a[116:119]\n"
"v_mfma_f32_16x16x32_bf16 a[120:123], a[248:251], v[20:23], a[120:123]\n"
"v_mfma_f32_16x16x32_bf16 a[124:127], a[252:255], v[20:23], a[124:127]\n"
"v_mfma_f32_16x16x32_bf16 v[56:59], v[40:43], v[20:23], v[56:59]\n"
"s_nop 7\n"
"v_mov_b32 v0, v44\n"
"v_mov_b32 v1, v52\n"
"s_nop 0\n"
"v_permlane16_swap_b32 v44, v0\n"
"v_permlane16_swap_b32 v52, v1\n"
"v_add_f32 v44, v44, v0\n"
"v_add_f32 v52, v52, v1\n"
"v_mov_b32 v0, v44\n"
"v_mov_b32 v1, v52\n"
"s_nop 0\n"
"v_permlane32_swap_b32 v44, v0\n"
"v_permlane32_swap_b32 v52, v1\n"
"v_add_f32 v44, v44, v0\n"
"v_add_f32 v52, v52, v1\n"
"v_mov_b32 v219, 0x5f800000\n"
"v_cmp_nle_f32 s[56:57], v44, v219\n"
"v_cmp_nle_f32 s[58:59], v48, v219\n"
"v_cmp_nle_f32 s[60:61], v52, v219\n"
"v_cmp_nle_f32 s[62:63], v56, v219\n"
"v_rcp_f32 v218, v44\n"
"v_rcp_f32 v220, v48\n"
"v_rcp_f32 v222, v52\n"
"v_rcp_f32 v224, v56\n"
"s_or_b64 s[56:57], s[56:57], s[58:59]\n"
"s_or_b64 s[60:61], s[60:61], s[62:63]\n"
"s_or_b64 s[56:57], s[56:57], s[60:61]\n"
"s_cselect_b32 s56, 1, 0\n"
"s_cmp_eq_u32 s45, 0\n"
"s_cselect_b32 s56, s56, 0\n"
"s_lshl_b32 s57, s41, 2\n"
"s_add_u32 s57, s57, s40\n"
"s_add_u32 s57, s57, 163776\n"
"v_mov_b32 v234, s56\n"
"v_mov_b32 v235, s57\n"
"ds_write_b32 v235, v234\n"
"s_and_b32 s60, s55, 4294967292\n"
"s_mov_b32 s23, 9576448\n"
"s_lshl_b32 s62, s60, 6\n"
"v_and_b32 v215, 3, v248\n"
"v_lshlrev_b32 v215, 6, v215\n"
"v_add_u32 v215, s62, v215\n"
"v_min_u32 v251, 1, v253\n"
"v_lshlrev_b32 v251, 28, v251\n"
"s_lshl_b32 s62, s55, 6\n"
"v_add_u32 v251, s62, v251\n"
"v_mov_b32 v252, s85\n"
"v_accvgpr_read_b32 v226, a0\n"
"v_accvgpr_read_b32 v227, a1\n"
"v_accvgpr_read_b32 v228, a2\n"
"v_accvgpr_read_b32 v229, a3\n"
"v_accvgpr_read_b32 v230, a4\n"
"v_accvgpr_read_b32 v231, a5\n"
"v_accvgpr_read_b32 v232, a6\n"
"v_accvgpr_read_b32 v233, a7\n"
"v_pk_mul_f32 v[226:227], v[226:227], v[218:219] op_sel_hi:[1,0]\n"
"v_pk_mul_f32 v[228:229], v[228:229], v[218:219] op_sel_hi:[1,0]\n"
"v_pk_mul_f32 v[230:231], v[230:231], v[218:219] op_sel_hi:[1,0]\n"
"v_pk_mul_f32 v[232:233], v[232:233], v[218:219] op_sel_hi:[1,0]\n"
"v_cvt_pk_bf16_f32 v0, v226, v227\n"
"v_cvt_pk_bf16_f32 v1, v228, v229\n"
"v_cvt_pk_bf16_f32 v2, v230, v231\n"
"v_cvt_pk_bf16_f32 v3, v232, v233\n"
"buffer_store_dwordx4 v[0:3], v207, s[28:31], s52 offen offset:0 sc1\n"
"v_accvgpr_read_b32 v226, a64\n"
"v_accvgpr_read_b32 v227, a65\n"
"v_accvgpr_read_b32 v228, a66\n"
"v_accvgpr_read_b32 v229, a67\n"
"v_accvgpr_read_b32 v230, a68\n"
"v_accvgpr_read_b32 v231, a69\n"
"v_accvgpr_read_b32 v232, a70\n"
"v_accvgpr_read_b32 v233, a71\n"
"v_pk_mul_f32 v[226:227], v[226:227], v[222:223] op_sel_hi:[1,0]\n"
"v_pk_mul_f32 v[228:229], v[228:229], v[222:223] op_sel_hi:[1,0]\n"
"v_pk_mul_f32 v[230:231], v[230:231], v[222:223] op_sel_hi:[1,0]\n"
"v_pk_mul_f32 v[232:233], v[232:233], v[222:223] op_sel_hi:[1,0]\n"
"v_cvt_pk_bf16_f32 v32, v226, v227\n"
"v_cvt_pk_bf16_f32 v33, v228, v229\n"
"v_cvt_pk_bf16_f32 v34, v230, v231\n"
"v_cvt_pk_bf16_f32 v35, v232, v233\n"
"buffer_store_dwordx4 v[32:35], v207, s[28:31], s52 offen offset:1024 sc1\n"
"v_accvgpr_read_b32 v226, a8\n"
"v_accvgpr_read_b32 v227, a9\n"
"v_accvgpr_read_b32 v228, a10\n"
"v_accvgpr_read_b32 v229, a11\n"
"v_accvgpr_read_b32 v230, a12\n"
"v_accvgpr_read_b32 v231, a13\n"
"v_accvgpr_read_b32 v232, a14\n"
"v_accvgpr_read_b32 v233, a15\n"
"v_pk_mul_f32 v[226:227], v[226:227], v[218:219] op_sel_hi:[1,0]\n"
"v_pk_mul_f32 v[228:229], v[228:229], v[218:219] op_sel_hi:[1,0]\n"
"v_pk_mul_f32 v[230:231], v[230:231], v[218:219] op_sel_hi:[1,0]\n"
"v_pk_mul_f32 v[232:233], v[232:233], v[218:219] op_sel_hi:[1,0]\n"
"v_cvt_pk_bf16_f32 v4, v226, v227\n"
"v_cvt_pk_bf16_f32 v5, v228, v229\n"
"v_cvt_pk_bf16_f32 v6, v230, v231\n"
"v_cvt_pk_bf16_f32 v7, v232, v233\n"
"buffer_store_dwordx4 v[4:7], v207, s[28:31], s52 offen offset:2048 sc1\n"
"v_accvgpr_read_b32 v226, a72\n"
"v_accvgpr_read_b32 v227, a73\n"
"v_accvgpr_read_b32 v228, a74\n"
"v_accvgpr_read_b32 v229, a75\n"
"v_accvgpr_read_b32 v230, a76\n"
"v_accvgpr_read_b32 v231, a77\n"
"v_accvgpr_read_b32 v232, a78\n"
"v_accvgpr_read_b32 v233, a79\n"
"v_pk_mul_f32 v[226:227], v[226:227], v[222:223] op_sel_hi:[1,0]\n"
"v_pk_mul_f32 v[228:229], v[228:229], v[222:223] op_sel_hi:[1,0]\n"
"v_pk_mul_f32 v[230:231], v[230:231], v[222:223] op_sel_hi:[1,0]\n"
"v_pk_mul_f32 v[232:233], v[232:233], v[222:223] op_sel_hi:[1,0]\n"
"v_cvt_pk_bf16_f32 v36, v226, v227\n"
"v_cvt_pk_bf16_f32 v37, v228, v229\n"
"v_cvt_pk_bf16_f32 v38, v230, v231\n"
"v_cvt_pk_bf16_f32 v39, v232, v233\n"
"buffer_store_dwordx4 v[36:39], v207, s[28:31], s52 offen offset:3072 sc1\n"
"s_add_u32 s52, s52, 4096\n"
"v_accvgpr_read_b32 v226, a16\n"
"v_accvgpr_read_b32 v227, a17\n"
"v_accvgpr_read_b32 v228, a18\n"
"v_accvgpr_read_b32 v229, a19\n"
"v_accvgpr_read_b32 v230, a20\n"
"v_accvgpr_read_b32 v231, a21\n"
"v_accvgpr_read_b32 v232, a22\n"
"v_accvgpr_read_b32 v233, a23\n"
"v_pk_mul_f32 v[226:227], v[226:227], v[218:219] op_sel_hi:[1,0]\n"
"v_pk_mul_f32 v[228:229], v[228:229], v[218:219] op_sel_hi:[1,0]\n"
"v_pk_mul_f32 v[230:231], v[230:231], v[218:219] op_sel_hi:[1,0]\n"
"v_pk_mul_f32 v[232:233], v[232:233], v[218:219] op_sel_hi:[1,0]\n"
"v_cvt_pk_bf16_f32 v8, v226, v227\n"
"v_cvt_pk_bf16_f32 v9, v228, v229\n"
"v_cvt_pk_bf16_f32 v10, v230, v231\n"
"v_cvt_pk_bf16_f32 v11, v232, v233\n"
"buffer_store_dwordx4 v[8:11], v207, s[28:31], s52 offen offset:0 sc1\n"
"v_accvgpr_read_b32 v226, a80\n"
"v_accvgpr_read_b32 v227, a81\n"
"v_accvgpr_read_b32 v228, a82\n"
"v_accvgpr_read_b32 v229, a83\n"
"v_accvgpr_read_b32 v230, a84\n"
"v_accvgpr_read_b32 v231, a85\n"
"v_accvgpr_read_b32 v232, a86\n"
"v_accvgpr_read_b32 v233, a87\n"
"v_pk_mul_f32 v[226:227], v[226:227], v[222:223] op_sel_hi:[1,0]\n"
"v_pk_mul_f32 v[228:229], v[228:229], v[222:223] op_sel_hi:[1,0]\n"
"v_pk_mul_f32 v[230:231], v[230:231], v[222:223] op_sel_hi:[1,0]\n"
"v_pk_mul_f32 v[232:233], v[232:233], v[222:223] op_sel_hi:[1,0]\n"
"v_cvt_pk_bf16_f32 v40, v226, v227\n"
"v_cvt_pk_bf16_f32 v41, v228, v229\n"
"v_cvt_pk_bf16_f32 v42, v230, v231\n"
"v_cvt_pk_bf16_f32 v43, v232, v233\n"
"buffer_store_dwordx4 v[40:43], v207, s[28:31], s52 offen offset:1024 sc1\n"
"v_accvgpr_read_b32 v226, a24\n"
"v_accvgpr_read_b32 v227, a25\n"
"v_accvgpr_read_b32 v228, a26\n"
"v_accvgpr_read_b32 v229, a27\n"
"v_accvgpr_read_b32 v230, a28\n"
"v_accvgpr_read_b32 v231, a29\n"
"v_accvgpr_read_b32 v232, a30\n"
"v_accvgpr_read_b32 v233, a31\n"
"v_pk_mul_f32 v[226:227], v[226:227], v[218:219] op_sel_hi:[1,0]\n"
"v_pk_mul_f32 v[228:229], v[228:229], v[218:219] op_sel_hi:[1,0]\n"
"v_pk_mul_f32 v[230:231], v[230:231], v[218:219] op_sel_hi:[1,0]\n"
"v_pk_mul_f32 v[232:233], v[232:233], v[218:219] op_sel_hi:[1,0]\n"
"v_cvt_pk_bf16_f32 v12, v226, v227\n"
"v_cvt_pk_bf16_f32 v13, v228, v229\n"
"v_cvt_pk_bf16_f32 v14, v230, v231\n"
"v_cvt_pk_bf16_f32 v15, v232, v233\n"
"buffer_store_dwordx4 v[12:15], v207, s[28:31], s52 offen offset:2048 sc1\n"
"v_accvgpr_read_b32 v226, a88\n"
"v_accvgpr_read_b32 v227, a89\n"
"v_accvgpr_read_b32 v228, a90\n"
"v_accvgpr_read_b32 v229, a91\n"
"v_accvgpr_read_b32 v230, a92\n"
"v_accvgpr_read_b32 v231, a93\n"
"v_accvgpr_read_b32 v232, a94\n"
"v_accvgpr_read_b32 v233, a95\n"
"v_pk_mul_f32 v[226:227], v[226:227], v[222:223] op_sel_hi:[1,0]\n"
"v_pk_mul_f32 v[228:229], v[228:229], v[222:223] op_sel_hi:[1,0]\n"
"v_pk_mul_f32 v[230:231], v[230:231], v[222:223] op_sel_hi:[1,0]\n"
"v_pk_mul_f32 v[232:233], v[232:233], v[222:223] op_sel_hi:[1,0]\n"
"v_cvt_pk_bf16_f32 v44, v226, v227\n"
"v_cvt_pk_bf16_f32 v45, v228, v229\n"
"v_cvt_pk_bf16_f32 v46, v230, v231\n"
"v_cvt_pk_bf16_f32 v47, v232, v233\n"
"buffer_store_dwordx4 v[44:47], v207, s[28:31], s52 offen offset:3072 sc1\n"
"s_add_u32 s52, s52, 4096\n"
"s_waitcnt vmcnt(29) lgkmcnt(0)\n"
"s_barrier\n"
"ds_read_b32 v234, v209\n"
"v_accvgpr_read_b32 v226, a32\n"
"v_accvgpr_read_b32 v227, a33\n"
"v_accvgpr_read_b32 v228, a34\n"
"v_accvgpr_read_b32 v229, a35\n"
"v_accvgpr_read_b32 v230, a36\n"
"v_accvgpr_read_b32 v231, a37\n"
"v_accvgpr_read_b32 v232, a38\n"
"v_accvgpr_read_b32 v233, a39\n"
"v_pk_mul_f32 v[226:227], v[226:227], v[220:221] op_sel_hi:[1,0]\n"
"v_pk_mul_f32 v[228:229], v[228:229], v[220:221] op_sel_hi:[1,0]\n"
"v_pk_mul_f32 v[230:231], v[230:231], v[220:221] op_sel_hi:[1,0]\n"
"v_pk_mul_f32 v[232:233], v[232:233], v[220:221] op_sel_hi:[1,0]\n"
"v_cvt_pk_bf16_f32 v16, v226, v227\n"
"v_cvt_pk_bf16_f32 v17, v228, v229\n"
"v_cvt_pk_bf16_f32 v18, v230, v231\n"
"v_cvt_pk_bf16_f32 v19, v232, v233\n"
"buffer_store_dwordx4 v[16:19], v207, s[28:31], s52 offen offset:0 sc1\n"
"s_add_u32 s61, s43, 1\n"
"s_and_b32 s61, s61, 3\n"
"s_add_u32 s61, s61, s60\n"
"s_lshl_b32 s61, s61, 2\n"
"s_add_u32 s61, s61, s41\n"
"s_lshl_b32 s61, s61, 14\n"
"s_add_u32 s20, s61, 9592832\n"
"v_accvgpr_read_b32 v226, a96\n"
"v_accvgpr_read_b32 v227, a97\n"
"v_accvgpr_read_b32 v228, a98\n"
"v_accvgpr_read_b32 v229, a99\n"
"v_accvgpr_read_b32 v230, a100\n"
"v_accvgpr_read_b32 v231, a101\n"
"v_accvgpr_read_b32 v232, a102\n"
"v_accvgpr_read_b32 v233, a103\n"
"v_pk_mul_f32 v[226:227], v[226:227], v[224:225] op_sel_hi:[1,0]\n"
"v_pk_mul_f32 v[228:229], v[228:229], v[224:225] op_sel_hi:[1,0]\n"
"v_pk_mul_f32 v[230:231], v[230:231], v[224:225] op_sel_hi:[1,0]\n"
"v_pk_mul_f32 v[232:233], v[232:233], v[224:225] op_sel_hi:[1,0]\n"
"v_cvt_pk_bf16_f32 v48, v226, v227\n"
"v_cvt_pk_bf16_f32 v49, v228, v229\n"
"v_cvt_pk_bf16_f32 v50, v230, v231\n"
"v_cvt_pk_bf16_f32 v51, v232, v233\n"
"buffer_store_dwordx4 v[48:51], v207, s[28:31], s52 offen offset:1024 sc1\n"
"s_add_u32 s61, s43, 2\n"
"s_and_b32 s61, s61, 3\n"
"s_add_u32 s61, s61, s60\n"
"s_lshl_b32 s61, s61, 2\n"
"s_add_u32 s61, s61, s41\n"
"s_lshl_b32 s61, s61, 14\n"
"s_add_u32 s21, s61, 9592832\n"
"v_accvgpr_read_b32 v226, a40\n"
"v_accvgpr_read_b32 v227, a41\n"
"v_accvgpr_read_b32 v228, a42\n"
"v_accvgpr_read_b32 v229, a43\n"
"v_accvgpr_read_b32 v230, a44\n"
"v_accvgpr_read_b32 v231, a45\n"
"v_accvgpr_read_b32 v232, a46\n"
"v_accvgpr_read_b32 v233, a47\n"
"v_pk_mul_f32 v[226:227], v[226:227], v[220:221] op_sel_hi:[1,0]\n"
"v_pk_mul_f32 v[228:229], v[228:229], v[220:221] op_sel_hi:[1,0]\n"
"v_pk_mul_f32 v[230:231], v[230:231], v[220:221] op_sel_hi:[1,0]\n"
"v_pk_mul_f32 v[232:233], v[232:233], v[220:221] op_sel_hi:[1,0]\n"
"v_cvt_pk_bf16_f32 v20, v226, v227\n"
"v_cvt_pk_bf16_f32 v21, v228, v229\n"
"v_cvt_pk_bf16_f32 v22, v230, v231\n"
"v_cvt_pk_bf16_f32 v23, v232, v233\n"
"buffer_store_dwordx4 v[20:23], v207, s[28:31], s52 offen offset:2048 sc1\n"
"s_add_u32 s61, s43, 3\n"
"s_and_b32 s61, s61, 3\n"
"s_add_u32 s61, s61, s60\n"
"s_lshl_b32 s61, s61, 2\n"
"s_add_u32 s61, s61, s41\n"
"s_lshl_b32 s61, s61, 14\n"
"s_add_u32 s22, s61, 9592832\n"
"v_accvgpr_read_b32 v226, a104\n"
"v_accvgpr_read_b32 v227, a105\n"
"v_accvgpr_read_b32 v228, a106\n"
"v_accvgpr_read_b32 v229, a107\n"
"v_accvgpr_read_b32 v230, a108\n"
"v_accvgpr_read_b32 v231, a109\n"
"v_accvgpr_read_b32 v232, a110\n"
"v_accvgpr_read_b32 v233, a111\n"
"v_pk_mul_f32 v[226:227], v[226:227], v[224:225] op_sel_hi:[1,0]\n"
"v_pk_mul_f32 v[228:229], v[228:229], v[224:225] op_sel_hi:[1,0]\n"
"v_pk_mul_f32 v[230:231], v[230:231], v[224:225] op_sel_hi:[1,0]\n"
"v_pk_mul_f32 v[232:233], v[232:233], v[224:225] op_sel_hi:[1,0]\n"
"v_cvt_pk_bf16_f32 v52, v226, v227\n"
"v_cvt_pk_bf16_f32 v53, v228, v229\n"
"v_cvt_pk_bf16_f32 v54, v230, v231\n"
"v_cvt_pk_bf16_f32 v55, v232, v233\n"
"buffer_store_dwordx4 v[52:55], v207, s[28:31], s52 offen offset:3072 sc1\n"
"s_add_u32 s52, s52, 4096\n"
"v_accvgpr_read_b32 v226, a48\n"
"v_accvgpr_read_b32 v227, a49\n"
"v_accvgpr_read_b32 v228, a50\n"
"v_accvgpr_read_b32 v229, a51\n"
"v_accvgpr_read_b32 v230, a52\n"
"v_accvgpr_read_b32 v231, a53\n"
"v_accvgpr_read_b32 v232, a54\n"
"v_accvgpr_read_b32 v233, a55\n"
"v_pk_mul_f32 v[226:227], v[226:227], v[220:221] op_sel_hi:[1,0]\n"
"v_pk_mul_f32 v[228:229], v[228:229], v[220:221] op_sel_hi:[1,0]\n"
"v_pk_mul_f32 v[230:231], v[230:231], v[220:221] op_sel_hi:[1,0]\n"
"v_pk_mul_f32 v[232:233], v[232:233], v[220:221] op_sel_hi:[1,0]\n"
"v_cvt_pk_bf16_f32 v24, v226, v227\n"
"v_cvt_pk_bf16_f32 v25, v228, v229\n"
"v_cvt_pk_bf16_f32 v26, v230, v231\n"
"v_cvt_pk_bf16_f32 v27, v232, v233\n"
"buffer_store_dwordx4 v[24:27], v207, s[28:31], s52 offen offset:0 sc1\n"
"v_accvgpr_read_b32 v226, a112\n"
"v_accvgpr_read_b32 v227, a113\n"
"v_accvgpr_read_b32 v228, a114\n"
"v_accvgpr_read_b32 v229, a115\n"
"v_accvgpr_read_b32 v230, a116\n"
"v_accvgpr_read_b32 v231, a117\n"
"v_accvgpr_read_b32 v232, a118\n"
"v_accvgpr_read_b32 v233, a119\n"
"v_pk_mul_f32 v[226:227], v[226:227], v[224:225] op_sel_hi:[1,0]\n"
"v_pk_mul_f32 v[228:229], v[228:229], v[224:225] op_sel_hi:[1,0]\n"
"v_pk_mul_f32 v[230:231], v[230:231], v[224:225] op_sel_hi:[1,0]\n"
"v_pk_mul_f32 v[232:233], v[232:233], v[224:225] op_sel_hi:[1,0]\n"
"v_cvt_pk_bf16_f32 v56, v226, v227\n"
"v_cvt_pk_bf16_f32 v57, v228, v229\n"
"v_cvt_pk_bf16_f32 v58, v230, v231\n"
"v_cvt_pk_bf16_f32 v59, v232, v233\n"
"buffer_store_dwordx4 v[56:59], v207, s[28:31], s52 offen offset:1024 sc1\n"
"v_accvgpr_read_b32 v226, a56\n"
"v_accvgpr_read_b32 v227, a57\n"
"v_accvgpr_read_b32 v228, a58\n"
"v_accvgpr_read_b32 v229, a59\n"
"v_accvgpr_read_b32 v230, a60\n"
"v_accvgpr_read_b32 v231, a61\n"
"v_accvgpr_read_b32 v232, a62\n"
"v_accvgpr_read_b32 v233, a63\n"
"v_pk_mul_f32 v[226:227], v[226:227], v[220:221] op_sel_hi:[1,0]\n"
"v_pk_mul_f32 v[228:229], v[228:229], v[220:221] op_sel_hi:[1,0]\n"
"v_pk_mul_f32 v[230:231], v[230:231], v[220:221] op_sel_hi:[1,0]\n"
"v_pk_mul_f32 v[232:233], v[232:233], v[220:221] op_sel_hi:[1,0]\n"
"v_cvt_pk_bf16_f32 v28, v226, v227\n"
"v_cvt_pk_bf16_f32 v29, v228, v229\n"
"v_cvt_pk_bf16_f32 v30, v230, v231\n"
"v_cvt_pk_bf16_f32 v31, v232, v233\n"
"buffer_store_dwordx4 v[28:31], v207, s[28:31], s52 offen offset:2048 sc1\n"
"v_accvgpr_read_b32 v226, a120\n"
"v_accvgpr_read_b32 v227, a121\n"
"v_accvgpr_read_b32 v228, a122\n"
"v_accvgpr_read_b32 v229, a123\n"
"v_accvgpr_read_b32 v230, a124\n"
"v_accvgpr_read_b32 v231, a125\n"
"v_accvgpr_read_b32 v232, a126\n"
"v_accvgpr_read_b32 v233, a127\n"
"v_pk_mul_f32 v[226:227], v[226:227], v[224:225] op_sel_hi:[1,0]\n"
"v_pk_mul_f32 v[228:229], v[228:229], v[224:225] op_sel_hi:[1,0]\n"
"v_pk_mul_f32 v[230:231], v[230:231], v[224:225] op_sel_hi:[1,0]\n"
"v_pk_mul_f32 v[232:233], v[232:233], v[224:225] op_sel_hi:[1,0]\n"
"v_cvt_pk_bf16_f32 v60, v226, v227\n"
"v_cvt_pk_bf16_f32 v61, v228, v229\n"
"v_cvt_pk_bf16_f32 v62, v230, v231\n"
"v_cvt_pk_bf16_f32 v63, v232, v233\n"
"buffer_store_dwordx4 v[60:63], v207, s[28:31], s52 offen offset:3072 sc1\n"
"s_add_u32 s52, s52, 4096\n"
"s_waitcnt lgkmcnt(0)\n"
"v_cmp_ne_u32 vcc, 0, v234\n"
"s_nop 0\n"
"s_cbranch_vccnz AT_fb\n"
"v_mfma_f32_16x16x32_bf16 v[64:67], a[128:131], v[0:3], 0\n"
"s_mov_b32 s56, 41984\n"
"ds_read_b64_tr_b16 a[200:201], v202 offset:9248\n"
"v_mfma_f32_16x16x32_bf16 v[68:71], a[128:131], v[32:35], 0\n"
"s_mov_b32 s57, 46592\n"
"ds_read_b64_tr_b16 a[202:203], v202 offset:13856\n"
"v_mfma_f32_16x16x32_bf16 v[72:75], a[132:135], v[0:3], 0\n"
"s_mov_b32 s58, 51200\n"
"ds_read_b64_tr_b16 a[204:205], v202 offset:9280\n"
"v_mfma_f32_16x16x32_bf16 v[76:79], a[132:135], v[32:35], 0\n"
"s_mov_b32 s59, 55808\n"
"ds_read_b64_tr_b16 a[206:207], v202 offset:13888\n"
"v_mfma_f32_16x16x32_bf16 v[80:83], a[136:139], v[0:3], 0\n"
"ds_read_b64_tr_b16 a[208:209], v202 offset:9312\n"
"v_lshlrev_b32 v246, 2, v254\n"
"v_mfma_f32_16x16x32_bf16 v[84:87], a[136:139], v[32:35], 0\n"
"s_lshl_b32 s54, s44, 2\n"
"ds_read_b64_tr_b16 a[210:211], v202 offset:13920\n"
"v_mfma_f32_16x16x32_bf16 v[88:91], a[140:143], v[0:3], 0\n"
"s_add_u32 s54, s54, s41\n"
"ds_read_b64_tr_b16 a[212:213], v202 offset:9344\n"
"v_mfma_f32_16x16x32_bf16 v[92:95], a[140:143], v[32:35], 0\n"
"s_mul_i32 s54, s54, 36864\n"
"ds_read_b64_tr_b16 a[214:215], v202 offset:13952\n"
"v_mfma_f32_16x16x32_bf16 v[96:99], a[144:147], v[0:3], 0\n"
"s_lshr_b32 s62, s43, 2\n"
"ds_read_b64_tr_b16 a[216:217], v202 offset:9376\n"
"v_mfma_f32_16x16x32_bf16 v[100:103], a[144:147], v[32:35], 0\n"
"s_mul_i32 s62, s62, 2359296\n"
"ds_read_b64_tr_b16 a[218:219], v202 offset:13984\n"
"v_mfma_f32_16x16x32_bf16 v[104:107], a[148:151], v[0:3], 0\n"
"s_add_u32 s54, s54, s62\n"
"ds_read_b64_tr_b16 a[220:221], v202 offset:9408\n"
"v_mfma_f32_16x16x32_bf16 v[108:111], a[148:151], v[32:35], 0\n"
"s_add_u32 s54, s54, 139264\n"
"ds_read_b64_tr_b16 a[222:223], v202 offset:14016\n"
"v_mfma_f32_16x16x32_bf16 v[112:115], a[152:155], v[0:3], 0\n"
"s_bfe_u32 s62, s43, 0x10001\n"
"ds_read_b64_tr_b16 a[128:129], v202 offset:9440\n"
"v_mfma_f32_16x16x32_bf16 v[116:119], a[152:155], v[32:35], 0\n"
"s_mul_i32 s62, s62, 9216\n"
"ds_read_b64_tr_b16 a[130:131], v202 offset:14048\n"
"v_mfma_f32_16x16x32_bf16 v[120:123], a[156:159], v[0:3], 0\n"
"s_add_u32 s54, s54, s62\n"
"ds_read_b64_tr_b16 a[132:133], v205 offset:9216\n"
"v_mfma_f32_16x16x32_bf16 v[124:127], a[156:159], v[32:35], 0\n"
"s_and_b32 s62, s43, 1\n"
"ds_read_b64_tr_b16 a[134:135], v205 offset:13824\n"
"v_mfma_f32_16x16x32_bf16 v[226:229], a[192:195], v[0:3], 0\n"
"s_lshl_b32 s49, s62, 3\n"
"ds_read_b64_tr_b16 a[136:137], v202 offset:18432\n"
"v_mfma_f32_16x16x32_bf16 v[230:233], a[192:195], v[32:35], 0\n"
"s_add_u32 s49, s49, s54\n"
"ds_read_b64_tr_b16 a[138:139], v202 offset:23040\n"
"v_mfma_f32_16x16x32_bf16 v[64:67], a[196:199], v[4:7], v[64:67]\n"
"s_add_u32 s49, s49, 4096\n"
"ds_read_b64_tr_b16 a[140:141], v202 offset:18464\n"
"v_mfma_f32_16x16x32_bf16 v[68:71], a[196:199], v[36:39], v[68:71]\n"
"s_waitcnt lgkmcnt(11)\n"
"s_mul_i32 s62, s62, 5120\n"
"ds_read_b64_tr_b16 a[142:143], v202 offset:23072\n"
"v_mfma_f32_16x16x32_bf16 v[72:75], a[200:203], v[4:7], v[72:75]\n"
"s_add_u32 s54, s54, s62\n"
"ds_read_b64_tr_b16 a[144:145], v202 offset:18496\n"
"v_mfma_f32_16x16x32_bf16 v[76:79], a[200:203], v[36:39], v[76:79]\n"
"s_mul_i32 s46, s55, 9216\n"
"ds_read_b64_tr_b16 a[146:147], v202 offset:23104\n"
"v_mfma_f32_16x16x32_bf16 v[80:83], a[204:207], v[4:7], v[80:83]\n"
"s_min_u32 s62, s41, 2\n"
"ds_read_b64_tr_b16 a[148:149], v202 offset:18528\n"
"v_mfma_f32_16x16x32_bf16 v[84:87], a[204:207], v[36:39], v[84:87]\n"
"s_mul_i32 s62, s62, 3072\n"
"ds_read_b64_tr_b16 a[150:151], v202 offset:23136\n"
"v_mfma_f32_16x16x32_bf16 v[88:91], a[208:211], v[4:7], v[88:91]\n"
"s_add_u32 s46, s46, s62\n"
"ds_read_b64_tr_b16 a[152:153], v202 offset:18560\n"
"v_mfma_f32_16x16x32_bf16 v[92:95], a[208:211], v[36:39], v[92:95]\n"
"s_add_u32 s45, s40, s62\n"
"ds_read_b64_tr_b16 a[154:155], v202 offset:23168\n"
"v_mfma_f32_16x16x32_bf16 v[96:99], a[212:215], v[4:7], v[96:99]\n"
"s_lshr_b32 s47, s55, 1\n"
"ds_read_b64_tr_b16 a[156:157], v202 offset:18592\n"
"v_mfma_f32_16x16x32_bf16 v[100:103], a[212:215], v[36:39], v[100:103]\n"
"s_waitcnt lgkmcnt(11)\n"
"s_mul_i32 s47, s47, 18432\n"
"ds_read_b64_tr_b16 a[158:159], v202 offset:23200\n"
"v_mfma_f32_16x16x32_bf16 v[104:107], a[216:219], v[4:7], v[104:107]\n"
"s_and_b32 s62, s55, 1\n"
"ds_read_b64_tr_b16 a[192:193], v202 offset:18624\n"
"v_mfma_f32_16x16x32_bf16 v[108:111], a[216:219], v[36:39], v[108:111]\n"
"s_lshl_b32 s62, s62, 7\n"
"ds_read_b64_tr_b16 a[194:195], v202 offset:23232\n"
"v_mfma_f32_16x16x32_bf16 v[112:115], a[220:223], v[4:7], v[112:115]\n"
"s_add_u32 s47, s47, s62\n"
"ds_read_b64_tr_b16 a[196:197], v202 offset:18656\n"
"v_mfma_f32_16x16x32_bf16 v[116:119], a[220:223], v[36:39], v[116:119]\n"
"s_add_u32 s47, s47, 139264\n"
"ds_read_b64_tr_b16 a[198:199], v202 offset:23264\n"
"v_mfma_f32_16x16x32_bf16 v[120:123], a[128:131], v[4:7], v[120:123]\n"
"s_mul_i32 s48, s55, 73728\n"
"ds_read_b64_tr_b16 a[200:201], v205 offset:18432\n"
"v_mfma_f32_16x16x32_bf16 v[124:127], a[128:131], v[36:39], v[124:127]\n"
"s_add_u32 s48, s48, 37888000\n"
"ds_read_b64_tr_b16 a[202:203], v205 offset:23040\n"
"v_mfma_f32_16x16x32_bf16 v[226:229], a[132:135], v[4:7], v[226:229]\n"
"s_add_u32 s62, s40, 119808\n"
"ds_read_b64_tr_b16 a[204:205], v200 offset:0\n"
"v_mfma_f32_16x16x32_bf16 v[230:233], a[132:135], v[36:39], v[230:233]\n"
"s_waitcnt lgkmcnt(11)\n"
"ds_read_b64_tr_b16 a[206:207], v200 offset:4608\n"
"v_and_b32 v206, 7, v248\n"
"v_mfma_f32_16x16x32_bf16 v[64:67], a[136:139], v[8:11], v[64:67]\n"
"ds_read_b64_tr_b16 a[208:209], v200 offset:32\n"
"v_lshrrev_b32 v255, 5, v253\n"
"v_mfma_f32_16x16x32_bf16 v[68:71], a[136:139], v[40:43], v[68:71]\n"
"ds_read_b64_tr_b16 a[210:211], v200 offset:4640\n"
"v_bfe_u32 v216, v253, 3, 2\n"
"v_mfma_f32_16x16x32_bf16 v[72:75], a[140:143], v[8:11], v[72:75]\n"
"ds_read_b64_tr_b16 a[212:213], v200 offset:64\n"
"v_lshlrev_b32 v241, 10, v255\n"
"v_mfma_f32_16x16x32_bf16 v[76:79], a[140:143], v[40:43], v[76:79]\n"
"ds_read_b64_tr_b16 a[214:215], v200 offset:4672\n"
"v_lshl_add_u32 v241, v216, 8, v241\n"
"v_mfma_f32_16x16x32_bf16 v[80:83], a[144:147], v[8:11], v[80:83]\n"
"ds_read_b64_tr_b16 a[216:217], v200 offset:96\n"
"v_lshl_add_u32 v241, v206, 4, v241\n"
"v_mfma_f32_16x16x32_bf16 v[84:87], a[144:147], v[40:43], v[84:87]\n"
"ds_read_b64_tr_b16 a[218:219], v200 offset:4704\n"
"v_add_u32 v242, 6144, v241\n"
"v_mfma_f32_16x16x32_bf16 v[88:91], a[148:151], v[8:11], v[88:91]\n"
"ds_read_b64_tr_b16 a[220:221], v200 offset:128\n"
"v_add_u32 v243, 12288, v241\n"
"v_mfma_f32_16x16x32_bf16 v[92:95], a[148:151], v[40:43], v[92:95]\n"
"s_waitcnt lgkmcnt(11)\n"
"ds_read_b64_tr_b16 a[222:223], v200 offset:4736\n"
"v_lshlrev_b32 v255, 5, v255\n"
"v_mfma_f32_16x16x32_bf16 v[96:99], a[152:155], v[8:11], v[96:99]\n"
"ds_read_b64_tr_b16 a[128:129], v200 offset:160\n"
"v_lshl_add_u32 v255, v216, 2, v255\n"
"v_mfma_f32_16x16x32_bf16 v[100:103], a[152:155], v[40:43], v[100:103]\n"
"ds_read_b64_tr_b16 a[130:131], v200 offset:4768\n"
"v_lshl_add_u32 v244, v255, 2, s62\n"
"v_mfma_f32_16x16x32_bf16 v[104:107], a[156:159], v[8:11], v[104:107]\n"
"ds_read_b64_tr_b16 a[132:133], v200 offset:192\n"
"v_mul_u32_u24 v206, 1152, v206\n"
"v_mfma_f32_16x16x32_bf16 v[108:111], a[156:159], v[40:43], v[108:111]\n"
"ds_read_b64_tr_b16 a[134:135], v200 offset:4800\n"
"v_lshl_add_u32 v240, v255, 1, v206\n"
"v_mfma_f32_16x16x32_bf16 v[112:115], a[192:195], v[8:11], v[112:115]\n"
"ds_read_b64_tr_b16 a[136:137], v200 offset:224\n"
"v_add_u32 v240, s40, v240\n"
"v_mfma_f32_16x16x32_bf16 v[116:119], a[192:195], v[40:43], v[116:119]\n"
"ds_read_b64_tr_b16 a[138:139], v200 offset:4832\n"
"v_add_u32 v245, v206, v255\n"
"v_mfma_f32_16x16x32_bf16 v[120:123], a[196:199], v[8:11], v[120:123]\n"
"ds_read_b64_tr_b16 a[140:141], v203 offset:0\n"
"v_lshlrev_b32 v245, 3, v245\n"
"v_mfma_f32_16x16x32_bf16 v[124:127], a[196:199], v[40:43], v[124:127]\n"
"s_waitcnt lgkmcnt(11)\n"
"ds_read_b64_tr_b16 a[142:143], v203 offset:4608\n"
"v_lshl_add_u32 v247, v253, 4, s62\n"
"v_mfma_f32_16x16x32_bf16 v[226:229], a[200:203], v[8:11], v[226:229]\n"
"s_mul_i32 s62, s41, 7\n"
"ds_read_b64_tr_b16 a[144:145], v200 offset:9216\n"
"v_mfma_f32_16x16x32_bf16 v[230:233], a[200:203], v[40:43], v[230:233]\n"
"s_min_u32 s62, s62, 20\n"
"ds_read_b64_tr_b16 a[146:147], v200 offset:13824\n"
"v_mfma_f32_16x16x32_bf16 v[64:67], a[204:207], v[12:15], v[64:67]\n"
"s_lshl_b32 s62, s62, 10\n"
"ds_read_b64_tr_b16 a[148:149], v200 offset:9248\n"
"v_mfma_f32_16x16x32_bf16 v[68:71], a[204:207], v[44:47], v[68:71]\n"
"s_add_u32 s50, s50, s62\n"
"ds_read_b64_tr_b16 a[150:151], v200 offset:13856\n"
"v_mfma_f32_16x16x32_bf16 v[72:75], a[208:211], v[12:15], v[72:75]\n"
"s_add_u32 s84, s40, s62\n"
"ds_read_b64_tr_b16 a[152:153], v200 offset:9280\n"
"v_mfma_f32_16x16x32_bf16 v[76:79], a[208:211], v[44:47], v[76:79]\n"
"s_add_u32 s86, s50, 4096\n"
"ds_read_b64_tr_b16 a[154:155], v200 offset:13888\n"
"v_mfma_f32_16x16x32_bf16 v[80:83], a[212:215], v[12:15], v[80:83]\n"
"ds_read_b64_tr_b16 a[156:157], v200 offset:9312\n"
"v_mfma_f32_16x16x32_bf16 v[84:87], a[212:215], v[44:47], v[84:87]\n"
"s_waitcnt lgkmcnt(11)\n"
"ds_read_b64_tr_b16 a[158:159], v200 offset:13920\n"
"v_mfma_f32_16x16x32_bf16 v[88:91], a[216:219], v[12:15], v[88:91]\n"
"ds_read_b64_tr_b16 a[192:193], v200 offset:9344\n"
"v_mfma_f32_16x16x32_bf16 v[92:95], a[216:219], v[44:47], v[92:95]\n"
"ds_read_b64_tr_b16 a[194:195], v200 offset:13952\n"
"v_mfma_f32_16x16x32_bf16 v[96:99], a[220:223], v[12:15], v[96:99]\n"
"ds_read_b64_tr_b16 a[196:197], v200 offset:9376\n"
"v_mfma_f32_16x16x32_bf16 v[100:103], a[220:223], v[44:47], v[100:103]\n"
"ds_read_b64_tr_b16 a[198:199], v200 offset:13984\n"
"v_mfma_f32_16x16x32_bf16 v[104:107], a[128:131], v[12:15], v[104:107]\n"
"ds_read_b64_tr_b16 a[200:201], v200 offset:9408\n"
"v_mfma_f32_16x16x32_bf16 v[108:111], a[128:131], v[44:47], v[108:111]\n"
"ds_read_b64_tr_b16 a[202:203], v200 offset:14016\n"
"v_mfma_f32_16x16x32_bf16 v[112:115], a[132:135], v[12:15], v[112:115]\n"
"ds_read_b64_tr_b16 a[204:205], v200 offset:9440\n"
"v_mfma_f32_16x16x32_bf16 v[116:119], a[132:135], v[44:47], v[116:119]\n"
"s_waitcnt lgkmcnt(11)\n"
"ds_read_b64_tr_b16 a[206:207], v200 offset:14048\n"
"v_mfma_f32_16x16x32_bf16 v[120:123], a[136:139], v[12:15], v[120:123]\n"
"ds_read_b64_tr_b16 a[208:209], v203 offset:9216\n"
"v_mfma_f32_16x16x32_bf16 v[124:127], a[136:139], v[44:47], v[124:127]\n"
"ds_read_b64_tr_b16 a[210:211], v203 offset:13824\n"
"v_mfma_f32_16x16x32_bf16 v[226:229], a[140:143], v[12:15], v[226:229]\n"
"ds_read_b64_tr_b16 a[212:213], v200 offset:18432\n"
"v_mfma_f32_16x16x32_bf16 v[230:233], a[140:143], v[44:47], v[230:233]\n"
"ds_read_b64_tr_b16 a[214:215], v200 offset:23040\n"
"v_mfma_f32_16x16x32_bf16 v[64:67], a[144:147], v[16:19], v[64:67]\n"
"ds_read_b64_tr_b16 a[216:217], v200 offset:18464\n"
"v_mfma_f32_16x16x32_bf16 v[68:71], a[144:147], v[48:51], v[68:71]\n"
"ds_read_b64_tr_b16 a[218:219], v200 offset:23072\n"
"v_mfma_f32_16x16x32_bf16 v[72:75], a[148:151], v[16:19], v[72:75]\n"
"s_waitcnt vmcnt(0)\n"
"s_barrier\n"
"ds_read_b64_tr_b16 a[220:221], v200 offset:18496\n"
"buffer_store_dword v252, v251, s[28:31], s23 offen sc1\n"
"v_mfma_f32_16x16x32_bf16 v[76:79], a[148:151], v[48:51], v[76:79]\n"
"s_waitcnt lgkmcnt(11)\n"
"ds_read_b64_tr_b16 a[222:223], v200 offset:23104\n"
"buffer_load_dwordx4 a[160:163], v246, s[28:31], s56 offen offset:0 sc1\n"
"v_mfma_f32_16x16x32_bf16 v[80:83], a[152:155], v[16:19], v[80:83]\n"
"s_mov_b32 m0, s51\n"
"ds_read_b64_tr_b16 a[128:129], v200 offset:18528\n"
"v_mfma_f32_16x16x32_bf16 v[84:87], a[152:155], v[48:51], v[84:87]\n"
"ds_read_b64_tr_b16 a[130:131], v200 offset:23136\n"
"buffer_load_dwordx4 v210, s[24:27], s36 offen offset:0 lds\n"
"v_mfma_f32_16x16x32_bf16 v[88:91], a[156:159], v[16:19], v[88:91]\n"
"ds_read_b64_tr_b16 a[132:133], v200 offset:18560\n"
"v_mfma_f32_16x16x32_bf16 v[92:95], a[156:159], v[48:51], v[92:95]\n"
"ds_read_b64_tr_b16 a[134:135], v200 offset:23168\n"
"v_mfma_f32_16x16x32_bf16 v[96:99], a[192:195], v[16:19], v[96:99]\n"
"ds_read_b64_tr_b16 a[136:137], v200 offset:18592\n"
"v_mfma_f32_16x16x32_bf16 v[100:103], a[192:195], v[48:51], v[100:103]\n"
"ds_read_b64_tr_b16 a[138:139], v200 offset:23200\n"
"buffer_load_dwordx4 a[164:167], v246, s[28:31], s56 offen offset:16 sc1\n"
"v_mfma_f32_16x16x32_bf16 v[104:107], a[196:199], v[16:19], v[104:107]\n"
"ds_read_b64_tr_b16 a[140:141], v200 offset:18624\n"
"v_mfma_f32_16x16x32_bf16 v[108:111], a[196:199], v[48:51], v[108:111]\n"
"s_waitcnt lgkmcnt(11)\n"
"s_mov_b32 m0, s51\n"
"ds_read_b64_tr_b16 a[142:143], v200 offset:23232\n"
"v_mfma_f32_16x16x32_bf16 v[112:115], a[200:203], v[16:19], v[112:115]\n"
"ds_read_b64_tr_b16 a[144:145], v200 offset:18656\n"
"buffer_load_dwordx4 v211, s[24:27], s36 offen offset:1024 lds\n"
"v_mfma_f32_16x16x32_bf16 v[116:119], a[200:203], v[48:51], v[116:119]\n"
"ds_read_b64_tr_b16 a[146:147], v200 offset:23264\n"
"v_mfma_f32_16x16x32_bf16 v[120:123], a[204:207], v[16:19], v[120:123]\n"
"ds_read_b64_tr_b16 a[148:149], v203 offset:18432\n"
"v_mfma_f32_16x16x32_bf16 v[124:127], a[204:207], v[48:51], v[124:127]\n"
"ds_read_b64_tr_b16 a[150:151], v203 offset:23040\n"
"buffer_load_dwordx4 a[168:171], v246, s[28:31], s57 offen offset:0 sc1\n"
"v_mfma_f32_16x16x32_bf16 v[226:229], a[208:211], v[16:19], v[226:229]\n"
"ds_read_b64_tr_b16 a[152:153], v200 offset:27648\n"
"v_mfma_f32_16x16x32_bf16 v[230:233], a[208:211], v[48:51], v[230:233]\n"
"ds_read_b64_tr_b16 a[154:155], v200 offset:32256\n"
"v_mfma_f32_16x16x32_bf16 v[64:67], a[212:215], v[20:23], v[64:67]\n"
"s_mov_b32 m0, s51\n"
"ds_read_b64_tr_b16 a[156:157], v200 offset:27680\n"
"v_mfma_f32_16x16x32_bf16 v[68:71], a[212:215], v[52:55], v[68:71]\n"
"s_waitcnt lgkmcnt(11)\n"
"ds_read_b64_tr_b16 a[158:159], v200 offset:32288\n"
"buffer_load_dwordx4 v212, s[24:27], s36 offen offset:2048 lds\n"
"v_mfma_f32_16x16x32_bf16 v[72:75], a[216:219], v[20:23], v[72:75]\n"
"ds_read_b64_tr_b16 a[192:193], v200 offset:27712\n"
"v_mfma_f32_16x16x32_bf16 v[76:79], a[216:219], v[52:55], v[76:79]\n"
"ds_read_b64_tr_b16 a[194:195], v200 offset:32320\n"
"buffer_load_dwordx4 a[172:175], v246, s[28:31], s57 offen offset:16 sc1\n"
"v_mfma_f32_16x16x32_bf16 v[80:83], a[220:223], v[20:23], v[80:83]\n"
"ds_read_b64_tr_b16 a[196:197], v200 offset:27744\n"
"v_mfma_f32_16x16x32_bf16 v[84:87], a[220:223], v[52:55], v[84:87]\n"
"ds_read_b64_tr_b16 a[198:199], v200 offset:32352\n"
"v_mfma_f32_16x16x32_bf16 v[88:91], a[128:131], v[20:23], v[88:91]\n"
"ds_read_b64_tr_b16 a[200:201], v200 offset:27776\n"
"v_mfma_f32_16x16x32_bf16 v[92:95], a[128:131], v[52:55], v[92:95]\n"
"s_mov_b32 m0, s51\n"
"ds_read_b64_tr_b16 a[202:203], v200 offset:32384\n"
"v_mfma_f32_16x16x32_bf16 v[96:99], a[132:135], v[20:23], v[96:99]\n"
"ds_read_b64_tr_b16 a[204:205], v200 offset:27808\n"
"buffer_load_dwordx4 v213, s[24:27], s36 offen offset:3072 lds\n"
"v_mfma_f32_16x16x32_bf16 v[100:103], a[132:135], v[52:55], v[100:103]\n"
"s_waitcnt lgkmcnt(11)\n"
"ds_read_b64_tr_b16 a[206:207], v200 offset:32416\n"
"buffer_load_dwordx4 a[176:179], v246, s[28:31], s58 offen offset:0 sc1\n"
"v_mfma_f32_16x16x32_bf16 v[104:107], a[136:139], v[20:23], v[104:107]\n"
"ds_read_b64_tr_b16 a[208:209], v200 offset:27840\n"
"v_mfma_f32_16x16x32_bf16 v[108:111], a[136:139], v[52:55], v[108:111]\n"
"ds_read_b64_tr_b16 a[210:211], v200 offset:32448\n"
"v_mfma_f32_16x16x32_bf16 v[112:115], a[140:143], v[20:23], v[112:115]\n"
"ds_read_b64_tr_b16 a[212:213], v200 offset:27872\n"
"v_mfma_f32_16x16x32_bf16 v[116:119], a[140:143], v[52:55], v[116:119]\n"
"ds_read_b64_tr_b16 a[214:215], v200 offset:32480\n"
"v_mfma_f32_16x16x32_bf16 v[120:123], a[144:147], v[20:23], v[120:123]\n"
"s_add_u32 m0, s51, 4096\n"
"ds_read_b64_tr_b16 a[216:217], v203 offset:27648\n"
"v_mfma_f32_16x16x32_bf16 v[124:127], a[144:147], v[52:55], v[124:127]\n"
"ds_read_b64_tr_b16 a[218:219], v203 offset:32256\n"
"buffer_load_dwordx4 v214, s[24:27], s36 offen offset:0 lds\n"
"v_mfma_f32_16x16x32_bf16 v[226:229], a[148:151], v[20:23], v[226:229]\n"
"ds_read_b64_tr_b16 a[220:221], v200 offset:36864\n"
"buffer_load_dwordx4 a[180:183], v246, s[28:31], s58 offen offset:16 sc1\n"
"v_mfma_f32_16x16x32_bf16 v[230:233], a[148:151], v[52:55], v[230:233]\n"
"s_waitcnt lgkmcnt(11)\n"
"ds_read_b64_tr_b16 a[222:223], v200 offset:41472\n"
"v_mfma_f32_16x16x32_bf16 v[64:67], a[152:155], v[24:27], v[64:67]\n"
"ds_read_b64_tr_b16 a[128:129], v200 offset:36896\n"
"v_mfma_f32_16x16x32_bf16 v[68:71], a[152:155], v[56:59], v[68:71]\n"
"ds_read_b64_tr_b16 a[130:131], v200 offset:41504\n"
"v_mfma_f32_16x16x32_bf16 v[72:75], a[156:159], v[24:27], v[72:75]\n"
"ds_read_b64_tr_b16 a[132:133], v200 offset:36928\n"
"v_mfma_f32_16x16x32_bf16 v[76:79], a[156:159], v[56:59], v[76:79]\n"
"s_add_u32 m0, s51, 4096\n"
"ds_read_b64_tr_b16 a[134:135], v200 offset:41536\n"
"v_mfma_f32_16x16x32_bf16 v[80:83], a[192:195], v[24:27], v[80:83]\n"
"ds_read_b64_tr_b16 a[136:137], v200 offset:36960\n"
"buffer_load_dwordx4 v236, s[24:27], s36 offen offset:1024 lds\n"
"v_mfma_f32_16x16x32_bf16 v[84:87], a[192:195], v[56:59], v[84:87]\n"
"ds_read_b64_tr_b16 a[138:139], v200 offset:41568\n"
"buffer_load_dwordx4 a[184:187], v246, s[28:31], s59 offen offset:0 sc1\n"
"v_mfma_f32_16x16x32_bf16 v[88:91], a[196:199], v[24:27], v[88:91]\n"
"ds_read_b64_tr_b16 a[140:141], v200 offset:36992\n"
"v_mfma_f32_16x16x32_bf16 v[92:95], a[196:199], v[56:59], v[92:95]\n"
"s_waitcnt lgkmcnt(11)\n"
"ds_read_b64_tr_b16 a[142:143], v200 offset:41600\n"
"v_mfma_f32_16x16x32_bf16 v[96:99], a[200:203], v[24:27], v[96:99]\n"
"ds_read_b64_tr_b16 a[144:145], v200 offset:37024\n"
"v_mfma_f32_16x16x32_bf16 v[100:103], a[200:203], v[56:59], v[100:103]\n"
"ds_read_b64_tr_b16 a[146:147], v200 offset:41632\n"
"v_mfma_f32_16x16x32_bf16 v[104:107], a[204:207], v[24:27], v[104:107]\n"
"s_add_u32 m0, s51, 4096\n"
"ds_read_b64_tr_b16 a[148:149], v200 offset:37056\n"
"v_mfma_f32_16x16x32_bf16 v[108:111], a[204:207], v[56:59], v[108:111]\n"
"ds_read_b64_tr_b16 a[150:151], v200 offset:41664\n"
"buffer_load_dwordx4 v237, s[24:27], s36 offen offset:2048 lds\n"
"v_mfma_f32_16x16x32_bf16 v[112:115], a[208:211], v[24:27], v[112:115]\n"
"ds_read_b64_tr_b16 a[152:153], v200 offset:37088\n"
"buffer_load_dwordx4 a[188:191], v246, s[28:31], s59 offen offset:16 sc1\n"
"v_mfma_f32_16x16x32_bf16 v[116:119], a[208:211], v[56:59], v[116:119]\n"
"ds_read_b64_tr_b16 a[154:155], v200 offset:41696\n"
"v_mfma_f32_16x16x32_bf16 v[120:123], a[212:215], v[24:27], v[120:123]\n"
"ds_read_b64_tr_b16 a[156:157], v203 offset:36864\n"
"v_mfma_f32_16x16x32_bf16 v[124:127], a[212:215], v[56:59], v[124:127]\n"
"s_waitcnt lgkmcnt(11)\n"
"ds_read_b64_tr_b16 a[158:159], v203 offset:41472\n"
"v_mfma_f32_16x16x32_bf16 v[226:229], a[216:219], v[24:27], v[226:229]\n"
"ds_read_b64_tr_b16 a[192:193], v200 offset:46080\n"
"v_mfma_f32_16x16x32_bf16 v[230:233], a[216:219], v[56:59], v[230:233]\n"
"s_add_u32 m0, s51, 4096\n"
"ds_read_b64_tr_b16 a[194:195], v200 offset:50688\n"
"v_mfma_f32_16x16x32_bf16 v[64:67], a[220:223], v[28:31], v[64:67]\n"
"ds_read_b64_tr_b16 a[196:197], v200 offset:46112\n"
"buffer_load_dwordx4 v238, s[24:27], s36 offen offset:3072 lds\n"
"v_mfma_f32_16x16x32_bf16 v[68:71], a[220:223], v[60:63], v[68:71]\n"
"ds_read_b64_tr_b16 a[198:199], v200 offset:50720\n"
"buffer_load_dwordx2 a[224:225], v254, s[80:83], 0 offen\n"
"v_mfma_f32_16x16x32_bf16 v[72:75], a[128:131], v[28:31], v[72:75]\n"
"ds_read_b64_tr_b16 a[200:201], v200 offset:46144\n"
"v_mfma_f32_16x16x32_bf16 v[76:79], a[128:131], v[60:63], v[76:79]\n"
"ds_read_b64_tr_b16 a[202:203], v200 offset:50752\n"
"v_mfma_f32_16x16x32_bf16 v[80:83], a[132:135], v[28:31], v[80:83]\n"
"ds_read_b64_tr_b16 a[204:205], v200 offset:46176\n"
"v_mfma_f32_16x16x32_bf16 v[84:87], a[132:135], v[60:63], v[84:87]\n"
"s_waitcnt lgkmcnt(11)\n"
"ds_read_b64_tr_b16 a[206:207], v200 offset:50784\n"
"v_mfma_f32_16x16x32_bf16 v[88:91], a[136:139], v[28:31], v[88:91]\n"
"s_add_u32 m0, s51, 8192\n"
"ds_read_b64_tr_b16 a[208:209], v200 offset:46208\n"
"v_mfma_f32_16x16x32_bf16 v[92:95], a[136:139], v[60:63], v[92:95]\n"
"ds_read_b64_tr_b16 a[210:211], v200 offset:50816\n"
"buffer_load_dwordx4 v239, s[24:27], s36 offen offset:0 lds\n"
"v_mfma_f32_16x16x32_bf16 v[96:99], a[140:143], v[28:31], v[96:99]\n"
"ds_read_b64_tr_b16 a[212:213], v200 offset:46240\n"
"v_mfma_f32_16x16x32_bf16 v[100:103], a[140:143], v[60:63], v[100:103]\n"
"ds_read_b64_tr_b16 a[214:215], v200 offset:50848\n"
"v_mfma_f32_16x16x32_bf16 v[104:107], a[144:147], v[28:31], v[104:107]\n"
"ds_read_b64_tr_b16 a[216:217], v200 offset:46272\n"
"v_mfma_f32_16x16x32_bf16 v[108:111], a[144:147], v[60:63], v[108:111]\n"
"ds_read_b64_tr_b16 a[218:219], v200 offset:50880\n"
"v_mfma_f32_16x16x32_bf16 v[112:115], a[148:151], v[28:31], v[112:115]\n"
"ds_read_b64_tr_b16 a[220:221], v200 offset:46304\n"
"v_mfma_f32_16x16x32_bf16 v[116:119], a[148:151], v[60:63], v[116:119]\n"
"s_waitcnt lgkmcnt(11)\n"
"ds_read_b64_tr_b16 a[222:223], v200 offset:50912\n"
"v_mfma_f32_16x16x32_bf16 v[120:123], a[152:155], v[28:31], v[120:123]\n"
"ds_read_b64_tr_b16 a[128:129], v203 offset:46080\n"
"buffer_load_dword v249, v215, s[28:31], s23 offen sc1\n"
"v_mfma_f32_16x16x32_bf16 v[124:127], a[152:155], v[60:63], v[124:127]\n"
"ds_read_b64_tr_b16 a[130:131], v203 offset:50688\n"
"v_mfma_f32_16x16x32_bf16 v[226:229], a[156:159], v[28:31], v[226:229]\n"
"ds_read_b64_tr_b16 a[132:133], v200 offset:55296\n"
"v_mfma_f32_16x16x32_bf16 v[230:233], a[156:159], v[60:63], v[230:233]\n"
"ds_read_b64_tr_b16 a[134:135], v200 offset:59904\n"
"s_barrier\n"
"s_waitcnt vmcnt(2)\n"
"s_mov_b32 s87, 0x80000\n"
"v_accvgpr_read_b32 v26, a160\n"
"v_accvgpr_read_b32 v27, a161\n"
"v_accvgpr_read_b32 v28, a162\n"
"v_accvgpr_read_b32 v29, a163\n"
"v_accvgpr_read_b32 v30, a164\n"
"v_accvgpr_read_b32 v31, a165\n"
"v_accvgpr_read_b32 v32, a166\n"
"v_accvgpr_read_b32 v33, a167\n"
"v_accvgpr_read_b32 v34, a168\n"
"v_accvgpr_read_b32 v35, a169\n"
"v_accvgpr_read_b32 v36, a170\n"
"v_accvgpr_read_b32 v37, a171\n"
"v_accvgpr_read_b32 v38, a172\n"
"v_accvgpr_read_b32 v39, a173\n"
"v_accvgpr_read_b32 v40, a174\n"
"v_accvgpr_read_b32 v41, a175\n"
"v_accvgpr_read_b32 v42, a176\n"
"v_accvgpr_read_b32 v43, a177\n"
"v_accvgpr_read_b32 v44, a178\n"
"v_accvgpr_read_b32 v45, a179\n"
"v_accvgpr_read_b32 v46, a180\n"
"v_accvgpr_read_b32 v47, a181\n"
"v_accvgpr_read_b32 v48, a182\n"
"v_accvgpr_read_b32 v49, a183\n"
"v_accvgpr_read_b32 v50, a184\n"
"v_accvgpr_read_b32 v51, a185\n"
"v_accvgpr_read_b32 v52, a186\n"
"v_accvgpr_read_b32 v53, a187\n"
"v_accvgpr_read_b32 v54, a188\n"
"v_accvgpr_read_b32 v55, a189\n"
"v_accvgpr_read_b32 v56, a190\n"
"v_accvgpr_read_b32 v57, a191\n"
"v_accvgpr_read_b32 v58, a224\n"
"v_accvgpr_read_b32 v59, a225\n"
"AT_tchk:\n"
"v_xor_b32 v24, s85, v27\n"
"v_xor_b32 v25, s85, v29\n"
"v_bitop3_b32 v24, v31, s85, v24 bitop3:0xbe\n"
"v_bitop3_b32 v25, v33, s85, v25 bitop3:0xbe\n"
"v_bitop3_b32 v24, v35, s85, v24 bitop3:0xbe\n"
"v_bitop3_b32 v25, v37, s85, v25 bitop3:0xbe\n"
"v_bitop3_b32 v24, v39, s85, v24 bitop3:0xbe\n"
"v_bitop3_b32 v25, v41, s85, v25 bitop3:0xbe\n"
"v_bitop3_b32 v24, v43, s85, v24 bitop3:0xbe\n"
"v_bitop3_b32 v25, v45, s85, v25 bitop3:0xbe\n"
"v_bitop3_b32 v24, v47, s85, v24 bitop3:0xbe\n"
"v_bitop3_b32 v25, v49, s85, v25 bitop3:0xbe\n"
"v_bitop3_b32 v24, v51, s85, v24 bitop3:0xbe\n"
"v_bitop3_b32 v25, v53, s85, v25 bitop3:0xbe\n"
"v_bitop3_b32 v24, v55, s85, v24 bitop3:0xbe\n"
"v_bitop3_b32 v25, v57, s85, v25 bitop3:0xbe\n"
"v_or_b32 v24, v24, v25\n"
"v_cmp_ne_u32 s[62:63], 0, v24\n"
"v_cmp_gt_u32 vcc, 144, v253\n"
"s_nop 1\n"
"s_and_b64 s[62:63], vcc, s[62:63]\n"
"s_cmp_eq_u64 s[62:63], 0\n"
"s_cbranch_scc1 AT_tok\n"
"s_sub_u32 s87, s87, 1\n"
"s_cmp_eq_u32 s87, 0\n"
"s_cbranch_scc1 AT_tok\n"
"s_sleep 2\n"
"buffer_load_dwordx4 v[26:29], v246, s[28:31], s56 offen offset:0 sc1\n"
"buffer_load_dwordx4 v[30:33], v246, s[28:31], s56 offen offset:16 sc1\n"
"buffer_load_dwordx4 v[34:37], v246, s[28:31], s57 offen offset:0 sc1\n"
"buffer_load_dwordx4 v[38:41], v246, s[28:31], s57 offen offset:16 sc1\n"
"buffer_load_dwordx4 v[42:45], v246, s[28:31], s58 offen offset:0 sc1\n"
"buffer_load_dwordx4 v[46:49], v246, s[28:31], s58 offen offset:16 sc1\n"
"buffer_load_dwordx4 v[50:53], v246, s[28:31], s59 offen offset:0 sc1\n"
"buffer_load_dwordx4 v[54:57], v246, s[28:31], s59 offen offset:16 sc1\n"
"s_waitcnt vmcnt(0)\n"
"s_branch AT_tchk\n"
"AT_tok:\n"
"s_waitcnt vmcnt(0)\n"
"v_cmp_ne_u32 vcc, s85, v249\n"
"s_nop 0\n"
"s_cbranch_vccnz AT_x1\n"
"AT_x2:\n"
"buffer_load_dwordx4 a[0:3], v207, s[28:31], s20 offen offset:0 sc1\n"
"buffer_load_dwordx4 a[4:7], v207, s[28:31], s20 offen offset:1024 sc1\n"
"buffer_load_dwordx4 a[8:11], v207, s[28:31], s21 offen offset:0 sc1\n"
"buffer_load_dwordx4 a[12:15], v207, s[28:31], s21 offen offset:1024 sc1\n"
"buffer_load_dwordx4 a[16:19], v207, s[28:31], s22 offen offset:0 sc1\n"
"buffer_load_dwordx4 a[20:23], v207, s[28:31], s22 offen offset:1024 sc1\n"
"buffer_load_dwordx4 a[24:27], v207, s[28:31], s20 offen offset:2048 sc1\n"
"buffer_load_dwordx4 a[28:31], v207, s[28:31], s20 offen offset:3072 sc1\n"
"s_add_u32 s20, s20, 4096\n"
"v_cmp_gt_u32 vcc, 144, v253\n"
"v_lshlrev_b32 v0, 16, v58\n"
"v_and_b32 v1, 0xffff0000, v58\n"
"v_lshlrev_b32 v2, 16, v59\n"
"v_and_b32 v3, 0xffff0000, v59\n"
"v_mov_b32 v4, v26\n"
"v_add_f32 v12, 1.0, v42\n"
"v_mov_b32 v16, v34\n"
"v_mov_b32 v20, v50\n"
"v_mul_f32 v8, v12, v0\n"
"v_mov_b32 v5, v28\n"
"v_add_f32 v13, 1.0, v44\n"
"v_mov_b32 v17, v36\n"
"v_mov_b32 v21, v52\n"
"v_mul_f32 v9, v13, v1\n"
"v_mov_b32 v6, v30\n"
"v_add_f32 v14, 1.0, v46\n"
"v_mov_b32 v18, v38\n"
"v_mov_b32 v22, v54\n"
"v_mul_f32 v10, v14, v2\n"
"v_mov_b32 v7, v32\n"
"v_add_f32 v15, 1.0, v48\n"
"v_mov_b32 v19, v40\n"
"v_mov_b32 v23, v56\n"
"v_mul_f32 v11, v15, v3\n"
"s_mov_b64 exec, vcc\n"
"ds_write_b128 v247, v[4:7] offset:0\n"
"ds_write_b128 v247, v[8:11] offset:2304\n"
"ds_write_b128 v247, v[12:15] offset:4608\n"
"ds_write_b128 v247, v[16:19] offset:6912\n"
"ds_write_b128 v247, v[20:23] offset:38912\n"
"s_mov_b64 exec, -1\n"
"v_add_f32 v24, v8, v9\n"
"v_mul_f32 v25, v16, v0\n"
"v_add_f32 v24, v24, v10\n"
"v_fmac_f32 v25, v17, v1\n"
"v_add_f32 v24, v24, v11\n"
"v_fmac_f32 v25, v18, v2\n"
"s_nop 0\n"
"v_fmac_f32 v25, v19, v3\n"
"s_nop 1\n"
"v_add_f32_dpp v24, v24, v24 row_ror:1 row_mask:0xf bank_mask:0xf\n"
"v_add_f32_dpp v25, v25, v25 row_ror:1 row_mask:0xf bank_mask:0xf\n"
"s_nop 0\n"
"v_add_f32_dpp v24, v24, v24 row_ror:2 row_mask:0xf bank_mask:0xf\n"
"v_add_f32_dpp v25, v25, v25 row_ror:2 row_mask:0xf bank_mask:0xf\n"
"s_nop 0\n"
"v_add_f32_dpp v24, v24, v24 row_ror:4 row_mask:0xf bank_mask:0xf\n"
"v_add_f32_dpp v25, v25, v25 row_ror:4 row_mask:0xf bank_mask:0xf\n"
"s_nop 0\n"
"v_add_f32_dpp v24, v24, v24 row_ror:8 row_mask:0xf bank_mask:0xf\n"
"v_add_f32_dpp v25, v25, v25 row_ror:8 row_mask:0xf bank_mask:0xf\n"
"s_nop 0\n"
"v_mov_b32 v26, v24\n"
"v_mov_b32 v27, v25\n"
"s_nop 0\n"
"v_permlane16_swap_b32 v24, v26\n"
"v_permlane16_swap_b32 v25, v27\n"
"v_add_f32 v24, v24, v26\n"
"v_add_f32 v25, v25, v27\n"
"v_mov_b32 v26, v24\n"
"v_mov_b32 v27, v25\n"
"s_nop 0\n"
"v_permlane32_swap_b32 v24, v26\n"
"v_permlane32_swap_b32 v25, v27\n"
"v_add_f32 v24, v24, v26\n"
"v_add_f32 v25, v25, v27\n"
"s_lshl_b32 s62, s41, 3\n"
"s_add_u32 s62, s62, s40\n"
"v_mov_b32 v26, s62\n"
"v_add_u32 v26, 161536, v26\n"
"s_mov_b64 exec, 1\n"
"ds_write_b64 v26, v[24:25]\n"
"s_mov_b64 exec, -1\n"
"s_waitcnt vmcnt(6)\n"
"v_mfma_f32_16x16x32_bf16 v[64:67], a[192:195], a[0:3], v[64:67]\n"
"ds_read_b64_tr_b16 a[136:137], v200 offset:55328\n"
"buffer_load_dwordx4 a[32:35], v207, s[28:31], s21 offen offset:2048 sc1\n"
"v_mfma_f32_16x16x32_bf16 v[68:71], a[192:195], a[4:7], v[68:71]\n"
"s_add_u32 m0, s53, 36864\n"
"ds_read_b64_tr_b16 a[138:139], v200 offset:59936\n"
"v_mfma_f32_16x16x32_bf16 v[72:75], a[196:199], a[0:3], v[72:75]\n"
"ds_read_b64_tr_b16 a[140:141], v200 offset:55360\n"
"buffer_load_dwordx4 v210, s[24:27], s37 offen offset:0 lds\n"
"v_mfma_f32_16x16x32_bf16 v[76:79], a[196:199], a[4:7], v[76:79]\n"
"s_waitcnt lgkmcnt(14)\n"
"ds_read_b64_tr_b16 a[142:143], v200 offset:59968\n"
"v_mfma_f32_16x16x32_bf16 v[80:83], a[200:203], a[0:3], v[80:83]\n"
"ds_read_b64_tr_b16 a[144:145], v200 offset:55392\n"
"v_mfma_f32_16x16x32_bf16 v[84:87], a[200:203], a[4:7], v[84:87]\n"
"ds_read_b64_tr_b16 a[146:147], v200 offset:60000\n"
"buffer_load_dwordx4 a[36:39], v207, s[28:31], s21 offen offset:3072 sc1\n"
"v_mfma_f32_16x16x32_bf16 v[88:91], a[204:207], a[0:3], v[88:91]\n"
"s_add_u32 s21, s21, 4096\n"
"ds_read_b64_tr_b16 a[148:149], v200 offset:55424\n"
"v_mfma_f32_16x16x32_bf16 v[92:95], a[204:207], a[4:7], v[92:95]\n"
"ds_read_b64_tr_b16 a[150:151], v200 offset:60032\n"
"v_mfma_f32_16x16x32_bf16 v[96:99], a[208:211], a[0:3], v[96:99]\n"
"s_add_u32 m0, s53, 36864\n"
"ds_read_b64_tr_b16 a[152:153], v200 offset:55456\n"
"v_mfma_f32_16x16x32_bf16 v[100:103], a[208:211], a[4:7], v[100:103]\n"
"ds_read_b64_tr_b16 a[154:155], v200 offset:60064\n"
"buffer_load_dwordx4 v211, s[24:27], s37 offen offset:1024 lds\n"
"v_mfma_f32_16x16x32_bf16 v[104:107], a[212:215], a[0:3], v[104:107]\n"
"ds_read_b64_tr_b16 a[156:157], v200 offset:55488\n"
"buffer_load_dwordx4 a[40:43], v207, s[28:31], s22 offen offset:2048 sc1\n"
"v_mfma_f32_16x16x32_bf16 v[108:111], a[212:215], a[4:7], v[108:111]\n"
"ds_read_b64_tr_b16 a[158:159], v200 offset:60096\n"
"v_mfma_f32_16x16x32_bf16 v[112:115], a[216:219], a[0:3], v[112:115]\n"
"ds_read_b64_tr_b16 a[192:193], v200 offset:55520\n"
"v_mfma_f32_16x16x32_bf16 v[116:119], a[216:219], a[4:7], v[116:119]\n"
"s_waitcnt lgkmcnt(11)\n"
"ds_read_b64_tr_b16 a[194:195], v200 offset:60128\n"
"v_mfma_f32_16x16x32_bf16 v[120:123], a[220:223], a[0:3], v[120:123]\n"
"ds_read_b64_tr_b16 a[196:197], v203 offset:55296\n"
"v_mfma_f32_16x16x32_bf16 v[124:127], a[220:223], a[4:7], v[124:127]\n"
"s_add_u32 m0, s53, 36864\n"
"ds_read_b64_tr_b16 a[198:199], v203 offset:59904\n"
"buffer_load_dwordx4 a[44:47], v207, s[28:31], s22 offen offset:3072 sc1\n"
"v_mfma_f32_16x16x32_bf16 v[226:229], a[128:131], a[0:3], v[226:229]\n"
"s_add_u32 s22, s22, 4096\n"
"ds_read_b64_tr_b16 a[200:201], v200 offset:64512\n"
"buffer_load_dwordx4 v212, s[24:27], s37 offen offset:2048 lds\n"
"v_mfma_f32_16x16x32_bf16 v[230:233], a[128:131], a[4:7], v[230:233]\n"
"s_waitcnt vmcnt(11)\n"
"ds_read_b64_tr_b16 a[202:203], v201 offset:3584\n"
"v_mfma_f32_16x16x32_bf16 v[64:67], a[132:135], a[8:11], v[64:67]\n"
"ds_read_b64_tr_b16 a[204:205], v200 offset:64544\n"
"v_mfma_f32_16x16x32_bf16 v[68:71], a[132:135], a[12:15], v[68:71]\n"
"ds_read_b64_tr_b16 a[206:207], v201 offset:3616\n"
"v_mfma_f32_16x16x32_bf16 v[72:75], a[136:139], a[8:11], v[72:75]\n"
"ds_read_b64_tr_b16 a[208:209], v200 offset:64576\n"
"buffer_load_dwordx4 a[48:51], v207, s[28:31], s20 offen offset:0 sc1\n"
"v_mfma_f32_16x16x32_bf16 v[76:79], a[136:139], a[12:15], v[76:79]\n"
"s_waitcnt lgkmcnt(11)\n"
"ds_read_b64_tr_b16 a[210:211], v201 offset:3648\n"
"v_mfma_f32_16x16x32_bf16 v[80:83], a[140:143], a[8:11], v[80:83]\n"
"s_add_u32 m0, s53, 36864\n"
"ds_read_b64_tr_b16 a[212:213], v200 offset:64608\n"
"v_mfma_f32_16x16x32_bf16 v[84:87], a[140:143], a[12:15], v[84:87]\n"
"ds_read_b64_tr_b16 a[214:215], v201 offset:3680\n"
"buffer_load_dwordx4 v213, s[24:27], s37 offen offset:3072 lds\n"
"v_mfma_f32_16x16x32_bf16 v[88:91], a[144:147], a[8:11], v[88:91]\n"
"ds_read_b64_tr_b16 a[216:217], v200 offset:64640\n"
"v_mfma_f32_16x16x32_bf16 v[92:95], a[144:147], a[12:15], v[92:95]\n"
"ds_read_b64_tr_b16 a[218:219], v201 offset:3712\n"
"buffer_load_dwordx4 a[52:55], v207, s[28:31], s20 offen offset:1024 sc1\n"
"v_mfma_f32_16x16x32_bf16 v[96:99], a[148:151], a[8:11], v[96:99]\n"
"ds_read_b64_tr_b16 a[220:221], v200 offset:64672\n"
"v_mfma_f32_16x16x32_bf16 v[100:103], a[148:151], a[12:15], v[100:103]\n"
"ds_read_b64_tr_b16 a[222:223], v201 offset:3744\n"
"v_mfma_f32_16x16x32_bf16 v[104:107], a[152:155], a[8:11], v[104:107]\n"
"ds_read_b64_tr_b16 a[128:129], v200 offset:64704\n"
"v_mfma_f32_16x16x32_bf16 v[108:111], a[152:155], a[12:15], v[108:111]\n"
"s_waitcnt lgkmcnt(11)\n"
"ds_read_b64_tr_b16 a[130:131], v201 offset:3776\n"
"v_mfma_f32_16x16x32_bf16 v[112:115], a[156:159], a[8:11], v[112:115]\n"
"s_add_u32 m0, s53, 40960\n"
"ds_read_b64_tr_b16 a[132:133], v200 offset:64736\n"
"buffer_load_dwordx4 a[56:59], v207, s[28:31], s21 offen offset:0 sc1\n"
"v_mfma_f32_16x16x32_bf16 v[116:119], a[156:159], a[12:15], v[116:119]\n"
"ds_read_b64_tr_b16 a[134:135], v201 offset:3808\n"
"buffer_load_dwordx4 v214, s[24:27], s37 offen offset:0 lds\n"
"v_mfma_f32_16x16x32_bf16 v[120:123], a[192:195], a[8:11], v[120:123]\n"
"ds_read_b64_tr_b16 a[136:137], v203 offset:64512\n"
"v_mfma_f32_16x16x32_bf16 v[124:127], a[192:195], a[12:15], v[124:127]\n"
"ds_read_b64_tr_b16 a[138:139], v204 offset:3584\n"
"v_mfma_f32_16x16x32_bf16 v[226:229], a[196:199], a[8:11], v[226:229]\n"
"ds_read_b64_tr_b16 a[140:141], v201 offset:8192\n"
"v_mfma_f32_16x16x32_bf16 v[230:233], a[196:199], a[12:15], v[230:233]\n"
"s_waitcnt vmcnt(14)\n"
"ds_read_b64_tr_b16 a[142:143], v201 offset:12800\n"
"v_mfma_f32_16x16x32_bf16 v[64:67], a[200:203], a[16:19], v[64:67]\n"
"ds_read_b64_tr_b16 a[144:145], v201 offset:8224\n"
"v_mfma_f32_16x16x32_bf16 v[68:71], a[200:203], a[20:23], v[68:71]\n"
"s_waitcnt lgkmcnt(11)\n"
"s_add_u32 m0, s53, 40960\n"
"ds_read_b64_tr_b16 a[146:147], v201 offset:12832\n"
"buffer_load_dwordx4 a[60:63], v207, s[28:31], s21 offen offset:1024 sc1\n"
"v_mfma_f32_16x16x32_bf16 v[72:75], a[204:207], a[16:19], v[72:75]\n"
"ds_read_b64_tr_b16 a[148:149], v201 offset:8256\n"
"buffer_load_dwordx4 v236, s[24:27], s37 offen offset:1024 lds\n"
"v_mfma_f32_16x16x32_bf16 v[76:79], a[204:207], a[20:23], v[76:79]\n"
"ds_read_b64_tr_b16 a[150:151], v201 offset:12864\n"
"v_mfma_f32_16x16x32_bf16 v[80:83], a[208:211], a[16:19], v[80:83]\n"
"ds_read_b64_tr_b16 a[152:153], v201 offset:8288\n"
"v_mfma_f32_16x16x32_bf16 v[84:87], a[208:211], a[20:23], v[84:87]\n"
"ds_read_b64_tr_b16 a[154:155], v201 offset:12896\n"
"v_mfma_f32_16x16x32_bf16 v[88:91], a[212:215], a[16:19], v[88:91]\n"
"ds_read_b64_tr_b16 a[156:157], v201 offset:8320\n"
"v_mfma_f32_16x16x32_bf16 v[92:95], a[212:215], a[20:23], v[92:95]\n"
"ds_read_b64_tr_b16 a[158:159], v201 offset:12928\n"
"v_mfma_f32_16x16x32_bf16 v[96:99], a[216:219], a[16:19], v[96:99]\n"
"s_add_u32 m0, s53, 40960\n"
"ds_read_b64_tr_b16 a[192:193], v201 offset:8352\n"
"buffer_load_dwordx4 a[64:67], v207, s[28:31], s22 offen offset:0 sc1\n"
"v_mfma_f32_16x16x32_bf16 v[100:103], a[216:219], a[20:23], v[100:103]\n"
"s_waitcnt lgkmcnt(11)\n"
"ds_read_b64_tr_b16 a[194:195], v201 offset:12960\n"
"buffer_load_dwordx4 v237, s[24:27], s37 offen offset:2048 lds\n"
"v_mfma_f32_16x16x32_bf16 v[104:107], a[220:223], a[16:19], v[104:107]\n"
"ds_read_b64_tr_b16 a[196:197], v201 offset:8384\n"
"v_mfma_f32_16x16x32_bf16 v[108:111], a[220:223], a[20:23], v[108:111]\n"
"ds_read_b64_tr_b16 a[198:199], v201 offset:12992\n"
"v_mfma_f32_16x16x32_bf16 v[112:115], a[128:131], a[16:19], v[112:115]\n"
"ds_read_b64_tr_b16 a[200:201], v201 offset:8416\n"
"v_mfma_f32_16x16x32_bf16 v[116:119], a[128:131], a[20:23], v[116:119]\n"
"ds_read_b64_tr_b16 a[202:203], v201 offset:13024\n"
"v_mfma_f32_16x16x32_bf16 v[120:123], a[132:135], a[16:19], v[120:123]\n"
"ds_read_b64_tr_b16 a[204:205], v204 offset:8192\n"
"v_mfma_f32_16x16x32_bf16 v[124:127], a[132:135], a[20:23], v[124:127]\n"
"s_add_u32 m0, s53, 40960\n"
"ds_read_b64_tr_b16 a[206:207], v204 offset:12800\n"
"buffer_load_dwordx4 a[68:71], v207, s[28:31], s22 offen offset:1024 sc1\n"
"v_mfma_f32_16x16x32_bf16 v[226:229], a[136:139], a[16:19], v[226:229]\n"
"ds_read_b64_tr_b16 a[208:209], v201 offset:17408\n"
"buffer_load_dwordx4 v238, s[24:27], s37 offen offset:3072 lds\n"
"v_mfma_f32_16x16x32_bf16 v[230:233], a[136:139], a[20:23], v[230:233]\n"
"s_waitcnt lgkmcnt(11)\n"
"s_waitcnt vmcnt(18)\n"
"ds_read_b64_tr_b16 a[210:211], v201 offset:22016\n"
"v_mfma_f32_16x16x32_bf16 v[64:67], a[140:143], a[24:27], v[64:67]\n"
"ds_read_b64_tr_b16 a[212:213], v201 offset:17440\n"
"v_mfma_f32_16x16x32_bf16 v[68:71], a[140:143], a[28:31], v[68:71]\n"
"ds_read_b64_tr_b16 a[214:215], v201 offset:22048\n"
"v_mfma_f32_16x16x32_bf16 v[72:75], a[144:147], a[24:27], v[72:75]\n"
"ds_read_b64_tr_b16 a[216:217], v201 offset:17472\n"
"v_mfma_f32_16x16x32_bf16 v[76:79], a[144:147], a[28:31], v[76:79]\n"
"ds_read_b64_tr_b16 a[218:219], v201 offset:22080\n"
"v_mfma_f32_16x16x32_bf16 v[80:83], a[148:151], a[24:27], v[80:83]\n"
"s_add_u32 m0, s53, 45056\n"
"ds_read_b64_tr_b16 a[220:221], v201 offset:17504\n"
"buffer_load_dwordx4 a[72:75], v207, s[28:31], s20 offen offset:2048 sc1\n"
"v_mfma_f32_16x16x32_bf16 v[84:87], a[148:151], a[28:31], v[84:87]\n"
"ds_read_b64_tr_b16 a[222:223], v201 offset:22112\n"
"buffer_load_dwordx4 v239, s[24:27], s37 offen offset:0 lds\n"
"v_mfma_f32_16x16x32_bf16 v[88:91], a[152:155], a[24:27], v[88:91]\n"
"ds_read_b64_tr_b16 a[128:129], v201 offset:17536\n"
"v_mfma_f32_16x16x32_bf16 v[92:95], a[152:155], a[28:31], v[92:95]\n"
"s_waitcnt lgkmcnt(11)\n"
"ds_read_b64_tr_b16 a[130:131], v201 offset:22144\n"
"v_mfma_f32_16x16x32_bf16 v[96:99], a[156:159], a[24:27], v[96:99]\n"
"ds_read_b64_tr_b16 a[132:133], v201 offset:17568\n"
"v_mfma_f32_16x16x32_bf16 v[100:103], a[156:159], a[28:31], v[100:103]\n"
"ds_read_b64_tr_b16 a[134:135], v201 offset:22176\n"
"v_mfma_f32_16x16x32_bf16 v[104:107], a[192:195], a[24:27], v[104:107]\n"
"ds_read_b64_tr_b16 a[136:137], v201 offset:17600\n"
"v_mfma_f32_16x16x32_bf16 v[108:111], a[192:195], a[28:31], v[108:111]\n"
"ds_read_b64_tr_b16 a[138:139], v201 offset:22208\n"
"buffer_load_dwordx4 a[76:79], v207, s[28:31], s20 offen offset:3072 sc1\n"
"v_mfma_f32_16x16x32_bf16 v[112:115], a[196:199], a[24:27], v[112:115]\n"
"s_add_u32 s20, s20, 4096\n"
"ds_read_b64_tr_b16 a[140:141], v201 offset:17632\n"
"v_mfma_f32_16x16x32_bf16 v[116:119], a[196:199], a[28:31], v[116:119]\n"
"ds_read_b64_tr_b16 a[142:143], v201 offset:22240\n"
"v_mfma_f32_16x16x32_bf16 v[120:123], a[200:203], a[24:27], v[120:123]\n"
"ds_read_b64_tr_b16 a[144:145], v204 offset:17408\n"
"v_mfma_f32_16x16x32_bf16 v[124:127], a[200:203], a[28:31], v[124:127]\n"
"s_waitcnt lgkmcnt(11)\n"
"ds_read_b64_tr_b16 a[146:147], v204 offset:22016\n"
"v_mfma_f32_16x16x32_bf16 v[226:229], a[204:207], a[24:27], v[226:229]\n"
"ds_read_b64_tr_b16 a[148:149], v201 offset:26624\n"
"v_mfma_f32_16x16x32_bf16 v[230:233], a[204:207], a[28:31], v[230:233]\n"
"s_waitcnt vmcnt(18)\n"
"ds_read_b64_tr_b16 a[150:151], v201 offset:31232\n"
"v_mfma_f32_16x16x32_bf16 v[64:67], a[208:211], a[32:35], v[64:67]\n"
"ds_read_b64_tr_b16 a[152:153], v201 offset:26656\n"
"buffer_load_dwordx4 a[80:83], v207, s[28:31], s21 offen offset:2048 sc1\n"
"v_mfma_f32_16x16x32_bf16 v[68:71], a[208:211], a[36:39], v[68:71]\n"
"ds_read_b64_tr_b16 a[154:155], v201 offset:31264\n"
"v_mfma_f32_16x16x32_bf16 v[72:75], a[212:215], a[32:35], v[72:75]\n"
"s_waitcnt vmcnt(31)\n"
"s_barrier\n"
"ds_read_b64_tr_b16 a[156:157], v201 offset:26688\n"
"v_mfma_f32_16x16x32_bf16 v[76:79], a[212:215], a[36:39], v[76:79]\n"
"ds_read_b64_tr_b16 a[158:159], v201 offset:31296\n"
"v_mfma_f32_16x16x32_bf16 v[80:83], a[216:219], a[32:35], v[80:83]\n"
"s_add_u32 m0, s53, 73728\n"
"ds_read_b64_tr_b16 a[192:193], v201 offset:26720\n"
"v_mfma_f32_16x16x32_bf16 v[84:87], a[216:219], a[36:39], v[84:87]\n"
"s_waitcnt lgkmcnt(11)\n"
"ds_read_b64_tr_b16 a[194:195], v201 offset:31328\n"
"buffer_load_dwordx4 v210, s[24:27], s38 offen offset:0 lds\n"
"v_mfma_f32_16x16x32_bf16 v[88:91], a[220:223], a[32:35], v[88:91]\n"
"ds_read_b64_tr_b16 a[196:197], v201 offset:26752\n"
"v_mfma_f32_16x16x32_bf16 v[92:95], a[220:223], a[36:39], v[92:95]\n"
"ds_read_b64_tr_b16 a[198:199], v201 offset:31360\n"
"buffer_load_dwordx4 a[84:87], v207, s[28:31], s21 offen offset:3072 sc1\n"
"v_mfma_f32_16x16x32_bf16 v[96:99], a[128:131], a[32:35], v[96:99]\n"
"s_add_u32 s21, s21, 4096\n"
"ds_read_b64_tr_b16 a[200:201], v201 offset:26784\n"
"v_mfma_f32_16x16x32_bf16 v[100:103], a[128:131], a[36:39], v[100:103]\n"
"ds_read_b64_tr_b16 a[202:203], v201 offset:31392\n"
"v_mfma_f32_16x16x32_bf16 v[104:107], a[132:135], a[32:35], v[104:107]\n"
"ds_read_b64_tr_b16 a[204:205], v201 offset:26816\n"
"v_mfma_f32_16x16x32_bf16 v[108:111], a[132:135], a[36:39], v[108:111]\n"
"s_add_u32 m0, s53, 73728\n"
"ds_read_b64_tr_b16 a[206:207], v201 offset:31424\n"
"v_mfma_f32_16x16x32_bf16 v[112:115], a[136:139], a[32:35], v[112:115]\n"
"ds_read_b64_tr_b16 a[208:209], v201 offset:26848\n"
"buffer_load_dwordx4 v211, s[24:27], s38 offen offset:1024 lds\n"
"v_mfma_f32_16x16x32_bf16 v[116:119], a[136:139], a[36:39], v[116:119]\n"
"s_waitcnt lgkmcnt(11)\n"
"ds_read_b64_tr_b16 a[210:211], v201 offset:31456\n"
"v_mfma_f32_16x16x32_bf16 v[120:123], a[140:143], a[32:35], v[120:123]\n"
"ds_read_b64_tr_b16 a[212:213], v204 offset:26624\n"
"buffer_load_dwordx4 a[88:91], v207, s[28:31], s22 offen offset:2048 sc1\n"
"v_mfma_f32_16x16x32_bf16 v[124:127], a[140:143], a[36:39], v[124:127]\n"
"ds_read_b64_tr_b16 a[214:215], v204 offset:31232\n"
"v_mfma_f32_16x16x32_bf16 v[226:229], a[144:147], a[32:35], v[226:229]\n"
"ds_read_b64_tr_b16 a[216:217], v201 offset:35840\n"
"v_mfma_f32_16x16x32_bf16 v[230:233], a[144:147], a[36:39], v[230:233]\n"
"s_waitcnt vmcnt(20)\n"
"ds_read_b64_tr_b16 a[218:219], v201 offset:40448\n"
"v_mfma_f32_16x16x32_bf16 v[64:67], a[148:151], a[40:43], v[64:67]\n"
"s_add_u32 m0, s53, 73728\n"
"ds_read_b64_tr_b16 a[220:221], v201 offset:35872\n"
"v_mfma_f32_16x16x32_bf16 v[68:71], a[148:151], a[44:47], v[68:71]\n"
"ds_read_b64_tr_b16 a[222:223], v201 offset:40480\n"
"buffer_load_dwordx4 v212, s[24:27], s38 offen offset:2048 lds\n"
"v_mfma_f32_16x16x32_bf16 v[72:75], a[152:155], a[40:43], v[72:75]\n"
"ds_read_b64_tr_b16 a[128:129], v201 offset:35904\n"
"v_mfma_f32_16x16x32_bf16 v[76:79], a[152:155], a[44:47], v[76:79]\n"
"s_waitcnt lgkmcnt(11)\n"
"ds_read_b64_tr_b16 a[130:131], v201 offset:40512\n"
"buffer_load_dwordx4 a[92:95], v207, s[28:31], s22 offen offset:3072 sc1\n"
"v_mfma_f32_16x16x32_bf16 v[80:83], a[156:159], a[40:43], v[80:83]\n"
"s_add_u32 s22, s22, 4096\n"
"ds_read_b64_tr_b16 a[132:133], v201 offset:35936\n"
"v_mfma_f32_16x16x32_bf16 v[84:87], a[156:159], a[44:47], v[84:87]\n"
"ds_read_b64_tr_b16 a[134:135], v201 offset:40544\n"
"v_mfma_f32_16x16x32_bf16 v[88:91], a[192:195], a[40:43], v[88:91]\n"
"ds_read_b64_tr_b16 a[136:137], v201 offset:35968\n"
"v_mfma_f32_16x16x32_bf16 v[92:95], a[192:195], a[44:47], v[92:95]\n"
"s_add_u32 m0, s53, 73728\n"
"ds_read_b64_tr_b16 a[138:139], v201 offset:40576\n"
"v_mfma_f32_16x16x32_bf16 v[96:99], a[196:199], a[40:43], v[96:99]\n"
"ds_read_b64_tr_b16 a[140:141], v201 offset:36000\n"
"buffer_load_dwordx4 v213, s[24:27], s38 offen offset:3072 lds\n"
"v_mfma_f32_16x16x32_bf16 v[100:103], a[196:199], a[44:47], v[100:103]\n"
"ds_read_b64_tr_b16 a[142:143], v201 offset:40608\n"
"v_mfma_f32_16x16x32_bf16 v[104:107], a[200:203], a[40:43], v[104:107]\n"
"ds_read_b64_tr_b16 a[144:145], v201 offset:36032\n"
"buffer_load_dwordx4 a[96:99], v207, s[28:31], s20 offen offset:0 sc1\n"
"v_mfma_f32_16x16x32_bf16 v[108:111], a[200:203], a[44:47], v[108:111]\n"
"s_waitcnt lgkmcnt(11)\n"
"ds_read_b64_tr_b16 a[146:147], v201 offset:40640\n"
"v_mfma_f32_16x16x32_bf16 v[112:115], a[204:207], a[40:43], v[112:115]\n"
"ds_read_b64_tr_b16 a[148:149], v201 offset:36064\n"
"v_mfma_f32_16x16x32_bf16 v[116:119], a[204:207], a[44:47], v[116:119]\n"
"ds_read_b64_tr_b16 a[150:151], v201 offset:40672\n"
"v_mfma_f32_16x16x32_bf16 v[120:123], a[208:211], a[40:43], v[120:123]\n"
"s_add_u32 m0, s53, 77824\n"
"ds_read_b64_tr_b16 a[152:153], v204 offset:35840\n"
"v_mfma_f32_16x16x32_bf16 v[124:127], a[208:211], a[44:47], v[124:127]\n"
"ds_read_b64_tr_b16 a[154:155], v204 offset:40448\n"
"buffer_load_dwordx4 v214, s[24:27], s38 offen offset:0 lds\n"
"v_mfma_f32_16x16x32_bf16 v[226:229], a[212:215], a[40:43], v[226:229]\n"
"ds_read_b64_tr_b16 a[156:157], v201 offset:45056\n"
"v_mfma_f32_16x16x32_bf16 v[230:233], a[212:215], a[44:47], v[230:233]\n"
"s_waitcnt vmcnt(21)\n"
"ds_read_b64_tr_b16 a[158:159], v201 offset:49664\n"
"buffer_load_dwordx4 a[100:103], v207, s[28:31], s20 offen offset:1024 sc1\n"
"v_mfma_f32_16x16x32_bf16 v[64:67], a[216:219], a[48:51], v[64:67]\n"
"ds_read_b64_tr_b16 a[192:193], v201 offset:45088\n"
"v_mfma_f32_16x16x32_bf16 v[68:71], a[216:219], a[52:55], v[68:71]\n"
"s_waitcnt lgkmcnt(11)\n"
"ds_read_b64_tr_b16 a[194:195], v201 offset:49696\n"
"v_mfma_f32_16x16x32_bf16 v[72:75], a[220:223], a[48:51], v[72:75]\n"
"ds_read_b64_tr_b16 a[196:197], v201 offset:45120\n"
"v_mfma_f32_16x16x32_bf16 v[76:79], a[220:223], a[52:55], v[76:79]\n"
"s_add_u32 m0, s53, 77824\n"
"ds_read_b64_tr_b16 a[198:199], v201 offset:49728\n"
"v_mfma_f32_16x16x32_bf16 v[80:83], a[128:131], a[48:51], v[80:83]\n"
"ds_read_b64_tr_b16 a[200:201], v201 offset:45152\n"
"buffer_load_dwordx4 v236, s[24:27], s38 offen offset:1024 lds\n"
"v_mfma_f32_16x16x32_bf16 v[84:87], a[128:131], a[52:55], v[84:87]\n"
"ds_read_b64_tr_b16 a[202:203], v201 offset:49760\n"
"v_mfma_f32_16x16x32_bf16 v[88:91], a[132:135], a[48:51], v[88:91]\n"
"ds_read_b64_tr_b16 a[204:205], v201 offset:45184\n"
"buffer_load_dwordx4 a[104:107], v207, s[28:31], s21 offen offset:0 sc1\n"
"v_mfma_f32_16x16x32_bf16 v[92:95], a[132:135], a[52:55], v[92:95]\n"
"ds_read_b64_tr_b16 a[206:207], v201 offset:49792\n"
"v_mfma_f32_16x16x32_bf16 v[96:99], a[136:139], a[48:51], v[96:99]\n"
"ds_read_b64_tr_b16 a[208:209], v201 offset:45216\n"
"v_mfma_f32_16x16x32_bf16 v[100:103], a[136:139], a[52:55], v[100:103]\n"
"s_waitcnt lgkmcnt(11)\n"
"ds_read_b64_tr_b16 a[210:211], v201 offset:49824\n"
"v_mfma_f32_16x16x32_bf16 v[104:107], a[140:143], a[48:51], v[104:107]\n"
"s_add_u32 m0, s53, 77824\n"
"ds_read_b64_tr_b16 a[212:213], v201 offset:45248\n"
"v_mfma_f32_16x16x32_bf16 v[108:111], a[140:143], a[52:55], v[108:111]\n"
"ds_read_b64_tr_b16 a[214:215], v201 offset:49856\n"
"buffer_load_dwordx4 v237, s[24:27], s38 offen offset:2048 lds\n"
"v_mfma_f32_16x16x32_bf16 v[112:115], a[144:147], a[48:51], v[112:115]\n"
"ds_read_b64_tr_b16 a[216:217], v201 offset:45280\n"
"v_mfma_f32_16x16x32_bf16 v[116:119], a[144:147], a[52:55], v[116:119]\n"
"ds_read_b64_tr_b16 a[218:219], v201 offset:49888\n"
"buffer_load_dwordx4 a[108:111], v207, s[28:31], s21 offen offset:1024 sc1\n"
"v_mfma_f32_16x16x32_bf16 v[120:123], a[148:151], a[48:51], v[120:123]\n"
"ds_read_b64_tr_b16 a[220:221], v204 offset:45056\n"
"v_mfma_f32_16x16x32_bf16 v[124:127], a[148:151], a[52:55], v[124:127]\n"
"ds_read_b64_tr_b16 a[222:223], v204 offset:49664\n"
"v_mfma_f32_16x16x32_bf16 v[226:229], a[152:155], a[48:51], v[226:229]\n"
"ds_read_b64_tr_b16 a[128:129], v202 offset:0\n"
"v_mfma_f32_16x16x32_bf16 v[230:233], a[152:155], a[52:55], v[230:233]\n"
"s_waitcnt lgkmcnt(11)\n"
"s_waitcnt vmcnt(23)\n"
"ds_read_b64_tr_b16 a[130:131], v202 offset:4608\n"
"v_mfma_f32_16x16x32_bf16 v[64:67], a[156:159], a[56:59], v[64:67]\n"
"s_add_u32 m0, s53, 77824\n"
"ds_read_b64_tr_b16 a[132:133], v202 offset:32\n"
"v_mfma_f32_16x16x32_bf16 v[68:71], a[156:159], a[60:63], v[68:71]\n"
"ds_read_b64_tr_b16 a[134:135], v202 offset:4640\n"
"buffer_load_dwordx4 v238, s[24:27], s38 offen offset:3072 lds\n"
"v_mfma_f32_16x16x32_bf16 v[72:75], a[192:195], a[56:59], v[72:75]\n"
"ds_read_b64_tr_b16 a[136:137], v202 offset:64\n"
"buffer_load_dwordx4 a[112:115], v207, s[28:31], s22 offen offset:0 sc1\n"
"v_mfma_f32_16x16x32_bf16 v[76:79], a[192:195], a[60:63], v[76:79]\n"
"ds_read_b64_tr_b16 a[138:139], v202 offset:4672\n"
"v_mfma_f32_16x16x32_bf16 v[80:83], a[196:199], a[56:59], v[80:83]\n"
"ds_read_b64_tr_b16 a[140:141], v202 offset:96\n"
"v_mfma_f32_16x16x32_bf16 v[84:87], a[196:199], a[60:63], v[84:87]\n"
"ds_read_b64_tr_b16 a[142:143], v202 offset:4704\n"
"v_mfma_f32_16x16x32_bf16 v[88:91], a[200:203], a[56:59], v[88:91]\n"
"ds_read_b64_tr_b16 a[144:145], v202 offset:128\n"
"v_mfma_f32_16x16x32_bf16 v[92:95], a[200:203], a[60:63], v[92:95]\n"
"s_waitcnt lgkmcnt(11)\n"
"s_add_u32 m0, s53, 81920\n"
"ds_read_b64_tr_b16 a[146:147], v202 offset:4736\n"
"v_mfma_f32_16x16x32_bf16 v[96:99], a[204:207], a[56:59], v[96:99]\n"
"ds_read_b64_tr_b16 a[148:149], v202 offset:160\n"
"buffer_load_dwordx4 v239, s[24:27], s38 offen offset:0 lds\n"
"v_mfma_f32_16x16x32_bf16 v[100:103], a[204:207], a[60:63], v[100:103]\n"
"ds_read_b64_tr_b16 a[150:151], v202 offset:4768\n"
"buffer_load_dwordx4 a[116:119], v207, s[28:31], s22 offen offset:1024 sc1\n"
"v_mfma_f32_16x16x32_bf16 v[104:107], a[208:211], a[56:59], v[104:107]\n"
"ds_read_b64_tr_b16 a[152:153], v202 offset:192\n"
"v_mfma_f32_16x16x32_bf16 v[108:111], a[208:211], a[60:63], v[108:111]\n"
"ds_read_b64_tr_b16 a[154:155], v202 offset:4800\n"
"v_mfma_f32_16x16x32_bf16 v[112:115], a[212:215], a[56:59], v[112:115]\n"
"ds_read_b64_tr_b16 a[156:157], v202 offset:224\n"
"v_mfma_f32_16x16x32_bf16 v[116:119], a[212:215], a[60:63], v[116:119]\n"
"ds_read_b64_tr_b16 a[158:159], v202 offset:4832\n"
"v_mfma_f32_16x16x32_bf16 v[120:123], a[216:219], a[56:59], v[120:123]\n"
"ds_read_b64_tr_b16 a[192:193], v205 offset:0\n"
"v_mfma_f32_16x16x32_bf16 v[124:127], a[216:219], a[60:63], v[124:127]\n"
"s_waitcnt lgkmcnt(11)\n"
"ds_read_b64_tr_b16 a[194:195], v205 offset:4608\n"
"v_mfma_f32_16x16x32_bf16 v[226:229], a[220:223], a[56:59], v[226:229]\n"
"ds_read_b64_tr_b16 a[196:197], v202 offset:9216\n"
"buffer_load_dwordx4 a[120:123], v207, s[28:31], s20 offen offset:2048 sc1\n"
"v_mfma_f32_16x16x32_bf16 v[230:233], a[220:223], a[60:63], v[230:233]\n"
"s_waitcnt vmcnt(24)\n"
"ds_read_b64_tr_b16 a[198:199], v202 offset:13824\n"
"v_mfma_f32_16x16x32_bf16 v[64:67], a[128:131], a[64:67], v[64:67]\n"
"ds_read_b64_tr_b16 a[200:201], v202 offset:9248\n"
"v_mfma_f32_16x16x32_bf16 v[68:71], a[128:131], a[68:71], v[68:71]\n"
"ds_read_b64_tr_b16 a[202:203], v202 offset:13856\n"
"v_mfma_f32_16x16x32_bf16 v[72:75], a[132:135], a[64:67], v[72:75]\n"
"s_waitcnt vmcnt(21)\n"
"s_barrier\n"
"ds_read_b64_tr_b16 a[204:205], v202 offset:9280\n"
"v_mfma_f32_16x16x32_bf16 v[76:79], a[132:135], a[68:71], v[76:79]\n"
"ds_read_b64_tr_b16 a[206:207], v202 offset:13888\n"
"v_mfma_f32_16x16x32_bf16 v[80:83], a[136:139], a[64:67], v[80:83]\n"
"ds_read_b64_tr_b16 a[208:209], v202 offset:9312\n"
"v_mfma_f32_16x16x32_bf16 v[84:87], a[136:139], a[68:71], v[84:87]\n"
"s_waitcnt lgkmcnt(11)\n"
"s_add_u32 m0, s53, 110592\n"
"ds_read_b64_tr_b16 a[210:211], v202 offset:13920\n"
"buffer_load_dwordx4 a[124:127], v207, s[28:31], s20 offen offset:3072 sc1\n"
"v_mfma_f32_16x16x32_bf16 v[88:91], a[140:143], a[64:67], v[88:91]\n"
"s_add_u32 s20, s20, 4096\n"
"ds_read_b64_tr_b16 a[212:213], v202 offset:9344\n"
"buffer_load_dwordx4 v210, s[24:27], s39 offen offset:0 lds\n"
"v_mfma_f32_16x16x32_bf16 v[92:95], a[140:143], a[68:71], v[92:95]\n"
"ds_read_b64_tr_b16 a[214:215], v202 offset:13952\n"
"v_mfma_f32_16x16x32_bf16 v[96:99], a[144:147], a[64:67], v[96:99]\n"
"ds_read_b64_tr_b16 a[216:217], v202 offset:9376\n"
"v_mfma_f32_16x16x32_bf16 v[100:103], a[144:147], a[68:71], v[100:103]\n"
"ds_read_b64_tr_b16 a[218:219], v202 offset:13984\n"
"v_mfma_f32_16x16x32_bf16 v[104:107], a[148:151], a[64:67], v[104:107]\n"
"ds_read_b64_tr_b16 a[220:221], v202 offset:9408\n"
"v_mfma_f32_16x16x32_bf16 v[108:111], a[148:151], a[68:71], v[108:111]\n"
"ds_read_b64_tr_b16 a[222:223], v202 offset:14016\n"
"v_mfma_f32_16x16x32_bf16 v[112:115], a[152:155], a[64:67], v[112:115]\n"
"s_add_u32 m0, s53, 110592\n"
"ds_read_b64_tr_b16 a[128:129], v202 offset:9440\n"
"buffer_load_dwordx4 v[0:3], v207, s[28:31], s21 offen offset:2048 sc1\n"
"v_mfma_f32_16x16x32_bf16 v[116:119], a[152:155], a[68:71], v[116:119]\n"
"s_waitcnt lgkmcnt(11)\n"
"ds_read_b64_tr_b16 a[130:131], v202 offset:14048\n"
"buffer_load_dwordx4 v211, s[24:27], s39 offen offset:1024 lds\n"
"v_mfma_f32_16x16x32_bf16 v[120:123], a[156:159], a[64:67], v[120:123]\n"
"ds_read_b64_tr_b16 a[132:133], v205 offset:9216\n"
"v_mfma_f32_16x16x32_bf16 v[124:127], a[156:159], a[68:71], v[124:127]\n"
"ds_read_b64_tr_b16 a[134:135], v205 offset:13824\n"
"v_mfma_f32_16x16x32_bf16 v[226:229], a[192:195], a[64:67], v[226:229]\n"
"ds_read_b64_tr_b16 a[136:137], v202 offset:18432\n"
"v_mfma_f32_16x16x32_bf16 v[230:233], a[192:195], a[68:71], v[230:233]\n"
"s_waitcnt vmcnt(24)\n"
"ds_read_b64_tr_b16 a[138:139], v202 offset:23040\n"
"v_mfma_f32_16x16x32_bf16 v[64:67], a[196:199], a[72:75], v[64:67]\n"
"ds_read_b64_tr_b16 a[140:141], v202 offset:18464\n"
"v_mfma_f32_16x16x32_bf16 v[68:71], a[196:199], a[76:79], v[68:71]\n"
"s_add_u32 m0, s53, 110592\n"
"ds_read_b64_tr_b16 a[142:143], v202 offset:23072\n"
"buffer_load_dwordx4 v[4:7], v207, s[28:31], s21 offen offset:3072 sc1\n"
"v_mfma_f32_16x16x32_bf16 v[72:75], a[200:203], a[72:75], v[72:75]\n"
"s_add_u32 s21, s21, 4096\n"
"ds_read_b64_tr_b16 a[144:145], v202 offset:18496\n"
"buffer_load_dwordx4 v212, s[24:27], s39 offen offset:2048 lds\n"
"v_mfma_f32_16x16x32_bf16 v[76:79], a[200:203], a[76:79], v[76:79]\n"
"s_waitcnt lgkmcnt(11)\n"
"ds_read_b64_tr_b16 a[146:147], v202 offset:23104\n"
"v_mfma_f32_16x16x32_bf16 v[80:83], a[204:207], a[72:75], v[80:83]\n"
"ds_read_b64_tr_b16 a[148:149], v202 offset:18528\n"
"v_mfma_f32_16x16x32_bf16 v[84:87], a[204:207], a[76:79], v[84:87]\n"
"ds_read_b64_tr_b16 a[150:151], v202 offset:23136\n"
"v_mfma_f32_16x16x32_bf16 v[88:91], a[208:211], a[72:75], v[88:91]\n"
"ds_read_b64_tr_b16 a[152:153], v202 offset:18560\n"
"v_mfma_f32_16x16x32_bf16 v[92:95], a[208:211], a[76:79], v[92:95]\n"
"ds_read_b64_tr_b16 a[154:155], v202 offset:23168\n"
"v_mfma_f32_16x16x32_bf16 v[96:99], a[212:215], a[72:75], v[96:99]\n"
"s_add_u32 m0, s53, 110592\n"
"ds_read_b64_tr_b16 a[156:157], v202 offset:18592\n"
"buffer_load_dwordx4 v[8:11], v207, s[28:31], s22 offen offset:2048 sc1\n"
"v_mfma_f32_16x16x32_bf16 v[100:103], a[212:215], a[76:79], v[100:103]\n"
"ds_read_b64_tr_b16 a[158:159], v202 offset:23200\n"
"buffer_load_dwordx4 v213, s[24:27], s39 offen offset:3072 lds\n"
"v_mfma_f32_16x16x32_bf16 v[104:107], a[216:219], a[72:75], v[104:107]\n"
"ds_read_b64_tr_b16 a[192:193], v202 offset:18624\n"
"v_mfma_f32_16x16x32_bf16 v[108:111], a[216:219], a[76:79], v[108:111]\n"
"s_waitcnt lgkmcnt(11)\n"
"ds_read_b64_tr_b16 a[194:195], v202 offset:23232\n"
"v_mfma_f32_16x16x32_bf16 v[112:115], a[220:223], a[72:75], v[112:115]\n"
"ds_read_b64_tr_b16 a[196:197], v202 offset:18656\n"
"v_mfma_f32_16x16x32_bf16 v[116:119], a[220:223], a[76:79], v[116:119]\n"
"ds_read_b64_tr_b16 a[198:199], v202 offset:23264\n"
"v_mfma_f32_16x16x32_bf16 v[120:123], a[128:131], a[72:75], v[120:123]\n"
"ds_read_b64_tr_b16 a[200:201], v205 offset:18432\n"
"v_mfma_f32_16x16x32_bf16 v[124:127], a[128:131], a[76:79], v[124:127]\n"
"s_add_u32 m0, s53, 114688\n"
"ds_read_b64_tr_b16 a[202:203], v205 offset:23040\n"
"buffer_load_dwordx4 v[12:15], v207, s[28:31], s22 offen offset:3072 sc1\n"
"v_mfma_f32_16x16x32_bf16 v[226:229], a[132:135], a[72:75], v[226:229]\n"
"s_add_u32 s22, s22, 4096\n"
"ds_read_b64_tr_b16 a[204:205], v200 offset:0\n"
"buffer_load_dwordx4 v214, s[24:27], s39 offen offset:0 lds\n"
"v_mfma_f32_16x16x32_bf16 v[230:233], a[132:135], a[76:79], v[230:233]\n"
"s_waitcnt vmcnt(27)\n"
"ds_read_b64_tr_b16 a[206:207], v200 offset:4608\n"
"v_mfma_f32_16x16x32_bf16 v[64:67], a[136:139], a[80:83], v[64:67]\n"
"ds_read_b64_tr_b16 a[208:209], v200 offset:32\n"
"v_mfma_f32_16x16x32_bf16 v[68:71], a[136:139], a[84:87], v[68:71]\n"
"s_waitcnt lgkmcnt(11)\n"
"ds_read_b64_tr_b16 a[210:211], v200 offset:4640\n"
"v_mfma_f32_16x16x32_bf16 v[72:75], a[140:143], a[80:83], v[72:75]\n"
"ds_read_b64_tr_b16 a[212:213], v200 offset:64\n"
"v_mfma_f32_16x16x32_bf16 v[76:79], a[140:143], a[84:87], v[76:79]\n"
"ds_read_b64_tr_b16 a[214:215], v200 offset:4672\n"
"v_mfma_f32_16x16x32_bf16 v[80:83], a[144:147], a[80:83], v[80:83]\n"
"s_add_u32 m0, s53, 114688\n"
"ds_read_b64_tr_b16 a[216:217], v200 offset:96\n"
"buffer_load_dwordx4 v[16:19], v207, s[28:31], s20 offen offset:0 sc1\n"
"v_mfma_f32_16x16x32_bf16 v[84:87], a[144:147], a[84:87], v[84:87]\n"
"ds_read_b64_tr_b16 a[218:219], v200 offset:4704\n"
"buffer_load_dwordx4 v236, s[24:27], s39 offen offset:1024 lds\n"
"v_mfma_f32_16x16x32_bf16 v[88:91], a[148:151], a[80:83], v[88:91]\n"
"ds_read_b64_tr_b16 a[220:221], v200 offset:128\n"
"v_mfma_f32_16x16x32_bf16 v[92:95], a[148:151], a[84:87], v[92:95]\n"
"ds_read_b64_tr_b16 a[222:223], v200 offset:4736\n"
"v_mfma_f32_16x16x32_bf16 v[96:99], a[152:155], a[80:83], v[96:99]\n"
"ds_read_b64_tr_b16 a[128:129], v200 offset:160\n"
"v_mfma_f32_16x16x32_bf16 v[100:103], a[152:155], a[84:87], v[100:103]\n"
"s_waitcnt lgkmcnt(11)\n"
"ds_read_b64_tr_b16 a[130:131], v200 offset:4768\n"
"v_mfma_f32_16x16x32_bf16 v[104:107], a[156:159], a[80:83], v[104:107]\n"
"ds_read_b64_tr_b16 a[132:133], v200 offset:192\n"
"v_mfma_f32_16x16x32_bf16 v[108:111], a[156:159], a[84:87], v[108:111]\n"
"s_add_u32 m0, s53, 114688\n"
"ds_read_b64_tr_b16 a[134:135], v200 offset:4800\n"
"buffer_load_dwordx4 v[20:23], v207, s[28:31], s20 offen offset:1024 sc1\n"
"v_mfma_f32_16x16x32_bf16 v[112:115], a[192:195], a[80:83], v[112:115]\n"
"ds_read_b64_tr_b16 a[136:137], v200 offset:224\n"
"buffer_load_dwordx4 v237, s[24:27], s39 offen offset:2048 lds\n"
"v_mfma_f32_16x16x32_bf16 v[116:119], a[192:195], a[84:87], v[116:119]\n"
"ds_read_b64_tr_b16 a[138:139], v200 offset:4832\n"
"v_mfma_f32_16x16x32_bf16 v[120:123], a[196:199], a[80:83], v[120:123]\n"
"ds_read_b64_tr_b16 a[140:141], v203 offset:0\n"
"v_mfma_f32_16x16x32_bf16 v[124:127], a[196:199], a[84:87], v[124:127]\n"
"ds_read_b64_tr_b16 a[142:143], v203 offset:4608\n"
"v_mfma_f32_16x16x32_bf16 v[226:229], a[200:203], a[80:83], v[226:229]\n"
"ds_read_b64_tr_b16 a[144:145], v200 offset:9216\n"
"v_mfma_f32_16x16x32_bf16 v[230:233], a[200:203], a[84:87], v[230:233]\n"
"s_waitcnt lgkmcnt(11)\n"
"s_waitcnt vmcnt(27)\n"
"ds_read_b64_tr_b16 a[146:147], v200 offset:13824\n"
"v_mfma_f32_16x16x32_bf16 v[64:67], a[204:207], a[88:91], v[64:67]\n"
"s_add_u32 m0, s53, 114688\n"
"ds_read_b64_tr_b16 a[148:149], v200 offset:9248\n"
"buffer_load_dwordx4 v[24:27], v207, s[28:31], s21 offen offset:0 sc1\n"
"v_mfma_f32_16x16x32_bf16 v[68:71], a[204:207], a[92:95], v[68:71]\n"
"ds_read_b64_tr_b16 a[150:151], v200 offset:13856\n"
"buffer_load_dwordx4 v238, s[24:27], s39 offen offset:3072 lds\n"
"v_mfma_f32_16x16x32_bf16 v[72:75], a[208:211], a[88:91], v[72:75]\n"
"ds_read_b64_tr_b16 a[152:153], v200 offset:9280\n"
"v_mfma_f32_16x16x32_bf16 v[76:79], a[208:211], a[92:95], v[76:79]\n"
"ds_read_b64_tr_b16 a[154:155], v200 offset:13888\n"
"v_mfma_f32_16x16x32_bf16 v[80:83], a[212:215], a[88:91], v[80:83]\n"
"ds_read_b64_tr_b16 a[156:157], v200 offset:9312\n"
"v_mfma_f32_16x16x32_bf16 v[84:87], a[212:215], a[92:95], v[84:87]\n"
"ds_read_b64_tr_b16 a[158:159], v200 offset:13920\n"
"v_mfma_f32_16x16x32_bf16 v[88:91], a[216:219], a[88:91], v[88:91]\n"
"ds_read_b64_tr_b16 a[192:193], v200 offset:9344\n"
"v_mfma_f32_16x16x32_bf16 v[92:95], a[216:219], a[92:95], v[92:95]\n"
"s_waitcnt lgkmcnt(11)\n"
"s_add_u32 m0, s53, 118784\n"
"ds_read_b64_tr_b16 a[194:195], v200 offset:13952\n"
"buffer_load_dwordx4 v[28:31], v207, s[28:31], s21 offen offset:1024 sc1\n"
"v_mfma_f32_16x16x32_bf16 v[96:99], a[220:223], a[88:91], v[96:99]\n"
"ds_read_b64_tr_b16 a[196:197], v200 offset:9376\n"
"buffer_load_dwordx4 v239, s[24:27], s39 offen offset:0 lds\n"
"v_mfma_f32_16x16x32_bf16 v[100:103], a[220:223], a[92:95], v[100:103]\n"
"ds_read_b64_tr_b16 a[198:199], v200 offset:13984\n"
"v_mfma_f32_16x16x32_bf16 v[104:107], a[128:131], a[88:91], v[104:107]\n"
"ds_read_b64_tr_b16 a[200:201], v200 offset:9408\n"
"v_mfma_f32_16x16x32_bf16 v[108:111], a[128:131], a[92:95], v[108:111]\n"
"ds_read_b64_tr_b16 a[202:203], v200 offset:14016\n"
"v_mfma_f32_16x16x32_bf16 v[112:115], a[132:135], a[88:91], v[112:115]\n"
"ds_read_b64_tr_b16 a[204:205], v200 offset:9440\n"
"v_mfma_f32_16x16x32_bf16 v[116:119], a[132:135], a[92:95], v[116:119]\n"
"ds_read_b64_tr_b16 a[206:207], v200 offset:14048\n"
"v_mfma_f32_16x16x32_bf16 v[120:123], a[136:139], a[88:91], v[120:123]\n"
"ds_read_b64_tr_b16 a[208:209], v203 offset:9216\n"
"buffer_load_dwordx4 v[32:35], v207, s[28:31], s22 offen offset:0 sc1\n"
"v_mfma_f32_16x16x32_bf16 v[124:127], a[136:139], a[92:95], v[124:127]\n"
"s_waitcnt lgkmcnt(11)\n"
"ds_read_b64_tr_b16 a[210:211], v203 offset:13824\n"
"v_mfma_f32_16x16x32_bf16 v[226:229], a[140:143], a[88:91], v[226:229]\n"
"ds_read_b64_tr_b16 a[212:213], v200 offset:18432\n"
"v_mfma_f32_16x16x32_bf16 v[230:233], a[140:143], a[92:95], v[230:233]\n"
"s_waitcnt vmcnt(28)\n"
"ds_read_b64_tr_b16 a[214:215], v200 offset:23040\n"
"v_mfma_f32_16x16x32_bf16 v[64:67], a[144:147], a[96:99], v[64:67]\n"
"ds_read_b64_tr_b16 a[216:217], v200 offset:18464\n"
"v_mfma_f32_16x16x32_bf16 v[68:71], a[144:147], a[100:103], v[68:71]\n"
"ds_read_b64_tr_b16 a[218:219], v200 offset:23072\n"
"v_mfma_f32_16x16x32_bf16 v[72:75], a[148:151], a[96:99], v[72:75]\n"
"ds_read_b64_tr_b16 a[220:221], v200 offset:18496\n"
"v_mfma_f32_16x16x32_bf16 v[76:79], a[148:151], a[100:103], v[76:79]\n"
"ds_read_b64_tr_b16 a[222:223], v200 offset:23104\n"
"buffer_load_dwordx4 v[36:39], v207, s[28:31], s22 offen offset:1024 sc1\n"
"v_mfma_f32_16x16x32_bf16 v[80:83], a[152:155], a[96:99], v[80:83]\n"
"ds_read_b64_tr_b16 a[128:129], v200 offset:18528\n"
"v_mfma_f32_16x16x32_bf16 v[84:87], a[152:155], a[100:103], v[84:87]\n"
"s_waitcnt lgkmcnt(11)\n"
"ds_read_b64_tr_b16 a[130:131], v200 offset:23136\n"
"v_mfma_f32_16x16x32_bf16 v[88:91], a[156:159], a[96:99], v[88:91]\n"
"ds_read_b64_tr_b16 a[132:133], v200 offset:18560\n"
"v_mfma_f32_16x16x32_bf16 v[92:95], a[156:159], a[100:103], v[92:95]\n"
"ds_read_b64_tr_b16 a[134:135], v200 offset:23168\n"
"v_mfma_f32_16x16x32_bf16 v[96:99], a[192:195], a[96:99], v[96:99]\n"
"ds_read_b64_tr_b16 a[136:137], v200 offset:18592\n"
"v_mfma_f32_16x16x32_bf16 v[100:103], a[192:195], a[100:103], v[100:103]\n"
"ds_read_b64_tr_b16 a[138:139], v200 offset:23200\n"
"v_mfma_f32_16x16x32_bf16 v[104:107], a[196:199], a[96:99], v[104:107]\n"
"ds_read_b64_tr_b16 a[140:141], v200 offset:18624\n"
"buffer_load_dwordx4 v[40:43], v207, s[28:31], s20 offen offset:2048 sc1\n"
"v_mfma_f32_16x16x32_bf16 v[108:111], a[196:199], a[100:103], v[108:111]\n"
"ds_read_b64_tr_b16 a[142:143], v200 offset:23232\n"
"v_mfma_f32_16x16x32_bf16 v[112:115], a[200:203], a[96:99], v[112:115]\n"
"ds_read_b64_tr_b16 a[144:145], v200 offset:18656\n"
"v_mfma_f32_16x16x32_bf16 v[116:119], a[200:203], a[100:103], v[116:119]\n"
"s_waitcnt lgkmcnt(11)\n"
"ds_read_b64_tr_b16 a[146:147], v200 offset:23264\n"
"v_mfma_f32_16x16x32_bf16 v[120:123], a[204:207], a[96:99], v[120:123]\n"
"ds_read_b64_tr_b16 a[148:149], v203 offset:18432\n"
"v_mfma_f32_16x16x32_bf16 v[124:127], a[204:207], a[100:103], v[124:127]\n"
"ds_read_b64_tr_b16 a[150:151], v203 offset:23040\n"
"v_mfma_f32_16x16x32_bf16 v[226:229], a[208:211], a[96:99], v[226:229]\n"
"ds_read_b64_tr_b16 a[152:153], v200 offset:27648\n"
"v_mfma_f32_16x16x32_bf16 v[230:233], a[208:211], a[100:103], v[230:233]\n"
"s_waitcnt vmcnt(26)\n"
"ds_read_b64_tr_b16 a[154:155], v200 offset:32256\n"
"buffer_load_dwordx4 v[44:47], v207, s[28:31], s20 offen offset:3072 sc1\n"
"v_mfma_f32_16x16x32_bf16 v[64:67], a[212:215], a[104:107], v[64:67]\n"
"s_add_u32 s20, s20, 4096\n"
"ds_read_b64_tr_b16 a[156:157], v200 offset:27680\n"
"v_mfma_f32_16x16x32_bf16 v[68:71], a[212:215], a[108:111], v[68:71]\n"
"ds_read_b64_tr_b16 a[158:159], v200 offset:32288\n"
"v_mfma_f32_16x16x32_bf16 v[72:75], a[216:219], a[104:107], v[72:75]\n"
"ds_read_b64_tr_b16 a[192:193], v200 offset:27712\n"
"v_mfma_f32_16x16x32_bf16 v[76:79], a[216:219], a[108:111], v[76:79]\n"
"s_waitcnt lgkmcnt(11)\n"
"ds_read_b64_tr_b16 a[194:195], v200 offset:32320\n"
"v_mfma_f32_16x16x32_bf16 v[80:83], a[220:223], a[104:107], v[80:83]\n"
"ds_read_b64_tr_b16 a[196:197], v200 offset:27744\n"
"v_mfma_f32_16x16x32_bf16 v[84:87], a[220:223], a[108:111], v[84:87]\n"
"ds_read_b64_tr_b16 a[198:199], v200 offset:32352\n"
"v_mfma_f32_16x16x32_bf16 v[88:91], a[128:131], a[104:107], v[88:91]\n"
"ds_read_b64_tr_b16 a[200:201], v200 offset:27776\n"
"buffer_load_dwordx4 v[48:51], v207, s[28:31], s21 offen offset:2048 sc1\n"
"v_mfma_f32_16x16x32_bf16 v[92:95], a[128:131], a[108:111], v[92:95]\n"
"ds_read_b64_tr_b16 a[202:203], v200 offset:32384\n"
"v_mfma_f32_16x16x32_bf16 v[96:99], a[132:135], a[104:107], v[96:99]\n"
"ds_read_b64_tr_b16 a[204:205], v200 offset:27808\n"
"v_mfma_f32_16x16x32_bf16 v[100:103], a[132:135], a[108:111], v[100:103]\n"
"ds_read_b64_tr_b16 a[206:207], v200 offset:32416\n"
"v_mfma_f32_16x16x32_bf16 v[104:107], a[136:139], a[104:107], v[104:107]\n"
"ds_read_b64_tr_b16 a[208:209], v200 offset:27840\n"
"v_mfma_f32_16x16x32_bf16 v[108:111], a[136:139], a[108:111], v[108:111]\n"
"s_waitcnt lgkmcnt(11)\n"
"ds_read_b64_tr_b16 a[210:211], v200 offset:32448\n"
"v_mfma_f32_16x16x32_bf16 v[112:115], a[140:143], a[104:107], v[112:115]\n"
"ds_read_b64_tr_b16 a[212:213], v200 offset:27872\n"
"v_mfma_f32_16x16x32_bf16 v[116:119], a[140:143], a[108:111], v[116:119]\n"
"ds_read_b64_tr_b16 a[214:215], v200 offset:32480\n"
"buffer_load_dwordx4 v[52:55], v207, s[28:31], s21 offen offset:3072 sc1\n"
"v_mfma_f32_16x16x32_bf16 v[120:123], a[144:147], a[104:107], v[120:123]\n"
"s_add_u32 s21, s21, 4096\n"
"ds_read_b64_tr_b16 a[216:217], v203 offset:27648\n"
"v_mfma_f32_16x16x32_bf16 v[124:127], a[144:147], a[108:111], v[124:127]\n"
"ds_read_b64_tr_b16 a[218:219], v203 offset:32256\n"
"v_mfma_f32_16x16x32_bf16 v[226:229], a[148:151], a[104:107], v[226:229]\n"
"ds_read_b64_tr_b16 a[220:221], v200 offset:36864\n"
"v_mfma_f32_16x16x32_bf16 v[230:233], a[148:151], a[108:111], v[230:233]\n"
"s_waitcnt vmcnt(25)\n"
"ds_read_b64_tr_b16 a[222:223], v200 offset:41472\n"
"v_mfma_f32_16x16x32_bf16 v[64:67], a[152:155], a[112:115], v[64:67]\n"
"ds_read_b64_tr_b16 a[128:129], v200 offset:36896\n"
"v_mfma_f32_16x16x32_bf16 v[68:71], a[152:155], a[116:119], v[68:71]\n"
"s_waitcnt lgkmcnt(11)\n"
"ds_read_b64_tr_b16 a[130:131], v200 offset:41504\n"
"v_mfma_f32_16x16x32_bf16 v[72:75], a[156:159], a[112:115], v[72:75]\n"
"ds_read_b64_tr_b16 a[132:133], v200 offset:36928\n"
"buffer_load_dwordx4 v[56:59], v207, s[28:31], s22 offen offset:2048 sc1\n"
"v_mfma_f32_16x16x32_bf16 v[76:79], a[156:159], a[116:119], v[76:79]\n"
"ds_read_b64_tr_b16 a[134:135], v200 offset:41536\n"
"v_mfma_f32_16x16x32_bf16 v[80:83], a[192:195], a[112:115], v[80:83]\n"
"ds_read_b64_tr_b16 a[136:137], v200 offset:36960\n"
"v_mfma_f32_16x16x32_bf16 v[84:87], a[192:195], a[116:119], v[84:87]\n"
"ds_read_b64_tr_b16 a[138:139], v200 offset:41568\n"
"v_mfma_f32_16x16x32_bf16 v[88:91], a[196:199], a[112:115], v[88:91]\n"
"ds_read_b64_tr_b16 a[140:141], v200 offset:36992\n"
"v_mfma_f32_16x16x32_bf16 v[92:95], a[196:199], a[116:119], v[92:95]\n"
"ds_read_b64_tr_b16 a[142:143], v200 offset:41600\n"
"v_mfma_f32_16x16x32_bf16 v[96:99], a[200:203], a[112:115], v[96:99]\n"
"ds_read_b64_tr_b16 a[144:145], v200 offset:37024\n"
"v_mfma_f32_16x16x32_bf16 v[100:103], a[200:203], a[116:119], v[100:103]\n"
"s_waitcnt lgkmcnt(11)\n"
"ds_read_b64_tr_b16 a[146:147], v200 offset:41632\n"
"buffer_load_dwordx4 v[60:63], v207, s[28:31], s22 offen offset:3072 sc1\n"
"v_mfma_f32_16x16x32_bf16 v[104:107], a[204:207], a[112:115], v[104:107]\n"
"s_add_u32 s22, s22, 4096\n"
"ds_read_b64_tr_b16 a[148:149], v200 offset:37056\n"
"v_mfma_f32_16x16x32_bf16 v[108:111], a[204:207], a[116:119], v[108:111]\n"
"ds_read_b64_tr_b16 a[150:151], v200 offset:41664\n"
"v_mfma_f32_16x16x32_bf16 v[112:115], a[208:211], a[112:115], v[112:115]\n"
"s_waitcnt vmcnt(8)\n"
"s_barrier\n"
"ds_read_b64_tr_b16 a[152:153], v200 offset:37088\n"
"v_mfma_f32_16x16x32_bf16 v[116:119], a[208:211], a[116:119], v[116:119]\n"
"ds_read_b64_tr_b16 a[154:155], v200 offset:41696\n"
"v_mfma_f32_16x16x32_bf16 v[120:123], a[212:215], a[112:115], v[120:123]\n"
"ds_read_b64_tr_b16 a[156:157], v203 offset:36864\n"
"v_mfma_f32_16x16x32_bf16 v[124:127], a[212:215], a[116:119], v[124:127]\n"
"ds_read_b64_tr_b16 a[158:159], v203 offset:41472\n"
"v_mfma_f32_16x16x32_bf16 v[226:229], a[216:219], a[112:115], v[226:229]\n"
"ds_read_b64_tr_b16 a[192:193], v203 offset:46080\n"
"v_mfma_f32_16x16x32_bf16 v[230:233], a[216:219], a[116:119], v[230:233]\n"
"s_waitcnt lgkmcnt(11)\n"
"s_waitcnt vmcnt(25)\n"
"ds_read_b64_tr_b16 a[194:195], v203 offset:50688\n"
"v_mfma_f32_16x16x32_bf16 v[64:67], a[220:223], a[120:123], v[64:67]\n"
"ds_read_b64_tr_b16 a[196:197], v203 offset:55296\n"
"v_mfma_f32_16x16x32_bf16 v[68:71], a[220:223], a[124:127], v[68:71]\n"
"ds_read_b64_tr_b16 a[198:199], v203 offset:59904\n"
"v_mfma_f32_16x16x32_bf16 v[72:75], a[128:131], a[120:123], v[72:75]\n"
"ds_read_b64_tr_b16 a[200:201], v203 offset:64512\n"
"v_mfma_f32_16x16x32_bf16 v[76:79], a[128:131], a[124:127], v[76:79]\n"
"ds_read_b64_tr_b16 a[202:203], v204 offset:3584\n"
"v_mfma_f32_16x16x32_bf16 v[80:83], a[132:135], a[120:123], v[80:83]\n"
"ds_read_b64_tr_b16 a[204:205], v204 offset:8192\n"
"v_mfma_f32_16x16x32_bf16 v[84:87], a[132:135], a[124:127], v[84:87]\n"
"ds_read_b64_tr_b16 a[206:207], v204 offset:12800\n"
"v_mfma_f32_16x16x32_bf16 v[88:91], a[136:139], a[120:123], v[88:91]\n"
"ds_read_b64_tr_b16 a[208:209], v204 offset:17408\n"
"v_mfma_f32_16x16x32_bf16 v[92:95], a[136:139], a[124:127], v[92:95]\n"
"s_waitcnt lgkmcnt(11)\n"
"ds_read_b64_tr_b16 a[210:211], v204 offset:22016\n"
"v_mfma_f32_16x16x32_bf16 v[96:99], a[140:143], a[120:123], v[96:99]\n"
"ds_read_b64_tr_b16 a[212:213], v204 offset:26624\n"
"v_mfma_f32_16x16x32_bf16 v[100:103], a[140:143], a[124:127], v[100:103]\n"
"ds_read_b64_tr_b16 a[214:215], v204 offset:31232\n"
"v_mfma_f32_16x16x32_bf16 v[104:107], a[144:147], a[120:123], v[104:107]\n"
"ds_read_b64_tr_b16 a[216:217], v204 offset:35840\n"
"v_mfma_f32_16x16x32_bf16 v[108:111], a[144:147], a[124:127], v[108:111]\n"
"ds_read_b64_tr_b16 a[218:219], v204 offset:40448\n"
"v_mfma_f32_16x16x32_bf16 v[112:115], a[148:151], a[120:123], v[112:115]\n"
"ds_read_b64_tr_b16 a[220:221], v204 offset:45056\n"
"v_mfma_f32_16x16x32_bf16 v[116:119], a[148:151], a[124:127], v[116:119]\n"
"ds_read_b64_tr_b16 a[222:223], v204 offset:49664\n"
"v_mfma_f32_16x16x32_bf16 v[120:123], a[152:155], a[120:123], v[120:123]\n"
"ds_read_b64_tr_b16 a[128:129], v200 offset:46080\n"
"v_mfma_f32_16x16x32_bf16 v[124:127], a[152:155], a[124:127], v[124:127]\n"
"s_waitcnt lgkmcnt(11)\n"
"s_mov_b32 m0, s45\n"
"ds_read_b64_tr_b16 a[130:131], v200 offset:50688\n"
"v_mfma_f32_16x16x32_bf16 v[226:229], a[156:159], a[120:123], v[226:229]\n"
"ds_read_b64_tr_b16 a[132:133], v200 offset:55296\n"
"buffer_load_dwordx4 v207, s[16:19], s46 offen offset:0 lds\n"
"v_mfma_f32_16x16x32_bf16 v[230:233], a[156:159], a[124:127], v[230:233]\n"
"s_waitcnt vmcnt(22)\n"
"ds_read_b64_tr_b16 a[134:135], v200 offset:59904\n"
"v_mfma_f32_16x16x32_bf16 v[226:229], a[192:195], v[0:3], v[226:229]\n"
"ds_read_b64_tr_b16 a[136:137], v200 offset:64512\n"
"v_mfma_f32_16x16x32_bf16 v[230:233], a[192:195], v[4:7], v[230:233]\n"
"s_waitcnt vmcnt(18)\n"
"ds_read_b64_tr_b16 a[138:139], v201 offset:3584\n"
"v_mfma_f32_16x16x32_bf16 v[226:229], a[196:199], v[8:11], v[226:229]\n"
"s_mov_b32 m0, s45\n"
"ds_read_b64_tr_b16 a[140:141], v201 offset:8192\n"
"v_accvgpr_write_b32 a48, v128\n"
"v_mfma_f32_16x16x32_bf16 v[230:233], a[196:199], v[12:15], v[230:233]\n"
"s_waitcnt vmcnt(14)\n"
"ds_read_b64_tr_b16 a[142:143], v201 offset:12800\n"
"buffer_load_dwordx4 v207, s[16:19], s46 offen offset:1024 lds\n"
"v_mfma_f32_16x16x32_bf16 v[226:229], a[200:203], v[16:19], v[226:229]\n"
"ds_read_b64_tr_b16 a[144:145], v201 offset:17408\n"
"v_accvgpr_write_b32 a49, v129\n"
"v_mfma_f32_16x16x32_bf16 v[230:233], a[200:203], v[20:23], v[230:233]\n"
"s_waitcnt lgkmcnt(11)\n"
"s_waitcnt vmcnt(11)\n"
"ds_read_b64_tr_b16 a[146:147], v201 offset:22016\n"
"v_accvgpr_write_b32 a50, v130\n"
"v_mfma_f32_16x16x32_bf16 v[226:229], a[204:207], v[24:27], v[226:229]\n"
"ds_read_b64_tr_b16 a[148:149], v201 offset:26624\n"
"v_accvgpr_write_b32 a51, v131\n"
"v_mfma_f32_16x16x32_bf16 v[230:233], a[204:207], v[28:31], v[230:233]\n"
"s_waitcnt vmcnt(8)\n"
"ds_read_b64_tr_b16 a[150:151], v201 offset:31232\n"
"v_accvgpr_write_b32 a52, v132\n"
"v_mfma_f32_16x16x32_bf16 v[226:229], a[208:211], v[32:35], v[226:229]\n"
"s_mov_b32 m0, s45\n"
"ds_read_b64_tr_b16 a[152:153], v201 offset:35840\n"
"v_accvgpr_write_b32 a53, v133\n"
"v_mfma_f32_16x16x32_bf16 v[230:233], a[208:211], v[36:39], v[230:233]\n"
"s_waitcnt vmcnt(6)\n"
"ds_read_b64_tr_b16 a[154:155], v201 offset:40448\n"
"buffer_load_dwordx4 v207, s[16:19], s46 offen offset:2048 lds\n"
"v_mfma_f32_16x16x32_bf16 v[226:229], a[212:215], v[40:43], v[226:229]\n"
"ds_read_b64_tr_b16 a[156:157], v201 offset:45056\n"
"v_accvgpr_write_b32 a54, v134\n"
"v_mfma_f32_16x16x32_bf16 v[230:233], a[212:215], v[44:47], v[230:233]\n"
"s_waitcnt vmcnt(5)\n"
"ds_read_b64_tr_b16 a[158:159], v201 offset:49664\n"
"v_accvgpr_write_b32 a55, v135\n"
"v_mfma_f32_16x16x32_bf16 v[226:229], a[216:219], v[48:51], v[226:229]\n"
"ds_read_b64_tr_b16 a[192:193], v200 offset:46112\n"
"v_accvgpr_write_b32 a56, v136\n"
"v_mfma_f32_16x16x32_bf16 v[230:233], a[216:219], v[52:55], v[230:233]\n"
"s_waitcnt lgkmcnt(11)\n"
"s_waitcnt vmcnt(3)\n"
"ds_read_b64_tr_b16 a[194:195], v200 offset:50720\n"
"v_accvgpr_write_b32 a57, v137\n"
"v_mfma_f32_16x16x32_bf16 v[226:229], a[220:223], v[56:59], v[226:229]\n"
"ds_read_b64_tr_b16 a[196:197], v200 offset:55328\n"
"v_accvgpr_write_b32 a58, v138\n"
"v_mfma_f32_16x16x32_bf16 v[230:233], a[220:223], v[60:63], v[230:233]\n"
"ds_read_b64_tr_b16 a[198:199], v200 offset:59936\n"
"v_accvgpr_write_b32 a59, v139\n"
"v_mfma_f32_16x16x32_bf16 v[64:67], a[128:131], v[0:3], v[64:67]\n"
"ds_read_b64_tr_b16 a[200:201], v200 offset:64544\n"
"v_accvgpr_write_b32 a60, v140\n"
"v_mfma_f32_16x16x32_bf16 v[68:71], a[128:131], v[4:7], v[68:71]\n"
"s_add_u32 m0, s84, 131072\n"
"ds_read_b64_tr_b16 a[202:203], v201 offset:3616\n"
"v_accvgpr_write_b32 a61, v141\n"
"v_mfma_f32_16x16x32_bf16 v[64:67], a[132:135], v[8:11], v[64:67]\n"
"ds_read_b64_tr_b16 a[204:205], v201 offset:8224\n"
"buffer_load_dwordx4 v207, s[72:75], s50 offen offset:0 lds\n"
"v_mfma_f32_16x16x32_bf16 v[68:71], a[132:135], v[12:15], v[68:71]\n"
"ds_read_b64_tr_b16 a[206:207], v201 offset:12832\n"
"v_cvt_pk_bf16_f32 v218, v226, v227\n"
"v_mfma_f32_16x16x32_bf16 v[64:67], a[136:139], v[16:19], v[64:67]\n"
"ds_read_b64_tr_b16 a[208:209], v201 offset:17440\n"
"v_cvt_pk_bf16_f32 v219, v228, v229\n"
"v_mfma_f32_16x16x32_bf16 v[68:71], a[136:139], v[20:23], v[68:71]\n"
"s_waitcnt lgkmcnt(11)\n"
"ds_read_b64_tr_b16 a[210:211], v201 offset:22048\n"
"v_cvt_pk_bf16_f32 v222, v230, v231\n"
"v_mfma_f32_16x16x32_bf16 v[64:67], a[140:143], v[24:27], v[64:67]\n"
"ds_read_b64_tr_b16 a[212:213], v201 offset:26656\n"
"v_cvt_pk_bf16_f32 v223, v232, v233\n"
"v_mfma_f32_16x16x32_bf16 v[68:71], a[140:143], v[28:31], v[68:71]\n"
"ds_read_b64_tr_b16 a[214:215], v201 offset:31264\n"
"v_accvgpr_write_b32 a62, v142\n"
"v_mfma_f32_16x16x32_bf16 v[64:67], a[144:147], v[32:35], v[64:67]\n"
"s_add_u32 m0, s84, 131072\n"
"ds_read_b64_tr_b16 a[216:217], v201 offset:35872\n"
"buffer_store_dwordx2 v[218:219], v207, s[28:31], s49 offen sc1\n"
"v_mfma_f32_16x16x32_bf16 v[68:71], a[144:147], v[36:39], v[68:71]\n"
"ds_read_b64_tr_b16 a[218:219], v201 offset:40480\n"
"buffer_load_dwordx4 v207, s[72:75], s50 offen offset:1024 lds\n"
"v_mfma_f32_16x16x32_bf16 v[64:67], a[148:151], v[40:43], v[64:67]\n"
"ds_read_b64_tr_b16 a[220:221], v201 offset:45088\n"
"buffer_store_dwordx2 v[222:223], v208, s[28:31], s49 offen sc1\n"
"v_mfma_f32_16x16x32_bf16 v[68:71], a[148:151], v[44:47], v[68:71]\n"
"ds_read_b64_tr_b16 a[222:223], v201 offset:49696\n"
"v_accvgpr_write_b32 a63, v143\n"
"v_mfma_f32_16x16x32_bf16 v[64:67], a[152:155], v[48:51], v[64:67]\n"
"ds_read_b64_tr_b16 a[128:129], v200 offset:46144\n"
"v_accvgpr_write_b32 a64, v144\n"
"v_mfma_f32_16x16x32_bf16 v[68:71], a[152:155], v[52:55], v[68:71]\n"
"s_waitcnt lgkmcnt(11)\n"
"ds_read_b64_tr_b16 a[130:131], v200 offset:50752\n"
"v_accvgpr_write_b32 a65, v145\n"
"v_mfma_f32_16x16x32_bf16 v[64:67], a[156:159], v[56:59], v[64:67]\n"
"s_add_u32 m0, s84, 131072\n"
"ds_read_b64_tr_b16 a[132:133], v200 offset:55360\n"
"v_accvgpr_write_b32 a66, v146\n"
"v_mfma_f32_16x16x32_bf16 v[68:71], a[156:159], v[60:63], v[68:71]\n"
"ds_read_b64_tr_b16 a[134:135], v200 offset:59968\n"
"buffer_load_dwordx4 v207, s[72:75], s50 offen offset:2048 lds\n"
"v_mfma_f32_16x16x32_bf16 v[72:75], a[192:195], v[0:3], v[72:75]\n"
"ds_read_b64_tr_b16 a[136:137], v200 offset:64576\n"
"v_accvgpr_write_b32 a67, v147\n"
"v_mfma_f32_16x16x32_bf16 v[76:79], a[192:195], v[4:7], v[76:79]\n"
"ds_read_b64_tr_b16 a[138:139], v201 offset:3648\n"
"v_accvgpr_write_b32 a68, v148\n"
"v_mfma_f32_16x16x32_bf16 v[72:75], a[196:199], v[8:11], v[72:75]\n"
"ds_read_b64_tr_b16 a[140:141], v201 offset:8256\n"
"v_accvgpr_write_b32 a69, v149\n"
"v_mfma_f32_16x16x32_bf16 v[76:79], a[196:199], v[12:15], v[76:79]\n"
"ds_read_b64_tr_b16 a[142:143], v201 offset:12864\n"
"v_cvt_pk_bf16_f32 v218, v64, v65\n"
"v_mfma_f32_16x16x32_bf16 v[72:75], a[200:203], v[16:19], v[72:75]\n"
"ds_read_b64_tr_b16 a[144:145], v201 offset:17472\n"
"v_cvt_pk_bf16_f32 v219, v66, v67\n"
"v_mfma_f32_16x16x32_bf16 v[76:79], a[200:203], v[20:23], v[76:79]\n"
"s_waitcnt lgkmcnt(11)\n"
"ds_read_b64_tr_b16 a[146:147], v201 offset:22080\n"
"v_cvt_pk_bf16_f32 v222, v68, v69\n"
"v_mfma_f32_16x16x32_bf16 v[72:75], a[204:207], v[24:27], v[72:75]\n"
"s_add_u32 m0, s84, 131072\n"
"ds_read_b64_tr_b16 a[148:149], v201 offset:26688\n"
"v_cvt_pk_bf16_f32 v223, v70, v71\n"
"v_mfma_f32_16x16x32_bf16 v[76:79], a[204:207], v[28:31], v[76:79]\n"
"ds_read_b64_tr_b16 a[150:151], v201 offset:31296\n"
"buffer_load_dwordx4 v207, s[72:75], s50 offen offset:3072 lds\n"
"v_mfma_f32_16x16x32_bf16 v[72:75], a[208:211], v[32:35], v[72:75]\n"
"ds_read_b64_tr_b16 a[152:153], v201 offset:35904\n"
"v_accvgpr_write_b32 a70, v150\n"
"v_mfma_f32_16x16x32_bf16 v[76:79], a[208:211], v[36:39], v[76:79]\n"
"ds_read_b64_tr_b16 a[154:155], v201 offset:40512\n"
"v_accvgpr_write_b32 a71, v151\n"
"v_mfma_f32_16x16x32_bf16 v[72:75], a[212:215], v[40:43], v[72:75]\n"
"ds_read_b64_tr_b16 a[156:157], v201 offset:45120\n"
"v_accvgpr_write_b32 a72, v152\n"
"v_mfma_f32_16x16x32_bf16 v[76:79], a[212:215], v[44:47], v[76:79]\n"
"ds_read_b64_tr_b16 a[158:159], v201 offset:49728\n"
"v_accvgpr_write_b32 a73, v153\n"
"v_mfma_f32_16x16x32_bf16 v[72:75], a[216:219], v[48:51], v[72:75]\n"
"s_add_u32 m0, s84, 135168\n"
"ds_read_b64_tr_b16 a[192:193], v200 offset:46176\n"
"v_accvgpr_write_b32 a74, v154\n"
"v_mfma_f32_16x16x32_bf16 v[76:79], a[216:219], v[52:55], v[76:79]\n"
"s_waitcnt lgkmcnt(11)\n"
"ds_read_b64_tr_b16 a[194:195], v200 offset:50784\n"
"buffer_load_dwordx4 v207, s[72:75], s86 offen offset:0 lds\n"
"v_mfma_f32_16x16x32_bf16 v[72:75], a[220:223], v[56:59], v[72:75]\n"
"ds_read_b64_tr_b16 a[196:197], v200 offset:55392\n"
"v_accvgpr_write_b32 a75, v155\n"
"v_mfma_f32_16x16x32_bf16 v[76:79], a[220:223], v[60:63], v[76:79]\n"
"ds_read_b64_tr_b16 a[198:199], v200 offset:60000\n"
"v_accvgpr_write_b32 a76, v156\n"
"v_mfma_f32_16x16x32_bf16 v[80:83], a[128:131], v[0:3], v[80:83]\n"
"ds_read_b64_tr_b16 a[200:201], v200 offset:64608\n"
"v_accvgpr_write_b32 a77, v157\n"
"v_mfma_f32_16x16x32_bf16 v[84:87], a[128:131], v[4:7], v[84:87]\n"
"ds_read_b64_tr_b16 a[202:203], v201 offset:3680\n"
"v_accvgpr_write_b32 a78, v158\n"
"v_mfma_f32_16x16x32_bf16 v[80:83], a[132:135], v[8:11], v[80:83]\n"
"ds_read_b64_tr_b16 a[204:205], v201 offset:8288\n"
"v_accvgpr_write_b32 a79, v159\n"
"v_mfma_f32_16x16x32_bf16 v[84:87], a[132:135], v[12:15], v[84:87]\n"
"ds_read_b64_tr_b16 a[206:207], v201 offset:12896\n"
"v_cvt_pk_bf16_f32 v220, v72, v73\n"
"v_mfma_f32_16x16x32_bf16 v[80:83], a[136:139], v[16:19], v[80:83]\n"
"ds_read_b64_tr_b16 a[208:209], v201 offset:17504\n"
"v_cvt_pk_bf16_f32 v221, v74, v75\n"
"v_mfma_f32_16x16x32_bf16 v[84:87], a[136:139], v[20:23], v[84:87]\n"
"s_waitcnt lgkmcnt(11)\n"
"ds_read_b64_tr_b16 a[210:211], v201 offset:22112\n"
"v_cvt_pk_bf16_f32 v224, v76, v77\n"
"v_mfma_f32_16x16x32_bf16 v[80:83], a[140:143], v[24:27], v[80:83]\n"
"s_add_u32 m0, s84, 135168\n"
"ds_read_b64_tr_b16 a[212:213], v201 offset:26720\n"
"v_cvt_pk_bf16_f32 v225, v78, v79\n"
"v_mfma_f32_16x16x32_bf16 v[84:87], a[140:143], v[28:31], v[84:87]\n"
"ds_read_b64_tr_b16 a[214:215], v201 offset:31328\n"
"buffer_load_dwordx4 v207, s[72:75], s86 offen offset:1024 lds\n"
"v_mfma_f32_16x16x32_bf16 v[80:83], a[144:147], v[32:35], v[80:83]\n"
"ds_read_b64_tr_b16 a[216:217], v201 offset:35936\n"
"buffer_store_dwordx4 v[218:221], v207, s[28:31], s54 offen offset:0 sc1\n"
"v_mfma_f32_16x16x32_bf16 v[84:87], a[144:147], v[36:39], v[84:87]\n"
"ds_read_b64_tr_b16 a[218:219], v201 offset:40544\n"
"v_accvgpr_write_b32 a80, v160\n"
"v_mfma_f32_16x16x32_bf16 v[80:83], a[148:151], v[40:43], v[80:83]\n"
"ds_read_b64_tr_b16 a[220:221], v201 offset:45152\n"
"buffer_store_dwordx4 v[222:225], v208, s[28:31], s54 offen offset:0 sc1\n"
"v_mfma_f32_16x16x32_bf16 v[84:87], a[148:151], v[44:47], v[84:87]\n"
"ds_read_b64_tr_b16 a[222:223], v201 offset:49760\n"
"v_accvgpr_write_b32 a81, v161\n"
"v_mfma_f32_16x16x32_bf16 v[80:83], a[152:155], v[48:51], v[80:83]\n"
"s_add_u32 m0, s84, 135168\n"
"ds_read_b64_tr_b16 a[128:129], v200 offset:46208\n"
"v_accvgpr_write_b32 a82, v162\n"
"v_mfma_f32_16x16x32_bf16 v[84:87], a[152:155], v[52:55], v[84:87]\n"
"s_waitcnt lgkmcnt(11)\n"
"ds_read_b64_tr_b16 a[130:131], v200 offset:50816\n"
"buffer_load_dwordx4 v207, s[72:75], s86 offen offset:2048 lds\n"
"v_mfma_f32_16x16x32_bf16 v[80:83], a[156:159], v[56:59], v[80:83]\n"
"ds_read_b64_tr_b16 a[132:133], v200 offset:55424\n"
"v_accvgpr_write_b32 a83, v163\n"
"v_mfma_f32_16x16x32_bf16 v[84:87], a[156:159], v[60:63], v[84:87]\n"
"ds_read_b64_tr_b16 a[134:135], v200 offset:60032\n"
"v_accvgpr_write_b32 a84, v164\n"
"v_mfma_f32_16x16x32_bf16 v[88:91], a[192:195], v[0:3], v[88:91]\n"
"ds_read_b64_tr_b16 a[136:137], v200 offset:64640\n"
"v_accvgpr_write_b32 a85, v165\n"
"v_mfma_f32_16x16x32_bf16 v[92:95], a[192:195], v[4:7], v[92:95]\n"
"ds_read_b64_tr_b16 a[138:139], v201 offset:3712\n"
"v_accvgpr_write_b32 a86, v166\n"
"v_mfma_f32_16x16x32_bf16 v[88:91], a[196:199], v[8:11], v[88:91]\n"
"ds_read_b64_tr_b16 a[140:141], v201 offset:8320\n"
"v_accvgpr_write_b32 a87, v167\n"
"v_mfma_f32_16x16x32_bf16 v[92:95], a[196:199], v[12:15], v[92:95]\n"
"ds_read_b64_tr_b16 a[142:143], v201 offset:12928\n"
"v_cvt_pk_bf16_f32 v218, v80, v81\n"
"v_mfma_f32_16x16x32_bf16 v[88:91], a[200:203], v[16:19], v[88:91]\n"
"ds_read_b64_tr_b16 a[144:145], v201 offset:17536\n"
"v_cvt_pk_bf16_f32 v219, v82, v83\n"
"v_mfma_f32_16x16x32_bf16 v[92:95], a[200:203], v[20:23], v[92:95]\n"
"s_waitcnt lgkmcnt(11)\n"
"ds_read_b64_tr_b16 a[146:147], v201 offset:22144\n"
"v_cvt_pk_bf16_f32 v222, v84, v85\n"
"v_mfma_f32_16x16x32_bf16 v[88:91], a[204:207], v[24:27], v[88:91]\n"
"ds_read_b64_tr_b16 a[148:149], v201 offset:26752\n"
"v_cvt_pk_bf16_f32 v223, v86, v87\n"
"v_mfma_f32_16x16x32_bf16 v[92:95], a[204:207], v[28:31], v[92:95]\n"
"ds_read_b64_tr_b16 a[150:151], v201 offset:31360\n"
"v_accvgpr_write_b32 a88, v168\n"
"v_mfma_f32_16x16x32_bf16 v[88:91], a[208:211], v[32:35], v[88:91]\n"
"ds_read_b64_tr_b16 a[152:153], v201 offset:35968\n"
"v_accvgpr_write_b32 a89, v169\n"
"v_mfma_f32_16x16x32_bf16 v[92:95], a[208:211], v[36:39], v[92:95]\n"
"ds_read_b64_tr_b16 a[154:155], v201 offset:40576\n"
"v_accvgpr_write_b32 a90, v170\n"
"v_mfma_f32_16x16x32_bf16 v[88:91], a[212:215], v[40:43], v[88:91]\n"
"ds_read_b64_tr_b16 a[156:157], v201 offset:45184\n"
"v_accvgpr_write_b32 a91, v171\n"
"v_mfma_f32_16x16x32_bf16 v[92:95], a[212:215], v[44:47], v[92:95]\n"
"ds_read_b64_tr_b16 a[158:159], v201 offset:49792\n"
"v_accvgpr_write_b32 a92, v172\n"
"v_mfma_f32_16x16x32_bf16 v[88:91], a[216:219], v[48:51], v[88:91]\n"
"ds_read_b64_tr_b16 a[192:193], v200 offset:46240\n"
"v_accvgpr_write_b32 a93, v173\n"
"v_mfma_f32_16x16x32_bf16 v[92:95], a[216:219], v[52:55], v[92:95]\n"
"s_waitcnt lgkmcnt(11)\n"
"ds_read_b64_tr_b16 a[194:195], v200 offset:50848\n"
"v_accvgpr_write_b32 a94, v174\n"
"v_mfma_f32_16x16x32_bf16 v[88:91], a[220:223], v[56:59], v[88:91]\n"
"ds_read_b64_tr_b16 a[196:197], v200 offset:55456\n"
"v_accvgpr_write_b32 a95, v175\n"
"v_mfma_f32_16x16x32_bf16 v[92:95], a[220:223], v[60:63], v[92:95]\n"
"ds_read_b64_tr_b16 a[198:199], v200 offset:60064\n"
"v_accvgpr_write_b32 a96, v176\n"
"v_mfma_f32_16x16x32_bf16 v[96:99], a[128:131], v[0:3], v[96:99]\n"
"ds_read_b64_tr_b16 a[200:201], v200 offset:64672\n"
"v_accvgpr_write_b32 a97, v177\n"
"v_mfma_f32_16x16x32_bf16 v[100:103], a[128:131], v[4:7], v[100:103]\n"
"ds_read_b64_tr_b16 a[202:203], v201 offset:3744\n"
"v_accvgpr_write_b32 a98, v178\n"
"v_mfma_f32_16x16x32_bf16 v[96:99], a[132:135], v[8:11], v[96:99]\n"
"ds_read_b64_tr_b16 a[204:205], v201 offset:8352\n"
"v_accvgpr_write_b32 a99, v179\n"
"v_mfma_f32_16x16x32_bf16 v[100:103], a[132:135], v[12:15], v[100:103]\n"
"ds_read_b64_tr_b16 a[206:207], v201 offset:12960\n"
"v_cvt_pk_bf16_f32 v220, v88, v89\n"
"v_mfma_f32_16x16x32_bf16 v[96:99], a[136:139], v[16:19], v[96:99]\n"
"ds_read_b64_tr_b16 a[208:209], v201 offset:17568\n"
"v_cvt_pk_bf16_f32 v221, v90, v91\n"
"v_mfma_f32_16x16x32_bf16 v[100:103], a[136:139], v[20:23], v[100:103]\n"
"s_waitcnt lgkmcnt(11)\n"
"ds_read_b64_tr_b16 a[210:211], v201 offset:22176\n"
"v_cvt_pk_bf16_f32 v224, v92, v93\n"
"v_mfma_f32_16x16x32_bf16 v[96:99], a[140:143], v[24:27], v[96:99]\n"
"ds_read_b64_tr_b16 a[212:213], v201 offset:26784\n"
"v_cvt_pk_bf16_f32 v225, v94, v95\n"
"v_mfma_f32_16x16x32_bf16 v[100:103], a[140:143], v[28:31], v[100:103]\n"
"ds_read_b64_tr_b16 a[214:215], v201 offset:31392\n"
"v_accvgpr_write_b32 a100, v180\n"
"v_mfma_f32_16x16x32_bf16 v[96:99], a[144:147], v[32:35], v[96:99]\n"
"ds_read_b64_tr_b16 a[216:217], v201 offset:36000\n"
"buffer_store_dwordx4 v[218:221], v207, s[28:31], s54 offen offset:1024 sc1\n"
"v_mfma_f32_16x16x32_bf16 v[100:103], a[144:147], v[36:39], v[100:103]\n"
"ds_read_b64_tr_b16 a[218:219], v201 offset:40608\n"
"v_accvgpr_write_b32 a101, v181\n"
"v_mfma_f32_16x16x32_bf16 v[96:99], a[148:151], v[40:43], v[96:99]\n"
"ds_read_b64_tr_b16 a[220:221], v201 offset:45216\n"
"buffer_store_dwordx4 v[222:225], v208, s[28:31], s54 offen offset:1024 sc1\n"
"v_mfma_f32_16x16x32_bf16 v[100:103], a[148:151], v[44:47], v[100:103]\n"
"ds_read_b64_tr_b16 a[222:223], v201 offset:49824\n"
"v_accvgpr_write_b32 a102, v182\n"
"v_mfma_f32_16x16x32_bf16 v[96:99], a[152:155], v[48:51], v[96:99]\n"
"ds_read_b64_tr_b16 a[128:129], v200 offset:46272\n"
"v_accvgpr_write_b32 a103, v183\n"
"v_mfma_f32_16x16x32_bf16 v[100:103], a[152:155], v[52:55], v[100:103]\n"
"s_waitcnt lgkmcnt(11)\n"
"ds_read_b64_tr_b16 a[130:131], v200 offset:50880\n"
"v_accvgpr_write_b32 a104, v184\n"
"v_mfma_f32_16x16x32_bf16 v[96:99], a[156:159], v[56:59], v[96:99]\n"
"ds_read_b64_tr_b16 a[132:133], v200 offset:55488\n"
"v_accvgpr_write_b32 a105, v185\n"
"v_mfma_f32_16x16x32_bf16 v[100:103], a[156:159], v[60:63], v[100:103]\n"
"ds_read_b64_tr_b16 a[134:135], v200 offset:60096\n"
"v_accvgpr_write_b32 a106, v186\n"
"v_mfma_f32_16x16x32_bf16 v[104:107], a[192:195], v[0:3], v[104:107]\n"
"ds_read_b64_tr_b16 a[136:137], v200 offset:64704\n"
"v_accvgpr_write_b32 a107, v187\n"
"v_mfma_f32_16x16x32_bf16 v[108:111], a[192:195], v[4:7], v[108:111]\n"
"ds_read_b64_tr_b16 a[138:139], v201 offset:3776\n"
"v_accvgpr_write_b32 a108, v188\n"
"v_mfma_f32_16x16x32_bf16 v[104:107], a[196:199], v[8:11], v[104:107]\n"
"ds_read_b64_tr_b16 a[140:141], v201 offset:8384\n"
"v_accvgpr_write_b32 a109, v189\n"
"v_mfma_f32_16x16x32_bf16 v[108:111], a[196:199], v[12:15], v[108:111]\n"
"ds_read_b64_tr_b16 a[142:143], v201 offset:12992\n"
"v_cvt_pk_bf16_f32 v218, v96, v97\n"
"v_mfma_f32_16x16x32_bf16 v[104:107], a[200:203], v[16:19], v[104:107]\n"
"ds_read_b64_tr_b16 a[144:145], v201 offset:17600\n"
"v_cvt_pk_bf16_f32 v219, v98, v99\n"
"v_mfma_f32_16x16x32_bf16 v[108:111], a[200:203], v[20:23], v[108:111]\n"
"s_waitcnt lgkmcnt(11)\n"
"ds_read_b64_tr_b16 a[146:147], v201 offset:22208\n"
"v_cvt_pk_bf16_f32 v222, v100, v101\n"
"v_mfma_f32_16x16x32_bf16 v[104:107], a[204:207], v[24:27], v[104:107]\n"
"ds_read_b64_tr_b16 a[148:149], v201 offset:26816\n"
"v_cvt_pk_bf16_f32 v223, v102, v103\n"
"v_mfma_f32_16x16x32_bf16 v[108:111], a[204:207], v[28:31], v[108:111]\n"
"ds_read_b64_tr_b16 a[150:151], v201 offset:31424\n"
"v_accvgpr_write_b32 a110, v190\n"
"v_mfma_f32_16x16x32_bf16 v[104:107], a[208:211], v[32:35], v[104:107]\n"
"s_waitcnt vmcnt(13)\n"
"s_barrier\n"
"ds_read_b64_tr_b16 a[152:153], v201 offset:36032\n"
"v_accvgpr_write_b32 a111, v191\n"
"v_mfma_f32_16x16x32_bf16 v[108:111], a[208:211], v[36:39], v[108:111]\n"
"ds_read_b64_tr_b16 a[154:155], v201 offset:40640\n"
"v_accvgpr_write_b32 a112, v192\n"
"v_mfma_f32_16x16x32_bf16 v[104:107], a[212:215], v[40:43], v[104:107]\n"
"ds_read_b64_tr_b16 a[156:157], v201 offset:45248\n"
"v_accvgpr_write_b32 a113, v193\n"
"v_mfma_f32_16x16x32_bf16 v[108:111], a[212:215], v[44:47], v[108:111]\n"
"ds_read_b64_tr_b16 a[158:159], v201 offset:49856\n"
"v_accvgpr_write_b32 a114, v194\n"
"v_mfma_f32_16x16x32_bf16 v[104:107], a[216:219], v[48:51], v[104:107]\n"
"ds_read_b64_tr_b16 a[192:193], v200 offset:46304\n"
"v_accvgpr_write_b32 a115, v195\n"
"v_mfma_f32_16x16x32_bf16 v[108:111], a[216:219], v[52:55], v[108:111]\n"
"s_waitcnt lgkmcnt(11)\n"
"ds_read_b64_tr_b16 a[194:195], v200 offset:50912\n"
"v_accvgpr_write_b32 a116, v196\n"
"v_mfma_f32_16x16x32_bf16 v[104:107], a[220:223], v[56:59], v[104:107]\n"
"ds_read_b64_tr_b16 a[196:197], v200 offset:55520\n"
"v_accvgpr_write_b32 a117, v197\n"
"v_mfma_f32_16x16x32_bf16 v[108:111], a[220:223], v[60:63], v[108:111]\n"
"ds_read_b64_tr_b16 a[198:199], v200 offset:60128\n"
"v_accvgpr_write_b32 a118, v198\n"
"v_mfma_f32_16x16x32_bf16 v[112:115], a[128:131], v[0:3], v[112:115]\n"
"ds_read_b64_tr_b16 a[200:201], v200 offset:64736\n"
"v_accvgpr_write_b32 a119, v199\n"
"v_mfma_f32_16x16x32_bf16 v[116:119], a[128:131], v[4:7], v[116:119]\n"
"ds_read_b64_tr_b16 a[202:203], v201 offset:3808\n"
"v_mfma_f32_16x16x32_bf16 v[112:115], a[132:135], v[8:11], v[112:115]\n"
"ds_read_b64_tr_b16 a[204:205], v201 offset:8416\n"
"v_mfma_f32_16x16x32_bf16 v[116:119], a[132:135], v[12:15], v[116:119]\n"
"ds_read_b64_tr_b16 a[206:207], v201 offset:13024\n"
"v_cvt_pk_bf16_f32 v220, v104, v105\n"
"v_mfma_f32_16x16x32_bf16 v[112:115], a[136:139], v[16:19], v[112:115]\n"
"ds_read_b64_tr_b16 a[208:209], v201 offset:17632\n"
"v_cvt_pk_bf16_f32 v221, v106, v107\n"
"v_mfma_f32_16x16x32_bf16 v[116:119], a[136:139], v[20:23], v[116:119]\n"
"s_waitcnt lgkmcnt(11)\n"
"ds_read_b64_tr_b16 a[210:211], v201 offset:22240\n"
"v_cvt_pk_bf16_f32 v224, v108, v109\n"
"v_mfma_f32_16x16x32_bf16 v[112:115], a[140:143], v[24:27], v[112:115]\n"
"ds_read_b64_tr_b16 a[212:213], v201 offset:26848\n"
"v_cvt_pk_bf16_f32 v225, v110, v111\n"
"v_mfma_f32_16x16x32_bf16 v[116:119], a[140:143], v[28:31], v[116:119]\n"
"ds_read_b64_tr_b16 a[214:215], v201 offset:31456\n"
"v_mfma_f32_16x16x32_bf16 v[112:115], a[144:147], v[32:35], v[112:115]\n"
"ds_read_b64_tr_b16 a[216:217], v201 offset:36064\n"
"buffer_store_dwordx4 v[218:221], v207, s[28:31], s54 offen offset:2048 sc1\n"
"v_mfma_f32_16x16x32_bf16 v[116:119], a[144:147], v[36:39], v[116:119]\n"
"ds_read_b64_tr_b16 a[218:219], v201 offset:40672\n"
"buffer_store_dword v252, v251, s[28:31], 0 offen sc1\n"
"v_mfma_f32_16x16x32_bf16 v[112:115], a[148:151], v[40:43], v[112:115]\n"
"ds_read_b64_tr_b16 a[220:221], v201 offset:45280\n"
"buffer_store_dwordx4 v[222:225], v208, s[28:31], s54 offen offset:2048 sc1\n"
"v_mfma_f32_16x16x32_bf16 v[116:119], a[148:151], v[44:47], v[116:119]\n"
"ds_read_b64_tr_b16 a[222:223], v201 offset:49888\n"
"v_mfma_f32_16x16x32_bf16 v[112:115], a[152:155], v[48:51], v[112:115]\n"
"s_nop 0\n"
"v_mfma_f32_16x16x32_bf16 v[116:119], a[152:155], v[52:55], v[116:119]\n"
"s_waitcnt lgkmcnt(10)\n"
"v_mfma_f32_16x16x32_bf16 v[112:115], a[156:159], v[56:59], v[112:115]\n"
"s_nop 0\n"
"v_mfma_f32_16x16x32_bf16 v[116:119], a[156:159], v[60:63], v[116:119]\n"
"s_nop 0\n"
"v_mfma_f32_16x16x32_bf16 v[120:123], a[192:195], v[0:3], v[120:123]\n"
"s_nop 0\n"
"v_mfma_f32_16x16x32_bf16 v[124:127], a[192:195], v[4:7], v[124:127]\n"
"s_nop 0\n"
"v_mfma_f32_16x16x32_bf16 v[120:123], a[196:199], v[8:11], v[120:123]\n"
"s_nop 0\n"
"v_mfma_f32_16x16x32_bf16 v[124:127], a[196:199], v[12:15], v[124:127]\n"
"v_cvt_pk_bf16_f32 v218, v112, v113\n"
"v_mfma_f32_16x16x32_bf16 v[120:123], a[200:203], v[16:19], v[120:123]\n"
"v_cvt_pk_bf16_f32 v219, v114, v115\n"
"v_mfma_f32_16x16x32_bf16 v[124:127], a[200:203], v[20:23], v[124:127]\n"
"s_waitcnt lgkmcnt(2)\n"
"v_cvt_pk_bf16_f32 v222, v116, v117\n"
"v_mfma_f32_16x16x32_bf16 v[120:123], a[204:207], v[24:27], v[120:123]\n"
"v_cvt_pk_bf16_f32 v223, v118, v119\n"
"v_mfma_f32_16x16x32_bf16 v[124:127], a[204:207], v[28:31], v[124:127]\n"
"s_nop 0\n"
"v_mfma_f32_16x16x32_bf16 v[120:123], a[208:211], v[32:35], v[120:123]\n"
"s_nop 0\n"
"v_mfma_f32_16x16x32_bf16 v[124:127], a[208:211], v[36:39], v[124:127]\n"
"s_nop 0\n"
"v_mfma_f32_16x16x32_bf16 v[120:123], a[212:215], v[40:43], v[120:123]\n"
"s_nop 0\n"
"v_mfma_f32_16x16x32_bf16 v[124:127], a[212:215], v[44:47], v[124:127]\n"
"s_nop 0\n"
"v_mfma_f32_16x16x32_bf16 v[120:123], a[216:219], v[48:51], v[120:123]\n"
"s_nop 0\n"
"v_mfma_f32_16x16x32_bf16 v[124:127], a[216:219], v[52:55], v[124:127]\n"
"s_waitcnt lgkmcnt(0)\n"
"v_mfma_f32_16x16x32_bf16 v[120:123], a[220:223], v[56:59], v[120:123]\n"
"s_nop 0\n"
"v_mfma_f32_16x16x32_bf16 v[124:127], a[220:223], v[60:63], v[124:127]\n"
"s_nop 0\n"
"s_nop 1\n"
"s_nop 1\n"
"s_nop 1\n"
"s_nop 1\n"
"v_cvt_pk_bf16_f32 v220, v120, v121\n"
"s_nop 1\n"
"v_cvt_pk_bf16_f32 v221, v122, v123\n"
"s_nop 1\n"
"v_cvt_pk_bf16_f32 v224, v124, v125\n"
"s_nop 1\n"
"v_cvt_pk_bf16_f32 v225, v126, v127\n"
"s_nop 1\n"
"s_nop 1\n"
"buffer_store_dwordx4 v[218:221], v207, s[28:31], s54 offen offset:3072 sc1\n"
"s_nop 1\n"
"s_nop 1\n"
"buffer_store_dwordx4 v[222:225], v208, s[28:31], s54 offen offset:3072 sc1\n"
"s_nop 1\n"
"s_add_u32 s56, s40, 161024\n"
"s_nop 1\n"
"v_mov_b32 v227, s56\n"
"v_mov_b32 v224, 0x3f80\n"
"v_mov_b32 v225, 0x3f800000\n"
"v_and_b32 v226, 7, v248\n"
"v_lshl_add_u32 v226, v226, 4, v227\n"
"s_mov_b32 s14, 0xfffff800\n"
"s_cmp_lg_u32 s41, 3\n"
"s_cbranch_scc1 AT_A0\n"
"s_mov_b64 s[8:9], 0\n"
"v_mov_b32 v224, 0xffff0000\n"
"v_accvgpr_read_b32 v1, a48\n"
"v_accvgpr_read_b32 v3, a49\n"
"v_accvgpr_read_b32 v5, a50\n"
"v_accvgpr_read_b32 v7, a51\n"
"v_lshlrev_b32 v0, 16, v1\n"
"v_and_b32 v1, v224, v1\n"
"v_lshlrev_b32 v2, 16, v3\n"
"v_and_b32 v3, v224, v3\n"
"v_lshlrev_b32 v4, 16, v5\n"
"v_and_b32 v5, v224, v5\n"
"v_lshlrev_b32 v6, 16, v7\n"
"v_and_b32 v7, v224, v7\n"
"v_accvgpr_read_b32 v9, a84\n"
"v_accvgpr_read_b32 v11, a85\n"
"v_accvgpr_read_b32 v13, a86\n"
"v_accvgpr_read_b32 v15, a87\n"
"v_lshlrev_b32 v8, 16, v9\n"
"v_and_b32 v9, v224, v9\n"
"v_lshlrev_b32 v10, 16, v11\n"
"v_and_b32 v11, v224, v11\n"
"v_lshlrev_b32 v12, 16, v13\n"
"v_and_b32 v13, v224, v13\n"
"v_lshlrev_b32 v14, 16, v15\n"
"v_and_b32 v15, v224, v15\n"
"v_accvgpr_read_b32 v17, a52\n"
"v_accvgpr_read_b32 v19, a53\n"
"v_accvgpr_read_b32 v21, a54\n"
"v_accvgpr_read_b32 v23, a55\n"
"v_lshlrev_b32 v16, 16, v17\n"
"v_and_b32 v17, v224, v17\n"
"v_lshlrev_b32 v18, 16, v19\n"
"v_and_b32 v19, v224, v19\n"
"v_lshlrev_b32 v20, 16, v21\n"
"v_and_b32 v21, v224, v21\n"
"v_lshlrev_b32 v22, 16, v23\n"
"v_and_b32 v23, v224, v23\n"
"v_accvgpr_read_b32 v25, a88\n"
"v_accvgpr_read_b32 v27, a89\n"
"v_accvgpr_read_b32 v29, a90\n"
"v_accvgpr_read_b32 v31, a91\n"
"v_lshlrev_b32 v24, 16, v25\n"
"v_and_b32 v25, v224, v25\n"
"v_lshlrev_b32 v26, 16, v27\n"
"v_and_b32 v27, v224, v27\n"
"v_lshlrev_b32 v28, 16, v29\n"
"v_and_b32 v29, v224, v29\n"
"v_lshlrev_b32 v30, 16, v31\n"
"v_and_b32 v31, v224, v31\n"
"v_accvgpr_read_b32 v33, a56\n"
"v_accvgpr_read_b32 v35, a57\n"
"v_accvgpr_read_b32 v37, a58\n"
"v_accvgpr_read_b32 v39, a59\n"
"v_lshlrev_b32 v32, 16, v33\n"
"v_and_b32 v33, v224, v33\n"
"v_lshlrev_b32 v34, 16, v35\n"
"v_and_b32 v35, v224, v35\n"
"v_lshlrev_b32 v36, 16, v37\n"
"v_and_b32 v37, v224, v37\n"
"v_lshlrev_b32 v38, 16, v39\n"
"v_and_b32 v39, v224, v39\n"
"v_accvgpr_read_b32 v41, a92\n"
"v_accvgpr_read_b32 v43, a93\n"
"v_accvgpr_read_b32 v45, a94\n"
"v_accvgpr_read_b32 v47, a95\n"
"v_lshlrev_b32 v40, 16, v41\n"
"v_and_b32 v41, v224, v41\n"
"v_lshlrev_b32 v42, 16, v43\n"
"v_and_b32 v43, v224, v43\n"
"v_lshlrev_b32 v44, 16, v45\n"
"v_and_b32 v45, v224, v45\n"
"v_lshlrev_b32 v46, 16, v47\n"
"v_and_b32 v47, v224, v47\n"
"v_accvgpr_read_b32 v49, a60\n"
"v_accvgpr_read_b32 v51, a61\n"
"v_accvgpr_read_b32 v53, a62\n"
"v_accvgpr_read_b32 v55, a63\n"
"v_lshlrev_b32 v48, 16, v49\n"
"v_and_b32 v49, v224, v49\n"
"v_lshlrev_b32 v50, 16, v51\n"
"v_and_b32 v51, v224, v51\n"
"v_lshlrev_b32 v52, 16, v53\n"
"v_and_b32 v53, v224, v53\n"
"v_lshlrev_b32 v54, 16, v55\n"
"v_and_b32 v55, v224, v55\n"
"v_accvgpr_read_b32 v57, a96\n"
"v_accvgpr_read_b32 v59, a97\n"
"v_accvgpr_read_b32 v61, a98\n"
"v_accvgpr_read_b32 v63, a99\n"
"v_lshlrev_b32 v56, 16, v57\n"
"v_and_b32 v57, v224, v57\n"
"v_lshlrev_b32 v58, 16, v59\n"
"v_and_b32 v59, v224, v59\n"
"v_lshlrev_b32 v60, 16, v61\n"
"v_and_b32 v61, v224, v61\n"
"v_lshlrev_b32 v62, 16, v63\n"
"v_and_b32 v63, v224, v63\n"
"v_accvgpr_read_b32 v65, a64\n"
"v_accvgpr_read_b32 v67, a65\n"
"v_accvgpr_read_b32 v69, a66\n"
"v_accvgpr_read_b32 v71, a67\n"
"v_lshlrev_b32 v64, 16, v65\n"
"v_and_b32 v65, v224, v65\n"
"v_lshlrev_b32 v66, 16, v67\n"
"v_and_b32 v67, v224, v67\n"
"v_lshlrev_b32 v68, 16, v69\n"
"v_and_b32 v69, v224, v69\n"
"v_lshlrev_b32 v70, 16, v71\n"
"v_and_b32 v71, v224, v71\n"
"v_accvgpr_read_b32 v73, a100\n"
"v_accvgpr_read_b32 v75, a101\n"
"v_accvgpr_read_b32 v77, a102\n"
"v_accvgpr_read_b32 v79, a103\n"
"v_lshlrev_b32 v72, 16, v73\n"
"v_and_b32 v73, v224, v73\n"
"v_lshlrev_b32 v74, 16, v75\n"
"v_and_b32 v75, v224, v75\n"
"v_lshlrev_b32 v76, 16, v77\n"
"v_and_b32 v77, v224, v77\n"
"v_lshlrev_b32 v78, 16, v79\n"
"v_and_b32 v79, v224, v79\n"
"v_accvgpr_read_b32 v81, a68\n"
"v_accvgpr_read_b32 v83, a69\n"
"v_accvgpr_read_b32 v85, a70\n"
"v_accvgpr_read_b32 v87, a71\n"
"v_lshlrev_b32 v80, 16, v81\n"
"v_and_b32 v81, v224, v81\n"
"v_lshlrev_b32 v82, 16, v83\n"
"v_and_b32 v83, v224, v83\n"
"v_lshlrev_b32 v84, 16, v85\n"
"v_and_b32 v85, v224, v85\n"
"v_lshlrev_b32 v86, 16, v87\n"
"v_and_b32 v87, v224, v87\n"
"v_accvgpr_read_b32 v89, a104\n"
"v_accvgpr_read_b32 v91, a105\n"
"v_accvgpr_read_b32 v93, a106\n"
"v_accvgpr_read_b32 v95, a107\n"
"v_lshlrev_b32 v88, 16, v89\n"
"v_and_b32 v89, v224, v89\n"
"v_lshlrev_b32 v90, 16, v91\n"
"v_and_b32 v91, v224, v91\n"
"v_lshlrev_b32 v92, 16, v93\n"
"v_and_b32 v93, v224, v93\n"
"v_lshlrev_b32 v94, 16, v95\n"
"v_and_b32 v95, v224, v95\n"
"v_accvgpr_read_b32 v97, a72\n"
"v_accvgpr_read_b32 v99, a73\n"
"v_accvgpr_read_b32 v101, a74\n"
"v_accvgpr_read_b32 v103, a75\n"
"v_lshlrev_b32 v96, 16, v97\n"
"v_and_b32 v97, v224, v97\n"
"v_lshlrev_b32 v98, 16, v99\n"
"v_and_b32 v99, v224, v99\n"
"v_lshlrev_b32 v100, 16, v101\n"
"v_and_b32 v101, v224, v101\n"
"v_lshlrev_b32 v102, 16, v103\n"
"v_and_b32 v103, v224, v103\n"
"v_accvgpr_read_b32 v105, a108\n"
"v_accvgpr_read_b32 v107, a109\n"
"v_accvgpr_read_b32 v109, a110\n"
"v_accvgpr_read_b32 v111, a111\n"
"v_lshlrev_b32 v104, 16, v105\n"
"v_and_b32 v105, v224, v105\n"
"v_lshlrev_b32 v106, 16, v107\n"
"v_and_b32 v107, v224, v107\n"
"v_lshlrev_b32 v108, 16, v109\n"
"v_and_b32 v109, v224, v109\n"
"v_lshlrev_b32 v110, 16, v111\n"
"v_and_b32 v111, v224, v111\n"
"v_accvgpr_read_b32 v113, a76\n"
"v_accvgpr_read_b32 v115, a77\n"
"v_accvgpr_read_b32 v117, a78\n"
"v_accvgpr_read_b32 v119, a79\n"
"v_lshlrev_b32 v112, 16, v113\n"
"v_and_b32 v113, v224, v113\n"
"v_lshlrev_b32 v114, 16, v115\n"
"v_and_b32 v115, v224, v115\n"
"v_lshlrev_b32 v116, 16, v117\n"
"v_and_b32 v117, v224, v117\n"
"v_lshlrev_b32 v118, 16, v119\n"
"v_and_b32 v119, v224, v119\n"
"v_accvgpr_read_b32 v121, a112\n"
"v_accvgpr_read_b32 v123, a113\n"
"v_accvgpr_read_b32 v125, a114\n"
"v_accvgpr_read_b32 v127, a115\n"
"v_lshlrev_b32 v120, 16, v121\n"
"v_and_b32 v121, v224, v121\n"
"v_lshlrev_b32 v122, 16, v123\n"
"v_and_b32 v123, v224, v123\n"
"v_lshlrev_b32 v124, 16, v125\n"
"v_and_b32 v125, v224, v125\n"
"v_lshlrev_b32 v126, 16, v127\n"
"v_and_b32 v127, v224, v127\n"
"v_accvgpr_read_b32 v129, a80\n"
"v_accvgpr_read_b32 v131, a81\n"
"v_accvgpr_read_b32 v133, a82\n"
"v_accvgpr_read_b32 v135, a83\n"
"v_lshlrev_b32 v128, 16, v129\n"
"v_and_b32 v129, v224, v129\n"
"v_lshlrev_b32 v130, 16, v131\n"
"v_and_b32 v131, v224, v131\n"
"v_lshlrev_b32 v132, 16, v133\n"
"v_and_b32 v133, v224, v133\n"
"v_lshlrev_b32 v134, 16, v135\n"
"v_and_b32 v135, v224, v135\n"
"v_accvgpr_read_b32 v137, a116\n"
"v_accvgpr_read_b32 v139, a117\n"
"v_accvgpr_read_b32 v141, a118\n"
"v_accvgpr_read_b32 v143, a119\n"
"v_lshlrev_b32 v136, 16, v137\n"
"v_and_b32 v137, v224, v137\n"
"v_lshlrev_b32 v138, 16, v139\n"
"v_and_b32 v139, v224, v139\n"
"v_lshlrev_b32 v140, 16, v141\n"
"v_and_b32 v141, v224, v141\n"
"v_lshlrev_b32 v142, 16, v143\n"
"v_and_b32 v143, v224, v143\n"
"v_lshrrev_b32 v242, 2, v248\n"
"v_lshlrev_b32 v241, 4, v242\n"
"v_lshlrev_b32 v242, 3, v242\n"
"v_lshrrev_b32 v243, 6, v254\n"
"v_mul_u32_u24 v243, 24576, v243\n"
"v_and_b32 v247, 7, v248\n"
"v_lshl_add_u32 v243, v247, 4, v243\n"
"v_and_b32 v247, 3, v248\n"
"v_and_b32 v244, 0x10000000, v254\n"
"v_or_b32 v243, v244, v243\n"
"v_lshrrev_b32 v244, 5, v248\n"
"v_mul_u32_u24 v244, 96, v244\n"
"v_and_b32 v246, 16, v248\n"
"v_subrev_u32 v252, 65536, v227\n"
"v_add3_u32 v244, v244, v246, v252\n"
"v_lshl_add_u32 v244, v247, 5, v244\n"
"s_mul_i32 s63, s41, 384\n"
"v_add_u32 v244, s63, v244\n"
"v_lshl_add_u32 v249, v248, 2, v252\n"
"s_lshl_b32 s63, s41, 8\n"
"v_add_u32 v251, s63, v249\n"
"v_add_u32 v252, s63, v252\n"
"s_add_u32 s63, s40, 131072\n"
"v_lshl_add_u32 v255, v253, 3, s63\n"
"s_min_u32 s63, s41, 2\n"
"s_mul_i32 s63, s63, 192\n"
"v_add_u32 v183, s63, v248\n"
"v_mul_u32_u24 v183, 911, v183\n"
"s_lshl_b32 s63, s55, 3\n"
"v_lshrrev_b32 v180, 16, v183\n"
"v_add_u32 v180, s63, v180\n"
"v_add_u32 v183, 58304, v183\n"
"v_lshrrev_b32 v181, 16, v183\n"
"v_add_u32 v181, s63, v181\n"
"v_add_u32 v183, 58304, v183\n"
"v_lshrrev_b32 v182, 16, v183\n"
"v_add_u32 v182, s63, v182\n"
"v_lshlrev_b32 v250, 4, v248\n"
"v_add_u32 v184, s45, v250\n"
"s_cmp_eq_u32 s41, 3\n"
"s_cselect_b32 s63, 0x10000000, 0\n"
"v_or_b32 v250, s63, v250\n"
"ds_read_b128 v[212:215], v227 offset:512\n"
"ds_read_b128 v[216:219], v227 offset:528\n"
"s_lshl_b32 s56, s55, 3\n"
"s_sub_u32 s56, 2047, s56\n"
"v_and_b32 v220, 7, v248\n"
"v_sub_u32 v220, s56, v220\n"
"s_waitcnt lgkmcnt(0)\n"
"v_add_f32 v212, v212, v214\n"
"v_add_f32 v213, v213, v215\n"
"v_add_f32 v212, v212, v216\n"
"v_add_f32 v213, v213, v217\n"
"v_mov_b32 v145, s85\n"
"v_mov_b32 v147, s85\n"
"s_mul_i32 s56, s55, 16\n"
"s_add_u32 s56, s56, 26370048\n"
"v_mov_b32 v148, 0\n"
"v_mul_u32_u24 v149, 16, v248\n"
"s_mov_b32 s57, 26370048\n"
"s_mov_b32 s58, 26371072\n"
"s_mov_b32 s59, 26372096\n"
"s_mov_b32 s60, 26373120\n"
"s_mov_b32 s87, 0x80000\n"
"s_waitcnt vmcnt(0)\n"
"s_barrier\n"
"ds_read_b128 v[200:203], v226 offset:0\n"
"ds_read_b128 v[204:207], v226 offset:128\n"
"ds_read_b128 v[208:211], v226 offset:256\n"
"s_waitcnt lgkmcnt(1)\n"
"v_add_f32 v200, v200, v204\n"
"v_add_f32 v201, v201, v205\n"
"v_add_f32 v202, v202, v206\n"
"s_waitcnt lgkmcnt(0)\n"
"v_add_f32 v200, v200, v208\n"
"v_add_f32 v201, v201, v209\n"
"v_add_f32 v202, v202, v210\n"
"v_mul_f32 v200, 0x3ae38e39, v200\n"
"v_mul_f32 v201, 0x3ae38e39, v201\n"
"v_fma_f32 v201, -v200, v200, v201\n"
"v_max_f32 v201, 0, v201\n"
"v_add_f32 v201, 0x3727c5ac, v201\n"
"v_rsq_f32 v192, v201\n"
"v_fma_f32 v202, -v200, v212, v202\n"
"v_fma_f32 v196, v192, v202, v213\n"
"v_ashrrev_i32 v203, 31, v196\n"
"v_or_b32 v203, 0x80000000, v203\n"
"v_xor_b32 v196, v196, v203\n"
"v_and_or_b32 v196, v196, s14, v220\n"
"v_mul_f32 v194, v200, v192\n"
"v_sub_f32 v194, 0, v194\n"
"v_mov_b32_dpp v203, v196 row_ror:1 row_mask:0xf bank_mask:0xf\n"
"v_min_u32 v198, v196, v203\n"
"v_max_u32 v197, v196, v203\n"
"s_nop 0\n"
"v_mov_b32_dpp v204, v198 row_ror:2 row_mask:0xf bank_mask:0xf\n"
"v_mov_b32_dpp v203, v197 row_ror:2 row_mask:0xf bank_mask:0xf\n"
"v_min_u32 v205, v197, v203\n"
"v_max_u32 v197, v197, v203\n"
"v_max3_u32 v198, v198, v204, v205\n"
"s_nop 0\n"
"v_mov_b32_dpp v203, v197 row_ror:4 row_mask:0xf bank_mask:0xf\n"
"v_mov_b32_dpp v204, v198 row_ror:4 row_mask:0xf bank_mask:0xf\n"
"v_min_u32 v205, v197, v203\n"
"v_max_u32 v197, v197, v203\n"
"v_max3_u32 v198, v198, v204, v205\n"
"v_mov_b32 v144, v197\n"
"v_mov_b32 v146, v198\n"
"s_mov_b64 exec, 1\n"
"buffer_store_dwordx4 v[144:147], v148, s[28:31], s56 offen sc1\n"
"s_mov_b64 exec, -1\n"
"AT_S2:\n"
"buffer_load_dwordx4 v[152:155], v149, s[28:31], s57 offen sc1\n"
"buffer_load_dwordx4 v[156:159], v149, s[28:31], s58 offen sc1\n"
"buffer_load_dwordx4 v[160:163], v149, s[28:31], s59 offen sc1\n"
"buffer_load_dwordx4 v[164:167], v149, s[28:31], s60 offen sc1\n"
"s_waitcnt vmcnt(0)\n"
"v_xor_b32 v150, s85, v153\n"
"v_xor_b32 v151, s85, v155\n"
"v_bitop3_b32 v150, v157, s85, v150 bitop3:0xbe\n"
"v_bitop3_b32 v151, v159, s85, v151 bitop3:0xbe\n"
"v_bitop3_b32 v150, v161, s85, v150 bitop3:0xbe\n"
"v_bitop3_b32 v151, v163, s85, v151 bitop3:0xbe\n"
"v_bitop3_b32 v150, v165, s85, v150 bitop3:0xbe\n"
"v_bitop3_b32 v151, v167, s85, v151 bitop3:0xbe\n"
"v_or_b32 v150, v150, v151\n"
"v_cmp_ne_u32 vcc, 0, v150\n"
"s_nop 0\n"
"s_cbranch_vccz AT_S3\n"
"s_sub_u32 s87, s87, 1\n"
"s_cmp_eq_u32 s87, 0\n"
"s_cbranch_scc1 AT_S3\n"
"s_sleep 1\n"
"s_branch AT_S2\n"
"AT_S3:\n"
"v_min_u32 v205, v152, v156\n"
"v_max_u32 v152, v152, v156\n"
"v_max3_u32 v154, v154, v158, v205\n"
"v_min_u32 v205, v160, v164\n"
"v_max_u32 v160, v160, v164\n"
"v_max3_u32 v162, v162, v166, v205\n"
"v_min_u32 v205, v152, v160\n"
"v_max_u32 v152, v152, v160\n"
"v_max3_u32 v154, v154, v162, v205\n"
"s_nop 0\n"
"v_mov_b32_dpp v156, v152 row_ror:1 row_mask:0xf bank_mask:0xf\n"
"v_mov_b32_dpp v158, v154 row_ror:1 row_mask:0xf bank_mask:0xf\n"
"v_min_u32 v205, v152, v156\n"
"v_max_u32 v152, v152, v156\n"
"v_max3_u32 v154, v154, v158, v205\n"
"s_nop 0\n"
"v_mov_b32_dpp v156, v152 row_ror:2 row_mask:0xf bank_mask:0xf\n"
"v_mov_b32_dpp v158, v154 row_ror:2 row_mask:0xf bank_mask:0xf\n"
"v_min_u32 v205, v152, v156\n"
"v_max_u32 v152, v152, v156\n"
"v_max3_u32 v154, v154, v158, v205\n"
"s_nop 0\n"
"v_mov_b32_dpp v156, v152 row_ror:4 row_mask:0xf bank_mask:0xf\n"
"v_mov_b32_dpp v158, v154 row_ror:4 row_mask:0xf bank_mask:0xf\n"
"v_min_u32 v205, v152, v156\n"
"v_max_u32 v152, v152, v156\n"
"v_max3_u32 v154, v154, v158, v205\n"
"s_nop 0\n"
"v_mov_b32_dpp v156, v152 row_ror:8 row_mask:0xf bank_mask:0xf\n"
"v_mov_b32_dpp v158, v154 row_ror:8 row_mask:0xf bank_mask:0xf\n"
"v_min_u32 v205, v152, v156\n"
"v_max_u32 v152, v152, v156\n"
"v_max3_u32 v154, v154, v158, v205\n"
"v_mov_b32 v156, v152\n"
"v_mov_b32 v158, v154\n"
"s_nop 0\n"
"v_permlane16_swap_b32 v152, v156\n"
"v_permlane16_swap_b32 v154, v158\n"
"v_min_u32 v205, v152, v156\n"
"v_max_u32 v152, v152, v156\n"
"v_max3_u32 v154, v154, v158, v205\n"
"v_mov_b32 v156, v152\n"
"v_mov_b32 v158, v154\n"
"s_nop 0\n"
"v_permlane32_swap_b32 v152, v156\n"
"v_permlane32_swap_b32 v154, v158\n"
"v_min_u32 v205, v152, v156\n"
"v_max_u32 v152, v152, v156\n"
"v_max3_u32 v154, v154, v158, v205\n"
"v_mov_b32 v153, v154\n"
"s_mov_b64 exec, 1\n"
"ds_write_b64 v227, v[152:153] offset:656\n"
"s_mov_b64 exec, -1\n"
"s_branch AT_B3\n"
"AT_A0:\n"
"v_and_b32 v192, 15, v248\n"
"v_lshlrev_b32 v192, 6, v192\n"
"s_lshl_b32 s57, s44, 10\n"
"buffer_load_dword v193, v192, s[28:31], s57 offen sc1\n"
"ds_read_b64 v[228:229], v240 offset:0\n"
"ds_read_b64 v[230:231], v240 offset:32\n"
"ds_read_b64 v[232:233], v240 offset:384\n"
"ds_read_b64 v[234:235], v240 offset:416\n"
"ds_read_b64 v[236:237], v240 offset:768\n"
"ds_read_b64 v[238:239], v240 offset:800\n"
"ds_read_b128 v[96:99], v244 offset:0\n"
"ds_read_b128 v[100:103], v244 offset:64\n"
"ds_read_b128 v[104:107], v244 offset:768\n"
"ds_read_b128 v[108:111], v244 offset:832\n"
"ds_read_b128 v[112:115], v244 offset:1536\n"
"ds_read_b128 v[116:119], v244 offset:1600\n"
"ds_read_b128 v[120:123], v244 offset:2304\n"
"ds_read_b128 v[124:127], v244 offset:2368\n"
"ds_read_b128 v[128:131], v244 offset:3072\n"
"ds_read_b128 v[132:135], v244 offset:3136\n"
"ds_read_b128 v[136:139], v244 offset:3840\n"
"ds_read_b128 v[140:143], v244 offset:3904\n"
"v_mov_b32 v48, 0\n"
"v_mov_b32 v49, 0\n"
"v_mov_b32 v50, 0\n"
"v_mov_b32 v51, 0\n"
"v_mov_b32 v52, 0\n"
"v_mov_b32 v53, 0\n"
"v_mov_b32 v54, 0\n"
"v_mov_b32 v55, 0\n"
"v_mov_b32 v56, 0\n"
"v_mov_b32 v57, 0\n"
"v_mov_b32 v58, 0\n"
"v_mov_b32 v59, 0\n"
"v_mov_b32 v60, 0\n"
"v_mov_b32 v61, 0\n"
"v_mov_b32 v62, 0\n"
"v_mov_b32 v63, 0\n"
"v_mov_b32 v64, 0\n"
"v_mov_b32 v65, 0\n"
"v_mov_b32 v66, 0\n"
"v_mov_b32 v67, 0\n"
"v_mov_b32 v68, 0\n"
"v_mov_b32 v69, 0\n"
"v_mov_b32 v70, 0\n"
"v_mov_b32 v71, 0\n"
"v_mov_b32 v72, 0\n"
"v_mov_b32 v73, 0\n"
"v_mov_b32 v74, 0\n"
"v_mov_b32 v75, 0\n"
"v_mov_b32 v76, 0\n"
"v_mov_b32 v77, 0\n"
"v_mov_b32 v78, 0\n"
"v_mov_b32 v79, 0\n"
"v_mov_b32 v80, 0\n"
"v_mov_b32 v81, 0\n"
"v_mov_b32 v82, 0\n"
"v_mov_b32 v83, 0\n"
"v_mov_b32 v84, 0\n"
"v_mov_b32 v85, 0\n"
"v_mov_b32 v86, 0\n"
"v_mov_b32 v87, 0\n"
"v_mov_b32 v88, 0\n"
"v_mov_b32 v89, 0\n"
"v_mov_b32 v90, 0\n"
"v_mov_b32 v91, 0\n"
"v_mov_b32 v92, 0\n"
"v_mov_b32 v93, 0\n"
"v_mov_b32 v94, 0\n"
"v_mov_b32 v95, 0\n"
"ds_read_b128 v[212:215], v227 offset:512\n"
"ds_read_b128 v[216:219], v227 offset:528\n"
"s_lshl_b32 s56, s55, 3\n"
"s_sub_u32 s56, 2047, s56\n"
"v_and_b32 v220, 7, v248\n"
"v_sub_u32 v220, s56, v220\n"
"s_waitcnt lgkmcnt(0)\n"
"v_add_f32 v212, v212, v214\n"
"v_add_f32 v213, v213, v215\n"
"v_add_f32 v212, v212, v216\n"
"v_add_f32 v213, v213, v217\n"
"s_mul_i32 s56, s41, 1536\n"
"s_add_u32 s56, s56, s40\n"
"s_add_u32 s56, s56, 16384\n"
"v_and_b32 v168, 32, v248\n"
"v_lshl_add_u32 v168, v168, 2, s56\n"
"v_bfe_u32 v169, v248, 3, 2\n"
"v_lshl_add_u32 v168, v169, 4, v168\n"
"v_lshrrev_b32 v169, 5, v248\n"
"v_mul_u32_u24 v169, 768, v169\n"
"v_and_b32 v170, 31, v248\n"
"v_lshl_add_u32 v169, v170, 3, v169\n"
"v_add_u32 v169, s56, v169\n"
"s_lshl_b32 s56, s41, 9\n"
"v_lshl_add_u32 v170, v170, 4, s56\n"
"s_mov_b32 s52, 0\n"
"s_mov_b32 s87, 0x80000\n"
"v_lshlrev_b32 v144, 16, v228\n"
"v_and_b32 v145, 0xffff0000, v228\n"
"v_lshlrev_b32 v146, 16, v229\n"
"v_and_b32 v147, 0xffff0000, v229\n"
"v_lshlrev_b32 v148, 16, v230\n"
"v_and_b32 v149, 0xffff0000, v230\n"
"v_lshlrev_b32 v150, 16, v231\n"
"v_and_b32 v151, 0xffff0000, v231\n"
"v_lshlrev_b32 v152, 16, v232\n"
"v_and_b32 v153, 0xffff0000, v232\n"
"v_lshlrev_b32 v154, 16, v233\n"
"v_and_b32 v155, 0xffff0000, v233\n"
"v_lshlrev_b32 v156, 16, v234\n"
"v_and_b32 v157, 0xffff0000, v234\n"
"v_lshlrev_b32 v158, 16, v235\n"
"v_and_b32 v159, 0xffff0000, v235\n"
"v_lshlrev_b32 v160, 16, v236\n"
"v_and_b32 v161, 0xffff0000, v236\n"
"v_lshlrev_b32 v162, 16, v237\n"
"v_and_b32 v163, 0xffff0000, v237\n"
"v_lshlrev_b32 v164, 16, v238\n"
"v_and_b32 v165, 0xffff0000, v238\n"
"v_lshlrev_b32 v166, 16, v239\n"
"v_and_b32 v167, 0xffff0000, v239\n"
"s_waitcnt vmcnt(0)\n"
"AT_A1:\n"
"v_cmp_eq_u32 vcc, s85, v193\n"
"s_sub_u32 s87, s87, 1\n"
"s_lshr_b32 s53, vcc_lo, 1\n"
"s_and_b32 s53, s53, vcc_lo\n"
"s_lshr_b32 s59, s53, 2\n"
"s_and_b32 s53, s53, s59\n"
"s_and_b32 s53, s53, 0x1111\n"
"s_cmp_lt_i32 s87, 0\n"
"s_cselect_b32 s53, 0x1111, s53\n"
"s_andn2_b32 s53, s53, s52\n"
"s_cmp_lg_u32 s53, 0\n"
"s_cbranch_scc1 AT_A2\n"
"s_sleep 2\n"
"AT_A1b:\n"
"buffer_load_dword v193, v192, s[28:31], s57 offen sc1\n"
"s_waitcnt vmcnt(0)\n"
"s_branch AT_A1\n"
"AT_A2:\n"
"s_bcnt1_i32_b32 s54, s53\n"
"s_bitcmp1_b32 s53, 0\n"
"s_cbranch_scc0 AT_Ai0\n"
"s_mov_b32 s56, s47\n"
"buffer_load_dwordx4 v[0:3], v241, s[28:31], s56 offen sc1\n"
"buffer_load_dwordx4 v[4:7], v242, s[28:31], s56 offen sc1\n"
"buffer_load_dwordx4 v[8:11], v243, s[28:31], s56 offen sc1\n"
"AT_Ai0:\n"
"s_bitcmp1_b32 s53, 4\n"
"s_cbranch_scc0 AT_Ai1\n"
"s_add_u32 s56, s47, 2359296\n"
"buffer_load_dwordx4 v[12:15], v241, s[28:31], s56 offen sc1\n"
"buffer_load_dwordx4 v[16:19], v242, s[28:31], s56 offen sc1\n"
"buffer_load_dwordx4 v[20:23], v243, s[28:31], s56 offen sc1\n"
"AT_Ai1:\n"
"s_bitcmp1_b32 s53, 8\n"
"s_cbranch_scc0 AT_Ai2\n"
"s_add_u32 s56, s47, 4718592\n"
"buffer_load_dwordx4 v[24:27], v241, s[28:31], s56 offen sc1\n"
"buffer_load_dwordx4 v[28:31], v242, s[28:31], s56 offen sc1\n"
"buffer_load_dwordx4 v[32:35], v243, s[28:31], s56 offen sc1\n"
"AT_Ai2:\n"
"s_bitcmp1_b32 s53, 12\n"
"s_cbranch_scc0 AT_Ai3\n"
"s_add_u32 s56, s47, 7077888\n"
"buffer_load_dwordx4 v[36:39], v241, s[28:31], s56 offen sc1\n"
"buffer_load_dwordx4 v[40:43], v242, s[28:31], s56 offen sc1\n"
"buffer_load_dwordx4 v[44:47], v243, s[28:31], s56 offen sc1\n"
"AT_Ai3:\n"
"s_bitcmp1_b32 s53, 0\n"
"s_cbranch_scc0 AT_Ar0\n"
"s_sub_u32 s54, s54, 1\n"
"s_cmp_eq_u32 s54, 0\n"
"s_cbranch_scc1 AT_Aw0_0\n"
"s_cmp_eq_u32 s54, 1\n"
"s_cbranch_scc1 AT_Aw0_1\n"
"s_cmp_eq_u32 s54, 2\n"
"s_cbranch_scc1 AT_Aw0_2\n"
"s_waitcnt vmcnt(9)\n"
"s_branch AT_Ax0\n"
"AT_Aw0_2:\n"
"s_waitcnt vmcnt(6)\n"
"s_branch AT_Ax0\n"
"AT_Aw0_1:\n"
"s_waitcnt vmcnt(3)\n"
"s_branch AT_Ax0\n"
"AT_Aw0_0:\n"
"s_waitcnt vmcnt(0)\n"
"AT_Ax0:\n"
"v_max3_u32 v194, v0, v1, v2\n"
"v_max3_u32 v195, v3, v4, v5\n"
"v_max3_u32 v194, v194, v6, v7\n"
"v_max3_u32 v195, v195, v8, v9\n"
"v_max3_u32 v194, v194, v10, v11\n"
"v_max_u32 v194, v194, v195\n"
"v_cmp_eq_u32 vcc, -1, v194\n"
"s_cmp_lt_i32 s87, 0\n"
"s_cselect_b64 vcc, 0, vcc\n"
"s_cbranch_vccnz AT_Ar0\n"
"s_bitset1_b32 s52, 0\n"
"v_dot2c_f32_bf16 v48, v0, v224\n"
"v_dot2c_f32_bf16 v49, v0, v225\n"
"v_dot2c_f32_bf16 v50, v1, v224\n"
"v_dot2c_f32_bf16 v51, v1, v225\n"
"v_dot2c_f32_bf16 v52, v2, v224\n"
"v_dot2c_f32_bf16 v53, v2, v225\n"
"v_dot2c_f32_bf16 v54, v3, v224\n"
"v_dot2c_f32_bf16 v55, v3, v225\n"
"v_dot2c_f32_bf16 v56, v4, v224\n"
"v_dot2c_f32_bf16 v57, v4, v225\n"
"v_dot2c_f32_bf16 v58, v5, v224\n"
"v_dot2c_f32_bf16 v59, v5, v225\n"
"v_dot2c_f32_bf16 v60, v6, v224\n"
"v_dot2c_f32_bf16 v61, v6, v225\n"
"v_dot2c_f32_bf16 v62, v7, v224\n"
"v_dot2c_f32_bf16 v63, v7, v225\n"
"v_dot2c_f32_bf16 v64, v8, v224\n"
"v_dot2c_f32_bf16 v65, v8, v225\n"
"v_dot2c_f32_bf16 v66, v9, v224\n"
"v_dot2c_f32_bf16 v67, v9, v225\n"
"v_dot2c_f32_bf16 v68, v10, v224\n"
"v_dot2c_f32_bf16 v69, v10, v225\n"
"v_dot2c_f32_bf16 v70, v11, v224\n"
"v_dot2c_f32_bf16 v71, v11, v225\n"
"AT_Ar0:\n"
"s_bitcmp1_b32 s53, 4\n"
"s_cbranch_scc0 AT_Ar1\n"
"s_sub_u32 s54, s54, 1\n"
"s_cmp_eq_u32 s54, 0\n"
"s_cbranch_scc1 AT_Aw1_0\n"
"s_cmp_eq_u32 s54, 1\n"
"s_cbranch_scc1 AT_Aw1_1\n"
"s_waitcnt vmcnt(6)\n"
"s_branch AT_Ax1\n"
"AT_Aw1_1:\n"
"s_waitcnt vmcnt(3)\n"
"s_branch AT_Ax1\n"
"AT_Aw1_0:\n"
"s_waitcnt vmcnt(0)\n"
"AT_Ax1:\n"
"v_max3_u32 v194, v12, v13, v14\n"
"v_max3_u32 v195, v15, v16, v17\n"
"v_max3_u32 v194, v194, v18, v19\n"
"v_max3_u32 v195, v195, v20, v21\n"
"v_max3_u32 v194, v194, v22, v23\n"
"v_max_u32 v194, v194, v195\n"
"v_cmp_eq_u32 vcc, -1, v194\n"
"s_cmp_lt_i32 s87, 0\n"
"s_cselect_b64 vcc, 0, vcc\n"
"s_cbranch_vccnz AT_Ar1\n"
"s_bitset1_b32 s52, 4\n"
"v_dot2c_f32_bf16 v48, v12, v224\n"
"v_dot2c_f32_bf16 v49, v12, v225\n"
"v_dot2c_f32_bf16 v50, v13, v224\n"
"v_dot2c_f32_bf16 v51, v13, v225\n"
"v_dot2c_f32_bf16 v52, v14, v224\n"
"v_dot2c_f32_bf16 v53, v14, v225\n"
"v_dot2c_f32_bf16 v54, v15, v224\n"
"v_dot2c_f32_bf16 v55, v15, v225\n"
"v_dot2c_f32_bf16 v56, v16, v224\n"
"v_dot2c_f32_bf16 v57, v16, v225\n"
"v_dot2c_f32_bf16 v58, v17, v224\n"
"v_dot2c_f32_bf16 v59, v17, v225\n"
"v_dot2c_f32_bf16 v60, v18, v224\n"
"v_dot2c_f32_bf16 v61, v18, v225\n"
"v_dot2c_f32_bf16 v62, v19, v224\n"
"v_dot2c_f32_bf16 v63, v19, v225\n"
"v_dot2c_f32_bf16 v64, v20, v224\n"
"v_dot2c_f32_bf16 v65, v20, v225\n"
"v_dot2c_f32_bf16 v66, v21, v224\n"
"v_dot2c_f32_bf16 v67, v21, v225\n"
"v_dot2c_f32_bf16 v68, v22, v224\n"
"v_dot2c_f32_bf16 v69, v22, v225\n"
"v_dot2c_f32_bf16 v70, v23, v224\n"
"v_dot2c_f32_bf16 v71, v23, v225\n"
"AT_Ar1:\n"
"s_bitcmp1_b32 s53, 8\n"
"s_cbranch_scc0 AT_Ar2\n"
"s_sub_u32 s54, s54, 1\n"
"s_cmp_eq_u32 s54, 0\n"
"s_cbranch_scc1 AT_Aw2_0\n"
"s_waitcnt vmcnt(3)\n"
"s_branch AT_Ax2\n"
"AT_Aw2_0:\n"
"s_waitcnt vmcnt(0)\n"
"AT_Ax2:\n"
"v_max3_u32 v194, v24, v25, v26\n"
"v_max3_u32 v195, v27, v28, v29\n"
"v_max3_u32 v194, v194, v30, v31\n"
"v_max3_u32 v195, v195, v32, v33\n"
"v_max3_u32 v194, v194, v34, v35\n"
"v_max_u32 v194, v194, v195\n"
"v_cmp_eq_u32 vcc, -1, v194\n"
"s_cmp_lt_i32 s87, 0\n"
"s_cselect_b64 vcc, 0, vcc\n"
"s_cbranch_vccnz AT_Ar2\n"
"s_bitset1_b32 s52, 8\n"
"v_dot2c_f32_bf16 v72, v24, v224\n"
"v_dot2c_f32_bf16 v73, v24, v225\n"
"v_dot2c_f32_bf16 v74, v25, v224\n"
"v_dot2c_f32_bf16 v75, v25, v225\n"
"v_dot2c_f32_bf16 v76, v26, v224\n"
"v_dot2c_f32_bf16 v77, v26, v225\n"
"v_dot2c_f32_bf16 v78, v27, v224\n"
"v_dot2c_f32_bf16 v79, v27, v225\n"
"v_dot2c_f32_bf16 v80, v28, v224\n"
"v_dot2c_f32_bf16 v81, v28, v225\n"
"v_dot2c_f32_bf16 v82, v29, v224\n"
"v_dot2c_f32_bf16 v83, v29, v225\n"
"v_dot2c_f32_bf16 v84, v30, v224\n"
"v_dot2c_f32_bf16 v85, v30, v225\n"
"v_dot2c_f32_bf16 v86, v31, v224\n"
"v_dot2c_f32_bf16 v87, v31, v225\n"
"v_dot2c_f32_bf16 v88, v32, v224\n"
"v_dot2c_f32_bf16 v89, v32, v225\n"
"v_dot2c_f32_bf16 v90, v33, v224\n"
"v_dot2c_f32_bf16 v91, v33, v225\n"
"v_dot2c_f32_bf16 v92, v34, v224\n"
"v_dot2c_f32_bf16 v93, v34, v225\n"
"v_dot2c_f32_bf16 v94, v35, v224\n"
"v_dot2c_f32_bf16 v95, v35, v225\n"
"AT_Ar2:\n"
"s_bitcmp1_b32 s53, 12\n"
"s_cbranch_scc0 AT_Ar3\n"
"s_sub_u32 s54, s54, 1\n"
"s_waitcnt vmcnt(0)\n"
"AT_Ax3:\n"
"v_max3_u32 v194, v36, v37, v38\n"
"v_max3_u32 v195, v39, v40, v41\n"
"v_max3_u32 v194, v194, v42, v43\n"
"v_max3_u32 v195, v195, v44, v45\n"
"v_max3_u32 v194, v194, v46, v47\n"
"v_max_u32 v194, v194, v195\n"
"v_cmp_eq_u32 vcc, -1, v194\n"
"s_cmp_lt_i32 s87, 0\n"
"s_cselect_b64 vcc, 0, vcc\n"
"s_cbranch_vccnz AT_Ar3\n"
"s_bitset1_b32 s52, 12\n"
"v_dot2c_f32_bf16 v72, v36, v224\n"
"v_dot2c_f32_bf16 v73, v36, v225\n"
"v_dot2c_f32_bf16 v74, v37, v224\n"
"v_dot2c_f32_bf16 v75, v37, v225\n"
"v_dot2c_f32_bf16 v76, v38, v224\n"
"v_dot2c_f32_bf16 v77, v38, v225\n"
"v_dot2c_f32_bf16 v78, v39, v224\n"
"v_dot2c_f32_bf16 v79, v39, v225\n"
"v_dot2c_f32_bf16 v80, v40, v224\n"
"v_dot2c_f32_bf16 v81, v40, v225\n"
"v_dot2c_f32_bf16 v82, v41, v224\n"
"v_dot2c_f32_bf16 v83, v41, v225\n"
"v_dot2c_f32_bf16 v84, v42, v224\n"
"v_dot2c_f32_bf16 v85, v42, v225\n"
"v_dot2c_f32_bf16 v86, v43, v224\n"
"v_dot2c_f32_bf16 v87, v43, v225\n"
"v_dot2c_f32_bf16 v88, v44, v224\n"
"v_dot2c_f32_bf16 v89, v44, v225\n"
"v_dot2c_f32_bf16 v90, v45, v224\n"
"v_dot2c_f32_bf16 v91, v45, v225\n"
"v_dot2c_f32_bf16 v92, v46, v224\n"
"v_dot2c_f32_bf16 v93, v46, v225\n"
"v_dot2c_f32_bf16 v94, v47, v224\n"
"v_dot2c_f32_bf16 v95, v47, v225\n"
"AT_Ar3:\n"
"s_cmp_lg_u32 s52, 0x1111\n"
"s_cbranch_scc1 AT_A1b\n"
"v_pk_add_f32 v[48:49], v[48:49], v[72:73]\n"
"v_pk_add_f32 v[50:51], v[50:51], v[74:75]\n"
"v_pk_add_f32 v[52:53], v[52:53], v[76:77]\n"
"v_pk_add_f32 v[54:55], v[54:55], v[78:79]\n"
"v_pk_add_f32 v[56:57], v[56:57], v[80:81]\n"
"v_pk_add_f32 v[58:59], v[58:59], v[82:83]\n"
"v_pk_add_f32 v[60:61], v[60:61], v[84:85]\n"
"v_pk_add_f32 v[62:63], v[62:63], v[86:87]\n"
"v_pk_add_f32 v[64:65], v[64:65], v[88:89]\n"
"v_pk_add_f32 v[66:67], v[66:67], v[90:91]\n"
"v_pk_add_f32 v[68:69], v[68:69], v[92:93]\n"
"v_pk_add_f32 v[70:71], v[70:71], v[94:95]\n"
"v_pk_fma_f32 v[48:49], v[48:49], v[96:97], v[144:145]\n"
"v_pk_fma_f32 v[50:51], v[50:51], v[98:99], v[146:147]\n"
"v_pk_fma_f32 v[52:53], v[52:53], v[100:101], v[148:149]\n"
"v_pk_fma_f32 v[54:55], v[54:55], v[102:103], v[150:151]\n"
"v_pk_fma_f32 v[56:57], v[56:57], v[104:105], v[152:153]\n"
"v_pk_fma_f32 v[58:59], v[58:59], v[106:107], v[154:155]\n"
"v_pk_fma_f32 v[60:61], v[60:61], v[108:109], v[156:157]\n"
"v_pk_fma_f32 v[62:63], v[62:63], v[110:111], v[158:159]\n"
"v_pk_fma_f32 v[64:65], v[64:65], v[112:113], v[160:161]\n"
"v_pk_fma_f32 v[66:67], v[66:67], v[114:115], v[162:163]\n"
"v_pk_fma_f32 v[68:69], v[68:69], v[116:117], v[164:165]\n"
"v_pk_fma_f32 v[70:71], v[70:71], v[118:119], v[166:167]\n"
"v_pk_mul_f32 v[194:195], v[48:49], v[48:49]\n"
"v_pk_mul_f32 v[196:197], v[48:49], v[120:121]\n"
"v_pk_add_f32 v[192:193], v[48:49], v[50:51]\n"
"v_pk_fma_f32 v[194:195], v[50:51], v[50:51], v[194:195]\n"
"v_pk_fma_f32 v[196:197], v[50:51], v[122:123], v[196:197]\n"
"v_pk_add_f32 v[192:193], v[192:193], v[52:53]\n"
"v_pk_fma_f32 v[194:195], v[52:53], v[52:53], v[194:195]\n"
"v_pk_fma_f32 v[196:197], v[52:53], v[124:125], v[196:197]\n"
"v_pk_add_f32 v[192:193], v[192:193], v[54:55]\n"
"v_pk_fma_f32 v[194:195], v[54:55], v[54:55], v[194:195]\n"
"v_pk_fma_f32 v[196:197], v[54:55], v[126:127], v[196:197]\n"
"v_pk_add_f32 v[192:193], v[192:193], v[56:57]\n"
"v_pk_fma_f32 v[194:195], v[56:57], v[56:57], v[194:195]\n"
"v_pk_fma_f32 v[196:197], v[56:57], v[128:129], v[196:197]\n"
"v_pk_add_f32 v[192:193], v[192:193], v[58:59]\n"
"v_pk_fma_f32 v[194:195], v[58:59], v[58:59], v[194:195]\n"
"v_pk_fma_f32 v[196:197], v[58:59], v[130:131], v[196:197]\n"
"v_pk_add_f32 v[192:193], v[192:193], v[60:61]\n"
"v_pk_fma_f32 v[194:195], v[60:61], v[60:61], v[194:195]\n"
"v_pk_fma_f32 v[196:197], v[60:61], v[132:133], v[196:197]\n"
"v_pk_add_f32 v[192:193], v[192:193], v[62:63]\n"
"v_pk_fma_f32 v[194:195], v[62:63], v[62:63], v[194:195]\n"
"v_pk_fma_f32 v[196:197], v[62:63], v[134:135], v[196:197]\n"
"v_pk_add_f32 v[192:193], v[192:193], v[64:65]\n"
"v_pk_fma_f32 v[194:195], v[64:65], v[64:65], v[194:195]\n"
"v_pk_fma_f32 v[196:197], v[64:65], v[136:137], v[196:197]\n"
"v_pk_add_f32 v[192:193], v[192:193], v[66:67]\n"
"v_pk_fma_f32 v[194:195], v[66:67], v[66:67], v[194:195]\n"
"v_pk_fma_f32 v[196:197], v[66:67], v[138:139], v[196:197]\n"
"v_pk_add_f32 v[192:193], v[192:193], v[68:69]\n"
"v_pk_fma_f32 v[194:195], v[68:69], v[68:69], v[194:195]\n"
"v_pk_fma_f32 v[196:197], v[68:69], v[140:141], v[196:197]\n"
"v_pk_add_f32 v[192:193], v[192:193], v[70:71]\n"
"v_pk_fma_f32 v[194:195], v[70:71], v[70:71], v[194:195]\n"
"v_pk_fma_f32 v[196:197], v[70:71], v[142:143], v[196:197]\n"
"v_add_f32 v192, v192, v193\n"
"v_add_f32 v193, v194, v195\n"
"v_add_f32 v194, v196, v197\n"
"s_lshl_b32 s56, s41, 7\n"
"v_add_u32 v199, s56, v226\n"
"v_add_f32_dpp v192, v192, v192 row_ror:8 row_mask:0xf bank_mask:0xf\n"
"v_add_f32_dpp v193, v193, v193 row_ror:8 row_mask:0xf bank_mask:0xf\n"
"v_add_f32_dpp v194, v194, v194 row_ror:8 row_mask:0xf bank_mask:0xf\n"
"v_mov_b32 v196, v192\n"
"v_mov_b32 v197, v193\n"
"v_mov_b32 v198, v194\n"
"v_permlane16_swap_b32 v192, v196\n"
"v_permlane16_swap_b32 v193, v197\n"
"v_permlane16_swap_b32 v194, v198\n"
"v_add_f32 v192, v192, v196\n"
"v_add_f32 v193, v193, v197\n"
"v_add_f32 v194, v194, v198\n"
"v_mov_b32 v196, v192\n"
"v_mov_b32 v197, v193\n"
"v_mov_b32 v198, v194\n"
"v_permlane32_swap_b32 v192, v196\n"
"v_permlane32_swap_b32 v193, v197\n"
"v_permlane32_swap_b32 v194, v198\n"
"v_add_f32 v192, v192, v196\n"
"v_add_f32 v193, v193, v197\n"
"v_add_f32 v194, v194, v198\n"
"s_mov_b64 exec, 0xff\n"
"ds_write_b128 v199, v[192:195]\n"
"s_mov_b64 exec, -1\n"
"v_mov_b32 v121, s85\n"
"v_mov_b32 v123, s85\n"
"v_mov_b32 v125, s85\n"
"v_mov_b32 v127, s85\n"
"v_mov_b32 v129, s85\n"
"v_mov_b32 v131, s85\n"
"s_waitcnt lgkmcnt(0)\n"
"s_barrier\n"
"ds_read_b128 v[200:203], v226 offset:0\n"
"ds_read_b128 v[204:207], v226 offset:128\n"
"ds_read_b128 v[208:211], v226 offset:256\n"
"ds_read_b128 v[0:3], v244 offset:4608\n"
"ds_read_b128 v[4:7], v244 offset:4672\n"
"ds_read_b128 v[8:11], v244 offset:5376\n"
"ds_read_b128 v[12:15], v244 offset:5440\n"
"ds_read_b128 v[16:19], v244 offset:6144\n"
"ds_read_b128 v[20:23], v244 offset:6208\n"
"ds_read_b128 v[24:27], v244 offset:6912\n"
"ds_read_b128 v[28:31], v244 offset:6976\n"
"ds_read_b128 v[32:35], v244 offset:7680\n"
"ds_read_b128 v[36:39], v244 offset:7744\n"
"ds_read_b128 v[40:43], v244 offset:8448\n"
"ds_read_b128 v[44:47], v244 offset:8512\n"
"s_waitcnt lgkmcnt(13)\n"
"v_add_f32 v200, v200, v204\n"
"v_add_f32 v201, v201, v205\n"
"v_add_f32 v202, v202, v206\n"
"s_waitcnt lgkmcnt(12)\n"
"v_add_f32 v200, v200, v208\n"
"v_add_f32 v201, v201, v209\n"
"v_add_f32 v202, v202, v210\n"
"v_mul_f32 v200, 0x3ae38e39, v200\n"
"v_mul_f32 v201, 0x3ae38e39, v201\n"
"v_fma_f32 v201, -v200, v200, v201\n"
"v_max_f32 v201, 0, v201\n"
"v_add_f32 v201, 0x3727c5ac, v201\n"
"v_rsq_f32 v192, v201\n"
"v_fma_f32 v202, -v200, v212, v202\n"
"v_fma_f32 v196, v192, v202, v213\n"
"v_ashrrev_i32 v203, 31, v196\n"
"v_or_b32 v203, 0x80000000, v203\n"
"v_xor_b32 v196, v196, v203\n"
"v_and_or_b32 v196, v196, s14, v220\n"
"v_mul_f32 v194, v200, v192\n"
"v_sub_f32 v194, 0, v194\n"
"v_mov_b32_dpp v203, v196 row_ror:1 row_mask:0xf bank_mask:0xf\n"
"v_min_u32 v198, v196, v203\n"
"v_max_u32 v197, v196, v203\n"
"s_nop 0\n"
"v_mov_b32_dpp v204, v198 row_ror:2 row_mask:0xf bank_mask:0xf\n"
"v_mov_b32_dpp v203, v197 row_ror:2 row_mask:0xf bank_mask:0xf\n"
"v_min_u32 v205, v197, v203\n"
"v_max_u32 v197, v197, v203\n"
"v_max3_u32 v198, v198, v204, v205\n"
"s_nop 0\n"
"v_mov_b32_dpp v203, v197 row_ror:4 row_mask:0xf bank_mask:0xf\n"
"v_mov_b32_dpp v204, v198 row_ror:4 row_mask:0xf bank_mask:0xf\n"
"v_min_u32 v205, v197, v203\n"
"v_max_u32 v197, v197, v203\n"
"v_max3_u32 v198, v198, v204, v205\n"
"v_cmp_ge_u32 s[8:9], v196, v198\n"
"ds_read_b128 v[72:75], v244 offset:38912\n"
"ds_read_b128 v[76:79], v244 offset:38976\n"
"ds_read_b128 v[80:83], v244 offset:39680\n"
"ds_read_b128 v[84:87], v244 offset:39744\n"
"ds_read_b128 v[88:91], v244 offset:40448\n"
"ds_read_b128 v[92:95], v244 offset:40512\n"
"v_pk_fma_f32 v[144:145], v[48:49], v[192:193], v[194:195] op_sel_hi:[1,0,0]\n"
"v_pk_fma_f32 v[146:147], v[50:51], v[192:193], v[194:195] op_sel_hi:[1,0,0]\n"
"v_pk_fma_f32 v[148:149], v[52:53], v[192:193], v[194:195] op_sel_hi:[1,0,0]\n"
"v_pk_fma_f32 v[150:151], v[54:55], v[192:193], v[194:195] op_sel_hi:[1,0,0]\n"
"v_pk_fma_f32 v[152:153], v[56:57], v[192:193], v[194:195] op_sel_hi:[1,0,0]\n"
"v_pk_fma_f32 v[154:155], v[58:59], v[192:193], v[194:195] op_sel_hi:[1,0,0]\n"
"v_pk_fma_f32 v[156:157], v[60:61], v[192:193], v[194:195] op_sel_hi:[1,0,0]\n"
"v_pk_fma_f32 v[158:159], v[62:63], v[192:193], v[194:195] op_sel_hi:[1,0,0]\n"
"v_pk_fma_f32 v[160:161], v[64:65], v[192:193], v[194:195] op_sel_hi:[1,0,0]\n"
"v_pk_fma_f32 v[162:163], v[66:67], v[192:193], v[194:195] op_sel_hi:[1,0,0]\n"
"v_pk_fma_f32 v[164:165], v[68:69], v[192:193], v[194:195] op_sel_hi:[1,0,0]\n"
"v_pk_fma_f32 v[166:167], v[70:71], v[192:193], v[194:195] op_sel_hi:[1,0,0]\n"
"s_waitcnt lgkmcnt(6)\n"
"v_fma_f32 v96, v144, v0, v24\n"
"v_fma_f32 v97, v145, v1, v25\n"
"v_fma_f32 v98, v146, v2, v26\n"
"v_fma_f32 v99, v147, v3, v27\n"
"v_fma_f32 v100, v148, v4, v28\n"
"v_fma_f32 v101, v149, v5, v29\n"
"v_fma_f32 v102, v150, v6, v30\n"
"v_fma_f32 v103, v151, v7, v31\n"
"v_fma_f32 v104, v152, v8, v32\n"
"v_fma_f32 v105, v153, v9, v33\n"
"v_fma_f32 v106, v154, v10, v34\n"
"v_fma_f32 v107, v155, v11, v35\n"
"v_fma_f32 v108, v156, v12, v36\n"
"v_fma_f32 v109, v157, v13, v37\n"
"v_fma_f32 v110, v158, v14, v38\n"
"v_fma_f32 v111, v159, v15, v39\n"
"v_fma_f32 v112, v160, v16, v40\n"
"v_fma_f32 v113, v161, v17, v41\n"
"v_fma_f32 v114, v162, v18, v42\n"
"v_fma_f32 v115, v163, v19, v43\n"
"v_fma_f32 v116, v164, v20, v44\n"
"v_fma_f32 v117, v165, v21, v45\n"
"v_fma_f32 v118, v166, v22, v46\n"
"v_fma_f32 v119, v167, v23, v47\n"
"v_cmp_ne_u32 vcc, v196, v197\n"
"v_add_u32 v135, 768, v168\n"
"v_cmp_gt_u32 s[58:59], 32, v248\n"
"v_cndmask_b32 v134, v168, v135, vcc\n"
"s_mov_b64 exec, s[8:9]\n"
"ds_write_b128 v134, v[96:99] offset:0\n"
"ds_write_b128 v134, v[100:103] offset:64\n"
"ds_write_b128 v134, v[104:107] offset:256\n"
"ds_write_b128 v134, v[108:111] offset:320\n"
"ds_write_b128 v134, v[112:115] offset:512\n"
"ds_write_b128 v134, v[116:119] offset:576\n"
"s_mov_b64 exec, -1\n"
"ds_read_b64 v[136:137], v169 offset:0\n"
"ds_read_b64 v[138:139], v169 offset:256\n"
"ds_read_b64 v[140:141], v169 offset:512\n"
"v_cndmask_b32 v135, v198, v197, s[58:59]\n"
"v_not_b32 v135, v135\n"
"v_and_b32 v135, 7, v135\n"
"v_mul_u32_u24 v135, 9216, v135\n"
"v_add_u32 v135, v135, v170\n"
"s_waitcnt lgkmcnt(0)\n"
"v_mov_b32 v120, v136\n"
"v_mov_b32 v122, v137\n"
"buffer_store_dwordx4 v[120:123], v135, s[28:31], s48 offen offset:0 sc1\n"
"v_mov_b32 v124, v138\n"
"v_mov_b32 v126, v139\n"
"buffer_store_dwordx4 v[124:127], v135, s[28:31], s48 offen offset:1536 sc1\n"
"v_mov_b32 v128, v140\n"
"v_mov_b32 v130, v141\n"
"buffer_store_dwordx4 v[128:131], v135, s[28:31], s48 offen offset:3072 sc1\n"
"v_fma_f32 v200, v72, v96, v48\n"
"v_fma_f32 v201, v73, v97, v49\n"
"v_fma_f32 v202, v74, v98, v50\n"
"v_fma_f32 v203, v75, v99, v51\n"
"v_fma_f32 v204, v76, v100, v52\n"
"v_fma_f32 v205, v77, v101, v53\n"
"v_fma_f32 v206, v78, v102, v54\n"
"v_fma_f32 v207, v79, v103, v55\n"
"v_fma_f32 v208, v80, v104, v56\n"
"v_fma_f32 v209, v81, v105, v57\n"
"v_fma_f32 v210, v82, v106, v58\n"
"v_fma_f32 v211, v83, v107, v59\n"
"v_fma_f32 v212, v84, v108, v60\n"
"v_fma_f32 v213, v85, v109, v61\n"
"v_fma_f32 v214, v86, v110, v62\n"
"v_fma_f32 v215, v87, v111, v63\n"
"v_fma_f32 v216, v88, v112, v64\n"
"v_fma_f32 v217, v89, v113, v65\n"
"v_fma_f32 v218, v90, v114, v66\n"
"v_fma_f32 v219, v91, v115, v67\n"
"v_fma_f32 v220, v92, v116, v68\n"
"v_fma_f32 v221, v93, v117, v69\n"
"v_fma_f32 v222, v94, v118, v70\n"
"v_fma_f32 v223, v95, v119, v71\n"
"v_cvt_pk_bf16_f32 v228, v200, v201\n"
"v_cvt_pk_bf16_f32 v229, v202, v203\n"
"v_cvt_pk_bf16_f32 v230, v204, v205\n"
"v_cvt_pk_bf16_f32 v231, v206, v207\n"
"v_cvt_pk_bf16_f32 v232, v208, v209\n"
"v_cvt_pk_bf16_f32 v233, v210, v211\n"
"v_cvt_pk_bf16_f32 v234, v212, v213\n"
"v_cvt_pk_bf16_f32 v235, v214, v215\n"
"v_cvt_pk_bf16_f32 v236, v216, v217\n"
"v_cvt_pk_bf16_f32 v237, v218, v219\n"
"v_cvt_pk_bf16_f32 v238, v220, v221\n"
"v_cvt_pk_bf16_f32 v239, v222, v223\n"
"ds_write_b64 v240, v[228:229] offset:0\n"
"ds_write_b64 v240, v[230:231] offset:32\n"
"ds_write_b64 v240, v[232:233] offset:384\n"
"ds_write_b64 v240, v[234:235] offset:416\n"
"ds_write_b64 v240, v[236:237] offset:768\n"
"ds_write_b64 v240, v[238:239] offset:800\n"
"s_mov_b64 s[8:9], -1\n"
"v_mov_b32 v225, v196\n"
"v_mov_b32 v224, 0xffff0000\n"
"v_accvgpr_read_b32 v1, a48\n"
"v_accvgpr_read_b32 v3, a49\n"
"v_accvgpr_read_b32 v5, a50\n"
"v_accvgpr_read_b32 v7, a51\n"
"v_lshlrev_b32 v0, 16, v1\n"
"v_and_b32 v1, v224, v1\n"
"v_lshlrev_b32 v2, 16, v3\n"
"v_and_b32 v3, v224, v3\n"
"v_lshlrev_b32 v4, 16, v5\n"
"v_and_b32 v5, v224, v5\n"
"v_lshlrev_b32 v6, 16, v7\n"
"v_and_b32 v7, v224, v7\n"
"v_accvgpr_read_b32 v9, a84\n"
"v_accvgpr_read_b32 v11, a85\n"
"v_accvgpr_read_b32 v13, a86\n"
"v_accvgpr_read_b32 v15, a87\n"
"v_lshlrev_b32 v8, 16, v9\n"
"v_and_b32 v9, v224, v9\n"
"v_lshlrev_b32 v10, 16, v11\n"
"v_and_b32 v11, v224, v11\n"
"v_lshlrev_b32 v12, 16, v13\n"
"v_and_b32 v13, v224, v13\n"
"v_lshlrev_b32 v14, 16, v15\n"
"v_and_b32 v15, v224, v15\n"
"v_accvgpr_read_b32 v17, a52\n"
"v_accvgpr_read_b32 v19, a53\n"
"v_accvgpr_read_b32 v21, a54\n"
"v_accvgpr_read_b32 v23, a55\n"
"v_lshlrev_b32 v16, 16, v17\n"
"v_and_b32 v17, v224, v17\n"
"v_lshlrev_b32 v18, 16, v19\n"
"v_and_b32 v19, v224, v19\n"
"v_lshlrev_b32 v20, 16, v21\n"
"v_and_b32 v21, v224, v21\n"
"v_lshlrev_b32 v22, 16, v23\n"
"v_and_b32 v23, v224, v23\n"
"v_accvgpr_read_b32 v25, a88\n"
"v_accvgpr_read_b32 v27, a89\n"
"v_accvgpr_read_b32 v29, a90\n"
"v_accvgpr_read_b32 v31, a91\n"
"v_lshlrev_b32 v24, 16, v25\n"
"v_and_b32 v25, v224, v25\n"
"v_lshlrev_b32 v26, 16, v27\n"
"v_and_b32 v27, v224, v27\n"
"v_lshlrev_b32 v28, 16, v29\n"
"v_and_b32 v29, v224, v29\n"
"v_lshlrev_b32 v30, 16, v31\n"
"v_and_b32 v31, v224, v31\n"
"v_accvgpr_read_b32 v33, a56\n"
"v_accvgpr_read_b32 v35, a57\n"
"v_accvgpr_read_b32 v37, a58\n"
"v_accvgpr_read_b32 v39, a59\n"
"v_lshlrev_b32 v32, 16, v33\n"
"v_and_b32 v33, v224, v33\n"
"v_lshlrev_b32 v34, 16, v35\n"
"v_and_b32 v35, v224, v35\n"
"v_lshlrev_b32 v36, 16, v37\n"
"v_and_b32 v37, v224, v37\n"
"v_lshlrev_b32 v38, 16, v39\n"
"v_and_b32 v39, v224, v39\n"
"v_accvgpr_read_b32 v41, a92\n"
"v_accvgpr_read_b32 v43, a93\n"
"v_accvgpr_read_b32 v45, a94\n"
"v_accvgpr_read_b32 v47, a95\n"
"v_lshlrev_b32 v40, 16, v41\n"
"v_and_b32 v41, v224, v41\n"
"v_lshlrev_b32 v42, 16, v43\n"
"v_and_b32 v43, v224, v43\n"
"v_lshlrev_b32 v44, 16, v45\n"
"v_and_b32 v45, v224, v45\n"
"v_lshlrev_b32 v46, 16, v47\n"
"v_and_b32 v47, v224, v47\n"
"v_accvgpr_read_b32 v49, a60\n"
"v_accvgpr_read_b32 v51, a61\n"
"v_accvgpr_read_b32 v53, a62\n"
"v_accvgpr_read_b32 v55, a63\n"
"v_lshlrev_b32 v48, 16, v49\n"
"v_and_b32 v49, v224, v49\n"
"v_lshlrev_b32 v50, 16, v51\n"
"v_and_b32 v51, v224, v51\n"
"v_lshlrev_b32 v52, 16, v53\n"
"v_and_b32 v53, v224, v53\n"
"v_lshlrev_b32 v54, 16, v55\n"
"v_and_b32 v55, v224, v55\n"
"v_accvgpr_read_b32 v57, a96\n"
"v_accvgpr_read_b32 v59, a97\n"
"v_accvgpr_read_b32 v61, a98\n"
"v_accvgpr_read_b32 v63, a99\n"
"v_lshlrev_b32 v56, 16, v57\n"
"v_and_b32 v57, v224, v57\n"
"v_lshlrev_b32 v58, 16, v59\n"
"v_and_b32 v59, v224, v59\n"
"v_lshlrev_b32 v60, 16, v61\n"
"v_and_b32 v61, v224, v61\n"
"v_lshlrev_b32 v62, 16, v63\n"
"v_and_b32 v63, v224, v63\n"
"v_accvgpr_read_b32 v65, a64\n"
"v_accvgpr_read_b32 v67, a65\n"
"v_accvgpr_read_b32 v69, a66\n"
"v_accvgpr_read_b32 v71, a67\n"
"v_lshlrev_b32 v64, 16, v65\n"
"v_and_b32 v65, v224, v65\n"
"v_lshlrev_b32 v66, 16, v67\n"
"v_and_b32 v67, v224, v67\n"
"v_lshlrev_b32 v68, 16, v69\n"
"v_and_b32 v69, v224, v69\n"
"v_lshlrev_b32 v70, 16, v71\n"
"v_and_b32 v71, v224, v71\n"
"v_accvgpr_read_b32 v73, a100\n"
"v_accvgpr_read_b32 v75, a101\n"
"v_accvgpr_read_b32 v77, a102\n"
"v_accvgpr_read_b32 v79, a103\n"
"v_lshlrev_b32 v72, 16, v73\n"
"v_and_b32 v73, v224, v73\n"
"v_lshlrev_b32 v74, 16, v75\n"
"v_and_b32 v75, v224, v75\n"
"v_lshlrev_b32 v76, 16, v77\n"
"v_and_b32 v77, v224, v77\n"
"v_lshlrev_b32 v78, 16, v79\n"
"v_and_b32 v79, v224, v79\n"
"v_accvgpr_read_b32 v81, a68\n"
"v_accvgpr_read_b32 v83, a69\n"
"v_accvgpr_read_b32 v85, a70\n"
"v_accvgpr_read_b32 v87, a71\n"
"v_lshlrev_b32 v80, 16, v81\n"
"v_and_b32 v81, v224, v81\n"
"v_lshlrev_b32 v82, 16, v83\n"
"v_and_b32 v83, v224, v83\n"
"v_lshlrev_b32 v84, 16, v85\n"
"v_and_b32 v85, v224, v85\n"
"v_lshlrev_b32 v86, 16, v87\n"
"v_and_b32 v87, v224, v87\n"
"v_accvgpr_read_b32 v89, a104\n"
"v_accvgpr_read_b32 v91, a105\n"
"v_accvgpr_read_b32 v93, a106\n"
"v_accvgpr_read_b32 v95, a107\n"
"v_lshlrev_b32 v88, 16, v89\n"
"v_and_b32 v89, v224, v89\n"
"v_lshlrev_b32 v90, 16, v91\n"
"v_and_b32 v91, v224, v91\n"
"v_lshlrev_b32 v92, 16, v93\n"
"v_and_b32 v93, v224, v93\n"
"v_lshlrev_b32 v94, 16, v95\n"
"v_and_b32 v95, v224, v95\n"
"v_accvgpr_read_b32 v97, a72\n"
"v_accvgpr_read_b32 v99, a73\n"
"v_accvgpr_read_b32 v101, a74\n"
"v_accvgpr_read_b32 v103, a75\n"
"v_lshlrev_b32 v96, 16, v97\n"
"v_and_b32 v97, v224, v97\n"
"v_lshlrev_b32 v98, 16, v99\n"
"v_and_b32 v99, v224, v99\n"
"v_lshlrev_b32 v100, 16, v101\n"
"v_and_b32 v101, v224, v101\n"
"v_lshlrev_b32 v102, 16, v103\n"
"v_and_b32 v103, v224, v103\n"
"v_accvgpr_read_b32 v105, a108\n"
"v_accvgpr_read_b32 v107, a109\n"
"v_accvgpr_read_b32 v109, a110\n"
"v_accvgpr_read_b32 v111, a111\n"
"v_lshlrev_b32 v104, 16, v105\n"
"v_and_b32 v105, v224, v105\n"
"v_lshlrev_b32 v106, 16, v107\n"
"v_and_b32 v107, v224, v107\n"
"v_lshlrev_b32 v108, 16, v109\n"
"v_and_b32 v109, v224, v109\n"
"v_lshlrev_b32 v110, 16, v111\n"
"v_and_b32 v111, v224, v111\n"
"v_accvgpr_read_b32 v113, a76\n"
"v_accvgpr_read_b32 v115, a77\n"
"v_accvgpr_read_b32 v117, a78\n"
"v_accvgpr_read_b32 v119, a79\n"
"v_lshlrev_b32 v112, 16, v113\n"
"v_and_b32 v113, v224, v113\n"
"v_lshlrev_b32 v114, 16, v115\n"
"v_and_b32 v115, v224, v115\n"
"v_lshlrev_b32 v116, 16, v117\n"
"v_and_b32 v117, v224, v117\n"
"v_lshlrev_b32 v118, 16, v119\n"
"v_and_b32 v119, v224, v119\n"
"v_accvgpr_read_b32 v121, a112\n"
"v_accvgpr_read_b32 v123, a113\n"
"v_accvgpr_read_b32 v125, a114\n"
"v_accvgpr_read_b32 v127, a115\n"
"v_lshlrev_b32 v120, 16, v121\n"
"v_and_b32 v121, v224, v121\n"
"v_lshlrev_b32 v122, 16, v123\n"
"v_and_b32 v123, v224, v123\n"
"v_lshlrev_b32 v124, 16, v125\n"
"v_and_b32 v125, v224, v125\n"
"v_lshlrev_b32 v126, 16, v127\n"
"v_and_b32 v127, v224, v127\n"
"v_accvgpr_read_b32 v129, a80\n"
"v_accvgpr_read_b32 v131, a81\n"
"v_accvgpr_read_b32 v133, a82\n"
"v_accvgpr_read_b32 v135, a83\n"
"v_lshlrev_b32 v128, 16, v129\n"
"v_and_b32 v129, v224, v129\n"
"v_lshlrev_b32 v130, 16, v131\n"
"v_and_b32 v131, v224, v131\n"
"v_lshlrev_b32 v132, 16, v133\n"
"v_and_b32 v133, v224, v133\n"
"v_lshlrev_b32 v134, 16, v135\n"
"v_and_b32 v135, v224, v135\n"
"v_accvgpr_read_b32 v137, a116\n"
"v_accvgpr_read_b32 v139, a117\n"
"v_accvgpr_read_b32 v141, a118\n"
"v_accvgpr_read_b32 v143, a119\n"
"v_lshlrev_b32 v136, 16, v137\n"
"v_and_b32 v137, v224, v137\n"
"v_lshlrev_b32 v138, 16, v139\n"
"v_and_b32 v139, v224, v139\n"
"v_lshlrev_b32 v140, 16, v141\n"
"v_and_b32 v141, v224, v141\n"
"v_lshlrev_b32 v142, 16, v143\n"
"v_and_b32 v143, v224, v143\n"
"v_lshrrev_b32 v242, 2, v248\n"
"v_lshlrev_b32 v241, 4, v242\n"
"v_lshlrev_b32 v242, 3, v242\n"
"v_lshrrev_b32 v243, 6, v254\n"
"v_mul_u32_u24 v243, 24576, v243\n"
"v_and_b32 v247, 7, v248\n"
"v_lshl_add_u32 v243, v247, 4, v243\n"
"v_and_b32 v247, 3, v248\n"
"v_and_b32 v244, 0x10000000, v254\n"
"v_or_b32 v243, v244, v243\n"
"v_lshrrev_b32 v244, 5, v248\n"
"v_mul_u32_u24 v244, 96, v244\n"
"v_and_b32 v246, 16, v248\n"
"v_subrev_u32 v252, 65536, v227\n"
"v_add3_u32 v244, v244, v246, v252\n"
"v_lshl_add_u32 v244, v247, 5, v244\n"
"s_mul_i32 s63, s41, 384\n"
"v_add_u32 v244, s63, v244\n"
"v_lshl_add_u32 v249, v248, 2, v252\n"
"s_lshl_b32 s63, s41, 8\n"
"v_add_u32 v251, s63, v249\n"
"v_add_u32 v252, s63, v252\n"
"s_add_u32 s63, s40, 131072\n"
"v_lshl_add_u32 v255, v253, 3, s63\n"
"s_min_u32 s63, s41, 2\n"
"s_mul_i32 s63, s63, 192\n"
"v_add_u32 v183, s63, v248\n"
"v_mul_u32_u24 v183, 911, v183\n"
"s_lshl_b32 s63, s55, 3\n"
"v_lshrrev_b32 v180, 16, v183\n"
"v_add_u32 v180, s63, v180\n"
"v_add_u32 v183, 58304, v183\n"
"v_lshrrev_b32 v181, 16, v183\n"
"v_add_u32 v181, s63, v181\n"
"v_add_u32 v183, 58304, v183\n"
"v_lshrrev_b32 v182, 16, v183\n"
"v_add_u32 v182, s63, v182\n"
"v_lshlrev_b32 v250, 4, v248\n"
"v_add_u32 v184, s45, v250\n"
"s_cmp_eq_u32 s41, 3\n"
"s_cselect_b32 s63, 0x10000000, 0\n"
"v_or_b32 v250, s63, v250\n"
"AT_B3:\n"
"s_waitcnt lgkmcnt(0)\n"
"s_barrier\n"
"ds_read_b64 v[246:247], v227 offset:656\n"
"s_mul_i32 s58, s41, 1152\n"
"s_add_u32 s58, s58, 37888000\n"
"s_waitcnt lgkmcnt(0)\n"
"v_readfirstlane_b32 s10, v246\n"
"v_readfirstlane_b32 s11, v247\n"
"s_nop 0\n"
"s_andn2_b32 s12, 0x7ff, s10\n"
"s_andn2_b32 s13, 0x7ff, s11\n"
"s_mul_i32 s56, s12, 9216\n"
"s_mul_i32 s57, s13, 9216\n"
"s_add_u32 s56, s56, s58\n"
"s_add_u32 s57, s57, s58\n"
"s_cmp_ge_u32 s42, 192\n"
"s_cbranch_scc1 AT_A4\n"
"buffer_load_dwordx4 v[144:147], v241, s[28:31], s56 offen offset:0 sc1\n"
"buffer_load_dwordx4 v[148:151], v241, s[28:31], s56 offen offset:256 sc1\n"
"buffer_load_dwordx4 v[152:155], v241, s[28:31], s56 offen offset:512 sc1\n"
"buffer_load_dwordx4 v[156:159], v241, s[28:31], s56 offen offset:768 sc1\n"
"buffer_load_dwordx2 v[160:161], v242, s[28:31], s56 offen offset:1024 sc1\n"
"buffer_load_dwordx4 v[162:165], v241, s[28:31], s57 offen offset:0 sc1\n"
"buffer_load_dwordx4 v[166:169], v241, s[28:31], s57 offen offset:256 sc1\n"
"buffer_load_dwordx4 v[170:173], v241, s[28:31], s57 offen offset:512 sc1\n"
"buffer_load_dwordx4 v[174:177], v241, s[28:31], s57 offen offset:768 sc1\n"
"buffer_load_dwordx2 v[178:179], v242, s[28:31], s57 offen offset:1024 sc1\n"
"AT_A4:\n"
"ds_read_b128 v[228:231], v184 offset:0\n"
"ds_read_b128 v[232:235], v184 offset:1024\n"
"ds_read_b128 v[236:239], v184 offset:2048\n"
"v_cmp_eq_u32 vcc, s10, v225\n"
"v_cmp_eq_u32 s[58:59], s11, v225\n"
"s_nop 0\n"
"s_or_b64 s[58:59], s[58:59], vcc\n"
"v_cmp_ne_u32 vcc, s12, v180\n"
"v_cmp_ne_u32 s[60:61], s13, v180\n"
"s_waitcnt lgkmcnt(2)\n"
"s_and_b64 exec, vcc, s[60:61]\n"
"buffer_store_dwordx4 v[228:231], v250, s[76:79], s46 offen offset:0\n"
"s_mov_b64 exec, -1\n"
"v_cmp_ne_u32 vcc, s12, v181\n"
"v_cmp_ne_u32 s[60:61], s13, v181\n"
"s_waitcnt lgkmcnt(1)\n"
"s_and_b64 exec, vcc, s[60:61]\n"
"buffer_store_dwordx4 v[232:235], v250, s[76:79], s46 offen offset:1024\n"
"s_mov_b64 exec, -1\n"
"v_cmp_ne_u32 vcc, s12, v182\n"
"v_cmp_ne_u32 s[60:61], s13, v182\n"
"s_waitcnt lgkmcnt(0)\n"
"s_and_b64 exec, vcc, s[60:61]\n"
"buffer_store_dwordx4 v[236:239], v250, s[76:79], s46 offen offset:2048\n"
"s_mov_b64 exec, -1\n"
"s_and_b64 exec, s[8:9], s[58:59]\n"
"s_cbranch_execz AT_A5\n"
"s_add_u32 s60, s48, 4608\n"
"v_mov_b32 v181, s85\n"
"v_mov_b32 v183, s85\n"
"v_mov_b32 v185, s85\n"
"v_mov_b32 v187, s85\n"
"v_mov_b32 v189, s85\n"
"v_mov_b32 v191, s85\n"
"v_mov_b32 v193, s85\n"
"v_mov_b32 v195, s85\n"
"v_mov_b32 v180, v200\n"
"v_mov_b32 v182, v201\n"
"buffer_store_dwordx4 v[180:183], v245, s[28:31], s60 offen offset:0 sc1\n"
"v_mov_b32 v184, v202\n"
"v_mov_b32 v186, v203\n"
"buffer_store_dwordx4 v[184:187], v245, s[28:31], s60 offen offset:16 sc1\n"
"v_mov_b32 v188, v204\n"
"v_mov_b32 v190, v205\n"
"buffer_store_dwordx4 v[188:191], v245, s[28:31], s60 offen offset:128 sc1\n"
"v_mov_b32 v192, v206\n"
"v_mov_b32 v194, v207\n"
"buffer_store_dwordx4 v[192:195], v245, s[28:31], s60 offen offset:144 sc1\n"
"v_mov_b32 v180, v208\n"
"v_mov_b32 v182, v209\n"
"buffer_store_dwordx4 v[180:183], v245, s[28:31], s60 offen offset:1536 sc1\n"
"v_mov_b32 v184, v210\n"
"v_mov_b32 v186, v211\n"
"buffer_store_dwordx4 v[184:187], v245, s[28:31], s60 offen offset:1552 sc1\n"
"v_mov_b32 v188, v212\n"
"v_mov_b32 v190, v213\n"
"buffer_store_dwordx4 v[188:191], v245, s[28:31], s60 offen offset:1664 sc1\n"
"v_mov_b32 v192, v214\n"
"v_mov_b32 v194, v215\n"
"buffer_store_dwordx4 v[192:195], v245, s[28:31], s60 offen offset:1680 sc1\n"
"v_mov_b32 v180, v216\n"
"v_mov_b32 v182, v217\n"
"buffer_store_dwordx4 v[180:183], v245, s[28:31], s60 offen offset:3072 sc1\n"
"v_mov_b32 v184, v218\n"
"v_mov_b32 v186, v219\n"
"buffer_store_dwordx4 v[184:187], v245, s[28:31], s60 offen offset:3088 sc1\n"
"v_mov_b32 v188, v220\n"
"v_mov_b32 v190, v221\n"
"buffer_store_dwordx4 v[188:191], v245, s[28:31], s60 offen offset:3200 sc1\n"
"v_mov_b32 v192, v222\n"
"v_mov_b32 v194, v223\n"
"buffer_store_dwordx4 v[192:195], v245, s[28:31], s60 offen offset:3216 sc1\n"
"AT_A5:\n"
"s_mov_b64 exec, -1\n"
"s_cmp_ge_u32 s42, 192\n"
"s_cbranch_scc1 AT_F0\n"
"s_mov_b32 s87, 0x80000\n"
"s_waitcnt vmcnt(3)\n"
"AT_A6:\n"
"v_xor_b32 v212, s85, v145\n"
"v_xor_b32 v213, s85, v147\n"
"v_bitop3_b32 v212, v149, s85, v212 bitop3:0xbe\n"
"v_bitop3_b32 v213, v151, s85, v213 bitop3:0xbe\n"
"v_bitop3_b32 v212, v153, s85, v212 bitop3:0xbe\n"
"v_bitop3_b32 v213, v155, s85, v213 bitop3:0xbe\n"
"v_bitop3_b32 v212, v157, s85, v212 bitop3:0xbe\n"
"v_bitop3_b32 v213, v159, s85, v213 bitop3:0xbe\n"
"v_bitop3_b32 v212, v161, s85, v212 bitop3:0xbe\n"
"v_bitop3_b32 v213, v163, s85, v213 bitop3:0xbe\n"
"v_bitop3_b32 v212, v165, s85, v212 bitop3:0xbe\n"
"v_bitop3_b32 v213, v167, s85, v213 bitop3:0xbe\n"
"v_bitop3_b32 v212, v169, s85, v212 bitop3:0xbe\n"
"v_bitop3_b32 v213, v171, s85, v213 bitop3:0xbe\n"
"v_bitop3_b32 v212, v173, s85, v212 bitop3:0xbe\n"
"v_bitop3_b32 v213, v175, s85, v213 bitop3:0xbe\n"
"v_bitop3_b32 v212, v177, s85, v212 bitop3:0xbe\n"
"v_bitop3_b32 v213, v179, s85, v213 bitop3:0xbe\n"
"v_or_b32 v212, v212, v213\n"
"v_cmp_ne_u32 vcc, 0, v212\n"
"s_nop 0\n"
"s_cbranch_vccz AT_A7\n"
"s_sub_u32 s87, s87, 1\n"
"s_cmp_eq_u32 s87, 0\n"
"s_cbranch_scc1 AT_A7\n"
"s_sleep 1\n"
"buffer_load_dwordx4 v[144:147], v241, s[28:31], s56 offen offset:0 sc1\n"
"buffer_load_dwordx4 v[148:151], v241, s[28:31], s56 offen offset:256 sc1\n"
"buffer_load_dwordx4 v[152:155], v241, s[28:31], s56 offen offset:512 sc1\n"
"buffer_load_dwordx4 v[156:159], v241, s[28:31], s56 offen offset:768 sc1\n"
"buffer_load_dwordx2 v[160:161], v242, s[28:31], s56 offen offset:1024 sc1\n"
"buffer_load_dwordx4 v[162:165], v241, s[28:31], s57 offen offset:0 sc1\n"
"buffer_load_dwordx4 v[166:169], v241, s[28:31], s57 offen offset:256 sc1\n"
"buffer_load_dwordx4 v[170:173], v241, s[28:31], s57 offen offset:512 sc1\n"
"buffer_load_dwordx4 v[174:177], v241, s[28:31], s57 offen offset:768 sc1\n"
"buffer_load_dwordx2 v[178:179], v242, s[28:31], s57 offen offset:1024 sc1\n"
"s_waitcnt vmcnt(0)\n"
"s_branch AT_A6\n"
"AT_A7:\n"
"v_pk_mul_f32 v[180:181], v[0:1], v[144:145] op_sel_hi:[1,0]\n"
"v_pk_mul_f32 v[182:183], v[2:3], v[144:145] op_sel_hi:[1,0]\n"
"v_pk_mul_f32 v[184:185], v[4:5], v[144:145] op_sel_hi:[1,0]\n"
"v_pk_mul_f32 v[186:187], v[6:7], v[144:145] op_sel_hi:[1,0]\n"
"v_pk_mul_f32 v[196:197], v[8:9], v[144:145] op_sel_hi:[1,0]\n"
"v_pk_mul_f32 v[198:199], v[10:11], v[144:145] op_sel_hi:[1,0]\n"
"v_pk_mul_f32 v[200:201], v[12:13], v[144:145] op_sel_hi:[1,0]\n"
"v_pk_mul_f32 v[202:203], v[14:15], v[144:145] op_sel_hi:[1,0]\n"
"v_pk_mul_f32 v[188:189], v[0:1], v[162:163] op_sel_hi:[1,0]\n"
"v_pk_mul_f32 v[190:191], v[2:3], v[162:163] op_sel_hi:[1,0]\n"
"v_pk_mul_f32 v[192:193], v[4:5], v[162:163] op_sel_hi:[1,0]\n"
"v_pk_mul_f32 v[194:195], v[6:7], v[162:163] op_sel_hi:[1,0]\n"
"v_pk_mul_f32 v[204:205], v[8:9], v[162:163] op_sel_hi:[1,0]\n"
"v_pk_mul_f32 v[206:207], v[10:11], v[162:163] op_sel_hi:[1,0]\n"
"v_pk_mul_f32 v[208:209], v[12:13], v[162:163] op_sel_hi:[1,0]\n"
"v_pk_mul_f32 v[210:211], v[14:15], v[162:163] op_sel_hi:[1,0]\n"
"ds_read_b64 v[2:3], v255 offset:0\n"
"ds_read_b64 v[6:7], v255 offset:1152\n"
"ds_read_b64 v[10:11], v255 offset:2304\n"
"ds_read_b64 v[14:15], v255 offset:3456\n"
"v_pk_fma_f32 v[180:181], v[16:17], v[146:147], v[180:181] op_sel_hi:[1,0,1]\n"
"v_pk_fma_f32 v[182:183], v[18:19], v[146:147], v[182:183] op_sel_hi:[1,0,1]\n"
"v_pk_fma_f32 v[184:185], v[20:21], v[146:147], v[184:185] op_sel_hi:[1,0,1]\n"
"v_pk_fma_f32 v[186:187], v[22:23], v[146:147], v[186:187] op_sel_hi:[1,0,1]\n"
"v_pk_fma_f32 v[196:197], v[24:25], v[146:147], v[196:197] op_sel_hi:[1,0,1]\n"
"v_pk_fma_f32 v[198:199], v[26:27], v[146:147], v[198:199] op_sel_hi:[1,0,1]\n"
"v_pk_fma_f32 v[200:201], v[28:29], v[146:147], v[200:201] op_sel_hi:[1,0,1]\n"
"v_pk_fma_f32 v[202:203], v[30:31], v[146:147], v[202:203] op_sel_hi:[1,0,1]\n"
"v_pk_fma_f32 v[188:189], v[16:17], v[164:165], v[188:189] op_sel_hi:[1,0,1]\n"
"v_pk_fma_f32 v[190:191], v[18:19], v[164:165], v[190:191] op_sel_hi:[1,0,1]\n"
"v_pk_fma_f32 v[192:193], v[20:21], v[164:165], v[192:193] op_sel_hi:[1,0,1]\n"
"v_pk_fma_f32 v[194:195], v[22:23], v[164:165], v[194:195] op_sel_hi:[1,0,1]\n"
"v_pk_fma_f32 v[204:205], v[24:25], v[164:165], v[204:205] op_sel_hi:[1,0,1]\n"
"v_pk_fma_f32 v[206:207], v[26:27], v[164:165], v[206:207] op_sel_hi:[1,0,1]\n"
"v_pk_fma_f32 v[208:209], v[28:29], v[164:165], v[208:209] op_sel_hi:[1,0,1]\n"
"v_pk_fma_f32 v[210:211], v[30:31], v[164:165], v[210:211] op_sel_hi:[1,0,1]\n"
"ds_read_b64 v[18:19], v255 offset:4608\n"
"ds_read_b64 v[22:23], v255 offset:5760\n"
"ds_read_b64 v[26:27], v255 offset:6912\n"
"ds_read_b64 v[30:31], v255 offset:8064\n"
"v_pk_fma_f32 v[180:181], v[32:33], v[148:149], v[180:181] op_sel_hi:[1,0,1]\n"
"v_pk_fma_f32 v[182:183], v[34:35], v[148:149], v[182:183] op_sel_hi:[1,0,1]\n"
"v_pk_fma_f32 v[184:185], v[36:37], v[148:149], v[184:185] op_sel_hi:[1,0,1]\n"
"v_pk_fma_f32 v[186:187], v[38:39], v[148:149], v[186:187] op_sel_hi:[1,0,1]\n"
"v_pk_fma_f32 v[196:197], v[40:41], v[148:149], v[196:197] op_sel_hi:[1,0,1]\n"
"v_pk_fma_f32 v[198:199], v[42:43], v[148:149], v[198:199] op_sel_hi:[1,0,1]\n"
"v_pk_fma_f32 v[200:201], v[44:45], v[148:149], v[200:201] op_sel_hi:[1,0,1]\n"
"v_pk_fma_f32 v[202:203], v[46:47], v[148:149], v[202:203] op_sel_hi:[1,0,1]\n"
"v_pk_fma_f32 v[188:189], v[32:33], v[166:167], v[188:189] op_sel_hi:[1,0,1]\n"
"v_pk_fma_f32 v[190:191], v[34:35], v[166:167], v[190:191] op_sel_hi:[1,0,1]\n"
"v_pk_fma_f32 v[192:193], v[36:37], v[166:167], v[192:193] op_sel_hi:[1,0,1]\n"
"v_pk_fma_f32 v[194:195], v[38:39], v[166:167], v[194:195] op_sel_hi:[1,0,1]\n"
"v_pk_fma_f32 v[204:205], v[40:41], v[166:167], v[204:205] op_sel_hi:[1,0,1]\n"
"v_pk_fma_f32 v[206:207], v[42:43], v[166:167], v[206:207] op_sel_hi:[1,0,1]\n"
"v_pk_fma_f32 v[208:209], v[44:45], v[166:167], v[208:209] op_sel_hi:[1,0,1]\n"
"v_pk_fma_f32 v[210:211], v[46:47], v[166:167], v[210:211] op_sel_hi:[1,0,1]\n"
"ds_read_b64 v[34:35], v255 offset:9216\n"
"ds_read_b64 v[38:39], v255 offset:10368\n"
"ds_read_b64 v[42:43], v255 offset:11520\n"
"ds_read_b64 v[46:47], v255 offset:12672\n"
"v_pk_fma_f32 v[180:181], v[48:49], v[150:151], v[180:181] op_sel_hi:[1,0,1]\n"
"v_pk_fma_f32 v[182:183], v[50:51], v[150:151], v[182:183] op_sel_hi:[1,0,1]\n"
"v_pk_fma_f32 v[184:185], v[52:53], v[150:151], v[184:185] op_sel_hi:[1,0,1]\n"
"v_pk_fma_f32 v[186:187], v[54:55], v[150:151], v[186:187] op_sel_hi:[1,0,1]\n"
"v_pk_fma_f32 v[196:197], v[56:57], v[150:151], v[196:197] op_sel_hi:[1,0,1]\n"
"v_pk_fma_f32 v[198:199], v[58:59], v[150:151], v[198:199] op_sel_hi:[1,0,1]\n"
"v_pk_fma_f32 v[200:201], v[60:61], v[150:151], v[200:201] op_sel_hi:[1,0,1]\n"
"v_pk_fma_f32 v[202:203], v[62:63], v[150:151], v[202:203] op_sel_hi:[1,0,1]\n"
"v_pk_fma_f32 v[188:189], v[48:49], v[168:169], v[188:189] op_sel_hi:[1,0,1]\n"
"v_pk_fma_f32 v[190:191], v[50:51], v[168:169], v[190:191] op_sel_hi:[1,0,1]\n"
"v_pk_fma_f32 v[192:193], v[52:53], v[168:169], v[192:193] op_sel_hi:[1,0,1]\n"
"v_pk_fma_f32 v[194:195], v[54:55], v[168:169], v[194:195] op_sel_hi:[1,0,1]\n"
"v_pk_fma_f32 v[204:205], v[56:57], v[168:169], v[204:205] op_sel_hi:[1,0,1]\n"
"v_pk_fma_f32 v[206:207], v[58:59], v[168:169], v[206:207] op_sel_hi:[1,0,1]\n"
"v_pk_fma_f32 v[208:209], v[60:61], v[168:169], v[208:209] op_sel_hi:[1,0,1]\n"
"v_pk_fma_f32 v[210:211], v[62:63], v[168:169], v[210:211] op_sel_hi:[1,0,1]\n"
"ds_read_b64 v[50:51], v255 offset:13824\n"
"ds_read_b64 v[54:55], v255 offset:14976\n"
"ds_read_b64 v[58:59], v255 offset:16128\n"
"ds_read_b64 v[62:63], v255 offset:17280\n"
"v_pk_fma_f32 v[180:181], v[64:65], v[152:153], v[180:181] op_sel_hi:[1,0,1]\n"
"v_pk_fma_f32 v[182:183], v[66:67], v[152:153], v[182:183] op_sel_hi:[1,0,1]\n"
"v_pk_fma_f32 v[184:185], v[68:69], v[152:153], v[184:185] op_sel_hi:[1,0,1]\n"
"v_pk_fma_f32 v[186:187], v[70:71], v[152:153], v[186:187] op_sel_hi:[1,0,1]\n"
"v_pk_fma_f32 v[196:197], v[72:73], v[152:153], v[196:197] op_sel_hi:[1,0,1]\n"
"v_pk_fma_f32 v[198:199], v[74:75], v[152:153], v[198:199] op_sel_hi:[1,0,1]\n"
"v_pk_fma_f32 v[200:201], v[76:77], v[152:153], v[200:201] op_sel_hi:[1,0,1]\n"
"v_pk_fma_f32 v[202:203], v[78:79], v[152:153], v[202:203] op_sel_hi:[1,0,1]\n"
"v_pk_fma_f32 v[188:189], v[64:65], v[170:171], v[188:189] op_sel_hi:[1,0,1]\n"
"v_pk_fma_f32 v[190:191], v[66:67], v[170:171], v[190:191] op_sel_hi:[1,0,1]\n"
"v_pk_fma_f32 v[192:193], v[68:69], v[170:171], v[192:193] op_sel_hi:[1,0,1]\n"
"v_pk_fma_f32 v[194:195], v[70:71], v[170:171], v[194:195] op_sel_hi:[1,0,1]\n"
"v_pk_fma_f32 v[204:205], v[72:73], v[170:171], v[204:205] op_sel_hi:[1,0,1]\n"
"v_pk_fma_f32 v[206:207], v[74:75], v[170:171], v[206:207] op_sel_hi:[1,0,1]\n"
"v_pk_fma_f32 v[208:209], v[76:77], v[170:171], v[208:209] op_sel_hi:[1,0,1]\n"
"v_pk_fma_f32 v[210:211], v[78:79], v[170:171], v[210:211] op_sel_hi:[1,0,1]\n"
"ds_read_b64 v[66:67], v255 offset:18432\n"
"ds_read_b64 v[70:71], v255 offset:19584\n"
"ds_read_b64 v[74:75], v255 offset:20736\n"
"ds_read_b64 v[78:79], v255 offset:21888\n"
"v_pk_fma_f32 v[180:181], v[80:81], v[154:155], v[180:181] op_sel_hi:[1,0,1]\n"
"v_pk_fma_f32 v[182:183], v[82:83], v[154:155], v[182:183] op_sel_hi:[1,0,1]\n"
"v_pk_fma_f32 v[184:185], v[84:85], v[154:155], v[184:185] op_sel_hi:[1,0,1]\n"
"v_pk_fma_f32 v[186:187], v[86:87], v[154:155], v[186:187] op_sel_hi:[1,0,1]\n"
"v_pk_fma_f32 v[196:197], v[88:89], v[154:155], v[196:197] op_sel_hi:[1,0,1]\n"
"v_pk_fma_f32 v[198:199], v[90:91], v[154:155], v[198:199] op_sel_hi:[1,0,1]\n"
"v_pk_fma_f32 v[200:201], v[92:93], v[154:155], v[200:201] op_sel_hi:[1,0,1]\n"
"v_pk_fma_f32 v[202:203], v[94:95], v[154:155], v[202:203] op_sel_hi:[1,0,1]\n"
"v_pk_fma_f32 v[188:189], v[80:81], v[172:173], v[188:189] op_sel_hi:[1,0,1]\n"
"v_pk_fma_f32 v[190:191], v[82:83], v[172:173], v[190:191] op_sel_hi:[1,0,1]\n"
"v_pk_fma_f32 v[192:193], v[84:85], v[172:173], v[192:193] op_sel_hi:[1,0,1]\n"
"v_pk_fma_f32 v[194:195], v[86:87], v[172:173], v[194:195] op_sel_hi:[1,0,1]\n"
"v_pk_fma_f32 v[204:205], v[88:89], v[172:173], v[204:205] op_sel_hi:[1,0,1]\n"
"v_pk_fma_f32 v[206:207], v[90:91], v[172:173], v[206:207] op_sel_hi:[1,0,1]\n"
"v_pk_fma_f32 v[208:209], v[92:93], v[172:173], v[208:209] op_sel_hi:[1,0,1]\n"
"v_pk_fma_f32 v[210:211], v[94:95], v[172:173], v[210:211] op_sel_hi:[1,0,1]\n"
"ds_read_b64 v[82:83], v255 offset:23040\n"
"ds_read_b64 v[86:87], v255 offset:24192\n"
"ds_read_b64 v[90:91], v255 offset:25344\n"
"ds_read_b64 v[94:95], v255 offset:26496\n"
"v_pk_fma_f32 v[180:181], v[96:97], v[156:157], v[180:181] op_sel_hi:[1,0,1]\n"
"v_pk_fma_f32 v[182:183], v[98:99], v[156:157], v[182:183] op_sel_hi:[1,0,1]\n"
"v_pk_fma_f32 v[184:185], v[100:101], v[156:157], v[184:185] op_sel_hi:[1,0,1]\n"
"v_pk_fma_f32 v[186:187], v[102:103], v[156:157], v[186:187] op_sel_hi:[1,0,1]\n"
"v_pk_fma_f32 v[196:197], v[104:105], v[156:157], v[196:197] op_sel_hi:[1,0,1]\n"
"v_pk_fma_f32 v[198:199], v[106:107], v[156:157], v[198:199] op_sel_hi:[1,0,1]\n"
"v_pk_fma_f32 v[200:201], v[108:109], v[156:157], v[200:201] op_sel_hi:[1,0,1]\n"
"v_pk_fma_f32 v[202:203], v[110:111], v[156:157], v[202:203] op_sel_hi:[1,0,1]\n"
"v_pk_fma_f32 v[188:189], v[96:97], v[174:175], v[188:189] op_sel_hi:[1,0,1]\n"
"v_pk_fma_f32 v[190:191], v[98:99], v[174:175], v[190:191] op_sel_hi:[1,0,1]\n"
"v_pk_fma_f32 v[192:193], v[100:101], v[174:175], v[192:193] op_sel_hi:[1,0,1]\n"
"v_pk_fma_f32 v[194:195], v[102:103], v[174:175], v[194:195] op_sel_hi:[1,0,1]\n"
"v_pk_fma_f32 v[204:205], v[104:105], v[174:175], v[204:205] op_sel_hi:[1,0,1]\n"
"v_pk_fma_f32 v[206:207], v[106:107], v[174:175], v[206:207] op_sel_hi:[1,0,1]\n"
"v_pk_fma_f32 v[208:209], v[108:109], v[174:175], v[208:209] op_sel_hi:[1,0,1]\n"
"v_pk_fma_f32 v[210:211], v[110:111], v[174:175], v[210:211] op_sel_hi:[1,0,1]\n"
"v_pk_fma_f32 v[180:181], v[112:113], v[158:159], v[180:181] op_sel_hi:[1,0,1]\n"
"v_pk_fma_f32 v[182:183], v[114:115], v[158:159], v[182:183] op_sel_hi:[1,0,1]\n"
"v_pk_fma_f32 v[184:185], v[116:117], v[158:159], v[184:185] op_sel_hi:[1,0,1]\n"
"v_pk_fma_f32 v[186:187], v[118:119], v[158:159], v[186:187] op_sel_hi:[1,0,1]\n"
"v_pk_fma_f32 v[196:197], v[120:121], v[158:159], v[196:197] op_sel_hi:[1,0,1]\n"
"v_pk_fma_f32 v[198:199], v[122:123], v[158:159], v[198:199] op_sel_hi:[1,0,1]\n"
"v_pk_fma_f32 v[200:201], v[124:125], v[158:159], v[200:201] op_sel_hi:[1,0,1]\n"
"v_pk_fma_f32 v[202:203], v[126:127], v[158:159], v[202:203] op_sel_hi:[1,0,1]\n"
"v_pk_fma_f32 v[188:189], v[112:113], v[176:177], v[188:189] op_sel_hi:[1,0,1]\n"
"v_pk_fma_f32 v[190:191], v[114:115], v[176:177], v[190:191] op_sel_hi:[1,0,1]\n"
"v_pk_fma_f32 v[192:193], v[116:117], v[176:177], v[192:193] op_sel_hi:[1,0,1]\n"
"v_pk_fma_f32 v[194:195], v[118:119], v[176:177], v[194:195] op_sel_hi:[1,0,1]\n"
"v_pk_fma_f32 v[204:205], v[120:121], v[176:177], v[204:205] op_sel_hi:[1,0,1]\n"
"v_pk_fma_f32 v[206:207], v[122:123], v[176:177], v[206:207] op_sel_hi:[1,0,1]\n"
"v_pk_fma_f32 v[208:209], v[124:125], v[176:177], v[208:209] op_sel_hi:[1,0,1]\n"
"v_pk_fma_f32 v[210:211], v[126:127], v[176:177], v[210:211] op_sel_hi:[1,0,1]\n"
"v_pk_fma_f32 v[180:181], v[128:129], v[160:161], v[180:181] op_sel_hi:[1,0,1]\n"
"v_pk_fma_f32 v[182:183], v[130:131], v[160:161], v[182:183] op_sel_hi:[1,0,1]\n"
"v_pk_fma_f32 v[184:185], v[132:133], v[160:161], v[184:185] op_sel_hi:[1,0,1]\n"
"v_pk_fma_f32 v[186:187], v[134:135], v[160:161], v[186:187] op_sel_hi:[1,0,1]\n"
"v_pk_fma_f32 v[196:197], v[136:137], v[160:161], v[196:197] op_sel_hi:[1,0,1]\n"
"v_pk_fma_f32 v[198:199], v[138:139], v[160:161], v[198:199] op_sel_hi:[1,0,1]\n"
"v_pk_fma_f32 v[200:201], v[140:141], v[160:161], v[200:201] op_sel_hi:[1,0,1]\n"
"v_pk_fma_f32 v[202:203], v[142:143], v[160:161], v[202:203] op_sel_hi:[1,0,1]\n"
"v_pk_fma_f32 v[188:189], v[128:129], v[178:179], v[188:189] op_sel_hi:[1,0,1]\n"
"v_pk_fma_f32 v[190:191], v[130:131], v[178:179], v[190:191] op_sel_hi:[1,0,1]\n"
"v_pk_fma_f32 v[192:193], v[132:133], v[178:179], v[192:193] op_sel_hi:[1,0,1]\n"
"v_pk_fma_f32 v[194:195], v[134:135], v[178:179], v[194:195] op_sel_hi:[1,0,1]\n"
"v_pk_fma_f32 v[204:205], v[136:137], v[178:179], v[204:205] op_sel_hi:[1,0,1]\n"
"v_pk_fma_f32 v[206:207], v[138:139], v[178:179], v[206:207] op_sel_hi:[1,0,1]\n"
"v_pk_fma_f32 v[208:209], v[140:141], v[178:179], v[208:209] op_sel_hi:[1,0,1]\n"
"v_pk_fma_f32 v[210:211], v[142:143], v[178:179], v[210:211] op_sel_hi:[1,0,1]\n"
"s_waitcnt lgkmcnt(0)\n"
"v_lshlrev_b32 v0, 16, v2\n"
"v_and_b32 v1, v224, v2\n"
"v_permlane32_swap_b32 v180, v188\n"
"v_permlane32_swap_b32 v181, v189\n"
"v_permlane32_swap_b32 v182, v190\n"
"v_permlane32_swap_b32 v183, v191\n"
"v_permlane32_swap_b32 v184, v192\n"
"v_permlane32_swap_b32 v185, v193\n"
"v_permlane32_swap_b32 v186, v194\n"
"v_permlane32_swap_b32 v187, v195\n"
"v_permlane32_swap_b32 v196, v204\n"
"v_permlane32_swap_b32 v197, v205\n"
"v_permlane32_swap_b32 v198, v206\n"
"v_permlane32_swap_b32 v199, v207\n"
"v_permlane32_swap_b32 v200, v208\n"
"v_permlane32_swap_b32 v201, v209\n"
"v_permlane32_swap_b32 v202, v210\n"
"v_permlane32_swap_b32 v203, v211\n"
"v_pk_add_f32 v[180:181], v[180:181], v[188:189]\n"
"v_pk_add_f32 v[182:183], v[182:183], v[190:191]\n"
"v_pk_add_f32 v[184:185], v[184:185], v[192:193]\n"
"v_pk_add_f32 v[186:187], v[186:187], v[194:195]\n"
"v_pk_add_f32 v[196:197], v[196:197], v[204:205]\n"
"v_pk_add_f32 v[198:199], v[198:199], v[206:207]\n"
"v_pk_add_f32 v[200:201], v[200:201], v[208:209]\n"
"v_pk_add_f32 v[202:203], v[202:203], v[210:211]\n"
"v_lshlrev_b32 v2, 16, v3\n"
"v_and_b32 v3, v224, v3\n"
"v_permlane16_swap_b32 v180, v184\n"
"v_permlane16_swap_b32 v181, v185\n"
"v_permlane16_swap_b32 v182, v186\n"
"v_permlane16_swap_b32 v183, v187\n"
"v_permlane16_swap_b32 v196, v200\n"
"v_permlane16_swap_b32 v197, v201\n"
"v_permlane16_swap_b32 v198, v202\n"
"v_permlane16_swap_b32 v199, v203\n"
"v_pk_add_f32 v[180:181], v[180:181], v[184:185]\n"
"v_pk_add_f32 v[182:183], v[182:183], v[186:187]\n"
"v_pk_add_f32 v[196:197], v[196:197], v[200:201]\n"
"v_pk_add_f32 v[198:199], v[198:199], v[202:203]\n"
"v_lshlrev_b32 v4, 16, v6\n"
"v_and_b32 v5, v224, v6\n"
"v_add_f32_dpp v180, v180, v180 row_ror:4 row_mask:0xf bank_mask:0xf\n"
"v_add_f32_dpp v181, v181, v181 row_ror:4 row_mask:0xf bank_mask:0xf\n"
"v_add_f32_dpp v182, v182, v182 row_ror:4 row_mask:0xf bank_mask:0xf\n"
"v_add_f32_dpp v183, v183, v183 row_ror:4 row_mask:0xf bank_mask:0xf\n"
"v_add_f32_dpp v196, v196, v196 row_ror:4 row_mask:0xf bank_mask:0xf\n"
"v_add_f32_dpp v197, v197, v197 row_ror:4 row_mask:0xf bank_mask:0xf\n"
"v_add_f32_dpp v198, v198, v198 row_ror:4 row_mask:0xf bank_mask:0xf\n"
"v_add_f32_dpp v199, v199, v199 row_ror:4 row_mask:0xf bank_mask:0xf\n"
"v_add_f32_dpp v180, v180, v180 row_ror:8 row_mask:0xf bank_mask:0xf\n"
"v_add_f32_dpp v181, v181, v181 row_ror:8 row_mask:0xf bank_mask:0xf\n"
"v_add_f32_dpp v182, v182, v182 row_ror:8 row_mask:0xf bank_mask:0xf\n"
"v_add_f32_dpp v183, v183, v183 row_ror:8 row_mask:0xf bank_mask:0xf\n"
"v_add_f32_dpp v196, v196, v196 row_ror:8 row_mask:0xf bank_mask:0xf\n"
"v_add_f32_dpp v197, v197, v197 row_ror:8 row_mask:0xf bank_mask:0xf\n"
"v_add_f32_dpp v198, v198, v198 row_ror:8 row_mask:0xf bank_mask:0xf\n"
"v_add_f32_dpp v199, v199, v199 row_ror:8 row_mask:0xf bank_mask:0xf\n"
"s_mov_b32 exec_lo, 0x00070007\n"
"s_mov_b32 exec_hi, 0x00070007\n"
"ds_write_b128 v244, v[180:183] offset:2048\n"
"ds_write_b128 v244, v[196:199] offset:2240\n"
"s_mov_b64 exec, -1\n"
"v_lshlrev_b32 v6, 16, v7\n"
"v_and_b32 v7, v224, v7\n"
"v_lshlrev_b32 v8, 16, v10\n"
"v_and_b32 v9, v224, v10\n"
"v_lshlrev_b32 v10, 16, v11\n"
"v_and_b32 v11, v224, v11\n"
"v_lshlrev_b32 v12, 16, v14\n"
"v_and_b32 v13, v224, v14\n"
"v_lshlrev_b32 v14, 16, v15\n"
"v_and_b32 v15, v224, v15\n"
"v_lshlrev_b32 v16, 16, v18\n"
"v_and_b32 v17, v224, v18\n"
"v_lshlrev_b32 v18, 16, v19\n"
"v_and_b32 v19, v224, v19\n"
"v_lshlrev_b32 v20, 16, v22\n"
"v_and_b32 v21, v224, v22\n"
"v_lshlrev_b32 v22, 16, v23\n"
"v_and_b32 v23, v224, v23\n"
"v_lshlrev_b32 v24, 16, v26\n"
"v_and_b32 v25, v224, v26\n"
"v_lshlrev_b32 v26, 16, v27\n"
"v_and_b32 v27, v224, v27\n"
"v_lshlrev_b32 v28, 16, v30\n"
"v_and_b32 v29, v224, v30\n"
"v_lshlrev_b32 v30, 16, v31\n"
"v_and_b32 v31, v224, v31\n"
"v_lshlrev_b32 v32, 16, v34\n"
"v_and_b32 v33, v224, v34\n"
"v_lshlrev_b32 v34, 16, v35\n"
"v_and_b32 v35, v224, v35\n"
"s_waitcnt lgkmcnt(0)\n"
"s_barrier\n"
"s_cmp_eq_u32 s41, 3\n"
"s_cbranch_scc1 AT_F1\n"
"ds_read_b32 v180, v249 offset:2048\n"
"ds_read_b32 v181, v249 offset:2240\n"
"ds_read_b32 v182, v249 offset:2432\n"
"ds_read_b32 v183, v249 offset:2624\n"
"ds_read_b32 v184, v249 offset:2816\n"
"ds_read_b32 v185, v249 offset:3008\n"
"ds_read_b32 v186, v249 offset:3200\n"
"ds_read_b32 v187, v249 offset:3392\n"
"v_lshlrev_b32 v36, 16, v38\n"
"v_and_b32 v37, v224, v38\n"
"v_lshlrev_b32 v38, 16, v39\n"
"v_and_b32 v39, v224, v39\n"
"v_lshlrev_b32 v40, 16, v42\n"
"v_and_b32 v41, v224, v42\n"
"v_lshlrev_b32 v42, 16, v43\n"
"v_and_b32 v43, v224, v43\n"
"v_lshlrev_b32 v44, 16, v46\n"
"v_and_b32 v45, v224, v46\n"
"v_lshlrev_b32 v46, 16, v47\n"
"v_and_b32 v47, v224, v47\n"
"v_lshlrev_b32 v48, 16, v50\n"
"v_and_b32 v49, v224, v50\n"
"v_lshlrev_b32 v50, 16, v51\n"
"v_and_b32 v51, v224, v51\n"
"v_lshlrev_b32 v52, 16, v54\n"
"v_and_b32 v53, v224, v54\n"
"v_lshlrev_b32 v54, 16, v55\n"
"v_and_b32 v55, v224, v55\n"
"v_lshlrev_b32 v56, 16, v58\n"
"v_and_b32 v57, v224, v58\n"
"v_lshlrev_b32 v58, 16, v59\n"
"v_and_b32 v59, v224, v59\n"
"s_waitcnt lgkmcnt(5)\n"
"v_add_f32 v180, v180, v182\n"
"s_waitcnt lgkmcnt(1)\n"
"v_add_f32 v184, v184, v186\n"
"v_add_f32 v181, v181, v183\n"
"v_add_f32 v180, v180, v184\n"
"s_waitcnt lgkmcnt(0)\n"
"v_add_f32 v185, v185, v187\n"
"v_mul_f32 v182, 0xbfb8aa3b, v180\n"
"v_add_f32 v181, v181, v185\n"
"v_exp_f32 v182, v182\n"
"v_mul_f32 v180, v180, v181\n"
"v_add_f32 v182, 1.0, v182\n"
"v_rcp_f32 v182, v182\n"
"v_lshlrev_b32 v60, 16, v62\n"
"v_mul_f32 v180, v180, v182\n"
"ds_write_b32 v251, v180 offset:4096\n"
"ds_read_b128 v[100:103], v252 offset:4096\n"
"ds_read_b128 v[124:127], v252 offset:4192\n"
"ds_read_b128 v[104:107], v252 offset:4112\n"
"ds_read_b128 v[128:131], v252 offset:4208\n"
"ds_read_b128 v[108:111], v252 offset:4128\n"
"ds_read_b128 v[132:135], v252 offset:4224\n"
"ds_read_b128 v[112:115], v252 offset:4144\n"
"ds_read_b128 v[136:139], v252 offset:4240\n"
"ds_read_b128 v[116:119], v252 offset:4160\n"
"ds_read_b128 v[140:143], v252 offset:4256\n"
"ds_read_b128 v[120:123], v252 offset:4176\n"
"ds_read_b128 v[144:147], v252 offset:4272\n"
"s_lshl_b32 s58, s42, 7\n"
"s_add_u32 s58, s58, 56762368\n"
"s_add_u32 s59, s58, 442368\n"
"s_add_u32 s60, s58, 884736\n"
"s_add_u32 s61, s58, 1327104\n"
"v_mov_b32 v161, s85\n"
"v_mov_b32 v163, s85\n"
"v_mov_b32 v165, s85\n"
"v_mov_b32 v167, s85\n"
"v_mov_b32 v169, s85\n"
"v_mov_b32 v171, s85\n"
"v_mov_b32 v173, s85\n"
"v_mov_b32 v175, s85\n"
"v_and_b32 v61, v224, v62\n"
"v_lshlrev_b32 v62, 16, v63\n"
"v_and_b32 v63, v224, v63\n"
"v_lshlrev_b32 v64, 16, v66\n"
"v_and_b32 v65, v224, v66\n"
"v_lshlrev_b32 v66, 16, v67\n"
"v_and_b32 v67, v224, v67\n"
"v_lshlrev_b32 v68, 16, v70\n"
"v_and_b32 v69, v224, v70\n"
"v_lshlrev_b32 v70, 16, v71\n"
"v_and_b32 v71, v224, v71\n"
"v_lshlrev_b32 v72, 16, v74\n"
"v_and_b32 v73, v224, v74\n"
"v_lshlrev_b32 v74, 16, v75\n"
"v_and_b32 v75, v224, v75\n"
"v_lshlrev_b32 v76, 16, v78\n"
"v_and_b32 v77, v224, v78\n"
"v_lshlrev_b32 v78, 16, v79\n"
"v_and_b32 v79, v224, v79\n"
"v_lshlrev_b32 v80, 16, v82\n"
"v_and_b32 v81, v224, v82\n"
"v_lshlrev_b32 v82, 16, v83\n"
"v_and_b32 v83, v224, v83\n"
"v_lshlrev_b32 v84, 16, v86\n"
"v_and_b32 v85, v224, v86\n"
"v_lshlrev_b32 v86, 16, v87\n"
"v_and_b32 v87, v224, v87\n"
"v_lshlrev_b32 v88, 16, v90\n"
"v_and_b32 v89, v224, v90\n"
"v_lshlrev_b32 v90, 16, v91\n"
"v_and_b32 v91, v224, v91\n"
"v_lshlrev_b32 v92, 16, v94\n"
"v_and_b32 v93, v224, v94\n"
"v_lshlrev_b32 v94, 16, v95\n"
"v_and_b32 v95, v224, v95\n"
"s_waitcnt lgkmcnt(11)\n"
"v_pk_mul_f32 v[150:151], v[0:1], v[100:101] op_sel:[0,0] op_sel_hi:[1,0]\n"
"v_pk_mul_f32 v[152:153], v[2:3], v[100:101] op_sel:[0,0] op_sel_hi:[1,0]\n"
"s_waitcnt lgkmcnt(10)\n"
"v_pk_mul_f32 v[154:155], v[0:1], v[124:125] op_sel:[0,0] op_sel_hi:[1,0]\n"
"v_pk_mul_f32 v[156:157], v[2:3], v[124:125] op_sel:[0,0] op_sel_hi:[1,0]\n"
"v_pk_fma_f32 v[150:151], v[4:5], v[100:101], v[150:151] op_sel:[0,1,0] op_sel_hi:[1,1,1]\n"
"v_pk_fma_f32 v[152:153], v[6:7], v[100:101], v[152:153] op_sel:[0,1,0] op_sel_hi:[1,1,1]\n"
"v_pk_fma_f32 v[154:155], v[4:5], v[124:125], v[154:155] op_sel:[0,1,0] op_sel_hi:[1,1,1]\n"
"v_pk_fma_f32 v[156:157], v[6:7], v[124:125], v[156:157] op_sel:[0,1,0] op_sel_hi:[1,1,1]\n"
"v_pk_fma_f32 v[150:151], v[8:9], v[102:103], v[150:151] op_sel:[0,0,0] op_sel_hi:[1,0,1]\n"
"v_pk_fma_f32 v[152:153], v[10:11], v[102:103], v[152:153] op_sel:[0,0,0] op_sel_hi:[1,0,1]\n"
"v_pk_fma_f32 v[154:155], v[8:9], v[126:127], v[154:155] op_sel:[0,0,0] op_sel_hi:[1,0,1]\n"
"v_pk_fma_f32 v[156:157], v[10:11], v[126:127], v[156:157] op_sel:[0,0,0] op_sel_hi:[1,0,1]\n"
"v_pk_fma_f32 v[150:151], v[12:13], v[102:103], v[150:151] op_sel:[0,1,0] op_sel_hi:[1,1,1]\n"
"v_pk_fma_f32 v[152:153], v[14:15], v[102:103], v[152:153] op_sel:[0,1,0] op_sel_hi:[1,1,1]\n"
"v_pk_fma_f32 v[154:155], v[12:13], v[126:127], v[154:155] op_sel:[0,1,0] op_sel_hi:[1,1,1]\n"
"v_pk_fma_f32 v[156:157], v[14:15], v[126:127], v[156:157] op_sel:[0,1,0] op_sel_hi:[1,1,1]\n"
"s_waitcnt lgkmcnt(9)\n"
"v_pk_fma_f32 v[150:151], v[16:17], v[104:105], v[150:151] op_sel:[0,0,0] op_sel_hi:[1,0,1]\n"
"v_pk_fma_f32 v[152:153], v[18:19], v[104:105], v[152:153] op_sel:[0,0,0] op_sel_hi:[1,0,1]\n"
"s_waitcnt lgkmcnt(8)\n"
"v_pk_fma_f32 v[154:155], v[16:17], v[128:129], v[154:155] op_sel:[0,0,0] op_sel_hi:[1,0,1]\n"
"v_pk_fma_f32 v[156:157], v[18:19], v[128:129], v[156:157] op_sel:[0,0,0] op_sel_hi:[1,0,1]\n"
"v_pk_fma_f32 v[150:151], v[20:21], v[104:105], v[150:151] op_sel:[0,1,0] op_sel_hi:[1,1,1]\n"
"v_pk_fma_f32 v[152:153], v[22:23], v[104:105], v[152:153] op_sel:[0,1,0] op_sel_hi:[1,1,1]\n"
"v_pk_fma_f32 v[154:155], v[20:21], v[128:129], v[154:155] op_sel:[0,1,0] op_sel_hi:[1,1,1]\n"
"v_pk_fma_f32 v[156:157], v[22:23], v[128:129], v[156:157] op_sel:[0,1,0] op_sel_hi:[1,1,1]\n"
"v_pk_fma_f32 v[150:151], v[24:25], v[106:107], v[150:151] op_sel:[0,0,0] op_sel_hi:[1,0,1]\n"
"v_pk_fma_f32 v[152:153], v[26:27], v[106:107], v[152:153] op_sel:[0,0,0] op_sel_hi:[1,0,1]\n"
"v_pk_fma_f32 v[154:155], v[24:25], v[130:131], v[154:155] op_sel:[0,0,0] op_sel_hi:[1,0,1]\n"
"v_pk_fma_f32 v[156:157], v[26:27], v[130:131], v[156:157] op_sel:[0,0,0] op_sel_hi:[1,0,1]\n"
"v_pk_fma_f32 v[150:151], v[28:29], v[106:107], v[150:151] op_sel:[0,1,0] op_sel_hi:[1,1,1]\n"
"v_pk_fma_f32 v[152:153], v[30:31], v[106:107], v[152:153] op_sel:[0,1,0] op_sel_hi:[1,1,1]\n"
"v_pk_fma_f32 v[154:155], v[28:29], v[130:131], v[154:155] op_sel:[0,1,0] op_sel_hi:[1,1,1]\n"
"v_pk_fma_f32 v[156:157], v[30:31], v[130:131], v[156:157] op_sel:[0,1,0] op_sel_hi:[1,1,1]\n"
"s_waitcnt lgkmcnt(7)\n"
"v_pk_fma_f32 v[150:151], v[32:33], v[108:109], v[150:151] op_sel:[0,0,0] op_sel_hi:[1,0,1]\n"
"v_pk_fma_f32 v[152:153], v[34:35], v[108:109], v[152:153] op_sel:[0,0,0] op_sel_hi:[1,0,1]\n"
"s_waitcnt lgkmcnt(6)\n"
"v_pk_fma_f32 v[154:155], v[32:33], v[132:133], v[154:155] op_sel:[0,0,0] op_sel_hi:[1,0,1]\n"
"v_pk_fma_f32 v[156:157], v[34:35], v[132:133], v[156:157] op_sel:[0,0,0] op_sel_hi:[1,0,1]\n"
"v_pk_fma_f32 v[150:151], v[36:37], v[108:109], v[150:151] op_sel:[0,1,0] op_sel_hi:[1,1,1]\n"
"v_pk_fma_f32 v[152:153], v[38:39], v[108:109], v[152:153] op_sel:[0,1,0] op_sel_hi:[1,1,1]\n"
"v_pk_fma_f32 v[154:155], v[36:37], v[132:133], v[154:155] op_sel:[0,1,0] op_sel_hi:[1,1,1]\n"
"v_pk_fma_f32 v[156:157], v[38:39], v[132:133], v[156:157] op_sel:[0,1,0] op_sel_hi:[1,1,1]\n"
"v_pk_fma_f32 v[150:151], v[40:41], v[110:111], v[150:151] op_sel:[0,0,0] op_sel_hi:[1,0,1]\n"
"v_pk_fma_f32 v[152:153], v[42:43], v[110:111], v[152:153] op_sel:[0,0,0] op_sel_hi:[1,0,1]\n"
"v_pk_fma_f32 v[154:155], v[40:41], v[134:135], v[154:155] op_sel:[0,0,0] op_sel_hi:[1,0,1]\n"
"v_pk_fma_f32 v[156:157], v[42:43], v[134:135], v[156:157] op_sel:[0,0,0] op_sel_hi:[1,0,1]\n"
"v_pk_fma_f32 v[150:151], v[44:45], v[110:111], v[150:151] op_sel:[0,1,0] op_sel_hi:[1,1,1]\n"
"v_pk_fma_f32 v[152:153], v[46:47], v[110:111], v[152:153] op_sel:[0,1,0] op_sel_hi:[1,1,1]\n"
"v_pk_fma_f32 v[154:155], v[44:45], v[134:135], v[154:155] op_sel:[0,1,0] op_sel_hi:[1,1,1]\n"
"v_pk_fma_f32 v[156:157], v[46:47], v[134:135], v[156:157] op_sel:[0,1,0] op_sel_hi:[1,1,1]\n"
"s_waitcnt lgkmcnt(5)\n"
"v_pk_fma_f32 v[150:151], v[48:49], v[112:113], v[150:151] op_sel:[0,0,0] op_sel_hi:[1,0,1]\n"
"v_pk_fma_f32 v[152:153], v[50:51], v[112:113], v[152:153] op_sel:[0,0,0] op_sel_hi:[1,0,1]\n"
"s_waitcnt lgkmcnt(4)\n"
"v_pk_fma_f32 v[154:155], v[48:49], v[136:137], v[154:155] op_sel:[0,0,0] op_sel_hi:[1,0,1]\n"
"v_pk_fma_f32 v[156:157], v[50:51], v[136:137], v[156:157] op_sel:[0,0,0] op_sel_hi:[1,0,1]\n"
"v_pk_fma_f32 v[150:151], v[52:53], v[112:113], v[150:151] op_sel:[0,1,0] op_sel_hi:[1,1,1]\n"
"v_pk_fma_f32 v[152:153], v[54:55], v[112:113], v[152:153] op_sel:[0,1,0] op_sel_hi:[1,1,1]\n"
"v_pk_fma_f32 v[154:155], v[52:53], v[136:137], v[154:155] op_sel:[0,1,0] op_sel_hi:[1,1,1]\n"
"v_pk_fma_f32 v[156:157], v[54:55], v[136:137], v[156:157] op_sel:[0,1,0] op_sel_hi:[1,1,1]\n"
"v_pk_fma_f32 v[150:151], v[56:57], v[114:115], v[150:151] op_sel:[0,0,0] op_sel_hi:[1,0,1]\n"
"v_pk_fma_f32 v[152:153], v[58:59], v[114:115], v[152:153] op_sel:[0,0,0] op_sel_hi:[1,0,1]\n"
"v_pk_fma_f32 v[154:155], v[56:57], v[138:139], v[154:155] op_sel:[0,0,0] op_sel_hi:[1,0,1]\n"
"v_pk_fma_f32 v[156:157], v[58:59], v[138:139], v[156:157] op_sel:[0,0,0] op_sel_hi:[1,0,1]\n"
"v_pk_fma_f32 v[150:151], v[60:61], v[114:115], v[150:151] op_sel:[0,1,0] op_sel_hi:[1,1,1]\n"
"v_pk_fma_f32 v[152:153], v[62:63], v[114:115], v[152:153] op_sel:[0,1,0] op_sel_hi:[1,1,1]\n"
"v_pk_fma_f32 v[154:155], v[60:61], v[138:139], v[154:155] op_sel:[0,1,0] op_sel_hi:[1,1,1]\n"
"v_pk_fma_f32 v[156:157], v[62:63], v[138:139], v[156:157] op_sel:[0,1,0] op_sel_hi:[1,1,1]\n"
"s_waitcnt lgkmcnt(3)\n"
"v_pk_fma_f32 v[150:151], v[64:65], v[116:117], v[150:151] op_sel:[0,0,0] op_sel_hi:[1,0,1]\n"
"v_pk_fma_f32 v[152:153], v[66:67], v[116:117], v[152:153] op_sel:[0,0,0] op_sel_hi:[1,0,1]\n"
"s_waitcnt lgkmcnt(2)\n"
"v_pk_fma_f32 v[154:155], v[64:65], v[140:141], v[154:155] op_sel:[0,0,0] op_sel_hi:[1,0,1]\n"
"v_pk_fma_f32 v[156:157], v[66:67], v[140:141], v[156:157] op_sel:[0,0,0] op_sel_hi:[1,0,1]\n"
"v_pk_fma_f32 v[150:151], v[68:69], v[116:117], v[150:151] op_sel:[0,1,0] op_sel_hi:[1,1,1]\n"
"v_pk_fma_f32 v[152:153], v[70:71], v[116:117], v[152:153] op_sel:[0,1,0] op_sel_hi:[1,1,1]\n"
"v_pk_fma_f32 v[154:155], v[68:69], v[140:141], v[154:155] op_sel:[0,1,0] op_sel_hi:[1,1,1]\n"
"v_pk_fma_f32 v[156:157], v[70:71], v[140:141], v[156:157] op_sel:[0,1,0] op_sel_hi:[1,1,1]\n"
"v_pk_fma_f32 v[150:151], v[72:73], v[118:119], v[150:151] op_sel:[0,0,0] op_sel_hi:[1,0,1]\n"
"v_pk_fma_f32 v[152:153], v[74:75], v[118:119], v[152:153] op_sel:[0,0,0] op_sel_hi:[1,0,1]\n"
"v_pk_fma_f32 v[154:155], v[72:73], v[142:143], v[154:155] op_sel:[0,0,0] op_sel_hi:[1,0,1]\n"
"v_pk_fma_f32 v[156:157], v[74:75], v[142:143], v[156:157] op_sel:[0,0,0] op_sel_hi:[1,0,1]\n"
"v_pk_fma_f32 v[150:151], v[76:77], v[118:119], v[150:151] op_sel:[0,1,0] op_sel_hi:[1,1,1]\n"
"v_pk_fma_f32 v[152:153], v[78:79], v[118:119], v[152:153] op_sel:[0,1,0] op_sel_hi:[1,1,1]\n"
"v_pk_fma_f32 v[154:155], v[76:77], v[142:143], v[154:155] op_sel:[0,1,0] op_sel_hi:[1,1,1]\n"
"v_pk_fma_f32 v[156:157], v[78:79], v[142:143], v[156:157] op_sel:[0,1,0] op_sel_hi:[1,1,1]\n"
"s_waitcnt lgkmcnt(1)\n"
"v_pk_fma_f32 v[150:151], v[80:81], v[120:121], v[150:151] op_sel:[0,0,0] op_sel_hi:[1,0,1]\n"
"v_pk_fma_f32 v[152:153], v[82:83], v[120:121], v[152:153] op_sel:[0,0,0] op_sel_hi:[1,0,1]\n"
"s_waitcnt lgkmcnt(0)\n"
"v_pk_fma_f32 v[154:155], v[80:81], v[144:145], v[154:155] op_sel:[0,0,0] op_sel_hi:[1,0,1]\n"
"v_pk_fma_f32 v[156:157], v[82:83], v[144:145], v[156:157] op_sel:[0,0,0] op_sel_hi:[1,0,1]\n"
"v_pk_fma_f32 v[150:151], v[84:85], v[120:121], v[150:151] op_sel:[0,1,0] op_sel_hi:[1,1,1]\n"
"v_pk_fma_f32 v[152:153], v[86:87], v[120:121], v[152:153] op_sel:[0,1,0] op_sel_hi:[1,1,1]\n"
"v_pk_fma_f32 v[154:155], v[84:85], v[144:145], v[154:155] op_sel:[0,1,0] op_sel_hi:[1,1,1]\n"
"v_pk_fma_f32 v[156:157], v[86:87], v[144:145], v[156:157] op_sel:[0,1,0] op_sel_hi:[1,1,1]\n"
"v_pk_fma_f32 v[150:151], v[88:89], v[122:123], v[150:151] op_sel:[0,0,0] op_sel_hi:[1,0,1]\n"
"v_pk_fma_f32 v[152:153], v[90:91], v[122:123], v[152:153] op_sel:[0,0,0] op_sel_hi:[1,0,1]\n"
"v_pk_fma_f32 v[154:155], v[88:89], v[146:147], v[154:155] op_sel:[0,0,0] op_sel_hi:[1,0,1]\n"
"v_pk_fma_f32 v[156:157], v[90:91], v[146:147], v[156:157] op_sel:[0,0,0] op_sel_hi:[1,0,1]\n"
"v_pk_fma_f32 v[150:151], v[92:93], v[122:123], v[150:151] op_sel:[0,1,0] op_sel_hi:[1,1,1]\n"
"v_pk_fma_f32 v[152:153], v[94:95], v[122:123], v[152:153] op_sel:[0,1,0] op_sel_hi:[1,1,1]\n"
"v_pk_fma_f32 v[154:155], v[92:93], v[146:147], v[154:155] op_sel:[0,1,0] op_sel_hi:[1,1,1]\n"
"v_pk_fma_f32 v[156:157], v[94:95], v[146:147], v[156:157] op_sel:[0,1,0] op_sel_hi:[1,1,1]\n"
"v_mov_b32 v160, v150\n"
"v_mov_b32 v162, v151\n"
"v_mov_b32 v164, v152\n"
"v_mov_b32 v166, v153\n"
"v_mov_b32 v168, v154\n"
"v_mov_b32 v170, v155\n"
"v_mov_b32 v172, v156\n"
"v_mov_b32 v174, v157\n"
"buffer_store_dwordx4 v[160:163], v243, s[28:31], s58 offen sc1\n"
"buffer_store_dwordx4 v[164:167], v243, s[28:31], s59 offen sc1\n"
"buffer_store_dwordx4 v[168:171], v243, s[28:31], s60 offen sc1\n"
"buffer_store_dwordx4 v[172:175], v243, s[28:31], s61 offen sc1\n"
"s_branch AT_F1\n"
"AT_F0:\n"
"s_sleep 30\n"
"AT_F1:\n"
"s_cmp_lt_u32 s42, 184\n"
"s_cbranch_scc1 AT_end\n"
"s_sub_u32 s56, s42, 184\n"
"s_cmp_ge_u32 s56, 36\n"
"s_cselect_b32 s57, 36, 0\n"
"s_cselect_b32 s59, s13, s12\n"
"s_cselect_b32 s60, s11, s10\n"
"s_sub_u32 s58, s56, s57\n"
"s_cmp_ge_u32 s58, 18\n"
"s_cselect_b32 s57, 574, 0\n"
"s_lshl_b32 s58, s58, 5\n"
"s_sub_u32 s58, s58, s57\n"
"s_mul_i32 s56, s56, 24576\n"
"s_add_u32 s56, s56, 56762368\n"
"s_mul_i32 s61, s59, 1152\n"
"s_add_u32 s61, s61, s58\n"
"s_lshl_b32 s61, s61, 3\n"
"s_add_u32 s61, s61, 37892608\n"
"v_lshlrev_b32 v30, 4, v253\n"
"v_add_u32 v31, 4096, v30\n"
"v_add_u32 v32, 8192, v30\n"
"v_add_u32 v33, 12288, v30\n"
"v_add_u32 v34, 16384, v30\n"
"v_add_u32 v35, 20480, v30\n"
"v_and_b32 v36, 15, v248\n"
"v_lshlrev_b32 v37, 3, v36\n"
"v_and_b32 v41, 14, v36\n"
"v_add_u32 v41, v41, v36\n"
"v_lshlrev_b32 v42, 3, v41\n"
"s_lshl_b32 s62, s58, 2\n"
"s_add_u32 s62, s62, s40\n"
"s_add_u32 s62, s62, 158720\n"
"v_lshl_add_u32 v38, v41, 2, s62\n"
"ds_read_b32 v39, v38\n"
"s_mov_b32 s87, 0x80000\n"
"AT_F2:\n"
"buffer_load_dwordx4 v[0:3], v30, s[28:31], s56 offen sc1\n"
"buffer_load_dwordx4 v[4:7], v31, s[28:31], s56 offen sc1\n"
"buffer_load_dwordx4 v[8:11], v32, s[28:31], s56 offen sc1\n"
"buffer_load_dwordx4 v[12:15], v33, s[28:31], s56 offen sc1\n"
"buffer_load_dwordx4 v[16:19], v34, s[28:31], s56 offen sc1\n"
"buffer_load_dwordx4 v[20:23], v35, s[28:31], s56 offen sc1\n"
"buffer_load_dwordx2 v[24:25], v42, s[28:31], s61 offen sc1\n"
"s_and_b32 s62, s60, s14\n"
"s_ashr_i32 s63, s62, 31\n"
"s_not_b32 s63, s63\n"
"s_and_b32 s63, s63, s14\n"
"s_or_b32 s63, s63, 0x80000000\n"
"s_xor_b32 s62, s62, s63\n"
"v_mov_b32 v40, s62\n"
"v_mul_f32 v40, 0xbfb8aa3b, v40\n"
"v_exp_f32 v40, v40\n"
"s_nop 0\n"
"v_add_f32 v40, 1.0, v40\n"
"v_rcp_f32 v40, v40\n"
"s_waitcnt vmcnt(0) lgkmcnt(0)\n"
"v_xor_b32 v26, s85, v1\n"
"v_xor_b32 v27, s85, v3\n"
"v_bitop3_b32 v26, v5, s85, v26 bitop3:0xbe\n"
"v_bitop3_b32 v27, v7, s85, v27 bitop3:0xbe\n"
"v_bitop3_b32 v26, v9, s85, v26 bitop3:0xbe\n"
"v_bitop3_b32 v27, v11, s85, v27 bitop3:0xbe\n"
"v_bitop3_b32 v26, v13, s85, v26 bitop3:0xbe\n"
"v_bitop3_b32 v27, v15, s85, v27 bitop3:0xbe\n"
"v_bitop3_b32 v26, v17, s85, v26 bitop3:0xbe\n"
"v_bitop3_b32 v27, v19, s85, v27 bitop3:0xbe\n"
"v_bitop3_b32 v26, v21, s85, v26 bitop3:0xbe\n"
"v_bitop3_b32 v27, v23, s85, v27 bitop3:0xbe\n"
"v_bitop3_b32 v26, v25, s85, v26 bitop3:0xbe\n"
"v_or_b32 v26, v26, v27\n"
"v_cmp_ne_u32 vcc, 0, v26\n"
"s_nop 0\n"
"s_cbranch_vccz AT_F3\n"
"s_sub_u32 s87, s87, 1\n"
"s_cmp_eq_u32 s87, 0\n"
"s_cbranch_scc1 AT_F3\n"
"s_sleep 2\n"
"s_branch AT_F2\n"
"AT_F3:\n"
"v_add_f32 v0, v0, v4\n"
"v_add_f32 v2, v2, v6\n"
"v_add_f32 v8, v8, v12\n"
"v_add_f32 v10, v10, v14\n"
"v_add_f32 v16, v16, v20\n"
"v_add_f32 v18, v18, v22\n"
"v_add_f32 v0, v0, v8\n"
"v_add_f32 v2, v2, v10\n"
"v_add_f32 v0, v0, v16\n"
"v_add_f32 v1, v2, v18\n"
"v_mul_f32 v40, v40, v39\n"
"s_lshl_b32 s62, s41, 6\n"
"v_add3_u32 v28, v37, v227, s62\n"
"v_add_f32_dpp v0, v0, v0 row_ror:8 row_mask:0xf bank_mask:0xf\n"
"v_add_f32_dpp v1, v1, v1 row_ror:8 row_mask:0xf bank_mask:0xf\n"
"s_nop 0\n"
"v_mov_b32 v2, v0\n"
"v_mov_b32 v3, v1\n"
"s_nop 0\n"
"v_permlane16_swap_b32 v0, v2\n"
"v_permlane16_swap_b32 v1, v3\n"
"v_add_f32 v0, v0, v2\n"
"v_add_f32 v1, v1, v3\n"
"v_mov_b32 v2, v0\n"
"v_mov_b32 v3, v1\n"
"s_nop 0\n"
"v_permlane32_swap_b32 v0, v2\n"
"v_permlane32_swap_b32 v1, v3\n"
"v_add_f32 v0, v0, v2\n"
"v_add_f32 v1, v1, v3\n"
"s_mov_b64 exec, 0xff\n"
"ds_write_b64 v28, v[0:1] offset:1024\n"
"s_mov_b64 exec, -1\n"
"v_lshl_add_u32 v28, v36, 2, v227\n"
"s_waitcnt lgkmcnt(0)\n"
"s_barrier\n"
"s_cmp_lg_u32 s41, 0\n"
"s_cbranch_scc1 AT_end\n"
"ds_read_b32 v0, v28 offset:1024\n"
"ds_read_b32 v1, v28 offset:1088\n"
"ds_read_b32 v2, v28 offset:1152\n"
"ds_read_b32 v3, v28 offset:1216\n"
"s_mul_i32 s61, s59, 576\n"
"s_add_u32 s61, s61, s58\n"
"s_lshl_b32 s61, s61, 1\n"
"v_lshlrev_b32 v5, 1, v41\n"
"s_waitcnt lgkmcnt(0)\n"
"v_add_f32 v0, v0, v1\n"
"v_add_f32 v2, v2, v3\n"
"v_add_f32 v0, v0, v2\n"
"v_fma_f32 v0, v0, v40, v24\n"
"v_cvt_pk_bf16_f32 v0, v0, v0\n"
"s_mov_b64 exec, 0xffff\n"
"buffer_store_short v0, v5, s[76:79], s61 offen\n"
"s_branch AT_end\n"
"AT_x1:\n"
"s_mov_b32 s87, 0x80000\n"
"AT_x3:\n"
"buffer_load_dword v249, v215, s[28:31], s23 offen sc1\n"
"s_waitcnt vmcnt(0)\n"
"v_cmp_ne_u32 vcc, s85, v249\n"
"s_sub_u32 s87, s87, 1\n"
"s_cbranch_vccz AT_x2\n"
"s_cmp_eq_u32 s87, 0\n"
"s_cbranch_scc1 AT_x2\n"
"s_sleep 1\n"
"s_branch AT_x3\n"
"AT_fb:\n"
"s_waitcnt vmcnt(0) lgkmcnt(0)\n"
"s_barrier\n"
"s_mov_b32 s45, 1\n"
"s_mov_b32 s87, 32\n"
"s_lshl_b32 s84, s41, 12\n"
"s_lshl_b32 s56, s43, 19\n"
"s_add_u32 s84, s84, s56\n"
"v_mov_b32 v160, 0xff800000\n"
"v_mov_b32 v161, 0xff800000\n"
"v_mov_b32 v162, 0xff800000\n"
"v_mov_b32 v163, 0xff800000\n"
"AT_fbl:\n"
"s_mov_b32 m0, s86\n"
"s_nop 0\n"
"buffer_load_dwordx4 v246, s[8:11], s84 offen offset:0 lds\n"
"buffer_load_dwordx4 v246, s[8:11], s84 offen offset:1024 lds\n"
"buffer_load_dwordx4 v247, s[8:11], s84 offen offset:2048 lds\n"
"buffer_load_dwordx4 v247, s[8:11], s84 offen offset:3072 lds\n"
"s_add_u32 s84, s84, 16384\n"
"s_waitcnt vmcnt(0)\n"
"s_barrier\n"
"ds_read_b128 a[128:131], v240 offset:0\n"
"ds_read_b128 a[132:135], v240 offset:256\n"
"ds_read_b128 a[136:139], v240 offset:512\n"
"ds_read_b128 a[140:143], v240 offset:768\n"
"ds_read_b128 a[144:147], v240 offset:4096\n"
"ds_read_b128 a[148:151], v240 offset:4352\n"
"ds_read_b128 a[152:155], v240 offset:4608\n"
"ds_read_b128 a[156:159], v240 offset:4864\n"
"s_waitcnt lgkmcnt(0)\n"
"v_mfma_f32_16x16x32_bf16 v[128:131], a[128:131], v[64:67], 0\n"
"v_mfma_f32_16x16x32_bf16 v[128:131], a[132:135], v[68:71], v[128:131]\n"
"v_mfma_f32_16x16x32_bf16 v[128:131], a[136:139], v[72:75], v[128:131]\n"
"v_mfma_f32_16x16x32_bf16 v[128:131], a[140:143], v[76:79], v[128:131]\n"
"v_mfma_f32_16x16x32_bf16 v[132:135], a[144:147], v[64:67], 0\n"
"v_mfma_f32_16x16x32_bf16 v[132:135], a[148:151], v[68:71], v[132:135]\n"
"v_mfma_f32_16x16x32_bf16 v[132:135], a[152:155], v[72:75], v[132:135]\n"
"v_mfma_f32_16x16x32_bf16 v[132:135], a[156:159], v[76:79], v[132:135]\n"
"v_mfma_f32_16x16x32_bf16 v[136:139], a[128:131], v[80:83], 0\n"
"v_mfma_f32_16x16x32_bf16 v[136:139], a[132:135], v[84:87], v[136:139]\n"
"v_mfma_f32_16x16x32_bf16 v[136:139], a[136:139], v[88:91], v[136:139]\n"
"v_mfma_f32_16x16x32_bf16 v[136:139], a[140:143], v[92:95], v[136:139]\n"
"v_mfma_f32_16x16x32_bf16 v[140:143], a[144:147], v[80:83], 0\n"
"v_mfma_f32_16x16x32_bf16 v[140:143], a[148:151], v[84:87], v[140:143]\n"
"v_mfma_f32_16x16x32_bf16 v[140:143], a[152:155], v[88:91], v[140:143]\n"
"v_mfma_f32_16x16x32_bf16 v[140:143], a[156:159], v[92:95], v[140:143]\n"
"v_mfma_f32_16x16x32_bf16 v[144:147], a[128:131], v[96:99], 0\n"
"v_mfma_f32_16x16x32_bf16 v[144:147], a[132:135], v[100:103], v[144:147]\n"
"v_mfma_f32_16x16x32_bf16 v[144:147], a[136:139], v[104:107], v[144:147]\n"
"v_mfma_f32_16x16x32_bf16 v[144:147], a[140:143], v[108:111], v[144:147]\n"
"v_mfma_f32_16x16x32_bf16 v[148:151], a[144:147], v[96:99], 0\n"
"v_mfma_f32_16x16x32_bf16 v[148:151], a[148:151], v[100:103], v[148:151]\n"
"v_mfma_f32_16x16x32_bf16 v[148:151], a[152:155], v[104:107], v[148:151]\n"
"v_mfma_f32_16x16x32_bf16 v[148:151], a[156:159], v[108:111], v[148:151]\n"
"v_mfma_f32_16x16x32_bf16 v[152:155], a[128:131], v[112:115], 0\n"
"v_mfma_f32_16x16x32_bf16 v[152:155], a[132:135], v[116:119], v[152:155]\n"
"v_mfma_f32_16x16x32_bf16 v[152:155], a[136:139], v[120:123], v[152:155]\n"
"v_mfma_f32_16x16x32_bf16 v[152:155], a[140:143], v[124:127], v[152:155]\n"
"v_mfma_f32_16x16x32_bf16 v[156:159], a[144:147], v[112:115], 0\n"
"v_mfma_f32_16x16x32_bf16 v[156:159], a[148:151], v[116:119], v[156:159]\n"
"v_mfma_f32_16x16x32_bf16 v[156:159], a[152:155], v[120:123], v[156:159]\n"
"v_mfma_f32_16x16x32_bf16 v[156:159], a[156:159], v[124:127], v[156:159]\n"
"s_nop 7\n"
"v_max3_f32 v160, v160, v128, v129\n"
"v_max3_f32 v160, v160, v130, v131\n"
"v_max3_f32 v160, v160, v132, v133\n"
"v_max3_f32 v160, v160, v134, v135\n"
"v_max3_f32 v161, v161, v136, v137\n"
"v_max3_f32 v161, v161, v138, v139\n"
"v_max3_f32 v161, v161, v140, v141\n"
"v_max3_f32 v161, v161, v142, v143\n"
"v_max3_f32 v162, v162, v144, v145\n"
"v_max3_f32 v162, v162, v146, v147\n"
"v_max3_f32 v162, v162, v148, v149\n"
"v_max3_f32 v162, v162, v150, v151\n"
"v_max3_f32 v163, v163, v152, v153\n"
"v_max3_f32 v163, v163, v154, v155\n"
"v_max3_f32 v163, v163, v156, v157\n"
"v_max3_f32 v163, v163, v158, v159\n"
"ds_read_b128 a[128:131], v240 offset:8192\n"
"ds_read_b128 a[132:135], v240 offset:8448\n"
"ds_read_b128 a[136:139], v240 offset:8704\n"
"ds_read_b128 a[140:143], v240 offset:8960\n"
"ds_read_b128 a[144:147], v240 offset:12288\n"
"ds_read_b128 a[148:151], v240 offset:12544\n"
"ds_read_b128 a[152:155], v240 offset:12800\n"
"ds_read_b128 a[156:159], v240 offset:13056\n"
"s_waitcnt lgkmcnt(0)\n"
"v_mfma_f32_16x16x32_bf16 v[128:131], a[128:131], v[64:67], 0\n"
"v_mfma_f32_16x16x32_bf16 v[128:131], a[132:135], v[68:71], v[128:131]\n"
"v_mfma_f32_16x16x32_bf16 v[128:131], a[136:139], v[72:75], v[128:131]\n"
"v_mfma_f32_16x16x32_bf16 v[128:131], a[140:143], v[76:79], v[128:131]\n"
"v_mfma_f32_16x16x32_bf16 v[132:135], a[144:147], v[64:67], 0\n"
"v_mfma_f32_16x16x32_bf16 v[132:135], a[148:151], v[68:71], v[132:135]\n"
"v_mfma_f32_16x16x32_bf16 v[132:135], a[152:155], v[72:75], v[132:135]\n"
"v_mfma_f32_16x16x32_bf16 v[132:135], a[156:159], v[76:79], v[132:135]\n"
"v_mfma_f32_16x16x32_bf16 v[136:139], a[128:131], v[80:83], 0\n"
"v_mfma_f32_16x16x32_bf16 v[136:139], a[132:135], v[84:87], v[136:139]\n"
"v_mfma_f32_16x16x32_bf16 v[136:139], a[136:139], v[88:91], v[136:139]\n"
"v_mfma_f32_16x16x32_bf16 v[136:139], a[140:143], v[92:95], v[136:139]\n"
"v_mfma_f32_16x16x32_bf16 v[140:143], a[144:147], v[80:83], 0\n"
"v_mfma_f32_16x16x32_bf16 v[140:143], a[148:151], v[84:87], v[140:143]\n"
"v_mfma_f32_16x16x32_bf16 v[140:143], a[152:155], v[88:91], v[140:143]\n"
"v_mfma_f32_16x16x32_bf16 v[140:143], a[156:159], v[92:95], v[140:143]\n"
"v_mfma_f32_16x16x32_bf16 v[144:147], a[128:131], v[96:99], 0\n"
"v_mfma_f32_16x16x32_bf16 v[144:147], a[132:135], v[100:103], v[144:147]\n"
"v_mfma_f32_16x16x32_bf16 v[144:147], a[136:139], v[104:107], v[144:147]\n"
"v_mfma_f32_16x16x32_bf16 v[144:147], a[140:143], v[108:111], v[144:147]\n"
"v_mfma_f32_16x16x32_bf16 v[148:151], a[144:147], v[96:99], 0\n"
"v_mfma_f32_16x16x32_bf16 v[148:151], a[148:151], v[100:103], v[148:151]\n"
"v_mfma_f32_16x16x32_bf16 v[148:151], a[152:155], v[104:107], v[148:151]\n"
"v_mfma_f32_16x16x32_bf16 v[148:151], a[156:159], v[108:111], v[148:151]\n"
"v_mfma_f32_16x16x32_bf16 v[152:155], a[128:131], v[112:115], 0\n"
"v_mfma_f32_16x16x32_bf16 v[152:155], a[132:135], v[116:119], v[152:155]\n"
"v_mfma_f32_16x16x32_bf16 v[152:155], a[136:139], v[120:123], v[152:155]\n"
"v_mfma_f32_16x16x32_bf16 v[152:155], a[140:143], v[124:127], v[152:155]\n"
"v_mfma_f32_16x16x32_bf16 v[156:159], a[144:147], v[112:115], 0\n"
"v_mfma_f32_16x16x32_bf16 v[156:159], a[148:151], v[116:119], v[156:159]\n"
"v_mfma_f32_16x16x32_bf16 v[156:159], a[152:155], v[120:123], v[156:159]\n"
"v_mfma_f32_16x16x32_bf16 v[156:159], a[156:159], v[124:127], v[156:159]\n"
"s_nop 7\n"
"v_max3_f32 v160, v160, v128, v129\n"
"v_max3_f32 v160, v160, v130, v131\n"
"v_max3_f32 v160, v160, v132, v133\n"
"v_max3_f32 v160, v160, v134, v135\n"
"v_max3_f32 v161, v161, v136, v137\n"
"v_max3_f32 v161, v161, v138, v139\n"
"v_max3_f32 v161, v161, v140, v141\n"
"v_max3_f32 v161, v161, v142, v143\n"
"v_max3_f32 v162, v162, v144, v145\n"
"v_max3_f32 v162, v162, v146, v147\n"
"v_max3_f32 v162, v162, v148, v149\n"
"v_max3_f32 v162, v162, v150, v151\n"
"v_max3_f32 v163, v163, v152, v153\n"
"v_max3_f32 v163, v163, v154, v155\n"
"v_max3_f32 v163, v163, v156, v157\n"
"v_max3_f32 v163, v163, v158, v159\n"
"s_barrier\n"
"s_sub_u32 s87, s87, 1\n"
"s_cmp_lg_u32 s87, 0\n"
"s_cbranch_scc1 AT_fbl\n"
"v_mov_b32 v164, v160\n"
"s_nop 1\n"
"v_permlane32_swap_b32 v160, v164\n"
"v_max_f32 v160, v160, v164\n"
"v_mov_b32 v164, v160\n"
"s_nop 1\n"
"v_permlane16_swap_b32 v160, v164\n"
"v_max_f32 v160, v160, v164\n"
"v_sub_f32 v24, 0, v160\n"
"v_sub_f32 v25, 0, v160\n"
"v_sub_f32 v26, 0, v160\n"
"v_sub_f32 v27, 0, v160\n"
"v_mov_b32 v164, v161\n"
"s_nop 1\n"
"v_permlane32_swap_b32 v161, v164\n"
"v_max_f32 v161, v161, v164\n"
"v_mov_b32 v164, v161\n"
"s_nop 1\n"
"v_permlane16_swap_b32 v161, v164\n"
"v_max_f32 v161, v161, v164\n"
"v_sub_f32 v28, 0, v161\n"
"v_sub_f32 v29, 0, v161\n"
"v_sub_f32 v30, 0, v161\n"
"v_sub_f32 v31, 0, v161\n"
"v_mov_b32 v164, v162\n"
"s_nop 1\n"
"v_permlane32_swap_b32 v162, v164\n"
"v_max_f32 v162, v162, v164\n"
"v_mov_b32 v164, v162\n"
"s_nop 1\n"
"v_permlane16_swap_b32 v162, v164\n"
"v_max_f32 v162, v162, v164\n"
"v_sub_f32 v32, 0, v162\n"
"v_sub_f32 v33, 0, v162\n"
"v_sub_f32 v34, 0, v162\n"
"v_sub_f32 v35, 0, v162\n"
"v_mov_b32 v164, v163\n"
"s_nop 1\n"
"v_permlane32_swap_b32 v163, v164\n"
"v_max_f32 v163, v163, v164\n"
"v_mov_b32 v164, v163\n"
"s_nop 1\n"
"v_permlane16_swap_b32 v163, v164\n"
"v_max_f32 v163, v163, v164\n"
"v_sub_f32 v36, 0, v163\n"
"v_sub_f32 v37, 0, v163\n"
"v_sub_f32 v38, 0, v163\n"
"v_sub_f32 v39, 0, v163\n"
"s_and_b32 s62, s42, 7\n"
"s_mul_i32 s46, s62, 663552\n"
"s_lshr_b32 s47, s43, 2\n"
"s_mul_i32 s47, s47, 1179648\n"
"s_getpc_b64 s[32:33]\n"
"AT_pc2:\n"
"s_sub_u32 s32, s32, AT_pc2-AT_start\n"
"s_subb_u32 s33, s33, 0\n"
"s_and_b32 s33, s33, 0xffff\n"
"s_mov_b32 s34, AT_kend-AT_start\n"
"s_mov_b32 s35, 0x20000\n"
"s_lshr_b32 s62, s42, 3\n"
"s_lshl_b32 s62, s62, 2\n"
"s_add_u32 s62, s62, s41\n"
"s_and_b32 s62, s62, 31\n"
"s_lshl_b32 s48, s62, 12\n"
"s_lshl_b32 s54, s44, 2\n"
"s_add_u32 s54, s54, s41\n"
"s_mul_i32 s54, s54, 36864\n"
"s_lshr_b32 s62, s43, 2\n"
"s_mul_i32 s62, s62, 2359296\n"
"s_add_u32 s54, s54, s62\n"
"s_add_u32 s54, s54, 139264\n"
"s_bfe_u32 s62, s43, 0x10001\n"
"s_mul_i32 s62, s62, 9216\n"
"s_add_u32 s54, s54, s62\n"
"s_and_b32 s62, s43, 1\n"
"s_lshl_b32 s49, s62, 3\n"
"s_add_u32 s49, s49, s54\n"
"s_add_u32 s49, s49, 4096\n"
"s_mul_i32 s62, s62, 5120\n"
"s_add_u32 s54, s54, s62\n"
"s_and_b32 s62, s43, 1\n"
"s_lshl_b32 s62, s62, 10\n"
"s_sub_u32 s54, s54, s62\n"
"s_add_u32 s49, s54, 4096\n"
"v_mov_b32 v40, 0x3f803f80\n"
"v_mov_b32 v41, 0x3f803f80\n"
"v_mov_b32 v42, 0x3f803f80\n"
"v_mov_b32 v43, 0x3f803f80\n"
"v_mov_b32 v20, 0\n"
"v_mov_b32 v21, 0\n"
"v_mov_b32 v22, 0\n"
"v_mov_b32 v23, 0\n"
"v_mov_b32 v44, 0\n"
"v_mov_b32 v52, 0\n"
"v_mov_b32 v216, -1\n"
"v_mov_b32 v217, -1\n"
"v_mov_b32 v218, -1\n"
"v_mov_b32 v219, -1\n"
"v_lshl_add_u32 v255, v248, 6, s48\n"
"v_lshlrev_b32 v207, 4, v248\n"
"v_add_u32 v208, 18432, v207\n"
"s_lshl_b32 s84, s41, 12\n"
"s_lshl_b32 s56, s43, 19\n"
"s_add_u32 s84, s84, s56\n"
"v_mov_b32 v251, 0x40000000\n"
"v_mov_b32 v252, 0x40000000\n"
"v_mov_b32 v253, 0x40000000\n"
"v_add_f32 v148, 0xc2480000, v161\n"
"v_add_f32 v149, 0xc2480000, v161\n"
"v_add_f32 v150, 0xc2480000, v161\n"
"v_add_f32 v151, 0xc2480000, v161\n"
"v_add_f32 v156, 0xc2480000, v163\n"
"v_add_f32 v157, 0xc2480000, v163\n"
"v_add_f32 v158, 0xc2480000, v163\n"
"v_add_f32 v159, 0xc2480000, v163\n"
"s_mov_b32 m0, s86\n"
"s_nop 0\n"
"buffer_load_dwordx4 v246, s[8:11], s84 offen offset:0 lds\n"
"buffer_load_dwordx4 v246, s[8:11], s84 offen offset:1024 lds\n"
"buffer_load_dwordx4 v247, s[8:11], s84 offen offset:2048 lds\n"
"buffer_load_dwordx4 v247, s[8:11], s84 offen offset:3072 lds\n"
"s_add_u32 m0, s86, 16384\n"
"s_nop 0\n"
"buffer_load_dwordx4 v246, s[12:15], s84 offen offset:0 lds\n"
"buffer_load_dwordx4 v247, s[12:15], s84 offen offset:1024 lds\n"
"buffer_load_dwordx4 v246, s[12:15], s84 offen offset:2048 lds\n"
"buffer_load_dwordx4 v247, s[12:15], s84 offen offset:3072 lds\n"
"s_add_u32 s84, s84, 16384\n"
"s_add_u32 m0, s86, 32768\n"
"s_nop 0\n"
"buffer_load_dwordx4 v246, s[8:11], s84 offen offset:0 lds\n"
"buffer_load_dwordx4 v246, s[8:11], s84 offen offset:1024 lds\n"
"buffer_load_dwordx4 v247, s[8:11], s84 offen offset:2048 lds\n"
"buffer_load_dwordx4 v247, s[8:11], s84 offen offset:3072 lds\n"
"s_add_u32 m0, s86, 49152\n"
"s_nop 0\n"
"buffer_load_dwordx4 v246, s[12:15], s84 offen offset:0 lds\n"
"buffer_load_dwordx4 v247, s[12:15], s84 offen offset:1024 lds\n"
"buffer_load_dwordx4 v246, s[12:15], s84 offen offset:2048 lds\n"
"buffer_load_dwordx4 v247, s[12:15], s84 offen offset:3072 lds\n"
"s_add_u32 s84, s84, 16384\n"
"s_add_u32 m0, s86, 65536\n"
"s_nop 0\n"
"buffer_load_dwordx4 v246, s[8:11], s84 offen offset:0 lds\n"
"buffer_load_dwordx4 v246, s[8:11], s84 offen offset:1024 lds\n"
"buffer_load_dwordx4 v247, s[8:11], s84 offen offset:2048 lds\n"
"buffer_load_dwordx4 v247, s[8:11], s84 offen offset:3072 lds\n"
"s_add_u32 m0, s86, 81920\n"
"s_nop 0\n"
"buffer_load_dwordx4 v246, s[12:15], s84 offen offset:0 lds\n"
"buffer_load_dwordx4 v247, s[12:15], s84 offen offset:1024 lds\n"
"buffer_load_dwordx4 v246, s[12:15], s84 offen offset:2048 lds\n"
"buffer_load_dwordx4 v247, s[12:15], s84 offen offset:3072 lds\n"
"s_add_u32 s84, s84, 16384\n"
"s_add_u32 m0, s86, 98304\n"
"s_nop 0\n"
"buffer_load_dwordx4 v246, s[8:11], s84 offen offset:0 lds\n"
"buffer_load_dwordx4 v246, s[8:11], s84 offen offset:1024 lds\n"
"buffer_load_dwordx4 v247, s[8:11], s84 offen offset:2048 lds\n"
"buffer_load_dwordx4 v247, s[8:11], s84 offen offset:3072 lds\n"
"s_add_u32 m0, s86, 114688\n"
"s_nop 0\n"
"buffer_load_dwordx4 v246, s[12:15], s84 offen offset:0 lds\n"
"buffer_load_dwordx4 v247, s[12:15], s84 offen offset:1024 lds\n"
"buffer_load_dwordx4 v246, s[12:15], s84 offen offset:2048 lds\n"
"buffer_load_dwordx4 v247, s[12:15], s84 offen offset:3072 lds\n"
"s_add_u32 s84, s84, 16384\n"
"s_waitcnt vmcnt(24)\n"
"s_barrier\n"
"ds_read_b128 a[128:131], v240 offset:0\n"
"ds_read_b128 a[132:135], v240 offset:256\n"
"ds_read_b128 a[136:139], v240 offset:512\n"
"ds_read_b128 a[140:143], v240 offset:768\n"
"ds_read_b128 a[144:147], v240 offset:4096\n"
"ds_read_b128 a[148:151], v240 offset:4352\n"
"ds_read_b128 a[152:155], v240 offset:4608\n"
"ds_read_b128 a[156:159], v240 offset:4864\n"
"ds_read_b64_tr_b16 a[192:193], v242 offset:16384\n"
"ds_read_b64_tr_b16 a[194:195], v242 offset:20480\n"
"ds_read_b64_tr_b16 a[196:197], v243 offset:16384\n"
"ds_read_b64_tr_b16 a[198:199], v243 offset:20480\n"
"ds_read_b64_tr_b16 a[200:201], v242 offset:16640\n"
"ds_read_b64_tr_b16 a[202:203], v242 offset:20736\n"
"ds_read_b64_tr_b16 a[204:205], v243 offset:16640\n"
"ds_read_b64_tr_b16 a[206:207], v243 offset:20736\n"
"ds_read_b64_tr_b16 a[208:209], v242 offset:16896\n"
"ds_read_b64_tr_b16 a[210:211], v242 offset:20992\n"
"ds_read_b64_tr_b16 a[212:213], v243 offset:16896\n"
"ds_read_b64_tr_b16 a[214:215], v243 offset:20992\n"
"ds_read_b64_tr_b16 a[216:217], v242 offset:17152\n"
"ds_read_b64_tr_b16 a[218:219], v242 offset:21248\n"
"ds_read_b64_tr_b16 a[220:221], v243 offset:17152\n"
"ds_read_b64_tr_b16 a[222:223], v243 offset:21248\n"
"s_waitcnt lgkmcnt(0)\n"
"s_branch AT_main\n"
"AT_end:\n"
"AT_kend:\n"
  :: [kp] "s"(__builtin_amdgcn_kernarg_segment_ptr()), [blk] "s"(blockIdx.x), [wid] "s"(wid), [lds] "s"(ldsb) : "v0", "v1", "v2", "v3", "v4", "v5", "v6", "v7", "v8", "v9", "v10", "v11", "v12", "v13", "v14", "v15", "v16", "v17", "v18", "v19", "v20", "v21", "v22", "v23", "v24", "v25", "v26", "v27", "v28", "v29", "v30", "v31", "v32", "v33", "v34", "v35", "v36", "v37", "v38", "v39", "v40", "v41", "v42", "v43", "v44", "v45", "v46", "v47", "v48", "v49", "v50", "v51", "v52", "v53", "v54", "v55", "v56", "v57", "v58", "v59", "v60", "v61", "v62", "v63", "v64", "v65", "v66", "v67", "v68", "v69", "v70", "v71", "v72", "v73", "v74", "v75", "v76", "v77", "v78", "v79", "v80", "v81", "v82", "v83", "v84", "v85", "v86", "v87", "v88", "v89", "v90", "v91", "v92", "v93", "v94", "v95", "v96", "v97", "v98", "v99", "v100", "v101", "v102", "v103", "v104", "v105", "v106", "v107", "v108", "v109", "v110", "v111", "v112", "v113", "v114", "v115", "v116", "v117", "v118", "v119", "v120", "v121", "v122", "v123", "v124", "v125", "v126", "v127", "v128", "v129", "v130", "v131", "v132", "v133", "v134", "v135", "v136", "v137", "v138", "v139", "v140", "v141", "v142", "v143", "v144", "v145", "v146", "v147", "v148", "v149", "v150", "v151", "v152", "v153", "v154", "v155", "v156", "v157", "v158", "v159", "v160", "v161", "v162", "v163", "v164", "v165", "v166", "v167", "v168", "v169", "v170", "v171", "v172", "v173", "v174", "v175", "v176", "v177", "v178", "v179", "v180", "v181", "v182", "v183", "v184", "v185", "v186", "v187", "v188", "v189", "v190", "v191", "v192", "v193", "v194", "v195", "v196", "v197", "v198", "v199", "v200", "v201", "v202", "v203", "v204", "v205", "v206", "v207", "v208", "v209", "v210", "v211", "v212", "v213", "v214", "v215", "v216", "v217", "v218", "v219", "v220", "v221", "v222", "v223", "v224", "v225", "v226", "v227", "v228", "v229", "v230", "v231", "v232", "v233", "v234", "v235", "v236", "v237", "v238", "v239", "v240", "v241", "v242", "v243", "v244", "v245", "v246", "v247", "v248", "v249", "v250", "v251", "v252", "v253", "v254", "v255", "a0", "a1", "a2", "a3", "a4", "a5", "a6", "a7", "a8", "a9", "a10", "a11", "a12", "a13", "a14", "a15", "a16", "a17", "a18", "a19", "a20", "a21", "a22", "a23", "a24", "a25", "a26", "a27", "a28", "a29", "a30", "a31", "a32", "a33", "a34", "a35", "a36", "a37", "a38", "a39", "a40", "a41", "a42", "a43", "a44", "a45", "a46", "a47", "a48", "a49", "a50", "a51", "a52", "a53", "a54", "a55", "a56", "a57", "a58", "a59", "a60", "a61", "a62", "a63", "a64", "a65", "a66", "a67", "a68", "a69", "a70", "a71", "a72", "a73", "a74", "a75", "a76", "a77", "a78", "a79", "a80", "a81", "a82", "a83", "a84", "a85", "a86", "a87", "a88", "a89", "a90", "a91", "a92", "a93", "a94", "a95", "a96", "a97", "a98", "a99", "a100", "a101", "a102", "a103", "a104", "a105", "a106", "a107", "a108", "a109", "a110", "a111", "a112", "a113", "a114", "a115", "a116", "a117", "a118", "a119", "a120", "a121", "a122", "a123", "a124", "a125", "a126", "a127", "a128", "a129", "a130", "a131", "a132", "a133", "a134", "a135", "a136", "a137", "a138", "a139", "a140", "a141", "a142", "a143", "a144", "a145", "a146", "a147", "a148", "a149", "a150", "a151", "a152", "a153", "a154", "a155", "a156", "a157", "a158", "a159", "a160", "a161", "a162", "a163", "a164", "a165", "a166", "a167", "a168", "a169", "a170", "a171", "a172", "a173", "a174", "a175", "a176", "a177", "a178", "a179", "a180", "a181", "a182", "a183", "a184", "a185", "a186", "a187", "a188", "a189", "a190", "a191", "a192", "a193", "a194", "a195", "a196", "a197", "a198", "a199", "a200", "a201", "a202", "a203", "a204", "a205", "a206", "a207", "a208", "a209", "a210", "a211", "a212", "a213", "a214", "a215", "a216", "a217", "a218", "a219", "a220", "a221", "a222", "a223", "a224", "a225", "a226", "a227", "a228", "a229", "a230", "a231", "a232", "a233", "a234", "a235", "a236", "a237", "a238", "a239", "a240", "a241", "a242", "a243", "a244", "a245", "a246", "a247", "a248", "a249", "a250", "a251", "a252", "a253", "a254", "a255", "s8", "s9", "s10", "s11", "s12", "s13", "s14", "s15", "s16", "s17", "s18", "s19", "s20", "s21", "s22", "s23", "s24", "s25", "s26", "s27", "s28", "s29", "s30", "s31", "s32", "s33", "s34", "s35", "s36", "s37", "s38", "s39", "s40", "s41", "s42", "s43", "s44", "s45", "s46", "s47", "s48", "s49", "s50", "s51", "s52", "s53", "s54", "s55", "s56", "s57", "s58", "s59", "s60", "s61", "s62", "s63", "s64", "s65", "s66", "s67", "s68", "s69", "s70", "s71", "s72", "s73", "s74", "s75", "s76", "s77", "s78", "s79", "s80", "s81", "s82", "s83", "s84", "s85", "s86", "s87", "s88", "s89", "s90", "s91", "vcc", "scc", "memory");
}

extern "C" void kernel_launch(void* const* d_in, const int* in_sizes, int n_in, void* d_out, int out_size, void* d_ws, size_t ws_size,
                              hipStream_t stream) {
  const unsigned F = 0x20000u;
  KArgs a;
  a.k = {d_in[1], HKV * SQ * 256u, F}; a.v = {d_in[2], HKV * SQ * 256u, F}; a.x = {d_in[0], SQ * DM * 2u, F}; a.wq = {d_in[3], DM * 4096u * 2u, F};
  a.wo = {d_in[4], DM * 4096u * 2u, F}; a.ws = {d_ws, (unsigned)WS_END, F}; a.c = {d_in[10], DM * 2u, F}; a.wmod = {d_in[11], DM * 6u * DM * 2u, F};
  a.wg = {d_in[7], DM * FF * 2u, F}; a.wu = {d_in[8], DM * FF * 2u, F}; a.wd = {d_in[9], DM * FF * 2u, F}; a.out = {d_out, SQ * DM * 2u, F}; a.wr = {d_in[6], DM * 2u, F};
  (void)hipFuncSetAttribute((const void*)k_main, hipFuncAttributeMaxDynamicSharedMemorySize, 163840);
  k_main<<<256, 256, 163840, stream>>>(a);
}
```

```cpp
#include <hip/hip_runtime.h>
#include <stdint.h>


constexpr int SQ = 2048, DM = 576, HKV = 16, FF = 4608;

constexpr size_t WS_FLAG = 0;
constexpr size_t WS_R    = 20480;
constexpr size_t WS_GRAN = 32768;
constexpr size_t WS_EPOCH = 65536;
constexpr size_t WS_ST   = 69632;
constexpr size_t WS_PART = 139264;
constexpr size_t WS_ROW  = WS_PART + (size_t)HKV * SQ * DM * 2;
constexpr size_t WS_Y    = WS_ROW + (size_t)SQ * DM * 16;
constexpr size_t WS_END  = WS_Y + 72 * 192 * 128;

struct Buf { const void* p; unsigned bytes; unsigned fmt; };
struct KArgs { Buf k, v, x, wq, wo, ws, c, wmod, wg, wu, wd, out, wr; };

__global__ __launch_bounds__(256, 1) void k_main(KArgs args) {
  extern __shared__ __attribute__((aligned(16))) char lds[];
  const unsigned wid = __builtin_amdgcn_readfirstlane(threadIdx.x >> 6);
  const unsigned ldsb = (unsigned)(size_t)lds;
  asm volatile(
"AT_start:\n"
"s_load_dwordx16 s[24:39], %[kp], 64\n"
"s_load_dwordx16 s[8:23], %[kp], 0\n"
"s_load_dwordx16 s[64:79], %[kp], 128\n"
"s_load_dwordx4 s[80:83], %[kp], 192\n"
"s_mov_b32 s42, %[blk]\n"
"s_mov_b32 s41, %[wid]\n"
"s_mov_b32 s40, %[lds]\n"
"s_mov_b32 s45, 0\n"
"v_mbcnt_lo_u32_b32 v248, -1, 0\n"
"v_mbcnt_hi_u32_b32 v248, -1, v248\n"
"s_and_b32 s56, s42, 3\n"
"s_lshr_b32 s57, s42, 3\n"
"s_and_b32 s58, s57, 3\n"
"s_lshl_b32 s56, s56, 2\n"
"s_add_u32 s43, s56, s58\n"
"s_lshr_b32 s44, s57, 2\n"
"s_and_b32 s56, s42, 7\n"
"s_lshr_b32 s56, s56, 2\n"
"s_lshl_b32 s56, s56, 3\n"
"s_add_u32 s44, s44, s56\n"
"s_lshr_b32 s56, s42, 5\n"
"s_lshl_b32 s56, s56, 3\n"
"s_and_b32 s58, s42, 7\n"
"s_add_u32 s56, s56, s58\n"
"s_lshl_b32 s56, s56, 2\n"
"s_and_b32 s57, s57, 3\n"
"s_add_u32 s55, s56, s57\n"
"v_lshrrev_b32 v226, 4, v248\n"
"v_bfe_u32 v227, v248, 2, 2\n"
"v_and_b32 v228, 3, v248\n"
"v_and_b32 v229, 15, v248\n"
"v_lshrrev_b32 v230, 3, v248\n"
"v_and_b32 v231, 7, v248\n"
"s_lshl_b32 s56, s41, 6\n"
"v_add_u32 v234, s56, v248\n"
"v_lshrrev_b32 v232, 1, v234\n"
"v_and_b32 v233, 1, v234\n"
"v_lshlrev_b32 v236, 1, v232\n"
"v_mul_u32_u24 v232, 6912, v232\n"
"v_lshl_add_u32 v235, v233, 4, v232\n"
"s_lshl_b32 s3, s55, 5\n"
"s_add_u32 s4, s3, 884736\n"
"s_add_u32 s5, s3, 1769472\n"
"s_add_u32 s6, s3, 2654208\n"
"s_add_u32 s7, s3, 3538944\n"
"v_lshrrev_b32 v232, 1, v230\n"
"v_xor_b32 v232, v232, v231\n"
"v_mul_u32_u24 v233, 1152, v230\n"
"v_lshl_add_u32 v222, v232, 4, v233\n"
"v_xor_b32 v232, 4, v232\n"
"v_lshl_add_u32 v223, v232, 4, v233\n"
"s_lshl_b32 s61, s44, 2\n"
"s_add_u32 s61, s61, s41\n"
"s_mul_i32 s46, s61, 36864\n"
"s_add_u32 s47, s46, 9216\n"
"s_add_u32 s48, s46, 18432\n"
"s_add_u32 s49, s46, 27648\n"
"v_lshl_add_u32 v215, v231, 7, v233\n"
"s_and_b32 s62, s43, 3\n"
"s_mul_i32 s62, s62, 9216\n"
"s_add_u32 s62, s62, s46\n"
"s_mul_i32 s63, s41, 36864\n"
"s_add_u32 s63, s63, s40\n"
"s_getpc_b64 s[84:85]\n"
"AT_pc0:\n"
"s_sub_u32 s84, s84, AT_pc0-AT_start\n"
"s_subb_u32 s85, s85, 0\n"
"s_and_b32 s85, s85, 0xffff\n"
"s_mov_b32 s86, AT_kend-AT_start\n"
"s_mov_b32 s87, 0x20000\n"
"s_cmp_ge_u32 s42, 224\n"
"s_cselect_b32 s86, s86, 0\n"
"s_lshr_b32 s52, s42, 3\n"
"s_lshl_b32 s52, s52, 2\n"
"s_add_u32 s52, s52, s41\n"
"s_and_b32 s52, s52, 15\n"
"s_lshl_b32 s52, s52, 12\n"
"v_lshlrev_b32 v214, 6, v248\n"
"buffer_load_dword v250, v214, s[84:87], s52 offen\n"
"s_add_u32 s52, s52, 65536\n"
"buffer_load_dword v250, v214, s[84:87], s52 offen\n"
"s_waitcnt lgkmcnt(0)\n"
"s_load_dword s85, s[28:29], 65536\n"
"s_cmp_lt_u32 s55, 216\n"
"s_cselect_b32 s38, s38, 0\n"
"buffer_load_ushort v164, v236, s[32:35], 0 offen offset:0\n"
"buffer_load_ushort v165, v236, s[32:35], 0 offen offset:256\n"
"buffer_load_ushort v166, v236, s[32:35], 0 offen offset:512\n"
"buffer_load_ushort v167, v236, s[32:35], 0 offen offset:768\n"
"buffer_load_ushort v168, v236, s[32:35], 0 offen offset:1024\n"
"buffer_load_dwordx4 v[144:147], v235, s[36:39], s3 offen\n"
"buffer_load_dwordx4 v[148:151], v235, s[36:39], s4 offen\n"
"buffer_load_dwordx4 v[152:155], v235, s[36:39], s5 offen\n"
"buffer_load_dwordx4 v[156:159], v235, s[36:39], s6 offen\n"
"buffer_load_dwordx4 v[160:163], v235, s[36:39], s7 offen\n"
"s_mov_b32 m0, s63\n"
"s_nop 0\n"
"buffer_load_dwordx4 v222, s[16:19], s46 offen offset:0 lds\n"
"s_add_u32 m0, s63, 1024\n"
"s_nop 0\n"
"buffer_load_dwordx4 v223, s[16:19], s47 offen offset:0 lds\n"
"s_add_u32 m0, s63, 2048\n"
"s_nop 0\n"
"buffer_load_dwordx4 v222, s[16:19], s48 offen offset:0 lds\n"
"s_add_u32 m0, s63, 3072\n"
"s_nop 0\n"
"buffer_load_dwordx4 v223, s[16:19], s49 offen offset:0 lds\n"
"s_add_u32 m0, s63, 3968\n"
"s_nop 0\n"
"buffer_load_dwordx4 v222, s[16:19], s46 offen offset:128 lds\n"
"s_add_u32 m0, s63, 4992\n"
"s_nop 0\n"
"buffer_load_dwordx4 v223, s[16:19], s47 offen offset:128 lds\n"
"s_add_u32 m0, s63, 6016\n"
"s_nop 0\n"
"buffer_load_dwordx4 v222, s[16:19], s48 offen offset:128 lds\n"
"s_add_u32 m0, s63, 7040\n"
"s_nop 0\n"
"buffer_load_dwordx4 v223, s[16:19], s49 offen offset:128 lds\n"
"s_lshl_b32 s56, s41, 12\n"
"s_add_u32 s86, s40, s56\n"
"s_lshl_b32 s84, s41, 12\n"
"s_lshl_b32 s56, s43, 19\n"
"s_add_u32 s84, s84, s56\n"
"s_lshl_b32 s50, s41, 17\n"
"s_lshl_b32 s56, s43, 9\n"
"s_add_u32 s50, s50, s56\n"
"s_lshl_b32 s56, s41, 11\n"
"s_add_u32 s51, s40, s56\n"
"s_add_u32 m0, s63, 7936\n"
"s_nop 0\n"
"buffer_load_dwordx4 v222, s[16:19], s46 offen offset:256 lds\n"
"s_mov_b32 s61, 32768\n"
"s_mov_b32 s62, 37376\n"
"v_lshlrev_b32 v232, 13, v227\n"
"v_lshl_add_u32 v232, v226, 6, v232\n"
"v_lshl_add_u32 v217, v228, 4, v232\n"
"v_and_b32 v232, 2, v227\n"
"v_xor_b32 v232, v232, v226\n"
"v_lshlrev_b32 v233, 10, v227\n"
"v_lshl_add_u32 v233, v228, 6, v233\n"
"v_lshl_add_u32 v233, v232, 4, v233\n"
"s_add_u32 m0, s63, 8960\n"
"s_nop 0\n"
"buffer_load_dwordx4 v223, s[16:19], s47 offen offset:256 lds\n"
"v_add_u32 v240, s40, v233\n"
"v_add_u32 v241, 0x10000, v240\n"
"v_lshlrev_b32 v233, 10, v226\n"
"v_lshl_add_u32 v233, v227, 6, v233\n"
"v_lshl_add_u32 v233, v228, 3, v233\n"
"v_add_u32 v233, s40, v233\n"
"v_and_b32 v232, 1, v226\n"
"v_lshl_add_u32 v242, v232, 5, v233\n"
"v_xor_b32 v243, 32, v242\n"
"v_add_u32 v244, 0x10000, v242\n"
"s_add_u32 m0, s63, 9984\n"
"s_nop 0\n"
"buffer_load_dwordx4 v222, s[16:19], s48 offen offset:256 lds\n"
"v_add_u32 v245, 0x10000, v243\n"
"v_lshlrev_b32 v233, 8, v227\n"
"v_lshl_add_u32 v233, v226, 6, v233\n"
"v_lshl_add_u32 v246, v228, 4, v233\n"
"v_xor_b32 v247, 32, v246\n"
"v_mul_u32_u24 v233, 16392, v232\n"
"v_lshrrev_b32 v232, 1, v226\n"
"v_lshl_add_u32 v233, v232, 11, v233\n"
"v_lshl_add_u32 v233, v227, 6, v233\n"
"v_lshl_add_u32 v233, v228, 4, v233\n"
"s_add_u32 m0, s63, 11008\n"
"s_nop 0\n"
"buffer_load_dwordx4 v223, s[16:19], s49 offen offset:256 lds\n"
"v_add_u32 v218, s40, v233\n"
"v_add_u32 v219, 65568, v218\n"
"v_lshrrev_b32 v232, 1, v229\n"
"v_xor_b32 v232, v232, v226\n"
"v_lshlrev_b32 v233, 7, v229\n"
"v_lshl_add_u32 v233, v232, 4, v233\n"
"v_add_u32 v220, s63, v233\n"
"v_xor_b32 v221, 64, v220\n"
"s_add_u32 s56, s40, 148480\n"
"v_lshl_add_u32 v224, v226, 4, s56\n"
"s_add_u32 m0, s63, 11904\n"
"s_nop 0\n"
"buffer_load_dwordx4 v222, s[16:19], s46 offen offset:384 lds\n"
"v_lshrrev_b32 v232, 3, v234\n"
"v_lshlrev_b32 v233, 8, v232\n"
"v_and_b32 v232, 1, v234\n"
"v_lshl_add_u32 v233, v232, 6, v233\n"
"v_bfe_u32 v232, v234, 1, 2\n"
"v_lshl_add_u32 v233, v232, 4, v233\n"
"v_add_u32 v216, s56, v233\n"
"v_lshlrev_b32 v225, 5, v234\n"
"v_mov_b32 v232, 0x40000000\n"
"v_cmp_gt_u32 vcc, 144, v234\n"
"s_nop 1\n"
"v_cndmask_b32 v225, v232, v225, vcc\n"
"s_lshl_b32 s56, s41, 8\n"
"s_add_u32 s57, s40, 147456\n"
"s_add_u32 s56, s56, s57\n"
"v_and_b32 v232, 1, v248\n"
"v_lshlrev_b32 v232, 5, v232\n"
"v_lshl_add_u32 v232, v226, 6, v232\n"
"v_add_u32 v238, s56, v232\n"
"v_lshlrev_b32 v232, 8, v226\n"
"s_add_u32 m0, s63, 12928\n"
"s_nop 0\n"
"buffer_load_dwordx4 v223, s[16:19], s47 offen offset:384 lds\n"
"v_lshl_add_u32 v232, v229, 2, v232\n"
"v_add_u32 v239, s57, v232\n"
"v_lshlrev_b32 v232, 6, v228\n"
"v_lshl_add_u32 v232, v226, 4, v232\n"
"v_add_u32 v254, s40, v232\n"
"s_lshr_b32 s57, s42, 3\n"
"s_lshl_b32 s57, s57, 2\n"
"s_add_u32 s57, s57, s41\n"
"s_sub_u32 s57, s57, 128\n"
"s_and_b32 s58, s42, 7\n"
"s_add_u32 m0, s63, 13952\n"
"s_nop 0\n"
"buffer_load_dwordx4 v222, s[16:19], s48 offen offset:384 lds\n"
"s_mul_i32 s59, s57, 9216\n"
"s_mul_i32 s60, s58, 1152\n"
"s_add_u32 s59, s59, s60\n"
"v_lshlrev_b32 v252, 6, v248\n"
"v_add_u32 v253, 0x40000000, v252\n"
"v_cmp_gt_u32 vcc, 18, v248\n"
"s_nop 1\n"
"v_cndmask_b32 v251, v253, v252, vcc\n"
"v_cmp_gt_u32 vcc, 36, v248\n"
"s_nop 1\n"
"v_cndmask_b32 v253, v253, v252, vcc\n"
"s_mul_i32 s60, s57, 1152\n"
"s_lshl_b32 s57, s60, 1\n"
"v_add_u32 v252, s60, v251\n"
"v_add_u32 v251, s59, v251\n"
"v_add_u32 v253, s57, v253\n"
"v_mov_b32 v237, 0xffff0000\n"
"v_mov_b32 v170, 0x3f803f80\n"
"v_mov_b32 v171, 0x3f803f80\n"
"v_mov_b32 v172, 0x3f803f80\n"
"s_add_u32 m0, s63, 14976\n"
"s_nop 0\n"
"buffer_load_dwordx4 v223, s[16:19], s49 offen offset:384 lds\n"
"v_mov_b32 v173, 0x3f803f80\n"
"s_waitcnt vmcnt(21)\n"
"v_lshlrev_b32 v164, 16, v164\n"
"v_mul_f32 v184, 0xbfb8aa3b, v164\n"
"v_lshlrev_b32 v165, 16, v165\n"
"v_mul_f32 v185, 0xbfb8aa3b, v165\n"
"v_lshlrev_b32 v166, 16, v166\n"
"v_mul_f32 v186, 0xbfb8aa3b, v166\n"
"v_lshlrev_b32 v167, 16, v167\n"
"v_mul_f32 v187, 0xbfb8aa3b, v167\n"
"v_lshlrev_b32 v168, 16, v168\n"
"v_mul_f32 v188, 0xbfb8aa3b, v168\n"
"v_exp_f32 v184, v184\n"
"s_add_u32 m0, s63, 15872\n"
"s_nop 0\n"
"buffer_load_dwordx4 v222, s[16:19], s46 offen offset:512 lds\n"
"v_exp_f32 v185, v185\n"
"v_exp_f32 v186, v186\n"
"v_exp_f32 v187, v187\n"
"v_exp_f32 v188, v188\n"
"v_add_f32 v184, 1.0, v184\n"
"v_add_f32 v185, 1.0, v185\n"
"v_add_f32 v186, 1.0, v186\n"
"v_add_f32 v187, 1.0, v187\n"
"v_add_f32 v188, 1.0, v188\n"
"v_rcp_f32 v184, v184\n"
"v_rcp_f32 v185, v185\n"
"v_rcp_f32 v186, v186\n"
"s_add_u32 m0, s63, 16896\n"
"s_nop 0\n"
"buffer_load_dwordx4 v223, s[16:19], s47 offen offset:512 lds\n"
"v_rcp_f32 v187, v187\n"
"v_rcp_f32 v188, v188\n"
"v_mul_f32 v164, v164, v184\n"
"v_mul_f32 v165, v165, v185\n"
"v_mul_f32 v166, v166, v186\n"
"v_mul_f32 v167, v167, v187\n"
"v_mul_f32 v168, v168, v188\n"
"v_cvt_pk_bf16_f32 v164, v164, v164\n"
"v_and_b32 v164, v237, v164\n"
"v_cvt_pk_bf16_f32 v165, v165, v165\n"
"v_and_b32 v165, v237, v165\n"
"v_cvt_pk_bf16_f32 v166, v166, v166\n"
"s_add_u32 m0, s63, 17920\n"
"s_nop 0\n"
"buffer_load_dwordx4 v222, s[16:19], s48 offen offset:512 lds\n"
"v_and_b32 v166, v237, v166\n"
"v_cvt_pk_bf16_f32 v167, v167, v167\n"
"v_and_b32 v167, v237, v167\n"
"v_cvt_pk_bf16_f32 v168, v168, v168\n"
"v_and_b32 v168, v237, v168\n"
"s_waitcnt vmcnt(23)\n"
"v_lshlrev_b32 v184, 16, v144\n"
"v_and_b32 v185, v237, v144\n"
"v_lshlrev_b32 v186, 16, v145\n"
"v_and_b32 v187, v237, v145\n"
"v_lshlrev_b32 v188, 16, v146\n"
"v_and_b32 v189, v237, v146\n"
"s_add_u32 m0, s63, 18944\n"
"s_nop 0\n"
"buffer_load_dwordx4 v223, s[16:19], s49 offen offset:512 lds\n"
"v_lshlrev_b32 v190, 16, v147\n"
"v_and_b32 v191, v237, v147\n"
"v_mul_f32 v176, v164, v184\n"
"v_mul_f32 v177, v164, v185\n"
"v_mul_f32 v178, v164, v186\n"
"v_mul_f32 v179, v164, v187\n"
"v_mul_f32 v180, v164, v188\n"
"v_mul_f32 v181, v164, v189\n"
"v_mul_f32 v182, v164, v190\n"
"v_mul_f32 v183, v164, v191\n"
"s_waitcnt vmcnt(23)\n"
"v_lshlrev_b32 v184, 16, v148\n"
"s_add_u32 m0, s63, 19840\n"
"s_nop 0\n"
"buffer_load_dwordx4 v222, s[16:19], s46 offen offset:640 lds\n"
"v_and_b32 v185, v237, v148\n"
"v_lshlrev_b32 v186, 16, v149\n"
"v_and_b32 v187, v237, v149\n"
"v_lshlrev_b32 v188, 16, v150\n"
"v_and_b32 v189, v237, v150\n"
"v_lshlrev_b32 v190, 16, v151\n"
"v_and_b32 v191, v237, v151\n"
"v_fmac_f32 v176, v165, v184\n"
"v_fmac_f32 v177, v165, v185\n"
"v_fmac_f32 v178, v165, v186\n"
"v_fmac_f32 v179, v165, v187\n"
"v_fmac_f32 v180, v165, v188\n"
"s_add_u32 m0, s63, 20864\n"
"s_nop 0\n"
"buffer_load_dwordx4 v223, s[16:19], s47 offen offset:640 lds\n"
"v_fmac_f32 v181, v165, v189\n"
"v_fmac_f32 v182, v165, v190\n"
"v_fmac_f32 v183, v165, v191\n"
"s_waitcnt vmcnt(24)\n"
"v_lshlrev_b32 v184, 16, v152\n"
"v_and_b32 v185, v237, v152\n"
"v_lshlrev_b32 v186, 16, v153\n"
"v_and_b32 v187, v237, v153\n"
"v_lshlrev_b32 v188, 16, v154\n"
"v_and_b32 v189, v237, v154\n"
"v_lshlrev_b32 v190, 16, v155\n"
"v_and_b32 v191, v237, v155\n"
"s_add_u32 m0, s63, 21888\n"
"s_nop 0\n"
"buffer_load_dwordx4 v222, s[16:19], s48 offen offset:640 lds\n"
"v_fmac_f32 v176, v166, v184\n"
"v_fmac_f32 v177, v166, v185\n"
"v_fmac_f32 v178, v166, v186\n"
"v_fmac_f32 v179, v166, v187\n"
"v_fmac_f32 v180, v166, v188\n"
"v_fmac_f32 v181, v166, v189\n"
"v_fmac_f32 v182, v166, v190\n"
"v_fmac_f32 v183, v166, v191\n"
"s_waitcnt vmcnt(24)\n"
"v_lshlrev_b32 v184, 16, v156\n"
"v_and_b32 v185, v237, v156\n"
"v_lshlrev_b32 v186, 16, v157\n"
"s_add_u32 m0, s63, 22912\n"
"s_nop 0\n"
"buffer_load_dwordx4 v223, s[16:19], s49 offen offset:640 lds\n"
"v_and_b32 v187, v237, v157\n"
"v_lshlrev_b32 v188, 16, v158\n"
"v_and_b32 v189, v237, v158\n"
"v_lshlrev_b32 v190, 16, v159\n"
"v_and_b32 v191, v237, v159\n"
"v_fmac_f32 v176, v167, v184\n"
"v_fmac_f32 v177, v167, v185\n"
"v_fmac_f32 v178, v167, v186\n"
"v_fmac_f32 v179, v167, v187\n"
"v_fmac_f32 v180, v167, v188\n"
"v_fmac_f32 v181, v167, v189\n"
"v_fmac_f32 v182, v167, v190\n"
"s_add_u32 m0, s63, 23808\n"
"s_nop 0\n"
"buffer_load_dwordx4 v222, s[16:19], s46 offen offset:768 lds\n"
"v_fmac_f32 v183, v167, v191\n"
"s_waitcnt vmcnt(25)\n"
"v_lshlrev_b32 v184, 16, v160\n"
"v_and_b32 v185, v237, v160\n"
"v_lshlrev_b32 v186, 16, v161\n"
"v_and_b32 v187, v237, v161\n"
"v_lshlrev_b32 v188, 16, v162\n"
"v_and_b32 v189, v237, v162\n"
"v_lshlrev_b32 v190, 16, v163\n"
"v_and_b32 v191, v237, v163\n"
"v_fmac_f32 v176, v168, v184\n"
"v_fmac_f32 v177, v168, v185\n"
"s_add_u32 m0, s63, 24832\n"
"s_nop 0\n"
"buffer_load_dwordx4 v223, s[16:19], s47 offen offset:768 lds\n"
"v_fmac_f32 v178, v168, v186\n"
"v_fmac_f32 v179, v168, v187\n"
"v_fmac_f32 v180, v168, v188\n"
"v_fmac_f32 v181, v168, v189\n"
"v_fmac_f32 v182, v168, v190\n"
"v_fmac_f32 v183, v168, v191\n"
"v_add_f32_dpp v176, v176, v176 row_ror:2 row_mask:0xf bank_mask:0xf\n"
"v_add_f32_dpp v177, v177, v177 row_ror:2 row_mask:0xf bank_mask:0xf\n"
"v_add_f32_dpp v178, v178, v178 row_ror:2 row_mask:0xf bank_mask:0xf\n"
"v_add_f32_dpp v179, v179, v179 row_ror:2 row_mask:0xf bank_mask:0xf\n"
"v_add_f32_dpp v180, v180, v180 row_ror:2 row_mask:0xf bank_mask:0xf\n"
"v_add_f32_dpp v181, v181, v181 row_ror:2 row_mask:0xf bank_mask:0xf\n"
"s_add_u32 m0, s63, 25856\n"
"s_nop 0\n"
"buffer_load_dwordx4 v222, s[16:19], s48 offen offset:768 lds\n"
"v_add_f32_dpp v182, v182, v182 row_ror:2 row_mask:0xf bank_mask:0xf\n"
"v_add_f32_dpp v183, v183, v183 row_ror:2 row_mask:0xf bank_mask:0xf\n"
"v_add_f32_dpp v176, v176, v176 row_ror:4 row_mask:0xf bank_mask:0xf\n"
"v_add_f32_dpp v177, v177, v177 row_ror:4 row_mask:0xf bank_mask:0xf\n"
"v_add_f32_dpp v178, v178, v178 row_ror:4 row_mask:0xf bank_mask:0xf\n"
"v_add_f32_dpp v179, v179, v179 row_ror:4 row_mask:0xf bank_mask:0xf\n"
"v_add_f32_dpp v180, v180, v180 row_ror:4 row_mask:0xf bank_mask:0xf\n"
"v_add_f32_dpp v181, v181, v181 row_ror:4 row_mask:0xf bank_mask:0xf\n"
"v_add_f32_dpp v182, v182, v182 row_ror:4 row_mask:0xf bank_mask:0xf\n"
"v_add_f32_dpp v183, v183, v183 row_ror:4 row_mask:0xf bank_mask:0xf\n"
"v_add_f32_dpp v176, v176, v176 row_ror:8 row_mask:0xf bank_mask:0xf\n"
"v_add_f32_dpp v177, v177, v177 row_ror:8 row_mask:0xf bank_mask:0xf\n"
"s_add_u32 m0, s63, 26880\n"
"s_nop 0\n"
"buffer_load_dwordx4 v223, s[16:19], s49 offen offset:768 lds\n"
"v_add_f32_dpp v178, v178, v178 row_ror:8 row_mask:0xf bank_mask:0xf\n"
"v_add_f32_dpp v179, v179, v179 row_ror:8 row_mask:0xf bank_mask:0xf\n"
"v_add_f32_dpp v180, v180, v180 row_ror:8 row_mask:0xf bank_mask:0xf\n"
"v_add_f32_dpp v181, v181, v181 row_ror:8 row_mask:0xf bank_mask:0xf\n"
"v_add_f32_dpp v182, v182, v182 row_ror:8 row_mask:0xf bank_mask:0xf\n"
"v_add_f32_dpp v183, v183, v183 row_ror:8 row_mask:0xf bank_mask:0xf\n"
"s_mov_b32 exec_lo, 0x00030003\n"
"s_mov_b32 exec_hi, 0x00030003\n"
"ds_write_b128 v238, v[176:179]\n"
"ds_write_b128 v238, v[180:183] offset:16\n"
"s_mov_b64 exec, -1\n"
"s_waitcnt lgkmcnt(0)\n"
"s_barrier\n"
"ds_read_b32 v184, v239 offset:0\n"
"ds_read_b32 v185, v239 offset:64\n"
"ds_read_b32 v186, v239 offset:128\n"
"ds_read_b32 v187, v239 offset:192\n"
"s_add_u32 s85, s85, 1\n"
"s_lshl_b32 s56, s55, 7\n"
"s_add_u32 s56, s56, 32768\n"
"s_lshl_b32 s58, s41, 2\n"
"s_lshl_b32 s58, 15, s58\n"
"v_lshlrev_b32 v190, 3, v248\n"
"v_mov_b32 v189, s85\n"
"s_waitcnt lgkmcnt(2)\n"
"v_add_f32 v184, v184, v185\n"
"s_waitcnt lgkmcnt(0)\n"
"v_add_f32 v186, v186, v187\n"
"v_add_f32 v184, v184, v186\n"
"v_mov_b32 v185, v184\n"
"s_nop 1\n"
"v_permlane32_swap_b32 v184, v185\n"
"v_add_f32 v184, v184, v185\n"
"v_mov_b32 v185, v184\n"
"s_nop 1\n"
"v_permlane16_swap_b32 v184, v185\n"
"v_add_f32 v184, v184, v185\n"
"v_cvt_pk_bf16_f32 v188, v184, v184\n"
"v_and_b32 v188, v237, v188\n"
"s_mov_b32 exec_lo, s58\n"
"s_mov_b32 exec_hi, 0\n"
"buffer_store_dwordx2 v[188:189], v190, s[28:31], s56 offen sc1\n"
"s_mov_b64 exec, -1\n"
"s_waitcnt vmcnt(25)\n"
"ds_read_b128 v[0:3], v220 offset:0\n"
"ds_read_b128 v[4:7], v220 offset:2048\n"
"ds_read_b128 v[8:11], v221 offset:0\n"
"ds_read_b128 v[12:15], v221 offset:2048\n"
"s_waitcnt vmcnt(21)\n"
"ds_read_b128 v[16:19], v220 offset:4096\n"
"ds_read_b128 v[20:23], v220 offset:6144\n"
"ds_read_b128 v[24:27], v221 offset:4096\n"
"ds_read_b128 v[28:31], v221 offset:6144\n"
"s_waitcnt lgkmcnt(7)\n"
"v_mfma_f32_16x16x32_bf16 v[192:195], v[170:173], v[0:3], 0\n"
"v_mfma_f32_16x16x32_bf16 v[200:203], v[0:3], v[0:3], 0\n"
"s_waitcnt lgkmcnt(6)\n"
"v_mfma_f32_16x16x32_bf16 v[196:199], v[170:173], v[4:7], 0\n"
"v_mfma_f32_16x16x32_bf16 v[204:207], v[4:7], v[4:7], 0\n"
"s_waitcnt lgkmcnt(5)\n"
"v_mfma_f32_16x16x32_bf16 v[192:195], v[170:173], v[8:11], v[192:195]\n"
"v_mfma_f32_16x16x32_bf16 v[200:203], v[8:11], v[8:11], v[200:203]\n"
"s_waitcnt lgkmcnt(4)\n"
"v_mfma_f32_16x16x32_bf16 v[196:199], v[170:173], v[12:15], v[196:199]\n"
"v_mfma_f32_16x16x32_bf16 v[204:207], v[12:15], v[12:15], v[204:207]\n"
"s_waitcnt lgkmcnt(3)\n"
"s_waitcnt vmcnt(17)\n"
"ds_read_b128 v[32:35], v220 offset:8192\n"
"ds_read_b128 v[36:39], v220 offset:10240\n"
"ds_read_b128 v[40:43], v221 offset:8192\n"
"ds_read_b128 v[44:47], v221 offset:10240\n"
"v_mfma_f32_16x16x32_bf16 v[192:195], v[170:173], v[16:19], v[192:195]\n"
"v_mfma_f32_16x16x32_bf16 v[200:203], v[16:19], v[16:19], v[200:203]\n"
"s_waitcnt lgkmcnt(6)\n"
"v_mfma_f32_16x16x32_bf16 v[196:199], v[170:173], v[20:23], v[196:199]\n"
"v_mfma_f32_16x16x32_bf16 v[204:207], v[20:23], v[20:23], v[204:207]\n"
"s_waitcnt lgkmcnt(4)\n"
"v_mfma_f32_16x16x32_bf16 v[192:195], v[170:173], v[24:27], v[192:195]\n"
"v_mfma_f32_16x16x32_bf16 v[200:203], v[24:27], v[24:27], v[200:203]\n"
"v_mfma_f32_16x16x32_bf16 v[196:199], v[170:173], v[28:31], v[196:199]\n"
"v_mfma_f32_16x16x32_bf16 v[204:207], v[28:31], v[28:31], v[204:207]\n"
"s_waitcnt lgkmcnt(3)\n"
"s_waitcnt vmcnt(13)\n"
"ds_read_b128 v[48:51], v220 offset:12288\n"
"ds_read_b128 v[52:55], v220 offset:14336\n"
"ds_read_b128 v[56:59], v221 offset:12288\n"
"ds_read_b128 v[60:63], v221 offset:14336\n"
"v_mfma_f32_16x16x32_bf16 v[192:195], v[170:173], v[32:35], v[192:195]\n"
"v_mfma_f32_16x16x32_bf16 v[200:203], v[32:35], v[32:35], v[200:203]\n"
"s_waitcnt lgkmcnt(6)\n"
"v_mfma_f32_16x16x32_bf16 v[196:199], v[170:173], v[36:39], v[196:199]\n"
"v_mfma_f32_16x16x32_bf16 v[204:207], v[36:39], v[36:39], v[204:207]\n"
"s_waitcnt lgkmcnt(4)\n"
"v_mfma_f32_16x16x32_bf16 v[192:195], v[170:173], v[40:43], v[192:195]\n"
"v_mfma_f32_16x16x32_bf16 v[200:203], v[40:43], v[40:43], v[200:203]\n"
"v_mfma_f32_16x16x32_bf16 v[196:199], v[170:173], v[44:47], v[196:199]\n"
"v_mfma_f32_16x16x32_bf16 v[204:207], v[44:47], v[44:47], v[204:207]\n"
"s_waitcnt lgkmcnt(3)\n"
"s_add_u32 m0, s63, 27776\n"
"s_nop 0\n"
"buffer_load_dwordx4 v222, s[16:19], s46 offen offset:896 lds\n"
"s_add_u32 m0, s63, 28800\n"
"s_nop 0\n"
"buffer_load_dwordx4 v223, s[16:19], s47 offen offset:896 lds\n"
"s_add_u32 m0, s63, 29824\n"
"s_nop 0\n"
"buffer_load_dwordx4 v222, s[16:19], s48 offen offset:896 lds\n"
"s_add_u32 m0, s63, 30848\n"
"s_nop 0\n"
"buffer_load_dwordx4 v223, s[16:19], s49 offen offset:896 lds\n"
"s_waitcnt vmcnt(13)\n"
"ds_read_b128 v[64:67], v220 offset:16384\n"
"ds_read_b128 v[68:71], v220 offset:18432\n"
"ds_read_b128 v[72:75], v221 offset:16384\n"
"ds_read_b128 v[76:79], v221 offset:18432\n"
"v_mfma_f32_16x16x32_bf16 v[192:195], v[170:173], v[48:51], v[192:195]\n"
"v_mfma_f32_16x16x32_bf16 v[200:203], v[48:51], v[48:51], v[200:203]\n"
"s_waitcnt lgkmcnt(6)\n"
"v_mfma_f32_16x16x32_bf16 v[196:199], v[170:173], v[52:55], v[196:199]\n"
"v_mfma_f32_16x16x32_bf16 v[204:207], v[52:55], v[52:55], v[204:207]\n"
"s_waitcnt lgkmcnt(4)\n"
"v_mfma_f32_16x16x32_bf16 v[192:195], v[170:173], v[56:59], v[192:195]\n"
"v_mfma_f32_16x16x32_bf16 v[200:203], v[56:59], v[56:59], v[200:203]\n"
"v_mfma_f32_16x16x32_bf16 v[196:199], v[170:173], v[60:63], v[196:199]\n"
"v_mfma_f32_16x16x32_bf16 v[204:207], v[60:63], v[60:63], v[204:207]\n"
"s_waitcnt lgkmcnt(3)\n"
"s_add_u32 m0, s63, 31744\n"
"s_nop 0\n"
"buffer_load_dwordx4 v222, s[16:19], s46 offen offset:1024 lds\n"
"s_add_u32 m0, s63, 32768\n"
"s_nop 0\n"
"buffer_load_dwordx4 v223, s[16:19], s47 offen offset:1024 lds\n"
"s_add_u32 m0, s63, 33792\n"
"s_nop 0\n"
"buffer_load_dwordx4 v222, s[16:19], s48 offen offset:1024 lds\n"
"s_add_u32 m0, s63, 34816\n"
"s_nop 0\n"
"buffer_load_dwordx4 v223, s[16:19], s49 offen offset:1024 lds\n"
"s_waitcnt vmcnt(13)\n"
"ds_read_b128 v[80:83], v220 offset:20480\n"
"ds_read_b128 v[84:87], v220 offset:22528\n"
"ds_read_b128 v[88:91], v221 offset:20480\n"
"ds_read_b128 v[92:95], v221 offset:22528\n"
"v_mfma_f32_16x16x32_bf16 v[192:195], v[170:173], v[64:67], v[192:195]\n"
"v_mfma_f32_16x16x32_bf16 v[200:203], v[64:67], v[64:67], v[200:203]\n"
"s_waitcnt lgkmcnt(6)\n"
"v_mfma_f32_16x16x32_bf16 v[196:199], v[170:173], v[68:71], v[196:199]\n"
"v_mfma_f32_16x16x32_bf16 v[204:207], v[68:71], v[68:71], v[204:207]\n"
"s_waitcnt lgkmcnt(4)\n"
"v_mfma_f32_16x16x32_bf16 v[192:195], v[170:173], v[72:75], v[192:195]\n"
"v_mfma_f32_16x16x32_bf16 v[200:203], v[72:75], v[72:75], v[200:203]\n"
"v_mfma_f32_16x16x32_bf16 v[196:199], v[170:173], v[76:79], v[196:199]\n"
"v_mfma_f32_16x16x32_bf16 v[204:207], v[76:79], v[76:79], v[204:207]\n"
"s_waitcnt lgkmcnt(3)\n"
"s_waitcnt vmcnt(9)\n"
"ds_read_b128 v[96:99], v220 offset:24576\n"
"ds_read_b128 v[100:103], v220 offset:26624\n"
"ds_read_b128 v[104:107], v221 offset:24576\n"
"ds_read_b128 v[108:111], v221 offset:26624\n"
"v_mfma_f32_16x16x32_bf16 v[192:195], v[170:173], v[80:83], v[192:195]\n"
"v_mfma_f32_16x16x32_bf16 v[200:203], v[80:83], v[80:83], v[200:203]\n"
"s_waitcnt lgkmcnt(6)\n"
"v_mfma_f32_16x16x32_bf16 v[196:199], v[170:173], v[84:87], v[196:199]\n"
"v_mfma_f32_16x16x32_bf16 v[204:207], v[84:87], v[84:87], v[204:207]\n"
"s_waitcnt lgkmcnt(4)\n"
"v_mfma_f32_16x16x32_bf16 v[192:195], v[170:173], v[88:91], v[192:195]\n"
"v_mfma_f32_16x16x32_bf16 v[200:203], v[88:91], v[88:91], v[200:203]\n"
"v_mfma_f32_16x16x32_bf16 v[196:199], v[170:173], v[92:95], v[196:199]\n"
"v_mfma_f32_16x16x32_bf16 v[204:207], v[92:95], v[92:95], v[204:207]\n"
"s_waitcnt lgkmcnt(3)\n"
"s_waitcnt vmcnt(4)\n"
"ds_read_b128 v[112:115], v220 offset:28672\n"
"ds_read_b128 v[116:119], v220 offset:30720\n"
"ds_read_b128 v[120:123], v221 offset:28672\n"
"ds_read_b128 v[124:127], v221 offset:30720\n"
"v_mfma_f32_16x16x32_bf16 v[192:195], v[170:173], v[96:99], v[192:195]\n"
"v_mfma_f32_16x16x32_bf16 v[200:203], v[96:99], v[96:99], v[200:203]\n"
"s_waitcnt lgkmcnt(6)\n"
"v_mfma_f32_16x16x32_bf16 v[196:199], v[170:173], v[100:103], v[196:199]\n"
"v_mfma_f32_16x16x32_bf16 v[204:207], v[100:103], v[100:103], v[204:207]\n"
"s_waitcnt lgkmcnt(4)\n"
"v_mfma_f32_16x16x32_bf16 v[192:195], v[170:173], v[104:107], v[192:195]\n"
"v_mfma_f32_16x16x32_bf16 v[200:203], v[104:107], v[104:107], v[200:203]\n"
"v_mfma_f32_16x16x32_bf16 v[196:199], v[170:173], v[108:111], v[196:199]\n"
"v_mfma_f32_16x16x32_bf16 v[204:207], v[108:111], v[108:111], v[204:207]\n"
"s_waitcnt lgkmcnt(3)\n"
"s_waitcnt vmcnt(0)\n"
"ds_read_b128 v[128:131], v220 offset:32768\n"
"ds_read_b128 v[132:135], v220 offset:34816\n"
"ds_read_b128 v[136:139], v221 offset:32768\n"
"ds_read_b128 v[140:143], v221 offset:34816\n"
"v_mfma_f32_16x16x32_bf16 v[192:195], v[170:173], v[112:115], v[192:195]\n"
"v_mfma_f32_16x16x32_bf16 v[200:203], v[112:115], v[112:115], v[200:203]\n"
"s_waitcnt lgkmcnt(6)\n"
"v_mfma_f32_16x16x32_bf16 v[196:199], v[170:173], v[116:119], v[196:199]\n"
"v_mfma_f32_16x16x32_bf16 v[204:207], v[116:119], v[116:119], v[204:207]\n"
"s_waitcnt lgkmcnt(4)\n"
"v_mfma_f32_16x16x32_bf16 v[192:195], v[170:173], v[120:123], v[192:195]\n"
"v_mfma_f32_16x16x32_bf16 v[200:203], v[120:123], v[120:123], v[200:203]\n"
"v_mfma_f32_16x16x32_bf16 v[196:199], v[170:173], v[124:127], v[196:199]\n"
"v_mfma_f32_16x16x32_bf16 v[204:207], v[124:127], v[124:127], v[204:207]\n"
"s_waitcnt lgkmcnt(0)\n"
"s_barrier\n"
"v_mfma_f32_16x16x32_bf16 v[192:195], v[170:173], v[128:131], v[192:195]\n"
"v_mfma_f32_16x16x32_bf16 v[200:203], v[128:131], v[128:131], v[200:203]\n"
"v_mfma_f32_16x16x32_bf16 v[196:199], v[170:173], v[132:135], v[196:199]\n"
"v_mfma_f32_16x16x32_bf16 v[204:207], v[132:135], v[132:135], v[204:207]\n"
"v_mfma_f32_16x16x32_bf16 v[192:195], v[170:173], v[136:139], v[192:195]\n"
"v_mfma_f32_16x16x32_bf16 v[200:203], v[136:139], v[136:139], v[200:203]\n"
"v_mfma_f32_16x16x32_bf16 v[196:199], v[170:173], v[140:143], v[196:199]\n"
"v_mfma_f32_16x16x32_bf16 v[204:207], v[140:143], v[140:143], v[204:207]\n"
"s_add_u32 m0, s51, 98352\n"
"s_nop 0\n"
"buffer_load_dwordx4 v217, s[20:23], s50 offen offset:0 lds\n"
"s_add_u32 m0, s51, 106288\n"
"s_nop 0\n"
"buffer_load_dwordx4 v217, s[20:23], s50 offen offset:256 lds\n"
"s_add_u32 s50, s50, 32768\n"
"v_cmp_eq_u32 s[56:57], 1, v228\n"
"v_cmp_eq_u32 s[58:59], 2, v228\n"
"v_cmp_eq_u32 vcc, 3, v228\n"
"s_nop 4\n"
"v_cndmask_b32 v200, v200, v201, s[56:57]\n"
"v_cndmask_b32 v200, v200, v202, s[58:59]\n"
"v_cndmask_b32 v200, v200, v203, vcc\n"
"v_cndmask_b32 v204, v204, v205, s[56:57]\n"
"v_cndmask_b32 v204, v204, v206, s[58:59]\n"
"v_cndmask_b32 v204, v204, v207, vcc\n"
"v_cmp_eq_u32 vcc, v226, v227\n"
"v_mov_b32 v208, v192\n"
"v_mov_b32 v212, v196\n"
"v_cndmask_b32 v210, 0, v200, vcc\n"
"v_cndmask_b32 v214, 0, v204, vcc\n"
"v_mov_b32 v192, v210\n"
"v_mov_b32 v193, v214\n"
"s_nop 0\n"
"v_permlane32_swap_b32 v210, v192\n"
"v_permlane32_swap_b32 v214, v193\n"
"v_add_f32 v210, v210, v192\n"
"v_add_f32 v214, v214, v193\n"
"v_mov_b32 v192, v210\n"
"v_mov_b32 v193, v214\n"
"s_nop 0\n"
"v_permlane16_swap_b32 v210, v192\n"
"v_permlane16_swap_b32 v214, v193\n"
"v_add_f32 v210, v210, v192\n"
"v_add_f32 v214, v214, v193\n"
"v_mul_f32 v208, 0x3ae38e39, v208\n"
"v_mul_f32 v210, 0x3ae38e39, v210\n"
"v_fma_f32 v210, -v208, v208, v210\n"
"v_max_f32 v210, 0, v210\n"
"v_add_f32 v210, 0x3727c5ac, v210\n"
"v_mul_f32 v212, 0x3ae38e39, v212\n"
"v_mul_f32 v214, 0x3ae38e39, v214\n"
"v_fma_f32 v214, -v212, v212, v214\n"
"v_max_f32 v214, 0, v214\n"
"v_add_f32 v214, 0x3727c5ac, v214\n"
"v_rsq_f32 v209, v210\n"
"v_rsq_f32 v213, v214\n"
"s_nop 0\n"
"v_mul_f32 v210, v208, v209\n"
"v_sub_f32 v210, 0, v210\n"
"v_mov_b32 v208, v209\n"
"v_mul_f32 v214, v212, v213\n"
"v_sub_f32 v214, 0, v214\n"
"v_mov_b32 v212, v213\n"
"v_lshlrev_b32 v176, 16, v0\n"
"v_and_b32 v177, v237, v0\n"
"v_lshlrev_b32 v178, 16, v1\n"
"v_and_b32 v179, v237, v1\n"
"v_lshlrev_b32 v180, 16, v2\n"
"v_and_b32 v181, v237, v2\n"
"v_lshlrev_b32 v182, 16, v3\n"
"v_and_b32 v183, v237, v3\n"
"v_pk_fma_f32 v[176:177], v[176:177], v[208:209], v[210:211] op_sel_hi:[1,0,0]\n"
"v_pk_fma_f32 v[178:179], v[178:179], v[208:209], v[210:211] op_sel_hi:[1,0,0]\n"
"v_pk_fma_f32 v[180:181], v[180:181], v[208:209], v[210:211] op_sel_hi:[1,0,0]\n"
"v_pk_fma_f32 v[182:183], v[182:183], v[208:209], v[210:211] op_sel_hi:[1,0,0]\n"
"v_cvt_pk_f16_f32 v0, v176, v177\n"
"v_cvt_pk_f16_f32 v1, v178, v179\n"
"v_cvt_pk_f16_f32 v2, v180, v181\n"
"v_cvt_pk_f16_f32 v3, v182, v183\n"
"v_lshlrev_b32 v184, 16, v4\n"
"v_and_b32 v185, v237, v4\n"
"v_lshlrev_b32 v186, 16, v5\n"
"v_and_b32 v187, v237, v5\n"
"v_lshlrev_b32 v188, 16, v6\n"
"s_add_u32 m0, s51, 99376\n"
"s_nop 0\n"
"buffer_load_dwordx4 v217, s[20:23], s50 offen offset:0 lds\n"
"v_and_b32 v189, v237, v6\n"
"v_lshlrev_b32 v190, 16, v7\n"
"v_and_b32 v191, v237, v7\n"
"v_pk_fma_f32 v[184:185], v[184:185], v[212:213], v[214:215] op_sel_hi:[1,0,0]\n"
"v_pk_fma_f32 v[186:187], v[186:187], v[212:213], v[214:215] op_sel_hi:[1,0,0]\n"
"v_pk_fma_f32 v[188:189], v[188:189], v[212:213], v[214:215] op_sel_hi:[1,0,0]\n"
"v_pk_fma_f32 v[190:191], v[190:191], v[212:213], v[214:215] op_sel_hi:[1,0,0]\n"
"v_cvt_pk_f16_f32 v4, v184, v185\n"
"v_cvt_pk_f16_f32 v5, v186, v187\n"
"v_cvt_pk_f16_f32 v6, v188, v189\n"
"v_cvt_pk_f16_f32 v7, v190, v191\n"
"v_lshlrev_b32 v176, 16, v8\n"
"v_and_b32 v177, v237, v8\n"
"v_lshlrev_b32 v178, 16, v9\n"
"v_and_b32 v179, v237, v9\n"
"v_lshlrev_b32 v180, 16, v10\n"
"v_and_b32 v181, v237, v10\n"
"v_lshlrev_b32 v182, 16, v11\n"
"v_and_b32 v183, v237, v11\n"
"v_pk_fma_f32 v[176:177], v[176:177], v[208:209], v[210:211] op_sel_hi:[1,0,0]\n"
"v_pk_fma_f32 v[178:179], v[178:179], v[208:209], v[210:211] op_sel_hi:[1,0,0]\n"
"s_add_u32 m0, s51, 107312\n"
"s_nop 0\n"
"buffer_load_dwordx4 v217, s[20:23], s50 offen offset:256 lds\n"
"s_add_u32 s50, s50, 32768\n"
"v_pk_fma_f32 v[180:181], v[180:181], v[208:209], v[210:211] op_sel_hi:[1,0,0]\n"
"v_pk_fma_f32 v[182:183], v[182:183], v[208:209], v[210:211] op_sel_hi:[1,0,0]\n"
"v_cvt_pk_f16_f32 v8, v176, v177\n"
"v_cvt_pk_f16_f32 v9, v178, v179\n"
"v_cvt_pk_f16_f32 v10, v180, v181\n"
"v_cvt_pk_f16_f32 v11, v182, v183\n"
"v_lshlrev_b32 v184, 16, v12\n"
"v_and_b32 v185, v237, v12\n"
"v_lshlrev_b32 v186, 16, v13\n"
"v_and_b32 v187, v237, v13\n"
"v_lshlrev_b32 v188, 16, v14\n"
"v_and_b32 v189, v237, v14\n"
"v_lshlrev_b32 v190, 16, v15\n"
"v_and_b32 v191, v237, v15\n"
"v_pk_fma_f32 v[184:185], v[184:185], v[212:213], v[214:215] op_sel_hi:[1,0,0]\n"
"v_pk_fma_f32 v[186:187], v[186:187], v[212:213], v[214:215] op_sel_hi:[1,0,0]\n"
"v_pk_fma_f32 v[188:189], v[188:189], v[212:213], v[214:215] op_sel_hi:[1,0,0]\n"
"v_pk_fma_f32 v[190:191], v[190:191], v[212:213], v[214:215] op_sel_hi:[1,0,0]\n"
"v_cvt_pk_f16_f32 v12, v184, v185\n"
"v_cvt_pk_f16_f32 v13, v186, v187\n"
"v_cvt_pk_f16_f32 v14, v188, v189\n"
"s_add_u32 m0, s51, 114744\n"
"s_nop 0\n"
"buffer_load_dwordx4 v217, s[20:23], s50 offen offset:0 lds\n"
"v_cvt_pk_f16_f32 v15, v190, v191\n"
"v_lshlrev_b32 v176, 16, v16\n"
"v_and_b32 v177, v237, v16\n"
"v_lshlrev_b32 v178, 16, v17\n"
"v_and_b32 v179, v237, v17\n"
"v_lshlrev_b32 v180, 16, v18\n"
"v_and_b32 v181, v237, v18\n"
"v_lshlrev_b32 v182, 16, v19\n"
"v_and_b32 v183, v237, v19\n"
"v_pk_fma_f32 v[176:177], v[176:177], v[208:209], v[210:211] op_sel_hi:[1,0,0]\n"
"v_pk_fma_f32 v[178:179], v[178:179], v[208:209], v[210:211] op_sel_hi:[1,0,0]\n"
"v_pk_fma_f32 v[180:181], v[180:181], v[208:209], v[210:211] op_sel_hi:[1,0,0]\n"
"v_pk_fma_f32 v[182:183], v[182:183], v[208:209], v[210:211] op_sel_hi:[1,0,0]\n"
"v_cvt_pk_f16_f32 v16, v176, v177\n"
"v_cvt_pk_f16_f32 v17, v178, v179\n"
"v_cvt_pk_f16_f32 v18, v180, v181\n"
"v_cvt_pk_f16_f32 v19, v182, v183\n"
"v_lshlrev_b32 v184, 16, v20\n"
"v_and_b32 v185, v237, v20\n"
"v_lshlrev_b32 v186, 16, v21\n"
"v_and_b32 v187, v237, v21\n"
"v_lshlrev_b32 v188, 16, v22\n"
"s_add_u32 m0, s51, 122680\n"
"s_nop 0\n"
"buffer_load_dwordx4 v217, s[20:23], s50 offen offset:256 lds\n"
"s_add_u32 s50, s50, 32768\n"
"v_and_b32 v189, v237, v22\n"
"v_lshlrev_b32 v190, 16, v23\n"
"v_and_b32 v191, v237, v23\n"
"v_pk_fma_f32 v[184:185], v[184:185], v[212:213], v[214:215] op_sel_hi:[1,0,0]\n"
"v_pk_fma_f32 v[186:187], v[186:187], v[212:213], v[214:215] op_sel_hi:[1,0,0]\n"
"v_pk_fma_f32 v[188:189], v[188:189], v[212:213], v[214:215] op_sel_hi:[1,0,0]\n"
"v_pk_fma_f32 v[190:191], v[190:191], v[212:213], v[214:215] op_sel_hi:[1,0,0]\n"
"v_cvt_pk_f16_f32 v20, v184, v185\n"
"v_cvt_pk_f16_f32 v21, v186, v187\n"
"v_cvt_pk_f16_f32 v22, v188, v189\n"
"v_cvt_pk_f16_f32 v23, v190, v191\n"
"v_lshlrev_b32 v176, 16, v24\n"
"v_and_b32 v177, v237, v24\n"
"v_lshlrev_b32 v178, 16, v25\n"
"v_and_b32 v179, v237, v25\n"
"v_lshlrev_b32 v180, 16, v26\n"
"v_and_b32 v181, v237, v26\n"
"v_lshlrev_b32 v182, 16, v27\n"
"v_and_b32 v183, v237, v27\n"
"v_pk_fma_f32 v[176:177], v[176:177], v[208:209], v[210:211] op_sel_hi:[1,0,0]\n"
"v_pk_fma_f32 v[178:179], v[178:179], v[208:209], v[210:211] op_sel_hi:[1,0,0]\n"
"s_add_u32 m0, s51, 115768\n"
"s_nop 0\n"
"buffer_load_dwordx4 v217, s[20:23], s50 offen offset:0 lds\n"
"v_pk_fma_f32 v[180:181], v[180:181], v[208:209], v[210:211] op_sel_hi:[1,0,0]\n"
"v_pk_fma_f32 v[182:183], v[182:183], v[208:209], v[210:211] op_sel_hi:[1,0,0]\n"
"v_cvt_pk_f16_f32 v24, v176, v177\n"
"v_cvt_pk_f16_f32 v25, v178, v179\n"
"v_cvt_pk_f16_f32 v26, v180, v181\n"
"v_cvt_pk_f16_f32 v27, v182, v183\n"
"v_lshlrev_b32 v184, 16, v28\n"
"v_and_b32 v185, v237, v28\n"
"v_lshlrev_b32 v186, 16, v29\n"
"v_and_b32 v187, v237, v29\n"
"v_lshlrev_b32 v188, 16, v30\n"
"v_and_b32 v189, v237, v30\n"
"v_lshlrev_b32 v190, 16, v31\n"
"v_and_b32 v191, v237, v31\n"
"v_pk_fma_f32 v[184:185], v[184:185], v[212:213], v[214:215] op_sel_hi:[1,0,0]\n"
"v_pk_fma_f32 v[186:187], v[186:187], v[212:213], v[214:215] op_sel_hi:[1,0,0]\n"
"v_pk_fma_f32 v[188:189], v[188:189], v[212:213], v[214:215] op_sel_hi:[1,0,0]\n"
"v_pk_fma_f32 v[190:191], v[190:191], v[212:213], v[214:215] op_sel_hi:[1,0,0]\n"
"v_cvt_pk_f16_f32 v28, v184, v185\n"
"v_cvt_pk_f16_f32 v29, v186, v187\n"
"v_cvt_pk_f16_f32 v30, v188, v189\n"
"s_add_u32 m0, s51, 123704\n"
"s_nop 0\n"
"buffer_load_dwordx4 v217, s[20:23], s50 offen offset:256 lds\n"
"s_add_u32 s50, s50, 425984\n"
"v_cvt_pk_f16_f32 v31, v190, v191\n"
"buffer_load_dwordx4 v[144:147], v225, s[28:31], s61 offen offset:0 sc1\n"
"buffer_load_dwordx4 v[148:151], v225, s[28:31], s61 offen offset:16 sc1\n"
"buffer_load_dwordx4 v[152:155], v225, s[28:31], s62 offen offset:0 sc1\n"
"buffer_load_dwordx4 v[156:159], v225, s[28:31], s62 offen offset:16 sc1\n"
"v_lshlrev_b32 v176, 16, v32\n"
"v_and_b32 v177, v237, v32\n"
"v_lshlrev_b32 v178, 16, v33\n"
"v_and_b32 v179, v237, v33\n"
"v_lshlrev_b32 v180, 16, v34\n"
"v_and_b32 v181, v237, v34\n"
"v_lshlrev_b32 v182, 16, v35\n"
"v_and_b32 v183, v237, v35\n"
"v_pk_fma_f32 v[176:177], v[176:177], v[208:209], v[210:211] op_sel_hi:[1,0,0]\n"
"v_pk_fma_f32 v[178:179], v[178:179], v[208:209], v[210:211] op_sel_hi:[1,0,0]\n"
"v_pk_fma_f32 v[180:181], v[180:181], v[208:209], v[210:211] op_sel_hi:[1,0,0]\n"
"v_pk_fma_f32 v[182:183], v[182:183], v[208:209], v[210:211] op_sel_hi:[1,0,0]\n"
"v_cvt_pk_f16_f32 v32, v176, v177\n"
"v_cvt_pk_f16_f32 v33, v178, v179\n"
"v_cvt_pk_f16_f32 v34, v180, v181\n"
"v_cvt_pk_f16_f32 v35, v182, v183\n"
"v_lshlrev_b32 v184, 16, v36\n"
"v_and_b32 v185, v237, v36\n"
"v_lshlrev_b32 v186, 16, v37\n"
"v_and_b32 v187, v237, v37\n"
"v_lshlrev_b32 v188, 16, v38\n"
"s_mov_b32 m0, s51\n"
"s_nop 0\n"
"buffer_load_dwordx4 v217, s[20:23], s50 offen offset:0 lds\n"
"v_and_b32 v189, v237, v38\n"
"v_lshlrev_b32 v190, 16, v39\n"
"v_and_b32 v191, v237, v39\n"
"v_pk_fma_f32 v[184:185], v[184:185], v[212:213], v[214:215] op_sel_hi:[1,0,0]\n"
"v_pk_fma_f32 v[186:187], v[186:187], v[212:213], v[214:215] op_sel_hi:[1,0,0]\n"
"v_pk_fma_f32 v[188:189], v[188:189], v[212:213], v[214:215] op_sel_hi:[1,0,0]\n"
"v_pk_fma_f32 v[190:191], v[190:191], v[212:213], v[214:215] op_sel_hi:[1,0,0]\n"
"v_cvt_pk_f16_f32 v36, v184, v185\n"
"v_cvt_pk_f16_f32 v37, v186, v187\n"
"v_cvt_pk_f16_f32 v38, v188, v189\n"
"v_cvt_pk_f16_f32 v39, v190, v191\n"
"v_lshlrev_b32 v176, 16, v40\n"
"v_and_b32 v177, v237, v40\n"
"v_lshlrev_b32 v178, 16, v41\n"
"v_and_b32 v179, v237, v41\n"
"v_lshlrev_b32 v180, 16, v42\n"
"v_and_b32 v181, v237, v42\n"
"v_lshlrev_b32 v182, 16, v43\n"
"v_and_b32 v183, v237, v43\n"
"v_pk_fma_f32 v[176:177], v[176:177], v[208:209], v[210:211] op_sel_hi:[1,0,0]\n"
"v_pk_fma_f32 v[178:179], v[178:179], v[208:209], v[210:211] op_sel_hi:[1,0,0]\n"
"s_add_u32 m0, s51, 7936\n"
"s_nop 0\n"
"buffer_load_dwordx4 v217, s[20:23], s50 offen offset:256 lds\n"
"s_add_u32 s50, s50, 32768\n"
"v_pk_fma_f32 v[180:181], v[180:181], v[208:209], v[210:211] op_sel_hi:[1,0,0]\n"
"v_pk_fma_f32 v[182:183], v[182:183], v[208:209], v[210:211] op_sel_hi:[1,0,0]\n"
"v_cvt_pk_f16_f32 v40, v176, v177\n"
"v_cvt_pk_f16_f32 v41, v178, v179\n"
"v_cvt_pk_f16_f32 v42, v180, v181\n"
"v_cvt_pk_f16_f32 v43, v182, v183\n"
"v_lshlrev_b32 v184, 16, v44\n"
"v_and_b32 v185, v237, v44\n"
"v_lshlrev_b32 v186, 16, v45\n"
"v_and_b32 v187, v237, v45\n"
"v_lshlrev_b32 v188, 16, v46\n"
"v_and_b32 v189, v237, v46\n"
"v_lshlrev_b32 v190, 16, v47\n"
"v_and_b32 v191, v237, v47\n"
"v_pk_fma_f32 v[184:185], v[184:185], v[212:213], v[214:215] op_sel_hi:[1,0,0]\n"
"v_pk_fma_f32 v[186:187], v[186:187], v[212:213], v[214:215] op_sel_hi:[1,0,0]\n"
"v_pk_fma_f32 v[188:189], v[188:189], v[212:213], v[214:215] op_sel_hi:[1,0,0]\n"
"v_pk_fma_f32 v[190:191], v[190:191], v[212:213], v[214:215] op_sel_hi:[1,0,0]\n"
"v_cvt_pk_f16_f32 v44, v184, v185\n"
"v_cvt_pk_f16_f32 v45, v186, v187\n"
"v_cvt_pk_f16_f32 v46, v188, v189\n"
"s_add_u32 m0, s51, 1024\n"
"s_nop 0\n"
"buffer_load_dwordx4 v217, s[20:23], s50 offen offset:0 lds\n"
"v_cvt_pk_f16_f32 v47, v190, v191\n"
"v_lshlrev_b32 v176, 16, v48\n"
"v_and_b32 v177, v237, v48\n"
"v_lshlrev_b32 v178, 16, v49\n"
"v_and_b32 v179, v237, v49\n"
"v_lshlrev_b32 v180, 16, v50\n"
"v_and_b32 v181, v237, v50\n"
"v_lshlrev_b32 v182, 16, v51\n"
"v_and_b32 v183, v237, v51\n"
"v_pk_fma_f32 v[176:177], v[176:177], v[208:209], v[210:211] op_sel_hi:[1,0,0]\n"
"v_pk_fma_f32 v[178:179], v[178:179], v[208:209], v[210:211] op_sel_hi:[1,0,0]\n"
"v_pk_fma_f32 v[180:181], v[180:181], v[208:209], v[210:211] op_sel_hi:[1,0,0]\n"
"v_pk_fma_f32 v[182:183], v[182:183], v[208:209], v[210:211] op_sel_hi:[1,0,0]\n"
"v_cvt_pk_f16_f32 v48, v176, v177\n"
"v_cvt_pk_f16_f32 v49, v178, v179\n"
"v_cvt_pk_f16_f32 v50, v180, v181\n"
"v_cvt_pk_f16_f32 v51, v182, v183\n"
"v_lshlrev_b32 v184, 16, v52\n"
"v_and_b32 v185, v237, v52\n"
"v_lshlrev_b32 v186, 16, v53\n"
"v_and_b32 v187, v237, v53\n"
"v_lshlrev_b32 v188, 16, v54\n"
"s_add_u32 m0, s51, 8960\n"
"s_nop 0\n"
"buffer_load_dwordx4 v217, s[20:23], s50 offen offset:256 lds\n"
"s_add_u32 s50, s50, 32768\n"
"v_and_b32 v189, v237, v54\n"
"v_lshlrev_b32 v190, 16, v55\n"
"v_and_b32 v191, v237, v55\n"
"v_pk_fma_f32 v[184:185], v[184:185], v[212:213], v[214:215] op_sel_hi:[1,0,0]\n"
"v_pk_fma_f32 v[186:187], v[186:187], v[212:213], v[214:215] op_sel_hi:[1,0,0]\n"
"v_pk_fma_f32 v[188:189], v[188:189], v[212:213], v[214:215] op_sel_hi:[1,0,0]\n"
"v_pk_fma_f32 v[190:191], v[190:191], v[212:213], v[214:215] op_sel_hi:[1,0,0]\n"
"v_cvt_pk_f16_f32 v52, v184, v185\n"
"v_cvt_pk_f16_f32 v53, v186, v187\n"
"v_cvt_pk_f16_f32 v54, v188, v189\n"
"v_cvt_pk_f16_f32 v55, v190, v191\n"
"v_lshlrev_b32 v176, 16, v56\n"
"v_and_b32 v177, v237, v56\n"
"v_lshlrev_b32 v178, 16, v57\n"
"v_and_b32 v179, v237, v57\n"
"v_lshlrev_b32 v180, 16, v58\n"
"v_and_b32 v181, v237, v58\n"
"v_lshlrev_b32 v182, 16, v59\n"
"v_and_b32 v183, v237, v59\n"
"v_pk_fma_f32 v[176:177], v[176:177], v[208:209], v[210:211] op_sel_hi:[1,0,0]\n"
"v_pk_fma_f32 v[178:179], v[178:179], v[208:209], v[210:211] op_sel_hi:[1,0,0]\n"
"s_add_u32 m0, s51, 16392\n"
"s_nop 0\n"
"buffer_load_dwordx4 v217, s[20:23], s50 offen offset:0 lds\n"
"v_pk_fma_f32 v[180:181], v[180:181], v[208:209], v[210:211] op_sel_hi:[1,0,0]\n"
"v_pk_fma_f32 v[182:183], v[182:183], v[208:209], v[210:211] op_sel_hi:[1,0,0]\n"
"v_cvt_pk_f16_f32 v56, v176, v177\n"
"v_cvt_pk_f16_f32 v57, v178, v179\n"
"v_cvt_pk_f16_f32 v58, v180, v181\n"
"v_cvt_pk_f16_f32 v59, v182, v183\n"
"v_lshlrev_b32 v184, 16, v60\n"
"v_and_b32 v185, v237, v60\n"
"v_lshlrev_b32 v186, 16, v61\n"
"v_and_b32 v187, v237, v61\n"
"v_lshlrev_b32 v188, 16, v62\n"
"v_and_b32 v189, v237, v62\n"
"v_lshlrev_b32 v190, 16, v63\n"
"v_and_b32 v191, v237, v63\n"
"v_pk_fma_f32 v[184:185], v[184:185], v[212:213], v[214:215] op_sel_hi:[1,0,0]\n"
"v_pk_fma_f32 v[186:187], v[186:187], v[212:213], v[214:215] op_sel_hi:[1,0,0]\n"
"v_pk_fma_f32 v[188:189], v[188:189], v[212:213], v[214:215] op_sel_hi:[1,0,0]\n"
"v_pk_fma_f32 v[190:191], v[190:191], v[212:213], v[214:215] op_sel_hi:[1,0,0]\n"
"v_cvt_pk_f16_f32 v60, v184, v185\n"
"v_cvt_pk_f16_f32 v61, v186, v187\n"
"v_cvt_pk_f16_f32 v62, v188, v189\n"
"s_add_u32 m0, s51, 24328\n"
"s_nop 0\n"
"buffer_load_dwordx4 v217, s[20:23], s50 offen offset:256 lds\n"
"s_add_u32 s50, s50, 32768\n"
"v_cvt_pk_f16_f32 v63, v190, v191\n"
"v_lshlrev_b32 v176, 16, v64\n"
"v_and_b32 v177, v237, v64\n"
"v_lshlrev_b32 v178, 16, v65\n"
"v_and_b32 v179, v237, v65\n"
"v_lshlrev_b32 v180, 16, v66\n"
"v_and_b32 v181, v237, v66\n"
"v_lshlrev_b32 v182, 16, v67\n"
"v_and_b32 v183, v237, v67\n"
"v_pk_fma_f32 v[176:177], v[176:177], v[208:209], v[210:211] op_sel_hi:[1,0,0]\n"
"v_pk_fma_f32 v[178:179], v[178:179], v[208:209], v[210:211] op_sel_hi:[1,0,0]\n"
"v_pk_fma_f32 v[180:181], v[180:181], v[208:209], v[210:211] op_sel_hi:[1,0,0]\n"
"v_pk_fma_f32 v[182:183], v[182:183], v[208:209], v[210:211] op_sel_hi:[1,0,0]\n"
"v_cvt_pk_f16_f32 v64, v176, v177\n"
"v_cvt_pk_f16_f32 v65, v178, v179\n"
"v_cvt_pk_f16_f32 v66, v180, v181\n"
"v_cvt_pk_f16_f32 v67, v182, v183\n"
"v_lshlrev_b32 v184, 16, v68\n"
"v_and_b32 v185, v237, v68\n"
"v_lshlrev_b32 v186, 16, v69\n"
"v_and_b32 v187, v237, v69\n"
"v_lshlrev_b32 v188, 16, v70\n"
"s_add_u32 m0, s51, 17416\n"
"s_nop 0\n"
"buffer_load_dwordx4 v217, s[20:23], s50 offen offset:0 lds\n"
"v_and_b32 v189, v237, v70\n"
"v_lshlrev_b32 v190, 16, v71\n"
"v_and_b32 v191, v237, v71\n"
"v_pk_fma_f32 v[184:185], v[184:185], v[212:213], v[214:215] op_sel_hi:[1,0,0]\n"
"v_pk_fma_f32 v[186:187], v[186:187], v[212:213], v[214:215] op_sel_hi:[1,0,0]\n"
"v_pk_fma_f32 v[188:189], v[188:189], v[212:213], v[214:215] op_sel_hi:[1,0,0]\n"
"v_pk_fma_f32 v[190:191], v[190:191], v[212:213], v[214:215] op_sel_hi:[1,0,0]\n"
"v_cvt_pk_f16_f32 v68, v184, v185\n"
"v_cvt_pk_f16_f32 v69, v186, v187\n"
"v_cvt_pk_f16_f32 v70, v188, v189\n"
"v_cvt_pk_f16_f32 v71, v190, v191\n"
"v_lshlrev_b32 v176, 16, v72\n"
"v_and_b32 v177, v237, v72\n"
"v_lshlrev_b32 v178, 16, v73\n"
"v_and_b32 v179, v237, v73\n"
"v_lshlrev_b32 v180, 16, v74\n"
"v_and_b32 v181, v237, v74\n"
"v_lshlrev_b32 v182, 16, v75\n"
"v_and_b32 v183, v237, v75\n"
"v_pk_fma_f32 v[176:177], v[176:177], v[208:209], v[210:211] op_sel_hi:[1,0,0]\n"
"v_pk_fma_f32 v[178:179], v[178:179], v[208:209], v[210:211] op_sel_hi:[1,0,0]\n"
"s_add_u32 m0, s51, 25352\n"
"s_nop 0\n"
"buffer_load_dwordx4 v217, s[20:23], s50 offen offset:256 lds\n"
"s_add_u32 s50, s50, 425984\n"
"v_pk_fma_f32 v[180:181], v[180:181], v[208:209], v[210:211] op_sel_hi:[1,0,0]\n"
"v_pk_fma_f32 v[182:183], v[182:183], v[208:209], v[210:211] op_sel_hi:[1,0,0]\n"
"v_cvt_pk_f16_f32 v72, v176, v177\n"
"v_cvt_pk_f16_f32 v73, v178, v179\n"
"v_cvt_pk_f16_f32 v74, v180, v181\n"
"v_cvt_pk_f16_f32 v75, v182, v183\n"
"v_lshlrev_b32 v184, 16, v76\n"
"v_and_b32 v185, v237, v76\n"
"v_lshlrev_b32 v186, 16, v77\n"
"v_and_b32 v187, v237, v77\n"
"v_lshlrev_b32 v188, 16, v78\n"
"v_and_b32 v189, v237, v78\n"
"v_lshlrev_b32 v190, 16, v79\n"
"v_and_b32 v191, v237, v79\n"
"v_pk_fma_f32 v[184:185], v[184:185], v[212:213], v[214:215] op_sel_hi:[1,0,0]\n"
"v_pk_fma_f32 v[186:187], v[186:187], v[212:213], v[214:215] op_sel_hi:[1,0,0]\n"
"v_pk_fma_f32 v[188:189], v[188:189], v[212:213], v[214:215] op_sel_hi:[1,0,0]\n"
"v_pk_fma_f32 v[190:191], v[190:191], v[212:213], v[214:215] op_sel_hi:[1,0,0]\n"
"v_cvt_pk_f16_f32 v76, v184, v185\n"
"v_cvt_pk_f16_f32 v77, v186, v187\n"
"v_cvt_pk_f16_f32 v78, v188, v189\n"
"s_add_u32 m0, s51, 32784\n"
"s_nop 0\n"
"buffer_load_dwordx4 v217, s[20:23], s50 offen offset:0 lds\n"
"v_cvt_pk_f16_f32 v79, v190, v191\n"
"v_lshlrev_b32 v176, 16, v80\n"
"v_and_b32 v177, v237, v80\n"
"v_lshlrev_b32 v178, 16, v81\n"
"v_and_b32 v179, v237, v81\n"
"v_lshlrev_b32 v180, 16, v82\n"
"v_and_b32 v181, v237, v82\n"
"v_lshlrev_b32 v182, 16, v83\n"
"v_and_b32 v183, v237, v83\n"
"v_pk_fma_f32 v[176:177], v[176:177], v[208:209], v[210:211] op_sel_hi:[1,0,0]\n"
"v_pk_fma_f32 v[178:179], v[178:179], v[208:209], v[210:211] op_sel_hi:[1,0,0]\n"
"v_pk_fma_f32 v[180:181], v[180:181], v[208:209], v[210:211] op_sel_hi:[1,0,0]\n"
"v_pk_fma_f32 v[182:183], v[182:183], v[208:209], v[210:211] op_sel_hi:[1,0,0]\n"
"v_cvt_pk_f16_f32 v80, v176, v177\n"
"v_cvt_pk_f16_f32 v81, v178, v179\n"
"v_cvt_pk_f16_f32 v82, v180, v181\n"
"v_cvt_pk_f16_f32 v83, v182, v183\n"
"v_lshlrev_b32 v184, 16, v84\n"
"v_and_b32 v185, v237, v84\n"
"v_lshlrev_b32 v186, 16, v85\n"
"v_and_b32 v187, v237, v85\n"
"v_lshlrev_b32 v188, 16, v86\n"
"s_add_u32 m0, s51, 40720\n"
"s_nop 0\n"
"buffer_load_dwordx4 v217, s[20:23], s50 offen offset:256 lds\n"
"s_add_u32 s50, s50, 32768\n"
"v_and_b32 v189, v237, v86\n"
"v_lshlrev_b32 v190, 16, v87\n"
"v_and_b32 v191, v237, v87\n"
"v_pk_fma_f32 v[184:185], v[184:185], v[212:213], v[214:215] op_sel_hi:[1,0,0]\n"
"v_pk_fma_f32 v[186:187], v[186:187], v[212:213], v[214:215] op_sel_hi:[1,0,0]\n"
"v_pk_fma_f32 v[188:189], v[188:189], v[212:213], v[214:215] op_sel_hi:[1,0,0]\n"
"v_pk_fma_f32 v[190:191], v[190:191], v[212:213], v[214:215] op_sel_hi:[1,0,0]\n"
"v_cvt_pk_f16_f32 v84, v184, v185\n"
"v_cvt_pk_f16_f32 v85, v186, v187\n"
"v_cvt_pk_f16_f32 v86, v188, v189\n"
"v_cvt_pk_f16_f32 v87, v190, v191\n"
"v_lshlrev_b32 v176, 16, v88\n"
"v_and_b32 v177, v237, v88\n"
"v_lshlrev_b32 v178, 16, v89\n"
"v_and_b32 v179, v237, v89\n"
"v_lshlrev_b32 v180, 16, v90\n"
"v_and_b32 v181, v237, v90\n"
"v_lshlrev_b32 v182, 16, v91\n"
"v_and_b32 v183, v237, v91\n"
"v_pk_fma_f32 v[176:177], v[176:177], v[208:209], v[210:211] op_sel_hi:[1,0,0]\n"
"v_pk_fma_f32 v[178:179], v[178:179], v[208:209], v[210:211] op_sel_hi:[1,0,0]\n"
"s_add_u32 m0, s51, 33808\n"
"s_nop 0\n"
"buffer_load_dwordx4 v217, s[20:23], s50 offen offset:0 lds\n"
"v_pk_fma_f32 v[180:181], v[180:181], v[208:209], v[210:211] op_sel_hi:[1,0,0]\n"
"v_pk_fma_f32 v[182:183], v[182:183], v[208:209], v[210:211] op_sel_hi:[1,0,0]\n"
"v_cvt_pk_f16_f32 v88, v176, v177\n"
"v_cvt_pk_f16_f32 v89, v178, v179\n"
"v_cvt_pk_f16_f32 v90, v180, v181\n"
"v_cvt_pk_f16_f32 v91, v182, v183\n"
"v_lshlrev_b32 v184, 16, v92\n"
"v_and_b32 v185, v237, v92\n"
"v_lshlrev_b32 v186, 16, v93\n"
"v_and_b32 v187, v237, v93\n"
"v_lshlrev_b32 v188, 16, v94\n"
"v_and_b32 v189, v237, v94\n"
"v_lshlrev_b32 v190, 16, v95\n"
"v_and_b32 v191, v237, v95\n"
"v_pk_fma_f32 v[184:185], v[184:185], v[212:213], v[214:215] op_sel_hi:[1,0,0]\n"
"v_pk_fma_f32 v[186:187], v[186:187], v[212:213], v[214:215] op_sel_hi:[1,0,0]\n"
"v_pk_fma_f32 v[188:189], v[188:189], v[212:213], v[214:215] op_sel_hi:[1,0,0]\n"
"v_pk_fma_f32 v[190:191], v[190:191], v[212:213], v[214:215] op_sel_hi:[1,0,0]\n"
"v_cvt_pk_f16_f32 v92, v184, v185\n"
"v_cvt_pk_f16_f32 v93, v186, v187\n"
"v_cvt_pk_f16_f32 v94, v188, v189\n"
"s_add_u32 m0, s51, 41744\n"
"s_nop 0\n"
"buffer_load_dwordx4 v217, s[20:23], s50 offen offset:256 lds\n"
"s_add_u32 s50, s50, 32768\n"
"v_cvt_pk_f16_f32 v95, v190, v191\n"
"v_lshlrev_b32 v176, 16, v96\n"
"v_and_b32 v177, v237, v96\n"
"v_lshlrev_b32 v178, 16, v97\n"
"v_and_b32 v179, v237, v97\n"
"v_lshlrev_b32 v180, 16, v98\n"
"v_and_b32 v181, v237, v98\n"
"v_lshlrev_b32 v182, 16, v99\n"
"v_and_b32 v183, v237, v99\n"
"v_pk_fma_f32 v[176:177], v[176:177], v[208:209], v[210:211] op_sel_hi:[1,0,0]\n"
"v_pk_fma_f32 v[178:179], v[178:179], v[208:209], v[210:211] op_sel_hi:[1,0,0]\n"
"v_pk_fma_f32 v[180:181], v[180:181], v[208:209], v[210:211] op_sel_hi:[1,0,0]\n"
"v_pk_fma_f32 v[182:183], v[182:183], v[208:209], v[210:211] op_sel_hi:[1,0,0]\n"
"v_cvt_pk_f16_f32 v96, v176, v177\n"
"v_cvt_pk_f16_f32 v97, v178, v179\n"
"v_cvt_pk_f16_f32 v98, v180, v181\n"
"v_cvt_pk_f16_f32 v99, v182, v183\n"
"v_lshlrev_b32 v184, 16, v100\n"
"v_and_b32 v185, v237, v100\n"
"v_lshlrev_b32 v186, 16, v101\n"
"v_and_b32 v187, v237, v101\n"
"v_lshlrev_b32 v188, 16, v102\n"
"s_add_u32 m0, s51, 49176\n"
"s_nop 0\n"
"buffer_load_dwordx4 v217, s[20:23], s50 offen offset:0 lds\n"
"v_and_b32 v189, v237, v102\n"
"v_lshlrev_b32 v190, 16, v103\n"
"v_and_b32 v191, v237, v103\n"
"v_pk_fma_f32 v[184:185], v[184:185], v[212:213], v[214:215] op_sel_hi:[1,0,0]\n"
"v_pk_fma_f32 v[186:187], v[186:187], v[212:213], v[214:215] op_sel_hi:[1,0,0]\n"
"v_pk_fma_f32 v[188:189], v[188:189], v[212:213], v[214:215] op_sel_hi:[1,0,0]\n"
"v_pk_fma_f32 v[190:191], v[190:191], v[212:213], v[214:215] op_sel_hi:[1,0,0]\n"
"v_cvt_pk_f16_f32 v100, v184, v185\n"
"v_cvt_pk_f16_f32 v101, v186, v187\n"
"v_cvt_pk_f16_f32 v102, v188, v189\n"
"v_cvt_pk_f16_f32 v103, v190, v191\n"
"v_lshlrev_b32 v176, 16, v104\n"
"v_and_b32 v177, v237, v104\n"
"v_lshlrev_b32 v178, 16, v105\n"
"v_and_b32 v179, v237, v105\n"
"v_lshlrev_b32 v180, 16, v106\n"
"v_and_b32 v181, v237, v106\n"
"v_lshlrev_b32 v182, 16, v107\n"
"v_and_b32 v183, v237, v107\n"
"v_pk_fma_f32 v[176:177], v[176:177], v[208:209], v[210:211] op_sel_hi:[1,0,0]\n"
"v_pk_fma_f32 v[178:179], v[178:179], v[208:209], v[210:211] op_sel_hi:[1,0,0]\n"
"s_add_u32 m0, s51, 57112\n"
"s_nop 0\n"
"buffer_load_dwordx4 v217, s[20:23], s50 offen offset:256 lds\n"
"s_add_u32 s50, s50, 32768\n"
"v_pk_fma_f32 v[180:181], v[180:181], v[208:209], v[210:211] op_sel_hi:[1,0,0]\n"
"v_pk_fma_f32 v[182:183], v[182:183], v[208:209], v[210:211] op_sel_hi:[1,0,0]\n"
"v_cvt_pk_f16_f32 v104, v176, v177\n"
"v_cvt_pk_f16_f32 v105, v178, v179\n"
"v_cvt_pk_f16_f32 v106, v180, v181\n"
"v_cvt_pk_f16_f32 v107, v182, v183\n"
"v_lshlrev_b32 v184, 16, v108\n"
"v_and_b32 v185, v237, v108\n"
"v_lshlrev_b32 v186, 16, v109\n"
"v_and_b32 v187, v237, v109\n"
"v_lshlrev_b32 v188, 16, v110\n"
"v_and_b32 v189, v237, v110\n"
"v_lshlrev_b32 v190, 16, v111\n"
"v_and_b32 v191, v237, v111\n"
"v_pk_fma_f32 v[184:185], v[184:185], v[212:213], v[214:215] op_sel_hi:[1,0,0]\n"
"v_pk_fma_f32 v[186:187], v[186:187], v[212:213], v[214:215] op_sel_hi:[1,0,0]\n"
"v_pk_fma_f32 v[188:189], v[188:189], v[212:213], v[214:215] op_sel_hi:[1,0,0]\n"
"v_pk_fma_f32 v[190:191], v[190:191], v[212:213], v[214:215] op_sel_hi:[1,0,0]\n"
"v_cvt_pk_f16_f32 v108, v184, v185\n"
"v_cvt_pk_f16_f32 v109, v186, v187\n"
"v_cvt_pk_f16_f32 v110, v188, v189\n"
"s_add_u32 m0, s51, 50200\n"
"s_nop 0\n"
"buffer_load_dwordx4 v217, s[20:23], s50 offen offset:0 lds\n"
"v_cvt_pk_f16_f32 v111, v190, v191\n"
"s_mov_b32 s87, 0x80000\n"
"s_waitcnt vmcnt(15)\n"
"AT_gchk:\n"
"v_xor_b32 v192, s85, v145\n"
"v_xor_b32 v193, s85, v147\n"
"v_bitop3_b32 v192, v149, s85, v192 bitop3:0xbe\n"
"v_bitop3_b32 v193, v151, s85, v193 bitop3:0xbe\n"
"v_bitop3_b32 v192, v153, s85, v192 bitop3:0xbe\n"
"v_bitop3_b32 v193, v155, s85, v193 bitop3:0xbe\n"
"v_bitop3_b32 v192, v157, s85, v192 bitop3:0xbe\n"
"v_bitop3_b32 v193, v159, s85, v193 bitop3:0xbe\n"
"v_or_b32 v192, v192, v193\n"
"v_cmp_ne_u32 s[56:57], 0, v192\n"
"v_cmp_gt_u32 vcc, 144, v234\n"
"s_nop 1\n"
"s_and_b64 vcc, vcc, s[56:57]\n"
"s_cbranch_vccz AT_gok\n"
"s_sub_u32 s87, s87, 1\n"
"s_cmp_eq_u32 s87, 0\n"
"s_cbranch_scc1 AT_gok\n"
"s_sleep 2\n"
"buffer_load_dwordx4 v[144:147], v225, s[28:31], s61 offen offset:0 sc1\n"
"buffer_load_dwordx4 v[148:151], v225, s[28:31], s61 offen offset:16 sc1\n"
"buffer_load_dwordx4 v[152:155], v225, s[28:31], s62 offen offset:0 sc1\n"
"buffer_load_dwordx4 v[156:159], v225, s[28:31], s62 offen offset:16 sc1\n"
"s_waitcnt vmcnt(0)\n"
"s_branch AT_gchk\n"
"AT_gok:\n"
"v_mov_b32 v236, 0x3e0293ee\n"
"v_mul_f32 v192, v236, v144\n"
"v_fma_f32 v196, v152, v236, v236\n"
"v_mul_f32 v193, v236, v146\n"
"v_fma_f32 v197, v154, v236, v236\n"
"v_mul_f32 v194, v236, v148\n"
"v_fma_f32 v198, v156, v236, v236\n"
"v_mul_f32 v195, v236, v150\n"
"v_fma_f32 v199, v158, v236, v236\n"
"ds_write_b128 v216, v[196:199]\n"
"ds_write_b128 v216, v[192:195] offset:128\n"
"v_lshlrev_b32 v176, 16, v112\n"
"v_and_b32 v177, v237, v112\n"
"v_lshlrev_b32 v178, 16, v113\n"
"v_and_b32 v179, v237, v113\n"
"v_lshlrev_b32 v180, 16, v114\n"
"v_and_b32 v181, v237, v114\n"
"v_lshlrev_b32 v182, 16, v115\n"
"v_and_b32 v183, v237, v115\n"
"v_pk_fma_f32 v[176:177], v[176:177], v[208:209], v[210:211] op_sel_hi:[1,0,0]\n"
"v_pk_fma_f32 v[178:179], v[178:179], v[208:209], v[210:211] op_sel_hi:[1,0,0]\n"
"v_pk_fma_f32 v[180:181], v[180:181], v[208:209], v[210:211] op_sel_hi:[1,0,0]\n"
"v_pk_fma_f32 v[182:183], v[182:183], v[208:209], v[210:211] op_sel_hi:[1,0,0]\n"
"v_cvt_pk_f16_f32 v112, v176, v177\n"
"v_cvt_pk_f16_f32 v113, v178, v179\n"
"v_cvt_pk_f16_f32 v114, v180, v181\n"
"v_cvt_pk_f16_f32 v115, v182, v183\n"
"v_lshlrev_b32 v184, 16, v116\n"
"v_and_b32 v185, v237, v116\n"
"v_lshlrev_b32 v186, 16, v117\n"
"v_and_b32 v187, v237, v117\n"
"v_lshlrev_b32 v188, 16, v118\n"
"s_add_u32 m0, s51, 58136\n"
"s_nop 0\n"
"buffer_load_dwordx4 v217, s[20:23], s50 offen offset:256 lds\n"
"s_add_u32 s50, s50, 425984\n"
"v_and_b32 v189, v237, v118\n"
"v_lshlrev_b32 v190, 16, v119\n"
"v_and_b32 v191, v237, v119\n"
"v_pk_fma_f32 v[184:185], v[184:185], v[212:213], v[214:215] op_sel_hi:[1,0,0]\n"
"v_pk_fma_f32 v[186:187], v[186:187], v[212:213], v[214:215] op_sel_hi:[1,0,0]\n"
"v_pk_fma_f32 v[188:189], v[188:189], v[212:213], v[214:215] op_sel_hi:[1,0,0]\n"
"v_pk_fma_f32 v[190:191], v[190:191], v[212:213], v[214:215] op_sel_hi:[1,0,0]\n"
"v_cvt_pk_f16_f32 v116, v184, v185\n"
"v_cvt_pk_f16_f32 v117, v186, v187\n"
"v_cvt_pk_f16_f32 v118, v188, v189\n"
"v_cvt_pk_f16_f32 v119, v190, v191\n"
"v_lshlrev_b32 v176, 16, v120\n"
"v_and_b32 v177, v237, v120\n"
"v_lshlrev_b32 v178, 16, v121\n"
"v_and_b32 v179, v237, v121\n"
"v_lshlrev_b32 v180, 16, v122\n"
"v_and_b32 v181, v237, v122\n"
"v_lshlrev_b32 v182, 16, v123\n"
"v_and_b32 v183, v237, v123\n"
"v_pk_fma_f32 v[176:177], v[176:177], v[208:209], v[210:211] op_sel_hi:[1,0,0]\n"
"v_pk_fma_f32 v[178:179], v[178:179], v[208:209], v[210:211] op_sel_hi:[1,0,0]\n"
"v_pk_fma_f32 v[180:181], v[180:181], v[208:209], v[210:211] op_sel_hi:[1,0,0]\n"
"v_pk_fma_f32 v[182:183], v[182:183], v[208:209], v[210:211] op_sel_hi:[1,0,0]\n"
"v_cvt_pk_f16_f32 v120, v176, v177\n"
"v_cvt_pk_f16_f32 v121, v178, v179\n"
"v_cvt_pk_f16_f32 v122, v180, v181\n"
"v_cvt_pk_f16_f32 v123, v182, v183\n"
"v_lshlrev_b32 v184, 16, v124\n"
"v_and_b32 v185, v237, v124\n"
"v_lshlrev_b32 v186, 16, v125\n"
"v_and_b32 v187, v237, v125\n"
"v_lshlrev_b32 v188, 16, v126\n"
"v_and_b32 v189, v237, v126\n"
"v_lshlrev_b32 v190, 16, v127\n"
"v_and_b32 v191, v237, v127\n"
"v_pk_fma_f32 v[184:185], v[184:185], v[212:213], v[214:215] op_sel_hi:[1,0,0]\n"
"v_pk_fma_f32 v[186:187], v[186:187], v[212:213], v[214:215] op_sel_hi:[1,0,0]\n"
"v_pk_fma_f32 v[188:189], v[188:189], v[212:213], v[214:215] op_sel_hi:[1,0,0]\n"
"v_pk_fma_f32 v[190:191], v[190:191], v[212:213], v[214:215] op_sel_hi:[1,0,0]\n"
"v_cvt_pk_f16_f32 v124, v184, v185\n"
"v_cvt_pk_f16_f32 v125, v186, v187\n"
"v_cvt_pk_f16_f32 v126, v188, v189\n"
"v_cvt_pk_f16_f32 v127, v190, v191\n"
"s_waitcnt vmcnt(20) lgkmcnt(0)\n"
"s_barrier\n"
"ds_read_b128 v[144:147], v224 offset:0\n"
"ds_read_b128 v[148:151], v224 offset:64\n"
"ds_read_b128 v[152:155], v224 offset:128\n"
"ds_read_b128 v[156:159], v224 offset:192\n"
"ds_read_b128 v[160:163], v224 offset:256\n"
"ds_read_b128 v[164:167], v224 offset:320\n"
"ds_read_b128 v[168:171], v224 offset:384\n"
"ds_read_b128 v[172:175], v224 offset:448\n"
"v_lshlrev_b32 v176, 16, v128\n"
"v_and_b32 v177, v237, v128\n"
"v_lshlrev_b32 v178, 16, v129\n"
"v_and_b32 v179, v237, v129\n"
"v_lshlrev_b32 v180, 16, v130\n"
"v_and_b32 v181, v237, v130\n"
"v_lshlrev_b32 v182, 16, v131\n"
"v_and_b32 v183, v237, v131\n"
"v_pk_fma_f32 v[176:177], v[176:177], v[208:209], v[210:211] op_sel_hi:[1,0,0]\n"
"v_pk_fma_f32 v[178:179], v[178:179], v[208:209], v[210:211] op_sel_hi:[1,0,0]\n"
"v_pk_fma_f32 v[180:181], v[180:181], v[208:209], v[210:211] op_sel_hi:[1,0,0]\n"
"v_pk_fma_f32 v[182:183], v[182:183], v[208:209], v[210:211] op_sel_hi:[1,0,0]\n"
"v_cvt_pk_f16_f32 v128, v176, v177\n"
"v_cvt_pk_f16_f32 v129, v178, v179\n"
"v_cvt_pk_f16_f32 v130, v180, v181\n"
"v_cvt_pk_f16_f32 v131, v182, v183\n"
"v_lshlrev_b32 v184, 16, v132\n"
"v_and_b32 v185, v237, v132\n"
"v_lshlrev_b32 v186, 16, v133\n"
"v_and_b32 v187, v237, v133\n"
"v_lshlrev_b32 v188, 16, v134\n"
"v_and_b32 v189, v237, v134\n"
"v_lshlrev_b32 v190, 16, v135\n"
"v_and_b32 v191, v237, v135\n"
"v_pk_fma_f32 v[184:185], v[184:185], v[212:213], v[214:215] op_sel_hi:[1,0,0]\n"
"v_pk_fma_f32 v[186:187], v[186:187], v[212:213], v[214:215] op_sel_hi:[1,0,0]\n"
"v_pk_fma_f32 v[188:189], v[188:189], v[212:213], v[214:215] op_sel_hi:[1,0,0]\n"
"v_pk_fma_f32 v[190:191], v[190:191], v[212:213], v[214:215] op_sel_hi:[1,0,0]\n"
"v_cvt_pk_f16_f32 v132, v184, v185\n"
"v_cvt_pk_f16_f32 v133, v186, v187\n"
"v_cvt_pk_f16_f32 v134, v188, v189\n"
"v_cvt_pk_f16_f32 v135, v190, v191\n"
"v_lshlrev_b32 v176, 16, v136\n"
"v_and_b32 v177, v237, v136\n"
"v_lshlrev_b32 v178, 16, v137\n"
"v_and_b32 v179, v237, v137\n"
"v_lshlrev_b32 v180, 16, v138\n"
"v_and_b32 v181, v237, v138\n"
"v_lshlrev_b32 v182, 16, v139\n"
"v_and_b32 v183, v237, v139\n"
"v_pk_fma_f32 v[176:177], v[176:177], v[208:209], v[210:211] op_sel_hi:[1,0,0]\n"
"v_pk_fma_f32 v[178:179], v[178:179], v[208:209], v[210:211] op_sel_hi:[1,0,0]\n"
"v_pk_fma_f32 v[180:181], v[180:181], v[208:209], v[210:211] op_sel_hi:[1,0,0]\n"
"v_pk_fma_f32 v[182:183], v[182:183], v[208:209], v[210:211] op_sel_hi:[1,0,0]\n"
"v_cvt_pk_f16_f32 v136, v176, v177\n"
"v_cvt_pk_f16_f32 v137, v178, v179\n"
"v_cvt_pk_f16_f32 v138, v180, v181\n"
"v_cvt_pk_f16_f32 v139, v182, v183\n"
"v_lshlrev_b32 v184, 16, v140\n"
"v_and_b32 v185, v237, v140\n"
"v_lshlrev_b32 v186, 16, v141\n"
"v_and_b32 v187, v237, v141\n"
"v_lshlrev_b32 v188, 16, v142\n"
"v_and_b32 v189, v237, v142\n"
"v_lshlrev_b32 v190, 16, v143\n"
"v_and_b32 v191, v237, v143\n"
"v_pk_fma_f32 v[184:185], v[184:185], v[212:213], v[214:215] op_sel_hi:[1,0,0]\n"
"v_pk_fma_f32 v[186:187], v[186:187], v[212:213], v[214:215] op_sel_hi:[1,0,0]\n"
"v_pk_fma_f32 v[188:189], v[188:189], v[212:213], v[214:215] op_sel_hi:[1,0,0]\n"
"v_pk_fma_f32 v[190:191], v[190:191], v[212:213], v[214:215] op_sel_hi:[1,0,0]\n"
"v_cvt_pk_f16_f32 v140, v184, v185\n"
"v_cvt_pk_f16_f32 v141, v186, v187\n"
"v_cvt_pk_f16_f32 v142, v188, v189\n"
"v_cvt_pk_f16_f32 v143, v190, v191\n"
"ds_read_b64_tr_b16 a[160:161], v219 offset:32784\n"
"s_waitcnt lgkmcnt(6)\n"
"v_fma_mix_f32 v176, v0, v144, v152 op_sel:[0,0,0] op_sel_hi:[1,0,0]\n"
"v_fma_mix_f32 v177, v0, v145, v153 op_sel:[1,0,0] op_sel_hi:[1,0,0]\n"
"v_fma_mix_f32 v178, v1, v146, v154 op_sel:[0,0,0] op_sel_hi:[1,0,0]\n"
"ds_read_b64_tr_b16 a[162:163], v219 offset:33808\n"
"v_fma_mix_f32 v179, v1, v147, v155 op_sel:[1,0,0] op_sel_hi:[1,0,0]\n"
"s_waitcnt lgkmcnt(6)\n"
"v_fma_mix_f32 v180, v2, v148, v156 op_sel:[0,0,0] op_sel_hi:[1,0,0]\n"
"v_fma_mix_f32 v181, v2, v149, v157 op_sel:[1,0,0] op_sel_hi:[1,0,0]\n"
"ds_read_b64_tr_b16 a[164:165], v219 offset:36880\n"
"v_fma_mix_f32 v182, v3, v150, v158 op_sel:[0,0,0] op_sel_hi:[1,0,0]\n"
"v_fma_mix_f32 v183, v3, v151, v159 op_sel:[1,0,0] op_sel_hi:[1,0,0]\n"
"v_cvt_pk_bf16_f32 v0, v176, v177\n"
"ds_read_b64_tr_b16 a[166:167], v219 offset:37904\n"
"v_cvt_pk_bf16_f32 v1, v178, v179\n"
"v_cvt_pk_bf16_f32 v2, v180, v181\n"
"v_cvt_pk_bf16_f32 v3, v182, v183\n"
"ds_read_b64_tr_b16 a[168:169], v219 offset:32792\n"
"v_fma_mix_f32 v184, v4, v144, v152 op_sel:[0,0,0] op_sel_hi:[1,0,0]\n"
"v_fma_mix_f32 v185, v4, v145, v153 op_sel:[1,0,0] op_sel_hi:[1,0,0]\n"
"v_fma_mix_f32 v186, v5, v146, v154 op_sel:[0,0,0] op_sel_hi:[1,0,0]\n"
"ds_read_b64_tr_b16 a[170:171], v219 offset:33816\n"
"v_fma_mix_f32 v187, v5, v147, v155 op_sel:[1,0,0] op_sel_hi:[1,0,0]\n"
"v_fma_mix_f32 v188, v6, v148, v156 op_sel:[0,0,0] op_sel_hi:[1,0,0]\n"
"v_fma_mix_f32 v189, v6, v149, v157 op_sel:[1,0,0] op_sel_hi:[1,0,0]\n"
"ds_read_b64_tr_b16 a[172:173], v219 offset:36888\n"
"v_fma_mix_f32 v190, v7, v150, v158 op_sel:[0,0,0] op_sel_hi:[1,0,0]\n"
"v_fma_mix_f32 v191, v7, v151, v159 op_sel:[1,0,0] op_sel_hi:[1,0,0]\n"
"v_cvt_pk_bf16_f32 v4, v184, v185\n"
"ds_read_b64_tr_b16 a[174:175], v219 offset:37912\n"
"ds_read_b128 v[144:147], v224 offset:512\n"
"v_cvt_pk_bf16_f32 v5, v186, v187\n"
"v_cvt_pk_bf16_f32 v6, v188, v189\n"
"ds_read_b64_tr_b16 a[176:177], v219 offset:33040\n"
"v_cvt_pk_bf16_f32 v7, v190, v191\n"
"s_waitcnt lgkmcnt(11)\n"
"v_fma_mix_f32 v176, v8, v160, v168 op_sel:[0,0,0] op_sel_hi:[1,0,0]\n"
"v_fma_mix_f32 v177, v8, v161, v169 op_sel:[1,0,0] op_sel_hi:[1,0,0]\n"
"ds_read_b64_tr_b16 a[178:179], v219 offset:34064\n"
"v_fma_mix_f32 v178, v9, v162, v170 op_sel:[0,0,0] op_sel_hi:[1,0,0]\n"
"v_fma_mix_f32 v179, v9, v163, v171 op_sel:[1,0,0] op_sel_hi:[1,0,0]\n"
"s_waitcnt lgkmcnt(11)\n"
"v_fma_mix_f32 v180, v10, v164, v172 op_sel:[0,0,0] op_sel_hi:[1,0,0]\n"
"ds_read_b64_tr_b16 a[180:181], v219 offset:37136\n"
"ds_read_b128 v[148:151], v224 offset:576\n"
"v_fma_mix_f32 v181, v10, v165, v173 op_sel:[1,0,0] op_sel_hi:[1,0,0]\n"
"v_fma_mix_f32 v182, v11, v166, v174 op_sel:[0,0,0] op_sel_hi:[1,0,0]\n"
"ds_read_b64_tr_b16 a[182:183], v219 offset:38160\n"
"v_fma_mix_f32 v183, v11, v167, v175 op_sel:[1,0,0] op_sel_hi:[1,0,0]\n"
"v_cvt_pk_bf16_f32 v8, v176, v177\n"
"v_cvt_pk_bf16_f32 v9, v178, v179\n"
"ds_read_b64_tr_b16 a[184:185], v219 offset:33048\n"
"v_cvt_pk_bf16_f32 v10, v180, v181\n"
"v_cvt_pk_bf16_f32 v11, v182, v183\n"
"v_fma_mix_f32 v184, v12, v160, v168 op_sel:[0,0,0] op_sel_hi:[1,0,0]\n"
"ds_read_b64_tr_b16 a[186:187], v219 offset:34072\n"
"ds_read_b128 v[152:155], v224 offset:640\n"
"v_fma_mix_f32 v185, v12, v161, v169 op_sel:[1,0,0] op_sel_hi:[1,0,0]\n"
"v_fma_mix_f32 v186, v13, v162, v170 op_sel:[0,0,0] op_sel_hi:[1,0,0]\n"
"ds_read_b64_tr_b16 a[188:189], v219 offset:37144\n"
"v_fma_mix_f32 v187, v13, v163, v171 op_sel:[1,0,0] op_sel_hi:[1,0,0]\n"
"v_fma_mix_f32 v188, v14, v164, v172 op_sel:[0,0,0] op_sel_hi:[1,0,0]\n"
"v_fma_mix_f32 v189, v14, v165, v173 op_sel:[1,0,0] op_sel_hi:[1,0,0]\n"
"ds_read_b64_tr_b16 a[190:191], v219 offset:38168\n"
"v_fma_mix_f32 v190, v15, v166, v174 op_sel:[0,0,0] op_sel_hi:[1,0,0]\n"
"v_fma_mix_f32 v191, v15, v167, v175 op_sel:[1,0,0] op_sel_hi:[1,0,0]\n"
"v_cvt_pk_bf16_f32 v12, v184, v185\n"
"ds_read_b64_tr_b16 a[224:225], v219 offset:33296\n"
"ds_read_b128 v[156:159], v224 offset:704\n"
"ds_read_b128 v[160:163], v224 offset:768\n"
"v_cvt_pk_bf16_f32 v13, v186, v187\n"
"ds_read_b64_tr_b16 a[226:227], v219 offset:34320\n"
"v_cvt_pk_bf16_f32 v14, v188, v189\n"
"v_cvt_pk_bf16_f32 v15, v190, v191\n"
"s_waitcnt lgkmcnt(6)\n"
"v_fma_mix_f32 v176, v16, v144, v152 op_sel:[0,0,0] op_sel_hi:[1,0,0]\n"
"ds_read_b64_tr_b16 a[228:229], v219 offset:37392\n"
"v_fma_mix_f32 v177, v16, v145, v153 op_sel:[1,0,0] op_sel_hi:[1,0,0]\n"
"v_fma_mix_f32 v178, v17, v146, v154 op_sel:[0,0,0] op_sel_hi:[1,0,0]\n"
"v_fma_mix_f32 v179, v17, v147, v155 op_sel:[1,0,0] op_sel_hi:[1,0,0]\n"
"ds_read_b64_tr_b16 a[230:231], v219 offset:38416\n"
"ds_read_b128 v[164:167], v224 offset:832\n"
"s_waitcnt lgkmcnt(5)\n"
"v_fma_mix_f32 v180, v18, v148, v156 op_sel:[0,0,0] op_sel_hi:[1,0,0]\n"
"v_fma_mix_f32 v181, v18, v149, v157 op_sel:[1,0,0] op_sel_hi:[1,0,0]\n"
"v_mfma_f32_16x16x32_bf16 a[0:3], a[160:163], v[0:3], 0\n"
"ds_read_b64_tr_b16 a[232:233], v219 offset:33304\n"
"v_fma_mix_f32 v182, v19, v150, v158 op_sel:[0,0,0] op_sel_hi:[1,0,0]\n"
"v_mfma_f32_16x16x32_bf16 a[0:3], a[164:167], v[8:11], a[0:3]\n"
"ds_read_b64_tr_b16 a[234:235], v219 offset:34328\n"
"v_fma_mix_f32 v183, v19, v151, v159 op_sel:[1,0,0] op_sel_hi:[1,0,0]\n"
"v_mfma_f32_16x16x32_bf16 a[4:7], a[160:163], v[4:7], 0\n"
"s_waitcnt vmcnt(8)\n"
"s_barrier\n"
"ds_read_b64_tr_b16 a[236:237], v219 offset:37400\n"
"ds_read_b128 v[168:171], v224 offset:896\n"
"v_mfma_f32_16x16x32_bf16 a[4:7], a[164:167], v[12:15], a[4:7]\n"
"ds_read_b64_tr_b16 a[238:239], v219 offset:38424\n"
"v_cvt_pk_bf16_f32 v16, v176, v177\n"
"v_mfma_f32_16x16x32_bf16 a[8:11], a[168:171], v[0:3], 0\n"
"s_add_u32 m0, s51, 65568\n"
"ds_read_b64_tr_b16 a[240:241], v219 offset:33552\n"
"v_cvt_pk_bf16_f32 v17, v178, v179\n"
"v_mfma_f32_16x16x32_bf16 a[8:11], a[172:175], v[8:11], a[8:11]\n"
"ds_read_b64_tr_b16 a[242:243], v219 offset:34576\n"
"buffer_load_dwordx4 v217, s[20:23], s50 offen offset:0 lds\n"
"v_mfma_f32_16x16x32_bf16 a[12:15], a[168:171], v[4:7], 0\n"
"ds_read_b64_tr_b16 a[244:245], v219 offset:37648\n"
"ds_read_b128 v[172:175], v224 offset:960\n"
"v_mfma_f32_16x16x32_bf16 a[12:15], a[172:175], v[12:15], a[12:15]\n"
"ds_read_b64_tr_b16 a[246:247], v219 offset:38672\n"
"v_cvt_pk_bf16_f32 v18, v180, v181\n"
"v_mfma_f32_16x16x32_bf16 a[16:19], a[176:179], v[0:3], 0\n"
"ds_read_b64_tr_b16 a[248:249], v219 offset:33560\n"
"v_cvt_pk_bf16_f32 v19, v182, v183\n"
"v_mfma_f32_16x16x32_bf16 a[16:19], a[180:183], v[8:11], a[16:19]\n"
"ds_read_b64_tr_b16 a[250:251], v219 offset:34584\n"
"v_fma_mix_f32 v184, v20, v144, v152 op_sel:[0,0,0] op_sel_hi:[1,0,0]\n"
"v_mfma_f32_16x16x32_bf16 a[20:23], a[176:179], v[4:7], 0\n"
"s_add_u32 m0, s51, 73504\n"
"ds_read_b64_tr_b16 a[252:253], v219 offset:37656\n"
"v_fma_mix_f32 v185, v20, v145, v153 op_sel:[1,0,0] op_sel_hi:[1,0,0]\n"
"v_mfma_f32_16x16x32_bf16 a[20:23], a[180:183], v[12:15], a[20:23]\n"
"ds_read_b64_tr_b16 a[254:255], v219 offset:38680\n"
"buffer_load_dwordx4 v217, s[20:23], s50 offen offset:256 lds\n"
"v_mfma_f32_16x16x32_bf16 a[24:27], a[184:187], v[0:3], 0\n"
"s_add_u32 s50, s50, 32768\n"
"ds_read_b64_tr_b16 a[160:161], v219 offset:40976\n"
"v_fma_mix_f32 v186, v21, v146, v154 op_sel:[0,0,0] op_sel_hi:[1,0,0]\n"
"v_mfma_f32_16x16x32_bf16 a[24:27], a[188:191], v[8:11], a[24:27]\n"
"ds_read_b64_tr_b16 a[162:163], v219 offset:42000\n"
"v_fma_mix_f32 v187, v21, v147, v155 op_sel:[1,0,0] op_sel_hi:[1,0,0]\n"
"v_mfma_f32_16x16x32_bf16 a[28:31], a[184:187], v[4:7], 0\n"
"ds_read_b64_tr_b16 a[164:165], v219 offset:45072\n"
"v_fma_mix_f32 v188, v22, v148, v156 op_sel:[0,0,0] op_sel_hi:[1,0,0]\n"
"v_mfma_f32_16x16x32_bf16 a[28:31], a[188:191], v[12:15], a[28:31]\n"
"s_waitcnt lgkmcnt(12)\n"
"ds_read_b64_tr_b16 a[166:167], v219 offset:46096\n"
"v_fma_mix_f32 v189, v22, v149, v157 op_sel:[1,0,0] op_sel_hi:[1,0,0]\n"
"v_mfma_f32_16x16x32_bf16 a[32:35], a[224:227], v[0:3], 0\n"
"s_add_u32 m0, s51, 66592\n"
"ds_read_b64_tr_b16 a[168:169], v219 offset:40984\n"
"v_fma_mix_f32 v190, v23, v150, v158 op_sel:[0,0,0] op_sel_hi:[1,0,0]\n"
"v_mfma_f32_16x16x32_bf16 a[32:35], a[228:231], v[8:11], a[32:35]\n"
"ds_read_b64_tr_b16 a[170:171], v219 offset:42008\n"
"buffer_load_dwordx4 v217, s[20:23], s50 offen offset:0 lds\n"
"v_mfma_f32_16x16x32_bf16 a[36:39], a[224:227], v[4:7], 0\n"
"ds_read_b64_tr_b16 a[172:173], v219 offset:45080\n"
"v_fma_mix_f32 v191, v23, v151, v159 op_sel:[1,0,0] op_sel_hi:[1,0,0]\n"
"v_mfma_f32_16x16x32_bf16 a[36:39], a[228:231], v[12:15], a[36:39]\n"
"ds_read_b64_tr_b16 a[174:175], v219 offset:46104\n"
"ds_read_b128 v[144:147], v224 offset:1024\n"
"v_mfma_f32_16x16x32_bf16 a[40:43], a[232:235], v[0:3], 0\n"
"ds_read_b64_tr_b16 a[176:177], v219 offset:41232\n"
"v_cvt_pk_bf16_f32 v20, v184, v185\n"
"v_mfma_f32_16x16x32_bf16 a[40:43], a[236:239], v[8:11], a[40:43]\n"
"ds_read_b64_tr_b16 a[178:179], v219 offset:42256\n"
"v_cvt_pk_bf16_f32 v21, v186, v187\n"
"v_mfma_f32_16x16x32_bf16 a[44:47], a[232:235], v[4:7], 0\n"
"s_add_u32 m0, s51, 74528\n"
"ds_read_b64_tr_b16 a[180:181], v219 offset:45328\n"
"ds_read_b128 v[148:151], v224 offset:1088\n"
"v_mfma_f32_16x16x32_bf16 a[44:47], a[236:239], v[12:15], a[44:47]\n"
"s_waitcnt lgkmcnt(13)\n"
"ds_read_b64_tr_b16 a[182:183], v219 offset:46352\n"
"buffer_load_dwordx4 v217, s[20:23], s50 offen offset:256 lds\n"
"v_mfma_f32_16x16x32_bf16 a[48:51], a[240:243], v[0:3], 0\n"
"s_add_u32 s50, s50, 32768\n"
"ds_read_b64_tr_b16 a[184:185], v219 offset:41240\n"
"v_cvt_pk_bf16_f32 v22, v188, v189\n"
"v_mfma_f32_16x16x32_bf16 a[48:51], a[244:247], v[8:11], a[48:51]\n"
"ds_read_b64_tr_b16 a[186:187], v219 offset:42264\n"
"ds_read_b128 v[152:155], v224 offset:1152\n"
"v_mfma_f32_16x16x32_bf16 a[52:55], a[240:243], v[4:7], 0\n"
"ds_read_b64_tr_b16 a[188:189], v219 offset:45336\n"
"v_cvt_pk_bf16_f32 v23, v190, v191\n"
"v_mfma_f32_16x16x32_bf16 a[52:55], a[244:247], v[12:15], a[52:55]\n"
"ds_read_b64_tr_b16 a[190:191], v219 offset:46360\n"
"v_fma_mix_f32 v176, v24, v160, v168 op_sel:[0,0,0] op_sel_hi:[1,0,0]\n"
"v_mfma_f32_16x16x32_bf16 a[56:59], a[248:251], v[0:3], 0\n"
"s_add_u32 m0, s51, 81960\n"
"ds_read_b64_tr_b16 a[224:225], v219 offset:41488\n"
"ds_read_b128 v[156:159], v224 offset:1216\n"
"v_mfma_f32_16x16x32_bf16 a[56:59], a[252:255], v[8:11], a[56:59]\n"
"ds_read_b64_tr_b16 a[226:227], v219 offset:42512\n"
"buffer_load_dwordx4 v217, s[20:23], s50 offen offset:0 lds\n"
"v_mfma_f32_16x16x32_bf16 a[60:63], a[248:251], v[4:7], 0\n"
"ds_read_b64_tr_b16 a[228:229], v219 offset:45584\n"
"v_fma_mix_f32 v177, v24, v161, v169 op_sel:[1,0,0] op_sel_hi:[1,0,0]\n"
"v_mfma_f32_16x16x32_bf16 a[60:63], a[252:255], v[12:15], a[60:63]\n"
"s_waitcnt lgkmcnt(14)\n"
"ds_read_b64_tr_b16 a[230:231], v219 offset:46608\n"
"v_fma_mix_f32 v178, v25, v162, v170 op_sel:[0,0,0] op_sel_hi:[1,0,0]\n"
"v_mfma_f32_16x16x32_bf16 a[64:67], a[160:163], v[0:3], 0\n"
"ds_read_b64_tr_b16 a[232:233], v219 offset:41496\n"
"v_fma_mix_f32 v179, v25, v163, v171 op_sel:[1,0,0] op_sel_hi:[1,0,0]\n"
"v_mfma_f32_16x16x32_bf16 a[64:67], a[164:167], v[8:11], a[64:67]\n"
"ds_read_b64_tr_b16 a[234:235], v219 offset:42520\n"
"v_fma_mix_f32 v180, v26, v164, v172 op_sel:[0,0,0] op_sel_hi:[1,0,0]\n"
"v_mfma_f32_16x16x32_bf16 a[68:71], a[160:163], v[4:7], 0\n"
"s_add_u32 m0, s51, 89896\n"
"ds_read_b64_tr_b16 a[236:237], v219 offset:45592\n"
"v_fma_mix_f32 v181, v26, v165, v173 op_sel:[1,0,0] op_sel_hi:[1,0,0]\n"
"v_mfma_f32_16x16x32_bf16 a[68:71], a[164:167], v[12:15], a[68:71]\n"
"ds_read_b64_tr_b16 a[238:239], v219 offset:46616\n"
"buffer_load_dwordx4 v217, s[20:23], s50 offen offset:256 lds\n"
"v_mfma_f32_16x16x32_bf16 a[72:75], a[168:171], v[0:3], 0\n"
"s_add_u32 s50, s50, 32768\n"
"ds_read_b64_tr_b16 a[240:241], v219 offset:41744\n"
"v_fma_mix_f32 v182, v27, v166, v174 op_sel:[0,0,0] op_sel_hi:[1,0,0]\n"
"v_mfma_f32_16x16x32_bf16 a[72:75], a[172:175], v[8:11], a[72:75]\n"
"ds_read_b64_tr_b16 a[242:243], v219 offset:42768\n"
"v_fma_mix_f32 v183, v27, v167, v175 op_sel:[1,0,0] op_sel_hi:[1,0,0]\n"
"v_mfma_f32_16x16x32_bf16 a[76:79], a[168:171], v[4:7], 0\n"
"ds_read_b64_tr_b16 a[244:245], v219 offset:45840\n"
"v_cvt_pk_bf16_f32 v24, v176, v177\n"
"v_mfma_f32_16x16x32_bf16 a[76:79], a[172:175], v[12:15], a[76:79]\n"
"s_waitcnt lgkmcnt(12)\n"
"ds_read_b64_tr_b16 a[246:247], v219 offset:46864\n"
"v_cvt_pk_bf16_f32 v25, v178, v179\n"
"v_mfma_f32_16x16x32_bf16 a[80:83], a[176:179], v[0:3], 0\n"
"s_add_u32 m0, s51, 82984\n"
"ds_read_b64_tr_b16 a[248:249], v219 offset:41752\n"
"v_cvt_pk_bf16_f32 v26, v180, v181\n"
"v_mfma_f32_16x16x32_bf16 a[80:83], a[180:183], v[8:11], a[80:83]\n"
"ds_read_b64_tr_b16 a[250:251], v219 offset:42776\n"
"buffer_load_dwordx4 v217, s[20:23], s50 offen offset:0 lds\n"
"v_mfma_f32_16x16x32_bf16 a[84:87], a[176:179], v[4:7], 0\n"
"ds_read_b64_tr_b16 a[252:253], v219 offset:45848\n"
"v_cvt_pk_bf16_f32 v27, v182, v183\n"
"v_mfma_f32_16x16x32_bf16 a[84:87], a[180:183], v[12:15], a[84:87]\n"
"ds_read_b64_tr_b16 a[254:255], v219 offset:46872\n"
"v_fma_mix_f32 v184, v28, v160, v168 op_sel:[0,0,0] op_sel_hi:[1,0,0]\n"
"v_mfma_f32_16x16x32_bf16 a[88:91], a[184:187], v[0:3], 0\n"
"ds_read_b64_tr_b16 a[160:161], v218 offset:0\n"
"v_fma_mix_f32 v185, v28, v161, v169 op_sel:[1,0,0] op_sel_hi:[1,0,0]\n"
"v_fma_mix_f32 v186, v29, v162, v170 op_sel:[0,0,0] op_sel_hi:[1,0,0]\n"
"v_mfma_f32_16x16x32_bf16 a[88:91], a[188:191], v[8:11], a[88:91]\n"
"ds_read_b64_tr_b16 a[162:163], v218 offset:1024\n"
"v_fma_mix_f32 v187, v29, v163, v171 op_sel:[1,0,0] op_sel_hi:[1,0,0]\n"
"v_mfma_f32_16x16x32_bf16 a[92:95], a[184:187], v[4:7], 0\n"
"s_add_u32 m0, s51, 90920\n"
"ds_read_b64_tr_b16 a[164:165], v218 offset:4096\n"
"v_fma_mix_f32 v188, v30, v164, v172 op_sel:[0,0,0] op_sel_hi:[1,0,0]\n"
"v_mfma_f32_16x16x32_bf16 a[92:95], a[188:191], v[12:15], a[92:95]\n"
"s_waitcnt lgkmcnt(11)\n"
"buffer_load_dwordx4 v217, s[20:23], s50 offen offset:256 lds\n"
"ds_read_b64_tr_b16 a[166:167], v218 offset:5120\n"
"v_mfma_f32_16x16x32_bf16 a[96:99], a[224:227], v[0:3], 0\n"
"s_add_u32 s50, s50, 425984\n"
"ds_read_b64_tr_b16 a[168:169], v218 offset:8\n"
"v_fma_mix_f32 v189, v30, v165, v173 op_sel:[1,0,0] op_sel_hi:[1,0,0]\n"
"v_mfma_f32_16x16x32_bf16 a[96:99], a[228:231], v[8:11], a[96:99]\n"
"ds_read_b64_tr_b16 a[170:171], v218 offset:1032\n"
"v_fma_mix_f32 v190, v31, v166, v174 op_sel:[0,0,0] op_sel_hi:[1,0,0]\n"
"v_mfma_f32_16x16x32_bf16 a[100:103], a[224:227], v[4:7], 0\n"
"ds_read_b64_tr_b16 a[172:173], v218 offset:4104\n"
"v_fma_mix_f32 v191, v31, v167, v175 op_sel:[1,0,0] op_sel_hi:[1,0,0]\n"
"v_mfma_f32_16x16x32_bf16 a[100:103], a[228:231], v[12:15], a[100:103]\n"
"ds_read_b64_tr_b16 a[174:175], v218 offset:5128\n"
"ds_read_b128 v[160:163], v224 offset:1280\n"
"v_mfma_f32_16x16x32_bf16 a[104:107], a[232:235], v[0:3], 0\n"
"ds_read_b64_tr_b16 a[176:177], v218 offset:256\n"
"v_cvt_pk_bf16_f32 v28, v184, v185\n"
"v_mfma_f32_16x16x32_bf16 a[104:107], a[236:239], v[8:11], a[104:107]\n"
"ds_read_b64_tr_b16 a[178:179], v218 offset:1280\n"
"v_cvt_pk_bf16_f32 v29, v186, v187\n"
"v_mfma_f32_16x16x32_bf16 a[108:111], a[232:235], v[4:7], 0\n"
"ds_read_b64_tr_b16 a[180:181], v218 offset:4352\n"
"ds_read_b128 v[164:167], v224 offset:1344\n"
"v_mfma_f32_16x16x32_bf16 a[108:111], a[236:239], v[12:15], a[108:111]\n"
"s_waitcnt lgkmcnt(13)\n"
"ds_read_b64_tr_b16 a[182:183], v218 offset:5376\n"
"v_cvt_pk_bf16_f32 v30, v188, v189\n"
"v_mfma_f32_16x16x32_bf16 a[112:115], a[240:243], v[0:3], 0\n"
"ds_read_b64_tr_b16 a[184:185], v218 offset:264\n"
"v_cvt_pk_bf16_f32 v31, v190, v191\n"
"v_mfma_f32_16x16x32_bf16 a[112:115], a[244:247], v[8:11], a[112:115]\n"
"ds_read_b64_tr_b16 a[186:187], v218 offset:1288\n"
"ds_read_b128 v[168:171], v224 offset:1408\n"
"v_mfma_f32_16x16x32_bf16 a[116:119], a[240:243], v[4:7], 0\n"
"ds_read_b64_tr_b16 a[188:189], v218 offset:4360\n"
"v_fma_mix_f32 v176, v32, v144, v152 op_sel:[0,0,0] op_sel_hi:[1,0,0]\n"
"v_mfma_f32_16x16x32_bf16 a[116:119], a[244:247], v[12:15], a[116:119]\n"
"ds_read_b64_tr_b16 a[190:191], v218 offset:5384\n"
"v_fma_mix_f32 v177, v32, v145, v153 op_sel:[1,0,0] op_sel_hi:[1,0,0]\n"
"v_mfma_f32_16x16x32_bf16 a[120:123], a[248:251], v[0:3], 0\n"
"ds_read_b64_tr_b16 a[224:225], v218 offset:512\n"
"ds_read_b128 v[172:175], v224 offset:1472\n"
"v_mfma_f32_16x16x32_bf16 a[120:123], a[252:255], v[8:11], a[120:123]\n"
"ds_read_b64_tr_b16 a[226:227], v218 offset:1536\n"
"v_fma_mix_f32 v178, v33, v146, v154 op_sel:[0,0,0] op_sel_hi:[1,0,0]\n"
"v_mfma_f32_16x16x32_bf16 a[124:127], a[248:251], v[4:7], 0\n"
"ds_read_b64_tr_b16 a[228:229], v218 offset:4608\n"
"v_fma_mix_f32 v179, v33, v147, v155 op_sel:[1,0,0] op_sel_hi:[1,0,0]\n"
"v_mfma_f32_16x16x32_bf16 a[124:127], a[252:255], v[12:15], a[124:127]\n"
"s_waitcnt lgkmcnt(14)\n"
"ds_read_b64_tr_b16 a[230:231], v218 offset:5632\n"
"v_fma_mix_f32 v180, v34, v148, v156 op_sel:[0,0,0] op_sel_hi:[1,0,0]\n"
"v_mfma_f32_16x16x32_bf16 a[0:3], a[160:163], v[16:19], a[0:3]\n"
"ds_read_b64_tr_b16 a[232:233], v218 offset:520\n"
"v_fma_mix_f32 v181, v34, v149, v157 op_sel:[1,0,0] op_sel_hi:[1,0,0]\n"
"v_mfma_f32_16x16x32_bf16 a[0:3], a[164:167], v[24:27], a[0:3]\n"
"ds_read_b64_tr_b16 a[234:235], v218 offset:1544\n"
"v_fma_mix_f32 v182, v35, v150, v158 op_sel:[0,0,0] op_sel_hi:[1,0,0]\n"
"v_mfma_f32_16x16x32_bf16 a[4:7], a[160:163], v[20:23], a[4:7]\n"
"s_waitcnt vmcnt(8)\n"
"s_barrier\n"
"ds_read_b64_tr_b16 a[236:237], v218 offset:4616\n"
"v_fma_mix_f32 v183, v35, v151, v159 op_sel:[1,0,0] op_sel_hi:[1,0,0]\n"
"v_mfma_f32_16x16x32_bf16 a[4:7], a[164:167], v[28:31], a[4:7]\n"
"ds_read_b64_tr_b16 a[238:239], v218 offset:5640\n"
"v_cvt_pk_bf16_f32 v32, v176, v177\n"
"v_mfma_f32_16x16x32_bf16 a[8:11], a[168:171], v[16:19], a[8:11]\n"
"s_add_u32 m0, s51, 98352\n"
"ds_read_b64_tr_b16 a[240:241], v218 offset:768\n"
"v_cvt_pk_bf16_f32 v33, v178, v179\n"
"v_mfma_f32_16x16x32_bf16 a[8:11], a[172:175], v[24:27], a[8:11]\n"
"ds_read_b64_tr_b16 a[242:243], v218 offset:1792\n"
"buffer_load_dwordx4 v217, s[20:23], s50 offen offset:0 lds\n"
"v_mfma_f32_16x16x32_bf16 a[12:15], a[168:171], v[20:23], a[12:15]\n"
"ds_read_b64_tr_b16 a[244:245], v218 offset:4864\n"
"v_cvt_pk_bf16_f32 v34, v180, v181\n"
"v_mfma_f32_16x16x32_bf16 a[12:15], a[172:175], v[28:31], a[12:15]\n"
"s_waitcnt lgkmcnt(12)\n"
"ds_read_b64_tr_b16 a[246:247], v218 offset:5888\n"
"v_cvt_pk_bf16_f32 v35, v182, v183\n"
"v_mfma_f32_16x16x32_bf16 a[16:19], a[176:179], v[16:19], a[16:19]\n"
"ds_read_b64_tr_b16 a[248:249], v218 offset:776\n"
"v_fma_mix_f32 v184, v36, v144, v152 op_sel:[0,0,0] op_sel_hi:[1,0,0]\n"
"v_mfma_f32_16x16x32_bf16 a[16:19], a[180:183], v[24:27], a[16:19]\n"
"ds_read_b64_tr_b16 a[250:251], v218 offset:1800\n"
"v_fma_mix_f32 v185, v36, v145, v153 op_sel:[1,0,0] op_sel_hi:[1,0,0]\n"
"v_mfma_f32_16x16x32_bf16 a[20:23], a[176:179], v[20:23], a[20:23]\n"
"s_add_u32 m0, s51, 106288\n"
"ds_read_b64_tr_b16 a[252:253], v218 offset:4872\n"
"v_fma_mix_f32 v186, v37, v146, v154 op_sel:[0,0,0] op_sel_hi:[1,0,0]\n"
"v_mfma_f32_16x16x32_bf16 a[20:23], a[180:183], v[28:31], a[20:23]\n"
"ds_read_b64_tr_b16 a[254:255], v218 offset:5896\n"
"buffer_load_dwordx4 v217, s[20:23], s50 offen offset:256 lds\n"
"v_mfma_f32_16x16x32_bf16 a[24:27], a[184:187], v[16:19], a[24:27]\n"
"s_add_u32 s50, s50, 32768\n"
"ds_read_b64_tr_b16 a[160:161], v218 offset:8192\n"
"v_fma_mix_f32 v187, v37, v147, v155 op_sel:[1,0,0] op_sel_hi:[1,0,0]\n"
"v_mfma_f32_16x16x32_bf16 a[24:27], a[188:191], v[24:27], a[24:27]\n"
"ds_read_b64_tr_b16 a[162:163], v218 offset:9216\n"
"v_fma_mix_f32 v188, v38, v148, v156 op_sel:[0,0,0] op_sel_hi:[1,0,0]\n"
"v_mfma_f32_16x16x32_bf16 a[28:31], a[184:187], v[20:23], a[28:31]\n"
"ds_read_b64_tr_b16 a[164:165], v218 offset:12288\n"
"v_fma_mix_f32 v189, v38, v149, v157 op_sel:[1,0,0] op_sel_hi:[1,0,0]\n"
"v_mfma_f32_16x16x32_bf16 a[28:31], a[188:191], v[28:31], a[28:31]\n"
"s_waitcnt lgkmcnt(11)\n"
"ds_read_b64_tr_b16 a[166:167], v218 offset:13312\n"
"v_fma_mix_f32 v190, v39, v150, v158 op_sel:[0,0,0] op_sel_hi:[1,0,0]\n"
"v_mfma_f32_16x16x32_bf16 a[32:35], a[224:227], v[16:19], a[32:35]\n"
"s_add_u32 m0, s51, 99376\n"
"ds_read_b64_tr_b16 a[168:169], v218 offset:8200\n"
"v_fma_mix_f32 v191, v39, v151, v159 op_sel:[1,0,0] op_sel_hi:[1,0,0]\n"
"v_mfma_f32_16x16x32_bf16 a[32:35], a[228:231], v[24:27], a[32:35]\n"
"s_and_b32 s62, s42, 7\n"
"ds_read_b64_tr_b16 a[170:171], v218 offset:9224\n"
"buffer_load_dwordx4 v217, s[20:23], s50 offen offset:0 lds\n"
"v_mfma_f32_16x16x32_bf16 a[36:39], a[224:227], v[20:23], a[36:39]\n"
"ds_read_b64_tr_b16 a[172:173], v218 offset:12296\n"
"ds_read_b128 v[144:147], v224 offset:1536\n"
"v_mfma_f32_16x16x32_bf16 a[36:39], a[228:231], v[28:31], a[36:39]\n"
"s_mul_i32 s46, s62, 663552\n"
"ds_read_b64_tr_b16 a[174:175], v218 offset:13320\n"
"v_cvt_pk_bf16_f32 v36, v184, v185\n"
"v_mfma_f32_16x16x32_bf16 a[40:43], a[232:235], v[16:19], a[40:43]\n"
"ds_read_b64_tr_b16 a[176:177], v218 offset:8448\n"
"v_cvt_pk_bf16_f32 v37, v186, v187\n"
"v_mfma_f32_16x16x32_bf16 a[40:43], a[236:239], v[24:27], a[40:43]\n"
"s_lshr_b32 s47, s43, 2\n"
"ds_read_b64_tr_b16 a[178:179], v218 offset:9472\n"
"ds_read_b128 v[148:151], v224 offset:1600\n"
"v_mfma_f32_16x16x32_bf16 a[44:47], a[232:235], v[20:23], a[44:47]\n"
"s_add_u32 m0, s51, 107312\n"
"ds_read_b64_tr_b16 a[180:181], v218 offset:12544\n"
"v_cvt_pk_bf16_f32 v38, v188, v189\n"
"v_mfma_f32_16x16x32_bf16 a[44:47], a[236:239], v[28:31], a[44:47]\n"
"s_waitcnt lgkmcnt(13)\n"
"s_mul_i32 s47, s47, 1179648\n"
"ds_read_b64_tr_b16 a[182:183], v218 offset:13568\n"
"buffer_load_dwordx4 v217, s[20:23], s50 offen offset:256 lds\n"
"v_mfma_f32_16x16x32_bf16 a[48:51], a[240:243], v[16:19], a[48:51]\n"
"s_add_u32 s50, s50, 32768\n"
"ds_read_b64_tr_b16 a[184:185], v218 offset:8456\n"
"ds_read_b128 v[152:155], v224 offset:1664\n"
"v_mfma_f32_16x16x32_bf16 a[48:51], a[244:247], v[24:27], a[48:51]\n"
"s_getpc_b64 s[32:33]\n"
"AT_pc1:\n"
"s_sub_u32 s32, s32, AT_pc1-AT_start\n"
"s_subb_u32 s33, s33, 0\n"
"ds_read_b64_tr_b16 a[186:187], v218 offset:9480\n"
"v_cvt_pk_bf16_f32 v39, v190, v191\n"
"v_mfma_f32_16x16x32_bf16 a[52:55], a[240:243], v[20:23], a[52:55]\n"
"ds_read_b64_tr_b16 a[188:189], v218 offset:12552\n"
"v_fma_mix_f32 v176, v40, v160, v168 op_sel:[0,0,0] op_sel_hi:[1,0,0]\n"
"v_mfma_f32_16x16x32_bf16 a[52:55], a[244:247], v[28:31], a[52:55]\n"
"s_and_b32 s33, s33, 0xffff\n"
"ds_read_b64_tr_b16 a[190:191], v218 offset:13576\n"
"ds_read_b128 v[156:159], v224 offset:1728\n"
"v_mfma_f32_16x16x32_bf16 a[56:59], a[248:251], v[16:19], a[56:59]\n"
"s_add_u32 m0, s51, 114744\n"
"ds_read_b64_tr_b16 a[224:225], v218 offset:8704\n"
"v_fma_mix_f32 v177, v40, v161, v169 op_sel:[1,0,0] op_sel_hi:[1,0,0]\n"
"v_mfma_f32_16x16x32_bf16 a[56:59], a[252:255], v[24:27], a[56:59]\n"
"s_mov_b32 s34, AT_kend-AT_start\n"
"ds_read_b64_tr_b16 a[226:227], v218 offset:9728\n"
"buffer_load_dwordx4 v217, s[20:23], s50 offen offset:0 lds\n"
"v_mfma_f32_16x16x32_bf16 a[60:63], a[248:251], v[20:23], a[60:63]\n"
"ds_read_b64_tr_b16 a[228:229], v218 offset:12800\n"
"v_fma_mix_f32 v178, v41, v162, v170 op_sel:[0,0,0] op_sel_hi:[1,0,0]\n"
"v_mfma_f32_16x16x32_bf16 a[60:63], a[252:255], v[28:31], a[60:63]\n"
"s_waitcnt lgkmcnt(14)\n"
"s_mov_b32 s35, 0x20000\n"
"ds_read_b64_tr_b16 a[230:231], v218 offset:13824\n"
"v_fma_mix_f32 v179, v41, v163, v171 op_sel:[1,0,0] op_sel_hi:[1,0,0]\n"
"v_mfma_f32_16x16x32_bf16 a[64:67], a[160:163], v[16:19], a[64:67]\n"
"ds_read_b64_tr_b16 a[232:233], v218 offset:8712\n"
"v_fma_mix_f32 v180, v42, v164, v172 op_sel:[0,0,0] op_sel_hi:[1,0,0]\n"
"v_mfma_f32_16x16x32_bf16 a[64:67], a[164:167], v[24:27], a[64:67]\n"
"s_lshr_b32 s62, s42, 3\n"
"ds_read_b64_tr_b16 a[234:235], v218 offset:9736\n"
"v_fma_mix_f32 v181, v42, v165, v173 op_sel:[1,0,0] op_sel_hi:[1,0,0]\n"
"v_mfma_f32_16x16x32_bf16 a[68:71], a[160:163], v[20:23], a[68:71]\n"
"s_add_u32 m0, s51, 122680\n"
"ds_read_b64_tr_b16 a[236:237], v218 offset:12808\n"
"v_fma_mix_f32 v182, v43, v166, v174 op_sel:[0,0,0] op_sel_hi:[1,0,0]\n"
"v_mfma_f32_16x16x32_bf16 a[68:71], a[164:167], v[28:31], a[68:71]\n"
"s_lshl_b32 s62, s62, 2\n"
"ds_read_b64_tr_b16 a[238:239], v218 offset:13832\n"
"buffer_load_dwordx4 v217, s[20:23], s50 offen offset:256 lds\n"
"v_mfma_f32_16x16x32_bf16 a[72:75], a[168:171], v[16:19], a[72:75]\n"
"s_add_u32 s50, s50, 32768\n"
"ds_read_b64_tr_b16 a[240:241], v218 offset:8960\n"
"v_fma_mix_f32 v183, v43, v167, v175 op_sel:[1,0,0] op_sel_hi:[1,0,0]\n"
"v_mfma_f32_16x16x32_bf16 a[72:75], a[172:175], v[24:27], a[72:75]\n"
"s_add_u32 s62, s62, s41\n"
"ds_read_b64_tr_b16 a[242:243], v218 offset:9984\n"
"v_cvt_pk_bf16_f32 v40, v176, v177\n"
"v_mfma_f32_16x16x32_bf16 a[76:79], a[168:171], v[20:23], a[76:79]\n"
"ds_read_b64_tr_b16 a[244:245], v218 offset:13056\n"
"v_cvt_pk_bf16_f32 v41, v178, v179\n"
"v_mfma_f32_16x16x32_bf16 a[76:79], a[172:175], v[28:31], a[76:79]\n"
"s_waitcnt lgkmcnt(11)\n"
"s_and_b32 s62, s62, 31\n"
"ds_read_b64_tr_b16 a[246:247], v218 offset:14080\n"
"v_cvt_pk_bf16_f32 v42, v180, v181\n"
"v_mfma_f32_16x16x32_bf16 a[80:83], a[176:179], v[16:19], a[80:83]\n"
"s_add_u32 m0, s51, 115768\n"
"ds_read_b64_tr_b16 a[248:249], v218 offset:8968\n"
"v_cvt_pk_bf16_f32 v43, v182, v183\n"
"v_mfma_f32_16x16x32_bf16 a[80:83], a[180:183], v[24:27], a[80:83]\n"
"s_lshl_b32 s48, s62, 12\n"
"ds_read_b64_tr_b16 a[250:251], v218 offset:9992\n"
"buffer_load_dwordx4 v217, s[20:23], s50 offen offset:0 lds\n"
"v_mfma_f32_16x16x32_bf16 a[84:87], a[176:179], v[20:23], a[84:87]\n"
"ds_read_b64_tr_b16 a[252:253], v218 offset:13064\n"
"v_fma_mix_f32 v184, v44, v160, v168 op_sel:[0,0,0] op_sel_hi:[1,0,0]\n"
"v_mfma_f32_16x16x32_bf16 a[84:87], a[180:183], v[28:31], a[84:87]\n"
"s_lshl_b32 s54, s44, 2\n"
"ds_read_b64_tr_b16 a[254:255], v218 offset:14088\n"
"v_fma_mix_f32 v185, v44, v161, v169 op_sel:[1,0,0] op_sel_hi:[1,0,0]\n"
"v_mfma_f32_16x16x32_bf16 a[88:91], a[184:187], v[16:19], a[88:91]\n"
"ds_read_b64_tr_b16 a[160:161], v218 offset:32784\n"
"v_fma_mix_f32 v186, v45, v162, v170 op_sel:[0,0,0] op_sel_hi:[1,0,0]\n"
"v_fma_mix_f32 v187, v45, v163, v171 op_sel:[1,0,0] op_sel_hi:[1,0,0]\n"
"v_mfma_f32_16x16x32_bf16 a[88:91], a[188:191], v[24:27], a[88:91]\n"
"s_add_u32 s54, s54, s41\n"
"ds_read_b64_tr_b16 a[162:163], v218 offset:33808\n"
"v_fma_mix_f32 v188, v46, v164, v172 op_sel:[0,0,0] op_sel_hi:[1,0,0]\n"
"v_mfma_f32_16x16x32_bf16 a[92:95], a[184:187], v[20:23], a[92:95]\n"
"s_add_u32 m0, s51, 123704\n"
"ds_read_b64_tr_b16 a[164:165], v218 offset:36880\n"
"v_fma_mix_f32 v189, v46, v165, v173 op_sel:[1,0,0] op_sel_hi:[1,0,0]\n"
"v_mfma_f32_16x16x32_bf16 a[92:95], a[188:191], v[28:31], a[92:95]\n"
"s_waitcnt lgkmcnt(11)\n"
"s_mul_i32 s54, s54, 36864\n"
"buffer_load_dwordx4 v217, s[20:23], s50 offen offset:256 lds\n"
"ds_read_b64_tr_b16 a[166:167], v218 offset:37904\n"
"v_mfma_f32_16x16x32_bf16 a[96:99], a[224:227], v[16:19], a[96:99]\n"
"s_add_u32 s50, s50, 425984\n"
"ds_read_b64_tr_b16 a[168:169], v218 offset:32792\n"
"v_fma_mix_f32 v190, v47, v166, v174 op_sel:[0,0,0] op_sel_hi:[1,0,0]\n"
"v_mfma_f32_16x16x32_bf16 a[96:99], a[228:231], v[24:27], a[96:99]\n"
"s_lshr_b32 s62, s43, 2\n"
"ds_read_b64_tr_b16 a[170:171], v218 offset:33816\n"
"v_fma_mix_f32 v191, v47, v167, v175 op_sel:[1,0,0] op_sel_hi:[1,0,0]\n"
"v_mfma_f32_16x16x32_bf16 a[100:103], a[224:227], v[20:23], a[100:103]\n"
"ds_read_b64_tr_b16 a[172:173], v218 offset:36888\n"
"ds_read_b128 v[160:163], v224 offset:1792\n"
"v_mfma_f32_16x16x32_bf16 a[100:103], a[228:231], v[28:31], a[100:103]\n"
"s_mul_i32 s62, s62, 2359296\n"
"ds_read_b64_tr_b16 a[174:175], v218 offset:37912\n"
"v_cvt_pk_bf16_f32 v44, v184, v185\n"
"v_mfma_f32_16x16x32_bf16 a[104:107], a[232:235], v[16:19], a[104:107]\n"
"ds_read_b64_tr_b16 a[176:177], v218 offset:33040\n"
"v_cvt_pk_bf16_f32 v45, v186, v187\n"
"v_mfma_f32_16x16x32_bf16 a[104:107], a[236:239], v[24:27], a[104:107]\n"
"s_add_u32 s54, s54, s62\n"
"ds_read_b64_tr_b16 a[178:179], v218 offset:34064\n"
"ds_read_b128 v[164:167], v224 offset:1856\n"
"v_mfma_f32_16x16x32_bf16 a[108:111], a[232:235], v[20:23], a[108:111]\n"
"ds_read_b64_tr_b16 a[180:181], v218 offset:37136\n"
"v_cvt_pk_bf16_f32 v46, v188, v189\n"
"v_mfma_f32_16x16x32_bf16 a[108:111], a[236:239], v[28:31], a[108:111]\n"
"s_waitcnt lgkmcnt(13)\n"
"s_add_u32 s54, s54, 139264\n"
"ds_read_b64_tr_b16 a[182:183], v218 offset:38160\n"
"v_cvt_pk_bf16_f32 v47, v190, v191\n"
"v_mfma_f32_16x16x32_bf16 a[112:115], a[240:243], v[16:19], a[112:115]\n"
"ds_read_b64_tr_b16 a[184:185], v218 offset:33048\n"
"ds_read_b128 v[168:171], v224 offset:1920\n"
"v_mfma_f32_16x16x32_bf16 a[112:115], a[244:247], v[24:27], a[112:115]\n"
"s_bfe_u32 s62, s43, 0x10001\n"
"ds_read_b64_tr_b16 a[186:187], v218 offset:34072\n"
"v_fma_mix_f32 v176, v48, v144, v152 op_sel:[0,0,0] op_sel_hi:[1,0,0]\n"
"v_mfma_f32_16x16x32_bf16 a[116:119], a[240:243], v[20:23], a[116:119]\n"
"ds_read_b64_tr_b16 a[188:189], v218 offset:37144\n"
"v_fma_mix_f32 v177, v48, v145, v153 op_sel:[1,0,0] op_sel_hi:[1,0,0]\n"
"v_mfma_f32_16x16x32_bf16 a[116:119], a[244:247], v[28:31], a[116:119]\n"
"s_mul_i32 s62, s62, 9216\n"
"ds_read_b64_tr_b16 a[190:191], v218 offset:38168\n"
"ds_read_b128 v[172:175], v224 offset:1984\n"
"v_mfma_f32_16x16x32_bf16 a[120:123], a[248:251], v[16:19], a[120:123]\n"
"ds_read_b64_tr_b16 a[224:225], v218 offset:33296\n"
"v_fma_mix_f32 v178, v49, v146, v154 op_sel:[0,0,0] op_sel_hi:[1,0,0]\n"
"v_mfma_f32_16x16x32_bf16 a[120:123], a[252:255], v[24:27], a[120:123]\n"
"s_add_u32 s54, s54, s62\n"
"ds_read_b64_tr_b16 a[226:227], v218 offset:34320\n"
"v_fma_mix_f32 v179, v49, v147, v155 op_sel:[1,0,0] op_sel_hi:[1,0,0]\n"
"v_mfma_f32_16x16x32_bf16 a[124:127], a[248:251], v[20:23], a[124:127]\n"
"ds_read_b64_tr_b16 a[228:229], v218 offset:37392\n"
"v_fma_mix_f32 v180, v50, v148, v156 op_sel:[0,0,0] op_sel_hi:[1,0,0]\n"
"v_mfma_f32_16x16x32_bf16 a[124:127], a[252:255], v[28:31], a[124:127]\n"
"s_waitcnt lgkmcnt(14)\n"
"s_and_b32 s62, s43, 1\n"
"ds_read_b64_tr_b16 a[230:231], v218 offset:38416\n"
"v_fma_mix_f32 v181, v50, v149, v157 op_sel:[1,0,0] op_sel_hi:[1,0,0]\n"
"v_mfma_f32_16x16x32_bf16 a[0:3], a[160:163], v[32:35], a[0:3]\n"
"ds_read_b64_tr_b16 a[232:233], v218 offset:33304\n"
"v_fma_mix_f32 v182, v51, v150, v158 op_sel:[0,0,0] op_sel_hi:[1,0,0]\n"
"v_mfma_f32_16x16x32_bf16 a[0:3], a[164:167], v[40:43], a[0:3]\n"
"s_lshl_b32 s49, s62, 3\n"
"ds_read_b64_tr_b16 a[234:235], v218 offset:34328\n"
"v_fma_mix_f32 v183, v51, v151, v159 op_sel:[1,0,0] op_sel_hi:[1,0,0]\n"
"v_mfma_f32_16x16x32_bf16 a[4:7], a[160:163], v[36:39], a[4:7]\n"
"s_waitcnt vmcnt(8)\n"
"s_barrier\n"
"ds_read_b64_tr_b16 a[236:237], v218 offset:37400\n"
"v_cvt_pk_bf16_f32 v48, v176, v177\n"
"v_mfma_f32_16x16x32_bf16 a[4:7], a[164:167], v[44:47], a[4:7]\n"
"s_add_u32 s49, s49, s54\n"
"ds_read_b64_tr_b16 a[238:239], v218 offset:38424\n"
"v_cvt_pk_bf16_f32 v49, v178, v179\n"
"v_mfma_f32_16x16x32_bf16 a[8:11], a[168:171], v[32:35], a[8:11]\n"
"s_mov_b32 m0, s51\n"
"ds_read_b64_tr_b16 a[240:241], v218 offset:33552\n"
"v_cvt_pk_bf16_f32 v50, v180, v181\n"
"v_mfma_f32_16x16x32_bf16 a[8:11], a[172:175], v[40:43], a[8:11]\n"
"s_add_u32 s49, s49, 4096\n"
"ds_read_b64_tr_b16 a[242:243], v218 offset:34576\n"
"buffer_load_dwordx4 v217, s[20:23], s50 offen offset:0 lds\n"
"v_mfma_f32_16x16x32_bf16 a[12:15], a[168:171], v[36:39], a[12:15]\n"
"ds_read_b64_tr_b16 a[244:245], v218 offset:37648\n"
"v_cvt_pk_bf16_f32 v51, v182, v183\n"
"v_mfma_f32_16x16x32_bf16 a[12:15], a[172:175], v[44:47], a[12:15]\n"
"s_waitcnt lgkmcnt(11)\n"
"s_mul_i32 s62, s62, 5120\n"
"ds_read_b64_tr_b16 a[246:247], v218 offset:38672\n"
"v_fma_mix_f32 v184, v52, v144, v152 op_sel:[0,0,0] op_sel_hi:[1,0,0]\n"
"v_mfma_f32_16x16x32_bf16 a[16:19], a[176:179], v[32:35], a[16:19]\n"
"ds_read_b64_tr_b16 a[248:249], v218 offset:33560\n"
"v_fma_mix_f32 v185, v52, v145, v153 op_sel:[1,0,0] op_sel_hi:[1,0,0]\n"
"v_mfma_f32_16x16x32_bf16 a[16:19], a[180:183], v[40:43], a[16:19]\n"
"s_add_u32 s54, s54, s62\n"
"ds_read_b64_tr_b16 a[250:251], v218 offset:34584\n"
"v_fma_mix_f32 v186, v53, v146, v154 op_sel:[0,0,0] op_sel_hi:[1,0,0]\n"
"v_mfma_f32_16x16x32_bf16 a[20:23], a[176:179], v[36:39], a[20:23]\n"
"s_add_u32 m0, s51, 7936\n"
"ds_read_b64_tr_b16 a[252:253], v218 offset:37656\n"
"v_fma_mix_f32 v187, v53, v147, v155 op_sel:[1,0,0] op_sel_hi:[1,0,0]\n"
"v_mfma_f32_16x16x32_bf16 a[20:23], a[180:183], v[44:47], a[20:23]\n"
"s_and_b32 s62, s43, 1\n"
"ds_read_b64_tr_b16 a[254:255], v218 offset:38680\n"
"buffer_load_dwordx4 v217, s[20:23], s50 offen offset:256 lds\n"
"v_mfma_f32_16x16x32_bf16 a[24:27], a[184:187], v[32:35], a[24:27]\n"
"s_add_u32 s50, s50, 32768\n"
"ds_read_b64_tr_b16 a[160:161], v218 offset:40976\n"
"v_fma_mix_f32 v188, v54, v148, v156 op_sel:[0,0,0] op_sel_hi:[1,0,0]\n"
"v_mfma_f32_16x16x32_bf16 a[24:27], a[188:191], v[40:43], a[24:27]\n"
"s_lshl_b32 s62, s62, 10\n"
"ds_read_b64_tr_b16 a[162:163], v218 offset:42000\n"
"v_fma_mix_f32 v189, v54, v149, v157 op_sel:[1,0,0] op_sel_hi:[1,0,0]\n"
"v_mfma_f32_16x16x32_bf16 a[28:31], a[184:187], v[36:39], a[28:31]\n"
"ds_read_b64_tr_b16 a[164:165], v218 offset:45072\n"
"v_fma_mix_f32 v190, v55, v150, v158 op_sel:[0,0,0] op_sel_hi:[1,0,0]\n"
"v_mfma_f32_16x16x32_bf16 a[28:31], a[188:191], v[44:47], a[28:31]\n"
"s_waitcnt lgkmcnt(11)\n"
"s_sub_u32 s54, s54, s62\n"
"ds_read_b64_tr_b16 a[166:167], v218 offset:46096\n"
"v_fma_mix_f32 v191, v55, v151, v159 op_sel:[1,0,0] op_sel_hi:[1,0,0]\n"
"v_mfma_f32_16x16x32_bf16 a[32:35], a[224:227], v[32:35], a[32:35]\n"
"s_add_u32 m0, s51, 1024\n"
"ds_read_b64_tr_b16 a[168:169], v218 offset:40984\n"
"ds_read_b128 v[144:147], v224 offset:2048\n"
"v_mfma_f32_16x16x32_bf16 a[32:35], a[228:231], v[40:43], a[32:35]\n"
"s_add_u32 s49, s54, 4096\n"
"ds_read_b64_tr_b16 a[170:171], v218 offset:42008\n"
"buffer_load_dwordx4 v217, s[20:23], s50 offen offset:0 lds\n"
"v_mfma_f32_16x16x32_bf16 a[36:39], a[224:227], v[36:39], a[36:39]\n"
"ds_read_b64_tr_b16 a[172:173], v218 offset:45080\n"
"v_cvt_pk_bf16_f32 v52, v184, v185\n"
"v_mfma_f32_16x16x32_bf16 a[36:39], a[228:231], v[44:47], a[36:39]\n"
"ds_read_b64_tr_b16 a[174:175], v218 offset:46104\n"
"ds_read_b128 v[148:151], v224 offset:2112\n"
"v_mfma_f32_16x16x32_bf16 a[40:43], a[232:235], v[32:35], a[40:43]\n"
"ds_read_b64_tr_b16 a[176:177], v218 offset:41232\n"
"v_cvt_pk_bf16_f32 v53, v186, v187\n"
"v_mfma_f32_16x16x32_bf16 a[40:43], a[236:239], v[40:43], a[40:43]\n"
"ds_read_b64_tr_b16 a[178:179], v218 offset:42256\n"
"v_cvt_pk_bf16_f32 v54, v188, v189\n"
"v_mfma_f32_16x16x32_bf16 a[44:47], a[232:235], v[36:39], a[44:47]\n"
"s_add_u32 m0, s51, 8960\n"
"ds_read_b64_tr_b16 a[180:181], v218 offset:45328\n"
"ds_read_b128 v[152:155], v224 offset:2176\n"
"v_mfma_f32_16x16x32_bf16 a[44:47], a[236:239], v[44:47], a[44:47]\n"
"s_waitcnt lgkmcnt(14)\n"
"ds_read_b64_tr_b16 a[182:183], v218 offset:46352\n"
"buffer_load_dwordx4 v217, s[20:23], s50 offen offset:256 lds\n"
"v_mfma_f32_16x16x32_bf16 a[48:51], a[240:243], v[32:35], a[48:51]\n"
"s_add_u32 s50, s50, 32768\n"
"ds_read_b64_tr_b16 a[184:185], v218 offset:41240\n"
"v_cvt_pk_bf16_f32 v55, v190, v191\n"
"v_mfma_f32_16x16x32_bf16 a[48:51], a[244:247], v[40:43], a[48:51]\n"
"ds_read_b64_tr_b16 a[186:187], v218 offset:42264\n"
"ds_read_b128 v[156:159], v224 offset:2240\n"
"v_mfma_f32_16x16x32_bf16 a[52:55], a[240:243], v[36:39], a[52:55]\n"
"ds_read_b64_tr_b16 a[188:189], v218 offset:45336\n"
"v_fma_mix_f32 v176, v56, v160, v168 op_sel:[0,0,0] op_sel_hi:[1,0,0]\n"
"v_mfma_f32_16x16x32_bf16 a[52:55], a[244:247], v[44:47], a[52:55]\n"
"ds_read_b64_tr_b16 a[190:191], v218 offset:46360\n"
"v_fma_mix_f32 v177, v56, v161, v169 op_sel:[1,0,0] op_sel_hi:[1,0,0]\n"
"v_mfma_f32_16x16x32_bf16 a[56:59], a[248:251], v[32:35], a[56:59]\n"
"s_add_u32 m0, s51, 16392\n"
"ds_read_b64_tr_b16 a[224:225], v218 offset:41488\n"
"v_fma_mix_f32 v178, v57, v162, v170 op_sel:[0,0,0] op_sel_hi:[1,0,0]\n"
"v_mfma_f32_16x16x32_bf16 a[56:59], a[252:255], v[40:43], a[56:59]\n"
"ds_read_b64_tr_b16 a[226:227], v218 offset:42512\n"
"buffer_load_dwordx4 v217, s[20:23], s50 offen offset:0 lds\n"
"v_mfma_f32_16x16x32_bf16 a[60:63], a[248:251], v[36:39], a[60:63]\n"
"ds_read_b64_tr_b16 a[228:229], v218 offset:45584\n"
"v_fma_mix_f32 v179, v57, v163, v171 op_sel:[1,0,0] op_sel_hi:[1,0,0]\n"
"v_mfma_f32_16x16x32_bf16 a[60:63], a[252:255], v[44:47], a[60:63]\n"
"s_waitcnt lgkmcnt(13)\n"
"ds_read_b64_tr_b16 a[230:231], v218 offset:46608\n"
"v_fma_mix_f32 v180, v58, v164, v172 op_sel:[0,0,0] op_sel_hi:[1,0,0]\n"
"v_mfma_f32_16x16x32_bf16 a[64:67], a[160:163], v[32:35], a[64:67]\n"
"ds_read_b64_tr_b16 a[232:233], v218 offset:41496\n"
"v_fma_mix_f32 v181, v58, v165, v173 op_sel:[1,0,0] op_sel_hi:[1,0,0]\n"
"v_mfma_f32_16x16x32_bf16 a[64:67], a[164:167], v[40:43], a[64:67]\n"
"ds_read_b64_tr_b16 a[234:235], v218 offset:42520\n"
"v_fma_mix_f32 v182, v59, v166, v174 op_sel:[0,0,0] op_sel_hi:[1,0,0]\n"
"v_mfma_f32_16x16x32_bf16 a[68:71], a[160:163], v[36:39], a[68:71]\n"
"s_add_u32 m0, s51, 24328\n"
"ds_read_b64_tr_b16 a[236:237], v218 offset:45592\n"
"v_fma_mix_f32 v183, v59, v167, v175 op_sel:[1,0,0] op_sel_hi:[1,0,0]\n"
"v_mfma_f32_16x16x32_bf16 a[68:71], a[164:167], v[44:47], a[68:71]\n"
"ds_read_b64_tr_b16 a[238:239], v218 offset:46616\n"
"buffer_load_dwordx4 v217, s[20:23], s50 offen offset:256 lds\n"
"v_mfma_f32_16x16x32_bf16 a[72:75], a[168:171], v[32:35], a[72:75]\n"
"s_add_u32 s50, s50, 32768\n"
"ds_read_b64_tr_b16 a[240:241], v218 offset:41744\n"
"v_cvt_pk_bf16_f32 v56, v176, v177\n"
"v_mfma_f32_16x16x32_bf16 a[72:75], a[172:175], v[40:43], a[72:75]\n"
"ds_read_b64_tr_b16 a[242:243], v218 offset:42768\n"
"v_cvt_pk_bf16_f32 v57, v178, v179\n"
"v_mfma_f32_16x16x32_bf16 a[76:79], a[168:171], v[36:39], a[76:79]\n"
"ds_read_b64_tr_b16 a[244:245], v218 offset:45840\n"
"v_cvt_pk_bf16_f32 v58, v180, v181\n"
"v_mfma_f32_16x16x32_bf16 a[76:79], a[172:175], v[44:47], a[76:79]\n"
"s_waitcnt lgkmcnt(11)\n"
"ds_read_b64_tr_b16 a[246:247], v218 offset:46864\n"
"v_cvt_pk_bf16_f32 v59, v182, v183\n"
"v_mfma_f32_16x16x32_bf16 a[80:83], a[176:179], v[32:35], a[80:83]\n"
"s_add_u32 m0, s51, 17416\n"
"ds_read_b64_tr_b16 a[248:249], v218 offset:41752\n"
"v_fma_mix_f32 v184, v60, v160, v168 op_sel:[0,0,0] op_sel_hi:[1,0,0]\n"
"v_mfma_f32_16x16x32_bf16 a[80:83], a[180:183], v[40:43], a[80:83]\n"
"ds_read_b64_tr_b16 a[250:251], v218 offset:42776\n"
"buffer_load_dwordx4 v217, s[20:23], s50 offen offset:0 lds\n"
"v_mfma_f32_16x16x32_bf16 a[84:87], a[176:179], v[36:39], a[84:87]\n"
"ds_read_b64_tr_b16 a[252:253], v218 offset:45848\n"
"v_fma_mix_f32 v185, v60, v161, v169 op_sel:[1,0,0] op_sel_hi:[1,0,0]\n"
"v_mfma_f32_16x16x32_bf16 a[84:87], a[180:183], v[44:47], a[84:87]\n"
"ds_read_b64_tr_b16 a[254:255], v218 offset:46872\n"
"v_fma_mix_f32 v186, v61, v162, v170 op_sel:[0,0,0] op_sel_hi:[1,0,0]\n"
"v_mfma_f32_16x16x32_bf16 a[88:91], a[184:187], v[32:35], a[88:91]\n"
"ds_read_b64_tr_b16 a[160:161], v219 offset:0\n"
"v_fma_mix_f32 v187, v61, v163, v171 op_sel:[1,0,0] op_sel_hi:[1,0,0]\n"
"v_fma_mix_f32 v188, v62, v164, v172 op_sel:[0,0,0] op_sel_hi:[1,0,0]\n"
"v_mfma_f32_16x16x32_bf16 a[88:91], a[188:191], v[40:43], a[88:91]\n"
"ds_read_b64_tr_b16 a[162:163], v219 offset:1024\n"
"v_fma_mix_f32 v189, v62, v165, v173 op_sel:[1,0,0] op_sel_hi:[1,0,0]\n"
"v_mfma_f32_16x16x32_bf16 a[92:95], a[184:187], v[36:39], a[92:95]\n"
"s_add_u32 m0, s51, 25352\n"
"ds_read_b64_tr_b16 a[164:165], v219 offset:4096\n"
"v_fma_mix_f32 v190, v63, v166, v174 op_sel:[0,0,0] op_sel_hi:[1,0,0]\n"
"v_mfma_f32_16x16x32_bf16 a[92:95], a[188:191], v[44:47], a[92:95]\n"
"s_waitcnt lgkmcnt(11)\n"
"buffer_load_dwordx4 v217, s[20:23], s50 offen offset:256 lds\n"
"ds_read_b64_tr_b16 a[166:167], v219 offset:5120\n"
"v_mfma_f32_16x16x32_bf16 a[96:99], a[224:227], v[32:35], a[96:99]\n"
"s_add_u32 s50, s50, 425984\n"
"ds_read_b64_tr_b16 a[168:169], v219 offset:8\n"
"v_fma_mix_f32 v191, v63, v167, v175 op_sel:[1,0,0] op_sel_hi:[1,0,0]\n"
"v_mfma_f32_16x16x32_bf16 a[96:99], a[228:231], v[40:43], a[96:99]\n"
"ds_read_b64_tr_b16 a[170:171], v219 offset:1032\n"
"ds_read_b128 v[160:163], v224 offset:2304\n"
"v_mfma_f32_16x16x32_bf16 a[100:103], a[224:227], v[36:39], a[100:103]\n"
"ds_read_b64_tr_b16 a[172:173], v219 offset:4104\n"
"v_cvt_pk_bf16_f32 v60, v184, v185\n"
"v_mfma_f32_16x16x32_bf16 a[100:103], a[228:231], v[44:47], a[100:103]\n"
"ds_read_b64_tr_b16 a[174:175], v219 offset:5128\n"
"v_cvt_pk_bf16_f32 v61, v186, v187\n"
"v_mfma_f32_16x16x32_bf16 a[104:107], a[232:235], v[32:35], a[104:107]\n"
"ds_read_b64_tr_b16 a[176:177], v219 offset:256\n"
"ds_read_b128 v[164:167], v224 offset:2368\n"
"v_mfma_f32_16x16x32_bf16 a[104:107], a[236:239], v[40:43], a[104:107]\n"
"ds_read_b64_tr_b16 a[178:179], v219 offset:1280\n"
"v_cvt_pk_bf16_f32 v62, v188, v189\n"
"v_mfma_f32_16x16x32_bf16 a[108:111], a[232:235], v[36:39], a[108:111]\n"
"ds_read_b64_tr_b16 a[180:181], v219 offset:4352\n"
"v_cvt_pk_bf16_f32 v63, v190, v191\n"
"v_mfma_f32_16x16x32_bf16 a[108:111], a[236:239], v[44:47], a[108:111]\n"
"s_waitcnt lgkmcnt(13)\n"
"ds_read_b64_tr_b16 a[182:183], v219 offset:5376\n"
"ds_read_b128 v[168:171], v224 offset:2432\n"
"v_mfma_f32_16x16x32_bf16 a[112:115], a[240:243], v[32:35], a[112:115]\n"
"ds_read_b64_tr_b16 a[184:185], v219 offset:264\n"
"v_fma_mix_f32 v176, v64, v144, v152 op_sel:[0,0,0] op_sel_hi:[1,0,0]\n"
"v_mfma_f32_16x16x32_bf16 a[112:115], a[244:247], v[40:43], a[112:115]\n"
"ds_read_b64_tr_b16 a[186:187], v219 offset:1288\n"
"v_fma_mix_f32 v177, v64, v145, v153 op_sel:[1,0,0] op_sel_hi:[1,0,0]\n"
"v_mfma_f32_16x16x32_bf16 a[116:119], a[240:243], v[36:39], a[116:119]\n"
"ds_read_b64_tr_b16 a[188:189], v219 offset:4360\n"
"ds_read_b128 v[172:175], v224 offset:2496\n"
"v_mfma_f32_16x16x32_bf16 a[116:119], a[244:247], v[44:47], a[116:119]\n"
"ds_read_b64_tr_b16 a[190:191], v219 offset:5384\n"
"v_fma_mix_f32 v178, v65, v146, v154 op_sel:[0,0,0] op_sel_hi:[1,0,0]\n"
"v_mfma_f32_16x16x32_bf16 a[120:123], a[248:251], v[32:35], a[120:123]\n"
"ds_read_b64_tr_b16 a[224:225], v219 offset:512\n"
"v_fma_mix_f32 v179, v65, v147, v155 op_sel:[1,0,0] op_sel_hi:[1,0,0]\n"
"v_mfma_f32_16x16x32_bf16 a[120:123], a[252:255], v[40:43], a[120:123]\n"
"ds_read_b64_tr_b16 a[226:227], v219 offset:1536\n"
"v_fma_mix_f32 v180, v66, v148, v156 op_sel:[0,0,0] op_sel_hi:[1,0,0]\n"
"v_mfma_f32_16x16x32_bf16 a[124:127], a[248:251], v[36:39], a[124:127]\n"
"ds_read_b64_tr_b16 a[228:229], v219 offset:4608\n"
"v_fma_mix_f32 v181, v66, v149, v157 op_sel:[1,0,0] op_sel_hi:[1,0,0]\n"
"v_mfma_f32_16x16x32_bf16 a[124:127], a[252:255], v[44:47], a[124:127]\n"
"s_waitcnt lgkmcnt(14)\n"
"ds_read_b64_tr_b16 a[230:231], v219 offset:5632\n"
"v_fma_mix_f32 v182, v67, v150, v158 op_sel:[0,0,0] op_sel_hi:[1,0,0]\n"
"v_mfma_f32_16x16x32_bf16 a[0:3], a[160:163], v[48:51], a[0:3]\n"
"ds_read_b64_tr_b16 a[232:233], v219 offset:520\n"
"v_fma_mix_f32 v183, v67, v151, v159 op_sel:[1,0,0] op_sel_hi:[1,0,0]\n"
"v_mfma_f32_16x16x32_bf16 a[0:3], a[164:167], v[56:59], a[0:3]\n"
"ds_read_b64_tr_b16 a[234:235], v219 offset:1544\n"
"v_cvt_pk_bf16_f32 v64, v176, v177\n"
"v_mfma_f32_16x16x32_bf16 a[4:7], a[160:163], v[52:55], a[4:7]\n"
"s_waitcnt vmcnt(8)\n"
"s_barrier\n"
"ds_read_b64_tr_b16 a[236:237], v219 offset:4616\n"
"v_cvt_pk_bf16_f32 v65, v178, v179\n"
"v_mfma_f32_16x16x32_bf16 a[4:7], a[164:167], v[60:63], a[4:7]\n"
"ds_read_b64_tr_b16 a[238:239], v219 offset:5640\n"
"v_cvt_pk_bf16_f32 v66, v180, v181\n"
"v_mfma_f32_16x16x32_bf16 a[8:11], a[168:171], v[48:51], a[8:11]\n"
"s_add_u32 m0, s51, 32784\n"
"ds_read_b64_tr_b16 a[240:241], v219 offset:768\n"
"v_cvt_pk_bf16_f32 v67, v182, v183\n"
"v_mfma_f32_16x16x32_bf16 a[8:11], a[172:175], v[56:59], a[8:11]\n"
"ds_read_b64_tr_b16 a[242:243], v219 offset:1792\n"
"buffer_load_dwordx4 v217, s[20:23], s50 offen offset:0 lds\n"
"v_mfma_f32_16x16x32_bf16 a[12:15], a[168:171], v[52:55], a[12:15]\n"
"ds_read_b64_tr_b16 a[244:245], v219 offset:4864\n"
"v_fma_mix_f32 v184, v68, v144, v152 op_sel:[0,0,0] op_sel_hi:[1,0,0]\n"
"v_mfma_f32_16x16x32_bf16 a[12:15], a[172:175], v[60:63], a[12:15]\n"
"s_waitcnt lgkmcnt(11)\n"
"ds_read_b64_tr_b16 a[246:247], v219 offset:5888\n"
"v_fma_mix_f32 v185, v68, v145, v153 op_sel:[1,0,0] op_sel_hi:[1,0,0]\n"
"v_mfma_f32_16x16x32_bf16 a[16:19], a[176:179], v[48:51], a[16:19]\n"
"ds_read_b64_tr_b16 a[248:249], v219 offset:776\n"
"v_fma_mix_f32 v186, v69, v146, v154 op_sel:[0,0,0] op_sel_hi:[1,0,0]\n"
"v_mfma_f32_16x16x32_bf16 a[16:19], a[180:183], v[56:59], a[16:19]\n"
"ds_read_b64_tr_b16 a[250:251], v219 offset:1800\n"
"v_fma_mix_f32 v187, v69, v147, v155 op_sel:[1,0,0] op_sel_hi:[1,0,0]\n"
"v_mfma_f32_16x16x32_bf16 a[20:23], a[176:179], v[52:55], a[20:23]\n"
"s_add_u32 m0, s51, 40720\n"
"ds_read_b64_tr_b16 a[252:253], v219 offset:4872\n"
"v_fma_mix_f32 v188, v70, v148, v156 op_sel:[0,0,0] op_sel_hi:[1,0,0]\n"
"v_mfma_f32_16x16x32_bf16 a[20:23], a[180:183], v[60:63], a[20:23]\n"
"ds_read_b64_tr_b16 a[254:255], v219 offset:5896\n"
"buffer_load_dwordx4 v217, s[20:23], s50 offen offset:256 lds\n"
"v_mfma_f32_16x16x32_bf16 a[24:27], a[184:187], v[48:51], a[24:27]\n"
"s_add_u32 s50, s50, 32768\n"
"ds_read_b64_tr_b16 a[160:161], v219 offset:8192\n"
"v_fma_mix_f32 v189, v70, v149, v157 op_sel:[1,0,0] op_sel_hi:[1,0,0]\n"
"v_mfma_f32_16x16x32_bf16 a[24:27], a[188:191], v[56:59], a[24:27]\n"
"ds_read_b64_tr_b16 a[162:163], v219 offset:9216\n"
"v_fma_mix_f32 v190, v71, v150, v158 op_sel:[0,0,0] op_sel_hi:[1,0,0]\n"
"v_mfma_f32_16x16x32_bf16 a[28:31], a[184:187], v[52:55], a[28:31]\n"
"ds_read_b64_tr_b16 a[164:165], v219 offset:12288\n"
"v_fma_mix_f32 v191, v71, v151, v159 op_sel:[1,0,0] op_sel_hi:[1,0,0]\n"
"v_mfma_f32_16x16x32_bf16 a[28:31], a[188:191], v[60:63], a[28:31]\n"
"s_waitcnt lgkmcnt(11)\n"
"ds_read_b64_tr_b16 a[166:167], v219 offset:13312\n"
"ds_read_b128 v[144:147], v224 offset:2560\n"
"v_mfma_f32_16x16x32_bf16 a[32:35], a[224:227], v[48:51], a[32:35]\n"
"s_add_u32 m0, s51, 33808\n"
"ds_read_b64_tr_b16 a[168:169], v219 offset:8200\n"
"v_cvt_pk_bf16_f32 v68, v184, v185\n"
"v_mfma_f32_16x16x32_bf16 a[32:35], a[228:231], v[56:59], a[32:35]\n"
"ds_read_b64_tr_b16 a[170:171], v219 offset:9224\n"
"buffer_load_dwordx4 v217, s[20:23], s50 offen offset:0 lds\n"
"v_mfma_f32_16x16x32_bf16 a[36:39], a[224:227], v[52:55], a[36:39]\n"
"ds_read_b64_tr_b16 a[172:173], v219 offset:12296\n"
"ds_read_b128 v[148:151], v224 offset:2624\n"
"v_mfma_f32_16x16x32_bf16 a[36:39], a[228:231], v[60:63], a[36:39]\n"
"ds_read_b64_tr_b16 a[174:175], v219 offset:13320\n"
"v_cvt_pk_bf16_f32 v69, v186, v187\n"
"v_mfma_f32_16x16x32_bf16 a[40:43], a[232:235], v[48:51], a[40:43]\n"
"ds_read_b64_tr_b16 a[176:177], v219 offset:8448\n"
"v_cvt_pk_bf16_f32 v70, v188, v189\n"
"v_mfma_f32_16x16x32_bf16 a[40:43], a[236:239], v[56:59], a[40:43]\n"
"ds_read_b64_tr_b16 a[178:179], v219 offset:9472\n"
"ds_read_b128 v[152:155], v224 offset:2688\n"
"v_mfma_f32_16x16x32_bf16 a[44:47], a[232:235], v[52:55], a[44:47]\n"
"s_add_u32 m0, s51, 41744\n"
"ds_read_b64_tr_b16 a[180:181], v219 offset:12544\n"
"v_cvt_pk_bf16_f32 v71, v190, v191\n"
"v_mfma_f32_16x16x32_bf16 a[44:47], a[236:239], v[60:63], a[44:47]\n"
"s_waitcnt lgkmcnt(14)\n"
"ds_read_b64_tr_b16 a[182:183], v219 offset:13568\n"
"buffer_load_dwordx4 v217, s[20:23], s50 offen offset:256 lds\n"
"v_mfma_f32_16x16x32_bf16 a[48:51], a[240:243], v[48:51], a[48:51]\n"
"s_add_u32 s50, s50, 32768\n"
"ds_read_b64_tr_b16 a[184:185], v219 offset:8456\n"
"ds_read_b128 v[156:159], v224 offset:2752\n"
"v_mfma_f32_16x16x32_bf16 a[48:51], a[244:247], v[56:59], a[48:51]\n"
"ds_read_b64_tr_b16 a[186:187], v219 offset:9480\n"
"v_fma_mix_f32 v176, v72, v160, v168 op_sel:[0,0,0] op_sel_hi:[1,0,0]\n"
"v_mfma_f32_16x16x32_bf16 a[52:55], a[240:243], v[52:55], a[52:55]\n"
"ds_read_b64_tr_b16 a[188:189], v219 offset:12552\n"
"v_fma_mix_f32 v177, v72, v161, v169 op_sel:[1,0,0] op_sel_hi:[1,0,0]\n"
"v_mfma_f32_16x16x32_bf16 a[52:55], a[244:247], v[60:63], a[52:55]\n"
"ds_read_b64_tr_b16 a[190:191], v219 offset:13576\n"
"v_fma_mix_f32 v178, v73, v162, v170 op_sel:[0,0,0] op_sel_hi:[1,0,0]\n"
"v_mfma_f32_16x16x32_bf16 a[56:59], a[248:251], v[48:51], a[56:59]\n"
"s_add_u32 m0, s51, 49176\n"
"ds_read_b64_tr_b16 a[224:225], v219 offset:8704\n"
"v_fma_mix_f32 v179, v73, v163, v171 op_sel:[1,0,0] op_sel_hi:[1,0,0]\n"
"v_mfma_f32_16x16x32_bf16 a[56:59], a[252:255], v[56:59], a[56:59]\n"
"ds_read_b64_tr_b16 a[226:227], v219 offset:9728\n"
"buffer_load_dwordx4 v217, s[20:23], s50 offen offset:0 lds\n"
"v_mfma_f32_16x16x32_bf16 a[60:63], a[248:251], v[52:55], a[60:63]\n"
"ds_read_b64_tr_b16 a[228:229], v219 offset:12800\n"
"v_fma_mix_f32 v180, v74, v164, v172 op_sel:[0,0,0] op_sel_hi:[1,0,0]\n"
"v_mfma_f32_16x16x32_bf16 a[60:63], a[252:255], v[60:63], a[60:63]\n"
"s_waitcnt lgkmcnt(13)\n"
"ds_read_b64_tr_b16 a[230:231], v219 offset:13824\n"
"v_fma_mix_f32 v181, v74, v165, v173 op_sel:[1,0,0] op_sel_hi:[1,0,0]\n"
"v_mfma_f32_16x16x32_bf16 a[64:67], a[160:163], v[48:51], a[64:67]\n"
"ds_read_b64_tr_b16 a[232:233], v219 offset:8712\n"
"v_fma_mix_f32 v182, v75, v166, v174 op_sel:[0,0,0] op_sel_hi:[1,0,0]\n"
"v_mfma_f32_16x16x32_bf16 a[64:67], a[164:167], v[56:59], a[64:67]\n"
"ds_read_b64_tr_b16 a[234:235], v219 offset:9736\n"
"v_fma_mix_f32 v183, v75, v167, v175 op_sel:[1,0,0] op_sel_hi:[1,0,0]\n"
"v_mfma_f32_16x16x32_bf16 a[68:71], a[160:163], v[52:55], a[68:71]\n"
"s_add_u32 m0, s51, 57112\n"
"ds_read_b64_tr_b16 a[236:237], v219 offset:12808\n"
"v_cvt_pk_bf16_f32 v72, v176, v177\n"
"v_mfma_f32_16x16x32_bf16 a[68:71], a[164:167], v[60:63], a[68:71]\n"
"ds_read_b64_tr_b16 a[238:239], v219 offset:13832\n"
"buffer_load_dwordx4 v217, s[20:23], s50 offen offset:256 lds\n"
"v_mfma_f32_16x16x32_bf16 a[72:75], a[168:171], v[48:51], a[72:75]\n"
"s_add_u32 s50, s50, 32768\n"
"ds_read_b64_tr_b16 a[240:241], v219 offset:8960\n"
"v_cvt_pk_bf16_f32 v73, v178, v179\n"
"v_mfma_f32_16x16x32_bf16 a[72:75], a[172:175], v[56:59], a[72:75]\n"
"ds_read_b64_tr_b16 a[242:243], v219 offset:9984\n"
"v_cvt_pk_bf16_f32 v74, v180, v181\n"
"v_mfma_f32_16x16x32_bf16 a[76:79], a[168:171], v[52:55], a[76:79]\n"
"ds_read_b64_tr_b16 a[244:245], v219 offset:13056\n"
"v_cvt_pk_bf16_f32 v75, v182, v183\n"
"v_mfma_f32_16x16x32_bf16 a[76:79], a[172:175], v[60:63], a[76:79]\n"
"s_waitcnt lgkmcnt(11)\n"
"ds_read_b64_tr_b16 a[246:247], v219 offset:14080\n"
"v_fma_mix_f32 v184, v76, v160, v168 op_sel:[0,0,0] op_sel_hi:[1,0,0]\n"
"v_mfma_f32_16x16x32_bf16 a[80:83], a[176:179], v[48:51], a[80:83]\n"
"s_add_u32 m0, s51, 50200\n"
"ds_read_b64_tr_b16 a[248:249], v219 offset:8968\n"
"v_fma_mix_f32 v185, v76, v161, v169 op_sel:[1,0,0] op_sel_hi:[1,0,0]\n"
"v_mfma_f32_16x16x32_bf16 a[80:83], a[180:183], v[56:59], a[80:83]\n"
"ds_read_b64_tr_b16 a[250:251], v219 offset:9992\n"
"buffer_load_dwordx4 v217, s[20:23], s50 offen offset:0 lds\n"
"v_mfma_f32_16x16x32_bf16 a[84:87], a[176:179], v[52:55], a[84:87]\n"
"ds_read_b64_tr_b16 a[252:253], v219 offset:13064\n"
"v_fma_mix_f32 v186, v77, v162, v170 op_sel:[0,0,0] op_sel_hi:[1,0,0]\n"
"v_mfma_f32_16x16x32_bf16 a[84:87], a[180:183], v[60:63], a[84:87]\n"
"ds_read_b64_tr_b16 a[254:255], v219 offset:14088\n"
"v_fma_mix_f32 v187, v77, v163, v171 op_sel:[1,0,0] op_sel_hi:[1,0,0]\n"
"v_mfma_f32_16x16x32_bf16 a[88:91], a[184:187], v[48:51], a[88:91]\n"
"ds_read_b64_tr_b16 a[160:161], v219 offset:32784\n"
"v_fma_mix_f32 v188, v78, v164, v172 op_sel:[0,0,0] op_sel_hi:[1,0,0]\n"
"v_fma_mix_f32 v189, v78, v165, v173 op_sel:[1,0,0] op_sel_hi:[1,0,0]\n"
"v_mfma_f32_16x16x32_bf16 a[88:91], a[188:191], v[56:59], a[88:91]\n"
"ds_read_b64_tr_b16 a[162:163], v219 offset:33808\n"
"v_fma_mix_f32 v190, v79, v166, v174 op_sel:[0,0,0] op_sel_hi:[1,0,0]\n"
"v_mfma_f32_16x16x32_bf16 a[92:95], a[184:187], v[52:55], a[92:95]\n"
"s_add_u32 m0, s51, 58136\n"
"ds_read_b64_tr_b16 a[164:165], v219 offset:36880\n"
"v_fma_mix_f32 v191, v79, v167, v175 op_sel:[1,0,0] op_sel_hi:[1,0,0]\n"
"v_mfma_f32_16x16x32_bf16 a[92:95], a[188:191], v[60:63], a[92:95]\n"
"s_waitcnt lgkmcnt(11)\n"
"buffer_load_dwordx4 v217, s[20:23], s50 offen offset:256 lds\n"
"ds_read_b64_tr_b16 a[166:167], v219 offset:37904\n"
"v_mfma_f32_16x16x32_bf16 a[96:99], a[224:227], v[48:51], a[96:99]\n"
"s_add_u32 s50, s50, 425984\n"
"ds_read_b64_tr_b16 a[168:169], v219 offset:32792\n"
"ds_read_b128 v[160:163], v224 offset:2816\n"
"v_mfma_f32_16x16x32_bf16 a[96:99], a[228:231], v[56:59], a[96:99]\n"
"ds_read_b64_tr_b16 a[170:171], v219 offset:33816\n"
"v_cvt_pk_bf16_f32 v76, v184, v185\n"
"v_mfma_f32_16x16x32_bf16 a[100:103], a[224:227], v[52:55], a[100:103]\n"
"ds_read_b64_tr_b16 a[172:173], v219 offset:36888\n"
"v_cvt_pk_bf16_f32 v77, v186, v187\n"
"v_mfma_f32_16x16x32_bf16 a[100:103], a[228:231], v[60:63], a[100:103]\n"
"ds_read_b64_tr_b16 a[174:175], v219 offset:37912\n"
"ds_read_b128 v[164:167], v224 offset:2880\n"
"v_mfma_f32_16x16x32_bf16 a[104:107], a[232:235], v[48:51], a[104:107]\n"
"ds_read_b64_tr_b16 a[176:177], v219 offset:33040\n"
"v_cvt_pk_bf16_f32 v78, v188, v189\n"
"v_mfma_f32_16x16x32_bf16 a[104:107], a[236:239], v[56:59], a[104:107]\n"
"ds_read_b64_tr_b16 a[178:179], v219 offset:34064\n"
"v_cvt_pk_bf16_f32 v79, v190, v191\n"
"v_mfma_f32_16x16x32_bf16 a[108:111], a[232:235], v[52:55], a[108:111]\n"
"ds_read_b64_tr_b16 a[180:181], v219 offset:37136\n"
"ds_read_b128 v[168:171], v224 offset:2944\n"
"v_mfma_f32_16x16x32_bf16 a[108:111], a[236:239], v[60:63], a[108:111]\n"
"s_waitcnt lgkmcnt(14)\n"
"ds_read_b64_tr_b16 a[182:183], v219 offset:38160\n"
"v_fma_mix_f32 v176, v80, v144, v152 op_sel:[0,0,0] op_sel_hi:[1,0,0]\n"
"v_mfma_f32_16x16x32_bf16 a[112:115], a[240:243], v[48:51], a[112:115]\n"
"ds_read_b64_tr_b16 a[184:185], v219 offset:33048\n"
"v_fma_mix_f32 v177, v80, v145, v153 op_sel:[1,0,0] op_sel_hi:[1,0,0]\n"
"v_mfma_f32_16x16x32_bf16 a[112:115], a[244:247], v[56:59], a[112:115]\n"
"ds_read_b64_tr_b16 a[186:187], v219 offset:34072\n"
"ds_read_b128 v[172:175], v224 offset:3008\n"
"v_mfma_f32_16x16x32_bf16 a[116:119], a[240:243], v[52:55], a[116:119]\n"
"ds_read_b64_tr_b16 a[188:189], v219 offset:37144\n"
"v_fma_mix_f32 v178, v81, v146, v154 op_sel:[0,0,0] op_sel_hi:[1,0,0]\n"
"v_mfma_f32_16x16x32_bf16 a[116:119], a[244:247], v[60:63], a[116:119]\n"
"ds_read_b64_tr_b16 a[190:191], v219 offset:38168\n"
"v_fma_mix_f32 v179, v81, v147, v155 op_sel:[1,0,0] op_sel_hi:[1,0,0]\n"
"v_mfma_f32_16x16x32_bf16 a[120:123], a[248:251], v[48:51], a[120:123]\n"
"ds_read_b64_tr_b16 a[224:225], v219 offset:33296\n"
"v_fma_mix_f32 v180, v82, v148, v156 op_sel:[0,0,0] op_sel_hi:[1,0,0]\n"
"v_mfma_f32_16x16x32_bf16 a[120:123], a[252:255], v[56:59], a[120:123]\n"
"ds_read_b64_tr_b16 a[226:227], v219 offset:34320\n"
"v_fma_mix_f32 v181, v82, v149, v157 op_sel:[1,0,0] op_sel_hi:[1,0,0]\n"
"v_mfma_f32_16x16x32_bf16 a[124:127], a[248:251], v[52:55], a[124:127]\n"
"ds_read_b64_tr_b16 a[228:229], v219 offset:37392\n"
"v_fma_mix_f32 v182, v83, v150, v158 op_sel:[0,0,0] op_sel_hi:[1,0,0]\n"
"v_mfma_f32_16x16x32_bf16 a[124:127], a[252:255], v[60:63], a[124:127]\n"
"s_waitcnt lgkmcnt(13)\n"
"ds_read_b64_tr_b16 a[230:231], v219 offset:38416\n"
"v_fma_mix_f32 v183, v83, v151, v159 op_sel:[1,0,0] op_sel_hi:[1,0,0]\n"
"v_mfma_f32_16x16x32_bf16 a[0:3], a[160:163], v[64:67], a[0:3]\n"
"ds_read_b64_tr_b16 a[232:233], v219 offset:33304\n"
"v_cvt_pk_bf16_f32 v80, v176, v177\n"
"v_mfma_f32_16x16x32_bf16 a[0:3], a[164:167], v[72:75], a[0:3]\n"
"ds_read_b64_tr_b16 a[234:235], v219 offset:34328\n"
"v_cvt_pk_bf16_f32 v81, v178, v179\n"
"v_mfma_f32_16x16x32_bf16 a[4:7], a[160:163], v[68:71], a[4:7]\n"
"s_waitcnt vmcnt(8)\n"
"s_barrier\n"
"ds_read_b64_tr_b16 a[236:237], v219 offset:37400\n"
"v_cvt_pk_bf16_f32 v82, v180, v181\n"
"v_mfma_f32_16x16x32_bf16 a[4:7], a[164:167], v[76:79], a[4:7]\n"
"ds_read_b64_tr_b16 a[238:239], v219 offset:38424\n"
"v_cvt_pk_bf16_f32 v83, v182, v183\n"
"v_mfma_f32_16x16x32_bf16 a[8:11], a[168:171], v[64:67], a[8:11]\n"
"s_add_u32 m0, s51, 65568\n"
"ds_read_b64_tr_b16 a[240:241], v219 offset:33552\n"
"v_fma_mix_f32 v184, v84, v144, v152 op_sel:[0,0,0] op_sel_hi:[1,0,0]\n"
"v_mfma_f32_16x16x32_bf16 a[8:11], a[172:175], v[72:75], a[8:11]\n"
"ds_read_b64_tr_b16 a[242:243], v219 offset:34576\n"
"buffer_load_dwordx4 v217, s[20:23], s50 offen offset:0 lds\n"
"v_mfma_f32_16x16x32_bf16 a[12:15], a[168:171], v[68:71], a[12:15]\n"
"ds_read_b64_tr_b16 a[244:245], v219 offset:37648\n"
"v_fma_mix_f32 v185, v84, v145, v153 op_sel:[1,0,0] op_sel_hi:[1,0,0]\n"
"v_mfma_f32_16x16x32_bf16 a[12:15], a[172:175], v[76:79], a[12:15]\n"
"s_waitcnt lgkmcnt(11)\n"
"ds_read_b64_tr_b16 a[246:247], v219 offset:38672\n"
"v_fma_mix_f32 v186, v85, v146, v154 op_sel:[0,0,0] op_sel_hi:[1,0,0]\n"
"v_mfma_f32_16x16x32_bf16 a[16:19], a[176:179], v[64:67], a[16:19]\n"
"ds_read_b64_tr_b16 a[248:249], v219 offset:33560\n"
"v_fma_mix_f32 v187, v85, v147, v155 op_sel:[1,0,0] op_sel_hi:[1,0,0]\n"
"v_mfma_f32_16x16x32_bf16 a[16:19], a[180:183], v[72:75], a[16:19]\n"
"ds_read_b64_tr_b16 a[250:251], v219 offset:34584\n"
"v_fma_mix_f32 v188, v86, v148, v156 op_sel:[0,0,0] op_sel_hi:[1,0,0]\n"
"v_mfma_f32_16x16x32_bf16 a[20:23], a[176:179], v[68:71], a[20:23]\n"
"s_add_u32 m0, s51, 73504\n"
"ds_read_b64_tr_b16 a[252:253], v219 offset:37656\n"
"v_fma_mix_f32 v189, v86, v149, v157 op_sel:[1,0,0] op_sel_hi:[1,0,0]\n"
"v_mfma_f32_16x16x32_bf16 a[20:23], a[180:183], v[76:79], a[20:23]\n"
"ds_read_b64_tr_b16 a[254:255], v219 offset:38680\n"
"buffer_load_dwordx4 v217, s[20:23], s50 offen offset:256 lds\n"
"v_mfma_f32_16x16x32_bf16 a[24:27], a[184:187], v[64:67], a[24:27]\n"
"s_add_u32 s50, s50, 32768\n"
"ds_read_b64_tr_b16 a[160:161], v219 offset:40976\n"
"v_fma_mix_f32 v190, v87, v150, v158 op_sel:[0,0,0] op_sel_hi:[1,0,0]\n"
"v_mfma_f32_16x16x32_bf16 a[24:27], a[188:191], v[72:75], a[24:27]\n"
"ds_read_b64_tr_b16 a[162:163], v219 offset:42000\n"
"v_fma_mix_f32 v191, v87, v151, v159 op_sel:[1,0,0] op_sel_hi:[1,0,0]\n"
"v_mfma_f32_16x16x32_bf16 a[28:31], a[184:187], v[68:71], a[28:31]\n"
"ds_read_b64_tr_b16 a[164:165], v219 offset:45072\n"
"ds_read_b128 v[144:147], v224 offset:3072\n"
"v_mfma_f32_16x16x32_bf16 a[28:31], a[188:191], v[76:79], a[28:31]\n"
"s_waitcnt lgkmcnt(12)\n"
"ds_read_b64_tr_b16 a[166:167], v219 offset:46096\n"
"v_cvt_pk_bf16_f32 v84, v184, v185\n"
"v_mfma_f32_16x16x32_bf16 a[32:35], a[224:227], v[64:67], a[32:35]\n"
"s_add_u32 m0, s51, 66592\n"
"ds_read_b64_tr_b16 a[168:169], v219 offset:40984\n"
"v_cvt_pk_bf16_f32 v85, v186, v187\n"
"v_mfma_f32_16x16x32_bf16 a[32:35], a[228:231], v[72:75], a[32:35]\n"
"ds_read_b64_tr_b16 a[170:171], v219 offset:42008\n"
"buffer_load_dwordx4 v217, s[20:23], s50 offen offset:0 lds\n"
"v_mfma_f32_16x16x32_bf16 a[36:39], a[224:227], v[68:71], a[36:39]\n"
"ds_read_b64_tr_b16 a[172:173], v219 offset:45080\n"
"ds_read_b128 v[148:151], v224 offset:3136\n"
"v_mfma_f32_16x16x32_bf16 a[36:39], a[228:231], v[76:79], a[36:39]\n"
"ds_read_b64_tr_b16 a[174:175], v219 offset:46104\n"
"v_cvt_pk_bf16_f32 v86, v188, v189\n"
"v_mfma_f32_16x16x32_bf16 a[40:43], a[232:235], v[64:67], a[40:43]\n"
"ds_read_b64_tr_b16 a[176:177], v219 offset:41232\n"
"v_cvt_pk_bf16_f32 v87, v190, v191\n"
"v_mfma_f32_16x16x32_bf16 a[40:43], a[236:239], v[72:75], a[40:43]\n"
"ds_read_b64_tr_b16 a[178:179], v219 offset:42256\n"
"ds_read_b128 v[152:155], v224 offset:3200\n"
"v_mfma_f32_16x16x32_bf16 a[44:47], a[232:235], v[68:71], a[44:47]\n"
"s_add_u32 m0, s51, 74528\n"
"ds_read_b64_tr_b16 a[180:181], v219 offset:45328\n"
"v_fma_mix_f32 v176, v88, v160, v168 op_sel:[0,0,0] op_sel_hi:[1,0,0]\n"
"v_mfma_f32_16x16x32_bf16 a[44:47], a[236:239], v[76:79], a[44:47]\n"
"s_waitcnt lgkmcnt(14)\n"
"ds_read_b64_tr_b16 a[182:183], v219 offset:46352\n"
"buffer_load_dwordx4 v217, s[20:23], s50 offen offset:256 lds\n"
"v_mfma_f32_16x16x32_bf16 a[48:51], a[240:243], v[64:67], a[48:51]\n"
"s_add_u32 s50, s50, 32768\n"
"ds_read_b64_tr_b16 a[184:185], v219 offset:41240\n"
"ds_read_b128 v[156:159], v224 offset:3264\n"
"v_mfma_f32_16x16x32_bf16 a[48:51], a[244:247], v[72:75], a[48:51]\n"
"ds_read_b64_tr_b16 a[186:187], v219 offset:42264\n"
"v_fma_mix_f32 v177, v88, v161, v169 op_sel:[1,0,0] op_sel_hi:[1,0,0]\n"
"v_mfma_f32_16x16x32_bf16 a[52:55], a[240:243], v[68:71], a[52:55]\n"
"ds_read_b64_tr_b16 a[188:189], v219 offset:45336\n"
"v_fma_mix_f32 v178, v89, v162, v170 op_sel:[0,0,0] op_sel_hi:[1,0,0]\n"
"v_mfma_f32_16x16x32_bf16 a[52:55], a[244:247], v[76:79], a[52:55]\n"
"ds_read_b64_tr_b16 a[190:191], v219 offset:46360\n"
"v_fma_mix_f32 v179, v89, v163, v171 op_sel:[1,0,0] op_sel_hi:[1,0,0]\n"
"v_mfma_f32_16x16x32_bf16 a[56:59], a[248:251], v[64:67], a[56:59]\n"
"s_add_u32 m0, s51, 81960\n"
"ds_read_b64_tr_b16 a[224:225], v219 offset:41488\n"
"v_fma_mix_f32 v180, v90, v164, v172 op_sel:[0,0,0] op_sel_hi:[1,0,0]\n"
"v_mfma_f32_16x16x32_bf16 a[56:59], a[252:255], v[72:75], a[56:59]\n"
"ds_read_b64_tr_b16 a[226:227], v219 offset:42512\n"
"buffer_load_dwordx4 v217, s[20:23], s50 offen offset:0 lds\n"
"v_mfma_f32_16x16x32_bf16 a[60:63], a[248:251], v[68:71], a[60:63]\n"
"ds_read_b64_tr_b16 a[228:229], v219 offset:45584\n"
"v_fma_mix_f32 v181, v90, v165, v173 op_sel:[1,0,0] op_sel_hi:[1,0,0]\n"
"v_mfma_f32_16x16x32_bf16 a[60:63], a[252:255], v[76:79], a[60:63]\n"
"s_waitcnt lgkmcnt(13)\n"
"ds_read_b64_tr_b16 a[230:231], v219 offset:46608\n"
"v_fma_mix_f32 v182, v91, v166, v174 op_sel:[0,0,0] op_sel_hi:[1,0,0]\n"
"v_mfma_f32_16x16x32_bf16 a[64:67], a[160:163], v[64:67], a[64:67]\n"
"ds_read_b64_tr_b16 a[232:233], v219 offset:41496\n"
"v_fma_mix_f32 v183, v91, v167, v175 op_sel:[1,0,0] op_sel_hi:[1,0,0]\n"
"v_mfma_f32_16x16x32_bf16 a[64:67], a[164:167], v[72:75], a[64:67]\n"
"ds_read_b64_tr_b16 a[234:235], v219 offset:42520\n"
"v_cvt_pk_bf16_f32 v88, v176, v177\n"
"v_mfma_f32_16x16x32_bf16 a[68:71], a[160:163], v[68:71], a[68:71]\n"
"s_add_u32 m0, s51, 89896\n"
"ds_read_b64_tr_b16 a[236:237], v219 offset:45592\n"
"v_cvt_pk_bf16_f32 v89, v178, v179\n"
"v_mfma_f32_16x16x32_bf16 a[68:71], a[164:167], v[76:79], a[68:71]\n"
"ds_read_b64_tr_b16 a[238:239], v219 offset:46616\n"
"buffer_load_dwordx4 v217, s[20:23], s50 offen offset:256 lds\n"
"v_mfma_f32_16x16x32_bf16 a[72:75], a[168:171], v[64:67], a[72:75]\n"
"s_add_u32 s50, s50, 32768\n"
"ds_read_b64_tr_b16 a[240:241], v219 offset:41744\n"
"v_cvt_pk_bf16_f32 v90, v180, v181\n"
"v_mfma_f32_16x16x32_bf16 a[72:75], a[172:175], v[72:75], a[72:75]\n"
"ds_read_b64_tr_b16 a[242:243], v219 offset:42768\n"
"v_cvt_pk_bf16_f32 v91, v182, v183\n"
"v_mfma_f32_16x16x32_bf16 a[76:79], a[168:171], v[68:71], a[76:79]\n"
"ds_read_b64_tr_b16 a[244:245], v219 offset:45840\n"
"v_fma_mix_f32 v184, v92, v160, v168 op_sel:[0,0,0] op_sel_hi:[1,0,0]\n"
"v_mfma_f32_16x16x32_bf16 a[76:79], a[172:175], v[76:79], a[76:79]\n"
"s_waitcnt lgkmcnt(11)\n"
"ds_read_b64_tr_b16 a[246:247], v219 offset:46864\n"
"v_fma_mix_f32 v185, v92, v161, v169 op_sel:[1,0,0] op_sel_hi:[1,0,0]\n"
"v_mfma_f32_16x16x32_bf16 a[80:83], a[176:179], v[64:67], a[80:83]\n"
"s_add_u32 m0, s51, 82984\n"
"ds_read_b64_tr_b16 a[248:249], v219 offset:41752\n"
"v_fma_mix_f32 v186, v93, v162, v170 op_sel:[0,0,0] op_sel_hi:[1,0,0]\n"
"v_mfma_f32_16x16x32_bf16 a[80:83], a[180:183], v[72:75], a[80:83]\n"
"ds_read_b64_tr_b16 a[250:251], v219 offset:42776\n"
"buffer_load_dwordx4 v217, s[20:23], s50 offen offset:0 lds\n"
"v_mfma_f32_16x16x32_bf16 a[84:87], a[176:179], v[68:71], a[84:87]\n"
"ds_read_b64_tr_b16 a[252:253], v219 offset:45848\n"
"v_fma_mix_f32 v187, v93, v163, v171 op_sel:[1,0,0] op_sel_hi:[1,0,0]\n"
"v_mfma_f32_16x16x32_bf16 a[84:87], a[180:183], v[76:79], a[84:87]\n"
"ds_read_b64_tr_b16 a[254:255], v219 offset:46872\n"
"v_fma_mix_f32 v188, v94, v164, v172 op_sel:[0,0,0] op_sel_hi:[1,0,0]\n"
"v_mfma_f32_16x16x32_bf16 a[88:91], a[184:187], v[64:67], a[88:91]\n"
"ds_read_b64_tr_b16 a[160:161], v218 offset:0\n"
"v_fma_mix_f32 v189, v94, v165, v173 op_sel:[1,0,0] op_sel_hi:[1,0,0]\n"
"v_fma_mix_f32 v190, v95, v166, v174 op_sel:[0,0,0] op_sel_hi:[1,0,0]\n"
"v_mfma_f32_16x16x32_bf16 a[88:91], a[188:191], v[72:75], a[88:91]\n"
"ds_read_b64_tr_b16 a[162:163], v218 offset:1024\n"
"v_fma_mix_f32 v191, v95, v167, v175 op_sel:[1,0,0] op_sel_hi:[1,0,0]\n"
"v_mfma_f32_16x16x32_bf16 a[92:95], a[184:187], v[68:71], a[92:95]\n"
"s_add_u32 m0, s51, 90920\n"
"ds_read_b64_tr_b16 a[164:165], v218 offset:4096\n"
"ds_read_b128 v[160:163], v224 offset:3328\n"
"v_mfma_f32_16x16x32_bf16 a[92:95], a[188:191], v[76:79], a[92:95]\n"
"s_waitcnt lgkmcnt(12)\n"
"buffer_load_dwordx4 v217, s[20:23], s50 offen offset:256 lds\n"
"ds_read_b64_tr_b16 a[166:167], v218 offset:5120\n"
"v_mfma_f32_16x16x32_bf16 a[96:99], a[224:227], v[64:67], a[96:99]\n"
"s_add_u32 s50, s50, 425984\n"
"ds_read_b64_tr_b16 a[168:169], v218 offset:8\n"
"v_cvt_pk_bf16_f32 v92, v184, v185\n"
"v_mfma_f32_16x16x32_bf16 a[96:99], a[228:231], v[72:75], a[96:99]\n"
"ds_read_b64_tr_b16 a[170:171], v218 offset:1032\n"
"ds_read_b128 v[164:167], v224 offset:3392\n"
"v_mfma_f32_16x16x32_bf16 a[100:103], a[224:227], v[68:71], a[100:103]\n"
"ds_read_b64_tr_b16 a[172:173], v218 offset:4104\n"
"v_cvt_pk_bf16_f32 v93, v186, v187\n"
"v_mfma_f32_16x16x32_bf16 a[100:103], a[228:231], v[76:79], a[100:103]\n"
"ds_read_b64_tr_b16 a[174:175], v218 offset:5128\n"
"v_cvt_pk_bf16_f32 v94, v188, v189\n"
"v_mfma_f32_16x16x32_bf16 a[104:107], a[232:235], v[64:67], a[104:107]\n"
"ds_read_b64_tr_b16 a[176:177], v218 offset:256\n"
"ds_read_b128 v[168:171], v224 offset:3456\n"
"v_mfma_f32_16x16x32_bf16 a[104:107], a[236:239], v[72:75], a[104:107]\n"
"ds_read_b64_tr_b16 a[178:179], v218 offset:1280\n"
"v_cvt_pk_bf16_f32 v95, v190, v191\n"
"v_mfma_f32_16x16x32_bf16 a[108:111], a[232:235], v[68:71], a[108:111]\n"
"ds_read_b64_tr_b16 a[180:181], v218 offset:4352\n"
"v_fma_mix_f32 v176, v96, v144, v152 op_sel:[0,0,0] op_sel_hi:[1,0,0]\n"
"v_mfma_f32_16x16x32_bf16 a[108:111], a[236:239], v[76:79], a[108:111]\n"
"s_waitcnt lgkmcnt(14)\n"
"ds_read_b64_tr_b16 a[182:183], v218 offset:5376\n"
"ds_read_b128 v[172:175], v224 offset:3520\n"
"v_mfma_f32_16x16x32_bf16 a[112:115], a[240:243], v[64:67], a[112:115]\n"
"ds_read_b64_tr_b16 a[184:185], v218 offset:264\n"
"v_fma_mix_f32 v177, v96, v145, v153 op_sel:[1,0,0] op_sel_hi:[1,0,0]\n"
"v_mfma_f32_16x16x32_bf16 a[112:115], a[244:247], v[72:75], a[112:115]\n"
"ds_read_b64_tr_b16 a[186:187], v218 offset:1288\n"
"v_fma_mix_f32 v178, v97, v146, v154 op_sel:[0,0,0] op_sel_hi:[1,0,0]\n"
"v_mfma_f32_16x16x32_bf16 a[116:119], a[240:243], v[68:71], a[116:119]\n"
"ds_read_b64_tr_b16 a[188:189], v218 offset:4360\n"
"v_fma_mix_f32 v179, v97, v147, v155 op_sel:[1,0,0] op_sel_hi:[1,0,0]\n"
"v_mfma_f32_16x16x32_bf16 a[116:119], a[244:247], v[76:79], a[116:119]\n"
"ds_read_b64_tr_b16 a[190:191], v218 offset:5384\n"
"v_fma_mix_f32 v180, v98, v148, v156 op_sel:[0,0,0] op_sel_hi:[1,0,0]\n"
"v_mfma_f32_16x16x32_bf16 a[120:123], a[248:251], v[64:67], a[120:123]\n"
"ds_read_b64_tr_b16 a[224:225], v218 offset:512\n"
"v_fma_mix_f32 v181, v98, v149, v157 op_sel:[1,0,0] op_sel_hi:[1,0,0]\n"
"v_mfma_f32_16x16x32_bf16 a[120:123], a[252:255], v[72:75], a[120:123]\n"
"ds_read_b64_tr_b16 a[226:227], v218 offset:1536\n"
"v_fma_mix_f32 v182, v99, v150, v158 op_sel:[0,0,0] op_sel_hi:[1,0,0]\n"
"v_mfma_f32_16x16x32_bf16 a[124:127], a[248:251], v[68:71], a[124:127]\n"
"ds_read_b64_tr_b16 a[228:229], v218 offset:4608\n"
"v_fma_mix_f32 v183, v99, v151, v159 op_sel:[1,0,0] op_sel_hi:[1,0,0]\n"
"v_mfma_f32_16x16x32_bf16 a[124:127], a[252:255], v[76:79], a[124:127]\n"
"s_waitcnt lgkmcnt(13)\n"
"ds_read_b64_tr_b16 a[230:231], v218 offset:5632\n"
"v_cvt_pk_bf16_f32 v96, v176, v177\n"
"v_mfma_f32_16x16x32_bf16 a[0:3], a[160:163], v[80:83], a[0:3]\n"
"ds_read_b64_tr_b16 a[232:233], v218 offset:520\n"
"v_cvt_pk_bf16_f32 v97, v178, v179\n"
"v_mfma_f32_16x16x32_bf16 a[0:3], a[164:167], v[88:91], a[0:3]\n"
"ds_read_b64_tr_b16 a[234:235], v218 offset:1544\n"
"v_cvt_pk_bf16_f32 v98, v180, v181\n"
"v_mfma_f32_16x16x32_bf16 a[4:7], a[160:163], v[84:87], a[4:7]\n"
"s_waitcnt vmcnt(8)\n"
"s_barrier\n"
"ds_read_b64_tr_b16 a[236:237], v218 offset:4616\n"
"v_cvt_pk_bf16_f32 v99, v182, v183\n"
"v_mfma_f32_16x16x32_bf16 a[4:7], a[164:167], v[92:95], a[4:7]\n"
"ds_read_b64_tr_b16 a[238:239], v218 offset:5640\n"
"v_fma_mix_f32 v184, v100, v144, v152 op_sel:[0,0,0] op_sel_hi:[1,0,0]\n"
"v_mfma_f32_16x16x32_bf16 a[8:11], a[168:171], v[80:83], a[8:11]\n"
"s_add_u32 m0, s51, 98352\n"
"ds_read_b64_tr_b16 a[240:241], v218 offset:768\n"
"v_fma_mix_f32 v185, v100, v145, v153 op_sel:[1,0,0] op_sel_hi:[1,0,0]\n"
"v_mfma_f32_16x16x32_bf16 a[8:11], a[172:175], v[88:91], a[8:11]\n"
"ds_read_b64_tr_b16 a[242:243], v218 offset:1792\n"
"buffer_load_dwordx4 v217, s[20:23], s50 offen offset:0 lds\n"
"v_mfma_f32_16x16x32_bf16 a[12:15], a[168:171], v[84:87], a[12:15]\n"
"ds_read_b64_tr_b16 a[244:245], v218 offset:4864\n"
"v_fma_mix_f32 v186, v101, v146, v154 op_sel:[0,0,0] op_sel_hi:[1,0,0]\n"
"v_mfma_f32_16x16x32_bf16 a[12:15], a[172:175], v[92:95], a[12:15]\n"
"s_waitcnt lgkmcnt(11)\n"
"ds_read_b64_tr_b16 a[246:247], v218 offset:5888\n"
"v_fma_mix_f32 v187, v101, v147, v155 op_sel:[1,0,0] op_sel_hi:[1,0,0]\n"
"v_mfma_f32_16x16x32_bf16 a[16:19], a[176:179], v[80:83], a[16:19]\n"
"ds_read_b64_tr_b16 a[248:249], v218 offset:776\n"
"v_fma_mix_f32 v188, v102, v148, v156 op_sel:[0,0,0] op_sel_hi:[1,0,0]\n"
"v_mfma_f32_16x16x32_bf16 a[16:19], a[180:183], v[88:91], a[16:19]\n"
"ds_read_b64_tr_b16 a[250:251], v218 offset:1800\n"
"v_fma_mix_f32 v189, v102, v149, v157 op_sel:[1,0,0] op_sel_hi:[1,0,0]\n"
"v_mfma_f32_16x16x32_bf16 a[20:23], a[176:179], v[84:87], a[20:23]\n"
"s_add_u32 m0, s51, 106288\n"
"ds_read_b64_tr_b16 a[252:253], v218 offset:4872\n"
"v_fma_mix_f32 v190, v103, v150, v158 op_sel:[0,0,0] op_sel_hi:[1,0,0]\n"
"v_mfma_f32_16x16x32_bf16 a[20:23], a[180:183], v[92:95], a[20:23]\n"
"ds_read_b64_tr_b16 a[254:255], v218 offset:5896\n"
"buffer_load_dwordx4 v217, s[20:23], s50 offen offset:256 lds\n"
"v_mfma_f32_16x16x32_bf16 a[24:27], a[184:187], v[80:83], a[24:27]\n"
"s_add_u32 s50, s50, 32768\n"
"ds_read_b64_tr_b16 a[160:161], v218 offset:8192\n"
"v_fma_mix_f32 v191, v103, v151, v159 op_sel:[1,0,0] op_sel_hi:[1,0,0]\n"
"v_mfma_f32_16x16x32_bf16 a[24:27], a[188:191], v[88:91], a[24:27]\n"
"ds_read_b64_tr_b16 a[162:163], v218 offset:9216\n"
"ds_read_b128 v[144:147], v224 offset:3584\n"
"v_mfma_f32_16x16x32_bf16 a[28:31], a[184:187], v[84:87], a[28:31]\n"
"ds_read_b64_tr_b16 a[164:165], v218 offset:12288\n"
"v_cvt_pk_bf16_f32 v100, v184, v185\n"
"v_mfma_f32_16x16x32_bf16 a[28:31], a[188:191], v[92:95], a[28:31]\n"
"s_waitcnt lgkmcnt(12)\n"
"ds_read_b64_tr_b16 a[166:167], v218 offset:13312\n"
"v_cvt_pk_bf16_f32 v101, v186, v187\n"
"v_mfma_f32_16x16x32_bf16 a[32:35], a[224:227], v[80:83], a[32:35]\n"
"s_add_u32 m0, s51, 99376\n"
"ds_read_b64_tr_b16 a[168:169], v218 offset:8200\n"
"ds_read_b128 v[148:151], v224 offset:3648\n"
"v_mfma_f32_16x16x32_bf16 a[32:35], a[228:231], v[88:91], a[32:35]\n"
"ds_read_b64_tr_b16 a[170:171], v218 offset:9224\n"
"buffer_load_dwordx4 v217, s[20:23], s50 offen offset:0 lds\n"
"v_mfma_f32_16x16x32_bf16 a[36:39], a[224:227], v[84:87], a[36:39]\n"
"ds_read_b64_tr_b16 a[172:173], v218 offset:12296\n"
"v_cvt_pk_bf16_f32 v102, v188, v189\n"
"v_mfma_f32_16x16x32_bf16 a[36:39], a[228:231], v[92:95], a[36:39]\n"
"ds_read_b64_tr_b16 a[174:175], v218 offset:13320\n"
"ds_read_b128 v[152:155], v224 offset:3712\n"
"v_mfma_f32_16x16x32_bf16 a[40:43], a[232:235], v[80:83], a[40:43]\n"
"ds_read_b64_tr_b16 a[176:177], v218 offset:8448\n"
"v_cvt_pk_bf16_f32 v103, v190, v191\n"
"v_mfma_f32_16x16x32_bf16 a[40:43], a[236:239], v[88:91], a[40:43]\n"
"ds_read_b64_tr_b16 a[178:179], v218 offset:9472\n"
"v_fma_mix_f32 v176, v104, v160, v168 op_sel:[0,0,0] op_sel_hi:[1,0,0]\n"
"v_mfma_f32_16x16x32_bf16 a[44:47], a[232:235], v[84:87], a[44:47]\n"
"s_add_u32 m0, s51, 107312\n"
"ds_read_b64_tr_b16 a[180:181], v218 offset:12544\n"
"ds_read_b128 v[156:159], v224 offset:3776\n"
"v_mfma_f32_16x16x32_bf16 a[44:47], a[236:239], v[92:95], a[44:47]\n"
"s_waitcnt lgkmcnt(14)\n"
"ds_read_b64_tr_b16 a[182:183], v218 offset:13568\n"
"buffer_load_dwordx4 v217, s[20:23], s50 offen offset:256 lds\n"
"v_mfma_f32_16x16x32_bf16 a[48:51], a[240:243], v[80:83], a[48:51]\n"
"s_add_u32 s50, s50, 32768\n"
"ds_read_b64_tr_b16 a[184:185], v218 offset:8456\n"
"v_fma_mix_f32 v177, v104, v161, v169 op_sel:[1,0,0] op_sel_hi:[1,0,0]\n"
"v_mfma_f32_16x16x32_bf16 a[48:51], a[244:247], v[88:91], a[48:51]\n"
"ds_read_b64_tr_b16 a[186:187], v218 offset:9480\n"
"v_fma_mix_f32 v178, v105, v162, v170 op_sel:[0,0,0] op_sel_hi:[1,0,0]\n"
"v_mfma_f32_16x16x32_bf16 a[52:55], a[240:243], v[84:87], a[52:55]\n"
"ds_read_b64_tr_b16 a[188:189], v218 offset:12552\n"
"v_fma_mix_f32 v179, v105, v163, v171 op_sel:[1,0,0] op_sel_hi:[1,0,0]\n"
"v_mfma_f32_16x16x32_bf16 a[52:55], a[244:247], v[92:95], a[52:55]\n"
"ds_read_b64_tr_b16 a[190:191], v218 offset:13576\n"
"v_fma_mix_f32 v180, v106, v164, v172 op_sel:[0,0,0] op_sel_hi:[1,0,0]\n"
"v_mfma_f32_16x16x32_bf16 a[56:59], a[248:251], v[80:83], a[56:59]\n"
"s_add_u32 m0, s51, 114744\n"
"ds_read_b64_tr_b16 a[224:225], v218 offset:8704\n"
"v_fma_mix_f32 v181, v106, v165, v173 op_sel:[1,0,0] op_sel_hi:[1,0,0]\n"
"v_mfma_f32_16x16x32_bf16 a[56:59], a[252:255], v[88:91], a[56:59]\n"
"ds_read_b64_tr_b16 a[226:227], v218 offset:9728\n"
"buffer_load_dwordx4 v217, s[20:23], s50 offen offset:0 lds\n"
"v_mfma_f32_16x16x32_bf16 a[60:63], a[248:251], v[84:87], a[60:63]\n"
"ds_read_b64_tr_b16 a[228:229], v218 offset:12800\n"
"v_fma_mix_f32 v182, v107, v166, v174 op_sel:[0,0,0] op_sel_hi:[1,0,0]\n"
"v_mfma_f32_16x16x32_bf16 a[60:63], a[252:255], v[92:95], a[60:63]\n"
"s_waitcnt lgkmcnt(12)\n"
"ds_read_b64_tr_b16 a[230:231], v218 offset:13824\n"
"v_fma_mix_f32 v183, v107, v167, v175 op_sel:[1,0,0] op_sel_hi:[1,0,0]\n"
"v_mfma_f32_16x16x32_bf16 a[64:67], a[160:163], v[80:83], a[64:67]\n"
"ds_read_b64_tr_b16 a[232:233], v218 offset:8712\n"
"v_cvt_pk_bf16_f32 v104, v176, v177\n"
"v_mfma_f32_16x16x32_bf16 a[64:67], a[164:167], v[88:91], a[64:67]\n"
"ds_read_b64_tr_b16 a[234:235], v218 offset:9736\n"
"v_cvt_pk_bf16_f32 v105, v178, v179\n"
"v_mfma_f32_16x16x32_bf16 a[68:71], a[160:163], v[84:87], a[68:71]\n"
"s_add_u32 m0, s51, 122680\n"
"ds_read_b64_tr_b16 a[236:237], v218 offset:12808\n"
"v_cvt_pk_bf16_f32 v106, v180, v181\n"
"v_mfma_f32_16x16x32_bf16 a[68:71], a[164:167], v[92:95], a[68:71]\n"
"ds_read_b64_tr_b16 a[238:239], v218 offset:13832\n"
"buffer_load_dwordx4 v217, s[20:23], s50 offen offset:256 lds\n"
"v_mfma_f32_16x16x32_bf16 a[72:75], a[168:171], v[80:83], a[72:75]\n"
"s_add_u32 s50, s50, 32768\n"
"ds_read_b64_tr_b16 a[240:241], v218 offset:8960\n"
"v_cvt_pk_bf16_f32 v107, v182, v183\n"
"v_mfma_f32_16x16x32_bf16 a[72:75], a[172:175], v[88:91], a[72:75]\n"
"ds_read_b64_tr_b16 a[242:243], v218 offset:9984\n"
"v_fma_mix_f32 v184, v108, v160, v168 op_sel:[0,0,0] op_sel_hi:[1,0,0]\n"
"v_mfma_f32_16x16x32_bf16 a[76:79], a[168:171], v[84:87], a[76:79]\n"
"ds_read_b64_tr_b16 a[244:245], v218 offset:13056\n"
"v_fma_mix_f32 v185, v108, v161, v169 op_sel:[1,0,0] op_sel_hi:[1,0,0]\n"
"v_mfma_f32_16x16x32_bf16 a[76:79], a[172:175], v[92:95], a[76:79]\n"
"s_waitcnt lgkmcnt(11)\n"
"ds_read_b64_tr_b16 a[246:247], v218 offset:14080\n"
"v_fma_mix_f32 v186, v109, v162, v170 op_sel:[0,0,0] op_sel_hi:[1,0,0]\n"
"v_mfma_f32_16x16x32_bf16 a[80:83], a[176:179], v[80:83], a[80:83]\n"
"s_add_u32 m0, s51, 115768\n"
"ds_read_b64_tr_b16 a[248:249], v218 offset:8968\n"
"v_fma_mix_f32 v187, v109, v163, v171 op_sel:[1,0,0] op_sel_hi:[1,0,0]\n"
"v_mfma_f32_16x16x32_bf16 a[80:83], a[180:183], v[88:91], a[80:83]\n"
"ds_read_b64_tr_b16 a[250:251], v218 offset:9992\n"
"buffer_load_dwordx4 v217, s[20:23], s50 offen offset:0 lds\n"
"v_mfma_f32_16x16x32_bf16 a[84:87], a[176:179], v[84:87], a[84:87]\n"
"ds_read_b64_tr_b16 a[252:253], v218 offset:13064\n"
"v_fma_mix_f32 v188, v110, v164, v172 op_sel:[0,0,0] op_sel_hi:[1,0,0]\n"
"v_mfma_f32_16x16x32_bf16 a[84:87], a[180:183], v[92:95], a[84:87]\n"
"ds_read_b64_tr_b16 a[254:255], v218 offset:14088\n"
"v_fma_mix_f32 v189, v110, v165, v173 op_sel:[1,0,0] op_sel_hi:[1,0,0]\n"
"v_mfma_f32_16x16x32_bf16 a[88:91], a[184:187], v[80:83], a[88:91]\n"
"ds_read_b64_tr_b16 a[160:161], v218 offset:32784\n"
"v_fma_mix_f32 v190, v111, v166, v174 op_sel:[0,0,0] op_sel_hi:[1,0,0]\n"
"v_fma_mix_f32 v191, v111, v167, v175 op_sel:[1,0,0] op_sel_hi:[1,0,0]\n"
"v_mfma_f32_16x16x32_bf16 a[88:91], a[188:191], v[88:91], a[88:91]\n"
"ds_read_b64_tr_b16 a[162:163], v218 offset:33808\n"
"ds_read_b128 v[160:163], v224 offset:3840\n"
"v_mfma_f32_16x16x32_bf16 a[92:95], a[184:187], v[84:87], a[92:95]\n"
"s_add_u32 m0, s51, 123704\n"
"ds_read_b64_tr_b16 a[164:165], v218 offset:36880\n"
"v_cvt_pk_bf16_f32 v108, v184, v185\n"
"v_mfma_f32_16x16x32_bf16 a[92:95], a[188:191], v[92:95], a[92:95]\n"
"s_waitcnt lgkmcnt(12)\n"
"buffer_load_dwordx4 v217, s[20:23], s50 offen offset:256 lds\n"
"ds_read_b64_tr_b16 a[166:167], v218 offset:37904\n"
"v_mfma_f32_16x16x32_bf16 a[96:99], a[224:227], v[80:83], a[96:99]\n"
"s_add_u32 s50, s50, 425984\n"
"ds_read_b64_tr_b16 a[168:169], v218 offset:32792\n"
"ds_read_b128 v[164:167], v224 offset:3904\n"
"v_mfma_f32_16x16x32_bf16 a[96:99], a[228:231], v[88:91], a[96:99]\n"
"ds_read_b64_tr_b16 a[170:171], v218 offset:33816\n"
"v_cvt_pk_bf16_f32 v109, v186, v187\n"
"v_mfma_f32_16x16x32_bf16 a[100:103], a[224:227], v[84:87], a[100:103]\n"
"ds_read_b64_tr_b16 a[172:173], v218 offset:36888\n"
"v_cvt_pk_bf16_f32 v110, v188, v189\n"
"v_mfma_f32_16x16x32_bf16 a[100:103], a[228:231], v[92:95], a[100:103]\n"
"ds_read_b64_tr_b16 a[174:175], v218 offset:37912\n"
"ds_read_b128 v[168:171], v224 offset:3968\n"
"v_mfma_f32_16x16x32_bf16 a[104:107], a[232:235], v[80:83], a[104:107]\n"
"ds_read_b64_tr_b16 a[176:177], v218 offset:33040\n"
"v_cvt_pk_bf16_f32 v111, v190, v191\n"
"v_mfma_f32_16x16x32_bf16 a[104:107], a[236:239], v[88:91], a[104:107]\n"
"ds_read_b64_tr_b16 a[178:179], v218 offset:34064\n"
"v_fma_mix_f32 v176, v112, v144, v152 op_sel:[0,0,0] op_sel_hi:[1,0,0]\n"
"v_mfma_f32_16x16x32_bf16 a[108:111], a[232:235], v[84:87], a[108:111]\n"
"ds_read_b64_tr_b16 a[180:181], v218 offset:37136\n"
"ds_read_b128 v[172:175], v224 offset:4032\n"
"v_mfma_f32_16x16x32_bf16 a[108:111], a[236:239], v[92:95], a[108:111]\n"
"s_waitcnt lgkmcnt(14)\n"
"ds_read_b64_tr_b16 a[182:183], v218 offset:38160\n"
"v_fma_mix_f32 v177, v112, v145, v153 op_sel:[1,0,0] op_sel_hi:[1,0,0]\n"
"v_mfma_f32_16x16x32_bf16 a[112:115], a[240:243], v[80:83], a[112:115]\n"
"ds_read_b64_tr_b16 a[184:185], v218 offset:33048\n"
"v_fma_mix_f32 v178, v113, v146, v154 op_sel:[0,0,0] op_sel_hi:[1,0,0]\n"
"v_mfma_f32_16x16x32_bf16 a[112:115], a[244:247], v[88:91], a[112:115]\n"
"ds_read_b64_tr_b16 a[186:187], v218 offset:34072\n"
"v_fma_mix_f32 v179, v113, v147, v155 op_sel:[1,0,0] op_sel_hi:[1,0,0]\n"
"v_mfma_f32_16x16x32_bf16 a[116:119], a[240:243], v[84:87], a[116:119]\n"
"ds_read_b64_tr_b16 a[188:189], v218 offset:37144\n"
"v_fma_mix_f32 v180, v114, v148, v156 op_sel:[0,0,0] op_sel_hi:[1,0,0]\n"
"v_mfma_f32_16x16x32_bf16 a[116:119], a[244:247], v[92:95], a[116:119]\n"
"ds_read_b64_tr_b16 a[190:191], v218 offset:38168\n"
"v_fma_mix_f32 v181, v114, v149, v157 op_sel:[1,0,0] op_sel_hi:[1,0,0]\n"
"v_mfma_f32_16x16x32_bf16 a[120:123], a[248:251], v[80:83], a[120:123]\n"
"ds_read_b64_tr_b16 a[224:225], v218 offset:33296\n"
"v_fma_mix_f32 v182, v115, v150, v158 op_sel:[0,0,0] op_sel_hi:[1,0,0]\n"
"v_mfma_f32_16x16x32_bf16 a[120:123], a[252:255], v[88:91], a[120:123]\n"
"ds_read_b64_tr_b16 a[226:227], v218 offset:34320\n"
"v_fma_mix_f32 v183, v115, v151, v159 op_sel:[1,0,0] op_sel_hi:[1,0,0]\n"
"v_mfma_f32_16x16x32_bf16 a[124:127], a[248:251], v[84:87], a[124:127]\n"
"ds_read_b64_tr_b16 a[228:229], v218 offset:37392\n"
"v_cvt_pk_bf16_f32 v112, v176, v177\n"
"v_mfma_f32_16x16x32_bf16 a[124:127], a[252:255], v[92:95], a[124:127]\n"
"s_waitcnt lgkmcnt(12)\n"
"ds_read_b64_tr_b16 a[230:231], v218 offset:38416\n"
"v_cvt_pk_bf16_f32 v113, v178, v179\n"
"v_mfma_f32_16x16x32_bf16 a[0:3], a[160:163], v[96:99], a[0:3]\n"
"ds_read_b64_tr_b16 a[232:233], v218 offset:33304\n"
"v_cvt_pk_bf16_f32 v114, v180, v181\n"
"v_mfma_f32_16x16x32_bf16 a[0:3], a[164:167], v[104:107], a[0:3]\n"
"ds_read_b64_tr_b16 a[234:235], v218 offset:34328\n"
"v_cvt_pk_bf16_f32 v115, v182, v183\n"
"v_mfma_f32_16x16x32_bf16 a[4:7], a[160:163], v[100:103], a[4:7]\n"
"s_waitcnt vmcnt(8)\n"
"s_barrier\n"
"ds_read_b64_tr_b16 a[236:237], v218 offset:37400\n"
"v_fma_mix_f32 v184, v116, v144, v152 op_sel:[0,0,0] op_sel_hi:[1,0,0]\n"
"v_mfma_f32_16x16x32_bf16 a[4:7], a[164:167], v[108:111], a[4:7]\n"
"ds_read_b64_tr_b16 a[238:239], v218 offset:38424\n"
"v_fma_mix_f32 v185, v116, v145, v153 op_sel:[1,0,0] op_sel_hi:[1,0,0]\n"
"v_mfma_f32_16x16x32_bf16 a[8:11], a[168:171], v[96:99], a[8:11]\n"
"s_mov_b32 m0, s86\n"
"ds_read_b64_tr_b16 a[240:241], v218 offset:33552\n"
"v_fma_mix_f32 v186, v117, v146, v154 op_sel:[0,0,0] op_sel_hi:[1,0,0]\n"
"v_mfma_f32_16x16x32_bf16 a[8:11], a[172:175], v[104:107], a[8:11]\n"
"ds_read_b64_tr_b16 a[242:243], v218 offset:34576\n"
"buffer_load_dwordx4 v246, s[8:11], s84 offen offset:0 lds\n"
"v_mfma_f32_16x16x32_bf16 a[12:15], a[168:171], v[100:103], a[12:15]\n"
"ds_read_b64_tr_b16 a[244:245], v218 offset:37648\n"
"v_fma_mix_f32 v187, v117, v147, v155 op_sel:[1,0,0] op_sel_hi:[1,0,0]\n"
"v_mfma_f32_16x16x32_bf16 a[12:15], a[172:175], v[108:111], a[12:15]\n"
"s_waitcnt lgkmcnt(11)\n"
"ds_read_b64_tr_b16 a[246:247], v218 offset:38672\n"
"v_fma_mix_f32 v188, v118, v148, v156 op_sel:[0,0,0] op_sel_hi:[1,0,0]\n"
"v_mfma_f32_16x16x32_bf16 a[16:19], a[176:179], v[96:99], a[16:19]\n"
"ds_read_b64_tr_b16 a[248:249], v218 offset:33560\n"
"v_fma_mix_f32 v189, v118, v149, v157 op_sel:[1,0,0] op_sel_hi:[1,0,0]\n"
"v_mfma_f32_16x16x32_bf16 a[16:19], a[180:183], v[104:107], a[16:19]\n"
"ds_read_b64_tr_b16 a[250:251], v218 offset:34584\n"
"v_fma_mix_f32 v190, v119, v150, v158 op_sel:[0,0,0] op_sel_hi:[1,0,0]\n"
"v_mfma_f32_16x16x32_bf16 a[20:23], a[176:179], v[100:103], a[20:23]\n"
"ds_read_b64_tr_b16 a[252:253], v218 offset:37656\n"
"v_fma_mix_f32 v191, v119, v151, v159 op_sel:[1,0,0] op_sel_hi:[1,0,0]\n"
"v_mfma_f32_16x16x32_bf16 a[20:23], a[180:183], v[108:111], a[20:23]\n"
"ds_read_b64_tr_b16 a[254:255], v218 offset:38680\n"
"buffer_load_dwordx4 v246, s[8:11], s84 offen offset:1024 lds\n"
"v_mfma_f32_16x16x32_bf16 a[24:27], a[184:187], v[96:99], a[24:27]\n"
"ds_read_b64_tr_b16 a[160:161], v218 offset:40976\n"
"ds_read_b128 v[144:147], v224 offset:4096\n"
"v_mfma_f32_16x16x32_bf16 a[24:27], a[188:191], v[104:107], a[24:27]\n"
"ds_read_b64_tr_b16 a[162:163], v218 offset:42000\n"
"v_cvt_pk_bf16_f32 v116, v184, v185\n"
"v_mfma_f32_16x16x32_bf16 a[28:31], a[184:187], v[100:103], a[28:31]\n"
"ds_read_b64_tr_b16 a[164:165], v218 offset:45072\n"
"v_cvt_pk_bf16_f32 v117, v186, v187\n"
"v_mfma_f32_16x16x32_bf16 a[28:31], a[188:191], v[108:111], a[28:31]\n"
"s_waitcnt lgkmcnt(12)\n"
"ds_read_b64_tr_b16 a[166:167], v218 offset:46096\n"
"ds_read_b128 v[148:151], v224 offset:4160\n"
"v_mfma_f32_16x16x32_bf16 a[32:35], a[224:227], v[96:99], a[32:35]\n"
"ds_read_b64_tr_b16 a[168:169], v218 offset:40984\n"
"v_cvt_pk_bf16_f32 v118, v188, v189\n"
"v_mfma_f32_16x16x32_bf16 a[32:35], a[228:231], v[104:107], a[32:35]\n"
"ds_read_b64_tr_b16 a[170:171], v218 offset:42008\n"
"buffer_load_dwordx4 v247, s[8:11], s84 offen offset:2048 lds\n"
"v_mfma_f32_16x16x32_bf16 a[36:39], a[224:227], v[100:103], a[36:39]\n"
"ds_read_b64_tr_b16 a[172:173], v218 offset:45080\n"
"ds_read_b128 v[152:155], v224 offset:4224\n"
"v_mfma_f32_16x16x32_bf16 a[36:39], a[228:231], v[108:111], a[36:39]\n"
"ds_read_b64_tr_b16 a[174:175], v218 offset:46104\n"
"v_cvt_pk_bf16_f32 v119, v190, v191\n"
"v_mfma_f32_16x16x32_bf16 a[40:43], a[232:235], v[96:99], a[40:43]\n"
"ds_read_b64_tr_b16 a[176:177], v218 offset:41232\n"
"v_fma_mix_f32 v176, v120, v160, v168 op_sel:[0,0,0] op_sel_hi:[1,0,0]\n"
"v_mfma_f32_16x16x32_bf16 a[40:43], a[236:239], v[104:107], a[40:43]\n"
"ds_read_b64_tr_b16 a[178:179], v218 offset:42256\n"
"ds_read_b128 v[156:159], v224 offset:4288\n"
"v_mfma_f32_16x16x32_bf16 a[44:47], a[232:235], v[100:103], a[44:47]\n"
"ds_read_b64_tr_b16 a[180:181], v218 offset:45328\n"
"v_fma_mix_f32 v177, v120, v161, v169 op_sel:[1,0,0] op_sel_hi:[1,0,0]\n"
"v_mfma_f32_16x16x32_bf16 a[44:47], a[236:239], v[108:111], a[44:47]\n"
"s_waitcnt lgkmcnt(14)\n"
"ds_read_b64_tr_b16 a[182:183], v218 offset:46352\n"
"buffer_load_dwordx4 v247, s[8:11], s84 offen offset:3072 lds\n"
"v_mfma_f32_16x16x32_bf16 a[48:51], a[240:243], v[96:99], a[48:51]\n"
"ds_read_b64_tr_b16 a[184:185], v218 offset:41240\n"
"v_fma_mix_f32 v178, v121, v162, v170 op_sel:[0,0,0] op_sel_hi:[1,0,0]\n"
"v_mfma_f32_16x16x32_bf16 a[48:51], a[244:247], v[104:107], a[48:51]\n"
"ds_read_b64_tr_b16 a[186:187], v218 offset:42264\n"
"v_fma_mix_f32 v179, v121, v163, v171 op_sel:[1,0,0] op_sel_hi:[1,0,0]\n"
"v_mfma_f32_16x16x32_bf16 a[52:55], a[240:243], v[100:103], a[52:55]\n"
"ds_read_b64_tr_b16 a[188:189], v218 offset:45336\n"
"v_fma_mix_f32 v180, v122, v164, v172 op_sel:[0,0,0] op_sel_hi:[1,0,0]\n"
"v_mfma_f32_16x16x32_bf16 a[52:55], a[244:247], v[108:111], a[52:55]\n"
"ds_read_b64_tr_b16 a[190:191], v218 offset:46360\n"
"v_fma_mix_f32 v181, v122, v165, v173 op_sel:[1,0,0] op_sel_hi:[1,0,0]\n"
"v_mfma_f32_16x16x32_bf16 a[56:59], a[248:251], v[96:99], a[56:59]\n"
"s_add_u32 m0, s86, 16384\n"
"ds_read_b64_tr_b16 a[224:225], v218 offset:41488\n"
"v_fma_mix_f32 v182, v123, v166, v174 op_sel:[0,0,0] op_sel_hi:[1,0,0]\n"
"v_mfma_f32_16x16x32_bf16 a[56:59], a[252:255], v[104:107], a[56:59]\n"
"ds_read_b64_tr_b16 a[226:227], v218 offset:42512\n"
"buffer_load_dwordx4 v246, s[12:15], s84 offen offset:0 lds\n"
"v_mfma_f32_16x16x32_bf16 a[60:63], a[248:251], v[100:103], a[60:63]\n"
"ds_read_b64_tr_b16 a[228:229], v218 offset:45584\n"
"v_fma_mix_f32 v183, v123, v167, v175 op_sel:[1,0,0] op_sel_hi:[1,0,0]\n"
"v_mfma_f32_16x16x32_bf16 a[60:63], a[252:255], v[108:111], a[60:63]\n"
"s_waitcnt lgkmcnt(12)\n"
"ds_read_b64_tr_b16 a[230:231], v218 offset:46608\n"
"v_cvt_pk_bf16_f32 v120, v176, v177\n"
"v_mfma_f32_16x16x32_bf16 a[64:67], a[160:163], v[96:99], a[64:67]\n"
"ds_read_b64_tr_b16 a[232:233], v218 offset:41496\n"
"v_cvt_pk_bf16_f32 v121, v178, v179\n"
"v_mfma_f32_16x16x32_bf16 a[64:67], a[164:167], v[104:107], a[64:67]\n"
"ds_read_b64_tr_b16 a[234:235], v218 offset:42520\n"
"v_cvt_pk_bf16_f32 v122, v180, v181\n"
"v_mfma_f32_16x16x32_bf16 a[68:71], a[160:163], v[100:103], a[68:71]\n"
"ds_read_b64_tr_b16 a[236:237], v218 offset:45592\n"
"v_cvt_pk_bf16_f32 v123, v182, v183\n"
"v_mfma_f32_16x16x32_bf16 a[68:71], a[164:167], v[108:111], a[68:71]\n"
"ds_read_b64_tr_b16 a[238:239], v218 offset:46616\n"
"buffer_load_dwordx4 v247, s[12:15], s84 offen offset:1024 lds\n"
"v_mfma_f32_16x16x32_bf16 a[72:75], a[168:171], v[96:99], a[72:75]\n"
"ds_read_b64_tr_b16 a[240:241], v218 offset:41744\n"
"v_fma_mix_f32 v184, v124, v160, v168 op_sel:[0,0,0] op_sel_hi:[1,0,0]\n"
"v_mfma_f32_16x16x32_bf16 a[72:75], a[172:175], v[104:107], a[72:75]\n"
"ds_read_b64_tr_b16 a[242:243], v218 offset:42768\n"
"v_fma_mix_f32 v185, v124, v161, v169 op_sel:[1,0,0] op_sel_hi:[1,0,0]\n"
"v_mfma_f32_16x16x32_bf16 a[76:79], a[168:171], v[100:103], a[76:79]\n"
"ds_read_b64_tr_b16 a[244:245], v218 offset:45840\n"
"v_fma_mix_f32 v186, v125, v162, v170 op_sel:[0,0,0] op_sel_hi:[1,0,0]\n"
"v_mfma_f32_16x16x32_bf16 a[76:79], a[172:175], v[108:111], a[76:79]\n"
"s_waitcnt lgkmcnt(11)\n"
"ds_read_b64_tr_b16 a[246:247], v218 offset:46864\n"
"v_fma_mix_f32 v187, v125, v163, v171 op_sel:[1,0,0] op_sel_hi:[1,0,0]\n"
"v_mfma_f32_16x16x32_bf16 a[80:83], a[176:179], v[96:99], a[80:83]\n"
"ds_read_b64_tr_b16 a[248:249], v218 offset:41752\n"
"v_fma_mix_f32 v188, v126, v164, v172 op_sel:[0,0,0] op_sel_hi:[1,0,0]\n"
"v_mfma_f32_16x16x32_bf16 a[80:83], a[180:183], v[104:107], a[80:83]\n"
"ds_read_b64_tr_b16 a[250:251], v218 offset:42776\n"
"buffer_load_dwordx4 v246, s[12:15], s84 offen offset:2048 lds\n"
"v_mfma_f32_16x16x32_bf16 a[84:87], a[176:179], v[100:103], a[84:87]\n"
"ds_read_b64_tr_b16 a[252:253], v218 offset:45848\n"
"v_fma_mix_f32 v189, v126, v165, v173 op_sel:[1,0,0] op_sel_hi:[1,0,0]\n"
"v_mfma_f32_16x16x32_bf16 a[84:87], a[180:183], v[108:111], a[84:87]\n"
"ds_read_b64_tr_b16 a[254:255], v218 offset:46872\n"
"v_fma_mix_f32 v190, v127, v166, v174 op_sel:[0,0,0] op_sel_hi:[1,0,0]\n"
"v_mfma_f32_16x16x32_bf16 a[88:91], a[184:187], v[96:99], a[88:91]\n"
"ds_read_b64_tr_b16 a[160:161], v219 offset:0\n"
"v_fma_mix_f32 v191, v127, v167, v175 op_sel:[1,0,0] op_sel_hi:[1,0,0]\n"
"v_cvt_pk_bf16_f32 v124, v184, v185\n"
"v_mfma_f32_16x16x32_bf16 a[88:91], a[188:191], v[104:107], a[88:91]\n"
"ds_read_b64_tr_b16 a[162:163], v219 offset:1024\n"
"ds_read_b128 v[160:163], v224 offset:4352\n"
"v_mfma_f32_16x16x32_bf16 a[92:95], a[184:187], v[100:103], a[92:95]\n"
"ds_read_b64_tr_b16 a[164:165], v219 offset:4096\n"
"v_cvt_pk_bf16_f32 v125, v186, v187\n"
"v_mfma_f32_16x16x32_bf16 a[92:95], a[188:191], v[108:111], a[92:95]\n"
"s_waitcnt lgkmcnt(12)\n"
"buffer_load_dwordx4 v247, s[12:15], s84 offen offset:3072 lds\n"
"ds_read_b64_tr_b16 a[166:167], v219 offset:5120\n"
"v_mfma_f32_16x16x32_bf16 a[96:99], a[224:227], v[96:99], a[96:99]\n"
"s_add_u32 s84, s84, 16384\n"
"ds_read_b64_tr_b16 a[168:169], v219 offset:8\n"
"ds_read_b128 v[164:167], v224 offset:4416\n"
"v_mfma_f32_16x16x32_bf16 a[96:99], a[228:231], v[104:107], a[96:99]\n"
"ds_read_b64_tr_b16 a[170:171], v219 offset:1032\n"
"v_cvt_pk_bf16_f32 v126, v188, v189\n"
"v_mfma_f32_16x16x32_bf16 a[100:103], a[224:227], v[100:103], a[100:103]\n"
"ds_read_b64_tr_b16 a[172:173], v219 offset:4104\n"
"v_cvt_pk_bf16_f32 v127, v190, v191\n"
"v_mfma_f32_16x16x32_bf16 a[100:103], a[228:231], v[108:111], a[100:103]\n"
"ds_read_b64_tr_b16 a[174:175], v219 offset:5128\n"
"ds_read_b128 v[168:171], v224 offset:4480\n"
"v_mfma_f32_16x16x32_bf16 a[104:107], a[232:235], v[96:99], a[104:107]\n"
"ds_read_b64_tr_b16 a[176:177], v219 offset:256\n"
"v_fma_mix_f32 v176, v128, v144, v152 op_sel:[0,0,0] op_sel_hi:[1,0,0]\n"
"v_mfma_f32_16x16x32_bf16 a[104:107], a[236:239], v[104:107], a[104:107]\n"
"ds_read_b64_tr_b16 a[178:179], v219 offset:1280\n"
"v_fma_mix_f32 v177, v128, v145, v153 op_sel:[1,0,0] op_sel_hi:[1,0,0]\n"
"v_mfma_f32_16x16x32_bf16 a[108:111], a[232:235], v[100:103], a[108:111]\n"
"ds_read_b64_tr_b16 a[180:181], v219 offset:4352\n"
"ds_read_b128 v[172:175], v224 offset:4544\n"
"v_mfma_f32_16x16x32_bf16 a[108:111], a[236:239], v[108:111], a[108:111]\n"
"s_waitcnt lgkmcnt(14)\n"
"ds_read_b64_tr_b16 a[182:183], v219 offset:5376\n"
"v_fma_mix_f32 v178, v129, v146, v154 op_sel:[0,0,0] op_sel_hi:[1,0,0]\n"
"v_mfma_f32_16x16x32_bf16 a[112:115], a[240:243], v[96:99], a[112:115]\n"
"ds_read_b64_tr_b16 a[184:185], v219 offset:264\n"
"v_fma_mix_f32 v179, v129, v147, v155 op_sel:[1,0,0] op_sel_hi:[1,0,0]\n"
"v_mfma_f32_16x16x32_bf16 a[112:115], a[244:247], v[104:107], a[112:115]\n"
"ds_read_b64_tr_b16 a[186:187], v219 offset:1288\n"
"v_fma_mix_f32 v180, v130, v148, v156 op_sel:[0,0,0] op_sel_hi:[1,0,0]\n"
"v_mfma_f32_16x16x32_bf16 a[116:119], a[240:243], v[100:103], a[116:119]\n"
"ds_read_b64_tr_b16 a[188:189], v219 offset:4360\n"
"v_fma_mix_f32 v181, v130, v149, v157 op_sel:[1,0,0] op_sel_hi:[1,0,0]\n"
"v_mfma_f32_16x16x32_bf16 a[116:119], a[244:247], v[108:111], a[116:119]\n"
"ds_read_b64_tr_b16 a[190:191], v219 offset:5384\n"
"v_fma_mix_f32 v182, v131, v150, v158 op_sel:[0,0,0] op_sel_hi:[1,0,0]\n"
"v_mfma_f32_16x16x32_bf16 a[120:123], a[248:251], v[96:99], a[120:123]\n"
"ds_read_b64_tr_b16 a[224:225], v219 offset:512\n"
"v_fma_mix_f32 v183, v131, v151, v159 op_sel:[1,0,0] op_sel_hi:[1,0,0]\n"
"v_mfma_f32_16x16x32_bf16 a[120:123], a[252:255], v[104:107], a[120:123]\n"
"ds_read_b64_tr_b16 a[226:227], v219 offset:1536\n"
"v_cvt_pk_bf16_f32 v128, v176, v177\n"
"v_mfma_f32_16x16x32_bf16 a[124:127], a[248:251], v[100:103], a[124:127]\n"
"ds_read_b64_tr_b16 a[228:229], v219 offset:4608\n"
"v_cvt_pk_bf16_f32 v129, v178, v179\n"
"v_mfma_f32_16x16x32_bf16 a[124:127], a[252:255], v[108:111], a[124:127]\n"
"s_waitcnt lgkmcnt(12)\n"
"ds_read_b64_tr_b16 a[230:231], v219 offset:5632\n"
"v_cvt_pk_bf16_f32 v130, v180, v181\n"
"v_mfma_f32_16x16x32_bf16 a[0:3], a[160:163], v[112:115], a[0:3]\n"
"ds_read_b64_tr_b16 a[232:233], v219 offset:520\n"
"v_cvt_pk_bf16_f32 v131, v182, v183\n"
"v_mfma_f32_16x16x32_bf16 a[0:3], a[164:167], v[120:123], a[0:3]\n"
"ds_read_b64_tr_b16 a[234:235], v219 offset:1544\n"
"v_fma_mix_f32 v184, v132, v144, v152 op_sel:[0,0,0] op_sel_hi:[1,0,0]\n"
"v_mfma_f32_16x16x32_bf16 a[4:7], a[160:163], v[116:119], a[4:7]\n"
"s_waitcnt vmcnt(8)\n"
"s_barrier\n"
"ds_read_b64_tr_b16 a[236:237], v219 offset:4616\n"
"v_fma_mix_f32 v185, v132, v145, v153 op_sel:[1,0,0] op_sel_hi:[1,0,0]\n"
"v_mfma_f32_16x16x32_bf16 a[4:7], a[164:167], v[124:127], a[4:7]\n"
"ds_read_b64_tr_b16 a[238:239], v219 offset:5640\n"
"v_fma_mix_f32 v186, v133, v146, v154 op_sel:[0,0,0] op_sel_hi:[1,0,0]\n"
"v_mfma_f32_16x16x32_bf16 a[8:11], a[168:171], v[112:115], a[8:11]\n"
"s_add_u32 m0, s86, 32768\n"
"ds_read_b64_tr_b16 a[240:241], v219 offset:768\n"
"v_fma_mix_f32 v187, v133, v147, v155 op_sel:[1,0,0] op_sel_hi:[1,0,0]\n"
"v_mfma_f32_16x16x32_bf16 a[8:11], a[172:175], v[120:123], a[8:11]\n"
"ds_read_b64_tr_b16 a[242:243], v219 offset:1792\n"
"buffer_load_dwordx4 v246, s[8:11], s84 offen offset:0 lds\n"
"v_mfma_f32_16x16x32_bf16 a[12:15], a[168:171], v[116:119], a[12:15]\n"
"ds_read_b64_tr_b16 a[244:245], v219 offset:4864\n"
"v_fma_mix_f32 v188, v134, v148, v156 op_sel:[0,0,0] op_sel_hi:[1,0,0]\n"
"v_mfma_f32_16x16x32_bf16 a[12:15], a[172:175], v[124:127], a[12:15]\n"
"s_waitcnt lgkmcnt(11)\n"
"ds_read_b64_tr_b16 a[246:247], v219 offset:5888\n"
"v_fma_mix_f32 v189, v134, v149, v157 op_sel:[1,0,0] op_sel_hi:[1,0,0]\n"
"v_mfma_f32_16x16x32_bf16 a[16:19], a[176:179], v[112:115], a[16:19]\n"
"ds_read_b64_tr_b16 a[248:249], v219 offset:776\n"
"v_fma_mix_f32 v190, v135, v150, v158 op_sel:[0,0,0] op_sel_hi:[1,0,0]\n"
"v_mfma_f32_16x16x32_bf16 a[16:19], a[180:183], v[120:123], a[16:19]\n"
"ds_read_b64_tr_b16 a[250:251], v219 offset:1800\n"
"v_fma_mix_f32 v191, v135, v151, v159 op_sel:[1,0,0] op_sel_hi:[1,0,0]\n"
"v_mfma_f32_16x16x32_bf16 a[20:23], a[176:179], v[116:119], a[20:23]\n"
"ds_read_b64_tr_b16 a[252:253], v219 offset:4872\n"
"v_cvt_pk_bf16_f32 v132, v184, v185\n"
"v_mfma_f32_16x16x32_bf16 a[20:23], a[180:183], v[124:127], a[20:23]\n"
"ds_read_b64_tr_b16 a[254:255], v219 offset:5896\n"
"buffer_load_dwordx4 v246, s[8:11], s84 offen offset:1024 lds\n"
"v_mfma_f32_16x16x32_bf16 a[24:27], a[184:187], v[112:115], a[24:27]\n"
"ds_read_b64_tr_b16 a[160:161], v219 offset:8192\n"
"v_cvt_pk_bf16_f32 v133, v186, v187\n"
"v_mfma_f32_16x16x32_bf16 a[24:27], a[188:191], v[120:123], a[24:27]\n"
"ds_read_b64_tr_b16 a[162:163], v219 offset:9216\n"
"v_cvt_pk_bf16_f32 v134, v188, v189\n"
"v_mfma_f32_16x16x32_bf16 a[28:31], a[184:187], v[116:119], a[28:31]\n"
"ds_read_b64_tr_b16 a[164:165], v219 offset:12288\n"
"v_cvt_pk_bf16_f32 v135, v190, v191\n"
"v_mfma_f32_16x16x32_bf16 a[28:31], a[188:191], v[124:127], a[28:31]\n"
"s_waitcnt lgkmcnt(11)\n"
"ds_read_b64_tr_b16 a[166:167], v219 offset:13312\n"
"v_fma_mix_f32 v176, v136, v160, v168 op_sel:[0,0,0] op_sel_hi:[1,0,0]\n"
"v_mfma_f32_16x16x32_bf16 a[32:35], a[224:227], v[112:115], a[32:35]\n"
"ds_read_b64_tr_b16 a[168:169], v219 offset:8200\n"
"v_fma_mix_f32 v177, v136, v161, v169 op_sel:[1,0,0] op_sel_hi:[1,0,0]\n"
"v_mfma_f32_16x16x32_bf16 a[32:35], a[228:231], v[120:123], a[32:35]\n"
"ds_read_b64_tr_b16 a[170:171], v219 offset:9224\n"
"buffer_load_dwordx4 v247, s[8:11], s84 offen offset:2048 lds\n"
"v_mfma_f32_16x16x32_bf16 a[36:39], a[224:227], v[116:119], a[36:39]\n"
"ds_read_b64_tr_b16 a[172:173], v219 offset:12296\n"
"v_fma_mix_f32 v178, v137, v162, v170 op_sel:[0,0,0] op_sel_hi:[1,0,0]\n"
"v_mfma_f32_16x16x32_bf16 a[36:39], a[228:231], v[124:127], a[36:39]\n"
"ds_read_b64_tr_b16 a[174:175], v219 offset:13320\n"
"v_fma_mix_f32 v179, v137, v163, v171 op_sel:[1,0,0] op_sel_hi:[1,0,0]\n"
"v_mfma_f32_16x16x32_bf16 a[40:43], a[232:235], v[112:115], a[40:43]\n"
"ds_read_b64_tr_b16 a[176:177], v219 offset:8448\n"
"v_fma_mix_f32 v180, v138, v164, v172 op_sel:[0,0,0] op_sel_hi:[1,0,0]\n"
"v_mfma_f32_16x16x32_bf16 a[40:43], a[236:239], v[120:123], a[40:43]\n"
"ds_read_b64_tr_b16 a[178:179], v219 offset:9472\n"
"v_fma_mix_f32 v181, v138, v165, v173 op_sel:[1,0,0] op_sel_hi:[1,0,0]\n"
"v_mfma_f32_16x16x32_bf16 a[44:47], a[232:235], v[116:119], a[44:47]\n"
"ds_read_b64_tr_b16 a[180:181], v219 offset:12544\n"
"v_fma_mix_f32 v182, v139, v166, v174 op_sel:[0,0,0] op_sel_hi:[1,0,0]\n"
"v_mfma_f32_16x16x32_bf16 a[44:47], a[236:239], v[124:127], a[44:47]\n"
"s_waitcnt lgkmcnt(11)\n"
"ds_read_b64_tr_b16 a[182:183], v219 offset:13568\n"
"buffer_load_dwordx4 v247, s[8:11], s84 offen offset:3072 lds\n"
"v_mfma_f32_16x16x32_bf16 a[48:51], a[240:243], v[112:115], a[48:51]\n"
"ds_read_b64_tr_b16 a[184:185], v219 offset:8456\n"
"v_fma_mix_f32 v183, v139, v167, v175 op_sel:[1,0,0] op_sel_hi:[1,0,0]\n"
"v_mfma_f32_16x16x32_bf16 a[48:51], a[244:247], v[120:123], a[48:51]\n"
"ds_read_b64_tr_b16 a[186:187], v219 offset:9480\n"
"v_cvt_pk_bf16_f32 v136, v176, v177\n"
"v_mfma_f32_16x16x32_bf16 a[52:55], a[240:243], v[116:119], a[52:55]\n"
"ds_read_b64_tr_b16 a[188:189], v219 offset:12552\n"
"v_cvt_pk_bf16_f32 v137, v178, v179\n"
"v_mfma_f32_16x16x32_bf16 a[52:55], a[244:247], v[124:127], a[52:55]\n"
"ds_read_b64_tr_b16 a[190:191], v219 offset:13576\n"
"v_cvt_pk_bf16_f32 v138, v180, v181\n"
"v_mfma_f32_16x16x32_bf16 a[56:59], a[248:251], v[112:115], a[56:59]\n"
"s_add_u32 m0, s86, 49152\n"
"ds_read_b64_tr_b16 a[224:225], v219 offset:8704\n"
"v_cvt_pk_bf16_f32 v139, v182, v183\n"
"v_mfma_f32_16x16x32_bf16 a[56:59], a[252:255], v[120:123], a[56:59]\n"
"ds_read_b64_tr_b16 a[226:227], v219 offset:9728\n"
"buffer_load_dwordx4 v246, s[12:15], s84 offen offset:0 lds\n"
"v_mfma_f32_16x16x32_bf16 a[60:63], a[248:251], v[116:119], a[60:63]\n"
"ds_read_b64_tr_b16 a[228:229], v219 offset:12800\n"
"v_fma_mix_f32 v184, v140, v160, v168 op_sel:[0,0,0] op_sel_hi:[1,0,0]\n"
"v_mfma_f32_16x16x32_bf16 a[60:63], a[252:255], v[124:127], a[60:63]\n"
"s_waitcnt lgkmcnt(11)\n"
"ds_read_b64_tr_b16 a[230:231], v219 offset:13824\n"
"v_fma_mix_f32 v185, v140, v161, v169 op_sel:[1,0,0] op_sel_hi:[1,0,0]\n"
"v_mfma_f32_16x16x32_bf16 a[64:67], a[160:163], v[112:115], a[64:67]\n"
"ds_read_b64_tr_b16 a[232:233], v219 offset:8712\n"
"v_fma_mix_f32 v186, v141, v162, v170 op_sel:[0,0,0] op_sel_hi:[1,0,0]\n"
"v_mfma_f32_16x16x32_bf16 a[64:67], a[164:167], v[120:123], a[64:67]\n"
"ds_read_b64_tr_b16 a[234:235], v219 offset:9736\n"
"v_fma_mix_f32 v187, v141, v163, v171 op_sel:[1,0,0] op_sel_hi:[1,0,0]\n"
"v_mfma_f32_16x16x32_bf16 a[68:71], a[160:163], v[116:119], a[68:71]\n"
"ds_read_b64_tr_b16 a[236:237], v219 offset:12808\n"
"v_fma_mix_f32 v188, v142, v164, v172 op_sel:[0,0,0] op_sel_hi:[1,0,0]\n"
"v_mfma_f32_16x16x32_bf16 a[68:71], a[164:167], v[124:127], a[68:71]\n"
"ds_read_b64_tr_b16 a[238:239], v219 offset:13832\n"
"buffer_load_dwordx4 v247, s[12:15], s84 offen offset:1024 lds\n"
"v_mfma_f32_16x16x32_bf16 a[72:75], a[168:171], v[112:115], a[72:75]\n"
"ds_read_b64_tr_b16 a[240:241], v219 offset:8960\n"
"v_fma_mix_f32 v189, v142, v165, v173 op_sel:[1,0,0] op_sel_hi:[1,0,0]\n"
"v_mfma_f32_16x16x32_bf16 a[72:75], a[172:175], v[120:123], a[72:75]\n"
"ds_read_b64_tr_b16 a[242:243], v219 offset:9984\n"
"v_fma_mix_f32 v190, v143, v166, v174 op_sel:[0,0,0] op_sel_hi:[1,0,0]\n"
"v_mfma_f32_16x16x32_bf16 a[76:79], a[168:171], v[116:119], a[76:79]\n"
"ds_read_b64_tr_b16 a[244:245], v219 offset:13056\n"
"v_fma_mix_f32 v191, v143, v167, v175 op_sel:[1,0,0] op_sel_hi:[1,0,0]\n"
"v_mfma_f32_16x16x32_bf16 a[76:79], a[172:175], v[124:127], a[76:79]\n"
"s_waitcnt lgkmcnt(11)\n"
"ds_read_b64_tr_b16 a[246:247], v219 offset:14080\n"
"v_cvt_pk_bf16_f32 v140, v184, v185\n"
"v_mfma_f32_16x16x32_bf16 a[80:83], a[176:179], v[112:115], a[80:83]\n"
"ds_read_b64_tr_b16 a[248:249], v219 offset:8968\n"
"v_cvt_pk_bf16_f32 v141, v186, v187\n"
"v_mfma_f32_16x16x32_bf16 a[80:83], a[180:183], v[120:123], a[80:83]\n"
"ds_read_b64_tr_b16 a[250:251], v219 offset:9992\n"
"buffer_load_dwordx4 v246, s[12:15], s84 offen offset:2048 lds\n"
"v_mfma_f32_16x16x32_bf16 a[84:87], a[176:179], v[116:119], a[84:87]\n"
"ds_read_b64_tr_b16 a[252:253], v219 offset:13064\n"
"v_cvt_pk_bf16_f32 v142, v188, v189\n"
"v_mfma_f32_16x16x32_bf16 a[84:87], a[180:183], v[124:127], a[84:87]\n"
"ds_read_b64_tr_b16 a[254:255], v219 offset:14088\n"
"v_cvt_pk_bf16_f32 v143, v190, v191\n"
"v_mfma_f32_16x16x32_bf16 a[88:91], a[184:187], v[112:115], a[88:91]\n"
"ds_read_b64_tr_b16 a[160:161], v219 offset:32784\n"
"v_mfma_f32_16x16x32_bf16 a[88:91], a[188:191], v[120:123], a[88:91]\n"
"ds_read_b64_tr_b16 a[162:163], v219 offset:33808\n"
"v_mfma_f32_16x16x32_bf16 a[92:95], a[184:187], v[116:119], a[92:95]\n"
"ds_read_b64_tr_b16 a[164:165], v219 offset:36880\n"
"v_mfma_f32_16x16x32_bf16 a[92:95], a[188:191], v[124:127], a[92:95]\n"
"s_waitcnt lgkmcnt(11)\n"
"buffer_load_dwordx4 v247, s[12:15], s84 offen offset:3072 lds\n"
"ds_read_b64_tr_b16 a[166:167], v219 offset:37904\n"
"v_mfma_f32_16x16x32_bf16 a[96:99], a[224:227], v[112:115], a[96:99]\n"
"s_add_u32 s84, s84, 16384\n"
"ds_read_b64_tr_b16 a[168:169], v219 offset:32792\n"
"v_mfma_f32_16x16x32_bf16 a[96:99], a[228:231], v[120:123], a[96:99]\n"
"ds_read_b64_tr_b16 a[170:171], v219 offset:33816\n"
"v_mfma_f32_16x16x32_bf16 a[100:103], a[224:227], v[116:119], a[100:103]\n"
"ds_read_b64_tr_b16 a[172:173], v219 offset:36888\n"
"v_mfma_f32_16x16x32_bf16 a[100:103], a[228:231], v[124:127], a[100:103]\n"
"ds_read_b64_tr_b16 a[174:175], v219 offset:37912\n"
"v_mfma_f32_16x16x32_bf16 a[104:107], a[232:235], v[112:115], a[104:107]\n"
"ds_read_b64_tr_b16 a[176:177], v219 offset:33040\n"
"v_mfma_f32_16x16x32_bf16 a[104:107], a[236:239], v[120:123], a[104:107]\n"
"ds_read_b64_tr_b16 a[178:179], v219 offset:34064\n"
"v_mfma_f32_16x16x32_bf16 a[108:111], a[232:235], v[116:119], a[108:111]\n"
"ds_read_b64_tr_b16 a[180:181], v219 offset:37136\n"
"v_mfma_f32_16x16x32_bf16 a[108:111], a[236:239], v[124:127], a[108:111]\n"
"s_waitcnt lgkmcnt(11)\n"
"ds_read_b64_tr_b16 a[182:183], v219 offset:38160\n"
"v_mfma_f32_16x16x32_bf16 a[112:115], a[240:243], v[112:115], a[112:115]\n"
"ds_read_b64_tr_b16 a[184:185], v219 offset:33048\n"
"v_mfma_f32_16x16x32_bf16 a[112:115], a[244:247], v[120:123], a[112:115]\n"
"ds_read_b64_tr_b16 a[186:187], v219 offset:34072\n"
"v_mfma_f32_16x16x32_bf16 a[116:119], a[240:243], v[116:119], a[116:119]\n"
"ds_read_b64_tr_b16 a[188:189], v219 offset:37144\n"
"v_mfma_f32_16x16x32_bf16 a[116:119], a[244:247], v[124:127], a[116:119]\n"
"ds_read_b64_tr_b16 a[190:191], v219 offset:38168\n"
"v_mfma_f32_16x16x32_bf16 a[120:123], a[248:251], v[112:115], a[120:123]\n"
"ds_read_b64_tr_b16 a[224:225], v219 offset:33296\n"
"v_mfma_f32_16x16x32_bf16 a[120:123], a[252:255], v[120:123], a[120:123]\n"
"ds_read_b64_tr_b16 a[226:227], v219 offset:34320\n"
"v_mfma_f32_16x16x32_bf16 a[124:127], a[248:251], v[116:119], a[124:127]\n"
"ds_read_b64_tr_b16 a[228:229], v219 offset:37392\n"
"v_mfma_f32_16x16x32_bf16 a[124:127], a[252:255], v[124:127], a[124:127]\n"
"s_waitcnt lgkmcnt(11)\n"
"ds_read_b64_tr_b16 a[230:231], v219 offset:38416\n"
"v_mfma_f32_16x16x32_bf16 a[0:3], a[160:163], v[128:131], a[0:3]\n"
"ds_read_b64_tr_b16 a[232:233], v219 offset:33304\n"
"v_mfma_f32_16x16x32_bf16 a[0:3], a[164:167], v[136:139], a[0:3]\n"
"ds_read_b64_tr_b16 a[234:235], v219 offset:34328\n"
"v_mfma_f32_16x16x32_bf16 a[4:7], a[160:163], v[132:135], a[4:7]\n"
"s_waitcnt vmcnt(8)\n"
"s_barrier\n"
"ds_read_b64_tr_b16 a[236:237], v219 offset:37400\n"
"v_mfma_f32_16x16x32_bf16 a[4:7], a[164:167], v[140:143], a[4:7]\n"
"ds_read_b64_tr_b16 a[238:239], v219 offset:38424\n"
"v_mfma_f32_16x16x32_bf16 a[8:11], a[168:171], v[128:131], a[8:11]\n"
"s_add_u32 m0, s86, 65536\n"
"ds_read_b64_tr_b16 a[240:241], v219 offset:33552\n"
"v_mfma_f32_16x16x32_bf16 a[8:11], a[172:175], v[136:139], a[8:11]\n"
"ds_read_b64_tr_b16 a[242:243], v219 offset:34576\n"
"buffer_load_dwordx4 v246, s[8:11], s84 offen offset:0 lds\n"
"v_mfma_f32_16x16x32_bf16 a[12:15], a[168:171], v[132:135], a[12:15]\n"
"ds_read_b64_tr_b16 a[244:245], v219 offset:37648\n"
"v_accvgpr_read_b32 v176, a0\n"
"v_mfma_f32_16x16x32_bf16 a[12:15], a[172:175], v[140:143], a[12:15]\n"
"s_waitcnt lgkmcnt(11)\n"
"ds_read_b64_tr_b16 a[246:247], v219 offset:38672\n"
"v_accvgpr_read_b32 v177, a1\n"
"v_mfma_f32_16x16x32_bf16 a[16:19], a[176:179], v[128:131], a[16:19]\n"
"ds_read_b64_tr_b16 a[248:249], v219 offset:33560\n"
"v_accvgpr_read_b32 v178, a2\n"
"v_mfma_f32_16x16x32_bf16 a[16:19], a[180:183], v[136:139], a[16:19]\n"
"ds_read_b64_tr_b16 a[250:251], v219 offset:34584\n"
"v_accvgpr_read_b32 v179, a3\n"
"v_mfma_f32_16x16x32_bf16 a[20:23], a[176:179], v[132:135], a[20:23]\n"
"ds_read_b64_tr_b16 a[252:253], v219 offset:37656\n"
"v_cvt_pk_bf16_f32 v64, v176, v177\n"
"v_mfma_f32_16x16x32_bf16 a[20:23], a[180:183], v[140:143], a[20:23]\n"
"ds_read_b64_tr_b16 a[254:255], v219 offset:38680\n"
"buffer_load_dwordx4 v246, s[8:11], s84 offen offset:1024 lds\n"
"v_mfma_f32_16x16x32_bf16 a[24:27], a[184:187], v[128:131], a[24:27]\n"
"ds_read_b64_tr_b16 a[160:161], v219 offset:40976\n"
"v_cvt_pk_bf16_f32 v65, v178, v179\n"
"v_mfma_f32_16x16x32_bf16 a[24:27], a[188:191], v[136:139], a[24:27]\n"
"ds_read_b64_tr_b16 a[162:163], v219 offset:42000\n"
"v_accvgpr_read_b32 v180, a4\n"
"v_mfma_f32_16x16x32_bf16 a[28:31], a[184:187], v[132:135], a[28:31]\n"
"ds_read_b64_tr_b16 a[164:165], v219 offset:45072\n"
"v_accvgpr_read_b32 v181, a5\n"
"v_mfma_f32_16x16x32_bf16 a[28:31], a[188:191], v[140:143], a[28:31]\n"
"s_waitcnt lgkmcnt(11)\n"
"ds_read_b64_tr_b16 a[166:167], v219 offset:46096\n"
"v_accvgpr_read_b32 v182, a6\n"
"v_mfma_f32_16x16x32_bf16 a[32:35], a[224:227], v[128:131], a[32:35]\n"
"ds_read_b64_tr_b16 a[168:169], v219 offset:40984\n"
"v_accvgpr_read_b32 v183, a7\n"
"v_mfma_f32_16x16x32_bf16 a[32:35], a[228:231], v[136:139], a[32:35]\n"
"ds_read_b64_tr_b16 a[170:171], v219 offset:42008\n"
"buffer_load_dwordx4 v247, s[8:11], s84 offen offset:2048 lds\n"
"v_mfma_f32_16x16x32_bf16 a[36:39], a[224:227], v[132:135], a[36:39]\n"
"ds_read_b64_tr_b16 a[172:173], v219 offset:45080\n"
"v_cvt_pk_bf16_f32 v96, v180, v181\n"
"v_mfma_f32_16x16x32_bf16 a[36:39], a[228:231], v[140:143], a[36:39]\n"
"ds_read_b64_tr_b16 a[174:175], v219 offset:46104\n"
"v_cvt_pk_bf16_f32 v97, v182, v183\n"
"v_mfma_f32_16x16x32_bf16 a[40:43], a[232:235], v[128:131], a[40:43]\n"
"ds_read_b64_tr_b16 a[176:177], v219 offset:41232\n"
"v_accvgpr_read_b32 v184, a8\n"
"v_mfma_f32_16x16x32_bf16 a[40:43], a[236:239], v[136:139], a[40:43]\n"
"ds_read_b64_tr_b16 a[178:179], v219 offset:42256\n"
"v_accvgpr_read_b32 v185, a9\n"
"v_mfma_f32_16x16x32_bf16 a[44:47], a[232:235], v[132:135], a[44:47]\n"
"ds_read_b64_tr_b16 a[180:181], v219 offset:45328\n"
"v_accvgpr_read_b32 v186, a10\n"
"v_mfma_f32_16x16x32_bf16 a[44:47], a[236:239], v[140:143], a[44:47]\n"
"s_waitcnt lgkmcnt(11)\n"
"ds_read_b64_tr_b16 a[182:183], v219 offset:46352\n"
"buffer_load_dwordx4 v247, s[8:11], s84 offen offset:3072 lds\n"
"v_mfma_f32_16x16x32_bf16 a[48:51], a[240:243], v[128:131], a[48:51]\n"
"ds_read_b64_tr_b16 a[184:185], v219 offset:41240\n"
"v_accvgpr_read_b32 v187, a11\n"
"v_mfma_f32_16x16x32_bf16 a[48:51], a[244:247], v[136:139], a[48:51]\n"
"ds_read_b64_tr_b16 a[186:187], v219 offset:42264\n"
"v_cvt_pk_bf16_f32 v66, v184, v185\n"
"v_mfma_f32_16x16x32_bf16 a[52:55], a[240:243], v[132:135], a[52:55]\n"
"ds_read_b64_tr_b16 a[188:189], v219 offset:45336\n"
"v_cvt_pk_bf16_f32 v67, v186, v187\n"
"v_mfma_f32_16x16x32_bf16 a[52:55], a[244:247], v[140:143], a[52:55]\n"
"ds_read_b64_tr_b16 a[190:191], v219 offset:46360\n"
"v_accvgpr_read_b32 v188, a12\n"
"v_mfma_f32_16x16x32_bf16 a[56:59], a[248:251], v[128:131], a[56:59]\n"
"s_add_u32 m0, s86, 81920\n"
"ds_read_b64_tr_b16 a[224:225], v219 offset:41488\n"
"v_accvgpr_read_b32 v189, a13\n"
"v_mfma_f32_16x16x32_bf16 a[56:59], a[252:255], v[136:139], a[56:59]\n"
"ds_read_b64_tr_b16 a[226:227], v219 offset:42512\n"
"buffer_load_dwordx4 v246, s[12:15], s84 offen offset:0 lds\n"
"v_mfma_f32_16x16x32_bf16 a[60:63], a[248:251], v[132:135], a[60:63]\n"
"ds_read_b64_tr_b16 a[228:229], v219 offset:45584\n"
"v_accvgpr_read_b32 v190, a14\n"
"v_mfma_f32_16x16x32_bf16 a[60:63], a[252:255], v[140:143], a[60:63]\n"
"s_waitcnt lgkmcnt(11)\n"
"ds_read_b64_tr_b16 a[230:231], v219 offset:46608\n"
"v_accvgpr_read_b32 v191, a15\n"
"v_mfma_f32_16x16x32_bf16 a[64:67], a[160:163], v[128:131], a[64:67]\n"
"ds_read_b64_tr_b16 a[232:233], v219 offset:41496\n"
"v_cvt_pk_bf16_f32 v98, v188, v189\n"
"v_mfma_f32_16x16x32_bf16 a[64:67], a[164:167], v[136:139], a[64:67]\n"
"ds_read_b64_tr_b16 a[234:235], v219 offset:42520\n"
"v_cvt_pk_bf16_f32 v99, v190, v191\n"
"v_mfma_f32_16x16x32_bf16 a[68:71], a[160:163], v[132:135], a[68:71]\n"
"ds_read_b64_tr_b16 a[236:237], v219 offset:45592\n"
"v_accvgpr_read_b32 v176, a16\n"
"v_mfma_f32_16x16x32_bf16 a[68:71], a[164:167], v[140:143], a[68:71]\n"
"ds_read_b64_tr_b16 a[238:239], v219 offset:46616\n"
"buffer_load_dwordx4 v247, s[12:15], s84 offen offset:1024 lds\n"
"v_mfma_f32_16x16x32_bf16 a[72:75], a[168:171], v[128:131], a[72:75]\n"
"ds_read_b64_tr_b16 a[240:241], v219 offset:41744\n"
"v_accvgpr_read_b32 v177, a17\n"
"v_mfma_f32_16x16x32_bf16 a[72:75], a[172:175], v[136:139], a[72:75]\n"
"ds_read_b64_tr_b16 a[242:243], v219 offset:42768\n"
"v_accvgpr_read_b32 v178, a18\n"
"v_mfma_f32_16x16x32_bf16 a[76:79], a[168:171], v[132:135], a[76:79]\n"
"ds_read_b64_tr_b16 a[244:245], v219 offset:45840\n"
"v_accvgpr_read_b32 v179, a19\n"
"v_mfma_f32_16x16x32_bf16 a[76:79], a[172:175], v[140:143], a[76:79]\n"
"s_waitcnt lgkmcnt(11)\n"
"ds_read_b64_tr_b16 a[246:247], v219 offset:46864\n"
"v_cvt_pk_bf16_f32 v68, v176, v177\n"
"v_mfma_f32_16x16x32_bf16 a[80:83], a[176:179], v[128:131], a[80:83]\n"
"ds_read_b64_tr_b16 a[248:249], v219 offset:41752\n"
"v_cvt_pk_bf16_f32 v69, v178, v179\n"
"v_mfma_f32_16x16x32_bf16 a[80:83], a[180:183], v[136:139], a[80:83]\n"
"ds_read_b64_tr_b16 a[250:251], v219 offset:42776\n"
"buffer_load_dwordx4 v246, s[12:15], s84 offen offset:2048 lds\n"
"v_mfma_f32_16x16x32_bf16 a[84:87], a[176:179], v[132:135], a[84:87]\n"
"ds_read_b64_tr_b16 a[252:253], v219 offset:45848\n"
"v_accvgpr_read_b32 v180, a20\n"
"v_mfma_f32_16x16x32_bf16 a[84:87], a[180:183], v[140:143], a[84:87]\n"
"ds_read_b64_tr_b16 a[254:255], v219 offset:46872\n"
"v_accvgpr_read_b32 v181, a21\n"
"v_mfma_f32_16x16x32_bf16 a[88:91], a[184:187], v[128:131], a[88:91]\n"
"ds_read_b128 a[160:163], v254 offset:0\n"
"v_accvgpr_read_b32 v182, a22\n"
"v_mfma_f32_16x16x32_bf16 a[88:91], a[188:191], v[136:139], a[88:91]\n"
"ds_read_b128 a[164:167], v254 offset:256\n"
"v_accvgpr_read_b32 v183, a23\n"
"v_mfma_f32_16x16x32_bf16 a[92:95], a[184:187], v[132:135], a[92:95]\n"
"ds_read_b128 a[168:171], v254 offset:512\n"
"v_cvt_pk_bf16_f32 v100, v180, v181\n"
"v_mfma_f32_16x16x32_bf16 a[92:95], a[188:191], v[140:143], a[92:95]\n"
"s_waitcnt lgkmcnt(11)\n"
"buffer_load_dwordx4 v247, s[12:15], s84 offen offset:3072 lds\n"
"ds_read_b128 a[172:175], v254 offset:768\n"
"v_mfma_f32_16x16x32_bf16 a[96:99], a[224:227], v[128:131], a[96:99]\n"
"s_add_u32 s84, s84, 16384\n"
"ds_read_b128 a[128:131], v240 offset:0\n"
"v_cvt_pk_bf16_f32 v101, v182, v183\n"
"v_mfma_f32_16x16x32_bf16 a[96:99], a[228:231], v[136:139], a[96:99]\n"
"ds_read_b128 a[132:135], v240 offset:256\n"
"v_accvgpr_read_b32 v184, a24\n"
"v_mfma_f32_16x16x32_bf16 a[100:103], a[224:227], v[132:135], a[100:103]\n"
"ds_read_b128 a[136:139], v240 offset:512\n"
"v_accvgpr_read_b32 v185, a25\n"
"v_mfma_f32_16x16x32_bf16 a[100:103], a[228:231], v[140:143], a[100:103]\n"
"ds_read_b128 a[140:143], v240 offset:768\n"
"v_accvgpr_read_b32 v186, a26\n"
"v_mfma_f32_16x16x32_bf16 a[104:107], a[232:235], v[128:131], a[104:107]\n"
"ds_read_b128 a[144:147], v240 offset:4096\n"
"v_accvgpr_read_b32 v187, a27\n"
"v_mfma_f32_16x16x32_bf16 a[104:107], a[236:239], v[136:139], a[104:107]\n"
"ds_read_b128 a[148:151], v240 offset:4352\n"
"v_cvt_pk_bf16_f32 v70, v184, v185\n"
"v_mfma_f32_16x16x32_bf16 a[108:111], a[232:235], v[132:135], a[108:111]\n"
"ds_read_b128 a[152:155], v240 offset:4608\n"
"v_cvt_pk_bf16_f32 v71, v186, v187\n"
"v_mfma_f32_16x16x32_bf16 a[108:111], a[236:239], v[140:143], a[108:111]\n"
"s_waitcnt lgkmcnt(11)\n"
"ds_read_b128 a[156:159], v240 offset:4864\n"
"v_accvgpr_read_b32 v188, a28\n"
"v_mfma_f32_16x16x32_bf16 a[112:115], a[240:243], v[128:131], a[112:115]\n"
"ds_read_b64_tr_b16 a[192:193], v242 offset:16384\n"
"v_accvgpr_read_b32 v189, a29\n"
"v_mfma_f32_16x16x32_bf16 a[112:115], a[244:247], v[136:139], a[112:115]\n"
"ds_read_b64_tr_b16 a[194:195], v242 offset:20480\n"
"v_accvgpr_read_b32 v190, a30\n"
"v_mfma_f32_16x16x32_bf16 a[116:119], a[240:243], v[132:135], a[116:119]\n"
"ds_read_b64_tr_b16 a[196:197], v243 offset:16384\n"
"v_accvgpr_read_b32 v191, a31\n"
"v_mfma_f32_16x16x32_bf16 a[116:119], a[244:247], v[140:143], a[116:119]\n"
"ds_read_b64_tr_b16 a[198:199], v243 offset:20480\n"
"v_cvt_pk_bf16_f32 v102, v188, v189\n"
"v_mfma_f32_16x16x32_bf16 a[120:123], a[248:251], v[128:131], a[120:123]\n"
"ds_read_b64_tr_b16 a[200:201], v242 offset:16640\n"
"v_cvt_pk_bf16_f32 v103, v190, v191\n"
"v_mfma_f32_16x16x32_bf16 a[120:123], a[252:255], v[136:139], a[120:123]\n"
"ds_read_b64_tr_b16 a[202:203], v242 offset:20736\n"
"v_accvgpr_read_b32 v176, a32\n"
"v_mfma_f32_16x16x32_bf16 a[124:127], a[248:251], v[132:135], a[124:127]\n"
"ds_read_b64_tr_b16 a[204:205], v243 offset:16640\n"
"v_accvgpr_read_b32 v177, a33\n"
"v_mfma_f32_16x16x32_bf16 a[124:127], a[252:255], v[140:143], a[124:127]\n"
"ds_read_b64_tr_b16 a[206:207], v243 offset:20736\n"
"v_accvgpr_read_b32 v178, a34\n"
"s_barrier\n"
"ds_read_b64_tr_b16 a[208:209], v242 offset:16896\n"
"v_accvgpr_read_b32 v179, a35\n"
"v_cvt_pk_bf16_f32 v72, v176, v177\n"
"v_cvt_pk_bf16_f32 v73, v178, v179\n"
"ds_read_b64_tr_b16 a[210:211], v242 offset:20992\n"
"v_accvgpr_read_b32 v180, a36\n"
"v_accvgpr_read_b32 v181, a37\n"
"v_accvgpr_read_b32 v182, a38\n"
"ds_read_b64_tr_b16 a[212:213], v243 offset:16896\n"
"v_accvgpr_read_b32 v183, a39\n"
"v_cvt_pk_bf16_f32 v104, v180, v181\n"
"v_cvt_pk_bf16_f32 v105, v182, v183\n"
"ds_read_b64_tr_b16 a[214:215], v243 offset:20992\n"
"v_accvgpr_read_b32 v184, a40\n"
"v_accvgpr_read_b32 v185, a41\n"
"v_accvgpr_read_b32 v186, a42\n"
"ds_read_b64_tr_b16 a[216:217], v242 offset:17152\n"
"v_accvgpr_read_b32 v187, a43\n"
"v_cvt_pk_bf16_f32 v74, v184, v185\n"
"v_cvt_pk_bf16_f32 v75, v186, v187\n"
"ds_read_b64_tr_b16 a[218:219], v242 offset:21248\n"
"v_accvgpr_read_b32 v188, a44\n"
"v_accvgpr_read_b32 v189, a45\n"
"v_accvgpr_read_b32 v190, a46\n"
"ds_read_b64_tr_b16 a[220:221], v243 offset:17152\n"
"v_accvgpr_read_b32 v191, a47\n"
"v_cvt_pk_bf16_f32 v106, v188, v189\n"
"v_cvt_pk_bf16_f32 v107, v190, v191\n"
"ds_read_b64_tr_b16 a[222:223], v243 offset:21248\n"
"v_accvgpr_read_b32 v176, a48\n"
"v_accvgpr_read_b32 v177, a49\n"
"v_accvgpr_read_b32 v178, a50\n"
"v_accvgpr_read_b32 v179, a51\n"
"v_cvt_pk_bf16_f32 v76, v176, v177\n"
"v_cvt_pk_bf16_f32 v77, v178, v179\n"
"v_accvgpr_read_b32 v180, a52\n"
"s_add_u32 m0, s86, 98304\n"
"v_accvgpr_read_b32 v181, a53\n"
"v_accvgpr_read_b32 v182, a54\n"
"v_accvgpr_read_b32 v183, a55\n"
"v_cvt_pk_bf16_f32 v108, v180, v181\n"
"buffer_load_dwordx4 v246, s[8:11], s84 offen offset:0 lds\n"
"v_cvt_pk_bf16_f32 v109, v182, v183\n"
"v_accvgpr_read_b32 v184, a56\n"
"v_accvgpr_read_b32 v185, a57\n"
"v_accvgpr_read_b32 v186, a58\n"
"v_accvgpr_read_b32 v187, a59\n"
"v_cvt_pk_bf16_f32 v78, v184, v185\n"
"v_cvt_pk_bf16_f32 v79, v186, v187\n"
"v_accvgpr_read_b32 v188, a60\n"
"v_accvgpr_read_b32 v189, a61\n"
"v_accvgpr_read_b32 v190, a62\n"
"v_accvgpr_read_b32 v191, a63\n"
"buffer_load_dwordx4 v246, s[8:11], s84 offen offset:1024 lds\n"
"v_cvt_pk_bf16_f32 v110, v188, v189\n"
"v_cvt_pk_bf16_f32 v111, v190, v191\n"
"v_accvgpr_read_b32 v176, a64\n"
"s_nop 1\n"
"v_accvgpr_read_b32 v177, a65\n"
"v_accvgpr_read_b32 v178, a66\n"
"v_accvgpr_read_b32 v179, a67\n"
"v_cvt_pk_bf16_f32 v80, v176, v177\n"
"s_waitcnt lgkmcnt(14)\n"
"v_mfma_f32_16x16x32_bf16 v[144:147], a[160:163], v[64:67], 0\n"
"v_cvt_pk_bf16_f32 v81, v178, v179\n"
"v_accvgpr_read_b32 v180, a68\n"
"v_mfma_f32_16x16x32_bf16 v[144:147], a[164:167], v[68:71], v[144:147]\n"
"buffer_load_dwordx4 v247, s[8:11], s84 offen offset:2048 lds\n"
"v_accvgpr_read_b32 v181, a69\n"
"v_mfma_f32_16x16x32_bf16 v[144:147], a[168:171], v[72:75], v[144:147]\n"
"v_accvgpr_read_b32 v182, a70\n"
"v_accvgpr_read_b32 v183, a71\n"
"v_mfma_f32_16x16x32_bf16 v[144:147], a[172:175], v[76:79], v[144:147]\n"
"v_cvt_pk_bf16_f32 v112, v180, v181\n"
"v_cvt_pk_bf16_f32 v113, v182, v183\n"
"v_mfma_f32_16x16x32_bf16 v[152:155], a[160:163], v[96:99], 0\n"
"buffer_load_dwordx4 v247, s[8:11], s84 offen offset:3072 lds\n"
"v_accvgpr_read_b32 v184, a72\n"
"v_mfma_f32_16x16x32_bf16 v[152:155], a[164:167], v[100:103], v[152:155]\n"
"v_accvgpr_read_b32 v185, a73\n"
"v_accvgpr_read_b32 v186, a74\n"
"v_mfma_f32_16x16x32_bf16 v[152:155], a[168:171], v[104:107], v[152:155]\n"
"s_add_u32 m0, s86, 114688\n"
"v_accvgpr_read_b32 v187, a75\n"
"v_cvt_pk_bf16_f32 v82, v184, v185\n"
"v_mfma_f32_16x16x32_bf16 v[152:155], a[172:175], v[108:111], v[152:155]\n"
"buffer_load_dwordx4 v246, s[12:15], s84 offen offset:0 lds\n"
"v_cvt_pk_bf16_f32 v83, v186, v187\n"
"v_accvgpr_read_b32 v188, a76\n"
"v_accvgpr_read_b32 v189, a77\n"
"v_accvgpr_read_b32 v190, a78\n"
"v_accvgpr_read_b32 v191, a79\n"
"v_cvt_pk_bf16_f32 v114, v188, v189\n"
"v_cvt_pk_bf16_f32 v115, v190, v191\n"
"v_accvgpr_read_b32 v176, a80\n"
"v_accvgpr_read_b32 v177, a81\n"
"buffer_load_dwordx4 v247, s[12:15], s84 offen offset:1024 lds\n"
"v_accvgpr_read_b32 v178, a82\n"
"v_accvgpr_read_b32 v179, a83\n"
"v_cvt_pk_bf16_f32 v84, v176, v177\n"
"v_cvt_pk_bf16_f32 v85, v178, v179\n"
"v_accvgpr_read_b32 v180, a84\n"
"v_accvgpr_read_b32 v181, a85\n"
"v_accvgpr_read_b32 v182, a86\n"
"v_accvgpr_read_b32 v183, a87\n"
"v_cvt_pk_bf16_f32 v116, v180, v181\n"
"v_cvt_pk_bf16_f32 v117, v182, v183\n"
"v_accvgpr_read_b32 v184, a88\n"
"buffer_load_dwordx4 v246, s[12:15], s84 offen offset:2048 lds\n"
"v_accvgpr_read_b32 v185, a89\n"
"v_accvgpr_read_b32 v186, a90\n"
"v_accvgpr_read_b32 v187, a91\n"
"v_cvt_pk_bf16_f32 v86, v184, v185\n"
"v_cvt_pk_bf16_f32 v87, v186, v187\n"
"v_accvgpr_read_b32 v188, a92\n"
"v_accvgpr_read_b32 v189, a93\n"
"v_accvgpr_read_b32 v190, a94\n"
"v_accvgpr_read_b32 v191, a95\n"
"v_cvt_pk_bf16_f32 v118, v188, v189\n"
"v_cvt_pk_bf16_f32 v119, v190, v191\n"
"buffer_load_dwordx4 v247, s[12:15], s84 offen offset:3072 lds\n"
"v_max3_f32 v144, v144, v145, v146\n"
"v_max_f32 v144, v144, v147\n"
"v_sub_f32 v24, 0xc2480000, v144\n"
"s_add_u32 s84, s84, 16384\n"
"v_sub_f32 v25, 0xc2480000, v144\n"
"v_sub_f32 v26, 0xc2480000, v144\n"
"v_sub_f32 v27, 0xc2480000, v144\n"
"v_accvgpr_read_b32 v176, a96\n"
"v_accvgpr_read_b32 v177, a97\n"
"v_accvgpr_read_b32 v178, a98\n"
"v_accvgpr_read_b32 v179, a99\n"
"v_cvt_pk_bf16_f32 v88, v176, v177\n"
"v_cvt_pk_bf16_f32 v89, v178, v179\n"
"v_accvgpr_read_b32 v180, a100\n"
"v_accvgpr_read_b32 v181, a101\n"
"v_accvgpr_read_b32 v182, a102\n"
"v_accvgpr_read_b32 v183, a103\n"
"v_cvt_pk_bf16_f32 v120, v180, v181\n"
"v_cvt_pk_bf16_f32 v121, v182, v183\n"
"v_accvgpr_read_b32 v184, a104\n"
"v_accvgpr_read_b32 v185, a105\n"
"v_accvgpr_read_b32 v186, a106\n"
"v_accvgpr_read_b32 v187, a107\n"
"v_cvt_pk_bf16_f32 v90, v184, v185\n"
"v_cvt_pk_bf16_f32 v91, v186, v187\n"
"v_accvgpr_read_b32 v188, a108\n"
"v_accvgpr_read_b32 v189, a109\n"
"v_accvgpr_read_b32 v190, a110\n"
"v_accvgpr_read_b32 v191, a111\n"
"v_cvt_pk_bf16_f32 v122, v188, v189\n"
"v_cvt_pk_bf16_f32 v123, v190, v191\n"
"v_accvgpr_read_b32 v176, a112\n"
"v_accvgpr_read_b32 v177, a113\n"
"v_accvgpr_read_b32 v178, a114\n"
"v_accvgpr_read_b32 v179, a115\n"
"v_cvt_pk_bf16_f32 v92, v176, v177\n"
"v_cvt_pk_bf16_f32 v93, v178, v179\n"
"v_accvgpr_read_b32 v180, a116\n"
"v_accvgpr_read_b32 v181, a117\n"
"v_accvgpr_read_b32 v182, a118\n"
"v_accvgpr_read_b32 v183, a119\n"
"v_cvt_pk_bf16_f32 v124, v180, v181\n"
"v_cvt_pk_bf16_f32 v125, v182, v183\n"
"v_accvgpr_read_b32 v184, a120\n"
"v_accvgpr_read_b32 v185, a121\n"
"v_accvgpr_read_b32 v186, a122\n"
"v_accvgpr_read_b32 v187, a123\n"
"v_cvt_pk_bf16_f32 v94, v184, v185\n"
"v_cvt_pk_bf16_f32 v95, v186, v187\n"
"v_accvgpr_read_b32 v188, a124\n"
"v_accvgpr_read_b32 v189, a125\n"
"v_accvgpr_read_b32 v190, a126\n"
"v_accvgpr_read_b32 v191, a127\n"
"v_cvt_pk_bf16_f32 v126, v188, v189\n"
"v_cvt_pk_bf16_f32 v127, v190, v191\n"
"s_nop 1\n"
"v_mfma_f32_16x16x32_bf16 v[148:151], a[160:163], v[80:83], 0\n"
"v_max3_f32 v152, v152, v153, v154\n"
"v_max_f32 v152, v152, v155\n"
"v_mfma_f32_16x16x32_bf16 v[148:151], a[164:167], v[84:87], v[148:151]\n"
"v_sub_f32 v32, 0xc2480000, v152\n"
"v_sub_f32 v33, 0xc2480000, v152\n"
"v_mfma_f32_16x16x32_bf16 v[148:151], a[168:171], v[88:91], v[148:151]\n"
"v_sub_f32 v34, 0xc2480000, v152\n"
"v_sub_f32 v35, 0xc2480000, v152\n"
"v_mfma_f32_16x16x32_bf16 v[148:151], a[172:175], v[92:95], v[148:151]\n"
"v_mov_b32 v40, 0x3f803f80\n"
"v_mov_b32 v41, 0x3f803f80\n"
"v_mfma_f32_16x16x32_bf16 v[156:159], a[160:163], v[112:115], 0\n"
"v_mov_b32 v42, 0x3f803f80\n"
"v_mov_b32 v43, 0x3f803f80\n"
"v_mfma_f32_16x16x32_bf16 v[156:159], a[164:167], v[116:119], v[156:159]\n"
"v_mov_b32 v20, 0\n"
"v_mov_b32 v21, 0\n"
"v_mfma_f32_16x16x32_bf16 v[156:159], a[168:171], v[120:123], v[156:159]\n"
"v_mov_b32 v22, 0\n"
"v_mov_b32 v23, 0\n"
"v_mfma_f32_16x16x32_bf16 v[156:159], a[172:175], v[124:127], v[156:159]\n"
"v_mov_b32 v44, 0\n"
"v_mov_b32 v52, 0\n"
"v_mov_b32 v216, -1\n"
"v_mov_b32 v217, -1\n"
"v_mov_b32 v218, -1\n"
"v_mov_b32 v219, -1\n"
"v_lshl_add_u32 v255, v248, 6, s48\n"
"v_lshlrev_b32 v207, 4, v248\n"
"v_add_u32 v208, 18432, v207\n"
"s_waitcnt lgkmcnt(0)\n"
"AT_main:\n"
"s_or_b32 s56, s42, s41\n"
"s_cmp_lg_u32 s56, 0\n"
"s_cbranch_scc1 AT_h1\n"
"v_lshlrev_b32 v214, 7, v248\n"
"s_mov_b32 s57, 32768\n"
"s_mov_b32 s58, 40960\n"
"s_mov_b32 s59, 49152\n"
"s_mov_b32 s60, 57344\n"
"buffer_load_dword v210, v214, s[28:31], s57 offen offset:4 sc1\n"
"buffer_load_dword v211, v214, s[28:31], s58 offen offset:4 sc1\n"
"buffer_load_dword v212, v214, s[28:31], s59 offen offset:4 sc1\n"
"buffer_load_dword v213, v214, s[28:31], s60 offen offset:4 sc1\n"
"AT_h1:\n"
"v_mfma_f32_16x16x32_bf16 v[0:3], a[128:131], v[64:67], v[24:27]\n"
"s_nop 0\n"
"ds_read_b128 a[160:163], v240 offset:8192\n"
"v_mfma_f32_16x16x32_bf16 v[0:3], a[132:135], v[68:71], v[0:3]\n"
"s_nop 0\n"
"v_mfma_f32_16x16x32_bf16 v[0:3], a[136:139], v[72:75], v[0:3]\n"
"s_nop 0\n"
"ds_read_b128 a[164:167], v240 offset:8448\n"
"v_mfma_f32_16x16x32_bf16 v[0:3], a[140:143], v[76:79], v[0:3]\n"
"v_max3_f32 v148, v148, v149, v150\n"
"v_max_f32 v148, v148, v151\n"
"v_mfma_f32_16x16x32_bf16 v[4:7], a[144:147], v[64:67], v[24:27]\n"
"ds_read_b128 a[168:171], v240 offset:8704\n"
"v_sub_f32 v28, 0xc2480000, v148\n"
"v_mfma_f32_16x16x32_bf16 v[4:7], a[148:151], v[68:71], v[4:7]\n"
"v_sub_f32 v29, 0xc2480000, v148\n"
"v_sub_f32 v30, 0xc2480000, v148\n"
"v_mfma_f32_16x16x32_bf16 v[4:7], a[152:155], v[72:75], v[4:7]\n"
"ds_read_b128 a[172:175], v240 offset:8960\n"
"v_sub_f32 v31, 0xc2480000, v148\n"
"v_mfma_f32_16x16x32_bf16 v[4:7], a[156:159], v[76:79], v[4:7]\n"
"s_nop 0\n"
"v_exp_f32 v0, v0\n"
"v_mfma_f32_16x16x32_bf16 a[96:99], v[40:43], v[20:23], 0\n"
"v_max3_f32 v156, v156, v157, v158\n"
"v_max_f32 v156, v156, v159\n"
"v_mfma_f32_16x16x32_bf16 a[100:103], v[40:43], v[20:23], 0\n"
"s_nop 0\n"
"v_exp_f32 v1, v1\n"
"v_mfma_f32_16x16x32_bf16 a[104:107], v[40:43], v[20:23], 0\n"
"ds_read_b128 a[176:179], v240 offset:12288\n"
"v_cvt_pk_bf16_f32 v16, v0, v1\n"
"v_mfma_f32_16x16x32_bf16 a[108:111], v[40:43], v[20:23], 0\n"
"s_nop 0\n"
"v_exp_f32 v2, v2\n"
"v_mfma_f32_16x16x32_bf16 a[112:115], v[40:43], v[20:23], 0\n"
"ds_read_b128 a[180:183], v240 offset:12544\n"
"v_add_f32 v44, v44, v0\n"
"v_mfma_f32_16x16x32_bf16 a[116:119], v[40:43], v[20:23], 0\n"
"s_nop 0\n"
"v_exp_f32 v3, v3\n"
"v_mfma_f32_16x16x32_bf16 a[120:123], v[40:43], v[20:23], 0\n"
"ds_read_b128 a[184:187], v240 offset:12800\n"
"v_cvt_pk_bf16_f32 v17, v2, v3\n"
"v_mfma_f32_16x16x32_bf16 a[124:127], v[40:43], v[20:23], 0\n"
"s_nop 0\n"
"v_exp_f32 v4, v4\n"
"v_mfma_f32_16x16x32_bf16 v[56:59], v[40:43], v[20:23], 0\n"
"ds_read_b128 a[188:191], v240 offset:13056\n"
"v_add_f32 v44, v44, v1\n"
"v_mfma_f32_16x16x32_bf16 v[8:11], a[128:131], v[80:83], v[28:31]\n"
"s_nop 0\n"
"v_exp_f32 v5, v5\n"
"v_mfma_f32_16x16x32_bf16 v[8:11], a[132:135], v[84:87], v[8:11]\n"
"ds_read_b64_tr_b16 a[224:225], v242 offset:24576\n"
"v_cvt_pk_bf16_f32 v18, v4, v5\n"
"v_mfma_f32_16x16x32_bf16 v[8:11], a[136:139], v[88:91], v[8:11]\n"
"s_nop 0\n"
"v_exp_f32 v6, v6\n"
"v_mfma_f32_16x16x32_bf16 v[8:11], a[140:143], v[92:95], v[8:11]\n"
"ds_read_b64_tr_b16 a[226:227], v242 offset:28672\n"
"v_add_f32 v44, v44, v2\n"
"v_mfma_f32_16x16x32_bf16 v[12:15], a[144:147], v[80:83], v[28:31]\n"
"s_nop 0\n"
"v_exp_f32 v7, v7\n"
"v_mfma_f32_16x16x32_bf16 v[12:15], a[148:151], v[84:87], v[12:15]\n"
"ds_read_b64_tr_b16 a[228:229], v243 offset:24576\n"
"v_cvt_pk_bf16_f32 v19, v6, v7\n"
"v_mfma_f32_16x16x32_bf16 v[12:15], a[152:155], v[88:91], v[12:15]\n"
"v_sub_f32 v36, 0xc2480000, v156\n"
"v_sub_f32 v37, 0xc2480000, v156\n"
"v_mfma_f32_16x16x32_bf16 v[12:15], a[156:159], v[92:95], v[12:15]\n"
"s_nop 0\n"
"v_exp_f32 v8, v8\n"
"v_mfma_f32_16x16x32_bf16 a[0:3], a[192:195], v[16:19], 0\n"
"ds_read_b64_tr_b16 a[230:231], v243 offset:28672\n"
"v_add_f32 v44, v44, v3\n"
"v_mfma_f32_16x16x32_bf16 a[4:7], a[196:199], v[16:19], 0\n"
"s_nop 0\n"
"v_exp_f32 v9, v9\n"
"v_mfma_f32_16x16x32_bf16 a[8:11], a[200:203], v[16:19], 0\n"
"ds_read_b64_tr_b16 a[232:233], v242 offset:24832\n"
"v_cvt_pk_bf16_f32 v20, v8, v9\n"
"v_mfma_f32_16x16x32_bf16 a[12:15], a[204:207], v[16:19], 0\n"
"s_nop 0\n"
"v_exp_f32 v10, v10\n"
"v_mfma_f32_16x16x32_bf16 a[16:19], a[208:211], v[16:19], 0\n"
"ds_read_b64_tr_b16 a[234:235], v242 offset:28928\n"
"v_add_f32 v44, v44, v4\n"
"v_mfma_f32_16x16x32_bf16 a[20:23], a[212:215], v[16:19], 0\n"
"s_nop 0\n"
"v_exp_f32 v11, v11\n"
"v_mfma_f32_16x16x32_bf16 a[24:27], a[216:219], v[16:19], 0\n"
"ds_read_b64_tr_b16 a[236:237], v243 offset:24832\n"
"v_cvt_pk_bf16_f32 v21, v10, v11\n"
"v_mfma_f32_16x16x32_bf16 a[28:31], a[220:223], v[16:19], 0\n"
"s_nop 0\n"
"v_exp_f32 v12, v12\n"
"v_mfma_f32_16x16x32_bf16 v[220:223], a[128:131], v[96:99], v[32:35]\n"
"ds_read_b64_tr_b16 a[238:239], v243 offset:28928\n"
"v_add_f32 v44, v44, v5\n"
"v_mfma_f32_16x16x32_bf16 v[220:223], a[132:135], v[100:103], v[220:223]\n"
"s_nop 0\n"
"v_exp_f32 v13, v13\n"
"v_mfma_f32_16x16x32_bf16 v[220:223], a[136:139], v[104:107], v[220:223]\n"
"ds_read_b64_tr_b16 a[240:241], v242 offset:25088\n"
"v_cvt_pk_bf16_f32 v22, v12, v13\n"
"v_mfma_f32_16x16x32_bf16 v[220:223], a[140:143], v[108:111], v[220:223]\n"
"s_nop 0\n"
"v_exp_f32 v14, v14\n"
"v_mfma_f32_16x16x32_bf16 v[224:227], a[144:147], v[96:99], v[32:35]\n"
"ds_read_b64_tr_b16 a[242:243], v242 offset:29184\n"
"v_add_f32 v44, v44, v6\n"
"v_mfma_f32_16x16x32_bf16 v[224:227], a[148:151], v[100:103], v[224:227]\n"
"s_nop 0\n"
"v_exp_f32 v15, v15\n"
"v_mfma_f32_16x16x32_bf16 v[224:227], a[152:155], v[104:107], v[224:227]\n"
"ds_read_b64_tr_b16 a[244:245], v243 offset:25088\n"
"v_cvt_pk_bf16_f32 v23, v14, v15\n"
"v_mfma_f32_16x16x32_bf16 v[224:227], a[156:159], v[108:111], v[224:227]\n"
"s_nop 0\n"
"v_exp_f32 v220, v220\n"
"v_mfma_f32_16x16x32_bf16 a[32:35], a[192:195], v[20:23], 0\n"
"v_sub_f32 v38, 0xc2480000, v156\n"
"v_sub_f32 v39, 0xc2480000, v156\n"
"v_mfma_f32_16x16x32_bf16 a[36:39], a[196:199], v[20:23], 0\n"
"s_nop 0\n"
"v_exp_f32 v221, v221\n"
"v_mfma_f32_16x16x32_bf16 a[40:43], a[200:203], v[20:23], 0\n"
"ds_read_b64_tr_b16 a[246:247], v243 offset:29184\n"
"v_cvt_pk_bf16_f32 v16, v220, v221\n"
"v_mfma_f32_16x16x32_bf16 a[44:47], a[204:207], v[20:23], 0\n"
"s_nop 0\n"
"v_exp_f32 v222, v222\n"
"v_mfma_f32_16x16x32_bf16 a[48:51], a[208:211], v[20:23], 0\n"
"ds_read_b64_tr_b16 a[248:249], v242 offset:25344\n"
"v_add_f32 v44, v44, v7\n"
"v_mfma_f32_16x16x32_bf16 a[52:55], a[212:215], v[20:23], 0\n"
"s_nop 0\n"
"v_exp_f32 v223, v223\n"
"v_mfma_f32_16x16x32_bf16 a[56:59], a[216:219], v[20:23], 0\n"
"ds_read_b64_tr_b16 a[250:251], v242 offset:29440\n"
"v_cvt_pk_bf16_f32 v17, v222, v223\n"
"v_mfma_f32_16x16x32_bf16 a[60:63], a[220:223], v[20:23], 0\n"
"s_nop 0\n"
"v_exp_f32 v224, v224\n"
"v_mfma_f32_16x16x32_bf16 v[48:51], v[40:43], v[20:23], 0\n"
"ds_read_b64_tr_b16 a[252:253], v243 offset:25344\n"
"v_add_f32 v52, v52, v220\n"
"v_mfma_f32_16x16x32_bf16 v[228:231], a[128:131], v[112:115], v[36:39]\n"
"s_nop 0\n"
"v_exp_f32 v225, v225\n"
"v_mfma_f32_16x16x32_bf16 v[228:231], a[132:135], v[116:119], v[228:231]\n"
"ds_read_b64_tr_b16 a[254:255], v243 offset:29440\n"
"v_cvt_pk_bf16_f32 v18, v224, v225\n"
"v_mfma_f32_16x16x32_bf16 v[228:231], a[136:139], v[120:123], v[228:231]\n"
"s_nop 0\n"
"v_exp_f32 v226, v226\n"
"v_mfma_f32_16x16x32_bf16 v[228:231], a[140:143], v[124:127], v[228:231]\n"
"v_add_f32 v52, v52, v221\n"
"v_add_f32 v52, v52, v222\n"
"v_mfma_f32_16x16x32_bf16 v[232:235], a[144:147], v[112:115], v[36:39]\n"
"s_nop 0\n"
"v_exp_f32 v227, v227\n"
"v_mfma_f32_16x16x32_bf16 v[232:235], a[148:151], v[116:119], v[232:235]\n"
"v_cvt_pk_bf16_f32 v19, v226, v227\n"
"v_add_f32 v52, v52, v223\n"
"v_mfma_f32_16x16x32_bf16 v[232:235], a[152:155], v[120:123], v[232:235]\n"
"s_nop 0\n"
"v_mfma_f32_16x16x32_bf16 v[232:235], a[156:159], v[124:127], v[232:235]\n"
"s_nop 0\n"
"v_exp_f32 v228, v228\n"
"v_mfma_f32_16x16x32_bf16 a[64:67], a[192:195], v[16:19], 0\n"
"v_add_f32 v52, v52, v224\n"
"v_add_f32 v52, v52, v225\n"
"v_mfma_f32_16x16x32_bf16 a[68:71], a[196:199], v[16:19], 0\n"
"s_nop 0\n"
"v_exp_f32 v229, v229\n"
"v_mfma_f32_16x16x32_bf16 a[72:75], a[200:203], v[16:19], 0\n"
"v_cvt_pk_bf16_f32 v20, v228, v229\n"
"v_add_f32 v52, v52, v226\n"
"v_mfma_f32_16x16x32_bf16 a[76:79], a[204:207], v[16:19], 0\n"
"s_nop 0\n"
"v_exp_f32 v230, v230\n"
"v_mfma_f32_16x16x32_bf16 a[80:83], a[208:211], v[16:19], 0\n"
"s_nop 0\n"
"v_add_f32 v52, v52, v227\n"
"v_mfma_f32_16x16x32_bf16 a[84:87], a[212:215], v[16:19], 0\n"
"s_nop 0\n"
"v_exp_f32 v231, v231\n"
"v_mfma_f32_16x16x32_bf16 a[88:91], a[216:219], v[16:19], 0\n"
"s_waitcnt vmcnt(16) lgkmcnt(0)\n"
"s_barrier\n"
"v_cvt_pk_bf16_f32 v21, v230, v231\n"
"v_mfma_f32_16x16x32_bf16 a[92:95], a[220:223], v[16:19], 0\n"
"s_nop 0\n"
"v_exp_f32 v232, v232\n"
"s_mov_b32 s87, 6\n"
".p2align 6\n"
"AT_loop:\n"
"v_mfma_f32_16x16x32_bf16 v[0:3], a[160:163], v[64:67], v[24:27]\n"
"ds_read_b128 a[128:131], v240 offset:32768\n"
"buffer_load_dword v250, v251, s[64:67], 0 offen\n"
"v_mfma_f32_16x16x32_bf16 v[0:3], a[164:167], v[68:71], v[0:3]\n"
"v_exp_f32 v233, v233\n"
"v_mfma_f32_16x16x32_bf16 v[0:3], a[168:171], v[72:75], v[0:3]\n"
"ds_read_b128 a[132:135], v240 offset:33024\n"
"v_cvt_pk_bf16_f32 v22, v232, v233\n"
"v_mfma_f32_16x16x32_bf16 v[0:3], a[172:175], v[76:79], v[0:3]\n"
"v_exp_f32 v234, v234\n"
"v_mfma_f32_16x16x32_bf16 v[4:7], a[176:179], v[64:67], v[24:27]\n"
"ds_read_b128 a[136:139], v240 offset:33280\n"
"v_mfma_f32_16x16x32_bf16 v[4:7], a[180:183], v[68:71], v[4:7]\n"
"v_exp_f32 v235, v235\n"
"v_mfma_f32_16x16x32_bf16 v[4:7], a[184:187], v[72:75], v[4:7]\n"
"ds_read_b128 a[140:143], v240 offset:33536\n"
"v_cvt_pk_bf16_f32 v23, v234, v235\n"
"v_mfma_f32_16x16x32_bf16 v[4:7], a[188:191], v[76:79], v[4:7]\n"
"s_mov_b32 m0, s86\n"
"v_exp_f32 v0, v0\n"
"v_mfma_f32_16x16x32_bf16 a[96:99], a[192:195], v[20:23], a[96:99]\n"
"buffer_load_dwordx4 v246, s[8:11], s84 offen offset:0 lds\n"
"v_mfma_f32_16x16x32_bf16 a[100:103], a[196:199], v[20:23], a[100:103]\n"
"v_exp_f32 v1, v1\n"
"v_mfma_f32_16x16x32_bf16 a[104:107], a[200:203], v[20:23], a[104:107]\n"
"ds_read_b128 a[144:147], v240 offset:36864\n"
"v_cvt_pk_bf16_f32 v16, v0, v1\n"
"v_mfma_f32_16x16x32_bf16 a[108:111], a[204:207], v[20:23], a[108:111]\n"
"v_exp_f32 v2, v2\n"
"v_mfma_f32_16x16x32_bf16 a[112:115], a[208:211], v[20:23], a[112:115]\n"
"ds_read_b128 a[148:151], v240 offset:37120\n"
"v_add_f32 v44, v44, v0\n"
"v_mfma_f32_16x16x32_bf16 a[116:119], a[212:215], v[20:23], a[116:119]\n"
"v_exp_f32 v3, v3\n"
"v_mfma_f32_16x16x32_bf16 a[120:123], a[216:219], v[20:23], a[120:123]\n"
"ds_read_b128 a[152:155], v240 offset:37376\n"
"v_cvt_pk_bf16_f32 v17, v2, v3\n"
"v_mfma_f32_16x16x32_bf16 a[124:127], a[220:223], v[20:23], a[124:127]\n"
"v_exp_f32 v4, v4\n"
"v_mfma_f32_16x16x32_bf16 v[56:59], v[40:43], v[20:23], v[56:59]\n"
"ds_read_b128 a[156:159], v240 offset:37632\n"
"v_add_f32 v44, v44, v1\n"
"v_mfma_f32_16x16x32_bf16 v[8:11], a[160:163], v[80:83], v[28:31]\n"
"v_exp_f32 v5, v5\n"
"v_mfma_f32_16x16x32_bf16 v[8:11], a[164:167], v[84:87], v[8:11]\n"
"ds_read_b64_tr_b16 a[192:193], v242 offset:49152\n"
"v_cvt_pk_bf16_f32 v18, v4, v5\n"
"v_mfma_f32_16x16x32_bf16 v[8:11], a[168:171], v[88:91], v[8:11]\n"
"v_exp_f32 v6, v6\n"
"v_mfma_f32_16x16x32_bf16 v[8:11], a[172:175], v[92:95], v[8:11]\n"
"ds_read_b64_tr_b16 a[194:195], v242 offset:53248\n"
"v_add_f32 v44, v44, v2\n"
"v_mfma_f32_16x16x32_bf16 v[12:15], a[176:179], v[80:83], v[28:31]\n"
"v_exp_f32 v7, v7\n"
"v_mfma_f32_16x16x32_bf16 v[12:15], a[180:183], v[84:87], v[12:15]\n"
"ds_read_b64_tr_b16 a[196:197], v243 offset:49152\n"
"v_cvt_pk_bf16_f32 v19, v6, v7\n"
"v_mfma_f32_16x16x32_bf16 v[12:15], a[184:187], v[88:91], v[12:15]\n"
"buffer_load_dwordx4 v246, s[8:11], s84 offen offset:1024 lds\n"
"v_mfma_f32_16x16x32_bf16 v[12:15], a[188:191], v[92:95], v[12:15]\n"
"s_waitcnt lgkmcnt(11)\n"
"v_exp_f32 v8, v8\n"
"v_mfma_f32_16x16x32_bf16 a[0:3], a[224:227], v[16:19], a[0:3]\n"
"ds_read_b64_tr_b16 a[198:199], v243 offset:53248\n"
"v_add_f32 v44, v44, v3\n"
"v_mfma_f32_16x16x32_bf16 a[4:7], a[228:231], v[16:19], a[4:7]\n"
"v_exp_f32 v9, v9\n"
"v_mfma_f32_16x16x32_bf16 a[8:11], a[232:235], v[16:19], a[8:11]\n"
"ds_read_b64_tr_b16 a[200:201], v242 offset:49408\n"
"v_cvt_pk_bf16_f32 v20, v8, v9\n"
"v_mfma_f32_16x16x32_bf16 a[12:15], a[236:239], v[16:19], a[12:15]\n"
"v_exp_f32 v10, v10\n"
"v_mfma_f32_16x16x32_bf16 a[16:19], a[240:243], v[16:19], a[16:19]\n"
"ds_read_b64_tr_b16 a[202:203], v242 offset:53504\n"
"v_add_f32 v44, v44, v4\n"
"v_mfma_f32_16x16x32_bf16 a[20:23], a[244:247], v[16:19], a[20:23]\n"
"v_exp_f32 v11, v11\n"
"v_mfma_f32_16x16x32_bf16 a[24:27], a[248:251], v[16:19], a[24:27]\n"
"ds_read_b64_tr_b16 a[204:205], v243 offset:49408\n"
"buffer_load_dword v250, v255, s[32:35], 0 offen\n"
"v_cvt_pk_bf16_f32 v21, v10, v11\n"
"v_mfma_f32_16x16x32_bf16 a[28:31], a[252:255], v[16:19], a[28:31]\n"
"v_exp_f32 v12, v12\n"
"v_mfma_f32_16x16x32_bf16 v[220:223], a[160:163], v[96:99], v[32:35]\n"
"ds_read_b64_tr_b16 a[206:207], v243 offset:53504\n"
"v_add_f32 v44, v44, v5\n"
"v_mfma_f32_16x16x32_bf16 v[220:223], a[164:167], v[100:103], v[220:223]\n"
"v_exp_f32 v13, v13\n"
"v_mfma_f32_16x16x32_bf16 v[220:223], a[168:171], v[104:107], v[220:223]\n"
"ds_read_b64_tr_b16 a[208:209], v242 offset:49664\n"
"v_cvt_pk_bf16_f32 v22, v12, v13\n"
"v_mfma_f32_16x16x32_bf16 v[220:223], a[172:175], v[108:111], v[220:223]\n"
"v_exp_f32 v14, v14\n"
"v_mfma_f32_16x16x32_bf16 v[224:227], a[176:179], v[96:99], v[32:35]\n"
"ds_read_b64_tr_b16 a[210:211], v242 offset:53760\n"
"v_add_f32 v44, v44, v6\n"
"v_mfma_f32_16x16x32_bf16 v[224:227], a[180:183], v[100:103], v[224:227]\n"
"v_exp_f32 v15, v15\n"
"v_mfma_f32_16x16x32_bf16 v[224:227], a[184:187], v[104:107], v[224:227]\n"
"ds_read_b64_tr_b16 a[212:213], v243 offset:49664\n"
"v_cvt_pk_bf16_f32 v23, v14, v15\n"
"v_mfma_f32_16x16x32_bf16 v[224:227], a[188:191], v[108:111], v[224:227]\n"
"v_exp_f32 v220, v220\n"
"v_mfma_f32_16x16x32_bf16 a[32:35], a[224:227], v[20:23], a[32:35]\n"
"buffer_load_dwordx4 v247, s[8:11], s84 offen offset:2048 lds\n"
"v_mfma_f32_16x16x32_bf16 a[36:39], a[228:231], v[20:23], a[36:39]\n"
"v_exp_f32 v221, v221\n"
"v_mfma_f32_16x16x32_bf16 a[40:43], a[232:235], v[20:23], a[40:43]\n"
"ds_read_b64_tr_b16 a[214:215], v243 offset:53760\n"
"v_cvt_pk_bf16_f32 v16, v220, v221\n"
"v_mfma_f32_16x16x32_bf16 a[44:47], a[236:239], v[20:23], a[44:47]\n"
"v_exp_f32 v222, v222\n"
"v_mfma_f32_16x16x32_bf16 a[48:51], a[240:243], v[20:23], a[48:51]\n"
"ds_read_b64_tr_b16 a[216:217], v242 offset:49920\n"
"v_add_f32 v44, v44, v7\n"
"v_mfma_f32_16x16x32_bf16 a[52:55], a[244:247], v[20:23], a[52:55]\n"
"v_exp_f32 v223, v223\n"
"v_mfma_f32_16x16x32_bf16 a[56:59], a[248:251], v[20:23], a[56:59]\n"
"ds_read_b64_tr_b16 a[218:219], v242 offset:54016\n"
"v_cvt_pk_bf16_f32 v17, v222, v223\n"
"v_mfma_f32_16x16x32_bf16 a[60:63], a[252:255], v[20:23], a[60:63]\n"
"v_exp_f32 v224, v224\n"
"v_mfma_f32_16x16x32_bf16 v[48:51], v[40:43], v[20:23], v[48:51]\n"
"ds_read_b64_tr_b16 a[220:221], v243 offset:49920\n"
"v_add_f32 v52, v52, v220\n"
"v_mfma_f32_16x16x32_bf16 v[228:231], a[160:163], v[112:115], v[36:39]\n"
"v_exp_f32 v225, v225\n"
"v_mfma_f32_16x16x32_bf16 v[228:231], a[164:167], v[116:119], v[228:231]\n"
"ds_read_b64_tr_b16 a[222:223], v243 offset:54016\n"
"v_cvt_pk_bf16_f32 v18, v224, v225\n"
"v_mfma_f32_16x16x32_bf16 v[228:231], a[168:171], v[120:123], v[228:231]\n"
"v_exp_f32 v226, v226\n"
"v_mfma_f32_16x16x32_bf16 v[228:231], a[172:175], v[124:127], v[228:231]\n"
"v_add_f32 v52, v52, v221\n"
"v_add_f32 v52, v52, v222\n"
"v_mfma_f32_16x16x32_bf16 v[232:235], a[176:179], v[112:115], v[36:39]\n"
"v_exp_f32 v227, v227\n"
"v_mfma_f32_16x16x32_bf16 v[232:235], a[180:183], v[116:119], v[232:235]\n"
"v_cvt_pk_bf16_f32 v19, v226, v227\n"
"v_add_f32 v52, v52, v223\n"
"v_mfma_f32_16x16x32_bf16 v[232:235], a[184:187], v[120:123], v[232:235]\n"
"buffer_load_dwordx4 v247, s[8:11], s84 offen offset:3072 lds\n"
"v_mfma_f32_16x16x32_bf16 v[232:235], a[188:191], v[124:127], v[232:235]\n"
"v_exp_f32 v228, v228\n"
"v_mfma_f32_16x16x32_bf16 a[64:67], a[224:227], v[16:19], a[64:67]\n"
"v_add_f32 v52, v52, v224\n"
"v_add_f32 v52, v52, v225\n"
"v_mfma_f32_16x16x32_bf16 a[68:71], a[228:231], v[16:19], a[68:71]\n"
"v_exp_f32 v229, v229\n"
"v_mfma_f32_16x16x32_bf16 a[72:75], a[232:235], v[16:19], a[72:75]\n"
"v_cvt_pk_bf16_f32 v20, v228, v229\n"
"v_add_f32 v52, v52, v226\n"
"v_mfma_f32_16x16x32_bf16 a[76:79], a[236:239], v[16:19], a[76:79]\n"
"v_exp_f32 v230, v230\n"
"v_mfma_f32_16x16x32_bf16 a[80:83], a[240:243], v[16:19], a[80:83]\n"
"v_add_f32 v52, v52, v227\n"
"v_mfma_f32_16x16x32_bf16 a[84:87], a[244:247], v[16:19], a[84:87]\n"
"v_exp_f32 v231, v231\n"
"v_mfma_f32_16x16x32_bf16 a[88:91], a[248:251], v[16:19], a[88:91]\n"
"s_waitcnt lgkmcnt(8)\n"
"v_cvt_pk_bf16_f32 v21, v230, v231\n"
"v_mfma_f32_16x16x32_bf16 a[92:95], a[252:255], v[16:19], a[92:95]\n"
"v_exp_f32 v232, v232\n"
"v_mfma_f32_16x16x32_bf16 v[0:3], a[128:131], v[64:67], v[24:27]\n"
"ds_read_b128 a[160:163], v240 offset:40960\n"
"v_mfma_f32_16x16x32_bf16 v[0:3], a[132:135], v[68:71], v[0:3]\n"
"v_exp_f32 v233, v233\n"
"v_mfma_f32_16x16x32_bf16 v[0:3], a[136:139], v[72:75], v[0:3]\n"
"ds_read_b128 a[164:167], v240 offset:41216\n"
"v_cvt_pk_bf16_f32 v22, v232, v233\n"
"v_mfma_f32_16x16x32_bf16 v[0:3], a[140:143], v[76:79], v[0:3]\n"
"v_exp_f32 v234, v234\n"
"v_mfma_f32_16x16x32_bf16 v[4:7], a[144:147], v[64:67], v[24:27]\n"
"ds_read_b128 a[168:171], v240 offset:41472\n"
"v_mfma_f32_16x16x32_bf16 v[4:7], a[148:151], v[68:71], v[4:7]\n"
"v_exp_f32 v235, v235\n"
"v_mfma_f32_16x16x32_bf16 v[4:7], a[152:155], v[72:75], v[4:7]\n"
"ds_read_b128 a[172:175], v240 offset:41728\n"
"v_cvt_pk_bf16_f32 v23, v234, v235\n"
"v_mfma_f32_16x16x32_bf16 v[4:7], a[156:159], v[76:79], v[4:7]\n"
"s_add_u32 m0, s86, 16384\n"
"v_exp_f32 v0, v0\n"
"v_mfma_f32_16x16x32_bf16 a[96:99], a[224:227], v[20:23], a[96:99]\n"
"buffer_load_dwordx4 v246, s[12:15], s84 offen offset:0 lds\n"
"v_mfma_f32_16x16x32_bf16 a[100:103], a[228:231], v[20:23], a[100:103]\n"
"v_exp_f32 v1, v1\n"
"v_mfma_f32_16x16x32_bf16 a[104:107], a[232:235], v[20:23], a[104:107]\n"
"ds_read_b128 a[176:179], v240 offset:45056\n"
"v_cvt_pk_bf16_f32 v16, v0, v1\n"
"v_mfma_f32_16x16x32_bf16 a[108:111], a[236:239], v[20:23], a[108:111]\n"
"v_exp_f32 v2, v2\n"
"v_mfma_f32_16x16x32_bf16 a[112:115], a[240:243], v[20:23], a[112:115]\n"
"ds_read_b128 a[180:183], v240 offset:45312\n"
"v_add_f32 v44, v44, v0\n"
"v_mfma_f32_16x16x32_bf16 a[116:119], a[244:247], v[20:23], a[116:119]\n"
"v_exp_f32 v3, v3\n"
"v_mfma_f32_16x16x32_bf16 a[120:123], a[248:251], v[20:23], a[120:123]\n"
"ds_read_b128 a[184:187], v240 offset:45568\n"
"v_cvt_pk_bf16_f32 v17, v2, v3\n"
"v_mfma_f32_16x16x32_bf16 a[124:127], a[252:255], v[20:23], a[124:127]\n"
"v_exp_f32 v4, v4\n"
"v_mfma_f32_16x16x32_bf16 v[56:59], v[40:43], v[20:23], v[56:59]\n"
"ds_read_b128 a[188:191], v240 offset:45824\n"
"v_add_f32 v44, v44, v1\n"
"v_mfma_f32_16x16x32_bf16 v[8:11], a[128:131], v[80:83], v[28:31]\n"
"v_exp_f32 v5, v5\n"
"v_mfma_f32_16x16x32_bf16 v[8:11], a[132:135], v[84:87], v[8:11]\n"
"ds_read_b64_tr_b16 a[224:225], v242 offset:57344\n"
"v_cvt_pk_bf16_f32 v18, v4, v5\n"
"v_mfma_f32_16x16x32_bf16 v[8:11], a[136:139], v[88:91], v[8:11]\n"
"v_exp_f32 v6, v6\n"
"v_mfma_f32_16x16x32_bf16 v[8:11], a[140:143], v[92:95], v[8:11]\n"
"ds_read_b64_tr_b16 a[226:227], v242 offset:61440\n"
"v_add_f32 v44, v44, v2\n"
"v_mfma_f32_16x16x32_bf16 v[12:15], a[144:147], v[80:83], v[28:31]\n"
"v_exp_f32 v7, v7\n"
"v_mfma_f32_16x16x32_bf16 v[12:15], a[148:151], v[84:87], v[12:15]\n"
"ds_read_b64_tr_b16 a[228:229], v243 offset:57344\n"
"v_cvt_pk_bf16_f32 v19, v6, v7\n"
"v_mfma_f32_16x16x32_bf16 v[12:15], a[152:155], v[88:91], v[12:15]\n"
"buffer_load_dwordx4 v247, s[12:15], s84 offen offset:1024 lds\n"
"v_mfma_f32_16x16x32_bf16 v[12:15], a[156:159], v[92:95], v[12:15]\n"
"s_waitcnt lgkmcnt(11)\n"
"v_exp_f32 v8, v8\n"
"v_mfma_f32_16x16x32_bf16 a[0:3], a[192:195], v[16:19], a[0:3]\n"
"ds_read_b64_tr_b16 a[230:231], v243 offset:61440\n"
"v_add_f32 v44, v44, v3\n"
"v_mfma_f32_16x16x32_bf16 a[4:7], a[196:199], v[16:19], a[4:7]\n"
"v_exp_f32 v9, v9\n"
"v_mfma_f32_16x16x32_bf16 a[8:11], a[200:203], v[16:19], a[8:11]\n"
"ds_read_b64_tr_b16 a[232:233], v242 offset:57600\n"
"v_cvt_pk_bf16_f32 v20, v8, v9\n"
"v_mfma_f32_16x16x32_bf16 a[12:15], a[204:207], v[16:19], a[12:15]\n"
"v_exp_f32 v10, v10\n"
"v_mfma_f32_16x16x32_bf16 a[16:19], a[208:211], v[16:19], a[16:19]\n"
"ds_read_b64_tr_b16 a[234:235], v242 offset:61696\n"
"v_add_f32 v44, v44, v4\n"
"v_mfma_f32_16x16x32_bf16 a[20:23], a[212:215], v[16:19], a[20:23]\n"
"v_exp_f32 v11, v11\n"
"v_mfma_f32_16x16x32_bf16 a[24:27], a[216:219], v[16:19], a[24:27]\n"
"ds_read_b64_tr_b16 a[236:237], v243 offset:57600\n"
"v_cvt_pk_bf16_f32 v21, v10, v11\n"
"v_mfma_f32_16x16x32_bf16 a[28:31], a[220:223], v[16:19], a[28:31]\n"
"v_exp_f32 v12, v12\n"
"v_mfma_f32_16x16x32_bf16 v[220:223], a[128:131], v[96:99], v[32:35]\n"
"ds_read_b64_tr_b16 a[238:239], v243 offset:61696\n"
"v_add_f32 v44, v44, v5\n"
"v_mfma_f32_16x16x32_bf16 v[220:223], a[132:135], v[100:103], v[220:223]\n"
"v_exp_f32 v13, v13\n"
"v_mfma_f32_16x16x32_bf16 v[220:223], a[136:139], v[104:107], v[220:223]\n"
"ds_read_b64_tr_b16 a[240:241], v242 offset:57856\n"
"v_cvt_pk_bf16_f32 v22, v12, v13\n"
"v_mfma_f32_16x16x32_bf16 v[220:223], a[140:143], v[108:111], v[220:223]\n"
"v_exp_f32 v14, v14\n"
"v_mfma_f32_16x16x32_bf16 v[224:227], a[144:147], v[96:99], v[32:35]\n"
"ds_read_b64_tr_b16 a[242:243], v242 offset:61952\n"
"v_add_f32 v44, v44, v6\n"
"v_mfma_f32_16x16x32_bf16 v[224:227], a[148:151], v[100:103], v[224:227]\n"
"v_exp_f32 v15, v15\n"
"v_mfma_f32_16x16x32_bf16 v[224:227], a[152:155], v[104:107], v[224:227]\n"
"ds_read_b64_tr_b16 a[244:245], v243 offset:57856\n"
"v_cvt_pk_bf16_f32 v23, v14, v15\n"
"v_mfma_f32_16x16x32_bf16 v[224:227], a[156:159], v[108:111], v[224:227]\n"
"v_exp_f32 v220, v220\n"
"v_mfma_f32_16x16x32_bf16 a[32:35], a[192:195], v[20:23], a[32:35]\n"
"buffer_load_dwordx4 v246, s[12:15], s84 offen offset:2048 lds\n"
"v_mfma_f32_16x16x32_bf16 a[36:39], a[196:199], v[20:23], a[36:39]\n"
"v_exp_f32 v221, v221\n"
"v_mfma_f32_16x16x32_bf16 a[40:43], a[200:203], v[20:23], a[40:43]\n"
"ds_read_b64_tr_b16 a[246:247], v243 offset:61952\n"
"v_cvt_pk_bf16_f32 v16, v220, v221\n"
"v_mfma_f32_16x16x32_bf16 a[44:47], a[204:207], v[20:23], a[44:47]\n"
"v_exp_f32 v222, v222\n"
"v_mfma_f32_16x16x32_bf16 a[48:51], a[208:211], v[20:23], a[48:51]\n"
"ds_read_b64_tr_b16 a[248:249], v242 offset:58112\n"
"v_add_f32 v44, v44, v7\n"
"v_mfma_f32_16x16x32_bf16 a[52:55], a[212:215], v[20:23], a[52:55]\n"
"v_exp_f32 v223, v223\n"
"v_mfma_f32_16x16x32_bf16 a[56:59], a[216:219], v[20:23], a[56:59]\n"
"ds_read_b64_tr_b16 a[250:251], v242 offset:62208\n"
"v_cvt_pk_bf16_f32 v17, v222, v223\n"
"v_mfma_f32_16x16x32_bf16 a[60:63], a[220:223], v[20:23], a[60:63]\n"
"v_exp_f32 v224, v224\n"
"v_mfma_f32_16x16x32_bf16 v[48:51], v[40:43], v[20:23], v[48:51]\n"
"ds_read_b64_tr_b16 a[252:253], v243 offset:58112\n"
"v_add_f32 v52, v52, v220\n"
"v_mfma_f32_16x16x32_bf16 v[228:231], a[128:131], v[112:115], v[36:39]\n"
"v_exp_f32 v225, v225\n"
"v_mfma_f32_16x16x32_bf16 v[228:231], a[132:135], v[116:119], v[228:231]\n"
"ds_read_b64_tr_b16 a[254:255], v243 offset:62208\n"
"v_cvt_pk_bf16_f32 v18, v224, v225\n"
"v_mfma_f32_16x16x32_bf16 v[228:231], a[136:139], v[120:123], v[228:231]\n"
"v_exp_f32 v226, v226\n"
"v_mfma_f32_16x16x32_bf16 v[228:231], a[140:143], v[124:127], v[228:231]\n"
"v_add_f32 v52, v52, v221\n"
"v_add_f32 v52, v52, v222\n"
"v_mfma_f32_16x16x32_bf16 v[232:235], a[144:147], v[112:115], v[36:39]\n"
"v_exp_f32 v227, v227\n"
"v_mfma_f32_16x16x32_bf16 v[232:235], a[148:151], v[116:119], v[232:235]\n"
"v_cvt_pk_bf16_f32 v19, v226, v227\n"
"v_add_f32 v52, v52, v223\n"
"v_mfma_f32_16x16x32_bf16 v[232:235], a[152:155], v[120:123], v[232:235]\n"
"buffer_load_dwordx4 v247, s[12:15], s84 offen offset:3072 lds\n"
"v_mfma_f32_16x16x32_bf16 v[232:235], a[156:159], v[124:127], v[232:235]\n"
"s_add_u32 s84, s84, 16384\n"
"v_exp_f32 v228, v228\n"
"v_mfma_f32_16x16x32_bf16 a[64:67], a[192:195], v[16:19], a[64:67]\n"
"v_add_f32 v52, v52, v224\n"
"v_add_f32 v52, v52, v225\n"
"v_mfma_f32_16x16x32_bf16 a[68:71], a[196:199], v[16:19], a[68:71]\n"
"v_exp_f32 v229, v229\n"
"v_mfma_f32_16x16x32_bf16 a[72:75], a[200:203], v[16:19], a[72:75]\n"
"v_cvt_pk_bf16_f32 v20, v228, v229\n"
"v_add_f32 v52, v52, v226\n"
"v_mfma_f32_16x16x32_bf16 a[76:79], a[204:207], v[16:19], a[76:79]\n"
"v_exp_f32 v230, v230\n"
"v_mfma_f32_16x16x32_bf16 a[80:83], a[208:211], v[16:19], a[80:83]\n"
"v_add_f32 v52, v52, v227\n"
"v_mfma_f32_16x16x32_bf16 a[84:87], a[212:215], v[16:19], a[84:87]\n"
"v_exp_f32 v231, v231\n"
"v_mfma_f32_16x16x32_bf16 a[88:91], a[216:219], v[16:19], a[88:91]\n"
"s_waitcnt vmcnt(18) lgkmcnt(0)\n"
"s_barrier\n"
"v_cvt_pk_bf16_f32 v21, v230, v231\n"
"v_mfma_f32_16x16x32_bf16 a[92:95], a[220:223], v[16:19], a[92:95]\n"
"v_exp_f32 v232, v232\n"
"v_mfma_f32_16x16x32_bf16 v[0:3], a[160:163], v[64:67], v[24:27]\n"
"ds_read_b128 a[128:131], v241 offset:0\n"
"buffer_store_dwordx4 v[216:219], v207, s[28:31], s54 offen sc1\n"
"v_mfma_f32_16x16x32_bf16 v[0:3], a[164:167], v[68:71], v[0:3]\n"
"v_exp_f32 v233, v233\n"
"v_mfma_f32_16x16x32_bf16 v[0:3], a[168:171], v[72:75], v[0:3]\n"
"ds_read_b128 a[132:135], v241 offset:256\n"
"v_cvt_pk_bf16_f32 v22, v232, v233\n"
"v_mfma_f32_16x16x32_bf16 v[0:3], a[172:175], v[76:79], v[0:3]\n"
"v_exp_f32 v234, v234\n"
"v_mfma_f32_16x16x32_bf16 v[4:7], a[176:179], v[64:67], v[24:27]\n"
"ds_read_b128 a[136:139], v241 offset:512\n"
"v_mfma_f32_16x16x32_bf16 v[4:7], a[180:183], v[68:71], v[4:7]\n"
"v_exp_f32 v235, v235\n"
"v_mfma_f32_16x16x32_bf16 v[4:7], a[184:187], v[72:75], v[4:7]\n"
"ds_read_b128 a[140:143], v241 offset:768\n"
"v_cvt_pk_bf16_f32 v23, v234, v235\n"
"v_mfma_f32_16x16x32_bf16 v[4:7], a[188:191], v[76:79], v[4:7]\n"
"s_add_u32 m0, s86, 32768\n"
"v_exp_f32 v0, v0\n"
"v_mfma_f32_16x16x32_bf16 a[96:99], a[192:195], v[20:23], a[96:99]\n"
"buffer_load_dwordx4 v246, s[8:11], s84 offen offset:0 lds\n"
"v_mfma_f32_16x16x32_bf16 a[100:103], a[196:199], v[20:23], a[100:103]\n"
"v_exp_f32 v1, v1\n"
"v_mfma_f32_16x16x32_bf16 a[104:107], a[200:203], v[20:23], a[104:107]\n"
"ds_read_b128 a[144:147], v241 offset:4096\n"
"v_cvt_pk_bf16_f32 v16, v0, v1\n"
"v_mfma_f32_16x16x32_bf16 a[108:111], a[204:207], v[20:23], a[108:111]\n"
"v_exp_f32 v2, v2\n"
"v_mfma_f32_16x16x32_bf16 a[112:115], a[208:211], v[20:23], a[112:115]\n"
"ds_read_b128 a[148:151], v241 offset:4352\n"
"v_add_f32 v44, v44, v0\n"
"v_mfma_f32_16x16x32_bf16 a[116:119], a[212:215], v[20:23], a[116:119]\n"
"v_exp_f32 v3, v3\n"
"v_mfma_f32_16x16x32_bf16 a[120:123], a[216:219], v[20:23], a[120:123]\n"
"ds_read_b128 a[152:155], v241 offset:4608\n"
"v_cvt_pk_bf16_f32 v17, v2, v3\n"
"v_mfma_f32_16x16x32_bf16 a[124:127], a[220:223], v[20:23], a[124:127]\n"
"v_exp_f32 v4, v4\n"
"v_mfma_f32_16x16x32_bf16 v[56:59], v[40:43], v[20:23], v[56:59]\n"
"ds_read_b128 a[156:159], v241 offset:4864\n"
"v_add_f32 v44, v44, v1\n"
"v_mfma_f32_16x16x32_bf16 v[8:11], a[160:163], v[80:83], v[28:31]\n"
"v_exp_f32 v5, v5\n"
"v_mfma_f32_16x16x32_bf16 v[8:11], a[164:167], v[84:87], v[8:11]\n"
"ds_read_b64_tr_b16 a[192:193], v244 offset:16384\n"
"v_cvt_pk_bf16_f32 v18, v4, v5\n"
"v_mfma_f32_16x16x32_bf16 v[8:11], a[168:171], v[88:91], v[8:11]\n"
"v_exp_f32 v6, v6\n"
"v_mfma_f32_16x16x32_bf16 v[8:11], a[172:175], v[92:95], v[8:11]\n"
"ds_read_b64_tr_b16 a[194:195], v244 offset:20480\n"
"v_add_f32 v44, v44, v2\n"
"v_mfma_f32_16x16x32_bf16 v[12:15], a[176:179], v[80:83], v[28:31]\n"
"v_exp_f32 v7, v7\n"
"v_mfma_f32_16x16x32_bf16 v[12:15], a[180:183], v[84:87], v[12:15]\n"
"ds_read_b64_tr_b16 a[196:197], v245 offset:16384\n"
"v_cvt_pk_bf16_f32 v19, v6, v7\n"
"v_mfma_f32_16x16x32_bf16 v[12:15], a[184:187], v[88:91], v[12:15]\n"
"buffer_load_dwordx4 v246, s[8:11], s84 offen offset:1024 lds\n"
"v_mfma_f32_16x16x32_bf16 v[12:15], a[188:191], v[92:95], v[12:15]\n"
"s_waitcnt lgkmcnt(11)\n"
"v_exp_f32 v8, v8\n"
"v_mfma_f32_16x16x32_bf16 a[0:3], a[224:227], v[16:19], a[0:3]\n"
"ds_read_b64_tr_b16 a[198:199], v245 offset:20480\n"
"v_add_f32 v44, v44, v3\n"
"v_mfma_f32_16x16x32_bf16 a[4:7], a[228:231], v[16:19], a[4:7]\n"
"v_exp_f32 v9, v9\n"
"v_mfma_f32_16x16x32_bf16 a[8:11], a[232:235], v[16:19], a[8:11]\n"
"ds_read_b64_tr_b16 a[200:201], v244 offset:16640\n"
"v_cvt_pk_bf16_f32 v20, v8, v9\n"
"v_mfma_f32_16x16x32_bf16 a[12:15], a[236:239], v[16:19], a[12:15]\n"
"v_exp_f32 v10, v10\n"
"v_mfma_f32_16x16x32_bf16 a[16:19], a[240:243], v[16:19], a[16:19]\n"
"ds_read_b64_tr_b16 a[202:203], v244 offset:20736\n"
"v_add_f32 v44, v44, v4\n"
"v_mfma_f32_16x16x32_bf16 a[20:23], a[244:247], v[16:19], a[20:23]\n"
"v_exp_f32 v11, v11\n"
"v_mfma_f32_16x16x32_bf16 a[24:27], a[248:251], v[16:19], a[24:27]\n"
"ds_read_b64_tr_b16 a[204:205], v245 offset:16640\n"
"buffer_load_dword v250, v251, s[68:71], 0 offen\n"
"v_cvt_pk_bf16_f32 v21, v10, v11\n"
"v_mfma_f32_16x16x32_bf16 a[28:31], a[252:255], v[16:19], a[28:31]\n"
"v_exp_f32 v12, v12\n"
"v_mfma_f32_16x16x32_bf16 v[220:223], a[160:163], v[96:99], v[32:35]\n"
"ds_read_b64_tr_b16 a[206:207], v245 offset:20736\n"
"v_add_f32 v44, v44, v5\n"
"v_mfma_f32_16x16x32_bf16 v[220:223], a[164:167], v[100:103], v[220:223]\n"
"v_exp_f32 v13, v13\n"
"v_mfma_f32_16x16x32_bf16 v[220:223], a[168:171], v[104:107], v[220:223]\n"
"ds_read_b64_tr_b16 a[208:209], v244 offset:16896\n"
"v_cvt_pk_bf16_f32 v22, v12, v13\n"
"v_mfma_f32_16x16x32_bf16 v[220:223], a[172:175], v[108:111], v[220:223]\n"
"v_exp_f32 v14, v14\n"
"v_mfma_f32_16x16x32_bf16 v[224:227], a[176:179], v[96:99], v[32:35]\n"
"ds_read_b64_tr_b16 a[210:211], v244 offset:20992\n"
"v_add_f32 v44, v44, v6\n"
"v_mfma_f32_16x16x32_bf16 v[224:227], a[180:183], v[100:103], v[224:227]\n"
"v_exp_f32 v15, v15\n"
"v_mfma_f32_16x16x32_bf16 v[224:227], a[184:187], v[104:107], v[224:227]\n"
"ds_read_b64_tr_b16 a[212:213], v245 offset:16896\n"
"v_cvt_pk_bf16_f32 v23, v14, v15\n"
"v_mfma_f32_16x16x32_bf16 v[224:227], a[188:191], v[108:111], v[224:227]\n"
"v_exp_f32 v220, v220\n"
"v_mfma_f32_16x16x32_bf16 a[32:35], a[224:227], v[20:23], a[32:35]\n"
"buffer_load_dwordx4 v247, s[8:11], s84 offen offset:2048 lds\n"
"v_add_u32 v251, 1179648, v251\n"
"v_mfma_f32_16x16x32_bf16 a[36:39], a[228:231], v[20:23], a[36:39]\n"
"v_exp_f32 v221, v221\n"
"v_mfma_f32_16x16x32_bf16 a[40:43], a[232:235], v[20:23], a[40:43]\n"
"ds_read_b64_tr_b16 a[214:215], v245 offset:20992\n"
"v_cvt_pk_bf16_f32 v16, v220, v221\n"
"v_mfma_f32_16x16x32_bf16 a[44:47], a[236:239], v[20:23], a[44:47]\n"
"v_exp_f32 v222, v222\n"
"v_mfma_f32_16x16x32_bf16 a[48:51], a[240:243], v[20:23], a[48:51]\n"
"ds_read_b64_tr_b16 a[216:217], v244 offset:17152\n"
"v_add_f32 v44, v44, v7\n"
"v_mfma_f32_16x16x32_bf16 a[52:55], a[244:247], v[20:23], a[52:55]\n"
"v_exp_f32 v223, v223\n"
"v_mfma_f32_16x16x32_bf16 a[56:59], a[248:251], v[20:23], a[56:59]\n"
"ds_read_b64_tr_b16 a[218:219], v244 offset:21248\n"
"v_cvt_pk_bf16_f32 v17, v222, v223\n"
"v_mfma_f32_16x16x32_bf16 a[60:63], a[252:255], v[20:23], a[60:63]\n"
"v_exp_f32 v224, v224\n"
"v_mfma_f32_16x16x32_bf16 v[48:51], v[40:43], v[20:23], v[48:51]\n"
"ds_read_b64_tr_b16 a[220:221], v245 offset:17152\n"
"v_add_f32 v52, v52, v220\n"
"v_mfma_f32_16x16x32_bf16 v[228:231], a[160:163], v[112:115], v[36:39]\n"
"v_exp_f32 v225, v225\n"
"v_mfma_f32_16x16x32_bf16 v[228:231], a[164:167], v[116:119], v[228:231]\n"
"ds_read_b64_tr_b16 a[222:223], v245 offset:21248\n"
"v_cvt_pk_bf16_f32 v18, v224, v225\n"
"v_mfma_f32_16x16x32_bf16 v[228:231], a[168:171], v[120:123], v[228:231]\n"
"v_exp_f32 v226, v226\n"
"v_mfma_f32_16x16x32_bf16 v[228:231], a[172:175], v[124:127], v[228:231]\n"
"v_add_f32 v52, v52, v221\n"
"v_add_f32 v52, v52, v222\n"
"v_mfma_f32_16x16x32_bf16 v[232:235], a[176:179], v[112:115], v[36:39]\n"
"v_exp_f32 v227, v227\n"
"v_mfma_f32_16x16x32_bf16 v[232:235], a[180:183], v[116:119], v[232:235]\n"
"v_cvt_pk_bf16_f32 v19, v226, v227\n"
"v_add_f32 v52, v52, v223\n"
"v_mfma_f32_16x16x32_bf16 v[232:235], a[184:187], v[120:123], v[232:235]\n"
"buffer_load_dwordx4 v247, s[8:11], s84 offen offset:3072 lds\n"
"v_mfma_f32_16x16x32_bf16 v[232:235], a[188:191], v[124:127], v[232:235]\n"
"v_exp_f32 v228, v228\n"
"v_mfma_f32_16x16x32_bf16 a[64:67], a[224:227], v[16:19], a[64:67]\n"
"v_add_f32 v52, v52, v224\n"
"v_add_f32 v52, v52, v225\n"
"v_mfma_f32_16x16x32_bf16 a[68:71], a[228:231], v[16:19], a[68:71]\n"
"v_exp_f32 v229, v229\n"
"v_mfma_f32_16x16x32_bf16 a[72:75], a[232:235], v[16:19], a[72:75]\n"
"v_cvt_pk_bf16_f32 v20, v228, v229\n"
"v_add_f32 v52, v52, v226\n"
"v_mfma_f32_16x16x32_bf16 a[76:79], a[236:239], v[16:19], a[76:79]\n"
"v_exp_f32 v230, v230\n"
"v_mfma_f32_16x16x32_bf16 a[80:83], a[240:243], v[16:19], a[80:83]\n"
"v_add_f32 v52, v52, v227\n"
"v_mfma_f32_16x16x32_bf16 a[84:87], a[244:247], v[16:19], a[84:87]\n"
"v_exp_f32 v231, v231\n"
"v_mfma_f32_16x16x32_bf16 a[88:91], a[248:251], v[16:19], a[88:91]\n"
"s_waitcnt lgkmcnt(8)\n"
"v_cvt_pk_bf16_f32 v21, v230, v231\n"
"v_mfma_f32_16x16x32_bf16 a[92:95], a[252:255], v[16:19], a[92:95]\n"
"v_exp_f32 v232, v232\n"
"v_mfma_f32_16x16x32_bf16 v[0:3], a[128:131], v[64:67], v[24:27]\n"
"ds_read_b128 a[160:163], v241 offset:8192\n"
"v_mfma_f32_16x16x32_bf16 v[0:3], a[132:135], v[68:71], v[0:3]\n"
"v_exp_f32 v233, v233\n"
"v_mfma_f32_16x16x32_bf16 v[0:3], a[136:139], v[72:75], v[0:3]\n"
"ds_read_b128 a[164:167], v241 offset:8448\n"
"v_cvt_pk_bf16_f32 v22, v232, v233\n"
"v_mfma_f32_16x16x32_bf16 v[0:3], a[140:143], v[76:79], v[0:3]\n"
"v_exp_f32 v234, v234\n"
"v_mfma_f32_16x16x32_bf16 v[4:7], a[144:147], v[64:67], v[24:27]\n"
"ds_read_b128 a[168:171], v241 offset:8704\n"
"v_mfma_f32_16x16x32_bf16 v[4:7], a[148:151], v[68:71], v[4:7]\n"
"v_exp_f32 v235, v235\n"
"v_mfma_f32_16x16x32_bf16 v[4:7], a[152:155], v[72:75], v[4:7]\n"
"ds_read_b128 a[172:175], v241 offset:8960\n"
"v_cvt_pk_bf16_f32 v23, v234, v235\n"
"v_mfma_f32_16x16x32_bf16 v[4:7], a[156:159], v[76:79], v[4:7]\n"
"s_add_u32 m0, s86, 49152\n"
"v_exp_f32 v0, v0\n"
"v_mfma_f32_16x16x32_bf16 a[96:99], a[224:227], v[20:23], a[96:99]\n"
"buffer_load_dwordx4 v246, s[12:15], s84 offen offset:0 lds\n"
"v_mfma_f32_16x16x32_bf16 a[100:103], a[228:231], v[20:23], a[100:103]\n"
"v_exp_f32 v1, v1\n"
"v_mfma_f32_16x16x32_bf16 a[104:107], a[232:235], v[20:23], a[104:107]\n"
"ds_read_b128 a[176:179], v241 offset:12288\n"
"v_cvt_pk_bf16_f32 v16, v0, v1\n"
"v_mfma_f32_16x16x32_bf16 a[108:111], a[236:239], v[20:23], a[108:111]\n"
"v_exp_f32 v2, v2\n"
"v_mfma_f32_16x16x32_bf16 a[112:115], a[240:243], v[20:23], a[112:115]\n"
"ds_read_b128 a[180:183], v241 offset:12544\n"
"v_add_f32 v44, v44, v0\n"
"v_mfma_f32_16x16x32_bf16 a[116:119], a[244:247], v[20:23], a[116:119]\n"
"v_exp_f32 v3, v3\n"
"v_mfma_f32_16x16x32_bf16 a[120:123], a[248:251], v[20:23], a[120:123]\n"
"ds_read_b128 a[184:187], v241 offset:12800\n"
"v_cvt_pk_bf16_f32 v17, v2, v3\n"
"v_mfma_f32_16x16x32_bf16 a[124:127], a[252:255], v[20:23], a[124:127]\n"
"v_exp_f32 v4, v4\n"
"v_mfma_f32_16x16x32_bf16 v[56:59], v[40:43], v[20:23], v[56:59]\n"
"ds_read_b128 a[188:191], v241 offset:13056\n"
"v_add_f32 v44, v44, v1\n"
"v_mfma_f32_16x16x32_bf16 v[8:11], a[128:131], v[80:83], v[28:31]\n"
"v_exp_f32 v5, v5\n"
"v_mfma_f32_16x16x32_bf16 v[8:11], a[132:135], v[84:87], v[8:11]\n"
"ds_read_b64_tr_b16 a[224:225], v244 offset:24576\n"
"v_cvt_pk_bf16_f32 v18, v4, v5\n"
"v_mfma_f32_16x16x32_bf16 v[8:11], a[136:139], v[88:91], v[8:11]\n"
"v_exp_f32 v6, v6\n"
"v_mfma_f32_16x16x32_bf16 v[8:11], a[140:143], v[92:95], v[8:11]\n"
"ds_read_b64_tr_b16 a[226:227], v244 offset:28672\n"
"v_add_f32 v44, v44, v2\n"
"v_mfma_f32_16x16x32_bf16 v[12:15], a[144:147], v[80:83], v[28:31]\n"
"v_exp_f32 v7, v7\n"
"v_mfma_f32_16x16x32_bf16 v[12:15], a[148:151], v[84:87], v[12:15]\n"
"ds_read_b64_tr_b16 a[228:229], v245 offset:24576\n"
"v_cvt_pk_bf16_f32 v19, v6, v7\n"
"v_mfma_f32_16x16x32_bf16 v[12:15], a[152:155], v[88:91], v[12:15]\n"
"buffer_load_dwordx4 v247, s[12:15], s84 offen offset:1024 lds\n"
"v_mfma_f32_16x16x32_bf16 v[12:15], a[156:159], v[92:95], v[12:15]\n"
"s_waitcnt lgkmcnt(11)\n"
"v_exp_f32 v8, v8\n"
"v_mfma_f32_16x16x32_bf16 a[0:3], a[192:195], v[16:19], a[0:3]\n"
"ds_read_b64_tr_b16 a[230:231], v245 offset:28672\n"
"v_add_f32 v44, v44, v3\n"
"v_mfma_f32_16x16x32_bf16 a[4:7], a[196:199], v[16:19], a[4:7]\n"
"v_exp_f32 v9, v9\n"
"v_mfma_f32_16x16x32_bf16 a[8:11], a[200:203], v[16:19], a[8:11]\n"
"ds_read_b64_tr_b16 a[232:233], v244 offset:24832\n"
"v_cvt_pk_bf16_f32 v20, v8, v9\n"
"v_mfma_f32_16x16x32_bf16 a[12:15], a[204:207], v[16:19], a[12:15]\n"
"v_exp_f32 v10, v10\n"
"v_mfma_f32_16x16x32_bf16 a[16:19], a[208:211], v[16:19], a[16:19]\n"
"ds_read_b64_tr_b16 a[234:235], v244 offset:28928\n"
"v_add_f32 v44, v44, v4\n"
"v_mfma_f32_16x16x32_bf16 a[20:23], a[212:215], v[16:19], a[20:23]\n"
"v_exp_f32 v11, v11\n"
"v_mfma_f32_16x16x32_bf16 a[24:27], a[216:219], v[16:19], a[24:27]\n"
"ds_read_b64_tr_b16 a[236:237], v245 offset:24832\n"
"v_cvt_pk_bf16_f32 v21, v10, v11\n"
"v_mfma_f32_16x16x32_bf16 a[28:31], a[220:223], v[16:19], a[28:31]\n"
"v_exp_f32 v12, v12\n"
"v_mfma_f32_16x16x32_bf16 v[220:223], a[128:131], v[96:99], v[32:35]\n"
"ds_read_b64_tr_b16 a[238:239], v245 offset:28928\n"
"v_add_f32 v44, v44, v5\n"
"v_mfma_f32_16x16x32_bf16 v[220:223], a[132:135], v[100:103], v[220:223]\n"
"v_exp_f32 v13, v13\n"
"v_mfma_f32_16x16x32_bf16 v[220:223], a[136:139], v[104:107], v[220:223]\n"
"ds_read_b64_tr_b16 a[240:241], v244 offset:25088\n"
"v_cvt_pk_bf16_f32 v22, v12, v13\n"
"v_mfma_f32_16x16x32_bf16 v[220:223], a[140:143], v[108:111], v[220:223]\n"
"v_exp_f32 v14, v14\n"
"v_mfma_f32_16x16x32_bf16 v[224:227], a[144:147], v[96:99], v[32:35]\n"
"ds_read_b64_tr_b16 a[242:243], v244 offset:29184\n"
"v_add_f32 v44, v44, v6\n"
"v_mfma_f32_16x16x32_bf16 v[224:227], a[148:151], v[100:103], v[224:227]\n"
"v_exp_f32 v15, v15\n"
"v_mfma_f32_16x16x32_bf16 v[224:227], a[152:155], v[104:107], v[224:227]\n"
"ds_read_b64_tr_b16 a[244:245], v245 offset:25088\n"
"v_cvt_pk_bf16_f32 v23, v14, v15\n"
"v_mfma_f32_16x16x32_bf16 v[224:227], a[156:159], v[108:111], v[224:227]\n"
"v_exp_f32 v220, v220\n"
"v_mfma_f32_16x16x32_bf16 a[32:35], a[192:195], v[20:23], a[32:35]\n"
"buffer_load_dwordx4 v246, s[12:15], s84 offen offset:2048 lds\n"
"v_mfma_f32_16x16x32_bf16 a[36:39], a[196:199], v[20:23], a[36:39]\n"
"v_exp_f32 v221, v221\n"
"v_mfma_f32_16x16x32_bf16 a[40:43], a[200:203], v[20:23], a[40:43]\n"
"ds_read_b64_tr_b16 a[246:247], v245 offset:29184\n"
"v_cvt_pk_bf16_f32 v16, v220, v221\n"
"v_mfma_f32_16x16x32_bf16 a[44:47], a[204:207], v[20:23], a[44:47]\n"
"v_exp_f32 v222, v222\n"
"v_mfma_f32_16x16x32_bf16 a[48:51], a[208:211], v[20:23], a[48:51]\n"
"ds_read_b64_tr_b16 a[248:249], v244 offset:25344\n"
"v_add_f32 v44, v44, v7\n"
"v_mfma_f32_16x16x32_bf16 a[52:55], a[212:215], v[20:23], a[52:55]\n"
"v_exp_f32 v223, v223\n"
"v_mfma_f32_16x16x32_bf16 a[56:59], a[216:219], v[20:23], a[56:59]\n"
"ds_read_b64_tr_b16 a[250:251], v244 offset:29440\n"
"v_cvt_pk_bf16_f32 v17, v222, v223\n"
"v_mfma_f32_16x16x32_bf16 a[60:63], a[220:223], v[20:23], a[60:63]\n"
"v_exp_f32 v224, v224\n"
"v_mfma_f32_16x16x32_bf16 v[48:51], v[40:43], v[20:23], v[48:51]\n"
"ds_read_b64_tr_b16 a[252:253], v245 offset:25344\n"
"v_add_f32 v52, v52, v220\n"
"v_mfma_f32_16x16x32_bf16 v[228:231], a[128:131], v[112:115], v[36:39]\n"
"v_exp_f32 v225, v225\n"
"v_mfma_f32_16x16x32_bf16 v[228:231], a[132:135], v[116:119], v[228:231]\n"
"ds_read_b64_tr_b16 a[254:255], v245 offset:29440\n"
"v_cvt_pk_bf16_f32 v18, v224, v225\n"
"v_mfma_f32_16x16x32_bf16 v[228:231], a[136:139], v[120:123], v[228:231]\n"
"v_exp_f32 v226, v226\n"
"v_mfma_f32_16x16x32_bf16 v[228:231], a[140:143], v[124:127], v[228:231]\n"
"v_add_f32 v52, v52, v221\n"
"v_add_f32 v52, v52, v222\n"
"v_mfma_f32_16x16x32_bf16 v[232:235], a[144:147], v[112:115], v[36:39]\n"
"v_exp_f32 v227, v227\n"
"v_mfma_f32_16x16x32_bf16 v[232:235], a[148:151], v[116:119], v[232:235]\n"
"v_cvt_pk_bf16_f32 v19, v226, v227\n"
"v_add_f32 v52, v52, v223\n"
"v_mfma_f32_16x16x32_bf16 v[232:235], a[152:155], v[120:123], v[232:235]\n"
"buffer_load_dwordx4 v247, s[12:15], s84 offen offset:3072 lds\n"
"v_mfma_f32_16x16x32_bf16 v[232:235], a[156:159], v[124:127], v[232:235]\n"
"s_add_u32 s84, s84, 16384\n"
"v_exp_f32 v228, v228\n"
"v_mfma_f32_16x16x32_bf16 a[64:67], a[192:195], v[16:19], a[64:67]\n"
"v_add_f32 v52, v52, v224\n"
"v_add_f32 v52, v52, v225\n"
"v_mfma_f32_16x16x32_bf16 a[68:71], a[196:199], v[16:19], a[68:71]\n"
"v_exp_f32 v229, v229\n"
"v_mfma_f32_16x16x32_bf16 a[72:75], a[200:203], v[16:19], a[72:75]\n"
"v_cvt_pk_bf16_f32 v20, v228, v229\n"
"v_add_f32 v52, v52, v226\n"
"v_mfma_f32_16x16x32_bf16 a[76:79], a[204:207], v[16:19], a[76:79]\n"
"v_exp_f32 v230, v230\n"
"v_mfma_f32_16x16x32_bf16 a[80:83], a[208:211], v[16:19], a[80:83]\n"
"v_add_f32 v52, v52, v227\n"
"v_mfma_f32_16x16x32_bf16 a[84:87], a[212:215], v[16:19], a[84:87]\n"
"v_exp_f32 v231, v231\n"
"v_mfma_f32_16x16x32_bf16 a[88:91], a[216:219], v[16:19], a[88:91]\n"
"s_waitcnt vmcnt(20) lgkmcnt(0)\n"
"s_barrier\n"
"v_cvt_pk_bf16_f32 v21, v230, v231\n"
"v_mfma_f32_16x16x32_bf16 a[92:95], a[220:223], v[16:19], a[92:95]\n"
"v_exp_f32 v232, v232\n"
"v_mfma_f32_16x16x32_bf16 v[0:3], a[160:163], v[64:67], v[24:27]\n"
"ds_read_b128 a[128:131], v241 offset:32768\n"
"buffer_load_dword v250, v252, s[72:75], s46 offen\n"
"v_mfma_f32_16x16x32_bf16 v[0:3], a[164:167], v[68:71], v[0:3]\n"
"v_exp_f32 v233, v233\n"
"v_mfma_f32_16x16x32_bf16 v[0:3], a[168:171], v[72:75], v[0:3]\n"
"ds_read_b128 a[132:135], v241 offset:33024\n"
"v_cvt_pk_bf16_f32 v22, v232, v233\n"
"v_mfma_f32_16x16x32_bf16 v[0:3], a[172:175], v[76:79], v[0:3]\n"
"v_exp_f32 v234, v234\n"
"v_mfma_f32_16x16x32_bf16 v[4:7], a[176:179], v[64:67], v[24:27]\n"
"ds_read_b128 a[136:139], v241 offset:33280\n"
"v_add_u32 v252, 147456, v252\n"
"v_mfma_f32_16x16x32_bf16 v[4:7], a[180:183], v[68:71], v[4:7]\n"
"v_exp_f32 v235, v235\n"
"v_mfma_f32_16x16x32_bf16 v[4:7], a[184:187], v[72:75], v[4:7]\n"
"ds_read_b128 a[140:143], v241 offset:33536\n"
"v_cvt_pk_bf16_f32 v23, v234, v235\n"
"v_mfma_f32_16x16x32_bf16 v[4:7], a[188:191], v[76:79], v[4:7]\n"
"s_add_u32 m0, s86, 65536\n"
"v_exp_f32 v0, v0\n"
"v_mfma_f32_16x16x32_bf16 a[96:99], a[192:195], v[20:23], a[96:99]\n"
"buffer_load_dwordx4 v246, s[8:11], s84 offen offset:0 lds\n"
"v_mfma_f32_16x16x32_bf16 a[100:103], a[196:199], v[20:23], a[100:103]\n"
"v_exp_f32 v1, v1\n"
"v_mfma_f32_16x16x32_bf16 a[104:107], a[200:203], v[20:23], a[104:107]\n"
"ds_read_b128 a[144:147], v241 offset:36864\n"
"v_cvt_pk_bf16_f32 v16, v0, v1\n"
"v_mfma_f32_16x16x32_bf16 a[108:111], a[204:207], v[20:23], a[108:111]\n"
"v_exp_f32 v2, v2\n"
"v_mfma_f32_16x16x32_bf16 a[112:115], a[208:211], v[20:23], a[112:115]\n"
"ds_read_b128 a[148:151], v241 offset:37120\n"
"v_add_f32 v44, v44, v0\n"
"v_mfma_f32_16x16x32_bf16 a[116:119], a[212:215], v[20:23], a[116:119]\n"
"v_exp_f32 v3, v3\n"
"v_mfma_f32_16x16x32_bf16 a[120:123], a[216:219], v[20:23], a[120:123]\n"
"ds_read_b128 a[152:155], v241 offset:37376\n"
"v_cvt_pk_bf16_f32 v17, v2, v3\n"
"v_mfma_f32_16x16x32_bf16 a[124:127], a[220:223], v[20:23], a[124:127]\n"
"v_exp_f32 v4, v4\n"
"v_mfma_f32_16x16x32_bf16 v[56:59], v[40:43], v[20:23], v[56:59]\n"
"ds_read_b128 a[156:159], v241 offset:37632\n"
"v_add_f32 v44, v44, v1\n"
"v_mfma_f32_16x16x32_bf16 v[8:11], a[160:163], v[80:83], v[28:31]\n"
"v_exp_f32 v5, v5\n"
"v_mfma_f32_16x16x32_bf16 v[8:11], a[164:167], v[84:87], v[8:11]\n"
"ds_read_b64_tr_b16 a[192:193], v244 offset:49152\n"
"v_cvt_pk_bf16_f32 v18, v4, v5\n"
"v_mfma_f32_16x16x32_bf16 v[8:11], a[168:171], v[88:91], v[8:11]\n"
"v_exp_f32 v6, v6\n"
"v_mfma_f32_16x16x32_bf16 v[8:11], a[172:175], v[92:95], v[8:11]\n"
"ds_read_b64_tr_b16 a[194:195], v244 offset:53248\n"
"v_add_f32 v44, v44, v2\n"
"v_mfma_f32_16x16x32_bf16 v[12:15], a[176:179], v[80:83], v[28:31]\n"
"v_exp_f32 v7, v7\n"
"v_mfma_f32_16x16x32_bf16 v[12:15], a[180:183], v[84:87], v[12:15]\n"
"ds_read_b64_tr_b16 a[196:197], v245 offset:49152\n"
"v_cvt_pk_bf16_f32 v19, v6, v7\n"
"v_mfma_f32_16x16x32_bf16 v[12:15], a[184:187], v[88:91], v[12:15]\n"
"buffer_load_dwordx4 v246, s[8:11], s84 offen offset:1024 lds\n"
"v_mfma_f32_16x16x32_bf16 v[12:15], a[188:191], v[92:95], v[12:15]\n"
"s_waitcnt lgkmcnt(11)\n"
"v_exp_f32 v8, v8\n"
"v_mfma_f32_16x16x32_bf16 a[0:3], a[224:227], v[16:19], a[0:3]\n"
"ds_read_b64_tr_b16 a[198:199], v245 offset:53248\n"
"v_add_f32 v44, v44, v3\n"
"v_mfma_f32_16x16x32_bf16 a[4:7], a[228:231], v[16:19], a[4:7]\n"
"v_exp_f32 v9, v9\n"
"v_mfma_f32_16x16x32_bf16 a[8:11], a[232:235], v[16:19], a[8:11]\n"
"ds_read_b64_tr_b16 a[200:201], v244 offset:49408\n"
"v_cvt_pk_bf16_f32 v20, v8, v9\n"
"v_mfma_f32_16x16x32_bf16 a[12:15], a[236:239], v[16:19], a[12:15]\n"
"v_exp_f32 v10, v10\n"
"v_mfma_f32_16x16x32_bf16 a[16:19], a[240:243], v[16:19], a[16:19]\n"
"ds_read_b64_tr_b16 a[202:203], v244 offset:53504\n"
"v_add_f32 v44, v44, v4\n"
"v_mfma_f32_16x16x32_bf16 a[20:23], a[244:247], v[16:19], a[20:23]\n"
"v_exp_f32 v11, v11\n"
"v_mfma_f32_16x16x32_bf16 a[24:27], a[248:251], v[16:19], a[24:27]\n"
"ds_read_b64_tr_b16 a[204:205], v245 offset:49408\n"
"v_cvt_pk_bf16_f32 v21, v10, v11\n"
"v_mfma_f32_16x16x32_bf16 a[28:31], a[252:255], v[16:19], a[28:31]\n"
"v_exp_f32 v12, v12\n"
"v_mfma_f32_16x16x32_bf16 v[220:223], a[160:163], v[96:99], v[32:35]\n"
"ds_read_b64_tr_b16 a[206:207], v245 offset:53504\n"
"v_add_f32 v44, v44, v5\n"
"v_mfma_f32_16x16x32_bf16 v[220:223], a[164:167], v[100:103], v[220:223]\n"
"v_exp_f32 v13, v13\n"
"v_mfma_f32_16x16x32_bf16 v[220:223], a[168:171], v[104:107], v[220:223]\n"
"ds_read_b64_tr_b16 a[208:209], v244 offset:49664\n"
"v_cvt_pk_bf16_f32 v22, v12, v13\n"
"v_mfma_f32_16x16x32_bf16 v[220:223], a[172:175], v[108:111], v[220:223]\n"
"v_exp_f32 v14, v14\n"
"v_mfma_f32_16x16x32_bf16 v[224:227], a[176:179], v[96:99], v[32:35]\n"
"ds_read_b64_tr_b16 a[210:211], v244 offset:53760\n"
"v_add_f32 v44, v44, v6\n"
"v_mfma_f32_16x16x32_bf16 v[224:227], a[180:183], v[100:103], v[224:227]\n"
"v_exp_f32 v15, v15\n"
"v_mfma_f32_16x16x32_bf16 v[224:227], a[184:187], v[104:107], v[224:227]\n"
"ds_read_b64_tr_b16 a[212:213], v245 offset:49664\n"
"v_cvt_pk_bf16_f32 v23, v14, v15\n"
"v_mfma_f32_16x16x32_bf16 v[224:227], a[188:191], v[108:111], v[224:227]\n"
"v_exp_f32 v220, v220\n"
"v_mfma_f32_16x16x32_bf16 a[32:35], a[224:227], v[20:23], a[32:35]\n"
"buffer_load_dwordx4 v247, s[8:11], s84 offen offset:2048 lds\n"
"v_mfma_f32_16x16x32_bf16 a[36:39], a[228:231], v[20:23], a[36:39]\n"
"v_exp_f32 v221, v221\n"
"v_mfma_f32_16x16x32_bf16 a[40:43], a[232:235], v[20:23], a[40:43]\n"
"ds_read_b64_tr_b16 a[214:215], v245 offset:53760\n"
"v_cvt_pk_bf16_f32 v16, v220, v221\n"
"v_mfma_f32_16x16x32_bf16 a[44:47], a[236:239], v[20:23], a[44:47]\n"
"v_exp_f32 v222, v222\n"
"v_mfma_f32_16x16x32_bf16 a[48:51], a[240:243], v[20:23], a[48:51]\n"
"ds_read_b64_tr_b16 a[216:217], v244 offset:49920\n"
"v_add_f32 v44, v44, v7\n"
"v_mfma_f32_16x16x32_bf16 a[52:55], a[244:247], v[20:23], a[52:55]\n"
"v_exp_f32 v223, v223\n"
"v_mfma_f32_16x16x32_bf16 a[56:59], a[248:251], v[20:23], a[56:59]\n"
"ds_read_b64_tr_b16 a[218:219], v244 offset:54016\n"
"v_cvt_pk_bf16_f32 v17, v222, v223\n"
"v_mfma_f32_16x16x32_bf16 a[60:63], a[252:255], v[20:23], a[60:63]\n"
"v_exp_f32 v224, v224\n"
"v_mfma_f32_16x16x32_bf16 v[48:51], v[40:43], v[20:23], v[48:51]\n"
"ds_read_b64_tr_b16 a[220:221], v245 offset:49920\n"
"v_add_f32 v52, v52, v220\n"
"v_mfma_f32_16x16x32_bf16 v[228:231], a[160:163], v[112:115], v[36:39]\n"
"v_exp_f32 v225, v225\n"
"v_mfma_f32_16x16x32_bf16 v[228:231], a[164:167], v[116:119], v[228:231]\n"
"ds_read_b64_tr_b16 a[222:223], v245 offset:54016\n"
"v_cvt_pk_bf16_f32 v18, v224, v225\n"
"v_mfma_f32_16x16x32_bf16 v[228:231], a[168:171], v[120:123], v[228:231]\n"
"v_exp_f32 v226, v226\n"
"v_mfma_f32_16x16x32_bf16 v[228:231], a[172:175], v[124:127], v[228:231]\n"
"v_add_f32 v52, v52, v221\n"
"v_add_f32 v52, v52, v222\n"
"v_mfma_f32_16x16x32_bf16 v[232:235], a[176:179], v[112:115], v[36:39]\n"
"v_exp_f32 v227, v227\n"
"v_mfma_f32_16x16x32_bf16 v[232:235], a[180:183], v[116:119], v[232:235]\n"
"v_cvt_pk_bf16_f32 v19, v226, v227\n"
"v_add_f32 v52, v52, v223\n"
"v_mfma_f32_16x16x32_bf16 v[232:235], a[184:187], v[120:123], v[232:235]\n"
"buffer_load_dwordx4 v247, s[8:11], s84 offen offset:3072 lds\n"
"v_mfma_f32_16x16x32_bf16 v[232:235], a[188:191], v[124:127], v[232:235]\n"
"v_exp_f32 v228, v228\n"
"v_mfma_f32_16x16x32_bf16 a[64:67], a[224:227], v[16:19], a[64:67]\n"
"v_add_f32 v52, v52, v224\n"
"v_add_f32 v52, v52, v225\n"
"v_mfma_f32_16x16x32_bf16 a[68:71], a[228:231], v[16:19], a[68:71]\n"
"v_exp_f32 v229, v229\n"
"v_mfma_f32_16x16x32_bf16 a[72:75], a[232:235], v[16:19], a[72:75]\n"
"v_cvt_pk_bf16_f32 v20, v228, v229\n"
"v_add_f32 v52, v52, v226\n"
"v_mfma_f32_16x16x32_bf16 a[76:79], a[236:239], v[16:19], a[76:79]\n"
"v_exp_f32 v230, v230\n"
"v_mfma_f32_16x16x32_bf16 a[80:83], a[240:243], v[16:19], a[80:83]\n"
"v_add_f32 v52, v52, v227\n"
"v_mfma_f32_16x16x32_bf16 a[84:87], a[244:247], v[16:19], a[84:87]\n"
"v_exp_f32 v231, v231\n"
"v_mfma_f32_16x16x32_bf16 a[88:91], a[248:251], v[16:19], a[88:91]\n"
"s_waitcnt lgkmcnt(8)\n"
"v_cvt_pk_bf16_f32 v21, v230, v231\n"
"v_mfma_f32_16x16x32_bf16 a[92:95], a[252:255], v[16:19], a[92:95]\n"
"v_exp_f32 v232, v232\n"
"v_mfma_f32_16x16x32_bf16 v[0:3], a[128:131], v[64:67], v[24:27]\n"
"ds_read_b128 a[160:163], v241 offset:40960\n"
"v_mfma_f32_16x16x32_bf16 v[0:3], a[132:135], v[68:71], v[0:3]\n"
"v_exp_f32 v233, v233\n"
"v_mfma_f32_16x16x32_bf16 v[0:3], a[136:139], v[72:75], v[0:3]\n"
"ds_read_b128 a[164:167], v241 offset:41216\n"
"v_cvt_pk_bf16_f32 v22, v232, v233\n"
"v_mfma_f32_16x16x32_bf16 v[0:3], a[140:143], v[76:79], v[0:3]\n"
"v_exp_f32 v234, v234\n"
"v_mfma_f32_16x16x32_bf16 v[4:7], a[144:147], v[64:67], v[24:27]\n"
"ds_read_b128 a[168:171], v241 offset:41472\n"
"v_mfma_f32_16x16x32_bf16 v[4:7], a[148:151], v[68:71], v[4:7]\n"
"v_exp_f32 v235, v235\n"
"v_mfma_f32_16x16x32_bf16 v[4:7], a[152:155], v[72:75], v[4:7]\n"
"ds_read_b128 a[172:175], v241 offset:41728\n"
"v_cvt_pk_bf16_f32 v23, v234, v235\n"
"v_mfma_f32_16x16x32_bf16 v[4:7], a[156:159], v[76:79], v[4:7]\n"
"s_add_u32 m0, s86, 81920\n"
"v_exp_f32 v0, v0\n"
"v_mfma_f32_16x16x32_bf16 a[96:99], a[224:227], v[20:23], a[96:99]\n"
"buffer_load_dwordx4 v246, s[12:15], s84 offen offset:0 lds\n"
"v_mfma_f32_16x16x32_bf16 a[100:103], a[228:231], v[20:23], a[100:103]\n"
"v_exp_f32 v1, v1\n"
"v_mfma_f32_16x16x32_bf16 a[104:107], a[232:235], v[20:23], a[104:107]\n"
"ds_read_b128 a[176:179], v241 offset:45056\n"
"v_cvt_pk_bf16_f32 v16, v0, v1\n"
"v_mfma_f32_16x16x32_bf16 a[108:111], a[236:239], v[20:23], a[108:111]\n"
"v_exp_f32 v2, v2\n"
"v_mfma_f32_16x16x32_bf16 a[112:115], a[240:243], v[20:23], a[112:115]\n"
"ds_read_b128 a[180:183], v241 offset:45312\n"
"v_add_f32 v44, v44, v0\n"
"v_mfma_f32_16x16x32_bf16 a[116:119], a[244:247], v[20:23], a[116:119]\n"
"v_exp_f32 v3, v3\n"
"v_mfma_f32_16x16x32_bf16 a[120:123], a[248:251], v[20:23], a[120:123]\n"
"ds_read_b128 a[184:187], v241 offset:45568\n"
"v_cvt_pk_bf16_f32 v17, v2, v3\n"
"v_mfma_f32_16x16x32_bf16 a[124:127], a[252:255], v[20:23], a[124:127]\n"
"v_exp_f32 v4, v4\n"
"v_mfma_f32_16x16x32_bf16 v[56:59], v[40:43], v[20:23], v[56:59]\n"
"ds_read_b128 a[188:191], v241 offset:45824\n"
"v_add_f32 v44, v44, v1\n"
"v_mfma_f32_16x16x32_bf16 v[8:11], a[128:131], v[80:83], v[28:31]\n"
"v_exp_f32 v5, v5\n"
"v_mfma_f32_16x16x32_bf16 v[8:11], a[132:135], v[84:87], v[8:11]\n"
"ds_read_b64_tr_b16 a[224:225], v244 offset:57344\n"
"v_cvt_pk_bf16_f32 v18, v4, v5\n"
"v_mfma_f32_16x16x32_bf16 v[8:11], a[136:139], v[88:91], v[8:11]\n"
"v_exp_f32 v6, v6\n"
"v_mfma_f32_16x16x32_bf16 v[8:11], a[140:143], v[92:95], v[8:11]\n"
"ds_read_b64_tr_b16 a[226:227], v244 offset:61440\n"
"v_add_f32 v44, v44, v2\n"
"v_mfma_f32_16x16x32_bf16 v[12:15], a[144:147], v[80:83], v[28:31]\n"
"v_exp_f32 v7, v7\n"
"v_mfma_f32_16x16x32_bf16 v[12:15], a[148:151], v[84:87], v[12:15]\n"
"ds_read_b64_tr_b16 a[228:229], v245 offset:57344\n"
"v_cvt_pk_bf16_f32 v19, v6, v7\n"
"v_mfma_f32_16x16x32_bf16 v[12:15], a[152:155], v[88:91], v[12:15]\n"
"buffer_load_dwordx4 v247, s[12:15], s84 offen offset:1024 lds\n"
"v_mfma_f32_16x16x32_bf16 v[12:15], a[156:159], v[92:95], v[12:15]\n"
"s_waitcnt lgkmcnt(11)\n"
"v_exp_f32 v8, v8\n"
"v_mfma_f32_16x16x32_bf16 a[0:3], a[192:195], v[16:19], a[0:3]\n"
"ds_read_b64_tr_b16 a[230:231], v245 offset:61440\n"
"v_add_f32 v44, v44, v3\n"
"v_mfma_f32_16x16x32_bf16 a[4:7], a[196:199], v[16:19], a[4:7]\n"
"v_exp_f32 v9, v9\n"
"v_mfma_f32_16x16x32_bf16 a[8:11], a[200:203], v[16:19], a[8:11]\n"
"ds_read_b64_tr_b16 a[232:233], v244 offset:57600\n"
"v_cvt_pk_bf16_f32 v20, v8, v9\n"
"v_mfma_f32_16x16x32_bf16 a[12:15], a[204:207], v[16:19], a[12:15]\n"
"v_exp_f32 v10, v10\n"
"v_mfma_f32_16x16x32_bf16 a[16:19], a[208:211], v[16:19], a[16:19]\n"
"ds_read_b64_tr_b16 a[234:235], v244 offset:61696\n"
"v_add_f32 v44, v44, v4\n"
"v_mfma_f32_16x16x32_bf16 a[20:23], a[212:215], v[16:19], a[20:23]\n"
"v_exp_f32 v11, v11\n"
"v_mfma_f32_16x16x32_bf16 a[24:27], a[216:219], v[16:19], a[24:27]\n"
"ds_read_b64_tr_b16 a[236:237], v245 offset:57600\n"
"v_cvt_pk_bf16_f32 v21, v10, v11\n"
"v_mfma_f32_16x16x32_bf16 a[28:31], a[220:223], v[16:19], a[28:31]\n"
"v_exp_f32 v12, v12\n"
"v_mfma_f32_16x16x32_bf16 v[220:223], a[128:131], v[96:99], v[32:35]\n"
"ds_read_b64_tr_b16 a[238:239], v245 offset:61696\n"
"v_add_f32 v44, v44, v5\n"
"v_mfma_f32_16x16x32_bf16 v[220:223], a[132:135], v[100:103], v[220:223]\n"
"v_exp_f32 v13, v13\n"
"v_mfma_f32_16x16x32_bf16 v[220:223], a[136:139], v[104:107], v[220:223]\n"
"ds_read_b64_tr_b16 a[240:241], v244 offset:57856\n"
"v_cvt_pk_bf16_f32 v22, v12, v13\n"
"v_mfma_f32_16x16x32_bf16 v[220:223], a[140:143], v[108:111], v[220:223]\n"
"v_exp_f32 v14, v14\n"
"v_mfma_f32_16x16x32_bf16 v[224:227], a[144:147], v[96:99], v[32:35]\n"
"ds_read_b64_tr_b16 a[242:243], v244 offset:61952\n"
"v_add_f32 v44, v44, v6\n"
"v_mfma_f32_16x16x32_bf16 v[224:227], a[148:151], v[100:103], v[224:227]\n"
"v_exp_f32 v15, v15\n"
"v_mfma_f32_16x16x32_bf16 v[224:227], a[152:155], v[104:107], v[224:227]\n"
"ds_read_b64_tr_b16 a[244:245], v245 offset:57856\n"
"v_cvt_pk_bf16_f32 v23, v14, v15\n"
"v_mfma_f32_16x16x32_bf16 v[224:227], a[156:159], v[108:111], v[224:227]\n"
"v_exp_f32 v220, v220\n"
"v_mfma_f32_16x16x32_bf16 a[32:35], a[192:195], v[20:23], a[32:35]\n"
"buffer_load_dwordx4 v246, s[12:15], s84 offen offset:2048 lds\n"
"v_mfma_f32_16x16x32_bf16 a[36:39], a[196:199], v[20:23], a[36:39]\n"
"v_exp_f32 v221, v221\n"
"v_mfma_f32_16x16x32_bf16 a[40:43], a[200:203], v[20:23], a[40:43]\n"
"ds_read_b64_tr_b16 a[246:247], v245 offset:61952\n"
"v_cvt_pk_bf16_f32 v16, v220, v221\n"
"v_mfma_f32_16x16x32_bf16 a[44:47], a[204:207], v[20:23], a[44:47]\n"
"v_exp_f32 v222, v222\n"
"v_mfma_f32_16x16x32_bf16 a[48:51], a[208:211], v[20:23], a[48:51]\n"
"ds_read_b64_tr_b16 a[248:249], v244 offset:58112\n"
"v_add_f32 v44, v44, v7\n"
"v_mfma_f32_16x16x32_bf16 a[52:55], a[212:215], v[20:23], a[52:55]\n"
"v_exp_f32 v223, v223\n"
"v_mfma_f32_16x16x32_bf16 a[56:59], a[216:219], v[20:23], a[56:59]\n"
"ds_read_b64_tr_b16 a[250:251], v244 offset:62208\n"
"v_cvt_pk_bf16_f32 v17, v222, v223\n"
"v_mfma_f32_16x16x32_bf16 a[60:63], a[220:223], v[20:23], a[60:63]\n"
"v_exp_f32 v224, v224\n"
"v_mfma_f32_16x16x32_bf16 v[48:51], v[40:43], v[20:23], v[48:51]\n"
"ds_read_b64_tr_b16 a[252:253], v245 offset:58112\n"
"v_add_f32 v52, v52, v220\n"
"v_mfma_f32_16x16x32_bf16 v[228:231], a[128:131], v[112:115], v[36:39]\n"
"v_exp_f32 v225, v225\n"
"v_mfma_f32_16x16x32_bf16 v[228:231], a[132:135], v[116:119], v[228:231]\n"
"ds_read_b64_tr_b16 a[254:255], v245 offset:62208\n"
"v_cvt_pk_bf16_f32 v18, v224, v225\n"
"v_mfma_f32_16x16x32_bf16 v[228:231], a[136:139], v[120:123], v[228:231]\n"
"v_exp_f32 v226, v226\n"
"v_mfma_f32_16x16x32_bf16 v[228:231], a[140:143], v[124:127], v[228:231]\n"
"v_add_f32 v52, v52, v221\n"
"v_add_f32 v52, v52, v222\n"
"v_mfma_f32_16x16x32_bf16 v[232:235], a[144:147], v[112:115], v[36:39]\n"
"v_exp_f32 v227, v227\n"
"v_mfma_f32_16x16x32_bf16 v[232:235], a[148:151], v[116:119], v[232:235]\n"
"v_cvt_pk_bf16_f32 v19, v226, v227\n"
"v_add_f32 v52, v52, v223\n"
"v_mfma_f32_16x16x32_bf16 v[232:235], a[152:155], v[120:123], v[232:235]\n"
"buffer_load_dwordx4 v247, s[12:15], s84 offen offset:3072 lds\n"
"v_mfma_f32_16x16x32_bf16 v[232:235], a[156:159], v[124:127], v[232:235]\n"
"s_add_u32 s84, s84, 16384\n"
"v_exp_f32 v228, v228\n"
"v_mfma_f32_16x16x32_bf16 a[64:67], a[192:195], v[16:19], a[64:67]\n"
"v_add_f32 v52, v52, v224\n"
"v_add_f32 v52, v52, v225\n"
"v_mfma_f32_16x16x32_bf16 a[68:71], a[196:199], v[16:19], a[68:71]\n"
"v_exp_f32 v229, v229\n"
"v_mfma_f32_16x16x32_bf16 a[72:75], a[200:203], v[16:19], a[72:75]\n"
"v_cvt_pk_bf16_f32 v20, v228, v229\n"
"v_add_f32 v52, v52, v226\n"
"v_mfma_f32_16x16x32_bf16 a[76:79], a[204:207], v[16:19], a[76:79]\n"
"v_exp_f32 v230, v230\n"
"v_mfma_f32_16x16x32_bf16 a[80:83], a[208:211], v[16:19], a[80:83]\n"
"v_add_f32 v52, v52, v227\n"
"v_mfma_f32_16x16x32_bf16 a[84:87], a[212:215], v[16:19], a[84:87]\n"
"v_exp_f32 v231, v231\n"
"v_mfma_f32_16x16x32_bf16 a[88:91], a[216:219], v[16:19], a[88:91]\n"
"s_waitcnt vmcnt(19) lgkmcnt(0)\n"
"s_barrier\n"
"v_cvt_pk_bf16_f32 v21, v230, v231\n"
"v_mfma_f32_16x16x32_bf16 a[92:95], a[220:223], v[16:19], a[92:95]\n"
"v_exp_f32 v232, v232\n"
"v_mfma_f32_16x16x32_bf16 v[0:3], a[160:163], v[64:67], v[24:27]\n"
"ds_read_b128 a[128:131], v240 offset:0\n"
"buffer_store_dwordx4 v[216:219], v208, s[28:31], s54 offen sc1\n"
"v_mfma_f32_16x16x32_bf16 v[0:3], a[164:167], v[68:71], v[0:3]\n"
"s_add_u32 s54, s54, 1024\n"
"v_exp_f32 v233, v233\n"
"v_mfma_f32_16x16x32_bf16 v[0:3], a[168:171], v[72:75], v[0:3]\n"
"s_min_u32 s54, s54, s49\n"
"ds_read_b128 a[132:135], v240 offset:256\n"
"v_cvt_pk_bf16_f32 v22, v232, v233\n"
"v_mfma_f32_16x16x32_bf16 v[0:3], a[172:175], v[76:79], v[0:3]\n"
"v_exp_f32 v234, v234\n"
"v_mfma_f32_16x16x32_bf16 v[4:7], a[176:179], v[64:67], v[24:27]\n"
"ds_read_b128 a[136:139], v240 offset:512\n"
"v_mfma_f32_16x16x32_bf16 v[4:7], a[180:183], v[68:71], v[4:7]\n"
"v_exp_f32 v235, v235\n"
"v_mfma_f32_16x16x32_bf16 v[4:7], a[184:187], v[72:75], v[4:7]\n"
"ds_read_b128 a[140:143], v240 offset:768\n"
"v_cvt_pk_bf16_f32 v23, v234, v235\n"
"v_mfma_f32_16x16x32_bf16 v[4:7], a[188:191], v[76:79], v[4:7]\n"
"s_add_u32 m0, s86, 98304\n"
"v_exp_f32 v0, v0\n"
"v_mfma_f32_16x16x32_bf16 a[96:99], a[192:195], v[20:23], a[96:99]\n"
"buffer_load_dwordx4 v246, s[8:11], s84 offen offset:0 lds\n"
"v_mfma_f32_16x16x32_bf16 a[100:103], a[196:199], v[20:23], a[100:103]\n"
"v_exp_f32 v1, v1\n"
"v_mfma_f32_16x16x32_bf16 a[104:107], a[200:203], v[20:23], a[104:107]\n"
"ds_read_b128 a[144:147], v240 offset:4096\n"
"v_cvt_pk_bf16_f32 v16, v0, v1\n"
"v_mfma_f32_16x16x32_bf16 a[108:111], a[204:207], v[20:23], a[108:111]\n"
"v_exp_f32 v2, v2\n"
"v_mfma_f32_16x16x32_bf16 a[112:115], a[208:211], v[20:23], a[112:115]\n"
"ds_read_b128 a[148:151], v240 offset:4352\n"
"v_add_f32 v44, v44, v0\n"
"v_mfma_f32_16x16x32_bf16 a[116:119], a[212:215], v[20:23], a[116:119]\n"
"v_exp_f32 v3, v3\n"
"v_mfma_f32_16x16x32_bf16 a[120:123], a[216:219], v[20:23], a[120:123]\n"
"ds_read_b128 a[152:155], v240 offset:4608\n"
"v_cvt_pk_bf16_f32 v17, v2, v3\n"
"v_mfma_f32_16x16x32_bf16 a[124:127], a[220:223], v[20:23], a[124:127]\n"
"v_exp_f32 v4, v4\n"
"v_mfma_f32_16x16x32_bf16 v[56:59], v[40:43], v[20:23], v[56:59]\n"
"ds_read_b128 a[156:159], v240 offset:4864\n"
"v_add_f32 v44, v44, v1\n"
"v_mfma_f32_16x16x32_bf16 v[8:11], a[160:163], v[80:83], v[28:31]\n"
"v_exp_f32 v5, v5\n"
"v_mfma_f32_16x16x32_bf16 v[8:11], a[164:167], v[84:87], v[8:11]\n"
"ds_read_b64_tr_b16 a[192:193], v242 offset:16384\n"
"v_cvt_pk_bf16_f32 v18, v4, v5\n"
"v_mfma_f32_16x16x32_bf16 v[8:11], a[168:171], v[88:91], v[8:11]\n"
"v_exp_f32 v6, v6\n"
"v_mfma_f32_16x16x32_bf16 v[8:11], a[172:175], v[92:95], v[8:11]\n"
"ds_read_b64_tr_b16 a[194:195], v242 offset:20480\n"
"v_add_f32 v44, v44, v2\n"
"v_mfma_f32_16x16x32_bf16 v[12:15], a[176:179], v[80:83], v[28:31]\n"
"v_exp_f32 v7, v7\n"
"v_mfma_f32_16x16x32_bf16 v[12:15], a[180:183], v[84:87], v[12:15]\n"
"ds_read_b64_tr_b16 a[196:197], v243 offset:16384\n"
"v_cvt_pk_bf16_f32 v19, v6, v7\n"
"v_mfma_f32_16x16x32_bf16 v[12:15], a[184:187], v[88:91], v[12:15]\n"
"buffer_load_dwordx4 v246, s[8:11], s84 offen offset:1024 lds\n"
"v_mfma_f32_16x16x32_bf16 v[12:15], a[188:191], v[92:95], v[12:15]\n"
"s_waitcnt lgkmcnt(11)\n"
"v_exp_f32 v8, v8\n"
"v_mfma_f32_16x16x32_bf16 a[0:3], a[224:227], v[16:19], a[0:3]\n"
"ds_read_b64_tr_b16 a[198:199], v243 offset:20480\n"
"v_add_f32 v44, v44, v3\n"
"v_mfma_f32_16x16x32_bf16 a[4:7], a[228:231], v[16:19], a[4:7]\n"
"v_exp_f32 v9, v9\n"
"v_mfma_f32_16x16x32_bf16 a[8:11], a[232:235], v[16:19], a[8:11]\n"
"ds_read_b64_tr_b16 a[200:201], v242 offset:16640\n"
"v_cvt_pk_bf16_f32 v20, v8, v9\n"
"v_mfma_f32_16x16x32_bf16 a[12:15], a[236:239], v[16:19], a[12:15]\n"
"v_exp_f32 v10, v10\n"
"v_mfma_f32_16x16x32_bf16 a[16:19], a[240:243], v[16:19], a[16:19]\n"
"ds_read_b64_tr_b16 a[202:203], v242 offset:20736\n"
"v_add_f32 v44, v44, v4\n"
"v_mfma_f32_16x16x32_bf16 a[20:23], a[244:247], v[16:19], a[20:23]\n"
"v_exp_f32 v11, v11\n"
"v_mfma_f32_16x16x32_bf16 a[24:27], a[248:251], v[16:19], a[24:27]\n"
"ds_read_b64_tr_b16 a[204:205], v243 offset:16640\n"
"buffer_load_dword v250, v253, s[24:27], s47 offen\n"
"v_cvt_pk_bf16_f32 v21, v10, v11\n"
"v_mfma_f32_16x16x32_bf16 a[28:31], a[252:255], v[16:19], a[28:31]\n"
"v_exp_f32 v12, v12\n"
"v_mfma_f32_16x16x32_bf16 v[220:223], a[160:163], v[96:99], v[32:35]\n"
"ds_read_b64_tr_b16 a[206:207], v243 offset:20736\n"
"v_add_f32 v44, v44, v5\n"
"v_mfma_f32_16x16x32_bf16 v[220:223], a[164:167], v[100:103], v[220:223]\n"
"v_exp_f32 v13, v13\n"
"v_mfma_f32_16x16x32_bf16 v[220:223], a[168:171], v[104:107], v[220:223]\n"
"ds_read_b64_tr_b16 a[208:209], v242 offset:16896\n"
"v_cvt_pk_bf16_f32 v22, v12, v13\n"
"v_mfma_f32_16x16x32_bf16 v[220:223], a[172:175], v[108:111], v[220:223]\n"
"v_exp_f32 v14, v14\n"
"v_mfma_f32_16x16x32_bf16 v[224:227], a[176:179], v[96:99], v[32:35]\n"
"ds_read_b64_tr_b16 a[210:211], v242 offset:20992\n"
"v_add_f32 v44, v44, v6\n"
"v_mfma_f32_16x16x32_bf16 v[224:227], a[180:183], v[100:103], v[224:227]\n"
"v_exp_f32 v15, v15\n"
"v_mfma_f32_16x16x32_bf16 v[224:227], a[184:187], v[104:107], v[224:227]\n"
"ds_read_b64_tr_b16 a[212:213], v243 offset:16896\n"
"v_cvt_pk_bf16_f32 v23, v14, v15\n"
"v_mfma_f32_16x16x32_bf16 v[224:227], a[188:191], v[108:111], v[224:227]\n"
"v_exp_f32 v220, v220\n"
"v_mfma_f32_16x16x32_bf16 a[32:35], a[224:227], v[20:23], a[32:35]\n"
"buffer_load_dwordx4 v247, s[8:11], s84 offen offset:2048 lds\n"
"v_add_u32 v253, 294912, v253\n"
"v_mfma_f32_16x16x32_bf16 a[36:39], a[228:231], v[20:23], a[36:39]\n"
"v_exp_f32 v221, v221\n"
"v_mfma_f32_16x16x32_bf16 a[40:43], a[232:235], v[20:23], a[40:43]\n"
"ds_read_b64_tr_b16 a[214:215], v243 offset:20992\n"
"v_cvt_pk_bf16_f32 v16, v220, v221\n"
"v_mfma_f32_16x16x32_bf16 a[44:47], a[236:239], v[20:23], a[44:47]\n"
"v_exp_f32 v222, v222\n"
"v_mfma_f32_16x16x32_bf16 a[48:51], a[240:243], v[20:23], a[48:51]\n"
"ds_read_b64_tr_b16 a[216:217], v242 offset:17152\n"
"v_add_f32 v44, v44, v7\n"
"v_mfma_f32_16x16x32_bf16 a[52:55], a[244:247], v[20:23], a[52:55]\n"
"v_exp_f32 v223, v223\n"
"v_mfma_f32_16x16x32_bf16 a[56:59], a[248:251], v[20:23], a[56:59]\n"
"ds_read_b64_tr_b16 a[218:219], v242 offset:21248\n"
"v_cvt_pk_bf16_f32 v17, v222, v223\n"
"v_mfma_f32_16x16x32_bf16 a[60:63], a[252:255], v[20:23], a[60:63]\n"
"v_exp_f32 v224, v224\n"
"v_mfma_f32_16x16x32_bf16 v[48:51], v[40:43], v[20:23], v[48:51]\n"
"ds_read_b64_tr_b16 a[220:221], v243 offset:17152\n"
"v_add_f32 v52, v52, v220\n"
"v_mfma_f32_16x16x32_bf16 v[228:231], a[160:163], v[112:115], v[36:39]\n"
"v_exp_f32 v225, v225\n"
"v_mfma_f32_16x16x32_bf16 v[228:231], a[164:167], v[116:119], v[228:231]\n"
"ds_read_b64_tr_b16 a[222:223], v243 offset:21248\n"
"v_cvt_pk_bf16_f32 v18, v224, v225\n"
"v_mfma_f32_16x16x32_bf16 v[228:231], a[168:171], v[120:123], v[228:231]\n"
"v_exp_f32 v226, v226\n"
"v_mfma_f32_16x16x32_bf16 v[228:231], a[172:175], v[124:127], v[228:231]\n"
"v_add_f32 v52, v52, v221\n"
"v_add_f32 v52, v52, v222\n"
"v_mfma_f32_16x16x32_bf16 v[232:235], a[176:179], v[112:115], v[36:39]\n"
"v_exp_f32 v227, v227\n"
"v_mfma_f32_16x16x32_bf16 v[232:235], a[180:183], v[116:119], v[232:235]\n"
"v_cvt_pk_bf16_f32 v19, v226, v227\n"
"v_add_f32 v52, v52, v223\n"
"v_mfma_f32_16x16x32_bf16 v[232:235], a[184:187], v[120:123], v[232:235]\n"
"buffer_load_dwordx4 v247, s[8:11], s84 offen offset:3072 lds\n"
"v_mfma_f32_16x16x32_bf16 v[232:235], a[188:191], v[124:127], v[232:235]\n"
"v_exp_f32 v228, v228\n"
"v_mfma_f32_16x16x32_bf16 a[64:67], a[224:227], v[16:19], a[64:67]\n"
"v_add_f32 v52, v52, v224\n"
"v_add_f32 v52, v52, v225\n"
"v_mfma_f32_16x16x32_bf16 a[68:71], a[228:231], v[16:19], a[68:71]\n"
"v_exp_f32 v229, v229\n"
"v_mfma_f32_16x16x32_bf16 a[72:75], a[232:235], v[16:19], a[72:75]\n"
"v_cvt_pk_bf16_f32 v20, v228, v229\n"
"v_add_f32 v52, v52, v226\n"
"v_mfma_f32_16x16x32_bf16 a[76:79], a[236:239], v[16:19], a[76:79]\n"
"v_exp_f32 v230, v230\n"
"v_mfma_f32_16x16x32_bf16 a[80:83], a[240:243], v[16:19], a[80:83]\n"
"v_add_f32 v52, v52, v227\n"
"v_mfma_f32_16x16x32_bf16 a[84:87], a[244:247], v[16:19], a[84:87]\n"
"v_exp_f32 v231, v231\n"
"v_mfma_f32_16x16x32_bf16 a[88:91], a[248:251], v[16:19], a[88:91]\n"
"s_waitcnt lgkmcnt(8)\n"
"v_cvt_pk_bf16_f32 v21, v230, v231\n"
"v_mfma_f32_16x16x32_bf16 a[92:95], a[252:255], v[16:19], a[92:95]\n"
"v_exp_f32 v232, v232\n"
"v_mfma_f32_16x16x32_bf16 v[0:3], a[128:131], v[64:67], v[24:27]\n"
"ds_read_b128 a[160:163], v240 offset:8192\n"
"v_mfma_f32_16x16x32_bf16 v[0:3], a[132:135], v[68:71], v[0:3]\n"
"v_exp_f32 v233, v233\n"
"v_mfma_f32_16x16x32_bf16 v[0:3], a[136:139], v[72:75], v[0:3]\n"
"ds_read_b128 a[164:167], v240 offset:8448\n"
"v_cvt_pk_bf16_f32 v22, v232, v233\n"
"v_mfma_f32_16x16x32_bf16 v[0:3], a[140:143], v[76:79], v[0:3]\n"
"v_exp_f32 v234, v234\n"
"v_mfma_f32_16x16x32_bf16 v[4:7], a[144:147], v[64:67], v[24:27]\n"
"ds_read_b128 a[168:171], v240 offset:8704\n"
"v_mfma_f32_16x16x32_bf16 v[4:7], a[148:151], v[68:71], v[4:7]\n"
"v_exp_f32 v235, v235\n"
"v_mfma_f32_16x16x32_bf16 v[4:7], a[152:155], v[72:75], v[4:7]\n"
"ds_read_b128 a[172:175], v240 offset:8960\n"
"v_cvt_pk_bf16_f32 v23, v234, v235\n"
"v_mfma_f32_16x16x32_bf16 v[4:7], a[156:159], v[76:79], v[4:7]\n"
"s_add_u32 m0, s86, 114688\n"
"v_exp_f32 v0, v0\n"
"v_mfma_f32_16x16x32_bf16 a[96:99], a[224:227], v[20:23], a[96:99]\n"
"buffer_load_dwordx4 v246, s[12:15], s84 offen offset:0 lds\n"
"v_mfma_f32_16x16x32_bf16 a[100:103], a[228:231], v[20:23], a[100:103]\n"
"v_exp_f32 v1, v1\n"
"v_mfma_f32_16x16x32_bf16 a[104:107], a[232:235], v[20:23], a[104:107]\n"
"ds_read_b128 a[176:179], v240 offset:12288\n"
"v_cvt_pk_bf16_f32 v16, v0, v1\n"
"v_mfma_f32_16x16x32_bf16 a[108:111], a[236:239], v[20:23], a[108:111]\n"
"v_exp_f32 v2, v2\n"
"v_mfma_f32_16x16x32_bf16 a[112:115], a[240:243], v[20:23], a[112:115]\n"
"ds_read_b128 a[180:183], v240 offset:12544\n"
"v_add_f32 v44, v44, v0\n"
"v_mfma_f32_16x16x32_bf16 a[116:119], a[244:247], v[20:23], a[116:119]\n"
"v_exp_f32 v3, v3\n"
"v_mfma_f32_16x16x32_bf16 a[120:123], a[248:251], v[20:23], a[120:123]\n"
"ds_read_b128 a[184:187], v240 offset:12800\n"
"v_cvt_pk_bf16_f32 v17, v2, v3\n"
"v_mfma_f32_16x16x32_bf16 a[124:127], a[252:255], v[20:23], a[124:127]\n"
"v_exp_f32 v4, v4\n"
"v_mfma_f32_16x16x32_bf16 v[56:59], v[40:43], v[20:23], v[56:59]\n"
"ds_read_b128 a[188:191], v240 offset:13056\n"
"v_add_f32 v44, v44, v1\n"
"v_mfma_f32_16x16x32_bf16 v[8:11], a[128:131], v[80:83], v[28:31]\n"
"v_exp_f32 v5, v5\n"
"v_mfma_f32_16x16x32_bf16 v[8:11], a[132:135], v[84:87], v[8:11]\n"
"ds_read_b64_tr_b16 a[224:225], v242 offset:24576\n"
"v_cvt_pk_bf16_f32 v18, v4, v5\n"
"v_mfma_f32_16x16x32_bf16 v[8:11], a[136:139], v[88:91], v[8:11]\n"
"v_exp_f32 v6, v6\n"
"v_mfma_f32_16x16x32_bf16 v[8:11], a[140:143], v[92:95], v[8:11]\n"
"ds_read_b64_tr_b16 a[226:227], v242 offset:28672\n"
"v_add_f32 v44, v44, v2\n"
"v_mfma_f32_16x16x32_bf16 v[12:15], a[144:147], v[80:83], v[28:31]\n"
"v_exp_f32 v7, v7\n"
"v_mfma_f32_16x16x32_bf16 v[12:15], a[148:151], v[84:87], v[12:15]\n"
"ds_read_b64_tr_b16 a[228:229], v243 offset:24576\n"
"v_cvt_pk_bf16_f32 v19, v6, v7\n"
"v_mfma_f32_16x16x32_bf16 v[12:15], a[152:155], v[88:91], v[12:15]\n"
"buffer_load_dwordx4 v247, s[12:15], s84 offen offset:1024 lds\n"
"v_mfma_f32_16x16x32_bf16 v[12:15], a[156:159], v[92:95], v[12:15]\n"
"s_waitcnt lgkmcnt(11)\n"
"v_exp_f32 v8, v8\n"
"v_mfma_f32_16x16x32_bf16 a[0:3], a[192:195], v[16:19], a[0:3]\n"
"ds_read_b64_tr_b16 a[230:231], v243 offset:28672\n"
"v_add_f32 v44, v44, v3\n"
"v_mfma_f32_16x16x32_bf16 a[4:7], a[196:199], v[16:19], a[4:7]\n"
"v_exp_f32 v9, v9\n"
"v_mfma_f32_16x16x32_bf16 a[8:11], a[200:203], v[16:19], a[8:11]\n"
"ds_read_b64_tr_b16 a[232:233], v242 offset:24832\n"
"v_cvt_pk_bf16_f32 v20, v8, v9\n"
"v_mfma_f32_16x16x32_bf16 a[12:15], a[204:207], v[16:19], a[12:15]\n"
"v_exp_f32 v10, v10\n"
"v_mfma_f32_16x16x32_bf16 a[16:19], a[208:211], v[16:19], a[16:19]\n"
"ds_read_b64_tr_b16 a[234:235], v242 offset:28928\n"
"v_add_f32 v44, v44, v4\n"
"v_mfma_f32_16x16x32_bf16 a[20:23], a[212:215], v[16:19], a[20:23]\n"
"v_exp_f32 v11, v11\n"
"v_mfma_f32_16x16x32_bf16 a[24:27], a[216:219], v[16:19], a[24:27]\n"
"ds_read_b64_tr_b16 a[236:237], v243 offset:24832\n"
"v_cvt_pk_bf16_f32 v21, v10, v11\n"
"v_mfma_f32_16x16x32_bf16 a[28:31], a[220:223], v[16:19], a[28:31]\n"
"v_exp_f32 v12, v12\n"
"v_mfma_f32_16x16x32_bf16 v[220:223], a[128:131], v[96:99], v[32:35]\n"
"ds_read_b64_tr_b16 a[238:239], v243 offset:28928\n"
"v_add_f32 v44, v44, v5\n"
"v_mfma_f32_16x16x32_bf16 v[220:223], a[132:135], v[100:103], v[220:223]\n"
"v_exp_f32 v13, v13\n"
"v_mfma_f32_16x16x32_bf16 v[220:223], a[136:139], v[104:107], v[220:223]\n"
"ds_read_b64_tr_b16 a[240:241], v242 offset:25088\n"
"v_cvt_pk_bf16_f32 v22, v12, v13\n"
"v_mfma_f32_16x16x32_bf16 v[220:223], a[140:143], v[108:111], v[220:223]\n"
"v_exp_f32 v14, v14\n"
"v_mfma_f32_16x16x32_bf16 v[224:227], a[144:147], v[96:99], v[32:35]\n"
"ds_read_b64_tr_b16 a[242:243], v242 offset:29184\n"
"v_add_f32 v44, v44, v6\n"
"v_mfma_f32_16x16x32_bf16 v[224:227], a[148:151], v[100:103], v[224:227]\n"
"v_exp_f32 v15, v15\n"
"v_mfma_f32_16x16x32_bf16 v[224:227], a[152:155], v[104:107], v[224:227]\n"
"ds_read_b64_tr_b16 a[244:245], v243 offset:25088\n"
"v_cvt_pk_bf16_f32 v23, v14, v15\n"
"v_mfma_f32_16x16x32_bf16 v[224:227], a[156:159], v[108:111], v[224:227]\n"
"v_exp_f32 v220, v220\n"
"v_mfma_f32_16x16x32_bf16 a[32:35], a[192:195], v[20:23], a[32:35]\n"
"buffer_load_dwordx4 v246, s[12:15], s84 offen offset:2048 lds\n"
"v_mfma_f32_16x16x32_bf16 a[36:39], a[196:199], v[20:23], a[36:39]\n"
"v_exp_f32 v221, v221\n"
"v_mfma_f32_16x16x32_bf16 a[40:43], a[200:203], v[20:23], a[40:43]\n"
"ds_read_b64_tr_b16 a[246:247], v243 offset:29184\n"
"v_cvt_pk_bf16_f32 v16, v220, v221\n"
"v_mfma_f32_16x16x32_bf16 a[44:47], a[204:207], v[20:23], a[44:47]\n"
"v_exp_f32 v222, v222\n"
"v_mfma_f32_16x16x32_bf16 a[48:51], a[208:211], v[20:23], a[48:51]\n"
"ds_read_b64_tr_b16 a[248:249], v242 offset:25344\n"
"v_add_f32 v44, v44, v7\n"
"v_mfma_f32_16x16x32_bf16 a[52:55], a[212:215], v[20:23], a[52:55]\n"
"v_exp_f32 v223, v223\n"
"v_mfma_f32_16x16x32_bf16 a[56:59], a[216:219], v[20:23], a[56:59]\n"
"ds_read_b64_tr_b16 a[250:251], v242 offset:29440\n"
"v_cvt_pk_bf16_f32 v17, v222, v223\n"
"v_mfma_f32_16x16x32_bf16 a[60:63], a[220:223], v[20:23], a[60:63]\n"
"v_exp_f32 v224, v224\n"
"v_mfma_f32_16x16x32_bf16 v[48:51], v[40:43], v[20:23], v[48:51]\n"
"ds_read_b64_tr_b16 a[252:253], v243 offset:25344\n"
"v_add_f32 v52, v52, v220\n"
"v_mfma_f32_16x16x32_bf16 v[228:231], a[128:131], v[112:115], v[36:39]\n"
"v_exp_f32 v225, v225\n"
"v_mfma_f32_16x16x32_bf16 v[228:231], a[132:135], v[116:119], v[228:231]\n"
"ds_read_b64_tr_b16 a[254:255], v243 offset:29440\n"
"v_cvt_pk_bf16_f32 v18, v224, v225\n"
"v_mfma_f32_16x16x32_bf16 v[228:231], a[136:139], v[120:123], v[228:231]\n"
"v_exp_f32 v226, v226\n"
"v_mfma_f32_16x16x32_bf16 v[228:231], a[140:143], v[124:127], v[228:231]\n"
"v_add_f32 v52, v52, v221\n"
"v_add_f32 v52, v52, v222\n"
"v_mfma_f32_16x16x32_bf16 v[232:235], a[144:147], v[112:115], v[36:39]\n"
"v_exp_f32 v227, v227\n"
"v_mfma_f32_16x16x32_bf16 v[232:235], a[148:151], v[116:119], v[232:235]\n"
"v_cvt_pk_bf16_f32 v19, v226, v227\n"
"v_add_f32 v52, v52, v223\n"
"v_mfma_f32_16x16x32_bf16 v[232:235], a[152:155], v[120:123], v[232:235]\n"
"buffer_load_dwordx4 v247, s[12:15], s84 offen offset:3072 lds\n"
"v_mfma_f32_16x16x32_bf16 v[232:235], a[156:159], v[124:127], v[232:235]\n"
"s_add_u32 s84, s84, 16384\n"
"v_exp_f32 v228, v228\n"
"v_mfma_f32_16x16x32_bf16 a[64:67], a[192:195], v[16:19], a[64:67]\n"
"s_sub_u32 s87, s87, 1\n"
"v_add_f32 v52, v52, v224\n"
"v_add_f32 v52, v52, v225\n"
"v_mfma_f32_16x16x32_bf16 a[68:71], a[196:199], v[16:19], a[68:71]\n"
"s_cmp_lg_u32 s87, 0\n"
"v_exp_f32 v229, v229\n"
"v_mfma_f32_16x16x32_bf16 a[72:75], a[200:203], v[16:19], a[72:75]\n"
"v_cvt_pk_bf16_f32 v20, v228, v229\n"
"v_add_f32 v52, v52, v226\n"
"v_mfma_f32_16x16x32_bf16 a[76:79], a[204:207], v[16:19], a[76:79]\n"
"v_exp_f32 v230, v230\n"
"v_mfma_f32_16x16x32_bf16 a[80:83], a[208:211], v[16:19], a[80:83]\n"
"v_add_f32 v52, v52, v227\n"
"v_mfma_f32_16x16x32_bf16 a[84:87], a[212:215], v[16:19], a[84:87]\n"
"v_exp_f32 v231, v231\n"
"v_mfma_f32_16x16x32_bf16 a[88:91], a[216:219], v[16:19], a[88:91]\n"
"s_waitcnt vmcnt(19) lgkmcnt(0)\n"
"s_barrier\n"
"v_cvt_pk_bf16_f32 v21, v230, v231\n"
"v_mfma_f32_16x16x32_bf16 a[92:95], a[220:223], v[16:19], a[92:95]\n"
"v_exp_f32 v232, v232\n"
"s_cbranch_scc1 AT_loop\n"
"s_cmp_lg_u32 s56, 0\n"
"s_cbranch_scc1 AT_h3\n"
"s_mov_b32 s61, 0x80000\n"
"AT_h2:\n"
"v_xor_b32 v210, s85, v210\n"
"v_xor_b32 v211, s85, v211\n"
"v_xor_b32 v212, s85, v212\n"
"v_xor_b32 v213, s85, v213\n"
"v_or3_b32 v210, v210, v211, v212\n"
"v_or_b32 v210, v210, v213\n"
"v_cmp_ne_u32 vcc, 0, v210\n"
"s_sub_u32 s61, s61, 1\n"
"s_cbranch_vccz AT_h4\n"
"s_cmp_eq_u32 s61, 0\n"
"s_cbranch_scc1 AT_h4\n"
"s_sleep 2\n"
"buffer_load_dword v210, v214, s[28:31], s57 offen offset:4 sc1\n"
"buffer_load_dword v211, v214, s[28:31], s58 offen offset:4 sc1\n"
"buffer_load_dword v212, v214, s[28:31], s59 offen offset:4 sc1\n"
"buffer_load_dword v213, v214, s[28:31], s60 offen offset:4 sc1\n"
"s_waitcnt vmcnt(0)\n"
"s_branch AT_h2\n"
"AT_h4:\n"
"v_mov_b32 v210, s85\n"
"v_mov_b32 v211, 65536\n"
"s_mov_b64 exec, 1\n"
"buffer_store_dword v210, v211, s[28:31], 0 offen sc1\n"
"s_mov_b64 exec, -1\n"
"AT_h3:\n"
"v_mfma_f32_16x16x32_bf16 v[0:3], a[160:163], v[64:67], v[24:27]\n"
"ds_read_b128 a[128:131], v240 offset:32768\n"
"buffer_load_dword v250, v255, s[32:35], 0 offen\n"
"v_mfma_f32_16x16x32_bf16 v[0:3], a[164:167], v[68:71], v[0:3]\n"
"v_exp_f32 v233, v233\n"
"v_mfma_f32_16x16x32_bf16 v[0:3], a[168:171], v[72:75], v[0:3]\n"
"ds_read_b128 a[132:135], v240 offset:33024\n"
"v_cvt_pk_bf16_f32 v22, v232, v233\n"
"v_mfma_f32_16x16x32_bf16 v[0:3], a[172:175], v[76:79], v[0:3]\n"
"v_exp_f32 v234, v234\n"
"v_mfma_f32_16x16x32_bf16 v[4:7], a[176:179], v[64:67], v[24:27]\n"
"ds_read_b128 a[136:139], v240 offset:33280\n"
"v_lshlrev_b32 v216, 6, v248\n"
"v_mfma_f32_16x16x32_bf16 v[4:7], a[180:183], v[68:71], v[4:7]\n"
"s_and_b32 s48, s42, 7\n"
"v_exp_f32 v235, v235\n"
"v_mfma_f32_16x16x32_bf16 v[4:7], a[184:187], v[72:75], v[4:7]\n"
"s_mul_i32 s48, s48, 576\n"
"ds_read_b128 a[140:143], v240 offset:33536\n"
"v_cvt_pk_bf16_f32 v23, v234, v235\n"
"v_mfma_f32_16x16x32_bf16 v[4:7], a[188:191], v[76:79], v[4:7]\n"
"s_mov_b32 m0, s86\n"
"v_exp_f32 v0, v0\n"
"v_mfma_f32_16x16x32_bf16 a[96:99], a[192:195], v[20:23], a[96:99]\n"
"s_lshr_b32 s62, s42, 3\n"
"buffer_load_dwordx4 v246, s[8:11], s84 offen offset:0 lds\n"
"v_mfma_f32_16x16x32_bf16 a[100:103], a[196:199], v[20:23], a[100:103]\n"
"s_mul_i32 s62, s62, 24\n"
"v_exp_f32 v1, v1\n"
"v_mfma_f32_16x16x32_bf16 a[104:107], a[200:203], v[20:23], a[104:107]\n"
"s_add_u32 s48, s48, s62\n"
"ds_read_b128 a[144:147], v240 offset:36864\n"
"v_cvt_pk_bf16_f32 v16, v0, v1\n"
"v_mfma_f32_16x16x32_bf16 a[108:111], a[204:207], v[20:23], a[108:111]\n"
"s_cmp_lt_u32 s42, 192\n"
"s_cselect_b32 s74, s74, 0\n"
"s_cselect_b32 s66, s66, 0\n"
"s_cselect_b32 s70, s70, 0\n"
"v_exp_f32 v2, v2\n"
"v_mfma_f32_16x16x32_bf16 a[112:115], a[208:211], v[20:23], a[112:115]\n"
"s_mul_i32 s50, s48, 1152\n"
"ds_read_b128 a[148:151], v240 offset:37120\n"
"v_add_f32 v44, v44, v0\n"
"v_mfma_f32_16x16x32_bf16 a[116:119], a[212:215], v[20:23], a[116:119]\n"
"s_lshl_b32 s62, s41, 13\n"
"v_exp_f32 v3, v3\n"
"v_mfma_f32_16x16x32_bf16 a[120:123], a[216:219], v[20:23], a[120:123]\n"
"s_add_u32 s46, s50, s62\n"
"ds_read_b128 a[152:155], v240 offset:37376\n"
"v_cvt_pk_bf16_f32 v17, v2, v3\n"
"v_mfma_f32_16x16x32_bf16 a[124:127], a[220:223], v[20:23], a[124:127]\n"
"s_add_u32 s47, s46, 4096\n"
"v_exp_f32 v4, v4\n"
"v_mfma_f32_16x16x32_bf16 v[56:59], v[40:43], v[20:23], v[56:59]\n"
"ds_read_b128 a[156:159], v240 offset:37632\n"
"v_add_f32 v44, v44, v1\n"
"v_mfma_f32_16x16x32_bf16 v[8:11], a[160:163], v[80:83], v[28:31]\n"
"v_exp_f32 v5, v5\n"
"v_mfma_f32_16x16x32_bf16 v[8:11], a[164:167], v[84:87], v[8:11]\n"
"ds_read_b64_tr_b16 a[192:193], v242 offset:49152\n"
"v_cvt_pk_bf16_f32 v18, v4, v5\n"
"v_mfma_f32_16x16x32_bf16 v[8:11], a[168:171], v[88:91], v[8:11]\n"
"v_exp_f32 v6, v6\n"
"v_mfma_f32_16x16x32_bf16 v[8:11], a[172:175], v[92:95], v[8:11]\n"
"ds_read_b64_tr_b16 a[194:195], v242 offset:53248\n"
"v_add_f32 v44, v44, v2\n"
"v_mfma_f32_16x16x32_bf16 v[12:15], a[176:179], v[80:83], v[28:31]\n"
"v_exp_f32 v7, v7\n"
"v_mfma_f32_16x16x32_bf16 v[12:15], a[180:183], v[84:87], v[12:15]\n"
"ds_read_b64_tr_b16 a[196:197], v243 offset:49152\n"
"v_cvt_pk_bf16_f32 v19, v6, v7\n"
"v_mfma_f32_16x16x32_bf16 v[12:15], a[184:187], v[88:91], v[12:15]\n"
"buffer_load_dwordx4 v246, s[8:11], s84 offen offset:1024 lds\n"
"v_mfma_f32_16x16x32_bf16 v[12:15], a[188:191], v[92:95], v[12:15]\n"
"s_waitcnt lgkmcnt(11)\n"
"v_exp_f32 v8, v8\n"
"v_mfma_f32_16x16x32_bf16 a[0:3], a[224:227], v[16:19], a[0:3]\n"
"ds_read_b64_tr_b16 a[198:199], v243 offset:53248\n"
"buffer_load_dword v250, v216, s[72:75], s46 offen\n"
"v_add_f32 v44, v44, v3\n"
"v_mfma_f32_16x16x32_bf16 a[4:7], a[228:231], v[16:19], a[4:7]\n"
"v_exp_f32 v9, v9\n"
"v_mfma_f32_16x16x32_bf16 a[8:11], a[232:235], v[16:19], a[8:11]\n"
"ds_read_b64_tr_b16 a[200:201], v242 offset:49408\n"
"v_cvt_pk_bf16_f32 v20, v8, v9\n"
"v_mfma_f32_16x16x32_bf16 a[12:15], a[236:239], v[16:19], a[12:15]\n"
"v_exp_f32 v10, v10\n"
"v_mfma_f32_16x16x32_bf16 a[16:19], a[240:243], v[16:19], a[16:19]\n"
"ds_read_b64_tr_b16 a[202:203], v242 offset:53504\n"
"v_add_f32 v44, v44, v4\n"
"v_mfma_f32_16x16x32_bf16 a[20:23], a[244:247], v[16:19], a[20:23]\n"
"v_exp_f32 v11, v11\n"
"v_mfma_f32_16x16x32_bf16 a[24:27], a[248:251], v[16:19], a[24:27]\n"
"ds_read_b64_tr_b16 a[204:205], v243 offset:49408\n"
"v_cvt_pk_bf16_f32 v21, v10, v11\n"
"v_mfma_f32_16x16x32_bf16 a[28:31], a[252:255], v[16:19], a[28:31]\n"
"v_exp_f32 v12, v12\n"
"v_mfma_f32_16x16x32_bf16 v[220:223], a[160:163], v[96:99], v[32:35]\n"
"ds_read_b64_tr_b16 a[206:207], v243 offset:53504\n"
"v_add_f32 v44, v44, v5\n"
"v_mfma_f32_16x16x32_bf16 v[220:223], a[164:167], v[100:103], v[220:223]\n"
"v_exp_f32 v13, v13\n"
"v_mfma_f32_16x16x32_bf16 v[220:223], a[168:171], v[104:107], v[220:223]\n"
"ds_read_b64_tr_b16 a[208:209], v242 offset:49664\n"
"v_cvt_pk_bf16_f32 v22, v12, v13\n"
"v_mfma_f32_16x16x32_bf16 v[220:223], a[172:175], v[108:111], v[220:223]\n"
"v_exp_f32 v14, v14\n"
"v_mfma_f32_16x16x32_bf16 v[224:227], a[176:179], v[96:99], v[32:35]\n"
"ds_read_b64_tr_b16 a[210:211], v242 offset:53760\n"
"v_add_f32 v44, v44, v6\n"
"v_mfma_f32_16x16x32_bf16 v[224:227], a[180:183], v[100:103], v[224:227]\n"
"v_exp_f32 v15, v15\n"
"v_mfma_f32_16x16x32_bf16 v[224:227], a[184:187], v[104:107], v[224:227]\n"
"ds_read_b64_tr_b16 a[212:213], v243 offset:49664\n"
"v_cvt_pk_bf16_f32 v23, v14, v15\n"
"v_mfma_f32_16x16x32_bf16 v[224:227], a[188:191], v[108:111], v[224:227]\n"
"v_exp_f32 v220, v220\n"
"v_mfma_f32_16x16x32_bf16 a[32:35], a[224:227], v[20:23], a[32:35]\n"
"buffer_load_dwordx4 v247, s[8:11], s84 offen offset:2048 lds\n"
"v_mfma_f32_16x16x32_bf16 a[36:39], a[228:231], v[20:23], a[36:39]\n"
"v_exp_f32 v221, v221\n"
"v_mfma_f32_16x16x32_bf16 a[40:43], a[232:235], v[20:23], a[40:43]\n"
"ds_read_b64_tr_b16 a[214:215], v243 offset:53760\n"
"v_cvt_pk_bf16_f32 v16, v220, v221\n"
"v_mfma_f32_16x16x32_bf16 a[44:47], a[236:239], v[20:23], a[44:47]\n"
"v_exp_f32 v222, v222\n"
"v_mfma_f32_16x16x32_bf16 a[48:51], a[240:243], v[20:23], a[48:51]\n"
"ds_read_b64_tr_b16 a[216:217], v242 offset:49920\n"
"v_add_f32 v44, v44, v7\n"
"v_mfma_f32_16x16x32_bf16 a[52:55], a[244:247], v[20:23], a[52:55]\n"
"v_exp_f32 v223, v223\n"
"v_mfma_f32_16x16x32_bf16 a[56:59], a[248:251], v[20:23], a[56:59]\n"
"ds_read_b64_tr_b16 a[218:219], v242 offset:54016\n"
"v_cvt_pk_bf16_f32 v17, v222, v223\n"
"v_mfma_f32_16x16x32_bf16 a[60:63], a[252:255], v[20:23], a[60:63]\n"
"v_exp_f32 v224, v224\n"
"v_mfma_f32_16x16x32_bf16 v[48:51], v[40:43], v[20:23], v[48:51]\n"
"ds_read_b64_tr_b16 a[220:221], v243 offset:49920\n"
"buffer_load_dword v250, v216, s[72:75], s47 offen\n"
"v_add_f32 v52, v52, v220\n"
"v_mfma_f32_16x16x32_bf16 v[228:231], a[160:163], v[112:115], v[36:39]\n"
"v_exp_f32 v225, v225\n"
"v_mfma_f32_16x16x32_bf16 v[228:231], a[164:167], v[116:119], v[228:231]\n"
"ds_read_b64_tr_b16 a[222:223], v243 offset:54016\n"
"v_cvt_pk_bf16_f32 v18, v224, v225\n"
"v_mfma_f32_16x16x32_bf16 v[228:231], a[168:171], v[120:123], v[228:231]\n"
"v_exp_f32 v226, v226\n"
"v_mfma_f32_16x16x32_bf16 v[228:231], a[172:175], v[124:127], v[228:231]\n"
"v_add_f32 v52, v52, v221\n"
"v_add_f32 v52, v52, v222\n"
"v_mfma_f32_16x16x32_bf16 v[232:235], a[176:179], v[112:115], v[36:39]\n"
"v_exp_f32 v227, v227\n"
"v_mfma_f32_16x16x32_bf16 v[232:235], a[180:183], v[116:119], v[232:235]\n"
"v_cvt_pk_bf16_f32 v19, v226, v227\n"
"v_add_f32 v52, v52, v223\n"
"v_mfma_f32_16x16x32_bf16 v[232:235], a[184:187], v[120:123], v[232:235]\n"
"buffer_load_dwordx4 v247, s[8:11], s84 offen offset:3072 lds\n"
"v_lshrrev_b32 v251, 2, v248\n"
"v_mfma_f32_16x16x32_bf16 v[232:235], a[188:191], v[124:127], v[232:235]\n"
"v_exp_f32 v228, v228\n"
"v_mfma_f32_16x16x32_bf16 a[64:67], a[224:227], v[16:19], a[64:67]\n"
"v_add_f32 v52, v52, v224\n"
"v_add_f32 v52, v52, v225\n"
"v_mfma_f32_16x16x32_bf16 a[68:71], a[228:231], v[16:19], a[68:71]\n"
"v_exp_f32 v229, v229\n"
"v_mfma_f32_16x16x32_bf16 a[72:75], a[232:235], v[16:19], a[72:75]\n"
"v_cvt_pk_bf16_f32 v20, v228, v229\n"
"v_add_f32 v52, v52, v226\n"
"v_mfma_f32_16x16x32_bf16 a[76:79], a[236:239], v[16:19], a[76:79]\n"
"v_exp_f32 v230, v230\n"
"v_mfma_f32_16x16x32_bf16 a[80:83], a[240:243], v[16:19], a[80:83]\n"
"v_add_f32 v52, v52, v227\n"
"v_mul_u32_u24 v251, 9216, v251\n"
"v_mfma_f32_16x16x32_bf16 a[84:87], a[244:247], v[16:19], a[84:87]\n"
"v_exp_f32 v231, v231\n"
"v_mfma_f32_16x16x32_bf16 a[88:91], a[248:251], v[16:19], a[88:91]\n"
"s_waitcnt lgkmcnt(8)\n"
"v_cvt_pk_bf16_f32 v21, v230, v231\n"
"v_and_b32 v217, 3, v248\n"
"v_mfma_f32_16x16x32_bf16 a[92:95], a[252:255], v[16:19], a[92:95]\n"
"v_exp_f32 v232, v232\n"
"v_mfma_f32_16x16x32_bf16 v[0:3], a[128:131], v[64:67], v[24:27]\n"
"ds_read_b128 a[160:163], v240 offset:40960\n"
"v_add_u32 v255, 1, v217\n"
"v_mfma_f32_16x16x32_bf16 v[0:3], a[132:135], v[68:71], v[0:3]\n"
"v_exp_f32 v233, v233\n"
"v_mfma_f32_16x16x32_bf16 v[0:3], a[136:139], v[72:75], v[0:3]\n"
"ds_read_b128 a[164:167], v240 offset:41216\n"
"v_cvt_pk_bf16_f32 v22, v232, v233\n"
"v_mfma_f32_16x16x32_bf16 v[0:3], a[140:143], v[76:79], v[0:3]\n"
"v_exp_f32 v234, v234\n"
"v_mfma_f32_16x16x32_bf16 v[4:7], a[144:147], v[64:67], v[24:27]\n"
"ds_read_b128 a[168:171], v240 offset:41472\n"
"v_and_b32 v255, 4, v255\n"
"v_mfma_f32_16x16x32_bf16 v[4:7], a[148:151], v[68:71], v[4:7]\n"
"v_exp_f32 v235, v235\n"
"v_mfma_f32_16x16x32_bf16 v[4:7], a[152:155], v[72:75], v[4:7]\n"
"ds_read_b128 a[172:175], v240 offset:41728\n"
"v_cvt_pk_bf16_f32 v23, v234, v235\n"
"v_mfma_f32_16x16x32_bf16 v[4:7], a[156:159], v[76:79], v[4:7]\n"
"s_add_u32 m0, s86, 16384\n"
"v_exp_f32 v0, v0\n"
"v_mfma_f32_16x16x32_bf16 a[96:99], a[224:227], v[20:23], a[96:99]\n"
"buffer_load_dwordx4 v246, s[12:15], s84 offen offset:0 lds\n"
"v_lshlrev_b32 v255, 26, v255\n"
"v_mfma_f32_16x16x32_bf16 a[100:103], a[228:231], v[20:23], a[100:103]\n"
"v_exp_f32 v1, v1\n"
"v_mfma_f32_16x16x32_bf16 a[104:107], a[232:235], v[20:23], a[104:107]\n"
"ds_read_b128 a[176:179], v240 offset:45056\n"
"v_cvt_pk_bf16_f32 v16, v0, v1\n"
"v_mfma_f32_16x16x32_bf16 a[108:111], a[236:239], v[20:23], a[108:111]\n"
"v_exp_f32 v2, v2\n"
"v_mfma_f32_16x16x32_bf16 a[112:115], a[240:243], v[20:23], a[112:115]\n"
"ds_read_b128 a[180:183], v240 offset:45312\n"
"v_add_f32 v44, v44, v0\n"
"v_mfma_f32_16x16x32_bf16 a[116:119], a[244:247], v[20:23], a[116:119]\n"
"v_exp_f32 v3, v3\n"
"v_mfma_f32_16x16x32_bf16 a[120:123], a[248:251], v[20:23], a[120:123]\n"
"ds_read_b128 a[184:187], v240 offset:45568\n"
"v_cvt_pk_bf16_f32 v17, v2, v3\n"
"v_mfma_f32_16x16x32_bf16 a[124:127], a[252:255], v[20:23], a[124:127]\n"
"v_exp_f32 v4, v4\n"
"v_mfma_f32_16x16x32_bf16 v[56:59], v[40:43], v[20:23], v[56:59]\n"
"ds_read_b128 a[188:191], v240 offset:45824\n"
"v_add_f32 v44, v44, v1\n"
"v_mfma_f32_16x16x32_bf16 v[8:11], a[128:131], v[80:83], v[28:31]\n"
"v_exp_f32 v5, v5\n"
"v_mfma_f32_16x16x32_bf16 v[8:11], a[132:135], v[84:87], v[8:11]\n"
"ds_read_b64_tr_b16 a[224:225], v242 offset:57344\n"
"v_cvt_pk_bf16_f32 v18, v4, v5\n"
"v_mfma_f32_16x16x32_bf16 v[8:11], a[136:139], v[88:91], v[8:11]\n"
"v_exp_f32 v6, v6\n"
"v_mfma_f32_16x16x32_bf16 v[8:11], a[140:143], v[92:95], v[8:11]\n"
"ds_read_b64_tr_b16 a[226:227], v242 offset:61440\n"
"v_add_f32 v44, v44, v2\n"
"v_mfma_f32_16x16x32_bf16 v[12:15], a[144:147], v[80:83], v[28:31]\n"
"v_exp_f32 v7, v7\n"
"v_mfma_f32_16x16x32_bf16 v[12:15], a[148:151], v[84:87], v[12:15]\n"
"ds_read_b64_tr_b16 a[228:229], v243 offset:57344\n"
"v_cvt_pk_bf16_f32 v19, v6, v7\n"
"v_mfma_f32_16x16x32_bf16 v[12:15], a[152:155], v[88:91], v[12:15]\n"
"buffer_load_dwordx4 v247, s[12:15], s84 offen offset:1024 lds\n"
"v_lshl_add_u32 v217, v217, 4, v255\n"
"v_mfma_f32_16x16x32_bf16 v[12:15], a[156:159], v[92:95], v[12:15]\n"
"s_waitcnt lgkmcnt(11)\n"
"v_exp_f32 v8, v8\n"
"v_mfma_f32_16x16x32_bf16 a[0:3], a[192:195], v[16:19], a[0:3]\n"
"ds_read_b64_tr_b16 a[230:231], v243 offset:61440\n"
"v_add_f32 v44, v44, v3\n"
"v_mfma_f32_16x16x32_bf16 a[4:7], a[196:199], v[16:19], a[4:7]\n"
"v_exp_f32 v9, v9\n"
"v_mfma_f32_16x16x32_bf16 a[8:11], a[200:203], v[16:19], a[8:11]\n"
"ds_read_b64_tr_b16 a[232:233], v242 offset:57600\n"
"v_cvt_pk_bf16_f32 v20, v8, v9\n"
"v_mfma_f32_16x16x32_bf16 a[12:15], a[204:207], v[16:19], a[12:15]\n"
"v_exp_f32 v10, v10\n"
"v_mfma_f32_16x16x32_bf16 a[16:19], a[208:211], v[16:19], a[16:19]\n"
"ds_read_b64_tr_b16 a[234:235], v242 offset:61696\n"
"v_add_f32 v44, v44, v4\n"
"v_mfma_f32_16x16x32_bf16 a[20:23], a[212:215], v[16:19], a[20:23]\n"
"v_exp_f32 v11, v11\n"
"v_mfma_f32_16x16x32_bf16 a[24:27], a[216:219], v[16:19], a[24:27]\n"
"ds_read_b64_tr_b16 a[236:237], v243 offset:57600\n"
"v_cvt_pk_bf16_f32 v21, v10, v11\n"
"v_mfma_f32_16x16x32_bf16 a[28:31], a[220:223], v[16:19], a[28:31]\n"
"v_exp_f32 v12, v12\n"
"v_mfma_f32_16x16x32_bf16 v[220:223], a[128:131], v[96:99], v[32:35]\n"
"ds_read_b64_tr_b16 a[238:239], v243 offset:61696\n"
"v_add_f32 v44, v44, v5\n"
"v_mfma_f32_16x16x32_bf16 v[220:223], a[132:135], v[100:103], v[220:223]\n"
"v_exp_f32 v13, v13\n"
"v_mfma_f32_16x16x32_bf16 v[220:223], a[136:139], v[104:107], v[220:223]\n"
"ds_read_b64_tr_b16 a[240:241], v242 offset:57856\n"
"v_cvt_pk_bf16_f32 v22, v12, v13\n"
"v_mfma_f32_16x16x32_bf16 v[220:223], a[140:143], v[108:111], v[220:223]\n"
"v_exp_f32 v14, v14\n"
"v_mfma_f32_16x16x32_bf16 v[224:227], a[144:147], v[96:99], v[32:35]\n"
"ds_read_b64_tr_b16 a[242:243], v242 offset:61952\n"
"v_add_f32 v44, v44, v6\n"
"v_mfma_f32_16x16x32_bf16 v[224:227], a[148:151], v[100:103], v[224:227]\n"
"v_exp_f32 v15, v15\n"
"v_mfma_f32_16x16x32_bf16 v[224:227], a[152:155], v[104:107], v[224:227]\n"
"ds_read_b64_tr_b16 a[244:245], v243 offset:57856\n"
"v_cvt_pk_bf16_f32 v23, v14, v15\n"
"v_mfma_f32_16x16x32_bf16 v[224:227], a[156:159], v[108:111], v[224:227]\n"
"v_exp_f32 v220, v220\n"
"v_mfma_f32_16x16x32_bf16 a[32:35], a[192:195], v[20:23], a[32:35]\n"
"buffer_load_dwordx4 v246, s[12:15], s84 offen offset:2048 lds\n"
"v_lshl_add_u32 v252, v251, 1, v217\n"
"v_mfma_f32_16x16x32_bf16 a[36:39], a[196:199], v[20:23], a[36:39]\n"
"v_exp_f32 v221, v221\n"
"v_mfma_f32_16x16x32_bf16 a[40:43], a[200:203], v[20:23], a[40:43]\n"
"ds_read_b64_tr_b16 a[246:247], v243 offset:61952\n"
"v_cvt_pk_bf16_f32 v16, v220, v221\n"
"v_mfma_f32_16x16x32_bf16 a[44:47], a[204:207], v[20:23], a[44:47]\n"
"v_exp_f32 v222, v222\n"
"v_mfma_f32_16x16x32_bf16 a[48:51], a[208:211], v[20:23], a[48:51]\n"
"ds_read_b64_tr_b16 a[248:249], v242 offset:58112\n"
"v_add_f32 v44, v44, v7\n"
"v_mfma_f32_16x16x32_bf16 a[52:55], a[212:215], v[20:23], a[52:55]\n"
"v_exp_f32 v223, v223\n"
"v_mfma_f32_16x16x32_bf16 a[56:59], a[216:219], v[20:23], a[56:59]\n"
"ds_read_b64_tr_b16 a[250:251], v242 offset:62208\n"
"v_cvt_pk_bf16_f32 v17, v222, v223\n"
"v_mfma_f32_16x16x32_bf16 a[60:63], a[220:223], v[20:23], a[60:63]\n"
"v_exp_f32 v224, v224\n"
"v_mfma_f32_16x16x32_bf16 v[48:51], v[40:43], v[20:23], v[48:51]\n"
"ds_read_b64_tr_b16 a[252:253], v243 offset:58112\n"
"v_add_f32 v52, v52, v220\n"
"v_mfma_f32_16x16x32_bf16 v[228:231], a[128:131], v[112:115], v[36:39]\n"
"v_exp_f32 v225, v225\n"
"v_mfma_f32_16x16x32_bf16 v[228:231], a[132:135], v[116:119], v[228:231]\n"
"ds_read_b64_tr_b16 a[254:255], v243 offset:62208\n"
"v_cvt_pk_bf16_f32 v18, v224, v225\n"
"v_mfma_f32_16x16x32_bf16 v[228:231], a[136:139], v[120:123], v[228:231]\n"
"v_exp_f32 v226, v226\n"
"v_mfma_f32_16x16x32_bf16 v[228:231], a[140:143], v[124:127], v[228:231]\n"
"v_add_f32 v52, v52, v221\n"
"v_add_f32 v52, v52, v222\n"
"v_mfma_f32_16x16x32_bf16 v[232:235], a[144:147], v[112:115], v[36:39]\n"
"v_exp_f32 v227, v227\n"
"v_mfma_f32_16x16x32_bf16 v[232:235], a[148:151], v[116:119], v[232:235]\n"
"v_cvt_pk_bf16_f32 v19, v226, v227\n"
"v_add_f32 v52, v52, v223\n"
"v_mfma_f32_16x16x32_bf16 v[232:235], a[152:155], v[120:123], v[232:235]\n"
"buffer_load_dwordx4 v247, s[12:15], s84 offen offset:3072 lds\n"
"v_add_u32 v251, v251, v217\n"
"v_mfma_f32_16x16x32_bf16 v[232:235], a[156:159], v[124:127], v[232:235]\n"
"s_add_u32 s84, s84, 16384\n"
"v_exp_f32 v228, v228\n"
"v_mfma_f32_16x16x32_bf16 a[64:67], a[192:195], v[16:19], a[64:67]\n"
"s_mul_i32 s62, s41, 1327104\n"
"v_add_f32 v52, v52, v224\n"
"v_add_f32 v52, v52, v225\n"
"v_mfma_f32_16x16x32_bf16 a[68:71], a[196:199], v[16:19], a[68:71]\n"
"s_lshl_b32 s49, s48, 1\n"
"v_exp_f32 v229, v229\n"
"v_mfma_f32_16x16x32_bf16 a[72:75], a[200:203], v[16:19], a[72:75]\n"
"s_add_u32 s49, s49, s62\n"
"v_cvt_pk_bf16_f32 v20, v228, v229\n"
"v_add_f32 v52, v52, v226\n"
"v_mfma_f32_16x16x32_bf16 a[76:79], a[204:207], v[16:19], a[76:79]\n"
"v_exp_f32 v230, v230\n"
"v_mfma_f32_16x16x32_bf16 a[80:83], a[208:211], v[16:19], a[80:83]\n"
"buffer_load_dwordx4 v[128:131], v252, s[64:67], s49 offen\n"
"v_add_f32 v52, v52, v227\n"
"v_mfma_f32_16x16x32_bf16 a[84:87], a[212:215], v[16:19], a[84:87]\n"
"v_exp_f32 v231, v231\n"
"v_mfma_f32_16x16x32_bf16 a[88:91], a[216:219], v[16:19], a[88:91]\n"
"s_waitcnt vmcnt(20) lgkmcnt(0)\n"
"s_barrier\n"
"v_cvt_pk_bf16_f32 v21, v230, v231\n"
"v_mul_u32_u24 v249, 3641, v248\n"
"v_mfma_f32_16x16x32_bf16 a[92:95], a[220:223], v[16:19], a[92:95]\n"
"s_movk_i32 s60, 864\n"
"v_exp_f32 v232, v232\n"
"v_mfma_f32_16x16x32_bf16 v[0:3], a[160:163], v[64:67], v[24:27]\n"
"ds_read_b128 a[128:131], v241 offset:0\n"
"v_add_u32 v206, 4096, v207\n"
"v_mfma_f32_16x16x32_bf16 v[0:3], a[164:167], v[68:71], v[0:3]\n"
"v_exp_f32 v233, v233\n"
"v_mfma_f32_16x16x32_bf16 v[0:3], a[168:171], v[72:75], v[0:3]\n"
"ds_read_b128 a[132:135], v241 offset:256\n"
"v_cvt_pk_bf16_f32 v22, v232, v233\n"
"v_mfma_f32_16x16x32_bf16 v[0:3], a[172:175], v[76:79], v[0:3]\n"
"v_exp_f32 v234, v234\n"
"v_mfma_f32_16x16x32_bf16 v[4:7], a[176:179], v[64:67], v[24:27]\n"
"ds_read_b128 a[136:139], v241 offset:512\n"
"v_lshrrev_b32 v217, 16, v249\n"
"v_mfma_f32_16x16x32_bf16 v[4:7], a[180:183], v[68:71], v[4:7]\n"
"v_exp_f32 v235, v235\n"
"v_mfma_f32_16x16x32_bf16 v[4:7], a[184:187], v[72:75], v[4:7]\n"
"ds_read_b128 a[140:143], v241 offset:768\n"
"v_cvt_pk_bf16_f32 v23, v234, v235\n"
"v_mfma_f32_16x16x32_bf16 v[4:7], a[188:191], v[76:79], v[4:7]\n"
"s_add_u32 m0, s86, 32768\n"
"v_exp_f32 v0, v0\n"
"v_mfma_f32_16x16x32_bf16 a[96:99], a[192:195], v[20:23], a[96:99]\n"
"buffer_load_dwordx4 v246, s[8:11], s84 offen offset:0 lds\n"
"v_mad_u32_u24 v210, v217, s60, v207\n"
"v_mfma_f32_16x16x32_bf16 a[100:103], a[196:199], v[20:23], a[100:103]\n"
"v_exp_f32 v1, v1\n"
"v_mfma_f32_16x16x32_bf16 a[104:107], a[200:203], v[20:23], a[104:107]\n"
"ds_read_b128 a[144:147], v241 offset:4096\n"
"v_cvt_pk_bf16_f32 v16, v0, v1\n"
"v_mfma_f32_16x16x32_bf16 a[108:111], a[204:207], v[20:23], a[108:111]\n"
"v_exp_f32 v2, v2\n"
"v_mfma_f32_16x16x32_bf16 a[112:115], a[208:211], v[20:23], a[112:115]\n"
"ds_read_b128 a[148:151], v241 offset:4352\n"
"v_add_f32 v44, v44, v0\n"
"v_mfma_f32_16x16x32_bf16 a[116:119], a[212:215], v[20:23], a[116:119]\n"
"v_exp_f32 v3, v3\n"
"v_mfma_f32_16x16x32_bf16 a[120:123], a[216:219], v[20:23], a[120:123]\n"
"ds_read_b128 a[152:155], v241 offset:4608\n"
"v_cvt_pk_bf16_f32 v17, v2, v3\n"
"v_mfma_f32_16x16x32_bf16 a[124:127], a[220:223], v[20:23], a[124:127]\n"
"v_exp_f32 v4, v4\n"
"v_mfma_f32_16x16x32_bf16 v[56:59], v[40:43], v[20:23], v[56:59]\n"
"ds_read_b128 a[156:159], v241 offset:4864\n"
"v_add_f32 v44, v44, v1\n"
"v_mfma_f32_16x16x32_bf16 v[8:11], a[160:163], v[80:83], v[28:31]\n"
"v_exp_f32 v5, v5\n"
"v_mfma_f32_16x16x32_bf16 v[8:11], a[164:167], v[84:87], v[8:11]\n"
"ds_read_b64_tr_b16 a[192:193], v244 offset:16384\n"
"v_cvt_pk_bf16_f32 v18, v4, v5\n"
"v_mfma_f32_16x16x32_bf16 v[8:11], a[168:171], v[88:91], v[8:11]\n"
"v_exp_f32 v6, v6\n"
"v_mfma_f32_16x16x32_bf16 v[8:11], a[172:175], v[92:95], v[8:11]\n"
"ds_read_b64_tr_b16 a[194:195], v244 offset:20480\n"
"v_add_f32 v44, v44, v2\n"
"v_mfma_f32_16x16x32_bf16 v[12:15], a[176:179], v[80:83], v[28:31]\n"
"v_exp_f32 v7, v7\n"
"v_mfma_f32_16x16x32_bf16 v[12:15], a[180:183], v[84:87], v[12:15]\n"
"ds_read_b64_tr_b16 a[196:197], v245 offset:16384\n"
"v_cvt_pk_bf16_f32 v19, v6, v7\n"
"v_mfma_f32_16x16x32_bf16 v[12:15], a[184:187], v[88:91], v[12:15]\n"
"buffer_load_dwordx4 v246, s[8:11], s84 offen offset:1024 lds\n"
"v_add_u32 v249, 233024, v249\n"
"v_mfma_f32_16x16x32_bf16 v[12:15], a[188:191], v[92:95], v[12:15]\n"
"s_waitcnt lgkmcnt(11)\n"
"v_exp_f32 v8, v8\n"
"v_mfma_f32_16x16x32_bf16 a[0:3], a[224:227], v[16:19], a[0:3]\n"
"ds_read_b64_tr_b16 a[198:199], v245 offset:20480\n"
"v_add_f32 v44, v44, v3\n"
"v_mfma_f32_16x16x32_bf16 a[4:7], a[228:231], v[16:19], a[4:7]\n"
"v_exp_f32 v9, v9\n"
"v_mfma_f32_16x16x32_bf16 a[8:11], a[232:235], v[16:19], a[8:11]\n"
"ds_read_b64_tr_b16 a[200:201], v244 offset:16640\n"
"v_cvt_pk_bf16_f32 v20, v8, v9\n"
"v_mfma_f32_16x16x32_bf16 a[12:15], a[236:239], v[16:19], a[12:15]\n"
"v_exp_f32 v10, v10\n"
"v_mfma_f32_16x16x32_bf16 a[16:19], a[240:243], v[16:19], a[16:19]\n"
"ds_read_b64_tr_b16 a[202:203], v244 offset:20736\n"
"v_add_f32 v44, v44, v4\n"
"v_mfma_f32_16x16x32_bf16 a[20:23], a[244:247], v[16:19], a[20:23]\n"
"v_exp_f32 v11, v11\n"
"v_mfma_f32_16x16x32_bf16 a[24:27], a[248:251], v[16:19], a[24:27]\n"
"ds_read_b64_tr_b16 a[204:205], v245 offset:16640\n"
"v_cvt_pk_bf16_f32 v21, v10, v11\n"
"v_mfma_f32_16x16x32_bf16 a[28:31], a[252:255], v[16:19], a[28:31]\n"
"v_exp_f32 v12, v12\n"
"v_mfma_f32_16x16x32_bf16 v[220:223], a[160:163], v[96:99], v[32:35]\n"
"ds_read_b64_tr_b16 a[206:207], v245 offset:20736\n"
"v_add_f32 v44, v44, v5\n"
"v_mfma_f32_16x16x32_bf16 v[220:223], a[164:167], v[100:103], v[220:223]\n"
"v_exp_f32 v13, v13\n"
"v_mfma_f32_16x16x32_bf16 v[220:223], a[168:171], v[104:107], v[220:223]\n"
"ds_read_b64_tr_b16 a[208:209], v244 offset:16896\n"
"v_cvt_pk_bf16_f32 v22, v12, v13\n"
"v_mfma_f32_16x16x32_bf16 v[220:223], a[172:175], v[108:111], v[220:223]\n"
"v_exp_f32 v14, v14\n"
"v_mfma_f32_16x16x32_bf16 v[224:227], a[176:179], v[96:99], v[32:35]\n"
"ds_read_b64_tr_b16 a[210:211], v244 offset:20992\n"
"v_add_f32 v44, v44, v6\n"
"v_mfma_f32_16x16x32_bf16 v[224:227], a[180:183], v[100:103], v[224:227]\n"
"v_exp_f32 v15, v15\n"
"v_mfma_f32_16x16x32_bf16 v[224:227], a[184:187], v[104:107], v[224:227]\n"
"ds_read_b64_tr_b16 a[212:213], v245 offset:16896\n"
"v_cvt_pk_bf16_f32 v23, v14, v15\n"
"v_mfma_f32_16x16x32_bf16 v[224:227], a[188:191], v[108:111], v[224:227]\n"
"v_exp_f32 v220, v220\n"
"v_mfma_f32_16x16x32_bf16 a[32:35], a[224:227], v[20:23], a[32:35]\n"
"buffer_load_dwordx4 v247, s[8:11], s84 offen offset:2048 lds\n"
"v_lshrrev_b32 v217, 16, v249\n"
"v_mfma_f32_16x16x32_bf16 a[36:39], a[228:231], v[20:23], a[36:39]\n"
"v_exp_f32 v221, v221\n"
"v_mfma_f32_16x16x32_bf16 a[40:43], a[232:235], v[20:23], a[40:43]\n"
"ds_read_b64_tr_b16 a[214:215], v245 offset:20992\n"
"v_cvt_pk_bf16_f32 v16, v220, v221\n"
"v_mfma_f32_16x16x32_bf16 a[44:47], a[236:239], v[20:23], a[44:47]\n"
"v_exp_f32 v222, v222\n"
"v_mfma_f32_16x16x32_bf16 a[48:51], a[240:243], v[20:23], a[48:51]\n"
"ds_read_b64_tr_b16 a[216:217], v244 offset:17152\n"
"v_add_f32 v44, v44, v7\n"
"v_mfma_f32_16x16x32_bf16 a[52:55], a[244:247], v[20:23], a[52:55]\n"
"v_exp_f32 v223, v223\n"
"v_mfma_f32_16x16x32_bf16 a[56:59], a[248:251], v[20:23], a[56:59]\n"
"ds_read_b64_tr_b16 a[218:219], v244 offset:21248\n"
"v_cvt_pk_bf16_f32 v17, v222, v223\n"
"v_mfma_f32_16x16x32_bf16 a[60:63], a[252:255], v[20:23], a[60:63]\n"
"v_exp_f32 v224, v224\n"
"v_mfma_f32_16x16x32_bf16 v[48:51], v[40:43], v[20:23], v[48:51]\n"
"ds_read_b64_tr_b16 a[220:221], v245 offset:17152\n"
"v_add_f32 v52, v52, v220\n"
"v_mfma_f32_16x16x32_bf16 v[228:231], a[160:163], v[112:115], v[36:39]\n"
"v_exp_f32 v225, v225\n"
"v_mfma_f32_16x16x32_bf16 v[228:231], a[164:167], v[116:119], v[228:231]\n"
"ds_read_b64_tr_b16 a[222:223], v245 offset:21248\n"
"v_cvt_pk_bf16_f32 v18, v224, v225\n"
"v_mfma_f32_16x16x32_bf16 v[228:231], a[168:171], v[120:123], v[228:231]\n"
"v_exp_f32 v226, v226\n"
"v_mfma_f32_16x16x32_bf16 v[228:231], a[172:175], v[124:127], v[228:231]\n"
"v_add_f32 v52, v52, v221\n"
"v_add_f32 v52, v52, v222\n"
"v_mfma_f32_16x16x32_bf16 v[232:235], a[176:179], v[112:115], v[36:39]\n"
"v_exp_f32 v227, v227\n"
"v_mfma_f32_16x16x32_bf16 v[232:235], a[180:183], v[116:119], v[232:235]\n"
"v_cvt_pk_bf16_f32 v19, v226, v227\n"
"v_add_f32 v52, v52, v223\n"
"v_mfma_f32_16x16x32_bf16 v[232:235], a[184:187], v[120:123], v[232:235]\n"
"buffer_load_dwordx4 v247, s[8:11], s84 offen offset:3072 lds\n"
"v_mad_u32_u24 v211, v217, s60, v207\n"
"v_mfma_f32_16x16x32_bf16 v[232:235], a[188:191], v[124:127], v[232:235]\n"
"v_exp_f32 v228, v228\n"
"v_mfma_f32_16x16x32_bf16 a[64:67], a[224:227], v[16:19], a[64:67]\n"
"v_add_f32 v52, v52, v224\n"
"v_add_f32 v52, v52, v225\n"
"v_mfma_f32_16x16x32_bf16 a[68:71], a[228:231], v[16:19], a[68:71]\n"
"v_exp_f32 v229, v229\n"
"v_mfma_f32_16x16x32_bf16 a[72:75], a[232:235], v[16:19], a[72:75]\n"
"v_cvt_pk_bf16_f32 v20, v228, v229\n"
"v_add_f32 v52, v52, v226\n"
"v_mfma_f32_16x16x32_bf16 a[76:79], a[236:239], v[16:19], a[76:79]\n"
"v_exp_f32 v230, v230\n"
"v_mfma_f32_16x16x32_bf16 a[80:83], a[240:243], v[16:19], a[80:83]\n"
"v_add_f32 v52, v52, v227\n"
"v_add_u32 v249, 233024, v249\n"
"v_mfma_f32_16x16x32_bf16 a[84:87], a[244:247], v[16:19], a[84:87]\n"
"v_exp_f32 v231, v231\n"
"v_mfma_f32_16x16x32_bf16 a[88:91], a[248:251], v[16:19], a[88:91]\n"
"s_waitcnt lgkmcnt(8)\n"
"v_cvt_pk_bf16_f32 v21, v230, v231\n"
"v_lshrrev_b32 v217, 16, v249\n"
"v_mfma_f32_16x16x32_bf16 a[92:95], a[252:255], v[16:19], a[92:95]\n"
"v_exp_f32 v232, v232\n"
"v_mfma_f32_16x16x32_bf16 v[0:3], a[128:131], v[64:67], v[24:27]\n"
"ds_read_b128 a[160:163], v241 offset:8192\n"
"v_mad_u32_u24 v212, v217, s60, v207\n"
"v_mfma_f32_16x16x32_bf16 v[0:3], a[132:135], v[68:71], v[0:3]\n"
"v_exp_f32 v233, v233\n"
"v_mfma_f32_16x16x32_bf16 v[0:3], a[136:139], v[72:75], v[0:3]\n"
"ds_read_b128 a[164:167], v241 offset:8448\n"
"v_cvt_pk_bf16_f32 v22, v232, v233\n"
"v_mfma_f32_16x16x32_bf16 v[0:3], a[140:143], v[76:79], v[0:3]\n"
"v_exp_f32 v234, v234\n"
"v_mfma_f32_16x16x32_bf16 v[4:7], a[144:147], v[64:67], v[24:27]\n"
"ds_read_b128 a[168:171], v241 offset:8704\n"
"v_add_u32 v249, 233024, v249\n"
"v_mfma_f32_16x16x32_bf16 v[4:7], a[148:151], v[68:71], v[4:7]\n"
"v_exp_f32 v235, v235\n"
"v_mfma_f32_16x16x32_bf16 v[4:7], a[152:155], v[72:75], v[4:7]\n"
"ds_read_b128 a[172:175], v241 offset:8960\n"
"v_cvt_pk_bf16_f32 v23, v234, v235\n"
"v_mfma_f32_16x16x32_bf16 v[4:7], a[156:159], v[76:79], v[4:7]\n"
"s_add_u32 m0, s86, 49152\n"
"v_exp_f32 v0, v0\n"
"v_mfma_f32_16x16x32_bf16 a[96:99], a[224:227], v[20:23], a[96:99]\n"
"buffer_load_dwordx4 v246, s[12:15], s84 offen offset:0 lds\n"
"v_lshrrev_b32 v217, 16, v249\n"
"v_mfma_f32_16x16x32_bf16 a[100:103], a[228:231], v[20:23], a[100:103]\n"
"v_exp_f32 v1, v1\n"
"v_mfma_f32_16x16x32_bf16 a[104:107], a[232:235], v[20:23], a[104:107]\n"
"ds_read_b128 a[176:179], v241 offset:12288\n"
"v_cvt_pk_bf16_f32 v16, v0, v1\n"
"v_mfma_f32_16x16x32_bf16 a[108:111], a[236:239], v[20:23], a[108:111]\n"
"v_exp_f32 v2, v2\n"
"v_mfma_f32_16x16x32_bf16 a[112:115], a[240:243], v[20:23], a[112:115]\n"
"ds_read_b128 a[180:183], v241 offset:12544\n"
"v_add_f32 v44, v44, v0\n"
"v_mfma_f32_16x16x32_bf16 a[116:119], a[244:247], v[20:23], a[116:119]\n"
"v_exp_f32 v3, v3\n"
"v_mfma_f32_16x16x32_bf16 a[120:123], a[248:251], v[20:23], a[120:123]\n"
"ds_read_b128 a[184:187], v241 offset:12800\n"
"v_cvt_pk_bf16_f32 v17, v2, v3\n"
"v_mfma_f32_16x16x32_bf16 a[124:127], a[252:255], v[20:23], a[124:127]\n"
"v_exp_f32 v4, v4\n"
"v_mfma_f32_16x16x32_bf16 v[56:59], v[40:43], v[20:23], v[56:59]\n"
"ds_read_b128 a[188:191], v241 offset:13056\n"
"v_add_f32 v44, v44, v1\n"
"v_mfma_f32_16x16x32_bf16 v[8:11], a[128:131], v[80:83], v[28:31]\n"
"v_exp_f32 v5, v5\n"
"v_mfma_f32_16x16x32_bf16 v[8:11], a[132:135], v[84:87], v[8:11]\n"
"ds_read_b64_tr_b16 a[224:225], v244 offset:24576\n"
"v_cvt_pk_bf16_f32 v18, v4, v5\n"
"v_mfma_f32_16x16x32_bf16 v[8:11], a[136:139], v[88:91], v[8:11]\n"
"v_exp_f32 v6, v6\n"
"v_mfma_f32_16x16x32_bf16 v[8:11], a[140:143], v[92:95], v[8:11]\n"
"ds_read_b64_tr_b16 a[226:227], v244 offset:28672\n"
"v_add_f32 v44, v44, v2\n"
"v_mfma_f32_16x16x32_bf16 v[12:15], a[144:147], v[80:83], v[28:31]\n"
"v_exp_f32 v7, v7\n"
"v_mfma_f32_16x16x32_bf16 v[12:15], a[148:151], v[84:87], v[12:15]\n"
"ds_read_b64_tr_b16 a[228:229], v245 offset:24576\n"
"v_cvt_pk_bf16_f32 v19, v6, v7\n"
"v_mfma_f32_16x16x32_bf16 v[12:15], a[152:155], v[88:91], v[12:15]\n"
"buffer_load_dwordx4 v247, s[12:15], s84 offen offset:1024 lds\n"
"v_mad_u32_u24 v213, v217, s60, v207\n"
"v_mfma_f32_16x16x32_bf16 v[12:15], a[156:159], v[92:95], v[12:15]\n"
"s_waitcnt lgkmcnt(11)\n"
"v_exp_f32 v8, v8\n"
"v_mfma_f32_16x16x32_bf16 a[0:3], a[192:195], v[16:19], a[0:3]\n"
"ds_read_b64_tr_b16 a[230:231], v245 offset:28672\n"
"v_add_f32 v44, v44, v3\n"
"v_mfma_f32_16x16x32_bf16 a[4:7], a[196:199], v[16:19], a[4:7]\n"
"v_exp_f32 v9, v9\n"
"v_mfma_f32_16x16x32_bf16 a[8:11], a[200:203], v[16:19], a[8:11]\n"
"ds_read_b64_tr_b16 a[232:233], v244 offset:24832\n"
"v_cvt_pk_bf16_f32 v20, v8, v9\n"
"v_mfma_f32_16x16x32_bf16 a[12:15], a[204:207], v[16:19], a[12:15]\n"
"v_exp_f32 v10, v10\n"
"v_mfma_f32_16x16x32_bf16 a[16:19], a[208:211], v[16:19], a[16:19]\n"
"ds_read_b64_tr_b16 a[234:235], v244 offset:28928\n"
"v_add_f32 v44, v44, v4\n"
"v_mfma_f32_16x16x32_bf16 a[20:23], a[212:215], v[16:19], a[20:23]\n"
"v_exp_f32 v11, v11\n"
"v_mfma_f32_16x16x32_bf16 a[24:27], a[216:219], v[16:19], a[24:27]\n"
"ds_read_b64_tr_b16 a[236:237], v245 offset:24832\n"
"v_cvt_pk_bf16_f32 v21, v10, v11\n"
"v_mfma_f32_16x16x32_bf16 a[28:31], a[220:223], v[16:19], a[28:31]\n"
"v_exp_f32 v12, v12\n"
"v_mfma_f32_16x16x32_bf16 v[220:223], a[128:131], v[96:99], v[32:35]\n"
"ds_read_b64_tr_b16 a[238:239], v245 offset:28928\n"
"v_add_f32 v44, v44, v5\n"
"v_mfma_f32_16x16x32_bf16 v[220:223], a[132:135], v[100:103], v[220:223]\n"
"v_exp_f32 v13, v13\n"
"v_mfma_f32_16x16x32_bf16 v[220:223], a[136:139], v[104:107], v[220:223]\n"
"ds_read_b64_tr_b16 a[240:241], v244 offset:25088\n"
"v_cvt_pk_bf16_f32 v22, v12, v13\n"
"v_mfma_f32_16x16x32_bf16 v[220:223], a[140:143], v[108:111], v[220:223]\n"
"v_exp_f32 v14, v14\n"
"v_mfma_f32_16x16x32_bf16 v[224:227], a[144:147], v[96:99], v[32:35]\n"
"ds_read_b64_tr_b16 a[242:243], v244 offset:29184\n"
"v_add_f32 v44, v44, v6\n"
"v_mfma_f32_16x16x32_bf16 v[224:227], a[148:151], v[100:103], v[224:227]\n"
"v_exp_f32 v15, v15\n"
"v_mfma_f32_16x16x32_bf16 v[224:227], a[152:155], v[104:107], v[224:227]\n"
"ds_read_b64_tr_b16 a[244:245], v245 offset:25088\n"
"v_cvt_pk_bf16_f32 v23, v14, v15\n"
"v_mfma_f32_16x16x32_bf16 v[224:227], a[156:159], v[108:111], v[224:227]\n"
"v_exp_f32 v220, v220\n"
"v_mfma_f32_16x16x32_bf16 a[32:35], a[192:195], v[20:23], a[32:35]\n"
"buffer_load_dwordx4 v246, s[12:15], s84 offen offset:2048 lds\n"
"v_add_u32 v249, 233024, v249\n"
"v_mfma_f32_16x16x32_bf16 a[36:39], a[196:199], v[20:23], a[36:39]\n"
"v_exp_f32 v221, v221\n"
"v_mfma_f32_16x16x32_bf16 a[40:43], a[200:203], v[20:23], a[40:43]\n"
"ds_read_b64_tr_b16 a[246:247], v245 offset:29184\n"
"v_cvt_pk_bf16_f32 v16, v220, v221\n"
"v_mfma_f32_16x16x32_bf16 a[44:47], a[204:207], v[20:23], a[44:47]\n"
"v_exp_f32 v222, v222\n"
"v_mfma_f32_16x16x32_bf16 a[48:51], a[208:211], v[20:23], a[48:51]\n"
"ds_read_b64_tr_b16 a[248:249], v244 offset:25344\n"
"v_add_f32 v44, v44, v7\n"
"v_mfma_f32_16x16x32_bf16 a[52:55], a[212:215], v[20:23], a[52:55]\n"
"v_exp_f32 v223, v223\n"
"v_mfma_f32_16x16x32_bf16 a[56:59], a[216:219], v[20:23], a[56:59]\n"
"ds_read_b64_tr_b16 a[250:251], v244 offset:29440\n"
"v_cvt_pk_bf16_f32 v17, v222, v223\n"
"v_mfma_f32_16x16x32_bf16 a[60:63], a[220:223], v[20:23], a[60:63]\n"
"v_exp_f32 v224, v224\n"
"v_mfma_f32_16x16x32_bf16 v[48:51], v[40:43], v[20:23], v[48:51]\n"
"ds_read_b64_tr_b16 a[252:253], v245 offset:25344\n"
"v_add_f32 v52, v52, v220\n"
"v_mfma_f32_16x16x32_bf16 v[228:231], a[128:131], v[112:115], v[36:39]\n"
"v_exp_f32 v225, v225\n"
"v_mfma_f32_16x16x32_bf16 v[228:231], a[132:135], v[116:119], v[228:231]\n"
"ds_read_b64_tr_b16 a[254:255], v245 offset:29440\n"
"v_cvt_pk_bf16_f32 v18, v224, v225\n"
"v_mfma_f32_16x16x32_bf16 v[228:231], a[136:139], v[120:123], v[228:231]\n"
"v_exp_f32 v226, v226\n"
"v_mfma_f32_16x16x32_bf16 v[228:231], a[140:143], v[124:127], v[228:231]\n"
"v_add_f32 v52, v52, v221\n"
"v_add_f32 v52, v52, v222\n"
"v_mfma_f32_16x16x32_bf16 v[232:235], a[144:147], v[112:115], v[36:39]\n"
"v_exp_f32 v227, v227\n"
"v_mfma_f32_16x16x32_bf16 v[232:235], a[148:151], v[116:119], v[232:235]\n"
"v_cvt_pk_bf16_f32 v19, v226, v227\n"
"v_add_f32 v52, v52, v223\n"
"v_mfma_f32_16x16x32_bf16 v[232:235], a[152:155], v[120:123], v[232:235]\n"
"buffer_load_dwordx4 v247, s[12:15], s84 offen offset:3072 lds\n"
"v_lshrrev_b32 v217, 16, v249\n"
"v_mfma_f32_16x16x32_bf16 v[232:235], a[156:159], v[124:127], v[232:235]\n"
"s_add_u32 s84, s84, 16384\n"
"v_exp_f32 v228, v228\n"
"v_mfma_f32_16x16x32_bf16 a[64:67], a[192:195], v[16:19], a[64:67]\n"
"buffer_load_dwordx4 v[164:167], v252, s[68:71], s49 offen\n"
"v_add_f32 v52, v52, v224\n"
"v_add_f32 v52, v52, v225\n"
"v_mfma_f32_16x16x32_bf16 a[68:71], a[196:199], v[16:19], a[68:71]\n"
"v_exp_f32 v229, v229\n"
"v_mfma_f32_16x16x32_bf16 a[72:75], a[200:203], v[16:19], a[72:75]\n"
"v_cvt_pk_bf16_f32 v20, v228, v229\n"
"v_add_f32 v52, v52, v226\n"
"v_mfma_f32_16x16x32_bf16 a[76:79], a[204:207], v[16:19], a[76:79]\n"
"v_exp_f32 v230, v230\n"
"v_mfma_f32_16x16x32_bf16 a[80:83], a[208:211], v[16:19], a[80:83]\n"
"v_add_f32 v52, v52, v227\n"
"v_mad_u32_u24 v214, v217, s60, v206\n"
"v_mfma_f32_16x16x32_bf16 a[84:87], a[212:215], v[16:19], a[84:87]\n"
"v_exp_f32 v231, v231\n"
"v_mfma_f32_16x16x32_bf16 a[88:91], a[216:219], v[16:19], a[88:91]\n"
"s_waitcnt vmcnt(21) lgkmcnt(0)\n"
"s_barrier\n"
"v_cvt_pk_bf16_f32 v21, v230, v231\n"
"v_add_u32 v249, 233024, v249\n"
"v_mfma_f32_16x16x32_bf16 a[92:95], a[220:223], v[16:19], a[92:95]\n"
"v_exp_f32 v232, v232\n"
"v_mfma_f32_16x16x32_bf16 v[0:3], a[160:163], v[64:67], v[24:27]\n"
"ds_read_b128 a[128:131], v241 offset:32768\n"
"v_lshrrev_b32 v217, 16, v249\n"
"v_mfma_f32_16x16x32_bf16 v[0:3], a[164:167], v[68:71], v[0:3]\n"
"v_exp_f32 v233, v233\n"
"v_mfma_f32_16x16x32_bf16 v[0:3], a[168:171], v[72:75], v[0:3]\n"
"ds_read_b128 a[132:135], v241 offset:33024\n"
"v_cvt_pk_bf16_f32 v22, v232, v233\n"
"v_mfma_f32_16x16x32_bf16 v[0:3], a[172:175], v[76:79], v[0:3]\n"
"v_exp_f32 v234, v234\n"
"v_mfma_f32_16x16x32_bf16 v[4:7], a[176:179], v[64:67], v[24:27]\n"
"ds_read_b128 a[136:139], v241 offset:33280\n"
"v_mad_u32_u24 v236, v217, s60, v206\n"
"v_mfma_f32_16x16x32_bf16 v[4:7], a[180:183], v[68:71], v[4:7]\n"
"v_exp_f32 v235, v235\n"
"v_mfma_f32_16x16x32_bf16 v[4:7], a[184:187], v[72:75], v[4:7]\n"
"ds_read_b128 a[140:143], v241 offset:33536\n"
"v_cvt_pk_bf16_f32 v23, v234, v235\n"
"v_mfma_f32_16x16x32_bf16 v[4:7], a[188:191], v[76:79], v[4:7]\n"
"s_add_u32 m0, s86, 65536\n"
"v_exp_f32 v0, v0\n"
"v_mfma_f32_16x16x32_bf16 a[96:99], a[192:195], v[20:23], a[96:99]\n"
"buffer_load_dwordx4 v246, s[8:11], s84 offen offset:0 lds\n"
"v_add_u32 v249, 233024, v249\n"
"v_mfma_f32_16x16x32_bf16 a[100:103], a[196:199], v[20:23], a[100:103]\n"
"v_exp_f32 v1, v1\n"
"v_mfma_f32_16x16x32_bf16 a[104:107], a[200:203], v[20:23], a[104:107]\n"
"ds_read_b128 a[144:147], v241 offset:36864\n"
"v_cvt_pk_bf16_f32 v16, v0, v1\n"
"v_mfma_f32_16x16x32_bf16 a[108:111], a[204:207], v[20:23], a[108:111]\n"
"v_exp_f32 v2, v2\n"
"v_mfma_f32_16x16x32_bf16 a[112:115], a[208:211], v[20:23], a[112:115]\n"
"ds_read_b128 a[148:151], v241 offset:37120\n"
"v_add_f32 v44, v44, v0\n"
"v_mfma_f32_16x16x32_bf16 a[116:119], a[212:215], v[20:23], a[116:119]\n"
"v_exp_f32 v3, v3\n"
"v_mfma_f32_16x16x32_bf16 a[120:123], a[216:219], v[20:23], a[120:123]\n"
"ds_read_b128 a[152:155], v241 offset:37376\n"
"v_cvt_pk_bf16_f32 v17, v2, v3\n"
"v_mfma_f32_16x16x32_bf16 a[124:127], a[220:223], v[20:23], a[124:127]\n"
"v_exp_f32 v4, v4\n"
"v_mfma_f32_16x16x32_bf16 v[56:59], v[40:43], v[20:23], v[56:59]\n"
"ds_read_b128 a[156:159], v241 offset:37632\n"
"v_add_f32 v44, v44, v1\n"
"v_mfma_f32_16x16x32_bf16 v[8:11], a[160:163], v[80:83], v[28:31]\n"
"v_exp_f32 v5, v5\n"
"v_mfma_f32_16x16x32_bf16 v[8:11], a[164:167], v[84:87], v[8:11]\n"
"ds_read_b64_tr_b16 a[192:193], v244 offset:49152\n"
"v_cvt_pk_bf16_f32 v18, v4, v5\n"
"v_mfma_f32_16x16x32_bf16 v[8:11], a[168:171], v[88:91], v[8:11]\n"
"v_exp_f32 v6, v6\n"
"v_mfma_f32_16x16x32_bf16 v[8:11], a[172:175], v[92:95], v[8:11]\n"
"ds_read_b64_tr_b16 a[194:195], v244 offset:53248\n"
"v_add_f32 v44, v44, v2\n"
"v_mfma_f32_16x16x32_bf16 v[12:15], a[176:179], v[80:83], v[28:31]\n"
"v_exp_f32 v7, v7\n"
"v_mfma_f32_16x16x32_bf16 v[12:15], a[180:183], v[84:87], v[12:15]\n"
"ds_read_b64_tr_b16 a[196:197], v245 offset:49152\n"
"v_cvt_pk_bf16_f32 v19, v6, v7\n"
"v_mfma_f32_16x16x32_bf16 v[12:15], a[184:187], v[88:91], v[12:15]\n"
"buffer_load_dwordx4 v246, s[8:11], s84 offen offset:1024 lds\n"
"v_lshrrev_b32 v217, 16, v249\n"
"v_mfma_f32_16x16x32_bf16 v[12:15], a[188:191], v[92:95], v[12:15]\n"
"s_waitcnt lgkmcnt(11)\n"
"v_exp_f32 v8, v8\n"
"v_mfma_f32_16x16x32_bf16 a[0:3], a[224:227], v[16:19], a[0:3]\n"
"ds_read_b64_tr_b16 a[198:199], v245 offset:53248\n"
"v_add_f32 v44, v44, v3\n"
"v_mfma_f32_16x16x32_bf16 a[4:7], a[228:231], v[16:19], a[4:7]\n"
"v_exp_f32 v9, v9\n"
"v_mfma_f32_16x16x32_bf16 a[8:11], a[232:235], v[16:19], a[8:11]\n"
"ds_read_b64_tr_b16 a[200:201], v244 offset:49408\n"
"v_cvt_pk_bf16_f32 v20, v8, v9\n"
"v_mfma_f32_16x16x32_bf16 a[12:15], a[236:239], v[16:19], a[12:15]\n"
"v_exp_f32 v10, v10\n"
"v_mfma_f32_16x16x32_bf16 a[16:19], a[240:243], v[16:19], a[16:19]\n"
"ds_read_b64_tr_b16 a[202:203], v244 offset:53504\n"
"v_add_f32 v44, v44, v4\n"
"v_mfma_f32_16x16x32_bf16 a[20:23], a[244:247], v[16:19], a[20:23]\n"
"v_exp_f32 v11, v11\n"
"v_mfma_f32_16x16x32_bf16 a[24:27], a[248:251], v[16:19], a[24:27]\n"
"ds_read_b64_tr_b16 a[204:205], v245 offset:49408\n"
"v_cvt_pk_bf16_f32 v21, v10, v11\n"
"v_mfma_f32_16x16x32_bf16 a[28:31], a[252:255], v[16:19], a[28:31]\n"
"v_exp_f32 v12, v12\n"
"v_mfma_f32_16x16x32_bf16 v[220:223], a[160:163], v[96:99], v[32:35]\n"
"ds_read_b64_tr_b16 a[206:207], v245 offset:53504\n"
"v_add_f32 v44, v44, v5\n"
"v_mfma_f32_16x16x32_bf16 v[220:223], a[164:167], v[100:103], v[220:223]\n"
"v_exp_f32 v13, v13\n"
"v_mfma_f32_16x16x32_bf16 v[220:223], a[168:171], v[104:107], v[220:223]\n"
"ds_read_b64_tr_b16 a[208:209], v244 offset:49664\n"
"v_cvt_pk_bf16_f32 v22, v12, v13\n"
"v_mfma_f32_16x16x32_bf16 v[220:223], a[172:175], v[108:111], v[220:223]\n"
"v_exp_f32 v14, v14\n"
"v_mfma_f32_16x16x32_bf16 v[224:227], a[176:179], v[96:99], v[32:35]\n"
"ds_read_b64_tr_b16 a[210:211], v244 offset:53760\n"
"v_add_f32 v44, v44, v6\n"
"v_mfma_f32_16x16x32_bf16 v[224:227], a[180:183], v[100:103], v[224:227]\n"
"v_exp_f32 v15, v15\n"
"v_mfma_f32_16x16x32_bf16 v[224:227], a[184:187], v[104:107], v[224:227]\n"
"ds_read_b64_tr_b16 a[212:213], v245 offset:49664\n"
"v_cvt_pk_bf16_f32 v23, v14, v15\n"
"v_mfma_f32_16x16x32_bf16 v[224:227], a[188:191], v[108:111], v[224:227]\n"
"v_exp_f32 v220, v220\n"
"v_mfma_f32_16x16x32_bf16 a[32:35], a[224:227], v[20:23], a[32:35]\n"
"buffer_load_dwordx4 v247, s[8:11], s84 offen offset:2048 lds\n"
"v_mad_u32_u24 v237, v217, s60, v206\n"
"v_mfma_f32_16x16x32_bf16 a[36:39], a[228:231], v[20:23], a[36:39]\n"
"v_exp_f32 v221, v221\n"
"v_mfma_f32_16x16x32_bf16 a[40:43], a[232:235], v[20:23], a[40:43]\n"
"ds_read_b64_tr_b16 a[214:215], v245 offset:53760\n"
"v_cvt_pk_bf16_f32 v16, v220, v221\n"
"v_mfma_f32_16x16x32_bf16 a[44:47], a[236:239], v[20:23], a[44:47]\n"
"v_exp_f32 v222, v222\n"
"v_mfma_f32_16x16x32_bf16 a[48:51], a[240:243], v[20:23], a[48:51]\n"
"ds_read_b64_tr_b16 a[216:217], v244 offset:49920\n"
"v_add_f32 v44, v44, v7\n"
"v_mfma_f32_16x16x32_bf16 a[52:55], a[244:247], v[20:23], a[52:55]\n"
"v_exp_f32 v223, v223\n"
"v_mfma_f32_16x16x32_bf16 a[56:59], a[248:251], v[20:23], a[56:59]\n"
"ds_read_b64_tr_b16 a[218:219], v244 offset:54016\n"
"v_cvt_pk_bf16_f32 v17, v222, v223\n"
"v_mfma_f32_16x16x32_bf16 a[60:63], a[252:255], v[20:23], a[60:63]\n"
"v_exp_f32 v224, v224\n"
"v_mfma_f32_16x16x32_bf16 v[48:51], v[40:43], v[20:23], v[48:51]\n"
"ds_read_b64_tr_b16 a[220:221], v245 offset:49920\n"
"v_add_f32 v52, v52, v220\n"
"v_mfma_f32_16x16x32_bf16 v[228:231], a[160:163], v[112:115], v[36:39]\n"
"v_exp_f32 v225, v225\n"
"v_mfma_f32_16x16x32_bf16 v[228:231], a[164:167], v[116:119], v[228:231]\n"
"ds_read_b64_tr_b16 a[222:223], v245 offset:54016\n"
"v_cvt_pk_bf16_f32 v18, v224, v225\n"
"v_mfma_f32_16x16x32_bf16 v[228:231], a[168:171], v[120:123], v[228:231]\n"
"v_exp_f32 v226, v226\n"
"v_mfma_f32_16x16x32_bf16 v[228:231], a[172:175], v[124:127], v[228:231]\n"
"v_add_f32 v52, v52, v221\n"
"v_add_f32 v52, v52, v222\n"
"v_mfma_f32_16x16x32_bf16 v[232:235], a[176:179], v[112:115], v[36:39]\n"
"v_exp_f32 v227, v227\n"
"v_mfma_f32_16x16x32_bf16 v[232:235], a[180:183], v[116:119], v[232:235]\n"
"v_cvt_pk_bf16_f32 v19, v226, v227\n"
"v_add_f32 v52, v52, v223\n"
"v_mfma_f32_16x16x32_bf16 v[232:235], a[184:187], v[120:123], v[232:235]\n"
"buffer_load_dwordx4 v247, s[8:11], s84 offen offset:3072 lds\n"
"v_add_u32 v249, 233024, v249\n"
"v_mfma_f32_16x16x32_bf16 v[232:235], a[188:191], v[124:127], v[232:235]\n"
"v_exp_f32 v228, v228\n"
"v_mfma_f32_16x16x32_bf16 a[64:67], a[224:227], v[16:19], a[64:67]\n"
"v_add_f32 v52, v52, v224\n"
"v_add_f32 v52, v52, v225\n"
"v_mfma_f32_16x16x32_bf16 a[68:71], a[228:231], v[16:19], a[68:71]\n"
"v_exp_f32 v229, v229\n"
"v_mfma_f32_16x16x32_bf16 a[72:75], a[232:235], v[16:19], a[72:75]\n"
"v_cvt_pk_bf16_f32 v20, v228, v229\n"
"v_add_f32 v52, v52, v226\n"
"v_mfma_f32_16x16x32_bf16 a[76:79], a[236:239], v[16:19], a[76:79]\n"
"v_exp_f32 v230, v230\n"
"v_mfma_f32_16x16x32_bf16 a[80:83], a[240:243], v[16:19], a[80:83]\n"
"v_add_f32 v52, v52, v227\n"
"v_lshrrev_b32 v217, 16, v249\n"
"v_mfma_f32_16x16x32_bf16 a[84:87], a[244:247], v[16:19], a[84:87]\n"
"v_exp_f32 v231, v231\n"
"v_mfma_f32_16x16x32_bf16 a[88:91], a[248:251], v[16:19], a[88:91]\n"
"s_waitcnt lgkmcnt(8)\n"
"v_cvt_pk_bf16_f32 v21, v230, v231\n"
"v_mad_u32_u24 v238, v217, s60, v206\n"
"v_mfma_f32_16x16x32_bf16 a[92:95], a[252:255], v[16:19], a[92:95]\n"
"v_exp_f32 v232, v232\n"
"v_mfma_f32_16x16x32_bf16 v[0:3], a[128:131], v[64:67], v[24:27]\n"
"ds_read_b128 a[160:163], v241 offset:40960\n"
"v_add_u32 v206, 4096, v206\n"
"v_mfma_f32_16x16x32_bf16 v[0:3], a[132:135], v[68:71], v[0:3]\n"
"v_exp_f32 v233, v233\n"
"v_mfma_f32_16x16x32_bf16 v[0:3], a[136:139], v[72:75], v[0:3]\n"
"ds_read_b128 a[164:167], v241 offset:41216\n"
"v_cvt_pk_bf16_f32 v22, v232, v233\n"
"v_mfma_f32_16x16x32_bf16 v[0:3], a[140:143], v[76:79], v[0:3]\n"
"v_exp_f32 v234, v234\n"
"v_mfma_f32_16x16x32_bf16 v[4:7], a[144:147], v[64:67], v[24:27]\n"
"ds_read_b128 a[168:171], v241 offset:41472\n"
"v_add_u32 v249, 233024, v249\n"
"v_mfma_f32_16x16x32_bf16 v[4:7], a[148:151], v[68:71], v[4:7]\n"
"v_exp_f32 v235, v235\n"
"v_mfma_f32_16x16x32_bf16 v[4:7], a[152:155], v[72:75], v[4:7]\n"
"ds_read_b128 a[172:175], v241 offset:41728\n"
"v_cvt_pk_bf16_f32 v23, v234, v235\n"
"v_mfma_f32_16x16x32_bf16 v[4:7], a[156:159], v[76:79], v[4:7]\n"
"s_add_u32 m0, s86, 81920\n"
"v_exp_f32 v0, v0\n"
"v_mfma_f32_16x16x32_bf16 a[96:99], a[224:227], v[20:23], a[96:99]\n"
"buffer_load_dwordx4 v246, s[12:15], s84 offen offset:0 lds\n"
"v_lshrrev_b32 v217, 16, v249\n"
"v_mfma_f32_16x16x32_bf16 a[100:103], a[228:231], v[20:23], a[100:103]\n"
"v_exp_f32 v1, v1\n"
"v_mfma_f32_16x16x32_bf16 a[104:107], a[232:235], v[20:23], a[104:107]\n"
"ds_read_b128 a[176:179], v241 offset:45056\n"
"v_cvt_pk_bf16_f32 v16, v0, v1\n"
"v_mfma_f32_16x16x32_bf16 a[108:111], a[236:239], v[20:23], a[108:111]\n"
"v_exp_f32 v2, v2\n"
"v_mfma_f32_16x16x32_bf16 a[112:115], a[240:243], v[20:23], a[112:115]\n"
"ds_read_b128 a[180:183], v241 offset:45312\n"
"v_add_f32 v44, v44, v0\n"
"v_mfma_f32_16x16x32_bf16 a[116:119], a[244:247], v[20:23], a[116:119]\n"
"v_exp_f32 v3, v3\n"
"v_mfma_f32_16x16x32_bf16 a[120:123], a[248:251], v[20:23], a[120:123]\n"
"ds_read_b128 a[184:187], v241 offset:45568\n"
"v_cvt_pk_bf16_f32 v17, v2, v3\n"
"v_mfma_f32_16x16x32_bf16 a[124:127], a[252:255], v[20:23], a[124:127]\n"
"v_exp_f32 v4, v4\n"
"v_mfma_f32_16x16x32_bf16 v[56:59], v[40:43], v[20:23], v[56:59]\n"
"ds_read_b128 a[188:191], v241 offset:45824\n"
"v_add_f32 v44, v44, v1\n"
"v_mfma_f32_16x16x32_bf16 v[8:11], a[128:131], v[80:83], v[28:31]\n"
"v_exp_f32 v5, v5\n"
"v_mfma_f32_16x16x32_bf16 v[8:11], a[132:135], v[84:87], v[8:11]\n"
"ds_read_b64_tr_b16 a[224:225], v244 offset:57344\n"
"v_cvt_pk_bf16_f32 v18, v4, v5\n"
"v_mfma_f32_16x16x32_bf16 v[8:11], a[136:139], v[88:91], v[8:11]\n"
"v_exp_f32 v6, v6\n"
"v_mfma_f32_16x16x32_bf16 v[8:11], a[140:143], v[92:95], v[8:11]\n"
"ds_read_b64_tr_b16 a[226:227], v244 offset:61440\n"
"v_add_f32 v44, v44, v2\n"
"v_mfma_f32_16x16x32_bf16 v[12:15], a[144:147], v[80:83], v[28:31]\n"
"v_exp_f32 v7, v7\n"
"v_mfma_f32_16x16x32_bf16 v[12:15], a[148:151], v[84:87], v[12:15]\n"
"ds_read_b64_tr_b16 a[228:229], v245 offset:57344\n"
"v_cvt_pk_bf16_f32 v19, v6, v7\n"
"v_mfma_f32_16x16x32_bf16 v[12:15], a[152:155], v[88:91], v[12:15]\n"
"buffer_load_dwordx4 v247, s[12:15], s84 offen offset:1024 lds\n"
"v_mad_u32_u24 v239, v217, s60, v206\n"
"v_mfma_f32_16x16x32_bf16 v[12:15], a[156:159], v[92:95], v[12:15]\n"
"s_waitcnt lgkmcnt(11)\n"
"v_exp_f32 v8, v8\n"
"v_mfma_f32_16x16x32_bf16 a[0:3], a[192:195], v[16:19], a[0:3]\n"
"s_and_b32 s56, s43, 3\n"
"ds_read_b64_tr_b16 a[230:231], v245 offset:61440\n"
"v_add_f32 v44, v44, v3\n"
"v_mfma_f32_16x16x32_bf16 a[4:7], a[196:199], v[16:19], a[4:7]\n"
"s_lshr_b32 s57, s43, 2\n"
"v_exp_f32 v9, v9\n"
"v_mfma_f32_16x16x32_bf16 a[8:11], a[200:203], v[16:19], a[8:11]\n"
"s_add_u32 s49, s49, 9216\n"
"ds_read_b64_tr_b16 a[232:233], v244 offset:57600\n"
"v_cvt_pk_bf16_f32 v20, v8, v9\n"
"v_mfma_f32_16x16x32_bf16 a[12:15], a[204:207], v[16:19], a[12:15]\n"
"v_exp_f32 v10, v10\n"
"v_mfma_f32_16x16x32_bf16 a[16:19], a[208:211], v[16:19], a[16:19]\n"
"ds_read_b64_tr_b16 a[234:235], v244 offset:61696\n"
"buffer_load_dwordx4 v[132:135], v252, s[64:67], s49 offen\n"
"v_add_f32 v44, v44, v4\n"
"v_mfma_f32_16x16x32_bf16 a[20:23], a[212:215], v[16:19], a[20:23]\n"
"s_mul_i32 s57, s57, 1179648\n"
"v_exp_f32 v11, v11\n"
"v_mfma_f32_16x16x32_bf16 a[24:27], a[216:219], v[16:19], a[24:27]\n"
"s_mul_i32 s62, s56, 288\n"
"ds_read_b64_tr_b16 a[236:237], v245 offset:57600\n"
"v_cvt_pk_bf16_f32 v21, v10, v11\n"
"v_mfma_f32_16x16x32_bf16 a[28:31], a[220:223], v[16:19], a[28:31]\n"
"s_add_u32 s57, s57, s62\n"
"v_exp_f32 v12, v12\n"
"v_mfma_f32_16x16x32_bf16 v[220:223], a[128:131], v[96:99], v[32:35]\n"
"s_lshl_b32 s58, s56, 3\n"
"ds_read_b64_tr_b16 a[238:239], v245 offset:61696\n"
"v_add_f32 v44, v44, v5\n"
"v_mfma_f32_16x16x32_bf16 v[220:223], a[132:135], v[100:103], v[220:223]\n"
"s_add_u32 s58, s58, s41\n"
"v_exp_f32 v13, v13\n"
"v_mfma_f32_16x16x32_bf16 v[220:223], a[136:139], v[104:107], v[220:223]\n"
"s_add_u32 s58, s58, 0\n"
"ds_read_b64_tr_b16 a[240:241], v244 offset:57856\n"
"v_cvt_pk_bf16_f32 v22, v12, v13\n"
"v_mfma_f32_16x16x32_bf16 v[220:223], a[140:143], v[108:111], v[220:223]\n"
"s_mul_i32 s58, s58, 36864\n"
"v_exp_f32 v14, v14\n"
"v_mfma_f32_16x16x32_bf16 v[224:227], a[144:147], v[96:99], v[32:35]\n"
"s_add_u32 s20, s58, s57\n"
"ds_read_b64_tr_b16 a[242:243], v244 offset:61952\n"
"v_add_f32 v44, v44, v6\n"
"v_mfma_f32_16x16x32_bf16 v[224:227], a[148:151], v[100:103], v[224:227]\n"
"s_lshl_b32 s58, s56, 3\n"
"v_exp_f32 v15, v15\n"
"v_mfma_f32_16x16x32_bf16 v[224:227], a[152:155], v[104:107], v[224:227]\n"
"s_add_u32 s58, s58, s41\n"
"ds_read_b64_tr_b16 a[244:245], v245 offset:57856\n"
"v_cvt_pk_bf16_f32 v23, v14, v15\n"
"v_mfma_f32_16x16x32_bf16 v[224:227], a[156:159], v[108:111], v[224:227]\n"
"s_add_u32 s58, s58, 4\n"
"v_exp_f32 v220, v220\n"
"v_mfma_f32_16x16x32_bf16 a[32:35], a[192:195], v[20:23], a[32:35]\n"
"s_mul_i32 s58, s58, 36864\n"
"buffer_load_dwordx4 v246, s[12:15], s84 offen offset:2048 lds\n"
"v_mfma_f32_16x16x32_bf16 a[36:39], a[196:199], v[20:23], a[36:39]\n"
"s_add_u32 s21, s58, s57\n"
"v_exp_f32 v221, v221\n"
"v_mfma_f32_16x16x32_bf16 a[40:43], a[200:203], v[20:23], a[40:43]\n"
"s_add_u32 s58, s41, 0\n"
"ds_read_b64_tr_b16 a[246:247], v245 offset:61952\n"
"v_cvt_pk_bf16_f32 v16, v220, v221\n"
"v_mfma_f32_16x16x32_bf16 a[44:47], a[204:207], v[20:23], a[44:47]\n"
"s_mul_i32 s59, s58, 43\n"
"v_exp_f32 v222, v222\n"
"v_mfma_f32_16x16x32_bf16 a[48:51], a[208:211], v[20:23], a[48:51]\n"
"s_lshr_b32 s59, s59, 7\n"
"ds_read_b64_tr_b16 a[248:249], v244 offset:58112\n"
"v_add_f32 v44, v44, v7\n"
"v_mfma_f32_16x16x32_bf16 a[52:55], a[212:215], v[20:23], a[52:55]\n"
"v_exp_f32 v223, v223\n"
"v_mfma_f32_16x16x32_bf16 a[56:59], a[216:219], v[20:23], a[56:59]\n"
"ds_read_b64_tr_b16 a[250:251], v244 offset:62208\n"
"v_cvt_pk_bf16_f32 v17, v222, v223\n"
"v_mfma_f32_16x16x32_bf16 a[60:63], a[220:223], v[20:23], a[60:63]\n"
"v_exp_f32 v224, v224\n"
"v_mfma_f32_16x16x32_bf16 v[48:51], v[40:43], v[20:23], v[48:51]\n"
"ds_read_b64_tr_b16 a[252:253], v245 offset:58112\n"
"v_add_f32 v52, v52, v220\n"
"v_mfma_f32_16x16x32_bf16 v[228:231], a[128:131], v[112:115], v[36:39]\n"
"v_exp_f32 v225, v225\n"
"v_mfma_f32_16x16x32_bf16 v[228:231], a[132:135], v[116:119], v[228:231]\n"
"ds_read_b64_tr_b16 a[254:255], v245 offset:62208\n"
"v_cvt_pk_bf16_f32 v18, v224, v225\n"
"v_mfma_f32_16x16x32_bf16 v[228:231], a[136:139], v[120:123], v[228:231]\n"
"v_exp_f32 v226, v226\n"
"v_mfma_f32_16x16x32_bf16 v[228:231], a[140:143], v[124:127], v[228:231]\n"
"buffer_load_dwordx4 v[168:171], v252, s[68:71], s49 offen\n"
"v_add_f32 v52, v52, v221\n"
"v_add_f32 v52, v52, v222\n"
"v_mfma_f32_16x16x32_bf16 v[232:235], a[144:147], v[112:115], v[36:39]\n"
"s_mul_i32 s60, s59, 3\n"
"v_exp_f32 v227, v227\n"
"v_mfma_f32_16x16x32_bf16 v[232:235], a[148:151], v[116:119], v[232:235]\n"
"s_sub_u32 s58, s58, s60\n"
"v_cvt_pk_bf16_f32 v19, v226, v227\n"
"v_add_f32 v52, v52, v223\n"
"v_mfma_f32_16x16x32_bf16 v[232:235], a[152:155], v[120:123], v[232:235]\n"
"s_add_u32 s58, s58, s56\n"
"buffer_load_dwordx4 v247, s[12:15], s84 offen offset:3072 lds\n"
"v_mfma_f32_16x16x32_bf16 v[232:235], a[156:159], v[124:127], v[232:235]\n"
"s_add_u32 s84, s84, 16384\n"
"v_exp_f32 v228, v228\n"
"v_mfma_f32_16x16x32_bf16 a[64:67], a[192:195], v[16:19], a[64:67]\n"
"s_add_u32 s58, s58, 1\n"
"v_add_f32 v52, v52, v224\n"
"v_add_f32 v52, v52, v225\n"
"v_mfma_f32_16x16x32_bf16 a[68:71], a[196:199], v[16:19], a[68:71]\n"
"s_and_b32 s58, s58, 3\n"
"v_exp_f32 v229, v229\n"
"v_mfma_f32_16x16x32_bf16 a[72:75], a[200:203], v[16:19], a[72:75]\n"
"s_lshl_b32 s58, s58, 3\n"
"v_cvt_pk_bf16_f32 v20, v228, v229\n"
"v_add_f32 v52, v52, v226\n"
"v_mfma_f32_16x16x32_bf16 a[76:79], a[204:207], v[16:19], a[76:79]\n"
"s_add_u32 s58, s58, s59\n"
"v_exp_f32 v230, v230\n"
"v_mfma_f32_16x16x32_bf16 a[80:83], a[208:211], v[16:19], a[80:83]\n"
"s_mul_i32 s58, s58, 36864\n"
"v_add_f32 v52, v52, v227\n"
"v_mfma_f32_16x16x32_bf16 a[84:87], a[212:215], v[16:19], a[84:87]\n"
"s_add_u32 s22, s58, s57\n"
"v_exp_f32 v231, v231\n"
"v_mfma_f32_16x16x32_bf16 a[88:91], a[216:219], v[16:19], a[88:91]\n"
"s_waitcnt vmcnt(20) lgkmcnt(0)\n"
"s_barrier\n"
"v_cvt_pk_bf16_f32 v21, v230, v231\n"
"v_mfma_f32_16x16x32_bf16 a[92:95], a[220:223], v[16:19], a[92:95]\n"
"s_add_u32 s58, s41, 4\n"
"v_exp_f32 v232, v232\n"
"v_mfma_f32_16x16x32_bf16 v[0:3], a[160:163], v[64:67], v[24:27]\n"
"s_mul_i32 s59, s58, 43\n"
"ds_read_b128 a[128:131], v240 offset:0\n"
"v_mfma_f32_16x16x32_bf16 v[0:3], a[164:167], v[68:71], v[0:3]\n"
"s_lshr_b32 s59, s59, 7\n"
"v_exp_f32 v233, v233\n"
"v_mfma_f32_16x16x32_bf16 v[0:3], a[168:171], v[72:75], v[0:3]\n"
"s_mul_i32 s60, s59, 3\n"
"ds_read_b128 a[132:135], v240 offset:256\n"
"v_cvt_pk_bf16_f32 v22, v232, v233\n"
"v_mfma_f32_16x16x32_bf16 v[0:3], a[172:175], v[76:79], v[0:3]\n"
"s_sub_u32 s58, s58, s60\n"
"v_exp_f32 v234, v234\n"
"v_mfma_f32_16x16x32_bf16 v[4:7], a[176:179], v[64:67], v[24:27]\n"
"s_add_u32 s58, s58, s56\n"
"ds_read_b128 a[136:139], v240 offset:512\n"
"v_mfma_f32_16x16x32_bf16 v[4:7], a[180:183], v[68:71], v[4:7]\n"
"s_add_u32 s58, s58, 1\n"
"v_exp_f32 v235, v235\n"
"v_mfma_f32_16x16x32_bf16 v[4:7], a[184:187], v[72:75], v[4:7]\n"
"s_add_u32 s49, s49, 285696\n"
"ds_read_b128 a[140:143], v240 offset:768\n"
"v_cvt_pk_bf16_f32 v23, v234, v235\n"
"v_mfma_f32_16x16x32_bf16 v[4:7], a[188:191], v[76:79], v[4:7]\n"
"s_add_u32 m0, s86, 98304\n"
"v_exp_f32 v0, v0\n"
"v_mfma_f32_16x16x32_bf16 a[96:99], a[192:195], v[20:23], a[96:99]\n"
"buffer_load_dwordx4 v246, s[8:11], s84 offen offset:0 lds\n"
"v_mfma_f32_16x16x32_bf16 a[100:103], a[196:199], v[20:23], a[100:103]\n"
"v_exp_f32 v1, v1\n"
"v_mfma_f32_16x16x32_bf16 a[104:107], a[200:203], v[20:23], a[104:107]\n"
"ds_read_b128 a[144:147], v240 offset:4096\n"
"v_cvt_pk_bf16_f32 v16, v0, v1\n"
"v_mfma_f32_16x16x32_bf16 a[108:111], a[204:207], v[20:23], a[108:111]\n"
"v_exp_f32 v2, v2\n"
"v_mfma_f32_16x16x32_bf16 a[112:115], a[208:211], v[20:23], a[112:115]\n"
"ds_read_b128 a[148:151], v240 offset:4352\n"
"buffer_load_dwordx4 v[136:139], v252, s[64:67], s49 offen\n"
"v_add_f32 v44, v44, v0\n"
"v_mfma_f32_16x16x32_bf16 a[116:119], a[212:215], v[20:23], a[116:119]\n"
"s_and_b32 s58, s58, 3\n"
"v_exp_f32 v3, v3\n"
"v_mfma_f32_16x16x32_bf16 a[120:123], a[216:219], v[20:23], a[120:123]\n"
"s_lshl_b32 s58, s58, 3\n"
"ds_read_b128 a[152:155], v240 offset:4608\n"
"v_cvt_pk_bf16_f32 v17, v2, v3\n"
"v_mfma_f32_16x16x32_bf16 a[124:127], a[220:223], v[20:23], a[124:127]\n"
"s_add_u32 s58, s58, s59\n"
"v_exp_f32 v4, v4\n"
"v_mfma_f32_16x16x32_bf16 v[56:59], v[40:43], v[20:23], v[56:59]\n"
"s_mul_i32 s58, s58, 36864\n"
"ds_read_b128 a[156:159], v240 offset:4864\n"
"v_add_f32 v44, v44, v1\n"
"v_mfma_f32_16x16x32_bf16 v[8:11], a[160:163], v[80:83], v[28:31]\n"
"s_add_u32 s23, s58, s57\n"
"v_exp_f32 v5, v5\n"
"v_mfma_f32_16x16x32_bf16 v[8:11], a[164:167], v[84:87], v[8:11]\n"
"s_add_u32 s58, s41, 8\n"
"ds_read_b64_tr_b16 a[192:193], v242 offset:16384\n"
"v_cvt_pk_bf16_f32 v18, v4, v5\n"
"v_mfma_f32_16x16x32_bf16 v[8:11], a[168:171], v[88:91], v[8:11]\n"
"s_mul_i32 s59, s58, 43\n"
"v_exp_f32 v6, v6\n"
"v_mfma_f32_16x16x32_bf16 v[8:11], a[172:175], v[92:95], v[8:11]\n"
"s_lshr_b32 s59, s59, 7\n"
"ds_read_b64_tr_b16 a[194:195], v242 offset:20480\n"
"v_add_f32 v44, v44, v2\n"
"v_mfma_f32_16x16x32_bf16 v[12:15], a[176:179], v[80:83], v[28:31]\n"
"s_mul_i32 s60, s59, 3\n"
"v_exp_f32 v7, v7\n"
"v_mfma_f32_16x16x32_bf16 v[12:15], a[180:183], v[84:87], v[12:15]\n"
"s_sub_u32 s58, s58, s60\n"
"ds_read_b64_tr_b16 a[196:197], v243 offset:16384\n"
"v_cvt_pk_bf16_f32 v19, v6, v7\n"
"v_mfma_f32_16x16x32_bf16 v[12:15], a[184:187], v[88:91], v[12:15]\n"
"s_add_u32 s58, s58, s56\n"
"buffer_load_dwordx4 v246, s[8:11], s84 offen offset:1024 lds\n"
"v_mfma_f32_16x16x32_bf16 v[12:15], a[188:191], v[92:95], v[12:15]\n"
"s_waitcnt lgkmcnt(11)\n"
"v_exp_f32 v8, v8\n"
"v_mfma_f32_16x16x32_bf16 a[0:3], a[224:227], v[16:19], a[0:3]\n"
"s_add_u32 s58, s58, 1\n"
"ds_read_b64_tr_b16 a[198:199], v243 offset:20480\n"
"v_add_f32 v44, v44, v3\n"
"v_mfma_f32_16x16x32_bf16 a[4:7], a[228:231], v[16:19], a[4:7]\n"
"s_and_b32 s58, s58, 3\n"
"v_exp_f32 v9, v9\n"
"v_mfma_f32_16x16x32_bf16 a[8:11], a[232:235], v[16:19], a[8:11]\n"
"s_lshl_b32 s58, s58, 3\n"
"ds_read_b64_tr_b16 a[200:201], v242 offset:16640\n"
"v_cvt_pk_bf16_f32 v20, v8, v9\n"
"v_mfma_f32_16x16x32_bf16 a[12:15], a[236:239], v[16:19], a[12:15]\n"
"s_add_u32 s58, s58, s59\n"
"v_exp_f32 v10, v10\n"
"v_mfma_f32_16x16x32_bf16 a[16:19], a[240:243], v[16:19], a[16:19]\n"
"s_mul_i32 s58, s58, 36864\n"
"ds_read_b64_tr_b16 a[202:203], v242 offset:20736\n"
"v_add_f32 v44, v44, v4\n"
"v_mfma_f32_16x16x32_bf16 a[20:23], a[244:247], v[16:19], a[20:23]\n"
"v_exp_f32 v11, v11\n"
"v_mfma_f32_16x16x32_bf16 a[24:27], a[248:251], v[16:19], a[24:27]\n"
"ds_read_b64_tr_b16 a[204:205], v243 offset:16640\n"
"v_cvt_pk_bf16_f32 v21, v10, v11\n"
"v_mfma_f32_16x16x32_bf16 a[28:31], a[252:255], v[16:19], a[28:31]\n"
"v_exp_f32 v12, v12\n"
"v_mfma_f32_16x16x32_bf16 v[220:223], a[160:163], v[96:99], v[32:35]\n"
"ds_read_b64_tr_b16 a[206:207], v243 offset:20736\n"
"v_add_f32 v44, v44, v5\n"
"v_mfma_f32_16x16x32_bf16 v[220:223], a[164:167], v[100:103], v[220:223]\n"
"v_exp_f32 v13, v13\n"
"v_mfma_f32_16x16x32_bf16 v[220:223], a[168:171], v[104:107], v[220:223]\n"
"ds_read_b64_tr_b16 a[208:209], v242 offset:16896\n"
"v_cvt_pk_bf16_f32 v22, v12, v13\n"
"v_mfma_f32_16x16x32_bf16 v[220:223], a[172:175], v[108:111], v[220:223]\n"
"v_exp_f32 v14, v14\n"
"v_mfma_f32_16x16x32_bf16 v[224:227], a[176:179], v[96:99], v[32:35]\n"
"ds_read_b64_tr_b16 a[210:211], v242 offset:20992\n"
"buffer_load_dwordx4 v[172:175], v252, s[68:71], s49 offen\n"
"v_add_f32 v44, v44, v6\n"
"v_mfma_f32_16x16x32_bf16 v[224:227], a[180:183], v[100:103], v[224:227]\n"
"s_add_u32 s36, s58, s57\n"
"v_exp_f32 v15, v15\n"
"v_mfma_f32_16x16x32_bf16 v[224:227], a[184:187], v[104:107], v[224:227]\n"
"s_add_u32 s58, s41, 12\n"
"ds_read_b64_tr_b16 a[212:213], v243 offset:16896\n"
"v_cvt_pk_bf16_f32 v23, v14, v15\n"
"v_mfma_f32_16x16x32_bf16 v[224:227], a[188:191], v[108:111], v[224:227]\n"
"s_mul_i32 s59, s58, 43\n"
"v_exp_f32 v220, v220\n"
"v_mfma_f32_16x16x32_bf16 a[32:35], a[224:227], v[20:23], a[32:35]\n"
"s_lshr_b32 s59, s59, 7\n"
"buffer_load_dwordx4 v247, s[8:11], s84 offen offset:2048 lds\n"
"v_mfma_f32_16x16x32_bf16 a[36:39], a[228:231], v[20:23], a[36:39]\n"
"s_mul_i32 s60, s59, 3\n"
"v_exp_f32 v221, v221\n"
"v_mfma_f32_16x16x32_bf16 a[40:43], a[232:235], v[20:23], a[40:43]\n"
"s_sub_u32 s58, s58, s60\n"
"ds_read_b64_tr_b16 a[214:215], v243 offset:20992\n"
"v_cvt_pk_bf16_f32 v16, v220, v221\n"
"v_mfma_f32_16x16x32_bf16 a[44:47], a[236:239], v[20:23], a[44:47]\n"
"s_add_u32 s58, s58, s56\n"
"v_exp_f32 v222, v222\n"
"v_mfma_f32_16x16x32_bf16 a[48:51], a[240:243], v[20:23], a[48:51]\n"
"s_add_u32 s58, s58, 1\n"
"ds_read_b64_tr_b16 a[216:217], v242 offset:17152\n"
"v_add_f32 v44, v44, v7\n"
"v_mfma_f32_16x16x32_bf16 a[52:55], a[244:247], v[20:23], a[52:55]\n"
"s_and_b32 s58, s58, 3\n"
"v_exp_f32 v223, v223\n"
"v_mfma_f32_16x16x32_bf16 a[56:59], a[248:251], v[20:23], a[56:59]\n"
"s_lshl_b32 s58, s58, 3\n"
"ds_read_b64_tr_b16 a[218:219], v242 offset:21248\n"
"v_cvt_pk_bf16_f32 v17, v222, v223\n"
"v_mfma_f32_16x16x32_bf16 a[60:63], a[252:255], v[20:23], a[60:63]\n"
"s_add_u32 s58, s58, s59\n"
"v_exp_f32 v224, v224\n"
"v_mfma_f32_16x16x32_bf16 v[48:51], v[40:43], v[20:23], v[48:51]\n"
"s_mul_i32 s58, s58, 36864\n"
"ds_read_b64_tr_b16 a[220:221], v243 offset:17152\n"
"v_add_f32 v52, v52, v220\n"
"v_mfma_f32_16x16x32_bf16 v[228:231], a[160:163], v[112:115], v[36:39]\n"
"s_add_u32 s37, s58, s57\n"
"v_exp_f32 v225, v225\n"
"v_mfma_f32_16x16x32_bf16 v[228:231], a[164:167], v[116:119], v[228:231]\n"
"s_add_u32 s58, s41, 16\n"
"ds_read_b64_tr_b16 a[222:223], v243 offset:21248\n"
"v_cvt_pk_bf16_f32 v18, v224, v225\n"
"v_mfma_f32_16x16x32_bf16 v[228:231], a[168:171], v[120:123], v[228:231]\n"
"s_mul_i32 s59, s58, 43\n"
"v_exp_f32 v226, v226\n"
"v_mfma_f32_16x16x32_bf16 v[228:231], a[172:175], v[124:127], v[228:231]\n"
"s_lshr_b32 s59, s59, 7\n"
"v_add_f32 v52, v52, v221\n"
"v_add_f32 v52, v52, v222\n"
"v_mfma_f32_16x16x32_bf16 v[232:235], a[176:179], v[112:115], v[36:39]\n"
"s_add_u32 s49, s49, 9216\n"
"v_exp_f32 v227, v227\n"
"v_mfma_f32_16x16x32_bf16 v[232:235], a[180:183], v[116:119], v[232:235]\n"
"v_cvt_pk_bf16_f32 v19, v226, v227\n"
"v_add_f32 v52, v52, v223\n"
"v_mfma_f32_16x16x32_bf16 v[232:235], a[184:187], v[120:123], v[232:235]\n"
"buffer_load_dwordx4 v247, s[8:11], s84 offen offset:3072 lds\n"
"v_mfma_f32_16x16x32_bf16 v[232:235], a[188:191], v[124:127], v[232:235]\n"
"v_exp_f32 v228, v228\n"
"v_mfma_f32_16x16x32_bf16 a[64:67], a[224:227], v[16:19], a[64:67]\n"
"v_add_f32 v52, v52, v224\n"
"v_add_f32 v52, v52, v225\n"
"v_mfma_f32_16x16x32_bf16 a[68:71], a[228:231], v[16:19], a[68:71]\n"
"v_exp_f32 v229, v229\n"
"v_mfma_f32_16x16x32_bf16 a[72:75], a[232:235], v[16:19], a[72:75]\n"
"v_cvt_pk_bf16_f32 v20, v228, v229\n"
"v_add_f32 v52, v52, v226\n"
"v_mfma_f32_16x16x32_bf16 a[76:79], a[236:239], v[16:19], a[76:79]\n"
"v_exp_f32 v230, v230\n"
"v_mfma_f32_16x16x32_bf16 a[80:83], a[240:243], v[16:19], a[80:83]\n"
"buffer_load_dwordx4 v[140:143], v252, s[64:67], s49 offen\n"
"v_add_f32 v52, v52, v227\n"
"v_mfma_f32_16x16x32_bf16 a[84:87], a[244:247], v[16:19], a[84:87]\n"
"s_mul_i32 s60, s59, 3\n"
"v_exp_f32 v231, v231\n"
"v_mfma_f32_16x16x32_bf16 a[88:91], a[248:251], v[16:19], a[88:91]\n"
"s_waitcnt lgkmcnt(8)\n"
"v_cvt_pk_bf16_f32 v21, v230, v231\n"
"v_mfma_f32_16x16x32_bf16 a[92:95], a[252:255], v[16:19], a[92:95]\n"
"s_sub_u32 s58, s58, s60\n"
"v_exp_f32 v232, v232\n"
"v_mfma_f32_16x16x32_bf16 v[0:3], a[128:131], v[64:67], v[24:27]\n"
"s_add_u32 s58, s58, s56\n"
"ds_read_b128 a[160:163], v240 offset:8192\n"
"v_mfma_f32_16x16x32_bf16 v[0:3], a[132:135], v[68:71], v[0:3]\n"
"s_add_u32 s58, s58, 1\n"
"v_exp_f32 v233, v233\n"
"v_mfma_f32_16x16x32_bf16 v[0:3], a[136:139], v[72:75], v[0:3]\n"
"s_and_b32 s58, s58, 3\n"
"ds_read_b128 a[164:167], v240 offset:8448\n"
"v_cvt_pk_bf16_f32 v22, v232, v233\n"
"v_mfma_f32_16x16x32_bf16 v[0:3], a[140:143], v[76:79], v[0:3]\n"
"s_lshl_b32 s58, s58, 3\n"
"v_exp_f32 v234, v234\n"
"v_mfma_f32_16x16x32_bf16 v[4:7], a[144:147], v[64:67], v[24:27]\n"
"s_add_u32 s58, s58, s59\n"
"ds_read_b128 a[168:171], v240 offset:8704\n"
"v_mfma_f32_16x16x32_bf16 v[4:7], a[148:151], v[68:71], v[4:7]\n"
"s_mul_i32 s58, s58, 36864\n"
"v_exp_f32 v235, v235\n"
"v_mfma_f32_16x16x32_bf16 v[4:7], a[152:155], v[72:75], v[4:7]\n"
"s_add_u32 s38, s58, s57\n"
"ds_read_b128 a[172:175], v240 offset:8960\n"
"v_cvt_pk_bf16_f32 v23, v234, v235\n"
"v_mfma_f32_16x16x32_bf16 v[4:7], a[156:159], v[76:79], v[4:7]\n"
"s_add_u32 m0, s86, 114688\n"
"v_exp_f32 v0, v0\n"
"v_mfma_f32_16x16x32_bf16 a[96:99], a[224:227], v[20:23], a[96:99]\n"
"s_add_u32 s58, s41, 20\n"
"buffer_load_dwordx4 v246, s[12:15], s84 offen offset:0 lds\n"
"v_mfma_f32_16x16x32_bf16 a[100:103], a[228:231], v[20:23], a[100:103]\n"
"s_mul_i32 s59, s58, 43\n"
"v_exp_f32 v1, v1\n"
"v_mfma_f32_16x16x32_bf16 a[104:107], a[232:235], v[20:23], a[104:107]\n"
"s_lshr_b32 s59, s59, 7\n"
"ds_read_b128 a[176:179], v240 offset:12288\n"
"v_cvt_pk_bf16_f32 v16, v0, v1\n"
"v_mfma_f32_16x16x32_bf16 a[108:111], a[236:239], v[20:23], a[108:111]\n"
"s_mul_i32 s60, s59, 3\n"
"v_exp_f32 v2, v2\n"
"v_mfma_f32_16x16x32_bf16 a[112:115], a[240:243], v[20:23], a[112:115]\n"
"s_sub_u32 s58, s58, s60\n"
"ds_read_b128 a[180:183], v240 offset:12544\n"
"v_add_f32 v44, v44, v0\n"
"v_mfma_f32_16x16x32_bf16 a[116:119], a[244:247], v[20:23], a[116:119]\n"
"s_add_u32 s58, s58, s56\n"
"v_exp_f32 v3, v3\n"
"v_mfma_f32_16x16x32_bf16 a[120:123], a[248:251], v[20:23], a[120:123]\n"
"s_add_u32 s58, s58, 1\n"
"ds_read_b128 a[184:187], v240 offset:12800\n"
"v_cvt_pk_bf16_f32 v17, v2, v3\n"
"v_mfma_f32_16x16x32_bf16 a[124:127], a[252:255], v[20:23], a[124:127]\n"
"v_exp_f32 v4, v4\n"
"v_mfma_f32_16x16x32_bf16 v[56:59], v[40:43], v[20:23], v[56:59]\n"
"ds_read_b128 a[188:191], v240 offset:13056\n"
"v_add_f32 v44, v44, v1\n"
"v_mfma_f32_16x16x32_bf16 v[8:11], a[128:131], v[80:83], v[28:31]\n"
"v_exp_f32 v5, v5\n"
"v_mfma_f32_16x16x32_bf16 v[8:11], a[132:135], v[84:87], v[8:11]\n"
"ds_read_b64_tr_b16 a[224:225], v242 offset:24576\n"
"v_cvt_pk_bf16_f32 v18, v4, v5\n"
"v_mfma_f32_16x16x32_bf16 v[8:11], a[136:139], v[88:91], v[8:11]\n"
"v_exp_f32 v6, v6\n"
"v_mfma_f32_16x16x32_bf16 v[8:11], a[140:143], v[92:95], v[8:11]\n"
"ds_read_b64_tr_b16 a[226:227], v242 offset:28672\n"
"buffer_load_dwordx4 v[176:179], v252, s[68:71], s49 offen\n"
"v_add_f32 v44, v44, v2\n"
"v_mfma_f32_16x16x32_bf16 v[12:15], a[144:147], v[80:83], v[28:31]\n"
"s_and_b32 s58, s58, 3\n"
"v_exp_f32 v7, v7\n"
"v_mfma_f32_16x16x32_bf16 v[12:15], a[148:151], v[84:87], v[12:15]\n"
"s_lshl_b32 s58, s58, 3\n"
"ds_read_b64_tr_b16 a[228:229], v243 offset:24576\n"
"v_cvt_pk_bf16_f32 v19, v6, v7\n"
"v_mfma_f32_16x16x32_bf16 v[12:15], a[152:155], v[88:91], v[12:15]\n"
"s_add_u32 s58, s58, s59\n"
"buffer_load_dwordx4 v247, s[12:15], s84 offen offset:1024 lds\n"
"v_mfma_f32_16x16x32_bf16 v[12:15], a[156:159], v[92:95], v[12:15]\n"
"s_waitcnt lgkmcnt(11)\n"
"v_exp_f32 v8, v8\n"
"v_mfma_f32_16x16x32_bf16 a[0:3], a[192:195], v[16:19], a[0:3]\n"
"s_mul_i32 s58, s58, 36864\n"
"ds_read_b64_tr_b16 a[230:231], v243 offset:28672\n"
"v_add_f32 v44, v44, v3\n"
"v_mfma_f32_16x16x32_bf16 a[4:7], a[196:199], v[16:19], a[4:7]\n"
"s_add_u32 s39, s58, s57\n"
"v_exp_f32 v9, v9\n"
"v_mfma_f32_16x16x32_bf16 a[8:11], a[200:203], v[16:19], a[8:11]\n"
"s_mul_i32 s58, s41, 9216\n"
"ds_read_b64_tr_b16 a[232:233], v242 offset:24832\n"
"v_cvt_pk_bf16_f32 v20, v8, v9\n"
"v_mfma_f32_16x16x32_bf16 a[12:15], a[204:207], v[16:19], a[12:15]\n"
"s_add_u32 s58, s58, s40\n"
"v_exp_f32 v10, v10\n"
"v_mfma_f32_16x16x32_bf16 a[16:19], a[208:211], v[16:19], a[16:19]\n"
"s_sub_u32 s53, s58, 27648\n"
"ds_read_b64_tr_b16 a[234:235], v242 offset:28928\n"
"v_add_f32 v44, v44, v4\n"
"v_mfma_f32_16x16x32_bf16 a[20:23], a[212:215], v[16:19], a[20:23]\n"
"s_add_u32 s58, s58, 131072\n"
"v_exp_f32 v11, v11\n"
"v_mfma_f32_16x16x32_bf16 a[24:27], a[216:219], v[16:19], a[24:27]\n"
"s_cmp_lt_u32 s41, 3\n"
"s_cselect_b32 s51, s58, s40\n"
"ds_read_b64_tr_b16 a[236:237], v243 offset:24832\n"
"v_cvt_pk_bf16_f32 v21, v10, v11\n"
"v_mfma_f32_16x16x32_bf16 a[28:31], a[220:223], v[16:19], a[28:31]\n"
"s_nop 0\n"
"v_exp_f32 v12, v12\n"
"v_mfma_f32_16x16x32_bf16 v[220:223], a[128:131], v[96:99], v[32:35]\n"
"s_and_b32 s58, s56, 1\n"
"ds_read_b64_tr_b16 a[238:239], v243 offset:28928\n"
"v_add_f32 v44, v44, v5\n"
"v_mfma_f32_16x16x32_bf16 v[220:223], a[132:135], v[100:103], v[220:223]\n"
"s_lshl_b32 s59, s58, 5\n"
"v_exp_f32 v13, v13\n"
"v_mfma_f32_16x16x32_bf16 v[220:223], a[136:139], v[104:107], v[220:223]\n"
"s_add_u32 s59, s59, s40\n"
"ds_read_b64_tr_b16 a[240:241], v242 offset:25088\n"
"v_cvt_pk_bf16_f32 v22, v12, v13\n"
"v_mfma_f32_16x16x32_bf16 v[220:223], a[140:143], v[108:111], v[220:223]\n"
"s_xor_b32 s60, s58, 1\n"
"v_exp_f32 v14, v14\n"
"v_mfma_f32_16x16x32_bf16 v[224:227], a[144:147], v[96:99], v[32:35]\n"
"s_lshl_b32 s60, s60, 8\n"
"ds_read_b64_tr_b16 a[242:243], v242 offset:29184\n"
"v_add_f32 v44, v44, v6\n"
"v_mfma_f32_16x16x32_bf16 v[224:227], a[148:151], v[100:103], v[224:227]\n"
"s_add_u32 s49, s49, 285696\n"
"v_exp_f32 v15, v15\n"
"v_mfma_f32_16x16x32_bf16 v[224:227], a[152:155], v[104:107], v[224:227]\n"
"ds_read_b64_tr_b16 a[244:245], v243 offset:25088\n"
"v_cvt_pk_bf16_f32 v23, v14, v15\n"
"v_mfma_f32_16x16x32_bf16 v[224:227], a[156:159], v[108:111], v[224:227]\n"
"v_exp_f32 v220, v220\n"
"v_mfma_f32_16x16x32_bf16 a[32:35], a[192:195], v[20:23], a[32:35]\n"
"buffer_load_dwordx4 v246, s[12:15], s84 offen offset:2048 lds\n"
"v_mfma_f32_16x16x32_bf16 a[36:39], a[196:199], v[20:23], a[36:39]\n"
"v_exp_f32 v221, v221\n"
"v_mfma_f32_16x16x32_bf16 a[40:43], a[200:203], v[20:23], a[40:43]\n"
"ds_read_b64_tr_b16 a[246:247], v243 offset:29184\n"
"v_cvt_pk_bf16_f32 v16, v220, v221\n"
"v_mfma_f32_16x16x32_bf16 a[44:47], a[204:207], v[20:23], a[44:47]\n"
"v_exp_f32 v222, v222\n"
"v_mfma_f32_16x16x32_bf16 a[48:51], a[208:211], v[20:23], a[48:51]\n"
"ds_read_b64_tr_b16 a[248:249], v242 offset:25344\n"
"buffer_load_dwordx4 v[144:147], v252, s[64:67], s49 offen\n"
"v_add_f32 v44, v44, v7\n"
"v_mfma_f32_16x16x32_bf16 a[52:55], a[212:215], v[20:23], a[52:55]\n"
"s_add_u32 s60, s60, s40\n"
"v_exp_f32 v223, v223\n"
"v_mfma_f32_16x16x32_bf16 a[56:59], a[216:219], v[20:23], a[56:59]\n"
"ds_read_b64_tr_b16 a[250:251], v242 offset:29440\n"
"v_cvt_pk_bf16_f32 v17, v222, v223\n"
"v_mfma_f32_16x16x32_bf16 a[60:63], a[220:223], v[20:23], a[60:63]\n"
"v_exp_f32 v224, v224\n"
"v_mfma_f32_16x16x32_bf16 v[48:51], v[40:43], v[20:23], v[48:51]\n"
"ds_read_b64_tr_b16 a[252:253], v243 offset:25344\n"
"v_add_f32 v52, v52, v220\n"
"v_mfma_f32_16x16x32_bf16 v[228:231], a[128:131], v[112:115], v[36:39]\n"
"v_exp_f32 v225, v225\n"
"v_mfma_f32_16x16x32_bf16 v[228:231], a[132:135], v[116:119], v[228:231]\n"
"ds_read_b64_tr_b16 a[254:255], v243 offset:29440\n"
"v_cvt_pk_bf16_f32 v18, v224, v225\n"
"v_mfma_f32_16x16x32_bf16 v[228:231], a[136:139], v[120:123], v[228:231]\n"
"v_exp_f32 v226, v226\n"
"v_mfma_f32_16x16x32_bf16 v[228:231], a[140:143], v[124:127], v[228:231]\n"
"v_add_f32 v52, v52, v221\n"
"v_add_f32 v52, v52, v222\n"
"v_mfma_f32_16x16x32_bf16 v[232:235], a[144:147], v[112:115], v[36:39]\n"
"v_exp_f32 v227, v227\n"
"v_mfma_f32_16x16x32_bf16 v[232:235], a[148:151], v[116:119], v[232:235]\n"
"v_cvt_pk_bf16_f32 v19, v226, v227\n"
"v_add_f32 v52, v52, v223\n"
"v_mfma_f32_16x16x32_bf16 v[232:235], a[152:155], v[120:123], v[232:235]\n"
"buffer_load_dwordx4 v247, s[12:15], s84 offen offset:3072 lds\n"
"v_lshrrev_b32 v217, 2, v248\n"
"v_mfma_f32_16x16x32_bf16 v[232:235], a[156:159], v[124:127], v[232:235]\n"
"s_add_u32 s84, s84, 16384\n"
"v_exp_f32 v228, v228\n"
"v_mfma_f32_16x16x32_bf16 a[64:67], a[192:195], v[16:19], a[64:67]\n"
"v_add_f32 v52, v52, v224\n"
"v_add_f32 v52, v52, v225\n"
"v_mfma_f32_16x16x32_bf16 a[68:71], a[196:199], v[16:19], a[68:71]\n"
"v_exp_f32 v229, v229\n"
"v_mfma_f32_16x16x32_bf16 a[72:75], a[200:203], v[16:19], a[72:75]\n"
"v_cvt_pk_bf16_f32 v20, v228, v229\n"
"v_add_f32 v52, v52, v226\n"
"v_mfma_f32_16x16x32_bf16 a[76:79], a[204:207], v[16:19], a[76:79]\n"
"v_exp_f32 v230, v230\n"
"v_mfma_f32_16x16x32_bf16 a[80:83], a[208:211], v[16:19], a[80:83]\n"
"v_add_f32 v52, v52, v227\n"
"v_mul_u32_u24 v203, 288, v217\n"
"v_mfma_f32_16x16x32_bf16 a[84:87], a[212:215], v[16:19], a[84:87]\n"
"v_exp_f32 v231, v231\n"
"v_mfma_f32_16x16x32_bf16 a[88:91], a[216:219], v[16:19], a[88:91]\n"
"s_waitcnt vmcnt(24) lgkmcnt(0)\n"
"s_barrier\n"
"v_cvt_pk_bf16_f32 v21, v230, v231\n"
"v_and_b32 v217, 3, v248\n"
"v_mfma_f32_16x16x32_bf16 a[92:95], a[220:223], v[16:19], a[92:95]\n"
"v_exp_f32 v232, v232\n"
"v_mfma_f32_16x16x32_bf16 v[0:3], a[160:163], v[64:67], v[24:27]\n"
"ds_read_b128 a[128:131], v240 offset:32768\n"
"v_lshl_add_u32 v203, v217, 3, v203\n"
"v_mfma_f32_16x16x32_bf16 v[0:3], a[164:167], v[68:71], v[0:3]\n"
"v_exp_f32 v233, v233\n"
"v_mfma_f32_16x16x32_bf16 v[0:3], a[168:171], v[72:75], v[0:3]\n"
"ds_read_b128 a[132:135], v240 offset:33024\n"
"v_cvt_pk_bf16_f32 v22, v232, v233\n"
"v_mfma_f32_16x16x32_bf16 v[0:3], a[172:175], v[76:79], v[0:3]\n"
"v_exp_f32 v234, v234\n"
"v_mfma_f32_16x16x32_bf16 v[4:7], a[176:179], v[64:67], v[24:27]\n"
"ds_read_b128 a[136:139], v240 offset:33280\n"
"v_add_u32 v200, s59, v203\n"
"v_mfma_f32_16x16x32_bf16 v[4:7], a[180:183], v[68:71], v[4:7]\n"
"v_exp_f32 v235, v235\n"
"v_mfma_f32_16x16x32_bf16 v[4:7], a[184:187], v[72:75], v[4:7]\n"
"ds_read_b128 a[140:143], v240 offset:33536\n"
"v_cvt_pk_bf16_f32 v23, v234, v235\n"
"v_mfma_f32_16x16x32_bf16 v[4:7], a[188:191], v[76:79], v[4:7]\n"
"s_mov_b32 m0, s51\n"
"v_exp_f32 v0, v0\n"
"v_mfma_f32_16x16x32_bf16 a[96:99], a[192:195], v[20:23], a[96:99]\n"
"buffer_load_dwordx4 v210, s[24:27], s20 offen offset:0 lds\n"
"v_add_u32 v203, s60, v203\n"
"v_mfma_f32_16x16x32_bf16 a[100:103], a[196:199], v[20:23], a[100:103]\n"
"v_exp_f32 v1, v1\n"
"v_mfma_f32_16x16x32_bf16 a[104:107], a[200:203], v[20:23], a[104:107]\n"
"ds_read_b128 a[144:147], v240 offset:36864\n"
"v_cvt_pk_bf16_f32 v16, v0, v1\n"
"v_mfma_f32_16x16x32_bf16 a[108:111], a[204:207], v[20:23], a[108:111]\n"
"v_exp_f32 v2, v2\n"
"v_mfma_f32_16x16x32_bf16 a[112:115], a[208:211], v[20:23], a[112:115]\n"
"ds_read_b128 a[148:151], v240 offset:37120\n"
"v_add_f32 v44, v44, v0\n"
"v_mfma_f32_16x16x32_bf16 a[116:119], a[212:215], v[20:23], a[116:119]\n"
"v_exp_f32 v3, v3\n"
"v_mfma_f32_16x16x32_bf16 a[120:123], a[216:219], v[20:23], a[120:123]\n"
"ds_read_b128 a[152:155], v240 offset:37376\n"
"v_cvt_pk_bf16_f32 v17, v2, v3\n"
"v_mfma_f32_16x16x32_bf16 a[124:127], a[220:223], v[20:23], a[124:127]\n"
"v_exp_f32 v4, v4\n"
"v_mfma_f32_16x16x32_bf16 v[56:59], v[40:43], v[20:23], v[56:59]\n"
"ds_read_b128 a[156:159], v240 offset:37632\n"
"v_add_f32 v44, v44, v1\n"
"v_mfma_f32_16x16x32_bf16 v[8:11], a[160:163], v[80:83], v[28:31]\n"
"v_exp_f32 v5, v5\n"
"v_mfma_f32_16x16x32_bf16 v[8:11], a[164:167], v[84:87], v[8:11]\n"
"ds_read_b64_tr_b16 a[192:193], v242 offset:49152\n"
"v_cvt_pk_bf16_f32 v18, v4, v5\n"
"v_mfma_f32_16x16x32_bf16 v[8:11], a[168:171], v[88:91], v[8:11]\n"
"v_exp_f32 v6, v6\n"
"v_mfma_f32_16x16x32_bf16 v[8:11], a[172:175], v[92:95], v[8:11]\n"
"ds_read_b64_tr_b16 a[194:195], v242 offset:53248\n"
"v_add_f32 v44, v44, v2\n"
"v_mfma_f32_16x16x32_bf16 v[12:15], a[176:179], v[80:83], v[28:31]\n"
"v_exp_f32 v7, v7\n"
"v_mfma_f32_16x16x32_bf16 v[12:15], a[180:183], v[84:87], v[12:15]\n"
"s_mov_b32 m0, s51\n"
"ds_read_b64_tr_b16 a[196:197], v243 offset:49152\n"
"v_cvt_pk_bf16_f32 v19, v6, v7\n"
"v_mfma_f32_16x16x32_bf16 v[12:15], a[184:187], v[88:91], v[12:15]\n"
"buffer_load_dwordx4 v211, s[24:27], s20 offen offset:1024 lds\n"
"v_add_u32 v201, 65536, v200\n"
"v_mfma_f32_16x16x32_bf16 v[12:15], a[188:191], v[92:95], v[12:15]\n"
"s_waitcnt lgkmcnt(11)\n"
"v_exp_f32 v8, v8\n"
"v_mfma_f32_16x16x32_bf16 a[0:3], a[224:227], v[16:19], a[0:3]\n"
"ds_read_b64_tr_b16 a[198:199], v243 offset:53248\n"
"v_add_f32 v44, v44, v3\n"
"v_mfma_f32_16x16x32_bf16 a[4:7], a[228:231], v[16:19], a[4:7]\n"
"v_exp_f32 v9, v9\n"
"v_mfma_f32_16x16x32_bf16 a[8:11], a[232:235], v[16:19], a[8:11]\n"
"ds_read_b64_tr_b16 a[200:201], v242 offset:49408\n"
"v_cvt_pk_bf16_f32 v20, v8, v9\n"
"v_mfma_f32_16x16x32_bf16 a[12:15], a[236:239], v[16:19], a[12:15]\n"
"v_exp_f32 v10, v10\n"
"v_mfma_f32_16x16x32_bf16 a[16:19], a[240:243], v[16:19], a[16:19]\n"
"ds_read_b64_tr_b16 a[202:203], v242 offset:53504\n"
"v_add_f32 v44, v44, v4\n"
"v_mfma_f32_16x16x32_bf16 a[20:23], a[244:247], v[16:19], a[20:23]\n"
"v_exp_f32 v11, v11\n"
"v_mfma_f32_16x16x32_bf16 a[24:27], a[248:251], v[16:19], a[24:27]\n"
"ds_read_b64_tr_b16 a[204:205], v243 offset:49408\n"
"v_cvt_pk_bf16_f32 v21, v10, v11\n"
"v_mfma_f32_16x16x32_bf16 a[28:31], a[252:255], v[16:19], a[28:31]\n"
"v_exp_f32 v12, v12\n"
"v_mfma_f32_16x16x32_bf16 v[220:223], a[160:163], v[96:99], v[32:35]\n"
"ds_read_b64_tr_b16 a[206:207], v243 offset:53504\n"
"v_add_f32 v44, v44, v5\n"
"v_mfma_f32_16x16x32_bf16 v[220:223], a[164:167], v[100:103], v[220:223]\n"
"v_exp_f32 v13, v13\n"
"v_mfma_f32_16x16x32_bf16 v[220:223], a[168:171], v[104:107], v[220:223]\n"
"ds_read_b64_tr_b16 a[208:209], v242 offset:49664\n"
"v_cvt_pk_bf16_f32 v22, v12, v13\n"
"v_mfma_f32_16x16x32_bf16 v[220:223], a[172:175], v[108:111], v[220:223]\n"
"v_exp_f32 v14, v14\n"
"v_mfma_f32_16x16x32_bf16 v[224:227], a[176:179], v[96:99], v[32:35]\n"
"ds_read_b64_tr_b16 a[210:211], v242 offset:53760\n"
"v_add_f32 v44, v44, v6\n"
"v_mfma_f32_16x16x32_bf16 v[224:227], a[180:183], v[100:103], v[224:227]\n"
"v_exp_f32 v15, v15\n"
"v_mfma_f32_16x16x32_bf16 v[224:227], a[184:187], v[104:107], v[224:227]\n"
"ds_read_b64_tr_b16 a[212:213], v243 offset:49664\n"
"v_cvt_pk_bf16_f32 v23, v14, v15\n"
"v_mfma_f32_16x16x32_bf16 v[224:227], a[188:191], v[108:111], v[224:227]\n"
"s_mov_b32 m0, s51\n"
"v_exp_f32 v220, v220\n"
"v_mfma_f32_16x16x32_bf16 a[32:35], a[224:227], v[20:23], a[32:35]\n"
"buffer_load_dwordx4 v212, s[24:27], s20 offen offset:2048 lds\n"
"v_add_u32 v204, 65536, v203\n"
"v_mfma_f32_16x16x32_bf16 a[36:39], a[228:231], v[20:23], a[36:39]\n"
"v_exp_f32 v221, v221\n"
"v_mfma_f32_16x16x32_bf16 a[40:43], a[232:235], v[20:23], a[40:43]\n"
"ds_read_b64_tr_b16 a[214:215], v243 offset:53760\n"
"v_cvt_pk_bf16_f32 v16, v220, v221\n"
"v_mfma_f32_16x16x32_bf16 a[44:47], a[236:239], v[20:23], a[44:47]\n"
"v_exp_f32 v222, v222\n"
"v_mfma_f32_16x16x32_bf16 a[48:51], a[240:243], v[20:23], a[48:51]\n"
"ds_read_b64_tr_b16 a[216:217], v242 offset:49920\n"
"v_add_f32 v44, v44, v7\n"
"v_mfma_f32_16x16x32_bf16 a[52:55], a[244:247], v[20:23], a[52:55]\n"
"v_exp_f32 v223, v223\n"
"v_mfma_f32_16x16x32_bf16 a[56:59], a[248:251], v[20:23], a[56:59]\n"
"ds_read_b64_tr_b16 a[218:219], v242 offset:54016\n"
"v_cvt_pk_bf16_f32 v17, v222, v223\n"
"v_mfma_f32_16x16x32_bf16 a[60:63], a[252:255], v[20:23], a[60:63]\n"
"v_exp_f32 v224, v224\n"
"v_mfma_f32_16x16x32_bf16 v[48:51], v[40:43], v[20:23], v[48:51]\n"
"ds_read_b64_tr_b16 a[220:221], v243 offset:49920\n"
"v_add_f32 v52, v52, v220\n"
"v_mfma_f32_16x16x32_bf16 v[228:231], a[160:163], v[112:115], v[36:39]\n"
"v_exp_f32 v225, v225\n"
"v_mfma_f32_16x16x32_bf16 v[228:231], a[164:167], v[116:119], v[228:231]\n"
"ds_read_b64_tr_b16 a[222:223], v243 offset:54016\n"
"v_cvt_pk_bf16_f32 v18, v224, v225\n"
"v_mfma_f32_16x16x32_bf16 v[228:231], a[168:171], v[120:123], v[228:231]\n"
"v_exp_f32 v226, v226\n"
"v_mfma_f32_16x16x32_bf16 v[228:231], a[172:175], v[124:127], v[228:231]\n"
"v_add_f32 v52, v52, v221\n"
"v_add_f32 v52, v52, v222\n"
"v_mfma_f32_16x16x32_bf16 v[232:235], a[176:179], v[112:115], v[36:39]\n"
"v_exp_f32 v227, v227\n"
"v_mfma_f32_16x16x32_bf16 v[232:235], a[180:183], v[116:119], v[232:235]\n"
"s_mov_b32 m0, s51\n"
"v_cvt_pk_bf16_f32 v19, v226, v227\n"
"v_add_f32 v52, v52, v223\n"
"v_mfma_f32_16x16x32_bf16 v[232:235], a[184:187], v[120:123], v[232:235]\n"
"buffer_load_dwordx4 v213, s[24:27], s20 offen offset:3072 lds\n"
"v_add_u32 v202, 131072, v200\n"
"v_mfma_f32_16x16x32_bf16 v[232:235], a[188:191], v[124:127], v[232:235]\n"
"v_exp_f32 v228, v228\n"
"v_mfma_f32_16x16x32_bf16 a[64:67], a[224:227], v[16:19], a[64:67]\n"
"v_add_f32 v52, v52, v224\n"
"v_add_f32 v52, v52, v225\n"
"v_mfma_f32_16x16x32_bf16 a[68:71], a[228:231], v[16:19], a[68:71]\n"
"v_exp_f32 v229, v229\n"
"v_mfma_f32_16x16x32_bf16 a[72:75], a[232:235], v[16:19], a[72:75]\n"
"v_cvt_pk_bf16_f32 v20, v228, v229\n"
"v_add_f32 v52, v52, v226\n"
"v_mfma_f32_16x16x32_bf16 a[76:79], a[236:239], v[16:19], a[76:79]\n"
"v_exp_f32 v230, v230\n"
"v_mfma_f32_16x16x32_bf16 a[80:83], a[240:243], v[16:19], a[80:83]\n"
"v_add_f32 v52, v52, v227\n"
"v_add_u32 v205, 131072, v203\n"
"v_mfma_f32_16x16x32_bf16 a[84:87], a[244:247], v[16:19], a[84:87]\n"
"s_add_u32 s62, s40, 163776\n"
"v_exp_f32 v231, v231\n"
"v_mfma_f32_16x16x32_bf16 a[88:91], a[248:251], v[16:19], a[88:91]\n"
"s_waitcnt lgkmcnt(8)\n"
"v_cvt_pk_bf16_f32 v21, v230, v231\n"
"v_lshl_add_u32 v209, v217, 2, s62\n"
"v_mfma_f32_16x16x32_bf16 a[92:95], a[252:255], v[16:19], a[92:95]\n"
"s_lshl_b32 s55, s44, 4\n"
"v_exp_f32 v232, v232\n"
"v_mfma_f32_16x16x32_bf16 v[0:3], a[128:131], v[64:67], v[24:27]\n"
"s_add_u32 s55, s55, s43\n"
"ds_read_b128 a[160:163], v240 offset:40960\n"
"v_mfma_f32_16x16x32_bf16 v[0:3], a[132:135], v[68:71], v[0:3]\n"
"s_lshl_b32 s52, s55, 2\n"
"v_exp_f32 v233, v233\n"
"v_mfma_f32_16x16x32_bf16 v[0:3], a[136:139], v[72:75], v[0:3]\n"
"ds_read_b128 a[164:167], v240 offset:41216\n"
"buffer_load_dwordx4 v[180:183], v252, s[68:71], s49 offen\n"
"v_cvt_pk_bf16_f32 v22, v232, v233\n"
"v_mfma_f32_16x16x32_bf16 v[0:3], a[140:143], v[76:79], v[0:3]\n"
"s_add_u32 s52, s52, s41\n"
"v_exp_f32 v234, v234\n"
"v_mfma_f32_16x16x32_bf16 v[4:7], a[144:147], v[64:67], v[24:27]\n"
"s_lshl_b32 s52, s52, 14\n"
"ds_read_b128 a[168:171], v240 offset:41472\n"
"v_mfma_f32_16x16x32_bf16 v[4:7], a[148:151], v[68:71], v[4:7]\n"
"s_add_u32 s52, s52, 9592832\n"
"v_exp_f32 v235, v235\n"
"v_mfma_f32_16x16x32_bf16 v[4:7], a[152:155], v[72:75], v[4:7]\n"
"s_add_u32 s49, s49, 9216\n"
"ds_read_b128 a[172:175], v240 offset:41728\n"
"v_cvt_pk_bf16_f32 v23, v234, v235\n"
"v_mfma_f32_16x16x32_bf16 v[4:7], a[156:159], v[76:79], v[4:7]\n"
"s_add_u32 m0, s51, 4096\n"
"v_exp_f32 v0, v0\n"
"v_mfma_f32_16x16x32_bf16 a[96:99], a[224:227], v[20:23], a[96:99]\n"
"buffer_load_dwordx4 v214, s[24:27], s20 offen offset:0 lds\n"
"v_mfma_f32_16x16x32_bf16 a[100:103], a[228:231], v[20:23], a[100:103]\n"
"v_exp_f32 v1, v1\n"
"v_mfma_f32_16x16x32_bf16 a[104:107], a[232:235], v[20:23], a[104:107]\n"
"ds_read_b128 a[176:179], v240 offset:45056\n"
"v_cvt_pk_bf16_f32 v16, v0, v1\n"
"v_mfma_f32_16x16x32_bf16 a[108:111], a[236:239], v[20:23], a[108:111]\n"
"v_exp_f32 v2, v2\n"
"v_mfma_f32_16x16x32_bf16 a[112:115], a[240:243], v[20:23], a[112:115]\n"
"ds_read_b128 a[180:183], v240 offset:45312\n"
"v_add_f32 v44, v44, v0\n"
"v_mfma_f32_16x16x32_bf16 a[116:119], a[244:247], v[20:23], a[116:119]\n"
"v_exp_f32 v3, v3\n"
"v_mfma_f32_16x16x32_bf16 a[120:123], a[248:251], v[20:23], a[120:123]\n"
"ds_read_b128 a[184:187], v240 offset:45568\n"
"v_cvt_pk_bf16_f32 v17, v2, v3\n"
"v_mfma_f32_16x16x32_bf16 a[124:127], a[252:255], v[20:23], a[124:127]\n"
"v_exp_f32 v4, v4\n"
"v_mfma_f32_16x16x32_bf16 v[56:59], v[40:43], v[20:23], v[56:59]\n"
"ds_read_b128 a[188:191], v240 offset:45824\n"
"v_add_f32 v44, v44, v1\n"
"v_mfma_f32_16x16x32_bf16 v[8:11], a[128:131], v[80:83], v[28:31]\n"
"v_exp_f32 v5, v5\n"
"v_mfma_f32_16x16x32_bf16 v[8:11], a[132:135], v[84:87], v[8:11]\n"
"ds_read_b64_tr_b16 a[224:225], v242 offset:57344\n"
"v_cvt_pk_bf16_f32 v18, v4, v5\n"
"v_mfma_f32_16x16x32_bf16 v[8:11], a[136:139], v[88:91], v[8:11]\n"
"v_exp_f32 v6, v6\n"
"v_mfma_f32_16x16x32_bf16 v[8:11], a[140:143], v[92:95], v[8:11]\n"
"ds_read_b64_tr_b16 a[226:227], v242 offset:61440\n"
"v_add_f32 v44, v44, v2\n"
"v_mfma_f32_16x16x32_bf16 v[12:15], a[144:147], v[80:83], v[28:31]\n"
"v_exp_f32 v7, v7\n"
"v_mfma_f32_16x16x32_bf16 v[12:15], a[148:151], v[84:87], v[12:15]\n"
"s_add_u32 m0, s51, 4096\n"
"ds_read_b64_tr_b16 a[228:229], v243 offset:57344\n"
"v_cvt_pk_bf16_f32 v19, v6, v7\n"
"v_mfma_f32_16x16x32_bf16 v[12:15], a[152:155], v[88:91], v[12:15]\n"
"buffer_load_dwordx4 v236, s[24:27], s20 offen offset:1024 lds\n"
"v_mfma_f32_16x16x32_bf16 v[12:15], a[156:159], v[92:95], v[12:15]\n"
"s_waitcnt lgkmcnt(11)\n"
"v_exp_f32 v8, v8\n"
"v_mfma_f32_16x16x32_bf16 a[0:3], a[192:195], v[16:19], a[0:3]\n"
"ds_read_b64_tr_b16 a[230:231], v243 offset:61440\n"
"v_add_f32 v44, v44, v3\n"
"v_mfma_f32_16x16x32_bf16 a[4:7], a[196:199], v[16:19], a[4:7]\n"
"v_exp_f32 v9, v9\n"
"v_mfma_f32_16x16x32_bf16 a[8:11], a[200:203], v[16:19], a[8:11]\n"
"ds_read_b64_tr_b16 a[232:233], v242 offset:57600\n"
"buffer_load_dwordx4 v[148:151], v252, s[64:67], s49 offen\n"
"v_cvt_pk_bf16_f32 v20, v8, v9\n"
"v_mfma_f32_16x16x32_bf16 a[12:15], a[204:207], v[16:19], a[12:15]\n"
"v_exp_f32 v10, v10\n"
"v_mfma_f32_16x16x32_bf16 a[16:19], a[208:211], v[16:19], a[16:19]\n"
"ds_read_b64_tr_b16 a[234:235], v242 offset:61696\n"
"v_add_f32 v44, v44, v4\n"
"v_mfma_f32_16x16x32_bf16 a[20:23], a[212:215], v[16:19], a[20:23]\n"
"s_add_u32 m0, s51, 4096\n"
"v_exp_f32 v11, v11\n"
"v_mfma_f32_16x16x32_bf16 a[24:27], a[216:219], v[16:19], a[24:27]\n"
"buffer_load_dwordx4 v237, s[24:27], s20 offen offset:2048 lds\n"
"ds_read_b64_tr_b16 a[236:237], v243 offset:57600\n"
"v_cvt_pk_bf16_f32 v21, v10, v11\n"
"v_mfma_f32_16x16x32_bf16 a[28:31], a[220:223], v[16:19], a[28:31]\n"
"v_exp_f32 v12, v12\n"
"v_mfma_f32_16x16x32_bf16 v[220:223], a[128:131], v[96:99], v[32:35]\n"
"ds_read_b64_tr_b16 a[238:239], v243 offset:61696\n"
"v_add_f32 v44, v44, v5\n"
"v_mfma_f32_16x16x32_bf16 v[220:223], a[132:135], v[100:103], v[220:223]\n"
"v_exp_f32 v13, v13\n"
"v_mfma_f32_16x16x32_bf16 v[220:223], a[136:139], v[104:107], v[220:223]\n"
"ds_read_b64_tr_b16 a[240:241], v242 offset:57856\n"
"v_cvt_pk_bf16_f32 v22, v12, v13\n"
"v_mfma_f32_16x16x32_bf16 v[220:223], a[140:143], v[108:111], v[220:223]\n"
"v_exp_f32 v14, v14\n"
"v_mfma_f32_16x16x32_bf16 v[224:227], a[144:147], v[96:99], v[32:35]\n"
"ds_read_b64_tr_b16 a[242:243], v242 offset:61952\n"
"v_add_f32 v44, v44, v6\n"
"v_mfma_f32_16x16x32_bf16 v[224:227], a[148:151], v[100:103], v[224:227]\n"
"v_exp_f32 v15, v15\n"
"v_mfma_f32_16x16x32_bf16 v[224:227], a[152:155], v[104:107], v[224:227]\n"
"ds_read_b64_tr_b16 a[244:245], v243 offset:57856\n"
"v_cvt_pk_bf16_f32 v23, v14, v15\n"
"v_mfma_f32_16x16x32_bf16 v[224:227], a[156:159], v[108:111], v[224:227]\n"
"s_add_u32 m0, s51, 4096\n"
"v_exp_f32 v220, v220\n"
"v_mfma_f32_16x16x32_bf16 a[32:35], a[192:195], v[20:23], a[32:35]\n"
"buffer_load_dwordx4 v238, s[24:27], s20 offen offset:3072 lds\n"
"v_mfma_f32_16x16x32_bf16 a[36:39], a[196:199], v[20:23], a[36:39]\n"
"v_exp_f32 v221, v221\n"
"v_mfma_f32_16x16x32_bf16 a[40:43], a[200:203], v[20:23], a[40:43]\n"
"ds_read_b64_tr_b16 a[246:247], v243 offset:61952\n"
"v_cvt_pk_bf16_f32 v16, v220, v221\n"
"v_mfma_f32_16x16x32_bf16 a[44:47], a[204:207], v[20:23], a[44:47]\n"
"v_exp_f32 v222, v222\n"
"v_mfma_f32_16x16x32_bf16 a[48:51], a[208:211], v[20:23], a[48:51]\n"
"ds_read_b64_tr_b16 a[248:249], v242 offset:58112\n"
"v_add_f32 v44, v44, v7\n"
"v_mfma_f32_16x16x32_bf16 a[52:55], a[212:215], v[20:23], a[52:55]\n"
"v_exp_f32 v223, v223\n"
"v_mfma_f32_16x16x32_bf16 a[56:59], a[216:219], v[20:23], a[56:59]\n"
"ds_read_b64_tr_b16 a[250:251], v242 offset:62208\n"
"v_cvt_pk_bf16_f32 v17, v222, v223\n"
"v_mfma_f32_16x16x32_bf16 a[60:63], a[220:223], v[20:23], a[60:63]\n"
"v_exp_f32 v224, v224\n"
"v_mfma_f32_16x16x32_bf16 v[48:51], v[40:43], v[20:23], v[48:51]\n"
"ds_read_b64_tr_b16 a[252:253], v243 offset:58112\n"
"v_add_f32 v52, v52, v220\n"
"v_mfma_f32_16x16x32_bf16 v[228:231], a[128:131], v[112:115], v[36:39]\n"
"v_exp_f32 v225, v225\n"
"v_mfma_f32_16x16x32_bf16 v[228:231], a[132:135], v[116:119], v[228:231]\n"
"ds_read_b64_tr_b16 a[254:255], v243 offset:62208\n"
"buffer_load_dwordx4 v[184:187], v252, s[68:71], s49 offen\n"
"v_cvt_pk_bf16_f32 v18, v224, v225\n"
"v_mfma_f32_16x16x32_bf16 v[228:231], a[136:139], v[120:123], v[228:231]\n"
"s_add_u32 s49, s49, 285696\n"
"v_exp_f32 v226, v226\n"
"v_mfma_f32_16x16x32_bf16 v[228:231], a[140:143], v[124:127], v[228:231]\n"
"v_add_f32 v52, v52, v221\n"
"v_add_f32 v52, v52, v222\n"
"v_mfma_f32_16x16x32_bf16 v[232:235], a[144:147], v[112:115], v[36:39]\n"
"v_exp_f32 v227, v227\n"
"v_mfma_f32_16x16x32_bf16 v[232:235], a[148:151], v[116:119], v[232:235]\n"
"s_add_u32 m0, s51, 8192\n"
"v_cvt_pk_bf16_f32 v19, v226, v227\n"
"v_add_f32 v52, v52, v223\n"
"v_mfma_f32_16x16x32_bf16 v[232:235], a[152:155], v[120:123], v[232:235]\n"
"buffer_load_dwordx4 v239, s[24:27], s20 offen offset:0 lds\n"
"v_mfma_f32_16x16x32_bf16 v[232:235], a[156:159], v[124:127], v[232:235]\n"
"v_exp_f32 v228, v228\n"
"v_mfma_f32_16x16x32_bf16 a[64:67], a[192:195], v[16:19], a[64:67]\n"
"v_add_f32 v52, v52, v224\n"
"v_add_f32 v52, v52, v225\n"
"v_mfma_f32_16x16x32_bf16 a[68:71], a[196:199], v[16:19], a[68:71]\n"
"v_exp_f32 v229, v229\n"
"v_mfma_f32_16x16x32_bf16 a[72:75], a[200:203], v[16:19], a[72:75]\n"
"v_cvt_pk_bf16_f32 v20, v228, v229\n"
"v_add_f32 v52, v52, v226\n"
"v_mfma_f32_16x16x32_bf16 a[76:79], a[204:207], v[16:19], a[76:79]\n"
"v_exp_f32 v230, v230\n"
"v_mfma_f32_16x16x32_bf16 a[80:83], a[208:211], v[16:19], a[80:83]\n"
"v_add_f32 v52, v52, v227\n"
"v_mfma_f32_16x16x32_bf16 a[84:87], a[212:215], v[16:19], a[84:87]\n"
"v_exp_f32 v231, v231\n"
"v_mfma_f32_16x16x32_bf16 a[88:91], a[216:219], v[16:19], a[88:91]\n"
"s_waitcnt vmcnt(25) lgkmcnt(0)\n"
"s_barrier\n"
"v_cvt_pk_bf16_f32 v21, v230, v231\n"
"v_mfma_f32_16x16x32_bf16 a[92:95], a[220:223], v[16:19], a[92:95]\n"
"v_exp_f32 v232, v232\n"
"v_mfma_f32_16x16x32_bf16 v[0:3], a[160:163], v[64:67], v[24:27]\n"
"ds_read_b128 a[128:131], v241 offset:0\n"
"v_mfma_f32_16x16x32_bf16 v[0:3], a[164:167], v[68:71], v[0:3]\n"
"v_exp_f32 v233, v233\n"
"v_mfma_f32_16x16x32_bf16 v[0:3], a[168:171], v[72:75], v[0:3]\n"
"ds_read_b128 a[132:135], v241 offset:256\n"
"v_cvt_pk_bf16_f32 v22, v232, v233\n"
"v_mfma_f32_16x16x32_bf16 v[0:3], a[172:175], v[76:79], v[0:3]\n"
"v_exp_f32 v234, v234\n"
"v_mfma_f32_16x16x32_bf16 v[4:7], a[176:179], v[64:67], v[24:27]\n"
"ds_read_b128 a[136:139], v241 offset:512\n"
"v_mfma_f32_16x16x32_bf16 v[4:7], a[180:183], v[68:71], v[4:7]\n"
"v_exp_f32 v235, v235\n"
"v_mfma_f32_16x16x32_bf16 v[4:7], a[184:187], v[72:75], v[4:7]\n"
"ds_read_b128 a[140:143], v241 offset:768\n"
"v_cvt_pk_bf16_f32 v23, v234, v235\n"
"v_mfma_f32_16x16x32_bf16 v[4:7], a[188:191], v[76:79], v[4:7]\n"
"s_add_u32 m0, s53, 36864\n"
"v_exp_f32 v0, v0\n"
"v_mfma_f32_16x16x32_bf16 a[96:99], a[192:195], v[20:23], a[96:99]\n"
"buffer_load_dwordx4 v210, s[24:27], s21 offen offset:0 lds\n"
"v_mfma_f32_16x16x32_bf16 a[100:103], a[196:199], v[20:23], a[100:103]\n"
"v_exp_f32 v1, v1\n"
"v_mfma_f32_16x16x32_bf16 a[104:107], a[200:203], v[20:23], a[104:107]\n"
"ds_read_b128 a[144:147], v241 offset:4096\n"
"buffer_load_dwordx4 v[152:155], v252, s[64:67], s49 offen\n"
"v_cvt_pk_bf16_f32 v16, v0, v1\n"
"v_mfma_f32_16x16x32_bf16 a[108:111], a[204:207], v[20:23], a[108:111]\n"
"v_exp_f32 v2, v2\n"
"v_mfma_f32_16x16x32_bf16 a[112:115], a[208:211], v[20:23], a[112:115]\n"
"ds_read_b128 a[148:151], v241 offset:4352\n"
"v_add_f32 v44, v44, v0\n"
"v_mfma_f32_16x16x32_bf16 a[116:119], a[212:215], v[20:23], a[116:119]\n"
"v_exp_f32 v3, v3\n"
"v_mfma_f32_16x16x32_bf16 a[120:123], a[216:219], v[20:23], a[120:123]\n"
"ds_read_b128 a[152:155], v241 offset:4608\n"
"v_cvt_pk_bf16_f32 v17, v2, v3\n"
"v_mfma_f32_16x16x32_bf16 a[124:127], a[220:223], v[20:23], a[124:127]\n"
"v_exp_f32 v4, v4\n"
"v_mfma_f32_16x16x32_bf16 v[56:59], v[40:43], v[20:23], v[56:59]\n"
"ds_read_b128 a[156:159], v241 offset:4864\n"
"v_add_f32 v44, v44, v1\n"
"v_mfma_f32_16x16x32_bf16 v[8:11], a[160:163], v[80:83], v[28:31]\n"
"v_exp_f32 v5, v5\n"
"v_mfma_f32_16x16x32_bf16 v[8:11], a[164:167], v[84:87], v[8:11]\n"
"ds_read_b64_tr_b16 a[192:193], v244 offset:16384\n"
"v_cvt_pk_bf16_f32 v18, v4, v5\n"
"v_mfma_f32_16x16x32_bf16 v[8:11], a[168:171], v[88:91], v[8:11]\n"
"v_exp_f32 v6, v6\n"
"v_mfma_f32_16x16x32_bf16 v[8:11], a[172:175], v[92:95], v[8:11]\n"
"ds_read_b64_tr_b16 a[194:195], v244 offset:20480\n"
"v_add_f32 v44, v44, v2\n"
"v_mfma_f32_16x16x32_bf16 v[12:15], a[176:179], v[80:83], v[28:31]\n"
"v_exp_f32 v7, v7\n"
"v_mfma_f32_16x16x32_bf16 v[12:15], a[180:183], v[84:87], v[12:15]\n"
"s_add_u32 m0, s53, 36864\n"
"ds_read_b64_tr_b16 a[196:197], v245 offset:16384\n"
"v_cvt_pk_bf16_f32 v19, v6, v7\n"
"v_mfma_f32_16x16x32_bf16 v[12:15], a[184:187], v[88:91], v[12:15]\n"
"buffer_load_dwordx4 v211, s[24:27], s21 offen offset:1024 lds\n"
"v_mfma_f32_16x16x32_bf16 v[12:15], a[188:191], v[92:95], v[12:15]\n"
"s_waitcnt lgkmcnt(11)\n"
"v_exp_f32 v8, v8\n"
"v_mfma_f32_16x16x32_bf16 a[0:3], a[224:227], v[16:19], a[0:3]\n"
"ds_read_b64_tr_b16 a[198:199], v245 offset:20480\n"
"v_add_f32 v44, v44, v3\n"
"v_mfma_f32_16x16x32_bf16 a[4:7], a[228:231], v[16:19], a[4:7]\n"
"v_exp_f32 v9, v9\n"
"v_mfma_f32_16x16x32_bf16 a[8:11], a[232:235], v[16:19], a[8:11]\n"
"ds_read_b64_tr_b16 a[200:201], v244 offset:16640\n"
"v_cvt_pk_bf16_f32 v20, v8, v9\n"
"v_mfma_f32_16x16x32_bf16 a[12:15], a[236:239], v[16:19], a[12:15]\n"
"v_exp_f32 v10, v10\n"
"v_mfma_f32_16x16x32_bf16 a[16:19], a[240:243], v[16:19], a[16:19]\n"
"ds_read_b64_tr_b16 a[202:203], v244 offset:20736\n"
"v_add_f32 v44, v44, v4\n"
"v_mfma_f32_16x16x32_bf16 a[20:23], a[244:247], v[16:19], a[20:23]\n"
"v_exp_f32 v11, v11\n"
"v_mfma_f32_16x16x32_bf16 a[24:27], a[248:251], v[16:19], a[24:27]\n"
"ds_read_b64_tr_b16 a[204:205], v245 offset:16640\n"
"v_cvt_pk_bf16_f32 v21, v10, v11\n"
"v_mfma_f32_16x16x32_bf16 a[28:31], a[252:255], v[16:19], a[28:31]\n"
"v_exp_f32 v12, v12\n"
"v_mfma_f32_16x16x32_bf16 v[220:223], a[160:163], v[96:99], v[32:35]\n"
"ds_read_b64_tr_b16 a[206:207], v245 offset:20736\n"
"v_add_f32 v44, v44, v5\n"
"v_mfma_f32_16x16x32_bf16 v[220:223], a[164:167], v[100:103], v[220:223]\n"
"v_exp_f32 v13, v13\n"
"v_mfma_f32_16x16x32_bf16 v[220:223], a[168:171], v[104:107], v[220:223]\n"
"ds_read_b64_tr_b16 a[208:209], v244 offset:16896\n"
"buffer_load_dwordx4 v[188:191], v252, s[68:71], s49 offen\n"
"v_cvt_pk_bf16_f32 v22, v12, v13\n"
"v_mfma_f32_16x16x32_bf16 v[220:223], a[172:175], v[108:111], v[220:223]\n"
"s_add_u32 s49, s49, 9216\n"
"v_exp_f32 v14, v14\n"
"v_mfma_f32_16x16x32_bf16 v[224:227], a[176:179], v[96:99], v[32:35]\n"
"ds_read_b64_tr_b16 a[210:211], v244 offset:20992\n"
"v_add_f32 v44, v44, v6\n"
"v_mfma_f32_16x16x32_bf16 v[224:227], a[180:183], v[100:103], v[224:227]\n"
"v_exp_f32 v15, v15\n"
"v_mfma_f32_16x16x32_bf16 v[224:227], a[184:187], v[104:107], v[224:227]\n"
"ds_read_b64_tr_b16 a[212:213], v245 offset:16896\n"
"v_cvt_pk_bf16_f32 v23, v14, v15\n"
"v_mfma_f32_16x16x32_bf16 v[224:227], a[188:191], v[108:111], v[224:227]\n"
"s_add_u32 m0, s53, 36864\n"
"v_exp_f32 v220, v220\n"
"v_mfma_f32_16x16x32_bf16 a[32:35], a[224:227], v[20:23], a[32:35]\n"
"buffer_load_dwordx4 v212, s[24:27], s21 offen offset:2048 lds\n"
"v_mfma_f32_16x16x32_bf16 a[36:39], a[228:231], v[20:23], a[36:39]\n"
"v_exp_f32 v221, v221\n"
"v_mfma_f32_16x16x32_bf16 a[40:43], a[232:235], v[20:23], a[40:43]\n"
"ds_read_b64_tr_b16 a[214:215], v245 offset:20992\n"
"v_cvt_pk_bf16_f32 v16, v220, v221\n"
"v_mfma_f32_16x16x32_bf16 a[44:47], a[236:239], v[20:23], a[44:47]\n"
"v_exp_f32 v222, v222\n"
"v_mfma_f32_16x16x32_bf16 a[48:51], a[240:243], v[20:23], a[48:51]\n"
"ds_read_b64_tr_b16 a[216:217], v244 offset:17152\n"
"v_add_f32 v44, v44, v7\n"
"v_mfma_f32_16x16x32_bf16 a[52:55], a[244:247], v[20:23], a[52:55]\n"
"v_exp_f32 v223, v223\n"
"v_mfma_f32_16x16x32_bf16 a[56:59], a[248:251], v[20:23], a[56:59]\n"
"ds_read_b64_tr_b16 a[218:219], v244 offset:21248\n"
"v_cvt_pk_bf16_f32 v17, v222, v223\n"
"v_mfma_f32_16x16x32_bf16 a[60:63], a[252:255], v[20:23], a[60:63]\n"
"v_exp_f32 v224, v224\n"
"v_mfma_f32_16x16x32_bf16 v[48:51], v[40:43], v[20:23], v[48:51]\n"
"ds_read_b64_tr_b16 a[220:221], v245 offset:17152\n"
"v_add_f32 v52, v52, v220\n"
"v_mfma_f32_16x16x32_bf16 v[228:231], a[160:163], v[112:115], v[36:39]\n"
"v_exp_f32 v225, v225\n"
"v_mfma_f32_16x16x32_bf16 v[228:231], a[164:167], v[116:119], v[228:231]\n"
"ds_read_b64_tr_b16 a[222:223], v245 offset:21248\n"
"v_cvt_pk_bf16_f32 v18, v224, v225\n"
"v_mfma_f32_16x16x32_bf16 v[228:231], a[168:171], v[120:123], v[228:231]\n"
"v_exp_f32 v226, v226\n"
"v_mfma_f32_16x16x32_bf16 v[228:231], a[172:175], v[124:127], v[228:231]\n"
"v_add_f32 v52, v52, v221\n"
"v_add_f32 v52, v52, v222\n"
"v_mfma_f32_16x16x32_bf16 v[232:235], a[176:179], v[112:115], v[36:39]\n"
"v_exp_f32 v227, v227\n"
"v_mfma_f32_16x16x32_bf16 v[232:235], a[180:183], v[116:119], v[232:235]\n"
"s_add_u32 m0, s53, 36864\n"
"v_cvt_pk_bf16_f32 v19, v226, v227\n"
"v_add_f32 v52, v52, v223\n"
"v_mfma_f32_16x16x32_bf16 v[232:235], a[184:187], v[120:123], v[232:235]\n"
"buffer_load_dwordx4 v213, s[24:27], s21 offen offset:3072 lds\n"
"v_mfma_f32_16x16x32_bf16 v[232:235], a[188:191], v[124:127], v[232:235]\n"
"v_exp_f32 v228, v228\n"
"v_mfma_f32_16x16x32_bf16 a[64:67], a[224:227], v[16:19], a[64:67]\n"
"v_add_f32 v52, v52, v224\n"
"v_add_f32 v52, v52, v225\n"
"v_mfma_f32_16x16x32_bf16 a[68:71], a[228:231], v[16:19], a[68:71]\n"
"v_exp_f32 v229, v229\n"
"v_mfma_f32_16x16x32_bf16 a[72:75], a[232:235], v[16:19], a[72:75]\n"
"buffer_load_dwordx4 v[156:159], v252, s[64:67], s49 offen\n"
"v_cvt_pk_bf16_f32 v20, v228, v229\n"
"v_add_f32 v52, v52, v226\n"
"v_mfma_f32_16x16x32_bf16 a[76:79], a[236:239], v[16:19], a[76:79]\n"
"v_exp_f32 v230, v230\n"
"v_mfma_f32_16x16x32_bf16 a[80:83], a[240:243], v[16:19], a[80:83]\n"
"v_add_f32 v52, v52, v227\n"
"v_mfma_f32_16x16x32_bf16 a[84:87], a[244:247], v[16:19], a[84:87]\n"
"v_exp_f32 v231, v231\n"
"v_mfma_f32_16x16x32_bf16 a[88:91], a[248:251], v[16:19], a[88:91]\n"
"s_waitcnt lgkmcnt(8)\n"
"v_cvt_pk_bf16_f32 v21, v230, v231\n"
"v_mfma_f32_16x16x32_bf16 a[92:95], a[252:255], v[16:19], a[92:95]\n"
"v_exp_f32 v232, v232\n"
"v_mfma_f32_16x16x32_bf16 v[0:3], a[128:131], v[64:67], v[24:27]\n"
"ds_read_b128 a[160:163], v241 offset:8192\n"
"v_mfma_f32_16x16x32_bf16 v[0:3], a[132:135], v[68:71], v[0:3]\n"
"v_exp_f32 v233, v233\n"
"v_mfma_f32_16x16x32_bf16 v[0:3], a[136:139], v[72:75], v[0:3]\n"
"ds_read_b128 a[164:167], v241 offset:8448\n"
"v_cvt_pk_bf16_f32 v22, v232, v233\n"
"v_mfma_f32_16x16x32_bf16 v[0:3], a[140:143], v[76:79], v[0:3]\n"
"v_exp_f32 v234, v234\n"
"v_mfma_f32_16x16x32_bf16 v[4:7], a[144:147], v[64:67], v[24:27]\n"
"ds_read_b128 a[168:171], v241 offset:8704\n"
"v_mfma_f32_16x16x32_bf16 v[4:7], a[148:151], v[68:71], v[4:7]\n"
"v_exp_f32 v235, v235\n"
"v_mfma_f32_16x16x32_bf16 v[4:7], a[152:155], v[72:75], v[4:7]\n"
"ds_read_b128 a[172:175], v241 offset:8960\n"
"v_cvt_pk_bf16_f32 v23, v234, v235\n"
"v_mfma_f32_16x16x32_bf16 v[4:7], a[156:159], v[76:79], v[4:7]\n"
"s_add_u32 m0, s53, 40960\n"
"v_exp_f32 v0, v0\n"
"v_mfma_f32_16x16x32_bf16 a[96:99], a[224:227], v[20:23], a[96:99]\n"
"buffer_load_dwordx4 v214, s[24:27], s21 offen offset:0 lds\n"
"v_mfma_f32_16x16x32_bf16 a[100:103], a[228:231], v[20:23], a[100:103]\n"
"v_exp_f32 v1, v1\n"
"v_mfma_f32_16x16x32_bf16 a[104:107], a[232:235], v[20:23], a[104:107]\n"
"ds_read_b128 a[176:179], v241 offset:12288\n"
"v_cvt_pk_bf16_f32 v16, v0, v1\n"
"v_mfma_f32_16x16x32_bf16 a[108:111], a[236:239], v[20:23], a[108:111]\n"
"v_exp_f32 v2, v2\n"
"v_mfma_f32_16x16x32_bf16 a[112:115], a[240:243], v[20:23], a[112:115]\n"
"ds_read_b128 a[180:183], v241 offset:12544\n"
"v_add_f32 v44, v44, v0\n"
"v_mfma_f32_16x16x32_bf16 a[116:119], a[244:247], v[20:23], a[116:119]\n"
"v_exp_f32 v3, v3\n"
"v_mfma_f32_16x16x32_bf16 a[120:123], a[248:251], v[20:23], a[120:123]\n"
"ds_read_b128 a[184:187], v241 offset:12800\n"
"v_cvt_pk_bf16_f32 v17, v2, v3\n"
"v_mfma_f32_16x16x32_bf16 a[124:127], a[252:255], v[20:23], a[124:127]\n"
"v_exp_f32 v4, v4\n"
"v_mfma_f32_16x16x32_bf16 v[56:59], v[40:43], v[20:23], v[56:59]\n"
"ds_read_b128 a[188:191], v241 offset:13056\n"
"v_add_f32 v44, v44, v1\n"
"v_mfma_f32_16x16x32_bf16 v[8:11], a[128:131], v[80:83], v[28:31]\n"
"v_exp_f32 v5, v5\n"
"v_mfma_f32_16x16x32_bf16 v[8:11], a[132:135], v[84:87], v[8:11]\n"
"ds_read_b64_tr_b16 a[224:225], v244 offset:24576\n"
"buffer_load_dwordx4 v[192:195], v252, s[68:71], s49 offen\n"
"v_cvt_pk_bf16_f32 v18, v4, v5\n"
"v_mfma_f32_16x16x32_bf16 v[8:11], a[136:139], v[88:91], v[8:11]\n"
"s_add_u32 s49, s49, 285696\n"
"v_exp_f32 v6, v6\n"
"v_mfma_f32_16x16x32_bf16 v[8:11], a[140:143], v[92:95], v[8:11]\n"
"ds_read_b64_tr_b16 a[226:227], v244 offset:28672\n"
"v_add_f32 v44, v44, v2\n"
"v_mfma_f32_16x16x32_bf16 v[12:15], a[144:147], v[80:83], v[28:31]\n"
"v_exp_f32 v7, v7\n"
"v_mfma_f32_16x16x32_bf16 v[12:15], a[148:151], v[84:87], v[12:15]\n"
"s_add_u32 m0, s53, 40960\n"
"ds_read_b64_tr_b16 a[228:229], v245 offset:24576\n"
"v_cvt_pk_bf16_f32 v19, v6, v7\n"
"v_mfma_f32_16x16x32_bf16 v[12:15], a[152:155], v[88:91], v[12:15]\n"
"buffer_load_dwordx4 v236, s[24:27], s21 offen offset:1024 lds\n"
"v_mfma_f32_16x16x32_bf16 v[12:15], a[156:159], v[92:95], v[12:15]\n"
"s_waitcnt lgkmcnt(11)\n"
"v_exp_f32 v8, v8\n"
"v_mfma_f32_16x16x32_bf16 a[0:3], a[192:195], v[16:19], a[0:3]\n"
"ds_read_b64_tr_b16 a[230:231], v245 offset:28672\n"
"v_add_f32 v44, v44, v3\n"
"v_mfma_f32_16x16x32_bf16 a[4:7], a[196:199], v[16:19], a[4:7]\n"
"v_exp_f32 v9, v9\n"
"v_mfma_f32_16x16x32_bf16 a[8:11], a[200:203], v[16:19], a[8:11]\n"
"ds_read_b64_tr_b16 a[232:233], v244 offset:24832\n"
"v_cvt_pk_bf16_f32 v20, v8, v9\n"
"v_mfma_f32_16x16x32_bf16 a[12:15], a[204:207], v[16:19], a[12:15]\n"
"v_exp_f32 v10, v10\n"
"v_mfma_f32_16x16x32_bf16 a[16:19], a[208:211], v[16:19], a[16:19]\n"
"ds_read_b64_tr_b16 a[234:235], v244 offset:28928\n"
"v_add_f32 v44, v44, v4\n"
"v_mfma_f32_16x16x32_bf16 a[20:23], a[212:215], v[16:19], a[20:23]\n"
"s_add_u32 m0, s53, 40960\n"
"v_exp_f32 v11, v11\n"
"v_mfma_f32_16x16x32_bf16 a[24:27], a[216:219], v[16:19], a[24:27]\n"
"buffer_load_dwordx4 v237, s[24:27], s21 offen offset:2048 lds\n"
"ds_read_b64_tr_b16 a[236:237], v245 offset:24832\n"
"v_cvt_pk_bf16_f32 v21, v10, v11\n"
"v_mfma_f32_16x16x32_bf16 a[28:31], a[220:223], v[16:19], a[28:31]\n"
"v_exp_f32 v12, v12\n"
"v_mfma_f32_16x16x32_bf16 v[220:223], a[128:131], v[96:99], v[32:35]\n"
"ds_read_b64_tr_b16 a[238:239], v245 offset:28928\n"
"v_add_f32 v44, v44, v5\n"
"v_mfma_f32_16x16x32_bf16 v[220:223], a[132:135], v[100:103], v[220:223]\n"
"v_exp_f32 v13, v13\n"
"v_mfma_f32_16x16x32_bf16 v[220:223], a[136:139], v[104:107], v[220:223]\n"
"ds_read_b64_tr_b16 a[240:241], v244 offset:25088\n"
"v_cvt_pk_bf16_f32 v22, v12, v13\n"
"v_mfma_f32_16x16x32_bf16 v[220:223], a[140:143], v[108:111], v[220:223]\n"
"v_exp_f32 v14, v14\n"
"v_mfma_f32_16x16x32_bf16 v[224:227], a[144:147], v[96:99], v[32:35]\n"
"ds_read_b64_tr_b16 a[242:243], v244 offset:29184\n"
"v_add_f32 v44, v44, v6\n"
"v_mfma_f32_16x16x32_bf16 v[224:227], a[148:151], v[100:103], v[224:227]\n"
"v_exp_f32 v15, v15\n"
"v_mfma_f32_16x16x32_bf16 v[224:227], a[152:155], v[104:107], v[224:227]\n"
"ds_read_b64_tr_b16 a[244:245], v245 offset:25088\n"
"v_cvt_pk_bf16_f32 v23, v14, v15\n"
"v_mfma_f32_16x16x32_bf16 v[224:227], a[156:159], v[108:111], v[224:227]\n"
"s_add_u32 m0, s53, 40960\n"
"v_exp_f32 v220, v220\n"
"v_mfma_f32_16x16x32_bf16 a[32:35], a[192:195], v[20:23], a[32:35]\n"
"buffer_load_dwordx4 v238, s[24:27], s21 offen offset:3072 lds\n"
"v_mfma_f32_16x16x32_bf16 a[36:39], a[196:199], v[20:23], a[36:39]\n"
"v_exp_f32 v221, v221\n"
"v_mfma_f32_16x16x32_bf16 a[40:43], a[200:203], v[20:23], a[40:43]\n"
"ds_read_b64_tr_b16 a[246:247], v245 offset:29184\n"
"buffer_load_dwordx4 v[160:163], v251, s[64:67], s49 offen\n"
"v_cvt_pk_bf16_f32 v16, v220, v221\n"
"v_mfma_f32_16x16x32_bf16 a[44:47], a[204:207], v[20:23], a[44:47]\n"
"v_exp_f32 v222, v222\n"
"v_mfma_f32_16x16x32_bf16 a[48:51], a[208:211], v[20:23], a[48:51]\n"
"ds_read_b64_tr_b16 a[248:249], v244 offset:25344\n"
"v_add_f32 v44, v44, v7\n"
"v_mfma_f32_16x16x32_bf16 a[52:55], a[212:215], v[20:23], a[52:55]\n"
"v_exp_f32 v223, v223\n"
"v_mfma_f32_16x16x32_bf16 a[56:59], a[216:219], v[20:23], a[56:59]\n"
"ds_read_b64_tr_b16 a[250:251], v244 offset:29440\n"
"v_cvt_pk_bf16_f32 v17, v222, v223\n"
"v_mfma_f32_16x16x32_bf16 a[60:63], a[220:223], v[20:23], a[60:63]\n"
"v_exp_f32 v224, v224\n"
"v_mfma_f32_16x16x32_bf16 v[48:51], v[40:43], v[20:23], v[48:51]\n"
"ds_read_b64_tr_b16 a[252:253], v245 offset:25344\n"
"v_add_f32 v52, v52, v220\n"
"v_mfma_f32_16x16x32_bf16 v[228:231], a[128:131], v[112:115], v[36:39]\n"
"v_exp_f32 v225, v225\n"
"v_mfma_f32_16x16x32_bf16 v[228:231], a[132:135], v[116:119], v[228:231]\n"
"ds_read_b64_tr_b16 a[254:255], v245 offset:29440\n"
"v_cvt_pk_bf16_f32 v18, v224, v225\n"
"v_mfma_f32_16x16x32_bf16 v[228:231], a[136:139], v[120:123], v[228:231]\n"
"v_exp_f32 v226, v226\n"
"v_mfma_f32_16x16x32_bf16 v[228:231], a[140:143], v[124:127], v[228:231]\n"
"v_add_f32 v52, v52, v221\n"
"v_add_f32 v52, v52, v222\n"
"v_mfma_f32_16x16x32_bf16 v[232:235], a[144:147], v[112:115], v[36:39]\n"
"v_exp_f32 v227, v227\n"
"v_mfma_f32_16x16x32_bf16 v[232:235], a[148:151], v[116:119], v[232:235]\n"
"s_add_u32 m0, s53, 45056\n"
"v_cvt_pk_bf16_f32 v19, v226, v227\n"
"v_add_f32 v52, v52, v223\n"
"v_mfma_f32_16x16x32_bf16 v[232:235], a[152:155], v[120:123], v[232:235]\n"
"buffer_load_dwordx4 v239, s[24:27], s21 offen offset:0 lds\n"
"v_mfma_f32_16x16x32_bf16 v[232:235], a[156:159], v[124:127], v[232:235]\n"
"v_exp_f32 v228, v228\n"
"v_mfma_f32_16x16x32_bf16 a[64:67], a[192:195], v[16:19], a[64:67]\n"
"v_add_f32 v52, v52, v224\n"
"v_add_f32 v52, v52, v225\n"
"v_mfma_f32_16x16x32_bf16 a[68:71], a[196:199], v[16:19], a[68:71]\n"
"v_exp_f32 v229, v229\n"
"v_mfma_f32_16x16x32_bf16 a[72:75], a[200:203], v[16:19], a[72:75]\n"
"v_cvt_pk_bf16_f32 v20, v228, v229\n"
"v_add_f32 v52, v52, v226\n"
"v_mfma_f32_16x16x32_bf16 a[76:79], a[204:207], v[16:19], a[76:79]\n"
"v_exp_f32 v230, v230\n"
"v_mfma_f32_16x16x32_bf16 a[80:83], a[208:211], v[16:19], a[80:83]\n"
"v_add_f32 v52, v52, v227\n"
"v_mfma_f32_16x16x32_bf16 a[84:87], a[212:215], v[16:19], a[84:87]\n"
"v_exp_f32 v231, v231\n"
"v_mfma_f32_16x16x32_bf16 a[88:91], a[216:219], v[16:19], a[88:91]\n"
"s_waitcnt vmcnt(26) lgkmcnt(0)\n"
"s_barrier\n"
"v_cvt_pk_bf16_f32 v21, v230, v231\n"
"v_mfma_f32_16x16x32_bf16 a[92:95], a[220:223], v[16:19], a[92:95]\n"
"v_exp_f32 v232, v232\n"
"v_mfma_f32_16x16x32_bf16 v[0:3], a[160:163], v[64:67], v[24:27]\n"
"ds_read_b128 a[128:131], v241 offset:32768\n"
"v_mfma_f32_16x16x32_bf16 v[0:3], a[164:167], v[68:71], v[0:3]\n"
"v_exp_f32 v233, v233\n"
"v_mfma_f32_16x16x32_bf16 v[0:3], a[168:171], v[72:75], v[0:3]\n"
"ds_read_b128 a[132:135], v241 offset:33024\n"
"buffer_load_dwordx4 v[196:199], v251, s[68:71], s49 offen\n"
"v_cvt_pk_bf16_f32 v22, v232, v233\n"
"v_mfma_f32_16x16x32_bf16 v[0:3], a[172:175], v[76:79], v[0:3]\n"
"s_lshl_b32 s62, s41, 6\n"
"v_exp_f32 v234, v234\n"
"v_mfma_f32_16x16x32_bf16 v[4:7], a[176:179], v[64:67], v[24:27]\n"
"ds_read_b128 a[136:139], v241 offset:33280\n"
"v_add_u32 v253, s62, v248\n"
"v_mfma_f32_16x16x32_bf16 v[4:7], a[180:183], v[68:71], v[4:7]\n"
"v_exp_f32 v235, v235\n"
"v_mfma_f32_16x16x32_bf16 v[4:7], a[184:187], v[72:75], v[4:7]\n"
"ds_read_b128 a[140:143], v241 offset:33536\n"
"v_cvt_pk_bf16_f32 v23, v234, v235\n"
"v_mfma_f32_16x16x32_bf16 v[4:7], a[188:191], v[76:79], v[4:7]\n"
"s_add_u32 m0, s53, 73728\n"
"v_exp_f32 v0, v0\n"
"v_mfma_f32_16x16x32_bf16 a[96:99], a[192:195], v[20:23], a[96:99]\n"
"buffer_load_dwordx4 v210, s[24:27], s22 offen offset:0 lds\n"
"v_mov_b32 v255, 0x10000000\n"
"v_mfma_f32_16x16x32_bf16 a[100:103], a[196:199], v[20:23], a[100:103]\n"
"v_exp_f32 v1, v1\n"
"v_mfma_f32_16x16x32_bf16 a[104:107], a[200:203], v[20:23], a[104:107]\n"
"ds_read_b128 a[144:147], v241 offset:36864\n"
"v_cvt_pk_bf16_f32 v16, v0, v1\n"
"v_mfma_f32_16x16x32_bf16 a[108:111], a[204:207], v[20:23], a[108:111]\n"
"v_exp_f32 v2, v2\n"
"v_mfma_f32_16x16x32_bf16 a[112:115], a[208:211], v[20:23], a[112:115]\n"
"ds_read_b128 a[148:151], v241 offset:37120\n"
"v_add_f32 v44, v44, v0\n"
"v_mfma_f32_16x16x32_bf16 a[116:119], a[212:215], v[20:23], a[116:119]\n"
"v_exp_f32 v3, v3\n"
"v_mfma_f32_16x16x32_bf16 a[120:123], a[216:219], v[20:23], a[120:123]\n"
"ds_read_b128 a[152:155], v241 offset:37376\n"
"v_cvt_pk_bf16_f32 v17, v2, v3\n"
"v_mfma_f32_16x16x32_bf16 a[124:127], a[220:223], v[20:23], a[124:127]\n"
"v_exp_f32 v4, v4\n"
"v_mfma_f32_16x16x32_bf16 v[56:59], v[40:43], v[20:23], v[56:59]\n"
"ds_read_b128 a[156:159], v241 offset:37632\n"
"v_add_f32 v44, v44, v1\n"
"v_mfma_f32_16x16x32_bf16 v[8:11], a[160:163], v[80:83], v[28:31]\n"
"v_exp_f32 v5, v5\n"
"v_mfma_f32_16x16x32_bf16 v[8:11], a[164:167], v[84:87], v[8:11]\n"
"ds_read_b64_tr_b16 a[192:193], v244 offset:49152\n"
"v_cvt_pk_bf16_f32 v18, v4, v5\n"
"v_mfma_f32_16x16x32_bf16 v[8:11], a[168:171], v[88:91], v[8:11]\n"
"v_exp_f32 v6, v6\n"
"v_mfma_f32_16x16x32_bf16 v[8:11], a[172:175], v[92:95], v[8:11]\n"
"ds_read_b64_tr_b16 a[194:195], v244 offset:53248\n"
"v_add_f32 v44, v44, v2\n"
"v_mfma_f32_16x16x32_bf16 v[12:15], a[176:179], v[80:83], v[28:31]\n"
"v_exp_f32 v7, v7\n"
"v_mfma_f32_16x16x32_bf16 v[12:15], a[180:183], v[84:87], v[12:15]\n"
"s_add_u32 m0, s53, 73728\n"
"ds_read_b64_tr_b16 a[196:197], v245 offset:49152\n"
"v_cvt_pk_bf16_f32 v19, v6, v7\n"
"v_mfma_f32_16x16x32_bf16 v[12:15], a[184:187], v[88:91], v[12:15]\n"
"buffer_load_dwordx4 v211, s[24:27], s22 offen offset:1024 lds\n"
"v_cmp_gt_u32 vcc, 144, v253\n"
"v_mfma_f32_16x16x32_bf16 v[12:15], a[188:191], v[92:95], v[12:15]\n"
"s_waitcnt lgkmcnt(11)\n"
"v_exp_f32 v8, v8\n"
"v_mfma_f32_16x16x32_bf16 a[0:3], a[224:227], v[16:19], a[0:3]\n"
"ds_read_b64_tr_b16 a[198:199], v245 offset:53248\n"
"v_add_f32 v44, v44, v3\n"
"v_mfma_f32_16x16x32_bf16 a[4:7], a[228:231], v[16:19], a[4:7]\n"
"v_exp_f32 v9, v9\n"
"v_mfma_f32_16x16x32_bf16 a[8:11], a[232:235], v[16:19], a[8:11]\n"
"ds_read_b64_tr_b16 a[200:201], v244 offset:49408\n"
"v_cvt_pk_bf16_f32 v20, v8, v9\n"
"v_mfma_f32_16x16x32_bf16 a[12:15], a[236:239], v[16:19], a[12:15]\n"
"v_exp_f32 v10, v10\n"
"v_mfma_f32_16x16x32_bf16 a[16:19], a[240:243], v[16:19], a[16:19]\n"
"ds_read_b64_tr_b16 a[202:203], v244 offset:53504\n"
"v_add_f32 v44, v44, v4\n"
"v_mfma_f32_16x16x32_bf16 a[20:23], a[244:247], v[16:19], a[20:23]\n"
"v_exp_f32 v11, v11\n"
"v_mfma_f32_16x16x32_bf16 a[24:27], a[248:251], v[16:19], a[24:27]\n"
"ds_read_b64_tr_b16 a[204:205], v245 offset:49408\n"
"v_cvt_pk_bf16_f32 v21, v10, v11\n"
"v_mfma_f32_16x16x32_bf16 a[28:31], a[252:255], v[16:19], a[28:31]\n"
"v_exp_f32 v12, v12\n"
"v_mfma_f32_16x16x32_bf16 v[220:223], a[160:163], v[96:99], v[32:35]\n"
"ds_read_b64_tr_b16 a[206:207], v245 offset:53504\n"
"v_add_f32 v44, v44, v5\n"
"v_mfma_f32_16x16x32_bf16 v[220:223], a[164:167], v[100:103], v[220:223]\n"
"v_exp_f32 v13, v13\n"
"v_mfma_f32_16x16x32_bf16 v[220:223], a[168:171], v[104:107], v[220:223]\n"
"ds_read_b64_tr_b16 a[208:209], v244 offset:49664\n"
"v_cvt_pk_bf16_f32 v22, v12, v13\n"
"v_mfma_f32_16x16x32_bf16 v[220:223], a[172:175], v[108:111], v[220:223]\n"
"v_exp_f32 v14, v14\n"
"v_mfma_f32_16x16x32_bf16 v[224:227], a[176:179], v[96:99], v[32:35]\n"
"ds_read_b64_tr_b16 a[210:211], v244 offset:53760\n"
"v_add_f32 v44, v44, v6\n"
"v_mfma_f32_16x16x32_bf16 v[224:227], a[180:183], v[100:103], v[224:227]\n"
"v_exp_f32 v15, v15\n"
"v_mfma_f32_16x16x32_bf16 v[224:227], a[184:187], v[104:107], v[224:227]\n"
"ds_read_b64_tr_b16 a[212:213], v245 offset:49664\n"
"v_cvt_pk_bf16_f32 v23, v14, v15\n"
"v_mfma_f32_16x16x32_bf16 v[224:227], a[188:191], v[108:111], v[224:227]\n"
"s_add_u32 m0, s53, 73728\n"
"v_exp_f32 v220, v220\n"
"v_mfma_f32_16x16x32_bf16 a[32:35], a[224:227], v[20:23], a[32:35]\n"
"buffer_load_dwordx4 v212, s[24:27], s22 offen offset:2048 lds\n"
"v_lshlrev_b32 v254, 3, v253\n"
"v_mfma_f32_16x16x32_bf16 a[36:39], a[228:231], v[20:23], a[36:39]\n"
"s_nop 0\n"
"v_exp_f32 v221, v221\n"
"v_mfma_f32_16x16x32_bf16 a[40:43], a[232:235], v[20:23], a[40:43]\n"
"ds_read_b64_tr_b16 a[214:215], v245 offset:53760\n"
"v_cvt_pk_bf16_f32 v16, v220, v221\n"
"v_mfma_f32_16x16x32_bf16 a[44:47], a[236:239], v[20:23], a[44:47]\n"
"v_exp_f32 v222, v222\n"
"v_mfma_f32_16x16x32_bf16 a[48:51], a[240:243], v[20:23], a[48:51]\n"
"ds_read_b64_tr_b16 a[216:217], v244 offset:49920\n"
"v_add_f32 v44, v44, v7\n"
"v_mfma_f32_16x16x32_bf16 a[52:55], a[244:247], v[20:23], a[52:55]\n"
"v_exp_f32 v223, v223\n"
"v_mfma_f32_16x16x32_bf16 a[56:59], a[248:251], v[20:23], a[56:59]\n"
"ds_read_b64_tr_b16 a[218:219], v244 offset:54016\n"
"v_cvt_pk_bf16_f32 v17, v222, v223\n"
"v_mfma_f32_16x16x32_bf16 a[60:63], a[252:255], v[20:23], a[60:63]\n"
"v_exp_f32 v224, v224\n"
"v_mfma_f32_16x16x32_bf16 v[48:51], v[40:43], v[20:23], v[48:51]\n"
"ds_read_b64_tr_b16 a[220:221], v245 offset:49920\n"
"v_add_f32 v52, v52, v220\n"
"v_mfma_f32_16x16x32_bf16 v[228:231], a[160:163], v[112:115], v[36:39]\n"
"v_exp_f32 v225, v225\n"
"v_mfma_f32_16x16x32_bf16 v[228:231], a[164:167], v[116:119], v[228:231]\n"
"ds_read_b64_tr_b16 a[222:223], v245 offset:54016\n"
"v_cvt_pk_bf16_f32 v18, v224, v225\n"
"v_mfma_f32_16x16x32_bf16 v[228:231], a[168:171], v[120:123], v[228:231]\n"
"v_exp_f32 v226, v226\n"
"v_mfma_f32_16x16x32_bf16 v[228:231], a[172:175], v[124:127], v[228:231]\n"
"v_add_f32 v52, v52, v221\n"
"v_add_f32 v52, v52, v222\n"
"v_mfma_f32_16x16x32_bf16 v[232:235], a[176:179], v[112:115], v[36:39]\n"
"v_exp_f32 v227, v227\n"
"v_mfma_f32_16x16x32_bf16 v[232:235], a[180:183], v[116:119], v[232:235]\n"
"s_add_u32 m0, s53, 73728\n"
"v_cvt_pk_bf16_f32 v19, v226, v227\n"
"v_add_f32 v52, v52, v223\n"
"v_mfma_f32_16x16x32_bf16 v[232:235], a[184:187], v[120:123], v[232:235]\n"
"buffer_load_dwordx4 v213, s[24:27], s22 offen offset:3072 lds\n"
"v_cndmask_b32 v254, v255, v254, vcc\n"
"v_mfma_f32_16x16x32_bf16 v[232:235], a[188:191], v[124:127], v[232:235]\n"
"v_exp_f32 v228, v228\n"
"v_mfma_f32_16x16x32_bf16 a[64:67], a[224:227], v[16:19], a[64:67]\n"
"v_add_f32 v52, v52, v224\n"
"v_add_f32 v52, v52, v225\n"
"v_mfma_f32_16x16x32_bf16 a[68:71], a[228:231], v[16:19], a[68:71]\n"
"v_exp_f32 v229, v229\n"
"v_mfma_f32_16x16x32_bf16 a[72:75], a[232:235], v[16:19], a[72:75]\n"
"v_cvt_pk_bf16_f32 v20, v228, v229\n"
"v_add_f32 v52, v52, v226\n"
"v_mfma_f32_16x16x32_bf16 a[76:79], a[236:239], v[16:19], a[76:79]\n"
"v_exp_f32 v230, v230\n"
"v_mfma_f32_16x16x32_bf16 a[80:83], a[240:243], v[16:19], a[80:83]\n"
"v_add_f32 v52, v52, v227\n"
"v_lshlrev_b32 v216, 6, v248\n"
"v_mfma_f32_16x16x32_bf16 a[84:87], a[244:247], v[16:19], a[84:87]\n"
"v_exp_f32 v231, v231\n"
"v_mfma_f32_16x16x32_bf16 a[88:91], a[248:251], v[16:19], a[88:91]\n"
"s_waitcnt lgkmcnt(8)\n"
"v_cvt_pk_bf16_f32 v21, v230, v231\n"
"v_lshrrev_b32 v215, 3, v248\n"
"v_mfma_f32_16x16x32_bf16 a[92:95], a[252:255], v[16:19], a[92:95]\n"
"v_exp_f32 v232, v232\n"
"v_mfma_f32_16x16x32_bf16 v[0:3], a[128:131], v[64:67], v[24:27]\n"
"ds_read_b128 a[160:163], v241 offset:40960\n"
"v_mul_u32_u24 v215, 1152, v215\n"
"v_mfma_f32_16x16x32_bf16 v[0:3], a[132:135], v[68:71], v[0:3]\n"
"v_exp_f32 v233, v233\n"
"v_mfma_f32_16x16x32_bf16 v[0:3], a[136:139], v[72:75], v[0:3]\n"
"ds_read_b128 a[164:167], v241 offset:41216\n"
"v_cvt_pk_bf16_f32 v22, v232, v233\n"
"v_mfma_f32_16x16x32_bf16 v[0:3], a[140:143], v[76:79], v[0:3]\n"
"v_exp_f32 v234, v234\n"
"v_mfma_f32_16x16x32_bf16 v[4:7], a[144:147], v[64:67], v[24:27]\n"
"ds_read_b128 a[168:171], v241 offset:41472\n"
"v_and_b32 v217, 7, v248\n"
"v_mfma_f32_16x16x32_bf16 v[4:7], a[148:151], v[68:71], v[4:7]\n"
"v_exp_f32 v235, v235\n"
"v_mfma_f32_16x16x32_bf16 v[4:7], a[152:155], v[72:75], v[4:7]\n"
"ds_read_b128 a[172:175], v241 offset:41728\n"
"v_cvt_pk_bf16_f32 v23, v234, v235\n"
"v_mfma_f32_16x16x32_bf16 v[4:7], a[156:159], v[76:79], v[4:7]\n"
"s_add_u32 m0, s53, 77824\n"
"v_exp_f32 v0, v0\n"
"v_mfma_f32_16x16x32_bf16 a[96:99], a[224:227], v[20:23], a[96:99]\n"
"buffer_load_dwordx4 v214, s[24:27], s22 offen offset:0 lds\n"
"v_lshl_add_u32 v215, v217, 7, v215\n"
"v_mfma_f32_16x16x32_bf16 a[100:103], a[228:231], v[20:23], a[100:103]\n"
"s_and_b32 s46, s44, 7\n"
"v_exp_f32 v1, v1\n"
"v_mfma_f32_16x16x32_bf16 a[104:107], a[232:235], v[20:23], a[104:107]\n"
"s_lshl_b32 s46, s46, 2\n"
"ds_read_b128 a[176:179], v241 offset:45056\n"
"v_cvt_pk_bf16_f32 v16, v0, v1\n"
"v_mfma_f32_16x16x32_bf16 a[108:111], a[236:239], v[20:23], a[108:111]\n"
"s_add_u32 s46, s46, s41\n"
"v_exp_f32 v2, v2\n"
"v_mfma_f32_16x16x32_bf16 a[112:115], a[240:243], v[20:23], a[112:115]\n"
"s_mul_i32 s46, s46, 9216\n"
"ds_read_b128 a[180:183], v241 offset:45312\n"
"v_add_f32 v44, v44, v0\n"
"v_mfma_f32_16x16x32_bf16 a[116:119], a[244:247], v[20:23], a[116:119]\n"
"s_mul_i32 s62, s43, 294912\n"
"v_exp_f32 v3, v3\n"
"v_mfma_f32_16x16x32_bf16 a[120:123], a[248:251], v[20:23], a[120:123]\n"
"s_add_u32 s46, s46, s62\n"
"ds_read_b128 a[184:187], v241 offset:45568\n"
"v_cvt_pk_bf16_f32 v17, v2, v3\n"
"v_mfma_f32_16x16x32_bf16 a[124:127], a[252:255], v[20:23], a[124:127]\n"
"v_exp_f32 v4, v4\n"
"v_mfma_f32_16x16x32_bf16 v[56:59], v[40:43], v[20:23], v[56:59]\n"
"ds_read_b128 a[188:191], v241 offset:45824\n"
"buffer_load_dword v250, v215, s[24:27], s46 offen offset:128\n"
"v_add_f32 v44, v44, v1\n"
"v_mfma_f32_16x16x32_bf16 v[8:11], a[128:131], v[80:83], v[28:31]\n"
"v_exp_f32 v5, v5\n"
"v_mfma_f32_16x16x32_bf16 v[8:11], a[132:135], v[84:87], v[8:11]\n"
"ds_read_b64_tr_b16 a[224:225], v244 offset:57344\n"
"v_cvt_pk_bf16_f32 v18, v4, v5\n"
"v_mfma_f32_16x16x32_bf16 v[8:11], a[136:139], v[88:91], v[8:11]\n"
"v_exp_f32 v6, v6\n"
"v_mfma_f32_16x16x32_bf16 v[8:11], a[140:143], v[92:95], v[8:11]\n"
"ds_read_b64_tr_b16 a[226:227], v244 offset:61440\n"
"v_add_f32 v44, v44, v2\n"
"v_mfma_f32_16x16x32_bf16 v[12:15], a[144:147], v[80:83], v[28:31]\n"
"v_exp_f32 v7, v7\n"
"v_mfma_f32_16x16x32_bf16 v[12:15], a[148:151], v[84:87], v[12:15]\n"
"s_add_u32 m0, s53, 77824\n"
"ds_read_b64_tr_b16 a[228:229], v245 offset:57344\n"
"v_cvt_pk_bf16_f32 v19, v6, v7\n"
"v_mfma_f32_16x16x32_bf16 v[12:15], a[152:155], v[88:91], v[12:15]\n"
"buffer_load_dwordx4 v236, s[24:27], s22 offen offset:1024 lds\n"
"v_mfma_f32_16x16x32_bf16 v[12:15], a[156:159], v[92:95], v[12:15]\n"
"s_waitcnt lgkmcnt(11)\n"
"v_exp_f32 v8, v8\n"
"v_mfma_f32_16x16x32_bf16 a[0:3], a[192:195], v[16:19], a[0:3]\n"
"ds_read_b64_tr_b16 a[230:231], v245 offset:61440\n"
"v_add_f32 v44, v44, v3\n"
"v_mfma_f32_16x16x32_bf16 a[4:7], a[196:199], v[16:19], a[4:7]\n"
"v_exp_f32 v9, v9\n"
"v_mfma_f32_16x16x32_bf16 a[8:11], a[200:203], v[16:19], a[8:11]\n"
"ds_read_b64_tr_b16 a[232:233], v244 offset:57600\n"
"v_cvt_pk_bf16_f32 v20, v8, v9\n"
"v_mfma_f32_16x16x32_bf16 a[12:15], a[204:207], v[16:19], a[12:15]\n"
"v_exp_f32 v10, v10\n"
"v_mfma_f32_16x16x32_bf16 a[16:19], a[208:211], v[16:19], a[16:19]\n"
"ds_read_b64_tr_b16 a[234:235], v244 offset:61696\n"
"v_add_f32 v44, v44, v4\n"
"v_mfma_f32_16x16x32_bf16 a[20:23], a[212:215], v[16:19], a[20:23]\n"
"s_add_u32 m0, s53, 77824\n"
"v_exp_f32 v11, v11\n"
"v_mfma_f32_16x16x32_bf16 a[24:27], a[216:219], v[16:19], a[24:27]\n"
"buffer_load_dwordx4 v237, s[24:27], s22 offen offset:2048 lds\n"
"ds_read_b64_tr_b16 a[236:237], v245 offset:57600\n"
"v_cvt_pk_bf16_f32 v21, v10, v11\n"
"v_mfma_f32_16x16x32_bf16 a[28:31], a[220:223], v[16:19], a[28:31]\n"
"v_exp_f32 v12, v12\n"
"v_mfma_f32_16x16x32_bf16 v[220:223], a[128:131], v[96:99], v[32:35]\n"
"ds_read_b64_tr_b16 a[238:239], v245 offset:61696\n"
"v_add_f32 v44, v44, v5\n"
"v_mfma_f32_16x16x32_bf16 v[220:223], a[132:135], v[100:103], v[220:223]\n"
"v_exp_f32 v13, v13\n"
"v_mfma_f32_16x16x32_bf16 v[220:223], a[136:139], v[104:107], v[220:223]\n"
"ds_read_b64_tr_b16 a[240:241], v244 offset:57856\n"
"v_cvt_pk_bf16_f32 v22, v12, v13\n"
"v_mfma_f32_16x16x32_bf16 v[220:223], a[140:143], v[108:111], v[220:223]\n"
"v_exp_f32 v14, v14\n"
"v_mfma_f32_16x16x32_bf16 v[224:227], a[144:147], v[96:99], v[32:35]\n"
"ds_read_b64_tr_b16 a[242:243], v244 offset:61952\n"
"v_add_f32 v44, v44, v6\n"
"v_mfma_f32_16x16x32_bf16 v[224:227], a[148:151], v[100:103], v[224:227]\n"
"v_exp_f32 v15, v15\n"
"v_mfma_f32_16x16x32_bf16 v[224:227], a[152:155], v[104:107], v[224:227]\n"
"ds_read_b64_tr_b16 a[244:245], v245 offset:57856\n"
"v_cvt_pk_bf16_f32 v23, v14, v15\n"
"v_mfma_f32_16x16x32_bf16 v[224:227], a[156:159], v[108:111], v[224:227]\n"
"s_add_u32 m0, s53, 77824\n"
"v_exp_f32 v220, v220\n"
"v_mfma_f32_16x16x32_bf16 a[32:35], a[192:195], v[20:23], a[32:35]\n"
"buffer_load_dwordx4 v238, s[24:27], s22 offen offset:3072 lds\n"
"v_mfma_f32_16x16x32_bf16 a[36:39], a[196:199], v[20:23], a[36:39]\n"
"v_exp_f32 v221, v221\n"
"v_mfma_f32_16x16x32_bf16 a[40:43], a[200:203], v[20:23], a[40:43]\n"
"ds_read_b64_tr_b16 a[246:247], v245 offset:61952\n"
"buffer_load_dword v250, v215, s[24:27], s46 offen offset:192\n"
"v_cvt_pk_bf16_f32 v16, v220, v221\n"
"v_mfma_f32_16x16x32_bf16 a[44:47], a[204:207], v[20:23], a[44:47]\n"
"v_exp_f32 v222, v222\n"
"v_mfma_f32_16x16x32_bf16 a[48:51], a[208:211], v[20:23], a[48:51]\n"
"ds_read_b64_tr_b16 a[248:249], v244 offset:58112\n"
"v_add_f32 v44, v44, v7\n"
"v_mfma_f32_16x16x32_bf16 a[52:55], a[212:215], v[20:23], a[52:55]\n"
"v_exp_f32 v223, v223\n"
"v_mfma_f32_16x16x32_bf16 a[56:59], a[216:219], v[20:23], a[56:59]\n"
"ds_read_b64_tr_b16 a[250:251], v244 offset:62208\n"
"v_cvt_pk_bf16_f32 v17, v222, v223\n"
"v_mfma_f32_16x16x32_bf16 a[60:63], a[220:223], v[20:23], a[60:63]\n"
"v_exp_f32 v224, v224\n"
"v_mfma_f32_16x16x32_bf16 v[48:51], v[40:43], v[20:23], v[48:51]\n"
"ds_read_b64_tr_b16 a[252:253], v245 offset:58112\n"
"v_add_f32 v52, v52, v220\n"
"v_mfma_f32_16x16x32_bf16 v[228:231], a[128:131], v[112:115], v[36:39]\n"
"v_exp_f32 v225, v225\n"
"v_mfma_f32_16x16x32_bf16 v[228:231], a[132:135], v[116:119], v[228:231]\n"
"ds_read_b64_tr_b16 a[254:255], v245 offset:62208\n"
"v_cvt_pk_bf16_f32 v18, v224, v225\n"
"v_mfma_f32_16x16x32_bf16 v[228:231], a[136:139], v[120:123], v[228:231]\n"
"v_exp_f32 v226, v226\n"
"v_mfma_f32_16x16x32_bf16 v[228:231], a[140:143], v[124:127], v[228:231]\n"
"v_add_f32 v52, v52, v221\n"
"v_add_f32 v52, v52, v222\n"
"v_mfma_f32_16x16x32_bf16 v[232:235], a[144:147], v[112:115], v[36:39]\n"
"v_exp_f32 v227, v227\n"
"v_mfma_f32_16x16x32_bf16 v[232:235], a[148:151], v[116:119], v[232:235]\n"
"s_add_u32 m0, s53, 81920\n"
"v_cvt_pk_bf16_f32 v19, v226, v227\n"
"v_add_f32 v52, v52, v223\n"
"v_mfma_f32_16x16x32_bf16 v[232:235], a[152:155], v[120:123], v[232:235]\n"
"buffer_load_dwordx4 v239, s[24:27], s22 offen offset:0 lds\n"
"v_mfma_f32_16x16x32_bf16 v[232:235], a[156:159], v[124:127], v[232:235]\n"
"v_exp_f32 v228, v228\n"
"v_mfma_f32_16x16x32_bf16 a[64:67], a[192:195], v[16:19], a[64:67]\n"
"v_add_f32 v52, v52, v224\n"
"v_add_f32 v52, v52, v225\n"
"v_mfma_f32_16x16x32_bf16 a[68:71], a[196:199], v[16:19], a[68:71]\n"
"v_exp_f32 v229, v229\n"
"v_mfma_f32_16x16x32_bf16 a[72:75], a[200:203], v[16:19], a[72:75]\n"
"v_cvt_pk_bf16_f32 v20, v228, v229\n"
"v_add_f32 v52, v52, v226\n"
"v_mfma_f32_16x16x32_bf16 a[76:79], a[204:207], v[16:19], a[76:79]\n"
"v_exp_f32 v230, v230\n"
"v_mfma_f32_16x16x32_bf16 a[80:83], a[208:211], v[16:19], a[80:83]\n"
"v_add_f32 v52, v52, v227\n"
"v_mfma_f32_16x16x32_bf16 a[84:87], a[212:215], v[16:19], a[84:87]\n"
"v_exp_f32 v231, v231\n"
"v_mfma_f32_16x16x32_bf16 a[88:91], a[216:219], v[16:19], a[88:91]\n"
"s_waitcnt vmcnt(26) lgkmcnt(0)\n"
"s_barrier\n"
"v_cvt_pk_bf16_f32 v21, v230, v231\n"
"v_mfma_f32_16x16x32_bf16 a[92:95], a[220:223], v[16:19], a[92:95]\n"
"v_exp_f32 v232, v232\n"
"v_mfma_f32_16x16x32_bf16 v[0:3], a[160:163], v[64:67], v[24:27]\n"
"ds_read_b64_tr_b16 a[128:129], v202 offset:0\n"
"v_mfma_f32_16x16x32_bf16 v[0:3], a[164:167], v[68:71], v[0:3]\n"
"s_add_u32 m0, s53, 110592\n"
"v_exp_f32 v233, v233\n"
"v_mfma_f32_16x16x32_bf16 v[0:3], a[168:171], v[72:75], v[0:3]\n"
"buffer_load_dwordx4 v210, s[24:27], s23 offen offset:0 lds\n"
"ds_read_b64_tr_b16 a[130:131], v202 offset:4608\n"
"v_cvt_pk_bf16_f32 v22, v232, v233\n"
"v_mfma_f32_16x16x32_bf16 v[0:3], a[172:175], v[76:79], v[0:3]\n"
"v_exp_f32 v234, v234\n"
"v_mfma_f32_16x16x32_bf16 v[4:7], a[176:179], v[64:67], v[24:27]\n"
"ds_read_b64_tr_b16 a[132:133], v202 offset:32\n"
"v_mfma_f32_16x16x32_bf16 v[4:7], a[180:183], v[68:71], v[4:7]\n"
"v_exp_f32 v235, v235\n"
"v_mfma_f32_16x16x32_bf16 v[4:7], a[184:187], v[72:75], v[4:7]\n"
"ds_read_b64_tr_b16 a[134:135], v202 offset:4640\n"
"v_cvt_pk_bf16_f32 v23, v234, v235\n"
"v_mfma_f32_16x16x32_bf16 v[4:7], a[188:191], v[76:79], v[4:7]\n"
"s_add_u32 m0, s53, 110592\n"
"v_exp_f32 v0, v0\n"
"v_mfma_f32_16x16x32_bf16 a[96:99], a[192:195], v[20:23], a[96:99]\n"
"buffer_load_dwordx4 v211, s[24:27], s23 offen offset:1024 lds\n"
"v_mfma_f32_16x16x32_bf16 a[100:103], a[196:199], v[20:23], a[100:103]\n"
"v_exp_f32 v1, v1\n"
"v_mfma_f32_16x16x32_bf16 a[104:107], a[200:203], v[20:23], a[104:107]\n"
"ds_read_b64_tr_b16 a[136:137], v202 offset:64\n"
"v_cvt_pk_bf16_f32 v16, v0, v1\n"
"v_mfma_f32_16x16x32_bf16 a[108:111], a[204:207], v[20:23], a[108:111]\n"
"v_exp_f32 v2, v2\n"
"v_mfma_f32_16x16x32_bf16 a[112:115], a[208:211], v[20:23], a[112:115]\n"
"ds_read_b64_tr_b16 a[138:139], v202 offset:4672\n"
"v_add_f32 v44, v44, v0\n"
"v_mfma_f32_16x16x32_bf16 a[116:119], a[212:215], v[20:23], a[116:119]\n"
"v_exp_f32 v3, v3\n"
"v_mfma_f32_16x16x32_bf16 a[120:123], a[216:219], v[20:23], a[120:123]\n"
"ds_read_b64_tr_b16 a[140:141], v202 offset:96\n"
"v_cvt_pk_bf16_f32 v17, v2, v3\n"
"v_mfma_f32_16x16x32_bf16 a[124:127], a[220:223], v[20:23], a[124:127]\n"
"s_add_u32 m0, s53, 110592\n"
"v_exp_f32 v4, v4\n"
"v_mfma_f32_16x16x32_bf16 v[56:59], v[40:43], v[20:23], v[56:59]\n"
"buffer_load_dwordx4 v212, s[24:27], s23 offen offset:2048 lds\n"
"ds_read_b64_tr_b16 a[142:143], v202 offset:4704\n"
"v_add_f32 v44, v44, v1\n"
"v_mfma_f32_16x16x32_bf16 v[8:11], a[160:163], v[80:83], v[28:31]\n"
"v_exp_f32 v5, v5\n"
"v_mfma_f32_16x16x32_bf16 v[8:11], a[164:167], v[84:87], v[8:11]\n"
"ds_read_b64_tr_b16 a[144:145], v202 offset:128\n"
"v_cvt_pk_bf16_f32 v18, v4, v5\n"
"v_mfma_f32_16x16x32_bf16 v[8:11], a[168:171], v[88:91], v[8:11]\n"
"v_exp_f32 v6, v6\n"
"v_mfma_f32_16x16x32_bf16 v[8:11], a[172:175], v[92:95], v[8:11]\n"
"ds_read_b64_tr_b16 a[146:147], v202 offset:4736\n"
"v_add_f32 v44, v44, v2\n"
"v_mfma_f32_16x16x32_bf16 v[12:15], a[176:179], v[80:83], v[28:31]\n"
"v_exp_f32 v7, v7\n"
"v_mfma_f32_16x16x32_bf16 v[12:15], a[180:183], v[84:87], v[12:15]\n"
"s_add_u32 m0, s53, 110592\n"
"ds_read_b64_tr_b16 a[148:149], v202 offset:160\n"
"v_cvt_pk_bf16_f32 v19, v6, v7\n"
"v_mfma_f32_16x16x32_bf16 v[12:15], a[184:187], v[88:91], v[12:15]\n"
"buffer_load_dwordx4 v213, s[24:27], s23 offen offset:3072 lds\n"
"v_mfma_f32_16x16x32_bf16 v[12:15], a[188:191], v[92:95], v[12:15]\n"
"v_exp_f32 v8, v8\n"
"v_mfma_f32_16x16x32_bf16 a[0:3], a[224:227], v[16:19], a[0:3]\n"
"ds_read_b64_tr_b16 a[150:151], v202 offset:4768\n"
"v_add_f32 v44, v44, v3\n"
"v_mfma_f32_16x16x32_bf16 a[4:7], a[228:231], v[16:19], a[4:7]\n"
"v_exp_f32 v9, v9\n"
"v_mfma_f32_16x16x32_bf16 a[8:11], a[232:235], v[16:19], a[8:11]\n"
"ds_read_b64_tr_b16 a[152:153], v202 offset:192\n"
"v_cvt_pk_bf16_f32 v20, v8, v9\n"
"v_mfma_f32_16x16x32_bf16 a[12:15], a[236:239], v[16:19], a[12:15]\n"
"v_exp_f32 v10, v10\n"
"v_mfma_f32_16x16x32_bf16 a[16:19], a[240:243], v[16:19], a[16:19]\n"
"ds_read_b64_tr_b16 a[154:155], v202 offset:4800\n"
"v_add_f32 v44, v44, v4\n"
"v_mfma_f32_16x16x32_bf16 a[20:23], a[244:247], v[16:19], a[20:23]\n"
"s_add_u32 m0, s53, 114688\n"
"v_exp_f32 v11, v11\n"
"v_mfma_f32_16x16x32_bf16 a[24:27], a[248:251], v[16:19], a[24:27]\n"
"buffer_load_dwordx4 v214, s[24:27], s23 offen offset:0 lds\n"
"ds_read_b64_tr_b16 a[156:157], v202 offset:224\n"
"v_cvt_pk_bf16_f32 v21, v10, v11\n"
"v_mfma_f32_16x16x32_bf16 a[28:31], a[252:255], v[16:19], a[28:31]\n"
"v_exp_f32 v12, v12\n"
"v_mfma_f32_16x16x32_bf16 v[220:223], a[160:163], v[96:99], v[32:35]\n"
"ds_read_b64_tr_b16 a[158:159], v202 offset:4832\n"
"v_add_f32 v44, v44, v5\n"
"v_mfma_f32_16x16x32_bf16 v[220:223], a[164:167], v[100:103], v[220:223]\n"
"v_exp_f32 v13, v13\n"
"v_mfma_f32_16x16x32_bf16 v[220:223], a[168:171], v[104:107], v[220:223]\n"
"ds_read_b64_tr_b16 a[192:193], v205 offset:0\n"
"v_cvt_pk_bf16_f32 v22, v12, v13\n"
"v_mfma_f32_16x16x32_bf16 v[220:223], a[172:175], v[108:111], v[220:223]\n"
"v_exp_f32 v14, v14\n"
"v_mfma_f32_16x16x32_bf16 v[224:227], a[176:179], v[96:99], v[32:35]\n"
"ds_read_b64_tr_b16 a[194:195], v205 offset:4608\n"
"v_add_f32 v44, v44, v6\n"
"v_mfma_f32_16x16x32_bf16 v[224:227], a[180:183], v[100:103], v[224:227]\n"
"v_exp_f32 v15, v15\n"
"v_mfma_f32_16x16x32_bf16 v[224:227], a[184:187], v[104:107], v[224:227]\n"
"ds_read_b64_tr_b16 a[196:197], v202 offset:9216\n"
"v_cvt_pk_bf16_f32 v23, v14, v15\n"
"v_mfma_f32_16x16x32_bf16 v[224:227], a[188:191], v[108:111], v[224:227]\n"
"s_add_u32 m0, s53, 114688\n"
"v_exp_f32 v220, v220\n"
"v_mfma_f32_16x16x32_bf16 a[32:35], a[224:227], v[20:23], a[32:35]\n"
"buffer_load_dwordx4 v236, s[24:27], s23 offen offset:1024 lds\n"
"v_mfma_f32_16x16x32_bf16 a[36:39], a[228:231], v[20:23], a[36:39]\n"
"v_exp_f32 v221, v221\n"
"v_mfma_f32_16x16x32_bf16 a[40:43], a[232:235], v[20:23], a[40:43]\n"
"ds_read_b64_tr_b16 a[198:199], v202 offset:13824\n"
"v_cvt_pk_bf16_f32 v16, v220, v221\n"
"v_mfma_f32_16x16x32_bf16 a[44:47], a[236:239], v[20:23], a[44:47]\n"
"s_add_u32 m0, s53, 114688\n"
"v_exp_f32 v222, v222\n"
"v_mfma_f32_16x16x32_bf16 a[48:51], a[240:243], v[20:23], a[48:51]\n"
"buffer_load_dwordx4 v237, s[24:27], s23 offen offset:2048 lds\n"
"v_add_f32 v44, v44, v7\n"
"v_mfma_f32_16x16x32_bf16 a[52:55], a[244:247], v[20:23], a[52:55]\n"
"v_exp_f32 v223, v223\n"
"v_mfma_f32_16x16x32_bf16 a[56:59], a[248:251], v[20:23], a[56:59]\n"
"v_cvt_pk_bf16_f32 v17, v222, v223\n"
"v_mfma_f32_16x16x32_bf16 a[60:63], a[252:255], v[20:23], a[60:63]\n"
"s_add_u32 m0, s53, 114688\n"
"v_exp_f32 v224, v224\n"
"v_mfma_f32_16x16x32_bf16 v[48:51], v[40:43], v[20:23], v[48:51]\n"
"buffer_load_dwordx4 v238, s[24:27], s23 offen offset:3072 lds\n"
"v_add_f32 v52, v52, v220\n"
"v_mfma_f32_16x16x32_bf16 v[228:231], a[160:163], v[112:115], v[36:39]\n"
"v_exp_f32 v225, v225\n"
"v_mfma_f32_16x16x32_bf16 v[228:231], a[164:167], v[116:119], v[228:231]\n"
"v_cvt_pk_bf16_f32 v18, v224, v225\n"
"v_mfma_f32_16x16x32_bf16 v[228:231], a[168:171], v[120:123], v[228:231]\n"
"v_exp_f32 v226, v226\n"
"v_mfma_f32_16x16x32_bf16 v[228:231], a[172:175], v[124:127], v[228:231]\n"
"v_add_f32 v52, v52, v221\n"
"v_add_f32 v52, v52, v222\n"
"v_mfma_f32_16x16x32_bf16 v[232:235], a[176:179], v[112:115], v[36:39]\n"
"v_exp_f32 v227, v227\n"
"v_mfma_f32_16x16x32_bf16 v[232:235], a[180:183], v[116:119], v[232:235]\n"
"s_add_u32 m0, s53, 118784\n"
"v_cvt_pk_bf16_f32 v19, v226, v227\n"
"v_add_f32 v52, v52, v223\n"
"v_mfma_f32_16x16x32_bf16 v[232:235], a[184:187], v[120:123], v[232:235]\n"
"buffer_load_dwordx4 v239, s[24:27], s23 offen offset:0 lds\n"
"v_mfma_f32_16x16x32_bf16 v[232:235], a[188:191], v[124:127], v[232:235]\n"
"v_exp_f32 v228, v228\n"
"v_mfma_f32_16x16x32_bf16 a[64:67], a[224:227], v[16:19], a[64:67]\n"
"v_add_f32 v52, v52, v224\n"
"v_add_f32 v52, v52, v225\n"
"v_mfma_f32_16x16x32_bf16 a[68:71], a[228:231], v[16:19], a[68:71]\n"
"v_exp_f32 v229, v229\n"
"v_mfma_f32_16x16x32_bf16 a[72:75], a[232:235], v[16:19], a[72:75]\n"
"v_cvt_pk_bf16_f32 v20, v228, v229\n"
"v_add_f32 v52, v52, v226\n"
"v_mfma_f32_16x16x32_bf16 a[76:79], a[236:239], v[16:19], a[76:79]\n"
"v_exp_f32 v230, v230\n"
"v_mfma_f32_16x16x32_bf16 a[80:83], a[240:243], v[16:19], a[80:83]\n"
"v_add_f32 v52, v52, v227\n"
"v_mfma_f32_16x16x32_bf16 a[84:87], a[244:247], v[16:19], a[84:87]\n"
"v_exp_f32 v231, v231\n"
"v_mfma_f32_16x16x32_bf16 a[88:91], a[248:251], v[16:19], a[88:91]\n"
"v_cvt_pk_bf16_f32 v21, v230, v231\n"
"v_mfma_f32_16x16x32_bf16 a[92:95], a[252:255], v[16:19], a[92:95]\n"
"v_exp_f32 v232, v232\n"
"v_exp_f32 v233, v233\n"
"s_nop 0\n"
"v_cvt_pk_bf16_f32 v22, v232, v233\n"
"s_nop 0\n"
"v_exp_f32 v234, v234\n"
"s_nop 0\n"
"v_exp_f32 v235, v235\n"
"s_nop 0\n"
"v_cvt_pk_bf16_f32 v23, v234, v235\n"
"s_nop 0\n"
"s_nop 1\n"
"v_mfma_f32_16x16x32_bf16 a[96:99], a[224:227], v[20:23], a[96:99]\n"
"v_mfma_f32_16x16x32_bf16 a[100:103], a[228:231], v[20:23], a[100:103]\n"
"v_mfma_f32_16x16x32_bf16 a[104:107], a[232:235], v[20:23], a[104:107]\n"
"v_mfma_f32_16x16x32_bf16 a[108:111], a[236:239], v[20:23], a[108:111]\n"
"v_mfma_f32_16x16x32_bf16 a[112:115], a[240:243], v[20:23], a[112:115]\n"
"v_mfma_f32_16x16x32_bf16 a[116:119], a[244:247], v[20:23], a[116:119]\n"
"v_mfma_f32_16x16x32_bf16 a[120:123], a[248:251], v[20:23], a[120:123]\n"
"v_mfma_f32_16x16x32_bf16 a[124:127], a[252:255], v[20:23], a[124:127]\n"
"v_mfma_f32_16x16x32_bf16 v[56:59], v[40:43], v[20:23], v[56:59]\n"
"s_nop 7\n"
"v_mov_b32 v0, v44\n"
"v_mov_b32 v1, v52\n"
"s_nop 0\n"
"v_permlane16_swap_b32 v44, v0\n"
"v_permlane16_swap_b32 v52, v1\n"
"v_add_f32 v44, v44, v0\n"
"v_add_f32 v52, v52, v1\n"
"v_mov_b32 v0, v44\n"
"v_mov_b32 v1, v52\n"
"s_nop 0\n"
"v_permlane32_swap_b32 v44, v0\n"
"v_permlane32_swap_b32 v52, v1\n"
"v_add_f32 v44, v44, v0\n"
"v_add_f32 v52, v52, v1\n"
"v_mov_b32 v219, 0x5f800000\n"
"v_cmp_nle_f32 s[56:57], v44, v219\n"
"v_cmp_nle_f32 s[58:59], v48, v219\n"
"v_cmp_nle_f32 s[60:61], v52, v219\n"
"v_cmp_nle_f32 s[62:63], v56, v219\n"
"v_rcp_f32 v218, v44\n"
"v_rcp_f32 v220, v48\n"
"v_rcp_f32 v222, v52\n"
"v_rcp_f32 v224, v56\n"
"s_or_b64 s[56:57], s[56:57], s[58:59]\n"
"s_or_b64 s[60:61], s[60:61], s[62:63]\n"
"s_or_b64 s[56:57], s[56:57], s[60:61]\n"
"s_cselect_b32 s56, 1, 0\n"
"s_cmp_eq_u32 s45, 0\n"
"s_cselect_b32 s56, s56, 0\n"
"s_lshl_b32 s57, s41, 2\n"
"s_add_u32 s57, s57, s40\n"
"s_add_u32 s57, s57, 163776\n"
"v_mov_b32 v234, s56\n"
"v_mov_b32 v235, s57\n"
"ds_write_b32 v235, v234\n"
"s_and_b32 s60, s55, 4294967292\n"
"s_mov_b32 s23, 9576448\n"
"s_lshl_b32 s62, s60, 6\n"
"v_and_b32 v215, 3, v248\n"
"v_lshlrev_b32 v215, 6, v215\n"
"v_add_u32 v215, s62, v215\n"
"v_min_u32 v251, 1, v253\n"
"v_lshlrev_b32 v251, 28, v251\n"
"s_lshl_b32 s62, s55, 6\n"
"v_add_u32 v251, s62, v251\n"
"v_mov_b32 v252, s85\n"
"v_accvgpr_read_b32 v226, a0\n"
"v_accvgpr_read_b32 v227, a1\n"
"v_accvgpr_read_b32 v228, a2\n"
"v_accvgpr_read_b32 v229, a3\n"
"v_accvgpr_read_b32 v230, a4\n"
"v_accvgpr_read_b32 v231, a5\n"
"v_accvgpr_read_b32 v232, a6\n"
"v_accvgpr_read_b32 v233, a7\n"
"v_pk_mul_f32 v[226:227], v[226:227], v[218:219] op_sel_hi:[1,0]\n"
"v_pk_mul_f32 v[228:229], v[228:229], v[218:219] op_sel_hi:[1,0]\n"
"v_pk_mul_f32 v[230:231], v[230:231], v[218:219] op_sel_hi:[1,0]\n"
"v_pk_mul_f32 v[232:233], v[232:233], v[218:219] op_sel_hi:[1,0]\n"
"v_cvt_pk_bf16_f32 v0, v226, v227\n"
"v_cvt_pk_bf16_f32 v1, v228, v229\n"
"v_cvt_pk_bf16_f32 v2, v230, v231\n"
"v_cvt_pk_bf16_f32 v3, v232, v233\n"
"buffer_store_dwordx4 v[0:3], v207, s[28:31], s52 offen offset:0 sc1\n"
"v_accvgpr_read_b32 v226, a64\n"
"v_accvgpr_read_b32 v227, a65\n"
"v_accvgpr_read_b32 v228, a66\n"
"v_accvgpr_read_b32 v229, a67\n"
"v_accvgpr_read_b32 v230, a68\n"
"v_accvgpr_read_b32 v231, a69\n"
"v_accvgpr_read_b32 v232, a70\n"
"v_accvgpr_read_b32 v233, a71\n"
"v_pk_mul_f32 v[226:227], v[226:227], v[222:223] op_sel_hi:[1,0]\n"
"v_pk_mul_f32 v[228:229], v[228:229], v[222:223] op_sel_hi:[1,0]\n"
"v_pk_mul_f32 v[230:231], v[230:231], v[222:223] op_sel_hi:[1,0]\n"
"v_pk_mul_f32 v[232:233], v[232:233], v[222:223] op_sel_hi:[1,0]\n"
"v_cvt_pk_bf16_f32 v32, v226, v227\n"
"v_cvt_pk_bf16_f32 v33, v228, v229\n"
"v_cvt_pk_bf16_f32 v34, v230, v231\n"
"v_cvt_pk_bf16_f32 v35, v232, v233\n"
"buffer_store_dwordx4 v[32:35], v207, s[28:31], s52 offen offset:1024 sc1\n"
"v_accvgpr_read_b32 v226, a8\n"
"v_accvgpr_read_b32 v227, a9\n"
"v_accvgpr_read_b32 v228, a10\n"
"v_accvgpr_read_b32 v229, a11\n"
"v_accvgpr_read_b32 v230, a12\n"
"v_accvgpr_read_b32 v231, a13\n"
"v_accvgpr_read_b32 v232, a14\n"
"v_accvgpr_read_b32 v233, a15\n"
"v_pk_mul_f32 v[226:227], v[226:227], v[218:219] op_sel_hi:[1,0]\n"
"v_pk_mul_f32 v[228:229], v[228:229], v[218:219] op_sel_hi:[1,0]\n"
"v_pk_mul_f32 v[230:231], v[230:231], v[218:219] op_sel_hi:[1,0]\n"
"v_pk_mul_f32 v[232:233], v[232:233], v[218:219] op_sel_hi:[1,0]\n"
"v_cvt_pk_bf16_f32 v4, v226, v227\n"
"v_cvt_pk_bf16_f32 v5, v228, v229\n"
"v_cvt_pk_bf16_f32 v6, v230, v231\n"
"v_cvt_pk_bf16_f32 v7, v232, v233\n"
"buffer_store_dwordx4 v[4:7], v207, s[28:31], s52 offen offset:2048 sc1\n"
"v_accvgpr_read_b32 v226, a72\n"
"v_accvgpr_read_b32 v227, a73\n"
"v_accvgpr_read_b32 v228, a74\n"
"v_accvgpr_read_b32 v229, a75\n"
"v_accvgpr_read_b32 v230, a76\n"
"v_accvgpr_read_b32 v231, a77\n"
"v_accvgpr_read_b32 v232, a78\n"
"v_accvgpr_read_b32 v233, a79\n"
"v_pk_mul_f32 v[226:227], v[226:227], v[222:223] op_sel_hi:[1,0]\n"
"v_pk_mul_f32 v[228:229], v[228:229], v[222:223] op_sel_hi:[1,0]\n"
"v_pk_mul_f32 v[230:231], v[230:231], v[222:223] op_sel_hi:[1,0]\n"
"v_pk_mul_f32 v[232:233], v[232:233], v[222:223] op_sel_hi:[1,0]\n"
"v_cvt_pk_bf16_f32 v36, v226, v227\n"
"v_cvt_pk_bf16_f32 v37, v228, v229\n"
"v_cvt_pk_bf16_f32 v38, v230, v231\n"
"v_cvt_pk_bf16_f32 v39, v232, v233\n"
"buffer_store_dwordx4 v[36:39], v207, s[28:31], s52 offen offset:3072 sc1\n"
"s_add_u32 s52, s52, 4096\n"
"v_accvgpr_read_b32 v226, a16\n"
"v_accvgpr_read_b32 v227, a17\n"
"v_accvgpr_read_b32 v228, a18\n"
"v_accvgpr_read_b32 v229, a19\n"
"v_accvgpr_read_b32 v230, a20\n"
"v_accvgpr_read_b32 v231, a21\n"
"v_accvgpr_read_b32 v232, a22\n"
"v_accvgpr_read_b32 v233, a23\n"
"v_pk_mul_f32 v[226:227], v[226:227], v[218:219] op_sel_hi:[1,0]\n"
"v_pk_mul_f32 v[228:229], v[228:229], v[218:219] op_sel_hi:[1,0]\n"
"v_pk_mul_f32 v[230:231], v[230:231], v[218:219] op_sel_hi:[1,0]\n"
"v_pk_mul_f32 v[232:233], v[232:233], v[218:219] op_sel_hi:[1,0]\n"
"v_cvt_pk_bf16_f32 v8, v226, v227\n"
"v_cvt_pk_bf16_f32 v9, v228, v229\n"
"v_cvt_pk_bf16_f32 v10, v230, v231\n"
"v_cvt_pk_bf16_f32 v11, v232, v233\n"
"buffer_store_dwordx4 v[8:11], v207, s[28:31], s52 offen offset:0 sc1\n"
"v_accvgpr_read_b32 v226, a80\n"
"v_accvgpr_read_b32 v227, a81\n"
"v_accvgpr_read_b32 v228, a82\n"
"v_accvgpr_read_b32 v229, a83\n"
"v_accvgpr_read_b32 v230, a84\n"
"v_accvgpr_read_b32 v231, a85\n"
"v_accvgpr_read_b32 v232, a86\n"
"v_accvgpr_read_b32 v233, a87\n"
"v_pk_mul_f32 v[226:227], v[226:227], v[222:223] op_sel_hi:[1,0]\n"
"v_pk_mul_f32 v[228:229], v[228:229], v[222:223] op_sel_hi:[1,0]\n"
"v_pk_mul_f32 v[230:231], v[230:231], v[222:223] op_sel_hi:[1,0]\n"
"v_pk_mul_f32 v[232:233], v[232:233], v[222:223] op_sel_hi:[1,0]\n"
"v_cvt_pk_bf16_f32 v40, v226, v227\n"
"v_cvt_pk_bf16_f32 v41, v228, v229\n"
"v_cvt_pk_bf16_f32 v42, v230, v231\n"
"v_cvt_pk_bf16_f32 v43, v232, v233\n"
"buffer_store_dwordx4 v[40:43], v207, s[28:31], s52 offen offset:1024 sc1\n"
"v_accvgpr_read_b32 v226, a24\n"
"v_accvgpr_read_b32 v227, a25\n"
"v_accvgpr_read_b32 v228, a26\n"
"v_accvgpr_read_b32 v229, a27\n"
"v_accvgpr_read_b32 v230, a28\n"
"v_accvgpr_read_b32 v231, a29\n"
"v_accvgpr_read_b32 v232, a30\n"
"v_accvgpr_read_b32 v233, a31\n"
"v_pk_mul_f32 v[226:227], v[226:227], v[218:219] op_sel_hi:[1,0]\n"
"v_pk_mul_f32 v[228:229], v[228:229], v[218:219] op_sel_hi:[1,0]\n"
"v_pk_mul_f32 v[230:231], v[230:231], v[218:219] op_sel_hi:[1,0]\n"
"v_pk_mul_f32 v[232:233], v[232:233], v[218:219] op_sel_hi:[1,0]\n"
"v_cvt_pk_bf16_f32 v12, v226, v227\n"
"v_cvt_pk_bf16_f32 v13, v228, v229\n"
"v_cvt_pk_bf16_f32 v14, v230, v231\n"
"v_cvt_pk_bf16_f32 v15, v232, v233\n"
"buffer_store_dwordx4 v[12:15], v207, s[28:31], s52 offen offset:2048 sc1\n"
"v_accvgpr_read_b32 v226, a88\n"
"v_accvgpr_read_b32 v227, a89\n"
"v_accvgpr_read_b32 v228, a90\n"
"v_accvgpr_read_b32 v229, a91\n"
"v_accvgpr_read_b32 v230, a92\n"
"v_accvgpr_read_b32 v231, a93\n"
"v_accvgpr_read_b32 v232, a94\n"
"v_accvgpr_read_b32 v233, a95\n"
"v_pk_mul_f32 v[226:227], v[226:227], v[222:223] op_sel_hi:[1,0]\n"
"v_pk_mul_f32 v[228:229], v[228:229], v[222:223] op_sel_hi:[1,0]\n"
"v_pk_mul_f32 v[230:231], v[230:231], v[222:223] op_sel_hi:[1,0]\n"
"v_pk_mul_f32 v[232:233], v[232:233], v[222:223] op_sel_hi:[1,0]\n"
"v_cvt_pk_bf16_f32 v44, v226, v227\n"
"v_cvt_pk_bf16_f32 v45, v228, v229\n"
"v_cvt_pk_bf16_f32 v46, v230, v231\n"
"v_cvt_pk_bf16_f32 v47, v232, v233\n"
"buffer_store_dwordx4 v[44:47], v207, s[28:31], s52 offen offset:3072 sc1\n"
"s_add_u32 s52, s52, 4096\n"
"s_waitcnt vmcnt(29) lgkmcnt(0)\n"
"s_barrier\n"
"ds_read_b32 v234, v209\n"
"v_accvgpr_read_b32 v226, a32\n"
"v_accvgpr_read_b32 v227, a33\n"
"v_accvgpr_read_b32 v228, a34\n"
"v_accvgpr_read_b32 v229, a35\n"
"v_accvgpr_read_b32 v230, a36\n"
"v_accvgpr_read_b32 v231, a37\n"
"v_accvgpr_read_b32 v232, a38\n"
"v_accvgpr_read_b32 v233, a39\n"
"v_pk_mul_f32 v[226:227], v[226:227], v[220:221] op_sel_hi:[1,0]\n"
"v_pk_mul_f32 v[228:229], v[228:229], v[220:221] op_sel_hi:[1,0]\n"
"v_pk_mul_f32 v[230:231], v[230:231], v[220:221] op_sel_hi:[1,0]\n"
"v_pk_mul_f32 v[232:233], v[232:233], v[220:221] op_sel_hi:[1,0]\n"
"v_cvt_pk_bf16_f32 v16, v226, v227\n"
"v_cvt_pk_bf16_f32 v17, v228, v229\n"
"v_cvt_pk_bf16_f32 v18, v230, v231\n"
"v_cvt_pk_bf16_f32 v19, v232, v233\n"
"buffer_store_dwordx4 v[16:19], v207, s[28:31], s52 offen offset:0 sc1\n"
"s_add_u32 s61, s43, 1\n"
"s_and_b32 s61, s61, 3\n"
"s_add_u32 s61, s61, s60\n"
"s_lshl_b32 s61, s61, 2\n"
"s_add_u32 s61, s61, s41\n"
"s_lshl_b32 s61, s61, 14\n"
"s_add_u32 s20, s61, 9592832\n"
"v_accvgpr_read_b32 v226, a96\n"
"v_accvgpr_read_b32 v227, a97\n"
"v_accvgpr_read_b32 v228, a98\n"
"v_accvgpr_read_b32 v229, a99\n"
"v_accvgpr_read_b32 v230, a100\n"
"v_accvgpr_read_b32 v231, a101\n"
"v_accvgpr_read_b32 v232, a102\n"
"v_accvgpr_read_b32 v233, a103\n"
"v_pk_mul_f32 v[226:227], v[226:227], v[224:225] op_sel_hi:[1,0]\n"
"v_pk_mul_f32 v[228:229], v[228:229], v[224:225] op_sel_hi:[1,0]\n"
"v_pk_mul_f32 v[230:231], v[230:231], v[224:225] op_sel_hi:[1,0]\n"
"v_pk_mul_f32 v[232:233], v[232:233], v[224:225] op_sel_hi:[1,0]\n"
"v_cvt_pk_bf16_f32 v48, v226, v227\n"
"v_cvt_pk_bf16_f32 v49, v228, v229\n"
"v_cvt_pk_bf16_f32 v50, v230, v231\n"
"v_cvt_pk_bf16_f32 v51, v232, v233\n"
"buffer_store_dwordx4 v[48:51], v207, s[28:31], s52 offen offset:1024 sc1\n"
"s_add_u32 s61, s43, 2\n"
"s_and_b32 s61, s61, 3\n"
"s_add_u32 s61, s61, s60\n"
"s_lshl_b32 s61, s61, 2\n"
"s_add_u32 s61, s61, s41\n"
"s_lshl_b32 s61, s61, 14\n"
"s_add_u32 s21, s61, 9592832\n"
"v_accvgpr_read_b32 v226, a40\n"
"v_accvgpr_read_b32 v227, a41\n"
"v_accvgpr_read_b32 v228, a42\n"
"v_accvgpr_read_b32 v229, a43\n"
"v_accvgpr_read_b32 v230, a44\n"
"v_accvgpr_read_b32 v231, a45\n"
"v_accvgpr_read_b32 v232, a46\n"
"v_accvgpr_read_b32 v233, a47\n"
"v_pk_mul_f32 v[226:227], v[226:227], v[220:221] op_sel_hi:[1,0]\n"
"v_pk_mul_f32 v[228:229], v[228:229], v[220:221] op_sel_hi:[1,0]\n"
"v_pk_mul_f32 v[230:231], v[230:231], v[220:221] op_sel_hi:[1,0]\n"
"v_pk_mul_f32 v[232:233], v[232:233], v[220:221] op_sel_hi:[1,0]\n"
"v_cvt_pk_bf16_f32 v20, v226, v227\n"
"v_cvt_pk_bf16_f32 v21, v228, v229\n"
"v_cvt_pk_bf16_f32 v22, v230, v231\n"
"v_cvt_pk_bf16_f32 v23, v232, v233\n"
"buffer_store_dwordx4 v[20:23], v207, s[28:31], s52 offen offset:2048 sc1\n"
"s_add_u32 s61, s43, 3\n"
"s_and_b32 s61, s61, 3\n"
"s_add_u32 s61, s61, s60\n"
"s_lshl_b32 s61, s61, 2\n"
"s_add_u32 s61, s61, s41\n"
"s_lshl_b32 s61, s61, 14\n"
"s_add_u32 s22, s61, 9592832\n"
"v_accvgpr_read_b32 v226, a104\n"
"v_accvgpr_read_b32 v227, a105\n"
"v_accvgpr_read_b32 v228, a106\n"
"v_accvgpr_read_b32 v229, a107\n"
"v_accvgpr_read_b32 v230, a108\n"
"v_accvgpr_read_b32 v231, a109\n"
"v_accvgpr_read_b32 v232, a110\n"
"v_accvgpr_read_b32 v233, a111\n"
"v_pk_mul_f32 v[226:227], v[226:227], v[224:225] op_sel_hi:[1,0]\n"
"v_pk_mul_f32 v[228:229], v[228:229], v[224:225] op_sel_hi:[1,0]\n"
"v_pk_mul_f32 v[230:231], v[230:231], v[224:225] op_sel_hi:[1,0]\n"
"v_pk_mul_f32 v[232:233], v[232:233], v[224:225] op_sel_hi:[1,0]\n"
"v_cvt_pk_bf16_f32 v52, v226, v227\n"
"v_cvt_pk_bf16_f32 v53, v228, v229\n"
"v_cvt_pk_bf16_f32 v54, v230, v231\n"
"v_cvt_pk_bf16_f32 v55, v232, v233\n"
"buffer_store_dwordx4 v[52:55], v207, s[28:31], s52 offen offset:3072 sc1\n"
"s_add_u32 s52, s52, 4096\n"
"v_accvgpr_read_b32 v226, a48\n"
"v_accvgpr_read_b32 v227, a49\n"
"v_accvgpr_read_b32 v228, a50\n"
"v_accvgpr_read_b32 v229, a51\n"
"v_accvgpr_read_b32 v230, a52\n"
"v_accvgpr_read_b32 v231, a53\n"
"v_accvgpr_read_b32 v232, a54\n"
"v_accvgpr_read_b32 v233, a55\n"
"v_pk_mul_f32 v[226:227], v[226:227], v[220:221] op_sel_hi:[1,0]\n"
"v_pk_mul_f32 v[228:229], v[228:229], v[220:221] op_sel_hi:[1,0]\n"
"v_pk_mul_f32 v[230:231], v[230:231], v[220:221] op_sel_hi:[1,0]\n"
"v_pk_mul_f32 v[232:233], v[232:233], v[220:221] op_sel_hi:[1,0]\n"
"v_cvt_pk_bf16_f32 v24, v226, v227\n"
"v_cvt_pk_bf16_f32 v25, v228, v229\n"
"v_cvt_pk_bf16_f32 v26, v230, v231\n"
"v_cvt_pk_bf16_f32 v27, v232, v233\n"
"buffer_store_dwordx4 v[24:27], v207, s[28:31], s52 offen offset:0 sc1\n"
"v_accvgpr_read_b32 v226, a112\n"
"v_accvgpr_read_b32 v227, a113\n"
"v_accvgpr_read_b32 v228, a114\n"
"v_accvgpr_read_b32 v229, a115\n"
"v_accvgpr_read_b32 v230, a116\n"
"v_accvgpr_read_b32 v231, a117\n"
"v_accvgpr_read_b32 v232, a118\n"
"v_accvgpr_read_b32 v233, a119\n"
"v_pk_mul_f32 v[226:227], v[226:227], v[224:225] op_sel_hi:[1,0]\n"
"v_pk_mul_f32 v[228:229], v[228:229], v[224:225] op_sel_hi:[1,0]\n"
"v_pk_mul_f32 v[230:231], v[230:231], v[224:225] op_sel_hi:[1,0]\n"
"v_pk_mul_f32 v[232:233], v[232:233], v[224:225] op_sel_hi:[1,0]\n"
"v_cvt_pk_bf16_f32 v56, v226, v227\n"
"v_cvt_pk_bf16_f32 v57, v228, v229\n"
"v_cvt_pk_bf16_f32 v58, v230, v231\n"
"v_cvt_pk_bf16_f32 v59, v232, v233\n"
"buffer_store_dwordx4 v[56:59], v207, s[28:31], s52 offen offset:1024 sc1\n"
"v_accvgpr_read_b32 v226, a56\n"
"v_accvgpr_read_b32 v227, a57\n"
"v_accvgpr_read_b32 v228, a58\n"
"v_accvgpr_read_b32 v229, a59\n"
"v_accvgpr_read_b32 v230, a60\n"
"v_accvgpr_read_b32 v231, a61\n"
"v_accvgpr_read_b32 v232, a62\n"
"v_accvgpr_read_b32 v233, a63\n"
"v_pk_mul_f32 v[226:227], v[226:227], v[220:221] op_sel_hi:[1,0]\n"
"v_pk_mul_f32 v[228:229], v[228:229], v[220:221] op_sel_hi:[1,0]\n"
"v_pk_mul_f32 v[230:231], v[230:231], v[220:221] op_sel_hi:[1,0]\n"
"v_pk_mul_f32 v[232:233], v[232:233], v[220:221] op_sel_hi:[1,0]\n"
"v_cvt_pk_bf16_f32 v28, v226, v227\n"
"v_cvt_pk_bf16_f32 v29, v228, v229\n"
"v_cvt_pk_bf16_f32 v30, v230, v231\n"
"v_cvt_pk_bf16_f32 v31, v232, v233\n"
"buffer_store_dwordx4 v[28:31], v207, s[28:31], s52 offen offset:2048 sc1\n"
"v_accvgpr_read_b32 v226, a120\n"
"v_accvgpr_read_b32 v227, a121\n"
"v_accvgpr_read_b32 v228, a122\n"
"v_accvgpr_read_b32 v229, a123\n"
"v_accvgpr_read_b32 v230, a124\n"
"v_accvgpr_read_b32 v231, a125\n"
"v_accvgpr_read_b32 v232, a126\n"
"v_accvgpr_read_b32 v233, a127\n"
"v_pk_mul_f32 v[226:227], v[226:227], v[224:225] op_sel_hi:[1,0]\n"
"v_pk_mul_f32 v[228:229], v[228:229], v[224:225] op_sel_hi:[1,0]\n"
"v_pk_mul_f32 v[230:231], v[230:231], v[224:225] op_sel_hi:[1,0]\n"
"v_pk_mul_f32 v[232:233], v[232:233], v[224:225] op_sel_hi:[1,0]\n"
"v_cvt_pk_bf16_f32 v60, v226, v227\n"
"v_cvt_pk_bf16_f32 v61, v228, v229\n"
"v_cvt_pk_bf16_f32 v62, v230, v231\n"
"v_cvt_pk_bf16_f32 v63, v232, v233\n"
"buffer_store_dwordx4 v[60:63], v207, s[28:31], s52 offen offset:3072 sc1\n"
"s_add_u32 s52, s52, 4096\n"
"s_waitcnt lgkmcnt(0)\n"
"v_cmp_ne_u32 vcc, 0, v234\n"
"s_nop 0\n"
"s_cbranch_vccnz AT_fb\n"
"v_mfma_f32_16x16x32_bf16 v[64:67], a[128:131], v[0:3], 0\n"
"s_mov_b32 s56, 41984\n"
"ds_read_b64_tr_b16 a[200:201], v202 offset:9248\n"
"v_mfma_f32_16x16x32_bf16 v[68:71], a[128:131], v[32:35], 0\n"
"s_mov_b32 s57, 46592\n"
"ds_read_b64_tr_b16 a[202:203], v202 offset:13856\n"
"v_mfma_f32_16x16x32_bf16 v[72:75], a[132:135], v[0:3], 0\n"
"s_mov_b32 s58, 51200\n"
"ds_read_b64_tr_b16 a[204:205], v202 offset:9280\n"
"v_mfma_f32_16x16x32_bf16 v[76:79], a[132:135], v[32:35], 0\n"
"s_mov_b32 s59, 55808\n"
"ds_read_b64_tr_b16 a[206:207], v202 offset:13888\n"
"v_mfma_f32_16x16x32_bf16 v[80:83], a[136:139], v[0:3], 0\n"
"ds_read_b64_tr_b16 a[208:209], v202 offset:9312\n"
"v_lshlrev_b32 v246, 2, v254\n"
"v_mfma_f32_16x16x32_bf16 v[84:87], a[136:139], v[32:35], 0\n"
"s_lshl_b32 s54, s44, 2\n"
"ds_read_b64_tr_b16 a[210:211], v202 offset:13920\n"
"v_mfma_f32_16x16x32_bf16 v[88:91], a[140:143], v[0:3], 0\n"
"s_add_u32 s54, s54, s41\n"
"ds_read_b64_tr_b16 a[212:213], v202 offset:9344\n"
"v_mfma_f32_16x16x32_bf16 v[92:95], a[140:143], v[32:35], 0\n"
"s_mul_i32 s54, s54, 36864\n"
"ds_read_b64_tr_b16 a[214:215], v202 offset:13952\n"
"v_mfma_f32_16x16x32_bf16 v[96:99], a[144:147], v[0:3], 0\n"
"s_lshr_b32 s62, s43, 2\n"
"ds_read_b64_tr_b16 a[216:217], v202 offset:9376\n"
"v_mfma_f32_16x16x32_bf16 v[100:103], a[144:147], v[32:35], 0\n"
"s_mul_i32 s62, s62, 2359296\n"
"ds_read_b64_tr_b16 a[218:219], v202 offset:13984\n"
"v_mfma_f32_16x16x32_bf16 v[104:107], a[148:151], v[0:3], 0\n"
"s_add_u32 s54, s54, s62\n"
"ds_read_b64_tr_b16 a[220:221], v202 offset:9408\n"
"v_mfma_f32_16x16x32_bf16 v[108:111], a[148:151], v[32:35], 0\n"
"s_add_u32 s54, s54, 139264\n"
"ds_read_b64_tr_b16 a[222:223], v202 offset:14016\n"
"v_mfma_f32_16x16x32_bf16 v[112:115], a[152:155], v[0:3], 0\n"
"s_bfe_u32 s62, s43, 0x10001\n"
"ds_read_b64_tr_b16 a[128:129], v202 offset:9440\n"
"v_mfma_f32_16x16x32_bf16 v[116:119], a[152:155], v[32:35], 0\n"
"s_mul_i32 s62, s62, 9216\n"
"ds_read_b64_tr_b16 a[130:131], v202 offset:14048\n"
"v_mfma_f32_16x16x32_bf16 v[120:123], a[156:159], v[0:3], 0\n"
"s_add_u32 s54, s54, s62\n"
"ds_read_b64_tr_b16 a[132:133], v205 offset:9216\n"
"v_mfma_f32_16x16x32_bf16 v[124:127], a[156:159], v[32:35], 0\n"
"s_and_b32 s62, s43, 1\n"
"ds_read_b64_tr_b16 a[134:135], v205 offset:13824\n"
"v_mfma_f32_16x16x32_bf16 v[226:229], a[192:195], v[0:3], 0\n"
"s_lshl_b32 s49, s62, 3\n"
"ds_read_b64_tr_b16 a[136:137], v202 offset:18432\n"
"v_mfma_f32_16x16x32_bf16 v[230:233], a[192:195], v[32:35], 0\n"
"s_add_u32 s49, s49, s54\n"
"ds_read_b64_tr_b16 a[138:139], v202 offset:23040\n"
"v_mfma_f32_16x16x32_bf16 v[64:67], a[196:199], v[4:7], v[64:67]\n"
"s_add_u32 s49, s49, 4096\n"
"ds_read_b64_tr_b16 a[140:141], v202 offset:18464\n"
"v_mfma_f32_16x16x32_bf16 v[68:71], a[196:199], v[36:39], v[68:71]\n"
"s_waitcnt lgkmcnt(11)\n"
"s_mul_i32 s62, s62, 5120\n"
"ds_read_b64_tr_b16 a[142:143], v202 offset:23072\n"
"v_mfma_f32_16x16x32_bf16 v[72:75], a[200:203], v[4:7], v[72:75]\n"
"s_add_u32 s54, s54, s62\n"
"ds_read_b64_tr_b16 a[144:145], v202 offset:18496\n"
"v_mfma_f32_16x16x32_bf16 v[76:79], a[200:203], v[36:39], v[76:79]\n"
"s_mul_i32 s46, s55, 9216\n"
"ds_read_b64_tr_b16 a[146:147], v202 offset:23104\n"
"v_mfma_f32_16x16x32_bf16 v[80:83], a[204:207], v[4:7], v[80:83]\n"
"s_min_u32 s62, s41, 2\n"
"ds_read_b64_tr_b16 a[148:149], v202 offset:18528\n"
"v_mfma_f32_16x16x32_bf16 v[84:87], a[204:207], v[36:39], v[84:87]\n"
"s_mul_i32 s62, s62, 3072\n"
"ds_read_b64_tr_b16 a[150:151], v202 offset:23136\n"
"v_mfma_f32_16x16x32_bf16 v[88:91], a[208:211], v[4:7], v[88:91]\n"
"s_add_u32 s46, s46, s62\n"
"ds_read_b64_tr_b16 a[152:153], v202 offset:18560\n"
"v_mfma_f32_16x16x32_bf16 v[92:95], a[208:211], v[36:39], v[92:95]\n"
"s_add_u32 s45, s40, s62\n"
"ds_read_b64_tr_b16 a[154:155], v202 offset:23168\n"
"v_mfma_f32_16x16x32_bf16 v[96:99], a[212:215], v[4:7], v[96:99]\n"
"s_lshr_b32 s47, s55, 1\n"
"ds_read_b64_tr_b16 a[156:157], v202 offset:18592\n"
"v_mfma_f32_16x16x32_bf16 v[100:103], a[212:215], v[36:39], v[100:103]\n"
"s_waitcnt lgkmcnt(11)\n"
"s_mul_i32 s47, s47, 18432\n"
"ds_read_b64_tr_b16 a[158:159], v202 offset:23200\n"
"v_mfma_f32_16x16x32_bf16 v[104:107], a[216:219], v[4:7], v[104:107]\n"
"s_and_b32 s62, s55, 1\n"
"ds_read_b64_tr_b16 a[192:193], v202 offset:18624\n"
"v_mfma_f32_16x16x32_bf16 v[108:111], a[216:219], v[36:39], v[108:111]\n"
"s_lshl_b32 s62, s62, 7\n"
"ds_read_b64_tr_b16 a[194:195], v202 offset:23232\n"
"v_mfma_f32_16x16x32_bf16 v[112:115], a[220:223], v[4:7], v[112:115]\n"
"s_add_u32 s47, s47, s62\n"
"ds_read_b64_tr_b16 a[196:197], v202 offset:18656\n"
"v_mfma_f32_16x16x32_bf16 v[116:119], a[220:223], v[36:39], v[116:119]\n"
"s_add_u32 s47, s47, 139264\n"
"ds_read_b64_tr_b16 a[198:199], v202 offset:23264\n"
"v_mfma_f32_16x16x32_bf16 v[120:123], a[128:131], v[4:7], v[120:123]\n"
"s_mul_i32 s48, s55, 73728\n"
"ds_read_b64_tr_b16 a[200:201], v205 offset:18432\n"
"v_mfma_f32_16x16x32_bf16 v[124:127], a[128:131], v[36:39], v[124:127]\n"
"s_add_u32 s48, s48, 37888000\n"
"ds_read_b64_tr_b16 a[202:203], v205 offset:23040\n"
"v_mfma_f32_16x16x32_bf16 v[226:229], a[132:135], v[4:7], v[226:229]\n"
"s_add_u32 s62, s40, 119808\n"
"ds_read_b64_tr_b16 a[204:205], v200 offset:0\n"
"v_mfma_f32_16x16x32_bf16 v[230:233], a[132:135], v[36:39], v[230:233]\n"
"s_waitcnt lgkmcnt(11)\n"
"ds_read_b64_tr_b16 a[206:207], v200 offset:4608\n"
"v_and_b32 v206, 7, v248\n"
"v_mfma_f32_16x16x32_bf16 v[64:67], a[136:139], v[8:11], v[64:67]\n"
"ds_read_b64_tr_b16 a[208:209], v200 offset:32\n"
"v_lshrrev_b32 v255, 5, v253\n"
"v_mfma_f32_16x16x32_bf16 v[68:71], a[136:139], v[40:43], v[68:71]\n"
"ds_read_b64_tr_b16 a[210:211], v200 offset:4640\n"
"v_bfe_u32 v216, v253, 3, 2\n"
"v_mfma_f32_16x16x32_bf16 v[72:75], a[140:143], v[8:11], v[72:75]\n"
"ds_read_b64_tr_b16 a[212:213], v200 offset:64\n"
"v_lshlrev_b32 v241, 10, v255\n"
"v_mfma_f32_16x16x32_bf16 v[76:79], a[140:143], v[40:43], v[76:79]\n"
"ds_read_b64_tr_b16 a[214:215], v200 offset:4672\n"
"v_lshl_add_u32 v241, v216, 8, v241\n"
"v_mfma_f32_16x16x32_bf16 v[80:83], a[144:147], v[8:11], v[80:83]\n"
"ds_read_b64_tr_b16 a[216:217], v200 offset:96\n"
"v_lshl_add_u32 v241, v206, 4, v241\n"
"v_mfma_f32_16x16x32_bf16 v[84:87], a[144:147], v[40:43], v[84:87]\n"
"ds_read_b64_tr_b16 a[218:219], v200 offset:4704\n"
"v_add_u32 v242, 6144, v241\n"
"v_mfma_f32_16x16x32_bf16 v[88:91], a[148:151], v[8:11], v[88:91]\n"
"ds_read_b64_tr_b16 a[220:221], v200 offset:128\n"
"v_add_u32 v243, 12288, v241\n"
"v_mfma_f32_16x16x32_bf16 v[92:95], a[148:151], v[40:43], v[92:95]\n"
"s_waitcnt lgkmcnt(11)\n"
"ds_read_b64_tr_b16 a[222:223], v200 offset:4736\n"
"v_lshlrev_b32 v255, 5, v255\n"
"v_mfma_f32_16x16x32_bf16 v[96:99], a[152:155], v[8:11], v[96:99]\n"
"ds_read_b64_tr_b16 a[128:129], v200 offset:160\n"
"v_lshl_add_u32 v255, v216, 2, v255\n"
"v_mfma_f32_16x16x32_bf16 v[100:103], a[152:155], v[40:43], v[100:103]\n"
"ds_read_b64_tr_b16 a[130:131], v200 offset:4768\n"
"v_lshl_add_u32 v244, v255, 2, s62\n"
"v_mfma_f32_16x16x32_bf16 v[104:107], a[156:159], v[8:11], v[104:107]\n"
"ds_read_b64_tr_b16 a[132:133], v200 offset:192\n"
"v_mul_u32_u24 v206, 1152, v206\n"
"v_mfma_f32_16x16x32_bf16 v[108:111], a[156:159], v[40:43], v[108:111]\n"
"ds_read_b64_tr_b16 a[134:135], v200 offset:4800\n"
"v_lshl_add_u32 v240, v255, 1, v206\n"
"v_mfma_f32_16x16x32_bf16 v[112:115], a[192:195], v[8:11], v[112:115]\n"
"ds_read_b64_tr_b16 a[136:137], v200 offset:224\n"
"v_add_u32 v240, s40, v240\n"
"v_mfma_f32_16x16x32_bf16 v[116:119], a[192:195], v[40:43], v[116:119]\n"
"ds_read_b64_tr_b16 a[138:139], v200 offset:4832\n"
"v_add_u32 v245, v206, v255\n"
"v_mfma_f32_16x16x32_bf16 v[120:123], a[196:199], v[8:11], v[120:123]\n"
"ds_read_b64_tr_b16 a[140:141], v203 offset:0\n"
"v_lshlrev_b32 v245, 3, v245\n"
"v_mfma_f32_16x16x32_bf16 v[124:127], a[196:199], v[40:43], v[124:127]\n"
"s_waitcnt lgkmcnt(11)\n"
"ds_read_b64_tr_b16 a[142:143], v203 offset:4608\n"
"v_lshl_add_u32 v247, v253, 4, s62\n"
"v_mfma_f32_16x16x32_bf16 v[226:229], a[200:203], v[8:11], v[226:229]\n"
"s_mul_i32 s62, s41, 7\n"
"ds_read_b64_tr_b16 a[144:145], v200 offset:9216\n"
"v_mfma_f32_16x16x32_bf16 v[230:233], a[200:203], v[40:43], v[230:233]\n"
"s_min_u32 s62, s62, 20\n"
"ds_read_b64_tr_b16 a[146:147], v200 offset:13824\n"
"v_mfma_f32_16x16x32_bf16 v[64:67], a[204:207], v[12:15], v[64:67]\n"
"s_lshl_b32 s62, s62, 10\n"
"ds_read_b64_tr_b16 a[148:149], v200 offset:9248\n"
"v_mfma_f32_16x16x32_bf16 v[68:71], a[204:207], v[44:47], v[68:71]\n"
"s_add_u32 s50, s50, s62\n"
"ds_read_b64_tr_b16 a[150:151], v200 offset:13856\n"
"v_mfma_f32_16x16x32_bf16 v[72:75], a[208:211], v[12:15], v[72:75]\n"
"s_add_u32 s84, s40, s62\n"
"ds_read_b64_tr_b16 a[152:153], v200 offset:9280\n"
"v_mfma_f32_16x16x32_bf16 v[76:79], a[208:211], v[44:47], v[76:79]\n"
"s_add_u32 s86, s50, 4096\n"
"ds_read_b64_tr_b16 a[154:155], v200 offset:13888\n"
"v_mfma_f32_16x16x32_bf16 v[80:83], a[212:215], v[12:15], v[80:83]\n"
"ds_read_b64_tr_b16 a[156:157], v200 offset:9312\n"
"v_mfma_f32_16x16x32_bf16 v[84:87], a[212:215], v[44:47], v[84:87]\n"
"s_waitcnt lgkmcnt(11)\n"
"ds_read_b64_tr_b16 a[158:159], v200 offset:13920\n"
"v_mfma_f32_16x16x32_bf16 v[88:91], a[216:219], v[12:15], v[88:91]\n"
"ds_read_b64_tr_b16 a[192:193], v200 offset:9344\n"
"v_mfma_f32_16x16x32_bf16 v[92:95], a[216:219], v[44:47], v[92:95]\n"
"ds_read_b64_tr_b16 a[194:195], v200 offset:13952\n"
"v_mfma_f32_16x16x32_bf16 v[96:99], a[220:223], v[12:15], v[96:99]\n"
"ds_read_b64_tr_b16 a[196:197], v200 offset:9376\n"
"v_mfma_f32_16x16x32_bf16 v[100:103], a[220:223], v[44:47], v[100:103]\n"
"ds_read_b64_tr_b16 a[198:199], v200 offset:13984\n"
"v_mfma_f32_16x16x32_bf16 v[104:107], a[128:131], v[12:15], v[104:107]\n"
"ds_read_b64_tr_b16 a[200:201], v200 offset:9408\n"
"v_mfma_f32_16x16x32_bf16 v[108:111], a[128:131], v[44:47], v[108:111]\n"
"ds_read_b64_tr_b16 a[202:203], v200 offset:14016\n"
"v_mfma_f32_16x16x32_bf16 v[112:115], a[132:135], v[12:15], v[112:115]\n"
"ds_read_b64_tr_b16 a[204:205], v200 offset:9440\n"
"v_mfma_f32_16x16x32_bf16 v[116:119], a[132:135], v[44:47], v[116:119]\n"
"s_waitcnt lgkmcnt(11)\n"
"ds_read_b64_tr_b16 a[206:207], v200 offset:14048\n"
"v_mfma_f32_16x16x32_bf16 v[120:123], a[136:139], v[12:15], v[120:123]\n"
"ds_read_b64_tr_b16 a[208:209], v203 offset:9216\n"
"v_mfma_f32_16x16x32_bf16 v[124:127], a[136:139], v[44:47], v[124:127]\n"
"ds_read_b64_tr_b16 a[210:211], v203 offset:13824\n"
"v_mfma_f32_16x16x32_bf16 v[226:229], a[140:143], v[12:15], v[226:229]\n"
"ds_read_b64_tr_b16 a[212:213], v200 offset:18432\n"
"v_mfma_f32_16x16x32_bf16 v[230:233], a[140:143], v[44:47], v[230:233]\n"
"ds_read_b64_tr_b16 a[214:215], v200 offset:23040\n"
"v_mfma_f32_16x16x32_bf16 v[64:67], a[144:147], v[16:19], v[64:67]\n"
"ds_read_b64_tr_b16 a[216:217], v200 offset:18464\n"
"v_mfma_f32_16x16x32_bf16 v[68:71], a[144:147], v[48:51], v[68:71]\n"
"ds_read_b64_tr_b16 a[218:219], v200 offset:23072\n"
"v_mfma_f32_16x16x32_bf16 v[72:75], a[148:151], v[16:19], v[72:75]\n"
"s_waitcnt vmcnt(0)\n"
"s_barrier\n"
"ds_read_b64_tr_b16 a[220:221], v200 offset:18496\n"
"buffer_store_dword v252, v251, s[28:31], s23 offen sc1\n"
"v_mfma_f32_16x16x32_bf16 v[76:79], a[148:151], v[48:51], v[76:79]\n"
"s_waitcnt lgkmcnt(11)\n"
"ds_read_b64_tr_b16 a[222:223], v200 offset:23104\n"
"buffer_load_dwordx4 a[160:163], v246, s[28:31], s56 offen offset:0 sc1\n"
"v_mfma_f32_16x16x32_bf16 v[80:83], a[152:155], v[16:19], v[80:83]\n"
"s_mov_b32 m0, s51\n"
"ds_read_b64_tr_b16 a[128:129], v200 offset:18528\n"
"v_mfma_f32_16x16x32_bf16 v[84:87], a[152:155], v[48:51], v[84:87]\n"
"ds_read_b64_tr_b16 a[130:131], v200 offset:23136\n"
"buffer_load_dwordx4 v210, s[24:27], s36 offen offset:0 lds\n"
"v_mfma_f32_16x16x32_bf16 v[88:91], a[156:159], v[16:19], v[88:91]\n"
"ds_read_b64_tr_b16 a[132:133], v200 offset:18560\n"
"v_mfma_f32_16x16x32_bf16 v[92:95], a[156:159], v[48:51], v[92:95]\n"
"ds_read_b64_tr_b16 a[134:135], v200 offset:23168\n"
"v_mfma_f32_16x16x32_bf16 v[96:99], a[192:195], v[16:19], v[96:99]\n"
"ds_read_b64_tr_b16 a[136:137], v200 offset:18592\n"
"v_mfma_f32_16x16x32_bf16 v[100:103], a[192:195], v[48:51], v[100:103]\n"
"ds_read_b64_tr_b16 a[138:139], v200 offset:23200\n"
"buffer_load_dwordx4 a[164:167], v246, s[28:31], s56 offen offset:16 sc1\n"
"v_mfma_f32_16x16x32_bf16 v[104:107], a[196:199], v[16:19], v[104:107]\n"
"ds_read_b64_tr_b16 a[140:141], v200 offset:18624\n"
"v_mfma_f32_16x16x32_bf16 v[108:111], a[196:199], v[48:51], v[108:111]\n"
"s_waitcnt lgkmcnt(11)\n"
"s_mov_b32 m0, s51\n"
"ds_read_b64_tr_b16 a[142:143], v200 offset:23232\n"
"v_mfma_f32_16x16x32_bf16 v[112:115], a[200:203], v[16:19], v[112:115]\n"
"ds_read_b64_tr_b16 a[144:145], v200 offset:18656\n"
"buffer_load_dwordx4 v211, s[24:27], s36 offen offset:1024 lds\n"
"v_mfma_f32_16x16x32_bf16 v[116:119], a[200:203], v[48:51], v[116:119]\n"
"ds_read_b64_tr_b16 a[146:147], v200 offset:23264\n"
"v_mfma_f32_16x16x32_bf16 v[120:123], a[204:207], v[16:19], v[120:123]\n"
"ds_read_b64_tr_b16 a[148:149], v203 offset:18432\n"
"v_mfma_f32_16x16x32_bf16 v[124:127], a[204:207], v[48:51], v[124:127]\n"
"ds_read_b64_tr_b16 a[150:151], v203 offset:23040\n"
"buffer_load_dwordx4 a[168:171], v246, s[28:31], s57 offen offset:0 sc1\n"
"v_mfma_f32_16x16x32_bf16 v[226:229], a[208:211], v[16:19], v[226:229]\n"
"ds_read_b64_tr_b16 a[152:153], v200 offset:27648\n"
"v_mfma_f32_16x16x32_bf16 v[230:233], a[208:211], v[48:51], v[230:233]\n"
"ds_read_b64_tr_b16 a[154:155], v200 offset:32256\n"
"v_mfma_f32_16x16x32_bf16 v[64:67], a[212:215], v[20:23], v[64:67]\n"
"s_mov_b32 m0, s51\n"
"ds_read_b64_tr_b16 a[156:157], v200 offset:27680\n"
"v_mfma_f32_16x16x32_bf16 v[68:71], a[212:215], v[52:55], v[68:71]\n"
"s_waitcnt lgkmcnt(11)\n"
"ds_read_b64_tr_b16 a[158:159], v200 offset:32288\n"
"buffer_load_dwordx4 v212, s[24:27], s36 offen offset:2048 lds\n"
"v_mfma_f32_16x16x32_bf16 v[72:75], a[216:219], v[20:23], v[72:75]\n"
"ds_read_b64_tr_b16 a[192:193], v200 offset:27712\n"
"v_mfma_f32_16x16x32_bf16 v[76:79], a[216:219], v[52:55], v[76:79]\n"
"ds_read_b64_tr_b16 a[194:195], v200 offset:32320\n"
"buffer_load_dwordx4 a[172:175], v246, s[28:31], s57 offen offset:16 sc1\n"
"v_mfma_f32_16x16x32_bf16 v[80:83], a[220:223], v[20:23], v[80:83]\n"
"ds_read_b64_tr_b16 a[196:197], v200 offset:27744\n"
"v_mfma_f32_16x16x32_bf16 v[84:87], a[220:223], v[52:55], v[84:87]\n"
"ds_read_b64_tr_b16 a[198:199], v200 offset:32352\n"
"v_mfma_f32_16x16x32_bf16 v[88:91], a[128:131], v[20:23], v[88:91]\n"
"ds_read_b64_tr_b16 a[200:201], v200 offset:27776\n"
"v_mfma_f32_16x16x32_bf16 v[92:95], a[128:131], v[52:55], v[92:95]\n"
"s_mov_b32 m0, s51\n"
"ds_read_b64_tr_b16 a[202:203], v200 offset:32384\n"
"v_mfma_f32_16x16x32_bf16 v[96:99], a[132:135], v[20:23], v[96:99]\n"
"ds_read_b64_tr_b16 a[204:205], v200 offset:27808\n"
"buffer_load_dwordx4 v213, s[24:27], s36 offen offset:3072 lds\n"
"v_mfma_f32_16x16x32_bf16 v[100:103], a[132:135], v[52:55], v[100:103]\n"
"s_waitcnt lgkmcnt(11)\n"
"ds_read_b64_tr_b16 a[206:207], v200 offset:32416\n"
"buffer_load_dwordx4 a[176:179], v246, s[28:31], s58 offen offset:0 sc1\n"
"v_mfma_f32_16x16x32_bf16 v[104:107], a[136:139], v[20:23], v[104:107]\n"
"ds_read_b64_tr_b16 a[208:209], v200 offset:27840\n"
"v_mfma_f32_16x16x32_bf16 v[108:111], a[136:139], v[52:55], v[108:111]\n"
"ds_read_b64_tr_b16 a[210:211], v200 offset:32448\n"
"v_mfma_f32_16x16x32_bf16 v[112:115], a[140:143], v[20:23], v[112:115]\n"
"ds_read_b64_tr_b16 a[212:213], v200 offset:27872\n"
"v_mfma_f32_16x16x32_bf16 v[116:119], a[140:143], v[52:55], v[116:119]\n"
"ds_read_b64_tr_b16 a[214:215], v200 offset:32480\n"
"v_mfma_f32_16x16x32_bf16 v[120:123], a[144:147], v[20:23], v[120:123]\n"
"s_add_u32 m0, s51, 4096\n"
"ds_read_b64_tr_b16 a[216:217], v203 offset:27648\n"
"v_mfma_f32_16x16x32_bf16 v[124:127], a[144:147], v[52:55], v[124:127]\n"
"ds_read_b64_tr_b16 a[218:219], v203 offset:32256\n"
"buffer_load_dwordx4 v214, s[24:27], s36 offen offset:0 lds\n"
"v_mfma_f32_16x16x32_bf16 v[226:229], a[148:151], v[20:23], v[226:229]\n"
"ds_read_b64_tr_b16 a[220:221], v200 offset:36864\n"
"buffer_load_dwordx4 a[180:183], v246, s[28:31], s58 offen offset:16 sc1\n"
"v_mfma_f32_16x16x32_bf16 v[230:233], a[148:151], v[52:55], v[230:233]\n"
"s_waitcnt lgkmcnt(11)\n"
"ds_read_b64_tr_b16 a[222:223], v200 offset:41472\n"
"v_mfma_f32_16x16x32_bf16 v[64:67], a[152:155], v[24:27], v[64:67]\n"
"ds_read_b64_tr_b16 a[128:129], v200 offset:36896\n"
"v_mfma_f32_16x16x32_bf16 v[68:71], a[152:155], v[56:59], v[68:71]\n"
"ds_read_b64_tr_b16 a[130:131], v200 offset:41504\n"
"v_mfma_f32_16x16x32_bf16 v[72:75], a[156:159], v[24:27], v[72:75]\n"
"ds_read_b64_tr_b16 a[132:133], v200 offset:36928\n"
"v_mfma_f32_16x16x32_bf16 v[76:79], a[156:159], v[56:59], v[76:79]\n"
"s_add_u32 m0, s51, 4096\n"
"ds_read_b64_tr_b16 a[134:135], v200 offset:41536\n"
"v_mfma_f32_16x16x32_bf16 v[80:83], a[192:195], v[24:27], v[80:83]\n"
"ds_read_b64_tr_b16 a[136:137], v200 offset:36960\n"
"buffer_load_dwordx4 v236, s[24:27], s36 offen offset:1024 lds\n"
"v_mfma_f32_16x16x32_bf16 v[84:87], a[192:195], v[56:59], v[84:87]\n"
"ds_read_b64_tr_b16 a[138:139], v200 offset:41568\n"
"buffer_load_dwordx4 a[184:187], v246, s[28:31], s59 offen offset:0 sc1\n"
"v_mfma_f32_16x16x32_bf16 v[88:91], a[196:199], v[24:27], v[88:91]\n"
"ds_read_b64_tr_b16 a[140:141], v200 offset:36992\n"
"v_mfma_f32_16x16x32_bf16 v[92:95], a[196:199], v[56:59], v[92:95]\n"
"s_waitcnt lgkmcnt(11)\n"
"ds_read_b64_tr_b16 a[142:143], v200 offset:41600\n"
"v_mfma_f32_16x16x32_bf16 v[96:99], a[200:203], v[24:27], v[96:99]\n"
"ds_read_b64_tr_b16 a[144:145], v200 offset:37024\n"
"v_mfma_f32_16x16x32_bf16 v[100:103], a[200:203], v[56:59], v[100:103]\n"
"ds_read_b64_tr_b16 a[146:147], v200 offset:41632\n"
"v_mfma_f32_16x16x32_bf16 v[104:107], a[204:207], v[24:27], v[104:107]\n"
"s_add_u32 m0, s51, 4096\n"
"ds_read_b64_tr_b16 a[148:149], v200 offset:37056\n"
"v_mfma_f32_16x16x32_bf16 v[108:111], a[204:207], v[56:59], v[108:111]\n"
"ds_read_b64_tr_b16 a[150:151], v200 offset:41664\n"
"buffer_load_dwordx4 v237, s[24:27], s36 offen offset:2048 lds\n"
"v_mfma_f32_16x16x32_bf16 v[112:115], a[208:211], v[24:27], v[112:115]\n"
"ds_read_b64_tr_b16 a[152:153], v200 offset:37088\n"
"buffer_load_dwordx4 a[188:191], v246, s[28:31], s59 offen offset:16 sc1\n"
"v_mfma_f32_16x16x32_bf16 v[116:119], a[208:211], v[56:59], v[116:119]\n"
"ds_read_b64_tr_b16 a[154:155], v200 offset:41696\n"
"v_mfma_f32_16x16x32_bf16 v[120:123], a[212:215], v[24:27], v[120:123]\n"
"ds_read_b64_tr_b16 a[156:157], v203 offset:36864\n"
"v_mfma_f32_16x16x32_bf16 v[124:127], a[212:215], v[56:59], v[124:127]\n"
"s_waitcnt lgkmcnt(11)\n"
"ds_read_b64_tr_b16 a[158:159], v203 offset:41472\n"
"v_mfma_f32_16x16x32_bf16 v[226:229], a[216:219], v[24:27], v[226:229]\n"
"ds_read_b64_tr_b16 a[192:193], v200 offset:46080\n"
"v_mfma_f32_16x16x32_bf16 v[230:233], a[216:219], v[56:59], v[230:233]\n"
"s_add_u32 m0, s51, 4096\n"
"ds_read_b64_tr_b16 a[194:195], v200 offset:50688\n"
"v_mfma_f32_16x16x32_bf16 v[64:67], a[220:223], v[28:31], v[64:67]\n"
"ds_read_b64_tr_b16 a[196:197], v200 offset:46112\n"
"buffer_load_dwordx4 v238, s[24:27], s36 offen offset:3072 lds\n"
"v_mfma_f32_16x16x32_bf16 v[68:71], a[220:223], v[60:63], v[68:71]\n"
"ds_read_b64_tr_b16 a[198:199], v200 offset:50720\n"
"buffer_load_dwordx2 a[224:225], v254, s[80:83], 0 offen\n"
"v_mfma_f32_16x16x32_bf16 v[72:75], a[128:131], v[28:31], v[72:75]\n"
"ds_read_b64_tr_b16 a[200:201], v200 offset:46144\n"
"v_mfma_f32_16x16x32_bf16 v[76:79], a[128:131], v[60:63], v[76:79]\n"
"ds_read_b64_tr_b16 a[202:203], v200 offset:50752\n"
"v_mfma_f32_16x16x32_bf16 v[80:83], a[132:135], v[28:31], v[80:83]\n"
"ds_read_b64_tr_b16 a[204:205], v200 offset:46176\n"
"v_mfma_f32_16x16x32_bf16 v[84:87], a[132:135], v[60:63], v[84:87]\n"
"s_waitcnt lgkmcnt(11)\n"
"ds_read_b64_tr_b16 a[206:207], v200 offset:50784\n"
"v_mfma_f32_16x16x32_bf16 v[88:91], a[136:139], v[28:31], v[88:91]\n"
"s_add_u32 m0, s51, 8192\n"
"ds_read_b64_tr_b16 a[208:209], v200 offset:46208\n"
"v_mfma_f32_16x16x32_bf16 v[92:95], a[136:139], v[60:63], v[92:95]\n"
"ds_read_b64_tr_b16 a[210:211], v200 offset:50816\n"
"buffer_load_dwordx4 v239, s[24:27], s36 offen offset:0 lds\n"
"v_mfma_f32_16x16x32_bf16 v[96:99], a[140:143], v[28:31], v[96:99]\n"
"ds_read_b64_tr_b16 a[212:213], v200 offset:46240\n"
"v_mfma_f32_16x16x32_bf16 v[100:103], a[140:143], v[60:63], v[100:103]\n"
"ds_read_b64_tr_b16 a[214:215], v200 offset:50848\n"
"v_mfma_f32_16x16x32_bf16 v[104:107], a[144:147], v[28:31], v[104:107]\n"
"ds_read_b64_tr_b16 a[216:217], v200 offset:46272\n"
"v_mfma_f32_16x16x32_bf16 v[108:111], a[144:147], v[60:63], v[108:111]\n"
"ds_read_b64_tr_b16 a[218:219], v200 offset:50880\n"
"v_mfma_f32_16x16x32_bf16 v[112:115], a[148:151], v[28:31], v[112:115]\n"
"ds_read_b64_tr_b16 a[220:221], v200 offset:46304\n"
"v_mfma_f32_16x16x32_bf16 v[116:119], a[148:151], v[60:63], v[116:119]\n"
"s_waitcnt lgkmcnt(11)\n"
"ds_read_b64_tr_b16 a[222:223], v200 offset:50912\n"
"v_mfma_f32_16x16x32_bf16 v[120:123], a[152:155], v[28:31], v[120:123]\n"
"ds_read_b64_tr_b16 a[128:129], v203 offset:46080\n"
"buffer_load_dword v249, v215, s[28:31], s23 offen sc1\n"
"v_mfma_f32_16x16x32_bf16 v[124:127], a[152:155], v[60:63], v[124:127]\n"
"ds_read_b64_tr_b16 a[130:131], v203 offset:50688\n"
"v_mfma_f32_16x16x32_bf16 v[226:229], a[156:159], v[28:31], v[226:229]\n"
"ds_read_b64_tr_b16 a[132:133], v200 offset:55296\n"
"v_mfma_f32_16x16x32_bf16 v[230:233], a[156:159], v[60:63], v[230:233]\n"
"ds_read_b64_tr_b16 a[134:135], v200 offset:59904\n"
"s_barrier\n"
"s_waitcnt vmcnt(2)\n"
"s_mov_b32 s87, 0x80000\n"
"v_accvgpr_read_b32 v26, a160\n"
"v_accvgpr_read_b32 v27, a161\n"
"v_accvgpr_read_b32 v28, a162\n"
"v_accvgpr_read_b32 v29, a163\n"
"v_accvgpr_read_b32 v30, a164\n"
"v_accvgpr_read_b32 v31, a165\n"
"v_accvgpr_read_b32 v32, a166\n"
"v_accvgpr_read_b32 v33, a167\n"
"v_accvgpr_read_b32 v34, a168\n"
"v_accvgpr_read_b32 v35, a169\n"
"v_accvgpr_read_b32 v36, a170\n"
"v_accvgpr_read_b32 v37, a171\n"
"v_accvgpr_read_b32 v38, a172\n"
"v_accvgpr_read_b32 v39, a173\n"
"v_accvgpr_read_b32 v40, a174\n"
"v_accvgpr_read_b32 v41, a175\n"
"v_accvgpr_read_b32 v42, a176\n"
"v_accvgpr_read_b32 v43, a177\n"
"v_accvgpr_read_b32 v44, a178\n"
"v_accvgpr_read_b32 v45, a179\n"
"v_accvgpr_read_b32 v46, a180\n"
"v_accvgpr_read_b32 v47, a181\n"
"v_accvgpr_read_b32 v48, a182\n"
"v_accvgpr_read_b32 v49, a183\n"
"v_accvgpr_read_b32 v50, a184\n"
"v_accvgpr_read_b32 v51, a185\n"
"v_accvgpr_read_b32 v52, a186\n"
"v_accvgpr_read_b32 v53, a187\n"
"v_accvgpr_read_b32 v54, a188\n"
"v_accvgpr_read_b32 v55, a189\n"
"v_accvgpr_read_b32 v56, a190\n"
"v_accvgpr_read_b32 v57, a191\n"
"v_accvgpr_read_b32 v58, a224\n"
"v_accvgpr_read_b32 v59, a225\n"
"AT_tchk:\n"
"v_xor_b32 v24, s85, v27\n"
"v_xor_b32 v25, s85, v29\n"
"v_bitop3_b32 v24, v31, s85, v24 bitop3:0xbe\n"
"v_bitop3_b32 v25, v33, s85, v25 bitop3:0xbe\n"
"v_bitop3_b32 v24, v35, s85, v24 bitop3:0xbe\n"
"v_bitop3_b32 v25, v37, s85, v25 bitop3:0xbe\n"
"v_bitop3_b32 v24, v39, s85, v24 bitop3:0xbe\n"
"v_bitop3_b32 v25, v41, s85, v25 bitop3:0xbe\n"
"v_bitop3_b32 v24, v43, s85, v24 bitop3:0xbe\n"
"v_bitop3_b32 v25, v45, s85, v25 bitop3:0xbe\n"
"v_bitop3_b32 v24, v47, s85, v24 bitop3:0xbe\n"
"v_bitop3_b32 v25, v49, s85, v25 bitop3:0xbe\n"
"v_bitop3_b32 v24, v51, s85, v24 bitop3:0xbe\n"
"v_bitop3_b32 v25, v53, s85, v25 bitop3:0xbe\n"
"v_bitop3_b32 v24, v55, s85, v24 bitop3:0xbe\n"
"v_bitop3_b32 v25, v57, s85, v25 bitop3:0xbe\n"
"v_or_b32 v24, v24, v25\n"
"v_cmp_ne_u32 s[62:63], 0, v24\n"
"v_cmp_gt_u32 vcc, 144, v253\n"
"s_nop 1\n"
"s_and_b64 s[62:63], vcc, s[62:63]\n"
"s_cmp_eq_u64 s[62:63], 0\n"
"s_cbranch_scc1 AT_tok\n"
"s_sub_u32 s87, s87, 1\n"
"s_cmp_eq_u32 s87, 0\n"
"s_cbranch_scc1 AT_tok\n"
"s_sleep 2\n"
"buffer_load_dwordx4 v[26:29], v246, s[28:31], s56 offen offset:0 sc1\n"
"buffer_load_dwordx4 v[30:33], v246, s[28:31], s56 offen offset:16 sc1\n"
"buffer_load_dwordx4 v[34:37], v246, s[28:31], s57 offen offset:0 sc1\n"
"buffer_load_dwordx4 v[38:41], v246, s[28:31], s57 offen offset:16 sc1\n"
"buffer_load_dwordx4 v[42:45], v246, s[28:31], s58 offen offset:0 sc1\n"
"buffer_load_dwordx4 v[46:49], v246, s[28:31], s58 offen offset:16 sc1\n"
"buffer_load_dwordx4 v[50:53], v246, s[28:31], s59 offen offset:0 sc1\n"
"buffer_load_dwordx4 v[54:57], v246, s[28:31], s59 offen offset:16 sc1\n"
"s_waitcnt vmcnt(0)\n"
"s_branch AT_tchk\n"
"AT_tok:\n"
"s_waitcnt vmcnt(0)\n"
"v_cmp_ne_u32 vcc, s85, v249\n"
"s_nop 0\n"
"s_cbranch_vccnz AT_x1\n"
"AT_x2:\n"
"buffer_load_dwordx4 a[0:3], v207, s[28:31], s20 offen offset:0 sc1\n"
"buffer_load_dwordx4 a[4:7], v207, s[28:31], s20 offen offset:1024 sc1\n"
"buffer_load_dwordx4 a[8:11], v207, s[28:31], s21 offen offset:0 sc1\n"
"buffer_load_dwordx4 a[12:15], v207, s[28:31], s21 offen offset:1024 sc1\n"
"buffer_load_dwordx4 a[16:19], v207, s[28:31], s22 offen offset:0 sc1\n"
"buffer_load_dwordx4 a[20:23], v207, s[28:31], s22 offen offset:1024 sc1\n"
"buffer_load_dwordx4 a[24:27], v207, s[28:31], s20 offen offset:2048 sc1\n"
"buffer_load_dwordx4 a[28:31], v207, s[28:31], s20 offen offset:3072 sc1\n"
"s_add_u32 s20, s20, 4096\n"
"v_cmp_gt_u32 vcc, 144, v253\n"
"v_lshlrev_b32 v0, 16, v58\n"
"v_and_b32 v1, 0xffff0000, v58\n"
"v_lshlrev_b32 v2, 16, v59\n"
"v_and_b32 v3, 0xffff0000, v59\n"
"v_mov_b32 v4, v26\n"
"v_add_f32 v12, 1.0, v42\n"
"v_mov_b32 v16, v34\n"
"v_mov_b32 v20, v50\n"
"v_mul_f32 v8, v12, v0\n"
"v_mov_b32 v5, v28\n"
"v_add_f32 v13, 1.0, v44\n"
"v_mov_b32 v17, v36\n"
"v_mov_b32 v21, v52\n"
"v_mul_f32 v9, v13, v1\n"
"v_mov_b32 v6, v30\n"
"v_add_f32 v14, 1.0, v46\n"
"v_mov_b32 v18, v38\n"
"v_mov_b32 v22, v54\n"
"v_mul_f32 v10, v14, v2\n"
"v_mov_b32 v7, v32\n"
"v_add_f32 v15, 1.0, v48\n"
"v_mov_b32 v19, v40\n"
"v_mov_b32 v23, v56\n"
"v_mul_f32 v11, v15, v3\n"
"s_mov_b64 exec, vcc\n"
"ds_write_b128 v247, v[4:7] offset:0\n"
"ds_write_b128 v247, v[8:11] offset:2304\n"
"ds_write_b128 v247, v[12:15] offset:4608\n"
"ds_write_b128 v247, v[16:19] offset:6912\n"
"ds_write_b128 v247, v[20:23] offset:38912\n"
"s_mov_b64 exec, -1\n"
"v_add_f32 v24, v8, v9\n"
"v_mul_f32 v25, v16, v0\n"
"v_add_f32 v24, v24, v10\n"
"v_fmac_f32 v25, v17, v1\n"
"v_add_f32 v24, v24, v11\n"
"v_fmac_f32 v25, v18, v2\n"
"s_nop 0\n"
"v_fmac_f32 v25, v19, v3\n"
"s_nop 1\n"
"v_add_f32_dpp v24, v24, v24 row_ror:1 row_mask:0xf bank_mask:0xf\n"
"v_add_f32_dpp v25, v25, v25 row_ror:1 row_mask:0xf bank_mask:0xf\n"
"s_nop 0\n"
"v_add_f32_dpp v24, v24, v24 row_ror:2 row_mask:0xf bank_mask:0xf\n"
"v_add_f32_dpp v25, v25, v25 row_ror:2 row_mask:0xf bank_mask:0xf\n"
"s_nop 0\n"
"v_add_f32_dpp v24, v24, v24 row_ror:4 row_mask:0xf bank_mask:0xf\n"
"v_add_f32_dpp v25, v25, v25 row_ror:4 row_mask:0xf bank_mask:0xf\n"
"s_nop 0\n"
"v_add_f32_dpp v24, v24, v24 row_ror:8 row_mask:0xf bank_mask:0xf\n"
"v_add_f32_dpp v25, v25, v25 row_ror:8 row_mask:0xf bank_mask:0xf\n"
"s_nop 0\n"
"v_mov_b32 v26, v24\n"
"v_mov_b32 v27, v25\n"
"s_nop 0\n"
"v_permlane16_swap_b32 v24, v26\n"
"v_permlane16_swap_b32 v25, v27\n"
"v_add_f32 v24, v24, v26\n"
"v_add_f32 v25, v25, v27\n"
"v_mov_b32 v26, v24\n"
"v_mov_b32 v27, v25\n"
"s_nop 0\n"
"v_permlane32_swap_b32 v24, v26\n"
"v_permlane32_swap_b32 v25, v27\n"
"v_add_f32 v24, v24, v26\n"
"v_add_f32 v25, v25, v27\n"
"s_lshl_b32 s62, s41, 3\n"
"s_add_u32 s62, s62, s40\n"
"v_mov_b32 v26, s62\n"
"v_add_u32 v26, 161536, v26\n"
"s_mov_b64 exec, 1\n"
"ds_write_b64 v26, v[24:25]\n"
"s_mov_b64 exec, -1\n"
"s_waitcnt vmcnt(6)\n"
"v_mfma_f32_16x16x32_bf16 v[64:67], a[192:195], a[0:3], v[64:67]\n"
"ds_read_b64_tr_b16 a[136:137], v200 offset:55328\n"
"buffer_load_dwordx4 a[32:35], v207, s[28:31], s21 offen offset:2048 sc1\n"
"v_mfma_f32_16x16x32_bf16 v[68:71], a[192:195], a[4:7], v[68:71]\n"
"s_add_u32 m0, s53, 36864\n"
"ds_read_b64_tr_b16 a[138:139], v200 offset:59936\n"
"v_mfma_f32_16x16x32_bf16 v[72:75], a[196:199], a[0:3], v[72:75]\n"
"ds_read_b64_tr_b16 a[140:141], v200 offset:55360\n"
"buffer_load_dwordx4 v210, s[24:27], s37 offen offset:0 lds\n"
"v_mfma_f32_16x16x32_bf16 v[76:79], a[196:199], a[4:7], v[76:79]\n"
"s_waitcnt lgkmcnt(14)\n"
"ds_read_b64_tr_b16 a[142:143], v200 offset:59968\n"
"v_mfma_f32_16x16x32_bf16 v[80:83], a[200:203], a[0:3], v[80:83]\n"
"ds_read_b64_tr_b16 a[144:145], v200 offset:55392\n"
"v_mfma_f32_16x16x32_bf16 v[84:87], a[200:203], a[4:7], v[84:87]\n"
"ds_read_b64_tr_b16 a[146:147], v200 offset:60000\n"
"buffer_load_dwordx4 a[36:39], v207, s[28:31], s21 offen offset:3072 sc1\n"
"v_mfma_f32_16x16x32_bf16 v[88:91], a[204:207], a[0:3], v[88:91]\n"
"s_add_u32 s21, s21, 4096\n"
"ds_read_b64_tr_b16 a[148:149], v200 offset:55424\n"
"v_mfma_f32_16x16x32_bf16 v[92:95], a[204:207], a[4:7], v[92:95]\n"
"ds_read_b64_tr_b16 a[150:151], v200 offset:60032\n"
"v_mfma_f32_16x16x32_bf16 v[96:99], a[208:211], a[0:3], v[96:99]\n"
"s_add_u32 m0, s53, 36864\n"
"ds_read_b64_tr_b16 a[152:153], v200 offset:55456\n"
"v_mfma_f32_16x16x32_bf16 v[100:103], a[208:211], a[4:7], v[100:103]\n"
"ds_read_b64_tr_b16 a[154:155], v200 offset:60064\n"
"buffer_load_dwordx4 v211, s[24:27], s37 offen offset:1024 lds\n"
"v_mfma_f32_16x16x32_bf16 v[104:107], a[212:215], a[0:3], v[104:107]\n"
"ds_read_b64_tr_b16 a[156:157], v200 offset:55488\n"
"buffer_load_dwordx4 a[40:43], v207, s[28:31], s22 offen offset:2048 sc1\n"
"v_mfma_f32_16x16x32_bf16 v[108:111], a[212:215], a[4:7], v[108:111]\n"
"ds_read_b64_tr_b16 a[158:159], v200 offset:60096\n"
"v_mfma_f32_16x16x32_bf16 v[112:115], a[216:219], a[0:3], v[112:115]\n"
"ds_read_b64_tr_b16 a[192:193], v200 offset:55520\n"
"v_mfma_f32_16x16x32_bf16 v[116:119], a[216:219], a[4:7], v[116:119]\n"
"s_waitcnt lgkmcnt(11)\n"
"ds_read_b64_tr_b16 a[194:195], v200 offset:60128\n"
"v_mfma_f32_16x16x32_bf16 v[120:123], a[220:223], a[0:3], v[120:123]\n"
"ds_read_b64_tr_b16 a[196:197], v203 offset:55296\n"
"v_mfma_f32_16x16x32_bf16 v[124:127], a[220:223], a[4:7], v[124:127]\n"
"s_add_u32 m0, s53, 36864\n"
"ds_read_b64_tr_b16 a[198:199], v203 offset:59904\n"
"buffer_load_dwordx4 a[44:47], v207, s[28:31], s22 offen offset:3072 sc1\n"
"v_mfma_f32_16x16x32_bf16 v[226:229], a[128:131], a[0:3], v[226:229]\n"
"s_add_u32 s22, s22, 4096\n"
"ds_read_b64_tr_b16 a[200:201], v200 offset:64512\n"
"buffer_load_dwordx4 v212, s[24:27], s37 offen offset:2048 lds\n"
"v_mfma_f32_16x16x32_bf16 v[230:233], a[128:131], a[4:7], v[230:233]\n"
"s_waitcnt vmcnt(11)\n"
"ds_read_b64_tr_b16 a[202:203], v201 offset:3584\n"
"v_mfma_f32_16x16x32_bf16 v[64:67], a[132:135], a[8:11], v[64:67]\n"
"ds_read_b64_tr_b16 a[204:205], v200 offset:64544\n"
"v_mfma_f32_16x16x32_bf16 v[68:71], a[132:135], a[12:15], v[68:71]\n"
"ds_read_b64_tr_b16 a[206:207], v201 offset:3616\n"
"v_mfma_f32_16x16x32_bf16 v[72:75], a[136:139], a[8:11], v[72:75]\n"
"ds_read_b64_tr_b16 a[208:209], v200 offset:64576\n"
"buffer_load_dwordx4 a[48:51], v207, s[28:31], s20 offen offset:0 sc1\n"
"v_mfma_f32_16x16x32_bf16 v[76:79], a[136:139], a[12:15], v[76:79]\n"
"s_waitcnt lgkmcnt(11)\n"
"ds_read_b64_tr_b16 a[210:211], v201 offset:3648\n"
"v_mfma_f32_16x16x32_bf16 v[80:83], a[140:143], a[8:11], v[80:83]\n"
"s_add_u32 m0, s53, 36864\n"
"ds_read_b64_tr_b16 a[212:213], v200 offset:64608\n"
"v_mfma_f32_16x16x32_bf16 v[84:87], a[140:143], a[12:15], v[84:87]\n"
"ds_read_b64_tr_b16 a[214:215], v201 offset:3680\n"
"buffer_load_dwordx4 v213, s[24:27], s37 offen offset:3072 lds\n"
"v_mfma_f32_16x16x32_bf16 v[88:91], a[144:147], a[8:11], v[88:91]\n"
"ds_read_b64_tr_b16 a[216:217], v200 offset:64640\n"
"v_mfma_f32_16x16x32_bf16 v[92:95], a[144:147], a[12:15], v[92:95]\n"
"ds_read_b64_tr_b16 a[218:219], v201 offset:3712\n"
"buffer_load_dwordx4 a[52:55], v207, s[28:31], s20 offen offset:1024 sc1\n"
"v_mfma_f32_16x16x32_bf16 v[96:99], a[148:151], a[8:11], v[96:99]\n"
"ds_read_b64_tr_b16 a[220:221], v200 offset:64672\n"
"v_mfma_f32_16x16x32_bf16 v[100:103], a[148:151], a[12:15], v[100:103]\n"
"ds_read_b64_tr_b16 a[222:223], v201 offset:3744\n"
"v_mfma_f32_16x16x32_bf16 v[104:107], a[152:155], a[8:11], v[104:107]\n"
"ds_read_b64_tr_b16 a[128:129], v200 offset:64704\n"
"v_mfma_f32_16x16x32_bf16 v[108:111], a[152:155], a[12:15], v[108:111]\n"
"s_waitcnt lgkmcnt(11)\n"
"ds_read_b64_tr_b16 a[130:131], v201 offset:3776\n"
"v_mfma_f32_16x16x32_bf16 v[112:115], a[156:159], a[8:11], v[112:115]\n"
"s_add_u32 m0, s53, 40960\n"
"ds_read_b64_tr_b16 a[132:133], v200 offset:64736\n"
"buffer_load_dwordx4 a[56:59], v207, s[28:31], s21 offen offset:0 sc1\n"
"v_mfma_f32_16x16x32_bf16 v[116:119], a[156:159], a[12:15], v[116:119]\n"
"ds_read_b64_tr_b16 a[134:135], v201 offset:3808\n"
"buffer_load_dwordx4 v214, s[24:27], s37 offen offset:0 lds\n"
"v_mfma_f32_16x16x32_bf16 v[120:123], a[192:195], a[8:11], v[120:123]\n"
"ds_read_b64_tr_b16 a[136:137], v203 offset:64512\n"
"v_mfma_f32_16x16x32_bf16 v[124:127], a[192:195], a[12:15], v[124:127]\n"
"ds_read_b64_tr_b16 a[138:139], v204 offset:3584\n"
"v_mfma_f32_16x16x32_bf16 v[226:229], a[196:199], a[8:11], v[226:229]\n"
"ds_read_b64_tr_b16 a[140:141], v201 offset:8192\n"
"v_mfma_f32_16x16x32_bf16 v[230:233], a[196:199], a[12:15], v[230:233]\n"
"s_waitcnt vmcnt(14)\n"
"ds_read_b64_tr_b16 a[142:143], v201 offset:12800\n"
"v_mfma_f32_16x16x32_bf16 v[64:67], a[200:203], a[16:19], v[64:67]\n"
"ds_read_b64_tr_b16 a[144:145], v201 offset:8224\n"
"v_mfma_f32_16x16x32_bf16 v[68:71], a[200:203], a[20:23], v[68:71]\n"
"s_waitcnt lgkmcnt(11)\n"
"s_add_u32 m0, s53, 40960\n"
"ds_read_b64_tr_b16 a[146:147], v201 offset:12832\n"
"buffer_load_dwordx4 a[60:63], v207, s[28:31], s21 offen offset:1024 sc1\n"
"v_mfma_f32_16x16x32_bf16 v[72:75], a[204:207], a[16:19], v[72:75]\n"
"ds_read_b64_tr_b16 a[148:149], v201 offset:8256\n"
"buffer_load_dwordx4 v236, s[24:27], s37 offen offset:1024 lds\n"
"v_mfma_f32_16x16x32_bf16 v[76:79], a[204:207], a[20:23], v[76:79]\n"
"ds_read_b64_tr_b16 a[150:151], v201 offset:12864\n"
"v_mfma_f32_16x16x32_bf16 v[80:83], a[208:211], a[16:19], v[80:83]\n"
"ds_read_b64_tr_b16 a[152:153], v201 offset:8288\n"
"v_mfma_f32_16x16x32_bf16 v[84:87], a[208:211], a[20:23], v[84:87]\n"
"ds_read_b64_tr_b16 a[154:155], v201 offset:12896\n"
"v_mfma_f32_16x16x32_bf16 v[88:91], a[212:215], a[16:19], v[88:91]\n"
"ds_read_b64_tr_b16 a[156:157], v201 offset:8320\n"
"v_mfma_f32_16x16x32_bf16 v[92:95], a[212:215], a[20:23], v[92:95]\n"
"ds_read_b64_tr_b16 a[158:159], v201 offset:12928\n"
"v_mfma_f32_16x16x32_bf16 v[96:99], a[216:219], a[16:19], v[96:99]\n"
"s_add_u32 m0, s53, 40960\n"
"ds_read_b64_tr_b16 a[192:193], v201 offset:8352\n"
"buffer_load_dwordx4 a[64:67], v207, s[28:31], s22 offen offset:0 sc1\n"
"v_mfma_f32_16x16x32_bf16 v[100:103], a[216:219], a[20:23], v[100:103]\n"
"s_waitcnt lgkmcnt(11)\n"
"ds_read_b64_tr_b16 a[194:195], v201 offset:12960\n"
"buffer_load_dwordx4 v237, s[24:27], s37 offen offset:2048 lds\n"
"v_mfma_f32_16x16x32_bf16 v[104:107], a[220:223], a[16:19], v[104:107]\n"
"ds_read_b64_tr_b16 a[196:197], v201 offset:8384\n"
"v_mfma_f32_16x16x32_bf16 v[108:111], a[220:223], a[20:23], v[108:111]\n"
"ds_read_b64_tr_b16 a[198:199], v201 offset:12992\n"
"v_mfma_f32_16x16x32_bf16 v[112:115], a[128:131], a[16:19], v[112:115]\n"
"ds_read_b64_tr_b16 a[200:201], v201 offset:8416\n"
"v_mfma_f32_16x16x32_bf16 v[116:119], a[128:131], a[20:23], v[116:119]\n"
"ds_read_b64_tr_b16 a[202:203], v201 offset:13024\n"
"v_mfma_f32_16x16x32_bf16 v[120:123], a[132:135], a[16:19], v[120:123]\n"
"ds_read_b64_tr_b16 a[204:205], v204 offset:8192\n"
"v_mfma_f32_16x16x32_bf16 v[124:127], a[132:135], a[20:23], v[124:127]\n"
"s_add_u32 m0, s53, 40960\n"
"ds_read_b64_tr_b16 a[206:207], v204 offset:12800\n"
"buffer_load_dwordx4 a[68:71], v207, s[28:31], s22 offen offset:1024 sc1\n"
"v_mfma_f32_16x16x32_bf16 v[226:229], a[136:139], a[16:19], v[226:229]\n"
"ds_read_b64_tr_b16 a[208:209], v201 offset:17408\n"
"buffer_load_dwordx4 v238, s[24:27], s37 offen offset:3072 lds\n"
"v_mfma_f32_16x16x32_bf16 v[230:233], a[136:139], a[20:23], v[230:233]\n"
"s_waitcnt lgkmcnt(11)\n"
"s_waitcnt vmcnt(18)\n"
"ds_read_b64_tr_b16 a[210:211], v201 offset:22016\n"
"v_mfma_f32_16x16x32_bf16 v[64:67], a[140:143], a[24:27], v[64:67]\n"
"ds_read_b64_tr_b16 a[212:213], v201 offset:17440\n"
"v_mfma_f32_16x16x32_bf16 v[68:71], a[140:143], a[28:31], v[68:71]\n"
"ds_read_b64_tr_b16 a[214:215], v201 offset:22048\n"
"v_mfma_f32_16x16x32_bf16 v[72:75], a[144:147], a[24:27], v[72:75]\n"
"ds_read_b64_tr_b16 a[216:217], v201 offset:17472\n"
"v_mfma_f32_16x16x32_bf16 v[76:79], a[144:147], a[28:31], v[76:79]\n"
"ds_read_b64_tr_b16 a[218:219], v201 offset:22080\n"
"v_mfma_f32_16x16x32_bf16 v[80:83], a[148:151], a[24:27], v[80:83]\n"
"s_add_u32 m0, s53, 45056\n"
"ds_read_b64_tr_b16 a[220:221], v201 offset:17504\n"
"buffer_load_dwordx4 a[72:75], v207, s[28:31], s20 offen offset:2048 sc1\n"
"v_mfma_f32_16x16x32_bf16 v[84:87], a[148:151], a[28:31], v[84:87]\n"
"ds_read_b64_tr_b16 a[222:223], v201 offset:22112\n"
"buffer_load_dwordx4 v239, s[24:27], s37 offen offset:0 lds\n"
"v_mfma_f32_16x16x32_bf16 v[88:91], a[152:155], a[24:27], v[88:91]\n"
"ds_read_b64_tr_b16 a[128:129], v201 offset:17536\n"
"v_mfma_f32_16x16x32_bf16 v[92:95], a[152:155], a[28:31], v[92:95]\n"
"s_waitcnt lgkmcnt(11)\n"
"ds_read_b64_tr_b16 a[130:131], v201 offset:22144\n"
"v_mfma_f32_16x16x32_bf16 v[96:99], a[156:159], a[24:27], v[96:99]\n"
"ds_read_b64_tr_b16 a[132:133], v201 offset:17568\n"
"v_mfma_f32_16x16x32_bf16 v[100:103], a[156:159], a[28:31], v[100:103]\n"
"ds_read_b64_tr_b16 a[134:135], v201 offset:22176\n"
"v_mfma_f32_16x16x32_bf16 v[104:107], a[192:195], a[24:27], v[104:107]\n"
"ds_read_b64_tr_b16 a[136:137], v201 offset:17600\n"
"v_mfma_f32_16x16x32_bf16 v[108:111], a[192:195], a[28:31], v[108:111]\n"
"ds_read_b64_tr_b16 a[138:139], v201 offset:22208\n"
"buffer_load_dwordx4 a[76:79], v207, s[28:31], s20 offen offset:3072 sc1\n"
"v_mfma_f32_16x16x32_bf16 v[112:115], a[196:199], a[24:27], v[112:115]\n"
"s_add_u32 s20, s20, 4096\n"
"ds_read_b64_tr_b16 a[140:141], v201 offset:17632\n"
"v_mfma_f32_16x16x32_bf16 v[116:119], a[196:199], a[28:31], v[116:119]\n"
"ds_read_b64_tr_b16 a[142:143], v201 offset:22240\n"
"v_mfma_f32_16x16x32_bf16 v[120:123], a[200:203], a[24:27], v[120:123]\n"
"ds_read_b64_tr_b16 a[144:145], v204 offset:17408\n"
"v_mfma_f32_16x16x32_bf16 v[124:127], a[200:203], a[28:31], v[124:127]\n"
"s_waitcnt lgkmcnt(11)\n"
"ds_read_b64_tr_b16 a[146:147], v204 offset:22016\n"
"v_mfma_f32_16x16x32_bf16 v[226:229], a[204:207], a[24:27], v[226:229]\n"
"ds_read_b64_tr_b16 a[148:149], v201 offset:26624\n"
"v_mfma_f32_16x16x32_bf16 v[230:233], a[204:207], a[28:31], v[230:233]\n"
"s_waitcnt vmcnt(18)\n"
"ds_read_b64_tr_b16 a[150:151], v201 offset:31232\n"
"v_mfma_f32_16x16x32_bf16 v[64:67], a[208:211], a[32:35], v[64:67]\n"
"ds_read_b64_tr_b16 a[152:153], v201 offset:26656\n"
"buffer_load_dwordx4 a[80:83], v207, s[28:31], s21 offen offset:2048 sc1\n"
"v_mfma_f32_16x16x32_bf16 v[68:71], a[208:211], a[36:39], v[68:71]\n"
"ds_read_b64_tr_b16 a[154:155], v201 offset:31264\n"
"v_mfma_f32_16x16x32_bf16 v[72:75], a[212:215], a[32:35], v[72:75]\n"
"s_waitcnt vmcnt(31)\n"
"s_barrier\n"
"ds_read_b64_tr_b16 a[156:157], v201 offset:26688\n"
"v_mfma_f32_16x16x32_bf16 v[76:79], a[212:215], a[36:39], v[76:79]\n"
"ds_read_b64_tr_b16 a[158:159], v201 offset:31296\n"
"v_mfma_f32_16x16x32_bf16 v[80:83], a[216:219], a[32:35], v[80:83]\n"
"s_add_u32 m0, s53, 73728\n"
"ds_read_b64_tr_b16 a[192:193], v201 offset:26720\n"
"v_mfma_f32_16x16x32_bf16 v[84:87], a[216:219], a[36:39], v[84:87]\n"
"s_waitcnt lgkmcnt(11)\n"
"ds_read_b64_tr_b16 a[194:195], v201 offset:31328\n"
"buffer_load_dwordx4 v210, s[24:27], s38 offen offset:0 lds\n"
"v_mfma_f32_16x16x32_bf16 v[88:91], a[220:223], a[32:35], v[88:91]\n"
"ds_read_b64_tr_b16 a[196:197], v201 offset:26752\n"
"v_mfma_f32_16x16x32_bf16 v[92:95], a[220:223], a[36:39], v[92:95]\n"
"ds_read_b64_tr_b16 a[198:199], v201 offset:31360\n"
"buffer_load_dwordx4 a[84:87], v207, s[28:31], s21 offen offset:3072 sc1\n"
"v_mfma_f32_16x16x32_bf16 v[96:99], a[128:131], a[32:35], v[96:99]\n"
"s_add_u32 s21, s21, 4096\n"
"ds_read_b64_tr_b16 a[200:201], v201 offset:26784\n"
"v_mfma_f32_16x16x32_bf16 v[100:103], a[128:131], a[36:39], v[100:103]\n"
"ds_read_b64_tr_b16 a[202:203], v201 offset:31392\n"
"v_mfma_f32_16x16x32_bf16 v[104:107], a[132:135], a[32:35], v[104:107]\n"
"ds_read_b64_tr_b16 a[204:205], v201 offset:26816\n"
"v_mfma_f32_16x16x32_bf16 v[108:111], a[132:135], a[36:39], v[108:111]\n"
"s_add_u32 m0, s53, 73728\n"
"ds_read_b64_tr_b16 a[206:207], v201 offset:31424\n"
"v_mfma_f32_16x16x32_bf16 v[112:115], a[136:139], a[32:35], v[112:115]\n"
"ds_read_b64_tr_b16 a[208:209], v201 offset:26848\n"
"buffer_load_dwordx4 v211, s[24:27], s38 offen offset:1024 lds\n"
"v_mfma_f32_16x16x32_bf16 v[116:119], a[136:139], a[36:39], v[116:119]\n"
"s_waitcnt lgkmcnt(11)\n"
"ds_read_b64_tr_b16 a[210:211], v201 offset:31456\n"
"v_mfma_f32_16x16x32_bf16 v[120:123], a[140:143], a[32:35], v[120:123]\n"
"ds_read_b64_tr_b16 a[212:213], v204 offset:26624\n"
"buffer_load_dwordx4 a[88:91], v207, s[28:31], s22 offen offset:2048 sc1\n"
"v_mfma_f32_16x16x32_bf16 v[124:127], a[140:143], a[36:39], v[124:127]\n"
"ds_read_b64_tr_b16 a[214:215], v204 offset:31232\n"
"v_mfma_f32_16x16x32_bf16 v[226:229], a[144:147], a[32:35], v[226:229]\n"
"ds_read_b64_tr_b16 a[216:217], v201 offset:35840\n"
"v_mfma_f32_16x16x32_bf16 v[230:233], a[144:147], a[36:39], v[230:233]\n"
"s_waitcnt vmcnt(20)\n"
"ds_read_b64_tr_b16 a[218:219], v201 offset:40448\n"
"v_mfma_f32_16x16x32_bf16 v[64:67], a[148:151], a[40:43], v[64:67]\n"
"s_add_u32 m0, s53, 73728\n"
"ds_read_b64_tr_b16 a[220:221], v201 offset:35872\n"
"v_mfma_f32_16x16x32_bf16 v[68:71], a[148:151], a[44:47], v[68:71]\n"
"ds_read_b64_tr_b16 a[222:223], v201 offset:40480\n"
"buffer_load_dwordx4 v212, s[24:27], s38 offen offset:2048 lds\n"
"v_mfma_f32_16x16x32_bf16 v[72:75], a[152:155], a[40:43], v[72:75]\n"
"ds_read_b64_tr_b16 a[128:129], v201 offset:35904\n"
"v_mfma_f32_16x16x32_bf16 v[76:79], a[152:155], a[44:47], v[76:79]\n"
"s_waitcnt lgkmcnt(11)\n"
"ds_read_b64_tr_b16 a[130:131], v201 offset:40512\n"
"buffer_load_dwordx4 a[92:95], v207, s[28:31], s22 offen offset:3072 sc1\n"
"v_mfma_f32_16x16x32_bf16 v[80:83], a[156:159], a[40:43], v[80:83]\n"
"s_add_u32 s22, s22, 4096\n"
"ds_read_b64_tr_b16 a[132:133], v201 offset:35936\n"
"v_mfma_f32_16x16x32_bf16 v[84:87], a[156:159], a[44:47], v[84:87]\n"
"ds_read_b64_tr_b16 a[134:135], v201 offset:40544\n"
"v_mfma_f32_16x16x32_bf16 v[88:91], a[192:195], a[40:43], v[88:91]\n"
"ds_read_b64_tr_b16 a[136:137], v201 offset:35968\n"
"v_mfma_f32_16x16x32_bf16 v[92:95], a[192:195], a[44:47], v[92:95]\n"
"s_add_u32 m0, s53, 73728\n"
"ds_read_b64_tr_b16 a[138:139], v201 offset:40576\n"
"v_mfma_f32_16x16x32_bf16 v[96:99], a[196:199], a[40:43], v[96:99]\n"
"ds_read_b64_tr_b16 a[140:141], v201 offset:36000\n"
"buffer_load_dwordx4 v213, s[24:27], s38 offen offset:3072 lds\n"
"v_mfma_f32_16x16x32_bf16 v[100:103], a[196:199], a[44:47], v[100:103]\n"
"ds_read_b64_tr_b16 a[142:143], v201 offset:40608\n"
"v_mfma_f32_16x16x32_bf16 v[104:107], a[200:203], a[40:43], v[104:107]\n"
"ds_read_b64_tr_b16 a[144:145], v201 offset:36032\n"
"buffer_load_dwordx4 a[96:99], v207, s[28:31], s20 offen offset:0 sc1\n"
"v_mfma_f32_16x16x32_bf16 v[108:111], a[200:203], a[44:47], v[108:111]\n"
"s_waitcnt lgkmcnt(11)\n"
"ds_read_b64_tr_b16 a[146:147], v201 offset:40640\n"
"v_mfma_f32_16x16x32_bf16 v[112:115], a[204:207], a[40:43], v[112:115]\n"
"ds_read_b64_tr_b16 a[148:149], v201 offset:36064\n"
"v_mfma_f32_16x16x32_bf16 v[116:119], a[204:207], a[44:47], v[116:119]\n"
"ds_read_b64_tr_b16 a[150:151], v201 offset:40672\n"
"v_mfma_f32_16x16x32_bf16 v[120:123], a[208:211], a[40:43], v[120:123]\n"
"s_add_u32 m0, s53, 77824\n"
"ds_read_b64_tr_b16 a[152:153], v204 offset:35840\n"
"v_mfma_f32_16x16x32_bf16 v[124:127], a[208:211], a[44:47], v[124:127]\n"
"ds_read_b64_tr_b16 a[154:155], v204 offset:40448\n"
"buffer_load_dwordx4 v214, s[24:27], s38 offen offset:0 lds\n"
"v_mfma_f32_16x16x32_bf16 v[226:229], a[212:215], a[40:43], v[226:229]\n"
"ds_read_b64_tr_b16 a[156:157], v201 offset:45056\n"
"v_mfma_f32_16x16x32_bf16 v[230:233], a[212:215], a[44:47], v[230:233]\n"
"s_waitcnt vmcnt(21)\n"
"ds_read_b64_tr_b16 a[158:159], v201 offset:49664\n"
"buffer_load_dwordx4 a[100:103], v207, s[28:31], s20 offen offset:1024 sc1\n"
"v_mfma_f32_16x16x32_bf16 v[64:67], a[216:219], a[48:51], v[64:67]\n"
"ds_read_b64_tr_b16 a[192:193], v201 offset:45088\n"
"v_mfma_f32_16x16x32_bf16 v[68:71], a[216:219], a[52:55], v[68:71]\n"
"s_waitcnt lgkmcnt(11)\n"
"ds_read_b64_tr_b16 a[194:195], v201 offset:49696\n"
"v_mfma_f32_16x16x32_bf16 v[72:75], a[220:223], a[48:51], v[72:75]\n"
"ds_read_b64_tr_b16 a[196:197], v201 offset:45120\n"
"v_mfma_f32_16x16x32_bf16 v[76:79], a[220:223], a[52:55], v[76:79]\n"
"s_add_u32 m0, s53, 77824\n"
"ds_read_b64_tr_b16 a[198:199], v201 offset:49728\n"
"v_mfma_f32_16x16x32_bf16 v[80:83], a[128:131], a[48:51], v[80:83]\n"
"ds_read_b64_tr_b16 a[200:201], v201 offset:45152\n"
"buffer_load_dwordx4 v236, s[24:27], s38 offen offset:1024 lds\n"
"v_mfma_f32_16x16x32_bf16 v[84:87], a[128:131], a[52:55], v[84:87]\n"
"ds_read_b64_tr_b16 a[202:203], v201 offset:49760\n"
"v_mfma_f32_16x16x32_bf16 v[88:91], a[132:135], a[48:51], v[88:91]\n"
"ds_read_b64_tr_b16 a[204:205], v201 offset:45184\n"
"buffer_load_dwordx4 a[104:107], v207, s[28:31], s21 offen offset:0 sc1\n"
"v_mfma_f32_16x16x32_bf16 v[92:95], a[132:135], a[52:55], v[92:95]\n"
"ds_read_b64_tr_b16 a[206:207], v201 offset:49792\n"
"v_mfma_f32_16x16x32_bf16 v[96:99], a[136:139], a[48:51], v[96:99]\n"
"ds_read_b64_tr_b16 a[208:209], v201 offset:45216\n"
"v_mfma_f32_16x16x32_bf16 v[100:103], a[136:139], a[52:55], v[100:103]\n"
"s_waitcnt lgkmcnt(11)\n"
"ds_read_b64_tr_b16 a[210:211], v201 offset:49824\n"
"v_mfma_f32_16x16x32_bf16 v[104:107], a[140:143], a[48:51], v[104:107]\n"
"s_add_u32 m0, s53, 77824\n"
"ds_read_b64_tr_b16 a[212:213], v201 offset:45248\n"
"v_mfma_f32_16x16x32_bf16 v[108:111], a[140:143], a[52:55], v[108:111]\n"
"ds_read_b64_tr_b16 a[214:215], v201 offset:49856\n"
"buffer_load_dwordx4 v237, s[24:27], s38 offen offset:2048 lds\n"
"v_mfma_f32_16x16x32_bf16 v[112:115], a[144:147], a[48:51], v[112:115]\n"
"ds_read_b64_tr_b16 a[216:217], v201 offset:45280\n"
"v_mfma_f32_16x16x32_bf16 v[116:119], a[144:147], a[52:55], v[116:119]\n"
"ds_read_b64_tr_b16 a[218:219], v201 offset:49888\n"
"buffer_load_dwordx4 a[108:111], v207, s[28:31], s21 offen offset:1024 sc1\n"
"v_mfma_f32_16x16x32_bf16 v[120:123], a[148:151], a[48:51], v[120:123]\n"
"ds_read_b64_tr_b16 a[220:221], v204 offset:45056\n"
"v_mfma_f32_16x16x32_bf16 v[124:127], a[148:151], a[52:55], v[124:127]\n"
"ds_read_b64_tr_b16 a[222:223], v204 offset:49664\n"
"v_mfma_f32_16x16x32_bf16 v[226:229], a[152:155], a[48:51], v[226:229]\n"
"ds_read_b64_tr_b16 a[128:129], v202 offset:0\n"
"v_mfma_f32_16x16x32_bf16 v[230:233], a[152:155], a[52:55], v[230:233]\n"
"s_waitcnt lgkmcnt(11)\n"
"s_waitcnt vmcnt(23)\n"
"ds_read_b64_tr_b16 a[130:131], v202 offset:4608\n"
"v_mfma_f32_16x16x32_bf16 v[64:67], a[156:159], a[56:59], v[64:67]\n"
"s_add_u32 m0, s53, 77824\n"
"ds_read_b64_tr_b16 a[132:133], v202 offset:32\n"
"v_mfma_f32_16x16x32_bf16 v[68:71], a[156:159], a[60:63], v[68:71]\n"
"ds_read_b64_tr_b16 a[134:135], v202 offset:4640\n"
"buffer_load_dwordx4 v238, s[24:27], s38 offen offset:3072 lds\n"
"v_mfma_f32_16x16x32_bf16 v[72:75], a[192:195], a[56:59], v[72:75]\n"
"ds_read_b64_tr_b16 a[136:137], v202 offset:64\n"
"buffer_load_dwordx4 a[112:115], v207, s[28:31], s22 offen offset:0 sc1\n"
"v_mfma_f32_16x16x32_bf16 v[76:79], a[192:195], a[60:63], v[76:79]\n"
"ds_read_b64_tr_b16 a[138:139], v202 offset:4672\n"
"v_mfma_f32_16x16x32_bf16 v[80:83], a[196:199], a[56:59], v[80:83]\n"
"ds_read_b64_tr_b16 a[140:141], v202 offset:96\n"
"v_mfma_f32_16x16x32_bf16 v[84:87], a[196:199], a[60:63], v[84:87]\n"
"ds_read_b64_tr_b16 a[142:143], v202 offset:4704\n"
"v_mfma_f32_16x16x32_bf16 v[88:91], a[200:203], a[56:59], v[88:91]\n"
"ds_read_b64_tr_b16 a[144:145], v202 offset:128\n"
"v_mfma_f32_16x16x32_bf16 v[92:95], a[200:203], a[60:63], v[92:95]\n"
"s_waitcnt lgkmcnt(11)\n"
"s_add_u32 m0, s53, 81920\n"
"ds_read_b64_tr_b16 a[146:147], v202 offset:4736\n"
"v_mfma_f32_16x16x32_bf16 v[96:99], a[204:207], a[56:59], v[96:99]\n"
"ds_read_b64_tr_b16 a[148:149], v202 offset:160\n"
"buffer_load_dwordx4 v239, s[24:27], s38 offen offset:0 lds\n"
"v_mfma_f32_16x16x32_bf16 v[100:103], a[204:207], a[60:63], v[100:103]\n"
"ds_read_b64_tr_b16 a[150:151], v202 offset:4768\n"
"buffer_load_dwordx4 a[116:119], v207, s[28:31], s22 offen offset:1024 sc1\n"
"v_mfma_f32_16x16x32_bf16 v[104:107], a[208:211], a[56:59], v[104:107]\n"
"ds_read_b64_tr_b16 a[152:153], v202 offset:192\n"
"v_mfma_f32_16x16x32_bf16 v[108:111], a[208:211], a[60:63], v[108:111]\n"
"ds_read_b64_tr_b16 a[154:155], v202 offset:4800\n"
"v_mfma_f32_16x16x32_bf16 v[112:115], a[212:215], a[56:59], v[112:115]\n"
"ds_read_b64_tr_b16 a[156:157], v202 offset:224\n"
"v_mfma_f32_16x16x32_bf16 v[116:119], a[212:215], a[60:63], v[116:119]\n"
"ds_read_b64_tr_b16 a[158:159], v202 offset:4832\n"
"v_mfma_f32_16x16x32_bf16 v[120:123], a[216:219], a[56:59], v[120:123]\n"
"ds_read_b64_tr_b16 a[192:193], v205 offset:0\n"
"v_mfma_f32_16x16x32_bf16 v[124:127], a[216:219], a[60:63], v[124:127]\n"
"s_waitcnt lgkmcnt(11)\n"
"ds_read_b64_tr_b16 a[194:195], v205 offset:4608\n"
"v_mfma_f32_16x16x32_bf16 v[226:229], a[220:223], a[56:59], v[226:229]\n"
"ds_read_b64_tr_b16 a[196:197], v202 offset:9216\n"
"buffer_load_dwordx4 a[120:123], v207, s[28:31], s20 offen offset:2048 sc1\n"
"v_mfma_f32_16x16x32_bf16 v[230:233], a[220:223], a[60:63], v[230:233]\n"
"s_waitcnt vmcnt(24)\n"
"ds_read_b64_tr_b16 a[198:199], v202 offset:13824\n"
"v_mfma_f32_16x16x32_bf16 v[64:67], a[128:131], a[64:67], v[64:67]\n"
"ds_read_b64_tr_b16 a[200:201], v202 offset:9248\n"
"v_mfma_f32_16x16x32_bf16 v[68:71], a[128:131], a[68:71], v[68:71]\n"
"ds_read_b64_tr_b16 a[202:203], v202 offset:13856\n"
"v_mfma_f32_16x16x32_bf16 v[72:75], a[132:135], a[64:67], v[72:75]\n"
"s_waitcnt vmcnt(21)\n"
"s_barrier\n"
"ds_read_b64_tr_b16 a[204:205], v202 offset:9280\n"
"v_mfma_f32_16x16x32_bf16 v[76:79], a[132:135], a[68:71], v[76:79]\n"
"ds_read_b64_tr_b16 a[206:207], v202 offset:13888\n"
"v_mfma_f32_16x16x32_bf16 v[80:83], a[136:139], a[64:67], v[80:83]\n"
"ds_read_b64_tr_b16 a[208:209], v202 offset:9312\n"
"v_mfma_f32_16x16x32_bf16 v[84:87], a[136:139], a[68:71], v[84:87]\n"
"s_waitcnt lgkmcnt(11)\n"
"s_add_u32 m0, s53, 110592\n"
"ds_read_b64_tr_b16 a[210:211], v202 offset:13920\n"
"buffer_load_dwordx4 a[124:127], v207, s[28:31], s20 offen offset:3072 sc1\n"
"v_mfma_f32_16x16x32_bf16 v[88:91], a[140:143], a[64:67], v[88:91]\n"
"s_add_u32 s20, s20, 4096\n"
"ds_read_b64_tr_b16 a[212:213], v202 offset:9344\n"
"buffer_load_dwordx4 v210, s[24:27], s39 offen offset:0 lds\n"
"v_mfma_f32_16x16x32_bf16 v[92:95], a[140:143], a[68:71], v[92:95]\n"
"ds_read_b64_tr_b16 a[214:215], v202 offset:13952\n"
"v_mfma_f32_16x16x32_bf16 v[96:99], a[144:147], a[64:67], v[96:99]\n"
"ds_read_b64_tr_b16 a[216:217], v202 offset:9376\n"
"v_mfma_f32_16x16x32_bf16 v[100:103], a[144:147], a[68:71], v[100:103]\n"
"ds_read_b64_tr_b16 a[218:219], v202 offset:13984\n"
"v_mfma_f32_16x16x32_bf16 v[104:107], a[148:151], a[64:67], v[104:107]\n"
"ds_read_b64_tr_b16 a[220:221], v202 offset:9408\n"
"v_mfma_f32_16x16x32_bf16 v[108:111], a[148:151], a[68:71], v[108:111]\n"
"ds_read_b64_tr_b16 a[222:223], v202 offset:14016\n"
"v_mfma_f32_16x16x32_bf16 v[112:115], a[152:155], a[64:67], v[112:115]\n"
"s_add_u32 m0, s53, 110592\n"
"ds_read_b64_tr_b16 a[128:129], v202 offset:9440\n"
"buffer_load_dwordx4 v[0:3], v207, s[28:31], s21 offen offset:2048 sc1\n"
"v_mfma_f32_16x16x32_bf16 v[116:119], a[152:155], a[68:71], v[116:119]\n"
"s_waitcnt lgkmcnt(11)\n"
"ds_read_b64_tr_b16 a[130:131], v202 offset:14048\n"
"buffer_load_dwordx4 v211, s[24:27], s39 offen offset:1024 lds\n"
"v_mfma_f32_16x16x32_bf16 v[120:123], a[156:159], a[64:67], v[120:123]\n"
"ds_read_b64_tr_b16 a[132:133], v205 offset:9216\n"
"v_mfma_f32_16x16x32_bf16 v[124:127], a[156:159], a[68:71], v[124:127]\n"
"ds_read_b64_tr_b16 a[134:135], v205 offset:13824\n"
"v_mfma_f32_16x16x32_bf16 v[226:229], a[192:195], a[64:67], v[226:229]\n"
"ds_read_b64_tr_b16 a[136:137], v202 offset:18432\n"
"v_mfma_f32_16x16x32_bf16 v[230:233], a[192:195], a[68:71], v[230:233]\n"
"s_waitcnt vmcnt(24)\n"
"ds_read_b64_tr_b16 a[138:139], v202 offset:23040\n"
"v_mfma_f32_16x16x32_bf16 v[64:67], a[196:199], a[72:75], v[64:67]\n"
"ds_read_b64_tr_b16 a[140:141], v202 offset:18464\n"
"v_mfma_f32_16x16x32_bf16 v[68:71], a[196:199], a[76:79], v[68:71]\n"
"s_add_u32 m0, s53, 110592\n"
"ds_read_b64_tr_b16 a[142:143], v202 offset:23072\n"
"buffer_load_dwordx4 v[4:7], v207, s[28:31], s21 offen offset:3072 sc1\n"
"v_mfma_f32_16x16x32_bf16 v[72:75], a[200:203], a[72:75], v[72:75]\n"
"s_add_u32 s21, s21, 4096\n"
"ds_read_b64_tr_b16 a[144:145], v202 offset:18496\n"
"buffer_load_dwordx4 v212, s[24:27], s39 offen offset:2048 lds\n"
"v_mfma_f32_16x16x32_bf16 v[76:79], a[200:203], a[76:79], v[76:79]\n"
"s_waitcnt lgkmcnt(11)\n"
"ds_read_b64_tr_b16 a[146:147], v202 offset:23104\n"
"v_mfma_f32_16x16x32_bf16 v[80:83], a[204:207], a[72:75], v[80:83]\n"
"ds_read_b64_tr_b16 a[148:149], v202 offset:18528\n"
"v_mfma_f32_16x16x32_bf16 v[84:87], a[204:207], a[76:79], v[84:87]\n"
"ds_read_b64_tr_b16 a[150:151], v202 offset:23136\n"
"v_mfma_f32_16x16x32_bf16 v[88:91], a[208:211], a[72:75], v[88:91]\n"
"ds_read_b64_tr_b16 a[152:153], v202 offset:18560\n"
"v_mfma_f32_16x16x32_bf16 v[92:95], a[208:211], a[76:79], v[92:95]\n"
"ds_read_b64_tr_b16 a[154:155], v202 offset:23168\n"
"v_mfma_f32_16x16x32_bf16 v[96:99], a[212:215], a[72:75], v[96:99]\n"
"s_add_u32 m0, s53, 110592\n"
"ds_read_b64_tr_b16 a[156:157], v202 offset:18592\n"
"buffer_load_dwordx4 v[8:11], v207, s[28:31], s22 offen offset:2048 sc1\n"
"v_mfma_f32_16x16x32_bf16 v[100:103], a[212:215], a[76:79], v[100:103]\n"
"ds_read_b64_tr_b16 a[158:159], v202 offset:23200\n"
"buffer_load_dwordx4 v213, s[24:27], s39 offen offset:3072 lds\n"
"v_mfma_f32_16x16x32_bf16 v[104:107], a[216:219], a[72:75], v[104:107]\n"
"ds_read_b64_tr_b16 a[192:193], v202 offset:18624\n"
"v_mfma_f32_16x16x32_bf16 v[108:111], a[216:219], a[76:79], v[108:111]\n"
"s_waitcnt lgkmcnt(11)\n"
"ds_read_b64_tr_b16 a[194:195], v202 offset:23232\n"
"v_mfma_f32_16x16x32_bf16 v[112:115], a[220:223], a[72:75], v[112:115]\n"
"ds_read_b64_tr_b16 a[196:197], v202 offset:18656\n"
"v_mfma_f32_16x16x32_bf16 v[116:119], a[220:223], a[76:79], v[116:119]\n"
"ds_read_b64_tr_b16 a[198:199], v202 offset:23264\n"
"v_mfma_f32_16x16x32_bf16 v[120:123], a[128:131], a[72:75], v[120:123]\n"
"ds_read_b64_tr_b16 a[200:201], v205 offset:18432\n"
"v_mfma_f32_16x16x32_bf16 v[124:127], a[128:131], a[76:79], v[124:127]\n"
"s_add_u32 m0, s53, 114688\n"
"ds_read_b64_tr_b16 a[202:203], v205 offset:23040\n"
"buffer_load_dwordx4 v[12:15], v207, s[28:31], s22 offen offset:3072 sc1\n"
"v_mfma_f32_16x16x32_bf16 v[226:229], a[132:135], a[72:75], v[226:229]\n"
"s_add_u32 s22, s22, 4096\n"
"ds_read_b64_tr_b16 a[204:205], v200 offset:0\n"
"buffer_load_dwordx4 v214, s[24:27], s39 offen offset:0 lds\n"
"v_mfma_f32_16x16x32_bf16 v[230:233], a[132:135], a[76:79], v[230:233]\n"
"s_waitcnt vmcnt(27)\n"
"ds_read_b64_tr_b16 a[206:207], v200 offset:4608\n"
"v_mfma_f32_16x16x32_bf16 v[64:67], a[136:139], a[80:83], v[64:67]\n"
"ds_read_b64_tr_b16 a[208:209], v200 offset:32\n"
"v_mfma_f32_16x16x32_bf16 v[68:71], a[136:139], a[84:87], v[68:71]\n"
"s_waitcnt lgkmcnt(11)\n"
"ds_read_b64_tr_b16 a[210:211], v200 offset:4640\n"
"v_mfma_f32_16x16x32_bf16 v[72:75], a[140:143], a[80:83], v[72:75]\n"
"ds_read_b64_tr_b16 a[212:213], v200 offset:64\n"
"v_mfma_f32_16x16x32_bf16 v[76:79], a[140:143], a[84:87], v[76:79]\n"
"ds_read_b64_tr_b16 a[214:215], v200 offset:4672\n"
"v_mfma_f32_16x16x32_bf16 v[80:83], a[144:147], a[80:83], v[80:83]\n"
"s_add_u32 m0, s53, 114688\n"
"ds_read_b64_tr_b16 a[216:217], v200 offset:96\n"
"buffer_load_dwordx4 v[16:19], v207, s[28:31], s20 offen offset:0 sc1\n"
"v_mfma_f32_16x16x32_bf16 v[84:87], a[144:147], a[84:87], v[84:87]\n"
"ds_read_b64_tr_b16 a[218:219], v200 offset:4704\n"
"buffer_load_dwordx4 v236, s[24:27], s39 offen offset:1024 lds\n"
"v_mfma_f32_16x16x32_bf16 v[88:91], a[148:151], a[80:83], v[88:91]\n"
"ds_read_b64_tr_b16 a[220:221], v200 offset:128\n"
"v_mfma_f32_16x16x32_bf16 v[92:95], a[148:151], a[84:87], v[92:95]\n"
"ds_read_b64_tr_b16 a[222:223], v200 offset:4736\n"
"v_mfma_f32_16x16x32_bf16 v[96:99], a[152:155], a[80:83], v[96:99]\n"
"ds_read_b64_tr_b16 a[128:129], v200 offset:160\n"
"v_mfma_f32_16x16x32_bf16 v[100:103], a[152:155], a[84:87], v[100:103]\n"
"s_waitcnt lgkmcnt(11)\n"
"ds_read_b64_tr_b16 a[130:131], v200 offset:4768\n"
"v_mfma_f32_16x16x32_bf16 v[104:107], a[156:159], a[80:83], v[104:107]\n"
"ds_read_b64_tr_b16 a[132:133], v200 offset:192\n"
"v_mfma_f32_16x16x32_bf16 v[108:111], a[156:159], a[84:87], v[108:111]\n"
"s_add_u32 m0, s53, 114688\n"
"ds_read_b64_tr_b16 a[134:135], v200 offset:4800\n"
"buffer_load_dwordx4 v[20:23], v207, s[28:31], s20 offen offset:1024 sc1\n"
"v_mfma_f32_16x16x32_bf16 v[112:115], a[192:195], a[80:83], v[112:115]\n"
"ds_read_b64_tr_b16 a[136:137], v200 offset:224\n"
"buffer_load_dwordx4 v237, s[24:27], s39 offen offset:2048 lds\n"
"v_mfma_f32_16x16x32_bf16 v[116:119], a[192:195], a[84:87], v[116:119]\n"
"ds_read_b64_tr_b16 a[138:139], v200 offset:4832\n"
"v_mfma_f32_16x16x32_bf16 v[120:123], a[196:199], a[80:83], v[120:123]\n"
"ds_read_b64_tr_b16 a[140:141], v203 offset:0\n"
"v_mfma_f32_16x16x32_bf16 v[124:127], a[196:199], a[84:87], v[124:127]\n"
"ds_read_b64_tr_b16 a[142:143], v203 offset:4608\n"
"v_mfma_f32_16x16x32_bf16 v[226:229], a[200:203], a[80:83], v[226:229]\n"
"ds_read_b64_tr_b16 a[144:145], v200 offset:9216\n"
"v_mfma_f32_16x16x32_bf16 v[230:233], a[200:203], a[84:87], v[230:233]\n"
"s_waitcnt lgkmcnt(11)\n"
"s_waitcnt vmcnt(27)\n"
"ds_read_b64_tr_b16 a[146:147], v200 offset:13824\n"
"v_mfma_f32_16x16x32_bf16 v[64:67], a[204:207], a[88:91], v[64:67]\n"
"s_add_u32 m0, s53, 114688\n"
"ds_read_b64_tr_b16 a[148:149], v200 offset:9248\n"
"buffer_load_dwordx4 v[24:27], v207, s[28:31], s21 offen offset:0 sc1\n"
"v_mfma_f32_16x16x32_bf16 v[68:71], a[204:207], a[92:95], v[68:71]\n"
"ds_read_b64_tr_b16 a[150:151], v200 offset:13856\n"
"buffer_load_dwordx4 v238, s[24:27], s39 offen offset:3072 lds\n"
"v_mfma_f32_16x16x32_bf16 v[72:75], a[208:211], a[88:91], v[72:75]\n"
"ds_read_b64_tr_b16 a[152:153], v200 offset:9280\n"
"v_mfma_f32_16x16x32_bf16 v[76:79], a[208:211], a[92:95], v[76:79]\n"
"ds_read_b64_tr_b16 a[154:155], v200 offset:13888\n"
"v_mfma_f32_16x16x32_bf16 v[80:83], a[212:215], a[88:91], v[80:83]\n"
"ds_read_b64_tr_b16 a[156:157], v200 offset:9312\n"
"v_mfma_f32_16x16x32_bf16 v[84:87], a[212:215], a[92:95], v[84:87]\n"
"ds_read_b64_tr_b16 a[158:159], v200 offset:13920\n"
"v_mfma_f32_16x16x32_bf16 v[88:91], a[216:219], a[88:91], v[88:91]\n"
"ds_read_b64_tr_b16 a[192:193], v200 offset:9344\n"
"v_mfma_f32_16x16x32_bf16 v[92:95], a[216:219], a[92:95], v[92:95]\n"
"s_waitcnt lgkmcnt(11)\n"
"s_add_u32 m0, s53, 118784\n"
"ds_read_b64_tr_b16 a[194:195], v200 offset:13952\n"
"buffer_load_dwordx4 v[28:31], v207, s[28:31], s21 offen offset:1024 sc1\n"
"v_mfma_f32_16x16x32_bf16 v[96:99], a[220:223], a[88:91], v[96:99]\n"
"ds_read_b64_tr_b16 a[196:197], v200 offset:9376\n"
"buffer_load_dwordx4 v239, s[24:27], s39 offen offset:0 lds\n"
"v_mfma_f32_16x16x32_bf16 v[100:103], a[220:223], a[92:95], v[100:103]\n"
"ds_read_b64_tr_b16 a[198:199], v200 offset:13984\n"
"v_mfma_f32_16x16x32_bf16 v[104:107], a[128:131], a[88:91], v[104:107]\n"
"ds_read_b64_tr_b16 a[200:201], v200 offset:9408\n"
"v_mfma_f32_16x16x32_bf16 v[108:111], a[128:131], a[92:95], v[108:111]\n"
"ds_read_b64_tr_b16 a[202:203], v200 offset:14016\n"
"v_mfma_f32_16x16x32_bf16 v[112:115], a[132:135], a[88:91], v[112:115]\n"
"ds_read_b64_tr_b16 a[204:205], v200 offset:9440\n"
"v_mfma_f32_16x16x32_bf16 v[116:119], a[132:135], a[92:95], v[116:119]\n"
"ds_read_b64_tr_b16 a[206:207], v200 offset:14048\n"
"v_mfma_f32_16x16x32_bf16 v[120:123], a[136:139], a[88:91], v[120:123]\n"
"ds_read_b64_tr_b16 a[208:209], v203 offset:9216\n"
"buffer_load_dwordx4 v[32:35], v207, s[28:31], s22 offen offset:0 sc1\n"
"v_mfma_f32_16x16x32_bf16 v[124:127], a[136:139], a[92:95], v[124:127]\n"
"s_waitcnt lgkmcnt(11)\n"
"ds_read_b64_tr_b16 a[210:211], v203 offset:13824\n"
"v_mfma_f32_16x16x32_bf16 v[226:229], a[140:143], a[88:91], v[226:229]\n"
"ds_read_b64_tr_b16 a[212:213], v200 offset:18432\n"
"v_mfma_f32_16x16x32_bf16 v[230:233], a[140:143], a[92:95], v[230:233]\n"
"s_waitcnt vmcnt(28)\n"
"ds_read_b64_tr_b16 a[214:215], v200 offset:23040\n"
"v_mfma_f32_16x16x32_bf16 v[64:67], a[144:147], a[96:99], v[64:67]\n"
"ds_read_b64_tr_b16 a[216:217], v200 offset:18464\n"
"v_mfma_f32_16x16x32_bf16 v[68:71], a[144:147], a[100:103], v[68:71]\n"
"ds_read_b64_tr_b16 a[218:219], v200 offset:23072\n"
"v_mfma_f32_16x16x32_bf16 v[72:75], a[148:151], a[96:99], v[72:75]\n"
"ds_read_b64_tr_b16 a[220:221], v200 offset:18496\n"
"v_mfma_f32_16x16x32_bf16 v[76:79], a[148:151], a[100:103], v[76:79]\n"
"ds_read_b64_tr_b16 a[222:223], v200 offset:23104\n"
"buffer_load_dwordx4 v[36:39], v207, s[28:31], s22 offen offset:1024 sc1\n"
"v_mfma_f32_16x16x32_bf16 v[80:83], a[152:155], a[96:99], v[80:83]\n"
"ds_read_b64_tr_b16 a[128:129], v200 offset:18528\n"
"v_mfma_f32_16x16x32_bf16 v[84:87], a[152:155], a[100:103], v[84:87]\n"
"s_waitcnt lgkmcnt(11)\n"
"ds_read_b64_tr_b16 a[130:131], v200 offset:23136\n"
"v_mfma_f32_16x16x32_bf16 v[88:91], a[156:159], a[96:99], v[88:91]\n"
"ds_read_b64_tr_b16 a[132:133], v200 offset:18560\n"
"v_mfma_f32_16x16x32_bf16 v[92:95], a[156:159], a[100:103], v[92:95]\n"
"ds_read_b64_tr_b16 a[134:135], v200 offset:23168\n"
"v_mfma_f32_16x16x32_bf16 v[96:99], a[192:195], a[96:99], v[96:99]\n"
"ds_read_b64_tr_b16 a[136:137], v200 offset:18592\n"
"v_mfma_f32_16x16x32_bf16 v[100:103], a[192:195], a[100:103], v[100:103]\n"
"ds_read_b64_tr_b16 a[138:139], v200 offset:23200\n"
"v_mfma_f32_16x16x32_bf16 v[104:107], a[196:199], a[96:99], v[104:107]\n"
"ds_read_b64_tr_b16 a[140:141], v200 offset:18624\n"
"buffer_load_dwordx4 v[40:43], v207, s[28:31], s20 offen offset:2048 sc1\n"
"v_mfma_f32_16x16x32_bf16 v[108:111], a[196:199], a[100:103], v[108:111]\n"
"ds_read_b64_tr_b16 a[142:143], v200 offset:23232\n"
"v_mfma_f32_16x16x32_bf16 v[112:115], a[200:203], a[96:99], v[112:115]\n"
"ds_read_b64_tr_b16 a[144:145], v200 offset:18656\n"
"v_mfma_f32_16x16x32_bf16 v[116:119], a[200:203], a[100:103], v[116:119]\n"
"s_waitcnt lgkmcnt(11)\n"
"ds_read_b64_tr_b16 a[146:147], v200 offset:23264\n"
"v_mfma_f32_16x16x32_bf16 v[120:123], a[204:207], a[96:99], v[120:123]\n"
"ds_read_b64_tr_b16 a[148:149], v203 offset:18432\n"
"v_mfma_f32_16x16x32_bf16 v[124:127], a[204:207], a[100:103], v[124:127]\n"
"ds_read_b64_tr_b16 a[150:151], v203 offset:23040\n"
"v_mfma_f32_16x16x32_bf16 v[226:229], a[208:211], a[96:99], v[226:229]\n"
"ds_read_b64_tr_b16 a[152:153], v200 offset:27648\n"
"v_mfma_f32_16x16x32_bf16 v[230:233], a[208:211], a[100:103], v[230:233]\n"
"s_waitcnt vmcnt(26)\n"
"ds_read_b64_tr_b16 a[154:155], v200 offset:32256\n"
"buffer_load_dwordx4 v[44:47], v207, s[28:31], s20 offen offset:3072 sc1\n"
"v_mfma_f32_16x16x32_bf16 v[64:67], a[212:215], a[104:107], v[64:67]\n"
"s_add_u32 s20, s20, 4096\n"
"ds_read_b64_tr_b16 a[156:157], v200 offset:27680\n"
"v_mfma_f32_16x16x32_bf16 v[68:71], a[212:215], a[108:111], v[68:71]\n"
"ds_read_b64_tr_b16 a[158:159], v200 offset:32288\n"
"v_mfma_f32_16x16x32_bf16 v[72:75], a[216:219], a[104:107], v[72:75]\n"
"ds_read_b64_tr_b16 a[192:193], v200 offset:27712\n"
"v_mfma_f32_16x16x32_bf16 v[76:79], a[216:219], a[108:111], v[76:79]\n"
"s_waitcnt lgkmcnt(11)\n"
"ds_read_b64_tr_b16 a[194:195], v200 offset:32320\n"
"v_mfma_f32_16x16x32_bf16 v[80:83], a[220:223], a[104:107], v[80:83]\n"
"ds_read_b64_tr_b16 a[196:197], v200 offset:27744\n"
"v_mfma_f32_16x16x32_bf16 v[84:87], a[220:223], a[108:111], v[84:87]\n"
"ds_read_b64_tr_b16 a[198:199], v200 offset:32352\n"
"v_mfma_f32_16x16x32_bf16 v[88:91], a[128:131], a[104:107], v[88:91]\n"
"ds_read_b64_tr_b16 a[200:201], v200 offset:27776\n"
"buffer_load_dwordx4 v[48:51], v207, s[28:31], s21 offen offset:2048 sc1\n"
"v_mfma_f32_16x16x32_bf16 v[92:95], a[128:131], a[108:111], v[92:95]\n"
"ds_read_b64_tr_b16 a[202:203], v200 offset:32384\n"
"v_mfma_f32_16x16x32_bf16 v[96:99], a[132:135], a[104:107], v[96:99]\n"
"ds_read_b64_tr_b16 a[204:205], v200 offset:27808\n"
"v_mfma_f32_16x16x32_bf16 v[100:103], a[132:135], a[108:111], v[100:103]\n"
"ds_read_b64_tr_b16 a[206:207], v200 offset:32416\n"
"v_mfma_f32_16x16x32_bf16 v[104:107], a[136:139], a[104:107], v[104:107]\n"
"ds_read_b64_tr_b16 a[208:209], v200 offset:27840\n"
"v_mfma_f32_16x16x32_bf16 v[108:111], a[136:139], a[108:111], v[108:111]\n"
"s_waitcnt lgkmcnt(11)\n"
"ds_read_b64_tr_b16 a[210:211], v200 offset:32448\n"
"v_mfma_f32_16x16x32_bf16 v[112:115], a[140:143], a[104:107], v[112:115]\n"
"ds_read_b64_tr_b16 a[212:213], v200 offset:27872\n"
"v_mfma_f32_16x16x32_bf16 v[116:119], a[140:143], a[108:111], v[116:119]\n"
"ds_read_b64_tr_b16 a[214:215], v200 offset:32480\n"
"buffer_load_dwordx4 v[52:55], v207, s[28:31], s21 offen offset:3072 sc1\n"
"v_mfma_f32_16x16x32_bf16 v[120:123], a[144:147], a[104:107], v[120:123]\n"
"s_add_u32 s21, s21, 4096\n"
"ds_read_b64_tr_b16 a[216:217], v203 offset:27648\n"
"v_mfma_f32_16x16x32_bf16 v[124:127], a[144:147], a[108:111], v[124:127]\n"
"ds_read_b64_tr_b16 a[218:219], v203 offset:32256\n"
"v_mfma_f32_16x16x32_bf16 v[226:229], a[148:151], a[104:107], v[226:229]\n"
"ds_read_b64_tr_b16 a[220:221], v200 offset:36864\n"
"v_mfma_f32_16x16x32_bf16 v[230:233], a[148:151], a[108:111], v[230:233]\n"
"s_waitcnt vmcnt(25)\n"
"ds_read_b64_tr_b16 a[222:223], v200 offset:41472\n"
"v_mfma_f32_16x16x32_bf16 v[64:67], a[152:155], a[112:115], v[64:67]\n"
"ds_read_b64_tr_b16 a[128:129], v200 offset:36896\n"
"v_mfma_f32_16x16x32_bf16 v[68:71], a[152:155], a[116:119], v[68:71]\n"
"s_waitcnt lgkmcnt(11)\n"
"ds_read_b64_tr_b16 a[130:131], v200 offset:41504\n"
"v_mfma_f32_16x16x32_bf16 v[72:75], a[156:159], a[112:115], v[72:75]\n"
"ds_read_b64_tr_b16 a[132:133], v200 offset:36928\n"
"buffer_load_dwordx4 v[56:59], v207, s[28:31], s22 offen offset:2048 sc1\n"
"v_mfma_f32_16x16x32_bf16 v[76:79], a[156:159], a[116:119], v[76:79]\n"
"ds_read_b64_tr_b16 a[134:135], v200 offset:41536\n"
"v_mfma_f32_16x16x32_bf16 v[80:83], a[192:195], a[112:115], v[80:83]\n"
"ds_read_b64_tr_b16 a[136:137], v200 offset:36960\n"
"v_mfma_f32_16x16x32_bf16 v[84:87], a[192:195], a[116:119], v[84:87]\n"
"ds_read_b64_tr_b16 a[138:139], v200 offset:41568\n"
"v_mfma_f32_16x16x32_bf16 v[88:91], a[196:199], a[112:115], v[88:91]\n"
"ds_read_b64_tr_b16 a[140:141], v200 offset:36992\n"
"v_mfma_f32_16x16x32_bf16 v[92:95], a[196:199], a[116:119], v[92:95]\n"
"ds_read_b64_tr_b16 a[142:143], v200 offset:41600\n"
"v_mfma_f32_16x16x32_bf16 v[96:99], a[200:203], a[112:115], v[96:99]\n"
"ds_read_b64_tr_b16 a[144:145], v200 offset:37024\n"
"v_mfma_f32_16x16x32_bf16 v[100:103], a[200:203], a[116:119], v[100:103]\n"
"s_waitcnt lgkmcnt(11)\n"
"ds_read_b64_tr_b16 a[146:147], v200 offset:41632\n"
"buffer_load_dwordx4 v[60:63], v207, s[28:31], s22 offen offset:3072 sc1\n"
"v_mfma_f32_16x16x32_bf16 v[104:107], a[204:207], a[112:115], v[104:107]\n"
"s_add_u32 s22, s22, 4096\n"
"ds_read_b64_tr_b16 a[148:149], v200 offset:37056\n"
"v_mfma_f32_16x16x32_bf16 v[108:111], a[204:207], a[116:119], v[108:111]\n"
"ds_read_b64_tr_b16 a[150:151], v200 offset:41664\n"
"v_mfma_f32_16x16x32_bf16 v[112:115], a[208:211], a[112:115], v[112:115]\n"
"s_waitcnt vmcnt(8)\n"
"s_barrier\n"
"ds_read_b64_tr_b16 a[152:153], v200 offset:37088\n"
"v_mfma_f32_16x16x32_bf16 v[116:119], a[208:211], a[116:119], v[116:119]\n"
"ds_read_b64_tr_b16 a[154:155], v200 offset:41696\n"
"v_mfma_f32_16x16x32_bf16 v[120:123], a[212:215], a[112:115], v[120:123]\n"
"ds_read_b64_tr_b16 a[156:157], v203 offset:36864\n"
"v_mfma_f32_16x16x32_bf16 v[124:127], a[212:215], a[116:119], v[124:127]\n"
"ds_read_b64_tr_b16 a[158:159], v203 offset:41472\n"
"v_mfma_f32_16x16x32_bf16 v[226:229], a[216:219], a[112:115], v[226:229]\n"
"ds_read_b64_tr_b16 a[192:193], v203 offset:46080\n"
"v_mfma_f32_16x16x32_bf16 v[230:233], a[216:219], a[116:119], v[230:233]\n"
"s_waitcnt lgkmcnt(11)\n"
"s_waitcnt vmcnt(25)\n"
"ds_read_b64_tr_b16 a[194:195], v203 offset:50688\n"
"v_mfma_f32_16x16x32_bf16 v[64:67], a[220:223], a[120:123], v[64:67]\n"
"ds_read_b64_tr_b16 a[196:197], v203 offset:55296\n"
"v_mfma_f32_16x16x32_bf16 v[68:71], a[220:223], a[124:127], v[68:71]\n"
"ds_read_b64_tr_b16 a[198:199], v203 offset:59904\n"
"v_mfma_f32_16x16x32_bf16 v[72:75], a[128:131], a[120:123], v[72:75]\n"
"ds_read_b64_tr_b16 a[200:201], v203 offset:64512\n"
"v_mfma_f32_16x16x32_bf16 v[76:79], a[128:131], a[124:127], v[76:79]\n"
"ds_read_b64_tr_b16 a[202:203], v204 offset:3584\n"
"v_mfma_f32_16x16x32_bf16 v[80:83], a[132:135], a[120:123], v[80:83]\n"
"ds_read_b64_tr_b16 a[204:205], v204 offset:8192\n"
"v_mfma_f32_16x16x32_bf16 v[84:87], a[132:135], a[124:127], v[84:87]\n"
"ds_read_b64_tr_b16 a[206:207], v204 offset:12800\n"
"v_mfma_f32_16x16x32_bf16 v[88:91], a[136:139], a[120:123], v[88:91]\n"
"ds_read_b64_tr_b16 a[208:209], v204 offset:17408\n"
"v_mfma_f32_16x16x32_bf16 v[92:95], a[136:139], a[124:127], v[92:95]\n"
"s_waitcnt lgkmcnt(11)\n"
"ds_read_b64_tr_b16 a[210:211], v204 offset:22016\n"
"v_mfma_f32_16x16x32_bf16 v[96:99], a[140:143], a[120:123], v[96:99]\n"
"ds_read_b64_tr_b16 a[212:213], v204 offset:26624\n"
"v_mfma_f32_16x16x32_bf16 v[100:103], a[140:143], a[124:127], v[100:103]\n"
"ds_read_b64_tr_b16 a[214:215], v204 offset:31232\n"
"v_mfma_f32_16x16x32_bf16 v[104:107], a[144:147], a[120:123], v[104:107]\n"
"ds_read_b64_tr_b16 a[216:217], v204 offset:35840\n"
"v_mfma_f32_16x16x32_bf16 v[108:111], a[144:147], a[124:127], v[108:111]\n"
"ds_read_b64_tr_b16 a[218:219], v204 offset:40448\n"
"v_mfma_f32_16x16x32_bf16 v[112:115], a[148:151], a[120:123], v[112:115]\n"
"ds_read_b64_tr_b16 a[220:221], v204 offset:45056\n"
"v_mfma_f32_16x16x32_bf16 v[116:119], a[148:151], a[124:127], v[116:119]\n"
"ds_read_b64_tr_b16 a[222:223], v204 offset:49664\n"
"v_mfma_f32_16x16x32_bf16 v[120:123], a[152:155], a[120:123], v[120:123]\n"
"ds_read_b64_tr_b16 a[128:129], v200 offset:46080\n"
"v_mfma_f32_16x16x32_bf16 v[124:127], a[152:155], a[124:127], v[124:127]\n"
"s_waitcnt lgkmcnt(11)\n"
"s_mov_b32 m0, s45\n"
"ds_read_b64_tr_b16 a[130:131], v200 offset:50688\n"
"v_mfma_f32_16x16x32_bf16 v[226:229], a[156:159], a[120:123], v[226:229]\n"
"ds_read_b64_tr_b16 a[132:133], v200 offset:55296\n"
"buffer_load_dwordx4 v207, s[16:19], s46 offen offset:0 lds\n"
"v_mfma_f32_16x16x32_bf16 v[230:233], a[156:159], a[124:127], v[230:233]\n"
"s_waitcnt vmcnt(22)\n"
"ds_read_b64_tr_b16 a[134:135], v200 offset:59904\n"
"v_mfma_f32_16x16x32_bf16 v[226:229], a[192:195], v[0:3], v[226:229]\n"
"ds_read_b64_tr_b16 a[136:137], v200 offset:64512\n"
"v_mfma_f32_16x16x32_bf16 v[230:233], a[192:195], v[4:7], v[230:233]\n"
"s_waitcnt vmcnt(18)\n"
"ds_read_b64_tr_b16 a[138:139], v201 offset:3584\n"
"v_mfma_f32_16x16x32_bf16 v[226:229], a[196:199], v[8:11], v[226:229]\n"
"s_mov_b32 m0, s45\n"
"ds_read_b64_tr_b16 a[140:141], v201 offset:8192\n"
"v_accvgpr_write_b32 a48, v128\n"
"v_mfma_f32_16x16x32_bf16 v[230:233], a[196:199], v[12:15], v[230:233]\n"
"s_waitcnt vmcnt(14)\n"
"ds_read_b64_tr_b16 a[142:143], v201 offset:12800\n"
"buffer_load_dwordx4 v207, s[16:19], s46 offen offset:1024 lds\n"
"v_mfma_f32_16x16x32_bf16 v[226:229], a[200:203], v[16:19], v[226:229]\n"
"ds_read_b64_tr_b16 a[144:145], v201 offset:17408\n"
"v_accvgpr_write_b32 a49, v129\n"
"v_mfma_f32_16x16x32_bf16 v[230:233], a[200:203], v[20:23], v[230:233]\n"
"s_waitcnt lgkmcnt(11)\n"
"s_waitcnt vmcnt(11)\n"
"ds_read_b64_tr_b16 a[146:147], v201 offset:22016\n"
"v_accvgpr_write_b32 a50, v130\n"
"v_mfma_f32_16x16x32_bf16 v[226:229], a[204:207], v[24:27], v[226:229]\n"
"ds_read_b64_tr_b16 a[148:149], v201 offset:26624\n"
"v_accvgpr_write_b32 a51, v131\n"
"v_mfma_f32_16x16x32_bf16 v[230:233], a[204:207], v[28:31], v[230:233]\n"
"s_waitcnt vmcnt(8)\n"
"ds_read_b64_tr_b16 a[150:151], v201 offset:31232\n"
"v_accvgpr_write_b32 a52, v132\n"
"v_mfma_f32_16x16x32_bf16 v[226:229], a[208:211], v[32:35], v[226:229]\n"
"s_mov_b32 m0, s45\n"
"ds_read_b64_tr_b16 a[152:153], v201 offset:35840\n"
"v_accvgpr_write_b32 a53, v133\n"
"v_mfma_f32_16x16x32_bf16 v[230:233], a[208:211], v[36:39], v[230:233]\n"
"s_waitcnt vmcnt(6)\n"
"ds_read_b64_tr_b16 a[154:155], v201 offset:40448\n"
"buffer_load_dwordx4 v207, s[16:19], s46 offen offset:2048 lds\n"
"v_mfma_f32_16x16x32_bf16 v[226:229], a[212:215], v[40:43], v[226:229]\n"
"ds_read_b64_tr_b16 a[156:157], v201 offset:45056\n"
"v_accvgpr_write_b32 a54, v134\n"
"v_mfma_f32_16x16x32_bf16 v[230:233], a[212:215], v[44:47], v[230:233]\n"
"s_waitcnt vmcnt(5)\n"
"ds_read_b64_tr_b16 a[158:159], v201 offset:49664\n"
"v_accvgpr_write_b32 a55, v135\n"
"v_mfma_f32_16x16x32_bf16 v[226:229], a[216:219], v[48:51], v[226:229]\n"
"ds_read_b64_tr_b16 a[192:193], v200 offset:46112\n"
"v_accvgpr_write_b32 a56, v136\n"
"v_mfma_f32_16x16x32_bf16 v[230:233], a[216:219], v[52:55], v[230:233]\n"
"s_waitcnt lgkmcnt(11)\n"
"s_waitcnt vmcnt(3)\n"
"ds_read_b64_tr_b16 a[194:195], v200 offset:50720\n"
"v_accvgpr_write_b32 a57, v137\n"
"v_mfma_f32_16x16x32_bf16 v[226:229], a[220:223], v[56:59], v[226:229]\n"
"ds_read_b64_tr_b16 a[196:197], v200 offset:55328\n"
"v_accvgpr_write_b32 a58, v138\n"
"v_mfma_f32_16x16x32_bf16 v[230:233], a[220:223], v[60:63], v[230:233]\n"
"ds_read_b64_tr_b16 a[198:199], v200 offset:59936\n"
"v_accvgpr_write_b32 a59, v139\n"
"v_mfma_f32_16x16x32_bf16 v[64:67], a[128:131], v[0:3], v[64:67]\n"
"ds_read_b64_tr_b16 a[200:201], v200 offset:64544\n"
"v_accvgpr_write_b32 a60, v140\n"
"v_mfma_f32_16x16x32_bf16 v[68:71], a[128:131], v[4:7], v[68:71]\n"
"s_add_u32 m0, s84, 131072\n"
"ds_read_b64_tr_b16 a[202:203], v201 offset:3616\n"
"v_accvgpr_write_b32 a61, v141\n"
"v_mfma_f32_16x16x32_bf16 v[64:67], a[132:135], v[8:11], v[64:67]\n"
"ds_read_b64_tr_b16 a[204:205], v201 offset:8224\n"
"buffer_load_dwordx4 v207, s[72:75], s50 offen offset:0 lds\n"
"v_mfma_f32_16x16x32_bf16 v[68:71], a[132:135], v[12:15], v[68:71]\n"
"ds_read_b64_tr_b16 a[206:207], v201 offset:12832\n"
"v_cvt_pk_bf16_f32 v218, v226, v227\n"
"v_mfma_f32_16x16x32_bf16 v[64:67], a[136:139], v[16:19], v[64:67]\n"
"ds_read_b64_tr_b16 a[208:209], v201 offset:17440\n"
"v_cvt_pk_bf16_f32 v219, v228, v229\n"
"v_mfma_f32_16x16x32_bf16 v[68:71], a[136:139], v[20:23], v[68:71]\n"
"s_waitcnt lgkmcnt(11)\n"
"ds_read_b64_tr_b16 a[210:211], v201 offset:22048\n"
"v_cvt_pk_bf16_f32 v222, v230, v231\n"
"v_mfma_f32_16x16x32_bf16 v[64:67], a[140:143], v[24:27], v[64:67]\n"
"ds_read_b64_tr_b16 a[212:213], v201 offset:26656\n"
"v_cvt_pk_bf16_f32 v223, v232, v233\n"
"v_mfma_f32_16x16x32_bf16 v[68:71], a[140:143], v[28:31], v[68:71]\n"
"ds_read_b64_tr_b16 a[214:215], v201 offset:31264\n"
"v_accvgpr_write_b32 a62, v142\n"
"v_mfma_f32_16x16x32_bf16 v[64:67], a[144:147], v[32:35], v[64:67]\n"
"s_add_u32 m0, s84, 131072\n"
"ds_read_b64_tr_b16 a[216:217], v201 offset:35872\n"
"buffer_store_dwordx2 v[218:219], v207, s[28:31], s49 offen sc1\n"
"v_mfma_f32_16x16x32_bf16 v[68:71], a[144:147], v[36:39], v[68:71]\n"
"ds_read_b64_tr_b16 a[218:219], v201 offset:40480\n"
"buffer_load_dwordx4 v207, s[72:75], s50 offen offset:1024 lds\n"
"v_mfma_f32_16x16x32_bf16 v[64:67], a[148:151], v[40:43], v[64:67]\n"
"ds_read_b64_tr_b16 a[220:221], v201 offset:45088\n"
"buffer_store_dwordx2 v[222:223], v208, s[28:31], s49 offen sc1\n"
"v_mfma_f32_16x16x32_bf16 v[68:71], a[148:151], v[44:47], v[68:71]\n"
"ds_read_b64_tr_b16 a[222:223], v201 offset:49696\n"
"v_accvgpr_write_b32 a63, v143\n"
"v_mfma_f32_16x16x32_bf16 v[64:67], a[152:155], v[48:51], v[64:67]\n"
"ds_read_b64_tr_b16 a[128:129], v200 offset:46144\n"
"v_accvgpr_write_b32 a64, v144\n"
"v_mfma_f32_16x16x32_bf16 v[68:71], a[152:155], v[52:55], v[68:71]\n"
"s_waitcnt lgkmcnt(11)\n"
"ds_read_b64_tr_b16 a[130:131], v200 offset:50752\n"
"v_accvgpr_write_b32 a65, v145\n"
"v_mfma_f32_16x16x32_bf16 v[64:67], a[156:159], v[56:59], v[64:67]\n"
"s_add_u32 m0, s84, 131072\n"
"ds_read_b64_tr_b16 a[132:133], v200 offset:55360\n"
"v_accvgpr_write_b32 a66, v146\n"
"v_mfma_f32_16x16x32_bf16 v[68:71], a[156:159], v[60:63], v[68:71]\n"
"ds_read_b64_tr_b16 a[134:135], v200 offset:59968\n"
"buffer_load_dwordx4 v207, s[72:75], s50 offen offset:2048 lds\n"
"v_mfma_f32_16x16x32_bf16 v[72:75], a[192:195], v[0:3], v[72:75]\n"
"ds_read_b64_tr_b16 a[136:137], v200 offset:64576\n"
"v_accvgpr_write_b32 a67, v147\n"
"v_mfma_f32_16x16x32_bf16 v[76:79], a[192:195], v[4:7], v[76:79]\n"
"ds_read_b64_tr_b16 a[138:139], v201 offset:3648\n"
"v_accvgpr_write_b32 a68, v148\n"
"v_mfma_f32_16x16x32_bf16 v[72:75], a[196:199], v[8:11], v[72:75]\n"
"ds_read_b64_tr_b16 a[140:141], v201 offset:8256\n"
"v_accvgpr_write_b32 a69, v149\n"
"v_mfma_f32_16x16x32_bf16 v[76:79], a[196:199], v[12:15], v[76:79]\n"
"ds_read_b64_tr_b16 a[142:143], v201 offset:12864\n"
"v_cvt_pk_bf16_f32 v218, v64, v65\n"
"v_mfma_f32_16x16x32_bf16 v[72:75], a[200:203], v[16:19], v[72:75]\n"
"ds_read_b64_tr_b16 a[144:145], v201 offset:17472\n"
"v_cvt_pk_bf16_f32 v219, v66, v67\n"
"v_mfma_f32_16x16x32_bf16 v[76:79], a[200:203], v[20:23], v[76:79]\n"
"s_waitcnt lgkmcnt(11)\n"
"ds_read_b64_tr_b16 a[146:147], v201 offset:22080\n"
"v_cvt_pk_bf16_f32 v222, v68, v69\n"
"v_mfma_f32_16x16x32_bf16 v[72:75], a[204:207], v[24:27], v[72:75]\n"
"s_add_u32 m0, s84, 131072\n"
"ds_read_b64_tr_b16 a[148:149], v201 offset:26688\n"
"v_cvt_pk_bf16_f32 v223, v70, v71\n"
"v_mfma_f32_16x16x32_bf16 v[76:79], a[204:207], v[28:31], v[76:79]\n"
"ds_read_b64_tr_b16 a[150:151], v201 offset:31296\n"
"buffer_load_dwordx4 v207, s[72:75], s50 offen offset:3072 lds\n"
"v_mfma_f32_16x16x32_bf16 v[72:75], a[208:211], v[32:35], v[72:75]\n"
"ds_read_b64_tr_b16 a[152:153], v201 offset:35904\n"
"v_accvgpr_write_b32 a70, v150\n"
"v_mfma_f32_16x16x32_bf16 v[76:79], a[208:211], v[36:39], v[76:79]\n"
"ds_read_b64_tr_b16 a[154:155], v201 offset:40512\n"
"v_accvgpr_write_b32 a71, v151\n"
"v_mfma_f32_16x16x32_bf16 v[72:75], a[212:215], v[40:43], v[72:75]\n"
"ds_read_b64_tr_b16 a[156:157], v201 offset:45120\n"
"v_accvgpr_write_b32 a72, v152\n"
"v_mfma_f32_16x16x32_bf16 v[76:79], a[212:215], v[44:47], v[76:79]\n"
"ds_read_b64_tr_b16 a[158:159], v201 offset:49728\n"
"v_accvgpr_write_b32 a73, v153\n"
"v_mfma_f32_16x16x32_bf16 v[72:75], a[216:219], v[48:51], v[72:75]\n"
"s_add_u32 m0, s84, 135168\n"
"ds_read_b64_tr_b16 a[192:193], v200 offset:46176\n"
"v_accvgpr_write_b32 a74, v154\n"
"v_mfma_f32_16x16x32_bf16 v[76:79], a[216:219], v[52:55], v[76:79]\n"
"s_waitcnt lgkmcnt(11)\n"
"ds_read_b64_tr_b16 a[194:195], v200 offset:50784\n"
"buffer_load_dwordx4 v207, s[72:75], s86 offen offset:0 lds\n"
"v_mfma_f32_16x16x32_bf16 v[72:75], a[220:223], v[56:59], v[72:75]\n"
"ds_read_b64_tr_b16 a[196:197], v200 offset:55392\n"
"v_accvgpr_write_b32 a75, v155\n"
"v_mfma_f32_16x16x32_bf16 v[76:79], a[220:223], v[60:63], v[76:79]\n"
"ds_read_b64_tr_b16 a[198:199], v200 offset:60000\n"
"v_accvgpr_write_b32 a76, v156\n"
"v_mfma_f32_16x16x32_bf16 v[80:83], a[128:131], v[0:3], v[80:83]\n"
"ds_read_b64_tr_b16 a[200:201], v200 offset:64608\n"
"v_accvgpr_write_b32 a77, v157\n"
"v_mfma_f32_16x16x32_bf16 v[84:87], a[128:131], v[4:7], v[84:87]\n"
"ds_read_b64_tr_b16 a[202:203], v201 offset:3680\n"
"v_accvgpr_write_b32 a78, v158\n"
"v_mfma_f32_16x16x32_bf16 v[80:83], a[132:135], v[8:11], v[80:83]\n"
"ds_read_b64_tr_b16 a[204:205], v201 offset:8288\n"
"v_accvgpr_write_b32 a79, v159\n"
"v_mfma_f32_16x16x32_bf16 v[84:87], a[132:135], v[12:15], v[84:87]\n"
"ds_read_b64_tr_b16 a[206:207], v201 offset:12896\n"
"v_cvt_pk_bf16_f32 v220, v72, v73\n"
"v_mfma_f32_16x16x32_bf16 v[80:83], a[136:139], v[16:19], v[80:83]\n"
"ds_read_b64_tr_b16 a[208:209], v201 offset:17504\n"
"v_cvt_pk_bf16_f32 v221, v74, v75\n"
"v_mfma_f32_16x16x32_bf16 v[84:87], a[136:139], v[20:23], v[84:87]\n"
"s_waitcnt lgkmcnt(11)\n"
"ds_read_b64_tr_b16 a[210:211], v201 offset:22112\n"
"v_cvt_pk_bf16_f32 v224, v76, v77\n"
"v_mfma_f32_16x16x32_bf16 v[80:83], a[140:143], v[24:27], v[80:83]\n"
"s_add_u32 m0, s84, 135168\n"
"ds_read_b64_tr_b16 a[212:213], v201 offset:26720\n"
"v_cvt_pk_bf16_f32 v225, v78, v79\n"
"v_mfma_f32_16x16x32_bf16 v[84:87], a[140:143], v[28:31], v[84:87]\n"
"ds_read_b64_tr_b16 a[214:215], v201 offset:31328\n"
"buffer_load_dwordx4 v207, s[72:75], s86 offen offset:1024 lds\n"
"v_mfma_f32_16x16x32_bf16 v[80:83], a[144:147], v[32:35], v[80:83]\n"
"ds_read_b64_tr_b16 a[216:217], v201 offset:35936\n"
"buffer_store_dwordx4 v[218:221], v207, s[28:31], s54 offen offset:0 sc1\n"
"v_mfma_f32_16x16x32_bf16 v[84:87], a[144:147], v[36:39], v[84:87]\n"
"ds_read_b64_tr_b16 a[218:219], v201 offset:40544\n"
"v_accvgpr_write_b32 a80, v160\n"
"v_mfma_f32_16x16x32_bf16 v[80:83], a[148:151], v[40:43], v[80:83]\n"
"ds_read_b64_tr_b16 a[220:221], v201 offset:45152\n"
"buffer_store_dwordx4 v[222:225], v208, s[28:31], s54 offen offset:0 sc1\n"
"v_mfma_f32_16x16x32_bf16 v[84:87], a[148:151], v[44:47], v[84:87]\n"
"ds_read_b64_tr_b16 a[222:223], v201 offset:49760\n"
"v_accvgpr_write_b32 a81, v161\n"
"v_mfma_f32_16x16x32_bf16 v[80:83], a[152:155], v[48:51], v[80:83]\n"
"s_add_u32 m0, s84, 135168\n"
"ds_read_b64_tr_b16 a[128:129], v200 offset:46208\n"
"v_accvgpr_write_b32 a82, v162\n"
"v_mfma_f32_16x16x32_bf16 v[84:87], a[152:155], v[52:55], v[84:87]\n"
"s_waitcnt lgkmcnt(11)\n"
"ds_read_b64_tr_b16 a[130:131], v200 offset:50816\n"
"buffer_load_dwordx4 v207, s[72:75], s86 offen offset:2048 lds\n"
"v_mfma_f32_16x16x32_bf16 v[80:83], a[156:159], v[56:59], v[80:83]\n"
"ds_read_b64_tr_b16 a[132:133], v200 offset:55424\n"
"v_accvgpr_write_b32 a83, v163\n"
"v_mfma_f32_16x16x32_bf16 v[84:87], a[156:159], v[60:63], v[84:87]\n"
"ds_read_b64_tr_b16 a[134:135], v200 offset:60032\n"
"v_accvgpr_write_b32 a84, v164\n"
"v_mfma_f32_16x16x32_bf16 v[88:91], a[192:195], v[0:3], v[88:91]\n"
"ds_read_b64_tr_b16 a[136:137], v200 offset:64640\n"
"v_accvgpr_write_b32 a85, v165\n"
"v_mfma_f32_16x16x32_bf16 v[92:95], a[192:195], v[4:7], v[92:95]\n"
"ds_read_b64_tr_b16 a[138:139], v201 offset:3712\n"
"v_accvgpr_write_b32 a86, v166\n"
"v_mfma_f32_16x16x32_bf16 v[88:91], a[196:199], v[8:11], v[88:91]\n"
"ds_read_b64_tr_b16 a[140:141], v201 offset:8320\n"
"v_accvgpr_write_b32 a87, v167\n"
"v_mfma_f32_16x16x32_bf16 v[92:95], a[196:199], v[12:15], v[92:95]\n"
"ds_read_b64_tr_b16 a[142:143], v201 offset:12928\n"
"v_cvt_pk_bf16_f32 v218, v80, v81\n"
"v_mfma_f32_16x16x32_bf16 v[88:91], a[200:203], v[16:19], v[88:91]\n"
"ds_read_b64_tr_b16 a[144:145], v201 offset:17536\n"
"v_cvt_pk_bf16_f32 v219, v82, v83\n"
"v_mfma_f32_16x16x32_bf16 v[92:95], a[200:203], v[20:23], v[92:95]\n"
"s_waitcnt lgkmcnt(11)\n"
"ds_read_b64_tr_b16 a[146:147], v201 offset:22144\n"
"v_cvt_pk_bf16_f32 v222, v84, v85\n"
"v_mfma_f32_16x16x32_bf16 v[88:91], a[204:207], v[24:27], v[88:91]\n"
"ds_read_b64_tr_b16 a[148:149], v201 offset:26752\n"
"v_cvt_pk_bf16_f32 v223, v86, v87\n"
"v_mfma_f32_16x16x32_bf16 v[92:95], a[204:207], v[28:31], v[92:95]\n"
"ds_read_b64_tr_b16 a[150:151], v201 offset:31360\n"
"v_accvgpr_write_b32 a88, v168\n"
"v_mfma_f32_16x16x32_bf16 v[88:91], a[208:211], v[32:35], v[88:91]\n"
"ds_read_b64_tr_b16 a[152:153], v201 offset:35968\n"
"v_accvgpr_write_b32 a89, v169\n"
"v_mfma_f32_16x16x32_bf16 v[92:95], a[208:211], v[36:39], v[92:95]\n"
"ds_read_b64_tr_b16 a[154:155], v201 offset:40576\n"
"v_accvgpr_write_b32 a90, v170\n"
"v_mfma_f32_16x16x32_bf16 v[88:91], a[212:215], v[40:43], v[88:91]\n"
"ds_read_b64_tr_b16 a[156:157], v201 offset:45184\n"
"v_accvgpr_write_b32 a91, v171\n"
"v_mfma_f32_16x16x32_bf16 v[92:95], a[212:215], v[44:47], v[92:95]\n"
"ds_read_b64_tr_b16 a[158:159], v201 offset:49792\n"
"v_accvgpr_write_b32 a92, v172\n"
"v_mfma_f32_16x16x32_bf16 v[88:91], a[216:219], v[48:51], v[88:91]\n"
"ds_read_b64_tr_b16 a[192:193], v200 offset:46240\n"
"v_accvgpr_write_b32 a93, v173\n"
"v_mfma_f32_16x16x32_bf16 v[92:95], a[216:219], v[52:55], v[92:95]\n"
"s_waitcnt lgkmcnt(11)\n"
"ds_read_b64_tr_b16 a[194:195], v200 offset:50848\n"
"v_accvgpr_write_b32 a94, v174\n"
"v_mfma_f32_16x16x32_bf16 v[88:91], a[220:223], v[56:59], v[88:91]\n"
"ds_read_b64_tr_b16 a[196:197], v200 offset:55456\n"
"v_accvgpr_write_b32 a95, v175\n"
"v_mfma_f32_16x16x32_bf16 v[92:95], a[220:223], v[60:63], v[92:95]\n"
"ds_read_b64_tr_b16 a[198:199], v200 offset:60064\n"
"v_accvgpr_write_b32 a96, v176\n"
"v_mfma_f32_16x16x32_bf16 v[96:99], a[128:131], v[0:3], v[96:99]\n"
"ds_read_b64_tr_b16 a[200:201], v200 offset:64672\n"
"v_accvgpr_write_b32 a97, v177\n"
"v_mfma_f32_16x16x32_bf16 v[100:103], a[128:131], v[4:7], v[100:103]\n"
"ds_read_b64_tr_b16 a[202:203], v201 offset:3744\n"
"v_accvgpr_write_b32 a98, v178\n"
"v_mfma_f32_16x16x32_bf16 v[96:99], a[132:135], v[8:11], v[96:99]\n"
"ds_read_b64_tr_b16 a[204:205], v201 offset:8352\n"
"v_accvgpr_write_b32 a99, v179\n"
"v_mfma_f32_16x16x32_bf16 v[100:103], a[132:135], v[12:15], v[100:103]\n"
"ds_read_b64_tr_b16 a[206:207], v201 offset:12960\n"
"v_cvt_pk_bf16_f32 v220, v88, v89\n"
"v_mfma_f32_16x16x32_bf16 v[96:99], a[136:139], v[16:19], v[96:99]\n"
"ds_read_b64_tr_b16 a[208:209], v201 offset:17568\n"
"v_cvt_pk_bf16_f32 v221, v90, v91\n"
"v_mfma_f32_16x16x32_bf16 v[100:103], a[136:139], v[20:23], v[100:103]\n"
"s_waitcnt lgkmcnt(11)\n"
"ds_read_b64_tr_b16 a[210:211], v201 offset:22176\n"
"v_cvt_pk_bf16_f32 v224, v92, v93\n"
"v_mfma_f32_16x16x32_bf16 v[96:99], a[140:143], v[24:27], v[96:99]\n"
"ds_read_b64_tr_b16 a[212:213], v201 offset:26784\n"
"v_cvt_pk_bf16_f32 v225, v94, v95\n"
"v_mfma_f32_16x16x32_bf16 v[100:103], a[140:143], v[28:31], v[100:103]\n"
"ds_read_b64_tr_b16 a[214:215], v201 offset:31392\n"
"v_accvgpr_write_b32 a100, v180\n"
"v_mfma_f32_16x16x32_bf16 v[96:99], a[144:147], v[32:35], v[96:99]\n"
"ds_read_b64_tr_b16 a[216:217], v201 offset:36000\n"
"buffer_store_dwordx4 v[218:221], v207, s[28:31], s54 offen offset:1024 sc1\n"
"v_mfma_f32_16x16x32_bf16 v[100:103], a[144:147], v[36:39], v[100:103]\n"
"ds_read_b64_tr_b16 a[218:219], v201 offset:40608\n"
"buffer_store_dword v252, v251, s[28:31], 0 offen sc1\n"
"v_mfma_f32_16x16x32_bf16 v[96:99], a[148:151], v[40:43], v[96:99]\n"
"ds_read_b64_tr_b16 a[220:221], v201 offset:45216\n"
"buffer_store_dwordx4 v[222:225], v208, s[28:31], s54 offen offset:1024 sc1\n"
"v_mfma_f32_16x16x32_bf16 v[100:103], a[148:151], v[44:47], v[100:103]\n"
"ds_read_b64_tr_b16 a[222:223], v201 offset:49824\n"
"v_accvgpr_write_b32 a101, v181\n"
"v_mfma_f32_16x16x32_bf16 v[96:99], a[152:155], v[48:51], v[96:99]\n"
"ds_read_b64_tr_b16 a[128:129], v200 offset:46272\n"
"v_accvgpr_write_b32 a102, v182\n"
"v_mfma_f32_16x16x32_bf16 v[100:103], a[152:155], v[52:55], v[100:103]\n"
"s_waitcnt lgkmcnt(11)\n"
"ds_read_b64_tr_b16 a[130:131], v200 offset:50880\n"
"v_accvgpr_write_b32 a103, v183\n"
"v_mfma_f32_16x16x32_bf16 v[96:99], a[156:159], v[56:59], v[96:99]\n"
"ds_read_b64_tr_b16 a[132:133], v200 offset:55488\n"
"v_accvgpr_write_b32 a104, v184\n"
"v_mfma_f32_16x16x32_bf16 v[100:103], a[156:159], v[60:63], v[100:103]\n"
"ds_read_b64_tr_b16 a[134:135], v200 offset:60096\n"
"v_accvgpr_write_b32 a105, v185\n"
"v_mfma_f32_16x16x32_bf16 v[104:107], a[192:195], v[0:3], v[104:107]\n"
"ds_read_b64_tr_b16 a[136:137], v200 offset:64704\n"
"v_accvgpr_write_b32 a106, v186\n"
"v_mfma_f32_16x16x32_bf16 v[108:111], a[192:195], v[4:7], v[108:111]\n"
"ds_read_b64_tr_b16 a[138:139], v201 offset:3776\n"
"v_accvgpr_write_b32 a107, v187\n"
"v_mfma_f32_16x16x32_bf16 v[104:107], a[196:199], v[8:11], v[104:107]\n"
"ds_read_b64_tr_b16 a[140:141], v201 offset:8384\n"
"v_accvgpr_write_b32 a108, v188\n"
"v_mfma_f32_16x16x32_bf16 v[108:111], a[196:199], v[12:15], v[108:111]\n"
"ds_read_b64_tr_b16 a[142:143], v201 offset:12992\n"
"v_cvt_pk_bf16_f32 v218, v96, v97\n"
"v_mfma_f32_16x16x32_bf16 v[104:107], a[200:203], v[16:19], v[104:107]\n"
"ds_read_b64_tr_b16 a[144:145], v201 offset:17600\n"
"v_cvt_pk_bf16_f32 v219, v98, v99\n"
"v_mfma_f32_16x16x32_bf16 v[108:111], a[200:203], v[20:23], v[108:111]\n"
"s_waitcnt lgkmcnt(11)\n"
"ds_read_b64_tr_b16 a[146:147], v201 offset:22208\n"
"v_cvt_pk_bf16_f32 v222, v100, v101\n"
"v_mfma_f32_16x16x32_bf16 v[104:107], a[204:207], v[24:27], v[104:107]\n"
"ds_read_b64_tr_b16 a[148:149], v201 offset:26816\n"
"v_cvt_pk_bf16_f32 v223, v102, v103\n"
"v_mfma_f32_16x16x32_bf16 v[108:111], a[204:207], v[28:31], v[108:111]\n"
"ds_read_b64_tr_b16 a[150:151], v201 offset:31424\n"
"v_accvgpr_write_b32 a109, v189\n"
"v_mfma_f32_16x16x32_bf16 v[104:107], a[208:211], v[32:35], v[104:107]\n"
"s_waitcnt vmcnt(14)\n"
"s_barrier\n"
"ds_read_b64_tr_b16 a[152:153], v201 offset:36032\n"
"v_accvgpr_write_b32 a110, v190\n"
"v_mfma_f32_16x16x32_bf16 v[108:111], a[208:211], v[36:39], v[108:111]\n"
"ds_read_b64_tr_b16 a[154:155], v201 offset:40640\n"
"v_accvgpr_write_b32 a111, v191\n"
"v_mfma_f32_16x16x32_bf16 v[104:107], a[212:215], v[40:43], v[104:107]\n"
"ds_read_b64_tr_b16 a[156:157], v201 offset:45248\n"
"v_accvgpr_write_b32 a112, v192\n"
"v_mfma_f32_16x16x32_bf16 v[108:111], a[212:215], v[44:47], v[108:111]\n"
"ds_read_b64_tr_b16 a[158:159], v201 offset:49856\n"
"v_accvgpr_write_b32 a113, v193\n"
"v_mfma_f32_16x16x32_bf16 v[104:107], a[216:219], v[48:51], v[104:107]\n"
"ds_read_b64_tr_b16 a[192:193], v200 offset:46304\n"
"v_accvgpr_write_b32 a114, v194\n"
"v_mfma_f32_16x16x32_bf16 v[108:111], a[216:219], v[52:55], v[108:111]\n"
"s_waitcnt lgkmcnt(11)\n"
"ds_read_b64_tr_b16 a[194:195], v200 offset:50912\n"
"v_accvgpr_write_b32 a115, v195\n"
"v_mfma_f32_16x16x32_bf16 v[104:107], a[220:223], v[56:59], v[104:107]\n"
"ds_read_b64_tr_b16 a[196:197], v200 offset:55520\n"
"v_accvgpr_write_b32 a116, v196\n"
"v_mfma_f32_16x16x32_bf16 v[108:111], a[220:223], v[60:63], v[108:111]\n"
"ds_read_b64_tr_b16 a[198:199], v200 offset:60128\n"
"v_accvgpr_write_b32 a117, v197\n"
"v_mfma_f32_16x16x32_bf16 v[112:115], a[128:131], v[0:3], v[112:115]\n"
"ds_read_b64_tr_b16 a[200:201], v200 offset:64736\n"
"v_accvgpr_write_b32 a118, v198\n"
"v_mfma_f32_16x16x32_bf16 v[116:119], a[128:131], v[4:7], v[116:119]\n"
"ds_read_b64_tr_b16 a[202:203], v201 offset:3808\n"
"v_accvgpr_write_b32 a119, v199\n"
"v_mfma_f32_16x16x32_bf16 v[112:115], a[132:135], v[8:11], v[112:115]\n"
"ds_read_b64_tr_b16 a[204:205], v201 offset:8416\n"
"v_mfma_f32_16x16x32_bf16 v[116:119], a[132:135], v[12:15], v[116:119]\n"
"ds_read_b64_tr_b16 a[206:207], v201 offset:13024\n"
"v_cvt_pk_bf16_f32 v220, v104, v105\n"
"v_mfma_f32_16x16x32_bf16 v[112:115], a[136:139], v[16:19], v[112:115]\n"
"ds_read_b64_tr_b16 a[208:209], v201 offset:17632\n"
"v_cvt_pk_bf16_f32 v221, v106, v107\n"
"v_mfma_f32_16x16x32_bf16 v[116:119], a[136:139], v[20:23], v[116:119]\n"
"s_waitcnt lgkmcnt(11)\n"
"ds_read_b64_tr_b16 a[210:211], v201 offset:22240\n"
"v_cvt_pk_bf16_f32 v224, v108, v109\n"
"v_mfma_f32_16x16x32_bf16 v[112:115], a[140:143], v[24:27], v[112:115]\n"
"ds_read_b64_tr_b16 a[212:213], v201 offset:26848\n"
"v_cvt_pk_bf16_f32 v225, v110, v111\n"
"v_mfma_f32_16x16x32_bf16 v[116:119], a[140:143], v[28:31], v[116:119]\n"
"ds_read_b64_tr_b16 a[214:215], v201 offset:31456\n"
"v_mfma_f32_16x16x32_bf16 v[112:115], a[144:147], v[32:35], v[112:115]\n"
"ds_read_b64_tr_b16 a[216:217], v201 offset:36064\n"
"buffer_store_dwordx4 v[218:221], v207, s[28:31], s54 offen offset:2048 sc1\n"
"v_mfma_f32_16x16x32_bf16 v[116:119], a[144:147], v[36:39], v[116:119]\n"
"ds_read_b64_tr_b16 a[218:219], v201 offset:40672\n"
"v_mfma_f32_16x16x32_bf16 v[112:115], a[148:151], v[40:43], v[112:115]\n"
"ds_read_b64_tr_b16 a[220:221], v201 offset:45280\n"
"buffer_store_dwordx4 v[222:225], v208, s[28:31], s54 offen offset:2048 sc1\n"
"v_mfma_f32_16x16x32_bf16 v[116:119], a[148:151], v[44:47], v[116:119]\n"
"ds_read_b64_tr_b16 a[222:223], v201 offset:49888\n"
"v_mfma_f32_16x16x32_bf16 v[112:115], a[152:155], v[48:51], v[112:115]\n"
"s_nop 0\n"
"v_mfma_f32_16x16x32_bf16 v[116:119], a[152:155], v[52:55], v[116:119]\n"
"s_waitcnt lgkmcnt(10)\n"
"v_mfma_f32_16x16x32_bf16 v[112:115], a[156:159], v[56:59], v[112:115]\n"
"s_nop 0\n"
"v_mfma_f32_16x16x32_bf16 v[116:119], a[156:159], v[60:63], v[116:119]\n"
"s_nop 0\n"
"v_mfma_f32_16x16x32_bf16 v[120:123], a[192:195], v[0:3], v[120:123]\n"
"s_nop 0\n"
"v_mfma_f32_16x16x32_bf16 v[124:127], a[192:195], v[4:7], v[124:127]\n"
"s_nop 0\n"
"v_mfma_f32_16x16x32_bf16 v[120:123], a[196:199], v[8:11], v[120:123]\n"
"s_nop 0\n"
"v_mfma_f32_16x16x32_bf16 v[124:127], a[196:199], v[12:15], v[124:127]\n"
"v_cvt_pk_bf16_f32 v218, v112, v113\n"
"v_mfma_f32_16x16x32_bf16 v[120:123], a[200:203], v[16:19], v[120:123]\n"
"v_cvt_pk_bf16_f32 v219, v114, v115\n"
"v_mfma_f32_16x16x32_bf16 v[124:127], a[200:203], v[20:23], v[124:127]\n"
"s_waitcnt lgkmcnt(2)\n"
"v_cvt_pk_bf16_f32 v222, v116, v117\n"
"v_mfma_f32_16x16x32_bf16 v[120:123], a[204:207], v[24:27], v[120:123]\n"
"v_cvt_pk_bf16_f32 v223, v118, v119\n"
"v_mfma_f32_16x16x32_bf16 v[124:127], a[204:207], v[28:31], v[124:127]\n"
"s_nop 0\n"
"v_mfma_f32_16x16x32_bf16 v[120:123], a[208:211], v[32:35], v[120:123]\n"
"s_nop 0\n"
"v_mfma_f32_16x16x32_bf16 v[124:127], a[208:211], v[36:39], v[124:127]\n"
"s_nop 0\n"
"v_mfma_f32_16x16x32_bf16 v[120:123], a[212:215], v[40:43], v[120:123]\n"
"s_nop 0\n"
"v_mfma_f32_16x16x32_bf16 v[124:127], a[212:215], v[44:47], v[124:127]\n"
"s_nop 0\n"
"v_mfma_f32_16x16x32_bf16 v[120:123], a[216:219], v[48:51], v[120:123]\n"
"s_nop 0\n"
"v_mfma_f32_16x16x32_bf16 v[124:127], a[216:219], v[52:55], v[124:127]\n"
"s_waitcnt lgkmcnt(0)\n"
"v_mfma_f32_16x16x32_bf16 v[120:123], a[220:223], v[56:59], v[120:123]\n"
"s_nop 0\n"
"v_mfma_f32_16x16x32_bf16 v[124:127], a[220:223], v[60:63], v[124:127]\n"
"s_nop 0\n"
"s_nop 1\n"
"s_nop 1\n"
"s_nop 1\n"
"s_nop 1\n"
"v_cvt_pk_bf16_f32 v220, v120, v121\n"
"s_nop 1\n"
"v_cvt_pk_bf16_f32 v221, v122, v123\n"
"s_nop 1\n"
"v_cvt_pk_bf16_f32 v224, v124, v125\n"
"s_nop 1\n"
"v_cvt_pk_bf16_f32 v225, v126, v127\n"
"s_nop 1\n"
"s_nop 1\n"
"buffer_store_dwordx4 v[218:221], v207, s[28:31], s54 offen offset:3072 sc1\n"
"s_nop 1\n"
"s_nop 1\n"
"buffer_store_dwordx4 v[222:225], v208, s[28:31], s54 offen offset:3072 sc1\n"
"s_nop 1\n"
"s_add_u32 s56, s40, 161024\n"
"s_nop 1\n"
"v_mov_b32 v227, s56\n"
"v_mov_b32 v224, 0x3f80\n"
"v_mov_b32 v225, 0x3f800000\n"
"v_and_b32 v226, 7, v248\n"
"v_lshl_add_u32 v226, v226, 4, v227\n"
"s_mov_b32 s14, 0xfffff800\n"
"s_cmp_lg_u32 s41, 3\n"
"s_cbranch_scc1 AT_A0\n"
"s_mov_b64 s[8:9], 0\n"
"v_mov_b32 v224, 0xffff0000\n"
"v_accvgpr_read_b32 v1, a48\n"
"v_accvgpr_read_b32 v3, a49\n"
"v_accvgpr_read_b32 v5, a50\n"
"v_accvgpr_read_b32 v7, a51\n"
"v_lshlrev_b32 v0, 16, v1\n"
"v_and_b32 v1, v224, v1\n"
"v_lshlrev_b32 v2, 16, v3\n"
"v_and_b32 v3, v224, v3\n"
"v_lshlrev_b32 v4, 16, v5\n"
"v_and_b32 v5, v224, v5\n"
"v_lshlrev_b32 v6, 16, v7\n"
"v_and_b32 v7, v224, v7\n"
"v_accvgpr_read_b32 v9, a84\n"
"v_accvgpr_read_b32 v11, a85\n"
"v_accvgpr_read_b32 v13, a86\n"
"v_accvgpr_read_b32 v15, a87\n"
"v_lshlrev_b32 v8, 16, v9\n"
"v_and_b32 v9, v224, v9\n"
"v_lshlrev_b32 v10, 16, v11\n"
"v_and_b32 v11, v224, v11\n"
"v_lshlrev_b32 v12, 16, v13\n"
"v_and_b32 v13, v224, v13\n"
"v_lshlrev_b32 v14, 16, v15\n"
"v_and_b32 v15, v224, v15\n"
"v_accvgpr_read_b32 v17, a52\n"
"v_accvgpr_read_b32 v19, a53\n"
"v_accvgpr_read_b32 v21, a54\n"
"v_accvgpr_read_b32 v23, a55\n"
"v_lshlrev_b32 v16, 16, v17\n"
"v_and_b32 v17, v224, v17\n"
"v_lshlrev_b32 v18, 16, v19\n"
"v_and_b32 v19, v224, v19\n"
"v_lshlrev_b32 v20, 16, v21\n"
"v_and_b32 v21, v224, v21\n"
"v_lshlrev_b32 v22, 16, v23\n"
"v_and_b32 v23, v224, v23\n"
"v_accvgpr_read_b32 v25, a88\n"
"v_accvgpr_read_b32 v27, a89\n"
"v_accvgpr_read_b32 v29, a90\n"
"v_accvgpr_read_b32 v31, a91\n"
"v_lshlrev_b32 v24, 16, v25\n"
"v_and_b32 v25, v224, v25\n"
"v_lshlrev_b32 v26, 16, v27\n"
"v_and_b32 v27, v224, v27\n"
"v_lshlrev_b32 v28, 16, v29\n"
"v_and_b32 v29, v224, v29\n"
"v_lshlrev_b32 v30, 16, v31\n"
"v_and_b32 v31, v224, v31\n"
"v_accvgpr_read_b32 v33, a56\n"
"v_accvgpr_read_b32 v35, a57\n"
"v_accvgpr_read_b32 v37, a58\n"
"v_accvgpr_read_b32 v39, a59\n"
"v_lshlrev_b32 v32, 16, v33\n"
"v_and_b32 v33, v224, v33\n"
"v_lshlrev_b32 v34, 16, v35\n"
"v_and_b32 v35, v224, v35\n"
"v_lshlrev_b32 v36, 16, v37\n"
"v_and_b32 v37, v224, v37\n"
"v_lshlrev_b32 v38, 16, v39\n"
"v_and_b32 v39, v224, v39\n"
"v_accvgpr_read_b32 v41, a92\n"
"v_accvgpr_read_b32 v43, a93\n"
"v_accvgpr_read_b32 v45, a94\n"
"v_accvgpr_read_b32 v47, a95\n"
"v_lshlrev_b32 v40, 16, v41\n"
"v_and_b32 v41, v224, v41\n"
"v_lshlrev_b32 v42, 16, v43\n"
"v_and_b32 v43, v224, v43\n"
"v_lshlrev_b32 v44, 16, v45\n"
"v_and_b32 v45, v224, v45\n"
"v_lshlrev_b32 v46, 16, v47\n"
"v_and_b32 v47, v224, v47\n"
"v_accvgpr_read_b32 v49, a60\n"
"v_accvgpr_read_b32 v51, a61\n"
"v_accvgpr_read_b32 v53, a62\n"
"v_accvgpr_read_b32 v55, a63\n"
"v_lshlrev_b32 v48, 16, v49\n"
"v_and_b32 v49, v224, v49\n"
"v_lshlrev_b32 v50, 16, v51\n"
"v_and_b32 v51, v224, v51\n"
"v_lshlrev_b32 v52, 16, v53\n"
"v_and_b32 v53, v224, v53\n"
"v_lshlrev_b32 v54, 16, v55\n"
"v_and_b32 v55, v224, v55\n"
"v_accvgpr_read_b32 v57, a96\n"
"v_accvgpr_read_b32 v59, a97\n"
"v_accvgpr_read_b32 v61, a98\n"
"v_accvgpr_read_b32 v63, a99\n"
"v_lshlrev_b32 v56, 16, v57\n"
"v_and_b32 v57, v224, v57\n"
"v_lshlrev_b32 v58, 16, v59\n"
"v_and_b32 v59, v224, v59\n"
"v_lshlrev_b32 v60, 16, v61\n"
"v_and_b32 v61, v224, v61\n"
"v_lshlrev_b32 v62, 16, v63\n"
"v_and_b32 v63, v224, v63\n"
"v_accvgpr_read_b32 v65, a64\n"
"v_accvgpr_read_b32 v67, a65\n"
"v_accvgpr_read_b32 v69, a66\n"
"v_accvgpr_read_b32 v71, a67\n"
"v_lshlrev_b32 v64, 16, v65\n"
"v_and_b32 v65, v224, v65\n"
"v_lshlrev_b32 v66, 16, v67\n"
"v_and_b32 v67, v224, v67\n"
"v_lshlrev_b32 v68, 16, v69\n"
"v_and_b32 v69, v224, v69\n"
"v_lshlrev_b32 v70, 16, v71\n"
"v_and_b32 v71, v224, v71\n"
"v_accvgpr_read_b32 v73, a100\n"
"v_accvgpr_read_b32 v75, a101\n"
"v_accvgpr_read_b32 v77, a102\n"
"v_accvgpr_read_b32 v79, a103\n"
"v_lshlrev_b32 v72, 16, v73\n"
"v_and_b32 v73, v224, v73\n"
"v_lshlrev_b32 v74, 16, v75\n"
"v_and_b32 v75, v224, v75\n"
"v_lshlrev_b32 v76, 16, v77\n"
"v_and_b32 v77, v224, v77\n"
"v_lshlrev_b32 v78, 16, v79\n"
"v_and_b32 v79, v224, v79\n"
"v_accvgpr_read_b32 v81, a68\n"
"v_accvgpr_read_b32 v83, a69\n"
"v_accvgpr_read_b32 v85, a70\n"
"v_accvgpr_read_b32 v87, a71\n"
"v_lshlrev_b32 v80, 16, v81\n"
"v_and_b32 v81, v224, v81\n"
"v_lshlrev_b32 v82, 16, v83\n"
"v_and_b32 v83, v224, v83\n"
"v_lshlrev_b32 v84, 16, v85\n"
"v_and_b32 v85, v224, v85\n"
"v_lshlrev_b32 v86, 16, v87\n"
"v_and_b32 v87, v224, v87\n"
"v_accvgpr_read_b32 v89, a104\n"
"v_accvgpr_read_b32 v91, a105\n"
"v_accvgpr_read_b32 v93, a106\n"
"v_accvgpr_read_b32 v95, a107\n"
"v_lshlrev_b32 v88, 16, v89\n"
"v_and_b32 v89, v224, v89\n"
"v_lshlrev_b32 v90, 16, v91\n"
"v_and_b32 v91, v224, v91\n"
"v_lshlrev_b32 v92, 16, v93\n"
"v_and_b32 v93, v224, v93\n"
"v_lshlrev_b32 v94, 16, v95\n"
"v_and_b32 v95, v224, v95\n"
"v_accvgpr_read_b32 v97, a72\n"
"v_accvgpr_read_b32 v99, a73\n"
"v_accvgpr_read_b32 v101, a74\n"
"v_accvgpr_read_b32 v103, a75\n"
"v_lshlrev_b32 v96, 16, v97\n"
"v_and_b32 v97, v224, v97\n"
"v_lshlrev_b32 v98, 16, v99\n"
"v_and_b32 v99, v224, v99\n"
"v_lshlrev_b32 v100, 16, v101\n"
"v_and_b32 v101, v224, v101\n"
"v_lshlrev_b32 v102, 16, v103\n"
"v_and_b32 v103, v224, v103\n"
"v_accvgpr_read_b32 v105, a108\n"
"v_accvgpr_read_b32 v107, a109\n"
"v_accvgpr_read_b32 v109, a110\n"
"v_accvgpr_read_b32 v111, a111\n"
"v_lshlrev_b32 v104, 16, v105\n"
"v_and_b32 v105, v224, v105\n"
"v_lshlrev_b32 v106, 16, v107\n"
"v_and_b32 v107, v224, v107\n"
"v_lshlrev_b32 v108, 16, v109\n"
"v_and_b32 v109, v224, v109\n"
"v_lshlrev_b32 v110, 16, v111\n"
"v_and_b32 v111, v224, v111\n"
"v_accvgpr_read_b32 v113, a76\n"
"v_accvgpr_read_b32 v115, a77\n"
"v_accvgpr_read_b32 v117, a78\n"
"v_accvgpr_read_b32 v119, a79\n"
"v_lshlrev_b32 v112, 16, v113\n"
"v_and_b32 v113, v224, v113\n"
"v_lshlrev_b32 v114, 16, v115\n"
"v_and_b32 v115, v224, v115\n"
"v_lshlrev_b32 v116, 16, v117\n"
"v_and_b32 v117, v224, v117\n"
"v_lshlrev_b32 v118, 16, v119\n"
"v_and_b32 v119, v224, v119\n"
"v_accvgpr_read_b32 v121, a112\n"
"v_accvgpr_read_b32 v123, a113\n"
"v_accvgpr_read_b32 v125, a114\n"
"v_accvgpr_read_b32 v127, a115\n"
"v_lshlrev_b32 v120, 16, v121\n"
"v_and_b32 v121, v224, v121\n"
"v_lshlrev_b32 v122, 16, v123\n"
"v_and_b32 v123, v224, v123\n"
"v_lshlrev_b32 v124, 16, v125\n"
"v_and_b32 v125, v224, v125\n"
"v_lshlrev_b32 v126, 16, v127\n"
"v_and_b32 v127, v224, v127\n"
"v_accvgpr_read_b32 v129, a80\n"
"v_accvgpr_read_b32 v131, a81\n"
"v_accvgpr_read_b32 v133, a82\n"
"v_accvgpr_read_b32 v135, a83\n"
"v_lshlrev_b32 v128, 16, v129\n"
"v_and_b32 v129, v224, v129\n"
"v_lshlrev_b32 v130, 16, v131\n"
"v_and_b32 v131, v224, v131\n"
"v_lshlrev_b32 v132, 16, v133\n"
"v_and_b32 v133, v224, v133\n"
"v_lshlrev_b32 v134, 16, v135\n"
"v_and_b32 v135, v224, v135\n"
"v_accvgpr_read_b32 v137, a116\n"
"v_accvgpr_read_b32 v139, a117\n"
"v_accvgpr_read_b32 v141, a118\n"
"v_accvgpr_read_b32 v143, a119\n"
"v_lshlrev_b32 v136, 16, v137\n"
"v_and_b32 v137, v224, v137\n"
"v_lshlrev_b32 v138, 16, v139\n"
"v_and_b32 v139, v224, v139\n"
"v_lshlrev_b32 v140, 16, v141\n"
"v_and_b32 v141, v224, v141\n"
"v_lshlrev_b32 v142, 16, v143\n"
"v_and_b32 v143, v224, v143\n"
"v_lshrrev_b32 v242, 2, v248\n"
"v_lshlrev_b32 v241, 4, v242\n"
"v_lshlrev_b32 v242, 3, v242\n"
"v_lshrrev_b32 v243, 6, v254\n"
"v_mul_u32_u24 v243, 24576, v243\n"
"v_and_b32 v247, 7, v248\n"
"v_lshl_add_u32 v243, v247, 4, v243\n"
"v_and_b32 v247, 3, v248\n"
"v_and_b32 v244, 0x10000000, v254\n"
"v_or_b32 v243, v244, v243\n"
"v_lshrrev_b32 v244, 5, v248\n"
"v_mul_u32_u24 v244, 96, v244\n"
"v_and_b32 v246, 16, v248\n"
"v_subrev_u32 v252, 65536, v227\n"
"v_add3_u32 v244, v244, v246, v252\n"
"v_lshl_add_u32 v244, v247, 5, v244\n"
"s_mul_i32 s63, s41, 384\n"
"v_add_u32 v244, s63, v244\n"
"v_lshl_add_u32 v249, v248, 2, v252\n"
"s_lshl_b32 s63, s41, 8\n"
"v_add_u32 v251, s63, v249\n"
"v_add_u32 v252, s63, v252\n"
"s_add_u32 s63, s40, 131072\n"
"v_lshl_add_u32 v255, v253, 3, s63\n"
"s_min_u32 s63, s41, 2\n"
"s_mul_i32 s63, s63, 192\n"
"v_add_u32 v183, s63, v248\n"
"v_mul_u32_u24 v183, 911, v183\n"
"s_lshl_b32 s63, s55, 3\n"
"v_lshrrev_b32 v180, 16, v183\n"
"v_add_u32 v180, s63, v180\n"
"v_add_u32 v183, 58304, v183\n"
"v_lshrrev_b32 v181, 16, v183\n"
"v_add_u32 v181, s63, v181\n"
"v_add_u32 v183, 58304, v183\n"
"v_lshrrev_b32 v182, 16, v183\n"
"v_add_u32 v182, s63, v182\n"
"v_lshlrev_b32 v250, 4, v248\n"
"v_add_u32 v184, s45, v250\n"
"s_cmp_eq_u32 s41, 3\n"
"s_cselect_b32 s63, 0x10000000, 0\n"
"v_or_b32 v250, s63, v250\n"
"ds_read_b128 v[212:215], v227 offset:512\n"
"ds_read_b128 v[216:219], v227 offset:528\n"
"s_lshl_b32 s56, s55, 3\n"
"s_sub_u32 s56, 2047, s56\n"
"v_and_b32 v220, 7, v248\n"
"v_sub_u32 v220, s56, v220\n"
"s_waitcnt lgkmcnt(0)\n"
"v_add_f32 v212, v212, v214\n"
"v_add_f32 v213, v213, v215\n"
"v_add_f32 v212, v212, v216\n"
"v_add_f32 v213, v213, v217\n"
"v_mov_b32 v145, s85\n"
"v_mov_b32 v147, s85\n"
"s_mul_i32 s56, s55, 64\n"
"s_add_u32 s56, s56, 26370048\n"
"v_mov_b32 v148, 0\n"
"v_mul_u32_u24 v149, 64, v248\n"
"s_mov_b32 s57, 26370048\n"
"s_mov_b32 s58, 26374144\n"
"s_mov_b32 s59, 26378240\n"
"s_mov_b32 s60, 26382336\n"
"s_mov_b32 s87, 0x80000\n"
"s_waitcnt vmcnt(0)\n"
"s_barrier\n"
"ds_read_b128 v[200:203], v226 offset:0\n"
"ds_read_b128 v[204:207], v226 offset:128\n"
"ds_read_b128 v[208:211], v226 offset:256\n"
"s_waitcnt lgkmcnt(1)\n"
"v_add_f32 v200, v200, v204\n"
"v_add_f32 v201, v201, v205\n"
"v_add_f32 v202, v202, v206\n"
"s_waitcnt lgkmcnt(0)\n"
"v_add_f32 v200, v200, v208\n"
"v_add_f32 v201, v201, v209\n"
"v_add_f32 v202, v202, v210\n"
"v_mul_f32 v200, 0x3ae38e39, v200\n"
"v_mul_f32 v201, 0x3ae38e39, v201\n"
"v_fma_f32 v201, -v200, v200, v201\n"
"v_max_f32 v201, 0, v201\n"
"v_add_f32 v201, 0x3727c5ac, v201\n"
"v_rsq_f32 v192, v201\n"
"v_fma_f32 v202, -v200, v212, v202\n"
"v_fma_f32 v196, v192, v202, v213\n"
"v_ashrrev_i32 v203, 31, v196\n"
"v_or_b32 v203, 0x80000000, v203\n"
"v_xor_b32 v196, v196, v203\n"
"v_and_or_b32 v196, v196, s14, v220\n"
"v_mul_f32 v194, v200, v192\n"
"v_sub_f32 v194, 0, v194\n"
"v_mov_b32_dpp v203, v196 row_ror:1 row_mask:0xf bank_mask:0xf\n"
"v_min_u32 v198, v196, v203\n"
"v_max_u32 v197, v196, v203\n"
"s_nop 0\n"
"v_mov_b32_dpp v204, v198 row_ror:2 row_mask:0xf bank_mask:0xf\n"
"v_mov_b32_dpp v203, v197 row_ror:2 row_mask:0xf bank_mask:0xf\n"
"v_min_u32 v205, v197, v203\n"
"v_max_u32 v197, v197, v203\n"
"v_max3_u32 v198, v198, v204, v205\n"
"s_nop 0\n"
"v_mov_b32_dpp v203, v197 row_ror:4 row_mask:0xf bank_mask:0xf\n"
"v_mov_b32_dpp v204, v198 row_ror:4 row_mask:0xf bank_mask:0xf\n"
"v_min_u32 v205, v197, v203\n"
"v_max_u32 v197, v197, v203\n"
"v_max3_u32 v198, v198, v204, v205\n"
"v_mov_b32 v144, v197\n"
"v_mov_b32 v146, v198\n"
"s_mov_b64 exec, 1\n"
"buffer_store_dwordx4 v[144:147], v148, s[28:31], s56 offen sc1\n"
"s_mov_b64 exec, -1\n"
"AT_S2:\n"
"buffer_load_dwordx4 v[152:155], v149, s[28:31], s57 offen sc1\n"
"buffer_load_dwordx4 v[156:159], v149, s[28:31], s58 offen sc1\n"
"buffer_load_dwordx4 v[160:163], v149, s[28:31], s59 offen sc1\n"
"buffer_load_dwordx4 v[164:167], v149, s[28:31], s60 offen sc1\n"
"s_waitcnt vmcnt(0)\n"
"v_xor_b32 v150, s85, v153\n"
"v_xor_b32 v151, s85, v155\n"
"v_bitop3_b32 v150, v157, s85, v150 bitop3:0xbe\n"
"v_bitop3_b32 v151, v159, s85, v151 bitop3:0xbe\n"
"v_bitop3_b32 v150, v161, s85, v150 bitop3:0xbe\n"
"v_bitop3_b32 v151, v163, s85, v151 bitop3:0xbe\n"
"v_bitop3_b32 v150, v165, s85, v150 bitop3:0xbe\n"
"v_bitop3_b32 v151, v167, s85, v151 bitop3:0xbe\n"
"v_or_b32 v150, v150, v151\n"
"v_cmp_ne_u32 vcc, 0, v150\n"
"s_nop 0\n"
"s_cbranch_vccz AT_S3\n"
"s_sub_u32 s87, s87, 1\n"
"s_cmp_eq_u32 s87, 0\n"
"s_cbranch_scc1 AT_S3\n"
"s_sleep 1\n"
"s_branch AT_S2\n"
"AT_S3:\n"
"v_min_u32 v205, v152, v156\n"
"v_max_u32 v152, v152, v156\n"
"v_max3_u32 v154, v154, v158, v205\n"
"v_min_u32 v205, v160, v164\n"
"v_max_u32 v160, v160, v164\n"
"v_max3_u32 v162, v162, v166, v205\n"
"v_min_u32 v205, v152, v160\n"
"v_max_u32 v152, v152, v160\n"
"v_max3_u32 v154, v154, v162, v205\n"
"s_nop 0\n"
"v_mov_b32_dpp v156, v152 row_ror:1 row_mask:0xf bank_mask:0xf\n"
"v_mov_b32_dpp v158, v154 row_ror:1 row_mask:0xf bank_mask:0xf\n"
"v_min_u32 v205, v152, v156\n"
"v_max_u32 v152, v152, v156\n"
"v_max3_u32 v154, v154, v158, v205\n"
"s_nop 0\n"
"v_mov_b32_dpp v156, v152 row_ror:2 row_mask:0xf bank_mask:0xf\n"
"v_mov_b32_dpp v158, v154 row_ror:2 row_mask:0xf bank_mask:0xf\n"
"v_min_u32 v205, v152, v156\n"
"v_max_u32 v152, v152, v156\n"
"v_max3_u32 v154, v154, v158, v205\n"
"s_nop 0\n"
"v_mov_b32_dpp v156, v152 row_ror:4 row_mask:0xf bank_mask:0xf\n"
"v_mov_b32_dpp v158, v154 row_ror:4 row_mask:0xf bank_mask:0xf\n"
"v_min_u32 v205, v152, v156\n"
"v_max_u32 v152, v152, v156\n"
"v_max3_u32 v154, v154, v158, v205\n"
"s_nop 0\n"
"v_mov_b32_dpp v156, v152 row_ror:8 row_mask:0xf bank_mask:0xf\n"
"v_mov_b32_dpp v158, v154 row_ror:8 row_mask:0xf bank_mask:0xf\n"
"v_min_u32 v205, v152, v156\n"
"v_max_u32 v152, v152, v156\n"
"v_max3_u32 v154, v154, v158, v205\n"
"v_mov_b32 v156, v152\n"
"v_mov_b32 v158, v154\n"
"s_nop 0\n"
"v_permlane16_swap_b32 v152, v156\n"
"v_permlane16_swap_b32 v154, v158\n"
"v_min_u32 v205, v152, v156\n"
"v_max_u32 v152, v152, v156\n"
"v_max3_u32 v154, v154, v158, v205\n"
"v_mov_b32 v156, v152\n"
"v_mov_b32 v158, v154\n"
"s_nop 0\n"
"v_permlane32_swap_b32 v152, v156\n"
"v_permlane32_swap_b32 v154, v158\n"
"v_min_u32 v205, v152, v156\n"
"v_max_u32 v152, v152, v156\n"
"v_max3_u32 v154, v154, v158, v205\n"
"v_mov_b32 v153, v154\n"
"s_mov_b64 exec, 1\n"
"ds_write_b64 v227, v[152:153] offset:656\n"
"s_mov_b64 exec, -1\n"
"s_branch AT_B3\n"
"AT_A0:\n"
"v_and_b32 v192, 15, v248\n"
"v_lshlrev_b32 v192, 6, v192\n"
"s_lshl_b32 s57, s44, 10\n"
"buffer_load_dword v193, v192, s[28:31], s57 offen sc1\n"
"ds_read_b64 v[228:229], v240 offset:0\n"
"ds_read_b64 v[230:231], v240 offset:32\n"
"ds_read_b64 v[232:233], v240 offset:384\n"
"ds_read_b64 v[234:235], v240 offset:416\n"
"ds_read_b64 v[236:237], v240 offset:768\n"
"ds_read_b64 v[238:239], v240 offset:800\n"
"ds_read_b128 v[96:99], v244 offset:0\n"
"ds_read_b128 v[100:103], v244 offset:64\n"
"ds_read_b128 v[104:107], v244 offset:768\n"
"ds_read_b128 v[108:111], v244 offset:832\n"
"ds_read_b128 v[112:115], v244 offset:1536\n"
"ds_read_b128 v[116:119], v244 offset:1600\n"
"ds_read_b128 v[120:123], v244 offset:2304\n"
"ds_read_b128 v[124:127], v244 offset:2368\n"
"ds_read_b128 v[128:131], v244 offset:3072\n"
"ds_read_b128 v[132:135], v244 offset:3136\n"
"ds_read_b128 v[136:139], v244 offset:3840\n"
"ds_read_b128 v[140:143], v244 offset:3904\n"
"v_mov_b32 v48, 0\n"
"v_mov_b32 v49, 0\n"
"v_mov_b32 v50, 0\n"
"v_mov_b32 v51, 0\n"
"v_mov_b32 v52, 0\n"
"v_mov_b32 v53, 0\n"
"v_mov_b32 v54, 0\n"
"v_mov_b32 v55, 0\n"
"v_mov_b32 v56, 0\n"
"v_mov_b32 v57, 0\n"
"v_mov_b32 v58, 0\n"
"v_mov_b32 v59, 0\n"
"v_mov_b32 v60, 0\n"
"v_mov_b32 v61, 0\n"
"v_mov_b32 v62, 0\n"
"v_mov_b32 v63, 0\n"
"v_mov_b32 v64, 0\n"
"v_mov_b32 v65, 0\n"
"v_mov_b32 v66, 0\n"
"v_mov_b32 v67, 0\n"
"v_mov_b32 v68, 0\n"
"v_mov_b32 v69, 0\n"
"v_mov_b32 v70, 0\n"
"v_mov_b32 v71, 0\n"
"v_mov_b32 v72, 0\n"
"v_mov_b32 v73, 0\n"
"v_mov_b32 v74, 0\n"
"v_mov_b32 v75, 0\n"
"v_mov_b32 v76, 0\n"
"v_mov_b32 v77, 0\n"
"v_mov_b32 v78, 0\n"
"v_mov_b32 v79, 0\n"
"v_mov_b32 v80, 0\n"
"v_mov_b32 v81, 0\n"
"v_mov_b32 v82, 0\n"
"v_mov_b32 v83, 0\n"
"v_mov_b32 v84, 0\n"
"v_mov_b32 v85, 0\n"
"v_mov_b32 v86, 0\n"
"v_mov_b32 v87, 0\n"
"v_mov_b32 v88, 0\n"
"v_mov_b32 v89, 0\n"
"v_mov_b32 v90, 0\n"
"v_mov_b32 v91, 0\n"
"v_mov_b32 v92, 0\n"
"v_mov_b32 v93, 0\n"
"v_mov_b32 v94, 0\n"
"v_mov_b32 v95, 0\n"
"ds_read_b128 v[212:215], v227 offset:512\n"
"ds_read_b128 v[216:219], v227 offset:528\n"
"s_lshl_b32 s56, s55, 3\n"
"s_sub_u32 s56, 2047, s56\n"
"v_and_b32 v220, 7, v248\n"
"v_sub_u32 v220, s56, v220\n"
"s_waitcnt lgkmcnt(0)\n"
"v_add_f32 v212, v212, v214\n"
"v_add_f32 v213, v213, v215\n"
"v_add_f32 v212, v212, v216\n"
"v_add_f32 v213, v213, v217\n"
"s_mul_i32 s56, s41, 1536\n"
"s_add_u32 s56, s56, s40\n"
"s_add_u32 s56, s56, 16384\n"
"v_and_b32 v168, 32, v248\n"
"v_lshl_add_u32 v168, v168, 2, s56\n"
"v_bfe_u32 v169, v248, 3, 2\n"
"v_lshl_add_u32 v168, v169, 4, v168\n"
"v_lshrrev_b32 v169, 5, v248\n"
"v_mul_u32_u24 v169, 768, v169\n"
"v_and_b32 v170, 31, v248\n"
"v_lshl_add_u32 v169, v170, 3, v169\n"
"v_add_u32 v169, s56, v169\n"
"s_lshl_b32 s56, s41, 9\n"
"v_lshl_add_u32 v170, v170, 4, s56\n"
"s_mov_b32 s52, 0\n"
"s_mov_b32 s87, 0x80000\n"
"v_lshlrev_b32 v144, 16, v228\n"
"v_and_b32 v145, 0xffff0000, v228\n"
"v_lshlrev_b32 v146, 16, v229\n"
"v_and_b32 v147, 0xffff0000, v229\n"
"v_lshlrev_b32 v148, 16, v230\n"
"v_and_b32 v149, 0xffff0000, v230\n"
"v_lshlrev_b32 v150, 16, v231\n"
"v_and_b32 v151, 0xffff0000, v231\n"
"v_lshlrev_b32 v152, 16, v232\n"
"v_and_b32 v153, 0xffff0000, v232\n"
"v_lshlrev_b32 v154, 16, v233\n"
"v_and_b32 v155, 0xffff0000, v233\n"
"v_lshlrev_b32 v156, 16, v234\n"
"v_and_b32 v157, 0xffff0000, v234\n"
"v_lshlrev_b32 v158, 16, v235\n"
"v_and_b32 v159, 0xffff0000, v235\n"
"v_lshlrev_b32 v160, 16, v236\n"
"v_and_b32 v161, 0xffff0000, v236\n"
"v_lshlrev_b32 v162, 16, v237\n"
"v_and_b32 v163, 0xffff0000, v237\n"
"v_lshlrev_b32 v164, 16, v238\n"
"v_and_b32 v165, 0xffff0000, v238\n"
"v_lshlrev_b32 v166, 16, v239\n"
"v_and_b32 v167, 0xffff0000, v239\n"
"s_waitcnt vmcnt(0)\n"
"AT_A1:\n"
"v_cmp_eq_u32 vcc, s85, v193\n"
"s_sub_u32 s87, s87, 1\n"
"s_lshr_b32 s53, vcc_lo, 1\n"
"s_and_b32 s53, s53, vcc_lo\n"
"s_lshr_b32 s59, s53, 2\n"
"s_and_b32 s53, s53, s59\n"
"s_and_b32 s53, s53, 0x1111\n"
"s_cmp_lt_i32 s87, 0\n"
"s_cselect_b32 s53, 0x1111, s53\n"
"s_andn2_b32 s53, s53, s52\n"
"s_cmp_lg_u32 s53, 0\n"
"s_cbranch_scc1 AT_A2\n"
"s_sleep 2\n"
"AT_A1b:\n"
"buffer_load_dword v193, v192, s[28:31], s57 offen sc1\n"
"s_waitcnt vmcnt(0)\n"
"s_branch AT_A1\n"
"AT_A2:\n"
"s_bcnt1_i32_b32 s54, s53\n"
"s_bitcmp1_b32 s53, 0\n"
"s_cbranch_scc0 AT_Ai0\n"
"s_mov_b32 s56, s47\n"
"buffer_load_dwordx4 v[0:3], v241, s[28:31], s56 offen sc1\n"
"buffer_load_dwordx4 v[4:7], v242, s[28:31], s56 offen sc1\n"
"buffer_load_dwordx4 v[8:11], v243, s[28:31], s56 offen sc1\n"
"AT_Ai0:\n"
"s_bitcmp1_b32 s53, 4\n"
"s_cbranch_scc0 AT_Ai1\n"
"s_add_u32 s56, s47, 2359296\n"
"buffer_load_dwordx4 v[12:15], v241, s[28:31], s56 offen sc1\n"
"buffer_load_dwordx4 v[16:19], v242, s[28:31], s56 offen sc1\n"
"buffer_load_dwordx4 v[20:23], v243, s[28:31], s56 offen sc1\n"
"AT_Ai1:\n"
"s_bitcmp1_b32 s53, 8\n"
"s_cbranch_scc0 AT_Ai2\n"
"s_add_u32 s56, s47, 4718592\n"
"buffer_load_dwordx4 v[24:27], v241, s[28:31], s56 offen sc1\n"
"buffer_load_dwordx4 v[28:31], v242, s[28:31], s56 offen sc1\n"
"buffer_load_dwordx4 v[32:35], v243, s[28:31], s56 offen sc1\n"
"AT_Ai2:\n"
"s_bitcmp1_b32 s53, 12\n"
"s_cbranch_scc0 AT_Ai3\n"
"s_add_u32 s56, s47, 7077888\n"
"buffer_load_dwordx4 v[36:39], v241, s[28:31], s56 offen sc1\n"
"buffer_load_dwordx4 v[40:43], v242, s[28:31], s56 offen sc1\n"
"buffer_load_dwordx4 v[44:47], v243, s[28:31], s56 offen sc1\n"
"AT_Ai3:\n"
"s_bitcmp1_b32 s53, 0\n"
"s_cbranch_scc0 AT_Ar0\n"
"s_sub_u32 s54, s54, 1\n"
"s_cmp_eq_u32 s54, 0\n"
"s_cbranch_scc1 AT_Aw0_0\n"
"s_cmp_eq_u32 s54, 1\n"
"s_cbranch_scc1 AT_Aw0_1\n"
"s_cmp_eq_u32 s54, 2\n"
"s_cbranch_scc1 AT_Aw0_2\n"
"s_waitcnt vmcnt(9)\n"
"s_branch AT_Ax0\n"
"AT_Aw0_2:\n"
"s_waitcnt vmcnt(6)\n"
"s_branch AT_Ax0\n"
"AT_Aw0_1:\n"
"s_waitcnt vmcnt(3)\n"
"s_branch AT_Ax0\n"
"AT_Aw0_0:\n"
"s_waitcnt vmcnt(0)\n"
"AT_Ax0:\n"
"v_max3_u32 v194, v0, v1, v2\n"
"v_max3_u32 v195, v3, v4, v5\n"
"v_max3_u32 v194, v194, v6, v7\n"
"v_max3_u32 v195, v195, v8, v9\n"
"v_max3_u32 v194, v194, v10, v11\n"
"v_max_u32 v194, v194, v195\n"
"v_cmp_eq_u32 vcc, -1, v194\n"
"s_cmp_lt_i32 s87, 0\n"
"s_cselect_b64 vcc, 0, vcc\n"
"s_cbranch_vccnz AT_Ar0\n"
"s_bitset1_b32 s52, 0\n"
"v_dot2c_f32_bf16 v48, v0, v224\n"
"v_dot2c_f32_bf16 v49, v0, v225\n"
"v_dot2c_f32_bf16 v50, v1, v224\n"
"v_dot2c_f32_bf16 v51, v1, v225\n"
"v_dot2c_f32_bf16 v52, v2, v224\n"
"v_dot2c_f32_bf16 v53, v2, v225\n"
"v_dot2c_f32_bf16 v54, v3, v224\n"
"v_dot2c_f32_bf16 v55, v3, v225\n"
"v_dot2c_f32_bf16 v56, v4, v224\n"
"v_dot2c_f32_bf16 v57, v4, v225\n"
"v_dot2c_f32_bf16 v58, v5, v224\n"
"v_dot2c_f32_bf16 v59, v5, v225\n"
"v_dot2c_f32_bf16 v60, v6, v224\n"
"v_dot2c_f32_bf16 v61, v6, v225\n"
"v_dot2c_f32_bf16 v62, v7, v224\n"
"v_dot2c_f32_bf16 v63, v7, v225\n"
"v_dot2c_f32_bf16 v64, v8, v224\n"
"v_dot2c_f32_bf16 v65, v8, v225\n"
"v_dot2c_f32_bf16 v66, v9, v224\n"
"v_dot2c_f32_bf16 v67, v9, v225\n"
"v_dot2c_f32_bf16 v68, v10, v224\n"
"v_dot2c_f32_bf16 v69, v10, v225\n"
"v_dot2c_f32_bf16 v70, v11, v224\n"
"v_dot2c_f32_bf16 v71, v11, v225\n"
"AT_Ar0:\n"
"s_bitcmp1_b32 s53, 4\n"
"s_cbranch_scc0 AT_Ar1\n"
"s_sub_u32 s54, s54, 1\n"
"s_cmp_eq_u32 s54, 0\n"
"s_cbranch_scc1 AT_Aw1_0\n"
"s_cmp_eq_u32 s54, 1\n"
"s_cbranch_scc1 AT_Aw1_1\n"
"s_waitcnt vmcnt(6)\n"
"s_branch AT_Ax1\n"
"AT_Aw1_1:\n"
"s_waitcnt vmcnt(3)\n"
"s_branch AT_Ax1\n"
"AT_Aw1_0:\n"
"s_waitcnt vmcnt(0)\n"
"AT_Ax1:\n"
"v_max3_u32 v194, v12, v13, v14\n"
"v_max3_u32 v195, v15, v16, v17\n"
"v_max3_u32 v194, v194, v18, v19\n"
"v_max3_u32 v195, v195, v20, v21\n"
"v_max3_u32 v194, v194, v22, v23\n"
"v_max_u32 v194, v194, v195\n"
"v_cmp_eq_u32 vcc, -1, v194\n"
"s_cmp_lt_i32 s87, 0\n"
"s_cselect_b64 vcc, 0, vcc\n"
"s_cbranch_vccnz AT_Ar1\n"
"s_bitset1_b32 s52, 4\n"
"v_dot2c_f32_bf16 v48, v12, v224\n"
"v_dot2c_f32_bf16 v49, v12, v225\n"
"v_dot2c_f32_bf16 v50, v13, v224\n"
"v_dot2c_f32_bf16 v51, v13, v225\n"
"v_dot2c_f32_bf16 v52, v14, v224\n"
"v_dot2c_f32_bf16 v53, v14, v225\n"
"v_dot2c_f32_bf16 v54, v15, v224\n"
"v_dot2c_f32_bf16 v55, v15, v225\n"
"v_dot2c_f32_bf16 v56, v16, v224\n"
"v_dot2c_f32_bf16 v57, v16, v225\n"
"v_dot2c_f32_bf16 v58, v17, v224\n"
"v_dot2c_f32_bf16 v59, v17, v225\n"
"v_dot2c_f32_bf16 v60, v18, v224\n"
"v_dot2c_f32_bf16 v61, v18, v225\n"
"v_dot2c_f32_bf16 v62, v19, v224\n"
"v_dot2c_f32_bf16 v63, v19, v225\n"
"v_dot2c_f32_bf16 v64, v20, v224\n"
"v_dot2c_f32_bf16 v65, v20, v225\n"
"v_dot2c_f32_bf16 v66, v21, v224\n"
"v_dot2c_f32_bf16 v67, v21, v225\n"
"v_dot2c_f32_bf16 v68, v22, v224\n"
"v_dot2c_f32_bf16 v69, v22, v225\n"
"v_dot2c_f32_bf16 v70, v23, v224\n"
"v_dot2c_f32_bf16 v71, v23, v225\n"
"AT_Ar1:\n"
"s_bitcmp1_b32 s53, 8\n"
"s_cbranch_scc0 AT_Ar2\n"
"s_sub_u32 s54, s54, 1\n"
"s_cmp_eq_u32 s54, 0\n"
"s_cbranch_scc1 AT_Aw2_0\n"
"s_waitcnt vmcnt(3)\n"
"s_branch AT_Ax2\n"
"AT_Aw2_0:\n"
"s_waitcnt vmcnt(0)\n"
"AT_Ax2:\n"
"v_max3_u32 v194, v24, v25, v26\n"
"v_max3_u32 v195, v27, v28, v29\n"
"v_max3_u32 v194, v194, v30, v31\n"
"v_max3_u32 v195, v195, v32, v33\n"
"v_max3_u32 v194, v194, v34, v35\n"
"v_max_u32 v194, v194, v195\n"
"v_cmp_eq_u32 vcc, -1, v194\n"
"s_cmp_lt_i32 s87, 0\n"
"s_cselect_b64 vcc, 0, vcc\n"
"s_cbranch_vccnz AT_Ar2\n"
"s_bitset1_b32 s52, 8\n"
"v_dot2c_f32_bf16 v72, v24, v224\n"
"v_dot2c_f32_bf16 v73, v24, v225\n"
"v_dot2c_f32_bf16 v74, v25, v224\n"
"v_dot2c_f32_bf16 v75, v25, v225\n"
"v_dot2c_f32_bf16 v76, v26, v224\n"
"v_dot2c_f32_bf16 v77, v26, v225\n"
"v_dot2c_f32_bf16 v78, v27, v224\n"
"v_dot2c_f32_bf16 v79, v27, v225\n"
"v_dot2c_f32_bf16 v80, v28, v224\n"
"v_dot2c_f32_bf16 v81, v28, v225\n"
"v_dot2c_f32_bf16 v82, v29, v224\n"
"v_dot2c_f32_bf16 v83, v29, v225\n"
"v_dot2c_f32_bf16 v84, v30, v224\n"
"v_dot2c_f32_bf16 v85, v30, v225\n"
"v_dot2c_f32_bf16 v86, v31, v224\n"
"v_dot2c_f32_bf16 v87, v31, v225\n"
"v_dot2c_f32_bf16 v88, v32, v224\n"
"v_dot2c_f32_bf16 v89, v32, v225\n"
"v_dot2c_f32_bf16 v90, v33, v224\n"
"v_dot2c_f32_bf16 v91, v33, v225\n"
"v_dot2c_f32_bf16 v92, v34, v224\n"
"v_dot2c_f32_bf16 v93, v34, v225\n"
"v_dot2c_f32_bf16 v94, v35, v224\n"
"v_dot2c_f32_bf16 v95, v35, v225\n"
"AT_Ar2:\n"
"s_bitcmp1_b32 s53, 12\n"
"s_cbranch_scc0 AT_Ar3\n"
"s_sub_u32 s54, s54, 1\n"
"s_waitcnt vmcnt(0)\n"
"AT_Ax3:\n"
"v_max3_u32 v194, v36, v37, v38\n"
"v_max3_u32 v195, v39, v40, v41\n"
"v_max3_u32 v194, v194, v42, v43\n"
"v_max3_u32 v195, v195, v44, v45\n"
"v_max3_u32 v194, v194, v46, v47\n"
"v_max_u32 v194, v194, v195\n"
"v_cmp_eq_u32 vcc, -1, v194\n"
"s_cmp_lt_i32 s87, 0\n"
"s_cselect_b64 vcc, 0, vcc\n"
"s_cbranch_vccnz AT_Ar3\n"
"s_bitset1_b32 s52, 12\n"
"v_dot2c_f32_bf16 v72, v36, v224\n"
"v_dot2c_f32_bf16 v73, v36, v225\n"
"v_dot2c_f32_bf16 v74, v37, v224\n"
"v_dot2c_f32_bf16 v75, v37, v225\n"
"v_dot2c_f32_bf16 v76, v38, v224\n"
"v_dot2c_f32_bf16 v77, v38, v225\n"
"v_dot2c_f32_bf16 v78, v39, v224\n"
"v_dot2c_f32_bf16 v79, v39, v225\n"
"v_dot2c_f32_bf16 v80, v40, v224\n"
"v_dot2c_f32_bf16 v81, v40, v225\n"
"v_dot2c_f32_bf16 v82, v41, v224\n"
"v_dot2c_f32_bf16 v83, v41, v225\n"
"v_dot2c_f32_bf16 v84, v42, v224\n"
"v_dot2c_f32_bf16 v85, v42, v225\n"
"v_dot2c_f32_bf16 v86, v43, v224\n"
"v_dot2c_f32_bf16 v87, v43, v225\n"
"v_dot2c_f32_bf16 v88, v44, v224\n"
"v_dot2c_f32_bf16 v89, v44, v225\n"
"v_dot2c_f32_bf16 v90, v45, v224\n"
"v_dot2c_f32_bf16 v91, v45, v225\n"
"v_dot2c_f32_bf16 v92, v46, v224\n"
"v_dot2c_f32_bf16 v93, v46, v225\n"
"v_dot2c_f32_bf16 v94, v47, v224\n"
"v_dot2c_f32_bf16 v95, v47, v225\n"
"AT_Ar3:\n"
"s_cmp_lg_u32 s52, 0x1111\n"
"s_cbranch_scc1 AT_A1b\n"
"v_pk_add_f32 v[48:49], v[48:49], v[72:73]\n"
"v_pk_add_f32 v[50:51], v[50:51], v[74:75]\n"
"v_pk_add_f32 v[52:53], v[52:53], v[76:77]\n"
"v_pk_add_f32 v[54:55], v[54:55], v[78:79]\n"
"v_pk_add_f32 v[56:57], v[56:57], v[80:81]\n"
"v_pk_add_f32 v[58:59], v[58:59], v[82:83]\n"
"v_pk_add_f32 v[60:61], v[60:61], v[84:85]\n"
"v_pk_add_f32 v[62:63], v[62:63], v[86:87]\n"
"v_pk_add_f32 v[64:65], v[64:65], v[88:89]\n"
"v_pk_add_f32 v[66:67], v[66:67], v[90:91]\n"
"v_pk_add_f32 v[68:69], v[68:69], v[92:93]\n"
"v_pk_add_f32 v[70:71], v[70:71], v[94:95]\n"
"v_pk_fma_f32 v[48:49], v[48:49], v[96:97], v[144:145]\n"
"v_pk_fma_f32 v[50:51], v[50:51], v[98:99], v[146:147]\n"
"v_pk_fma_f32 v[52:53], v[52:53], v[100:101], v[148:149]\n"
"v_pk_fma_f32 v[54:55], v[54:55], v[102:103], v[150:151]\n"
"v_pk_fma_f32 v[56:57], v[56:57], v[104:105], v[152:153]\n"
"v_pk_fma_f32 v[58:59], v[58:59], v[106:107], v[154:155]\n"
"v_pk_fma_f32 v[60:61], v[60:61], v[108:109], v[156:157]\n"
"v_pk_fma_f32 v[62:63], v[62:63], v[110:111], v[158:159]\n"
"v_pk_fma_f32 v[64:65], v[64:65], v[112:113], v[160:161]\n"
"v_pk_fma_f32 v[66:67], v[66:67], v[114:115], v[162:163]\n"
"v_pk_fma_f32 v[68:69], v[68:69], v[116:117], v[164:165]\n"
"v_pk_fma_f32 v[70:71], v[70:71], v[118:119], v[166:167]\n"
"v_pk_mul_f32 v[194:195], v[48:49], v[48:49]\n"
"v_pk_mul_f32 v[196:197], v[48:49], v[120:121]\n"
"v_pk_add_f32 v[192:193], v[48:49], v[50:51]\n"
"v_pk_fma_f32 v[194:195], v[50:51], v[50:51], v[194:195]\n"
"v_pk_fma_f32 v[196:197], v[50:51], v[122:123], v[196:197]\n"
"v_pk_add_f32 v[192:193], v[192:193], v[52:53]\n"
"v_pk_fma_f32 v[194:195], v[52:53], v[52:53], v[194:195]\n"
"v_pk_fma_f32 v[196:197], v[52:53], v[124:125], v[196:197]\n"
"v_pk_add_f32 v[192:193], v[192:193], v[54:55]\n"
"v_pk_fma_f32 v[194:195], v[54:55], v[54:55], v[194:195]\n"
"v_pk_fma_f32 v[196:197], v[54:55], v[126:127], v[196:197]\n"
"v_pk_add_f32 v[192:193], v[192:193], v[56:57]\n"
"v_pk_fma_f32 v[194:195], v[56:57], v[56:57], v[194:195]\n"
"v_pk_fma_f32 v[196:197], v[56:57], v[128:129], v[196:197]\n"
"v_pk_add_f32 v[192:193], v[192:193], v[58:59]\n"
"v_pk_fma_f32 v[194:195], v[58:59], v[58:59], v[194:195]\n"
"v_pk_fma_f32 v[196:197], v[58:59], v[130:131], v[196:197]\n"
"v_pk_add_f32 v[192:193], v[192:193], v[60:61]\n"
"v_pk_fma_f32 v[194:195], v[60:61], v[60:61], v[194:195]\n"
"v_pk_fma_f32 v[196:197], v[60:61], v[132:133], v[196:197]\n"
"v_pk_add_f32 v[192:193], v[192:193], v[62:63]\n"
"v_pk_fma_f32 v[194:195], v[62:63], v[62:63], v[194:195]\n"
"v_pk_fma_f32 v[196:197], v[62:63], v[134:135], v[196:197]\n"
"v_pk_add_f32 v[192:193], v[192:193], v[64:65]\n"
"v_pk_fma_f32 v[194:195], v[64:65], v[64:65], v[194:195]\n"
"v_pk_fma_f32 v[196:197], v[64:65], v[136:137], v[196:197]\n"
"v_pk_add_f32 v[192:193], v[192:193], v[66:67]\n"
"v_pk_fma_f32 v[194:195], v[66:67], v[66:67], v[194:195]\n"
"v_pk_fma_f32 v[196:197], v[66:67], v[138:139], v[196:197]\n"
"v_pk_add_f32 v[192:193], v[192:193], v[68:69]\n"
"v_pk_fma_f32 v[194:195], v[68:69], v[68:69], v[194:195]\n"
"v_pk_fma_f32 v[196:197], v[68:69], v[140:141], v[196:197]\n"
"v_pk_add_f32 v[192:193], v[192:193], v[70:71]\n"
"v_pk_fma_f32 v[194:195], v[70:71], v[70:71], v[194:195]\n"
"v_pk_fma_f32 v[196:197], v[70:71], v[142:143], v[196:197]\n"
"v_add_f32 v192, v192, v193\n"
"v_add_f32 v193, v194, v195\n"
"v_add_f32 v194, v196, v197\n"
"s_lshl_b32 s56, s41, 7\n"
"v_add_u32 v199, s56, v226\n"
"v_add_f32_dpp v192, v192, v192 row_ror:8 row_mask:0xf bank_mask:0xf\n"
"v_add_f32_dpp v193, v193, v193 row_ror:8 row_mask:0xf bank_mask:0xf\n"
"v_add_f32_dpp v194, v194, v194 row_ror:8 row_mask:0xf bank_mask:0xf\n"
"v_mov_b32 v196, v192\n"
"v_mov_b32 v197, v193\n"
"v_mov_b32 v198, v194\n"
"v_permlane16_swap_b32 v192, v196\n"
"v_permlane16_swap_b32 v193, v197\n"
"v_permlane16_swap_b32 v194, v198\n"
"v_add_f32 v192, v192, v196\n"
"v_add_f32 v193, v193, v197\n"
"v_add_f32 v194, v194, v198\n"
"v_mov_b32 v196, v192\n"
"v_mov_b32 v197, v193\n"
"v_mov_b32 v198, v194\n"
"v_permlane32_swap_b32 v192, v196\n"
"v_permlane32_swap_b32 v193, v197\n"
"v_permlane32_swap_b32 v194, v198\n"
"v_add_f32 v192, v192, v196\n"
"v_add_f32 v193, v193, v197\n"
"v_add_f32 v194, v194, v198\n"
"s_mov_b64 exec, 0xff\n"
"ds_write_b128 v199, v[192:195]\n"
"s_mov_b64 exec, -1\n"
"v_mov_b32 v121, s85\n"
"v_mov_b32 v123, s85\n"
"v_mov_b32 v125, s85\n"
"v_mov_b32 v127, s85\n"
"v_mov_b32 v129, s85\n"
"v_mov_b32 v131, s85\n"
"s_waitcnt lgkmcnt(0)\n"
"s_barrier\n"
"ds_read_b128 v[200:203], v226 offset:0\n"
"ds_read_b128 v[204:207], v226 offset:128\n"
"ds_read_b128 v[208:211], v226 offset:256\n"
"ds_read_b128 v[0:3], v244 offset:4608\n"
"ds_read_b128 v[4:7], v244 offset:4672\n"
"ds_read_b128 v[8:11], v244 offset:5376\n"
"ds_read_b128 v[12:15], v244 offset:5440\n"
"ds_read_b128 v[16:19], v244 offset:6144\n"
"ds_read_b128 v[20:23], v244 offset:6208\n"
"ds_read_b128 v[24:27], v244 offset:6912\n"
"ds_read_b128 v[28:31], v244 offset:6976\n"
"ds_read_b128 v[32:35], v244 offset:7680\n"
"ds_read_b128 v[36:39], v244 offset:7744\n"
"ds_read_b128 v[40:43], v244 offset:8448\n"
"ds_read_b128 v[44:47], v244 offset:8512\n"
"s_waitcnt lgkmcnt(13)\n"
"v_add_f32 v200, v200, v204\n"
"v_add_f32 v201, v201, v205\n"
"v_add_f32 v202, v202, v206\n"
"s_waitcnt lgkmcnt(12)\n"
"v_add_f32 v200, v200, v208\n"
"v_add_f32 v201, v201, v209\n"
"v_add_f32 v202, v202, v210\n"
"v_mul_f32 v200, 0x3ae38e39, v200\n"
"v_mul_f32 v201, 0x3ae38e39, v201\n"
"v_fma_f32 v201, -v200, v200, v201\n"
"v_max_f32 v201, 0, v201\n"
"v_add_f32 v201, 0x3727c5ac, v201\n"
"v_rsq_f32 v192, v201\n"
"v_fma_f32 v202, -v200, v212, v202\n"
"v_fma_f32 v196, v192, v202, v213\n"
"v_ashrrev_i32 v203, 31, v196\n"
"v_or_b32 v203, 0x80000000, v203\n"
"v_xor_b32 v196, v196, v203\n"
"v_and_or_b32 v196, v196, s14, v220\n"
"v_mul_f32 v194, v200, v192\n"
"v_sub_f32 v194, 0, v194\n"
"v_mov_b32_dpp v203, v196 row_ror:1 row_mask:0xf bank_mask:0xf\n"
"v_min_u32 v198, v196, v203\n"
"v_max_u32 v197, v196, v203\n"
"s_nop 0\n"
"v_mov_b32_dpp v204, v198 row_ror:2 row_mask:0xf bank_mask:0xf\n"
"v_mov_b32_dpp v203, v197 row_ror:2 row_mask:0xf bank_mask:0xf\n"
"v_min_u32 v205, v197, v203\n"
"v_max_u32 v197, v197, v203\n"
"v_max3_u32 v198, v198, v204, v205\n"
"s_nop 0\n"
"v_mov_b32_dpp v203, v197 row_ror:4 row_mask:0xf bank_mask:0xf\n"
"v_mov_b32_dpp v204, v198 row_ror:4 row_mask:0xf bank_mask:0xf\n"
"v_min_u32 v205, v197, v203\n"
"v_max_u32 v197, v197, v203\n"
"v_max3_u32 v198, v198, v204, v205\n"
"v_cmp_ge_u32 s[8:9], v196, v198\n"
"ds_read_b128 v[72:75], v244 offset:38912\n"
"ds_read_b128 v[76:79], v244 offset:38976\n"
"ds_read_b128 v[80:83], v244 offset:39680\n"
"ds_read_b128 v[84:87], v244 offset:39744\n"
"ds_read_b128 v[88:91], v244 offset:40448\n"
"ds_read_b128 v[92:95], v244 offset:40512\n"
"v_pk_fma_f32 v[144:145], v[48:49], v[192:193], v[194:195] op_sel_hi:[1,0,0]\n"
"v_pk_fma_f32 v[146:147], v[50:51], v[192:193], v[194:195] op_sel_hi:[1,0,0]\n"
"v_pk_fma_f32 v[148:149], v[52:53], v[192:193], v[194:195] op_sel_hi:[1,0,0]\n"
"v_pk_fma_f32 v[150:151], v[54:55], v[192:193], v[194:195] op_sel_hi:[1,0,0]\n"
"v_pk_fma_f32 v[152:153], v[56:57], v[192:193], v[194:195] op_sel_hi:[1,0,0]\n"
"v_pk_fma_f32 v[154:155], v[58:59], v[192:193], v[194:195] op_sel_hi:[1,0,0]\n"
"v_pk_fma_f32 v[156:157], v[60:61], v[192:193], v[194:195] op_sel_hi:[1,0,0]\n"
"v_pk_fma_f32 v[158:159], v[62:63], v[192:193], v[194:195] op_sel_hi:[1,0,0]\n"
"v_pk_fma_f32 v[160:161], v[64:65], v[192:193], v[194:195] op_sel_hi:[1,0,0]\n"
"v_pk_fma_f32 v[162:163], v[66:67], v[192:193], v[194:195] op_sel_hi:[1,0,0]\n"
"v_pk_fma_f32 v[164:165], v[68:69], v[192:193], v[194:195] op_sel_hi:[1,0,0]\n"
"v_pk_fma_f32 v[166:167], v[70:71], v[192:193], v[194:195] op_sel_hi:[1,0,0]\n"
"s_waitcnt lgkmcnt(6)\n"
"v_fma_f32 v96, v144, v0, v24\n"
"v_fma_f32 v97, v145, v1, v25\n"
"v_fma_f32 v98, v146, v2, v26\n"
"v_fma_f32 v99, v147, v3, v27\n"
"v_fma_f32 v100, v148, v4, v28\n"
"v_fma_f32 v101, v149, v5, v29\n"
"v_fma_f32 v102, v150, v6, v30\n"
"v_fma_f32 v103, v151, v7, v31\n"
"v_fma_f32 v104, v152, v8, v32\n"
"v_fma_f32 v105, v153, v9, v33\n"
"v_fma_f32 v106, v154, v10, v34\n"
"v_fma_f32 v107, v155, v11, v35\n"
"v_fma_f32 v108, v156, v12, v36\n"
"v_fma_f32 v109, v157, v13, v37\n"
"v_fma_f32 v110, v158, v14, v38\n"
"v_fma_f32 v111, v159, v15, v39\n"
"v_fma_f32 v112, v160, v16, v40\n"
"v_fma_f32 v113, v161, v17, v41\n"
"v_fma_f32 v114, v162, v18, v42\n"
"v_fma_f32 v115, v163, v19, v43\n"
"v_fma_f32 v116, v164, v20, v44\n"
"v_fma_f32 v117, v165, v21, v45\n"
"v_fma_f32 v118, v166, v22, v46\n"
"v_fma_f32 v119, v167, v23, v47\n"
"v_cmp_ne_u32 vcc, v196, v197\n"
"v_add_u32 v135, 768, v168\n"
"v_cmp_gt_u32 s[58:59], 32, v248\n"
"v_cndmask_b32 v134, v168, v135, vcc\n"
"s_mov_b64 exec, s[8:9]\n"
"ds_write_b128 v134, v[96:99] offset:0\n"
"ds_write_b128 v134, v[100:103] offset:64\n"
"ds_write_b128 v134, v[104:107] offset:256\n"
"ds_write_b128 v134, v[108:111] offset:320\n"
"ds_write_b128 v134, v[112:115] offset:512\n"
"ds_write_b128 v134, v[116:119] offset:576\n"
"s_mov_b64 exec, -1\n"
"ds_read_b64 v[136:137], v169 offset:0\n"
"ds_read_b64 v[138:139], v169 offset:256\n"
"ds_read_b64 v[140:141], v169 offset:512\n"
"v_cndmask_b32 v135, v198, v197, s[58:59]\n"
"v_not_b32 v135, v135\n"
"v_and_b32 v135, 7, v135\n"
"v_mul_u32_u24 v135, 9216, v135\n"
"v_add_u32 v135, v135, v170\n"
"s_waitcnt lgkmcnt(0)\n"
"v_mov_b32 v120, v136\n"
"v_mov_b32 v122, v137\n"
"buffer_store_dwordx4 v[120:123], v135, s[28:31], s48 offen offset:0 sc1\n"
"v_mov_b32 v124, v138\n"
"v_mov_b32 v126, v139\n"
"buffer_store_dwordx4 v[124:127], v135, s[28:31], s48 offen offset:1536 sc1\n"
"v_mov_b32 v128, v140\n"
"v_mov_b32 v130, v141\n"
"buffer_store_dwordx4 v[128:131], v135, s[28:31], s48 offen offset:3072 sc1\n"
"v_fma_f32 v200, v72, v96, v48\n"
"v_fma_f32 v201, v73, v97, v49\n"
"v_fma_f32 v202, v74, v98, v50\n"
"v_fma_f32 v203, v75, v99, v51\n"
"v_fma_f32 v204, v76, v100, v52\n"
"v_fma_f32 v205, v77, v101, v53\n"
"v_fma_f32 v206, v78, v102, v54\n"
"v_fma_f32 v207, v79, v103, v55\n"
"v_fma_f32 v208, v80, v104, v56\n"
"v_fma_f32 v209, v81, v105, v57\n"
"v_fma_f32 v210, v82, v106, v58\n"
"v_fma_f32 v211, v83, v107, v59\n"
"v_fma_f32 v212, v84, v108, v60\n"
"v_fma_f32 v213, v85, v109, v61\n"
"v_fma_f32 v214, v86, v110, v62\n"
"v_fma_f32 v215, v87, v111, v63\n"
"v_fma_f32 v216, v88, v112, v64\n"
"v_fma_f32 v217, v89, v113, v65\n"
"v_fma_f32 v218, v90, v114, v66\n"
"v_fma_f32 v219, v91, v115, v67\n"
"v_fma_f32 v220, v92, v116, v68\n"
"v_fma_f32 v221, v93, v117, v69\n"
"v_fma_f32 v222, v94, v118, v70\n"
"v_fma_f32 v223, v95, v119, v71\n"
"v_cvt_pk_bf16_f32 v228, v200, v201\n"
"v_cvt_pk_bf16_f32 v229, v202, v203\n"
"v_cvt_pk_bf16_f32 v230, v204, v205\n"
"v_cvt_pk_bf16_f32 v231, v206, v207\n"
"v_cvt_pk_bf16_f32 v232, v208, v209\n"
"v_cvt_pk_bf16_f32 v233, v210, v211\n"
"v_cvt_pk_bf16_f32 v234, v212, v213\n"
"v_cvt_pk_bf16_f32 v235, v214, v215\n"
"v_cvt_pk_bf16_f32 v236, v216, v217\n"
"v_cvt_pk_bf16_f32 v237, v218, v219\n"
"v_cvt_pk_bf16_f32 v238, v220, v221\n"
"v_cvt_pk_bf16_f32 v239, v222, v223\n"
"ds_write_b64 v240, v[228:229] offset:0\n"
"ds_write_b64 v240, v[230:231] offset:32\n"
"ds_write_b64 v240, v[232:233] offset:384\n"
"ds_write_b64 v240, v[234:235] offset:416\n"
"ds_write_b64 v240, v[236:237] offset:768\n"
"ds_write_b64 v240, v[238:239] offset:800\n"
"s_mov_b64 s[8:9], -1\n"
"v_mov_b32 v225, v196\n"
"v_mov_b32 v224, 0xffff0000\n"
"v_accvgpr_read_b32 v1, a48\n"
"v_accvgpr_read_b32 v3, a49\n"
"v_accvgpr_read_b32 v5, a50\n"
"v_accvgpr_read_b32 v7, a51\n"
"v_lshlrev_b32 v0, 16, v1\n"
"v_and_b32 v1, v224, v1\n"
"v_lshlrev_b32 v2, 16, v3\n"
"v_and_b32 v3, v224, v3\n"
"v_lshlrev_b32 v4, 16, v5\n"
"v_and_b32 v5, v224, v5\n"
"v_lshlrev_b32 v6, 16, v7\n"
"v_and_b32 v7, v224, v7\n"
"v_accvgpr_read_b32 v9, a84\n"
"v_accvgpr_read_b32 v11, a85\n"
"v_accvgpr_read_b32 v13, a86\n"
"v_accvgpr_read_b32 v15, a87\n"
"v_lshlrev_b32 v8, 16, v9\n"
"v_and_b32 v9, v224, v9\n"
"v_lshlrev_b32 v10, 16, v11\n"
"v_and_b32 v11, v224, v11\n"
"v_lshlrev_b32 v12, 16, v13\n"
"v_and_b32 v13, v224, v13\n"
"v_lshlrev_b32 v14, 16, v15\n"
"v_and_b32 v15, v224, v15\n"
"v_accvgpr_read_b32 v17, a52\n"
"v_accvgpr_read_b32 v19, a53\n"
"v_accvgpr_read_b32 v21, a54\n"
"v_accvgpr_read_b32 v23, a55\n"
"v_lshlrev_b32 v16, 16, v17\n"
"v_and_b32 v17, v224, v17\n"
"v_lshlrev_b32 v18, 16, v19\n"
"v_and_b32 v19, v224, v19\n"
"v_lshlrev_b32 v20, 16, v21\n"
"v_and_b32 v21, v224, v21\n"
"v_lshlrev_b32 v22, 16, v23\n"
"v_and_b32 v23, v224, v23\n"
"v_accvgpr_read_b32 v25, a88\n"
"v_accvgpr_read_b32 v27, a89\n"
"v_accvgpr_read_b32 v29, a90\n"
"v_accvgpr_read_b32 v31, a91\n"
"v_lshlrev_b32 v24, 16, v25\n"
"v_and_b32 v25, v224, v25\n"
"v_lshlrev_b32 v26, 16, v27\n"
"v_and_b32 v27, v224, v27\n"
"v_lshlrev_b32 v28, 16, v29\n"
"v_and_b32 v29, v224, v29\n"
"v_lshlrev_b32 v30, 16, v31\n"
"v_and_b32 v31, v224, v31\n"
"v_accvgpr_read_b32 v33, a56\n"
"v_accvgpr_read_b32 v35, a57\n"
"v_accvgpr_read_b32 v37, a58\n"
"v_accvgpr_read_b32 v39, a59\n"
"v_lshlrev_b32 v32, 16, v33\n"
"v_and_b32 v33, v224, v33\n"
"v_lshlrev_b32 v34, 16, v35\n"
"v_and_b32 v35, v224, v35\n"
"v_lshlrev_b32 v36, 16, v37\n"
"v_and_b32 v37, v224, v37\n"
"v_lshlrev_b32 v38, 16, v39\n"
"v_and_b32 v39, v224, v39\n"
"v_accvgpr_read_b32 v41, a92\n"
"v_accvgpr_read_b32 v43, a93\n"
"v_accvgpr_read_b32 v45, a94\n"
"v_accvgpr_read_b32 v47, a95\n"
"v_lshlrev_b32 v40, 16, v41\n"
"v_and_b32 v41, v224, v41\n"
"v_lshlrev_b32 v42, 16, v43\n"
"v_and_b32 v43, v224, v43\n"
"v_lshlrev_b32 v44, 16, v45\n"
"v_and_b32 v45, v224, v45\n"
"v_lshlrev_b32 v46, 16, v47\n"
"v_and_b32 v47, v224, v47\n"
"v_accvgpr_read_b32 v49, a60\n"
"v_accvgpr_read_b32 v51, a61\n"
"v_accvgpr_read_b32 v53, a62\n"
"v_accvgpr_read_b32 v55, a63\n"
"v_lshlrev_b32 v48, 16, v49\n"
"v_and_b32 v49, v224, v49\n"
"v_lshlrev_b32 v50, 16, v51\n"
"v_and_b32 v51, v224, v51\n"
"v_lshlrev_b32 v52, 16, v53\n"
"v_and_b32 v53, v224, v53\n"
"v_lshlrev_b32 v54, 16, v55\n"
"v_and_b32 v55, v224, v55\n"
"v_accvgpr_read_b32 v57, a96\n"
"v_accvgpr_read_b32 v59, a97\n"
"v_accvgpr_read_b32 v61, a98\n"
"v_accvgpr_read_b32 v63, a99\n"
"v_lshlrev_b32 v56, 16, v57\n"
"v_and_b32 v57, v224, v57\n"
"v_lshlrev_b32 v58, 16, v59\n"
"v_and_b32 v59, v224, v59\n"
"v_lshlrev_b32 v60, 16, v61\n"
"v_and_b32 v61, v224, v61\n"
"v_lshlrev_b32 v62, 16, v63\n"
"v_and_b32 v63, v224, v63\n"
"v_accvgpr_read_b32 v65, a64\n"
"v_accvgpr_read_b32 v67, a65\n"
"v_accvgpr_read_b32 v69, a66\n"
"v_accvgpr_read_b32 v71, a67\n"
"v_lshlrev_b32 v64, 16, v65\n"
"v_and_b32 v65, v224, v65\n"
"v_lshlrev_b32 v66, 16, v67\n"
"v_and_b32 v67, v224, v67\n"
"v_lshlrev_b32 v68, 16, v69\n"
"v_and_b32 v69, v224, v69\n"
"v_lshlrev_b32 v70, 16, v71\n"
"v_and_b32 v71, v224, v71\n"
"v_accvgpr_read_b32 v73, a100\n"
"v_accvgpr_read_b32 v75, a101\n"
"v_accvgpr_read_b32 v77, a102\n"
"v_accvgpr_read_b32 v79, a103\n"
"v_lshlrev_b32 v72, 16, v73\n"
"v_and_b32 v73, v224, v73\n"
"v_lshlrev_b32 v74, 16, v75\n"
"v_and_b32 v75, v224, v75\n"
"v_lshlrev_b32 v76, 16, v77\n"
"v_and_b32 v77, v224, v77\n"
"v_lshlrev_b32 v78, 16, v79\n"
"v_and_b32 v79, v224, v79\n"
"v_accvgpr_read_b32 v81, a68\n"
"v_accvgpr_read_b32 v83, a69\n"
"v_accvgpr_read_b32 v85, a70\n"
"v_accvgpr_read_b32 v87, a71\n"
"v_lshlrev_b32 v80, 16, v81\n"
"v_and_b32 v81, v224, v81\n"
"v_lshlrev_b32 v82, 16, v83\n"
"v_and_b32 v83, v224, v83\n"
"v_lshlrev_b32 v84, 16, v85\n"
"v_and_b32 v85, v224, v85\n"
"v_lshlrev_b32 v86, 16, v87\n"
"v_and_b32 v87, v224, v87\n"
"v_accvgpr_read_b32 v89, a104\n"
"v_accvgpr_read_b32 v91, a105\n"
"v_accvgpr_read_b32 v93, a106\n"
"v_accvgpr_read_b32 v95, a107\n"
"v_lshlrev_b32 v88, 16, v89\n"
"v_and_b32 v89, v224, v89\n"
"v_lshlrev_b32 v90, 16, v91\n"
"v_and_b32 v91, v224, v91\n"
"v_lshlrev_b32 v92, 16, v93\n"
"v_and_b32 v93, v224, v93\n"
"v_lshlrev_b32 v94, 16, v95\n"
"v_and_b32 v95, v224, v95\n"
"v_accvgpr_read_b32 v97, a72\n"
"v_accvgpr_read_b32 v99, a73\n"
"v_accvgpr_read_b32 v101, a74\n"
"v_accvgpr_read_b32 v103, a75\n"
"v_lshlrev_b32 v96, 16, v97\n"
"v_and_b32 v97, v224, v97\n"
"v_lshlrev_b32 v98, 16, v99\n"
"v_and_b32 v99, v224, v99\n"
"v_lshlrev_b32 v100, 16, v101\n"
"v_and_b32 v101, v224, v101\n"
"v_lshlrev_b32 v102, 16, v103\n"
"v_and_b32 v103, v224, v103\n"
"v_accvgpr_read_b32 v105, a108\n"
"v_accvgpr_read_b32 v107, a109\n"
"v_accvgpr_read_b32 v109, a110\n"
"v_accvgpr_read_b32 v111, a111\n"
"v_lshlrev_b32 v104, 16, v105\n"
"v_and_b32 v105, v224, v105\n"
"v_lshlrev_b32 v106, 16, v107\n"
"v_and_b32 v107, v224, v107\n"
"v_lshlrev_b32 v108, 16, v109\n"
"v_and_b32 v109, v224, v109\n"
"v_lshlrev_b32 v110, 16, v111\n"
"v_and_b32 v111, v224, v111\n"
"v_accvgpr_read_b32 v113, a76\n"
"v_accvgpr_read_b32 v115, a77\n"
"v_accvgpr_read_b32 v117, a78\n"
"v_accvgpr_read_b32 v119, a79\n"
"v_lshlrev_b32 v112, 16, v113\n"
"v_and_b32 v113, v224, v113\n"
"v_lshlrev_b32 v114, 16, v115\n"
"v_and_b32 v115, v224, v115\n"
"v_lshlrev_b32 v116, 16, v117\n"
"v_and_b32 v117, v224, v117\n"
"v_lshlrev_b32 v118, 16, v119\n"
"v_and_b32 v119, v224, v119\n"
"v_accvgpr_read_b32 v121, a112\n"
"v_accvgpr_read_b32 v123, a113\n"
"v_accvgpr_read_b32 v125, a114\n"
"v_accvgpr_read_b32 v127, a115\n"
"v_lshlrev_b32 v120, 16, v121\n"
"v_and_b32 v121, v224, v121\n"
"v_lshlrev_b32 v122, 16, v123\n"
"v_and_b32 v123, v224, v123\n"
"v_lshlrev_b32 v124, 16, v125\n"
"v_and_b32 v125, v224, v125\n"
"v_lshlrev_b32 v126, 16, v127\n"
"v_and_b32 v127, v224, v127\n"
"v_accvgpr_read_b32 v129, a80\n"
"v_accvgpr_read_b32 v131, a81\n"
"v_accvgpr_read_b32 v133, a82\n"
"v_accvgpr_read_b32 v135, a83\n"
"v_lshlrev_b32 v128, 16, v129\n"
"v_and_b32 v129, v224, v129\n"
"v_lshlrev_b32 v130, 16, v131\n"
"v_and_b32 v131, v224, v131\n"
"v_lshlrev_b32 v132, 16, v133\n"
"v_and_b32 v133, v224, v133\n"
"v_lshlrev_b32 v134, 16, v135\n"
"v_and_b32 v135, v224, v135\n"
"v_accvgpr_read_b32 v137, a116\n"
"v_accvgpr_read_b32 v139, a117\n"
"v_accvgpr_read_b32 v141, a118\n"
"v_accvgpr_read_b32 v143, a119\n"
"v_lshlrev_b32 v136, 16, v137\n"
"v_and_b32 v137, v224, v137\n"
"v_lshlrev_b32 v138, 16, v139\n"
"v_and_b32 v139, v224, v139\n"
"v_lshlrev_b32 v140, 16, v141\n"
"v_and_b32 v141, v224, v141\n"
"v_lshlrev_b32 v142, 16, v143\n"
"v_and_b32 v143, v224, v143\n"
"v_lshrrev_b32 v242, 2, v248\n"
"v_lshlrev_b32 v241, 4, v242\n"
"v_lshlrev_b32 v242, 3, v242\n"
"v_lshrrev_b32 v243, 6, v254\n"
"v_mul_u32_u24 v243, 24576, v243\n"
"v_and_b32 v247, 7, v248\n"
"v_lshl_add_u32 v243, v247, 4, v243\n"
"v_and_b32 v247, 3, v248\n"
"v_and_b32 v244, 0x10000000, v254\n"
"v_or_b32 v243, v244, v243\n"
"v_lshrrev_b32 v244, 5, v248\n"
"v_mul_u32_u24 v244, 96, v244\n"
"v_and_b32 v246, 16, v248\n"
"v_subrev_u32 v252, 65536, v227\n"
"v_add3_u32 v244, v244, v246, v252\n"
"v_lshl_add_u32 v244, v247, 5, v244\n"
"s_mul_i32 s63, s41, 384\n"
"v_add_u32 v244, s63, v244\n"
"v_lshl_add_u32 v249, v248, 2, v252\n"
"s_lshl_b32 s63, s41, 8\n"
"v_add_u32 v251, s63, v249\n"
"v_add_u32 v252, s63, v252\n"
"s_add_u32 s63, s40, 131072\n"
"v_lshl_add_u32 v255, v253, 3, s63\n"
"s_min_u32 s63, s41, 2\n"
"s_mul_i32 s63, s63, 192\n"
"v_add_u32 v183, s63, v248\n"
"v_mul_u32_u24 v183, 911, v183\n"
"s_lshl_b32 s63, s55, 3\n"
"v_lshrrev_b32 v180, 16, v183\n"
"v_add_u32 v180, s63, v180\n"
"v_add_u32 v183, 58304, v183\n"
"v_lshrrev_b32 v181, 16, v183\n"
"v_add_u32 v181, s63, v181\n"
"v_add_u32 v183, 58304, v183\n"
"v_lshrrev_b32 v182, 16, v183\n"
"v_add_u32 v182, s63, v182\n"
"v_lshlrev_b32 v250, 4, v248\n"
"v_add_u32 v184, s45, v250\n"
"s_cmp_eq_u32 s41, 3\n"
"s_cselect_b32 s63, 0x10000000, 0\n"
"v_or_b32 v250, s63, v250\n"
"AT_B3:\n"
"s_waitcnt lgkmcnt(0)\n"
"s_barrier\n"
"ds_read_b64 v[246:247], v227 offset:656\n"
"s_mul_i32 s58, s41, 1152\n"
"s_add_u32 s58, s58, 37888000\n"
"s_waitcnt lgkmcnt(0)\n"
"v_readfirstlane_b32 s10, v246\n"
"v_readfirstlane_b32 s11, v247\n"
"s_nop 0\n"
"s_andn2_b32 s12, 0x7ff, s10\n"
"s_andn2_b32 s13, 0x7ff, s11\n"
"s_mul_i32 s56, s12, 9216\n"
"s_mul_i32 s57, s13, 9216\n"
"s_add_u32 s56, s56, s58\n"
"s_add_u32 s57, s57, s58\n"
"s_cmp_ge_u32 s42, 192\n"
"s_cbranch_scc1 AT_A4\n"
"buffer_load_dwordx4 v[144:147], v241, s[28:31], s56 offen offset:0 sc1\n"
"buffer_load_dwordx4 v[148:151], v241, s[28:31], s56 offen offset:256 sc1\n"
"buffer_load_dwordx4 v[152:155], v241, s[28:31], s56 offen offset:512 sc1\n"
"buffer_load_dwordx4 v[156:159], v241, s[28:31], s56 offen offset:768 sc1\n"
"buffer_load_dwordx2 v[160:161], v242, s[28:31], s56 offen offset:1024 sc1\n"
"buffer_load_dwordx4 v[162:165], v241, s[28:31], s57 offen offset:0 sc1\n"
"buffer_load_dwordx4 v[166:169], v241, s[28:31], s57 offen offset:256 sc1\n"
"buffer_load_dwordx4 v[170:173], v241, s[28:31], s57 offen offset:512 sc1\n"
"buffer_load_dwordx4 v[174:177], v241, s[28:31], s57 offen offset:768 sc1\n"
"buffer_load_dwordx2 v[178:179], v242, s[28:31], s57 offen offset:1024 sc1\n"
"AT_A4:\n"
"ds_read_b128 v[228:231], v184 offset:0\n"
"ds_read_b128 v[232:235], v184 offset:1024\n"
"ds_read_b128 v[236:239], v184 offset:2048\n"
"v_cmp_eq_u32 vcc, s10, v225\n"
"v_cmp_eq_u32 s[58:59], s11, v225\n"
"s_nop 0\n"
"s_or_b64 s[58:59], s[58:59], vcc\n"
"v_cmp_ne_u32 vcc, s12, v180\n"
"v_cmp_ne_u32 s[60:61], s13, v180\n"
"s_waitcnt lgkmcnt(2)\n"
"s_and_b64 exec, vcc, s[60:61]\n"
"buffer_store_dwordx4 v[228:231], v250, s[76:79], s46 offen offset:0 sc1\n"
"s_mov_b64 exec, -1\n"
"v_cmp_ne_u32 vcc, s12, v181\n"
"v_cmp_ne_u32 s[60:61], s13, v181\n"
"s_waitcnt lgkmcnt(1)\n"
"s_and_b64 exec, vcc, s[60:61]\n"
"buffer_store_dwordx4 v[232:235], v250, s[76:79], s46 offen offset:1024 sc1\n"
"s_mov_b64 exec, -1\n"
"v_cmp_ne_u32 vcc, s12, v182\n"
"v_cmp_ne_u32 s[60:61], s13, v182\n"
"s_waitcnt lgkmcnt(0)\n"
"s_and_b64 exec, vcc, s[60:61]\n"
"buffer_store_dwordx4 v[236:239], v250, s[76:79], s46 offen offset:2048 sc1\n"
"s_mov_b64 exec, -1\n"
"s_and_b64 exec, s[8:9], s[58:59]\n"
"s_cbranch_execz AT_A5\n"
"s_add_u32 s60, s48, 4608\n"
"v_mov_b32 v181, s85\n"
"v_mov_b32 v183, s85\n"
"v_mov_b32 v185, s85\n"
"v_mov_b32 v187, s85\n"
"v_mov_b32 v189, s85\n"
"v_mov_b32 v191, s85\n"
"v_mov_b32 v193, s85\n"
"v_mov_b32 v195, s85\n"
"v_mov_b32 v180, v200\n"
"v_mov_b32 v182, v201\n"
"buffer_store_dwordx4 v[180:183], v245, s[28:31], s60 offen offset:0 sc1\n"
"v_mov_b32 v184, v202\n"
"v_mov_b32 v186, v203\n"
"buffer_store_dwordx4 v[184:187], v245, s[28:31], s60 offen offset:16 sc1\n"
"v_mov_b32 v188, v204\n"
"v_mov_b32 v190, v205\n"
"buffer_store_dwordx4 v[188:191], v245, s[28:31], s60 offen offset:128 sc1\n"
"v_mov_b32 v192, v206\n"
"v_mov_b32 v194, v207\n"
"buffer_store_dwordx4 v[192:195], v245, s[28:31], s60 offen offset:144 sc1\n"
"v_mov_b32 v180, v208\n"
"v_mov_b32 v182, v209\n"
"buffer_store_dwordx4 v[180:183], v245, s[28:31], s60 offen offset:1536 sc1\n"
"v_mov_b32 v184, v210\n"
"v_mov_b32 v186, v211\n"
"buffer_store_dwordx4 v[184:187], v245, s[28:31], s60 offen offset:1552 sc1\n"
"v_mov_b32 v188, v212\n"
"v_mov_b32 v190, v213\n"
"buffer_store_dwordx4 v[188:191], v245, s[28:31], s60 offen offset:1664 sc1\n"
"v_mov_b32 v192, v214\n"
"v_mov_b32 v194, v215\n"
"buffer_store_dwordx4 v[192:195], v245, s[28:31], s60 offen offset:1680 sc1\n"
"v_mov_b32 v180, v216\n"
"v_mov_b32 v182, v217\n"
"buffer_store_dwordx4 v[180:183], v245, s[28:31], s60 offen offset:3072 sc1\n"
"v_mov_b32 v184, v218\n"
"v_mov_b32 v186, v219\n"
"buffer_store_dwordx4 v[184:187], v245, s[28:31], s60 offen offset:3088 sc1\n"
"v_mov_b32 v188, v220\n"
"v_mov_b32 v190, v221\n"
"buffer_store_dwordx4 v[188:191], v245, s[28:31], s60 offen offset:3200 sc1\n"
"v_mov_b32 v192, v222\n"
"v_mov_b32 v194, v223\n"
"buffer_store_dwordx4 v[192:195], v245, s[28:31], s60 offen offset:3216 sc1\n"
"AT_A5:\n"
"s_mov_b64 exec, -1\n"
"s_cmp_ge_u32 s42, 192\n"
"s_cbranch_scc1 AT_F0\n"
"s_mov_b32 s87, 0x80000\n"
"s_waitcnt vmcnt(3)\n"
"AT_A6:\n"
"v_xor_b32 v212, s85, v145\n"
"v_xor_b32 v213, s85, v147\n"
"v_bitop3_b32 v212, v149, s85, v212 bitop3:0xbe\n"
"v_bitop3_b32 v213, v151, s85, v213 bitop3:0xbe\n"
"v_bitop3_b32 v212, v153, s85, v212 bitop3:0xbe\n"
"v_bitop3_b32 v213, v155, s85, v213 bitop3:0xbe\n"
"v_bitop3_b32 v212, v157, s85, v212 bitop3:0xbe\n"
"v_bitop3_b32 v213, v159, s85, v213 bitop3:0xbe\n"
"v_bitop3_b32 v212, v161, s85, v212 bitop3:0xbe\n"
"v_bitop3_b32 v213, v163, s85, v213 bitop3:0xbe\n"
"v_bitop3_b32 v212, v165, s85, v212 bitop3:0xbe\n"
"v_bitop3_b32 v213, v167, s85, v213 bitop3:0xbe\n"
"v_bitop3_b32 v212, v169, s85, v212 bitop3:0xbe\n"
"v_bitop3_b32 v213, v171, s85, v213 bitop3:0xbe\n"
"v_bitop3_b32 v212, v173, s85, v212 bitop3:0xbe\n"
"v_bitop3_b32 v213, v175, s85, v213 bitop3:0xbe\n"
"v_bitop3_b32 v212, v177, s85, v212 bitop3:0xbe\n"
"v_bitop3_b32 v213, v179, s85, v213 bitop3:0xbe\n"
"v_or_b32 v212, v212, v213\n"
"v_cmp_ne_u32 vcc, 0, v212\n"
"s_nop 0\n"
"s_cbranch_vccz AT_A7\n"
"s_sub_u32 s87, s87, 1\n"
"s_cmp_eq_u32 s87, 0\n"
"s_cbranch_scc1 AT_A7\n"
"s_sleep 1\n"
"buffer_load_dwordx4 v[144:147], v241, s[28:31], s56 offen offset:0 sc1\n"
"buffer_load_dwordx4 v[148:151], v241, s[28:31], s56 offen offset:256 sc1\n"
"buffer_load_dwordx4 v[152:155], v241, s[28:31], s56 offen offset:512 sc1\n"
"buffer_load_dwordx4 v[156:159], v241, s[28:31], s56 offen offset:768 sc1\n"
"buffer_load_dwordx2 v[160:161], v242, s[28:31], s56 offen offset:1024 sc1\n"
"buffer_load_dwordx4 v[162:165], v241, s[28:31], s57 offen offset:0 sc1\n"
"buffer_load_dwordx4 v[166:169], v241, s[28:31], s57 offen offset:256 sc1\n"
"buffer_load_dwordx4 v[170:173], v241, s[28:31], s57 offen offset:512 sc1\n"
"buffer_load_dwordx4 v[174:177], v241, s[28:31], s57 offen offset:768 sc1\n"
"buffer_load_dwordx2 v[178:179], v242, s[28:31], s57 offen offset:1024 sc1\n"
"s_waitcnt vmcnt(0)\n"
"s_branch AT_A6\n"
"AT_A7:\n"
"v_pk_mul_f32 v[180:181], v[0:1], v[144:145] op_sel_hi:[1,0]\n"
"v_pk_mul_f32 v[182:183], v[2:3], v[144:145] op_sel_hi:[1,0]\n"
"v_pk_mul_f32 v[184:185], v[4:5], v[144:145] op_sel_hi:[1,0]\n"
"v_pk_mul_f32 v[186:187], v[6:7], v[144:145] op_sel_hi:[1,0]\n"
"v_pk_mul_f32 v[196:197], v[8:9], v[144:145] op_sel_hi:[1,0]\n"
"v_pk_mul_f32 v[198:199], v[10:11], v[144:145] op_sel_hi:[1,0]\n"
"v_pk_mul_f32 v[200:201], v[12:13], v[144:145] op_sel_hi:[1,0]\n"
"v_pk_mul_f32 v[202:203], v[14:15], v[144:145] op_sel_hi:[1,0]\n"
"v_pk_mul_f32 v[188:189], v[0:1], v[162:163] op_sel_hi:[1,0]\n"
"v_pk_mul_f32 v[190:191], v[2:3], v[162:163] op_sel_hi:[1,0]\n"
"v_pk_mul_f32 v[192:193], v[4:5], v[162:163] op_sel_hi:[1,0]\n"
"v_pk_mul_f32 v[194:195], v[6:7], v[162:163] op_sel_hi:[1,0]\n"
"v_pk_mul_f32 v[204:205], v[8:9], v[162:163] op_sel_hi:[1,0]\n"
"v_pk_mul_f32 v[206:207], v[10:11], v[162:163] op_sel_hi:[1,0]\n"
"v_pk_mul_f32 v[208:209], v[12:13], v[162:163] op_sel_hi:[1,0]\n"
"v_pk_mul_f32 v[210:211], v[14:15], v[162:163] op_sel_hi:[1,0]\n"
"ds_read_b64 v[2:3], v255 offset:0\n"
"ds_read_b64 v[6:7], v255 offset:1152\n"
"ds_read_b64 v[10:11], v255 offset:2304\n"
"ds_read_b64 v[14:15], v255 offset:3456\n"
"v_pk_fma_f32 v[180:181], v[16:17], v[146:147], v[180:181] op_sel_hi:[1,0,1]\n"
"v_pk_fma_f32 v[182:183], v[18:19], v[146:147], v[182:183] op_sel_hi:[1,0,1]\n"
"v_pk_fma_f32 v[184:185], v[20:21], v[146:147], v[184:185] op_sel_hi:[1,0,1]\n"
"v_pk_fma_f32 v[186:187], v[22:23], v[146:147], v[186:187] op_sel_hi:[1,0,1]\n"
"v_pk_fma_f32 v[196:197], v[24:25], v[146:147], v[196:197] op_sel_hi:[1,0,1]\n"
"v_pk_fma_f32 v[198:199], v[26:27], v[146:147], v[198:199] op_sel_hi:[1,0,1]\n"
"v_pk_fma_f32 v[200:201], v[28:29], v[146:147], v[200:201] op_sel_hi:[1,0,1]\n"
"v_pk_fma_f32 v[202:203], v[30:31], v[146:147], v[202:203] op_sel_hi:[1,0,1]\n"
"v_pk_fma_f32 v[188:189], v[16:17], v[164:165], v[188:189] op_sel_hi:[1,0,1]\n"
"v_pk_fma_f32 v[190:191], v[18:19], v[164:165], v[190:191] op_sel_hi:[1,0,1]\n"
"v_pk_fma_f32 v[192:193], v[20:21], v[164:165], v[192:193] op_sel_hi:[1,0,1]\n"
"v_pk_fma_f32 v[194:195], v[22:23], v[164:165], v[194:195] op_sel_hi:[1,0,1]\n"
"v_pk_fma_f32 v[204:205], v[24:25], v[164:165], v[204:205] op_sel_hi:[1,0,1]\n"
"v_pk_fma_f32 v[206:207], v[26:27], v[164:165], v[206:207] op_sel_hi:[1,0,1]\n"
"v_pk_fma_f32 v[208:209], v[28:29], v[164:165], v[208:209] op_sel_hi:[1,0,1]\n"
"v_pk_fma_f32 v[210:211], v[30:31], v[164:165], v[210:211] op_sel_hi:[1,0,1]\n"
"ds_read_b64 v[18:19], v255 offset:4608\n"
"ds_read_b64 v[22:23], v255 offset:5760\n"
"ds_read_b64 v[26:27], v255 offset:6912\n"
"ds_read_b64 v[30:31], v255 offset:8064\n"
"v_pk_fma_f32 v[180:181], v[32:33], v[148:149], v[180:181] op_sel_hi:[1,0,1]\n"
"v_pk_fma_f32 v[182:183], v[34:35], v[148:149], v[182:183] op_sel_hi:[1,0,1]\n"
"v_pk_fma_f32 v[184:185], v[36:37], v[148:149], v[184:185] op_sel_hi:[1,0,1]\n"
"v_pk_fma_f32 v[186:187], v[38:39], v[148:149], v[186:187] op_sel_hi:[1,0,1]\n"
"v_pk_fma_f32 v[196:197], v[40:41], v[148:149], v[196:197] op_sel_hi:[1,0,1]\n"
"v_pk_fma_f32 v[198:199], v[42:43], v[148:149], v[198:199] op_sel_hi:[1,0,1]\n"
"v_pk_fma_f32 v[200:201], v[44:45], v[148:149], v[200:201] op_sel_hi:[1,0,1]\n"
"v_pk_fma_f32 v[202:203], v[46:47], v[148:149], v[202:203] op_sel_hi:[1,0,1]\n"
"v_pk_fma_f32 v[188:189], v[32:33], v[166:167], v[188:189] op_sel_hi:[1,0,1]\n"
"v_pk_fma_f32 v[190:191], v[34:35], v[166:167], v[190:191] op_sel_hi:[1,0,1]\n"
"v_pk_fma_f32 v[192:193], v[36:37], v[166:167], v[192:193] op_sel_hi:[1,0,1]\n"
"v_pk_fma_f32 v[194:195], v[38:39], v[166:167], v[194:195] op_sel_hi:[1,0,1]\n"
"v_pk_fma_f32 v[204:205], v[40:41], v[166:167], v[204:205] op_sel_hi:[1,0,1]\n"
"v_pk_fma_f32 v[206:207], v[42:43], v[166:167], v[206:207] op_sel_hi:[1,0,1]\n"
"v_pk_fma_f32 v[208:209], v[44:45], v[166:167], v[208:209] op_sel_hi:[1,0,1]\n"
"v_pk_fma_f32 v[210:211], v[46:47], v[166:167], v[210:211] op_sel_hi:[1,0,1]\n"
"ds_read_b64 v[34:35], v255 offset:9216\n"
"ds_read_b64 v[38:39], v255 offset:10368\n"
"ds_read_b64 v[42:43], v255 offset:11520\n"
"ds_read_b64 v[46:47], v255 offset:12672\n"
"v_pk_fma_f32 v[180:181], v[48:49], v[150:151], v[180:181] op_sel_hi:[1,0,1]\n"
"v_pk_fma_f32 v[182:183], v[50:51], v[150:151], v[182:183] op_sel_hi:[1,0,1]\n"
"v_pk_fma_f32 v[184:185], v[52:53], v[150:151], v[184:185] op_sel_hi:[1,0,1]\n"
"v_pk_fma_f32 v[186:187], v[54:55], v[150:151], v[186:187] op_sel_hi:[1,0,1]\n"
"v_pk_fma_f32 v[196:197], v[56:57], v[150:151], v[196:197] op_sel_hi:[1,0,1]\n"
"v_pk_fma_f32 v[198:199], v[58:59], v[150:151], v[198:199] op_sel_hi:[1,0,1]\n"
"v_pk_fma_f32 v[200:201], v[60:61], v[150:151], v[200:201] op_sel_hi:[1,0,1]\n"
"v_pk_fma_f32 v[202:203], v[62:63], v[150:151], v[202:203] op_sel_hi:[1,0,1]\n"
"v_pk_fma_f32 v[188:189], v[48:49], v[168:169], v[188:189] op_sel_hi:[1,0,1]\n"
"v_pk_fma_f32 v[190:191], v[50:51], v[168:169], v[190:191] op_sel_hi:[1,0,1]\n"
"v_pk_fma_f32 v[192:193], v[52:53], v[168:169], v[192:193] op_sel_hi:[1,0,1]\n"
"v_pk_fma_f32 v[194:195], v[54:55], v[168:169], v[194:195] op_sel_hi:[1,0,1]\n"
"v_pk_fma_f32 v[204:205], v[56:57], v[168:169], v[204:205] op_sel_hi:[1,0,1]\n"
"v_pk_fma_f32 v[206:207], v[58:59], v[168:169], v[206:207] op_sel_hi:[1,0,1]\n"
"v_pk_fma_f32 v[208:209], v[60:61], v[168:169], v[208:209] op_sel_hi:[1,0,1]\n"
"v_pk_fma_f32 v[210:211], v[62:63], v[168:169], v[210:211] op_sel_hi:[1,0,1]\n"
"ds_read_b64 v[50:51], v255 offset:13824\n"
"ds_read_b64 v[54:55], v255 offset:14976\n"
"ds_read_b64 v[58:59], v255 offset:16128\n"
"ds_read_b64 v[62:63], v255 offset:17280\n"
"v_pk_fma_f32 v[180:181], v[64:65], v[152:153], v[180:181] op_sel_hi:[1,0,1]\n"
"v_pk_fma_f32 v[182:183], v[66:67], v[152:153], v[182:183] op_sel_hi:[1,0,1]\n"
"v_pk_fma_f32 v[184:185], v[68:69], v[152:153], v[184:185] op_sel_hi:[1,0,1]\n"
"v_pk_fma_f32 v[186:187], v[70:71], v[152:153], v[186:187] op_sel_hi:[1,0,1]\n"
"v_pk_fma_f32 v[196:197], v[72:73], v[152:153], v[196:197] op_sel_hi:[1,0,1]\n"
"v_pk_fma_f32 v[198:199], v[74:75], v[152:153], v[198:199] op_sel_hi:[1,0,1]\n"
"v_pk_fma_f32 v[200:201], v[76:77], v[152:153], v[200:201] op_sel_hi:[1,0,1]\n"
"v_pk_fma_f32 v[202:203], v[78:79], v[152:153], v[202:203] op_sel_hi:[1,0,1]\n"
"v_pk_fma_f32 v[188:189], v[64:65], v[170:171], v[188:189] op_sel_hi:[1,0,1]\n"
"v_pk_fma_f32 v[190:191], v[66:67], v[170:171], v[190:191] op_sel_hi:[1,0,1]\n"
"v_pk_fma_f32 v[192:193], v[68:69], v[170:171], v[192:193] op_sel_hi:[1,0,1]\n"
"v_pk_fma_f32 v[194:195], v[70:71], v[170:171], v[194:195] op_sel_hi:[1,0,1]\n"
"v_pk_fma_f32 v[204:205], v[72:73], v[170:171], v[204:205] op_sel_hi:[1,0,1]\n"
"v_pk_fma_f32 v[206:207], v[74:75], v[170:171], v[206:207] op_sel_hi:[1,0,1]\n"
"v_pk_fma_f32 v[208:209], v[76:77], v[170:171], v[208:209] op_sel_hi:[1,0,1]\n"
"v_pk_fma_f32 v[210:211], v[78:79], v[170:171], v[210:211] op_sel_hi:[1,0,1]\n"
"ds_read_b64 v[66:67], v255 offset:18432\n"
"ds_read_b64 v[70:71], v255 offset:19584\n"
"ds_read_b64 v[74:75], v255 offset:20736\n"
"ds_read_b64 v[78:79], v255 offset:21888\n"
"v_pk_fma_f32 v[180:181], v[80:81], v[154:155], v[180:181] op_sel_hi:[1,0,1]\n"
"v_pk_fma_f32 v[182:183], v[82:83], v[154:155], v[182:183] op_sel_hi:[1,0,1]\n"
"v_pk_fma_f32 v[184:185], v[84:85], v[154:155], v[184:185] op_sel_hi:[1,0,1]\n"
"v_pk_fma_f32 v[186:187], v[86:87], v[154:155], v[186:187] op_sel_hi:[1,0,1]\n"
"v_pk_fma_f32 v[196:197], v[88:89], v[154:155], v[196:197] op_sel_hi:[1,0,1]\n"
"v_pk_fma_f32 v[198:199], v[90:91], v[154:155], v[198:199] op_sel_hi:[1,0,1]\n"
"v_pk_fma_f32 v[200:201], v[92:93], v[154:155], v[200:201] op_sel_hi:[1,0,1]\n"
"v_pk_fma_f32 v[202:203], v[94:95], v[154:155], v[202:203] op_sel_hi:[1,0,1]\n"
"v_pk_fma_f32 v[188:189], v[80:81], v[172:173], v[188:189] op_sel_hi:[1,0,1]\n"
"v_pk_fma_f32 v[190:191], v[82:83], v[172:173], v[190:191] op_sel_hi:[1,0,1]\n"
"v_pk_fma_f32 v[192:193], v[84:85], v[172:173], v[192:193] op_sel_hi:[1,0,1]\n"
"v_pk_fma_f32 v[194:195], v[86:87], v[172:173], v[194:195] op_sel_hi:[1,0,1]\n"
"v_pk_fma_f32 v[204:205], v[88:89], v[172:173], v[204:205] op_sel_hi:[1,0,1]\n"
"v_pk_fma_f32 v[206:207], v[90:91], v[172:173], v[206:207] op_sel_hi:[1,0,1]\n"
"v_pk_fma_f32 v[208:209], v[92:93], v[172:173], v[208:209] op_sel_hi:[1,0,1]\n"
"v_pk_fma_f32 v[210:211], v[94:95], v[172:173], v[210:211] op_sel_hi:[1,0,1]\n"
"ds_read_b64 v[82:83], v255 offset:23040\n"
"ds_read_b64 v[86:87], v255 offset:24192\n"
"ds_read_b64 v[90:91], v255 offset:25344\n"
"ds_read_b64 v[94:95], v255 offset:26496\n"
"v_pk_fma_f32 v[180:181], v[96:97], v[156:157], v[180:181] op_sel_hi:[1,0,1]\n"
"v_pk_fma_f32 v[182:183], v[98:99], v[156:157], v[182:183] op_sel_hi:[1,0,1]\n"
"v_pk_fma_f32 v[184:185], v[100:101], v[156:157], v[184:185] op_sel_hi:[1,0,1]\n"
"v_pk_fma_f32 v[186:187], v[102:103], v[156:157], v[186:187] op_sel_hi:[1,0,1]\n"
"v_pk_fma_f32 v[196:197], v[104:105], v[156:157], v[196:197] op_sel_hi:[1,0,1]\n"
"v_pk_fma_f32 v[198:199], v[106:107], v[156:157], v[198:199] op_sel_hi:[1,0,1]\n"
"v_pk_fma_f32 v[200:201], v[108:109], v[156:157], v[200:201] op_sel_hi:[1,0,1]\n"
"v_pk_fma_f32 v[202:203], v[110:111], v[156:157], v[202:203] op_sel_hi:[1,0,1]\n"
"v_pk_fma_f32 v[188:189], v[96:97], v[174:175], v[188:189] op_sel_hi:[1,0,1]\n"
"v_pk_fma_f32 v[190:191], v[98:99], v[174:175], v[190:191] op_sel_hi:[1,0,1]\n"
"v_pk_fma_f32 v[192:193], v[100:101], v[174:175], v[192:193] op_sel_hi:[1,0,1]\n"
"v_pk_fma_f32 v[194:195], v[102:103], v[174:175], v[194:195] op_sel_hi:[1,0,1]\n"
"v_pk_fma_f32 v[204:205], v[104:105], v[174:175], v[204:205] op_sel_hi:[1,0,1]\n"
"v_pk_fma_f32 v[206:207], v[106:107], v[174:175], v[206:207] op_sel_hi:[1,0,1]\n"
"v_pk_fma_f32 v[208:209], v[108:109], v[174:175], v[208:209] op_sel_hi:[1,0,1]\n"
"v_pk_fma_f32 v[210:211], v[110:111], v[174:175], v[210:211] op_sel_hi:[1,0,1]\n"
"v_pk_fma_f32 v[180:181], v[112:113], v[158:159], v[180:181] op_sel_hi:[1,0,1]\n"
"v_pk_fma_f32 v[182:183], v[114:115], v[158:159], v[182:183] op_sel_hi:[1,0,1]\n"
"v_pk_fma_f32 v[184:185], v[116:117], v[158:159], v[184:185] op_sel_hi:[1,0,1]\n"
"v_pk_fma_f32 v[186:187], v[118:119], v[158:159], v[186:187] op_sel_hi:[1,0,1]\n"
"v_pk_fma_f32 v[196:197], v[120:121], v[158:159], v[196:197] op_sel_hi:[1,0,1]\n"
"v_pk_fma_f32 v[198:199], v[122:123], v[158:159], v[198:199] op_sel_hi:[1,0,1]\n"
"v_pk_fma_f32 v[200:201], v[124:125], v[158:159], v[200:201] op_sel_hi:[1,0,1]\n"
"v_pk_fma_f32 v[202:203], v[126:127], v[158:159], v[202:203] op_sel_hi:[1,0,1]\n"
"v_pk_fma_f32 v[188:189], v[112:113], v[176:177], v[188:189] op_sel_hi:[1,0,1]\n"
"v_pk_fma_f32 v[190:191], v[114:115], v[176:177], v[190:191] op_sel_hi:[1,0,1]\n"
"v_pk_fma_f32 v[192:193], v[116:117], v[176:177], v[192:193] op_sel_hi:[1,0,1]\n"
"v_pk_fma_f32 v[194:195], v[118:119], v[176:177], v[194:195] op_sel_hi:[1,0,1]\n"
"v_pk_fma_f32 v[204:205], v[120:121], v[176:177], v[204:205] op_sel_hi:[1,0,1]\n"
"v_pk_fma_f32 v[206:207], v[122:123], v[176:177], v[206:207] op_sel_hi:[1,0,1]\n"
"v_pk_fma_f32 v[208:209], v[124:125], v[176:177], v[208:209] op_sel_hi:[1,0,1]\n"
"v_pk_fma_f32 v[210:211], v[126:127], v[176:177], v[210:211] op_sel_hi:[1,0,1]\n"
"v_pk_fma_f32 v[180:181], v[128:129], v[160:161], v[180:181] op_sel_hi:[1,0,1]\n"
"v_pk_fma_f32 v[182:183], v[130:131], v[160:161], v[182:183] op_sel_hi:[1,0,1]\n"
"v_pk_fma_f32 v[184:185], v[132:133], v[160:161], v[184:185] op_sel_hi:[1,0,1]\n"
"v_pk_fma_f32 v[186:187], v[134:135], v[160:161], v[186:187] op_sel_hi:[1,0,1]\n"
"v_pk_fma_f32 v[196:197], v[136:137], v[160:161], v[196:197] op_sel_hi:[1,0,1]\n"
"v_pk_fma_f32 v[198:199], v[138:139], v[160:161], v[198:199] op_sel_hi:[1,0,1]\n"
"v_pk_fma_f32 v[200:201], v[140:141], v[160:161], v[200:201] op_sel_hi:[1,0,1]\n"
"v_pk_fma_f32 v[202:203], v[142:143], v[160:161], v[202:203] op_sel_hi:[1,0,1]\n"
"v_pk_fma_f32 v[188:189], v[128:129], v[178:179], v[188:189] op_sel_hi:[1,0,1]\n"
"v_pk_fma_f32 v[190:191], v[130:131], v[178:179], v[190:191] op_sel_hi:[1,0,1]\n"
"v_pk_fma_f32 v[192:193], v[132:133], v[178:179], v[192:193] op_sel_hi:[1,0,1]\n"
"v_pk_fma_f32 v[194:195], v[134:135], v[178:179], v[194:195] op_sel_hi:[1,0,1]\n"
"v_pk_fma_f32 v[204:205], v[136:137], v[178:179], v[204:205] op_sel_hi:[1,0,1]\n"
"v_pk_fma_f32 v[206:207], v[138:139], v[178:179], v[206:207] op_sel_hi:[1,0,1]\n"
"v_pk_fma_f32 v[208:209], v[140:141], v[178:179], v[208:209] op_sel_hi:[1,0,1]\n"
"v_pk_fma_f32 v[210:211], v[142:143], v[178:179], v[210:211] op_sel_hi:[1,0,1]\n"
"s_waitcnt lgkmcnt(0)\n"
"v_lshlrev_b32 v0, 16, v2\n"
"v_and_b32 v1, v224, v2\n"
"v_permlane32_swap_b32 v180, v188\n"
"v_permlane32_swap_b32 v181, v189\n"
"v_permlane32_swap_b32 v182, v190\n"
"v_permlane32_swap_b32 v183, v191\n"
"v_permlane32_swap_b32 v184, v192\n"
"v_permlane32_swap_b32 v185, v193\n"
"v_permlane32_swap_b32 v186, v194\n"
"v_permlane32_swap_b32 v187, v195\n"
"v_permlane32_swap_b32 v196, v204\n"
"v_permlane32_swap_b32 v197, v205\n"
"v_permlane32_swap_b32 v198, v206\n"
"v_permlane32_swap_b32 v199, v207\n"
"v_permlane32_swap_b32 v200, v208\n"
"v_permlane32_swap_b32 v201, v209\n"
"v_permlane32_swap_b32 v202, v210\n"
"v_permlane32_swap_b32 v203, v211\n"
"v_pk_add_f32 v[180:181], v[180:181], v[188:189]\n"
"v_pk_add_f32 v[182:183], v[182:183], v[190:191]\n"
"v_pk_add_f32 v[184:185], v[184:185], v[192:193]\n"
"v_pk_add_f32 v[186:187], v[186:187], v[194:195]\n"
"v_pk_add_f32 v[196:197], v[196:197], v[204:205]\n"
"v_pk_add_f32 v[198:199], v[198:199], v[206:207]\n"
"v_pk_add_f32 v[200:201], v[200:201], v[208:209]\n"
"v_pk_add_f32 v[202:203], v[202:203], v[210:211]\n"
"v_lshlrev_b32 v2, 16, v3\n"
"v_and_b32 v3, v224, v3\n"
"v_permlane16_swap_b32 v180, v184\n"
"v_permlane16_swap_b32 v181, v185\n"
"v_permlane16_swap_b32 v182, v186\n"
"v_permlane16_swap_b32 v183, v187\n"
"v_permlane16_swap_b32 v196, v200\n"
"v_permlane16_swap_b32 v197, v201\n"
"v_permlane16_swap_b32 v198, v202\n"
"v_permlane16_swap_b32 v199, v203\n"
"v_pk_add_f32 v[180:181], v[180:181], v[184:185]\n"
"v_pk_add_f32 v[182:183], v[182:183], v[186:187]\n"
"v_pk_add_f32 v[196:197], v[196:197], v[200:201]\n"
"v_pk_add_f32 v[198:199], v[198:199], v[202:203]\n"
"v_lshlrev_b32 v4, 16, v6\n"
"v_and_b32 v5, v224, v6\n"
"v_add_f32_dpp v180, v180, v180 row_ror:4 row_mask:0xf bank_mask:0xf\n"
"v_add_f32_dpp v181, v181, v181 row_ror:4 row_mask:0xf bank_mask:0xf\n"
"v_add_f32_dpp v182, v182, v182 row_ror:4 row_mask:0xf bank_mask:0xf\n"
"v_add_f32_dpp v183, v183, v183 row_ror:4 row_mask:0xf bank_mask:0xf\n"
"v_add_f32_dpp v196, v196, v196 row_ror:4 row_mask:0xf bank_mask:0xf\n"
"v_add_f32_dpp v197, v197, v197 row_ror:4 row_mask:0xf bank_mask:0xf\n"
"v_add_f32_dpp v198, v198, v198 row_ror:4 row_mask:0xf bank_mask:0xf\n"
"v_add_f32_dpp v199, v199, v199 row_ror:4 row_mask:0xf bank_mask:0xf\n"
"v_add_f32_dpp v180, v180, v180 row_ror:8 row_mask:0xf bank_mask:0xf\n"
"v_add_f32_dpp v181, v181, v181 row_ror:8 row_mask:0xf bank_mask:0xf\n"
"v_add_f32_dpp v182, v182, v182 row_ror:8 row_mask:0xf bank_mask:0xf\n"
"v_add_f32_dpp v183, v183, v183 row_ror:8 row_mask:0xf bank_mask:0xf\n"
"v_add_f32_dpp v196, v196, v196 row_ror:8 row_mask:0xf bank_mask:0xf\n"
"v_add_f32_dpp v197, v197, v197 row_ror:8 row_mask:0xf bank_mask:0xf\n"
"v_add_f32_dpp v198, v198, v198 row_ror:8 row_mask:0xf bank_mask:0xf\n"
"v_add_f32_dpp v199, v199, v199 row_ror:8 row_mask:0xf bank_mask:0xf\n"
"s_mov_b32 exec_lo, 0x00070007\n"
"s_mov_b32 exec_hi, 0x00070007\n"
"ds_write_b128 v244, v[180:183] offset:2048\n"
"ds_write_b128 v244, v[196:199] offset:2240\n"
"s_mov_b64 exec, -1\n"
"v_lshlrev_b32 v6, 16, v7\n"
"v_and_b32 v7, v224, v7\n"
"v_lshlrev_b32 v8, 16, v10\n"
"v_and_b32 v9, v224, v10\n"
"v_lshlrev_b32 v10, 16, v11\n"
"v_and_b32 v11, v224, v11\n"
"v_lshlrev_b32 v12, 16, v14\n"
"v_and_b32 v13, v224, v14\n"
"v_lshlrev_b32 v14, 16, v15\n"
"v_and_b32 v15, v224, v15\n"
"v_lshlrev_b32 v16, 16, v18\n"
"v_and_b32 v17, v224, v18\n"
"s_waitcnt lgkmcnt(0)\n"
"s_barrier\n"
"s_cmp_eq_u32 s41, 3\n"
"s_cbranch_scc1 AT_F1\n"
"ds_read_b32 v180, v249 offset:2048\n"
"ds_read_b32 v181, v249 offset:2240\n"
"ds_read_b32 v182, v249 offset:2432\n"
"ds_read_b32 v183, v249 offset:2624\n"
"ds_read_b32 v184, v249 offset:2816\n"
"ds_read_b32 v185, v249 offset:3008\n"
"ds_read_b32 v186, v249 offset:3200\n"
"ds_read_b32 v187, v249 offset:3392\n"
"v_lshlrev_b32 v18, 16, v19\n"
"v_and_b32 v19, v224, v19\n"
"v_lshlrev_b32 v20, 16, v22\n"
"v_and_b32 v21, v224, v22\n"
"v_lshlrev_b32 v22, 16, v23\n"
"v_and_b32 v23, v224, v23\n"
"v_lshlrev_b32 v24, 16, v26\n"
"v_and_b32 v25, v224, v26\n"
"v_lshlrev_b32 v26, 16, v27\n"
"v_and_b32 v27, v224, v27\n"
"v_lshlrev_b32 v28, 16, v30\n"
"v_and_b32 v29, v224, v30\n"
"v_lshlrev_b32 v30, 16, v31\n"
"v_and_b32 v31, v224, v31\n"
"v_lshlrev_b32 v32, 16, v34\n"
"v_and_b32 v33, v224, v34\n"
"v_lshlrev_b32 v34, 16, v35\n"
"v_and_b32 v35, v224, v35\n"
"v_lshlrev_b32 v36, 16, v38\n"
"v_and_b32 v37, v224, v38\n"
"v_lshlrev_b32 v38, 16, v39\n"
"v_and_b32 v39, v224, v39\n"
"v_lshlrev_b32 v40, 16, v42\n"
"v_and_b32 v41, v224, v42\n"
"s_waitcnt lgkmcnt(5)\n"
"v_add_f32 v180, v180, v182\n"
"s_waitcnt lgkmcnt(1)\n"
"v_add_f32 v184, v184, v186\n"
"v_add_f32 v181, v181, v183\n"
"v_add_f32 v180, v180, v184\n"
"s_waitcnt lgkmcnt(0)\n"
"v_add_f32 v185, v185, v187\n"
"v_mul_f32 v182, 0xbfb8aa3b, v180\n"
"v_add_f32 v181, v181, v185\n"
"v_exp_f32 v182, v182\n"
"v_mul_f32 v180, v180, v181\n"
"v_add_f32 v182, 1.0, v182\n"
"v_rcp_f32 v182, v182\n"
"v_lshlrev_b32 v42, 16, v43\n"
"v_mul_f32 v180, v180, v182\n"
"ds_write_b32 v251, v180 offset:4096\n"
"ds_read_b128 v[100:103], v252 offset:4096\n"
"ds_read_b128 v[124:127], v252 offset:4192\n"
"ds_read_b128 v[104:107], v252 offset:4112\n"
"ds_read_b128 v[128:131], v252 offset:4208\n"
"ds_read_b128 v[108:111], v252 offset:4128\n"
"ds_read_b128 v[132:135], v252 offset:4224\n"
"ds_read_b128 v[112:115], v252 offset:4144\n"
"ds_read_b128 v[136:139], v252 offset:4240\n"
"ds_read_b128 v[116:119], v252 offset:4160\n"
"ds_read_b128 v[140:143], v252 offset:4256\n"
"ds_read_b128 v[120:123], v252 offset:4176\n"
"ds_read_b128 v[144:147], v252 offset:4272\n"
"s_lshl_b32 s58, s42, 7\n"
"s_add_u32 s58, s58, 56762368\n"
"s_add_u32 s59, s58, 442368\n"
"s_add_u32 s60, s58, 884736\n"
"s_add_u32 s61, s58, 1327104\n"
"v_mov_b32 v161, s85\n"
"v_mov_b32 v163, s85\n"
"v_mov_b32 v165, s85\n"
"v_mov_b32 v167, s85\n"
"v_mov_b32 v169, s85\n"
"v_mov_b32 v171, s85\n"
"v_mov_b32 v173, s85\n"
"v_mov_b32 v175, s85\n"
"v_and_b32 v43, v224, v43\n"
"v_lshlrev_b32 v44, 16, v46\n"
"v_and_b32 v45, v224, v46\n"
"v_lshlrev_b32 v46, 16, v47\n"
"v_and_b32 v47, v224, v47\n"
"v_lshlrev_b32 v48, 16, v50\n"
"v_and_b32 v49, v224, v50\n"
"v_lshlrev_b32 v50, 16, v51\n"
"v_and_b32 v51, v224, v51\n"
"v_lshlrev_b32 v52, 16, v54\n"
"v_and_b32 v53, v224, v54\n"
"v_lshlrev_b32 v54, 16, v55\n"
"v_and_b32 v55, v224, v55\n"
"v_lshlrev_b32 v56, 16, v58\n"
"v_and_b32 v57, v224, v58\n"
"v_lshlrev_b32 v58, 16, v59\n"
"v_and_b32 v59, v224, v59\n"
"v_lshlrev_b32 v60, 16, v62\n"
"v_and_b32 v61, v224, v62\n"
"v_lshlrev_b32 v62, 16, v63\n"
"v_and_b32 v63, v224, v63\n"
"v_lshlrev_b32 v64, 16, v66\n"
"v_and_b32 v65, v224, v66\n"
"v_lshlrev_b32 v66, 16, v67\n"
"v_and_b32 v67, v224, v67\n"
"v_lshlrev_b32 v68, 16, v70\n"
"v_and_b32 v69, v224, v70\n"
"v_lshlrev_b32 v70, 16, v71\n"
"v_and_b32 v71, v224, v71\n"
"v_lshlrev_b32 v72, 16, v74\n"
"v_and_b32 v73, v224, v74\n"
"v_lshlrev_b32 v74, 16, v75\n"
"v_and_b32 v75, v224, v75\n"
"v_lshlrev_b32 v76, 16, v78\n"
"v_and_b32 v77, v224, v78\n"
"v_lshlrev_b32 v78, 16, v79\n"
"v_and_b32 v79, v224, v79\n"
"v_lshlrev_b32 v80, 16, v82\n"
"v_and_b32 v81, v224, v82\n"
"v_lshlrev_b32 v82, 16, v83\n"
"v_and_b32 v83, v224, v83\n"
"v_lshlrev_b32 v84, 16, v86\n"
"v_and_b32 v85, v224, v86\n"
"v_lshlrev_b32 v86, 16, v87\n"
"v_and_b32 v87, v224, v87\n"
"v_lshlrev_b32 v88, 16, v90\n"
"v_and_b32 v89, v224, v90\n"
"v_lshlrev_b32 v90, 16, v91\n"
"v_and_b32 v91, v224, v91\n"
"v_lshlrev_b32 v92, 16, v94\n"
"v_and_b32 v93, v224, v94\n"
"v_lshlrev_b32 v94, 16, v95\n"
"v_and_b32 v95, v224, v95\n"
"s_waitcnt lgkmcnt(11)\n"
"v_pk_mul_f32 v[150:151], v[0:1], v[100:101] op_sel:[0,0] op_sel_hi:[1,0]\n"
"v_pk_mul_f32 v[152:153], v[2:3], v[100:101] op_sel:[0,0] op_sel_hi:[1,0]\n"
"s_waitcnt lgkmcnt(10)\n"
"v_pk_mul_f32 v[154:155], v[0:1], v[124:125] op_sel:[0,0] op_sel_hi:[1,0]\n"
"v_pk_mul_f32 v[156:157], v[2:3], v[124:125] op_sel:[0,0] op_sel_hi:[1,0]\n"
"v_pk_fma_f32 v[150:151], v[4:5], v[100:101], v[150:151] op_sel:[0,1,0] op_sel_hi:[1,1,1]\n"
"v_pk_fma_f32 v[152:153], v[6:7], v[100:101], v[152:153] op_sel:[0,1,0] op_sel_hi:[1,1,1]\n"
"v_pk_fma_f32 v[154:155], v[4:5], v[124:125], v[154:155] op_sel:[0,1,0] op_sel_hi:[1,1,1]\n"
"v_pk_fma_f32 v[156:157], v[6:7], v[124:125], v[156:157] op_sel:[0,1,0] op_sel_hi:[1,1,1]\n"
"v_pk_fma_f32 v[150:151], v[8:9], v[102:103], v[150:151] op_sel:[0,0,0] op_sel_hi:[1,0,1]\n"
"v_pk_fma_f32 v[152:153], v[10:11], v[102:103], v[152:153] op_sel:[0,0,0] op_sel_hi:[1,0,1]\n"
"v_pk_fma_f32 v[154:155], v[8:9], v[126:127], v[154:155] op_sel:[0,0,0] op_sel_hi:[1,0,1]\n"
"v_pk_fma_f32 v[156:157], v[10:11], v[126:127], v[156:157] op_sel:[0,0,0] op_sel_hi:[1,0,1]\n"
"v_pk_fma_f32 v[150:151], v[12:13], v[102:103], v[150:151] op_sel:[0,1,0] op_sel_hi:[1,1,1]\n"
"v_pk_fma_f32 v[152:153], v[14:15], v[102:103], v[152:153] op_sel:[0,1,0] op_sel_hi:[1,1,1]\n"
"v_pk_fma_f32 v[154:155], v[12:13], v[126:127], v[154:155] op_sel:[0,1,0] op_sel_hi:[1,1,1]\n"
"v_pk_fma_f32 v[156:157], v[14:15], v[126:127], v[156:157] op_sel:[0,1,0] op_sel_hi:[1,1,1]\n"
"s_waitcnt lgkmcnt(9)\n"
"v_pk_fma_f32 v[150:151], v[16:17], v[104:105], v[150:151] op_sel:[0,0,0] op_sel_hi:[1,0,1]\n"
"v_pk_fma_f32 v[152:153], v[18:19], v[104:105], v[152:153] op_sel:[0,0,0] op_sel_hi:[1,0,1]\n"
"s_waitcnt lgkmcnt(8)\n"
"v_pk_fma_f32 v[154:155], v[16:17], v[128:129], v[154:155] op_sel:[0,0,0] op_sel_hi:[1,0,1]\n"
"v_pk_fma_f32 v[156:157], v[18:19], v[128:129], v[156:157] op_sel:[0,0,0] op_sel_hi:[1,0,1]\n"
"v_pk_fma_f32 v[150:151], v[20:21], v[104:105], v[150:151] op_sel:[0,1,0] op_sel_hi:[1,1,1]\n"
"v_pk_fma_f32 v[152:153], v[22:23], v[104:105], v[152:153] op_sel:[0,1,0] op_sel_hi:[1,1,1]\n"
"v_pk_fma_f32 v[154:155], v[20:21], v[128:129], v[154:155] op_sel:[0,1,0] op_sel_hi:[1,1,1]\n"
"v_pk_fma_f32 v[156:157], v[22:23], v[128:129], v[156:157] op_sel:[0,1,0] op_sel_hi:[1,1,1]\n"
"v_pk_fma_f32 v[150:151], v[24:25], v[106:107], v[150:151] op_sel:[0,0,0] op_sel_hi:[1,0,1]\n"
"v_pk_fma_f32 v[152:153], v[26:27], v[106:107], v[152:153] op_sel:[0,0,0] op_sel_hi:[1,0,1]\n"
"v_pk_fma_f32 v[154:155], v[24:25], v[130:131], v[154:155] op_sel:[0,0,0] op_sel_hi:[1,0,1]\n"
"v_pk_fma_f32 v[156:157], v[26:27], v[130:131], v[156:157] op_sel:[0,0,0] op_sel_hi:[1,0,1]\n"
"v_pk_fma_f32 v[150:151], v[28:29], v[106:107], v[150:151] op_sel:[0,1,0] op_sel_hi:[1,1,1]\n"
"v_pk_fma_f32 v[152:153], v[30:31], v[106:107], v[152:153] op_sel:[0,1,0] op_sel_hi:[1,1,1]\n"
"v_pk_fma_f32 v[154:155], v[28:29], v[130:131], v[154:155] op_sel:[0,1,0] op_sel_hi:[1,1,1]\n"
"v_pk_fma_f32 v[156:157], v[30:31], v[130:131], v[156:157] op_sel:[0,1,0] op_sel_hi:[1,1,1]\n"
"s_waitcnt lgkmcnt(7)\n"
"v_pk_fma_f32 v[150:151], v[32:33], v[108:109], v[150:151] op_sel:[0,0,0] op_sel_hi:[1,0,1]\n"
"v_pk_fma_f32 v[152:153], v[34:35], v[108:109], v[152:153] op_sel:[0,0,0] op_sel_hi:[1,0,1]\n"
"s_waitcnt lgkmcnt(6)\n"
"v_pk_fma_f32 v[154:155], v[32:33], v[132:133], v[154:155] op_sel:[0,0,0] op_sel_hi:[1,0,1]\n"
"v_pk_fma_f32 v[156:157], v[34:35], v[132:133], v[156:157] op_sel:[0,0,0] op_sel_hi:[1,0,1]\n"
"v_pk_fma_f32 v[150:151], v[36:37], v[108:109], v[150:151] op_sel:[0,1,0] op_sel_hi:[1,1,1]\n"
"v_pk_fma_f32 v[152:153], v[38:39], v[108:109], v[152:153] op_sel:[0,1,0] op_sel_hi:[1,1,1]\n"
"v_pk_fma_f32 v[154:155], v[36:37], v[132:133], v[154:155] op_sel:[0,1,0] op_sel_hi:[1,1,1]\n"
"v_pk_fma_f32 v[156:157], v[38:39], v[132:133], v[156:157] op_sel:[0,1,0] op_sel_hi:[1,1,1]\n"
"v_pk_fma_f32 v[150:151], v[40:41], v[110:111], v[150:151] op_sel:[0,0,0] op_sel_hi:[1,0,1]\n"
"v_pk_fma_f32 v[152:153], v[42:43], v[110:111], v[152:153] op_sel:[0,0,0] op_sel_hi:[1,0,1]\n"
"v_pk_fma_f32 v[154:155], v[40:41], v[134:135], v[154:155] op_sel:[0,0,0] op_sel_hi:[1,0,1]\n"
"v_pk_fma_f32 v[156:157], v[42:43], v[134:135], v[156:157] op_sel:[0,0,0] op_sel_hi:[1,0,1]\n"
"v_pk_fma_f32 v[150:151], v[44:45], v[110:111], v[150:151] op_sel:[0,1,0] op_sel_hi:[1,1,1]\n"
"v_pk_fma_f32 v[152:153], v[46:47], v[110:111], v[152:153] op_sel:[0,1,0] op_sel_hi:[1,1,1]\n"
"v_pk_fma_f32 v[154:155], v[44:45], v[134:135], v[154:155] op_sel:[0,1,0] op_sel_hi:[1,1,1]\n"
"v_pk_fma_f32 v[156:157], v[46:47], v[134:135], v[156:157] op_sel:[0,1,0] op_sel_hi:[1,1,1]\n"
"s_waitcnt lgkmcnt(5)\n"
"v_pk_fma_f32 v[150:151], v[48:49], v[112:113], v[150:151] op_sel:[0,0,0] op_sel_hi:[1,0,1]\n"
"v_pk_fma_f32 v[152:153], v[50:51], v[112:113], v[152:153] op_sel:[0,0,0] op_sel_hi:[1,0,1]\n"
"s_waitcnt lgkmcnt(4)\n"
"v_pk_fma_f32 v[154:155], v[48:49], v[136:137], v[154:155] op_sel:[0,0,0] op_sel_hi:[1,0,1]\n"
"v_pk_fma_f32 v[156:157], v[50:51], v[136:137], v[156:157] op_sel:[0,0,0] op_sel_hi:[1,0,1]\n"
"v_pk_fma_f32 v[150:151], v[52:53], v[112:113], v[150:151] op_sel:[0,1,0] op_sel_hi:[1,1,1]\n"
"v_pk_fma_f32 v[152:153], v[54:55], v[112:113], v[152:153] op_sel:[0,1,0] op_sel_hi:[1,1,1]\n"
"v_pk_fma_f32 v[154:155], v[52:53], v[136:137], v[154:155] op_sel:[0,1,0] op_sel_hi:[1,1,1]\n"
"v_pk_fma_f32 v[156:157], v[54:55], v[136:137], v[156:157] op_sel:[0,1,0] op_sel_hi:[1,1,1]\n"
"v_pk_fma_f32 v[150:151], v[56:57], v[114:115], v[150:151] op_sel:[0,0,0] op_sel_hi:[1,0,1]\n"
"v_pk_fma_f32 v[152:153], v[58:59], v[114:115], v[152:153] op_sel:[0,0,0] op_sel_hi:[1,0,1]\n"
"v_pk_fma_f32 v[154:155], v[56:57], v[138:139], v[154:155] op_sel:[0,0,0] op_sel_hi:[1,0,1]\n"
"v_pk_fma_f32 v[156:157], v[58:59], v[138:139], v[156:157] op_sel:[0,0,0] op_sel_hi:[1,0,1]\n"
"v_pk_fma_f32 v[150:151], v[60:61], v[114:115], v[150:151] op_sel:[0,1,0] op_sel_hi:[1,1,1]\n"
"v_pk_fma_f32 v[152:153], v[62:63], v[114:115], v[152:153] op_sel:[0,1,0] op_sel_hi:[1,1,1]\n"
"v_pk_fma_f32 v[154:155], v[60:61], v[138:139], v[154:155] op_sel:[0,1,0] op_sel_hi:[1,1,1]\n"
"v_pk_fma_f32 v[156:157], v[62:63], v[138:139], v[156:157] op_sel:[0,1,0] op_sel_hi:[1,1,1]\n"
"s_waitcnt lgkmcnt(3)\n"
"v_pk_fma_f32 v[150:151], v[64:65], v[116:117], v[150:151] op_sel:[0,0,0] op_sel_hi:[1,0,1]\n"
"v_pk_fma_f32 v[152:153], v[66:67], v[116:117], v[152:153] op_sel:[0,0,0] op_sel_hi:[1,0,1]\n"
"s_waitcnt lgkmcnt(2)\n"
"v_pk_fma_f32 v[154:155], v[64:65], v[140:141], v[154:155] op_sel:[0,0,0] op_sel_hi:[1,0,1]\n"
"v_pk_fma_f32 v[156:157], v[66:67], v[140:141], v[156:157] op_sel:[0,0,0] op_sel_hi:[1,0,1]\n"
"v_pk_fma_f32 v[150:151], v[68:69], v[116:117], v[150:151] op_sel:[0,1,0] op_sel_hi:[1,1,1]\n"
"v_pk_fma_f32 v[152:153], v[70:71], v[116:117], v[152:153] op_sel:[0,1,0] op_sel_hi:[1,1,1]\n"
"v_pk_fma_f32 v[154:155], v[68:69], v[140:141], v[154:155] op_sel:[0,1,0] op_sel_hi:[1,1,1]\n"
"v_pk_fma_f32 v[156:157], v[70:71], v[140:141], v[156:157] op_sel:[0,1,0] op_sel_hi:[1,1,1]\n"
"v_pk_fma_f32 v[150:151], v[72:73], v[118:119], v[150:151] op_sel:[0,0,0] op_sel_hi:[1,0,1]\n"
"v_pk_fma_f32 v[152:153], v[74:75], v[118:119], v[152:153] op_sel:[0,0,0] op_sel_hi:[1,0,1]\n"
"v_pk_fma_f32 v[154:155], v[72:73], v[142:143], v[154:155] op_sel:[0,0,0] op_sel_hi:[1,0,1]\n"
"v_pk_fma_f32 v[156:157], v[74:75], v[142:143], v[156:157] op_sel:[0,0,0] op_sel_hi:[1,0,1]\n"
"v_pk_fma_f32 v[150:151], v[76:77], v[118:119], v[150:151] op_sel:[0,1,0] op_sel_hi:[1,1,1]\n"
"v_pk_fma_f32 v[152:153], v[78:79], v[118:119], v[152:153] op_sel:[0,1,0] op_sel_hi:[1,1,1]\n"
"v_pk_fma_f32 v[154:155], v[76:77], v[142:143], v[154:155] op_sel:[0,1,0] op_sel_hi:[1,1,1]\n"
"v_pk_fma_f32 v[156:157], v[78:79], v[142:143], v[156:157] op_sel:[0,1,0] op_sel_hi:[1,1,1]\n"
"s_waitcnt lgkmcnt(1)\n"
"v_pk_fma_f32 v[150:151], v[80:81], v[120:121], v[150:151] op_sel:[0,0,0] op_sel_hi:[1,0,1]\n"
"v_pk_fma_f32 v[152:153], v[82:83], v[120:121], v[152:153] op_sel:[0,0,0] op_sel_hi:[1,0,1]\n"
"s_waitcnt lgkmcnt(0)\n"
"v_pk_fma_f32 v[154:155], v[80:81], v[144:145], v[154:155] op_sel:[0,0,0] op_sel_hi:[1,0,1]\n"
"v_pk_fma_f32 v[156:157], v[82:83], v[144:145], v[156:157] op_sel:[0,0,0] op_sel_hi:[1,0,1]\n"
"v_pk_fma_f32 v[150:151], v[84:85], v[120:121], v[150:151] op_sel:[0,1,0] op_sel_hi:[1,1,1]\n"
"v_pk_fma_f32 v[152:153], v[86:87], v[120:121], v[152:153] op_sel:[0,1,0] op_sel_hi:[1,1,1]\n"
"v_pk_fma_f32 v[154:155], v[84:85], v[144:145], v[154:155] op_sel:[0,1,0] op_sel_hi:[1,1,1]\n"
"v_pk_fma_f32 v[156:157], v[86:87], v[144:145], v[156:157] op_sel:[0,1,0] op_sel_hi:[1,1,1]\n"
"v_pk_fma_f32 v[150:151], v[88:89], v[122:123], v[150:151] op_sel:[0,0,0] op_sel_hi:[1,0,1]\n"
"v_pk_fma_f32 v[152:153], v[90:91], v[122:123], v[152:153] op_sel:[0,0,0] op_sel_hi:[1,0,1]\n"
"v_pk_fma_f32 v[154:155], v[88:89], v[146:147], v[154:155] op_sel:[0,0,0] op_sel_hi:[1,0,1]\n"
"v_pk_fma_f32 v[156:157], v[90:91], v[146:147], v[156:157] op_sel:[0,0,0] op_sel_hi:[1,0,1]\n"
"v_pk_fma_f32 v[150:151], v[92:93], v[122:123], v[150:151] op_sel:[0,1,0] op_sel_hi:[1,1,1]\n"
"v_pk_fma_f32 v[152:153], v[94:95], v[122:123], v[152:153] op_sel:[0,1,0] op_sel_hi:[1,1,1]\n"
"v_pk_fma_f32 v[154:155], v[92:93], v[146:147], v[154:155] op_sel:[0,1,0] op_sel_hi:[1,1,1]\n"
"v_pk_fma_f32 v[156:157], v[94:95], v[146:147], v[156:157] op_sel:[0,1,0] op_sel_hi:[1,1,1]\n"
"v_mov_b32 v160, v150\n"
"v_mov_b32 v162, v151\n"
"v_mov_b32 v164, v152\n"
"v_mov_b32 v166, v153\n"
"v_mov_b32 v168, v154\n"
"v_mov_b32 v170, v155\n"
"v_mov_b32 v172, v156\n"
"v_mov_b32 v174, v157\n"
"buffer_store_dwordx4 v[160:163], v243, s[28:31], s58 offen sc1\n"
"buffer_store_dwordx4 v[164:167], v243, s[28:31], s59 offen sc1\n"
"buffer_store_dwordx4 v[168:171], v243, s[28:31], s60 offen sc1\n"
"buffer_store_dwordx4 v[172:175], v243, s[28:31], s61 offen sc1\n"
"s_branch AT_F1\n"
"AT_F0:\n"
"s_sleep 70\n"
"AT_F1:\n"
"s_cmp_lt_u32 s42, 184\n"
"s_cbranch_scc1 AT_end\n"
"s_sub_u32 s56, s42, 184\n"
"s_cmp_ge_u32 s56, 36\n"
"s_cselect_b32 s57, 36, 0\n"
"s_cselect_b32 s59, s13, s12\n"
"s_cselect_b32 s60, s11, s10\n"
"s_sub_u32 s58, s56, s57\n"
"s_cmp_ge_u32 s58, 18\n"
"s_cselect_b32 s57, 574, 0\n"
"s_lshl_b32 s58, s58, 5\n"
"s_sub_u32 s58, s58, s57\n"
"s_mul_i32 s56, s56, 24576\n"
"s_add_u32 s56, s56, 56762368\n"
"s_mul_i32 s61, s59, 1152\n"
"s_add_u32 s61, s61, s58\n"
"s_lshl_b32 s61, s61, 3\n"
"s_add_u32 s61, s61, 37892608\n"
"v_lshlrev_b32 v30, 4, v253\n"
"v_add_u32 v31, 4096, v30\n"
"v_add_u32 v32, 8192, v30\n"
"v_add_u32 v33, 12288, v30\n"
"v_add_u32 v34, 16384, v30\n"
"v_add_u32 v35, 20480, v30\n"
"v_and_b32 v36, 15, v248\n"
"v_lshlrev_b32 v37, 3, v36\n"
"v_and_b32 v41, 14, v36\n"
"v_add_u32 v41, v41, v36\n"
"v_lshlrev_b32 v42, 3, v41\n"
"s_lshl_b32 s62, s58, 2\n"
"s_add_u32 s62, s62, s40\n"
"s_add_u32 s62, s62, 158720\n"
"v_lshl_add_u32 v38, v41, 2, s62\n"
"ds_read_b32 v39, v38\n"
"s_mov_b32 s87, 0x80000\n"
"AT_F2:\n"
"buffer_load_dwordx4 v[0:3], v30, s[28:31], s56 offen sc1\n"
"buffer_load_dwordx4 v[4:7], v31, s[28:31], s56 offen sc1\n"
"buffer_load_dwordx4 v[8:11], v32, s[28:31], s56 offen sc1\n"
"buffer_load_dwordx4 v[12:15], v33, s[28:31], s56 offen sc1\n"
"buffer_load_dwordx4 v[16:19], v34, s[28:31], s56 offen sc1\n"
"buffer_load_dwordx4 v[20:23], v35, s[28:31], s56 offen sc1\n"
"buffer_load_dwordx2 v[24:25], v42, s[28:31], s61 offen sc1\n"
"s_and_b32 s62, s60, s14\n"
"s_ashr_i32 s63, s62, 31\n"
"s_not_b32 s63, s63\n"
"s_and_b32 s63, s63, s14\n"
"s_or_b32 s63, s63, 0x80000000\n"
"s_xor_b32 s62, s62, s63\n"
"v_mov_b32 v40, s62\n"
"v_mul_f32 v40, 0xbfb8aa3b, v40\n"
"v_exp_f32 v40, v40\n"
"s_nop 0\n"
"v_add_f32 v40, 1.0, v40\n"
"v_rcp_f32 v40, v40\n"
"s_waitcnt vmcnt(0) lgkmcnt(0)\n"
"v_xor_b32 v26, s85, v1\n"
"v_xor_b32 v27, s85, v3\n"
"v_bitop3_b32 v26, v5, s85, v26 bitop3:0xbe\n"
"v_bitop3_b32 v27, v7, s85, v27 bitop3:0xbe\n"
"v_bitop3_b32 v26, v9, s85, v26 bitop3:0xbe\n"
"v_bitop3_b32 v27, v11, s85, v27 bitop3:0xbe\n"
"v_bitop3_b32 v26, v13, s85, v26 bitop3:0xbe\n"
"v_bitop3_b32 v27, v15, s85, v27 bitop3:0xbe\n"
"v_bitop3_b32 v26, v17, s85, v26 bitop3:0xbe\n"
"v_bitop3_b32 v27, v19, s85, v27 bitop3:0xbe\n"
"v_bitop3_b32 v26, v21, s85, v26 bitop3:0xbe\n"
"v_bitop3_b32 v27, v23, s85, v27 bitop3:0xbe\n"
"v_bitop3_b32 v26, v25, s85, v26 bitop3:0xbe\n"
"v_or_b32 v26, v26, v27\n"
"v_cmp_ne_u32 vcc, 0, v26\n"
"s_nop 0\n"
"s_cbranch_vccz AT_F3\n"
"s_sub_u32 s87, s87, 1\n"
"s_cmp_eq_u32 s87, 0\n"
"s_cbranch_scc1 AT_F3\n"
"s_sleep 2\n"
"s_branch AT_F2\n"
"AT_F3:\n"
"v_add_f32 v0, v0, v4\n"
"v_add_f32 v2, v2, v6\n"
"v_add_f32 v8, v8, v12\n"
"v_add_f32 v10, v10, v14\n"
"v_add_f32 v16, v16, v20\n"
"v_add_f32 v18, v18, v22\n"
"v_add_f32 v0, v0, v8\n"
"v_add_f32 v2, v2, v10\n"
"v_add_f32 v0, v0, v16\n"
"v_add_f32 v1, v2, v18\n"
"v_mul_f32 v40, v40, v39\n"
"s_lshl_b32 s62, s41, 6\n"
"v_add3_u32 v28, v37, v227, s62\n"
"v_add_f32_dpp v0, v0, v0 row_ror:8 row_mask:0xf bank_mask:0xf\n"
"v_add_f32_dpp v1, v1, v1 row_ror:8 row_mask:0xf bank_mask:0xf\n"
"s_nop 0\n"
"v_mov_b32 v2, v0\n"
"v_mov_b32 v3, v1\n"
"s_nop 0\n"
"v_permlane16_swap_b32 v0, v2\n"
"v_permlane16_swap_b32 v1, v3\n"
"v_add_f32 v0, v0, v2\n"
"v_add_f32 v1, v1, v3\n"
"v_mov_b32 v2, v0\n"
"v_mov_b32 v3, v1\n"
"s_nop 0\n"
"v_permlane32_swap_b32 v0, v2\n"
"v_permlane32_swap_b32 v1, v3\n"
"v_add_f32 v0, v0, v2\n"
"v_add_f32 v1, v1, v3\n"
"s_mov_b64 exec, 0xff\n"
"ds_write_b64 v28, v[0:1] offset:1024\n"
"s_mov_b64 exec, -1\n"
"v_lshl_add_u32 v28, v36, 2, v227\n"
"s_waitcnt lgkmcnt(0)\n"
"s_barrier\n"
"s_cmp_lg_u32 s41, 0\n"
"s_cbranch_scc1 AT_end\n"
"ds_read_b32 v0, v28 offset:1024\n"
"ds_read_b32 v1, v28 offset:1088\n"
"ds_read_b32 v2, v28 offset:1152\n"
"ds_read_b32 v3, v28 offset:1216\n"
"s_mul_i32 s61, s59, 576\n"
"s_add_u32 s61, s61, s58\n"
"s_lshl_b32 s61, s61, 1\n"
"v_lshlrev_b32 v5, 1, v41\n"
"s_waitcnt lgkmcnt(0)\n"
"v_add_f32 v0, v0, v1\n"
"v_add_f32 v2, v2, v3\n"
"v_add_f32 v0, v0, v2\n"
"v_fma_f32 v0, v0, v40, v24\n"
"v_cvt_pk_bf16_f32 v0, v0, v0\n"
"s_mov_b64 exec, 0xffff\n"
"buffer_store_short v0, v5, s[76:79], s61 offen sc1\n"
"s_branch AT_end\n"
"AT_x1:\n"
"s_mov_b32 s87, 0x80000\n"
"AT_x3:\n"
"buffer_load_dword v249, v215, s[28:31], s23 offen sc1\n"
"s_waitcnt vmcnt(0)\n"
"v_cmp_ne_u32 vcc, s85, v249\n"
"s_sub_u32 s87, s87, 1\n"
"s_cbranch_vccz AT_x2\n"
"s_cmp_eq_u32 s87, 0\n"
"s_cbranch_scc1 AT_x2\n"
"s_sleep 1\n"
"s_branch AT_x3\n"
"AT_fb:\n"
"s_waitcnt vmcnt(0) lgkmcnt(0)\n"
"s_barrier\n"
"s_mov_b32 s45, 1\n"
"s_mov_b32 s87, 32\n"
"s_lshl_b32 s84, s41, 12\n"
"s_lshl_b32 s56, s43, 19\n"
"s_add_u32 s84, s84, s56\n"
"v_mov_b32 v160, 0xff800000\n"
"v_mov_b32 v161, 0xff800000\n"
"v_mov_b32 v162, 0xff800000\n"
"v_mov_b32 v163, 0xff800000\n"
"AT_fbl:\n"
"s_mov_b32 m0, s86\n"
"s_nop 0\n"
"buffer_load_dwordx4 v246, s[8:11], s84 offen offset:0 lds\n"
"buffer_load_dwordx4 v246, s[8:11], s84 offen offset:1024 lds\n"
"buffer_load_dwordx4 v247, s[8:11], s84 offen offset:2048 lds\n"
"buffer_load_dwordx4 v247, s[8:11], s84 offen offset:3072 lds\n"
"s_add_u32 s84, s84, 16384\n"
"s_waitcnt vmcnt(0)\n"
"s_barrier\n"
"ds_read_b128 a[128:131], v240 offset:0\n"
"ds_read_b128 a[132:135], v240 offset:256\n"
"ds_read_b128 a[136:139], v240 offset:512\n"
"ds_read_b128 a[140:143], v240 offset:768\n"
"ds_read_b128 a[144:147], v240 offset:4096\n"
"ds_read_b128 a[148:151], v240 offset:4352\n"
"ds_read_b128 a[152:155], v240 offset:4608\n"
"ds_read_b128 a[156:159], v240 offset:4864\n"
"s_waitcnt lgkmcnt(0)\n"
"v_mfma_f32_16x16x32_bf16 v[128:131], a[128:131], v[64:67], 0\n"
"v_mfma_f32_16x16x32_bf16 v[128:131], a[132:135], v[68:71], v[128:131]\n"
"v_mfma_f32_16x16x32_bf16 v[128:131], a[136:139], v[72:75], v[128:131]\n"
"v_mfma_f32_16x16x32_bf16 v[128:131], a[140:143], v[76:79], v[128:131]\n"
"v_mfma_f32_16x16x32_bf16 v[132:135], a[144:147], v[64:67], 0\n"
"v_mfma_f32_16x16x32_bf16 v[132:135], a[148:151], v[68:71], v[132:135]\n"
"v_mfma_f32_16x16x32_bf16 v[132:135], a[152:155], v[72:75], v[132:135]\n"
"v_mfma_f32_16x16x32_bf16 v[132:135], a[156:159], v[76:79], v[132:135]\n"
"v_mfma_f32_16x16x32_bf16 v[136:139], a[128:131], v[80:83], 0\n"
"v_mfma_f32_16x16x32_bf16 v[136:139], a[132:135], v[84:87], v[136:139]\n"
"v_mfma_f32_16x16x32_bf16 v[136:139], a[136:139], v[88:91], v[136:139]\n"
"v_mfma_f32_16x16x32_bf16 v[136:139], a[140:143], v[92:95], v[136:139]\n"
"v_mfma_f32_16x16x32_bf16 v[140:143], a[144:147], v[80:83], 0\n"
"v_mfma_f32_16x16x32_bf16 v[140:143], a[148:151], v[84:87], v[140:143]\n"
"v_mfma_f32_16x16x32_bf16 v[140:143], a[152:155], v[88:91], v[140:143]\n"
"v_mfma_f32_16x16x32_bf16 v[140:143], a[156:159], v[92:95], v[140:143]\n"
"v_mfma_f32_16x16x32_bf16 v[144:147], a[128:131], v[96:99], 0\n"
"v_mfma_f32_16x16x32_bf16 v[144:147], a[132:135], v[100:103], v[144:147]\n"
"v_mfma_f32_16x16x32_bf16 v[144:147], a[136:139], v[104:107], v[144:147]\n"
"v_mfma_f32_16x16x32_bf16 v[144:147], a[140:143], v[108:111], v[144:147]\n"
"v_mfma_f32_16x16x32_bf16 v[148:151], a[144:147], v[96:99], 0\n"
"v_mfma_f32_16x16x32_bf16 v[148:151], a[148:151], v[100:103], v[148:151]\n"
"v_mfma_f32_16x16x32_bf16 v[148:151], a[152:155], v[104:107], v[148:151]\n"
"v_mfma_f32_16x16x32_bf16 v[148:151], a[156:159], v[108:111], v[148:151]\n"
"v_mfma_f32_16x16x32_bf16 v[152:155], a[128:131], v[112:115], 0\n"
"v_mfma_f32_16x16x32_bf16 v[152:155], a[132:135], v[116:119], v[152:155]\n"
"v_mfma_f32_16x16x32_bf16 v[152:155], a[136:139], v[120:123], v[152:155]\n"
"v_mfma_f32_16x16x32_bf16 v[152:155], a[140:143], v[124:127], v[152:155]\n"
"v_mfma_f32_16x16x32_bf16 v[156:159], a[144:147], v[112:115], 0\n"
"v_mfma_f32_16x16x32_bf16 v[156:159], a[148:151], v[116:119], v[156:159]\n"
"v_mfma_f32_16x16x32_bf16 v[156:159], a[152:155], v[120:123], v[156:159]\n"
"v_mfma_f32_16x16x32_bf16 v[156:159], a[156:159], v[124:127], v[156:159]\n"
"s_nop 7\n"
"v_max3_f32 v160, v160, v128, v129\n"
"v_max3_f32 v160, v160, v130, v131\n"
"v_max3_f32 v160, v160, v132, v133\n"
"v_max3_f32 v160, v160, v134, v135\n"
"v_max3_f32 v161, v161, v136, v137\n"
"v_max3_f32 v161, v161, v138, v139\n"
"v_max3_f32 v161, v161, v140, v141\n"
"v_max3_f32 v161, v161, v142, v143\n"
"v_max3_f32 v162, v162, v144, v145\n"
"v_max3_f32 v162, v162, v146, v147\n"
"v_max3_f32 v162, v162, v148, v149\n"
"v_max3_f32 v162, v162, v150, v151\n"
"v_max3_f32 v163, v163, v152, v153\n"
"v_max3_f32 v163, v163, v154, v155\n"
"v_max3_f32 v163, v163, v156, v157\n"
"v_max3_f32 v163, v163, v158, v159\n"
"ds_read_b128 a[128:131], v240 offset:8192\n"
"ds_read_b128 a[132:135], v240 offset:8448\n"
"ds_read_b128 a[136:139], v240 offset:8704\n"
"ds_read_b128 a[140:143], v240 offset:8960\n"
"ds_read_b128 a[144:147], v240 offset:12288\n"
"ds_read_b128 a[148:151], v240 offset:12544\n"
"ds_read_b128 a[152:155], v240 offset:12800\n"
"ds_read_b128 a[156:159], v240 offset:13056\n"
"s_waitcnt lgkmcnt(0)\n"
"v_mfma_f32_16x16x32_bf16 v[128:131], a[128:131], v[64:67], 0\n"
"v_mfma_f32_16x16x32_bf16 v[128:131], a[132:135], v[68:71], v[128:131]\n"
"v_mfma_f32_16x16x32_bf16 v[128:131], a[136:139], v[72:75], v[128:131]\n"
"v_mfma_f32_16x16x32_bf16 v[128:131], a[140:143], v[76:79], v[128:131]\n"
"v_mfma_f32_16x16x32_bf16 v[132:135], a[144:147], v[64:67], 0\n"
"v_mfma_f32_16x16x32_bf16 v[132:135], a[148:151], v[68:71], v[132:135]\n"
"v_mfma_f32_16x16x32_bf16 v[132:135], a[152:155], v[72:75], v[132:135]\n"
"v_mfma_f32_16x16x32_bf16 v[132:135], a[156:159], v[76:79], v[132:135]\n"
"v_mfma_f32_16x16x32_bf16 v[136:139], a[128:131], v[80:83], 0\n"
"v_mfma_f32_16x16x32_bf16 v[136:139], a[132:135], v[84:87], v[136:139]\n"
"v_mfma_f32_16x16x32_bf16 v[136:139], a[136:139], v[88:91], v[136:139]\n"
"v_mfma_f32_16x16x32_bf16 v[136:139], a[140:143], v[92:95], v[136:139]\n"
"v_mfma_f32_16x16x32_bf16 v[140:143], a[144:147], v[80:83], 0\n"
"v_mfma_f32_16x16x32_bf16 v[140:143], a[148:151], v[84:87], v[140:143]\n"
"v_mfma_f32_16x16x32_bf16 v[140:143], a[152:155], v[88:91], v[140:143]\n"
"v_mfma_f32_16x16x32_bf16 v[140:143], a[156:159], v[92:95], v[140:143]\n"
"v_mfma_f32_16x16x32_bf16 v[144:147], a[128:131], v[96:99], 0\n"
"v_mfma_f32_16x16x32_bf16 v[144:147], a[132:135], v[100:103], v[144:147]\n"
"v_mfma_f32_16x16x32_bf16 v[144:147], a[136:139], v[104:107], v[144:147]\n"
"v_mfma_f32_16x16x32_bf16 v[144:147], a[140:143], v[108:111], v[144:147]\n"
"v_mfma_f32_16x16x32_bf16 v[148:151], a[144:147], v[96:99], 0\n"
"v_mfma_f32_16x16x32_bf16 v[148:151], a[148:151], v[100:103], v[148:151]\n"
"v_mfma_f32_16x16x32_bf16 v[148:151], a[152:155], v[104:107], v[148:151]\n"
"v_mfma_f32_16x16x32_bf16 v[148:151], a[156:159], v[108:111], v[148:151]\n"
"v_mfma_f32_16x16x32_bf16 v[152:155], a[128:131], v[112:115], 0\n"
"v_mfma_f32_16x16x32_bf16 v[152:155], a[132:135], v[116:119], v[152:155]\n"
"v_mfma_f32_16x16x32_bf16 v[152:155], a[136:139], v[120:123], v[152:155]\n"
"v_mfma_f32_16x16x32_bf16 v[152:155], a[140:143], v[124:127], v[152:155]\n"
"v_mfma_f32_16x16x32_bf16 v[156:159], a[144:147], v[112:115], 0\n"
"v_mfma_f32_16x16x32_bf16 v[156:159], a[148:151], v[116:119], v[156:159]\n"
"v_mfma_f32_16x16x32_bf16 v[156:159], a[152:155], v[120:123], v[156:159]\n"
"v_mfma_f32_16x16x32_bf16 v[156:159], a[156:159], v[124:127], v[156:159]\n"
"s_nop 7\n"
"v_max3_f32 v160, v160, v128, v129\n"
"v_max3_f32 v160, v160, v130, v131\n"
"v_max3_f32 v160, v160, v132, v133\n"
"v_max3_f32 v160, v160, v134, v135\n"
"v_max3_f32 v161, v161, v136, v137\n"
"v_max3_f32 v161, v161, v138, v139\n"
"v_max3_f32 v161, v161, v140, v141\n"
"v_max3_f32 v161, v161, v142, v143\n"
"v_max3_f32 v162, v162, v144, v145\n"
"v_max3_f32 v162, v162, v146, v147\n"
"v_max3_f32 v162, v162, v148, v149\n"
"v_max3_f32 v162, v162, v150, v151\n"
"v_max3_f32 v163, v163, v152, v153\n"
"v_max3_f32 v163, v163, v154, v155\n"
"v_max3_f32 v163, v163, v156, v157\n"
"v_max3_f32 v163, v163, v158, v159\n"
"s_barrier\n"
"s_sub_u32 s87, s87, 1\n"
"s_cmp_lg_u32 s87, 0\n"
"s_cbranch_scc1 AT_fbl\n"
"v_mov_b32 v164, v160\n"
"s_nop 1\n"
"v_permlane32_swap_b32 v160, v164\n"
"v_max_f32 v160, v160, v164\n"
"v_mov_b32 v164, v160\n"
"s_nop 1\n"
"v_permlane16_swap_b32 v160, v164\n"
"v_max_f32 v160, v160, v164\n"
"v_sub_f32 v24, 0, v160\n"
"v_sub_f32 v25, 0, v160\n"
"v_sub_f32 v26, 0, v160\n"
"v_sub_f32 v27, 0, v160\n"
"v_mov_b32 v164, v161\n"
"s_nop 1\n"
"v_permlane32_swap_b32 v161, v164\n"
"v_max_f32 v161, v161, v164\n"
"v_mov_b32 v164, v161\n"
"s_nop 1\n"
"v_permlane16_swap_b32 v161, v164\n"
"v_max_f32 v161, v161, v164\n"
"v_sub_f32 v28, 0, v161\n"
"v_sub_f32 v29, 0, v161\n"
"v_sub_f32 v30, 0, v161\n"
"v_sub_f32 v31, 0, v161\n"
"v_mov_b32 v164, v162\n"
"s_nop 1\n"
"v_permlane32_swap_b32 v162, v164\n"
"v_max_f32 v162, v162, v164\n"
"v_mov_b32 v164, v162\n"
"s_nop 1\n"
"v_permlane16_swap_b32 v162, v164\n"
"v_max_f32 v162, v162, v164\n"
"v_sub_f32 v32, 0, v162\n"
"v_sub_f32 v33, 0, v162\n"
"v_sub_f32 v34, 0, v162\n"
"v_sub_f32 v35, 0, v162\n"
"v_mov_b32 v164, v163\n"
"s_nop 1\n"
"v_permlane32_swap_b32 v163, v164\n"
"v_max_f32 v163, v163, v164\n"
"v_mov_b32 v164, v163\n"
"s_nop 1\n"
"v_permlane16_swap_b32 v163, v164\n"
"v_max_f32 v163, v163, v164\n"
"v_sub_f32 v36, 0, v163\n"
"v_sub_f32 v37, 0, v163\n"
"v_sub_f32 v38, 0, v163\n"
"v_sub_f32 v39, 0, v163\n"
"s_and_b32 s62, s42, 7\n"
"s_mul_i32 s46, s62, 663552\n"
"s_lshr_b32 s47, s43, 2\n"
"s_mul_i32 s47, s47, 1179648\n"
"s_getpc_b64 s[32:33]\n"
"AT_pc2:\n"
"s_sub_u32 s32, s32, AT_pc2-AT_start\n"
"s_subb_u32 s33, s33, 0\n"
"s_and_b32 s33, s33, 0xffff\n"
"s_mov_b32 s34, AT_kend-AT_start\n"
"s_mov_b32 s35, 0x20000\n"
"s_lshr_b32 s62, s42, 3\n"
"s_lshl_b32 s62, s62, 2\n"
"s_add_u32 s62, s62, s41\n"
"s_and_b32 s62, s62, 31\n"
"s_lshl_b32 s48, s62, 12\n"
"s_lshl_b32 s54, s44, 2\n"
"s_add_u32 s54, s54, s41\n"
"s_mul_i32 s54, s54, 36864\n"
"s_lshr_b32 s62, s43, 2\n"
"s_mul_i32 s62, s62, 2359296\n"
"s_add_u32 s54, s54, s62\n"
"s_add_u32 s54, s54, 139264\n"
"s_bfe_u32 s62, s43, 0x10001\n"
"s_mul_i32 s62, s62, 9216\n"
"s_add_u32 s54, s54, s62\n"
"s_and_b32 s62, s43, 1\n"
"s_lshl_b32 s49, s62, 3\n"
"s_add_u32 s49, s49, s54\n"
"s_add_u32 s49, s49, 4096\n"
"s_mul_i32 s62, s62, 5120\n"
"s_add_u32 s54, s54, s62\n"
"s_and_b32 s62, s43, 1\n"
"s_lshl_b32 s62, s62, 10\n"
"s_sub_u32 s54, s54, s62\n"
"s_add_u32 s49, s54, 4096\n"
"v_mov_b32 v40, 0x3f803f80\n"
"v_mov_b32 v41, 0x3f803f80\n"
"v_mov_b32 v42, 0x3f803f80\n"
"v_mov_b32 v43, 0x3f803f80\n"
"v_mov_b32 v20, 0\n"
"v_mov_b32 v21, 0\n"
"v_mov_b32 v22, 0\n"
"v_mov_b32 v23, 0\n"
"v_mov_b32 v44, 0\n"
"v_mov_b32 v52, 0\n"
"v_mov_b32 v216, -1\n"
"v_mov_b32 v217, -1\n"
"v_mov_b32 v218, -1\n"
"v_mov_b32 v219, -1\n"
"v_lshl_add_u32 v255, v248, 6, s48\n"
"v_lshlrev_b32 v207, 4, v248\n"
"v_add_u32 v208, 18432, v207\n"
"s_lshl_b32 s84, s41, 12\n"
"s_lshl_b32 s56, s43, 19\n"
"s_add_u32 s84, s84, s56\n"
"v_mov_b32 v251, 0x40000000\n"
"v_mov_b32 v252, 0x40000000\n"
"v_mov_b32 v253, 0x40000000\n"
"v_add_f32 v148, 0xc2480000, v161\n"
"v_add_f32 v149, 0xc2480000, v161\n"
"v_add_f32 v150, 0xc2480000, v161\n"
"v_add_f32 v151, 0xc2480000, v161\n"
"v_add_f32 v156, 0xc2480000, v163\n"
"v_add_f32 v157, 0xc2480000, v163\n"
"v_add_f32 v158, 0xc2480000, v163\n"
"v_add_f32 v159, 0xc2480000, v163\n"
"s_mov_b32 m0, s86\n"
"s_nop 0\n"
"buffer_load_dwordx4 v246, s[8:11], s84 offen offset:0 lds\n"
"buffer_load_dwordx4 v246, s[8:11], s84 offen offset:1024 lds\n"
"buffer_load_dwordx4 v247, s[8:11], s84 offen offset:2048 lds\n"
"buffer_load_dwordx4 v247, s[8:11], s84 offen offset:3072 lds\n"
"s_add_u32 m0, s86, 16384\n"
"s_nop 0\n"
"buffer_load_dwordx4 v246, s[12:15], s84 offen offset:0 lds\n"
"buffer_load_dwordx4 v247, s[12:15], s84 offen offset:1024 lds\n"
"buffer_load_dwordx4 v246, s[12:15], s84 offen offset:2048 lds\n"
"buffer_load_dwordx4 v247, s[12:15], s84 offen offset:3072 lds\n"
"s_add_u32 s84, s84, 16384\n"
"s_add_u32 m0, s86, 32768\n"
"s_nop 0\n"
"buffer_load_dwordx4 v246, s[8:11], s84 offen offset:0 lds\n"
"buffer_load_dwordx4 v246, s[8:11], s84 offen offset:1024 lds\n"
"buffer_load_dwordx4 v247, s[8:11], s84 offen offset:2048 lds\n"
"buffer_load_dwordx4 v247, s[8:11], s84 offen offset:3072 lds\n"
"s_add_u32 m0, s86, 49152\n"
"s_nop 0\n"
"buffer_load_dwordx4 v246, s[12:15], s84 offen offset:0 lds\n"
"buffer_load_dwordx4 v247, s[12:15], s84 offen offset:1024 lds\n"
"buffer_load_dwordx4 v246, s[12:15], s84 offen offset:2048 lds\n"
"buffer_load_dwordx4 v247, s[12:15], s84 offen offset:3072 lds\n"
"s_add_u32 s84, s84, 16384\n"
"s_add_u32 m0, s86, 65536\n"
"s_nop 0\n"
"buffer_load_dwordx4 v246, s[8:11], s84 offen offset:0 lds\n"
"buffer_load_dwordx4 v246, s[8:11], s84 offen offset:1024 lds\n"
"buffer_load_dwordx4 v247, s[8:11], s84 offen offset:2048 lds\n"
"buffer_load_dwordx4 v247, s[8:11], s84 offen offset:3072 lds\n"
"s_add_u32 m0, s86, 81920\n"
"s_nop 0\n"
"buffer_load_dwordx4 v246, s[12:15], s84 offen offset:0 lds\n"
"buffer_load_dwordx4 v247, s[12:15], s84 offen offset:1024 lds\n"
"buffer_load_dwordx4 v246, s[12:15], s84 offen offset:2048 lds\n"
"buffer_load_dwordx4 v247, s[12:15], s84 offen offset:3072 lds\n"
"s_add_u32 s84, s84, 16384\n"
"s_add_u32 m0, s86, 98304\n"
"s_nop 0\n"
"buffer_load_dwordx4 v246, s[8:11], s84 offen offset:0 lds\n"
"buffer_load_dwordx4 v246, s[8:11], s84 offen offset:1024 lds\n"
"buffer_load_dwordx4 v247, s[8:11], s84 offen offset:2048 lds\n"
"buffer_load_dwordx4 v247, s[8:11], s84 offen offset:3072 lds\n"
"s_add_u32 m0, s86, 114688\n"
"s_nop 0\n"
"buffer_load_dwordx4 v246, s[12:15], s84 offen offset:0 lds\n"
"buffer_load_dwordx4 v247, s[12:15], s84 offen offset:1024 lds\n"
"buffer_load_dwordx4 v246, s[12:15], s84 offen offset:2048 lds\n"
"buffer_load_dwordx4 v247, s[12:15], s84 offen offset:3072 lds\n"
"s_add_u32 s84, s84, 16384\n"
"s_waitcnt vmcnt(24)\n"
"s_barrier\n"
"ds_read_b128 a[128:131], v240 offset:0\n"
"ds_read_b128 a[132:135], v240 offset:256\n"
"ds_read_b128 a[136:139], v240 offset:512\n"
"ds_read_b128 a[140:143], v240 offset:768\n"
"ds_read_b128 a[144:147], v240 offset:4096\n"
"ds_read_b128 a[148:151], v240 offset:4352\n"
"ds_read_b128 a[152:155], v240 offset:4608\n"
"ds_read_b128 a[156:159], v240 offset:4864\n"
"ds_read_b64_tr_b16 a[192:193], v242 offset:16384\n"
"ds_read_b64_tr_b16 a[194:195], v242 offset:20480\n"
"ds_read_b64_tr_b16 a[196:197], v243 offset:16384\n"
"ds_read_b64_tr_b16 a[198:199], v243 offset:20480\n"
"ds_read_b64_tr_b16 a[200:201], v242 offset:16640\n"
"ds_read_b64_tr_b16 a[202:203], v242 offset:20736\n"
"ds_read_b64_tr_b16 a[204:205], v243 offset:16640\n"
"ds_read_b64_tr_b16 a[206:207], v243 offset:20736\n"
"ds_read_b64_tr_b16 a[208:209], v242 offset:16896\n"
"ds_read_b64_tr_b16 a[210:211], v242 offset:20992\n"
"ds_read_b64_tr_b16 a[212:213], v243 offset:16896\n"
"ds_read_b64_tr_b16 a[214:215], v243 offset:20992\n"
"ds_read_b64_tr_b16 a[216:217], v242 offset:17152\n"
"ds_read_b64_tr_b16 a[218:219], v242 offset:21248\n"
"ds_read_b64_tr_b16 a[220:221], v243 offset:17152\n"
"ds_read_b64_tr_b16 a[222:223], v243 offset:21248\n"
"s_waitcnt lgkmcnt(0)\n"
"s_branch AT_main\n"
"AT_end:\n"
"AT_kend:\n"
  :: [kp] "s"(__builtin_amdgcn_kernarg_segment_ptr()), [blk] "s"(blockIdx.x), [wid] "s"(wid), [lds] "s"(ldsb) : "v0", "v1", "v2", "v3", "v4", "v5", "v6", "v7", "v8", "v9", "v10", "v11", "v12", "v13", "v14", "v15", "v16", "v17", "v18", "v19", "v20", "v21", "v22", "v23", "v24", "v25", "v26", "v27", "v28", "v29", "v30", "v31", "v32", "v33", "v34", "v35", "v36", "v37", "v38", "v39", "v40", "v41", "v42", "v43", "v44", "v45", "v46", "v47", "v48", "v49", "v50", "v51", "v52", "v53", "v54", "v55", "v56", "v57", "v58", "v59", "v60", "v61", "v62", "v63", "v64", "v65", "v66", "v67", "v68", "v69", "v70", "v71", "v72", "v73", "v74", "v75", "v76", "v77", "v78", "v79", "v80", "v81", "v82", "v83", "v84", "v85", "v86", "v87", "v88", "v89", "v90", "v91", "v92", "v93", "v94", "v95", "v96", "v97", "v98", "v99", "v100", "v101", "v102", "v103", "v104", "v105", "v106", "v107", "v108", "v109", "v110", "v111", "v112", "v113", "v114", "v115", "v116", "v117", "v118", "v119", "v120", "v121", "v122", "v123", "v124", "v125", "v126", "v127", "v128", "v129", "v130", "v131", "v132", "v133", "v134", "v135", "v136", "v137", "v138", "v139", "v140", "v141", "v142", "v143", "v144", "v145", "v146", "v147", "v148", "v149", "v150", "v151", "v152", "v153", "v154", "v155", "v156", "v157", "v158", "v159", "v160", "v161", "v162", "v163", "v164", "v165", "v166", "v167", "v168", "v169", "v170", "v171", "v172", "v173", "v174", "v175", "v176", "v177", "v178", "v179", "v180", "v181", "v182", "v183", "v184", "v185", "v186", "v187", "v188", "v189", "v190", "v191", "v192", "v193", "v194", "v195", "v196", "v197", "v198", "v199", "v200", "v201", "v202", "v203", "v204", "v205", "v206", "v207", "v208", "v209", "v210", "v211", "v212", "v213", "v214", "v215", "v216", "v217", "v218", "v219", "v220", "v221", "v222", "v223", "v224", "v225", "v226", "v227", "v228", "v229", "v230", "v231", "v232", "v233", "v234", "v235", "v236", "v237", "v238", "v239", "v240", "v241", "v242", "v243", "v244", "v245", "v246", "v247", "v248", "v249", "v250", "v251", "v252", "v253", "v254", "v255", "a0", "a1", "a2", "a3", "a4", "a5", "a6", "a7", "a8", "a9", "a10", "a11", "a12", "a13", "a14", "a15", "a16", "a17", "a18", "a19", "a20", "a21", "a22", "a23", "a24", "a25", "a26", "a27", "a28", "a29", "a30", "a31", "a32", "a33", "a34", "a35", "a36", "a37", "a38", "a39", "a40", "a41", "a42", "a43", "a44", "a45", "a46", "a47", "a48", "a49", "a50", "a51", "a52", "a53", "a54", "a55", "a56", "a57", "a58", "a59", "a60", "a61", "a62", "a63", "a64", "a65", "a66", "a67", "a68", "a69", "a70", "a71", "a72", "a73", "a74", "a75", "a76", "a77", "a78", "a79", "a80", "a81", "a82", "a83", "a84", "a85", "a86", "a87", "a88", "a89", "a90", "a91", "a92", "a93", "a94", "a95", "a96", "a97", "a98", "a99", "a100", "a101", "a102", "a103", "a104", "a105", "a106", "a107", "a108", "a109", "a110", "a111", "a112", "a113", "a114", "a115", "a116", "a117", "a118", "a119", "a120", "a121", "a122", "a123", "a124", "a125", "a126", "a127", "a128", "a129", "a130", "a131", "a132", "a133", "a134", "a135", "a136", "a137", "a138", "a139", "a140", "a141", "a142", "a143", "a144", "a145", "a146", "a147", "a148", "a149", "a150", "a151", "a152", "a153", "a154", "a155", "a156", "a157", "a158", "a159", "a160", "a161", "a162", "a163", "a164", "a165", "a166", "a167", "a168", "a169", "a170", "a171", "a172", "a173", "a174", "a175", "a176", "a177", "a178", "a179", "a180", "a181", "a182", "a183", "a184", "a185", "a186", "a187", "a188", "a189", "a190", "a191", "a192", "a193", "a194", "a195", "a196", "a197", "a198", "a199", "a200", "a201", "a202", "a203", "a204", "a205", "a206", "a207", "a208", "a209", "a210", "a211", "a212", "a213", "a214", "a215", "a216", "a217", "a218", "a219", "a220", "a221", "a222", "a223", "a224", "a225", "a226", "a227", "a228", "a229", "a230", "a231", "a232", "a233", "a234", "a235", "a236", "a237", "a238", "a239", "a240", "a241", "a242", "a243", "a244", "a245", "a246", "a247", "a248", "a249", "a250", "a251", "a252", "a253", "a254", "a255", "s8", "s9", "s10", "s11", "s12", "s13", "s14", "s15", "s16", "s17", "s18", "s19", "s20", "s21", "s22", "s23", "s24", "s25", "s26", "s27", "s28", "s29", "s30", "s31", "s32", "s33", "s34", "s35", "s36", "s37", "s38", "s39", "s40", "s41", "s42", "s43", "s44", "s45", "s46", "s47", "s48", "s49", "s50", "s51", "s52", "s53", "s54", "s55", "s56", "s57", "s58", "s59", "s60", "s61", "s62", "s63", "s64", "s65", "s66", "s67", "s68", "s69", "s70", "s71", "s72", "s73", "s74", "s75", "s76", "s77", "s78", "s79", "s80", "s81", "s82", "s83", "s84", "s85", "s86", "s87", "s88", "s89", "s90", "s91", "vcc", "scc", "memory");
}

extern "C" void kernel_launch(void* const* d_in, const int* in_sizes, int n_in, void* d_out, int out_size, void* d_ws, size_t ws_size,
                              hipStream_t stream) {
  const unsigned F = 0x20000u;
  KArgs a;
  a.k = {d_in[1], HKV * SQ * 256u, F}; a.v = {d_in[2], HKV * SQ * 256u, F}; a.x = {d_in[0], SQ * DM * 2u, F}; a.wq = {d_in[3], DM * 4096u * 2u, F};
  a.wo = {d_in[4], DM * 4096u * 2u, F}; a.ws = {d_ws, (unsigned)WS_END, F}; a.c = {d_in[10], DM * 2u, F}; a.wmod = {d_in[11], DM * 6u * DM * 2u, F};
  a.wg = {d_in[7], DM * FF * 2u, F}; a.wu = {d_in[8], DM * FF * 2u, F}; a.wd = {d_in[9], DM * FF * 2u, F}; a.out = {d_out, SQ * DM * 2u, F}; a.wr = {d_in[6], DM * 2u, F};
  (void)hipFuncSetAttribute((const void*)k_main, hipFuncAttributeMaxDynamicSharedMemorySize, 163840);
  k_main<<<256, 256, 163840, stream>>>(a);
}
```

```cpp
#include <hip/hip_runtime.h>
#include <stdint.h>


constexpr int SQ = 2048, DM = 576, HKV = 16, FF = 4608;

constexpr size_t WS_FLAG = 0;
constexpr size_t WS_R    = 20480;
constexpr size_t WS_GRAN = 32768;
constexpr size_t WS_EPOCH = 65536;
constexpr size_t WS_ST   = 69632;
constexpr size_t WS_PART = 139264;
constexpr size_t WS_ROW  = WS_PART + (size_t)HKV * SQ * DM * 2;
constexpr size_t WS_Y    = WS_ROW + (size_t)SQ * DM * 16;
constexpr size_t WS_END  = WS_Y + 72 * 192 * 128;

struct Buf { const void* p; unsigned bytes; unsigned fmt; };
struct KArgs { Buf k, v, x, wq, wo, ws, c, wmod, wg, wu, wd, out, wr; };

__global__ __launch_bounds__(256, 1) void k_main(KArgs args) {
  extern __shared__ __attribute__((aligned(16))) char lds[];
  const unsigned wid = __builtin_amdgcn_readfirstlane(threadIdx.x >> 6);
  const unsigned ldsb = (unsigned)(size_t)lds;
  asm volatile(
"AT_start:\n"
"s_load_dwordx16 s[24:39], %[kp], 64\n"
"s_load_dwordx16 s[8:23], %[kp], 0\n"
"s_load_dwordx16 s[64:79], %[kp], 128\n"
"s_load_dwordx4 s[80:83], %[kp], 192\n"
"s_mov_b32 s42, %[blk]\n"
"s_mov_b32 s41, %[wid]\n"
"s_mov_b32 s40, %[lds]\n"
"s_mov_b32 s45, 0\n"
"v_mbcnt_lo_u32_b32 v248, -1, 0\n"
"v_mbcnt_hi_u32_b32 v248, -1, v248\n"
"s_and_b32 s56, s42, 3\n"
"s_lshr_b32 s57, s42, 3\n"
"s_and_b32 s58, s57, 3\n"
"s_lshl_b32 s56, s56, 2\n"
"s_add_u32 s43, s56, s58\n"
"s_lshr_b32 s44, s57, 2\n"
"s_and_b32 s56, s42, 7\n"
"s_lshr_b32 s56, s56, 2\n"
"s_lshl_b32 s56, s56, 3\n"
"s_add_u32 s44, s44, s56\n"
"s_lshr_b32 s56, s42, 5\n"
"s_lshl_b32 s56, s56, 3\n"
"s_and_b32 s58, s42, 7\n"
"s_add_u32 s56, s56, s58\n"
"s_lshl_b32 s56, s56, 2\n"
"s_and_b32 s57, s57, 3\n"
"s_add_u32 s55, s56, s57\n"
"v_lshrrev_b32 v226, 4, v248\n"
"v_bfe_u32 v227, v248, 2, 2\n"
"v_and_b32 v228, 3, v248\n"
"v_and_b32 v229, 15, v248\n"
"v_lshrrev_b32 v230, 3, v248\n"
"v_and_b32 v231, 7, v248\n"
"s_lshl_b32 s56, s41, 6\n"
"v_add_u32 v234, s56, v248\n"
"v_lshrrev_b32 v232, 1, v234\n"
"v_and_b32 v233, 1, v234\n"
"v_lshlrev_b32 v236, 1, v232\n"
"v_mul_u32_u24 v232, 6912, v232\n"
"v_lshl_add_u32 v235, v233, 4, v232\n"
"s_lshl_b32 s3, s55, 5\n"
"s_add_u32 s4, s3, 884736\n"
"s_add_u32 s5, s3, 1769472\n"
"s_add_u32 s6, s3, 2654208\n"
"s_add_u32 s7, s3, 3538944\n"
"v_lshrrev_b32 v232, 1, v230\n"
"v_xor_b32 v232, v232, v231\n"
"v_mul_u32_u24 v233, 1152, v230\n"
"v_lshl_add_u32 v222, v232, 4, v233\n"
"v_xor_b32 v232, 4, v232\n"
"v_lshl_add_u32 v223, v232, 4, v233\n"
"s_lshl_b32 s61, s44, 2\n"
"s_add_u32 s61, s61, s41\n"
"s_mul_i32 s46, s61, 36864\n"
"s_add_u32 s47, s46, 8192\n"
"s_add_u32 s48, s46, 16384\n"
"s_add_u32 s49, s46, 24576\n"
"s_mul_i32 s63, s41, 36864\n"
"s_add_u32 s63, s63, s40\n"
"s_getpc_b64 s[84:85]\n"
"AT_pc0:\n"
"s_sub_u32 s84, s84, AT_pc0-AT_start\n"
"s_subb_u32 s85, s85, 0\n"
"s_and_b32 s85, s85, 0xffff\n"
"s_mov_b32 s86, AT_kend-AT_start\n"
"s_mov_b32 s87, 0x20000\n"
"s_cmp_ge_u32 s42, 224\n"
"s_cselect_b32 s86, s86, 0\n"
"s_lshr_b32 s52, s42, 3\n"
"s_lshl_b32 s52, s52, 2\n"
"s_add_u32 s52, s52, s41\n"
"s_and_b32 s52, s52, 15\n"
"s_lshl_b32 s52, s52, 12\n"
"v_lshlrev_b32 v214, 6, v248\n"
"buffer_load_dword v250, v214, s[84:87], s52 offen\n"
"s_add_u32 s52, s52, 65536\n"
"buffer_load_dword v250, v214, s[84:87], s52 offen\n"
"s_waitcnt lgkmcnt(0)\n"
"s_load_dword s85, s[28:29], 65536\n"
"s_cmp_lt_u32 s55, 216\n"
"s_cselect_b32 s38, s38, 0\n"
"buffer_load_ushort v164, v236, s[32:35], 0 offen offset:0\n"
"buffer_load_ushort v165, v236, s[32:35], 0 offen offset:256\n"
"buffer_load_ushort v166, v236, s[32:35], 0 offen offset:512\n"
"buffer_load_ushort v167, v236, s[32:35], 0 offen offset:768\n"
"buffer_load_ushort v168, v236, s[32:35], 0 offen offset:1024\n"
"buffer_load_dwordx4 v[144:147], v235, s[36:39], s3 offen\n"
"buffer_load_dwordx4 v[148:151], v235, s[36:39], s4 offen\n"
"buffer_load_dwordx4 v[152:155], v235, s[36:39], s5 offen\n"
"buffer_load_dwordx4 v[156:159], v235, s[36:39], s6 offen\n"
"buffer_load_dwordx4 v[160:163], v235, s[36:39], s7 offen\n"
"s_mov_b32 m0, s63\n"
"s_nop 0\n"
"buffer_load_dwordx4 v222, s[16:19], s46 offen offset:0 lds\n"
"buffer_load_dwordx4 v223, s[16:19], s47 offen offset:1024 lds\n"
"buffer_load_dwordx4 v222, s[16:19], s48 offen offset:2048 lds\n"
"buffer_load_dwordx4 v223, s[16:19], s49 offen offset:3072 lds\n"
"s_add_u32 m0, s63, 3968\n"
"s_nop 0\n"
"buffer_load_dwordx4 v222, s[16:19], s46 offen offset:128 lds\n"
"buffer_load_dwordx4 v223, s[16:19], s47 offen offset:1152 lds\n"
"buffer_load_dwordx4 v222, s[16:19], s48 offen offset:2176 lds\n"
"buffer_load_dwordx4 v223, s[16:19], s49 offen offset:3200 lds\n"
"s_lshl_b32 s56, s41, 12\n"
"s_add_u32 s86, s40, s56\n"
"s_lshl_b32 s84, s41, 12\n"
"s_lshl_b32 s56, s43, 19\n"
"s_add_u32 s84, s84, s56\n"
"s_lshl_b32 s50, s41, 17\n"
"s_lshl_b32 s56, s43, 9\n"
"s_add_u32 s50, s50, s56\n"
"s_lshl_b32 s56, s41, 11\n"
"s_add_u32 s51, s40, s56\n"
"s_mov_b32 s61, 32768\n"
"s_mov_b32 s62, 37376\n"
"v_lshlrev_b32 v232, 13, v227\n"
"s_add_u32 m0, s63, 7936\n"
"v_lshl_add_u32 v232, v226, 6, v232\n"
"buffer_load_dwordx4 v222, s[16:19], s46 offen offset:256 lds\n"
"v_lshl_add_u32 v217, v228, 4, v232\n"
"v_and_b32 v232, 2, v227\n"
"v_xor_b32 v232, v232, v226\n"
"v_lshlrev_b32 v233, 10, v227\n"
"v_lshl_add_u32 v233, v228, 6, v233\n"
"v_lshl_add_u32 v233, v232, 4, v233\n"
"v_add_u32 v240, s40, v233\n"
"buffer_load_dwordx4 v223, s[16:19], s47 offen offset:1280 lds\n"
"v_add_u32 v241, 0x10000, v240\n"
"v_lshlrev_b32 v233, 10, v226\n"
"v_lshl_add_u32 v233, v227, 6, v233\n"
"v_lshl_add_u32 v233, v228, 3, v233\n"
"v_add_u32 v233, s40, v233\n"
"v_and_b32 v232, 1, v226\n"
"v_lshl_add_u32 v242, v232, 5, v233\n"
"buffer_load_dwordx4 v222, s[16:19], s48 offen offset:2304 lds\n"
"v_xor_b32 v243, 32, v242\n"
"v_add_u32 v244, 0x10000, v242\n"
"v_add_u32 v245, 0x10000, v243\n"
"v_lshlrev_b32 v233, 8, v227\n"
"v_lshl_add_u32 v233, v226, 6, v233\n"
"v_lshl_add_u32 v246, v228, 4, v233\n"
"v_xor_b32 v247, 32, v246\n"
"buffer_load_dwordx4 v223, s[16:19], s49 offen offset:3328 lds\n"
"v_mul_u32_u24 v233, 16392, v232\n"
"v_lshrrev_b32 v232, 1, v226\n"
"v_lshl_add_u32 v233, v232, 11, v233\n"
"v_lshl_add_u32 v233, v227, 6, v233\n"
"v_lshl_add_u32 v233, v228, 4, v233\n"
"v_add_u32 v218, s40, v233\n"
"s_add_u32 m0, s63, 11904\n"
"v_add_u32 v219, 65568, v218\n"
"buffer_load_dwordx4 v222, s[16:19], s46 offen offset:384 lds\n"
"v_lshrrev_b32 v232, 1, v229\n"
"v_xor_b32 v232, v232, v226\n"
"v_lshlrev_b32 v233, 7, v229\n"
"v_lshl_add_u32 v233, v232, 4, v233\n"
"v_add_u32 v220, s63, v233\n"
"v_xor_b32 v221, 64, v220\n"
"s_add_u32 s56, s40, 148480\n"
"v_lshl_add_u32 v224, v226, 4, s56\n"
"v_lshrrev_b32 v232, 3, v234\n"
"v_lshlrev_b32 v233, 8, v232\n"
"v_and_b32 v232, 1, v234\n"
"v_lshl_add_u32 v233, v232, 6, v233\n"
"v_bfe_u32 v232, v234, 1, 2\n"
"v_lshl_add_u32 v233, v232, 4, v233\n"
"buffer_load_dwordx4 v223, s[16:19], s47 offen offset:1408 lds\n"
"v_add_u32 v216, s56, v233\n"
"v_lshlrev_b32 v225, 5, v234\n"
"v_mov_b32 v232, 0x40000000\n"
"v_cmp_gt_u32 vcc, 144, v234\n"
"s_nop 1\n"
"v_cndmask_b32 v225, v232, v225, vcc\n"
"s_lshl_b32 s56, s41, 8\n"
"s_add_u32 s57, s40, 147456\n"
"s_add_u32 s56, s56, s57\n"
"v_and_b32 v232, 1, v248\n"
"v_lshlrev_b32 v232, 5, v232\n"
"v_lshl_add_u32 v232, v226, 6, v232\n"
"v_add_u32 v238, s56, v232\n"
"v_lshlrev_b32 v232, 8, v226\n"
"buffer_load_dwordx4 v222, s[16:19], s48 offen offset:2432 lds\n"
"v_lshl_add_u32 v232, v229, 2, v232\n"
"v_add_u32 v239, s57, v232\n"
"v_lshlrev_b32 v232, 6, v228\n"
"v_lshl_add_u32 v232, v226, 4, v232\n"
"v_add_u32 v254, s40, v232\n"
"s_lshr_b32 s57, s42, 3\n"
"s_lshl_b32 s57, s57, 2\n"
"s_add_u32 s57, s57, s41\n"
"s_sub_u32 s57, s57, 128\n"
"s_and_b32 s58, s42, 7\n"
"s_mul_i32 s59, s57, 9216\n"
"s_mul_i32 s60, s58, 1152\n"
"s_add_u32 s59, s59, s60\n"
"v_lshlrev_b32 v252, 6, v248\n"
"buffer_load_dwordx4 v223, s[16:19], s49 offen offset:3456 lds\n"
"v_add_u32 v253, 0x40000000, v252\n"
"v_cmp_gt_u32 vcc, 18, v248\n"
"s_nop 1\n"
"v_cndmask_b32 v251, v253, v252, vcc\n"
"v_cmp_gt_u32 vcc, 36, v248\n"
"s_nop 1\n"
"v_cndmask_b32 v253, v253, v252, vcc\n"
"s_mul_i32 s60, s57, 1152\n"
"s_lshl_b32 s57, s60, 1\n"
"v_add_u32 v252, s60, v251\n"
"v_add_u32 v251, s59, v251\n"
"v_add_u32 v253, s57, v253\n"
"v_mov_b32 v237, 0xffff0000\n"
"s_add_u32 m0, s63, 15872\n"
"v_mov_b32 v232, 0x3f803f80\n"
"buffer_load_dwordx4 v222, s[16:19], s46 offen offset:512 lds\n"
"v_accvgpr_write_b32 a0, v232\n"
"v_accvgpr_write_b32 a1, v232\n"
"v_accvgpr_write_b32 a2, v232\n"
"v_accvgpr_write_b32 a3, v232\n"
"s_waitcnt vmcnt(22)\n"
"v_lshlrev_b32 v164, 16, v164\n"
"v_mul_f32 v184, 0xbfb8aa3b, v164\n"
"v_lshlrev_b32 v165, 16, v165\n"
"v_mul_f32 v185, 0xbfb8aa3b, v165\n"
"v_lshlrev_b32 v166, 16, v166\n"
"v_mul_f32 v186, 0xbfb8aa3b, v166\n"
"v_lshlrev_b32 v167, 16, v167\n"
"v_mul_f32 v187, 0xbfb8aa3b, v167\n"
"v_lshlrev_b32 v168, 16, v168\n"
"v_mul_f32 v188, 0xbfb8aa3b, v168\n"
"v_exp_f32 v184, v184\n"
"buffer_load_dwordx4 v223, s[16:19], s47 offen offset:1536 lds\n"
"v_exp_f32 v185, v185\n"
"v_exp_f32 v186, v186\n"
"v_exp_f32 v187, v187\n"
"v_exp_f32 v188, v188\n"
"v_add_f32 v184, 1.0, v184\n"
"v_add_f32 v185, 1.0, v185\n"
"v_add_f32 v186, 1.0, v186\n"
"v_add_f32 v187, 1.0, v187\n"
"v_add_f32 v188, 1.0, v188\n"
"v_rcp_f32 v184, v184\n"
"v_rcp_f32 v185, v185\n"
"v_rcp_f32 v186, v186\n"
"buffer_load_dwordx4 v222, s[16:19], s48 offen offset:2560 lds\n"
"v_rcp_f32 v187, v187\n"
"v_rcp_f32 v188, v188\n"
"v_mul_f32 v164, v164, v184\n"
"v_mul_f32 v165, v165, v185\n"
"v_mul_f32 v166, v166, v186\n"
"v_mul_f32 v167, v167, v187\n"
"v_mul_f32 v168, v168, v188\n"
"v_cvt_pk_bf16_f32 v164, v164, v164\n"
"v_and_b32 v164, v237, v164\n"
"v_cvt_pk_bf16_f32 v165, v165, v165\n"
"v_and_b32 v165, v237, v165\n"
"v_cvt_pk_bf16_f32 v166, v166, v166\n"
"buffer_load_dwordx4 v223, s[16:19], s49 offen offset:3584 lds\n"
"v_and_b32 v166, v237, v166\n"
"v_cvt_pk_bf16_f32 v167, v167, v167\n"
"v_and_b32 v167, v237, v167\n"
"v_cvt_pk_bf16_f32 v168, v168, v168\n"
"v_and_b32 v168, v237, v168\n"
"s_waitcnt vmcnt(24)\n"
"v_lshlrev_b32 v184, 16, v144\n"
"v_and_b32 v185, v237, v144\n"
"v_lshlrev_b32 v186, 16, v145\n"
"v_and_b32 v187, v237, v145\n"
"v_lshlrev_b32 v188, 16, v146\n"
"s_add_u32 m0, s63, 19840\n"
"v_and_b32 v189, v237, v146\n"
"buffer_load_dwordx4 v222, s[16:19], s46 offen offset:640 lds\n"
"v_lshlrev_b32 v190, 16, v147\n"
"v_and_b32 v191, v237, v147\n"
"v_mul_f32 v176, v164, v184\n"
"v_mul_f32 v177, v164, v185\n"
"v_mul_f32 v178, v164, v186\n"
"v_mul_f32 v179, v164, v187\n"
"v_mul_f32 v180, v164, v188\n"
"v_mul_f32 v181, v164, v189\n"
"v_mul_f32 v182, v164, v190\n"
"v_mul_f32 v183, v164, v191\n"
"s_waitcnt vmcnt(24)\n"
"v_lshlrev_b32 v184, 16, v148\n"
"buffer_load_dwordx4 v223, s[16:19], s47 offen offset:1664 lds\n"
"v_and_b32 v185, v237, v148\n"
"v_lshlrev_b32 v186, 16, v149\n"
"v_and_b32 v187, v237, v149\n"
"v_lshlrev_b32 v188, 16, v150\n"
"v_and_b32 v189, v237, v150\n"
"v_lshlrev_b32 v190, 16, v151\n"
"v_and_b32 v191, v237, v151\n"
"v_fmac_f32 v176, v165, v184\n"
"v_fmac_f32 v177, v165, v185\n"
"v_fmac_f32 v178, v165, v186\n"
"v_fmac_f32 v179, v165, v187\n"
"v_fmac_f32 v180, v165, v188\n"
"buffer_load_dwordx4 v222, s[16:19], s48 offen offset:2688 lds\n"
"v_fmac_f32 v181, v165, v189\n"
"v_fmac_f32 v182, v165, v190\n"
"v_fmac_f32 v183, v165, v191\n"
"s_waitcnt vmcnt(25)\n"
"v_lshlrev_b32 v184, 16, v152\n"
"v_and_b32 v185, v237, v152\n"
"v_lshlrev_b32 v186, 16, v153\n"
"v_and_b32 v187, v237, v153\n"
"v_lshlrev_b32 v188, 16, v154\n"
"v_and_b32 v189, v237, v154\n"
"v_lshlrev_b32 v190, 16, v155\n"
"v_and_b32 v191, v237, v155\n"
"buffer_load_dwordx4 v223, s[16:19], s49 offen offset:3712 lds\n"
"v_fmac_f32 v176, v166, v184\n"
"v_fmac_f32 v177, v166, v185\n"
"v_fmac_f32 v178, v166, v186\n"
"v_fmac_f32 v179, v166, v187\n"
"v_fmac_f32 v180, v166, v188\n"
"v_fmac_f32 v181, v166, v189\n"
"v_fmac_f32 v182, v166, v190\n"
"v_fmac_f32 v183, v166, v191\n"
"s_waitcnt vmcnt(25)\n"
"v_lshlrev_b32 v184, 16, v156\n"
"v_and_b32 v185, v237, v156\n"
"s_add_u32 m0, s63, 23808\n"
"v_lshlrev_b32 v186, 16, v157\n"
"buffer_load_dwordx4 v222, s[16:19], s46 offen offset:768 lds\n"
"v_and_b32 v187, v237, v157\n"
"v_lshlrev_b32 v188, 16, v158\n"
"v_and_b32 v189, v237, v158\n"
"v_lshlrev_b32 v190, 16, v159\n"
"v_and_b32 v191, v237, v159\n"
"v_fmac_f32 v176, v167, v184\n"
"v_fmac_f32 v177, v167, v185\n"
"v_fmac_f32 v178, v167, v186\n"
"v_fmac_f32 v179, v167, v187\n"
"v_fmac_f32 v180, v167, v188\n"
"v_fmac_f32 v181, v167, v189\n"
"v_fmac_f32 v182, v167, v190\n"
"buffer_load_dwordx4 v223, s[16:19], s47 offen offset:1792 lds\n"
"v_fmac_f32 v183, v167, v191\n"
"s_waitcnt vmcnt(26)\n"
"v_lshlrev_b32 v184, 16, v160\n"
"v_and_b32 v185, v237, v160\n"
"v_lshlrev_b32 v186, 16, v161\n"
"v_and_b32 v187, v237, v161\n"
"v_lshlrev_b32 v188, 16, v162\n"
"v_and_b32 v189, v237, v162\n"
"v_lshlrev_b32 v190, 16, v163\n"
"v_and_b32 v191, v237, v163\n"
"v_fmac_f32 v176, v168, v184\n"
"v_fmac_f32 v177, v168, v185\n"
"buffer_load_dwordx4 v222, s[16:19], s48 offen offset:2816 lds\n"
"v_fmac_f32 v178, v168, v186\n"
"v_fmac_f32 v179, v168, v187\n"
"v_fmac_f32 v180, v168, v188\n"
"v_fmac_f32 v181, v168, v189\n"
"v_fmac_f32 v182, v168, v190\n"
"v_fmac_f32 v183, v168, v191\n"
"v_add_f32_dpp v176, v176, v176 row_ror:2 row_mask:0xf bank_mask:0xf\n"
"v_add_f32_dpp v177, v177, v177 row_ror:2 row_mask:0xf bank_mask:0xf\n"
"v_add_f32_dpp v178, v178, v178 row_ror:2 row_mask:0xf bank_mask:0xf\n"
"v_add_f32_dpp v179, v179, v179 row_ror:2 row_mask:0xf bank_mask:0xf\n"
"v_add_f32_dpp v180, v180, v180 row_ror:2 row_mask:0xf bank_mask:0xf\n"
"v_add_f32_dpp v181, v181, v181 row_ror:2 row_mask:0xf bank_mask:0xf\n"
"buffer_load_dwordx4 v223, s[16:19], s49 offen offset:3840 lds\n"
"v_add_f32_dpp v182, v182, v182 row_ror:2 row_mask:0xf bank_mask:0xf\n"
"v_add_f32_dpp v183, v183, v183 row_ror:2 row_mask:0xf bank_mask:0xf\n"
"v_add_f32_dpp v176, v176, v176 row_ror:4 row_mask:0xf bank_mask:0xf\n"
"v_add_f32_dpp v177, v177, v177 row_ror:4 row_mask:0xf bank_mask:0xf\n"
"v_add_f32_dpp v178, v178, v178 row_ror:4 row_mask:0xf bank_mask:0xf\n"
"v_add_f32_dpp v179, v179, v179 row_ror:4 row_mask:0xf bank_mask:0xf\n"
"v_add_f32_dpp v180, v180, v180 row_ror:4 row_mask:0xf bank_mask:0xf\n"
"v_add_f32_dpp v181, v181, v181 row_ror:4 row_mask:0xf bank_mask:0xf\n"
"v_add_f32_dpp v182, v182, v182 row_ror:4 row_mask:0xf bank_mask:0xf\n"
"v_add_f32_dpp v183, v183, v183 row_ror:4 row_mask:0xf bank_mask:0xf\n"
"v_add_f32_dpp v176, v176, v176 row_ror:8 row_mask:0xf bank_mask:0xf\n"
"s_add_u32 m0, s63, 27776\n"
"v_add_f32_dpp v177, v177, v177 row_ror:8 row_mask:0xf bank_mask:0xf\n"
"buffer_load_dwordx4 v222, s[16:19], s46 offen offset:896 lds\n"
"v_add_f32_dpp v178, v178, v178 row_ror:8 row_mask:0xf bank_mask:0xf\n"
"v_add_f32_dpp v179, v179, v179 row_ror:8 row_mask:0xf bank_mask:0xf\n"
"v_add_f32_dpp v180, v180, v180 row_ror:8 row_mask:0xf bank_mask:0xf\n"
"v_add_f32_dpp v181, v181, v181 row_ror:8 row_mask:0xf bank_mask:0xf\n"
"v_add_f32_dpp v182, v182, v182 row_ror:8 row_mask:0xf bank_mask:0xf\n"
"v_add_f32_dpp v183, v183, v183 row_ror:8 row_mask:0xf bank_mask:0xf\n"
"s_mov_b32 exec_lo, 0x00030003\n"
"s_mov_b32 exec_hi, 0x00030003\n"
"ds_write_b128 v238, v[176:179]\n"
"ds_write_b128 v238, v[180:183] offset:16\n"
"s_mov_b64 exec, -1\n"
"s_waitcnt lgkmcnt(0)\n"
"s_barrier\n"
"ds_read_b32 v184, v239 offset:0\n"
"ds_read_b32 v185, v239 offset:64\n"
"ds_read_b32 v186, v239 offset:128\n"
"ds_read_b32 v187, v239 offset:192\n"
"s_add_u32 s85, s85, 1\n"
"s_lshl_b32 s56, s55, 7\n"
"s_add_u32 s56, s56, 32768\n"
"s_lshl_b32 s58, s41, 2\n"
"s_lshl_b32 s58, 15, s58\n"
"v_lshlrev_b32 v190, 3, v248\n"
"v_mov_b32 v189, s85\n"
"s_waitcnt lgkmcnt(2)\n"
"v_add_f32 v184, v184, v185\n"
"s_waitcnt lgkmcnt(0)\n"
"v_add_f32 v186, v186, v187\n"
"v_add_f32 v184, v184, v186\n"
"v_mov_b32 v185, v184\n"
"s_nop 1\n"
"v_permlane32_swap_b32 v184, v185\n"
"v_add_f32 v184, v184, v185\n"
"v_mov_b32 v185, v184\n"
"s_nop 1\n"
"v_permlane16_swap_b32 v184, v185\n"
"v_add_f32 v184, v184, v185\n"
"v_cvt_pk_bf16_f32 v188, v184, v184\n"
"v_and_b32 v188, v237, v188\n"
"s_mov_b32 exec_lo, s58\n"
"s_mov_b32 exec_hi, 0\n"
"buffer_store_dwordx2 v[188:189], v190, s[28:31], s56 offen sc1\n"
"s_mov_b64 exec, -1\n"
"s_waitcnt vmcnt(26)\n"
"ds_read_b128 v[0:3], v220 offset:0\n"
"ds_read_b128 v[4:7], v220 offset:2048\n"
"ds_read_b128 v[8:11], v221 offset:0\n"
"ds_read_b128 v[12:15], v221 offset:2048\n"
"s_waitcnt vmcnt(22)\n"
"ds_read_b128 v[16:19], v220 offset:4096\n"
"ds_read_b128 v[20:23], v220 offset:6144\n"
"ds_read_b128 v[24:27], v221 offset:4096\n"
"ds_read_b128 v[28:31], v221 offset:6144\n"
"s_waitcnt lgkmcnt(7)\n"
"v_mfma_f32_16x16x32_bf16 v[192:195], a[0:3], v[0:3], 0\n"
"v_mfma_f32_16x16x32_bf16 v[200:203], v[0:3], v[0:3], 0\n"
"s_waitcnt lgkmcnt(6)\n"
"v_mfma_f32_16x16x32_bf16 v[196:199], a[0:3], v[4:7], 0\n"
"v_mfma_f32_16x16x32_bf16 v[204:207], v[4:7], v[4:7], 0\n"
"s_waitcnt lgkmcnt(5)\n"
"v_mfma_f32_16x16x32_bf16 v[192:195], a[0:3], v[8:11], v[192:195]\n"
"v_mfma_f32_16x16x32_bf16 v[200:203], v[8:11], v[8:11], v[200:203]\n"
"s_waitcnt lgkmcnt(4)\n"
"v_mfma_f32_16x16x32_bf16 v[196:199], a[0:3], v[12:15], v[196:199]\n"
"v_mfma_f32_16x16x32_bf16 v[204:207], v[12:15], v[12:15], v[204:207]\n"
"s_waitcnt lgkmcnt(3)\n"
"s_waitcnt vmcnt(18)\n"
"ds_read_b128 v[32:35], v220 offset:8192\n"
"ds_read_b128 v[36:39], v220 offset:10240\n"
"ds_read_b128 v[40:43], v221 offset:8192\n"
"ds_read_b128 v[44:47], v221 offset:10240\n"
"v_mfma_f32_16x16x32_bf16 v[192:195], a[0:3], v[16:19], v[192:195]\n"
"v_lshlrev_b32 v144, 16, v0\n"
"v_and_b32 v0, v237, v0\n"
"v_mfma_f32_16x16x32_bf16 v[200:203], v[16:19], v[16:19], v[200:203]\n"
"s_waitcnt lgkmcnt(6)\n"
"v_lshlrev_b32 v145, 16, v1\n"
"v_and_b32 v1, v237, v1\n"
"v_mfma_f32_16x16x32_bf16 v[196:199], a[0:3], v[20:23], v[196:199]\n"
"v_lshlrev_b32 v146, 16, v2\n"
"v_and_b32 v2, v237, v2\n"
"v_mfma_f32_16x16x32_bf16 v[204:207], v[20:23], v[20:23], v[204:207]\n"
"s_waitcnt lgkmcnt(4)\n"
"v_lshlrev_b32 v147, 16, v3\n"
"v_and_b32 v3, v237, v3\n"
"v_mfma_f32_16x16x32_bf16 v[192:195], a[0:3], v[24:27], v[192:195]\n"
"v_lshlrev_b32 v148, 16, v4\n"
"v_and_b32 v4, v237, v4\n"
"v_mfma_f32_16x16x32_bf16 v[200:203], v[24:27], v[24:27], v[200:203]\n"
"v_lshlrev_b32 v149, 16, v5\n"
"v_and_b32 v5, v237, v5\n"
"v_mfma_f32_16x16x32_bf16 v[196:199], a[0:3], v[28:31], v[196:199]\n"
"v_lshlrev_b32 v150, 16, v6\n"
"v_and_b32 v6, v237, v6\n"
"v_mfma_f32_16x16x32_bf16 v[204:207], v[28:31], v[28:31], v[204:207]\n"
"s_waitcnt lgkmcnt(3)\n"
"v_lshlrev_b32 v151, 16, v7\n"
"v_and_b32 v7, v237, v7\n"
"s_waitcnt vmcnt(14)\n"
"ds_read_b128 v[48:51], v220 offset:12288\n"
"ds_read_b128 v[52:55], v220 offset:14336\n"
"ds_read_b128 v[56:59], v221 offset:12288\n"
"ds_read_b128 v[60:63], v221 offset:14336\n"
"v_mfma_f32_16x16x32_bf16 v[192:195], a[0:3], v[32:35], v[192:195]\n"
"v_lshlrev_b32 v152, 16, v8\n"
"v_and_b32 v8, v237, v8\n"
"v_mfma_f32_16x16x32_bf16 v[200:203], v[32:35], v[32:35], v[200:203]\n"
"s_waitcnt lgkmcnt(6)\n"
"v_lshlrev_b32 v153, 16, v9\n"
"v_and_b32 v9, v237, v9\n"
"v_mfma_f32_16x16x32_bf16 v[196:199], a[0:3], v[36:39], v[196:199]\n"
"v_lshlrev_b32 v154, 16, v10\n"
"v_and_b32 v10, v237, v10\n"
"v_mfma_f32_16x16x32_bf16 v[204:207], v[36:39], v[36:39], v[204:207]\n"
"s_waitcnt lgkmcnt(4)\n"
"v_lshlrev_b32 v155, 16, v11\n"
"v_and_b32 v11, v237, v11\n"
"v_mfma_f32_16x16x32_bf16 v[192:195], a[0:3], v[40:43], v[192:195]\n"
"v_lshlrev_b32 v156, 16, v12\n"
"v_and_b32 v12, v237, v12\n"
"v_mfma_f32_16x16x32_bf16 v[200:203], v[40:43], v[40:43], v[200:203]\n"
"v_lshlrev_b32 v157, 16, v13\n"
"v_and_b32 v13, v237, v13\n"
"v_mfma_f32_16x16x32_bf16 v[196:199], a[0:3], v[44:47], v[196:199]\n"
"v_lshlrev_b32 v158, 16, v14\n"
"v_and_b32 v14, v237, v14\n"
"v_mfma_f32_16x16x32_bf16 v[204:207], v[44:47], v[44:47], v[204:207]\n"
"s_waitcnt lgkmcnt(3)\n"
"v_lshlrev_b32 v159, 16, v15\n"
"v_and_b32 v15, v237, v15\n"
"buffer_load_dwordx4 v223, s[16:19], s47 offen offset:1920 lds\n"
"buffer_load_dwordx4 v222, s[16:19], s48 offen offset:2944 lds\n"
"buffer_load_dwordx4 v223, s[16:19], s49 offen offset:3968 lds\n"
"s_waitcnt vmcnt(13)\n"
"ds_read_b128 v[64:67], v220 offset:16384\n"
"ds_read_b128 v[68:71], v220 offset:18432\n"
"ds_read_b128 v[72:75], v221 offset:16384\n"
"ds_read_b128 v[76:79], v221 offset:18432\n"
"v_mfma_f32_16x16x32_bf16 v[192:195], a[0:3], v[48:51], v[192:195]\n"
"v_lshlrev_b32 v160, 16, v16\n"
"v_and_b32 v16, v237, v16\n"
"v_mfma_f32_16x16x32_bf16 v[200:203], v[48:51], v[48:51], v[200:203]\n"
"s_waitcnt lgkmcnt(6)\n"
"v_lshlrev_b32 v161, 16, v17\n"
"v_and_b32 v17, v237, v17\n"
"v_mfma_f32_16x16x32_bf16 v[196:199], a[0:3], v[52:55], v[196:199]\n"
"v_lshlrev_b32 v162, 16, v18\n"
"v_and_b32 v18, v237, v18\n"
"v_mfma_f32_16x16x32_bf16 v[204:207], v[52:55], v[52:55], v[204:207]\n"
"s_waitcnt lgkmcnt(4)\n"
"v_lshlrev_b32 v163, 16, v19\n"
"v_and_b32 v19, v237, v19\n"
"v_mfma_f32_16x16x32_bf16 v[192:195], a[0:3], v[56:59], v[192:195]\n"
"v_lshlrev_b32 v164, 16, v20\n"
"v_and_b32 v20, v237, v20\n"
"v_mfma_f32_16x16x32_bf16 v[200:203], v[56:59], v[56:59], v[200:203]\n"
"v_lshlrev_b32 v165, 16, v21\n"
"v_and_b32 v21, v237, v21\n"
"v_mfma_f32_16x16x32_bf16 v[196:199], a[0:3], v[60:63], v[196:199]\n"
"v_lshlrev_b32 v166, 16, v22\n"
"v_and_b32 v22, v237, v22\n"
"v_mfma_f32_16x16x32_bf16 v[204:207], v[60:63], v[60:63], v[204:207]\n"
"s_waitcnt lgkmcnt(3)\n"
"v_lshlrev_b32 v167, 16, v23\n"
"v_and_b32 v23, v237, v23\n"
"s_add_u32 m0, s63, 31744\n"
"s_nop 0\n"
"buffer_load_dwordx4 v222, s[16:19], s46 offen offset:1024 lds\n"
"buffer_load_dwordx4 v223, s[16:19], s47 offen offset:2048 lds\n"
"buffer_load_dwordx4 v222, s[16:19], s48 offen offset:3072 lds\n"
"s_add_u32 s49, s49, 128\n"
"s_add_u32 m0, s63, 31872\n"
"s_nop 0\n"
"buffer_load_dwordx4 v223, s[16:19], s49 offen offset:3968 lds\n"
"s_waitcnt vmcnt(13)\n"
"ds_read_b128 v[80:83], v220 offset:20480\n"
"ds_read_b128 v[84:87], v220 offset:22528\n"
"ds_read_b128 v[88:91], v221 offset:20480\n"
"ds_read_b128 v[92:95], v221 offset:22528\n"
"v_mfma_f32_16x16x32_bf16 v[192:195], a[0:3], v[64:67], v[192:195]\n"
"v_lshlrev_b32 v168, 16, v24\n"
"v_and_b32 v24, v237, v24\n"
"v_mfma_f32_16x16x32_bf16 v[200:203], v[64:67], v[64:67], v[200:203]\n"
"s_waitcnt lgkmcnt(6)\n"
"v_lshlrev_b32 v169, 16, v25\n"
"v_and_b32 v25, v237, v25\n"
"v_mfma_f32_16x16x32_bf16 v[196:199], a[0:3], v[68:71], v[196:199]\n"
"v_lshlrev_b32 v170, 16, v26\n"
"v_and_b32 v26, v237, v26\n"
"v_mfma_f32_16x16x32_bf16 v[204:207], v[68:71], v[68:71], v[204:207]\n"
"s_waitcnt lgkmcnt(4)\n"
"v_lshlrev_b32 v171, 16, v27\n"
"v_and_b32 v27, v237, v27\n"
"v_mfma_f32_16x16x32_bf16 v[192:195], a[0:3], v[72:75], v[192:195]\n"
"v_lshlrev_b32 v172, 16, v28\n"
"v_and_b32 v28, v237, v28\n"
"v_mfma_f32_16x16x32_bf16 v[200:203], v[72:75], v[72:75], v[200:203]\n"
"v_lshlrev_b32 v173, 16, v29\n"
"v_and_b32 v29, v237, v29\n"
"v_mfma_f32_16x16x32_bf16 v[196:199], a[0:3], v[76:79], v[196:199]\n"
"v_lshlrev_b32 v174, 16, v30\n"
"v_and_b32 v30, v237, v30\n"
"v_mfma_f32_16x16x32_bf16 v[204:207], v[76:79], v[76:79], v[204:207]\n"
"s_waitcnt lgkmcnt(3)\n"
"v_lshlrev_b32 v175, 16, v31\n"
"v_and_b32 v31, v237, v31\n"
"s_waitcnt vmcnt(9)\n"
"ds_read_b128 v[96:99], v220 offset:24576\n"
"ds_read_b128 v[100:103], v220 offset:26624\n"
"ds_read_b128 v[104:107], v221 offset:24576\n"
"ds_read_b128 v[108:111], v221 offset:26624\n"
"v_mfma_f32_16x16x32_bf16 v[192:195], a[0:3], v[80:83], v[192:195]\n"
"v_lshlrev_b32 v176, 16, v32\n"
"v_and_b32 v32, v237, v32\n"
"v_mfma_f32_16x16x32_bf16 v[200:203], v[80:83], v[80:83], v[200:203]\n"
"s_waitcnt lgkmcnt(6)\n"
"v_lshlrev_b32 v177, 16, v33\n"
"v_and_b32 v33, v237, v33\n"
"v_mfma_f32_16x16x32_bf16 v[196:199], a[0:3], v[84:87], v[196:199]\n"
"v_lshlrev_b32 v178, 16, v34\n"
"v_and_b32 v34, v237, v34\n"
"v_mfma_f32_16x16x32_bf16 v[204:207], v[84:87], v[84:87], v[204:207]\n"
"s_waitcnt lgkmcnt(4)\n"
"v_lshlrev_b32 v179, 16, v35\n"
"v_and_b32 v35, v237, v35\n"
"v_mfma_f32_16x16x32_bf16 v[192:195], a[0:3], v[88:91], v[192:195]\n"
"v_lshlrev_b32 v180, 16, v36\n"
"v_and_b32 v36, v237, v36\n"
"v_mfma_f32_16x16x32_bf16 v[200:203], v[88:91], v[88:91], v[200:203]\n"
"v_lshlrev_b32 v181, 16, v37\n"
"v_and_b32 v37, v237, v37\n"
"v_mfma_f32_16x16x32_bf16 v[196:199], a[0:3], v[92:95], v[196:199]\n"
"v_lshlrev_b32 v182, 16, v38\n"
"v_and_b32 v38, v237, v38\n"
"v_mfma_f32_16x16x32_bf16 v[204:207], v[92:95], v[92:95], v[204:207]\n"
"s_waitcnt lgkmcnt(3)\n"
"v_lshlrev_b32 v183, 16, v39\n"
"v_and_b32 v39, v237, v39\n"
"s_waitcnt vmcnt(4)\n"
"ds_read_b128 v[112:115], v220 offset:28672\n"
"ds_read_b128 v[116:119], v220 offset:30720\n"
"ds_read_b128 v[120:123], v221 offset:28672\n"
"ds_read_b128 v[124:127], v221 offset:30720\n"
"v_mfma_f32_16x16x32_bf16 v[192:195], a[0:3], v[96:99], v[192:195]\n"
"v_lshlrev_b32 v184, 16, v40\n"
"v_and_b32 v40, v237, v40\n"
"v_mfma_f32_16x16x32_bf16 v[200:203], v[96:99], v[96:99], v[200:203]\n"
"s_waitcnt lgkmcnt(6)\n"
"v_lshlrev_b32 v185, 16, v41\n"
"v_and_b32 v41, v237, v41\n"
"v_mfma_f32_16x16x32_bf16 v[196:199], a[0:3], v[100:103], v[196:199]\n"
"v_lshlrev_b32 v186, 16, v42\n"
"v_and_b32 v42, v237, v42\n"
"v_mfma_f32_16x16x32_bf16 v[204:207], v[100:103], v[100:103], v[204:207]\n"
"s_waitcnt lgkmcnt(4)\n"
"v_lshlrev_b32 v187, 16, v43\n"
"v_and_b32 v43, v237, v43\n"
"v_mfma_f32_16x16x32_bf16 v[192:195], a[0:3], v[104:107], v[192:195]\n"
"v_lshlrev_b32 v188, 16, v44\n"
"v_and_b32 v44, v237, v44\n"
"v_mfma_f32_16x16x32_bf16 v[200:203], v[104:107], v[104:107], v[200:203]\n"
"v_lshlrev_b32 v189, 16, v45\n"
"v_and_b32 v45, v237, v45\n"
"v_mfma_f32_16x16x32_bf16 v[196:199], a[0:3], v[108:111], v[196:199]\n"
"v_lshlrev_b32 v190, 16, v46\n"
"v_and_b32 v46, v237, v46\n"
"v_mfma_f32_16x16x32_bf16 v[204:207], v[108:111], v[108:111], v[204:207]\n"
"s_waitcnt lgkmcnt(3)\n"
"v_lshlrev_b32 v191, 16, v47\n"
"v_and_b32 v47, v237, v47\n"
"s_waitcnt vmcnt(0)\n"
"ds_read_b128 v[128:131], v220 offset:32768\n"
"ds_read_b128 v[132:135], v220 offset:34816\n"
"ds_read_b128 v[136:139], v221 offset:32768\n"
"ds_read_b128 v[140:143], v221 offset:34816\n"
"v_mfma_f32_16x16x32_bf16 v[192:195], a[0:3], v[112:115], v[192:195]\n"
"v_mfma_f32_16x16x32_bf16 v[200:203], v[112:115], v[112:115], v[200:203]\n"
"s_waitcnt lgkmcnt(6)\n"
"v_mfma_f32_16x16x32_bf16 v[196:199], a[0:3], v[116:119], v[196:199]\n"
"v_mfma_f32_16x16x32_bf16 v[204:207], v[116:119], v[116:119], v[204:207]\n"
"s_waitcnt lgkmcnt(4)\n"
"v_mfma_f32_16x16x32_bf16 v[192:195], a[0:3], v[120:123], v[192:195]\n"
"v_mfma_f32_16x16x32_bf16 v[200:203], v[120:123], v[120:123], v[200:203]\n"
"v_mfma_f32_16x16x32_bf16 v[196:199], a[0:3], v[124:127], v[196:199]\n"
"v_mfma_f32_16x16x32_bf16 v[204:207], v[124:127], v[124:127], v[204:207]\n"
"s_waitcnt lgkmcnt(0)\n"
"s_barrier\n"
"v_mfma_f32_16x16x32_bf16 v[192:195], a[0:3], v[128:131], v[192:195]\n"
"v_cmp_eq_u32 s[56:57], 1, v228\n"
"v_cmp_eq_u32 s[58:59], 2, v228\n"
"v_mfma_f32_16x16x32_bf16 v[200:203], v[128:131], v[128:131], v[200:203]\n"
"v_cmp_eq_u32 vcc, 3, v228\n"
"v_mfma_f32_16x16x32_bf16 v[196:199], a[0:3], v[132:135], v[196:199]\n"
"v_mfma_f32_16x16x32_bf16 v[204:207], v[132:135], v[132:135], v[204:207]\n"
"v_mfma_f32_16x16x32_bf16 v[192:195], a[0:3], v[136:139], v[192:195]\n"
"v_mfma_f32_16x16x32_bf16 v[200:203], v[136:139], v[136:139], v[200:203]\n"
"v_mfma_f32_16x16x32_bf16 v[196:199], a[0:3], v[140:143], v[196:199]\n"
"v_mfma_f32_16x16x32_bf16 v[204:207], v[140:143], v[140:143], v[204:207]\n"
"s_add_u32 m0, s51, 98352\n"
"s_nop 0\n"
"buffer_load_dwordx4 v217, s[20:23], s50 offen offset:0 lds\n"
"s_add_u32 m0, s51, 106288\n"
"s_nop 0\n"
"buffer_load_dwordx4 v217, s[20:23], s50 offen offset:256 lds\n"
"s_add_u32 s50, s50, 32768\n"
"s_add_u32 m0, s51, 99376\n"
"s_nop 0\n"
"buffer_load_dwordx4 v217, s[20:23], s50 offen offset:0 lds\n"
"s_add_u32 m0, s51, 107312\n"
"s_nop 0\n"
"buffer_load_dwordx4 v217, s[20:23], s50 offen offset:256 lds\n"
"s_add_u32 s50, s50, 32768\n"
"s_nop 1\n"
"v_cndmask_b32 v200, v200, v201, s[56:57]\n"
"v_cndmask_b32 v200, v200, v202, s[58:59]\n"
"v_cndmask_b32 v200, v200, v203, vcc\n"
"v_cndmask_b32 v204, v204, v205, s[56:57]\n"
"v_cndmask_b32 v204, v204, v206, s[58:59]\n"
"v_cndmask_b32 v204, v204, v207, vcc\n"
"v_cmp_eq_u32 vcc, v226, v227\n"
"v_mul_f32 v209, 0x3ae38e39, v192\n"
"v_mul_f32 v213, 0x3ae38e39, v196\n"
"v_cndmask_b32 v210, 0, v200, vcc\n"
"v_cndmask_b32 v214, 0, v204, vcc\n"
"v_mov_b32 v192, v210\n"
"v_mov_b32 v193, v214\n"
"v_mul_f32 v211, v209, v209\n"
"v_permlane32_swap_b32 v210, v192\n"
"v_permlane32_swap_b32 v214, v193\n"
"v_add_f32 v210, v210, v192\n"
"v_add_f32 v214, v214, v193\n"
"v_mov_b32 v192, v210\n"
"v_mov_b32 v193, v214\n"
"v_mul_f32 v215, v213, v213\n"
"v_permlane16_swap_b32 v210, v192\n"
"v_permlane16_swap_b32 v214, v193\n"
"v_add_f32 v210, v210, v192\n"
"v_add_f32 v214, v214, v193\n"
"v_mov_b32 v192, 0x3ae38e39\n"
"v_fma_f32 v210, v210, v192, -v211\n"
"v_fma_f32 v214, v214, v192, -v215\n"
"v_max_f32 v210, 0, v210\n"
"v_max_f32 v214, 0, v214\n"
"v_add_f32 v210, 0x3727c5ac, v210\n"
"v_add_f32 v214, 0x3727c5ac, v214\n"
"v_rsq_f32 v208, v210\n"
"v_rsq_f32 v212, v214\n"
"v_mul_f32_e64 v210, -v209, v208\n"
"v_mul_f32_e64 v214, -v213, v212\n"
"v_pk_fma_f32 v[144:145], v[144:145], v[208:209], v[210:211] op_sel_hi:[1,0,0]\n"
"v_pk_fma_f32 v[146:147], v[146:147], v[208:209], v[210:211] op_sel_hi:[1,0,0]\n"
"v_pk_fma_f32 v[0:1], v[0:1], v[208:209], v[210:211] op_sel_hi:[1,0,0]\n"
"v_pk_fma_f32 v[2:3], v[2:3], v[208:209], v[210:211] op_sel_hi:[1,0,0]\n"
"v_cvt_pk_f16_f32 v0, v144, v0\n"
"v_cvt_pk_f16_f32 v1, v145, v1\n"
"v_cvt_pk_f16_f32 v2, v146, v2\n"
"v_cvt_pk_f16_f32 v3, v147, v3\n"
"v_pk_fma_f32 v[148:149], v[148:149], v[212:213], v[214:215] op_sel_hi:[1,0,0]\n"
"v_pk_fma_f32 v[150:151], v[150:151], v[212:213], v[214:215] op_sel_hi:[1,0,0]\n"
"v_pk_fma_f32 v[4:5], v[4:5], v[212:213], v[214:215] op_sel_hi:[1,0,0]\n"
"v_pk_fma_f32 v[6:7], v[6:7], v[212:213], v[214:215] op_sel_hi:[1,0,0]\n"
"v_cvt_pk_f16_f32 v4, v148, v4\n"
"v_cvt_pk_f16_f32 v5, v149, v5\n"
"v_cvt_pk_f16_f32 v6, v150, v6\n"
"v_cvt_pk_f16_f32 v7, v151, v7\n"
"v_pk_fma_f32 v[152:153], v[152:153], v[208:209], v[210:211] op_sel_hi:[1,0,0]\n"
"v_pk_fma_f32 v[154:155], v[154:155], v[208:209], v[210:211] op_sel_hi:[1,0,0]\n"
"v_pk_fma_f32 v[8:9], v[8:9], v[208:209], v[210:211] op_sel_hi:[1,0,0]\n"
"s_add_u32 m0, s51, 114744\n"
"v_pk_fma_f32 v[10:11], v[10:11], v[208:209], v[210:211] op_sel_hi:[1,0,0]\n"
"buffer_load_dwordx4 v217, s[20:23], s50 offen offset:0 lds\n"
"v_cvt_pk_f16_f32 v8, v152, v8\n"
"v_cvt_pk_f16_f32 v9, v153, v9\n"
"v_cvt_pk_f16_f32 v10, v154, v10\n"
"v_cvt_pk_f16_f32 v11, v155, v11\n"
"v_pk_fma_f32 v[156:157], v[156:157], v[212:213], v[214:215] op_sel_hi:[1,0,0]\n"
"v_pk_fma_f32 v[158:159], v[158:159], v[212:213], v[214:215] op_sel_hi:[1,0,0]\n"
"v_pk_fma_f32 v[12:13], v[12:13], v[212:213], v[214:215] op_sel_hi:[1,0,0]\n"
"v_pk_fma_f32 v[14:15], v[14:15], v[212:213], v[214:215] op_sel_hi:[1,0,0]\n"
"v_cvt_pk_f16_f32 v12, v156, v12\n"
"v_cvt_pk_f16_f32 v13, v157, v13\n"
"v_cvt_pk_f16_f32 v14, v158, v14\n"
"v_cvt_pk_f16_f32 v15, v159, v15\n"
"v_pk_fma_f32 v[160:161], v[160:161], v[208:209], v[210:211] op_sel_hi:[1,0,0]\n"
"v_pk_fma_f32 v[162:163], v[162:163], v[208:209], v[210:211] op_sel_hi:[1,0,0]\n"
"v_pk_fma_f32 v[16:17], v[16:17], v[208:209], v[210:211] op_sel_hi:[1,0,0]\n"
"v_pk_fma_f32 v[18:19], v[18:19], v[208:209], v[210:211] op_sel_hi:[1,0,0]\n"
"v_cvt_pk_f16_f32 v16, v160, v16\n"
"v_cvt_pk_f16_f32 v17, v161, v17\n"
"v_cvt_pk_f16_f32 v18, v162, v18\n"
"v_cvt_pk_f16_f32 v19, v163, v19\n"
"v_pk_fma_f32 v[164:165], v[164:165], v[212:213], v[214:215] op_sel_hi:[1,0,0]\n"
"v_pk_fma_f32 v[166:167], v[166:167], v[212:213], v[214:215] op_sel_hi:[1,0,0]\n"
"v_pk_fma_f32 v[20:21], v[20:21], v[212:213], v[214:215] op_sel_hi:[1,0,0]\n"
"v_pk_fma_f32 v[22:23], v[22:23], v[212:213], v[214:215] op_sel_hi:[1,0,0]\n"
"v_cvt_pk_f16_f32 v20, v164, v20\n"
"v_cvt_pk_f16_f32 v21, v165, v21\n"
"v_cvt_pk_f16_f32 v22, v166, v22\n"
"v_cvt_pk_f16_f32 v23, v167, v23\n"
"v_pk_fma_f32 v[168:169], v[168:169], v[208:209], v[210:211] op_sel_hi:[1,0,0]\n"
"v_pk_fma_f32 v[170:171], v[170:171], v[208:209], v[210:211] op_sel_hi:[1,0,0]\n"
"v_pk_fma_f32 v[24:25], v[24:25], v[208:209], v[210:211] op_sel_hi:[1,0,0]\n"
"s_add_u32 m0, s51, 122680\n"
"v_pk_fma_f32 v[26:27], v[26:27], v[208:209], v[210:211] op_sel_hi:[1,0,0]\n"
"buffer_load_dwordx4 v217, s[20:23], s50 offen offset:256 lds\n"
"s_add_u32 s50, s50, 32768\n"
"v_cvt_pk_f16_f32 v24, v168, v24\n"
"v_cvt_pk_f16_f32 v25, v169, v25\n"
"v_cvt_pk_f16_f32 v26, v170, v26\n"
"v_cvt_pk_f16_f32 v27, v171, v27\n"
"v_pk_fma_f32 v[172:173], v[172:173], v[212:213], v[214:215] op_sel_hi:[1,0,0]\n"
"v_pk_fma_f32 v[174:175], v[174:175], v[212:213], v[214:215] op_sel_hi:[1,0,0]\n"
"v_pk_fma_f32 v[28:29], v[28:29], v[212:213], v[214:215] op_sel_hi:[1,0,0]\n"
"v_pk_fma_f32 v[30:31], v[30:31], v[212:213], v[214:215] op_sel_hi:[1,0,0]\n"
"v_cvt_pk_f16_f32 v28, v172, v28\n"
"v_cvt_pk_f16_f32 v29, v173, v29\n"
"v_cvt_pk_f16_f32 v30, v174, v30\n"
"v_cvt_pk_f16_f32 v31, v175, v31\n"
"buffer_load_dwordx4 v[144:147], v225, s[28:31], s61 offen offset:0 sc1\n"
"buffer_load_dwordx4 v[148:151], v225, s[28:31], s61 offen offset:16 sc1\n"
"buffer_load_dwordx4 v[152:155], v225, s[28:31], s62 offen offset:0 sc1\n"
"buffer_load_dwordx4 v[156:159], v225, s[28:31], s62 offen offset:16 sc1\n"
"v_pk_fma_f32 v[176:177], v[176:177], v[208:209], v[210:211] op_sel_hi:[1,0,0]\n"
"v_pk_fma_f32 v[178:179], v[178:179], v[208:209], v[210:211] op_sel_hi:[1,0,0]\n"
"v_pk_fma_f32 v[32:33], v[32:33], v[208:209], v[210:211] op_sel_hi:[1,0,0]\n"
"v_pk_fma_f32 v[34:35], v[34:35], v[208:209], v[210:211] op_sel_hi:[1,0,0]\n"
"v_cvt_pk_f16_f32 v32, v176, v32\n"
"v_cvt_pk_f16_f32 v33, v177, v33\n"
"v_cvt_pk_f16_f32 v34, v178, v34\n"
"v_cvt_pk_f16_f32 v35, v179, v35\n"
"v_pk_fma_f32 v[180:181], v[180:181], v[212:213], v[214:215] op_sel_hi:[1,0,0]\n"
"v_pk_fma_f32 v[182:183], v[182:183], v[212:213], v[214:215] op_sel_hi:[1,0,0]\n"
"v_pk_fma_f32 v[36:37], v[36:37], v[212:213], v[214:215] op_sel_hi:[1,0,0]\n"
"v_pk_fma_f32 v[38:39], v[38:39], v[212:213], v[214:215] op_sel_hi:[1,0,0]\n"
"v_cvt_pk_f16_f32 v36, v180, v36\n"
"v_cvt_pk_f16_f32 v37, v181, v37\n"
"v_cvt_pk_f16_f32 v38, v182, v38\n"
"v_cvt_pk_f16_f32 v39, v183, v39\n"
"v_pk_fma_f32 v[184:185], v[184:185], v[208:209], v[210:211] op_sel_hi:[1,0,0]\n"
"v_pk_fma_f32 v[186:187], v[186:187], v[208:209], v[210:211] op_sel_hi:[1,0,0]\n"
"v_pk_fma_f32 v[40:41], v[40:41], v[208:209], v[210:211] op_sel_hi:[1,0,0]\n"
"s_add_u32 m0, s51, 115768\n"
"v_pk_fma_f32 v[42:43], v[42:43], v[208:209], v[210:211] op_sel_hi:[1,0,0]\n"
"buffer_load_dwordx4 v217, s[20:23], s50 offen offset:0 lds\n"
"v_cvt_pk_f16_f32 v40, v184, v40\n"
"v_cvt_pk_f16_f32 v41, v185, v41\n"
"v_cvt_pk_f16_f32 v42, v186, v42\n"
"v_cvt_pk_f16_f32 v43, v187, v43\n"
"v_pk_fma_f32 v[188:189], v[188:189], v[212:213], v[214:215] op_sel_hi:[1,0,0]\n"
"v_pk_fma_f32 v[190:191], v[190:191], v[212:213], v[214:215] op_sel_hi:[1,0,0]\n"
"v_pk_fma_f32 v[44:45], v[44:45], v[212:213], v[214:215] op_sel_hi:[1,0,0]\n"
"v_pk_fma_f32 v[46:47], v[46:47], v[212:213], v[214:215] op_sel_hi:[1,0,0]\n"
"v_cvt_pk_f16_f32 v44, v188, v44\n"
"v_cvt_pk_f16_f32 v45, v189, v45\n"
"v_cvt_pk_f16_f32 v46, v190, v46\n"
"v_cvt_pk_f16_f32 v47, v191, v47\n"
"v_lshlrev_b32 v176, 16, v48\n"
"v_and_b32 v177, v237, v48\n"
"v_lshlrev_b32 v178, 16, v49\n"
"v_and_b32 v179, v237, v49\n"
"v_lshlrev_b32 v180, 16, v50\n"
"v_and_b32 v181, v237, v50\n"
"v_lshlrev_b32 v182, 16, v51\n"
"v_and_b32 v183, v237, v51\n"
"v_pk_fma_f32 v[176:177], v[176:177], v[208:209], v[210:211] op_sel_hi:[1,0,0]\n"
"v_pk_fma_f32 v[178:179], v[178:179], v[208:209], v[210:211] op_sel_hi:[1,0,0]\n"
"v_pk_fma_f32 v[180:181], v[180:181], v[208:209], v[210:211] op_sel_hi:[1,0,0]\n"
"v_pk_fma_f32 v[182:183], v[182:183], v[208:209], v[210:211] op_sel_hi:[1,0,0]\n"
"v_cvt_pk_f16_f32 v48, v176, v177\n"
"v_cvt_pk_f16_f32 v49, v178, v179\n"
"v_cvt_pk_f16_f32 v50, v180, v181\n"
"v_cvt_pk_f16_f32 v51, v182, v183\n"
"v_lshlrev_b32 v184, 16, v52\n"
"v_and_b32 v185, v237, v52\n"
"v_lshlrev_b32 v186, 16, v53\n"
"s_add_u32 m0, s51, 123704\n"
"v_and_b32 v187, v237, v53\n"
"buffer_load_dwordx4 v217, s[20:23], s50 offen offset:256 lds\n"
"s_add_u32 s50, s50, 425984\n"
"v_lshlrev_b32 v188, 16, v54\n"
"v_and_b32 v189, v237, v54\n"
"v_lshlrev_b32 v190, 16, v55\n"
"v_and_b32 v191, v237, v55\n"
"v_pk_fma_f32 v[184:185], v[184:185], v[212:213], v[214:215] op_sel_hi:[1,0,0]\n"
"v_pk_fma_f32 v[186:187], v[186:187], v[212:213], v[214:215] op_sel_hi:[1,0,0]\n"
"v_pk_fma_f32 v[188:189], v[188:189], v[212:213], v[214:215] op_sel_hi:[1,0,0]\n"
"v_pk_fma_f32 v[190:191], v[190:191], v[212:213], v[214:215] op_sel_hi:[1,0,0]\n"
"v_cvt_pk_f16_f32 v52, v184, v185\n"
"v_cvt_pk_f16_f32 v53, v186, v187\n"
"v_cvt_pk_f16_f32 v54, v188, v189\n"
"v_cvt_pk_f16_f32 v55, v190, v191\n"
"v_lshlrev_b32 v176, 16, v56\n"
"v_and_b32 v177, v237, v56\n"
"v_lshlrev_b32 v178, 16, v57\n"
"v_and_b32 v179, v237, v57\n"
"v_lshlrev_b32 v180, 16, v58\n"
"v_and_b32 v181, v237, v58\n"
"v_lshlrev_b32 v182, 16, v59\n"
"s_mov_b32 m0, s51\n"
"v_and_b32 v183, v237, v59\n"
"buffer_load_dwordx4 v217, s[20:23], s50 offen offset:0 lds\n"
"v_pk_fma_f32 v[176:177], v[176:177], v[208:209], v[210:211] op_sel_hi:[1,0,0]\n"
"v_pk_fma_f32 v[178:179], v[178:179], v[208:209], v[210:211] op_sel_hi:[1,0,0]\n"
"v_pk_fma_f32 v[180:181], v[180:181], v[208:209], v[210:211] op_sel_hi:[1,0,0]\n"
"v_pk_fma_f32 v[182:183], v[182:183], v[208:209], v[210:211] op_sel_hi:[1,0,0]\n"
"v_cvt_pk_f16_f32 v56, v176, v177\n"
"v_cvt_pk_f16_f32 v57, v178, v179\n"
"v_cvt_pk_f16_f32 v58, v180, v181\n"
"v_cvt_pk_f16_f32 v59, v182, v183\n"
"v_lshlrev_b32 v184, 16, v60\n"
"v_and_b32 v185, v237, v60\n"
"v_lshlrev_b32 v186, 16, v61\n"
"v_and_b32 v187, v237, v61\n"
"v_lshlrev_b32 v188, 16, v62\n"
"v_and_b32 v189, v237, v62\n"
"v_lshlrev_b32 v190, 16, v63\n"
"v_and_b32 v191, v237, v63\n"
"v_pk_fma_f32 v[184:185], v[184:185], v[212:213], v[214:215] op_sel_hi:[1,0,0]\n"
"v_pk_fma_f32 v[186:187], v[186:187], v[212:213], v[214:215] op_sel_hi:[1,0,0]\n"
"v_pk_fma_f32 v[188:189], v[188:189], v[212:213], v[214:215] op_sel_hi:[1,0,0]\n"
"s_add_u32 m0, s51, 7936\n"
"v_pk_fma_f32 v[190:191], v[190:191], v[212:213], v[214:215] op_sel_hi:[1,0,0]\n"
"buffer_load_dwordx4 v217, s[20:23], s50 offen offset:256 lds\n"
"s_add_u32 s50, s50, 32768\n"
"v_cvt_pk_f16_f32 v60, v184, v185\n"
"v_cvt_pk_f16_f32 v61, v186, v187\n"
"v_cvt_pk_f16_f32 v62, v188, v189\n"
"v_cvt_pk_f16_f32 v63, v190, v191\n"
"v_lshlrev_b32 v176, 16, v64\n"
"v_and_b32 v177, v237, v64\n"
"v_lshlrev_b32 v178, 16, v65\n"
"v_and_b32 v179, v237, v65\n"
"v_lshlrev_b32 v180, 16, v66\n"
"v_and_b32 v181, v237, v66\n"
"v_lshlrev_b32 v182, 16, v67\n"
"v_and_b32 v183, v237, v67\n"
"v_pk_fma_f32 v[176:177], v[176:177], v[208:209], v[210:211] op_sel_hi:[1,0,0]\n"
"v_pk_fma_f32 v[178:179], v[178:179], v[208:209], v[210:211] op_sel_hi:[1,0,0]\n"
"v_pk_fma_f32 v[180:181], v[180:181], v[208:209], v[210:211] op_sel_hi:[1,0,0]\n"
"v_pk_fma_f32 v[182:183], v[182:183], v[208:209], v[210:211] op_sel_hi:[1,0,0]\n"
"v_cvt_pk_f16_f32 v64, v176, v177\n"
"v_cvt_pk_f16_f32 v65, v178, v179\n"
"v_cvt_pk_f16_f32 v66, v180, v181\n"
"v_cvt_pk_f16_f32 v67, v182, v183\n"
"v_lshlrev_b32 v184, 16, v68\n"
"v_and_b32 v185, v237, v68\n"
"v_lshlrev_b32 v186, 16, v69\n"
"s_add_u32 m0, s51, 1024\n"
"v_and_b32 v187, v237, v69\n"
"buffer_load_dwordx4 v217, s[20:23], s50 offen offset:0 lds\n"
"v_lshlrev_b32 v188, 16, v70\n"
"v_and_b32 v189, v237, v70\n"
"v_lshlrev_b32 v190, 16, v71\n"
"v_and_b32 v191, v237, v71\n"
"v_pk_fma_f32 v[184:185], v[184:185], v[212:213], v[214:215] op_sel_hi:[1,0,0]\n"
"v_pk_fma_f32 v[186:187], v[186:187], v[212:213], v[214:215] op_sel_hi:[1,0,0]\n"
"v_pk_fma_f32 v[188:189], v[188:189], v[212:213], v[214:215] op_sel_hi:[1,0,0]\n"
"v_pk_fma_f32 v[190:191], v[190:191], v[212:213], v[214:215] op_sel_hi:[1,0,0]\n"
"v_cvt_pk_f16_f32 v68, v184, v185\n"
"v_cvt_pk_f16_f32 v69, v186, v187\n"
"v_cvt_pk_f16_f32 v70, v188, v189\n"
"v_cvt_pk_f16_f32 v71, v190, v191\n"
"v_lshlrev_b32 v176, 16, v72\n"
"v_and_b32 v177, v237, v72\n"
"v_lshlrev_b32 v178, 16, v73\n"
"v_and_b32 v179, v237, v73\n"
"v_lshlrev_b32 v180, 16, v74\n"
"v_and_b32 v181, v237, v74\n"
"v_lshlrev_b32 v182, 16, v75\n"
"s_add_u32 m0, s51, 8960\n"
"v_and_b32 v183, v237, v75\n"
"buffer_load_dwordx4 v217, s[20:23], s50 offen offset:256 lds\n"
"s_add_u32 s50, s50, 32768\n"
"v_pk_fma_f32 v[176:177], v[176:177], v[208:209], v[210:211] op_sel_hi:[1,0,0]\n"
"v_pk_fma_f32 v[178:179], v[178:179], v[208:209], v[210:211] op_sel_hi:[1,0,0]\n"
"v_pk_fma_f32 v[180:181], v[180:181], v[208:209], v[210:211] op_sel_hi:[1,0,0]\n"
"v_pk_fma_f32 v[182:183], v[182:183], v[208:209], v[210:211] op_sel_hi:[1,0,0]\n"
"v_cvt_pk_f16_f32 v72, v176, v177\n"
"v_cvt_pk_f16_f32 v73, v178, v179\n"
"v_cvt_pk_f16_f32 v74, v180, v181\n"
"v_cvt_pk_f16_f32 v75, v182, v183\n"
"v_lshlrev_b32 v184, 16, v76\n"
"v_and_b32 v185, v237, v76\n"
"v_lshlrev_b32 v186, 16, v77\n"
"v_and_b32 v187, v237, v77\n"
"v_lshlrev_b32 v188, 16, v78\n"
"v_and_b32 v189, v237, v78\n"
"v_lshlrev_b32 v190, 16, v79\n"
"v_and_b32 v191, v237, v79\n"
"v_pk_fma_f32 v[184:185], v[184:185], v[212:213], v[214:215] op_sel_hi:[1,0,0]\n"
"v_pk_fma_f32 v[186:187], v[186:187], v[212:213], v[214:215] op_sel_hi:[1,0,0]\n"
"v_pk_fma_f32 v[188:189], v[188:189], v[212:213], v[214:215] op_sel_hi:[1,0,0]\n"
"s_add_u32 m0, s51, 16392\n"
"v_pk_fma_f32 v[190:191], v[190:191], v[212:213], v[214:215] op_sel_hi:[1,0,0]\n"
"buffer_load_dwordx4 v217, s[20:23], s50 offen offset:0 lds\n"
"v_cvt_pk_f16_f32 v76, v184, v185\n"
"v_cvt_pk_f16_f32 v77, v186, v187\n"
"v_cvt_pk_f16_f32 v78, v188, v189\n"
"v_cvt_pk_f16_f32 v79, v190, v191\n"
"v_lshlrev_b32 v176, 16, v80\n"
"v_and_b32 v177, v237, v80\n"
"v_lshlrev_b32 v178, 16, v81\n"
"v_and_b32 v179, v237, v81\n"
"v_lshlrev_b32 v180, 16, v82\n"
"v_and_b32 v181, v237, v82\n"
"v_lshlrev_b32 v182, 16, v83\n"
"v_and_b32 v183, v237, v83\n"
"v_pk_fma_f32 v[176:177], v[176:177], v[208:209], v[210:211] op_sel_hi:[1,0,0]\n"
"v_pk_fma_f32 v[178:179], v[178:179], v[208:209], v[210:211] op_sel_hi:[1,0,0]\n"
"v_pk_fma_f32 v[180:181], v[180:181], v[208:209], v[210:211] op_sel_hi:[1,0,0]\n"
"v_pk_fma_f32 v[182:183], v[182:183], v[208:209], v[210:211] op_sel_hi:[1,0,0]\n"
"v_cvt_pk_f16_f32 v80, v176, v177\n"
"v_cvt_pk_f16_f32 v81, v178, v179\n"
"v_cvt_pk_f16_f32 v82, v180, v181\n"
"v_cvt_pk_f16_f32 v83, v182, v183\n"
"v_lshlrev_b32 v184, 16, v84\n"
"v_and_b32 v185, v237, v84\n"
"v_lshlrev_b32 v186, 16, v85\n"
"s_add_u32 m0, s51, 24328\n"
"v_and_b32 v187, v237, v85\n"
"buffer_load_dwordx4 v217, s[20:23], s50 offen offset:256 lds\n"
"s_add_u32 s50, s50, 32768\n"
"v_lshlrev_b32 v188, 16, v86\n"
"v_and_b32 v189, v237, v86\n"
"v_lshlrev_b32 v190, 16, v87\n"
"v_and_b32 v191, v237, v87\n"
"v_pk_fma_f32 v[184:185], v[184:185], v[212:213], v[214:215] op_sel_hi:[1,0,0]\n"
"v_pk_fma_f32 v[186:187], v[186:187], v[212:213], v[214:215] op_sel_hi:[1,0,0]\n"
"v_pk_fma_f32 v[188:189], v[188:189], v[212:213], v[214:215] op_sel_hi:[1,0,0]\n"
"v_pk_fma_f32 v[190:191], v[190:191], v[212:213], v[214:215] op_sel_hi:[1,0,0]\n"
"v_cvt_pk_f16_f32 v84, v184, v185\n"
"v_cvt_pk_f16_f32 v85, v186, v187\n"
"v_cvt_pk_f16_f32 v86, v188, v189\n"
"v_cvt_pk_f16_f32 v87, v190, v191\n"
"v_lshlrev_b32 v176, 16, v88\n"
"v_and_b32 v177, v237, v88\n"
"v_lshlrev_b32 v178, 16, v89\n"
"v_and_b32 v179, v237, v89\n"
"v_lshlrev_b32 v180, 16, v90\n"
"v_and_b32 v181, v237, v90\n"
"v_lshlrev_b32 v182, 16, v91\n"
"s_add_u32 m0, s51, 17416\n"
"v_and_b32 v183, v237, v91\n"
"buffer_load_dwordx4 v217, s[20:23], s50 offen offset:0 lds\n"
"v_pk_fma_f32 v[176:177], v[176:177], v[208:209], v[210:211] op_sel_hi:[1,0,0]\n"
"v_pk_fma_f32 v[178:179], v[178:179], v[208:209], v[210:211] op_sel_hi:[1,0,0]\n"
"v_pk_fma_f32 v[180:181], v[180:181], v[208:209], v[210:211] op_sel_hi:[1,0,0]\n"
"v_pk_fma_f32 v[182:183], v[182:183], v[208:209], v[210:211] op_sel_hi:[1,0,0]\n"
"v_cvt_pk_f16_f32 v88, v176, v177\n"
"v_cvt_pk_f16_f32 v89, v178, v179\n"
"v_cvt_pk_f16_f32 v90, v180, v181\n"
"v_cvt_pk_f16_f32 v91, v182, v183\n"
"v_lshlrev_b32 v184, 16, v92\n"
"v_and_b32 v185, v237, v92\n"
"v_lshlrev_b32 v186, 16, v93\n"
"v_and_b32 v187, v237, v93\n"
"v_lshlrev_b32 v188, 16, v94\n"
"v_and_b32 v189, v237, v94\n"
"v_lshlrev_b32 v190, 16, v95\n"
"v_and_b32 v191, v237, v95\n"
"v_pk_fma_f32 v[184:185], v[184:185], v[212:213], v[214:215] op_sel_hi:[1,0,0]\n"
"v_pk_fma_f32 v[186:187], v[186:187], v[212:213], v[214:215] op_sel_hi:[1,0,0]\n"
"v_pk_fma_f32 v[188:189], v[188:189], v[212:213], v[214:215] op_sel_hi:[1,0,0]\n"
"s_add_u32 m0, s51, 25352\n"
"v_pk_fma_f32 v[190:191], v[190:191], v[212:213], v[214:215] op_sel_hi:[1,0,0]\n"
"buffer_load_dwordx4 v217, s[20:23], s50 offen offset:256 lds\n"
"s_add_u32 s50, s50, 425984\n"
"v_cvt_pk_f16_f32 v92, v184, v185\n"
"v_cvt_pk_f16_f32 v93, v186, v187\n"
"v_cvt_pk_f16_f32 v94, v188, v189\n"
"v_cvt_pk_f16_f32 v95, v190, v191\n"
"v_lshlrev_b32 v176, 16, v96\n"
"v_and_b32 v177, v237, v96\n"
"v_lshlrev_b32 v178, 16, v97\n"
"v_and_b32 v179, v237, v97\n"
"v_lshlrev_b32 v180, 16, v98\n"
"v_and_b32 v181, v237, v98\n"
"v_lshlrev_b32 v182, 16, v99\n"
"v_and_b32 v183, v237, v99\n"
"v_pk_fma_f32 v[176:177], v[176:177], v[208:209], v[210:211] op_sel_hi:[1,0,0]\n"
"v_pk_fma_f32 v[178:179], v[178:179], v[208:209], v[210:211] op_sel_hi:[1,0,0]\n"
"v_pk_fma_f32 v[180:181], v[180:181], v[208:209], v[210:211] op_sel_hi:[1,0,0]\n"
"v_pk_fma_f32 v[182:183], v[182:183], v[208:209], v[210:211] op_sel_hi:[1,0,0]\n"
"v_cvt_pk_f16_f32 v96, v176, v177\n"
"v_cvt_pk_f16_f32 v97, v178, v179\n"
"v_cvt_pk_f16_f32 v98, v180, v181\n"
"v_cvt_pk_f16_f32 v99, v182, v183\n"
"v_lshlrev_b32 v184, 16, v100\n"
"v_and_b32 v185, v237, v100\n"
"v_lshlrev_b32 v186, 16, v101\n"
"s_add_u32 m0, s51, 32784\n"
"v_and_b32 v187, v237, v101\n"
"buffer_load_dwordx4 v217, s[20:23], s50 offen offset:0 lds\n"
"v_lshlrev_b32 v188, 16, v102\n"
"v_and_b32 v189, v237, v102\n"
"v_lshlrev_b32 v190, 16, v103\n"
"v_and_b32 v191, v237, v103\n"
"v_pk_fma_f32 v[184:185], v[184:185], v[212:213], v[214:215] op_sel_hi:[1,0,0]\n"
"v_pk_fma_f32 v[186:187], v[186:187], v[212:213], v[214:215] op_sel_hi:[1,0,0]\n"
"v_pk_fma_f32 v[188:189], v[188:189], v[212:213], v[214:215] op_sel_hi:[1,0,0]\n"
"v_pk_fma_f32 v[190:191], v[190:191], v[212:213], v[214:215] op_sel_hi:[1,0,0]\n"
"v_cvt_pk_f16_f32 v100, v184, v185\n"
"v_cvt_pk_f16_f32 v101, v186, v187\n"
"v_cvt_pk_f16_f32 v102, v188, v189\n"
"v_cvt_pk_f16_f32 v103, v190, v191\n"
"v_lshlrev_b32 v176, 16, v104\n"
"v_and_b32 v177, v237, v104\n"
"v_lshlrev_b32 v178, 16, v105\n"
"v_and_b32 v179, v237, v105\n"
"v_lshlrev_b32 v180, 16, v106\n"
"v_and_b32 v181, v237, v106\n"
"v_lshlrev_b32 v182, 16, v107\n"
"s_add_u32 m0, s51, 40720\n"
"v_and_b32 v183, v237, v107\n"
"buffer_load_dwordx4 v217, s[20:23], s50 offen offset:256 lds\n"
"s_add_u32 s50, s50, 32768\n"
"v_pk_fma_f32 v[176:177], v[176:177], v[208:209], v[210:211] op_sel_hi:[1,0,0]\n"
"v_pk_fma_f32 v[178:179], v[178:179], v[208:209], v[210:211] op_sel_hi:[1,0,0]\n"
"v_pk_fma_f32 v[180:181], v[180:181], v[208:209], v[210:211] op_sel_hi:[1,0,0]\n"
"v_pk_fma_f32 v[182:183], v[182:183], v[208:209], v[210:211] op_sel_hi:[1,0,0]\n"
"v_cvt_pk_f16_f32 v104, v176, v177\n"
"v_cvt_pk_f16_f32 v105, v178, v179\n"
"v_cvt_pk_f16_f32 v106, v180, v181\n"
"v_cvt_pk_f16_f32 v107, v182, v183\n"
"v_lshlrev_b32 v184, 16, v108\n"
"v_and_b32 v185, v237, v108\n"
"v_lshlrev_b32 v186, 16, v109\n"
"v_and_b32 v187, v237, v109\n"
"v_lshlrev_b32 v188, 16, v110\n"
"v_and_b32 v189, v237, v110\n"
"v_lshlrev_b32 v190, 16, v111\n"
"v_and_b32 v191, v237, v111\n"
"v_pk_fma_f32 v[184:185], v[184:185], v[212:213], v[214:215] op_sel_hi:[1,0,0]\n"
"v_pk_fma_f32 v[186:187], v[186:187], v[212:213], v[214:215] op_sel_hi:[1,0,0]\n"
"v_pk_fma_f32 v[188:189], v[188:189], v[212:213], v[214:215] op_sel_hi:[1,0,0]\n"
"s_add_u32 m0, s51, 33808\n"
"v_pk_fma_f32 v[190:191], v[190:191], v[212:213], v[214:215] op_sel_hi:[1,0,0]\n"
"buffer_load_dwordx4 v217, s[20:23], s50 offen offset:0 lds\n"
"v_cvt_pk_f16_f32 v108, v184, v185\n"
"v_cvt_pk_f16_f32 v109, v186, v187\n"
"v_cvt_pk_f16_f32 v110, v188, v189\n"
"v_cvt_pk_f16_f32 v111, v190, v191\n"
"s_mov_b32 s87, 0x80000\n"
"s_waitcnt vmcnt(13)\n"
"AT_gchk:\n"
"v_xor_b32 v192, s85, v145\n"
"v_xor_b32 v193, s85, v147\n"
"v_bitop3_b32 v192, v149, s85, v192 bitop3:0xbe\n"
"v_bitop3_b32 v193, v151, s85, v193 bitop3:0xbe\n"
"v_bitop3_b32 v192, v153, s85, v192 bitop3:0xbe\n"
"v_bitop3_b32 v193, v155, s85, v193 bitop3:0xbe\n"
"v_bitop3_b32 v192, v157, s85, v192 bitop3:0xbe\n"
"v_bitop3_b32 v193, v159, s85, v193 bitop3:0xbe\n"
"v_or_b32 v192, v192, v193\n"
"v_cmp_ne_u32 s[56:57], 0, v192\n"
"v_cmp_gt_u32 vcc, 144, v234\n"
"s_nop 1\n"
"s_and_b64 vcc, vcc, s[56:57]\n"
"s_cbranch_vccz AT_gok\n"
"s_sub_u32 s87, s87, 1\n"
"s_cmp_eq_u32 s87, 0\n"
"s_cbranch_scc1 AT_gok\n"
"s_sleep 2\n"
"buffer_load_dwordx4 v[144:147], v225, s[28:31], s61 offen offset:0 sc1\n"
"buffer_load_dwordx4 v[148:151], v225, s[28:31], s61 offen offset:16 sc1\n"
"buffer_load_dwordx4 v[152:155], v225, s[28:31], s62 offen offset:0 sc1\n"
"buffer_load_dwordx4 v[156:159], v225, s[28:31], s62 offen offset:16 sc1\n"
"s_waitcnt vmcnt(0)\n"
"s_branch AT_gchk\n"
"AT_gok:\n"
"v_mov_b32 v236, 0x3e0293ee\n"
"v_mul_f32 v192, v236, v144\n"
"v_fma_f32 v196, v152, v236, v236\n"
"v_mul_f32 v193, v236, v146\n"
"v_fma_f32 v197, v154, v236, v236\n"
"v_mul_f32 v194, v236, v148\n"
"v_fma_f32 v198, v156, v236, v236\n"
"v_mul_f32 v195, v236, v150\n"
"v_fma_f32 v199, v158, v236, v236\n"
"ds_write_b128 v216, v[196:199]\n"
"ds_write_b128 v216, v[192:195] offset:128\n"
"v_lshlrev_b32 v176, 16, v112\n"
"v_and_b32 v177, v237, v112\n"
"v_lshlrev_b32 v178, 16, v113\n"
"v_and_b32 v179, v237, v113\n"
"v_lshlrev_b32 v180, 16, v114\n"
"v_and_b32 v181, v237, v114\n"
"v_lshlrev_b32 v182, 16, v115\n"
"v_and_b32 v183, v237, v115\n"
"v_pk_fma_f32 v[176:177], v[176:177], v[208:209], v[210:211] op_sel_hi:[1,0,0]\n"
"v_pk_fma_f32 v[178:179], v[178:179], v[208:209], v[210:211] op_sel_hi:[1,0,0]\n"
"v_pk_fma_f32 v[180:181], v[180:181], v[208:209], v[210:211] op_sel_hi:[1,0,0]\n"
"v_pk_fma_f32 v[182:183], v[182:183], v[208:209], v[210:211] op_sel_hi:[1,0,0]\n"
"v_cvt_pk_f16_f32 v112, v176, v177\n"
"v_cvt_pk_f16_f32 v113, v178, v179\n"
"v_cvt_pk_f16_f32 v114, v180, v181\n"
"v_cvt_pk_f16_f32 v115, v182, v183\n"
"v_lshlrev_b32 v184, 16, v116\n"
"v_and_b32 v185, v237, v116\n"
"v_lshlrev_b32 v186, 16, v117\n"
"s_add_u32 m0, s51, 41744\n"
"v_and_b32 v187, v237, v117\n"
"buffer_load_dwordx4 v217, s[20:23], s50 offen offset:256 lds\n"
"s_add_u32 s50, s50, 32768\n"
"v_lshlrev_b32 v188, 16, v118\n"
"v_and_b32 v189, v237, v118\n"
"v_lshlrev_b32 v190, 16, v119\n"
"v_and_b32 v191, v237, v119\n"
"v_pk_fma_f32 v[184:185], v[184:185], v[212:213], v[214:215] op_sel_hi:[1,0,0]\n"
"v_pk_fma_f32 v[186:187], v[186:187], v[212:213], v[214:215] op_sel_hi:[1,0,0]\n"
"v_pk_fma_f32 v[188:189], v[188:189], v[212:213], v[214:215] op_sel_hi:[1,0,0]\n"
"v_pk_fma_f32 v[190:191], v[190:191], v[212:213], v[214:215] op_sel_hi:[1,0,0]\n"
"v_cvt_pk_f16_f32 v116, v184, v185\n"
"v_cvt_pk_f16_f32 v117, v186, v187\n"
"v_cvt_pk_f16_f32 v118, v188, v189\n"
"v_cvt_pk_f16_f32 v119, v190, v191\n"
"v_lshlrev_b32 v176, 16, v120\n"
"v_and_b32 v177, v237, v120\n"
"v_lshlrev_b32 v178, 16, v121\n"
"v_and_b32 v179, v237, v121\n"
"v_lshlrev_b32 v180, 16, v122\n"
"v_and_b32 v181, v237, v122\n"
"v_lshlrev_b32 v182, 16, v123\n"
"s_add_u32 m0, s51, 49176\n"
"v_and_b32 v183, v237, v123\n"
"buffer_load_dwordx4 v217, s[20:23], s50 offen offset:0 lds\n"
"v_pk_fma_f32 v[176:177], v[176:177], v[208:209], v[210:211] op_sel_hi:[1,0,0]\n"
"v_pk_fma_f32 v[178:179], v[178:179], v[208:209], v[210:211] op_sel_hi:[1,0,0]\n"
"v_pk_fma_f32 v[180:181], v[180:181], v[208:209], v[210:211] op_sel_hi:[1,0,0]\n"
"v_pk_fma_f32 v[182:183], v[182:183], v[208:209], v[210:211] op_sel_hi:[1,0,0]\n"
"v_cvt_pk_f16_f32 v120, v176, v177\n"
"v_cvt_pk_f16_f32 v121, v178, v179\n"
"v_cvt_pk_f16_f32 v122, v180, v181\n"
"v_cvt_pk_f16_f32 v123, v182, v183\n"
"v_lshlrev_b32 v184, 16, v124\n"
"v_and_b32 v185, v237, v124\n"
"v_lshlrev_b32 v186, 16, v125\n"
"v_and_b32 v187, v237, v125\n"
"v_lshlrev_b32 v188, 16, v126\n"
"v_and_b32 v189, v237, v126\n"
"v_lshlrev_b32 v190, 16, v127\n"
"v_and_b32 v191, v237, v127\n"
"v_pk_fma_f32 v[184:185], v[184:185], v[212:213], v[214:215] op_sel_hi:[1,0,0]\n"
"v_pk_fma_f32 v[186:187], v[186:187], v[212:213], v[214:215] op_sel_hi:[1,0,0]\n"
"v_pk_fma_f32 v[188:189], v[188:189], v[212:213], v[214:215] op_sel_hi:[1,0,0]\n"
"s_add_u32 m0, s51, 57112\n"
"v_pk_fma_f32 v[190:191], v[190:191], v[212:213], v[214:215] op_sel_hi:[1,0,0]\n"
"buffer_load_dwordx4 v217, s[20:23], s50 offen offset:256 lds\n"
"s_add_u32 s50, s50, 32768\n"
"v_cvt_pk_f16_f32 v124, v184, v185\n"
"v_cvt_pk_f16_f32 v125, v186, v187\n"
"v_cvt_pk_f16_f32 v126, v188, v189\n"
"v_cvt_pk_f16_f32 v127, v190, v191\n"
"s_waitcnt vmcnt(14) lgkmcnt(0)\n"
"s_barrier\n"
"ds_read_b128 v[144:147], v224 offset:0\n"
"ds_read_b128 v[148:151], v224 offset:64\n"
"ds_read_b128 v[152:155], v224 offset:128\n"
"ds_read_b128 v[156:159], v224 offset:192\n"
"ds_read_b128 v[160:163], v224 offset:256\n"
"ds_read_b128 v[164:167], v224 offset:320\n"
"ds_read_b128 v[168:171], v224 offset:384\n"
"ds_read_b128 v[172:175], v224 offset:448\n"
"v_lshlrev_b32 v176, 16, v128\n"
"v_and_b32 v177, v237, v128\n"
"v_lshlrev_b32 v178, 16, v129\n"
"v_and_b32 v179, v237, v129\n"
"v_lshlrev_b32 v180, 16, v130\n"
"v_and_b32 v181, v237, v130\n"
"v_lshlrev_b32 v182, 16, v131\n"
"v_and_b32 v183, v237, v131\n"
"v_pk_fma_f32 v[176:177], v[176:177], v[208:209], v[210:211] op_sel_hi:[1,0,0]\n"
"v_pk_fma_f32 v[178:179], v[178:179], v[208:209], v[210:211] op_sel_hi:[1,0,0]\n"
"v_pk_fma_f32 v[180:181], v[180:181], v[208:209], v[210:211] op_sel_hi:[1,0,0]\n"
"v_pk_fma_f32 v[182:183], v[182:183], v[208:209], v[210:211] op_sel_hi:[1,0,0]\n"
"v_cvt_pk_f16_f32 v128, v176, v177\n"
"v_cvt_pk_f16_f32 v129, v178, v179\n"
"v_cvt_pk_f16_f32 v130, v180, v181\n"
"v_cvt_pk_f16_f32 v131, v182, v183\n"
"v_lshlrev_b32 v184, 16, v132\n"
"v_and_b32 v185, v237, v132\n"
"v_lshlrev_b32 v186, 16, v133\n"
"s_add_u32 m0, s51, 50200\n"
"v_and_b32 v187, v237, v133\n"
"buffer_load_dwordx4 v217, s[20:23], s50 offen offset:0 lds\n"
"v_lshlrev_b32 v188, 16, v134\n"
"v_and_b32 v189, v237, v134\n"
"v_lshlrev_b32 v190, 16, v135\n"
"v_and_b32 v191, v237, v135\n"
"v_pk_fma_f32 v[184:185], v[184:185], v[212:213], v[214:215] op_sel_hi:[1,0,0]\n"
"v_pk_fma_f32 v[186:187], v[186:187], v[212:213], v[214:215] op_sel_hi:[1,0,0]\n"
"v_pk_fma_f32 v[188:189], v[188:189], v[212:213], v[214:215] op_sel_hi:[1,0,0]\n"
"v_pk_fma_f32 v[190:191], v[190:191], v[212:213], v[214:215] op_sel_hi:[1,0,0]\n"
"v_cvt_pk_f16_f32 v132, v184, v185\n"
"v_cvt_pk_f16_f32 v133, v186, v187\n"
"v_cvt_pk_f16_f32 v134, v188, v189\n"
"v_cvt_pk_f16_f32 v135, v190, v191\n"
"v_lshlrev_b32 v176, 16, v136\n"
"v_and_b32 v177, v237, v136\n"
"v_lshlrev_b32 v178, 16, v137\n"
"v_and_b32 v179, v237, v137\n"
"v_lshlrev_b32 v180, 16, v138\n"
"v_and_b32 v181, v237, v138\n"
"v_lshlrev_b32 v182, 16, v139\n"
"s_add_u32 m0, s51, 58136\n"
"v_and_b32 v183, v237, v139\n"
"buffer_load_dwordx4 v217, s[20:23], s50 offen offset:256 lds\n"
"s_add_u32 s50, s50, 425984\n"
"v_pk_fma_f32 v[176:177], v[176:177], v[208:209], v[210:211] op_sel_hi:[1,0,0]\n"
"v_pk_fma_f32 v[178:179], v[178:179], v[208:209], v[210:211] op_sel_hi:[1,0,0]\n"
"v_pk_fma_f32 v[180:181], v[180:181], v[208:209], v[210:211] op_sel_hi:[1,0,0]\n"
"v_pk_fma_f32 v[182:183], v[182:183], v[208:209], v[210:211] op_sel_hi:[1,0,0]\n"
"v_cvt_pk_f16_f32 v136, v176, v177\n"
"v_cvt_pk_f16_f32 v137, v178, v179\n"
"v_cvt_pk_f16_f32 v138, v180, v181\n"
"v_cvt_pk_f16_f32 v139, v182, v183\n"
"v_lshlrev_b32 v184, 16, v140\n"
"v_and_b32 v185, v237, v140\n"
"v_lshlrev_b32 v186, 16, v141\n"
"v_and_b32 v187, v237, v141\n"
"v_lshlrev_b32 v188, 16, v142\n"
"v_and_b32 v189, v237, v142\n"
"v_lshlrev_b32 v190, 16, v143\n"
"v_and_b32 v191, v237, v143\n"
"v_pk_fma_f32 v[184:185], v[184:185], v[212:213], v[214:215] op_sel_hi:[1,0,0]\n"
"v_pk_fma_f32 v[186:187], v[186:187], v[212:213], v[214:215] op_sel_hi:[1,0,0]\n"
"v_pk_fma_f32 v[188:189], v[188:189], v[212:213], v[214:215] op_sel_hi:[1,0,0]\n"
"v_pk_fma_f32 v[190:191], v[190:191], v[212:213], v[214:215] op_sel_hi:[1,0,0]\n"
"v_cvt_pk_f16_f32 v140, v184, v185\n"
"v_cvt_pk_f16_f32 v141, v186, v187\n"
"v_cvt_pk_f16_f32 v142, v188, v189\n"
"v_cvt_pk_f16_f32 v143, v190, v191\n"
"ds_read_b64_tr_b16 a[160:161], v219 offset:32784\n"
"s_waitcnt lgkmcnt(6)\n"
"v_fma_mix_f32 v176, v0, v144, v152 op_sel:[0,0,0] op_sel_hi:[1,0,0]\n"
"v_fma_mix_f32 v177, v0, v145, v153 op_sel:[1,0,0] op_sel_hi:[1,0,0]\n"
"v_fma_mix_f32 v178, v1, v146, v154 op_sel:[0,0,0] op_sel_hi:[1,0,0]\n"
"ds_read_b64_tr_b16 a[162:163], v219 offset:33808\n"
"v_fma_mix_f32 v179, v1, v147, v155 op_sel:[1,0,0] op_sel_hi:[1,0,0]\n"
"s_waitcnt lgkmcnt(6)\n"
"v_fma_mix_f32 v180, v2, v148, v156 op_sel:[0,0,0] op_sel_hi:[1,0,0]\n"
"v_fma_mix_f32 v181, v2, v149, v157 op_sel:[1,0,0] op_sel_hi:[1,0,0]\n"
"ds_read_b64_tr_b16 a[164:165], v219 offset:36880\n"
"v_fma_mix_f32 v182, v3, v150, v158 op_sel:[0,0,0] op_sel_hi:[1,0,0]\n"
"v_fma_mix_f32 v183, v3, v151, v159 op_sel:[1,0,0] op_sel_hi:[1,0,0]\n"
"v_cvt_pk_bf16_f32 v0, v176, v177\n"
"ds_read_b64_tr_b16 a[166:167], v219 offset:37904\n"
"v_cvt_pk_bf16_f32 v1, v178, v179\n"
"v_cvt_pk_bf16_f32 v2, v180, v181\n"
"v_cvt_pk_bf16_f32 v3, v182, v183\n"
"ds_read_b64_tr_b16 a[168:169], v219 offset:32792\n"
"v_fma_mix_f32 v184, v4, v144, v152 op_sel:[0,0,0] op_sel_hi:[1,0,0]\n"
"v_fma_mix_f32 v185, v4, v145, v153 op_sel:[1,0,0] op_sel_hi:[1,0,0]\n"
"v_fma_mix_f32 v186, v5, v146, v154 op_sel:[0,0,0] op_sel_hi:[1,0,0]\n"
"ds_read_b64_tr_b16 a[170:171], v219 offset:33816\n"
"v_fma_mix_f32 v187, v5, v147, v155 op_sel:[1,0,0] op_sel_hi:[1,0,0]\n"
"v_fma_mix_f32 v188, v6, v148, v156 op_sel:[0,0,0] op_sel_hi:[1,0,0]\n"
"v_fma_mix_f32 v189, v6, v149, v157 op_sel:[1,0,0] op_sel_hi:[1,0,0]\n"
"ds_read_b64_tr_b16 a[172:173], v219 offset:36888\n"
"v_fma_mix_f32 v190, v7, v150, v158 op_sel:[0,0,0] op_sel_hi:[1,0,0]\n"
"v_fma_mix_f32 v191, v7, v151, v159 op_sel:[1,0,0] op_sel_hi:[1,0,0]\n"
"v_cvt_pk_bf16_f32 v4, v184, v185\n"
"ds_read_b64_tr_b16 a[174:175], v219 offset:37912\n"
"ds_read_b128 v[144:147], v224 offset:512\n"
"v_cvt_pk_bf16_f32 v5, v186, v187\n"
"v_cvt_pk_bf16_f32 v6, v188, v189\n"
"ds_read_b64_tr_b16 a[176:177], v219 offset:33040\n"
"v_cvt_pk_bf16_f32 v7, v190, v191\n"
"s_waitcnt lgkmcnt(11)\n"
"v_fma_mix_f32 v176, v8, v160, v168 op_sel:[0,0,0] op_sel_hi:[1,0,0]\n"
"v_fma_mix_f32 v177, v8, v161, v169 op_sel:[1,0,0] op_sel_hi:[1,0,0]\n"
"ds_read_b64_tr_b16 a[178:179], v219 offset:34064\n"
"v_fma_mix_f32 v178, v9, v162, v170 op_sel:[0,0,0] op_sel_hi:[1,0,0]\n"
"v_fma_mix_f32 v179, v9, v163, v171 op_sel:[1,0,0] op_sel_hi:[1,0,0]\n"
"s_waitcnt lgkmcnt(11)\n"
"v_fma_mix_f32 v180, v10, v164, v172 op_sel:[0,0,0] op_sel_hi:[1,0,0]\n"
"ds_read_b64_tr_b16 a[180:181], v219 offset:37136\n"
"ds_read_b128 v[148:151], v224 offset:576\n"
"v_fma_mix_f32 v181, v10, v165, v173 op_sel:[1,0,0] op_sel_hi:[1,0,0]\n"
"v_fma_mix_f32 v182, v11, v166, v174 op_sel:[0,0,0] op_sel_hi:[1,0,0]\n"
"ds_read_b64_tr_b16 a[182:183], v219 offset:38160\n"
"v_fma_mix_f32 v183, v11, v167, v175 op_sel:[1,0,0] op_sel_hi:[1,0,0]\n"
"v_cvt_pk_bf16_f32 v8, v176, v177\n"
"v_cvt_pk_bf16_f32 v9, v178, v179\n"
"ds_read_b64_tr_b16 a[184:185], v219 offset:33048\n"
"v_cvt_pk_bf16_f32 v10, v180, v181\n"
"v_cvt_pk_bf16_f32 v11, v182, v183\n"
"v_fma_mix_f32 v184, v12, v160, v168 op_sel:[0,0,0] op_sel_hi:[1,0,0]\n"
"ds_read_b64_tr_b16 a[186:187], v219 offset:34072\n"
"ds_read_b128 v[152:155], v224 offset:640\n"
"v_fma_mix_f32 v185, v12, v161, v169 op_sel:[1,0,0] op_sel_hi:[1,0,0]\n"
"v_fma_mix_f32 v186, v13, v162, v170 op_sel:[0,0,0] op_sel_hi:[1,0,0]\n"
"ds_read_b64_tr_b16 a[188:189], v219 offset:37144\n"
"v_fma_mix_f32 v187, v13, v163, v171 op_sel:[1,0,0] op_sel_hi:[1,0,0]\n"
"v_fma_mix_f32 v188, v14, v164, v172 op_sel:[0,0,0] op_sel_hi:[1,0,0]\n"
"v_fma_mix_f32 v189, v14, v165, v173 op_sel:[1,0,0] op_sel_hi:[1,0,0]\n"
"ds_read_b64_tr_b16 a[190:191], v219 offset:38168\n"
"v_fma_mix_f32 v190, v15, v166, v174 op_sel:[0,0,0] op_sel_hi:[1,0,0]\n"
"v_fma_mix_f32 v191, v15, v167, v175 op_sel:[1,0,0] op_sel_hi:[1,0,0]\n"
"v_cvt_pk_bf16_f32 v12, v184, v185\n"
"ds_read_b64_tr_b16 a[224:225], v219 offset:33296\n"
"ds_read_b128 v[156:159], v224 offset:704\n"
"ds_read_b128 v[160:163], v224 offset:768\n"
"v_cvt_pk_bf16_f32 v13, v186, v187\n"
"ds_read_b64_tr_b16 a[226:227], v219 offset:34320\n"
"v_cvt_pk_bf16_f32 v14, v188, v189\n"
"v_cvt_pk_bf16_f32 v15, v190, v191\n"
"s_waitcnt lgkmcnt(6)\n"
"v_fma_mix_f32 v176, v16, v144, v152 op_sel:[0,0,0] op_sel_hi:[1,0,0]\n"
"ds_read_b64_tr_b16 a[228:229], v219 offset:37392\n"
"v_fma_mix_f32 v177, v16, v145, v153 op_sel:[1,0,0] op_sel_hi:[1,0,0]\n"
"v_fma_mix_f32 v178, v17, v146, v154 op_sel:[0,0,0] op_sel_hi:[1,0,0]\n"
"v_fma_mix_f32 v179, v17, v147, v155 op_sel:[1,0,0] op_sel_hi:[1,0,0]\n"
"ds_read_b64_tr_b16 a[230:231], v219 offset:38416\n"
"ds_read_b128 v[164:167], v224 offset:832\n"
"s_waitcnt lgkmcnt(5)\n"
"v_fma_mix_f32 v180, v18, v148, v156 op_sel:[0,0,0] op_sel_hi:[1,0,0]\n"
"v_fma_mix_f32 v181, v18, v149, v157 op_sel:[1,0,0] op_sel_hi:[1,0,0]\n"
"v_mfma_f32_16x16x32_bf16 a[0:3], a[160:163], v[0:3], 0\n"
"ds_read_b64_tr_b16 a[232:233], v219 offset:33304\n"
"v_fma_mix_f32 v182, v19, v150, v158 op_sel:[0,0,0] op_sel_hi:[1,0,0]\n"
"v_mfma_f32_16x16x32_bf16 a[0:3], a[164:167], v[8:11], a[0:3]\n"
"s_waitcnt vmcnt(8)\n"
"ds_read_b64_tr_b16 a[234:235], v219 offset:34328\n"
"v_fma_mix_f32 v183, v19, v151, v159 op_sel:[1,0,0] op_sel_hi:[1,0,0]\n"
"v_mfma_f32_16x16x32_bf16 a[4:7], a[160:163], v[4:7], 0\n"
"s_barrier\n"
"ds_read_b64_tr_b16 a[236:237], v219 offset:37400\n"
"ds_read_b128 v[168:171], v224 offset:896\n"
"v_mfma_f32_16x16x32_bf16 a[4:7], a[164:167], v[12:15], a[4:7]\n"
"ds_read_b64_tr_b16 a[238:239], v219 offset:38424\n"
"v_cvt_pk_bf16_f32 v16, v176, v177\n"
"v_mfma_f32_16x16x32_bf16 a[8:11], a[168:171], v[0:3], 0\n"
"s_add_u32 m0, s51, 65568\n"
"ds_read_b64_tr_b16 a[240:241], v219 offset:33552\n"
"v_cvt_pk_bf16_f32 v17, v178, v179\n"
"v_mfma_f32_16x16x32_bf16 a[8:11], a[172:175], v[8:11], a[8:11]\n"
"ds_read_b64_tr_b16 a[242:243], v219 offset:34576\n"
"buffer_load_dwordx4 v217, s[20:23], s50 offen offset:0 lds\n"
"v_mfma_f32_16x16x32_bf16 a[12:15], a[168:171], v[4:7], 0\n"
"ds_read_b64_tr_b16 a[244:245], v219 offset:37648\n"
"ds_read_b128 v[172:175], v224 offset:960\n"
"v_mfma_f32_16x16x32_bf16 a[12:15], a[172:175], v[12:15], a[12:15]\n"
"ds_read_b64_tr_b16 a[246:247], v219 offset:38672\n"
"v_cvt_pk_bf16_f32 v18, v180, v181\n"
"v_mfma_f32_16x16x32_bf16 a[16:19], a[176:179], v[0:3], 0\n"
"ds_read_b64_tr_b16 a[248:249], v219 offset:33560\n"
"v_cvt_pk_bf16_f32 v19, v182, v183\n"
"v_mfma_f32_16x16x32_bf16 a[16:19], a[180:183], v[8:11], a[16:19]\n"
"ds_read_b64_tr_b16 a[250:251], v219 offset:34584\n"
"v_fma_mix_f32 v184, v20, v144, v152 op_sel:[0,0,0] op_sel_hi:[1,0,0]\n"
"v_mfma_f32_16x16x32_bf16 a[20:23], a[176:179], v[4:7], 0\n"
"s_add_u32 m0, s51, 73504\n"
"ds_read_b64_tr_b16 a[252:253], v219 offset:37656\n"
"v_fma_mix_f32 v185, v20, v145, v153 op_sel:[1,0,0] op_sel_hi:[1,0,0]\n"
"v_mfma_f32_16x16x32_bf16 a[20:23], a[180:183], v[12:15], a[20:23]\n"
"ds_read_b64_tr_b16 a[254:255], v219 offset:38680\n"
"buffer_load_dwordx4 v217, s[20:23], s50 offen offset:256 lds\n"
"v_mfma_f32_16x16x32_bf16 a[24:27], a[184:187], v[0:3], 0\n"
"s_add_u32 s50, s50, 32768\n"
"ds_read_b64_tr_b16 a[160:161], v219 offset:40976\n"
"v_fma_mix_f32 v186, v21, v146, v154 op_sel:[0,0,0] op_sel_hi:[1,0,0]\n"
"v_mfma_f32_16x16x32_bf16 a[24:27], a[188:191], v[8:11], a[24:27]\n"
"ds_read_b64_tr_b16 a[162:163], v219 offset:42000\n"
"v_fma_mix_f32 v187, v21, v147, v155 op_sel:[1,0,0] op_sel_hi:[1,0,0]\n"
"v_mfma_f32_16x16x32_bf16 a[28:31], a[184:187], v[4:7], 0\n"
"ds_read_b64_tr_b16 a[164:165], v219 offset:45072\n"
"v_fma_mix_f32 v188, v22, v148, v156 op_sel:[0,0,0] op_sel_hi:[1,0,0]\n"
"v_mfma_f32_16x16x32_bf16 a[28:31], a[188:191], v[12:15], a[28:31]\n"
"s_waitcnt lgkmcnt(12)\n"
"ds_read_b64_tr_b16 a[166:167], v219 offset:46096\n"
"v_fma_mix_f32 v189, v22, v149, v157 op_sel:[1,0,0] op_sel_hi:[1,0,0]\n"
"v_mfma_f32_16x16x32_bf16 a[32:35], a[224:227], v[0:3], 0\n"
"s_add_u32 m0, s51, 66592\n"
"ds_read_b64_tr_b16 a[168:169], v219 offset:40984\n"
"v_fma_mix_f32 v190, v23, v150, v158 op_sel:[0,0,0] op_sel_hi:[1,0,0]\n"
"v_mfma_f32_16x16x32_bf16 a[32:35], a[228:231], v[8:11], a[32:35]\n"
"ds_read_b64_tr_b16 a[170:171], v219 offset:42008\n"
"buffer_load_dwordx4 v217, s[20:23], s50 offen offset:0 lds\n"
"v_mfma_f32_16x16x32_bf16 a[36:39], a[224:227], v[4:7], 0\n"
"ds_read_b64_tr_b16 a[172:173], v219 offset:45080\n"
"v_fma_mix_f32 v191, v23, v151, v159 op_sel:[1,0,0] op_sel_hi:[1,0,0]\n"
"v_mfma_f32_16x16x32_bf16 a[36:39], a[228:231], v[12:15], a[36:39]\n"
"ds_read_b64_tr_b16 a[174:175], v219 offset:46104\n"
"ds_read_b128 v[144:147], v224 offset:1024\n"
"v_mfma_f32_16x16x32_bf16 a[40:43], a[232:235], v[0:3], 0\n"
"ds_read_b64_tr_b16 a[176:177], v219 offset:41232\n"
"v_cvt_pk_bf16_f32 v20, v184, v185\n"
"v_mfma_f32_16x16x32_bf16 a[40:43], a[236:239], v[8:11], a[40:43]\n"
"ds_read_b64_tr_b16 a[178:179], v219 offset:42256\n"
"v_cvt_pk_bf16_f32 v21, v186, v187\n"
"v_mfma_f32_16x16x32_bf16 a[44:47], a[232:235], v[4:7], 0\n"
"s_add_u32 m0, s51, 74528\n"
"ds_read_b64_tr_b16 a[180:181], v219 offset:45328\n"
"ds_read_b128 v[148:151], v224 offset:1088\n"
"v_mfma_f32_16x16x32_bf16 a[44:47], a[236:239], v[12:15], a[44:47]\n"
"s_waitcnt lgkmcnt(13)\n"
"ds_read_b64_tr_b16 a[182:183], v219 offset:46352\n"
"buffer_load_dwordx4 v217, s[20:23], s50 offen offset:256 lds\n"
"v_mfma_f32_16x16x32_bf16 a[48:51], a[240:243], v[0:3], 0\n"
"s_add_u32 s50, s50, 32768\n"
"ds_read_b64_tr_b16 a[184:185], v219 offset:41240\n"
"v_cvt_pk_bf16_f32 v22, v188, v189\n"
"v_mfma_f32_16x16x32_bf16 a[48:51], a[244:247], v[8:11], a[48:51]\n"
"ds_read_b64_tr_b16 a[186:187], v219 offset:42264\n"
"ds_read_b128 v[152:155], v224 offset:1152\n"
"v_mfma_f32_16x16x32_bf16 a[52:55], a[240:243], v[4:7], 0\n"
"ds_read_b64_tr_b16 a[188:189], v219 offset:45336\n"
"v_cvt_pk_bf16_f32 v23, v190, v191\n"
"v_mfma_f32_16x16x32_bf16 a[52:55], a[244:247], v[12:15], a[52:55]\n"
"ds_read_b64_tr_b16 a[190:191], v219 offset:46360\n"
"v_fma_mix_f32 v176, v24, v160, v168 op_sel:[0,0,0] op_sel_hi:[1,0,0]\n"
"v_mfma_f32_16x16x32_bf16 a[56:59], a[248:251], v[0:3], 0\n"
"s_add_u32 m0, s51, 81960\n"
"ds_read_b64_tr_b16 a[224:225], v219 offset:41488\n"
"ds_read_b128 v[156:159], v224 offset:1216\n"
"v_mfma_f32_16x16x32_bf16 a[56:59], a[252:255], v[8:11], a[56:59]\n"
"ds_read_b64_tr_b16 a[226:227], v219 offset:42512\n"
"buffer_load_dwordx4 v217, s[20:23], s50 offen offset:0 lds\n"
"v_mfma_f32_16x16x32_bf16 a[60:63], a[248:251], v[4:7], 0\n"
"ds_read_b64_tr_b16 a[228:229], v219 offset:45584\n"
"v_fma_mix_f32 v177, v24, v161, v169 op_sel:[1,0,0] op_sel_hi:[1,0,0]\n"
"v_mfma_f32_16x16x32_bf16 a[60:63], a[252:255], v[12:15], a[60:63]\n"
"s_waitcnt lgkmcnt(14)\n"
"ds_read_b64_tr_b16 a[230:231], v219 offset:46608\n"
"v_fma_mix_f32 v178, v25, v162, v170 op_sel:[0,0,0] op_sel_hi:[1,0,0]\n"
"v_mfma_f32_16x16x32_bf16 a[64:67], a[160:163], v[0:3], 0\n"
"ds_read_b64_tr_b16 a[232:233], v219 offset:41496\n"
"v_fma_mix_f32 v179, v25, v163, v171 op_sel:[1,0,0] op_sel_hi:[1,0,0]\n"
"v_mfma_f32_16x16x32_bf16 a[64:67], a[164:167], v[8:11], a[64:67]\n"
"ds_read_b64_tr_b16 a[234:235], v219 offset:42520\n"
"v_fma_mix_f32 v180, v26, v164, v172 op_sel:[0,0,0] op_sel_hi:[1,0,0]\n"
"v_mfma_f32_16x16x32_bf16 a[68:71], a[160:163], v[4:7], 0\n"
"s_add_u32 m0, s51, 89896\n"
"ds_read_b64_tr_b16 a[236:237], v219 offset:45592\n"
"v_fma_mix_f32 v181, v26, v165, v173 op_sel:[1,0,0] op_sel_hi:[1,0,0]\n"
"v_mfma_f32_16x16x32_bf16 a[68:71], a[164:167], v[12:15], a[68:71]\n"
"ds_read_b64_tr_b16 a[238:239], v219 offset:46616\n"
"buffer_load_dwordx4 v217, s[20:23], s50 offen offset:256 lds\n"
"v_mfma_f32_16x16x32_bf16 a[72:75], a[168:171], v[0:3], 0\n"
"s_add_u32 s50, s50, 32768\n"
"ds_read_b64_tr_b16 a[240:241], v219 offset:41744\n"
"v_fma_mix_f32 v182, v27, v166, v174 op_sel:[0,0,0] op_sel_hi:[1,0,0]\n"
"v_mfma_f32_16x16x32_bf16 a[72:75], a[172:175], v[8:11], a[72:75]\n"
"ds_read_b64_tr_b16 a[242:243], v219 offset:42768\n"
"v_fma_mix_f32 v183, v27, v167, v175 op_sel:[1,0,0] op_sel_hi:[1,0,0]\n"
"v_mfma_f32_16x16x32_bf16 a[76:79], a[168:171], v[4:7], 0\n"
"ds_read_b64_tr_b16 a[244:245], v219 offset:45840\n"
"v_cvt_pk_bf16_f32 v24, v176, v177\n"
"v_mfma_f32_16x16x32_bf16 a[76:79], a[172:175], v[12:15], a[76:79]\n"
"s_waitcnt lgkmcnt(12)\n"
"ds_read_b64_tr_b16 a[246:247], v219 offset:46864\n"
"v_cvt_pk_bf16_f32 v25, v178, v179\n"
"v_mfma_f32_16x16x32_bf16 a[80:83], a[176:179], v[0:3], 0\n"
"s_add_u32 m0, s51, 82984\n"
"ds_read_b64_tr_b16 a[248:249], v219 offset:41752\n"
"v_cvt_pk_bf16_f32 v26, v180, v181\n"
"v_mfma_f32_16x16x32_bf16 a[80:83], a[180:183], v[8:11], a[80:83]\n"
"ds_read_b64_tr_b16 a[250:251], v219 offset:42776\n"
"buffer_load_dwordx4 v217, s[20:23], s50 offen offset:0 lds\n"
"v_mfma_f32_16x16x32_bf16 a[84:87], a[176:179], v[4:7], 0\n"
"ds_read_b64_tr_b16 a[252:253], v219 offset:45848\n"
"v_cvt_pk_bf16_f32 v27, v182, v183\n"
"v_mfma_f32_16x16x32_bf16 a[84:87], a[180:183], v[12:15], a[84:87]\n"
"ds_read_b64_tr_b16 a[254:255], v219 offset:46872\n"
"v_fma_mix_f32 v184, v28, v160, v168 op_sel:[0,0,0] op_sel_hi:[1,0,0]\n"
"v_mfma_f32_16x16x32_bf16 v[236:239], a[184:187], v[0:3], 0\n"
"ds_read_b64_tr_b16 a[160:161], v218 offset:0\n"
"v_fma_mix_f32 v185, v28, v161, v169 op_sel:[1,0,0] op_sel_hi:[1,0,0]\n"
"v_fma_mix_f32 v186, v29, v162, v170 op_sel:[0,0,0] op_sel_hi:[1,0,0]\n"
"v_mfma_f32_16x16x32_bf16 v[236:239], a[188:191], v[8:11], v[236:239]\n"
"ds_read_b64_tr_b16 a[162:163], v218 offset:1024\n"
"v_fma_mix_f32 v187, v29, v163, v171 op_sel:[1,0,0] op_sel_hi:[1,0,0]\n"
"v_mfma_f32_16x16x32_bf16 v[232:235], a[184:187], v[4:7], 0\n"
"s_add_u32 m0, s51, 90920\n"
"ds_read_b64_tr_b16 a[164:165], v218 offset:4096\n"
"v_fma_mix_f32 v188, v30, v164, v172 op_sel:[0,0,0] op_sel_hi:[1,0,0]\n"
"v_mfma_f32_16x16x32_bf16 v[232:235], a[188:191], v[12:15], v[232:235]\n"
"s_waitcnt lgkmcnt(11)\n"
"buffer_load_dwordx4 v217, s[20:23], s50 offen offset:256 lds\n"
"ds_read_b64_tr_b16 a[166:167], v218 offset:5120\n"
"v_mfma_f32_16x16x32_bf16 v[228:231], a[224:227], v[0:3], 0\n"
"s_add_u32 s50, s50, 425984\n"
"ds_read_b64_tr_b16 a[168:169], v218 offset:8\n"
"v_fma_mix_f32 v189, v30, v165, v173 op_sel:[1,0,0] op_sel_hi:[1,0,0]\n"
"v_mfma_f32_16x16x32_bf16 v[228:231], a[228:231], v[8:11], v[228:231]\n"
"ds_read_b64_tr_b16 a[170:171], v218 offset:1032\n"
"v_fma_mix_f32 v190, v31, v166, v174 op_sel:[0,0,0] op_sel_hi:[1,0,0]\n"
"v_mfma_f32_16x16x32_bf16 v[220:223], a[224:227], v[4:7], 0\n"
"ds_read_b64_tr_b16 a[172:173], v218 offset:4104\n"
"v_fma_mix_f32 v191, v31, v167, v175 op_sel:[1,0,0] op_sel_hi:[1,0,0]\n"
"v_mfma_f32_16x16x32_bf16 v[220:223], a[228:231], v[12:15], v[220:223]\n"
"ds_read_b64_tr_b16 a[174:175], v218 offset:5128\n"
"ds_read_b128 v[160:163], v224 offset:1280\n"
"v_mfma_f32_16x16x32_bf16 v[212:215], a[232:235], v[0:3], 0\n"
"ds_read_b64_tr_b16 a[176:177], v218 offset:256\n"
"v_cvt_pk_bf16_f32 v28, v184, v185\n"
"v_mfma_f32_16x16x32_bf16 v[212:215], a[236:239], v[8:11], v[212:215]\n"
"ds_read_b64_tr_b16 a[178:179], v218 offset:1280\n"
"v_cvt_pk_bf16_f32 v29, v186, v187\n"
"v_mfma_f32_16x16x32_bf16 v[208:211], a[232:235], v[4:7], 0\n"
"ds_read_b64_tr_b16 a[180:181], v218 offset:4352\n"
"ds_read_b128 v[164:167], v224 offset:1344\n"
"v_mfma_f32_16x16x32_bf16 v[208:211], a[236:239], v[12:15], v[208:211]\n"
"s_waitcnt lgkmcnt(13)\n"
"ds_read_b64_tr_b16 a[182:183], v218 offset:5376\n"
"v_cvt_pk_bf16_f32 v30, v188, v189\n"
"v_mfma_f32_16x16x32_bf16 v[204:207], a[240:243], v[0:3], 0\n"
"ds_read_b64_tr_b16 a[184:185], v218 offset:264\n"
"v_cvt_pk_bf16_f32 v31, v190, v191\n"
"v_mfma_f32_16x16x32_bf16 v[204:207], a[244:247], v[8:11], v[204:207]\n"
"ds_read_b64_tr_b16 a[186:187], v218 offset:1288\n"
"ds_read_b128 v[168:171], v224 offset:1408\n"
"v_mfma_f32_16x16x32_bf16 v[200:203], a[240:243], v[4:7], 0\n"
"ds_read_b64_tr_b16 a[188:189], v218 offset:4360\n"
"v_fma_mix_f32 v176, v32, v144, v152 op_sel:[0,0,0] op_sel_hi:[1,0,0]\n"
"v_mfma_f32_16x16x32_bf16 v[200:203], a[244:247], v[12:15], v[200:203]\n"
"ds_read_b64_tr_b16 a[190:191], v218 offset:5384\n"
"v_fma_mix_f32 v177, v32, v145, v153 op_sel:[1,0,0] op_sel_hi:[1,0,0]\n"
"v_mfma_f32_16x16x32_bf16 v[196:199], a[248:251], v[0:3], 0\n"
"ds_read_b64_tr_b16 a[224:225], v218 offset:512\n"
"ds_read_b128 v[172:175], v224 offset:1472\n"
"v_mfma_f32_16x16x32_bf16 v[196:199], a[252:255], v[8:11], v[196:199]\n"
"ds_read_b64_tr_b16 a[226:227], v218 offset:1536\n"
"v_fma_mix_f32 v178, v33, v146, v154 op_sel:[0,0,0] op_sel_hi:[1,0,0]\n"
"v_mfma_f32_16x16x32_bf16 v[192:195], a[248:251], v[4:7], 0\n"
"ds_read_b64_tr_b16 a[228:229], v218 offset:4608\n"
"v_fma_mix_f32 v179, v33, v147, v155 op_sel:[1,0,0] op_sel_hi:[1,0,0]\n"
"v_mfma_f32_16x16x32_bf16 v[192:195], a[252:255], v[12:15], v[192:195]\n"
"s_waitcnt lgkmcnt(14)\n"
"ds_read_b64_tr_b16 a[230:231], v218 offset:5632\n"
"v_fma_mix_f32 v180, v34, v148, v156 op_sel:[0,0,0] op_sel_hi:[1,0,0]\n"
"v_mfma_f32_16x16x32_bf16 a[0:3], a[160:163], v[16:19], a[0:3]\n"
"ds_read_b64_tr_b16 a[232:233], v218 offset:520\n"
"v_fma_mix_f32 v181, v34, v149, v157 op_sel:[1,0,0] op_sel_hi:[1,0,0]\n"
"v_mfma_f32_16x16x32_bf16 a[0:3], a[164:167], v[24:27], a[0:3]\n"
"s_waitcnt vmcnt(8)\n"
"ds_read_b64_tr_b16 a[234:235], v218 offset:1544\n"
"v_fma_mix_f32 v182, v35, v150, v158 op_sel:[0,0,0] op_sel_hi:[1,0,0]\n"
"v_mfma_f32_16x16x32_bf16 a[4:7], a[160:163], v[20:23], a[4:7]\n"
"s_barrier\n"
"ds_read_b64_tr_b16 a[236:237], v218 offset:4616\n"
"v_fma_mix_f32 v183, v35, v151, v159 op_sel:[1,0,0] op_sel_hi:[1,0,0]\n"
"v_mfma_f32_16x16x32_bf16 a[4:7], a[164:167], v[28:31], a[4:7]\n"
"ds_read_b64_tr_b16 a[238:239], v218 offset:5640\n"
"v_cvt_pk_bf16_f32 v32, v176, v177\n"
"v_mfma_f32_16x16x32_bf16 a[8:11], a[168:171], v[16:19], a[8:11]\n"
"s_add_u32 m0, s51, 98352\n"
"ds_read_b64_tr_b16 a[240:241], v218 offset:768\n"
"v_cvt_pk_bf16_f32 v33, v178, v179\n"
"v_mfma_f32_16x16x32_bf16 a[8:11], a[172:175], v[24:27], a[8:11]\n"
"ds_read_b64_tr_b16 a[242:243], v218 offset:1792\n"
"buffer_load_dwordx4 v217, s[20:23], s50 offen offset:0 lds\n"
"v_mfma_f32_16x16x32_bf16 a[12:15], a[168:171], v[20:23], a[12:15]\n"
"ds_read_b64_tr_b16 a[244:245], v218 offset:4864\n"
"v_cvt_pk_bf16_f32 v34, v180, v181\n"
"v_mfma_f32_16x16x32_bf16 a[12:15], a[172:175], v[28:31], a[12:15]\n"
"s_waitcnt lgkmcnt(12)\n"
"ds_read_b64_tr_b16 a[246:247], v218 offset:5888\n"
"v_cvt_pk_bf16_f32 v35, v182, v183\n"
"v_mfma_f32_16x16x32_bf16 a[16:19], a[176:179], v[16:19], a[16:19]\n"
"ds_read_b64_tr_b16 a[248:249], v218 offset:776\n"
"v_fma_mix_f32 v184, v36, v144, v152 op_sel:[0,0,0] op_sel_hi:[1,0,0]\n"
"v_mfma_f32_16x16x32_bf16 a[16:19], a[180:183], v[24:27], a[16:19]\n"
"ds_read_b64_tr_b16 a[250:251], v218 offset:1800\n"
"v_fma_mix_f32 v185, v36, v145, v153 op_sel:[1,0,0] op_sel_hi:[1,0,0]\n"
"v_mfma_f32_16x16x32_bf16 a[20:23], a[176:179], v[20:23], a[20:23]\n"
"s_add_u32 m0, s51, 106288\n"
"ds_read_b64_tr_b16 a[252:253], v218 offset:4872\n"
"v_fma_mix_f32 v186, v37, v146, v154 op_sel:[0,0,0] op_sel_hi:[1,0,0]\n"
"v_mfma_f32_16x16x32_bf16 a[20:23], a[180:183], v[28:31], a[20:23]\n"
"ds_read_b64_tr_b16 a[254:255], v218 offset:5896\n"
"buffer_load_dwordx4 v217, s[20:23], s50 offen offset:256 lds\n"
"v_mfma_f32_16x16x32_bf16 a[24:27], a[184:187], v[16:19], a[24:27]\n"
"s_add_u32 s50, s50, 32768\n"
"ds_read_b64_tr_b16 a[160:161], v218 offset:8192\n"
"v_fma_mix_f32 v187, v37, v147, v155 op_sel:[1,0,0] op_sel_hi:[1,0,0]\n"
"v_mfma_f32_16x16x32_bf16 a[24:27], a[188:191], v[24:27], a[24:27]\n"
"ds_read_b64_tr_b16 a[162:163], v218 offset:9216\n"
"v_fma_mix_f32 v188, v38, v148, v156 op_sel:[0,0,0] op_sel_hi:[1,0,0]\n"
"v_mfma_f32_16x16x32_bf16 a[28:31], a[184:187], v[20:23], a[28:31]\n"
"ds_read_b64_tr_b16 a[164:165], v218 offset:12288\n"
"v_fma_mix_f32 v189, v38, v149, v157 op_sel:[1,0,0] op_sel_hi:[1,0,0]\n"
"v_mfma_f32_16x16x32_bf16 a[28:31], a[188:191], v[28:31], a[28:31]\n"
"s_waitcnt lgkmcnt(11)\n"
"ds_read_b64_tr_b16 a[166:167], v218 offset:13312\n"
"v_fma_mix_f32 v190, v39, v150, v158 op_sel:[0,0,0] op_sel_hi:[1,0,0]\n"
"v_mfma_f32_16x16x32_bf16 a[32:35], a[224:227], v[16:19], a[32:35]\n"
"s_add_u32 m0, s51, 99376\n"
"ds_read_b64_tr_b16 a[168:169], v218 offset:8200\n"
"v_fma_mix_f32 v191, v39, v151, v159 op_sel:[1,0,0] op_sel_hi:[1,0,0]\n"
"v_mfma_f32_16x16x32_bf16 a[32:35], a[228:231], v[24:27], a[32:35]\n"
"s_and_b32 s62, s42, 7\n"
"ds_read_b64_tr_b16 a[170:171], v218 offset:9224\n"
"buffer_load_dwordx4 v217, s[20:23], s50 offen offset:0 lds\n"
"v_mfma_f32_16x16x32_bf16 a[36:39], a[224:227], v[20:23], a[36:39]\n"
"ds_read_b64_tr_b16 a[172:173], v218 offset:12296\n"
"ds_read_b128 v[144:147], v224 offset:1536\n"
"v_mfma_f32_16x16x32_bf16 a[36:39], a[228:231], v[28:31], a[36:39]\n"
"s_mul_i32 s46, s62, 663552\n"
"ds_read_b64_tr_b16 a[174:175], v218 offset:13320\n"
"v_cvt_pk_bf16_f32 v36, v184, v185\n"
"v_mfma_f32_16x16x32_bf16 a[40:43], a[232:235], v[16:19], a[40:43]\n"
"ds_read_b64_tr_b16 a[176:177], v218 offset:8448\n"
"v_cvt_pk_bf16_f32 v37, v186, v187\n"
"v_mfma_f32_16x16x32_bf16 a[40:43], a[236:239], v[24:27], a[40:43]\n"
"s_lshr_b32 s47, s43, 2\n"
"ds_read_b64_tr_b16 a[178:179], v218 offset:9472\n"
"ds_read_b128 v[148:151], v224 offset:1600\n"
"v_mfma_f32_16x16x32_bf16 a[44:47], a[232:235], v[20:23], a[44:47]\n"
"s_add_u32 m0, s51, 107312\n"
"ds_read_b64_tr_b16 a[180:181], v218 offset:12544\n"
"v_cvt_pk_bf16_f32 v38, v188, v189\n"
"v_mfma_f32_16x16x32_bf16 a[44:47], a[236:239], v[28:31], a[44:47]\n"
"s_waitcnt lgkmcnt(13)\n"
"s_mul_i32 s47, s47, 1179648\n"
"ds_read_b64_tr_b16 a[182:183], v218 offset:13568\n"
"buffer_load_dwordx4 v217, s[20:23], s50 offen offset:256 lds\n"
"v_mfma_f32_16x16x32_bf16 a[48:51], a[240:243], v[16:19], a[48:51]\n"
"s_add_u32 s50, s50, 32768\n"
"ds_read_b64_tr_b16 a[184:185], v218 offset:8456\n"
"ds_read_b128 v[152:155], v224 offset:1664\n"
"v_mfma_f32_16x16x32_bf16 a[48:51], a[244:247], v[24:27], a[48:51]\n"
"s_getpc_b64 s[32:33]\n"
"AT_pc1:\n"
"s_sub_u32 s32, s32, AT_pc1-AT_start\n"
"s_subb_u32 s33, s33, 0\n"
"ds_read_b64_tr_b16 a[186:187], v218 offset:9480\n"
"v_cvt_pk_bf16_f32 v39, v190, v191\n"
"v_mfma_f32_16x16x32_bf16 a[52:55], a[240:243], v[20:23], a[52:55]\n"
"ds_read_b64_tr_b16 a[188:189], v218 offset:12552\n"
"v_fma_mix_f32 v176, v40, v160, v168 op_sel:[0,0,0] op_sel_hi:[1,0,0]\n"
"v_mfma_f32_16x16x32_bf16 a[52:55], a[244:247], v[28:31], a[52:55]\n"
"s_and_b32 s33, s33, 0xffff\n"
"ds_read_b64_tr_b16 a[190:191], v218 offset:13576\n"
"ds_read_b128 v[156:159], v224 offset:1728\n"
"v_mfma_f32_16x16x32_bf16 a[56:59], a[248:251], v[16:19], a[56:59]\n"
"s_add_u32 m0, s51, 114744\n"
"ds_read_b64_tr_b16 a[224:225], v218 offset:8704\n"
"v_fma_mix_f32 v177, v40, v161, v169 op_sel:[1,0,0] op_sel_hi:[1,0,0]\n"
"v_mfma_f32_16x16x32_bf16 a[56:59], a[252:255], v[24:27], a[56:59]\n"
"s_mov_b32 s34, AT_kend-AT_start\n"
"ds_read_b64_tr_b16 a[226:227], v218 offset:9728\n"
"buffer_load_dwordx4 v217, s[20:23], s50 offen offset:0 lds\n"
"v_mfma_f32_16x16x32_bf16 a[60:63], a[248:251], v[20:23], a[60:63]\n"
"ds_read_b64_tr_b16 a[228:229], v218 offset:12800\n"
"v_fma_mix_f32 v178, v41, v162, v170 op_sel:[0,0,0] op_sel_hi:[1,0,0]\n"
"v_mfma_f32_16x16x32_bf16 a[60:63], a[252:255], v[28:31], a[60:63]\n"
"s_waitcnt lgkmcnt(14)\n"
"s_mov_b32 s35, 0x20000\n"
"ds_read_b64_tr_b16 a[230:231], v218 offset:13824\n"
"v_fma_mix_f32 v179, v41, v163, v171 op_sel:[1,0,0] op_sel_hi:[1,0,0]\n"
"v_mfma_f32_16x16x32_bf16 a[64:67], a[160:163], v[16:19], a[64:67]\n"
"ds_read_b64_tr_b16 a[232:233], v218 offset:8712\n"
"v_fma_mix_f32 v180, v42, v164, v172 op_sel:[0,0,0] op_sel_hi:[1,0,0]\n"
"v_mfma_f32_16x16x32_bf16 a[64:67], a[164:167], v[24:27], a[64:67]\n"
"s_lshr_b32 s62, s42, 3\n"
"ds_read_b64_tr_b16 a[234:235], v218 offset:9736\n"
"v_fma_mix_f32 v181, v42, v165, v173 op_sel:[1,0,0] op_sel_hi:[1,0,0]\n"
"v_mfma_f32_16x16x32_bf16 a[68:71], a[160:163], v[20:23], a[68:71]\n"
"s_add_u32 m0, s51, 122680\n"
"ds_read_b64_tr_b16 a[236:237], v218 offset:12808\n"
"v_fma_mix_f32 v182, v43, v166, v174 op_sel:[0,0,0] op_sel_hi:[1,0,0]\n"
"v_mfma_f32_16x16x32_bf16 a[68:71], a[164:167], v[28:31], a[68:71]\n"
"s_lshl_b32 s62, s62, 2\n"
"ds_read_b64_tr_b16 a[238:239], v218 offset:13832\n"
"buffer_load_dwordx4 v217, s[20:23], s50 offen offset:256 lds\n"
"v_mfma_f32_16x16x32_bf16 a[72:75], a[168:171], v[16:19], a[72:75]\n"
"s_add_u32 s50, s50, 32768\n"
"ds_read_b64_tr_b16 a[240:241], v218 offset:8960\n"
"v_fma_mix_f32 v183, v43, v167, v175 op_sel:[1,0,0] op_sel_hi:[1,0,0]\n"
"v_mfma_f32_16x16x32_bf16 a[72:75], a[172:175], v[24:27], a[72:75]\n"
"s_add_u32 s62, s62, s41\n"
"ds_read_b64_tr_b16 a[242:243], v218 offset:9984\n"
"v_cvt_pk_bf16_f32 v40, v176, v177\n"
"v_mfma_f32_16x16x32_bf16 a[76:79], a[168:171], v[20:23], a[76:79]\n"
"ds_read_b64_tr_b16 a[244:245], v218 offset:13056\n"
"v_cvt_pk_bf16_f32 v41, v178, v179\n"
"v_mfma_f32_16x16x32_bf16 a[76:79], a[172:175], v[28:31], a[76:79]\n"
"s_waitcnt lgkmcnt(11)\n"
"s_and_b32 s62, s62, 31\n"
"ds_read_b64_tr_b16 a[246:247], v218 offset:14080\n"
"v_cvt_pk_bf16_f32 v42, v180, v181\n"
"v_mfma_f32_16x16x32_bf16 a[80:83], a[176:179], v[16:19], a[80:83]\n"
"s_add_u32 m0, s51, 115768\n"
"ds_read_b64_tr_b16 a[248:249], v218 offset:8968\n"
"v_cvt_pk_bf16_f32 v43, v182, v183\n"
"v_mfma_f32_16x16x32_bf16 a[80:83], a[180:183], v[24:27], a[80:83]\n"
"s_lshl_b32 s48, s62, 12\n"
"ds_read_b64_tr_b16 a[250:251], v218 offset:9992\n"
"buffer_load_dwordx4 v217, s[20:23], s50 offen offset:0 lds\n"
"v_mfma_f32_16x16x32_bf16 a[84:87], a[176:179], v[20:23], a[84:87]\n"
"ds_read_b64_tr_b16 a[252:253], v218 offset:13064\n"
"v_fma_mix_f32 v184, v44, v160, v168 op_sel:[0,0,0] op_sel_hi:[1,0,0]\n"
"v_mfma_f32_16x16x32_bf16 a[84:87], a[180:183], v[28:31], a[84:87]\n"
"s_lshl_b32 s54, s44, 2\n"
"ds_read_b64_tr_b16 a[254:255], v218 offset:14088\n"
"v_fma_mix_f32 v185, v44, v161, v169 op_sel:[1,0,0] op_sel_hi:[1,0,0]\n"
"v_mfma_f32_16x16x32_bf16 v[236:239], a[184:187], v[16:19], v[236:239]\n"
"ds_read_b64_tr_b16 a[160:161], v218 offset:32784\n"
"v_fma_mix_f32 v186, v45, v162, v170 op_sel:[0,0,0] op_sel_hi:[1,0,0]\n"
"v_fma_mix_f32 v187, v45, v163, v171 op_sel:[1,0,0] op_sel_hi:[1,0,0]\n"
"v_mfma_f32_16x16x32_bf16 v[236:239], a[188:191], v[24:27], v[236:239]\n"
"s_add_u32 s54, s54, s41\n"
"ds_read_b64_tr_b16 a[162:163], v218 offset:33808\n"
"v_fma_mix_f32 v188, v46, v164, v172 op_sel:[0,0,0] op_sel_hi:[1,0,0]\n"
"v_mfma_f32_16x16x32_bf16 v[232:235], a[184:187], v[20:23], v[232:235]\n"
"s_add_u32 m0, s51, 123704\n"
"ds_read_b64_tr_b16 a[164:165], v218 offset:36880\n"
"v_fma_mix_f32 v189, v46, v165, v173 op_sel:[1,0,0] op_sel_hi:[1,0,0]\n"
"v_mfma_f32_16x16x32_bf16 v[232:235], a[188:191], v[28:31], v[232:235]\n"
"s_waitcnt lgkmcnt(11)\n"
"s_mul_i32 s54, s54, 36864\n"
"buffer_load_dwordx4 v217, s[20:23], s50 offen offset:256 lds\n"
"ds_read_b64_tr_b16 a[166:167], v218 offset:37904\n"
"v_mfma_f32_16x16x32_bf16 v[228:231], a[224:227], v[16:19], v[228:231]\n"
"s_add_u32 s50, s50, 425984\n"
"ds_read_b64_tr_b16 a[168:169], v218 offset:32792\n"
"v_fma_mix_f32 v190, v47, v166, v174 op_sel:[0,0,0] op_sel_hi:[1,0,0]\n"
"v_mfma_f32_16x16x32_bf16 v[228:231], a[228:231], v[24:27], v[228:231]\n"
"s_lshr_b32 s62, s43, 2\n"
"ds_read_b64_tr_b16 a[170:171], v218 offset:33816\n"
"v_fma_mix_f32 v191, v47, v167, v175 op_sel:[1,0,0] op_sel_hi:[1,0,0]\n"
"v_mfma_f32_16x16x32_bf16 v[220:223], a[224:227], v[20:23], v[220:223]\n"
"ds_read_b64_tr_b16 a[172:173], v218 offset:36888\n"
"ds_read_b128 v[160:163], v224 offset:1792\n"
"v_mfma_f32_16x16x32_bf16 v[220:223], a[228:231], v[28:31], v[220:223]\n"
"s_mul_i32 s62, s62, 2359296\n"
"ds_read_b64_tr_b16 a[174:175], v218 offset:37912\n"
"v_cvt_pk_bf16_f32 v44, v184, v185\n"
"v_mfma_f32_16x16x32_bf16 v[212:215], a[232:235], v[16:19], v[212:215]\n"
"ds_read_b64_tr_b16 a[176:177], v218 offset:33040\n"
"v_cvt_pk_bf16_f32 v45, v186, v187\n"
"v_mfma_f32_16x16x32_bf16 v[212:215], a[236:239], v[24:27], v[212:215]\n"
"s_add_u32 s54, s54, s62\n"
"ds_read_b64_tr_b16 a[178:179], v218 offset:34064\n"
"ds_read_b128 v[164:167], v224 offset:1856\n"
"v_mfma_f32_16x16x32_bf16 v[208:211], a[232:235], v[20:23], v[208:211]\n"
"ds_read_b64_tr_b16 a[180:181], v218 offset:37136\n"
"v_cvt_pk_bf16_f32 v46, v188, v189\n"
"v_mfma_f32_16x16x32_bf16 v[208:211], a[236:239], v[28:31], v[208:211]\n"
"s_waitcnt lgkmcnt(13)\n"
"s_add_u32 s54, s54, 139264\n"
"ds_read_b64_tr_b16 a[182:183], v218 offset:38160\n"
"v_cvt_pk_bf16_f32 v47, v190, v191\n"
"v_mfma_f32_16x16x32_bf16 v[204:207], a[240:243], v[16:19], v[204:207]\n"
"ds_read_b64_tr_b16 a[184:185], v218 offset:33048\n"
"ds_read_b128 v[168:171], v224 offset:1920\n"
"v_mfma_f32_16x16x32_bf16 v[204:207], a[244:247], v[24:27], v[204:207]\n"
"s_bfe_u32 s62, s43, 0x10001\n"
"ds_read_b64_tr_b16 a[186:187], v218 offset:34072\n"
"v_fma_mix_f32 v176, v48, v144, v152 op_sel:[0,0,0] op_sel_hi:[1,0,0]\n"
"v_mfma_f32_16x16x32_bf16 v[200:203], a[240:243], v[20:23], v[200:203]\n"
"ds_read_b64_tr_b16 a[188:189], v218 offset:37144\n"
"v_fma_mix_f32 v177, v48, v145, v153 op_sel:[1,0,0] op_sel_hi:[1,0,0]\n"
"v_mfma_f32_16x16x32_bf16 v[200:203], a[244:247], v[28:31], v[200:203]\n"
"s_mul_i32 s62, s62, 9216\n"
"ds_read_b64_tr_b16 a[190:191], v218 offset:38168\n"
"ds_read_b128 v[172:175], v224 offset:1984\n"
"v_mfma_f32_16x16x32_bf16 v[196:199], a[248:251], v[16:19], v[196:199]\n"
"ds_read_b64_tr_b16 a[224:225], v218 offset:33296\n"
"v_fma_mix_f32 v178, v49, v146, v154 op_sel:[0,0,0] op_sel_hi:[1,0,0]\n"
"v_mfma_f32_16x16x32_bf16 v[196:199], a[252:255], v[24:27], v[196:199]\n"
"s_add_u32 s54, s54, s62\n"
"ds_read_b64_tr_b16 a[226:227], v218 offset:34320\n"
"v_fma_mix_f32 v179, v49, v147, v155 op_sel:[1,0,0] op_sel_hi:[1,0,0]\n"
"v_mfma_f32_16x16x32_bf16 v[192:195], a[248:251], v[20:23], v[192:195]\n"
"ds_read_b64_tr_b16 a[228:229], v218 offset:37392\n"
"v_fma_mix_f32 v180, v50, v148, v156 op_sel:[0,0,0] op_sel_hi:[1,0,0]\n"
"v_mfma_f32_16x16x32_bf16 v[192:195], a[252:255], v[28:31], v[192:195]\n"
"s_waitcnt lgkmcnt(14)\n"
"s_and_b32 s62, s43, 1\n"
"ds_read_b64_tr_b16 a[230:231], v218 offset:38416\n"
"v_fma_mix_f32 v181, v50, v149, v157 op_sel:[1,0,0] op_sel_hi:[1,0,0]\n"
"v_mfma_f32_16x16x32_bf16 a[0:3], a[160:163], v[32:35], a[0:3]\n"
"ds_read_b64_tr_b16 a[232:233], v218 offset:33304\n"
"v_fma_mix_f32 v182, v51, v150, v158 op_sel:[0,0,0] op_sel_hi:[1,0,0]\n"
"v_mfma_f32_16x16x32_bf16 a[0:3], a[164:167], v[40:43], a[0:3]\n"
"s_lshl_b32 s49, s62, 3\n"
"ds_read_b64_tr_b16 a[234:235], v218 offset:34328\n"
"v_fma_mix_f32 v183, v51, v151, v159 op_sel:[1,0,0] op_sel_hi:[1,0,0]\n"
"v_mfma_f32_16x16x32_bf16 a[4:7], a[160:163], v[36:39], a[4:7]\n"
"s_waitcnt vmcnt(8)\n"
"s_barrier\n"
"ds_read_b64_tr_b16 a[236:237], v218 offset:37400\n"
"v_cvt_pk_bf16_f32 v48, v176, v177\n"
"v_mfma_f32_16x16x32_bf16 a[4:7], a[164:167], v[44:47], a[4:7]\n"
"s_add_u32 s49, s49, s54\n"
"ds_read_b64_tr_b16 a[238:239], v218 offset:38424\n"
"v_cvt_pk_bf16_f32 v49, v178, v179\n"
"v_mfma_f32_16x16x32_bf16 a[8:11], a[168:171], v[32:35], a[8:11]\n"
"s_mov_b32 m0, s51\n"
"ds_read_b64_tr_b16 a[240:241], v218 offset:33552\n"
"v_cvt_pk_bf16_f32 v50, v180, v181\n"
"v_mfma_f32_16x16x32_bf16 a[8:11], a[172:175], v[40:43], a[8:11]\n"
"s_add_u32 s49, s49, 4096\n"
"ds_read_b64_tr_b16 a[242:243], v218 offset:34576\n"
"buffer_load_dwordx4 v217, s[20:23], s50 offen offset:0 lds\n"
"v_mfma_f32_16x16x32_bf16 a[12:15], a[168:171], v[36:39], a[12:15]\n"
"ds_read_b64_tr_b16 a[244:245], v218 offset:37648\n"
"v_cvt_pk_bf16_f32 v51, v182, v183\n"
"v_mfma_f32_16x16x32_bf16 a[12:15], a[172:175], v[44:47], a[12:15]\n"
"s_waitcnt lgkmcnt(11)\n"
"s_mul_i32 s62, s62, 5120\n"
"ds_read_b64_tr_b16 a[246:247], v218 offset:38672\n"
"v_fma_mix_f32 v184, v52, v144, v152 op_sel:[0,0,0] op_sel_hi:[1,0,0]\n"
"v_mfma_f32_16x16x32_bf16 a[16:19], a[176:179], v[32:35], a[16:19]\n"
"ds_read_b64_tr_b16 a[248:249], v218 offset:33560\n"
"v_fma_mix_f32 v185, v52, v145, v153 op_sel:[1,0,0] op_sel_hi:[1,0,0]\n"
"v_mfma_f32_16x16x32_bf16 a[16:19], a[180:183], v[40:43], a[16:19]\n"
"s_add_u32 s54, s54, s62\n"
"ds_read_b64_tr_b16 a[250:251], v218 offset:34584\n"
"v_fma_mix_f32 v186, v53, v146, v154 op_sel:[0,0,0] op_sel_hi:[1,0,0]\n"
"v_mfma_f32_16x16x32_bf16 a[20:23], a[176:179], v[36:39], a[20:23]\n"
"s_add_u32 m0, s51, 7936\n"
"ds_read_b64_tr_b16 a[252:253], v218 offset:37656\n"
"v_fma_mix_f32 v187, v53, v147, v155 op_sel:[1,0,0] op_sel_hi:[1,0,0]\n"
"v_mfma_f32_16x16x32_bf16 a[20:23], a[180:183], v[44:47], a[20:23]\n"
"s_and_b32 s62, s43, 1\n"
"ds_read_b64_tr_b16 a[254:255], v218 offset:38680\n"
"buffer_load_dwordx4 v217, s[20:23], s50 offen offset:256 lds\n"
"v_mfma_f32_16x16x32_bf16 a[24:27], a[184:187], v[32:35], a[24:27]\n"
"s_add_u32 s50, s50, 32768\n"
"ds_read_b64_tr_b16 a[160:161], v218 offset:40976\n"
"v_fma_mix_f32 v188, v54, v148, v156 op_sel:[0,0,0] op_sel_hi:[1,0,0]\n"
"v_mfma_f32_16x16x32_bf16 a[24:27], a[188:191], v[40:43], a[24:27]\n"
"s_lshl_b32 s62, s62, 10\n"
"ds_read_b64_tr_b16 a[162:163], v218 offset:42000\n"
"v_fma_mix_f32 v189, v54, v149, v157 op_sel:[1,0,0] op_sel_hi:[1,0,0]\n"
"v_mfma_f32_16x16x32_bf16 a[28:31], a[184:187], v[36:39], a[28:31]\n"
"ds_read_b64_tr_b16 a[164:165], v218 offset:45072\n"
"v_fma_mix_f32 v190, v55, v150, v158 op_sel:[0,0,0] op_sel_hi:[1,0,0]\n"
"v_mfma_f32_16x16x32_bf16 a[28:31], a[188:191], v[44:47], a[28:31]\n"
"s_waitcnt lgkmcnt(11)\n"
"s_sub_u32 s54, s54, s62\n"
"ds_read_b64_tr_b16 a[166:167], v218 offset:46096\n"
"v_fma_mix_f32 v191, v55, v151, v159 op_sel:[1,0,0] op_sel_hi:[1,0,0]\n"
"v_mfma_f32_16x16x32_bf16 a[32:35], a[224:227], v[32:35], a[32:35]\n"
"s_add_u32 m0, s51, 1024\n"
"ds_read_b64_tr_b16 a[168:169], v218 offset:40984\n"
"ds_read_b128 v[144:147], v224 offset:2048\n"
"v_mfma_f32_16x16x32_bf16 a[32:35], a[228:231], v[40:43], a[32:35]\n"
"s_add_u32 s49, s54, 4096\n"
"ds_read_b64_tr_b16 a[170:171], v218 offset:42008\n"
"buffer_load_dwordx4 v217, s[20:23], s50 offen offset:0 lds\n"
"v_mfma_f32_16x16x32_bf16 a[36:39], a[224:227], v[36:39], a[36:39]\n"
"ds_read_b64_tr_b16 a[172:173], v218 offset:45080\n"
"v_cvt_pk_bf16_f32 v52, v184, v185\n"
"v_mfma_f32_16x16x32_bf16 a[36:39], a[228:231], v[44:47], a[36:39]\n"
"s_lshl_b32 s52, s44, 4\n"
"ds_read_b64_tr_b16 a[174:175], v218 offset:46104\n"
"ds_read_b128 v[148:151], v224 offset:2112\n"
"v_mfma_f32_16x16x32_bf16 a[40:43], a[232:235], v[32:35], a[40:43]\n"
"ds_read_b64_tr_b16 a[176:177], v218 offset:41232\n"
"v_cvt_pk_bf16_f32 v53, v186, v187\n"
"v_mfma_f32_16x16x32_bf16 a[40:43], a[236:239], v[40:43], a[40:43]\n"
"s_add_u32 s52, s52, s43\n"
"ds_read_b64_tr_b16 a[178:179], v218 offset:42256\n"
"v_cvt_pk_bf16_f32 v54, v188, v189\n"
"v_mfma_f32_16x16x32_bf16 a[44:47], a[232:235], v[36:39], a[44:47]\n"
"s_add_u32 m0, s51, 8960\n"
"ds_read_b64_tr_b16 a[180:181], v218 offset:45328\n"
"ds_read_b128 v[152:155], v224 offset:2176\n"
"v_mfma_f32_16x16x32_bf16 a[44:47], a[236:239], v[44:47], a[44:47]\n"
"s_waitcnt lgkmcnt(14)\n"
"s_mul_i32 s52, s52, 18432\n"
"ds_read_b64_tr_b16 a[182:183], v218 offset:46352\n"
"buffer_load_dwordx4 v217, s[20:23], s50 offen offset:256 lds\n"
"v_mfma_f32_16x16x32_bf16 a[48:51], a[240:243], v[32:35], a[48:51]\n"
"s_add_u32 s50, s50, 32768\n"
"ds_read_b64_tr_b16 a[184:185], v218 offset:41240\n"
"v_cvt_pk_bf16_f32 v55, v190, v191\n"
"v_mfma_f32_16x16x32_bf16 a[48:51], a[244:247], v[40:43], a[48:51]\n"
"s_mul_i32 s62, s41, 5\n"
"ds_read_b64_tr_b16 a[186:187], v218 offset:42264\n"
"ds_read_b128 v[156:159], v224 offset:2240\n"
"v_mfma_f32_16x16x32_bf16 a[52:55], a[240:243], v[36:39], a[52:55]\n"
"ds_read_b64_tr_b16 a[188:189], v218 offset:45336\n"
"v_fma_mix_f32 v176, v56, v160, v168 op_sel:[0,0,0] op_sel_hi:[1,0,0]\n"
"v_mfma_f32_16x16x32_bf16 a[52:55], a[244:247], v[44:47], a[52:55]\n"
"s_min_u32 s62, s62, 13\n"
"ds_read_b64_tr_b16 a[190:191], v218 offset:46360\n"
"v_fma_mix_f32 v177, v56, v161, v169 op_sel:[1,0,0] op_sel_hi:[1,0,0]\n"
"v_mfma_f32_16x16x32_bf16 a[56:59], a[248:251], v[32:35], a[56:59]\n"
"s_add_u32 m0, s51, 16392\n"
"ds_read_b64_tr_b16 a[224:225], v218 offset:41488\n"
"v_fma_mix_f32 v178, v57, v162, v170 op_sel:[0,0,0] op_sel_hi:[1,0,0]\n"
"v_mfma_f32_16x16x32_bf16 a[56:59], a[252:255], v[40:43], a[56:59]\n"
"s_lshl_b32 s62, s62, 10\n"
"ds_read_b64_tr_b16 a[226:227], v218 offset:42512\n"
"buffer_load_dwordx4 v217, s[20:23], s50 offen offset:0 lds\n"
"v_mfma_f32_16x16x32_bf16 a[60:63], a[248:251], v[36:39], a[60:63]\n"
"ds_read_b64_tr_b16 a[228:229], v218 offset:45584\n"
"v_fma_mix_f32 v179, v57, v163, v171 op_sel:[1,0,0] op_sel_hi:[1,0,0]\n"
"v_mfma_f32_16x16x32_bf16 a[60:63], a[252:255], v[44:47], a[60:63]\n"
"s_waitcnt lgkmcnt(13)\n"
"s_add_u32 s52, s52, s62\n"
"ds_read_b64_tr_b16 a[230:231], v218 offset:46608\n"
"v_fma_mix_f32 v180, v58, v164, v172 op_sel:[0,0,0] op_sel_hi:[1,0,0]\n"
"v_mfma_f32_16x16x32_bf16 a[64:67], a[160:163], v[32:35], a[64:67]\n"
"ds_read_b64_tr_b16 a[232:233], v218 offset:41496\n"
"v_fma_mix_f32 v181, v58, v165, v173 op_sel:[1,0,0] op_sel_hi:[1,0,0]\n"
"v_mfma_f32_16x16x32_bf16 a[64:67], a[164:167], v[40:43], a[64:67]\n"
"s_add_u32 s52, s52, 26402816\n"
"ds_read_b64_tr_b16 a[234:235], v218 offset:42520\n"
"v_fma_mix_f32 v182, v59, v166, v174 op_sel:[0,0,0] op_sel_hi:[1,0,0]\n"
"v_mfma_f32_16x16x32_bf16 a[68:71], a[160:163], v[36:39], a[68:71]\n"
"s_add_u32 m0, s51, 24328\n"
"ds_read_b64_tr_b16 a[236:237], v218 offset:45592\n"
"v_fma_mix_f32 v183, v59, v167, v175 op_sel:[1,0,0] op_sel_hi:[1,0,0]\n"
"v_mfma_f32_16x16x32_bf16 a[68:71], a[164:167], v[44:47], a[68:71]\n"
"s_add_u32 s53, s52, 4096\n"
"ds_read_b64_tr_b16 a[238:239], v218 offset:46616\n"
"buffer_load_dwordx4 v217, s[20:23], s50 offen offset:256 lds\n"
"v_mfma_f32_16x16x32_bf16 a[72:75], a[168:171], v[32:35], a[72:75]\n"
"s_add_u32 s50, s50, 32768\n"
"ds_read_b64_tr_b16 a[240:241], v218 offset:41744\n"
"v_cvt_pk_bf16_f32 v56, v176, v177\n"
"v_mfma_f32_16x16x32_bf16 a[72:75], a[172:175], v[40:43], a[72:75]\n"
"ds_read_b64_tr_b16 a[242:243], v218 offset:42768\n"
"v_cvt_pk_bf16_f32 v57, v178, v179\n"
"v_mfma_f32_16x16x32_bf16 a[76:79], a[168:171], v[36:39], a[76:79]\n"
"ds_read_b64_tr_b16 a[244:245], v218 offset:45840\n"
"v_cvt_pk_bf16_f32 v58, v180, v181\n"
"v_mfma_f32_16x16x32_bf16 a[76:79], a[172:175], v[44:47], a[76:79]\n"
"s_waitcnt lgkmcnt(11)\n"
"ds_read_b64_tr_b16 a[246:247], v218 offset:46864\n"
"v_cvt_pk_bf16_f32 v59, v182, v183\n"
"v_mfma_f32_16x16x32_bf16 a[80:83], a[176:179], v[32:35], a[80:83]\n"
"s_add_u32 m0, s51, 17416\n"
"ds_read_b64_tr_b16 a[248:249], v218 offset:41752\n"
"v_fma_mix_f32 v184, v60, v160, v168 op_sel:[0,0,0] op_sel_hi:[1,0,0]\n"
"v_mfma_f32_16x16x32_bf16 a[80:83], a[180:183], v[40:43], a[80:83]\n"
"ds_read_b64_tr_b16 a[250:251], v218 offset:42776\n"
"buffer_load_dwordx4 v217, s[20:23], s50 offen offset:0 lds\n"
"v_mfma_f32_16x16x32_bf16 a[84:87], a[176:179], v[36:39], a[84:87]\n"
"ds_read_b64_tr_b16 a[252:253], v218 offset:45848\n"
"v_fma_mix_f32 v185, v60, v161, v169 op_sel:[1,0,0] op_sel_hi:[1,0,0]\n"
"v_mfma_f32_16x16x32_bf16 a[84:87], a[180:183], v[44:47], a[84:87]\n"
"ds_read_b64_tr_b16 a[254:255], v218 offset:46872\n"
"v_fma_mix_f32 v186, v61, v162, v170 op_sel:[0,0,0] op_sel_hi:[1,0,0]\n"
"v_mfma_f32_16x16x32_bf16 v[236:239], a[184:187], v[32:35], v[236:239]\n"
"ds_read_b64_tr_b16 a[160:161], v219 offset:0\n"
"v_fma_mix_f32 v187, v61, v163, v171 op_sel:[1,0,0] op_sel_hi:[1,0,0]\n"
"v_fma_mix_f32 v188, v62, v164, v172 op_sel:[0,0,0] op_sel_hi:[1,0,0]\n"
"v_mfma_f32_16x16x32_bf16 v[236:239], a[188:191], v[40:43], v[236:239]\n"
"ds_read_b64_tr_b16 a[162:163], v219 offset:1024\n"
"v_fma_mix_f32 v189, v62, v165, v173 op_sel:[1,0,0] op_sel_hi:[1,0,0]\n"
"v_mfma_f32_16x16x32_bf16 v[232:235], a[184:187], v[36:39], v[232:235]\n"
"s_add_u32 m0, s51, 25352\n"
"ds_read_b64_tr_b16 a[164:165], v219 offset:4096\n"
"v_fma_mix_f32 v190, v63, v166, v174 op_sel:[0,0,0] op_sel_hi:[1,0,0]\n"
"v_mfma_f32_16x16x32_bf16 v[232:235], a[188:191], v[44:47], v[232:235]\n"
"s_waitcnt lgkmcnt(11)\n"
"buffer_load_dwordx4 v217, s[20:23], s50 offen offset:256 lds\n"
"ds_read_b64_tr_b16 a[166:167], v219 offset:5120\n"
"v_mfma_f32_16x16x32_bf16 v[228:231], a[224:227], v[32:35], v[228:231]\n"
"s_add_u32 s50, s50, 425984\n"
"ds_read_b64_tr_b16 a[168:169], v219 offset:8\n"
"v_fma_mix_f32 v191, v63, v167, v175 op_sel:[1,0,0] op_sel_hi:[1,0,0]\n"
"v_mfma_f32_16x16x32_bf16 v[228:231], a[228:231], v[40:43], v[228:231]\n"
"ds_read_b64_tr_b16 a[170:171], v219 offset:1032\n"
"ds_read_b128 v[160:163], v224 offset:2304\n"
"v_mfma_f32_16x16x32_bf16 v[220:223], a[224:227], v[36:39], v[220:223]\n"
"ds_read_b64_tr_b16 a[172:173], v219 offset:4104\n"
"v_cvt_pk_bf16_f32 v60, v184, v185\n"
"v_mfma_f32_16x16x32_bf16 v[220:223], a[228:231], v[44:47], v[220:223]\n"
"ds_read_b64_tr_b16 a[174:175], v219 offset:5128\n"
"v_cvt_pk_bf16_f32 v61, v186, v187\n"
"v_mfma_f32_16x16x32_bf16 v[212:215], a[232:235], v[32:35], v[212:215]\n"
"ds_read_b64_tr_b16 a[176:177], v219 offset:256\n"
"ds_read_b128 v[164:167], v224 offset:2368\n"
"v_mfma_f32_16x16x32_bf16 v[212:215], a[236:239], v[40:43], v[212:215]\n"
"ds_read_b64_tr_b16 a[178:179], v219 offset:1280\n"
"v_cvt_pk_bf16_f32 v62, v188, v189\n"
"v_mfma_f32_16x16x32_bf16 v[208:211], a[232:235], v[36:39], v[208:211]\n"
"ds_read_b64_tr_b16 a[180:181], v219 offset:4352\n"
"v_cvt_pk_bf16_f32 v63, v190, v191\n"
"v_mfma_f32_16x16x32_bf16 v[208:211], a[236:239], v[44:47], v[208:211]\n"
"s_waitcnt lgkmcnt(13)\n"
"ds_read_b64_tr_b16 a[182:183], v219 offset:5376\n"
"ds_read_b128 v[168:171], v224 offset:2432\n"
"v_mfma_f32_16x16x32_bf16 v[204:207], a[240:243], v[32:35], v[204:207]\n"
"ds_read_b64_tr_b16 a[184:185], v219 offset:264\n"
"v_fma_mix_f32 v176, v64, v144, v152 op_sel:[0,0,0] op_sel_hi:[1,0,0]\n"
"v_mfma_f32_16x16x32_bf16 v[204:207], a[244:247], v[40:43], v[204:207]\n"
"ds_read_b64_tr_b16 a[186:187], v219 offset:1288\n"
"v_fma_mix_f32 v177, v64, v145, v153 op_sel:[1,0,0] op_sel_hi:[1,0,0]\n"
"v_mfma_f32_16x16x32_bf16 v[200:203], a[240:243], v[36:39], v[200:203]\n"
"ds_read_b64_tr_b16 a[188:189], v219 offset:4360\n"
"ds_read_b128 v[172:175], v224 offset:2496\n"
"v_mfma_f32_16x16x32_bf16 v[200:203], a[244:247], v[44:47], v[200:203]\n"
"ds_read_b64_tr_b16 a[190:191], v219 offset:5384\n"
"v_fma_mix_f32 v178, v65, v146, v154 op_sel:[0,0,0] op_sel_hi:[1,0,0]\n"
"v_mfma_f32_16x16x32_bf16 v[196:199], a[248:251], v[32:35], v[196:199]\n"
"ds_read_b64_tr_b16 a[224:225], v219 offset:512\n"
"v_fma_mix_f32 v179, v65, v147, v155 op_sel:[1,0,0] op_sel_hi:[1,0,0]\n"
"v_mfma_f32_16x16x32_bf16 v[196:199], a[252:255], v[40:43], v[196:199]\n"
"ds_read_b64_tr_b16 a[226:227], v219 offset:1536\n"
"v_fma_mix_f32 v180, v66, v148, v156 op_sel:[0,0,0] op_sel_hi:[1,0,0]\n"
"v_mfma_f32_16x16x32_bf16 v[192:195], a[248:251], v[36:39], v[192:195]\n"
"ds_read_b64_tr_b16 a[228:229], v219 offset:4608\n"
"v_fma_mix_f32 v181, v66, v149, v157 op_sel:[1,0,0] op_sel_hi:[1,0,0]\n"
"v_mfma_f32_16x16x32_bf16 v[192:195], a[252:255], v[44:47], v[192:195]\n"
"s_waitcnt lgkmcnt(14)\n"
"ds_read_b64_tr_b16 a[230:231], v219 offset:5632\n"
"v_fma_mix_f32 v182, v67, v150, v158 op_sel:[0,0,0] op_sel_hi:[1,0,0]\n"
"v_mfma_f32_16x16x32_bf16 a[0:3], a[160:163], v[48:51], a[0:3]\n"
"ds_read_b64_tr_b16 a[232:233], v219 offset:520\n"
"v_fma_mix_f32 v183, v67, v151, v159 op_sel:[1,0,0] op_sel_hi:[1,0,0]\n"
"v_mfma_f32_16x16x32_bf16 a[0:3], a[164:167], v[56:59], a[0:3]\n"
"s_waitcnt vmcnt(8)\n"
"ds_read_b64_tr_b16 a[234:235], v219 offset:1544\n"
"v_cvt_pk_bf16_f32 v64, v176, v177\n"
"v_mfma_f32_16x16x32_bf16 a[4:7], a[160:163], v[52:55], a[4:7]\n"
"s_barrier\n"
"ds_read_b64_tr_b16 a[236:237], v219 offset:4616\n"
"v_cvt_pk_bf16_f32 v65, v178, v179\n"
"v_mfma_f32_16x16x32_bf16 a[4:7], a[164:167], v[60:63], a[4:7]\n"
"ds_read_b64_tr_b16 a[238:239], v219 offset:5640\n"
"v_cvt_pk_bf16_f32 v66, v180, v181\n"
"v_mfma_f32_16x16x32_bf16 a[8:11], a[168:171], v[48:51], a[8:11]\n"
"s_add_u32 m0, s51, 32784\n"
"ds_read_b64_tr_b16 a[240:241], v219 offset:768\n"
"v_cvt_pk_bf16_f32 v67, v182, v183\n"
"v_mfma_f32_16x16x32_bf16 a[8:11], a[172:175], v[56:59], a[8:11]\n"
"ds_read_b64_tr_b16 a[242:243], v219 offset:1792\n"
"buffer_load_dwordx4 v217, s[20:23], s50 offen offset:0 lds\n"
"v_mfma_f32_16x16x32_bf16 a[12:15], a[168:171], v[52:55], a[12:15]\n"
"ds_read_b64_tr_b16 a[244:245], v219 offset:4864\n"
"v_fma_mix_f32 v184, v68, v144, v152 op_sel:[0,0,0] op_sel_hi:[1,0,0]\n"
"v_mfma_f32_16x16x32_bf16 a[12:15], a[172:175], v[60:63], a[12:15]\n"
"s_waitcnt lgkmcnt(11)\n"
"ds_read_b64_tr_b16 a[246:247], v219 offset:5888\n"
"v_fma_mix_f32 v185, v68, v145, v153 op_sel:[1,0,0] op_sel_hi:[1,0,0]\n"
"v_mfma_f32_16x16x32_bf16 a[16:19], a[176:179], v[48:51], a[16:19]\n"
"ds_read_b64_tr_b16 a[248:249], v219 offset:776\n"
"v_fma_mix_f32 v186, v69, v146, v154 op_sel:[0,0,0] op_sel_hi:[1,0,0]\n"
"v_mfma_f32_16x16x32_bf16 a[16:19], a[180:183], v[56:59], a[16:19]\n"
"ds_read_b64_tr_b16 a[250:251], v219 offset:1800\n"
"v_fma_mix_f32 v187, v69, v147, v155 op_sel:[1,0,0] op_sel_hi:[1,0,0]\n"
"v_mfma_f32_16x16x32_bf16 a[20:23], a[176:179], v[52:55], a[20:23]\n"
"s_add_u32 m0, s51, 40720\n"
"ds_read_b64_tr_b16 a[252:253], v219 offset:4872\n"
"v_fma_mix_f32 v188, v70, v148, v156 op_sel:[0,0,0] op_sel_hi:[1,0,0]\n"
"v_mfma_f32_16x16x32_bf16 a[20:23], a[180:183], v[60:63], a[20:23]\n"
"ds_read_b64_tr_b16 a[254:255], v219 offset:5896\n"
"buffer_load_dwordx4 v217, s[20:23], s50 offen offset:256 lds\n"
"v_mfma_f32_16x16x32_bf16 a[24:27], a[184:187], v[48:51], a[24:27]\n"
"s_add_u32 s50, s50, 32768\n"
"ds_read_b64_tr_b16 a[160:161], v219 offset:8192\n"
"v_fma_mix_f32 v189, v70, v149, v157 op_sel:[1,0,0] op_sel_hi:[1,0,0]\n"
"v_mfma_f32_16x16x32_bf16 a[24:27], a[188:191], v[56:59], a[24:27]\n"
"ds_read_b64_tr_b16 a[162:163], v219 offset:9216\n"
"v_fma_mix_f32 v190, v71, v150, v158 op_sel:[0,0,0] op_sel_hi:[1,0,0]\n"
"v_mfma_f32_16x16x32_bf16 a[28:31], a[184:187], v[52:55], a[28:31]\n"
"ds_read_b64_tr_b16 a[164:165], v219 offset:12288\n"
"v_fma_mix_f32 v191, v71, v151, v159 op_sel:[1,0,0] op_sel_hi:[1,0,0]\n"
"v_mfma_f32_16x16x32_bf16 a[28:31], a[188:191], v[60:63], a[28:31]\n"
"s_waitcnt lgkmcnt(11)\n"
"ds_read_b64_tr_b16 a[166:167], v219 offset:13312\n"
"ds_read_b128 v[144:147], v224 offset:2560\n"
"v_mfma_f32_16x16x32_bf16 a[32:35], a[224:227], v[48:51], a[32:35]\n"
"s_add_u32 m0, s51, 33808\n"
"ds_read_b64_tr_b16 a[168:169], v219 offset:8200\n"
"v_cvt_pk_bf16_f32 v68, v184, v185\n"
"v_mfma_f32_16x16x32_bf16 a[32:35], a[228:231], v[56:59], a[32:35]\n"
"ds_read_b64_tr_b16 a[170:171], v219 offset:9224\n"
"buffer_load_dwordx4 v217, s[20:23], s50 offen offset:0 lds\n"
"v_mfma_f32_16x16x32_bf16 a[36:39], a[224:227], v[52:55], a[36:39]\n"
"ds_read_b64_tr_b16 a[172:173], v219 offset:12296\n"
"ds_read_b128 v[148:151], v224 offset:2624\n"
"v_mfma_f32_16x16x32_bf16 a[36:39], a[228:231], v[60:63], a[36:39]\n"
"ds_read_b64_tr_b16 a[174:175], v219 offset:13320\n"
"v_cvt_pk_bf16_f32 v69, v186, v187\n"
"v_mfma_f32_16x16x32_bf16 a[40:43], a[232:235], v[48:51], a[40:43]\n"
"ds_read_b64_tr_b16 a[176:177], v219 offset:8448\n"
"v_cvt_pk_bf16_f32 v70, v188, v189\n"
"v_mfma_f32_16x16x32_bf16 a[40:43], a[236:239], v[56:59], a[40:43]\n"
"ds_read_b64_tr_b16 a[178:179], v219 offset:9472\n"
"ds_read_b128 v[152:155], v224 offset:2688\n"
"v_mfma_f32_16x16x32_bf16 a[44:47], a[232:235], v[52:55], a[44:47]\n"
"s_add_u32 m0, s51, 41744\n"
"ds_read_b64_tr_b16 a[180:181], v219 offset:12544\n"
"v_cvt_pk_bf16_f32 v71, v190, v191\n"
"v_mfma_f32_16x16x32_bf16 a[44:47], a[236:239], v[60:63], a[44:47]\n"
"s_waitcnt lgkmcnt(14)\n"
"ds_read_b64_tr_b16 a[182:183], v219 offset:13568\n"
"buffer_load_dwordx4 v217, s[20:23], s50 offen offset:256 lds\n"
"v_mfma_f32_16x16x32_bf16 a[48:51], a[240:243], v[48:51], a[48:51]\n"
"s_add_u32 s50, s50, 32768\n"
"ds_read_b64_tr_b16 a[184:185], v219 offset:8456\n"
"ds_read_b128 v[156:159], v224 offset:2752\n"
"v_mfma_f32_16x16x32_bf16 a[48:51], a[244:247], v[56:59], a[48:51]\n"
"ds_read_b64_tr_b16 a[186:187], v219 offset:9480\n"
"v_fma_mix_f32 v176, v72, v160, v168 op_sel:[0,0,0] op_sel_hi:[1,0,0]\n"
"v_mfma_f32_16x16x32_bf16 a[52:55], a[240:243], v[52:55], a[52:55]\n"
"ds_read_b64_tr_b16 a[188:189], v219 offset:12552\n"
"v_fma_mix_f32 v177, v72, v161, v169 op_sel:[1,0,0] op_sel_hi:[1,0,0]\n"
"v_mfma_f32_16x16x32_bf16 a[52:55], a[244:247], v[60:63], a[52:55]\n"
"ds_read_b64_tr_b16 a[190:191], v219 offset:13576\n"
"v_fma_mix_f32 v178, v73, v162, v170 op_sel:[0,0,0] op_sel_hi:[1,0,0]\n"
"v_mfma_f32_16x16x32_bf16 a[56:59], a[248:251], v[48:51], a[56:59]\n"
"s_add_u32 m0, s51, 49176\n"
"ds_read_b64_tr_b16 a[224:225], v219 offset:8704\n"
"v_fma_mix_f32 v179, v73, v163, v171 op_sel:[1,0,0] op_sel_hi:[1,0,0]\n"
"v_mfma_f32_16x16x32_bf16 a[56:59], a[252:255], v[56:59], a[56:59]\n"
"ds_read_b64_tr_b16 a[226:227], v219 offset:9728\n"
"buffer_load_dwordx4 v217, s[20:23], s50 offen offset:0 lds\n"
"v_mfma_f32_16x16x32_bf16 a[60:63], a[248:251], v[52:55], a[60:63]\n"
"ds_read_b64_tr_b16 a[228:229], v219 offset:12800\n"
"v_fma_mix_f32 v180, v74, v164, v172 op_sel:[0,0,0] op_sel_hi:[1,0,0]\n"
"v_mfma_f32_16x16x32_bf16 a[60:63], a[252:255], v[60:63], a[60:63]\n"
"s_waitcnt lgkmcnt(13)\n"
"ds_read_b64_tr_b16 a[230:231], v219 offset:13824\n"
"v_fma_mix_f32 v181, v74, v165, v173 op_sel:[1,0,0] op_sel_hi:[1,0,0]\n"
"v_mfma_f32_16x16x32_bf16 a[64:67], a[160:163], v[48:51], a[64:67]\n"
"ds_read_b64_tr_b16 a[232:233], v219 offset:8712\n"
"v_fma_mix_f32 v182, v75, v166, v174 op_sel:[0,0,0] op_sel_hi:[1,0,0]\n"
"v_mfma_f32_16x16x32_bf16 a[64:67], a[164:167], v[56:59], a[64:67]\n"
"ds_read_b64_tr_b16 a[234:235], v219 offset:9736\n"
"v_fma_mix_f32 v183, v75, v167, v175 op_sel:[1,0,0] op_sel_hi:[1,0,0]\n"
"v_mfma_f32_16x16x32_bf16 a[68:71], a[160:163], v[52:55], a[68:71]\n"
"s_add_u32 m0, s51, 57112\n"
"ds_read_b64_tr_b16 a[236:237], v219 offset:12808\n"
"v_cvt_pk_bf16_f32 v72, v176, v177\n"
"v_mfma_f32_16x16x32_bf16 a[68:71], a[164:167], v[60:63], a[68:71]\n"
"ds_read_b64_tr_b16 a[238:239], v219 offset:13832\n"
"buffer_load_dwordx4 v217, s[20:23], s50 offen offset:256 lds\n"
"v_mfma_f32_16x16x32_bf16 a[72:75], a[168:171], v[48:51], a[72:75]\n"
"s_add_u32 s50, s50, 32768\n"
"ds_read_b64_tr_b16 a[240:241], v219 offset:8960\n"
"v_cvt_pk_bf16_f32 v73, v178, v179\n"
"v_mfma_f32_16x16x32_bf16 a[72:75], a[172:175], v[56:59], a[72:75]\n"
"ds_read_b64_tr_b16 a[242:243], v219 offset:9984\n"
"v_cvt_pk_bf16_f32 v74, v180, v181\n"
"v_mfma_f32_16x16x32_bf16 a[76:79], a[168:171], v[52:55], a[76:79]\n"
"ds_read_b64_tr_b16 a[244:245], v219 offset:13056\n"
"v_cvt_pk_bf16_f32 v75, v182, v183\n"
"v_mfma_f32_16x16x32_bf16 a[76:79], a[172:175], v[60:63], a[76:79]\n"
"s_waitcnt lgkmcnt(11)\n"
"ds_read_b64_tr_b16 a[246:247], v219 offset:14080\n"
"v_fma_mix_f32 v184, v76, v160, v168 op_sel:[0,0,0] op_sel_hi:[1,0,0]\n"
"v_mfma_f32_16x16x32_bf16 a[80:83], a[176:179], v[48:51], a[80:83]\n"
"s_add_u32 m0, s51, 50200\n"
"ds_read_b64_tr_b16 a[248:249], v219 offset:8968\n"
"v_fma_mix_f32 v185, v76, v161, v169 op_sel:[1,0,0] op_sel_hi:[1,0,0]\n"
"v_mfma_f32_16x16x32_bf16 a[80:83], a[180:183], v[56:59], a[80:83]\n"
"ds_read_b64_tr_b16 a[250:251], v219 offset:9992\n"
"buffer_load_dwordx4 v217, s[20:23], s50 offen offset:0 lds\n"
"v_mfma_f32_16x16x32_bf16 a[84:87], a[176:179], v[52:55], a[84:87]\n"
"ds_read_b64_tr_b16 a[252:253], v219 offset:13064\n"
"v_fma_mix_f32 v186, v77, v162, v170 op_sel:[0,0,0] op_sel_hi:[1,0,0]\n"
"v_mfma_f32_16x16x32_bf16 a[84:87], a[180:183], v[60:63], a[84:87]\n"
"ds_read_b64_tr_b16 a[254:255], v219 offset:14088\n"
"v_fma_mix_f32 v187, v77, v163, v171 op_sel:[1,0,0] op_sel_hi:[1,0,0]\n"
"v_mfma_f32_16x16x32_bf16 v[236:239], a[184:187], v[48:51], v[236:239]\n"
"ds_read_b64_tr_b16 a[160:161], v219 offset:32784\n"
"v_fma_mix_f32 v188, v78, v164, v172 op_sel:[0,0,0] op_sel_hi:[1,0,0]\n"
"v_fma_mix_f32 v189, v78, v165, v173 op_sel:[1,0,0] op_sel_hi:[1,0,0]\n"
"v_mfma_f32_16x16x32_bf16 v[236:239], a[188:191], v[56:59], v[236:239]\n"
"ds_read_b64_tr_b16 a[162:163], v219 offset:33808\n"
"v_fma_mix_f32 v190, v79, v166, v174 op_sel:[0,0,0] op_sel_hi:[1,0,0]\n"
"v_mfma_f32_16x16x32_bf16 v[232:235], a[184:187], v[52:55], v[232:235]\n"
"s_add_u32 m0, s51, 58136\n"
"ds_read_b64_tr_b16 a[164:165], v219 offset:36880\n"
"v_fma_mix_f32 v191, v79, v167, v175 op_sel:[1,0,0] op_sel_hi:[1,0,0]\n"
"v_mfma_f32_16x16x32_bf16 v[232:235], a[188:191], v[60:63], v[232:235]\n"
"s_waitcnt lgkmcnt(11)\n"
"buffer_load_dwordx4 v217, s[20:23], s50 offen offset:256 lds\n"
"ds_read_b64_tr_b16 a[166:167], v219 offset:37904\n"
"v_mfma_f32_16x16x32_bf16 v[228:231], a[224:227], v[48:51], v[228:231]\n"
"s_add_u32 s50, s50, 425984\n"
"ds_read_b64_tr_b16 a[168:169], v219 offset:32792\n"
"ds_read_b128 v[160:163], v224 offset:2816\n"
"v_mfma_f32_16x16x32_bf16 v[228:231], a[228:231], v[56:59], v[228:231]\n"
"ds_read_b64_tr_b16 a[170:171], v219 offset:33816\n"
"v_cvt_pk_bf16_f32 v76, v184, v185\n"
"v_mfma_f32_16x16x32_bf16 v[220:223], a[224:227], v[52:55], v[220:223]\n"
"ds_read_b64_tr_b16 a[172:173], v219 offset:36888\n"
"v_cvt_pk_bf16_f32 v77, v186, v187\n"
"v_mfma_f32_16x16x32_bf16 v[220:223], a[228:231], v[60:63], v[220:223]\n"
"ds_read_b64_tr_b16 a[174:175], v219 offset:37912\n"
"ds_read_b128 v[164:167], v224 offset:2880\n"
"v_mfma_f32_16x16x32_bf16 v[212:215], a[232:235], v[48:51], v[212:215]\n"
"ds_read_b64_tr_b16 a[176:177], v219 offset:33040\n"
"v_cvt_pk_bf16_f32 v78, v188, v189\n"
"v_mfma_f32_16x16x32_bf16 v[212:215], a[236:239], v[56:59], v[212:215]\n"
"ds_read_b64_tr_b16 a[178:179], v219 offset:34064\n"
"v_cvt_pk_bf16_f32 v79, v190, v191\n"
"v_mfma_f32_16x16x32_bf16 v[208:211], a[232:235], v[52:55], v[208:211]\n"
"ds_read_b64_tr_b16 a[180:181], v219 offset:37136\n"
"ds_read_b128 v[168:171], v224 offset:2944\n"
"v_mfma_f32_16x16x32_bf16 v[208:211], a[236:239], v[60:63], v[208:211]\n"
"s_waitcnt lgkmcnt(14)\n"
"ds_read_b64_tr_b16 a[182:183], v219 offset:38160\n"
"v_fma_mix_f32 v176, v80, v144, v152 op_sel:[0,0,0] op_sel_hi:[1,0,0]\n"
"v_mfma_f32_16x16x32_bf16 v[204:207], a[240:243], v[48:51], v[204:207]\n"
"ds_read_b64_tr_b16 a[184:185], v219 offset:33048\n"
"v_fma_mix_f32 v177, v80, v145, v153 op_sel:[1,0,0] op_sel_hi:[1,0,0]\n"
"v_mfma_f32_16x16x32_bf16 v[204:207], a[244:247], v[56:59], v[204:207]\n"
"ds_read_b64_tr_b16 a[186:187], v219 offset:34072\n"
"ds_read_b128 v[172:175], v224 offset:3008\n"
"v_mfma_f32_16x16x32_bf16 v[200:203], a[240:243], v[52:55], v[200:203]\n"
"ds_read_b64_tr_b16 a[188:189], v219 offset:37144\n"
"v_fma_mix_f32 v178, v81, v146, v154 op_sel:[0,0,0] op_sel_hi:[1,0,0]\n"
"v_mfma_f32_16x16x32_bf16 v[200:203], a[244:247], v[60:63], v[200:203]\n"
"ds_read_b64_tr_b16 a[190:191], v219 offset:38168\n"
"v_fma_mix_f32 v179, v81, v147, v155 op_sel:[1,0,0] op_sel_hi:[1,0,0]\n"
"v_mfma_f32_16x16x32_bf16 v[196:199], a[248:251], v[48:51], v[196:199]\n"
"ds_read_b64_tr_b16 a[224:225], v219 offset:33296\n"
"v_fma_mix_f32 v180, v82, v148, v156 op_sel:[0,0,0] op_sel_hi:[1,0,0]\n"
"v_mfma_f32_16x16x32_bf16 v[196:199], a[252:255], v[56:59], v[196:199]\n"
"ds_read_b64_tr_b16 a[226:227], v219 offset:34320\n"
"v_fma_mix_f32 v181, v82, v149, v157 op_sel:[1,0,0] op_sel_hi:[1,0,0]\n"
"v_mfma_f32_16x16x32_bf16 v[192:195], a[248:251], v[52:55], v[192:195]\n"
"ds_read_b64_tr_b16 a[228:229], v219 offset:37392\n"
"v_fma_mix_f32 v182, v83, v150, v158 op_sel:[0,0,0] op_sel_hi:[1,0,0]\n"
"v_mfma_f32_16x16x32_bf16 v[192:195], a[252:255], v[60:63], v[192:195]\n"
"s_waitcnt lgkmcnt(13)\n"
"ds_read_b64_tr_b16 a[230:231], v219 offset:38416\n"
"v_fma_mix_f32 v183, v83, v151, v159 op_sel:[1,0,0] op_sel_hi:[1,0,0]\n"
"v_mfma_f32_16x16x32_bf16 a[0:3], a[160:163], v[64:67], a[0:3]\n"
"ds_read_b64_tr_b16 a[232:233], v219 offset:33304\n"
"v_cvt_pk_bf16_f32 v80, v176, v177\n"
"v_mfma_f32_16x16x32_bf16 a[0:3], a[164:167], v[72:75], a[0:3]\n"
"s_waitcnt vmcnt(8)\n"
"ds_read_b64_tr_b16 a[234:235], v219 offset:34328\n"
"v_cvt_pk_bf16_f32 v81, v178, v179\n"
"v_mfma_f32_16x16x32_bf16 a[4:7], a[160:163], v[68:71], a[4:7]\n"
"s_barrier\n"
"ds_read_b64_tr_b16 a[236:237], v219 offset:37400\n"
"v_cvt_pk_bf16_f32 v82, v180, v181\n"
"v_mfma_f32_16x16x32_bf16 a[4:7], a[164:167], v[76:79], a[4:7]\n"
"ds_read_b64_tr_b16 a[238:239], v219 offset:38424\n"
"v_cvt_pk_bf16_f32 v83, v182, v183\n"
"v_mfma_f32_16x16x32_bf16 a[8:11], a[168:171], v[64:67], a[8:11]\n"
"s_add_u32 m0, s51, 65568\n"
"ds_read_b64_tr_b16 a[240:241], v219 offset:33552\n"
"v_fma_mix_f32 v184, v84, v144, v152 op_sel:[0,0,0] op_sel_hi:[1,0,0]\n"
"v_mfma_f32_16x16x32_bf16 a[8:11], a[172:175], v[72:75], a[8:11]\n"
"ds_read_b64_tr_b16 a[242:243], v219 offset:34576\n"
"buffer_load_dwordx4 v217, s[20:23], s50 offen offset:0 lds\n"
"v_mfma_f32_16x16x32_bf16 a[12:15], a[168:171], v[68:71], a[12:15]\n"
"ds_read_b64_tr_b16 a[244:245], v219 offset:37648\n"
"v_fma_mix_f32 v185, v84, v145, v153 op_sel:[1,0,0] op_sel_hi:[1,0,0]\n"
"v_mfma_f32_16x16x32_bf16 a[12:15], a[172:175], v[76:79], a[12:15]\n"
"s_waitcnt lgkmcnt(11)\n"
"ds_read_b64_tr_b16 a[246:247], v219 offset:38672\n"
"v_fma_mix_f32 v186, v85, v146, v154 op_sel:[0,0,0] op_sel_hi:[1,0,0]\n"
"v_mfma_f32_16x16x32_bf16 a[16:19], a[176:179], v[64:67], a[16:19]\n"
"ds_read_b64_tr_b16 a[248:249], v219 offset:33560\n"
"v_fma_mix_f32 v187, v85, v147, v155 op_sel:[1,0,0] op_sel_hi:[1,0,0]\n"
"v_mfma_f32_16x16x32_bf16 a[16:19], a[180:183], v[72:75], a[16:19]\n"
"ds_read_b64_tr_b16 a[250:251], v219 offset:34584\n"
"v_fma_mix_f32 v188, v86, v148, v156 op_sel:[0,0,0] op_sel_hi:[1,0,0]\n"
"v_mfma_f32_16x16x32_bf16 a[20:23], a[176:179], v[68:71], a[20:23]\n"
"s_add_u32 m0, s51, 73504\n"
"ds_read_b64_tr_b16 a[252:253], v219 offset:37656\n"
"v_fma_mix_f32 v189, v86, v149, v157 op_sel:[1,0,0] op_sel_hi:[1,0,0]\n"
"v_mfma_f32_16x16x32_bf16 a[20:23], a[180:183], v[76:79], a[20:23]\n"
"ds_read_b64_tr_b16 a[254:255], v219 offset:38680\n"
"buffer_load_dwordx4 v217, s[20:23], s50 offen offset:256 lds\n"
"v_mfma_f32_16x16x32_bf16 a[24:27], a[184:187], v[64:67], a[24:27]\n"
"s_add_u32 s50, s50, 32768\n"
"ds_read_b64_tr_b16 a[160:161], v219 offset:40976\n"
"v_fma_mix_f32 v190, v87, v150, v158 op_sel:[0,0,0] op_sel_hi:[1,0,0]\n"
"v_mfma_f32_16x16x32_bf16 a[24:27], a[188:191], v[72:75], a[24:27]\n"
"ds_read_b64_tr_b16 a[162:163], v219 offset:42000\n"
"v_fma_mix_f32 v191, v87, v151, v159 op_sel:[1,0,0] op_sel_hi:[1,0,0]\n"
"v_mfma_f32_16x16x32_bf16 a[28:31], a[184:187], v[68:71], a[28:31]\n"
"ds_read_b64_tr_b16 a[164:165], v219 offset:45072\n"
"ds_read_b128 v[144:147], v224 offset:3072\n"
"v_mfma_f32_16x16x32_bf16 a[28:31], a[188:191], v[76:79], a[28:31]\n"
"s_waitcnt lgkmcnt(12)\n"
"ds_read_b64_tr_b16 a[166:167], v219 offset:46096\n"
"v_cvt_pk_bf16_f32 v84, v184, v185\n"
"v_mfma_f32_16x16x32_bf16 a[32:35], a[224:227], v[64:67], a[32:35]\n"
"s_add_u32 m0, s51, 66592\n"
"ds_read_b64_tr_b16 a[168:169], v219 offset:40984\n"
"v_cvt_pk_bf16_f32 v85, v186, v187\n"
"v_mfma_f32_16x16x32_bf16 a[32:35], a[228:231], v[72:75], a[32:35]\n"
"ds_read_b64_tr_b16 a[170:171], v219 offset:42008\n"
"buffer_load_dwordx4 v217, s[20:23], s50 offen offset:0 lds\n"
"v_mfma_f32_16x16x32_bf16 a[36:39], a[224:227], v[68:71], a[36:39]\n"
"ds_read_b64_tr_b16 a[172:173], v219 offset:45080\n"
"ds_read_b128 v[148:151], v224 offset:3136\n"
"v_mfma_f32_16x16x32_bf16 a[36:39], a[228:231], v[76:79], a[36:39]\n"
"ds_read_b64_tr_b16 a[174:175], v219 offset:46104\n"
"v_cvt_pk_bf16_f32 v86, v188, v189\n"
"v_mfma_f32_16x16x32_bf16 a[40:43], a[232:235], v[64:67], a[40:43]\n"
"ds_read_b64_tr_b16 a[176:177], v219 offset:41232\n"
"v_cvt_pk_bf16_f32 v87, v190, v191\n"
"v_mfma_f32_16x16x32_bf16 a[40:43], a[236:239], v[72:75], a[40:43]\n"
"ds_read_b64_tr_b16 a[178:179], v219 offset:42256\n"
"ds_read_b128 v[152:155], v224 offset:3200\n"
"v_mfma_f32_16x16x32_bf16 a[44:47], a[232:235], v[68:71], a[44:47]\n"
"s_add_u32 m0, s51, 74528\n"
"ds_read_b64_tr_b16 a[180:181], v219 offset:45328\n"
"v_fma_mix_f32 v176, v88, v160, v168 op_sel:[0,0,0] op_sel_hi:[1,0,0]\n"
"v_mfma_f32_16x16x32_bf16 a[44:47], a[236:239], v[76:79], a[44:47]\n"
"s_waitcnt lgkmcnt(14)\n"
"ds_read_b64_tr_b16 a[182:183], v219 offset:46352\n"
"buffer_load_dwordx4 v217, s[20:23], s50 offen offset:256 lds\n"
"v_mfma_f32_16x16x32_bf16 a[48:51], a[240:243], v[64:67], a[48:51]\n"
"s_add_u32 s50, s50, 32768\n"
"ds_read_b64_tr_b16 a[184:185], v219 offset:41240\n"
"ds_read_b128 v[156:159], v224 offset:3264\n"
"v_mfma_f32_16x16x32_bf16 a[48:51], a[244:247], v[72:75], a[48:51]\n"
"ds_read_b64_tr_b16 a[186:187], v219 offset:42264\n"
"v_fma_mix_f32 v177, v88, v161, v169 op_sel:[1,0,0] op_sel_hi:[1,0,0]\n"
"v_mfma_f32_16x16x32_bf16 a[52:55], a[240:243], v[68:71], a[52:55]\n"
"ds_read_b64_tr_b16 a[188:189], v219 offset:45336\n"
"v_fma_mix_f32 v178, v89, v162, v170 op_sel:[0,0,0] op_sel_hi:[1,0,0]\n"
"v_mfma_f32_16x16x32_bf16 a[52:55], a[244:247], v[76:79], a[52:55]\n"
"ds_read_b64_tr_b16 a[190:191], v219 offset:46360\n"
"v_fma_mix_f32 v179, v89, v163, v171 op_sel:[1,0,0] op_sel_hi:[1,0,0]\n"
"v_mfma_f32_16x16x32_bf16 a[56:59], a[248:251], v[64:67], a[56:59]\n"
"s_add_u32 m0, s51, 81960\n"
"ds_read_b64_tr_b16 a[224:225], v219 offset:41488\n"
"v_fma_mix_f32 v180, v90, v164, v172 op_sel:[0,0,0] op_sel_hi:[1,0,0]\n"
"v_mfma_f32_16x16x32_bf16 a[56:59], a[252:255], v[72:75], a[56:59]\n"
"ds_read_b64_tr_b16 a[226:227], v219 offset:42512\n"
"buffer_load_dwordx4 v217, s[20:23], s50 offen offset:0 lds\n"
"v_mfma_f32_16x16x32_bf16 a[60:63], a[248:251], v[68:71], a[60:63]\n"
"ds_read_b64_tr_b16 a[228:229], v219 offset:45584\n"
"v_fma_mix_f32 v181, v90, v165, v173 op_sel:[1,0,0] op_sel_hi:[1,0,0]\n"
"v_mfma_f32_16x16x32_bf16 a[60:63], a[252:255], v[76:79], a[60:63]\n"
"s_waitcnt lgkmcnt(13)\n"
"ds_read_b64_tr_b16 a[230:231], v219 offset:46608\n"
"v_fma_mix_f32 v182, v91, v166, v174 op_sel:[0,0,0] op_sel_hi:[1,0,0]\n"
"v_mfma_f32_16x16x32_bf16 a[64:67], a[160:163], v[64:67], a[64:67]\n"
"ds_read_b64_tr_b16 a[232:233], v219 offset:41496\n"
"v_fma_mix_f32 v183, v91, v167, v175 op_sel:[1,0,0] op_sel_hi:[1,0,0]\n"
"v_mfma_f32_16x16x32_bf16 a[64:67], a[164:167], v[72:75], a[64:67]\n"
"ds_read_b64_tr_b16 a[234:235], v219 offset:42520\n"
"v_cvt_pk_bf16_f32 v88, v176, v177\n"
"v_mfma_f32_16x16x32_bf16 a[68:71], a[160:163], v[68:71], a[68:71]\n"
"s_add_u32 m0, s51, 89896\n"
"ds_read_b64_tr_b16 a[236:237], v219 offset:45592\n"
"v_cvt_pk_bf16_f32 v89, v178, v179\n"
"v_mfma_f32_16x16x32_bf16 a[68:71], a[164:167], v[76:79], a[68:71]\n"
"ds_read_b64_tr_b16 a[238:239], v219 offset:46616\n"
"buffer_load_dwordx4 v217, s[20:23], s50 offen offset:256 lds\n"
"v_mfma_f32_16x16x32_bf16 a[72:75], a[168:171], v[64:67], a[72:75]\n"
"s_add_u32 s50, s50, 32768\n"
"ds_read_b64_tr_b16 a[240:241], v219 offset:41744\n"
"v_cvt_pk_bf16_f32 v90, v180, v181\n"
"v_mfma_f32_16x16x32_bf16 a[72:75], a[172:175], v[72:75], a[72:75]\n"
"ds_read_b64_tr_b16 a[242:243], v219 offset:42768\n"
"v_cvt_pk_bf16_f32 v91, v182, v183\n"
"v_mfma_f32_16x16x32_bf16 a[76:79], a[168:171], v[68:71], a[76:79]\n"
"ds_read_b64_tr_b16 a[244:245], v219 offset:45840\n"
"v_fma_mix_f32 v184, v92, v160, v168 op_sel:[0,0,0] op_sel_hi:[1,0,0]\n"
"v_mfma_f32_16x16x32_bf16 a[76:79], a[172:175], v[76:79], a[76:79]\n"
"s_waitcnt lgkmcnt(11)\n"
"ds_read_b64_tr_b16 a[246:247], v219 offset:46864\n"
"v_fma_mix_f32 v185, v92, v161, v169 op_sel:[1,0,0] op_sel_hi:[1,0,0]\n"
"v_mfma_f32_16x16x32_bf16 a[80:83], a[176:179], v[64:67], a[80:83]\n"
"s_add_u32 m0, s51, 82984\n"
"ds_read_b64_tr_b16 a[248:249], v219 offset:41752\n"
"v_fma_mix_f32 v186, v93, v162, v170 op_sel:[0,0,0] op_sel_hi:[1,0,0]\n"
"v_mfma_f32_16x16x32_bf16 a[80:83], a[180:183], v[72:75], a[80:83]\n"
"ds_read_b64_tr_b16 a[250:251], v219 offset:42776\n"
"buffer_load_dwordx4 v217, s[20:23], s50 offen offset:0 lds\n"
"v_mfma_f32_16x16x32_bf16 a[84:87], a[176:179], v[68:71], a[84:87]\n"
"ds_read_b64_tr_b16 a[252:253], v219 offset:45848\n"
"v_fma_mix_f32 v187, v93, v163, v171 op_sel:[1,0,0] op_sel_hi:[1,0,0]\n"
"v_mfma_f32_16x16x32_bf16 a[84:87], a[180:183], v[76:79], a[84:87]\n"
"ds_read_b64_tr_b16 a[254:255], v219 offset:46872\n"
"v_fma_mix_f32 v188, v94, v164, v172 op_sel:[0,0,0] op_sel_hi:[1,0,0]\n"
"v_mfma_f32_16x16x32_bf16 v[236:239], a[184:187], v[64:67], v[236:239]\n"
"ds_read_b64_tr_b16 a[160:161], v218 offset:0\n"
"v_fma_mix_f32 v189, v94, v165, v173 op_sel:[1,0,0] op_sel_hi:[1,0,0]\n"
"v_fma_mix_f32 v190, v95, v166, v174 op_sel:[0,0,0] op_sel_hi:[1,0,0]\n"
"v_mfma_f32_16x16x32_bf16 v[236:239], a[188:191], v[72:75], v[236:239]\n"
"ds_read_b64_tr_b16 a[162:163], v218 offset:1024\n"
"v_fma_mix_f32 v191, v95, v167, v175 op_sel:[1,0,0] op_sel_hi:[1,0,0]\n"
"v_mfma_f32_16x16x32_bf16 v[232:235], a[184:187], v[68:71], v[232:235]\n"
"s_add_u32 m0, s51, 90920\n"
"ds_read_b64_tr_b16 a[164:165], v218 offset:4096\n"
"ds_read_b128 v[160:163], v224 offset:3328\n"
"v_mfma_f32_16x16x32_bf16 v[232:235], a[188:191], v[76:79], v[232:235]\n"
"s_waitcnt lgkmcnt(12)\n"
"buffer_load_dwordx4 v217, s[20:23], s50 offen offset:256 lds\n"
"ds_read_b64_tr_b16 a[166:167], v218 offset:5120\n"
"v_mfma_f32_16x16x32_bf16 v[228:231], a[224:227], v[64:67], v[228:231]\n"
"s_add_u32 s50, s50, 425984\n"
"ds_read_b64_tr_b16 a[168:169], v218 offset:8\n"
"v_cvt_pk_bf16_f32 v92, v184, v185\n"
"v_mfma_f32_16x16x32_bf16 v[228:231], a[228:231], v[72:75], v[228:231]\n"
"ds_read_b64_tr_b16 a[170:171], v218 offset:1032\n"
"ds_read_b128 v[164:167], v224 offset:3392\n"
"v_mfma_f32_16x16x32_bf16 v[220:223], a[224:227], v[68:71], v[220:223]\n"
"ds_read_b64_tr_b16 a[172:173], v218 offset:4104\n"
"v_cvt_pk_bf16_f32 v93, v186, v187\n"
"v_mfma_f32_16x16x32_bf16 v[220:223], a[228:231], v[76:79], v[220:223]\n"
"ds_read_b64_tr_b16 a[174:175], v218 offset:5128\n"
"v_cvt_pk_bf16_f32 v94, v188, v189\n"
"v_mfma_f32_16x16x32_bf16 v[212:215], a[232:235], v[64:67], v[212:215]\n"
"ds_read_b64_tr_b16 a[176:177], v218 offset:256\n"
"ds_read_b128 v[168:171], v224 offset:3456\n"
"v_mfma_f32_16x16x32_bf16 v[212:215], a[236:239], v[72:75], v[212:215]\n"
"ds_read_b64_tr_b16 a[178:179], v218 offset:1280\n"
"v_cvt_pk_bf16_f32 v95, v190, v191\n"
"v_mfma_f32_16x16x32_bf16 v[208:211], a[232:235], v[68:71], v[208:211]\n"
"ds_read_b64_tr_b16 a[180:181], v218 offset:4352\n"
"v_fma_mix_f32 v176, v96, v144, v152 op_sel:[0,0,0] op_sel_hi:[1,0,0]\n"
"v_mfma_f32_16x16x32_bf16 v[208:211], a[236:239], v[76:79], v[208:211]\n"
"s_waitcnt lgkmcnt(14)\n"
"ds_read_b64_tr_b16 a[182:183], v218 offset:5376\n"
"ds_read_b128 v[172:175], v224 offset:3520\n"
"v_mfma_f32_16x16x32_bf16 v[204:207], a[240:243], v[64:67], v[204:207]\n"
"ds_read_b64_tr_b16 a[184:185], v218 offset:264\n"
"v_fma_mix_f32 v177, v96, v145, v153 op_sel:[1,0,0] op_sel_hi:[1,0,0]\n"
"v_mfma_f32_16x16x32_bf16 v[204:207], a[244:247], v[72:75], v[204:207]\n"
"ds_read_b64_tr_b16 a[186:187], v218 offset:1288\n"
"v_fma_mix_f32 v178, v97, v146, v154 op_sel:[0,0,0] op_sel_hi:[1,0,0]\n"
"v_mfma_f32_16x16x32_bf16 v[200:203], a[240:243], v[68:71], v[200:203]\n"
"ds_read_b64_tr_b16 a[188:189], v218 offset:4360\n"
"v_fma_mix_f32 v179, v97, v147, v155 op_sel:[1,0,0] op_sel_hi:[1,0,0]\n"
"v_mfma_f32_16x16x32_bf16 v[200:203], a[244:247], v[76:79], v[200:203]\n"
"ds_read_b64_tr_b16 a[190:191], v218 offset:5384\n"
"v_fma_mix_f32 v180, v98, v148, v156 op_sel:[0,0,0] op_sel_hi:[1,0,0]\n"
"v_mfma_f32_16x16x32_bf16 v[196:199], a[248:251], v[64:67], v[196:199]\n"
"ds_read_b64_tr_b16 a[224:225], v218 offset:512\n"
"v_fma_mix_f32 v181, v98, v149, v157 op_sel:[1,0,0] op_sel_hi:[1,0,0]\n"
"v_mfma_f32_16x16x32_bf16 v[196:199], a[252:255], v[72:75], v[196:199]\n"
"ds_read_b64_tr_b16 a[226:227], v218 offset:1536\n"
"v_fma_mix_f32 v182, v99, v150, v158 op_sel:[0,0,0] op_sel_hi:[1,0,0]\n"
"v_mfma_f32_16x16x32_bf16 v[192:195], a[248:251], v[68:71], v[192:195]\n"
"ds_read_b64_tr_b16 a[228:229], v218 offset:4608\n"
"v_fma_mix_f32 v183, v99, v151, v159 op_sel:[1,0,0] op_sel_hi:[1,0,0]\n"
"v_mfma_f32_16x16x32_bf16 v[192:195], a[252:255], v[76:79], v[192:195]\n"
"s_waitcnt lgkmcnt(13)\n"
"ds_read_b64_tr_b16 a[230:231], v218 offset:5632\n"
"v_cvt_pk_bf16_f32 v96, v176, v177\n"
"v_mfma_f32_16x16x32_bf16 a[0:3], a[160:163], v[80:83], a[0:3]\n"
"ds_read_b64_tr_b16 a[232:233], v218 offset:520\n"
"v_cvt_pk_bf16_f32 v97, v178, v179\n"
"v_mfma_f32_16x16x32_bf16 a[0:3], a[164:167], v[88:91], a[0:3]\n"
"s_waitcnt vmcnt(8)\n"
"ds_read_b64_tr_b16 a[234:235], v218 offset:1544\n"
"v_cvt_pk_bf16_f32 v98, v180, v181\n"
"v_mfma_f32_16x16x32_bf16 a[4:7], a[160:163], v[84:87], a[4:7]\n"
"s_barrier\n"
"ds_read_b64_tr_b16 a[236:237], v218 offset:4616\n"
"v_cvt_pk_bf16_f32 v99, v182, v183\n"
"v_mfma_f32_16x16x32_bf16 a[4:7], a[164:167], v[92:95], a[4:7]\n"
"ds_read_b64_tr_b16 a[238:239], v218 offset:5640\n"
"v_fma_mix_f32 v184, v100, v144, v152 op_sel:[0,0,0] op_sel_hi:[1,0,0]\n"
"v_mfma_f32_16x16x32_bf16 a[8:11], a[168:171], v[80:83], a[8:11]\n"
"s_add_u32 m0, s51, 98352\n"
"ds_read_b64_tr_b16 a[240:241], v218 offset:768\n"
"v_fma_mix_f32 v185, v100, v145, v153 op_sel:[1,0,0] op_sel_hi:[1,0,0]\n"
"v_mfma_f32_16x16x32_bf16 a[8:11], a[172:175], v[88:91], a[8:11]\n"
"ds_read_b64_tr_b16 a[242:243], v218 offset:1792\n"
"buffer_load_dwordx4 v217, s[20:23], s50 offen offset:0 lds\n"
"v_mfma_f32_16x16x32_bf16 a[12:15], a[168:171], v[84:87], a[12:15]\n"
"ds_read_b64_tr_b16 a[244:245], v218 offset:4864\n"
"v_fma_mix_f32 v186, v101, v146, v154 op_sel:[0,0,0] op_sel_hi:[1,0,0]\n"
"v_mfma_f32_16x16x32_bf16 a[12:15], a[172:175], v[92:95], a[12:15]\n"
"s_waitcnt lgkmcnt(11)\n"
"ds_read_b64_tr_b16 a[246:247], v218 offset:5888\n"
"v_fma_mix_f32 v187, v101, v147, v155 op_sel:[1,0,0] op_sel_hi:[1,0,0]\n"
"v_mfma_f32_16x16x32_bf16 a[16:19], a[176:179], v[80:83], a[16:19]\n"
"ds_read_b64_tr_b16 a[248:249], v218 offset:776\n"
"v_fma_mix_f32 v188, v102, v148, v156 op_sel:[0,0,0] op_sel_hi:[1,0,0]\n"
"v_mfma_f32_16x16x32_bf16 a[16:19], a[180:183], v[88:91], a[16:19]\n"
"ds_read_b64_tr_b16 a[250:251], v218 offset:1800\n"
"v_fma_mix_f32 v189, v102, v149, v157 op_sel:[1,0,0] op_sel_hi:[1,0,0]\n"
"v_mfma_f32_16x16x32_bf16 a[20:23], a[176:179], v[84:87], a[20:23]\n"
"s_add_u32 m0, s51, 106288\n"
"ds_read_b64_tr_b16 a[252:253], v218 offset:4872\n"
"v_fma_mix_f32 v190, v103, v150, v158 op_sel:[0,0,0] op_sel_hi:[1,0,0]\n"
"v_mfma_f32_16x16x32_bf16 a[20:23], a[180:183], v[92:95], a[20:23]\n"
"ds_read_b64_tr_b16 a[254:255], v218 offset:5896\n"
"buffer_load_dwordx4 v217, s[20:23], s50 offen offset:256 lds\n"
"v_mfma_f32_16x16x32_bf16 a[24:27], a[184:187], v[80:83], a[24:27]\n"
"s_add_u32 s50, s50, 32768\n"
"ds_read_b64_tr_b16 a[160:161], v218 offset:8192\n"
"v_fma_mix_f32 v191, v103, v151, v159 op_sel:[1,0,0] op_sel_hi:[1,0,0]\n"
"v_mfma_f32_16x16x32_bf16 a[24:27], a[188:191], v[88:91], a[24:27]\n"
"ds_read_b64_tr_b16 a[162:163], v218 offset:9216\n"
"ds_read_b128 v[144:147], v224 offset:3584\n"
"v_mfma_f32_16x16x32_bf16 a[28:31], a[184:187], v[84:87], a[28:31]\n"
"ds_read_b64_tr_b16 a[164:165], v218 offset:12288\n"
"v_cvt_pk_bf16_f32 v100, v184, v185\n"
"v_mfma_f32_16x16x32_bf16 a[28:31], a[188:191], v[92:95], a[28:31]\n"
"s_waitcnt lgkmcnt(12)\n"
"ds_read_b64_tr_b16 a[166:167], v218 offset:13312\n"
"v_cvt_pk_bf16_f32 v101, v186, v187\n"
"v_mfma_f32_16x16x32_bf16 a[32:35], a[224:227], v[80:83], a[32:35]\n"
"s_add_u32 m0, s51, 99376\n"
"ds_read_b64_tr_b16 a[168:169], v218 offset:8200\n"
"ds_read_b128 v[148:151], v224 offset:3648\n"
"v_mfma_f32_16x16x32_bf16 a[32:35], a[228:231], v[88:91], a[32:35]\n"
"ds_read_b64_tr_b16 a[170:171], v218 offset:9224\n"
"buffer_load_dwordx4 v217, s[20:23], s50 offen offset:0 lds\n"
"v_mfma_f32_16x16x32_bf16 a[36:39], a[224:227], v[84:87], a[36:39]\n"
"ds_read_b64_tr_b16 a[172:173], v218 offset:12296\n"
"v_cvt_pk_bf16_f32 v102, v188, v189\n"
"v_mfma_f32_16x16x32_bf16 a[36:39], a[228:231], v[92:95], a[36:39]\n"
"ds_read_b64_tr_b16 a[174:175], v218 offset:13320\n"
"ds_read_b128 v[152:155], v224 offset:3712\n"
"v_mfma_f32_16x16x32_bf16 a[40:43], a[232:235], v[80:83], a[40:43]\n"
"ds_read_b64_tr_b16 a[176:177], v218 offset:8448\n"
"v_cvt_pk_bf16_f32 v103, v190, v191\n"
"v_mfma_f32_16x16x32_bf16 a[40:43], a[236:239], v[88:91], a[40:43]\n"
"ds_read_b64_tr_b16 a[178:179], v218 offset:9472\n"
"v_fma_mix_f32 v176, v104, v160, v168 op_sel:[0,0,0] op_sel_hi:[1,0,0]\n"
"v_mfma_f32_16x16x32_bf16 a[44:47], a[232:235], v[84:87], a[44:47]\n"
"s_add_u32 m0, s51, 107312\n"
"ds_read_b64_tr_b16 a[180:181], v218 offset:12544\n"
"ds_read_b128 v[156:159], v224 offset:3776\n"
"v_mfma_f32_16x16x32_bf16 a[44:47], a[236:239], v[92:95], a[44:47]\n"
"s_waitcnt lgkmcnt(14)\n"
"ds_read_b64_tr_b16 a[182:183], v218 offset:13568\n"
"buffer_load_dwordx4 v217, s[20:23], s50 offen offset:256 lds\n"
"v_mfma_f32_16x16x32_bf16 a[48:51], a[240:243], v[80:83], a[48:51]\n"
"s_add_u32 s50, s50, 32768\n"
"ds_read_b64_tr_b16 a[184:185], v218 offset:8456\n"
"v_fma_mix_f32 v177, v104, v161, v169 op_sel:[1,0,0] op_sel_hi:[1,0,0]\n"
"v_mfma_f32_16x16x32_bf16 a[48:51], a[244:247], v[88:91], a[48:51]\n"
"ds_read_b64_tr_b16 a[186:187], v218 offset:9480\n"
"v_fma_mix_f32 v178, v105, v162, v170 op_sel:[0,0,0] op_sel_hi:[1,0,0]\n"
"v_mfma_f32_16x16x32_bf16 a[52:55], a[240:243], v[84:87], a[52:55]\n"
"ds_read_b64_tr_b16 a[188:189], v218 offset:12552\n"
"v_fma_mix_f32 v179, v105, v163, v171 op_sel:[1,0,0] op_sel_hi:[1,0,0]\n"
"v_mfma_f32_16x16x32_bf16 a[52:55], a[244:247], v[92:95], a[52:55]\n"
"ds_read_b64_tr_b16 a[190:191], v218 offset:13576\n"
"v_fma_mix_f32 v180, v106, v164, v172 op_sel:[0,0,0] op_sel_hi:[1,0,0]\n"
"v_mfma_f32_16x16x32_bf16 a[56:59], a[248:251], v[80:83], a[56:59]\n"
"s_add_u32 m0, s51, 114744\n"
"ds_read_b64_tr_b16 a[224:225], v218 offset:8704\n"
"v_fma_mix_f32 v181, v106, v165, v173 op_sel:[1,0,0] op_sel_hi:[1,0,0]\n"
"v_mfma_f32_16x16x32_bf16 a[56:59], a[252:255], v[88:91], a[56:59]\n"
"ds_read_b64_tr_b16 a[226:227], v218 offset:9728\n"
"buffer_load_dwordx4 v217, s[20:23], s50 offen offset:0 lds\n"
"v_mfma_f32_16x16x32_bf16 a[60:63], a[248:251], v[84:87], a[60:63]\n"
"ds_read_b64_tr_b16 a[228:229], v218 offset:12800\n"
"v_fma_mix_f32 v182, v107, v166, v174 op_sel:[0,0,0] op_sel_hi:[1,0,0]\n"
"v_mfma_f32_16x16x32_bf16 a[60:63], a[252:255], v[92:95], a[60:63]\n"
"s_waitcnt lgkmcnt(12)\n"
"ds_read_b64_tr_b16 a[230:231], v218 offset:13824\n"
"v_fma_mix_f32 v183, v107, v167, v175 op_sel:[1,0,0] op_sel_hi:[1,0,0]\n"
"v_mfma_f32_16x16x32_bf16 a[64:67], a[160:163], v[80:83], a[64:67]\n"
"ds_read_b64_tr_b16 a[232:233], v218 offset:8712\n"
"v_cvt_pk_bf16_f32 v104, v176, v177\n"
"v_mfma_f32_16x16x32_bf16 a[64:67], a[164:167], v[88:91], a[64:67]\n"
"ds_read_b64_tr_b16 a[234:235], v218 offset:9736\n"
"v_cvt_pk_bf16_f32 v105, v178, v179\n"
"v_mfma_f32_16x16x32_bf16 a[68:71], a[160:163], v[84:87], a[68:71]\n"
"s_add_u32 m0, s51, 122680\n"
"ds_read_b64_tr_b16 a[236:237], v218 offset:12808\n"
"v_cvt_pk_bf16_f32 v106, v180, v181\n"
"v_mfma_f32_16x16x32_bf16 a[68:71], a[164:167], v[92:95], a[68:71]\n"
"ds_read_b64_tr_b16 a[238:239], v218 offset:13832\n"
"buffer_load_dwordx4 v217, s[20:23], s50 offen offset:256 lds\n"
"v_mfma_f32_16x16x32_bf16 a[72:75], a[168:171], v[80:83], a[72:75]\n"
"s_add_u32 s50, s50, 32768\n"
"ds_read_b64_tr_b16 a[240:241], v218 offset:8960\n"
"v_cvt_pk_bf16_f32 v107, v182, v183\n"
"v_mfma_f32_16x16x32_bf16 a[72:75], a[172:175], v[88:91], a[72:75]\n"
"ds_read_b64_tr_b16 a[242:243], v218 offset:9984\n"
"v_fma_mix_f32 v184, v108, v160, v168 op_sel:[0,0,0] op_sel_hi:[1,0,0]\n"
"v_mfma_f32_16x16x32_bf16 a[76:79], a[168:171], v[84:87], a[76:79]\n"
"ds_read_b64_tr_b16 a[244:245], v218 offset:13056\n"
"v_fma_mix_f32 v185, v108, v161, v169 op_sel:[1,0,0] op_sel_hi:[1,0,0]\n"
"v_mfma_f32_16x16x32_bf16 a[76:79], a[172:175], v[92:95], a[76:79]\n"
"s_waitcnt lgkmcnt(11)\n"
"ds_read_b64_tr_b16 a[246:247], v218 offset:14080\n"
"v_fma_mix_f32 v186, v109, v162, v170 op_sel:[0,0,0] op_sel_hi:[1,0,0]\n"
"v_mfma_f32_16x16x32_bf16 a[80:83], a[176:179], v[80:83], a[80:83]\n"
"s_add_u32 m0, s51, 115768\n"
"ds_read_b64_tr_b16 a[248:249], v218 offset:8968\n"
"v_fma_mix_f32 v187, v109, v163, v171 op_sel:[1,0,0] op_sel_hi:[1,0,0]\n"
"v_mfma_f32_16x16x32_bf16 a[80:83], a[180:183], v[88:91], a[80:83]\n"
"ds_read_b64_tr_b16 a[250:251], v218 offset:9992\n"
"buffer_load_dwordx4 v217, s[20:23], s50 offen offset:0 lds\n"
"v_mfma_f32_16x16x32_bf16 a[84:87], a[176:179], v[84:87], a[84:87]\n"
"ds_read_b64_tr_b16 a[252:253], v218 offset:13064\n"
"v_fma_mix_f32 v188, v110, v164, v172 op_sel:[0,0,0] op_sel_hi:[1,0,0]\n"
"v_mfma_f32_16x16x32_bf16 a[84:87], a[180:183], v[92:95], a[84:87]\n"
"ds_read_b64_tr_b16 a[254:255], v218 offset:14088\n"
"v_fma_mix_f32 v189, v110, v165, v173 op_sel:[1,0,0] op_sel_hi:[1,0,0]\n"
"v_mfma_f32_16x16x32_bf16 v[236:239], a[184:187], v[80:83], v[236:239]\n"
"ds_read_b64_tr_b16 a[160:161], v218 offset:32784\n"
"v_fma_mix_f32 v190, v111, v166, v174 op_sel:[0,0,0] op_sel_hi:[1,0,0]\n"
"v_fma_mix_f32 v191, v111, v167, v175 op_sel:[1,0,0] op_sel_hi:[1,0,0]\n"
"v_mfma_f32_16x16x32_bf16 v[236:239], a[188:191], v[88:91], v[236:239]\n"
"ds_read_b64_tr_b16 a[162:163], v218 offset:33808\n"
"ds_read_b128 v[160:163], v224 offset:3840\n"
"v_mfma_f32_16x16x32_bf16 v[232:235], a[184:187], v[84:87], v[232:235]\n"
"s_add_u32 m0, s51, 123704\n"
"ds_read_b64_tr_b16 a[164:165], v218 offset:36880\n"
"v_cvt_pk_bf16_f32 v108, v184, v185\n"
"v_mfma_f32_16x16x32_bf16 v[232:235], a[188:191], v[92:95], v[232:235]\n"
"s_waitcnt lgkmcnt(12)\n"
"buffer_load_dwordx4 v217, s[20:23], s50 offen offset:256 lds\n"
"ds_read_b64_tr_b16 a[166:167], v218 offset:37904\n"
"v_mfma_f32_16x16x32_bf16 v[228:231], a[224:227], v[80:83], v[228:231]\n"
"s_add_u32 s50, s50, 425984\n"
"ds_read_b64_tr_b16 a[168:169], v218 offset:32792\n"
"ds_read_b128 v[164:167], v224 offset:3904\n"
"v_mfma_f32_16x16x32_bf16 v[228:231], a[228:231], v[88:91], v[228:231]\n"
"ds_read_b64_tr_b16 a[170:171], v218 offset:33816\n"
"v_cvt_pk_bf16_f32 v109, v186, v187\n"
"v_mfma_f32_16x16x32_bf16 v[220:223], a[224:227], v[84:87], v[220:223]\n"
"ds_read_b64_tr_b16 a[172:173], v218 offset:36888\n"
"v_cvt_pk_bf16_f32 v110, v188, v189\n"
"v_mfma_f32_16x16x32_bf16 v[220:223], a[228:231], v[92:95], v[220:223]\n"
"ds_read_b64_tr_b16 a[174:175], v218 offset:37912\n"
"ds_read_b128 v[168:171], v224 offset:3968\n"
"v_mfma_f32_16x16x32_bf16 v[212:215], a[232:235], v[80:83], v[212:215]\n"
"ds_read_b64_tr_b16 a[176:177], v218 offset:33040\n"
"v_cvt_pk_bf16_f32 v111, v190, v191\n"
"v_mfma_f32_16x16x32_bf16 v[212:215], a[236:239], v[88:91], v[212:215]\n"
"ds_read_b64_tr_b16 a[178:179], v218 offset:34064\n"
"v_fma_mix_f32 v176, v112, v144, v152 op_sel:[0,0,0] op_sel_hi:[1,0,0]\n"
"v_mfma_f32_16x16x32_bf16 v[208:211], a[232:235], v[84:87], v[208:211]\n"
"ds_read_b64_tr_b16 a[180:181], v218 offset:37136\n"
"ds_read_b128 v[172:175], v224 offset:4032\n"
"v_mfma_f32_16x16x32_bf16 v[208:211], a[236:239], v[92:95], v[208:211]\n"
"s_waitcnt lgkmcnt(14)\n"
"ds_read_b64_tr_b16 a[182:183], v218 offset:38160\n"
"v_fma_mix_f32 v177, v112, v145, v153 op_sel:[1,0,0] op_sel_hi:[1,0,0]\n"
"v_mfma_f32_16x16x32_bf16 v[204:207], a[240:243], v[80:83], v[204:207]\n"
"ds_read_b64_tr_b16 a[184:185], v218 offset:33048\n"
"v_fma_mix_f32 v178, v113, v146, v154 op_sel:[0,0,0] op_sel_hi:[1,0,0]\n"
"v_mfma_f32_16x16x32_bf16 v[204:207], a[244:247], v[88:91], v[204:207]\n"
"ds_read_b64_tr_b16 a[186:187], v218 offset:34072\n"
"v_fma_mix_f32 v179, v113, v147, v155 op_sel:[1,0,0] op_sel_hi:[1,0,0]\n"
"v_mfma_f32_16x16x32_bf16 v[200:203], a[240:243], v[84:87], v[200:203]\n"
"ds_read_b64_tr_b16 a[188:189], v218 offset:37144\n"
"v_fma_mix_f32 v180, v114, v148, v156 op_sel:[0,0,0] op_sel_hi:[1,0,0]\n"
"v_mfma_f32_16x16x32_bf16 v[200:203], a[244:247], v[92:95], v[200:203]\n"
"ds_read_b64_tr_b16 a[190:191], v218 offset:38168\n"
"v_fma_mix_f32 v181, v114, v149, v157 op_sel:[1,0,0] op_sel_hi:[1,0,0]\n"
"v_mfma_f32_16x16x32_bf16 v[196:199], a[248:251], v[80:83], v[196:199]\n"
"ds_read_b64_tr_b16 a[224:225], v218 offset:33296\n"
"v_fma_mix_f32 v182, v115, v150, v158 op_sel:[0,0,0] op_sel_hi:[1,0,0]\n"
"v_mfma_f32_16x16x32_bf16 v[196:199], a[252:255], v[88:91], v[196:199]\n"
"ds_read_b64_tr_b16 a[226:227], v218 offset:34320\n"
"v_fma_mix_f32 v183, v115, v151, v159 op_sel:[1,0,0] op_sel_hi:[1,0,0]\n"
"v_mfma_f32_16x16x32_bf16 v[192:195], a[248:251], v[84:87], v[192:195]\n"
"ds_read_b64_tr_b16 a[228:229], v218 offset:37392\n"
"v_cvt_pk_bf16_f32 v112, v176, v177\n"
"v_mfma_f32_16x16x32_bf16 v[192:195], a[252:255], v[92:95], v[192:195]\n"
"s_waitcnt lgkmcnt(12)\n"
"ds_read_b64_tr_b16 a[230:231], v218 offset:38416\n"
"v_cvt_pk_bf16_f32 v113, v178, v179\n"
"v_mfma_f32_16x16x32_bf16 a[0:3], a[160:163], v[96:99], a[0:3]\n"
"ds_read_b64_tr_b16 a[232:233], v218 offset:33304\n"
"v_cvt_pk_bf16_f32 v114, v180, v181\n"
"v_mfma_f32_16x16x32_bf16 a[0:3], a[164:167], v[104:107], a[0:3]\n"
"s_waitcnt vmcnt(8)\n"
"ds_read_b64_tr_b16 a[234:235], v218 offset:34328\n"
"v_cvt_pk_bf16_f32 v115, v182, v183\n"
"v_mfma_f32_16x16x32_bf16 a[4:7], a[160:163], v[100:103], a[4:7]\n"
"s_barrier\n"
"ds_read_b64_tr_b16 a[236:237], v218 offset:37400\n"
"v_fma_mix_f32 v184, v116, v144, v152 op_sel:[0,0,0] op_sel_hi:[1,0,0]\n"
"v_mfma_f32_16x16x32_bf16 a[4:7], a[164:167], v[108:111], a[4:7]\n"
"ds_read_b64_tr_b16 a[238:239], v218 offset:38424\n"
"v_fma_mix_f32 v185, v116, v145, v153 op_sel:[1,0,0] op_sel_hi:[1,0,0]\n"
"v_mfma_f32_16x16x32_bf16 a[8:11], a[168:171], v[96:99], a[8:11]\n"
"s_mov_b32 m0, s86\n"
"ds_read_b64_tr_b16 a[240:241], v218 offset:33552\n"
"v_fma_mix_f32 v186, v117, v146, v154 op_sel:[0,0,0] op_sel_hi:[1,0,0]\n"
"v_mfma_f32_16x16x32_bf16 a[8:11], a[172:175], v[104:107], a[8:11]\n"
"ds_read_b64_tr_b16 a[242:243], v218 offset:34576\n"
"buffer_load_dwordx4 v246, s[8:11], s84 offen offset:0 lds\n"
"v_mfma_f32_16x16x32_bf16 a[12:15], a[168:171], v[100:103], a[12:15]\n"
"ds_read_b64_tr_b16 a[244:245], v218 offset:37648\n"
"v_fma_mix_f32 v187, v117, v147, v155 op_sel:[1,0,0] op_sel_hi:[1,0,0]\n"
"v_mfma_f32_16x16x32_bf16 a[12:15], a[172:175], v[108:111], a[12:15]\n"
"s_waitcnt lgkmcnt(11)\n"
"ds_read_b64_tr_b16 a[246:247], v218 offset:38672\n"
"v_fma_mix_f32 v188, v118, v148, v156 op_sel:[0,0,0] op_sel_hi:[1,0,0]\n"
"v_mfma_f32_16x16x32_bf16 a[16:19], a[176:179], v[96:99], a[16:19]\n"
"ds_read_b64_tr_b16 a[248:249], v218 offset:33560\n"
"v_fma_mix_f32 v189, v118, v149, v157 op_sel:[1,0,0] op_sel_hi:[1,0,0]\n"
"v_mfma_f32_16x16x32_bf16 a[16:19], a[180:183], v[104:107], a[16:19]\n"
"ds_read_b64_tr_b16 a[250:251], v218 offset:34584\n"
"v_fma_mix_f32 v190, v119, v150, v158 op_sel:[0,0,0] op_sel_hi:[1,0,0]\n"
"v_mfma_f32_16x16x32_bf16 a[20:23], a[176:179], v[100:103], a[20:23]\n"
"ds_read_b64_tr_b16 a[252:253], v218 offset:37656\n"
"v_fma_mix_f32 v191, v119, v151, v159 op_sel:[1,0,0] op_sel_hi:[1,0,0]\n"
"v_mfma_f32_16x16x32_bf16 a[20:23], a[180:183], v[108:111], a[20:23]\n"
"ds_read_b64_tr_b16 a[254:255], v218 offset:38680\n"
"buffer_load_dwordx4 v246, s[8:11], s84 offen offset:1024 lds\n"
"v_mfma_f32_16x16x32_bf16 a[24:27], a[184:187], v[96:99], a[24:27]\n"
"ds_read_b64_tr_b16 a[160:161], v218 offset:40976\n"
"ds_read_b128 v[144:147], v224 offset:4096\n"
"v_mfma_f32_16x16x32_bf16 a[24:27], a[188:191], v[104:107], a[24:27]\n"
"ds_read_b64_tr_b16 a[162:163], v218 offset:42000\n"
"v_cvt_pk_bf16_f32 v116, v184, v185\n"
"v_mfma_f32_16x16x32_bf16 a[28:31], a[184:187], v[100:103], a[28:31]\n"
"ds_read_b64_tr_b16 a[164:165], v218 offset:45072\n"
"v_cvt_pk_bf16_f32 v117, v186, v187\n"
"v_mfma_f32_16x16x32_bf16 a[28:31], a[188:191], v[108:111], a[28:31]\n"
"s_waitcnt lgkmcnt(12)\n"
"ds_read_b64_tr_b16 a[166:167], v218 offset:46096\n"
"ds_read_b128 v[148:151], v224 offset:4160\n"
"v_mfma_f32_16x16x32_bf16 a[32:35], a[224:227], v[96:99], a[32:35]\n"
"ds_read_b64_tr_b16 a[168:169], v218 offset:40984\n"
"v_cvt_pk_bf16_f32 v118, v188, v189\n"
"v_mfma_f32_16x16x32_bf16 a[32:35], a[228:231], v[104:107], a[32:35]\n"
"ds_read_b64_tr_b16 a[170:171], v218 offset:42008\n"
"buffer_load_dwordx4 v247, s[8:11], s84 offen offset:2048 lds\n"
"v_mfma_f32_16x16x32_bf16 a[36:39], a[224:227], v[100:103], a[36:39]\n"
"ds_read_b64_tr_b16 a[172:173], v218 offset:45080\n"
"ds_read_b128 v[152:155], v224 offset:4224\n"
"v_mfma_f32_16x16x32_bf16 a[36:39], a[228:231], v[108:111], a[36:39]\n"
"ds_read_b64_tr_b16 a[174:175], v218 offset:46104\n"
"v_cvt_pk_bf16_f32 v119, v190, v191\n"
"v_mfma_f32_16x16x32_bf16 a[40:43], a[232:235], v[96:99], a[40:43]\n"
"ds_read_b64_tr_b16 a[176:177], v218 offset:41232\n"
"v_fma_mix_f32 v176, v120, v160, v168 op_sel:[0,0,0] op_sel_hi:[1,0,0]\n"
"v_mfma_f32_16x16x32_bf16 a[40:43], a[236:239], v[104:107], a[40:43]\n"
"ds_read_b64_tr_b16 a[178:179], v218 offset:42256\n"
"ds_read_b128 v[156:159], v224 offset:4288\n"
"v_mfma_f32_16x16x32_bf16 a[44:47], a[232:235], v[100:103], a[44:47]\n"
"ds_read_b64_tr_b16 a[180:181], v218 offset:45328\n"
"v_fma_mix_f32 v177, v120, v161, v169 op_sel:[1,0,0] op_sel_hi:[1,0,0]\n"
"v_mfma_f32_16x16x32_bf16 a[44:47], a[236:239], v[108:111], a[44:47]\n"
"s_waitcnt lgkmcnt(14)\n"
"ds_read_b64_tr_b16 a[182:183], v218 offset:46352\n"
"buffer_load_dwordx4 v247, s[8:11], s84 offen offset:3072 lds\n"
"v_mfma_f32_16x16x32_bf16 a[48:51], a[240:243], v[96:99], a[48:51]\n"
"ds_read_b64_tr_b16 a[184:185], v218 offset:41240\n"
"v_fma_mix_f32 v178, v121, v162, v170 op_sel:[0,0,0] op_sel_hi:[1,0,0]\n"
"v_mfma_f32_16x16x32_bf16 a[48:51], a[244:247], v[104:107], a[48:51]\n"
"ds_read_b64_tr_b16 a[186:187], v218 offset:42264\n"
"v_fma_mix_f32 v179, v121, v163, v171 op_sel:[1,0,0] op_sel_hi:[1,0,0]\n"
"v_mfma_f32_16x16x32_bf16 a[52:55], a[240:243], v[100:103], a[52:55]\n"
"ds_read_b64_tr_b16 a[188:189], v218 offset:45336\n"
"v_fma_mix_f32 v180, v122, v164, v172 op_sel:[0,0,0] op_sel_hi:[1,0,0]\n"
"v_mfma_f32_16x16x32_bf16 a[52:55], a[244:247], v[108:111], a[52:55]\n"
"ds_read_b64_tr_b16 a[190:191], v218 offset:46360\n"
"v_fma_mix_f32 v181, v122, v165, v173 op_sel:[1,0,0] op_sel_hi:[1,0,0]\n"
"v_mfma_f32_16x16x32_bf16 a[56:59], a[248:251], v[96:99], a[56:59]\n"
"s_add_u32 m0, s86, 16384\n"
"ds_read_b64_tr_b16 a[224:225], v218 offset:41488\n"
"v_fma_mix_f32 v182, v123, v166, v174 op_sel:[0,0,0] op_sel_hi:[1,0,0]\n"
"v_mfma_f32_16x16x32_bf16 a[56:59], a[252:255], v[104:107], a[56:59]\n"
"ds_read_b64_tr_b16 a[226:227], v218 offset:42512\n"
"buffer_load_dwordx4 v246, s[12:15], s84 offen offset:0 lds\n"
"v_mfma_f32_16x16x32_bf16 a[60:63], a[248:251], v[100:103], a[60:63]\n"
"ds_read_b64_tr_b16 a[228:229], v218 offset:45584\n"
"v_fma_mix_f32 v183, v123, v167, v175 op_sel:[1,0,0] op_sel_hi:[1,0,0]\n"
"v_mfma_f32_16x16x32_bf16 a[60:63], a[252:255], v[108:111], a[60:63]\n"
"s_waitcnt lgkmcnt(12)\n"
"ds_read_b64_tr_b16 a[230:231], v218 offset:46608\n"
"v_cvt_pk_bf16_f32 v120, v176, v177\n"
"v_mfma_f32_16x16x32_bf16 a[64:67], a[160:163], v[96:99], a[64:67]\n"
"ds_read_b64_tr_b16 a[232:233], v218 offset:41496\n"
"v_cvt_pk_bf16_f32 v121, v178, v179\n"
"v_mfma_f32_16x16x32_bf16 a[64:67], a[164:167], v[104:107], a[64:67]\n"
"ds_read_b64_tr_b16 a[234:235], v218 offset:42520\n"
"v_cvt_pk_bf16_f32 v122, v180, v181\n"
"v_mfma_f32_16x16x32_bf16 a[68:71], a[160:163], v[100:103], a[68:71]\n"
"ds_read_b64_tr_b16 a[236:237], v218 offset:45592\n"
"v_cvt_pk_bf16_f32 v123, v182, v183\n"
"v_mfma_f32_16x16x32_bf16 a[68:71], a[164:167], v[108:111], a[68:71]\n"
"ds_read_b64_tr_b16 a[238:239], v218 offset:46616\n"
"buffer_load_dwordx4 v247, s[12:15], s84 offen offset:1024 lds\n"
"v_mfma_f32_16x16x32_bf16 a[72:75], a[168:171], v[96:99], a[72:75]\n"
"ds_read_b64_tr_b16 a[240:241], v218 offset:41744\n"
"v_fma_mix_f32 v184, v124, v160, v168 op_sel:[0,0,0] op_sel_hi:[1,0,0]\n"
"v_mfma_f32_16x16x32_bf16 a[72:75], a[172:175], v[104:107], a[72:75]\n"
"ds_read_b64_tr_b16 a[242:243], v218 offset:42768\n"
"v_fma_mix_f32 v185, v124, v161, v169 op_sel:[1,0,0] op_sel_hi:[1,0,0]\n"
"v_mfma_f32_16x16x32_bf16 a[76:79], a[168:171], v[100:103], a[76:79]\n"
"ds_read_b64_tr_b16 a[244:245], v218 offset:45840\n"
"v_fma_mix_f32 v186, v125, v162, v170 op_sel:[0,0,0] op_sel_hi:[1,0,0]\n"
"v_mfma_f32_16x16x32_bf16 a[76:79], a[172:175], v[108:111], a[76:79]\n"
"s_waitcnt lgkmcnt(11)\n"
"ds_read_b64_tr_b16 a[246:247], v218 offset:46864\n"
"v_fma_mix_f32 v187, v125, v163, v171 op_sel:[1,0,0] op_sel_hi:[1,0,0]\n"
"v_mfma_f32_16x16x32_bf16 a[80:83], a[176:179], v[96:99], a[80:83]\n"
"ds_read_b64_tr_b16 a[248:249], v218 offset:41752\n"
"v_fma_mix_f32 v188, v126, v164, v172 op_sel:[0,0,0] op_sel_hi:[1,0,0]\n"
"v_mfma_f32_16x16x32_bf16 a[80:83], a[180:183], v[104:107], a[80:83]\n"
"ds_read_b64_tr_b16 a[250:251], v218 offset:42776\n"
"buffer_load_dwordx4 v246, s[12:15], s84 offen offset:2048 lds\n"
"v_mfma_f32_16x16x32_bf16 a[84:87], a[176:179], v[100:103], a[84:87]\n"
"ds_read_b64_tr_b16 a[252:253], v218 offset:45848\n"
"v_fma_mix_f32 v189, v126, v165, v173 op_sel:[1,0,0] op_sel_hi:[1,0,0]\n"
"v_mfma_f32_16x16x32_bf16 a[84:87], a[180:183], v[108:111], a[84:87]\n"
"ds_read_b64_tr_b16 a[254:255], v218 offset:46872\n"
"v_fma_mix_f32 v190, v127, v166, v174 op_sel:[0,0,0] op_sel_hi:[1,0,0]\n"
"v_mfma_f32_16x16x32_bf16 v[236:239], a[184:187], v[96:99], v[236:239]\n"
"ds_read_b64_tr_b16 a[160:161], v219 offset:0\n"
"v_fma_mix_f32 v191, v127, v167, v175 op_sel:[1,0,0] op_sel_hi:[1,0,0]\n"
"v_cvt_pk_bf16_f32 v124, v184, v185\n"
"v_mfma_f32_16x16x32_bf16 v[236:239], a[188:191], v[104:107], v[236:239]\n"
"ds_read_b64_tr_b16 a[162:163], v219 offset:1024\n"
"ds_read_b128 v[160:163], v224 offset:4352\n"
"v_mfma_f32_16x16x32_bf16 v[232:235], a[184:187], v[100:103], v[232:235]\n"
"ds_read_b64_tr_b16 a[164:165], v219 offset:4096\n"
"v_cvt_pk_bf16_f32 v125, v186, v187\n"
"v_mfma_f32_16x16x32_bf16 v[232:235], a[188:191], v[108:111], v[232:235]\n"
"s_waitcnt lgkmcnt(12)\n"
"buffer_load_dwordx4 v247, s[12:15], s84 offen offset:3072 lds\n"
"ds_read_b64_tr_b16 a[166:167], v219 offset:5120\n"
"v_mfma_f32_16x16x32_bf16 v[228:231], a[224:227], v[96:99], v[228:231]\n"
"s_add_u32 s84, s84, 16384\n"
"ds_read_b64_tr_b16 a[168:169], v219 offset:8\n"
"ds_read_b128 v[164:167], v224 offset:4416\n"
"v_mfma_f32_16x16x32_bf16 v[228:231], a[228:231], v[104:107], v[228:231]\n"
"ds_read_b64_tr_b16 a[170:171], v219 offset:1032\n"
"v_cvt_pk_bf16_f32 v126, v188, v189\n"
"v_mfma_f32_16x16x32_bf16 v[220:223], a[224:227], v[100:103], v[220:223]\n"
"ds_read_b64_tr_b16 a[172:173], v219 offset:4104\n"
"v_cvt_pk_bf16_f32 v127, v190, v191\n"
"v_mfma_f32_16x16x32_bf16 v[220:223], a[228:231], v[108:111], v[220:223]\n"
"ds_read_b64_tr_b16 a[174:175], v219 offset:5128\n"
"ds_read_b128 v[168:171], v224 offset:4480\n"
"v_mfma_f32_16x16x32_bf16 v[212:215], a[232:235], v[96:99], v[212:215]\n"
"ds_read_b64_tr_b16 a[176:177], v219 offset:256\n"
"v_fma_mix_f32 v176, v128, v144, v152 op_sel:[0,0,0] op_sel_hi:[1,0,0]\n"
"v_mfma_f32_16x16x32_bf16 v[212:215], a[236:239], v[104:107], v[212:215]\n"
"ds_read_b64_tr_b16 a[178:179], v219 offset:1280\n"
"v_fma_mix_f32 v177, v128, v145, v153 op_sel:[1,0,0] op_sel_hi:[1,0,0]\n"
"v_mfma_f32_16x16x32_bf16 v[208:211], a[232:235], v[100:103], v[208:211]\n"
"ds_read_b64_tr_b16 a[180:181], v219 offset:4352\n"
"ds_read_b128 v[172:175], v224 offset:4544\n"
"v_mfma_f32_16x16x32_bf16 v[208:211], a[236:239], v[108:111], v[208:211]\n"
"s_waitcnt lgkmcnt(14)\n"
"ds_read_b64_tr_b16 a[182:183], v219 offset:5376\n"
"v_fma_mix_f32 v178, v129, v146, v154 op_sel:[0,0,0] op_sel_hi:[1,0,0]\n"
"v_mfma_f32_16x16x32_bf16 v[204:207], a[240:243], v[96:99], v[204:207]\n"
"ds_read_b64_tr_b16 a[184:185], v219 offset:264\n"
"v_fma_mix_f32 v179, v129, v147, v155 op_sel:[1,0,0] op_sel_hi:[1,0,0]\n"
"v_mfma_f32_16x16x32_bf16 v[204:207], a[244:247], v[104:107], v[204:207]\n"
"ds_read_b64_tr_b16 a[186:187], v219 offset:1288\n"
"v_fma_mix_f32 v180, v130, v148, v156 op_sel:[0,0,0] op_sel_hi:[1,0,0]\n"
"v_mfma_f32_16x16x32_bf16 v[200:203], a[240:243], v[100:103], v[200:203]\n"
"ds_read_b64_tr_b16 a[188:189], v219 offset:4360\n"
"v_fma_mix_f32 v181, v130, v149, v157 op_sel:[1,0,0] op_sel_hi:[1,0,0]\n"
"v_mfma_f32_16x16x32_bf16 v[200:203], a[244:247], v[108:111], v[200:203]\n"
"ds_read_b64_tr_b16 a[190:191], v219 offset:5384\n"
"v_fma_mix_f32 v182, v131, v150, v158 op_sel:[0,0,0] op_sel_hi:[1,0,0]\n"
"v_mfma_f32_16x16x32_bf16 v[196:199], a[248:251], v[96:99], v[196:199]\n"
"ds_read_b64_tr_b16 a[224:225], v219 offset:512\n"
"v_fma_mix_f32 v183, v131, v151, v159 op_sel:[1,0,0] op_sel_hi:[1,0,0]\n"
"v_mfma_f32_16x16x32_bf16 v[196:199], a[252:255], v[104:107], v[196:199]\n"
"ds_read_b64_tr_b16 a[226:227], v219 offset:1536\n"
"v_cvt_pk_bf16_f32 v128, v176, v177\n"
"v_mfma_f32_16x16x32_bf16 v[192:195], a[248:251], v[100:103], v[192:195]\n"
"ds_read_b64_tr_b16 a[228:229], v219 offset:4608\n"
"v_cvt_pk_bf16_f32 v129, v178, v179\n"
"v_mfma_f32_16x16x32_bf16 v[192:195], a[252:255], v[108:111], v[192:195]\n"
"s_waitcnt lgkmcnt(12)\n"
"ds_read_b64_tr_b16 a[230:231], v219 offset:5632\n"
"v_cvt_pk_bf16_f32 v130, v180, v181\n"
"v_mfma_f32_16x16x32_bf16 a[0:3], a[160:163], v[112:115], a[0:3]\n"
"ds_read_b64_tr_b16 a[232:233], v219 offset:520\n"
"v_cvt_pk_bf16_f32 v131, v182, v183\n"
"v_mfma_f32_16x16x32_bf16 a[0:3], a[164:167], v[120:123], a[0:3]\n"
"s_waitcnt vmcnt(8)\n"
"ds_read_b64_tr_b16 a[234:235], v219 offset:1544\n"
"v_fma_mix_f32 v184, v132, v144, v152 op_sel:[0,0,0] op_sel_hi:[1,0,0]\n"
"v_mfma_f32_16x16x32_bf16 a[4:7], a[160:163], v[116:119], a[4:7]\n"
"s_barrier\n"
"ds_read_b64_tr_b16 a[236:237], v219 offset:4616\n"
"v_fma_mix_f32 v185, v132, v145, v153 op_sel:[1,0,0] op_sel_hi:[1,0,0]\n"
"v_mfma_f32_16x16x32_bf16 a[4:7], a[164:167], v[124:127], a[4:7]\n"
"ds_read_b64_tr_b16 a[238:239], v219 offset:5640\n"
"v_fma_mix_f32 v186, v133, v146, v154 op_sel:[0,0,0] op_sel_hi:[1,0,0]\n"
"v_mfma_f32_16x16x32_bf16 a[8:11], a[168:171], v[112:115], a[8:11]\n"
"s_add_u32 m0, s86, 32768\n"
"ds_read_b64_tr_b16 a[240:241], v219 offset:768\n"
"v_fma_mix_f32 v187, v133, v147, v155 op_sel:[1,0,0] op_sel_hi:[1,0,0]\n"
"v_mfma_f32_16x16x32_bf16 a[8:11], a[172:175], v[120:123], a[8:11]\n"
"ds_read_b64_tr_b16 a[242:243], v219 offset:1792\n"
"buffer_load_dwordx4 v246, s[8:11], s84 offen offset:0 lds\n"
"v_mfma_f32_16x16x32_bf16 a[12:15], a[168:171], v[116:119], a[12:15]\n"
"ds_read_b64_tr_b16 a[244:245], v219 offset:4864\n"
"v_fma_mix_f32 v188, v134, v148, v156 op_sel:[0,0,0] op_sel_hi:[1,0,0]\n"
"v_mfma_f32_16x16x32_bf16 a[12:15], a[172:175], v[124:127], a[12:15]\n"
"s_waitcnt lgkmcnt(11)\n"
"ds_read_b64_tr_b16 a[246:247], v219 offset:5888\n"
"v_fma_mix_f32 v189, v134, v149, v157 op_sel:[1,0,0] op_sel_hi:[1,0,0]\n"
"v_mfma_f32_16x16x32_bf16 a[16:19], a[176:179], v[112:115], a[16:19]\n"
"ds_read_b64_tr_b16 a[248:249], v219 offset:776\n"
"v_fma_mix_f32 v190, v135, v150, v158 op_sel:[0,0,0] op_sel_hi:[1,0,0]\n"
"v_mfma_f32_16x16x32_bf16 a[16:19], a[180:183], v[120:123], a[16:19]\n"
"ds_read_b64_tr_b16 a[250:251], v219 offset:1800\n"
"v_fma_mix_f32 v191, v135, v151, v159 op_sel:[1,0,0] op_sel_hi:[1,0,0]\n"
"v_mfma_f32_16x16x32_bf16 a[20:23], a[176:179], v[116:119], a[20:23]\n"
"ds_read_b64_tr_b16 a[252:253], v219 offset:4872\n"
"v_cvt_pk_bf16_f32 v132, v184, v185\n"
"v_mfma_f32_16x16x32_bf16 a[20:23], a[180:183], v[124:127], a[20:23]\n"
"ds_read_b64_tr_b16 a[254:255], v219 offset:5896\n"
"buffer_load_dwordx4 v246, s[8:11], s84 offen offset:1024 lds\n"
"v_mfma_f32_16x16x32_bf16 a[24:27], a[184:187], v[112:115], a[24:27]\n"
"ds_read_b64_tr_b16 a[160:161], v219 offset:8192\n"
"v_cvt_pk_bf16_f32 v133, v186, v187\n"
"v_mfma_f32_16x16x32_bf16 a[24:27], a[188:191], v[120:123], a[24:27]\n"
"ds_read_b64_tr_b16 a[162:163], v219 offset:9216\n"
"v_cvt_pk_bf16_f32 v134, v188, v189\n"
"v_mfma_f32_16x16x32_bf16 a[28:31], a[184:187], v[116:119], a[28:31]\n"
"ds_read_b64_tr_b16 a[164:165], v219 offset:12288\n"
"v_cvt_pk_bf16_f32 v135, v190, v191\n"
"v_mfma_f32_16x16x32_bf16 a[28:31], a[188:191], v[124:127], a[28:31]\n"
"s_waitcnt lgkmcnt(11)\n"
"ds_read_b64_tr_b16 a[166:167], v219 offset:13312\n"
"v_fma_mix_f32 v176, v136, v160, v168 op_sel:[0,0,0] op_sel_hi:[1,0,0]\n"
"v_mfma_f32_16x16x32_bf16 a[32:35], a[224:227], v[112:115], a[32:35]\n"
"ds_read_b64_tr_b16 a[168:169], v219 offset:8200\n"
"v_fma_mix_f32 v177, v136, v161, v169 op_sel:[1,0,0] op_sel_hi:[1,0,0]\n"
"v_mfma_f32_16x16x32_bf16 a[32:35], a[228:231], v[120:123], a[32:35]\n"
"ds_read_b64_tr_b16 a[170:171], v219 offset:9224\n"
"buffer_load_dwordx4 v247, s[8:11], s84 offen offset:2048 lds\n"
"v_mfma_f32_16x16x32_bf16 a[36:39], a[224:227], v[116:119], a[36:39]\n"
"ds_read_b64_tr_b16 a[172:173], v219 offset:12296\n"
"v_fma_mix_f32 v178, v137, v162, v170 op_sel:[0,0,0] op_sel_hi:[1,0,0]\n"
"v_mfma_f32_16x16x32_bf16 a[36:39], a[228:231], v[124:127], a[36:39]\n"
"ds_read_b64_tr_b16 a[174:175], v219 offset:13320\n"
"v_fma_mix_f32 v179, v137, v163, v171 op_sel:[1,0,0] op_sel_hi:[1,0,0]\n"
"v_mfma_f32_16x16x32_bf16 a[40:43], a[232:235], v[112:115], a[40:43]\n"
"ds_read_b64_tr_b16 a[176:177], v219 offset:8448\n"
"v_fma_mix_f32 v180, v138, v164, v172 op_sel:[0,0,0] op_sel_hi:[1,0,0]\n"
"v_mfma_f32_16x16x32_bf16 a[40:43], a[236:239], v[120:123], a[40:43]\n"
"ds_read_b64_tr_b16 a[178:179], v219 offset:9472\n"
"v_fma_mix_f32 v181, v138, v165, v173 op_sel:[1,0,0] op_sel_hi:[1,0,0]\n"
"v_mfma_f32_16x16x32_bf16 a[44:47], a[232:235], v[116:119], a[44:47]\n"
"ds_read_b64_tr_b16 a[180:181], v219 offset:12544\n"
"v_fma_mix_f32 v182, v139, v166, v174 op_sel:[0,0,0] op_sel_hi:[1,0,0]\n"
"v_mfma_f32_16x16x32_bf16 a[44:47], a[236:239], v[124:127], a[44:47]\n"
"s_waitcnt lgkmcnt(11)\n"
"ds_read_b64_tr_b16 a[182:183], v219 offset:13568\n"
"buffer_load_dwordx4 v247, s[8:11], s84 offen offset:3072 lds\n"
"v_mfma_f32_16x16x32_bf16 a[48:51], a[240:243], v[112:115], a[48:51]\n"
"ds_read_b64_tr_b16 a[184:185], v219 offset:8456\n"
"v_fma_mix_f32 v183, v139, v167, v175 op_sel:[1,0,0] op_sel_hi:[1,0,0]\n"
"v_mfma_f32_16x16x32_bf16 a[48:51], a[244:247], v[120:123], a[48:51]\n"
"ds_read_b64_tr_b16 a[186:187], v219 offset:9480\n"
"v_cvt_pk_bf16_f32 v136, v176, v177\n"
"v_mfma_f32_16x16x32_bf16 a[52:55], a[240:243], v[116:119], a[52:55]\n"
"ds_read_b64_tr_b16 a[188:189], v219 offset:12552\n"
"v_cvt_pk_bf16_f32 v137, v178, v179\n"
"v_mfma_f32_16x16x32_bf16 a[52:55], a[244:247], v[124:127], a[52:55]\n"
"ds_read_b64_tr_b16 a[190:191], v219 offset:13576\n"
"v_cvt_pk_bf16_f32 v138, v180, v181\n"
"v_mfma_f32_16x16x32_bf16 a[56:59], a[248:251], v[112:115], a[56:59]\n"
"s_add_u32 m0, s86, 49152\n"
"ds_read_b64_tr_b16 a[224:225], v219 offset:8704\n"
"v_cvt_pk_bf16_f32 v139, v182, v183\n"
"v_mfma_f32_16x16x32_bf16 a[56:59], a[252:255], v[120:123], a[56:59]\n"
"ds_read_b64_tr_b16 a[226:227], v219 offset:9728\n"
"buffer_load_dwordx4 v246, s[12:15], s84 offen offset:0 lds\n"
"v_mfma_f32_16x16x32_bf16 a[60:63], a[248:251], v[116:119], a[60:63]\n"
"ds_read_b64_tr_b16 a[228:229], v219 offset:12800\n"
"v_fma_mix_f32 v184, v140, v160, v168 op_sel:[0,0,0] op_sel_hi:[1,0,0]\n"
"v_mfma_f32_16x16x32_bf16 a[60:63], a[252:255], v[124:127], a[60:63]\n"
"s_waitcnt lgkmcnt(11)\n"
"ds_read_b64_tr_b16 a[230:231], v219 offset:13824\n"
"v_fma_mix_f32 v185, v140, v161, v169 op_sel:[1,0,0] op_sel_hi:[1,0,0]\n"
"v_mfma_f32_16x16x32_bf16 a[64:67], a[160:163], v[112:115], a[64:67]\n"
"ds_read_b64_tr_b16 a[232:233], v219 offset:8712\n"
"v_fma_mix_f32 v186, v141, v162, v170 op_sel:[0,0,0] op_sel_hi:[1,0,0]\n"
"v_mfma_f32_16x16x32_bf16 a[64:67], a[164:167], v[120:123], a[64:67]\n"
"ds_read_b64_tr_b16 a[234:235], v219 offset:9736\n"
"v_fma_mix_f32 v187, v141, v163, v171 op_sel:[1,0,0] op_sel_hi:[1,0,0]\n"
"v_mfma_f32_16x16x32_bf16 a[68:71], a[160:163], v[116:119], a[68:71]\n"
"ds_read_b64_tr_b16 a[236:237], v219 offset:12808\n"
"v_fma_mix_f32 v188, v142, v164, v172 op_sel:[0,0,0] op_sel_hi:[1,0,0]\n"
"v_mfma_f32_16x16x32_bf16 a[68:71], a[164:167], v[124:127], a[68:71]\n"
"ds_read_b64_tr_b16 a[238:239], v219 offset:13832\n"
"buffer_load_dwordx4 v247, s[12:15], s84 offen offset:1024 lds\n"
"v_mfma_f32_16x16x32_bf16 a[72:75], a[168:171], v[112:115], a[72:75]\n"
"ds_read_b64_tr_b16 a[240:241], v219 offset:8960\n"
"v_fma_mix_f32 v189, v142, v165, v173 op_sel:[1,0,0] op_sel_hi:[1,0,0]\n"
"v_mfma_f32_16x16x32_bf16 a[72:75], a[172:175], v[120:123], a[72:75]\n"
"ds_read_b64_tr_b16 a[242:243], v219 offset:9984\n"
"v_fma_mix_f32 v190, v143, v166, v174 op_sel:[0,0,0] op_sel_hi:[1,0,0]\n"
"v_mfma_f32_16x16x32_bf16 a[76:79], a[168:171], v[116:119], a[76:79]\n"
"ds_read_b64_tr_b16 a[244:245], v219 offset:13056\n"
"v_fma_mix_f32 v191, v143, v167, v175 op_sel:[1,0,0] op_sel_hi:[1,0,0]\n"
"v_mfma_f32_16x16x32_bf16 a[76:79], a[172:175], v[124:127], a[76:79]\n"
"s_waitcnt lgkmcnt(11)\n"
"ds_read_b64_tr_b16 a[246:247], v219 offset:14080\n"
"v_cvt_pk_bf16_f32 v140, v184, v185\n"
"v_mfma_f32_16x16x32_bf16 a[80:83], a[176:179], v[112:115], a[80:83]\n"
"ds_read_b64_tr_b16 a[248:249], v219 offset:8968\n"
"v_cvt_pk_bf16_f32 v141, v186, v187\n"
"v_mfma_f32_16x16x32_bf16 a[80:83], a[180:183], v[120:123], a[80:83]\n"
"ds_read_b64_tr_b16 a[250:251], v219 offset:9992\n"
"buffer_load_dwordx4 v246, s[12:15], s84 offen offset:2048 lds\n"
"v_mfma_f32_16x16x32_bf16 a[84:87], a[176:179], v[116:119], a[84:87]\n"
"ds_read_b64_tr_b16 a[252:253], v219 offset:13064\n"
"v_cvt_pk_bf16_f32 v142, v188, v189\n"
"v_mfma_f32_16x16x32_bf16 a[84:87], a[180:183], v[124:127], a[84:87]\n"
"ds_read_b64_tr_b16 a[254:255], v219 offset:14088\n"
"v_cvt_pk_bf16_f32 v143, v190, v191\n"
"v_mfma_f32_16x16x32_bf16 v[236:239], a[184:187], v[112:115], v[236:239]\n"
"ds_read_b64_tr_b16 a[160:161], v219 offset:32784\n"
"v_mfma_f32_16x16x32_bf16 v[236:239], a[188:191], v[120:123], v[236:239]\n"
"ds_read_b64_tr_b16 a[162:163], v219 offset:33808\n"
"v_mfma_f32_16x16x32_bf16 v[232:235], a[184:187], v[116:119], v[232:235]\n"
"ds_read_b64_tr_b16 a[164:165], v219 offset:36880\n"
"v_mfma_f32_16x16x32_bf16 v[232:235], a[188:191], v[124:127], v[232:235]\n"
"s_waitcnt lgkmcnt(11)\n"
"buffer_load_dwordx4 v247, s[12:15], s84 offen offset:3072 lds\n"
"ds_read_b64_tr_b16 a[166:167], v219 offset:37904\n"
"v_mfma_f32_16x16x32_bf16 v[228:231], a[224:227], v[112:115], v[228:231]\n"
"s_add_u32 s84, s84, 16384\n"
"ds_read_b64_tr_b16 a[168:169], v219 offset:32792\n"
"v_mfma_f32_16x16x32_bf16 v[228:231], a[228:231], v[120:123], v[228:231]\n"
"ds_read_b64_tr_b16 a[170:171], v219 offset:33816\n"
"v_mfma_f32_16x16x32_bf16 v[220:223], a[224:227], v[116:119], v[220:223]\n"
"ds_read_b64_tr_b16 a[172:173], v219 offset:36888\n"
"v_mfma_f32_16x16x32_bf16 v[220:223], a[228:231], v[124:127], v[220:223]\n"
"ds_read_b64_tr_b16 a[174:175], v219 offset:37912\n"
"v_mfma_f32_16x16x32_bf16 v[212:215], a[232:235], v[112:115], v[212:215]\n"
"ds_read_b64_tr_b16 a[176:177], v219 offset:33040\n"
"v_mfma_f32_16x16x32_bf16 v[212:215], a[236:239], v[120:123], v[212:215]\n"
"ds_read_b64_tr_b16 a[178:179], v219 offset:34064\n"
"v_mfma_f32_16x16x32_bf16 v[208:211], a[232:235], v[116:119], v[208:211]\n"
"ds_read_b64_tr_b16 a[180:181], v219 offset:37136\n"
"v_mfma_f32_16x16x32_bf16 v[208:211], a[236:239], v[124:127], v[208:211]\n"
"s_waitcnt lgkmcnt(11)\n"
"ds_read_b64_tr_b16 a[182:183], v219 offset:38160\n"
"v_mfma_f32_16x16x32_bf16 v[204:207], a[240:243], v[112:115], v[204:207]\n"
"ds_read_b64_tr_b16 a[184:185], v219 offset:33048\n"
"v_mfma_f32_16x16x32_bf16 v[204:207], a[244:247], v[120:123], v[204:207]\n"
"ds_read_b64_tr_b16 a[186:187], v219 offset:34072\n"
"v_mfma_f32_16x16x32_bf16 v[200:203], a[240:243], v[116:119], v[200:203]\n"
"ds_read_b64_tr_b16 a[188:189], v219 offset:37144\n"
"v_mfma_f32_16x16x32_bf16 v[200:203], a[244:247], v[124:127], v[200:203]\n"
"ds_read_b64_tr_b16 a[190:191], v219 offset:38168\n"
"v_mfma_f32_16x16x32_bf16 v[196:199], a[248:251], v[112:115], v[196:199]\n"
"ds_read_b64_tr_b16 a[224:225], v219 offset:33296\n"
"v_mfma_f32_16x16x32_bf16 v[196:199], a[252:255], v[120:123], v[196:199]\n"
"ds_read_b64_tr_b16 a[226:227], v219 offset:34320\n"
"v_mfma_f32_16x16x32_bf16 v[192:195], a[248:251], v[116:119], v[192:195]\n"
"ds_read_b64_tr_b16 a[228:229], v219 offset:37392\n"
"v_mfma_f32_16x16x32_bf16 v[192:195], a[252:255], v[124:127], v[192:195]\n"
"s_waitcnt lgkmcnt(11)\n"
"ds_read_b64_tr_b16 a[230:231], v219 offset:38416\n"
"v_mfma_f32_16x16x32_bf16 a[0:3], a[160:163], v[128:131], a[0:3]\n"
"ds_read_b64_tr_b16 a[232:233], v219 offset:33304\n"
"v_mfma_f32_16x16x32_bf16 a[0:3], a[164:167], v[136:139], a[0:3]\n"
"s_waitcnt vmcnt(8)\n"
"ds_read_b64_tr_b16 a[234:235], v219 offset:34328\n"
"v_mfma_f32_16x16x32_bf16 a[4:7], a[160:163], v[132:135], a[4:7]\n"
"s_barrier\n"
"ds_read_b64_tr_b16 a[236:237], v219 offset:37400\n"
"v_mfma_f32_16x16x32_bf16 a[4:7], a[164:167], v[140:143], a[4:7]\n"
"ds_read_b64_tr_b16 a[238:239], v219 offset:38424\n"
"v_mfma_f32_16x16x32_bf16 a[8:11], a[168:171], v[128:131], a[8:11]\n"
"s_add_u32 m0, s86, 65536\n"
"ds_read_b64_tr_b16 a[240:241], v219 offset:33552\n"
"v_mfma_f32_16x16x32_bf16 a[8:11], a[172:175], v[136:139], a[8:11]\n"
"ds_read_b64_tr_b16 a[242:243], v219 offset:34576\n"
"buffer_load_dwordx4 v246, s[8:11], s84 offen offset:0 lds\n"
"v_mfma_f32_16x16x32_bf16 a[12:15], a[168:171], v[132:135], a[12:15]\n"
"ds_read_b64_tr_b16 a[244:245], v219 offset:37648\n"
"v_accvgpr_read_b32 v176, a0\n"
"v_mfma_f32_16x16x32_bf16 a[12:15], a[172:175], v[140:143], a[12:15]\n"
"s_waitcnt lgkmcnt(11)\n"
"ds_read_b64_tr_b16 a[246:247], v219 offset:38672\n"
"v_accvgpr_read_b32 v177, a1\n"
"v_mfma_f32_16x16x32_bf16 a[16:19], a[176:179], v[128:131], a[16:19]\n"
"ds_read_b64_tr_b16 a[248:249], v219 offset:33560\n"
"v_accvgpr_read_b32 v178, a2\n"
"v_mfma_f32_16x16x32_bf16 a[16:19], a[180:183], v[136:139], a[16:19]\n"
"ds_read_b64_tr_b16 a[250:251], v219 offset:34584\n"
"v_accvgpr_read_b32 v179, a3\n"
"v_mfma_f32_16x16x32_bf16 a[20:23], a[176:179], v[132:135], a[20:23]\n"
"ds_read_b64_tr_b16 a[252:253], v219 offset:37656\n"
"v_cvt_pk_bf16_f32 v64, v176, v177\n"
"v_mfma_f32_16x16x32_bf16 a[20:23], a[180:183], v[140:143], a[20:23]\n"
"ds_read_b64_tr_b16 a[254:255], v219 offset:38680\n"
"buffer_load_dwordx4 v246, s[8:11], s84 offen offset:1024 lds\n"
"v_mfma_f32_16x16x32_bf16 a[24:27], a[184:187], v[128:131], a[24:27]\n"
"ds_read_b64_tr_b16 a[160:161], v219 offset:40976\n"
"v_cvt_pk_bf16_f32 v65, v178, v179\n"
"v_mfma_f32_16x16x32_bf16 a[24:27], a[188:191], v[136:139], a[24:27]\n"
"ds_read_b64_tr_b16 a[162:163], v219 offset:42000\n"
"v_accvgpr_read_b32 v180, a4\n"
"v_mfma_f32_16x16x32_bf16 a[28:31], a[184:187], v[132:135], a[28:31]\n"
"ds_read_b64_tr_b16 a[164:165], v219 offset:45072\n"
"v_accvgpr_read_b32 v181, a5\n"
"v_mfma_f32_16x16x32_bf16 a[28:31], a[188:191], v[140:143], a[28:31]\n"
"s_waitcnt lgkmcnt(11)\n"
"ds_read_b64_tr_b16 a[166:167], v219 offset:46096\n"
"v_accvgpr_read_b32 v182, a6\n"
"v_mfma_f32_16x16x32_bf16 a[32:35], a[224:227], v[128:131], a[32:35]\n"
"ds_read_b64_tr_b16 a[168:169], v219 offset:40984\n"
"v_accvgpr_read_b32 v183, a7\n"
"v_mfma_f32_16x16x32_bf16 a[32:35], a[228:231], v[136:139], a[32:35]\n"
"ds_read_b64_tr_b16 a[170:171], v219 offset:42008\n"
"buffer_load_dwordx4 v247, s[8:11], s84 offen offset:2048 lds\n"
"v_mfma_f32_16x16x32_bf16 a[36:39], a[224:227], v[132:135], a[36:39]\n"
"ds_read_b64_tr_b16 a[172:173], v219 offset:45080\n"
"v_cvt_pk_bf16_f32 v96, v180, v181\n"
"v_mfma_f32_16x16x32_bf16 a[36:39], a[228:231], v[140:143], a[36:39]\n"
"ds_read_b64_tr_b16 a[174:175], v219 offset:46104\n"
"v_cvt_pk_bf16_f32 v97, v182, v183\n"
"v_mfma_f32_16x16x32_bf16 a[40:43], a[232:235], v[128:131], a[40:43]\n"
"ds_read_b64_tr_b16 a[176:177], v219 offset:41232\n"
"v_accvgpr_read_b32 v184, a8\n"
"v_mfma_f32_16x16x32_bf16 a[40:43], a[236:239], v[136:139], a[40:43]\n"
"ds_read_b64_tr_b16 a[178:179], v219 offset:42256\n"
"v_accvgpr_read_b32 v185, a9\n"
"v_mfma_f32_16x16x32_bf16 a[44:47], a[232:235], v[132:135], a[44:47]\n"
"ds_read_b64_tr_b16 a[180:181], v219 offset:45328\n"
"v_accvgpr_read_b32 v186, a10\n"
"v_mfma_f32_16x16x32_bf16 a[44:47], a[236:239], v[140:143], a[44:47]\n"
"s_waitcnt lgkmcnt(11)\n"
"ds_read_b64_tr_b16 a[182:183], v219 offset:46352\n"
"buffer_load_dwordx4 v247, s[8:11], s84 offen offset:3072 lds\n"
"v_mfma_f32_16x16x32_bf16 a[48:51], a[240:243], v[128:131], a[48:51]\n"
"ds_read_b64_tr_b16 a[184:185], v219 offset:41240\n"
"v_accvgpr_read_b32 v187, a11\n"
"v_mfma_f32_16x16x32_bf16 a[48:51], a[244:247], v[136:139], a[48:51]\n"
"ds_read_b64_tr_b16 a[186:187], v219 offset:42264\n"
"v_cvt_pk_bf16_f32 v66, v184, v185\n"
"v_mfma_f32_16x16x32_bf16 a[52:55], a[240:243], v[132:135], a[52:55]\n"
"ds_read_b64_tr_b16 a[188:189], v219 offset:45336\n"
"v_cvt_pk_bf16_f32 v67, v186, v187\n"
"v_mfma_f32_16x16x32_bf16 a[52:55], a[244:247], v[140:143], a[52:55]\n"
"ds_read_b64_tr_b16 a[190:191], v219 offset:46360\n"
"v_accvgpr_read_b32 v188, a12\n"
"v_mfma_f32_16x16x32_bf16 a[56:59], a[248:251], v[128:131], a[56:59]\n"
"s_add_u32 m0, s86, 81920\n"
"ds_read_b64_tr_b16 a[224:225], v219 offset:41488\n"
"v_accvgpr_read_b32 v189, a13\n"
"v_mfma_f32_16x16x32_bf16 a[56:59], a[252:255], v[136:139], a[56:59]\n"
"ds_read_b64_tr_b16 a[226:227], v219 offset:42512\n"
"buffer_load_dwordx4 v246, s[12:15], s84 offen offset:0 lds\n"
"v_mfma_f32_16x16x32_bf16 a[60:63], a[248:251], v[132:135], a[60:63]\n"
"ds_read_b64_tr_b16 a[228:229], v219 offset:45584\n"
"v_accvgpr_read_b32 v190, a14\n"
"v_mfma_f32_16x16x32_bf16 a[60:63], a[252:255], v[140:143], a[60:63]\n"
"s_waitcnt lgkmcnt(11)\n"
"ds_read_b64_tr_b16 a[230:231], v219 offset:46608\n"
"v_accvgpr_read_b32 v191, a15\n"
"v_mfma_f32_16x16x32_bf16 a[64:67], a[160:163], v[128:131], a[64:67]\n"
"ds_read_b64_tr_b16 a[232:233], v219 offset:41496\n"
"v_cvt_pk_bf16_f32 v98, v188, v189\n"
"v_mfma_f32_16x16x32_bf16 a[64:67], a[164:167], v[136:139], a[64:67]\n"
"ds_read_b64_tr_b16 a[234:235], v219 offset:42520\n"
"v_cvt_pk_bf16_f32 v99, v190, v191\n"
"v_mfma_f32_16x16x32_bf16 a[68:71], a[160:163], v[132:135], a[68:71]\n"
"ds_read_b64_tr_b16 a[236:237], v219 offset:45592\n"
"v_accvgpr_read_b32 v176, a16\n"
"v_mfma_f32_16x16x32_bf16 a[68:71], a[164:167], v[140:143], a[68:71]\n"
"ds_read_b64_tr_b16 a[238:239], v219 offset:46616\n"
"buffer_load_dwordx4 v247, s[12:15], s84 offen offset:1024 lds\n"
"v_mfma_f32_16x16x32_bf16 a[72:75], a[168:171], v[128:131], a[72:75]\n"
"ds_read_b64_tr_b16 a[240:241], v219 offset:41744\n"
"v_accvgpr_read_b32 v177, a17\n"
"v_mfma_f32_16x16x32_bf16 a[72:75], a[172:175], v[136:139], a[72:75]\n"
"ds_read_b64_tr_b16 a[242:243], v219 offset:42768\n"
"v_accvgpr_read_b32 v178, a18\n"
"v_mfma_f32_16x16x32_bf16 a[76:79], a[168:171], v[132:135], a[76:79]\n"
"ds_read_b64_tr_b16 a[244:245], v219 offset:45840\n"
"v_accvgpr_read_b32 v179, a19\n"
"v_mfma_f32_16x16x32_bf16 a[76:79], a[172:175], v[140:143], a[76:79]\n"
"s_waitcnt lgkmcnt(11)\n"
"ds_read_b64_tr_b16 a[246:247], v219 offset:46864\n"
"v_cvt_pk_bf16_f32 v68, v176, v177\n"
"v_mfma_f32_16x16x32_bf16 a[80:83], a[176:179], v[128:131], a[80:83]\n"
"ds_read_b64_tr_b16 a[248:249], v219 offset:41752\n"
"v_cvt_pk_bf16_f32 v69, v178, v179\n"
"v_mfma_f32_16x16x32_bf16 a[80:83], a[180:183], v[136:139], a[80:83]\n"
"ds_read_b64_tr_b16 a[250:251], v219 offset:42776\n"
"buffer_load_dwordx4 v246, s[12:15], s84 offen offset:2048 lds\n"
"v_mfma_f32_16x16x32_bf16 a[84:87], a[176:179], v[132:135], a[84:87]\n"
"ds_read_b64_tr_b16 a[252:253], v219 offset:45848\n"
"v_accvgpr_read_b32 v180, a20\n"
"v_mfma_f32_16x16x32_bf16 a[84:87], a[180:183], v[140:143], a[84:87]\n"
"ds_read_b64_tr_b16 a[254:255], v219 offset:46872\n"
"v_accvgpr_read_b32 v181, a21\n"
"v_mfma_f32_16x16x32_bf16 v[236:239], a[184:187], v[128:131], v[236:239]\n"
"ds_read_b128 a[160:163], v254 offset:0\n"
"v_accvgpr_read_b32 v182, a22\n"
"v_mfma_f32_16x16x32_bf16 v[236:239], a[188:191], v[136:139], v[236:239]\n"
"ds_read_b128 a[164:167], v254 offset:256\n"
"v_accvgpr_read_b32 v183, a23\n"
"v_mfma_f32_16x16x32_bf16 v[232:235], a[184:187], v[132:135], v[232:235]\n"
"ds_read_b128 a[168:171], v254 offset:512\n"
"v_cvt_pk_bf16_f32 v100, v180, v181\n"
"v_mfma_f32_16x16x32_bf16 v[232:235], a[188:191], v[140:143], v[232:235]\n"
"s_waitcnt lgkmcnt(11)\n"
"buffer_load_dwordx4 v247, s[12:15], s84 offen offset:3072 lds\n"
"ds_read_b128 a[172:175], v254 offset:768\n"
"v_mfma_f32_16x16x32_bf16 v[228:231], a[224:227], v[128:131], v[228:231]\n"
"s_add_u32 s84, s84, 16384\n"
"ds_read_b128 a[128:131], v240 offset:0\n"
"v_cvt_pk_bf16_f32 v101, v182, v183\n"
"v_mfma_f32_16x16x32_bf16 v[228:231], a[228:231], v[136:139], v[228:231]\n"
"ds_read_b128 a[132:135], v240 offset:256\n"
"v_accvgpr_read_b32 v184, a24\n"
"v_mfma_f32_16x16x32_bf16 v[220:223], a[224:227], v[132:135], v[220:223]\n"
"ds_read_b128 a[136:139], v240 offset:512\n"
"v_accvgpr_read_b32 v185, a25\n"
"v_mfma_f32_16x16x32_bf16 v[220:223], a[228:231], v[140:143], v[220:223]\n"
"ds_read_b128 a[140:143], v240 offset:768\n"
"v_accvgpr_read_b32 v186, a26\n"
"v_mfma_f32_16x16x32_bf16 v[212:215], a[232:235], v[128:131], v[212:215]\n"
"ds_read_b128 a[144:147], v240 offset:4096\n"
"v_accvgpr_read_b32 v187, a27\n"
"v_mfma_f32_16x16x32_bf16 v[212:215], a[236:239], v[136:139], v[212:215]\n"
"ds_read_b128 a[148:151], v240 offset:4352\n"
"v_cvt_pk_bf16_f32 v70, v184, v185\n"
"v_mfma_f32_16x16x32_bf16 v[208:211], a[232:235], v[132:135], v[208:211]\n"
"ds_read_b128 a[152:155], v240 offset:4608\n"
"v_cvt_pk_bf16_f32 v71, v186, v187\n"
"v_mfma_f32_16x16x32_bf16 v[208:211], a[236:239], v[140:143], v[208:211]\n"
"s_waitcnt lgkmcnt(11)\n"
"ds_read_b128 a[156:159], v240 offset:4864\n"
"v_accvgpr_read_b32 v188, a28\n"
"v_mfma_f32_16x16x32_bf16 v[204:207], a[240:243], v[128:131], v[204:207]\n"
"ds_read_b64_tr_b16 a[192:193], v242 offset:16384\n"
"v_accvgpr_read_b32 v189, a29\n"
"v_mfma_f32_16x16x32_bf16 v[204:207], a[244:247], v[136:139], v[204:207]\n"
"ds_read_b64_tr_b16 a[194:195], v242 offset:20480\n"
"v_accvgpr_read_b32 v190, a30\n"
"v_mfma_f32_16x16x32_bf16 v[200:203], a[240:243], v[132:135], v[200:203]\n"
"ds_read_b64_tr_b16 a[196:197], v243 offset:16384\n"
"v_accvgpr_read_b32 v191, a31\n"
"v_mfma_f32_16x16x32_bf16 v[200:203], a[244:247], v[140:143], v[200:203]\n"
"ds_read_b64_tr_b16 a[198:199], v243 offset:20480\n"
"v_cvt_pk_bf16_f32 v102, v188, v189\n"
"v_mfma_f32_16x16x32_bf16 v[196:199], a[248:251], v[128:131], v[196:199]\n"
"ds_read_b64_tr_b16 a[200:201], v242 offset:16640\n"
"v_cvt_pk_bf16_f32 v103, v190, v191\n"
"v_mfma_f32_16x16x32_bf16 v[196:199], a[252:255], v[136:139], v[196:199]\n"
"ds_read_b64_tr_b16 a[202:203], v242 offset:20736\n"
"v_accvgpr_read_b32 v176, a32\n"
"v_mfma_f32_16x16x32_bf16 v[192:195], a[248:251], v[132:135], v[192:195]\n"
"ds_read_b64_tr_b16 a[204:205], v243 offset:16640\n"
"v_accvgpr_read_b32 v177, a33\n"
"v_mfma_f32_16x16x32_bf16 v[192:195], a[252:255], v[140:143], v[192:195]\n"
"ds_read_b64_tr_b16 a[206:207], v243 offset:20736\n"
"v_accvgpr_read_b32 v178, a34\n"
"s_barrier\n"
"ds_read_b64_tr_b16 a[208:209], v242 offset:16896\n"
"v_accvgpr_read_b32 v179, a35\n"
"v_cvt_pk_bf16_f32 v72, v176, v177\n"
"v_cvt_pk_bf16_f32 v73, v178, v179\n"
"ds_read_b64_tr_b16 a[210:211], v242 offset:20992\n"
"v_accvgpr_read_b32 v180, a36\n"
"v_accvgpr_read_b32 v181, a37\n"
"v_accvgpr_read_b32 v182, a38\n"
"ds_read_b64_tr_b16 a[212:213], v243 offset:16896\n"
"v_accvgpr_read_b32 v183, a39\n"
"v_cvt_pk_bf16_f32 v104, v180, v181\n"
"v_cvt_pk_bf16_f32 v105, v182, v183\n"
"ds_read_b64_tr_b16 a[214:215], v243 offset:20992\n"
"v_accvgpr_read_b32 v184, a40\n"
"v_accvgpr_read_b32 v185, a41\n"
"v_accvgpr_read_b32 v186, a42\n"
"ds_read_b64_tr_b16 a[216:217], v242 offset:17152\n"
"v_accvgpr_read_b32 v187, a43\n"
"v_cvt_pk_bf16_f32 v74, v184, v185\n"
"v_cvt_pk_bf16_f32 v75, v186, v187\n"
"ds_read_b64_tr_b16 a[218:219], v242 offset:21248\n"
"v_accvgpr_read_b32 v188, a44\n"
"v_accvgpr_read_b32 v189, a45\n"
"v_accvgpr_read_b32 v190, a46\n"
"ds_read_b64_tr_b16 a[220:221], v243 offset:17152\n"
"v_accvgpr_read_b32 v191, a47\n"
"v_cvt_pk_bf16_f32 v106, v188, v189\n"
"v_cvt_pk_bf16_f32 v107, v190, v191\n"
"ds_read_b64_tr_b16 a[222:223], v243 offset:21248\n"
"v_accvgpr_read_b32 v176, a48\n"
"v_accvgpr_read_b32 v177, a49\n"
"v_accvgpr_read_b32 v178, a50\n"
"v_accvgpr_read_b32 v179, a51\n"
"v_cvt_pk_bf16_f32 v76, v176, v177\n"
"v_cvt_pk_bf16_f32 v77, v178, v179\n"
"v_accvgpr_read_b32 v180, a52\n"
"v_accvgpr_read_b32 v181, a53\n"
"v_accvgpr_read_b32 v182, a54\n"
"v_accvgpr_read_b32 v183, a55\n"
"v_cvt_pk_bf16_f32 v108, v180, v181\n"
"v_cvt_pk_bf16_f32 v109, v182, v183\n"
"v_accvgpr_read_b32 v184, a56\n"
"v_accvgpr_read_b32 v185, a57\n"
"v_accvgpr_read_b32 v186, a58\n"
"v_accvgpr_read_b32 v187, a59\n"
"v_cvt_pk_bf16_f32 v78, v184, v185\n"
"v_cvt_pk_bf16_f32 v79, v186, v187\n"
"v_accvgpr_read_b32 v188, a60\n"
"v_accvgpr_read_b32 v189, a61\n"
"v_accvgpr_read_b32 v190, a62\n"
"v_accvgpr_read_b32 v191, a63\n"
"v_cvt_pk_bf16_f32 v110, v188, v189\n"
"v_cvt_pk_bf16_f32 v111, v190, v191\n"
"v_accvgpr_read_b32 v176, a64\n"
"v_accvgpr_read_b32 v177, a65\n"
"v_accvgpr_read_b32 v178, a66\n"
"s_nop 1\n"
"v_accvgpr_read_b32 v179, a67\n"
"v_cvt_pk_bf16_f32 v80, v176, v177\n"
"v_cvt_pk_bf16_f32 v81, v178, v179\n"
"v_accvgpr_read_b32 v180, a68\n"
"s_waitcnt lgkmcnt(14)\n"
"v_mfma_f32_16x16x32_bf16 v[144:147], a[160:163], v[64:67], 0\n"
"v_accvgpr_read_b32 v181, a69\n"
"v_accvgpr_read_b32 v182, a70\n"
"v_mfma_f32_16x16x32_bf16 v[144:147], a[164:167], v[68:71], v[144:147]\n"
"v_accvgpr_read_b32 v183, a71\n"
"v_cvt_pk_bf16_f32 v112, v180, v181\n"
"v_mfma_f32_16x16x32_bf16 v[144:147], a[168:171], v[72:75], v[144:147]\n"
"v_cvt_pk_bf16_f32 v113, v182, v183\n"
"v_accvgpr_read_b32 v184, a72\n"
"v_mfma_f32_16x16x32_bf16 v[144:147], a[172:175], v[76:79], v[144:147]\n"
"v_accvgpr_read_b32 v185, a73\n"
"v_accvgpr_read_b32 v186, a74\n"
"v_mfma_f32_16x16x32_bf16 v[152:155], a[160:163], v[96:99], 0\n"
"v_accvgpr_read_b32 v187, a75\n"
"v_cvt_pk_bf16_f32 v82, v184, v185\n"
"v_mfma_f32_16x16x32_bf16 v[152:155], a[164:167], v[100:103], v[152:155]\n"
"v_cvt_pk_bf16_f32 v83, v186, v187\n"
"v_accvgpr_read_b32 v188, a76\n"
"v_mfma_f32_16x16x32_bf16 v[152:155], a[168:171], v[104:107], v[152:155]\n"
"v_accvgpr_read_b32 v189, a77\n"
"v_accvgpr_read_b32 v190, a78\n"
"v_mfma_f32_16x16x32_bf16 v[152:155], a[172:175], v[108:111], v[152:155]\n"
"v_accvgpr_read_b32 v191, a79\n"
"v_cvt_pk_bf16_f32 v114, v188, v189\n"
"v_cvt_pk_bf16_f32 v115, v190, v191\n"
"v_accvgpr_read_b32 v176, a80\n"
"v_accvgpr_read_b32 v177, a81\n"
"v_accvgpr_read_b32 v178, a82\n"
"v_accvgpr_read_b32 v179, a83\n"
"v_cvt_pk_bf16_f32 v84, v176, v177\n"
"v_cvt_pk_bf16_f32 v85, v178, v179\n"
"v_accvgpr_read_b32 v180, a84\n"
"v_accvgpr_read_b32 v181, a85\n"
"v_accvgpr_read_b32 v182, a86\n"
"v_accvgpr_read_b32 v183, a87\n"
"v_cvt_pk_bf16_f32 v116, v180, v181\n"
"v_cvt_pk_bf16_f32 v117, v182, v183\n"
"v_cvt_pk_bf16_f32 v86, v236, v237\n"
"v_cvt_pk_bf16_f32 v87, v238, v239\n"
"v_cvt_pk_bf16_f32 v118, v232, v233\n"
"v_cvt_pk_bf16_f32 v119, v234, v235\n"
"v_cvt_pk_bf16_f32 v88, v228, v229\n"
"v_cvt_pk_bf16_f32 v89, v230, v231\n"
"v_cvt_pk_bf16_f32 v120, v220, v221\n"
"v_cvt_pk_bf16_f32 v121, v222, v223\n"
"v_cvt_pk_bf16_f32 v90, v212, v213\n"
"v_cvt_pk_bf16_f32 v91, v214, v215\n"
"v_cvt_pk_bf16_f32 v122, v208, v209\n"
"v_cvt_pk_bf16_f32 v123, v210, v211\n"
"v_cvt_pk_bf16_f32 v92, v204, v205\n"
"v_cvt_pk_bf16_f32 v93, v206, v207\n"
"v_cvt_pk_bf16_f32 v124, v200, v201\n"
"v_cvt_pk_bf16_f32 v125, v202, v203\n"
"v_cvt_pk_bf16_f32 v94, v196, v197\n"
"v_cvt_pk_bf16_f32 v95, v198, v199\n"
"v_cvt_pk_bf16_f32 v126, v192, v193\n"
"v_cvt_pk_bf16_f32 v127, v194, v195\n"
"v_max3_f32 v144, v144, v145, v146\n"
"v_max_f32 v144, v144, v147\n"
"v_sub_f32 v24, 0xc2480000, v144\n"
"v_sub_f32 v25, 0xc2480000, v144\n"
"v_sub_f32 v26, 0xc2480000, v144\n"
"v_sub_f32 v27, 0xc2480000, v144\n"
"s_nop 1\n"
"v_mfma_f32_16x16x32_bf16 v[148:151], a[160:163], v[80:83], 0\n"
"v_max3_f32 v152, v152, v153, v154\n"
"v_max_f32 v152, v152, v155\n"
"v_mfma_f32_16x16x32_bf16 v[148:151], a[164:167], v[84:87], v[148:151]\n"
"v_sub_f32 v32, 0xc2480000, v152\n"
"v_sub_f32 v33, 0xc2480000, v152\n"
"v_mfma_f32_16x16x32_bf16 v[148:151], a[168:171], v[88:91], v[148:151]\n"
"v_sub_f32 v34, 0xc2480000, v152\n"
"v_sub_f32 v35, 0xc2480000, v152\n"
"v_mfma_f32_16x16x32_bf16 v[148:151], a[172:175], v[92:95], v[148:151]\n"
"v_mov_b32 v40, 0x3f803f80\n"
"v_mov_b32 v41, 0x3f803f80\n"
"v_mfma_f32_16x16x32_bf16 v[156:159], a[160:163], v[112:115], 0\n"
"v_mov_b32 v42, 0x3f803f80\n"
"v_mov_b32 v43, 0x3f803f80\n"
"v_mfma_f32_16x16x32_bf16 v[156:159], a[164:167], v[116:119], v[156:159]\n"
"v_mov_b32 v20, 0\n"
"v_mov_b32 v21, 0\n"
"v_mfma_f32_16x16x32_bf16 v[156:159], a[168:171], v[120:123], v[156:159]\n"
"v_mov_b32 v22, 0\n"
"v_mov_b32 v23, 0\n"
"v_mfma_f32_16x16x32_bf16 v[156:159], a[172:175], v[124:127], v[156:159]\n"
"v_mov_b32 v44, 0\n"
"v_mov_b32 v52, 0\n"
"v_mov_b32 v216, -1\n"
"v_mov_b32 v217, -1\n"
"v_mov_b32 v218, -1\n"
"v_mov_b32 v219, -1\n"
"v_lshl_add_u32 v255, v248, 6, s48\n"
"v_lshlrev_b32 v207, 4, v248\n"
"v_add_u32 v208, 18432, v207\n"
"s_waitcnt lgkmcnt(0)\n"
"AT_main:\n"
"s_or_b32 s56, s42, s41\n"
"s_cmp_lg_u32 s56, 0\n"
"s_cbranch_scc1 AT_h1\n"
"v_lshlrev_b32 v214, 7, v248\n"
"s_mov_b32 s57, 32768\n"
"s_mov_b32 s58, 40960\n"
"s_mov_b32 s59, 49152\n"
"s_mov_b32 s60, 57344\n"
"buffer_load_dword v210, v214, s[28:31], s57 offen offset:4 sc1\n"
"buffer_load_dword v211, v214, s[28:31], s58 offen offset:4 sc1\n"
"buffer_load_dword v212, v214, s[28:31], s59 offen offset:4 sc1\n"
"buffer_load_dword v213, v214, s[28:31], s60 offen offset:4 sc1\n"
"AT_h1:\n"
"v_mfma_f32_16x16x32_bf16 v[0:3], a[128:131], v[64:67], v[24:27]\n"
"s_nop 0\n"
"ds_read_b128 a[160:163], v240 offset:8192\n"
"v_mfma_f32_16x16x32_bf16 v[0:3], a[132:135], v[68:71], v[0:3]\n"
"s_add_u32 m0, s86, 98304\n"
"v_mfma_f32_16x16x32_bf16 v[0:3], a[136:139], v[72:75], v[0:3]\n"
"s_nop 0\n"
"buffer_load_dwordx4 v246, s[8:11], s84 offen offset:0 lds\n"
"ds_read_b128 a[164:167], v240 offset:8448\n"
"v_mfma_f32_16x16x32_bf16 v[0:3], a[140:143], v[76:79], v[0:3]\n"
"s_nop 0\n"
"v_mfma_f32_16x16x32_bf16 v[4:7], a[144:147], v[64:67], v[24:27]\n"
"ds_read_b128 a[168:171], v240 offset:8704\n"
"v_max3_f32 v148, v148, v149, v150\n"
"v_mfma_f32_16x16x32_bf16 v[4:7], a[148:151], v[68:71], v[4:7]\n"
"v_max_f32 v148, v148, v151\n"
"v_sub_f32 v28, 0xc2480000, v148\n"
"v_mfma_f32_16x16x32_bf16 v[4:7], a[152:155], v[72:75], v[4:7]\n"
"ds_read_b128 a[172:175], v240 offset:8960\n"
"v_sub_f32 v29, 0xc2480000, v148\n"
"v_mfma_f32_16x16x32_bf16 v[4:7], a[156:159], v[76:79], v[4:7]\n"
"v_exp_f32 v0, v0\n"
"v_sub_f32 v30, 0xc2480000, v148\n"
"v_mfma_f32_16x16x32_bf16 a[96:99], v[40:43], v[20:23], 0\n"
"buffer_load_dwordx4 v246, s[8:11], s84 offen offset:1024 lds\n"
"v_sub_f32 v31, 0xc2480000, v148\n"
"v_mfma_f32_16x16x32_bf16 a[100:103], v[40:43], v[20:23], 0\n"
"v_exp_f32 v1, v1\n"
"v_max3_f32 v156, v156, v157, v158\n"
"v_mfma_f32_16x16x32_bf16 a[104:107], v[40:43], v[20:23], 0\n"
"ds_read_b128 a[176:179], v240 offset:12288\n"
"v_cvt_pk_bf16_f32 v16, v0, v1\n"
"v_max_f32 v156, v156, v159\n"
"v_mfma_f32_16x16x32_bf16 a[108:111], v[40:43], v[20:23], 0\n"
"v_exp_f32 v2, v2\n"
"v_sub_f32 v36, 0xc2480000, v156\n"
"v_mfma_f32_16x16x32_bf16 a[112:115], v[40:43], v[20:23], 0\n"
"ds_read_b128 a[180:183], v240 offset:12544\n"
"v_add_f32 v44, v44, v0\n"
"v_sub_f32 v37, 0xc2480000, v156\n"
"v_mfma_f32_16x16x32_bf16 a[116:119], v[40:43], v[20:23], 0\n"
"v_exp_f32 v3, v3\n"
"v_sub_f32 v38, 0xc2480000, v156\n"
"v_mfma_f32_16x16x32_bf16 a[120:123], v[40:43], v[20:23], 0\n"
"ds_read_b128 a[184:187], v240 offset:12800\n"
"v_cvt_pk_bf16_f32 v17, v2, v3\n"
"v_sub_f32 v39, 0xc2480000, v156\n"
"v_mfma_f32_16x16x32_bf16 a[124:127], v[40:43], v[20:23], 0\n"
"s_nop 0\n"
"v_exp_f32 v4, v4\n"
"v_mfma_f32_16x16x32_bf16 v[56:59], v[40:43], v[20:23], 0\n"
"buffer_load_dwordx4 v247, s[8:11], s84 offen offset:2048 lds\n"
"ds_read_b128 a[188:191], v240 offset:13056\n"
"v_add_f32 v44, v44, v1\n"
"v_mfma_f32_16x16x32_bf16 v[8:11], a[128:131], v[80:83], v[28:31]\n"
"s_nop 0\n"
"v_exp_f32 v5, v5\n"
"v_mfma_f32_16x16x32_bf16 v[8:11], a[132:135], v[84:87], v[8:11]\n"
"ds_read_b64_tr_b16 a[224:225], v242 offset:24576\n"
"v_cvt_pk_bf16_f32 v18, v4, v5\n"
"v_mfma_f32_16x16x32_bf16 v[8:11], a[136:139], v[88:91], v[8:11]\n"
"s_nop 0\n"
"v_exp_f32 v6, v6\n"
"v_mfma_f32_16x16x32_bf16 v[8:11], a[140:143], v[92:95], v[8:11]\n"
"ds_read_b64_tr_b16 a[226:227], v242 offset:28672\n"
"v_add_f32 v44, v44, v2\n"
"v_mfma_f32_16x16x32_bf16 v[12:15], a[144:147], v[80:83], v[28:31]\n"
"s_nop 0\n"
"v_exp_f32 v7, v7\n"
"v_mfma_f32_16x16x32_bf16 v[12:15], a[148:151], v[84:87], v[12:15]\n"
"ds_read_b64_tr_b16 a[228:229], v243 offset:24576\n"
"v_cvt_pk_bf16_f32 v19, v6, v7\n"
"v_mfma_f32_16x16x32_bf16 v[12:15], a[152:155], v[88:91], v[12:15]\n"
"s_nop 0\n"
"buffer_load_dwordx4 v247, s[8:11], s84 offen offset:3072 lds\n"
"v_mfma_f32_16x16x32_bf16 v[12:15], a[156:159], v[92:95], v[12:15]\n"
"s_nop 0\n"
"v_exp_f32 v8, v8\n"
"v_mfma_f32_16x16x32_bf16 a[0:3], a[192:195], v[16:19], 0\n"
"ds_read_b64_tr_b16 a[230:231], v243 offset:28672\n"
"v_add_f32 v44, v44, v3\n"
"v_mfma_f32_16x16x32_bf16 a[4:7], a[196:199], v[16:19], 0\n"
"s_nop 0\n"
"v_exp_f32 v9, v9\n"
"v_mfma_f32_16x16x32_bf16 a[8:11], a[200:203], v[16:19], 0\n"
"ds_read_b64_tr_b16 a[232:233], v242 offset:24832\n"
"v_cvt_pk_bf16_f32 v20, v8, v9\n"
"v_mfma_f32_16x16x32_bf16 a[12:15], a[204:207], v[16:19], 0\n"
"s_nop 0\n"
"v_exp_f32 v10, v10\n"
"v_mfma_f32_16x16x32_bf16 a[16:19], a[208:211], v[16:19], 0\n"
"ds_read_b64_tr_b16 a[234:235], v242 offset:28928\n"
"v_add_f32 v44, v44, v4\n"
"v_mfma_f32_16x16x32_bf16 a[20:23], a[212:215], v[16:19], 0\n"
"s_add_u32 m0, s86, 114688\n"
"v_exp_f32 v11, v11\n"
"v_mfma_f32_16x16x32_bf16 a[24:27], a[216:219], v[16:19], 0\n"
"buffer_load_dwordx4 v246, s[12:15], s84 offen offset:0 lds\n"
"ds_read_b64_tr_b16 a[236:237], v243 offset:24832\n"
"v_cvt_pk_bf16_f32 v21, v10, v11\n"
"v_mfma_f32_16x16x32_bf16 a[28:31], a[220:223], v[16:19], 0\n"
"s_nop 0\n"
"v_exp_f32 v12, v12\n"
"v_mfma_f32_16x16x32_bf16 v[220:223], a[128:131], v[96:99], v[32:35]\n"
"ds_read_b64_tr_b16 a[238:239], v243 offset:28928\n"
"v_add_f32 v44, v44, v5\n"
"v_mfma_f32_16x16x32_bf16 v[220:223], a[132:135], v[100:103], v[220:223]\n"
"s_nop 0\n"
"v_exp_f32 v13, v13\n"
"v_mfma_f32_16x16x32_bf16 v[220:223], a[136:139], v[104:107], v[220:223]\n"
"ds_read_b64_tr_b16 a[240:241], v242 offset:25088\n"
"v_cvt_pk_bf16_f32 v22, v12, v13\n"
"v_mfma_f32_16x16x32_bf16 v[220:223], a[140:143], v[108:111], v[220:223]\n"
"s_nop 0\n"
"v_exp_f32 v14, v14\n"
"v_mfma_f32_16x16x32_bf16 v[224:227], a[144:147], v[96:99], v[32:35]\n"
"ds_read_b64_tr_b16 a[242:243], v242 offset:29184\n"
"v_add_f32 v44, v44, v6\n"
"v_mfma_f32_16x16x32_bf16 v[224:227], a[148:151], v[100:103], v[224:227]\n"
"s_nop 0\n"
"v_exp_f32 v15, v15\n"
"v_mfma_f32_16x16x32_bf16 v[224:227], a[152:155], v[104:107], v[224:227]\n"
"ds_read_b64_tr_b16 a[244:245], v243 offset:25088\n"
"v_cvt_pk_bf16_f32 v23, v14, v15\n"
"v_mfma_f32_16x16x32_bf16 v[224:227], a[156:159], v[108:111], v[224:227]\n"
"s_nop 0\n"
"v_exp_f32 v220, v220\n"
"v_mfma_f32_16x16x32_bf16 a[32:35], a[192:195], v[20:23], 0\n"
"s_nop 0\n"
"buffer_load_dwordx4 v247, s[12:15], s84 offen offset:1024 lds\n"
"v_mfma_f32_16x16x32_bf16 a[36:39], a[196:199], v[20:23], 0\n"
"s_nop 0\n"
"v_exp_f32 v221, v221\n"
"v_mfma_f32_16x16x32_bf16 a[40:43], a[200:203], v[20:23], 0\n"
"ds_read_b64_tr_b16 a[246:247], v243 offset:29184\n"
"v_cvt_pk_bf16_f32 v16, v220, v221\n"
"v_mfma_f32_16x16x32_bf16 a[44:47], a[204:207], v[20:23], 0\n"
"s_nop 0\n"
"v_exp_f32 v222, v222\n"
"v_mfma_f32_16x16x32_bf16 a[48:51], a[208:211], v[20:23], 0\n"
"ds_read_b64_tr_b16 a[248:249], v242 offset:25344\n"
"v_add_f32 v44, v44, v7\n"
"v_mfma_f32_16x16x32_bf16 a[52:55], a[212:215], v[20:23], 0\n"
"s_nop 0\n"
"v_exp_f32 v223, v223\n"
"v_mfma_f32_16x16x32_bf16 a[56:59], a[216:219], v[20:23], 0\n"
"ds_read_b64_tr_b16 a[250:251], v242 offset:29440\n"
"v_cvt_pk_bf16_f32 v17, v222, v223\n"
"v_mfma_f32_16x16x32_bf16 a[60:63], a[220:223], v[20:23], 0\n"
"s_nop 0\n"
"v_exp_f32 v224, v224\n"
"v_mfma_f32_16x16x32_bf16 v[48:51], v[40:43], v[20:23], 0\n"
"buffer_load_dwordx4 v246, s[12:15], s84 offen offset:2048 lds\n"
"ds_read_b64_tr_b16 a[252:253], v243 offset:25344\n"
"v_add_f32 v52, v52, v220\n"
"v_mfma_f32_16x16x32_bf16 v[228:231], a[128:131], v[112:115], v[36:39]\n"
"s_nop 0\n"
"v_exp_f32 v225, v225\n"
"v_mfma_f32_16x16x32_bf16 v[228:231], a[132:135], v[116:119], v[228:231]\n"
"ds_read_b64_tr_b16 a[254:255], v243 offset:29440\n"
"v_cvt_pk_bf16_f32 v18, v224, v225\n"
"v_mfma_f32_16x16x32_bf16 v[228:231], a[136:139], v[120:123], v[228:231]\n"
"s_nop 0\n"
"v_exp_f32 v226, v226\n"
"v_mfma_f32_16x16x32_bf16 v[228:231], a[140:143], v[124:127], v[228:231]\n"
"v_add_f32 v52, v52, v221\n"
"v_add_f32 v52, v52, v222\n"
"v_mfma_f32_16x16x32_bf16 v[232:235], a[144:147], v[112:115], v[36:39]\n"
"s_nop 0\n"
"v_exp_f32 v227, v227\n"
"v_mfma_f32_16x16x32_bf16 v[232:235], a[148:151], v[116:119], v[232:235]\n"
"v_cvt_pk_bf16_f32 v19, v226, v227\n"
"v_add_f32 v52, v52, v223\n"
"v_mfma_f32_16x16x32_bf16 v[232:235], a[152:155], v[120:123], v[232:235]\n"
"s_nop 0\n"
"buffer_load_dwordx4 v247, s[12:15], s84 offen offset:3072 lds\n"
"v_mfma_f32_16x16x32_bf16 v[232:235], a[156:159], v[124:127], v[232:235]\n"
"s_add_u32 s84, s84, 16384\n"
"v_exp_f32 v228, v228\n"
"v_mfma_f32_16x16x32_bf16 a[64:67], a[192:195], v[16:19], 0\n"
"v_add_f32 v52, v52, v224\n"
"v_add_f32 v52, v52, v225\n"
"v_mfma_f32_16x16x32_bf16 a[68:71], a[196:199], v[16:19], 0\n"
"s_nop 0\n"
"v_exp_f32 v229, v229\n"
"v_mfma_f32_16x16x32_bf16 a[72:75], a[200:203], v[16:19], 0\n"
"v_cvt_pk_bf16_f32 v20, v228, v229\n"
"v_add_f32 v52, v52, v226\n"
"v_mfma_f32_16x16x32_bf16 a[76:79], a[204:207], v[16:19], 0\n"
"s_waitcnt vmcnt(16) lgkmcnt(0)\n"
"v_exp_f32 v230, v230\n"
"v_mfma_f32_16x16x32_bf16 a[80:83], a[208:211], v[16:19], 0\n"
"s_barrier\n"
"v_mfma_f32_16x16x32_bf16 a[84:87], a[212:215], v[16:19], 0\n"
"s_nop 0\n"
"v_exp_f32 v231, v231\n"
"v_mfma_f32_16x16x32_bf16 a[88:91], a[216:219], v[16:19], 0\n"
"v_cvt_pk_bf16_f32 v21, v230, v231\n"
"v_add_f32 v52, v52, v227\n"
"v_mfma_f32_16x16x32_bf16 a[92:95], a[220:223], v[16:19], 0\n"
"s_nop 0\n"
"v_exp_f32 v232, v232\n"
"s_mov_b32 s87, 6\n"
".p2align 6\n"
"AT_loop:\n"
"v_mfma_f32_16x16x32_bf16 v[0:3], a[160:163], v[64:67], v[24:27]\n"
"ds_read_b128 a[128:131], v240 offset:32768\n"
"buffer_load_dword v250, v251, s[64:67], 0 offen\n"
"v_mfma_f32_16x16x32_bf16 v[0:3], a[164:167], v[68:71], v[0:3]\n"
"v_exp_f32 v233, v233\n"
"v_mfma_f32_16x16x32_bf16 v[0:3], a[168:171], v[72:75], v[0:3]\n"
"ds_read_b128 a[132:135], v240 offset:33024\n"
"v_cvt_pk_bf16_f32 v22, v232, v233\n"
"v_mfma_f32_16x16x32_bf16 v[0:3], a[172:175], v[76:79], v[0:3]\n"
"v_exp_f32 v234, v234\n"
"v_mfma_f32_16x16x32_bf16 v[4:7], a[176:179], v[64:67], v[24:27]\n"
"ds_read_b128 a[136:139], v240 offset:33280\n"
"v_mfma_f32_16x16x32_bf16 v[4:7], a[180:183], v[68:71], v[4:7]\n"
"v_exp_f32 v235, v235\n"
"v_mfma_f32_16x16x32_bf16 v[4:7], a[184:187], v[72:75], v[4:7]\n"
"ds_read_b128 a[140:143], v240 offset:33536\n"
"v_cvt_pk_bf16_f32 v23, v234, v235\n"
"v_mfma_f32_16x16x32_bf16 v[4:7], a[188:191], v[76:79], v[4:7]\n"
"s_mov_b32 m0, s86\n"
"v_exp_f32 v0, v0\n"
"v_mfma_f32_16x16x32_bf16 a[96:99], a[192:195], v[20:23], a[96:99]\n"
"buffer_load_dwordx4 v246, s[8:11], s84 offen offset:0 lds\n"
"v_mfma_f32_16x16x32_bf16 a[100:103], a[196:199], v[20:23], a[100:103]\n"
"v_exp_f32 v1, v1\n"
"v_mfma_f32_16x16x32_bf16 a[104:107], a[200:203], v[20:23], a[104:107]\n"
"ds_read_b128 a[144:147], v240 offset:36864\n"
"v_cvt_pk_bf16_f32 v16, v0, v1\n"
"v_mfma_f32_16x16x32_bf16 a[108:111], a[204:207], v[20:23], a[108:111]\n"
"v_exp_f32 v2, v2\n"
"v_mfma_f32_16x16x32_bf16 a[112:115], a[208:211], v[20:23], a[112:115]\n"
"ds_read_b128 a[148:151], v240 offset:37120\n"
"v_add_f32 v44, v44, v0\n"
"v_mfma_f32_16x16x32_bf16 a[116:119], a[212:215], v[20:23], a[116:119]\n"
"v_exp_f32 v3, v3\n"
"v_mfma_f32_16x16x32_bf16 a[120:123], a[216:219], v[20:23], a[120:123]\n"
"ds_read_b128 a[152:155], v240 offset:37376\n"
"v_cvt_pk_bf16_f32 v17, v2, v3\n"
"v_mfma_f32_16x16x32_bf16 a[124:127], a[220:223], v[20:23], a[124:127]\n"
"v_exp_f32 v4, v4\n"
"v_mfma_f32_16x16x32_bf16 v[56:59], v[40:43], v[20:23], v[56:59]\n"
"ds_read_b128 a[156:159], v240 offset:37632\n"
"v_add_f32 v44, v44, v1\n"
"v_mfma_f32_16x16x32_bf16 v[8:11], a[160:163], v[80:83], v[28:31]\n"
"v_exp_f32 v5, v5\n"
"v_mfma_f32_16x16x32_bf16 v[8:11], a[164:167], v[84:87], v[8:11]\n"
"ds_read_b64_tr_b16 a[192:193], v242 offset:49152\n"
"v_cvt_pk_bf16_f32 v18, v4, v5\n"
"v_mfma_f32_16x16x32_bf16 v[8:11], a[168:171], v[88:91], v[8:11]\n"
"v_exp_f32 v6, v6\n"
"v_mfma_f32_16x16x32_bf16 v[8:11], a[172:175], v[92:95], v[8:11]\n"
"ds_read_b64_tr_b16 a[194:195], v242 offset:53248\n"
"v_add_f32 v44, v44, v2\n"
"v_mfma_f32_16x16x32_bf16 v[12:15], a[176:179], v[80:83], v[28:31]\n"
"v_exp_f32 v7, v7\n"
"v_mfma_f32_16x16x32_bf16 v[12:15], a[180:183], v[84:87], v[12:15]\n"
"ds_read_b64_tr_b16 a[196:197], v243 offset:49152\n"
"v_cvt_pk_bf16_f32 v19, v6, v7\n"
"v_mfma_f32_16x16x32_bf16 v[12:15], a[184:187], v[88:91], v[12:15]\n"
"buffer_load_dwordx4 v246, s[8:11], s84 offen offset:1024 lds\n"
"v_mfma_f32_16x16x32_bf16 v[12:15], a[188:191], v[92:95], v[12:15]\n"
"s_waitcnt lgkmcnt(11)\n"
"v_exp_f32 v8, v8\n"
"v_mfma_f32_16x16x32_bf16 a[0:3], a[224:227], v[16:19], a[0:3]\n"
"ds_read_b64_tr_b16 a[198:199], v243 offset:53248\n"
"v_add_f32 v44, v44, v3\n"
"v_mfma_f32_16x16x32_bf16 a[4:7], a[228:231], v[16:19], a[4:7]\n"
"v_exp_f32 v9, v9\n"
"v_mfma_f32_16x16x32_bf16 a[8:11], a[232:235], v[16:19], a[8:11]\n"
"ds_read_b64_tr_b16 a[200:201], v242 offset:49408\n"
"v_cvt_pk_bf16_f32 v20, v8, v9\n"
"v_mfma_f32_16x16x32_bf16 a[12:15], a[236:239], v[16:19], a[12:15]\n"
"v_exp_f32 v10, v10\n"
"v_mfma_f32_16x16x32_bf16 a[16:19], a[240:243], v[16:19], a[16:19]\n"
"ds_read_b64_tr_b16 a[202:203], v242 offset:53504\n"
"v_add_f32 v44, v44, v4\n"
"v_mfma_f32_16x16x32_bf16 a[20:23], a[244:247], v[16:19], a[20:23]\n"
"v_exp_f32 v11, v11\n"
"v_mfma_f32_16x16x32_bf16 a[24:27], a[248:251], v[16:19], a[24:27]\n"
"ds_read_b64_tr_b16 a[204:205], v243 offset:49408\n"
"v_cvt_pk_bf16_f32 v21, v10, v11\n"
"v_mfma_f32_16x16x32_bf16 a[28:31], a[252:255], v[16:19], a[28:31]\n"
"v_exp_f32 v12, v12\n"
"v_mfma_f32_16x16x32_bf16 v[220:223], a[160:163], v[96:99], v[32:35]\n"
"ds_read_b64_tr_b16 a[206:207], v243 offset:53504\n"
"v_add_f32 v44, v44, v5\n"
"v_mfma_f32_16x16x32_bf16 v[220:223], a[164:167], v[100:103], v[220:223]\n"
"v_exp_f32 v13, v13\n"
"v_mfma_f32_16x16x32_bf16 v[220:223], a[168:171], v[104:107], v[220:223]\n"
"ds_read_b64_tr_b16 a[208:209], v242 offset:49664\n"
"v_cvt_pk_bf16_f32 v22, v12, v13\n"
"v_mfma_f32_16x16x32_bf16 v[220:223], a[172:175], v[108:111], v[220:223]\n"
"v_exp_f32 v14, v14\n"
"v_mfma_f32_16x16x32_bf16 v[224:227], a[176:179], v[96:99], v[32:35]\n"
"ds_read_b64_tr_b16 a[210:211], v242 offset:53760\n"
"v_add_f32 v44, v44, v6\n"
"v_mfma_f32_16x16x32_bf16 v[224:227], a[180:183], v[100:103], v[224:227]\n"
"v_exp_f32 v15, v15\n"
"v_mfma_f32_16x16x32_bf16 v[224:227], a[184:187], v[104:107], v[224:227]\n"
"ds_read_b64_tr_b16 a[212:213], v243 offset:49664\n"
"v_cvt_pk_bf16_f32 v23, v14, v15\n"
"v_mfma_f32_16x16x32_bf16 v[224:227], a[188:191], v[108:111], v[224:227]\n"
"v_exp_f32 v220, v220\n"
"v_mfma_f32_16x16x32_bf16 a[32:35], a[224:227], v[20:23], a[32:35]\n"
"buffer_load_dwordx4 v247, s[8:11], s84 offen offset:2048 lds\n"
"v_mfma_f32_16x16x32_bf16 a[36:39], a[228:231], v[20:23], a[36:39]\n"
"v_exp_f32 v221, v221\n"
"v_mfma_f32_16x16x32_bf16 a[40:43], a[232:235], v[20:23], a[40:43]\n"
"ds_read_b64_tr_b16 a[214:215], v243 offset:53760\n"
"v_cvt_pk_bf16_f32 v16, v220, v221\n"
"v_mfma_f32_16x16x32_bf16 a[44:47], a[236:239], v[20:23], a[44:47]\n"
"v_exp_f32 v222, v222\n"
"v_mfma_f32_16x16x32_bf16 a[48:51], a[240:243], v[20:23], a[48:51]\n"
"ds_read_b64_tr_b16 a[216:217], v242 offset:49920\n"
"v_add_f32 v44, v44, v7\n"
"v_mfma_f32_16x16x32_bf16 a[52:55], a[244:247], v[20:23], a[52:55]\n"
"v_exp_f32 v223, v223\n"
"v_mfma_f32_16x16x32_bf16 a[56:59], a[248:251], v[20:23], a[56:59]\n"
"ds_read_b64_tr_b16 a[218:219], v242 offset:54016\n"
"v_cvt_pk_bf16_f32 v17, v222, v223\n"
"v_mfma_f32_16x16x32_bf16 a[60:63], a[252:255], v[20:23], a[60:63]\n"
"v_exp_f32 v224, v224\n"
"v_mfma_f32_16x16x32_bf16 v[48:51], v[40:43], v[20:23], v[48:51]\n"
"ds_read_b64_tr_b16 a[220:221], v243 offset:49920\n"
"v_add_f32 v52, v52, v220\n"
"v_mfma_f32_16x16x32_bf16 v[228:231], a[160:163], v[112:115], v[36:39]\n"
"v_exp_f32 v225, v225\n"
"v_mfma_f32_16x16x32_bf16 v[228:231], a[164:167], v[116:119], v[228:231]\n"
"ds_read_b64_tr_b16 a[222:223], v243 offset:54016\n"
"v_cvt_pk_bf16_f32 v18, v224, v225\n"
"v_mfma_f32_16x16x32_bf16 v[228:231], a[168:171], v[120:123], v[228:231]\n"
"v_exp_f32 v226, v226\n"
"v_mfma_f32_16x16x32_bf16 v[228:231], a[172:175], v[124:127], v[228:231]\n"
"v_add_f32 v52, v52, v221\n"
"v_add_f32 v52, v52, v222\n"
"v_mfma_f32_16x16x32_bf16 v[232:235], a[176:179], v[112:115], v[36:39]\n"
"v_exp_f32 v227, v227\n"
"v_mfma_f32_16x16x32_bf16 v[232:235], a[180:183], v[116:119], v[232:235]\n"
"v_cvt_pk_bf16_f32 v19, v226, v227\n"
"v_add_f32 v52, v52, v223\n"
"v_mfma_f32_16x16x32_bf16 v[232:235], a[184:187], v[120:123], v[232:235]\n"
"buffer_load_dwordx4 v247, s[8:11], s84 offen offset:3072 lds\n"
"v_mfma_f32_16x16x32_bf16 v[232:235], a[188:191], v[124:127], v[232:235]\n"
"v_exp_f32 v228, v228\n"
"v_mfma_f32_16x16x32_bf16 a[64:67], a[224:227], v[16:19], a[64:67]\n"
"v_add_f32 v52, v52, v224\n"
"v_add_f32 v52, v52, v225\n"
"v_mfma_f32_16x16x32_bf16 a[68:71], a[228:231], v[16:19], a[68:71]\n"
"v_exp_f32 v229, v229\n"
"v_mfma_f32_16x16x32_bf16 a[72:75], a[232:235], v[16:19], a[72:75]\n"
"v_cvt_pk_bf16_f32 v20, v228, v229\n"
"v_add_f32 v52, v52, v226\n"
"v_mfma_f32_16x16x32_bf16 a[76:79], a[236:239], v[16:19], a[76:79]\n"
"v_exp_f32 v230, v230\n"
"v_mfma_f32_16x16x32_bf16 a[80:83], a[240:243], v[16:19], a[80:83]\n"
"s_waitcnt lgkmcnt(8)\n"
"buffer_load_dword v250, v255, s[32:35], 0 offen\n"
"v_mfma_f32_16x16x32_bf16 a[84:87], a[244:247], v[16:19], a[84:87]\n"
"v_exp_f32 v231, v231\n"
"v_mfma_f32_16x16x32_bf16 a[88:91], a[248:251], v[16:19], a[88:91]\n"
"v_cvt_pk_bf16_f32 v21, v230, v231\n"
"v_add_f32 v52, v52, v227\n"
"v_mfma_f32_16x16x32_bf16 a[92:95], a[252:255], v[16:19], a[92:95]\n"
"v_exp_f32 v232, v232\n"
"v_mfma_f32_16x16x32_bf16 v[0:3], a[128:131], v[64:67], v[24:27]\n"
"ds_read_b128 a[160:163], v240 offset:40960\n"
"v_mfma_f32_16x16x32_bf16 v[0:3], a[132:135], v[68:71], v[0:3]\n"
"v_exp_f32 v233, v233\n"
"v_mfma_f32_16x16x32_bf16 v[0:3], a[136:139], v[72:75], v[0:3]\n"
"ds_read_b128 a[164:167], v240 offset:41216\n"
"v_cvt_pk_bf16_f32 v22, v232, v233\n"
"v_mfma_f32_16x16x32_bf16 v[0:3], a[140:143], v[76:79], v[0:3]\n"
"v_exp_f32 v234, v234\n"
"v_mfma_f32_16x16x32_bf16 v[4:7], a[144:147], v[64:67], v[24:27]\n"
"ds_read_b128 a[168:171], v240 offset:41472\n"
"v_mfma_f32_16x16x32_bf16 v[4:7], a[148:151], v[68:71], v[4:7]\n"
"v_exp_f32 v235, v235\n"
"v_mfma_f32_16x16x32_bf16 v[4:7], a[152:155], v[72:75], v[4:7]\n"
"ds_read_b128 a[172:175], v240 offset:41728\n"
"v_cvt_pk_bf16_f32 v23, v234, v235\n"
"v_mfma_f32_16x16x32_bf16 v[4:7], a[156:159], v[76:79], v[4:7]\n"
"s_add_u32 m0, s86, 16384\n"
"v_exp_f32 v0, v0\n"
"v_mfma_f32_16x16x32_bf16 a[96:99], a[224:227], v[20:23], a[96:99]\n"
"buffer_load_dwordx4 v246, s[12:15], s84 offen offset:0 lds\n"
"v_mfma_f32_16x16x32_bf16 a[100:103], a[228:231], v[20:23], a[100:103]\n"
"v_exp_f32 v1, v1\n"
"v_mfma_f32_16x16x32_bf16 a[104:107], a[232:235], v[20:23], a[104:107]\n"
"ds_read_b128 a[176:179], v240 offset:45056\n"
"v_cvt_pk_bf16_f32 v16, v0, v1\n"
"v_mfma_f32_16x16x32_bf16 a[108:111], a[236:239], v[20:23], a[108:111]\n"
"v_exp_f32 v2, v2\n"
"v_mfma_f32_16x16x32_bf16 a[112:115], a[240:243], v[20:23], a[112:115]\n"
"ds_read_b128 a[180:183], v240 offset:45312\n"
"v_add_f32 v44, v44, v0\n"
"v_mfma_f32_16x16x32_bf16 a[116:119], a[244:247], v[20:23], a[116:119]\n"
"v_exp_f32 v3, v3\n"
"v_mfma_f32_16x16x32_bf16 a[120:123], a[248:251], v[20:23], a[120:123]\n"
"ds_read_b128 a[184:187], v240 offset:45568\n"
"v_cvt_pk_bf16_f32 v17, v2, v3\n"
"v_mfma_f32_16x16x32_bf16 a[124:127], a[252:255], v[20:23], a[124:127]\n"
"v_exp_f32 v4, v4\n"
"v_mfma_f32_16x16x32_bf16 v[56:59], v[40:43], v[20:23], v[56:59]\n"
"ds_read_b128 a[188:191], v240 offset:45824\n"
"v_add_f32 v44, v44, v1\n"
"v_mfma_f32_16x16x32_bf16 v[8:11], a[128:131], v[80:83], v[28:31]\n"
"v_exp_f32 v5, v5\n"
"v_mfma_f32_16x16x32_bf16 v[8:11], a[132:135], v[84:87], v[8:11]\n"
"ds_read_b64_tr_b16 a[224:225], v242 offset:57344\n"
"v_cvt_pk_bf16_f32 v18, v4, v5\n"
"v_mfma_f32_16x16x32_bf16 v[8:11], a[136:139], v[88:91], v[8:11]\n"
"v_exp_f32 v6, v6\n"
"v_mfma_f32_16x16x32_bf16 v[8:11], a[140:143], v[92:95], v[8:11]\n"
"ds_read_b64_tr_b16 a[226:227], v242 offset:61440\n"
"v_add_f32 v44, v44, v2\n"
"v_mfma_f32_16x16x32_bf16 v[12:15], a[144:147], v[80:83], v[28:31]\n"
"v_exp_f32 v7, v7\n"
"v_mfma_f32_16x16x32_bf16 v[12:15], a[148:151], v[84:87], v[12:15]\n"
"ds_read_b64_tr_b16 a[228:229], v243 offset:57344\n"
"v_cvt_pk_bf16_f32 v19, v6, v7\n"
"v_mfma_f32_16x16x32_bf16 v[12:15], a[152:155], v[88:91], v[12:15]\n"
"buffer_load_dwordx4 v247, s[12:15], s84 offen offset:1024 lds\n"
"v_mfma_f32_16x16x32_bf16 v[12:15], a[156:159], v[92:95], v[12:15]\n"
"s_waitcnt lgkmcnt(11)\n"
"v_exp_f32 v8, v8\n"
"v_mfma_f32_16x16x32_bf16 a[0:3], a[192:195], v[16:19], a[0:3]\n"
"ds_read_b64_tr_b16 a[230:231], v243 offset:61440\n"
"v_add_f32 v44, v44, v3\n"
"v_mfma_f32_16x16x32_bf16 a[4:7], a[196:199], v[16:19], a[4:7]\n"
"v_exp_f32 v9, v9\n"
"v_mfma_f32_16x16x32_bf16 a[8:11], a[200:203], v[16:19], a[8:11]\n"
"ds_read_b64_tr_b16 a[232:233], v242 offset:57600\n"
"v_cvt_pk_bf16_f32 v20, v8, v9\n"
"v_mfma_f32_16x16x32_bf16 a[12:15], a[204:207], v[16:19], a[12:15]\n"
"v_exp_f32 v10, v10\n"
"v_mfma_f32_16x16x32_bf16 a[16:19], a[208:211], v[16:19], a[16:19]\n"
"ds_read_b64_tr_b16 a[234:235], v242 offset:61696\n"
"v_add_f32 v44, v44, v4\n"
"v_mfma_f32_16x16x32_bf16 a[20:23], a[212:215], v[16:19], a[20:23]\n"
"v_exp_f32 v11, v11\n"
"v_mfma_f32_16x16x32_bf16 a[24:27], a[216:219], v[16:19], a[24:27]\n"
"ds_read_b64_tr_b16 a[236:237], v243 offset:57600\n"
"v_cvt_pk_bf16_f32 v21, v10, v11\n"
"v_mfma_f32_16x16x32_bf16 a[28:31], a[220:223], v[16:19], a[28:31]\n"
"v_exp_f32 v12, v12\n"
"v_mfma_f32_16x16x32_bf16 v[220:223], a[128:131], v[96:99], v[32:35]\n"
"ds_read_b64_tr_b16 a[238:239], v243 offset:61696\n"
"v_add_f32 v44, v44, v5\n"
"v_mfma_f32_16x16x32_bf16 v[220:223], a[132:135], v[100:103], v[220:223]\n"
"v_exp_f32 v13, v13\n"
"v_mfma_f32_16x16x32_bf16 v[220:223], a[136:139], v[104:107], v[220:223]\n"
"ds_read_b64_tr_b16 a[240:241], v242 offset:57856\n"
"v_cvt_pk_bf16_f32 v22, v12, v13\n"
"v_mfma_f32_16x16x32_bf16 v[220:223], a[140:143], v[108:111], v[220:223]\n"
"v_exp_f32 v14, v14\n"
"v_mfma_f32_16x16x32_bf16 v[224:227], a[144:147], v[96:99], v[32:35]\n"
"ds_read_b64_tr_b16 a[242:243], v242 offset:61952\n"
"v_add_f32 v44, v44, v6\n"
"v_mfma_f32_16x16x32_bf16 v[224:227], a[148:151], v[100:103], v[224:227]\n"
"v_exp_f32 v15, v15\n"
"v_mfma_f32_16x16x32_bf16 v[224:227], a[152:155], v[104:107], v[224:227]\n"
"ds_read_b64_tr_b16 a[244:245], v243 offset:57856\n"
"v_cvt_pk_bf16_f32 v23, v14, v15\n"
"v_mfma_f32_16x16x32_bf16 v[224:227], a[156:159], v[108:111], v[224:227]\n"
"v_exp_f32 v220, v220\n"
"v_mfma_f32_16x16x32_bf16 a[32:35], a[192:195], v[20:23], a[32:35]\n"
"buffer_load_dwordx4 v246, s[12:15], s84 offen offset:2048 lds\n"
"v_mfma_f32_16x16x32_bf16 a[36:39], a[196:199], v[20:23], a[36:39]\n"
"v_exp_f32 v221, v221\n"
"v_mfma_f32_16x16x32_bf16 a[40:43], a[200:203], v[20:23], a[40:43]\n"
"ds_read_b64_tr_b16 a[246:247], v243 offset:61952\n"
"v_cvt_pk_bf16_f32 v16, v220, v221\n"
"v_mfma_f32_16x16x32_bf16 a[44:47], a[204:207], v[20:23], a[44:47]\n"
"v_exp_f32 v222, v222\n"
"v_mfma_f32_16x16x32_bf16 a[48:51], a[208:211], v[20:23], a[48:51]\n"
"ds_read_b64_tr_b16 a[248:249], v242 offset:58112\n"
"v_add_f32 v44, v44, v7\n"
"v_mfma_f32_16x16x32_bf16 a[52:55], a[212:215], v[20:23], a[52:55]\n"
"v_exp_f32 v223, v223\n"
"v_mfma_f32_16x16x32_bf16 a[56:59], a[216:219], v[20:23], a[56:59]\n"
"ds_read_b64_tr_b16 a[250:251], v242 offset:62208\n"
"v_cvt_pk_bf16_f32 v17, v222, v223\n"
"v_mfma_f32_16x16x32_bf16 a[60:63], a[220:223], v[20:23], a[60:63]\n"
"v_exp_f32 v224, v224\n"
"v_mfma_f32_16x16x32_bf16 v[48:51], v[40:43], v[20:23], v[48:51]\n"
"ds_read_b64_tr_b16 a[252:253], v243 offset:58112\n"
"v_add_f32 v52, v52, v220\n"
"v_mfma_f32_16x16x32_bf16 v[228:231], a[128:131], v[112:115], v[36:39]\n"
"v_exp_f32 v225, v225\n"
"v_mfma_f32_16x16x32_bf16 v[228:231], a[132:135], v[116:119], v[228:231]\n"
"ds_read_b64_tr_b16 a[254:255], v243 offset:62208\n"
"v_cvt_pk_bf16_f32 v18, v224, v225\n"
"v_mfma_f32_16x16x32_bf16 v[228:231], a[136:139], v[120:123], v[228:231]\n"
"v_exp_f32 v226, v226\n"
"v_mfma_f32_16x16x32_bf16 v[228:231], a[140:143], v[124:127], v[228:231]\n"
"v_add_f32 v52, v52, v221\n"
"v_add_f32 v52, v52, v222\n"
"v_mfma_f32_16x16x32_bf16 v[232:235], a[144:147], v[112:115], v[36:39]\n"
"v_exp_f32 v227, v227\n"
"v_mfma_f32_16x16x32_bf16 v[232:235], a[148:151], v[116:119], v[232:235]\n"
"v_cvt_pk_bf16_f32 v19, v226, v227\n"
"v_add_f32 v52, v52, v223\n"
"v_mfma_f32_16x16x32_bf16 v[232:235], a[152:155], v[120:123], v[232:235]\n"
"buffer_load_dwordx4 v247, s[12:15], s84 offen offset:3072 lds\n"
"v_mfma_f32_16x16x32_bf16 v[232:235], a[156:159], v[124:127], v[232:235]\n"
"s_add_u32 s84, s84, 16384\n"
"v_exp_f32 v228, v228\n"
"v_mfma_f32_16x16x32_bf16 a[64:67], a[192:195], v[16:19], a[64:67]\n"
"v_add_f32 v52, v52, v224\n"
"v_add_f32 v52, v52, v225\n"
"v_mfma_f32_16x16x32_bf16 a[68:71], a[196:199], v[16:19], a[68:71]\n"
"v_exp_f32 v229, v229\n"
"v_mfma_f32_16x16x32_bf16 a[72:75], a[200:203], v[16:19], a[72:75]\n"
"v_cvt_pk_bf16_f32 v20, v228, v229\n"
"v_add_f32 v52, v52, v226\n"
"v_mfma_f32_16x16x32_bf16 a[76:79], a[204:207], v[16:19], a[76:79]\n"
"s_waitcnt vmcnt(18) lgkmcnt(0)\n"
"v_exp_f32 v230, v230\n"
"v_mfma_f32_16x16x32_bf16 a[80:83], a[208:211], v[16:19], a[80:83]\n"
"s_barrier\n"
"v_mfma_f32_16x16x32_bf16 a[84:87], a[212:215], v[16:19], a[84:87]\n"
"v_exp_f32 v231, v231\n"
"v_mfma_f32_16x16x32_bf16 a[88:91], a[216:219], v[16:19], a[88:91]\n"
"v_cvt_pk_bf16_f32 v21, v230, v231\n"
"v_add_f32 v52, v52, v227\n"
"v_mfma_f32_16x16x32_bf16 a[92:95], a[220:223], v[16:19], a[92:95]\n"
"v_exp_f32 v232, v232\n"
"v_mfma_f32_16x16x32_bf16 v[0:3], a[160:163], v[64:67], v[24:27]\n"
"ds_read_b128 a[128:131], v241 offset:0\n"
"buffer_store_dwordx4 v[216:219], v207, s[28:31], s54 offen sc1\n"
"v_mfma_f32_16x16x32_bf16 v[0:3], a[164:167], v[68:71], v[0:3]\n"
"v_exp_f32 v233, v233\n"
"v_mfma_f32_16x16x32_bf16 v[0:3], a[168:171], v[72:75], v[0:3]\n"
"ds_read_b128 a[132:135], v241 offset:256\n"
"v_cvt_pk_bf16_f32 v22, v232, v233\n"
"v_mfma_f32_16x16x32_bf16 v[0:3], a[172:175], v[76:79], v[0:3]\n"
"v_exp_f32 v234, v234\n"
"v_mfma_f32_16x16x32_bf16 v[4:7], a[176:179], v[64:67], v[24:27]\n"
"ds_read_b128 a[136:139], v241 offset:512\n"
"v_mfma_f32_16x16x32_bf16 v[4:7], a[180:183], v[68:71], v[4:7]\n"
"v_exp_f32 v235, v235\n"
"v_mfma_f32_16x16x32_bf16 v[4:7], a[184:187], v[72:75], v[4:7]\n"
"ds_read_b128 a[140:143], v241 offset:768\n"
"v_cvt_pk_bf16_f32 v23, v234, v235\n"
"v_mfma_f32_16x16x32_bf16 v[4:7], a[188:191], v[76:79], v[4:7]\n"
"s_add_u32 m0, s86, 32768\n"
"v_exp_f32 v0, v0\n"
"v_mfma_f32_16x16x32_bf16 a[96:99], a[192:195], v[20:23], a[96:99]\n"
"buffer_load_dwordx4 v246, s[8:11], s84 offen offset:0 lds\n"
"v_mfma_f32_16x16x32_bf16 a[100:103], a[196:199], v[20:23], a[100:103]\n"
"v_exp_f32 v1, v1\n"
"v_mfma_f32_16x16x32_bf16 a[104:107], a[200:203], v[20:23], a[104:107]\n"
"ds_read_b128 a[144:147], v241 offset:4096\n"
"v_cvt_pk_bf16_f32 v16, v0, v1\n"
"v_mfma_f32_16x16x32_bf16 a[108:111], a[204:207], v[20:23], a[108:111]\n"
"v_exp_f32 v2, v2\n"
"v_mfma_f32_16x16x32_bf16 a[112:115], a[208:211], v[20:23], a[112:115]\n"
"ds_read_b128 a[148:151], v241 offset:4352\n"
"v_add_f32 v44, v44, v0\n"
"v_mfma_f32_16x16x32_bf16 a[116:119], a[212:215], v[20:23], a[116:119]\n"
"v_exp_f32 v3, v3\n"
"v_mfma_f32_16x16x32_bf16 a[120:123], a[216:219], v[20:23], a[120:123]\n"
"ds_read_b128 a[152:155], v241 offset:4608\n"
"v_cvt_pk_bf16_f32 v17, v2, v3\n"
"v_mfma_f32_16x16x32_bf16 a[124:127], a[220:223], v[20:23], a[124:127]\n"
"v_exp_f32 v4, v4\n"
"v_mfma_f32_16x16x32_bf16 v[56:59], v[40:43], v[20:23], v[56:59]\n"
"ds_read_b128 a[156:159], v241 offset:4864\n"
"v_add_f32 v44, v44, v1\n"
"v_mfma_f32_16x16x32_bf16 v[8:11], a[160:163], v[80:83], v[28:31]\n"
"v_exp_f32 v5, v5\n"
"v_mfma_f32_16x16x32_bf16 v[8:11], a[164:167], v[84:87], v[8:11]\n"
"ds_read_b64_tr_b16 a[192:193], v244 offset:16384\n"
"v_cvt_pk_bf16_f32 v18, v4, v5\n"
"v_mfma_f32_16x16x32_bf16 v[8:11], a[168:171], v[88:91], v[8:11]\n"
"v_exp_f32 v6, v6\n"
"v_mfma_f32_16x16x32_bf16 v[8:11], a[172:175], v[92:95], v[8:11]\n"
"ds_read_b64_tr_b16 a[194:195], v244 offset:20480\n"
"v_add_f32 v44, v44, v2\n"
"v_mfma_f32_16x16x32_bf16 v[12:15], a[176:179], v[80:83], v[28:31]\n"
"v_exp_f32 v7, v7\n"
"v_mfma_f32_16x16x32_bf16 v[12:15], a[180:183], v[84:87], v[12:15]\n"
"ds_read_b64_tr_b16 a[196:197], v245 offset:16384\n"
"v_cvt_pk_bf16_f32 v19, v6, v7\n"
"v_mfma_f32_16x16x32_bf16 v[12:15], a[184:187], v[88:91], v[12:15]\n"
"buffer_load_dwordx4 v246, s[8:11], s84 offen offset:1024 lds\n"
"v_mfma_f32_16x16x32_bf16 v[12:15], a[188:191], v[92:95], v[12:15]\n"
"s_waitcnt lgkmcnt(11)\n"
"v_exp_f32 v8, v8\n"
"v_mfma_f32_16x16x32_bf16 a[0:3], a[224:227], v[16:19], a[0:3]\n"
"ds_read_b64_tr_b16 a[198:199], v245 offset:20480\n"
"v_add_f32 v44, v44, v3\n"
"v_mfma_f32_16x16x32_bf16 a[4:7], a[228:231], v[16:19], a[4:7]\n"
"v_exp_f32 v9, v9\n"
"v_mfma_f32_16x16x32_bf16 a[8:11], a[232:235], v[16:19], a[8:11]\n"
"ds_read_b64_tr_b16 a[200:201], v244 offset:16640\n"
"v_cvt_pk_bf16_f32 v20, v8, v9\n"
"v_mfma_f32_16x16x32_bf16 a[12:15], a[236:239], v[16:19], a[12:15]\n"
"v_exp_f32 v10, v10\n"
"v_mfma_f32_16x16x32_bf16 a[16:19], a[240:243], v[16:19], a[16:19]\n"
"ds_read_b64_tr_b16 a[202:203], v244 offset:20736\n"
"v_add_f32 v44, v44, v4\n"
"v_mfma_f32_16x16x32_bf16 a[20:23], a[244:247], v[16:19], a[20:23]\n"
"v_exp_f32 v11, v11\n"
"v_mfma_f32_16x16x32_bf16 a[24:27], a[248:251], v[16:19], a[24:27]\n"
"ds_read_b64_tr_b16 a[204:205], v245 offset:16640\n"
"v_cvt_pk_bf16_f32 v21, v10, v11\n"
"v_mfma_f32_16x16x32_bf16 a[28:31], a[252:255], v[16:19], a[28:31]\n"
"v_exp_f32 v12, v12\n"
"v_mfma_f32_16x16x32_bf16 v[220:223], a[160:163], v[96:99], v[32:35]\n"
"ds_read_b64_tr_b16 a[206:207], v245 offset:20736\n"
"v_add_f32 v44, v44, v5\n"
"v_mfma_f32_16x16x32_bf16 v[220:223], a[164:167], v[100:103], v[220:223]\n"
"v_exp_f32 v13, v13\n"
"v_mfma_f32_16x16x32_bf16 v[220:223], a[168:171], v[104:107], v[220:223]\n"
"ds_read_b64_tr_b16 a[208:209], v244 offset:16896\n"
"v_cvt_pk_bf16_f32 v22, v12, v13\n"
"v_mfma_f32_16x16x32_bf16 v[220:223], a[172:175], v[108:111], v[220:223]\n"
"v_exp_f32 v14, v14\n"
"v_mfma_f32_16x16x32_bf16 v[224:227], a[176:179], v[96:99], v[32:35]\n"
"ds_read_b64_tr_b16 a[210:211], v244 offset:20992\n"
"v_add_f32 v44, v44, v6\n"
"v_mfma_f32_16x16x32_bf16 v[224:227], a[180:183], v[100:103], v[224:227]\n"
"v_exp_f32 v15, v15\n"
"v_mfma_f32_16x16x32_bf16 v[224:227], a[184:187], v[104:107], v[224:227]\n"
"ds_read_b64_tr_b16 a[212:213], v245 offset:16896\n"
"v_cvt_pk_bf16_f32 v23, v14, v15\n"
"v_mfma_f32_16x16x32_bf16 v[224:227], a[188:191], v[108:111], v[224:227]\n"
"v_exp_f32 v220, v220\n"
"v_mfma_f32_16x16x32_bf16 a[32:35], a[224:227], v[20:23], a[32:35]\n"
"buffer_load_dwordx4 v247, s[8:11], s84 offen offset:2048 lds\n"
"v_mfma_f32_16x16x32_bf16 a[36:39], a[228:231], v[20:23], a[36:39]\n"
"v_exp_f32 v221, v221\n"
"v_mfma_f32_16x16x32_bf16 a[40:43], a[232:235], v[20:23], a[40:43]\n"
"ds_read_b64_tr_b16 a[214:215], v245 offset:20992\n"
"v_cvt_pk_bf16_f32 v16, v220, v221\n"
"v_mfma_f32_16x16x32_bf16 a[44:47], a[236:239], v[20:23], a[44:47]\n"
"v_exp_f32 v222, v222\n"
"v_mfma_f32_16x16x32_bf16 a[48:51], a[240:243], v[20:23], a[48:51]\n"
"ds_read_b64_tr_b16 a[216:217], v244 offset:17152\n"
"v_add_f32 v44, v44, v7\n"
"v_mfma_f32_16x16x32_bf16 a[52:55], a[244:247], v[20:23], a[52:55]\n"
"v_exp_f32 v223, v223\n"
"v_mfma_f32_16x16x32_bf16 a[56:59], a[248:251], v[20:23], a[56:59]\n"
"ds_read_b64_tr_b16 a[218:219], v244 offset:21248\n"
"v_cvt_pk_bf16_f32 v17, v222, v223\n"
"v_mfma_f32_16x16x32_bf16 a[60:63], a[252:255], v[20:23], a[60:63]\n"
"v_exp_f32 v224, v224\n"
"v_mfma_f32_16x16x32_bf16 v[48:51], v[40:43], v[20:23], v[48:51]\n"
"ds_read_b64_tr_b16 a[220:221], v245 offset:17152\n"
"v_add_f32 v52, v52, v220\n"
"v_mfma_f32_16x16x32_bf16 v[228:231], a[160:163], v[112:115], v[36:39]\n"
"v_exp_f32 v225, v225\n"
"v_mfma_f32_16x16x32_bf16 v[228:231], a[164:167], v[116:119], v[228:231]\n"
"ds_read_b64_tr_b16 a[222:223], v245 offset:21248\n"
"v_cvt_pk_bf16_f32 v18, v224, v225\n"
"v_mfma_f32_16x16x32_bf16 v[228:231], a[168:171], v[120:123], v[228:231]\n"
"v_exp_f32 v226, v226\n"
"v_mfma_f32_16x16x32_bf16 v[228:231], a[172:175], v[124:127], v[228:231]\n"
"v_add_f32 v52, v52, v221\n"
"v_add_f32 v52, v52, v222\n"
"v_mfma_f32_16x16x32_bf16 v[232:235], a[176:179], v[112:115], v[36:39]\n"
"v_exp_f32 v227, v227\n"
"v_mfma_f32_16x16x32_bf16 v[232:235], a[180:183], v[116:119], v[232:235]\n"
"v_cvt_pk_bf16_f32 v19, v226, v227\n"
"v_add_f32 v52, v52, v223\n"
"v_mfma_f32_16x16x32_bf16 v[232:235], a[184:187], v[120:123], v[232:235]\n"
"buffer_load_dwordx4 v247, s[8:11], s84 offen offset:3072 lds\n"
"v_mfma_f32_16x16x32_bf16 v[232:235], a[188:191], v[124:127], v[232:235]\n"
"v_exp_f32 v228, v228\n"
"v_mfma_f32_16x16x32_bf16 a[64:67], a[224:227], v[16:19], a[64:67]\n"
"v_add_f32 v52, v52, v224\n"
"v_add_f32 v52, v52, v225\n"
"v_mfma_f32_16x16x32_bf16 a[68:71], a[228:231], v[16:19], a[68:71]\n"
"v_exp_f32 v229, v229\n"
"v_mfma_f32_16x16x32_bf16 a[72:75], a[232:235], v[16:19], a[72:75]\n"
"v_cvt_pk_bf16_f32 v20, v228, v229\n"
"v_add_f32 v52, v52, v226\n"
"v_mfma_f32_16x16x32_bf16 a[76:79], a[236:239], v[16:19], a[76:79]\n"
"v_exp_f32 v230, v230\n"
"v_mfma_f32_16x16x32_bf16 a[80:83], a[240:243], v[16:19], a[80:83]\n"
"s_waitcnt lgkmcnt(8)\n"
"buffer_load_dword v250, v251, s[68:71], 0 offen\n"
"v_mfma_f32_16x16x32_bf16 a[84:87], a[244:247], v[16:19], a[84:87]\n"
"v_exp_f32 v231, v231\n"
"v_mfma_f32_16x16x32_bf16 a[88:91], a[248:251], v[16:19], a[88:91]\n"
"v_cvt_pk_bf16_f32 v21, v230, v231\n"
"v_add_f32 v52, v52, v227\n"
"v_mfma_f32_16x16x32_bf16 a[92:95], a[252:255], v[16:19], a[92:95]\n"
"v_exp_f32 v232, v232\n"
"v_mfma_f32_16x16x32_bf16 v[0:3], a[128:131], v[64:67], v[24:27]\n"
"ds_read_b128 a[160:163], v241 offset:8192\n"
"v_add_u32 v251, 1179648, v251\n"
"v_mfma_f32_16x16x32_bf16 v[0:3], a[132:135], v[68:71], v[0:3]\n"
"v_exp_f32 v233, v233\n"
"v_mfma_f32_16x16x32_bf16 v[0:3], a[136:139], v[72:75], v[0:3]\n"
"ds_read_b128 a[164:167], v241 offset:8448\n"
"v_cvt_pk_bf16_f32 v22, v232, v233\n"
"v_mfma_f32_16x16x32_bf16 v[0:3], a[140:143], v[76:79], v[0:3]\n"
"v_exp_f32 v234, v234\n"
"v_mfma_f32_16x16x32_bf16 v[4:7], a[144:147], v[64:67], v[24:27]\n"
"ds_read_b128 a[168:171], v241 offset:8704\n"
"v_mfma_f32_16x16x32_bf16 v[4:7], a[148:151], v[68:71], v[4:7]\n"
"v_exp_f32 v235, v235\n"
"v_mfma_f32_16x16x32_bf16 v[4:7], a[152:155], v[72:75], v[4:7]\n"
"ds_read_b128 a[172:175], v241 offset:8960\n"
"v_cvt_pk_bf16_f32 v23, v234, v235\n"
"v_mfma_f32_16x16x32_bf16 v[4:7], a[156:159], v[76:79], v[4:7]\n"
"s_add_u32 m0, s86, 49152\n"
"v_exp_f32 v0, v0\n"
"v_mfma_f32_16x16x32_bf16 a[96:99], a[224:227], v[20:23], a[96:99]\n"
"buffer_load_dwordx4 v246, s[12:15], s84 offen offset:0 lds\n"
"v_mfma_f32_16x16x32_bf16 a[100:103], a[228:231], v[20:23], a[100:103]\n"
"v_exp_f32 v1, v1\n"
"v_mfma_f32_16x16x32_bf16 a[104:107], a[232:235], v[20:23], a[104:107]\n"
"ds_read_b128 a[176:179], v241 offset:12288\n"
"v_cvt_pk_bf16_f32 v16, v0, v1\n"
"v_mfma_f32_16x16x32_bf16 a[108:111], a[236:239], v[20:23], a[108:111]\n"
"v_exp_f32 v2, v2\n"
"v_mfma_f32_16x16x32_bf16 a[112:115], a[240:243], v[20:23], a[112:115]\n"
"ds_read_b128 a[180:183], v241 offset:12544\n"
"v_add_f32 v44, v44, v0\n"
"v_mfma_f32_16x16x32_bf16 a[116:119], a[244:247], v[20:23], a[116:119]\n"
"v_exp_f32 v3, v3\n"
"v_mfma_f32_16x16x32_bf16 a[120:123], a[248:251], v[20:23], a[120:123]\n"
"ds_read_b128 a[184:187], v241 offset:12800\n"
"v_cvt_pk_bf16_f32 v17, v2, v3\n"
"v_mfma_f32_16x16x32_bf16 a[124:127], a[252:255], v[20:23], a[124:127]\n"
"v_exp_f32 v4, v4\n"
"v_mfma_f32_16x16x32_bf16 v[56:59], v[40:43], v[20:23], v[56:59]\n"
"ds_read_b128 a[188:191], v241 offset:13056\n"
"v_add_f32 v44, v44, v1\n"
"v_mfma_f32_16x16x32_bf16 v[8:11], a[128:131], v[80:83], v[28:31]\n"
"v_exp_f32 v5, v5\n"
"v_mfma_f32_16x16x32_bf16 v[8:11], a[132:135], v[84:87], v[8:11]\n"
"ds_read_b64_tr_b16 a[224:225], v244 offset:24576\n"
"v_cvt_pk_bf16_f32 v18, v4, v5\n"
"v_mfma_f32_16x16x32_bf16 v[8:11], a[136:139], v[88:91], v[8:11]\n"
"v_exp_f32 v6, v6\n"
"v_mfma_f32_16x16x32_bf16 v[8:11], a[140:143], v[92:95], v[8:11]\n"
"ds_read_b64_tr_b16 a[226:227], v244 offset:28672\n"
"v_add_f32 v44, v44, v2\n"
"v_mfma_f32_16x16x32_bf16 v[12:15], a[144:147], v[80:83], v[28:31]\n"
"v_exp_f32 v7, v7\n"
"v_mfma_f32_16x16x32_bf16 v[12:15], a[148:151], v[84:87], v[12:15]\n"
"ds_read_b64_tr_b16 a[228:229], v245 offset:24576\n"
"v_cvt_pk_bf16_f32 v19, v6, v7\n"
"v_mfma_f32_16x16x32_bf16 v[12:15], a[152:155], v[88:91], v[12:15]\n"
"buffer_load_dwordx4 v247, s[12:15], s84 offen offset:1024 lds\n"
"v_mfma_f32_16x16x32_bf16 v[12:15], a[156:159], v[92:95], v[12:15]\n"
"s_waitcnt lgkmcnt(11)\n"
"v_exp_f32 v8, v8\n"
"v_mfma_f32_16x16x32_bf16 a[0:3], a[192:195], v[16:19], a[0:3]\n"
"ds_read_b64_tr_b16 a[230:231], v245 offset:28672\n"
"v_add_f32 v44, v44, v3\n"
"v_mfma_f32_16x16x32_bf16 a[4:7], a[196:199], v[16:19], a[4:7]\n"
"v_exp_f32 v9, v9\n"
"v_mfma_f32_16x16x32_bf16 a[8:11], a[200:203], v[16:19], a[8:11]\n"
"ds_read_b64_tr_b16 a[232:233], v244 offset:24832\n"
"v_cvt_pk_bf16_f32 v20, v8, v9\n"
"v_mfma_f32_16x16x32_bf16 a[12:15], a[204:207], v[16:19], a[12:15]\n"
"v_exp_f32 v10, v10\n"
"v_mfma_f32_16x16x32_bf16 a[16:19], a[208:211], v[16:19], a[16:19]\n"
"ds_read_b64_tr_b16 a[234:235], v244 offset:28928\n"
"v_add_f32 v44, v44, v4\n"
"v_mfma_f32_16x16x32_bf16 a[20:23], a[212:215], v[16:19], a[20:23]\n"
"v_exp_f32 v11, v11\n"
"v_mfma_f32_16x16x32_bf16 a[24:27], a[216:219], v[16:19], a[24:27]\n"
"ds_read_b64_tr_b16 a[236:237], v245 offset:24832\n"
"v_cvt_pk_bf16_f32 v21, v10, v11\n"
"v_mfma_f32_16x16x32_bf16 a[28:31], a[220:223], v[16:19], a[28:31]\n"
"v_exp_f32 v12, v12\n"
"v_mfma_f32_16x16x32_bf16 v[220:223], a[128:131], v[96:99], v[32:35]\n"
"ds_read_b64_tr_b16 a[238:239], v245 offset:28928\n"
"v_add_f32 v44, v44, v5\n"
"v_mfma_f32_16x16x32_bf16 v[220:223], a[132:135], v[100:103], v[220:223]\n"
"v_exp_f32 v13, v13\n"
"v_mfma_f32_16x16x32_bf16 v[220:223], a[136:139], v[104:107], v[220:223]\n"
"ds_read_b64_tr_b16 a[240:241], v244 offset:25088\n"
"v_cvt_pk_bf16_f32 v22, v12, v13\n"
"v_mfma_f32_16x16x32_bf16 v[220:223], a[140:143], v[108:111], v[220:223]\n"
"v_exp_f32 v14, v14\n"
"v_mfma_f32_16x16x32_bf16 v[224:227], a[144:147], v[96:99], v[32:35]\n"
"ds_read_b64_tr_b16 a[242:243], v244 offset:29184\n"
"v_add_f32 v44, v44, v6\n"
"v_mfma_f32_16x16x32_bf16 v[224:227], a[148:151], v[100:103], v[224:227]\n"
"v_exp_f32 v15, v15\n"
"v_mfma_f32_16x16x32_bf16 v[224:227], a[152:155], v[104:107], v[224:227]\n"
"ds_read_b64_tr_b16 a[244:245], v245 offset:25088\n"
"v_cvt_pk_bf16_f32 v23, v14, v15\n"
"v_mfma_f32_16x16x32_bf16 v[224:227], a[156:159], v[108:111], v[224:227]\n"
"v_exp_f32 v220, v220\n"
"v_mfma_f32_16x16x32_bf16 a[32:35], a[192:195], v[20:23], a[32:35]\n"
"buffer_load_dwordx4 v246, s[12:15], s84 offen offset:2048 lds\n"
"v_mfma_f32_16x16x32_bf16 a[36:39], a[196:199], v[20:23], a[36:39]\n"
"v_exp_f32 v221, v221\n"
"v_mfma_f32_16x16x32_bf16 a[40:43], a[200:203], v[20:23], a[40:43]\n"
"ds_read_b64_tr_b16 a[246:247], v245 offset:29184\n"
"v_cvt_pk_bf16_f32 v16, v220, v221\n"
"v_mfma_f32_16x16x32_bf16 a[44:47], a[204:207], v[20:23], a[44:47]\n"
"v_exp_f32 v222, v222\n"
"v_mfma_f32_16x16x32_bf16 a[48:51], a[208:211], v[20:23], a[48:51]\n"
"ds_read_b64_tr_b16 a[248:249], v244 offset:25344\n"
"v_add_f32 v44, v44, v7\n"
"v_mfma_f32_16x16x32_bf16 a[52:55], a[212:215], v[20:23], a[52:55]\n"
"v_exp_f32 v223, v223\n"
"v_mfma_f32_16x16x32_bf16 a[56:59], a[216:219], v[20:23], a[56:59]\n"
"ds_read_b64_tr_b16 a[250:251], v244 offset:29440\n"
"v_cvt_pk_bf16_f32 v17, v222, v223\n"
"v_mfma_f32_16x16x32_bf16 a[60:63], a[220:223], v[20:23], a[60:63]\n"
"v_exp_f32 v224, v224\n"
"v_mfma_f32_16x16x32_bf16 v[48:51], v[40:43], v[20:23], v[48:51]\n"
"ds_read_b64_tr_b16 a[252:253], v245 offset:25344\n"
"v_add_f32 v52, v52, v220\n"
"v_mfma_f32_16x16x32_bf16 v[228:231], a[128:131], v[112:115], v[36:39]\n"
"v_exp_f32 v225, v225\n"
"v_mfma_f32_16x16x32_bf16 v[228:231], a[132:135], v[116:119], v[228:231]\n"
"ds_read_b64_tr_b16 a[254:255], v245 offset:29440\n"
"v_cvt_pk_bf16_f32 v18, v224, v225\n"
"v_mfma_f32_16x16x32_bf16 v[228:231], a[136:139], v[120:123], v[228:231]\n"
"v_exp_f32 v226, v226\n"
"v_mfma_f32_16x16x32_bf16 v[228:231], a[140:143], v[124:127], v[228:231]\n"
"v_add_f32 v52, v52, v221\n"
"v_add_f32 v52, v52, v222\n"
"v_mfma_f32_16x16x32_bf16 v[232:235], a[144:147], v[112:115], v[36:39]\n"
"v_exp_f32 v227, v227\n"
"v_mfma_f32_16x16x32_bf16 v[232:235], a[148:151], v[116:119], v[232:235]\n"
"v_cvt_pk_bf16_f32 v19, v226, v227\n"
"v_add_f32 v52, v52, v223\n"
"v_mfma_f32_16x16x32_bf16 v[232:235], a[152:155], v[120:123], v[232:235]\n"
"buffer_load_dwordx4 v247, s[12:15], s84 offen offset:3072 lds\n"
"v_mfma_f32_16x16x32_bf16 v[232:235], a[156:159], v[124:127], v[232:235]\n"
"s_add_u32 s84, s84, 16384\n"
"v_exp_f32 v228, v228\n"
"v_mfma_f32_16x16x32_bf16 a[64:67], a[192:195], v[16:19], a[64:67]\n"
"v_add_f32 v52, v52, v224\n"
"v_add_f32 v52, v52, v225\n"
"v_mfma_f32_16x16x32_bf16 a[68:71], a[196:199], v[16:19], a[68:71]\n"
"v_exp_f32 v229, v229\n"
"v_mfma_f32_16x16x32_bf16 a[72:75], a[200:203], v[16:19], a[72:75]\n"
"v_cvt_pk_bf16_f32 v20, v228, v229\n"
"v_add_f32 v52, v52, v226\n"
"v_mfma_f32_16x16x32_bf16 a[76:79], a[204:207], v[16:19], a[76:79]\n"
"s_waitcnt vmcnt(20) lgkmcnt(0)\n"
"v_exp_f32 v230, v230\n"
"v_mfma_f32_16x16x32_bf16 a[80:83], a[208:211], v[16:19], a[80:83]\n"
"s_barrier\n"
"v_mfma_f32_16x16x32_bf16 a[84:87], a[212:215], v[16:19], a[84:87]\n"
"v_exp_f32 v231, v231\n"
"v_mfma_f32_16x16x32_bf16 a[88:91], a[216:219], v[16:19], a[88:91]\n"
"v_cvt_pk_bf16_f32 v21, v230, v231\n"
"v_add_f32 v52, v52, v227\n"
"v_mfma_f32_16x16x32_bf16 a[92:95], a[220:223], v[16:19], a[92:95]\n"
"v_exp_f32 v232, v232\n"
"v_mfma_f32_16x16x32_bf16 v[0:3], a[160:163], v[64:67], v[24:27]\n"
"ds_read_b128 a[128:131], v241 offset:32768\n"
"buffer_store_dwordx4 v[216:219], v207, s[28:31], s52 offen sc1\n"
"v_mfma_f32_16x16x32_bf16 v[0:3], a[164:167], v[68:71], v[0:3]\n"
"s_add_u32 s52, s52, 1024\n"
"v_exp_f32 v233, v233\n"
"v_mfma_f32_16x16x32_bf16 v[0:3], a[168:171], v[72:75], v[0:3]\n"
"s_min_u32 s52, s52, s53\n"
"ds_read_b128 a[132:135], v241 offset:33024\n"
"v_cvt_pk_bf16_f32 v22, v232, v233\n"
"v_mfma_f32_16x16x32_bf16 v[0:3], a[172:175], v[76:79], v[0:3]\n"
"v_exp_f32 v234, v234\n"
"v_mfma_f32_16x16x32_bf16 v[4:7], a[176:179], v[64:67], v[24:27]\n"
"ds_read_b128 a[136:139], v241 offset:33280\n"
"v_mfma_f32_16x16x32_bf16 v[4:7], a[180:183], v[68:71], v[4:7]\n"
"v_exp_f32 v235, v235\n"
"v_mfma_f32_16x16x32_bf16 v[4:7], a[184:187], v[72:75], v[4:7]\n"
"ds_read_b128 a[140:143], v241 offset:33536\n"
"v_cvt_pk_bf16_f32 v23, v234, v235\n"
"v_mfma_f32_16x16x32_bf16 v[4:7], a[188:191], v[76:79], v[4:7]\n"
"s_add_u32 m0, s86, 65536\n"
"v_exp_f32 v0, v0\n"
"v_mfma_f32_16x16x32_bf16 a[96:99], a[192:195], v[20:23], a[96:99]\n"
"buffer_load_dwordx4 v246, s[8:11], s84 offen offset:0 lds\n"
"v_mfma_f32_16x16x32_bf16 a[100:103], a[196:199], v[20:23], a[100:103]\n"
"v_exp_f32 v1, v1\n"
"v_mfma_f32_16x16x32_bf16 a[104:107], a[200:203], v[20:23], a[104:107]\n"
"ds_read_b128 a[144:147], v241 offset:36864\n"
"v_cvt_pk_bf16_f32 v16, v0, v1\n"
"v_mfma_f32_16x16x32_bf16 a[108:111], a[204:207], v[20:23], a[108:111]\n"
"v_exp_f32 v2, v2\n"
"v_mfma_f32_16x16x32_bf16 a[112:115], a[208:211], v[20:23], a[112:115]\n"
"ds_read_b128 a[148:151], v241 offset:37120\n"
"v_add_f32 v44, v44, v0\n"
"v_mfma_f32_16x16x32_bf16 a[116:119], a[212:215], v[20:23], a[116:119]\n"
"v_exp_f32 v3, v3\n"
"v_mfma_f32_16x16x32_bf16 a[120:123], a[216:219], v[20:23], a[120:123]\n"
"ds_read_b128 a[152:155], v241 offset:37376\n"
"v_cvt_pk_bf16_f32 v17, v2, v3\n"
"v_mfma_f32_16x16x32_bf16 a[124:127], a[220:223], v[20:23], a[124:127]\n"
"v_exp_f32 v4, v4\n"
"v_mfma_f32_16x16x32_bf16 v[56:59], v[40:43], v[20:23], v[56:59]\n"
"ds_read_b128 a[156:159], v241 offset:37632\n"
"v_add_f32 v44, v44, v1\n"
"v_mfma_f32_16x16x32_bf16 v[8:11], a[160:163], v[80:83], v[28:31]\n"
"v_exp_f32 v5, v5\n"
"v_mfma_f32_16x16x32_bf16 v[8:11], a[164:167], v[84:87], v[8:11]\n"
"ds_read_b64_tr_b16 a[192:193], v244 offset:49152\n"
"v_cvt_pk_bf16_f32 v18, v4, v5\n"
"v_mfma_f32_16x16x32_bf16 v[8:11], a[168:171], v[88:91], v[8:11]\n"
"v_exp_f32 v6, v6\n"
"v_mfma_f32_16x16x32_bf16 v[8:11], a[172:175], v[92:95], v[8:11]\n"
"ds_read_b64_tr_b16 a[194:195], v244 offset:53248\n"
"v_add_f32 v44, v44, v2\n"
"v_mfma_f32_16x16x32_bf16 v[12:15], a[176:179], v[80:83], v[28:31]\n"
"v_exp_f32 v7, v7\n"
"v_mfma_f32_16x16x32_bf16 v[12:15], a[180:183], v[84:87], v[12:15]\n"
"ds_read_b64_tr_b16 a[196:197], v245 offset:49152\n"
"v_cvt_pk_bf16_f32 v19, v6, v7\n"
"v_mfma_f32_16x16x32_bf16 v[12:15], a[184:187], v[88:91], v[12:15]\n"
"buffer_load_dwordx4 v246, s[8:11], s84 offen offset:1024 lds\n"
"v_mfma_f32_16x16x32_bf16 v[12:15], a[188:191], v[92:95], v[12:15]\n"
"s_waitcnt lgkmcnt(11)\n"
"v_exp_f32 v8, v8\n"
"v_mfma_f32_16x16x32_bf16 a[0:3], a[224:227], v[16:19], a[0:3]\n"
"ds_read_b64_tr_b16 a[198:199], v245 offset:53248\n"
"v_add_f32 v44, v44, v3\n"
"v_mfma_f32_16x16x32_bf16 a[4:7], a[228:231], v[16:19], a[4:7]\n"
"v_exp_f32 v9, v9\n"
"v_mfma_f32_16x16x32_bf16 a[8:11], a[232:235], v[16:19], a[8:11]\n"
"ds_read_b64_tr_b16 a[200:201], v244 offset:49408\n"
"v_cvt_pk_bf16_f32 v20, v8, v9\n"
"v_mfma_f32_16x16x32_bf16 a[12:15], a[236:239], v[16:19], a[12:15]\n"
"v_exp_f32 v10, v10\n"
"v_mfma_f32_16x16x32_bf16 a[16:19], a[240:243], v[16:19], a[16:19]\n"
"ds_read_b64_tr_b16 a[202:203], v244 offset:53504\n"
"v_add_f32 v44, v44, v4\n"
"v_mfma_f32_16x16x32_bf16 a[20:23], a[244:247], v[16:19], a[20:23]\n"
"v_exp_f32 v11, v11\n"
"v_mfma_f32_16x16x32_bf16 a[24:27], a[248:251], v[16:19], a[24:27]\n"
"ds_read_b64_tr_b16 a[204:205], v245 offset:49408\n"
"v_cvt_pk_bf16_f32 v21, v10, v11\n"
"v_mfma_f32_16x16x32_bf16 a[28:31], a[252:255], v[16:19], a[28:31]\n"
"v_exp_f32 v12, v12\n"
"v_mfma_f32_16x16x32_bf16 v[220:223], a[160:163], v[96:99], v[32:35]\n"
"ds_read_b64_tr_b16 a[206:207], v245 offset:53504\n"
"v_add_f32 v44, v44, v5\n"
"v_mfma_f32_16x16x32_bf16 v[220:223], a[164:167], v[100:103], v[220:223]\n"
"v_exp_f32 v13, v13\n"
"v_mfma_f32_16x16x32_bf16 v[220:223], a[168:171], v[104:107], v[220:223]\n"
"ds_read_b64_tr_b16 a[208:209], v244 offset:49664\n"
"v_cvt_pk_bf16_f32 v22, v12, v13\n"
"v_mfma_f32_16x16x32_bf16 v[220:223], a[172:175], v[108:111], v[220:223]\n"
"v_exp_f32 v14, v14\n"
"v_mfma_f32_16x16x32_bf16 v[224:227], a[176:179], v[96:99], v[32:35]\n"
"ds_read_b64_tr_b16 a[210:211], v244 offset:53760\n"
"v_add_f32 v44, v44, v6\n"
"v_mfma_f32_16x16x32_bf16 v[224:227], a[180:183], v[100:103], v[224:227]\n"
"v_exp_f32 v15, v15\n"
"v_mfma_f32_16x16x32_bf16 v[224:227], a[184:187], v[104:107], v[224:227]\n"
"ds_read_b64_tr_b16 a[212:213], v245 offset:49664\n"
"v_cvt_pk_bf16_f32 v23, v14, v15\n"
"v_mfma_f32_16x16x32_bf16 v[224:227], a[188:191], v[108:111], v[224:227]\n"
"v_exp_f32 v220, v220\n"
"v_mfma_f32_16x16x32_bf16 a[32:35], a[224:227], v[20:23], a[32:35]\n"
"buffer_load_dwordx4 v247, s[8:11], s84 offen offset:2048 lds\n"
"v_mfma_f32_16x16x32_bf16 a[36:39], a[228:231], v[20:23], a[36:39]\n"
"v_exp_f32 v221, v221\n"
"v_mfma_f32_16x16x32_bf16 a[40:43], a[232:235], v[20:23], a[40:43]\n"
"ds_read_b64_tr_b16 a[214:215], v245 offset:53760\n"
"v_cvt_pk_bf16_f32 v16, v220, v221\n"
"v_mfma_f32_16x16x32_bf16 a[44:47], a[236:239], v[20:23], a[44:47]\n"
"v_exp_f32 v222, v222\n"
"v_mfma_f32_16x16x32_bf16 a[48:51], a[240:243], v[20:23], a[48:51]\n"
"ds_read_b64_tr_b16 a[216:217], v244 offset:49920\n"
"v_add_f32 v44, v44, v7\n"
"v_mfma_f32_16x16x32_bf16 a[52:55], a[244:247], v[20:23], a[52:55]\n"
"v_exp_f32 v223, v223\n"
"v_mfma_f32_16x16x32_bf16 a[56:59], a[248:251], v[20:23], a[56:59]\n"
"ds_read_b64_tr_b16 a[218:219], v244 offset:54016\n"
"v_cvt_pk_bf16_f32 v17, v222, v223\n"
"v_mfma_f32_16x16x32_bf16 a[60:63], a[252:255], v[20:23], a[60:63]\n"
"v_exp_f32 v224, v224\n"
"v_mfma_f32_16x16x32_bf16 v[48:51], v[40:43], v[20:23], v[48:51]\n"
"ds_read_b64_tr_b16 a[220:221], v245 offset:49920\n"
"v_add_f32 v52, v52, v220\n"
"v_mfma_f32_16x16x32_bf16 v[228:231], a[160:163], v[112:115], v[36:39]\n"
"v_exp_f32 v225, v225\n"
"v_mfma_f32_16x16x32_bf16 v[228:231], a[164:167], v[116:119], v[228:231]\n"
"ds_read_b64_tr_b16 a[222:223], v245 offset:54016\n"
"v_cvt_pk_bf16_f32 v18, v224, v225\n"
"v_mfma_f32_16x16x32_bf16 v[228:231], a[168:171], v[120:123], v[228:231]\n"
"v_exp_f32 v226, v226\n"
"v_mfma_f32_16x16x32_bf16 v[228:231], a[172:175], v[124:127], v[228:231]\n"
"v_add_f32 v52, v52, v221\n"
"v_add_f32 v52, v52, v222\n"
"v_mfma_f32_16x16x32_bf16 v[232:235], a[176:179], v[112:115], v[36:39]\n"
"v_exp_f32 v227, v227\n"
"v_mfma_f32_16x16x32_bf16 v[232:235], a[180:183], v[116:119], v[232:235]\n"
"v_cvt_pk_bf16_f32 v19, v226, v227\n"
"v_add_f32 v52, v52, v223\n"
"v_mfma_f32_16x16x32_bf16 v[232:235], a[184:187], v[120:123], v[232:235]\n"
"buffer_load_dwordx4 v247, s[8:11], s84 offen offset:3072 lds\n"
"v_mfma_f32_16x16x32_bf16 v[232:235], a[188:191], v[124:127], v[232:235]\n"
"v_exp_f32 v228, v228\n"
"v_mfma_f32_16x16x32_bf16 a[64:67], a[224:227], v[16:19], a[64:67]\n"
"v_add_f32 v52, v52, v224\n"
"v_add_f32 v52, v52, v225\n"
"v_mfma_f32_16x16x32_bf16 a[68:71], a[228:231], v[16:19], a[68:71]\n"
"v_exp_f32 v229, v229\n"
"v_mfma_f32_16x16x32_bf16 a[72:75], a[232:235], v[16:19], a[72:75]\n"
"v_cvt_pk_bf16_f32 v20, v228, v229\n"
"v_add_f32 v52, v52, v226\n"
"v_mfma_f32_16x16x32_bf16 a[76:79], a[236:239], v[16:19], a[76:79]\n"
"v_exp_f32 v230, v230\n"
"v_mfma_f32_16x16x32_bf16 a[80:83], a[240:243], v[16:19], a[80:83]\n"
"s_waitcnt lgkmcnt(8)\n"
"buffer_load_dword v250, v252, s[72:75], s46 offen\n"
"v_mfma_f32_16x16x32_bf16 a[84:87], a[244:247], v[16:19], a[84:87]\n"
"v_exp_f32 v231, v231\n"
"v_mfma_f32_16x16x32_bf16 a[88:91], a[248:251], v[16:19], a[88:91]\n"
"v_cvt_pk_bf16_f32 v21, v230, v231\n"
"v_add_f32 v52, v52, v227\n"
"v_mfma_f32_16x16x32_bf16 a[92:95], a[252:255], v[16:19], a[92:95]\n"
"v_exp_f32 v232, v232\n"
"v_mfma_f32_16x16x32_bf16 v[0:3], a[128:131], v[64:67], v[24:27]\n"
"ds_read_b128 a[160:163], v241 offset:40960\n"
"v_add_u32 v252, 147456, v252\n"
"v_mfma_f32_16x16x32_bf16 v[0:3], a[132:135], v[68:71], v[0:3]\n"
"v_exp_f32 v233, v233\n"
"v_mfma_f32_16x16x32_bf16 v[0:3], a[136:139], v[72:75], v[0:3]\n"
"ds_read_b128 a[164:167], v241 offset:41216\n"
"v_cvt_pk_bf16_f32 v22, v232, v233\n"
"v_mfma_f32_16x16x32_bf16 v[0:3], a[140:143], v[76:79], v[0:3]\n"
"v_exp_f32 v234, v234\n"
"v_mfma_f32_16x16x32_bf16 v[4:7], a[144:147], v[64:67], v[24:27]\n"
"ds_read_b128 a[168:171], v241 offset:41472\n"
"v_mfma_f32_16x16x32_bf16 v[4:7], a[148:151], v[68:71], v[4:7]\n"
"v_exp_f32 v235, v235\n"
"v_mfma_f32_16x16x32_bf16 v[4:7], a[152:155], v[72:75], v[4:7]\n"
"ds_read_b128 a[172:175], v241 offset:41728\n"
"v_cvt_pk_bf16_f32 v23, v234, v235\n"
"v_mfma_f32_16x16x32_bf16 v[4:7], a[156:159], v[76:79], v[4:7]\n"
"s_add_u32 m0, s86, 81920\n"
"v_exp_f32 v0, v0\n"
"v_mfma_f32_16x16x32_bf16 a[96:99], a[224:227], v[20:23], a[96:99]\n"
"buffer_load_dwordx4 v246, s[12:15], s84 offen offset:0 lds\n"
"v_mfma_f32_16x16x32_bf16 a[100:103], a[228:231], v[20:23], a[100:103]\n"
"v_exp_f32 v1, v1\n"
"v_mfma_f32_16x16x32_bf16 a[104:107], a[232:235], v[20:23], a[104:107]\n"
"ds_read_b128 a[176:179], v241 offset:45056\n"
"v_cvt_pk_bf16_f32 v16, v0, v1\n"
"v_mfma_f32_16x16x32_bf16 a[108:111], a[236:239], v[20:23], a[108:111]\n"
"v_exp_f32 v2, v2\n"
"v_mfma_f32_16x16x32_bf16 a[112:115], a[240:243], v[20:23], a[112:115]\n"
"ds_read_b128 a[180:183], v241 offset:45312\n"
"v_add_f32 v44, v44, v0\n"
"v_mfma_f32_16x16x32_bf16 a[116:119], a[244:247], v[20:23], a[116:119]\n"
"v_exp_f32 v3, v3\n"
"v_mfma_f32_16x16x32_bf16 a[120:123], a[248:251], v[20:23], a[120:123]\n"
"ds_read_b128 a[184:187], v241 offset:45568\n"
"v_cvt_pk_bf16_f32 v17, v2, v3\n"
"v_mfma_f32_16x16x32_bf16 a[124:127], a[252:255], v[20:23], a[124:127]\n"
"v_exp_f32 v4, v4\n"
"v_mfma_f32_16x16x32_bf16 v[56:59], v[40:43], v[20:23], v[56:59]\n"
"ds_read_b128 a[188:191], v241 offset:45824\n"
"v_add_f32 v44, v44, v1\n"
"v_mfma_f32_16x16x32_bf16 v[8:11], a[128:131], v[80:83], v[28:31]\n"
"v_exp_f32 v5, v5\n"
"v_mfma_f32_16x16x32_bf16 v[8:11], a[132:135], v[84:87], v[8:11]\n"
"ds_read_b64_tr_b16 a[224:225], v244 offset:57344\n"
"v_cvt_pk_bf16_f32 v18, v4, v5\n"
"v_mfma_f32_16x16x32_bf16 v[8:11], a[136:139], v[88:91], v[8:11]\n"
"v_exp_f32 v6, v6\n"
"v_mfma_f32_16x16x32_bf16 v[8:11], a[140:143], v[92:95], v[8:11]\n"
"ds_read_b64_tr_b16 a[226:227], v244 offset:61440\n"
"v_add_f32 v44, v44, v2\n"
"v_mfma_f32_16x16x32_bf16 v[12:15], a[144:147], v[80:83], v[28:31]\n"
"v_exp_f32 v7, v7\n"
"v_mfma_f32_16x16x32_bf16 v[12:15], a[148:151], v[84:87], v[12:15]\n"
"ds_read_b64_tr_b16 a[228:229], v245 offset:57344\n"
"v_cvt_pk_bf16_f32 v19, v6, v7\n"
"v_mfma_f32_16x16x32_bf16 v[12:15], a[152:155], v[88:91], v[12:15]\n"
"buffer_load_dwordx4 v247, s[12:15], s84 offen offset:1024 lds\n"
"v_mfma_f32_16x16x32_bf16 v[12:15], a[156:159], v[92:95], v[12:15]\n"
"s_waitcnt lgkmcnt(11)\n"
"v_exp_f32 v8, v8\n"
"v_mfma_f32_16x16x32_bf16 a[0:3], a[192:195], v[16:19], a[0:3]\n"
"ds_read_b64_tr_b16 a[230:231], v245 offset:61440\n"
"v_add_f32 v44, v44, v3\n"
"v_mfma_f32_16x16x32_bf16 a[4:7], a[196:199], v[16:19], a[4:7]\n"
"v_exp_f32 v9, v9\n"
"v_mfma_f32_16x16x32_bf16 a[8:11], a[200:203], v[16:19], a[8:11]\n"
"ds_read_b64_tr_b16 a[232:233], v244 offset:57600\n"
"v_cvt_pk_bf16_f32 v20, v8, v9\n"
"v_mfma_f32_16x16x32_bf16 a[12:15], a[204:207], v[16:19], a[12:15]\n"
"v_exp_f32 v10, v10\n"
"v_mfma_f32_16x16x32_bf16 a[16:19], a[208:211], v[16:19], a[16:19]\n"
"ds_read_b64_tr_b16 a[234:235], v244 offset:61696\n"
"v_add_f32 v44, v44, v4\n"
"v_mfma_f32_16x16x32_bf16 a[20:23], a[212:215], v[16:19], a[20:23]\n"
"v_exp_f32 v11, v11\n"
"v_mfma_f32_16x16x32_bf16 a[24:27], a[216:219], v[16:19], a[24:27]\n"
"ds_read_b64_tr_b16 a[236:237], v245 offset:57600\n"
"v_cvt_pk_bf16_f32 v21, v10, v11\n"
"v_mfma_f32_16x16x32_bf16 a[28:31], a[220:223], v[16:19], a[28:31]\n"
"v_exp_f32 v12, v12\n"
"v_mfma_f32_16x16x32_bf16 v[220:223], a[128:131], v[96:99], v[32:35]\n"
"ds_read_b64_tr_b16 a[238:239], v245 offset:61696\n"
"v_add_f32 v44, v44, v5\n"
"v_mfma_f32_16x16x32_bf16 v[220:223], a[132:135], v[100:103], v[220:223]\n"
"v_exp_f32 v13, v13\n"
"v_mfma_f32_16x16x32_bf16 v[220:223], a[136:139], v[104:107], v[220:223]\n"
"ds_read_b64_tr_b16 a[240:241], v244 offset:57856\n"
"v_cvt_pk_bf16_f32 v22, v12, v13\n"
"v_mfma_f32_16x16x32_bf16 v[220:223], a[140:143], v[108:111], v[220:223]\n"
"v_exp_f32 v14, v14\n"
"v_mfma_f32_16x16x32_bf16 v[224:227], a[144:147], v[96:99], v[32:35]\n"
"ds_read_b64_tr_b16 a[242:243], v244 offset:61952\n"
"v_add_f32 v44, v44, v6\n"
"v_mfma_f32_16x16x32_bf16 v[224:227], a[148:151], v[100:103], v[224:227]\n"
"v_exp_f32 v15, v15\n"
"v_mfma_f32_16x16x32_bf16 v[224:227], a[152:155], v[104:107], v[224:227]\n"
"ds_read_b64_tr_b16 a[244:245], v245 offset:57856\n"
"v_cvt_pk_bf16_f32 v23, v14, v15\n"
"v_mfma_f32_16x16x32_bf16 v[224:227], a[156:159], v[108:111], v[224:227]\n"
"v_exp_f32 v220, v220\n"
"v_mfma_f32_16x16x32_bf16 a[32:35], a[192:195], v[20:23], a[32:35]\n"
"buffer_load_dwordx4 v246, s[12:15], s84 offen offset:2048 lds\n"
"v_mfma_f32_16x16x32_bf16 a[36:39], a[196:199], v[20:23], a[36:39]\n"
"v_exp_f32 v221, v221\n"
"v_mfma_f32_16x16x32_bf16 a[40:43], a[200:203], v[20:23], a[40:43]\n"
"ds_read_b64_tr_b16 a[246:247], v245 offset:61952\n"
"v_cvt_pk_bf16_f32 v16, v220, v221\n"
"v_mfma_f32_16x16x32_bf16 a[44:47], a[204:207], v[20:23], a[44:47]\n"
"v_exp_f32 v222, v222\n"
"v_mfma_f32_16x16x32_bf16 a[48:51], a[208:211], v[20:23], a[48:51]\n"
"ds_read_b64_tr_b16 a[248:249], v244 offset:58112\n"
"v_add_f32 v44, v44, v7\n"
"v_mfma_f32_16x16x32_bf16 a[52:55], a[212:215], v[20:23], a[52:55]\n"
"v_exp_f32 v223, v223\n"
"v_mfma_f32_16x16x32_bf16 a[56:59], a[216:219], v[20:23], a[56:59]\n"
"ds_read_b64_tr_b16 a[250:251], v244 offset:62208\n"
"v_cvt_pk_bf16_f32 v17, v222, v223\n"
"v_mfma_f32_16x16x32_bf16 a[60:63], a[220:223], v[20:23], a[60:63]\n"
"v_exp_f32 v224, v224\n"
"v_mfma_f32_16x16x32_bf16 v[48:51], v[40:43], v[20:23], v[48:51]\n"
"ds_read_b64_tr_b16 a[252:253], v245 offset:58112\n"
"v_add_f32 v52, v52, v220\n"
"v_mfma_f32_16x16x32_bf16 v[228:231], a[128:131], v[112:115], v[36:39]\n"
"v_exp_f32 v225, v225\n"
"v_mfma_f32_16x16x32_bf16 v[228:231], a[132:135], v[116:119], v[228:231]\n"
"ds_read_b64_tr_b16 a[254:255], v245 offset:62208\n"
"v_cvt_pk_bf16_f32 v18, v224, v225\n"
"v_mfma_f32_16x16x32_bf16 v[228:231], a[136:139], v[120:123], v[228:231]\n"
"v_exp_f32 v226, v226\n"
"v_mfma_f32_16x16x32_bf16 v[228:231], a[140:143], v[124:127], v[228:231]\n"
"v_add_f32 v52, v52, v221\n"
"v_add_f32 v52, v52, v222\n"
"v_mfma_f32_16x16x32_bf16 v[232:235], a[144:147], v[112:115], v[36:39]\n"
"v_exp_f32 v227, v227\n"
"v_mfma_f32_16x16x32_bf16 v[232:235], a[148:151], v[116:119], v[232:235]\n"
"v_cvt_pk_bf16_f32 v19, v226, v227\n"
"v_add_f32 v52, v52, v223\n"
"v_mfma_f32_16x16x32_bf16 v[232:235], a[152:155], v[120:123], v[232:235]\n"
"buffer_load_dwordx4 v247, s[12:15], s84 offen offset:3072 lds\n"
"v_mfma_f32_16x16x32_bf16 v[232:235], a[156:159], v[124:127], v[232:235]\n"
"s_add_u32 s84, s84, 16384\n"
"v_exp_f32 v228, v228\n"
"v_mfma_f32_16x16x32_bf16 a[64:67], a[192:195], v[16:19], a[64:67]\n"
"v_add_f32 v52, v52, v224\n"
"v_add_f32 v52, v52, v225\n"
"v_mfma_f32_16x16x32_bf16 a[68:71], a[196:199], v[16:19], a[68:71]\n"
"v_exp_f32 v229, v229\n"
"v_mfma_f32_16x16x32_bf16 a[72:75], a[200:203], v[16:19], a[72:75]\n"
"v_cvt_pk_bf16_f32 v20, v228, v229\n"
"v_add_f32 v52, v52, v226\n"
"v_mfma_f32_16x16x32_bf16 a[76:79], a[204:207], v[16:19], a[76:79]\n"
"s_waitcnt vmcnt(20) lgkmcnt(0)\n"
"v_exp_f32 v230, v230\n"
"v_mfma_f32_16x16x32_bf16 a[80:83], a[208:211], v[16:19], a[80:83]\n"
"s_barrier\n"
"v_mfma_f32_16x16x32_bf16 a[84:87], a[212:215], v[16:19], a[84:87]\n"
"v_exp_f32 v231, v231\n"
"v_mfma_f32_16x16x32_bf16 a[88:91], a[216:219], v[16:19], a[88:91]\n"
"v_cvt_pk_bf16_f32 v21, v230, v231\n"
"v_add_f32 v52, v52, v227\n"
"v_mfma_f32_16x16x32_bf16 a[92:95], a[220:223], v[16:19], a[92:95]\n"
"v_exp_f32 v232, v232\n"
"v_mfma_f32_16x16x32_bf16 v[0:3], a[160:163], v[64:67], v[24:27]\n"
"ds_read_b128 a[128:131], v240 offset:0\n"
"buffer_store_dwordx4 v[216:219], v208, s[28:31], s54 offen sc1\n"
"v_mfma_f32_16x16x32_bf16 v[0:3], a[164:167], v[68:71], v[0:3]\n"
"s_add_u32 s54, s54, 1024\n"
"v_exp_f32 v233, v233\n"
"v_mfma_f32_16x16x32_bf16 v[0:3], a[168:171], v[72:75], v[0:3]\n"
"s_min_u32 s54, s54, s49\n"
"ds_read_b128 a[132:135], v240 offset:256\n"
"v_cvt_pk_bf16_f32 v22, v232, v233\n"
"v_mfma_f32_16x16x32_bf16 v[0:3], a[172:175], v[76:79], v[0:3]\n"
"v_exp_f32 v234, v234\n"
"v_mfma_f32_16x16x32_bf16 v[4:7], a[176:179], v[64:67], v[24:27]\n"
"ds_read_b128 a[136:139], v240 offset:512\n"
"v_mfma_f32_16x16x32_bf16 v[4:7], a[180:183], v[68:71], v[4:7]\n"
"v_exp_f32 v235, v235\n"
"v_mfma_f32_16x16x32_bf16 v[4:7], a[184:187], v[72:75], v[4:7]\n"
"ds_read_b128 a[140:143], v240 offset:768\n"
"v_cvt_pk_bf16_f32 v23, v234, v235\n"
"v_mfma_f32_16x16x32_bf16 v[4:7], a[188:191], v[76:79], v[4:7]\n"
"s_add_u32 m0, s86, 98304\n"
"v_exp_f32 v0, v0\n"
"v_mfma_f32_16x16x32_bf16 a[96:99], a[192:195], v[20:23], a[96:99]\n"
"buffer_load_dwordx4 v246, s[8:11], s84 offen offset:0 lds\n"
"v_mfma_f32_16x16x32_bf16 a[100:103], a[196:199], v[20:23], a[100:103]\n"
"v_exp_f32 v1, v1\n"
"v_mfma_f32_16x16x32_bf16 a[104:107], a[200:203], v[20:23], a[104:107]\n"
"ds_read_b128 a[144:147], v240 offset:4096\n"
"v_cvt_pk_bf16_f32 v16, v0, v1\n"
"v_mfma_f32_16x16x32_bf16 a[108:111], a[204:207], v[20:23], a[108:111]\n"
"v_exp_f32 v2, v2\n"
"v_mfma_f32_16x16x32_bf16 a[112:115], a[208:211], v[20:23], a[112:115]\n"
"ds_read_b128 a[148:151], v240 offset:4352\n"
"v_add_f32 v44, v44, v0\n"
"v_mfma_f32_16x16x32_bf16 a[116:119], a[212:215], v[20:23], a[116:119]\n"
"v_exp_f32 v3, v3\n"
"v_mfma_f32_16x16x32_bf16 a[120:123], a[216:219], v[20:23], a[120:123]\n"
"ds_read_b128 a[152:155], v240 offset:4608\n"
"v_cvt_pk_bf16_f32 v17, v2, v3\n"
"v_mfma_f32_16x16x32_bf16 a[124:127], a[220:223], v[20:23], a[124:127]\n"
"v_exp_f32 v4, v4\n"
"v_mfma_f32_16x16x32_bf16 v[56:59], v[40:43], v[20:23], v[56:59]\n"
"ds_read_b128 a[156:159], v240 offset:4864\n"
"v_add_f32 v44, v44, v1\n"
"v_mfma_f32_16x16x32_bf16 v[8:11], a[160:163], v[80:83], v[28:31]\n"
"v_exp_f32 v5, v5\n"
"v_mfma_f32_16x16x32_bf16 v[8:11], a[164:167], v[84:87], v[8:11]\n"
"ds_read_b64_tr_b16 a[192:193], v242 offset:16384\n"
"v_cvt_pk_bf16_f32 v18, v4, v5\n"
"v_mfma_f32_16x16x32_bf16 v[8:11], a[168:171], v[88:91], v[8:11]\n"
"v_exp_f32 v6, v6\n"
"v_mfma_f32_16x16x32_bf16 v[8:11], a[172:175], v[92:95], v[8:11]\n"
"ds_read_b64_tr_b16 a[194:195], v242 offset:20480\n"
"v_add_f32 v44, v44, v2\n"
"v_mfma_f32_16x16x32_bf16 v[12:15], a[176:179], v[80:83], v[28:31]\n"
"v_exp_f32 v7, v7\n"
"v_mfma_f32_16x16x32_bf16 v[12:15], a[180:183], v[84:87], v[12:15]\n"
"ds_read_b64_tr_b16 a[196:197], v243 offset:16384\n"
"v_cvt_pk_bf16_f32 v19, v6, v7\n"
"v_mfma_f32_16x16x32_bf16 v[12:15], a[184:187], v[88:91], v[12:15]\n"
"buffer_load_dwordx4 v246, s[8:11], s84 offen offset:1024 lds\n"
"v_mfma_f32_16x16x32_bf16 v[12:15], a[188:191], v[92:95], v[12:15]\n"
"s_waitcnt lgkmcnt(11)\n"
"v_exp_f32 v8, v8\n"
"v_mfma_f32_16x16x32_bf16 a[0:3], a[224:227], v[16:19], a[0:3]\n"
"ds_read_b64_tr_b16 a[198:199], v243 offset:20480\n"
"v_add_f32 v44, v44, v3\n"
"v_mfma_f32_16x16x32_bf16 a[4:7], a[228:231], v[16:19], a[4:7]\n"
"v_exp_f32 v9, v9\n"
"v_mfma_f32_16x16x32_bf16 a[8:11], a[232:235], v[16:19], a[8:11]\n"
"ds_read_b64_tr_b16 a[200:201], v242 offset:16640\n"
"v_cvt_pk_bf16_f32 v20, v8, v9\n"
"v_mfma_f32_16x16x32_bf16 a[12:15], a[236:239], v[16:19], a[12:15]\n"
"v_exp_f32 v10, v10\n"
"v_mfma_f32_16x16x32_bf16 a[16:19], a[240:243], v[16:19], a[16:19]\n"
"ds_read_b64_tr_b16 a[202:203], v242 offset:20736\n"
"v_add_f32 v44, v44, v4\n"
"v_mfma_f32_16x16x32_bf16 a[20:23], a[244:247], v[16:19], a[20:23]\n"
"v_exp_f32 v11, v11\n"
"v_mfma_f32_16x16x32_bf16 a[24:27], a[248:251], v[16:19], a[24:27]\n"
"ds_read_b64_tr_b16 a[204:205], v243 offset:16640\n"
"v_cvt_pk_bf16_f32 v21, v10, v11\n"
"v_mfma_f32_16x16x32_bf16 a[28:31], a[252:255], v[16:19], a[28:31]\n"
"v_exp_f32 v12, v12\n"
"v_mfma_f32_16x16x32_bf16 v[220:223], a[160:163], v[96:99], v[32:35]\n"
"ds_read_b64_tr_b16 a[206:207], v243 offset:20736\n"
"v_add_f32 v44, v44, v5\n"
"v_mfma_f32_16x16x32_bf16 v[220:223], a[164:167], v[100:103], v[220:223]\n"
"v_exp_f32 v13, v13\n"
"v_mfma_f32_16x16x32_bf16 v[220:223], a[168:171], v[104:107], v[220:223]\n"
"ds_read_b64_tr_b16 a[208:209], v242 offset:16896\n"
"v_cvt_pk_bf16_f32 v22, v12, v13\n"
"v_mfma_f32_16x16x32_bf16 v[220:223], a[172:175], v[108:111], v[220:223]\n"
"v_exp_f32 v14, v14\n"
"v_mfma_f32_16x16x32_bf16 v[224:227], a[176:179], v[96:99], v[32:35]\n"
"ds_read_b64_tr_b16 a[210:211], v242 offset:20992\n"
"v_add_f32 v44, v44, v6\n"
"v_mfma_f32_16x16x32_bf16 v[224:227], a[180:183], v[100:103], v[224:227]\n"
"v_exp_f32 v15, v15\n"
"v_mfma_f32_16x16x32_bf16 v[224:227], a[184:187], v[104:107], v[224:227]\n"
"ds_read_b64_tr_b16 a[212:213], v243 offset:16896\n"
"v_cvt_pk_bf16_f32 v23, v14, v15\n"
"v_mfma_f32_16x16x32_bf16 v[224:227], a[188:191], v[108:111], v[224:227]\n"
"v_exp_f32 v220, v220\n"
"v_mfma_f32_16x16x32_bf16 a[32:35], a[224:227], v[20:23], a[32:35]\n"
"buffer_load_dwordx4 v247, s[8:11], s84 offen offset:2048 lds\n"
"v_mfma_f32_16x16x32_bf16 a[36:39], a[228:231], v[20:23], a[36:39]\n"
"v_exp_f32 v221, v221\n"
"v_mfma_f32_16x16x32_bf16 a[40:43], a[232:235], v[20:23], a[40:43]\n"
"ds_read_b64_tr_b16 a[214:215], v243 offset:20992\n"
"v_cvt_pk_bf16_f32 v16, v220, v221\n"
"v_mfma_f32_16x16x32_bf16 a[44:47], a[236:239], v[20:23], a[44:47]\n"
"v_exp_f32 v222, v222\n"
"v_mfma_f32_16x16x32_bf16 a[48:51], a[240:243], v[20:23], a[48:51]\n"
"ds_read_b64_tr_b16 a[216:217], v242 offset:17152\n"
"v_add_f32 v44, v44, v7\n"
"v_mfma_f32_16x16x32_bf16 a[52:55], a[244:247], v[20:23], a[52:55]\n"
"v_exp_f32 v223, v223\n"
"v_mfma_f32_16x16x32_bf16 a[56:59], a[248:251], v[20:23], a[56:59]\n"
"ds_read_b64_tr_b16 a[218:219], v242 offset:21248\n"
"v_cvt_pk_bf16_f32 v17, v222, v223\n"
"v_mfma_f32_16x16x32_bf16 a[60:63], a[252:255], v[20:23], a[60:63]\n"
"v_exp_f32 v224, v224\n"
"v_mfma_f32_16x16x32_bf16 v[48:51], v[40:43], v[20:23], v[48:51]\n"
"ds_read_b64_tr_b16 a[220:221], v243 offset:17152\n"
"v_add_f32 v52, v52, v220\n"
"v_mfma_f32_16x16x32_bf16 v[228:231], a[160:163], v[112:115], v[36:39]\n"
"v_exp_f32 v225, v225\n"
"v_mfma_f32_16x16x32_bf16 v[228:231], a[164:167], v[116:119], v[228:231]\n"
"ds_read_b64_tr_b16 a[222:223], v243 offset:21248\n"
"v_cvt_pk_bf16_f32 v18, v224, v225\n"
"v_mfma_f32_16x16x32_bf16 v[228:231], a[168:171], v[120:123], v[228:231]\n"
"v_exp_f32 v226, v226\n"
"v_mfma_f32_16x16x32_bf16 v[228:231], a[172:175], v[124:127], v[228:231]\n"
"v_add_f32 v52, v52, v221\n"
"v_add_f32 v52, v52, v222\n"
"v_mfma_f32_16x16x32_bf16 v[232:235], a[176:179], v[112:115], v[36:39]\n"
"v_exp_f32 v227, v227\n"
"v_mfma_f32_16x16x32_bf16 v[232:235], a[180:183], v[116:119], v[232:235]\n"
"v_cvt_pk_bf16_f32 v19, v226, v227\n"
"v_add_f32 v52, v52, v223\n"
"v_mfma_f32_16x16x32_bf16 v[232:235], a[184:187], v[120:123], v[232:235]\n"
"buffer_load_dwordx4 v247, s[8:11], s84 offen offset:3072 lds\n"
"v_mfma_f32_16x16x32_bf16 v[232:235], a[188:191], v[124:127], v[232:235]\n"
"v_exp_f32 v228, v228\n"
"v_mfma_f32_16x16x32_bf16 a[64:67], a[224:227], v[16:19], a[64:67]\n"
"v_add_f32 v52, v52, v224\n"
"v_add_f32 v52, v52, v225\n"
"v_mfma_f32_16x16x32_bf16 a[68:71], a[228:231], v[16:19], a[68:71]\n"
"v_exp_f32 v229, v229\n"
"v_mfma_f32_16x16x32_bf16 a[72:75], a[232:235], v[16:19], a[72:75]\n"
"v_cvt_pk_bf16_f32 v20, v228, v229\n"
"v_add_f32 v52, v52, v226\n"
"v_mfma_f32_16x16x32_bf16 a[76:79], a[236:239], v[16:19], a[76:79]\n"
"v_exp_f32 v230, v230\n"
"v_mfma_f32_16x16x32_bf16 a[80:83], a[240:243], v[16:19], a[80:83]\n"
"s_waitcnt lgkmcnt(8)\n"
"buffer_load_dword v250, v253, s[24:27], s47 offen\n"
"v_mfma_f32_16x16x32_bf16 a[84:87], a[244:247], v[16:19], a[84:87]\n"
"v_exp_f32 v231, v231\n"
"v_mfma_f32_16x16x32_bf16 a[88:91], a[248:251], v[16:19], a[88:91]\n"
"v_cvt_pk_bf16_f32 v21, v230, v231\n"
"v_add_f32 v52, v52, v227\n"
"v_mfma_f32_16x16x32_bf16 a[92:95], a[252:255], v[16:19], a[92:95]\n"
"v_exp_f32 v232, v232\n"
"v_mfma_f32_16x16x32_bf16 v[0:3], a[128:131], v[64:67], v[24:27]\n"
"ds_read_b128 a[160:163], v240 offset:8192\n"
"v_add_u32 v253, 294912, v253\n"
"v_mfma_f32_16x16x32_bf16 v[0:3], a[132:135], v[68:71], v[0:3]\n"
"v_exp_f32 v233, v233\n"
"v_mfma_f32_16x16x32_bf16 v[0:3], a[136:139], v[72:75], v[0:3]\n"
"ds_read_b128 a[164:167], v240 offset:8448\n"
"v_cvt_pk_bf16_f32 v22, v232, v233\n"
"v_mfma_f32_16x16x32_bf16 v[0:3], a[140:143], v[76:79], v[0:3]\n"
"v_exp_f32 v234, v234\n"
"v_mfma_f32_16x16x32_bf16 v[4:7], a[144:147], v[64:67], v[24:27]\n"
"ds_read_b128 a[168:171], v240 offset:8704\n"
"v_mfma_f32_16x16x32_bf16 v[4:7], a[148:151], v[68:71], v[4:7]\n"
"v_exp_f32 v235, v235\n"
"v_mfma_f32_16x16x32_bf16 v[4:7], a[152:155], v[72:75], v[4:7]\n"
"ds_read_b128 a[172:175], v240 offset:8960\n"
"v_cvt_pk_bf16_f32 v23, v234, v235\n"
"v_mfma_f32_16x16x32_bf16 v[4:7], a[156:159], v[76:79], v[4:7]\n"
"s_add_u32 m0, s86, 114688\n"
"v_exp_f32 v0, v0\n"
"v_mfma_f32_16x16x32_bf16 a[96:99], a[224:227], v[20:23], a[96:99]\n"
"buffer_load_dwordx4 v246, s[12:15], s84 offen offset:0 lds\n"
"v_mfma_f32_16x16x32_bf16 a[100:103], a[228:231], v[20:23], a[100:103]\n"
"v_exp_f32 v1, v1\n"
"v_mfma_f32_16x16x32_bf16 a[104:107], a[232:235], v[20:23], a[104:107]\n"
"ds_read_b128 a[176:179], v240 offset:12288\n"
"v_cvt_pk_bf16_f32 v16, v0, v1\n"
"v_mfma_f32_16x16x32_bf16 a[108:111], a[236:239], v[20:23], a[108:111]\n"
"v_exp_f32 v2, v2\n"
"v_mfma_f32_16x16x32_bf16 a[112:115], a[240:243], v[20:23], a[112:115]\n"
"ds_read_b128 a[180:183], v240 offset:12544\n"
"v_add_f32 v44, v44, v0\n"
"v_mfma_f32_16x16x32_bf16 a[116:119], a[244:247], v[20:23], a[116:119]\n"
"v_exp_f32 v3, v3\n"
"v_mfma_f32_16x16x32_bf16 a[120:123], a[248:251], v[20:23], a[120:123]\n"
"ds_read_b128 a[184:187], v240 offset:12800\n"
"v_cvt_pk_bf16_f32 v17, v2, v3\n"
"v_mfma_f32_16x16x32_bf16 a[124:127], a[252:255], v[20:23], a[124:127]\n"
"v_exp_f32 v4, v4\n"
"v_mfma_f32_16x16x32_bf16 v[56:59], v[40:43], v[20:23], v[56:59]\n"
"ds_read_b128 a[188:191], v240 offset:13056\n"
"v_add_f32 v44, v44, v1\n"
"v_mfma_f32_16x16x32_bf16 v[8:11], a[128:131], v[80:83], v[28:31]\n"
"v_exp_f32 v5, v5\n"
"v_mfma_f32_16x16x32_bf16 v[8:11], a[132:135], v[84:87], v[8:11]\n"
"ds_read_b64_tr_b16 a[224:225], v242 offset:24576\n"
"v_cvt_pk_bf16_f32 v18, v4, v5\n"
"v_mfma_f32_16x16x32_bf16 v[8:11], a[136:139], v[88:91], v[8:11]\n"
"v_exp_f32 v6, v6\n"
"v_mfma_f32_16x16x32_bf16 v[8:11], a[140:143], v[92:95], v[8:11]\n"
"ds_read_b64_tr_b16 a[226:227], v242 offset:28672\n"
"v_add_f32 v44, v44, v2\n"
"v_mfma_f32_16x16x32_bf16 v[12:15], a[144:147], v[80:83], v[28:31]\n"
"v_exp_f32 v7, v7\n"
"v_mfma_f32_16x16x32_bf16 v[12:15], a[148:151], v[84:87], v[12:15]\n"
"ds_read_b64_tr_b16 a[228:229], v243 offset:24576\n"
"v_cvt_pk_bf16_f32 v19, v6, v7\n"
"v_mfma_f32_16x16x32_bf16 v[12:15], a[152:155], v[88:91], v[12:15]\n"
"buffer_load_dwordx4 v247, s[12:15], s84 offen offset:1024 lds\n"
"v_mfma_f32_16x16x32_bf16 v[12:15], a[156:159], v[92:95], v[12:15]\n"
"s_waitcnt lgkmcnt(11)\n"
"v_exp_f32 v8, v8\n"
"v_mfma_f32_16x16x32_bf16 a[0:3], a[192:195], v[16:19], a[0:3]\n"
"ds_read_b64_tr_b16 a[230:231], v243 offset:28672\n"
"v_add_f32 v44, v44, v3\n"
"v_mfma_f32_16x16x32_bf16 a[4:7], a[196:199], v[16:19], a[4:7]\n"
"v_exp_f32 v9, v9\n"
"v_mfma_f32_16x16x32_bf16 a[8:11], a[200:203], v[16:19], a[8:11]\n"
"ds_read_b64_tr_b16 a[232:233], v242 offset:24832\n"
"v_cvt_pk_bf16_f32 v20, v8, v9\n"
"v_mfma_f32_16x16x32_bf16 a[12:15], a[204:207], v[16:19], a[12:15]\n"
"v_exp_f32 v10, v10\n"
"v_mfma_f32_16x16x32_bf16 a[16:19], a[208:211], v[16:19], a[16:19]\n"
"ds_read_b64_tr_b16 a[234:235], v242 offset:28928\n"
"v_add_f32 v44, v44, v4\n"
"v_mfma_f32_16x16x32_bf16 a[20:23], a[212:215], v[16:19], a[20:23]\n"
"v_exp_f32 v11, v11\n"
"v_mfma_f32_16x16x32_bf16 a[24:27], a[216:219], v[16:19], a[24:27]\n"
"ds_read_b64_tr_b16 a[236:237], v243 offset:24832\n"
"v_cvt_pk_bf16_f32 v21, v10, v11\n"
"v_mfma_f32_16x16x32_bf16 a[28:31], a[220:223], v[16:19], a[28:31]\n"
"v_exp_f32 v12, v12\n"
"v_mfma_f32_16x16x32_bf16 v[220:223], a[128:131], v[96:99], v[32:35]\n"
"ds_read_b64_tr_b16 a[238:239], v243 offset:28928\n"
"v_add_f32 v44, v44, v5\n"
"v_mfma_f32_16x16x32_bf16 v[220:223], a[132:135], v[100:103], v[220:223]\n"
"v_exp_f32 v13, v13\n"
"v_mfma_f32_16x16x32_bf16 v[220:223], a[136:139], v[104:107], v[220:223]\n"
"ds_read_b64_tr_b16 a[240:241], v242 offset:25088\n"
"v_cvt_pk_bf16_f32 v22, v12, v13\n"
"v_mfma_f32_16x16x32_bf16 v[220:223], a[140:143], v[108:111], v[220:223]\n"
"v_exp_f32 v14, v14\n"
"v_mfma_f32_16x16x32_bf16 v[224:227], a[144:147], v[96:99], v[32:35]\n"
"ds_read_b64_tr_b16 a[242:243], v242 offset:29184\n"
"v_add_f32 v44, v44, v6\n"
"v_mfma_f32_16x16x32_bf16 v[224:227], a[148:151], v[100:103], v[224:227]\n"
"v_exp_f32 v15, v15\n"
"v_mfma_f32_16x16x32_bf16 v[224:227], a[152:155], v[104:107], v[224:227]\n"
"ds_read_b64_tr_b16 a[244:245], v243 offset:25088\n"
"v_cvt_pk_bf16_f32 v23, v14, v15\n"
"v_mfma_f32_16x16x32_bf16 v[224:227], a[156:159], v[108:111], v[224:227]\n"
"v_exp_f32 v220, v220\n"
"v_mfma_f32_16x16x32_bf16 a[32:35], a[192:195], v[20:23], a[32:35]\n"
"buffer_load_dwordx4 v246, s[12:15], s84 offen offset:2048 lds\n"
"v_mfma_f32_16x16x32_bf16 a[36:39], a[196:199], v[20:23], a[36:39]\n"
"v_exp_f32 v221, v221\n"
"v_mfma_f32_16x16x32_bf16 a[40:43], a[200:203], v[20:23], a[40:43]\n"
"ds_read_b64_tr_b16 a[246:247], v243 offset:29184\n"
"v_cvt_pk_bf16_f32 v16, v220, v221\n"
"v_mfma_f32_16x16x32_bf16 a[44:47], a[204:207], v[20:23], a[44:47]\n"
"v_exp_f32 v222, v222\n"
"v_mfma_f32_16x16x32_bf16 a[48:51], a[208:211], v[20:23], a[48:51]\n"
"ds_read_b64_tr_b16 a[248:249], v242 offset:25344\n"
"v_add_f32 v44, v44, v7\n"
"v_mfma_f32_16x16x32_bf16 a[52:55], a[212:215], v[20:23], a[52:55]\n"
"v_exp_f32 v223, v223\n"
"v_mfma_f32_16x16x32_bf16 a[56:59], a[216:219], v[20:23], a[56:59]\n"
"ds_read_b64_tr_b16 a[250:251], v242 offset:29440\n"
"v_cvt_pk_bf16_f32 v17, v222, v223\n"
"v_mfma_f32_16x16x32_bf16 a[60:63], a[220:223], v[20:23], a[60:63]\n"
"v_exp_f32 v224, v224\n"
"v_mfma_f32_16x16x32_bf16 v[48:51], v[40:43], v[20:23], v[48:51]\n"
"ds_read_b64_tr_b16 a[252:253], v243 offset:25344\n"
"v_add_f32 v52, v52, v220\n"
"v_mfma_f32_16x16x32_bf16 v[228:231], a[128:131], v[112:115], v[36:39]\n"
"v_exp_f32 v225, v225\n"
"v_mfma_f32_16x16x32_bf16 v[228:231], a[132:135], v[116:119], v[228:231]\n"
"ds_read_b64_tr_b16 a[254:255], v243 offset:29440\n"
"v_cvt_pk_bf16_f32 v18, v224, v225\n"
"v_mfma_f32_16x16x32_bf16 v[228:231], a[136:139], v[120:123], v[228:231]\n"
"v_exp_f32 v226, v226\n"
"v_mfma_f32_16x16x32_bf16 v[228:231], a[140:143], v[124:127], v[228:231]\n"
"v_add_f32 v52, v52, v221\n"
"v_add_f32 v52, v52, v222\n"
"v_mfma_f32_16x16x32_bf16 v[232:235], a[144:147], v[112:115], v[36:39]\n"
"v_exp_f32 v227, v227\n"
"v_mfma_f32_16x16x32_bf16 v[232:235], a[148:151], v[116:119], v[232:235]\n"
"v_cvt_pk_bf16_f32 v19, v226, v227\n"
"v_add_f32 v52, v52, v223\n"
"v_mfma_f32_16x16x32_bf16 v[232:235], a[152:155], v[120:123], v[232:235]\n"
"buffer_load_dwordx4 v247, s[12:15], s84 offen offset:3072 lds\n"
"v_mfma_f32_16x16x32_bf16 v[232:235], a[156:159], v[124:127], v[232:235]\n"
"s_add_u32 s84, s84, 16384\n"
"v_exp_f32 v228, v228\n"
"v_mfma_f32_16x16x32_bf16 a[64:67], a[192:195], v[16:19], a[64:67]\n"
"s_sub_u32 s87, s87, 1\n"
"v_add_f32 v52, v52, v224\n"
"v_add_f32 v52, v52, v225\n"
"v_mfma_f32_16x16x32_bf16 a[68:71], a[196:199], v[16:19], a[68:71]\n"
"s_cmp_lg_u32 s87, 0\n"
"v_exp_f32 v229, v229\n"
"v_mfma_f32_16x16x32_bf16 a[72:75], a[200:203], v[16:19], a[72:75]\n"
"v_cvt_pk_bf16_f32 v20, v228, v229\n"
"v_add_f32 v52, v52, v226\n"
"v_mfma_f32_16x16x32_bf16 a[76:79], a[204:207], v[16:19], a[76:79]\n"
"s_waitcnt vmcnt(20) lgkmcnt(0)\n"
"v_exp_f32 v230, v230\n"
"v_mfma_f32_16x16x32_bf16 a[80:83], a[208:211], v[16:19], a[80:83]\n"
"s_barrier\n"
"v_mfma_f32_16x16x32_bf16 a[84:87], a[212:215], v[16:19], a[84:87]\n"
"v_exp_f32 v231, v231\n"
"v_mfma_f32_16x16x32_bf16 a[88:91], a[216:219], v[16:19], a[88:91]\n"
"v_cvt_pk_bf16_f32 v21, v230, v231\n"
"v_add_f32 v52, v52, v227\n"
"v_mfma_f32_16x16x32_bf16 a[92:95], a[220:223], v[16:19], a[92:95]\n"
"v_exp_f32 v232, v232\n"
"s_cbranch_scc1 AT_loop\n"
"s_cmp_lg_u32 s56, 0\n"
"s_cbranch_scc1 AT_h3\n"
"s_mov_b32 s61, 0x80000\n"
"AT_h2:\n"
"v_xor_b32 v210, s85, v210\n"
"v_xor_b32 v211, s85, v211\n"
"v_xor_b32 v212, s85, v212\n"
"v_xor_b32 v213, s85, v213\n"
"v_or3_b32 v210, v210, v211, v212\n"
"v_or_b32 v210, v210, v213\n"
"v_cmp_ne_u32 vcc, 0, v210\n"
"s_sub_u32 s61, s61, 1\n"
"s_cbranch_vccz AT_h4\n"
"s_cmp_eq_u32 s61, 0\n"
"s_cbranch_scc1 AT_h4\n"
"s_sleep 2\n"
"buffer_load_dword v210, v214, s[28:31], s57 offen offset:4 sc1\n"
"buffer_load_dword v211, v214, s[28:31], s58 offen offset:4 sc1\n"
"buffer_load_dword v212, v214, s[28:31], s59 offen offset:4 sc1\n"
"buffer_load_dword v213, v214, s[28:31], s60 offen offset:4 sc1\n"
"s_waitcnt vmcnt(0)\n"
"s_branch AT_h2\n"
"AT_h4:\n"
"v_mov_b32 v210, s85\n"
"v_mov_b32 v211, 65536\n"
"s_mov_b64 exec, 1\n"
"buffer_store_dword v210, v211, s[28:31], 0 offen sc1\n"
"s_mov_b64 exec, -1\n"
"AT_h3:\n"
"v_mfma_f32_16x16x32_bf16 v[0:3], a[160:163], v[64:67], v[24:27]\n"
"ds_read_b128 a[128:131], v240 offset:32768\n"
"buffer_load_dword v250, v255, s[32:35], 0 offen\n"
"v_mfma_f32_16x16x32_bf16 v[0:3], a[164:167], v[68:71], v[0:3]\n"
"v_exp_f32 v233, v233\n"
"v_mfma_f32_16x16x32_bf16 v[0:3], a[168:171], v[72:75], v[0:3]\n"
"ds_read_b128 a[132:135], v240 offset:33024\n"
"v_cvt_pk_bf16_f32 v22, v232, v233\n"
"v_mfma_f32_16x16x32_bf16 v[0:3], a[172:175], v[76:79], v[0:3]\n"
"v_exp_f32 v234, v234\n"
"v_mfma_f32_16x16x32_bf16 v[4:7], a[176:179], v[64:67], v[24:27]\n"
"ds_read_b128 a[136:139], v240 offset:33280\n"
"v_lshlrev_b32 v216, 6, v248\n"
"v_mfma_f32_16x16x32_bf16 v[4:7], a[180:183], v[68:71], v[4:7]\n"
"s_and_b32 s48, s42, 7\n"
"v_exp_f32 v235, v235\n"
"v_mfma_f32_16x16x32_bf16 v[4:7], a[184:187], v[72:75], v[4:7]\n"
"s_mul_i32 s48, s48, 576\n"
"ds_read_b128 a[140:143], v240 offset:33536\n"
"v_cvt_pk_bf16_f32 v23, v234, v235\n"
"v_mfma_f32_16x16x32_bf16 v[4:7], a[188:191], v[76:79], v[4:7]\n"
"s_mov_b32 m0, s86\n"
"v_exp_f32 v0, v0\n"
"v_mfma_f32_16x16x32_bf16 a[96:99], a[192:195], v[20:23], a[96:99]\n"
"s_lshr_b32 s62, s42, 3\n"
"buffer_load_dwordx4 v246, s[8:11], s84 offen offset:0 lds\n"
"v_mfma_f32_16x16x32_bf16 a[100:103], a[196:199], v[20:23], a[100:103]\n"
"s_mul_i32 s62, s62, 24\n"
"v_exp_f32 v1, v1\n"
"v_mfma_f32_16x16x32_bf16 a[104:107], a[200:203], v[20:23], a[104:107]\n"
"s_add_u32 s48, s48, s62\n"
"ds_read_b128 a[144:147], v240 offset:36864\n"
"v_cvt_pk_bf16_f32 v16, v0, v1\n"
"v_mfma_f32_16x16x32_bf16 a[108:111], a[204:207], v[20:23], a[108:111]\n"
"s_cmp_lt_u32 s42, 192\n"
"s_cselect_b32 s74, s74, 0\n"
"s_cselect_b32 s66, s66, 0\n"
"s_cselect_b32 s70, s70, 0\n"
"v_exp_f32 v2, v2\n"
"v_mfma_f32_16x16x32_bf16 a[112:115], a[208:211], v[20:23], a[112:115]\n"
"s_mul_i32 s50, s48, 1152\n"
"ds_read_b128 a[148:151], v240 offset:37120\n"
"v_add_f32 v44, v44, v0\n"
"v_mfma_f32_16x16x32_bf16 a[116:119], a[212:215], v[20:23], a[116:119]\n"
"s_lshl_b32 s62, s41, 13\n"
"v_exp_f32 v3, v3\n"
"v_mfma_f32_16x16x32_bf16 a[120:123], a[216:219], v[20:23], a[120:123]\n"
"s_add_u32 s46, s50, s62\n"
"ds_read_b128 a[152:155], v240 offset:37376\n"
"v_cvt_pk_bf16_f32 v17, v2, v3\n"
"v_mfma_f32_16x16x32_bf16 a[124:127], a[220:223], v[20:23], a[124:127]\n"
"s_add_u32 s47, s46, 4096\n"
"v_exp_f32 v4, v4\n"
"v_mfma_f32_16x16x32_bf16 v[56:59], v[40:43], v[20:23], v[56:59]\n"
"ds_read_b128 a[156:159], v240 offset:37632\n"
"v_add_f32 v44, v44, v1\n"
"v_mfma_f32_16x16x32_bf16 v[8:11], a[160:163], v[80:83], v[28:31]\n"
"v_exp_f32 v5, v5\n"
"v_mfma_f32_16x16x32_bf16 v[8:11], a[164:167], v[84:87], v[8:11]\n"
"ds_read_b64_tr_b16 a[192:193], v242 offset:49152\n"
"v_cvt_pk_bf16_f32 v18, v4, v5\n"
"v_mfma_f32_16x16x32_bf16 v[8:11], a[168:171], v[88:91], v[8:11]\n"
"v_exp_f32 v6, v6\n"
"v_mfma_f32_16x16x32_bf16 v[8:11], a[172:175], v[92:95], v[8:11]\n"
"ds_read_b64_tr_b16 a[194:195], v242 offset:53248\n"
"v_add_f32 v44, v44, v2\n"
"v_mfma_f32_16x16x32_bf16 v[12:15], a[176:179], v[80:83], v[28:31]\n"
"v_exp_f32 v7, v7\n"
"v_mfma_f32_16x16x32_bf16 v[12:15], a[180:183], v[84:87], v[12:15]\n"
"ds_read_b64_tr_b16 a[196:197], v243 offset:49152\n"
"v_cvt_pk_bf16_f32 v19, v6, v7\n"
"v_mfma_f32_16x16x32_bf16 v[12:15], a[184:187], v[88:91], v[12:15]\n"
"buffer_load_dwordx4 v246, s[8:11], s84 offen offset:1024 lds\n"
"v_mfma_f32_16x16x32_bf16 v[12:15], a[188:191], v[92:95], v[12:15]\n"
"s_waitcnt lgkmcnt(11)\n"
"v_exp_f32 v8, v8\n"
"v_mfma_f32_16x16x32_bf16 a[0:3], a[224:227], v[16:19], a[0:3]\n"
"ds_read_b64_tr_b16 a[198:199], v243 offset:53248\n"
"v_add_f32 v44, v44, v3\n"
"v_mfma_f32_16x16x32_bf16 a[4:7], a[228:231], v[16:19], a[4:7]\n"
"v_exp_f32 v9, v9\n"
"v_mfma_f32_16x16x32_bf16 a[8:11], a[232:235], v[16:19], a[8:11]\n"
"ds_read_b64_tr_b16 a[200:201], v242 offset:49408\n"
"v_cvt_pk_bf16_f32 v20, v8, v9\n"
"v_mfma_f32_16x16x32_bf16 a[12:15], a[236:239], v[16:19], a[12:15]\n"
"v_exp_f32 v10, v10\n"
"v_mfma_f32_16x16x32_bf16 a[16:19], a[240:243], v[16:19], a[16:19]\n"
"ds_read_b64_tr_b16 a[202:203], v242 offset:53504\n"
"buffer_load_dword v250, v216, s[72:75], s46 offen\n"
"v_add_f32 v44, v44, v4\n"
"v_mfma_f32_16x16x32_bf16 a[20:23], a[244:247], v[16:19], a[20:23]\n"
"v_exp_f32 v11, v11\n"
"v_mfma_f32_16x16x32_bf16 a[24:27], a[248:251], v[16:19], a[24:27]\n"
"ds_read_b64_tr_b16 a[204:205], v243 offset:49408\n"
"v_cvt_pk_bf16_f32 v21, v10, v11\n"
"v_mfma_f32_16x16x32_bf16 a[28:31], a[252:255], v[16:19], a[28:31]\n"
"v_exp_f32 v12, v12\n"
"v_mfma_f32_16x16x32_bf16 v[220:223], a[160:163], v[96:99], v[32:35]\n"
"ds_read_b64_tr_b16 a[206:207], v243 offset:53504\n"
"v_add_f32 v44, v44, v5\n"
"v_mfma_f32_16x16x32_bf16 v[220:223], a[164:167], v[100:103], v[220:223]\n"
"v_exp_f32 v13, v13\n"
"v_mfma_f32_16x16x32_bf16 v[220:223], a[168:171], v[104:107], v[220:223]\n"
"ds_read_b64_tr_b16 a[208:209], v242 offset:49664\n"
"v_cvt_pk_bf16_f32 v22, v12, v13\n"
"v_mfma_f32_16x16x32_bf16 v[220:223], a[172:175], v[108:111], v[220:223]\n"
"v_exp_f32 v14, v14\n"
"v_mfma_f32_16x16x32_bf16 v[224:227], a[176:179], v[96:99], v[32:35]\n"
"ds_read_b64_tr_b16 a[210:211], v242 offset:53760\n"
"v_add_f32 v44, v44, v6\n"
"v_mfma_f32_16x16x32_bf16 v[224:227], a[180:183], v[100:103], v[224:227]\n"
"v_exp_f32 v15, v15\n"
"v_mfma_f32_16x16x32_bf16 v[224:227], a[184:187], v[104:107], v[224:227]\n"
"ds_read_b64_tr_b16 a[212:213], v243 offset:49664\n"
"v_cvt_pk_bf16_f32 v23, v14, v15\n"
"v_mfma_f32_16x16x32_bf16 v[224:227], a[188:191], v[108:111], v[224:227]\n"
"v_exp_f32 v220, v220\n"
"v_mfma_f32_16x16x32_bf16 a[32:35], a[224:227], v[20:23], a[32:35]\n"
"buffer_load_dwordx4 v247, s[8:11], s84 offen offset:2048 lds\n"
"v_mfma_f32_16x16x32_bf16 a[36:39], a[228:231], v[20:23], a[36:39]\n"
"v_exp_f32 v221, v221\n"
"v_mfma_f32_16x16x32_bf16 a[40:43], a[232:235], v[20:23], a[40:43]\n"
"ds_read_b64_tr_b16 a[214:215], v243 offset:53760\n"
"v_cvt_pk_bf16_f32 v16, v220, v221\n"
"v_mfma_f32_16x16x32_bf16 a[44:47], a[236:239], v[20:23], a[44:47]\n"
"v_exp_f32 v222, v222\n"
"v_mfma_f32_16x16x32_bf16 a[48:51], a[240:243], v[20:23], a[48:51]\n"
"ds_read_b64_tr_b16 a[216:217], v242 offset:49920\n"
"v_add_f32 v44, v44, v7\n"
"v_mfma_f32_16x16x32_bf16 a[52:55], a[244:247], v[20:23], a[52:55]\n"
"v_exp_f32 v223, v223\n"
"v_mfma_f32_16x16x32_bf16 a[56:59], a[248:251], v[20:23], a[56:59]\n"
"ds_read_b64_tr_b16 a[218:219], v242 offset:54016\n"
"v_cvt_pk_bf16_f32 v17, v222, v223\n"
"v_mfma_f32_16x16x32_bf16 a[60:63], a[252:255], v[20:23], a[60:63]\n"
"v_exp_f32 v224, v224\n"
"v_mfma_f32_16x16x32_bf16 v[48:51], v[40:43], v[20:23], v[48:51]\n"
"ds_read_b64_tr_b16 a[220:221], v243 offset:49920\n"
"v_add_f32 v52, v52, v220\n"
"v_mfma_f32_16x16x32_bf16 v[228:231], a[160:163], v[112:115], v[36:39]\n"
"v_exp_f32 v225, v225\n"
"v_mfma_f32_16x16x32_bf16 v[228:231], a[164:167], v[116:119], v[228:231]\n"
"ds_read_b64_tr_b16 a[222:223], v243 offset:54016\n"
"v_cvt_pk_bf16_f32 v18, v224, v225\n"
"v_mfma_f32_16x16x32_bf16 v[228:231], a[168:171], v[120:123], v[228:231]\n"
"v_exp_f32 v226, v226\n"
"v_mfma_f32_16x16x32_bf16 v[228:231], a[172:175], v[124:127], v[228:231]\n"
"v_add_f32 v52, v52, v221\n"
"v_add_f32 v52, v52, v222\n"
"v_mfma_f32_16x16x32_bf16 v[232:235], a[176:179], v[112:115], v[36:39]\n"
"v_exp_f32 v227, v227\n"
"v_mfma_f32_16x16x32_bf16 v[232:235], a[180:183], v[116:119], v[232:235]\n"
"v_cvt_pk_bf16_f32 v19, v226, v227\n"
"v_add_f32 v52, v52, v223\n"
"v_mfma_f32_16x16x32_bf16 v[232:235], a[184:187], v[120:123], v[232:235]\n"
"buffer_load_dwordx4 v247, s[8:11], s84 offen offset:3072 lds\n"
"v_mfma_f32_16x16x32_bf16 v[232:235], a[188:191], v[124:127], v[232:235]\n"
"v_exp_f32 v228, v228\n"
"v_mfma_f32_16x16x32_bf16 a[64:67], a[224:227], v[16:19], a[64:67]\n"
"buffer_load_dword v250, v216, s[72:75], s47 offen\n"
"v_add_f32 v52, v52, v224\n"
"v_add_f32 v52, v52, v225\n"
"v_mfma_f32_16x16x32_bf16 a[68:71], a[228:231], v[16:19], a[68:71]\n"
"v_exp_f32 v229, v229\n"
"v_mfma_f32_16x16x32_bf16 a[72:75], a[232:235], v[16:19], a[72:75]\n"
"v_cvt_pk_bf16_f32 v20, v228, v229\n"
"v_add_f32 v52, v52, v226\n"
"v_mfma_f32_16x16x32_bf16 a[76:79], a[236:239], v[16:19], a[76:79]\n"
"v_exp_f32 v230, v230\n"
"v_mfma_f32_16x16x32_bf16 a[80:83], a[240:243], v[16:19], a[80:83]\n"
"s_waitcnt lgkmcnt(8)\n"
"v_lshrrev_b32 v251, 2, v248\n"
"v_mfma_f32_16x16x32_bf16 a[84:87], a[244:247], v[16:19], a[84:87]\n"
"v_exp_f32 v231, v231\n"
"v_mfma_f32_16x16x32_bf16 a[88:91], a[248:251], v[16:19], a[88:91]\n"
"v_cvt_pk_bf16_f32 v21, v230, v231\n"
"v_add_f32 v52, v52, v227\n"
"v_mfma_f32_16x16x32_bf16 a[92:95], a[252:255], v[16:19], a[92:95]\n"
"v_exp_f32 v232, v232\n"
"v_mfma_f32_16x16x32_bf16 v[0:3], a[128:131], v[64:67], v[24:27]\n"
"ds_read_b128 a[160:163], v240 offset:40960\n"
"v_mul_u32_u24 v251, 9216, v251\n"
"v_mfma_f32_16x16x32_bf16 v[0:3], a[132:135], v[68:71], v[0:3]\n"
"v_exp_f32 v233, v233\n"
"v_mfma_f32_16x16x32_bf16 v[0:3], a[136:139], v[72:75], v[0:3]\n"
"ds_read_b128 a[164:167], v240 offset:41216\n"
"v_cvt_pk_bf16_f32 v22, v232, v233\n"
"v_mfma_f32_16x16x32_bf16 v[0:3], a[140:143], v[76:79], v[0:3]\n"
"v_exp_f32 v234, v234\n"
"v_mfma_f32_16x16x32_bf16 v[4:7], a[144:147], v[64:67], v[24:27]\n"
"ds_read_b128 a[168:171], v240 offset:41472\n"
"v_and_b32 v217, 3, v248\n"
"v_mfma_f32_16x16x32_bf16 v[4:7], a[148:151], v[68:71], v[4:7]\n"
"v_exp_f32 v235, v235\n"
"v_mfma_f32_16x16x32_bf16 v[4:7], a[152:155], v[72:75], v[4:7]\n"
"ds_read_b128 a[172:175], v240 offset:41728\n"
"v_cvt_pk_bf16_f32 v23, v234, v235\n"
"v_mfma_f32_16x16x32_bf16 v[4:7], a[156:159], v[76:79], v[4:7]\n"
"s_add_u32 m0, s86, 16384\n"
"v_exp_f32 v0, v0\n"
"v_mfma_f32_16x16x32_bf16 a[96:99], a[224:227], v[20:23], a[96:99]\n"
"buffer_load_dwordx4 v246, s[12:15], s84 offen offset:0 lds\n"
"v_add_u32 v255, 1, v217\n"
"v_mfma_f32_16x16x32_bf16 a[100:103], a[228:231], v[20:23], a[100:103]\n"
"v_exp_f32 v1, v1\n"
"v_mfma_f32_16x16x32_bf16 a[104:107], a[232:235], v[20:23], a[104:107]\n"
"ds_read_b128 a[176:179], v240 offset:45056\n"
"v_cvt_pk_bf16_f32 v16, v0, v1\n"
"v_mfma_f32_16x16x32_bf16 a[108:111], a[236:239], v[20:23], a[108:111]\n"
"v_exp_f32 v2, v2\n"
"v_mfma_f32_16x16x32_bf16 a[112:115], a[240:243], v[20:23], a[112:115]\n"
"ds_read_b128 a[180:183], v240 offset:45312\n"
"v_add_f32 v44, v44, v0\n"
"v_mfma_f32_16x16x32_bf16 a[116:119], a[244:247], v[20:23], a[116:119]\n"
"v_exp_f32 v3, v3\n"
"v_mfma_f32_16x16x32_bf16 a[120:123], a[248:251], v[20:23], a[120:123]\n"
"ds_read_b128 a[184:187], v240 offset:45568\n"
"v_cvt_pk_bf16_f32 v17, v2, v3\n"
"v_mfma_f32_16x16x32_bf16 a[124:127], a[252:255], v[20:23], a[124:127]\n"
"v_exp_f32 v4, v4\n"
"v_and_b32 v255, 4, v255\n"
"v_mfma_f32_16x16x32_bf16 v[56:59], v[40:43], v[20:23], v[56:59]\n"
"ds_read_b128 a[188:191], v240 offset:45824\n"
"v_add_f32 v44, v44, v1\n"
"v_mfma_f32_16x16x32_bf16 v[8:11], a[128:131], v[80:83], v[28:31]\n"
"v_exp_f32 v5, v5\n"
"v_mfma_f32_16x16x32_bf16 v[8:11], a[132:135], v[84:87], v[8:11]\n"
"ds_read_b64_tr_b16 a[224:225], v242 offset:57344\n"
"v_cvt_pk_bf16_f32 v18, v4, v5\n"
"v_mfma_f32_16x16x32_bf16 v[8:11], a[136:139], v[88:91], v[8:11]\n"
"v_exp_f32 v6, v6\n"
"v_mfma_f32_16x16x32_bf16 v[8:11], a[140:143], v[92:95], v[8:11]\n"
"ds_read_b64_tr_b16 a[226:227], v242 offset:61440\n"
"v_add_f32 v44, v44, v2\n"
"v_mfma_f32_16x16x32_bf16 v[12:15], a[144:147], v[80:83], v[28:31]\n"
"v_exp_f32 v7, v7\n"
"v_mfma_f32_16x16x32_bf16 v[12:15], a[148:151], v[84:87], v[12:15]\n"
"ds_read_b64_tr_b16 a[228:229], v243 offset:57344\n"
"v_cvt_pk_bf16_f32 v19, v6, v7\n"
"v_lshlrev_b32 v255, 26, v255\n"
"v_mfma_f32_16x16x32_bf16 v[12:15], a[152:155], v[88:91], v[12:15]\n"
"buffer_load_dwordx4 v247, s[12:15], s84 offen offset:1024 lds\n"
"v_lshl_add_u32 v217, v217, 4, v255\n"
"v_mfma_f32_16x16x32_bf16 v[12:15], a[156:159], v[92:95], v[12:15]\n"
"s_waitcnt lgkmcnt(11)\n"
"v_exp_f32 v8, v8\n"
"v_mfma_f32_16x16x32_bf16 a[0:3], a[192:195], v[16:19], a[0:3]\n"
"ds_read_b64_tr_b16 a[230:231], v243 offset:61440\n"
"v_add_f32 v44, v44, v3\n"
"v_mfma_f32_16x16x32_bf16 a[4:7], a[196:199], v[16:19], a[4:7]\n"
"v_exp_f32 v9, v9\n"
"v_mfma_f32_16x16x32_bf16 a[8:11], a[200:203], v[16:19], a[8:11]\n"
"ds_read_b64_tr_b16 a[232:233], v242 offset:57600\n"
"v_cvt_pk_bf16_f32 v20, v8, v9\n"
"v_mfma_f32_16x16x32_bf16 a[12:15], a[204:207], v[16:19], a[12:15]\n"
"v_exp_f32 v10, v10\n"
"v_mfma_f32_16x16x32_bf16 a[16:19], a[208:211], v[16:19], a[16:19]\n"
"ds_read_b64_tr_b16 a[234:235], v242 offset:61696\n"
"v_add_f32 v44, v44, v4\n"
"v_mfma_f32_16x16x32_bf16 a[20:23], a[212:215], v[16:19], a[20:23]\n"
"v_exp_f32 v11, v11\n"
"v_lshl_add_u32 v252, v251, 1, v217\n"
"v_mfma_f32_16x16x32_bf16 a[24:27], a[216:219], v[16:19], a[24:27]\n"
"ds_read_b64_tr_b16 a[236:237], v243 offset:57600\n"
"v_cvt_pk_bf16_f32 v21, v10, v11\n"
"v_mfma_f32_16x16x32_bf16 a[28:31], a[220:223], v[16:19], a[28:31]\n"
"v_exp_f32 v12, v12\n"
"v_mfma_f32_16x16x32_bf16 v[220:223], a[128:131], v[96:99], v[32:35]\n"
"ds_read_b64_tr_b16 a[238:239], v243 offset:61696\n"
"v_add_f32 v44, v44, v5\n"
"v_mfma_f32_16x16x32_bf16 v[220:223], a[132:135], v[100:103], v[220:223]\n"
"v_exp_f32 v13, v13\n"
"v_mfma_f32_16x16x32_bf16 v[220:223], a[136:139], v[104:107], v[220:223]\n"
"ds_read_b64_tr_b16 a[240:241], v242 offset:57856\n"
"v_cvt_pk_bf16_f32 v22, v12, v13\n"
"v_mfma_f32_16x16x32_bf16 v[220:223], a[140:143], v[108:111], v[220:223]\n"
"v_exp_f32 v14, v14\n"
"v_mfma_f32_16x16x32_bf16 v[224:227], a[144:147], v[96:99], v[32:35]\n"
"ds_read_b64_tr_b16 a[242:243], v242 offset:61952\n"
"v_add_f32 v44, v44, v6\n"
"v_add_u32 v251, v251, v217\n"
"v_mfma_f32_16x16x32_bf16 v[224:227], a[148:151], v[100:103], v[224:227]\n"
"s_mul_i32 s62, s41, 1327104\n"
"v_exp_f32 v15, v15\n"
"v_mfma_f32_16x16x32_bf16 v[224:227], a[152:155], v[104:107], v[224:227]\n"
"s_lshl_b32 s49, s48, 1\n"
"ds_read_b64_tr_b16 a[244:245], v243 offset:57856\n"
"v_cvt_pk_bf16_f32 v23, v14, v15\n"
"v_mfma_f32_16x16x32_bf16 v[224:227], a[156:159], v[108:111], v[224:227]\n"
"s_add_u32 s49, s49, s62\n"
"v_exp_f32 v220, v220\n"
"v_mfma_f32_16x16x32_bf16 a[32:35], a[192:195], v[20:23], a[32:35]\n"
"buffer_load_dwordx4 v246, s[12:15], s84 offen offset:2048 lds\n"
"v_mfma_f32_16x16x32_bf16 a[36:39], a[196:199], v[20:23], a[36:39]\n"
"v_exp_f32 v221, v221\n"
"v_mfma_f32_16x16x32_bf16 a[40:43], a[200:203], v[20:23], a[40:43]\n"
"ds_read_b64_tr_b16 a[246:247], v243 offset:61952\n"
"buffer_load_dwordx4 v[128:131], v252, s[64:67], s49 offen\n"
"v_cvt_pk_bf16_f32 v16, v220, v221\n"
"v_mfma_f32_16x16x32_bf16 a[44:47], a[204:207], v[20:23], a[44:47]\n"
"v_exp_f32 v222, v222\n"
"v_mfma_f32_16x16x32_bf16 a[48:51], a[208:211], v[20:23], a[48:51]\n"
"ds_read_b64_tr_b16 a[248:249], v242 offset:58112\n"
"v_add_f32 v44, v44, v7\n"
"v_mfma_f32_16x16x32_bf16 a[52:55], a[212:215], v[20:23], a[52:55]\n"
"v_exp_f32 v223, v223\n"
"v_mfma_f32_16x16x32_bf16 a[56:59], a[216:219], v[20:23], a[56:59]\n"
"ds_read_b64_tr_b16 a[250:251], v242 offset:62208\n"
"v_cvt_pk_bf16_f32 v17, v222, v223\n"
"v_mfma_f32_16x16x32_bf16 a[60:63], a[220:223], v[20:23], a[60:63]\n"
"v_exp_f32 v224, v224\n"
"v_mfma_f32_16x16x32_bf16 v[48:51], v[40:43], v[20:23], v[48:51]\n"
"ds_read_b64_tr_b16 a[252:253], v243 offset:58112\n"
"v_add_f32 v52, v52, v220\n"
"v_mfma_f32_16x16x32_bf16 v[228:231], a[128:131], v[112:115], v[36:39]\n"
"v_exp_f32 v225, v225\n"
"v_mul_u32_u24 v249, 3641, v248\n"
"v_mfma_f32_16x16x32_bf16 v[228:231], a[132:135], v[116:119], v[228:231]\n"
"s_movk_i32 s60, 864\n"
"ds_read_b64_tr_b16 a[254:255], v243 offset:62208\n"
"v_cvt_pk_bf16_f32 v18, v224, v225\n"
"v_mfma_f32_16x16x32_bf16 v[228:231], a[136:139], v[120:123], v[228:231]\n"
"v_exp_f32 v226, v226\n"
"v_mfma_f32_16x16x32_bf16 v[228:231], a[140:143], v[124:127], v[228:231]\n"
"v_add_f32 v52, v52, v221\n"
"v_add_f32 v52, v52, v222\n"
"v_mfma_f32_16x16x32_bf16 v[232:235], a[144:147], v[112:115], v[36:39]\n"
"v_exp_f32 v227, v227\n"
"v_mfma_f32_16x16x32_bf16 v[232:235], a[148:151], v[116:119], v[232:235]\n"
"v_cvt_pk_bf16_f32 v19, v226, v227\n"
"v_add_f32 v52, v52, v223\n"
"v_mfma_f32_16x16x32_bf16 v[232:235], a[152:155], v[120:123], v[232:235]\n"
"buffer_load_dwordx4 v247, s[12:15], s84 offen offset:3072 lds\n"
"v_add_u32 v206, 4096, v207\n"
"v_mfma_f32_16x16x32_bf16 v[232:235], a[156:159], v[124:127], v[232:235]\n"
"s_add_u32 s84, s84, 16384\n"
"v_exp_f32 v228, v228\n"
"v_mfma_f32_16x16x32_bf16 a[64:67], a[192:195], v[16:19], a[64:67]\n"
"v_add_f32 v52, v52, v224\n"
"v_add_f32 v52, v52, v225\n"
"v_mfma_f32_16x16x32_bf16 a[68:71], a[196:199], v[16:19], a[68:71]\n"
"v_exp_f32 v229, v229\n"
"v_mfma_f32_16x16x32_bf16 a[72:75], a[200:203], v[16:19], a[72:75]\n"
"v_cvt_pk_bf16_f32 v20, v228, v229\n"
"v_add_f32 v52, v52, v226\n"
"v_mfma_f32_16x16x32_bf16 a[76:79], a[204:207], v[16:19], a[76:79]\n"
"s_waitcnt vmcnt(20) lgkmcnt(0)\n"
"v_exp_f32 v230, v230\n"
"v_mfma_f32_16x16x32_bf16 a[80:83], a[208:211], v[16:19], a[80:83]\n"
"s_barrier\n"
"v_mfma_f32_16x16x32_bf16 a[84:87], a[212:215], v[16:19], a[84:87]\n"
"v_exp_f32 v231, v231\n"
"v_mfma_f32_16x16x32_bf16 a[88:91], a[216:219], v[16:19], a[88:91]\n"
"v_cvt_pk_bf16_f32 v21, v230, v231\n"
"v_add_f32 v52, v52, v227\n"
"v_lshrrev_b32 v217, 16, v249\n"
"v_mfma_f32_16x16x32_bf16 a[92:95], a[220:223], v[16:19], a[92:95]\n"
"v_exp_f32 v232, v232\n"
"v_mfma_f32_16x16x32_bf16 v[0:3], a[160:163], v[64:67], v[24:27]\n"
"ds_read_b128 a[128:131], v241 offset:0\n"
"v_mad_u32_u24 v210, v217, s60, v207\n"
"v_mfma_f32_16x16x32_bf16 v[0:3], a[164:167], v[68:71], v[0:3]\n"
"v_exp_f32 v233, v233\n"
"v_mfma_f32_16x16x32_bf16 v[0:3], a[168:171], v[72:75], v[0:3]\n"
"ds_read_b128 a[132:135], v241 offset:256\n"
"v_cvt_pk_bf16_f32 v22, v232, v233\n"
"v_mfma_f32_16x16x32_bf16 v[0:3], a[172:175], v[76:79], v[0:3]\n"
"v_exp_f32 v234, v234\n"
"v_mfma_f32_16x16x32_bf16 v[4:7], a[176:179], v[64:67], v[24:27]\n"
"ds_read_b128 a[136:139], v241 offset:512\n"
"v_add_u32 v249, 233024, v249\n"
"v_mfma_f32_16x16x32_bf16 v[4:7], a[180:183], v[68:71], v[4:7]\n"
"v_exp_f32 v235, v235\n"
"v_mfma_f32_16x16x32_bf16 v[4:7], a[184:187], v[72:75], v[4:7]\n"
"ds_read_b128 a[140:143], v241 offset:768\n"
"v_cvt_pk_bf16_f32 v23, v234, v235\n"
"v_mfma_f32_16x16x32_bf16 v[4:7], a[188:191], v[76:79], v[4:7]\n"
"s_add_u32 m0, s86, 32768\n"
"v_exp_f32 v0, v0\n"
"v_mfma_f32_16x16x32_bf16 a[96:99], a[192:195], v[20:23], a[96:99]\n"
"buffer_load_dwordx4 v246, s[8:11], s84 offen offset:0 lds\n"
"v_lshrrev_b32 v217, 16, v249\n"
"v_mfma_f32_16x16x32_bf16 a[100:103], a[196:199], v[20:23], a[100:103]\n"
"v_exp_f32 v1, v1\n"
"v_mfma_f32_16x16x32_bf16 a[104:107], a[200:203], v[20:23], a[104:107]\n"
"ds_read_b128 a[144:147], v241 offset:4096\n"
"v_cvt_pk_bf16_f32 v16, v0, v1\n"
"v_mfma_f32_16x16x32_bf16 a[108:111], a[204:207], v[20:23], a[108:111]\n"
"v_exp_f32 v2, v2\n"
"v_mfma_f32_16x16x32_bf16 a[112:115], a[208:211], v[20:23], a[112:115]\n"
"ds_read_b128 a[148:151], v241 offset:4352\n"
"v_add_f32 v44, v44, v0\n"
"v_mfma_f32_16x16x32_bf16 a[116:119], a[212:215], v[20:23], a[116:119]\n"
"v_exp_f32 v3, v3\n"
"v_mfma_f32_16x16x32_bf16 a[120:123], a[216:219], v[20:23], a[120:123]\n"
"ds_read_b128 a[152:155], v241 offset:4608\n"
"v_cvt_pk_bf16_f32 v17, v2, v3\n"
"v_mfma_f32_16x16x32_bf16 a[124:127], a[220:223], v[20:23], a[124:127]\n"
"v_exp_f32 v4, v4\n"
"v_mad_u32_u24 v211, v217, s60, v207\n"
"v_mfma_f32_16x16x32_bf16 v[56:59], v[40:43], v[20:23], v[56:59]\n"
"ds_read_b128 a[156:159], v241 offset:4864\n"
"v_add_f32 v44, v44, v1\n"
"v_mfma_f32_16x16x32_bf16 v[8:11], a[160:163], v[80:83], v[28:31]\n"
"v_exp_f32 v5, v5\n"
"v_mfma_f32_16x16x32_bf16 v[8:11], a[164:167], v[84:87], v[8:11]\n"
"ds_read_b64_tr_b16 a[192:193], v244 offset:16384\n"
"v_cvt_pk_bf16_f32 v18, v4, v5\n"
"v_mfma_f32_16x16x32_bf16 v[8:11], a[168:171], v[88:91], v[8:11]\n"
"v_exp_f32 v6, v6\n"
"v_mfma_f32_16x16x32_bf16 v[8:11], a[172:175], v[92:95], v[8:11]\n"
"ds_read_b64_tr_b16 a[194:195], v244 offset:20480\n"
"v_add_f32 v44, v44, v2\n"
"v_mfma_f32_16x16x32_bf16 v[12:15], a[176:179], v[80:83], v[28:31]\n"
"v_exp_f32 v7, v7\n"
"v_mfma_f32_16x16x32_bf16 v[12:15], a[180:183], v[84:87], v[12:15]\n"
"ds_read_b64_tr_b16 a[196:197], v245 offset:16384\n"
"v_cvt_pk_bf16_f32 v19, v6, v7\n"
"v_add_u32 v249, 233024, v249\n"
"v_mfma_f32_16x16x32_bf16 v[12:15], a[184:187], v[88:91], v[12:15]\n"
"buffer_load_dwordx4 v246, s[8:11], s84 offen offset:1024 lds\n"
"v_lshrrev_b32 v217, 16, v249\n"
"v_mfma_f32_16x16x32_bf16 v[12:15], a[188:191], v[92:95], v[12:15]\n"
"s_waitcnt lgkmcnt(11)\n"
"v_exp_f32 v8, v8\n"
"v_mfma_f32_16x16x32_bf16 a[0:3], a[224:227], v[16:19], a[0:3]\n"
"ds_read_b64_tr_b16 a[198:199], v245 offset:20480\n"
"v_add_f32 v44, v44, v3\n"
"v_mfma_f32_16x16x32_bf16 a[4:7], a[228:231], v[16:19], a[4:7]\n"
"v_exp_f32 v9, v9\n"
"v_mfma_f32_16x16x32_bf16 a[8:11], a[232:235], v[16:19], a[8:11]\n"
"ds_read_b64_tr_b16 a[200:201], v244 offset:16640\n"
"v_cvt_pk_bf16_f32 v20, v8, v9\n"
"v_mfma_f32_16x16x32_bf16 a[12:15], a[236:239], v[16:19], a[12:15]\n"
"v_exp_f32 v10, v10\n"
"v_mfma_f32_16x16x32_bf16 a[16:19], a[240:243], v[16:19], a[16:19]\n"
"ds_read_b64_tr_b16 a[202:203], v244 offset:20736\n"
"v_add_f32 v44, v44, v4\n"
"v_mfma_f32_16x16x32_bf16 a[20:23], a[244:247], v[16:19], a[20:23]\n"
"v_exp_f32 v11, v11\n"
"v_mad_u32_u24 v212, v217, s60, v207\n"
"v_mfma_f32_16x16x32_bf16 a[24:27], a[248:251], v[16:19], a[24:27]\n"
"ds_read_b64_tr_b16 a[204:205], v245 offset:16640\n"
"v_cvt_pk_bf16_f32 v21, v10, v11\n"
"v_mfma_f32_16x16x32_bf16 a[28:31], a[252:255], v[16:19], a[28:31]\n"
"v_exp_f32 v12, v12\n"
"v_mfma_f32_16x16x32_bf16 v[220:223], a[160:163], v[96:99], v[32:35]\n"
"ds_read_b64_tr_b16 a[206:207], v245 offset:20736\n"
"v_add_f32 v44, v44, v5\n"
"v_mfma_f32_16x16x32_bf16 v[220:223], a[164:167], v[100:103], v[220:223]\n"
"v_exp_f32 v13, v13\n"
"v_mfma_f32_16x16x32_bf16 v[220:223], a[168:171], v[104:107], v[220:223]\n"
"ds_read_b64_tr_b16 a[208:209], v244 offset:16896\n"
"v_cvt_pk_bf16_f32 v22, v12, v13\n"
"v_mfma_f32_16x16x32_bf16 v[220:223], a[172:175], v[108:111], v[220:223]\n"
"v_exp_f32 v14, v14\n"
"v_mfma_f32_16x16x32_bf16 v[224:227], a[176:179], v[96:99], v[32:35]\n"
"ds_read_b64_tr_b16 a[210:211], v244 offset:20992\n"
"v_add_f32 v44, v44, v6\n"
"v_add_u32 v249, 233024, v249\n"
"v_mfma_f32_16x16x32_bf16 v[224:227], a[180:183], v[100:103], v[224:227]\n"
"v_exp_f32 v15, v15\n"
"v_mfma_f32_16x16x32_bf16 v[224:227], a[184:187], v[104:107], v[224:227]\n"
"ds_read_b64_tr_b16 a[212:213], v245 offset:16896\n"
"v_cvt_pk_bf16_f32 v23, v14, v15\n"
"v_mfma_f32_16x16x32_bf16 v[224:227], a[188:191], v[108:111], v[224:227]\n"
"v_exp_f32 v220, v220\n"
"v_mfma_f32_16x16x32_bf16 a[32:35], a[224:227], v[20:23], a[32:35]\n"
"buffer_load_dwordx4 v247, s[8:11], s84 offen offset:2048 lds\n"
"v_lshrrev_b32 v217, 16, v249\n"
"v_mfma_f32_16x16x32_bf16 a[36:39], a[228:231], v[20:23], a[36:39]\n"
"v_exp_f32 v221, v221\n"
"v_mfma_f32_16x16x32_bf16 a[40:43], a[232:235], v[20:23], a[40:43]\n"
"ds_read_b64_tr_b16 a[214:215], v245 offset:20992\n"
"v_cvt_pk_bf16_f32 v16, v220, v221\n"
"v_mfma_f32_16x16x32_bf16 a[44:47], a[236:239], v[20:23], a[44:47]\n"
"v_exp_f32 v222, v222\n"
"v_mfma_f32_16x16x32_bf16 a[48:51], a[240:243], v[20:23], a[48:51]\n"
"ds_read_b64_tr_b16 a[216:217], v244 offset:17152\n"
"v_add_f32 v44, v44, v7\n"
"v_mfma_f32_16x16x32_bf16 a[52:55], a[244:247], v[20:23], a[52:55]\n"
"v_exp_f32 v223, v223\n"
"v_mfma_f32_16x16x32_bf16 a[56:59], a[248:251], v[20:23], a[56:59]\n"
"ds_read_b64_tr_b16 a[218:219], v244 offset:21248\n"
"v_cvt_pk_bf16_f32 v17, v222, v223\n"
"v_mfma_f32_16x16x32_bf16 a[60:63], a[252:255], v[20:23], a[60:63]\n"
"v_exp_f32 v224, v224\n"
"v_mad_u32_u24 v213, v217, s60, v207\n"
"v_mfma_f32_16x16x32_bf16 v[48:51], v[40:43], v[20:23], v[48:51]\n"
"ds_read_b64_tr_b16 a[220:221], v245 offset:17152\n"
"v_add_f32 v52, v52, v220\n"
"v_mfma_f32_16x16x32_bf16 v[228:231], a[160:163], v[112:115], v[36:39]\n"
"v_exp_f32 v225, v225\n"
"v_mfma_f32_16x16x32_bf16 v[228:231], a[164:167], v[116:119], v[228:231]\n"
"ds_read_b64_tr_b16 a[222:223], v245 offset:21248\n"
"v_cvt_pk_bf16_f32 v18, v224, v225\n"
"v_mfma_f32_16x16x32_bf16 v[228:231], a[168:171], v[120:123], v[228:231]\n"
"v_exp_f32 v226, v226\n"
"v_mfma_f32_16x16x32_bf16 v[228:231], a[172:175], v[124:127], v[228:231]\n"
"v_add_f32 v52, v52, v221\n"
"v_add_f32 v52, v52, v222\n"
"v_mfma_f32_16x16x32_bf16 v[232:235], a[176:179], v[112:115], v[36:39]\n"
"v_exp_f32 v227, v227\n"
"v_mfma_f32_16x16x32_bf16 v[232:235], a[180:183], v[116:119], v[232:235]\n"
"v_cvt_pk_bf16_f32 v19, v226, v227\n"
"v_add_f32 v52, v52, v223\n"
"v_add_u32 v249, 233024, v249\n"
"v_mfma_f32_16x16x32_bf16 v[232:235], a[184:187], v[120:123], v[232:235]\n"
"buffer_load_dwordx4 v247, s[8:11], s84 offen offset:3072 lds\n"
"v_lshrrev_b32 v217, 16, v249\n"
"v_mfma_f32_16x16x32_bf16 v[232:235], a[188:191], v[124:127], v[232:235]\n"
"v_exp_f32 v228, v228\n"
"v_mfma_f32_16x16x32_bf16 a[64:67], a[224:227], v[16:19], a[64:67]\n"
"buffer_load_dwordx4 v[164:167], v252, s[68:71], s49 offen\n"
"v_add_f32 v52, v52, v224\n"
"v_add_f32 v52, v52, v225\n"
"v_mfma_f32_16x16x32_bf16 a[68:71], a[228:231], v[16:19], a[68:71]\n"
"v_exp_f32 v229, v229\n"
"v_mfma_f32_16x16x32_bf16 a[72:75], a[232:235], v[16:19], a[72:75]\n"
"v_cvt_pk_bf16_f32 v20, v228, v229\n"
"v_add_f32 v52, v52, v226\n"
"v_mfma_f32_16x16x32_bf16 a[76:79], a[236:239], v[16:19], a[76:79]\n"
"v_exp_f32 v230, v230\n"
"v_mfma_f32_16x16x32_bf16 a[80:83], a[240:243], v[16:19], a[80:83]\n"
"s_waitcnt lgkmcnt(8)\n"
"v_mad_u32_u24 v214, v217, s60, v206\n"
"v_mfma_f32_16x16x32_bf16 a[84:87], a[244:247], v[16:19], a[84:87]\n"
"v_exp_f32 v231, v231\n"
"v_mfma_f32_16x16x32_bf16 a[88:91], a[248:251], v[16:19], a[88:91]\n"
"v_cvt_pk_bf16_f32 v21, v230, v231\n"
"v_add_f32 v52, v52, v227\n"
"v_mfma_f32_16x16x32_bf16 a[92:95], a[252:255], v[16:19], a[92:95]\n"
"v_exp_f32 v232, v232\n"
"v_mfma_f32_16x16x32_bf16 v[0:3], a[128:131], v[64:67], v[24:27]\n"
"ds_read_b128 a[160:163], v241 offset:8192\n"
"v_add_u32 v249, 233024, v249\n"
"v_mfma_f32_16x16x32_bf16 v[0:3], a[132:135], v[68:71], v[0:3]\n"
"v_exp_f32 v233, v233\n"
"v_mfma_f32_16x16x32_bf16 v[0:3], a[136:139], v[72:75], v[0:3]\n"
"ds_read_b128 a[164:167], v241 offset:8448\n"
"v_cvt_pk_bf16_f32 v22, v232, v233\n"
"v_mfma_f32_16x16x32_bf16 v[0:3], a[140:143], v[76:79], v[0:3]\n"
"v_exp_f32 v234, v234\n"
"v_mfma_f32_16x16x32_bf16 v[4:7], a[144:147], v[64:67], v[24:27]\n"
"ds_read_b128 a[168:171], v241 offset:8704\n"
"v_lshrrev_b32 v217, 16, v249\n"
"v_mfma_f32_16x16x32_bf16 v[4:7], a[148:151], v[68:71], v[4:7]\n"
"v_exp_f32 v235, v235\n"
"v_mfma_f32_16x16x32_bf16 v[4:7], a[152:155], v[72:75], v[4:7]\n"
"ds_read_b128 a[172:175], v241 offset:8960\n"
"v_cvt_pk_bf16_f32 v23, v234, v235\n"
"v_mfma_f32_16x16x32_bf16 v[4:7], a[156:159], v[76:79], v[4:7]\n"
"s_add_u32 m0, s86, 49152\n"
"v_exp_f32 v0, v0\n"
"v_mfma_f32_16x16x32_bf16 a[96:99], a[224:227], v[20:23], a[96:99]\n"
"buffer_load_dwordx4 v246, s[12:15], s84 offen offset:0 lds\n"
"v_mad_u32_u24 v236, v217, s60, v206\n"
"v_mfma_f32_16x16x32_bf16 a[100:103], a[228:231], v[20:23], a[100:103]\n"
"v_exp_f32 v1, v1\n"
"v_mfma_f32_16x16x32_bf16 a[104:107], a[232:235], v[20:23], a[104:107]\n"
"ds_read_b128 a[176:179], v241 offset:12288\n"
"v_cvt_pk_bf16_f32 v16, v0, v1\n"
"v_mfma_f32_16x16x32_bf16 a[108:111], a[236:239], v[20:23], a[108:111]\n"
"v_exp_f32 v2, v2\n"
"v_mfma_f32_16x16x32_bf16 a[112:115], a[240:243], v[20:23], a[112:115]\n"
"ds_read_b128 a[180:183], v241 offset:12544\n"
"v_add_f32 v44, v44, v0\n"
"v_mfma_f32_16x16x32_bf16 a[116:119], a[244:247], v[20:23], a[116:119]\n"
"v_exp_f32 v3, v3\n"
"v_mfma_f32_16x16x32_bf16 a[120:123], a[248:251], v[20:23], a[120:123]\n"
"ds_read_b128 a[184:187], v241 offset:12800\n"
"v_cvt_pk_bf16_f32 v17, v2, v3\n"
"v_mfma_f32_16x16x32_bf16 a[124:127], a[252:255], v[20:23], a[124:127]\n"
"v_exp_f32 v4, v4\n"
"v_add_u32 v249, 233024, v249\n"
"v_mfma_f32_16x16x32_bf16 v[56:59], v[40:43], v[20:23], v[56:59]\n"
"ds_read_b128 a[188:191], v241 offset:13056\n"
"v_add_f32 v44, v44, v1\n"
"v_mfma_f32_16x16x32_bf16 v[8:11], a[128:131], v[80:83], v[28:31]\n"
"v_exp_f32 v5, v5\n"
"v_mfma_f32_16x16x32_bf16 v[8:11], a[132:135], v[84:87], v[8:11]\n"
"ds_read_b64_tr_b16 a[224:225], v244 offset:24576\n"
"v_cvt_pk_bf16_f32 v18, v4, v5\n"
"v_mfma_f32_16x16x32_bf16 v[8:11], a[136:139], v[88:91], v[8:11]\n"
"v_exp_f32 v6, v6\n"
"v_mfma_f32_16x16x32_bf16 v[8:11], a[140:143], v[92:95], v[8:11]\n"
"ds_read_b64_tr_b16 a[226:227], v244 offset:28672\n"
"v_add_f32 v44, v44, v2\n"
"v_mfma_f32_16x16x32_bf16 v[12:15], a[144:147], v[80:83], v[28:31]\n"
"v_exp_f32 v7, v7\n"
"v_mfma_f32_16x16x32_bf16 v[12:15], a[148:151], v[84:87], v[12:15]\n"
"ds_read_b64_tr_b16 a[228:229], v245 offset:24576\n"
"v_cvt_pk_bf16_f32 v19, v6, v7\n"
"v_lshrrev_b32 v217, 16, v249\n"
"v_mfma_f32_16x16x32_bf16 v[12:15], a[152:155], v[88:91], v[12:15]\n"
"buffer_load_dwordx4 v247, s[12:15], s84 offen offset:1024 lds\n"
"v_mad_u32_u24 v237, v217, s60, v206\n"
"v_mfma_f32_16x16x32_bf16 v[12:15], a[156:159], v[92:95], v[12:15]\n"
"s_waitcnt lgkmcnt(11)\n"
"v_exp_f32 v8, v8\n"
"v_mfma_f32_16x16x32_bf16 a[0:3], a[192:195], v[16:19], a[0:3]\n"
"ds_read_b64_tr_b16 a[230:231], v245 offset:28672\n"
"v_add_f32 v44, v44, v3\n"
"v_mfma_f32_16x16x32_bf16 a[4:7], a[196:199], v[16:19], a[4:7]\n"
"v_exp_f32 v9, v9\n"
"v_mfma_f32_16x16x32_bf16 a[8:11], a[200:203], v[16:19], a[8:11]\n"
"ds_read_b64_tr_b16 a[232:233], v244 offset:24832\n"
"v_cvt_pk_bf16_f32 v20, v8, v9\n"
"v_mfma_f32_16x16x32_bf16 a[12:15], a[204:207], v[16:19], a[12:15]\n"
"v_exp_f32 v10, v10\n"
"v_mfma_f32_16x16x32_bf16 a[16:19], a[208:211], v[16:19], a[16:19]\n"
"ds_read_b64_tr_b16 a[234:235], v244 offset:28928\n"
"v_add_f32 v44, v44, v4\n"
"v_mfma_f32_16x16x32_bf16 a[20:23], a[212:215], v[16:19], a[20:23]\n"
"v_exp_f32 v11, v11\n"
"v_add_u32 v249, 233024, v249\n"
"v_mfma_f32_16x16x32_bf16 a[24:27], a[216:219], v[16:19], a[24:27]\n"
"ds_read_b64_tr_b16 a[236:237], v245 offset:24832\n"
"v_cvt_pk_bf16_f32 v21, v10, v11\n"
"v_mfma_f32_16x16x32_bf16 a[28:31], a[220:223], v[16:19], a[28:31]\n"
"v_exp_f32 v12, v12\n"
"v_mfma_f32_16x16x32_bf16 v[220:223], a[128:131], v[96:99], v[32:35]\n"
"ds_read_b64_tr_b16 a[238:239], v245 offset:28928\n"
"v_add_f32 v44, v44, v5\n"
"v_mfma_f32_16x16x32_bf16 v[220:223], a[132:135], v[100:103], v[220:223]\n"
"v_exp_f32 v13, v13\n"
"v_mfma_f32_16x16x32_bf16 v[220:223], a[136:139], v[104:107], v[220:223]\n"
"ds_read_b64_tr_b16 a[240:241], v244 offset:25088\n"
"v_cvt_pk_bf16_f32 v22, v12, v13\n"
"v_mfma_f32_16x16x32_bf16 v[220:223], a[140:143], v[108:111], v[220:223]\n"
"v_exp_f32 v14, v14\n"
"v_mfma_f32_16x16x32_bf16 v[224:227], a[144:147], v[96:99], v[32:35]\n"
"ds_read_b64_tr_b16 a[242:243], v244 offset:29184\n"
"v_add_f32 v44, v44, v6\n"
"v_lshrrev_b32 v217, 16, v249\n"
"v_mfma_f32_16x16x32_bf16 v[224:227], a[148:151], v[100:103], v[224:227]\n"
"v_exp_f32 v15, v15\n"
"v_mfma_f32_16x16x32_bf16 v[224:227], a[152:155], v[104:107], v[224:227]\n"
"ds_read_b64_tr_b16 a[244:245], v245 offset:25088\n"
"v_cvt_pk_bf16_f32 v23, v14, v15\n"
"v_mfma_f32_16x16x32_bf16 v[224:227], a[156:159], v[108:111], v[224:227]\n"
"v_exp_f32 v220, v220\n"
"v_mfma_f32_16x16x32_bf16 a[32:35], a[192:195], v[20:23], a[32:35]\n"
"buffer_load_dwordx4 v246, s[12:15], s84 offen offset:2048 lds\n"
"v_mad_u32_u24 v238, v217, s60, v206\n"
"v_mfma_f32_16x16x32_bf16 a[36:39], a[196:199], v[20:23], a[36:39]\n"
"v_exp_f32 v221, v221\n"
"v_mfma_f32_16x16x32_bf16 a[40:43], a[200:203], v[20:23], a[40:43]\n"
"ds_read_b64_tr_b16 a[246:247], v245 offset:29184\n"
"v_cvt_pk_bf16_f32 v16, v220, v221\n"
"v_mfma_f32_16x16x32_bf16 a[44:47], a[204:207], v[20:23], a[44:47]\n"
"v_exp_f32 v222, v222\n"
"v_mfma_f32_16x16x32_bf16 a[48:51], a[208:211], v[20:23], a[48:51]\n"
"ds_read_b64_tr_b16 a[248:249], v244 offset:25344\n"
"v_add_f32 v44, v44, v7\n"
"v_mfma_f32_16x16x32_bf16 a[52:55], a[212:215], v[20:23], a[52:55]\n"
"v_exp_f32 v223, v223\n"
"v_mfma_f32_16x16x32_bf16 a[56:59], a[216:219], v[20:23], a[56:59]\n"
"ds_read_b64_tr_b16 a[250:251], v244 offset:29440\n"
"v_cvt_pk_bf16_f32 v17, v222, v223\n"
"v_mfma_f32_16x16x32_bf16 a[60:63], a[220:223], v[20:23], a[60:63]\n"
"v_exp_f32 v224, v224\n"
"v_add_u32 v206, 4096, v206\n"
"v_mfma_f32_16x16x32_bf16 v[48:51], v[40:43], v[20:23], v[48:51]\n"
"ds_read_b64_tr_b16 a[252:253], v245 offset:25344\n"
"v_add_f32 v52, v52, v220\n"
"v_mfma_f32_16x16x32_bf16 v[228:231], a[128:131], v[112:115], v[36:39]\n"
"v_exp_f32 v225, v225\n"
"v_mfma_f32_16x16x32_bf16 v[228:231], a[132:135], v[116:119], v[228:231]\n"
"ds_read_b64_tr_b16 a[254:255], v245 offset:29440\n"
"v_cvt_pk_bf16_f32 v18, v224, v225\n"
"v_mfma_f32_16x16x32_bf16 v[228:231], a[136:139], v[120:123], v[228:231]\n"
"v_exp_f32 v226, v226\n"
"v_mfma_f32_16x16x32_bf16 v[228:231], a[140:143], v[124:127], v[228:231]\n"
"v_add_f32 v52, v52, v221\n"
"v_add_f32 v52, v52, v222\n"
"v_mfma_f32_16x16x32_bf16 v[232:235], a[144:147], v[112:115], v[36:39]\n"
"v_exp_f32 v227, v227\n"
"v_mfma_f32_16x16x32_bf16 v[232:235], a[148:151], v[116:119], v[232:235]\n"
"v_cvt_pk_bf16_f32 v19, v226, v227\n"
"v_add_f32 v52, v52, v223\n"
"v_add_u32 v249, 233024, v249\n"
"v_mfma_f32_16x16x32_bf16 v[232:235], a[152:155], v[120:123], v[232:235]\n"
"buffer_load_dwordx4 v247, s[12:15], s84 offen offset:3072 lds\n"
"v_lshrrev_b32 v217, 16, v249\n"
"v_mfma_f32_16x16x32_bf16 v[232:235], a[156:159], v[124:127], v[232:235]\n"
"s_add_u32 s84, s84, 16384\n"
"v_exp_f32 v228, v228\n"
"v_mfma_f32_16x16x32_bf16 a[64:67], a[192:195], v[16:19], a[64:67]\n"
"v_add_f32 v52, v52, v224\n"
"v_add_f32 v52, v52, v225\n"
"v_mfma_f32_16x16x32_bf16 a[68:71], a[196:199], v[16:19], a[68:71]\n"
"v_exp_f32 v229, v229\n"
"v_mfma_f32_16x16x32_bf16 a[72:75], a[200:203], v[16:19], a[72:75]\n"
"v_cvt_pk_bf16_f32 v20, v228, v229\n"
"v_add_f32 v52, v52, v226\n"
"v_mfma_f32_16x16x32_bf16 a[76:79], a[204:207], v[16:19], a[76:79]\n"
"s_waitcnt vmcnt(21) lgkmcnt(0)\n"
"v_exp_f32 v230, v230\n"
"v_mfma_f32_16x16x32_bf16 a[80:83], a[208:211], v[16:19], a[80:83]\n"
"s_barrier\n"
"v_mfma_f32_16x16x32_bf16 a[84:87], a[212:215], v[16:19], a[84:87]\n"
"v_exp_f32 v231, v231\n"
"v_mfma_f32_16x16x32_bf16 a[88:91], a[216:219], v[16:19], a[88:91]\n"
"v_cvt_pk_bf16_f32 v21, v230, v231\n"
"v_add_f32 v52, v52, v227\n"
"v_mad_u32_u24 v239, v217, s60, v206\n"
"v_mfma_f32_16x16x32_bf16 a[92:95], a[220:223], v[16:19], a[92:95]\n"
"s_and_b32 s56, s43, 3\n"
"v_exp_f32 v232, v232\n"
"v_mfma_f32_16x16x32_bf16 v[0:3], a[160:163], v[64:67], v[24:27]\n"
"s_lshr_b32 s57, s43, 2\n"
"ds_read_b128 a[128:131], v241 offset:32768\n"
"v_mfma_f32_16x16x32_bf16 v[0:3], a[164:167], v[68:71], v[0:3]\n"
"s_add_u32 s49, s49, 9216\n"
"v_exp_f32 v233, v233\n"
"v_mfma_f32_16x16x32_bf16 v[0:3], a[168:171], v[72:75], v[0:3]\n"
"ds_read_b128 a[132:135], v241 offset:33024\n"
"buffer_load_dwordx4 v[132:135], v252, s[64:67], s49 offen\n"
"v_cvt_pk_bf16_f32 v22, v232, v233\n"
"v_mfma_f32_16x16x32_bf16 v[0:3], a[172:175], v[76:79], v[0:3]\n"
"s_mul_i32 s57, s57, 1179648\n"
"v_exp_f32 v234, v234\n"
"v_mfma_f32_16x16x32_bf16 v[4:7], a[176:179], v[64:67], v[24:27]\n"
"s_mul_i32 s62, s56, 288\n"
"ds_read_b128 a[136:139], v241 offset:33280\n"
"v_mfma_f32_16x16x32_bf16 v[4:7], a[180:183], v[68:71], v[4:7]\n"
"s_add_u32 s57, s57, s62\n"
"v_exp_f32 v235, v235\n"
"v_mfma_f32_16x16x32_bf16 v[4:7], a[184:187], v[72:75], v[4:7]\n"
"s_lshl_b32 s58, s56, 3\n"
"ds_read_b128 a[140:143], v241 offset:33536\n"
"v_cvt_pk_bf16_f32 v23, v234, v235\n"
"v_mfma_f32_16x16x32_bf16 v[4:7], a[188:191], v[76:79], v[4:7]\n"
"s_add_u32 m0, s86, 65536\n"
"v_exp_f32 v0, v0\n"
"v_mfma_f32_16x16x32_bf16 a[96:99], a[192:195], v[20:23], a[96:99]\n"
"s_add_u32 s58, s58, s41\n"
"buffer_load_dwordx4 v246, s[8:11], s84 offen offset:0 lds\n"
"v_mfma_f32_16x16x32_bf16 a[100:103], a[196:199], v[20:23], a[100:103]\n"
"s_add_u32 s58, s58, 0\n"
"v_exp_f32 v1, v1\n"
"v_mfma_f32_16x16x32_bf16 a[104:107], a[200:203], v[20:23], a[104:107]\n"
"s_mul_i32 s58, s58, 36864\n"
"ds_read_b128 a[144:147], v241 offset:36864\n"
"v_cvt_pk_bf16_f32 v16, v0, v1\n"
"v_mfma_f32_16x16x32_bf16 a[108:111], a[204:207], v[20:23], a[108:111]\n"
"s_add_u32 s20, s58, s57\n"
"v_exp_f32 v2, v2\n"
"v_mfma_f32_16x16x32_bf16 a[112:115], a[208:211], v[20:23], a[112:115]\n"
"s_lshl_b32 s58, s56, 3\n"
"ds_read_b128 a[148:151], v241 offset:37120\n"
"v_add_f32 v44, v44, v0\n"
"v_mfma_f32_16x16x32_bf16 a[116:119], a[212:215], v[20:23], a[116:119]\n"
"s_add_u32 s58, s58, s41\n"
"v_exp_f32 v3, v3\n"
"v_mfma_f32_16x16x32_bf16 a[120:123], a[216:219], v[20:23], a[120:123]\n"
"s_add_u32 s58, s58, 4\n"
"ds_read_b128 a[152:155], v241 offset:37376\n"
"v_cvt_pk_bf16_f32 v17, v2, v3\n"
"v_mfma_f32_16x16x32_bf16 a[124:127], a[220:223], v[20:23], a[124:127]\n"
"s_mul_i32 s58, s58, 36864\n"
"v_exp_f32 v4, v4\n"
"v_mfma_f32_16x16x32_bf16 v[56:59], v[40:43], v[20:23], v[56:59]\n"
"s_add_u32 s21, s58, s57\n"
"ds_read_b128 a[156:159], v241 offset:37632\n"
"v_add_f32 v44, v44, v1\n"
"v_mfma_f32_16x16x32_bf16 v[8:11], a[160:163], v[80:83], v[28:31]\n"
"s_add_u32 s58, s41, 0\n"
"v_exp_f32 v5, v5\n"
"v_mfma_f32_16x16x32_bf16 v[8:11], a[164:167], v[84:87], v[8:11]\n"
"s_mul_i32 s59, s58, 43\n"
"ds_read_b64_tr_b16 a[192:193], v244 offset:49152\n"
"v_cvt_pk_bf16_f32 v18, v4, v5\n"
"v_mfma_f32_16x16x32_bf16 v[8:11], a[168:171], v[88:91], v[8:11]\n"
"s_lshr_b32 s59, s59, 7\n"
"v_exp_f32 v6, v6\n"
"v_mfma_f32_16x16x32_bf16 v[8:11], a[172:175], v[92:95], v[8:11]\n"
"ds_read_b64_tr_b16 a[194:195], v244 offset:53248\n"
"v_add_f32 v44, v44, v2\n"
"v_mfma_f32_16x16x32_bf16 v[12:15], a[176:179], v[80:83], v[28:31]\n"
"v_exp_f32 v7, v7\n"
"v_mfma_f32_16x16x32_bf16 v[12:15], a[180:183], v[84:87], v[12:15]\n"
"ds_read_b64_tr_b16 a[196:197], v245 offset:49152\n"
"v_cvt_pk_bf16_f32 v19, v6, v7\n"
"v_mfma_f32_16x16x32_bf16 v[12:15], a[184:187], v[88:91], v[12:15]\n"
"buffer_load_dwordx4 v246, s[8:11], s84 offen offset:1024 lds\n"
"v_mfma_f32_16x16x32_bf16 v[12:15], a[188:191], v[92:95], v[12:15]\n"
"s_waitcnt lgkmcnt(11)\n"
"v_exp_f32 v8, v8\n"
"v_mfma_f32_16x16x32_bf16 a[0:3], a[224:227], v[16:19], a[0:3]\n"
"ds_read_b64_tr_b16 a[198:199], v245 offset:53248\n"
"v_add_f32 v44, v44, v3\n"
"v_mfma_f32_16x16x32_bf16 a[4:7], a[228:231], v[16:19], a[4:7]\n"
"v_exp_f32 v9, v9\n"
"v_mfma_f32_16x16x32_bf16 a[8:11], a[232:235], v[16:19], a[8:11]\n"
"ds_read_b64_tr_b16 a[200:201], v244 offset:49408\n"
"v_cvt_pk_bf16_f32 v20, v8, v9\n"
"v_mfma_f32_16x16x32_bf16 a[12:15], a[236:239], v[16:19], a[12:15]\n"
"v_exp_f32 v10, v10\n"
"v_mfma_f32_16x16x32_bf16 a[16:19], a[240:243], v[16:19], a[16:19]\n"
"ds_read_b64_tr_b16 a[202:203], v244 offset:53504\n"
"v_add_f32 v44, v44, v4\n"
"v_mfma_f32_16x16x32_bf16 a[20:23], a[244:247], v[16:19], a[20:23]\n"
"v_exp_f32 v11, v11\n"
"v_mfma_f32_16x16x32_bf16 a[24:27], a[248:251], v[16:19], a[24:27]\n"
"ds_read_b64_tr_b16 a[204:205], v245 offset:49408\n"
"buffer_load_dwordx4 v[168:171], v252, s[68:71], s49 offen\n"
"v_cvt_pk_bf16_f32 v21, v10, v11\n"
"v_mfma_f32_16x16x32_bf16 a[28:31], a[252:255], v[16:19], a[28:31]\n"
"s_mul_i32 s60, s59, 3\n"
"v_exp_f32 v12, v12\n"
"v_mfma_f32_16x16x32_bf16 v[220:223], a[160:163], v[96:99], v[32:35]\n"
"s_sub_u32 s58, s58, s60\n"
"ds_read_b64_tr_b16 a[206:207], v245 offset:53504\n"
"v_add_f32 v44, v44, v5\n"
"v_mfma_f32_16x16x32_bf16 v[220:223], a[164:167], v[100:103], v[220:223]\n"
"s_add_u32 s58, s58, s56\n"
"v_exp_f32 v13, v13\n"
"v_mfma_f32_16x16x32_bf16 v[220:223], a[168:171], v[104:107], v[220:223]\n"
"s_add_u32 s58, s58, 1\n"
"ds_read_b64_tr_b16 a[208:209], v244 offset:49664\n"
"v_cvt_pk_bf16_f32 v22, v12, v13\n"
"v_mfma_f32_16x16x32_bf16 v[220:223], a[172:175], v[108:111], v[220:223]\n"
"s_and_b32 s58, s58, 3\n"
"v_exp_f32 v14, v14\n"
"v_mfma_f32_16x16x32_bf16 v[224:227], a[176:179], v[96:99], v[32:35]\n"
"s_lshl_b32 s58, s58, 3\n"
"ds_read_b64_tr_b16 a[210:211], v244 offset:53760\n"
"v_add_f32 v44, v44, v6\n"
"v_mfma_f32_16x16x32_bf16 v[224:227], a[180:183], v[100:103], v[224:227]\n"
"s_add_u32 s58, s58, s59\n"
"v_exp_f32 v15, v15\n"
"v_mfma_f32_16x16x32_bf16 v[224:227], a[184:187], v[104:107], v[224:227]\n"
"s_mul_i32 s58, s58, 36864\n"
"ds_read_b64_tr_b16 a[212:213], v245 offset:49664\n"
"v_cvt_pk_bf16_f32 v23, v14, v15\n"
"v_mfma_f32_16x16x32_bf16 v[224:227], a[188:191], v[108:111], v[224:227]\n"
"s_add_u32 s22, s58, s57\n"
"v_exp_f32 v220, v220\n"
"v_mfma_f32_16x16x32_bf16 a[32:35], a[224:227], v[20:23], a[32:35]\n"
"s_add_u32 s58, s41, 4\n"
"buffer_load_dwordx4 v247, s[8:11], s84 offen offset:2048 lds\n"
"v_mfma_f32_16x16x32_bf16 a[36:39], a[228:231], v[20:23], a[36:39]\n"
"s_mul_i32 s59, s58, 43\n"
"v_exp_f32 v221, v221\n"
"v_mfma_f32_16x16x32_bf16 a[40:43], a[232:235], v[20:23], a[40:43]\n"
"s_lshr_b32 s59, s59, 7\n"
"ds_read_b64_tr_b16 a[214:215], v245 offset:53760\n"
"v_cvt_pk_bf16_f32 v16, v220, v221\n"
"v_mfma_f32_16x16x32_bf16 a[44:47], a[236:239], v[20:23], a[44:47]\n"
"s_mul_i32 s60, s59, 3\n"
"v_exp_f32 v222, v222\n"
"v_mfma_f32_16x16x32_bf16 a[48:51], a[240:243], v[20:23], a[48:51]\n"
"s_sub_u32 s58, s58, s60\n"
"ds_read_b64_tr_b16 a[216:217], v244 offset:49920\n"
"v_add_f32 v44, v44, v7\n"
"v_mfma_f32_16x16x32_bf16 a[52:55], a[244:247], v[20:23], a[52:55]\n"
"s_add_u32 s58, s58, s56\n"
"v_exp_f32 v223, v223\n"
"v_mfma_f32_16x16x32_bf16 a[56:59], a[248:251], v[20:23], a[56:59]\n"
"s_add_u32 s58, s58, 1\n"
"ds_read_b64_tr_b16 a[218:219], v244 offset:54016\n"
"v_cvt_pk_bf16_f32 v17, v222, v223\n"
"v_mfma_f32_16x16x32_bf16 a[60:63], a[252:255], v[20:23], a[60:63]\n"
"s_add_u32 s49, s49, 285696\n"
"v_exp_f32 v224, v224\n"
"v_mfma_f32_16x16x32_bf16 v[48:51], v[40:43], v[20:23], v[48:51]\n"
"ds_read_b64_tr_b16 a[220:221], v245 offset:49920\n"
"v_add_f32 v52, v52, v220\n"
"v_mfma_f32_16x16x32_bf16 v[228:231], a[160:163], v[112:115], v[36:39]\n"
"v_exp_f32 v225, v225\n"
"v_mfma_f32_16x16x32_bf16 v[228:231], a[164:167], v[116:119], v[228:231]\n"
"ds_read_b64_tr_b16 a[222:223], v245 offset:54016\n"
"v_cvt_pk_bf16_f32 v18, v224, v225\n"
"v_mfma_f32_16x16x32_bf16 v[228:231], a[168:171], v[120:123], v[228:231]\n"
"v_exp_f32 v226, v226\n"
"v_mfma_f32_16x16x32_bf16 v[228:231], a[172:175], v[124:127], v[228:231]\n"
"v_add_f32 v52, v52, v221\n"
"v_add_f32 v52, v52, v222\n"
"v_mfma_f32_16x16x32_bf16 v[232:235], a[176:179], v[112:115], v[36:39]\n"
"v_exp_f32 v227, v227\n"
"v_mfma_f32_16x16x32_bf16 v[232:235], a[180:183], v[116:119], v[232:235]\n"
"v_cvt_pk_bf16_f32 v19, v226, v227\n"
"v_add_f32 v52, v52, v223\n"
"v_mfma_f32_16x16x32_bf16 v[232:235], a[184:187], v[120:123], v[232:235]\n"
"buffer_load_dwordx4 v247, s[8:11], s84 offen offset:3072 lds\n"
"v_mfma_f32_16x16x32_bf16 v[232:235], a[188:191], v[124:127], v[232:235]\n"
"v_exp_f32 v228, v228\n"
"v_mfma_f32_16x16x32_bf16 a[64:67], a[224:227], v[16:19], a[64:67]\n"
"v_add_f32 v52, v52, v224\n"
"v_add_f32 v52, v52, v225\n"
"v_mfma_f32_16x16x32_bf16 a[68:71], a[228:231], v[16:19], a[68:71]\n"
"v_exp_f32 v229, v229\n"
"v_mfma_f32_16x16x32_bf16 a[72:75], a[232:235], v[16:19], a[72:75]\n"
"buffer_load_dwordx4 v[136:139], v252, s[64:67], s49 offen\n"
"v_cvt_pk_bf16_f32 v20, v228, v229\n"
"v_add_f32 v52, v52, v226\n"
"v_mfma_f32_16x16x32_bf16 a[76:79], a[236:239], v[16:19], a[76:79]\n"
"s_and_b32 s58, s58, 3\n"
"v_exp_f32 v230, v230\n"
"v_mfma_f32_16x16x32_bf16 a[80:83], a[240:243], v[16:19], a[80:83]\n"
"s_waitcnt lgkmcnt(8)\n"
"v_mfma_f32_16x16x32_bf16 a[84:87], a[244:247], v[16:19], a[84:87]\n"
"s_lshl_b32 s58, s58, 3\n"
"v_exp_f32 v231, v231\n"
"v_mfma_f32_16x16x32_bf16 a[88:91], a[248:251], v[16:19], a[88:91]\n"
"s_add_u32 s58, s58, s59\n"
"v_cvt_pk_bf16_f32 v21, v230, v231\n"
"v_add_f32 v52, v52, v227\n"
"v_mfma_f32_16x16x32_bf16 a[92:95], a[252:255], v[16:19], a[92:95]\n"
"s_mul_i32 s58, s58, 36864\n"
"v_exp_f32 v232, v232\n"
"v_mfma_f32_16x16x32_bf16 v[0:3], a[128:131], v[64:67], v[24:27]\n"
"s_add_u32 s23, s58, s57\n"
"ds_read_b128 a[160:163], v241 offset:40960\n"
"v_mfma_f32_16x16x32_bf16 v[0:3], a[132:135], v[68:71], v[0:3]\n"
"s_add_u32 s58, s41, 8\n"
"v_exp_f32 v233, v233\n"
"v_mfma_f32_16x16x32_bf16 v[0:3], a[136:139], v[72:75], v[0:3]\n"
"s_mul_i32 s59, s58, 43\n"
"ds_read_b128 a[164:167], v241 offset:41216\n"
"v_cvt_pk_bf16_f32 v22, v232, v233\n"
"v_mfma_f32_16x16x32_bf16 v[0:3], a[140:143], v[76:79], v[0:3]\n"
"s_lshr_b32 s59, s59, 7\n"
"v_exp_f32 v234, v234\n"
"v_mfma_f32_16x16x32_bf16 v[4:7], a[144:147], v[64:67], v[24:27]\n"
"s_mul_i32 s60, s59, 3\n"
"ds_read_b128 a[168:171], v241 offset:41472\n"
"v_mfma_f32_16x16x32_bf16 v[4:7], a[148:151], v[68:71], v[4:7]\n"
"s_sub_u32 s58, s58, s60\n"
"v_exp_f32 v235, v235\n"
"v_mfma_f32_16x16x32_bf16 v[4:7], a[152:155], v[72:75], v[4:7]\n"
"s_add_u32 s58, s58, s56\n"
"ds_read_b128 a[172:175], v241 offset:41728\n"
"v_cvt_pk_bf16_f32 v23, v234, v235\n"
"v_mfma_f32_16x16x32_bf16 v[4:7], a[156:159], v[76:79], v[4:7]\n"
"s_add_u32 m0, s86, 81920\n"
"v_exp_f32 v0, v0\n"
"v_mfma_f32_16x16x32_bf16 a[96:99], a[224:227], v[20:23], a[96:99]\n"
"s_add_u32 s58, s58, 1\n"
"buffer_load_dwordx4 v246, s[12:15], s84 offen offset:0 lds\n"
"v_mfma_f32_16x16x32_bf16 a[100:103], a[228:231], v[20:23], a[100:103]\n"
"s_and_b32 s58, s58, 3\n"
"v_exp_f32 v1, v1\n"
"v_mfma_f32_16x16x32_bf16 a[104:107], a[232:235], v[20:23], a[104:107]\n"
"s_lshl_b32 s58, s58, 3\n"
"ds_read_b128 a[176:179], v241 offset:45056\n"
"v_cvt_pk_bf16_f32 v16, v0, v1\n"
"v_mfma_f32_16x16x32_bf16 a[108:111], a[236:239], v[20:23], a[108:111]\n"
"s_add_u32 s58, s58, s59\n"
"v_exp_f32 v2, v2\n"
"v_mfma_f32_16x16x32_bf16 a[112:115], a[240:243], v[20:23], a[112:115]\n"
"s_mul_i32 s58, s58, 36864\n"
"ds_read_b128 a[180:183], v241 offset:45312\n"
"v_add_f32 v44, v44, v0\n"
"v_mfma_f32_16x16x32_bf16 a[116:119], a[244:247], v[20:23], a[116:119]\n"
"v_exp_f32 v3, v3\n"
"v_mfma_f32_16x16x32_bf16 a[120:123], a[248:251], v[20:23], a[120:123]\n"
"ds_read_b128 a[184:187], v241 offset:45568\n"
"v_cvt_pk_bf16_f32 v17, v2, v3\n"
"v_mfma_f32_16x16x32_bf16 a[124:127], a[252:255], v[20:23], a[124:127]\n"
"v_exp_f32 v4, v4\n"
"v_mfma_f32_16x16x32_bf16 v[56:59], v[40:43], v[20:23], v[56:59]\n"
"ds_read_b128 a[188:191], v241 offset:45824\n"
"v_add_f32 v44, v44, v1\n"
"v_mfma_f32_16x16x32_bf16 v[8:11], a[128:131], v[80:83], v[28:31]\n"
"v_exp_f32 v5, v5\n"
"v_mfma_f32_16x16x32_bf16 v[8:11], a[132:135], v[84:87], v[8:11]\n"
"ds_read_b64_tr_b16 a[224:225], v244 offset:57344\n"
"v_cvt_pk_bf16_f32 v18, v4, v5\n"
"v_mfma_f32_16x16x32_bf16 v[8:11], a[136:139], v[88:91], v[8:11]\n"
"v_exp_f32 v6, v6\n"
"v_mfma_f32_16x16x32_bf16 v[8:11], a[140:143], v[92:95], v[8:11]\n"
"ds_read_b64_tr_b16 a[226:227], v244 offset:61440\n"
"v_add_f32 v44, v44, v2\n"
"v_mfma_f32_16x16x32_bf16 v[12:15], a[144:147], v[80:83], v[28:31]\n"
"v_exp_f32 v7, v7\n"
"v_mfma_f32_16x16x32_bf16 v[12:15], a[148:151], v[84:87], v[12:15]\n"
"ds_read_b64_tr_b16 a[228:229], v245 offset:57344\n"
"buffer_load_dwordx4 v[172:175], v252, s[68:71], s49 offen\n"
"v_cvt_pk_bf16_f32 v19, v6, v7\n"
"v_mfma_f32_16x16x32_bf16 v[12:15], a[152:155], v[88:91], v[12:15]\n"
"s_add_u32 s36, s58, s57\n"
"buffer_load_dwordx4 v247, s[12:15], s84 offen offset:1024 lds\n"
"v_mfma_f32_16x16x32_bf16 v[12:15], a[156:159], v[92:95], v[12:15]\n"
"s_waitcnt lgkmcnt(11)\n"
"v_exp_f32 v8, v8\n"
"v_mfma_f32_16x16x32_bf16 a[0:3], a[192:195], v[16:19], a[0:3]\n"
"s_add_u32 s58, s41, 12\n"
"ds_read_b64_tr_b16 a[230:231], v245 offset:61440\n"
"v_add_f32 v44, v44, v3\n"
"v_mfma_f32_16x16x32_bf16 a[4:7], a[196:199], v[16:19], a[4:7]\n"
"s_mul_i32 s59, s58, 43\n"
"v_exp_f32 v9, v9\n"
"v_mfma_f32_16x16x32_bf16 a[8:11], a[200:203], v[16:19], a[8:11]\n"
"s_lshr_b32 s59, s59, 7\n"
"ds_read_b64_tr_b16 a[232:233], v244 offset:57600\n"
"v_cvt_pk_bf16_f32 v20, v8, v9\n"
"v_mfma_f32_16x16x32_bf16 a[12:15], a[204:207], v[16:19], a[12:15]\n"
"s_mul_i32 s60, s59, 3\n"
"v_exp_f32 v10, v10\n"
"v_mfma_f32_16x16x32_bf16 a[16:19], a[208:211], v[16:19], a[16:19]\n"
"s_sub_u32 s58, s58, s60\n"
"ds_read_b64_tr_b16 a[234:235], v244 offset:61696\n"
"v_add_f32 v44, v44, v4\n"
"v_mfma_f32_16x16x32_bf16 a[20:23], a[212:215], v[16:19], a[20:23]\n"
"s_add_u32 s58, s58, s56\n"
"v_exp_f32 v11, v11\n"
"v_mfma_f32_16x16x32_bf16 a[24:27], a[216:219], v[16:19], a[24:27]\n"
"s_add_u32 s58, s58, 1\n"
"ds_read_b64_tr_b16 a[236:237], v245 offset:57600\n"
"v_cvt_pk_bf16_f32 v21, v10, v11\n"
"v_mfma_f32_16x16x32_bf16 a[28:31], a[220:223], v[16:19], a[28:31]\n"
"s_and_b32 s58, s58, 3\n"
"v_exp_f32 v12, v12\n"
"v_mfma_f32_16x16x32_bf16 v[220:223], a[128:131], v[96:99], v[32:35]\n"
"s_lshl_b32 s58, s58, 3\n"
"ds_read_b64_tr_b16 a[238:239], v245 offset:61696\n"
"v_add_f32 v44, v44, v5\n"
"v_mfma_f32_16x16x32_bf16 v[220:223], a[132:135], v[100:103], v[220:223]\n"
"s_add_u32 s58, s58, s59\n"
"v_exp_f32 v13, v13\n"
"v_mfma_f32_16x16x32_bf16 v[220:223], a[136:139], v[104:107], v[220:223]\n"
"s_mul_i32 s58, s58, 36864\n"
"ds_read_b64_tr_b16 a[240:241], v244 offset:57856\n"
"v_cvt_pk_bf16_f32 v22, v12, v13\n"
"v_mfma_f32_16x16x32_bf16 v[220:223], a[140:143], v[108:111], v[220:223]\n"
"s_add_u32 s37, s58, s57\n"
"v_exp_f32 v14, v14\n"
"v_mfma_f32_16x16x32_bf16 v[224:227], a[144:147], v[96:99], v[32:35]\n"
"s_add_u32 s58, s41, 16\n"
"ds_read_b64_tr_b16 a[242:243], v244 offset:61952\n"
"v_add_f32 v44, v44, v6\n"
"v_mfma_f32_16x16x32_bf16 v[224:227], a[148:151], v[100:103], v[224:227]\n"
"s_mul_i32 s59, s58, 43\n"
"v_exp_f32 v15, v15\n"
"v_mfma_f32_16x16x32_bf16 v[224:227], a[152:155], v[104:107], v[224:227]\n"
"s_lshr_b32 s59, s59, 7\n"
"ds_read_b64_tr_b16 a[244:245], v245 offset:57856\n"
"v_cvt_pk_bf16_f32 v23, v14, v15\n"
"v_mfma_f32_16x16x32_bf16 v[224:227], a[156:159], v[108:111], v[224:227]\n"
"s_add_u32 s49, s49, 9216\n"
"v_exp_f32 v220, v220\n"
"v_mfma_f32_16x16x32_bf16 a[32:35], a[192:195], v[20:23], a[32:35]\n"
"buffer_load_dwordx4 v246, s[12:15], s84 offen offset:2048 lds\n"
"v_mfma_f32_16x16x32_bf16 a[36:39], a[196:199], v[20:23], a[36:39]\n"
"v_exp_f32 v221, v221\n"
"v_mfma_f32_16x16x32_bf16 a[40:43], a[200:203], v[20:23], a[40:43]\n"
"ds_read_b64_tr_b16 a[246:247], v245 offset:61952\n"
"v_cvt_pk_bf16_f32 v16, v220, v221\n"
"v_mfma_f32_16x16x32_bf16 a[44:47], a[204:207], v[20:23], a[44:47]\n"
"v_exp_f32 v222, v222\n"
"v_mfma_f32_16x16x32_bf16 a[48:51], a[208:211], v[20:23], a[48:51]\n"
"ds_read_b64_tr_b16 a[248:249], v244 offset:58112\n"
"v_add_f32 v44, v44, v7\n"
"v_mfma_f32_16x16x32_bf16 a[52:55], a[212:215], v[20:23], a[52:55]\n"
"v_exp_f32 v223, v223\n"
"v_mfma_f32_16x16x32_bf16 a[56:59], a[216:219], v[20:23], a[56:59]\n"
"ds_read_b64_tr_b16 a[250:251], v244 offset:62208\n"
"v_cvt_pk_bf16_f32 v17, v222, v223\n"
"v_mfma_f32_16x16x32_bf16 a[60:63], a[220:223], v[20:23], a[60:63]\n"
"v_exp_f32 v224, v224\n"
"v_mfma_f32_16x16x32_bf16 v[48:51], v[40:43], v[20:23], v[48:51]\n"
"ds_read_b64_tr_b16 a[252:253], v245 offset:58112\n"
"v_add_f32 v52, v52, v220\n"
"v_mfma_f32_16x16x32_bf16 v[228:231], a[128:131], v[112:115], v[36:39]\n"
"v_exp_f32 v225, v225\n"
"v_mfma_f32_16x16x32_bf16 v[228:231], a[132:135], v[116:119], v[228:231]\n"
"ds_read_b64_tr_b16 a[254:255], v245 offset:62208\n"
"buffer_load_dwordx4 v[140:143], v252, s[64:67], s49 offen\n"
"v_cvt_pk_bf16_f32 v18, v224, v225\n"
"v_mfma_f32_16x16x32_bf16 v[228:231], a[136:139], v[120:123], v[228:231]\n"
"s_mul_i32 s60, s59, 3\n"
"v_exp_f32 v226, v226\n"
"v_mfma_f32_16x16x32_bf16 v[228:231], a[140:143], v[124:127], v[228:231]\n"
"s_sub_u32 s58, s58, s60\n"
"v_add_f32 v52, v52, v221\n"
"v_add_f32 v52, v52, v222\n"
"v_mfma_f32_16x16x32_bf16 v[232:235], a[144:147], v[112:115], v[36:39]\n"
"s_add_u32 s58, s58, s56\n"
"v_exp_f32 v227, v227\n"
"v_mfma_f32_16x16x32_bf16 v[232:235], a[148:151], v[116:119], v[232:235]\n"
"s_add_u32 s58, s58, 1\n"
"v_cvt_pk_bf16_f32 v19, v226, v227\n"
"v_add_f32 v52, v52, v223\n"
"v_mfma_f32_16x16x32_bf16 v[232:235], a[152:155], v[120:123], v[232:235]\n"
"s_and_b32 s58, s58, 3\n"
"buffer_load_dwordx4 v247, s[12:15], s84 offen offset:3072 lds\n"
"v_mfma_f32_16x16x32_bf16 v[232:235], a[156:159], v[124:127], v[232:235]\n"
"s_add_u32 s84, s84, 16384\n"
"v_exp_f32 v228, v228\n"
"v_mfma_f32_16x16x32_bf16 a[64:67], a[192:195], v[16:19], a[64:67]\n"
"s_lshl_b32 s58, s58, 3\n"
"v_add_f32 v52, v52, v224\n"
"v_add_f32 v52, v52, v225\n"
"v_mfma_f32_16x16x32_bf16 a[68:71], a[196:199], v[16:19], a[68:71]\n"
"s_add_u32 s58, s58, s59\n"
"v_exp_f32 v229, v229\n"
"v_mfma_f32_16x16x32_bf16 a[72:75], a[200:203], v[16:19], a[72:75]\n"
"s_mul_i32 s58, s58, 36864\n"
"v_cvt_pk_bf16_f32 v20, v228, v229\n"
"v_add_f32 v52, v52, v226\n"
"v_mfma_f32_16x16x32_bf16 a[76:79], a[204:207], v[16:19], a[76:79]\n"
"s_waitcnt vmcnt(22) lgkmcnt(0)\n"
"v_exp_f32 v230, v230\n"
"v_mfma_f32_16x16x32_bf16 a[80:83], a[208:211], v[16:19], a[80:83]\n"
"s_barrier\n"
"v_mfma_f32_16x16x32_bf16 a[84:87], a[212:215], v[16:19], a[84:87]\n"
"s_add_u32 s38, s58, s57\n"
"v_exp_f32 v231, v231\n"
"v_mfma_f32_16x16x32_bf16 a[88:91], a[216:219], v[16:19], a[88:91]\n"
"s_add_u32 s58, s41, 20\n"
"v_cvt_pk_bf16_f32 v21, v230, v231\n"
"v_add_f32 v52, v52, v227\n"
"v_mfma_f32_16x16x32_bf16 a[92:95], a[220:223], v[16:19], a[92:95]\n"
"s_mul_i32 s59, s58, 43\n"
"v_exp_f32 v232, v232\n"
"v_mfma_f32_16x16x32_bf16 v[0:3], a[160:163], v[64:67], v[24:27]\n"
"s_lshr_b32 s59, s59, 7\n"
"ds_read_b128 a[128:131], v240 offset:0\n"
"v_mfma_f32_16x16x32_bf16 v[0:3], a[164:167], v[68:71], v[0:3]\n"
"s_mul_i32 s60, s59, 3\n"
"v_exp_f32 v233, v233\n"
"v_mfma_f32_16x16x32_bf16 v[0:3], a[168:171], v[72:75], v[0:3]\n"
"s_sub_u32 s58, s58, s60\n"
"ds_read_b128 a[132:135], v240 offset:256\n"
"v_cvt_pk_bf16_f32 v22, v232, v233\n"
"v_mfma_f32_16x16x32_bf16 v[0:3], a[172:175], v[76:79], v[0:3]\n"
"s_add_u32 s58, s58, s56\n"
"v_exp_f32 v234, v234\n"
"v_mfma_f32_16x16x32_bf16 v[4:7], a[176:179], v[64:67], v[24:27]\n"
"s_add_u32 s58, s58, 1\n"
"ds_read_b128 a[136:139], v240 offset:512\n"
"v_mfma_f32_16x16x32_bf16 v[4:7], a[180:183], v[68:71], v[4:7]\n"
"v_exp_f32 v235, v235\n"
"v_mfma_f32_16x16x32_bf16 v[4:7], a[184:187], v[72:75], v[4:7]\n"
"ds_read_b128 a[140:143], v240 offset:768\n"
"v_cvt_pk_bf16_f32 v23, v234, v235\n"
"v_mfma_f32_16x16x32_bf16 v[4:7], a[188:191], v[76:79], v[4:7]\n"
"s_add_u32 m0, s86, 98304\n"
"v_exp_f32 v0, v0\n"
"v_mfma_f32_16x16x32_bf16 a[96:99], a[192:195], v[20:23], a[96:99]\n"
"buffer_load_dwordx4 v246, s[8:11], s84 offen offset:0 lds\n"
"v_mfma_f32_16x16x32_bf16 a[100:103], a[196:199], v[20:23], a[100:103]\n"
"v_exp_f32 v1, v1\n"
"v_mfma_f32_16x16x32_bf16 a[104:107], a[200:203], v[20:23], a[104:107]\n"
"ds_read_b128 a[144:147], v240 offset:4096\n"
"v_cvt_pk_bf16_f32 v16, v0, v1\n"
"v_mfma_f32_16x16x32_bf16 a[108:111], a[204:207], v[20:23], a[108:111]\n"
"v_exp_f32 v2, v2\n"
"v_mfma_f32_16x16x32_bf16 a[112:115], a[208:211], v[20:23], a[112:115]\n"
"ds_read_b128 a[148:151], v240 offset:4352\n"
"v_add_f32 v44, v44, v0\n"
"v_mfma_f32_16x16x32_bf16 a[116:119], a[212:215], v[20:23], a[116:119]\n"
"v_exp_f32 v3, v3\n"
"v_mfma_f32_16x16x32_bf16 a[120:123], a[216:219], v[20:23], a[120:123]\n"
"ds_read_b128 a[152:155], v240 offset:4608\n"
"buffer_load_dwordx4 v[176:179], v252, s[68:71], s49 offen\n"
"v_cvt_pk_bf16_f32 v17, v2, v3\n"
"v_mfma_f32_16x16x32_bf16 a[124:127], a[220:223], v[20:23], a[124:127]\n"
"s_and_b32 s58, s58, 3\n"
"v_exp_f32 v4, v4\n"
"v_mfma_f32_16x16x32_bf16 v[56:59], v[40:43], v[20:23], v[56:59]\n"
"s_lshl_b32 s58, s58, 3\n"
"ds_read_b128 a[156:159], v240 offset:4864\n"
"v_add_f32 v44, v44, v1\n"
"v_mfma_f32_16x16x32_bf16 v[8:11], a[160:163], v[80:83], v[28:31]\n"
"s_add_u32 s58, s58, s59\n"
"v_exp_f32 v5, v5\n"
"v_mfma_f32_16x16x32_bf16 v[8:11], a[164:167], v[84:87], v[8:11]\n"
"s_mul_i32 s58, s58, 36864\n"
"ds_read_b64_tr_b16 a[192:193], v242 offset:16384\n"
"v_cvt_pk_bf16_f32 v18, v4, v5\n"
"v_mfma_f32_16x16x32_bf16 v[8:11], a[168:171], v[88:91], v[8:11]\n"
"s_add_u32 s39, s58, s57\n"
"v_exp_f32 v6, v6\n"
"v_mfma_f32_16x16x32_bf16 v[8:11], a[172:175], v[92:95], v[8:11]\n"
"s_mul_i32 s58, s41, 9216\n"
"ds_read_b64_tr_b16 a[194:195], v242 offset:20480\n"
"v_add_f32 v44, v44, v2\n"
"v_mfma_f32_16x16x32_bf16 v[12:15], a[176:179], v[80:83], v[28:31]\n"
"s_add_u32 s58, s58, s40\n"
"v_exp_f32 v7, v7\n"
"v_mfma_f32_16x16x32_bf16 v[12:15], a[180:183], v[84:87], v[12:15]\n"
"s_sub_u32 s53, s58, 27648\n"
"ds_read_b64_tr_b16 a[196:197], v243 offset:16384\n"
"v_cvt_pk_bf16_f32 v19, v6, v7\n"
"v_mfma_f32_16x16x32_bf16 v[12:15], a[184:187], v[88:91], v[12:15]\n"
"s_add_u32 s58, s58, 131072\n"
"buffer_load_dwordx4 v246, s[8:11], s84 offen offset:1024 lds\n"
"v_mfma_f32_16x16x32_bf16 v[12:15], a[188:191], v[92:95], v[12:15]\n"
"s_waitcnt lgkmcnt(11)\n"
"v_exp_f32 v8, v8\n"
"v_mfma_f32_16x16x32_bf16 a[0:3], a[224:227], v[16:19], a[0:3]\n"
"s_cmp_lt_u32 s41, 3\n"
"s_cselect_b32 s51, s58, s40\n"
"ds_read_b64_tr_b16 a[198:199], v243 offset:20480\n"
"v_add_f32 v44, v44, v3\n"
"v_mfma_f32_16x16x32_bf16 a[4:7], a[228:231], v[16:19], a[4:7]\n"
"s_nop 0\n"
"v_exp_f32 v9, v9\n"
"v_mfma_f32_16x16x32_bf16 a[8:11], a[232:235], v[16:19], a[8:11]\n"
"s_and_b32 s58, s56, 1\n"
"ds_read_b64_tr_b16 a[200:201], v242 offset:16640\n"
"v_cvt_pk_bf16_f32 v20, v8, v9\n"
"v_mfma_f32_16x16x32_bf16 a[12:15], a[236:239], v[16:19], a[12:15]\n"
"s_lshl_b32 s59, s58, 5\n"
"v_exp_f32 v10, v10\n"
"v_mfma_f32_16x16x32_bf16 a[16:19], a[240:243], v[16:19], a[16:19]\n"
"s_add_u32 s59, s59, s40\n"
"ds_read_b64_tr_b16 a[202:203], v242 offset:20736\n"
"v_add_f32 v44, v44, v4\n"
"v_mfma_f32_16x16x32_bf16 a[20:23], a[244:247], v[16:19], a[20:23]\n"
"s_xor_b32 s60, s58, 1\n"
"v_exp_f32 v11, v11\n"
"v_mfma_f32_16x16x32_bf16 a[24:27], a[248:251], v[16:19], a[24:27]\n"
"s_lshl_b32 s60, s60, 8\n"
"ds_read_b64_tr_b16 a[204:205], v243 offset:16640\n"
"v_cvt_pk_bf16_f32 v21, v10, v11\n"
"v_mfma_f32_16x16x32_bf16 a[28:31], a[252:255], v[16:19], a[28:31]\n"
"s_add_u32 s49, s49, 285696\n"
"v_exp_f32 v12, v12\n"
"v_mfma_f32_16x16x32_bf16 v[220:223], a[160:163], v[96:99], v[32:35]\n"
"ds_read_b64_tr_b16 a[206:207], v243 offset:20736\n"
"v_add_f32 v44, v44, v5\n"
"v_mfma_f32_16x16x32_bf16 v[220:223], a[164:167], v[100:103], v[220:223]\n"
"v_exp_f32 v13, v13\n"
"v_mfma_f32_16x16x32_bf16 v[220:223], a[168:171], v[104:107], v[220:223]\n"
"ds_read_b64_tr_b16 a[208:209], v242 offset:16896\n"
"v_cvt_pk_bf16_f32 v22, v12, v13\n"
"v_mfma_f32_16x16x32_bf16 v[220:223], a[172:175], v[108:111], v[220:223]\n"
"v_exp_f32 v14, v14\n"
"v_mfma_f32_16x16x32_bf16 v[224:227], a[176:179], v[96:99], v[32:35]\n"
"ds_read_b64_tr_b16 a[210:211], v242 offset:20992\n"
"v_add_f32 v44, v44, v6\n"
"v_mfma_f32_16x16x32_bf16 v[224:227], a[180:183], v[100:103], v[224:227]\n"
"v_exp_f32 v15, v15\n"
"v_mfma_f32_16x16x32_bf16 v[224:227], a[184:187], v[104:107], v[224:227]\n"
"ds_read_b64_tr_b16 a[212:213], v243 offset:16896\n"
"v_cvt_pk_bf16_f32 v23, v14, v15\n"
"v_mfma_f32_16x16x32_bf16 v[224:227], a[188:191], v[108:111], v[224:227]\n"
"v_exp_f32 v220, v220\n"
"v_mfma_f32_16x16x32_bf16 a[32:35], a[224:227], v[20:23], a[32:35]\n"
"buffer_load_dwordx4 v247, s[8:11], s84 offen offset:2048 lds\n"
"v_mfma_f32_16x16x32_bf16 a[36:39], a[228:231], v[20:23], a[36:39]\n"
"v_exp_f32 v221, v221\n"
"v_mfma_f32_16x16x32_bf16 a[40:43], a[232:235], v[20:23], a[40:43]\n"
"ds_read_b64_tr_b16 a[214:215], v243 offset:20992\n"
"buffer_load_dwordx4 v[144:147], v252, s[64:67], s49 offen\n"
"v_cvt_pk_bf16_f32 v16, v220, v221\n"
"v_mfma_f32_16x16x32_bf16 a[44:47], a[236:239], v[20:23], a[44:47]\n"
"s_add_u32 s60, s60, s40\n"
"v_exp_f32 v222, v222\n"
"v_mfma_f32_16x16x32_bf16 a[48:51], a[240:243], v[20:23], a[48:51]\n"
"ds_read_b64_tr_b16 a[216:217], v242 offset:17152\n"
"v_add_f32 v44, v44, v7\n"
"v_mfma_f32_16x16x32_bf16 a[52:55], a[244:247], v[20:23], a[52:55]\n"
"v_exp_f32 v223, v223\n"
"v_mfma_f32_16x16x32_bf16 a[56:59], a[248:251], v[20:23], a[56:59]\n"
"ds_read_b64_tr_b16 a[218:219], v242 offset:21248\n"
"v_cvt_pk_bf16_f32 v17, v222, v223\n"
"v_mfma_f32_16x16x32_bf16 a[60:63], a[252:255], v[20:23], a[60:63]\n"
"v_exp_f32 v224, v224\n"
"v_mfma_f32_16x16x32_bf16 v[48:51], v[40:43], v[20:23], v[48:51]\n"
"ds_read_b64_tr_b16 a[220:221], v243 offset:17152\n"
"v_add_f32 v52, v52, v220\n"
"v_mfma_f32_16x16x32_bf16 v[228:231], a[160:163], v[112:115], v[36:39]\n"
"v_exp_f32 v225, v225\n"
"v_mfma_f32_16x16x32_bf16 v[228:231], a[164:167], v[116:119], v[228:231]\n"
"ds_read_b64_tr_b16 a[222:223], v243 offset:21248\n"
"v_cvt_pk_bf16_f32 v18, v224, v225\n"
"v_lshrrev_b32 v217, 2, v248\n"
"v_mfma_f32_16x16x32_bf16 v[228:231], a[168:171], v[120:123], v[228:231]\n"
"v_exp_f32 v226, v226\n"
"v_mfma_f32_16x16x32_bf16 v[228:231], a[172:175], v[124:127], v[228:231]\n"
"v_add_f32 v52, v52, v221\n"
"v_add_f32 v52, v52, v222\n"
"v_mfma_f32_16x16x32_bf16 v[232:235], a[176:179], v[112:115], v[36:39]\n"
"v_exp_f32 v227, v227\n"
"v_mfma_f32_16x16x32_bf16 v[232:235], a[180:183], v[116:119], v[232:235]\n"
"v_cvt_pk_bf16_f32 v19, v226, v227\n"
"v_add_f32 v52, v52, v223\n"
"v_mfma_f32_16x16x32_bf16 v[232:235], a[184:187], v[120:123], v[232:235]\n"
"buffer_load_dwordx4 v247, s[8:11], s84 offen offset:3072 lds\n"
"v_mul_u32_u24 v203, 288, v217\n"
"v_mfma_f32_16x16x32_bf16 v[232:235], a[188:191], v[124:127], v[232:235]\n"
"v_exp_f32 v228, v228\n"
"v_mfma_f32_16x16x32_bf16 a[64:67], a[224:227], v[16:19], a[64:67]\n"
"v_add_f32 v52, v52, v224\n"
"v_add_f32 v52, v52, v225\n"
"v_mfma_f32_16x16x32_bf16 a[68:71], a[228:231], v[16:19], a[68:71]\n"
"v_exp_f32 v229, v229\n"
"v_mfma_f32_16x16x32_bf16 a[72:75], a[232:235], v[16:19], a[72:75]\n"
"v_cvt_pk_bf16_f32 v20, v228, v229\n"
"v_add_f32 v52, v52, v226\n"
"v_mfma_f32_16x16x32_bf16 a[76:79], a[236:239], v[16:19], a[76:79]\n"
"v_exp_f32 v230, v230\n"
"v_mfma_f32_16x16x32_bf16 a[80:83], a[240:243], v[16:19], a[80:83]\n"
"s_waitcnt lgkmcnt(8)\n"
"v_and_b32 v217, 3, v248\n"
"v_mfma_f32_16x16x32_bf16 a[84:87], a[244:247], v[16:19], a[84:87]\n"
"v_exp_f32 v231, v231\n"
"v_mfma_f32_16x16x32_bf16 a[88:91], a[248:251], v[16:19], a[88:91]\n"
"v_cvt_pk_bf16_f32 v21, v230, v231\n"
"v_add_f32 v52, v52, v227\n"
"v_mfma_f32_16x16x32_bf16 a[92:95], a[252:255], v[16:19], a[92:95]\n"
"v_exp_f32 v232, v232\n"
"v_mfma_f32_16x16x32_bf16 v[0:3], a[128:131], v[64:67], v[24:27]\n"
"ds_read_b128 a[160:163], v240 offset:8192\n"
"v_lshl_add_u32 v203, v217, 3, v203\n"
"v_mfma_f32_16x16x32_bf16 v[0:3], a[132:135], v[68:71], v[0:3]\n"
"v_exp_f32 v233, v233\n"
"v_mfma_f32_16x16x32_bf16 v[0:3], a[136:139], v[72:75], v[0:3]\n"
"ds_read_b128 a[164:167], v240 offset:8448\n"
"v_cvt_pk_bf16_f32 v22, v232, v233\n"
"v_mfma_f32_16x16x32_bf16 v[0:3], a[140:143], v[76:79], v[0:3]\n"
"v_exp_f32 v234, v234\n"
"v_mfma_f32_16x16x32_bf16 v[4:7], a[144:147], v[64:67], v[24:27]\n"
"ds_read_b128 a[168:171], v240 offset:8704\n"
"v_add_u32 v200, s59, v203\n"
"v_mfma_f32_16x16x32_bf16 v[4:7], a[148:151], v[68:71], v[4:7]\n"
"v_exp_f32 v235, v235\n"
"v_mfma_f32_16x16x32_bf16 v[4:7], a[152:155], v[72:75], v[4:7]\n"
"ds_read_b128 a[172:175], v240 offset:8960\n"
"v_cvt_pk_bf16_f32 v23, v234, v235\n"
"v_mfma_f32_16x16x32_bf16 v[4:7], a[156:159], v[76:79], v[4:7]\n"
"s_add_u32 m0, s86, 114688\n"
"v_exp_f32 v0, v0\n"
"v_mfma_f32_16x16x32_bf16 a[96:99], a[224:227], v[20:23], a[96:99]\n"
"buffer_load_dwordx4 v246, s[12:15], s84 offen offset:0 lds\n"
"v_add_u32 v203, s60, v203\n"
"v_mfma_f32_16x16x32_bf16 a[100:103], a[228:231], v[20:23], a[100:103]\n"
"v_exp_f32 v1, v1\n"
"v_mfma_f32_16x16x32_bf16 a[104:107], a[232:235], v[20:23], a[104:107]\n"
"ds_read_b128 a[176:179], v240 offset:12288\n"
"v_cvt_pk_bf16_f32 v16, v0, v1\n"
"v_mfma_f32_16x16x32_bf16 a[108:111], a[236:239], v[20:23], a[108:111]\n"
"v_exp_f32 v2, v2\n"
"v_mfma_f32_16x16x32_bf16 a[112:115], a[240:243], v[20:23], a[112:115]\n"
"ds_read_b128 a[180:183], v240 offset:12544\n"
"v_add_f32 v44, v44, v0\n"
"v_mfma_f32_16x16x32_bf16 a[116:119], a[244:247], v[20:23], a[116:119]\n"
"v_exp_f32 v3, v3\n"
"v_mfma_f32_16x16x32_bf16 a[120:123], a[248:251], v[20:23], a[120:123]\n"
"ds_read_b128 a[184:187], v240 offset:12800\n"
"v_cvt_pk_bf16_f32 v17, v2, v3\n"
"v_mfma_f32_16x16x32_bf16 a[124:127], a[252:255], v[20:23], a[124:127]\n"
"v_exp_f32 v4, v4\n"
"v_add_u32 v201, 65536, v200\n"
"v_mfma_f32_16x16x32_bf16 v[56:59], v[40:43], v[20:23], v[56:59]\n"
"ds_read_b128 a[188:191], v240 offset:13056\n"
"v_add_f32 v44, v44, v1\n"
"v_mfma_f32_16x16x32_bf16 v[8:11], a[128:131], v[80:83], v[28:31]\n"
"v_exp_f32 v5, v5\n"
"v_mfma_f32_16x16x32_bf16 v[8:11], a[132:135], v[84:87], v[8:11]\n"
"ds_read_b64_tr_b16 a[224:225], v242 offset:24576\n"
"v_cvt_pk_bf16_f32 v18, v4, v5\n"
"v_mfma_f32_16x16x32_bf16 v[8:11], a[136:139], v[88:91], v[8:11]\n"
"v_exp_f32 v6, v6\n"
"v_mfma_f32_16x16x32_bf16 v[8:11], a[140:143], v[92:95], v[8:11]\n"
"ds_read_b64_tr_b16 a[226:227], v242 offset:28672\n"
"v_add_f32 v44, v44, v2\n"
"v_mfma_f32_16x16x32_bf16 v[12:15], a[144:147], v[80:83], v[28:31]\n"
"v_exp_f32 v7, v7\n"
"v_mfma_f32_16x16x32_bf16 v[12:15], a[148:151], v[84:87], v[12:15]\n"
"ds_read_b64_tr_b16 a[228:229], v243 offset:24576\n"
"v_cvt_pk_bf16_f32 v19, v6, v7\n"
"v_add_u32 v204, 65536, v203\n"
"v_mfma_f32_16x16x32_bf16 v[12:15], a[152:155], v[88:91], v[12:15]\n"
"buffer_load_dwordx4 v247, s[12:15], s84 offen offset:1024 lds\n"
"v_add_u32 v202, 131072, v200\n"
"v_mfma_f32_16x16x32_bf16 v[12:15], a[156:159], v[92:95], v[12:15]\n"
"s_waitcnt lgkmcnt(11)\n"
"v_exp_f32 v8, v8\n"
"v_mfma_f32_16x16x32_bf16 a[0:3], a[192:195], v[16:19], a[0:3]\n"
"ds_read_b64_tr_b16 a[230:231], v243 offset:28672\n"
"v_add_f32 v44, v44, v3\n"
"v_mfma_f32_16x16x32_bf16 a[4:7], a[196:199], v[16:19], a[4:7]\n"
"v_exp_f32 v9, v9\n"
"v_mfma_f32_16x16x32_bf16 a[8:11], a[200:203], v[16:19], a[8:11]\n"
"ds_read_b64_tr_b16 a[232:233], v242 offset:24832\n"
"v_cvt_pk_bf16_f32 v20, v8, v9\n"
"v_mfma_f32_16x16x32_bf16 a[12:15], a[204:207], v[16:19], a[12:15]\n"
"v_exp_f32 v10, v10\n"
"v_mfma_f32_16x16x32_bf16 a[16:19], a[208:211], v[16:19], a[16:19]\n"
"ds_read_b64_tr_b16 a[234:235], v242 offset:28928\n"
"v_add_f32 v44, v44, v4\n"
"v_mfma_f32_16x16x32_bf16 a[20:23], a[212:215], v[16:19], a[20:23]\n"
"v_exp_f32 v11, v11\n"
"v_add_u32 v205, 131072, v203\n"
"v_mfma_f32_16x16x32_bf16 a[24:27], a[216:219], v[16:19], a[24:27]\n"
"s_add_u32 s62, s40, 163776\n"
"ds_read_b64_tr_b16 a[236:237], v243 offset:24832\n"
"v_cvt_pk_bf16_f32 v21, v10, v11\n"
"v_mfma_f32_16x16x32_bf16 a[28:31], a[220:223], v[16:19], a[28:31]\n"
"v_exp_f32 v12, v12\n"
"v_mfma_f32_16x16x32_bf16 v[220:223], a[128:131], v[96:99], v[32:35]\n"
"ds_read_b64_tr_b16 a[238:239], v243 offset:28928\n"
"v_add_f32 v44, v44, v5\n"
"v_mfma_f32_16x16x32_bf16 v[220:223], a[132:135], v[100:103], v[220:223]\n"
"v_exp_f32 v13, v13\n"
"v_mfma_f32_16x16x32_bf16 v[220:223], a[136:139], v[104:107], v[220:223]\n"
"ds_read_b64_tr_b16 a[240:241], v242 offset:25088\n"
"v_cvt_pk_bf16_f32 v22, v12, v13\n"
"v_mfma_f32_16x16x32_bf16 v[220:223], a[140:143], v[108:111], v[220:223]\n"
"v_exp_f32 v14, v14\n"
"v_mfma_f32_16x16x32_bf16 v[224:227], a[144:147], v[96:99], v[32:35]\n"
"ds_read_b64_tr_b16 a[242:243], v242 offset:29184\n"
"v_add_f32 v44, v44, v6\n"
"v_mfma_f32_16x16x32_bf16 v[224:227], a[148:151], v[100:103], v[224:227]\n"
"v_exp_f32 v15, v15\n"
"v_lshl_add_u32 v209, v217, 2, s62\n"
"v_mfma_f32_16x16x32_bf16 v[224:227], a[152:155], v[104:107], v[224:227]\n"
"s_lshl_b32 s55, s44, 4\n"
"ds_read_b64_tr_b16 a[244:245], v243 offset:25088\n"
"v_cvt_pk_bf16_f32 v23, v14, v15\n"
"v_mfma_f32_16x16x32_bf16 v[224:227], a[156:159], v[108:111], v[224:227]\n"
"s_add_u32 s55, s55, s43\n"
"v_exp_f32 v220, v220\n"
"v_mfma_f32_16x16x32_bf16 a[32:35], a[192:195], v[20:23], a[32:35]\n"
"s_lshl_b32 s52, s55, 2\n"
"buffer_load_dwordx4 v246, s[12:15], s84 offen offset:2048 lds\n"
"v_mfma_f32_16x16x32_bf16 a[36:39], a[196:199], v[20:23], a[36:39]\n"
"v_exp_f32 v221, v221\n"
"v_mfma_f32_16x16x32_bf16 a[40:43], a[200:203], v[20:23], a[40:43]\n"
"ds_read_b64_tr_b16 a[246:247], v243 offset:29184\n"
"buffer_load_dwordx4 v[180:183], v252, s[68:71], s49 offen\n"
"v_cvt_pk_bf16_f32 v16, v220, v221\n"
"v_mfma_f32_16x16x32_bf16 a[44:47], a[204:207], v[20:23], a[44:47]\n"
"s_add_u32 s52, s52, s41\n"
"v_exp_f32 v222, v222\n"
"v_mfma_f32_16x16x32_bf16 a[48:51], a[208:211], v[20:23], a[48:51]\n"
"s_lshl_b32 s52, s52, 14\n"
"ds_read_b64_tr_b16 a[248:249], v242 offset:25344\n"
"v_add_f32 v44, v44, v7\n"
"v_mfma_f32_16x16x32_bf16 a[52:55], a[212:215], v[20:23], a[52:55]\n"
"s_add_u32 s52, s52, 9592832\n"
"v_exp_f32 v223, v223\n"
"v_mfma_f32_16x16x32_bf16 a[56:59], a[216:219], v[20:23], a[56:59]\n"
"s_add_u32 s49, s49, 9216\n"
"ds_read_b64_tr_b16 a[250:251], v242 offset:29440\n"
"v_cvt_pk_bf16_f32 v17, v222, v223\n"
"v_mfma_f32_16x16x32_bf16 a[60:63], a[220:223], v[20:23], a[60:63]\n"
"v_exp_f32 v224, v224\n"
"v_mfma_f32_16x16x32_bf16 v[48:51], v[40:43], v[20:23], v[48:51]\n"
"ds_read_b64_tr_b16 a[252:253], v243 offset:25344\n"
"v_add_f32 v52, v52, v220\n"
"v_mfma_f32_16x16x32_bf16 v[228:231], a[128:131], v[112:115], v[36:39]\n"
"v_exp_f32 v225, v225\n"
"v_mfma_f32_16x16x32_bf16 v[228:231], a[132:135], v[116:119], v[228:231]\n"
"ds_read_b64_tr_b16 a[254:255], v243 offset:29440\n"
"v_cvt_pk_bf16_f32 v18, v224, v225\n"
"v_mfma_f32_16x16x32_bf16 v[228:231], a[136:139], v[120:123], v[228:231]\n"
"v_exp_f32 v226, v226\n"
"v_mfma_f32_16x16x32_bf16 v[228:231], a[140:143], v[124:127], v[228:231]\n"
"v_add_f32 v52, v52, v221\n"
"v_add_f32 v52, v52, v222\n"
"v_mfma_f32_16x16x32_bf16 v[232:235], a[144:147], v[112:115], v[36:39]\n"
"v_exp_f32 v227, v227\n"
"v_mfma_f32_16x16x32_bf16 v[232:235], a[148:151], v[116:119], v[232:235]\n"
"v_cvt_pk_bf16_f32 v19, v226, v227\n"
"v_add_f32 v52, v52, v223\n"
"v_mfma_f32_16x16x32_bf16 v[232:235], a[152:155], v[120:123], v[232:235]\n"
"buffer_load_dwordx4 v247, s[12:15], s84 offen offset:3072 lds\n"
"v_mfma_f32_16x16x32_bf16 v[232:235], a[156:159], v[124:127], v[232:235]\n"
"s_add_u32 s84, s84, 16384\n"
"v_exp_f32 v228, v228\n"
"v_mfma_f32_16x16x32_bf16 a[64:67], a[192:195], v[16:19], a[64:67]\n"
"v_add_f32 v52, v52, v224\n"
"v_add_f32 v52, v52, v225\n"
"v_mfma_f32_16x16x32_bf16 a[68:71], a[196:199], v[16:19], a[68:71]\n"
"v_exp_f32 v229, v229\n"
"v_mfma_f32_16x16x32_bf16 a[72:75], a[200:203], v[16:19], a[72:75]\n"
"v_cvt_pk_bf16_f32 v20, v228, v229\n"
"v_add_f32 v52, v52, v226\n"
"v_mfma_f32_16x16x32_bf16 a[76:79], a[204:207], v[16:19], a[76:79]\n"
"s_waitcnt vmcnt(24) lgkmcnt(0)\n"
"v_exp_f32 v230, v230\n"
"v_mfma_f32_16x16x32_bf16 a[80:83], a[208:211], v[16:19], a[80:83]\n"
"s_barrier\n"
"v_mfma_f32_16x16x32_bf16 a[84:87], a[212:215], v[16:19], a[84:87]\n"
"v_exp_f32 v231, v231\n"
"v_mfma_f32_16x16x32_bf16 a[88:91], a[216:219], v[16:19], a[88:91]\n"
"v_cvt_pk_bf16_f32 v21, v230, v231\n"
"v_add_f32 v52, v52, v227\n"
"v_mfma_f32_16x16x32_bf16 a[92:95], a[220:223], v[16:19], a[92:95]\n"
"v_exp_f32 v232, v232\n"
"v_mfma_f32_16x16x32_bf16 v[0:3], a[160:163], v[64:67], v[24:27]\n"
"ds_read_b128 a[128:131], v240 offset:32768\n"
"v_mfma_f32_16x16x32_bf16 v[0:3], a[164:167], v[68:71], v[0:3]\n"
"v_exp_f32 v233, v233\n"
"v_mfma_f32_16x16x32_bf16 v[0:3], a[168:171], v[72:75], v[0:3]\n"
"ds_read_b128 a[132:135], v240 offset:33024\n"
"v_cvt_pk_bf16_f32 v22, v232, v233\n"
"v_mfma_f32_16x16x32_bf16 v[0:3], a[172:175], v[76:79], v[0:3]\n"
"v_exp_f32 v234, v234\n"
"v_mfma_f32_16x16x32_bf16 v[4:7], a[176:179], v[64:67], v[24:27]\n"
"ds_read_b128 a[136:139], v240 offset:33280\n"
"v_mfma_f32_16x16x32_bf16 v[4:7], a[180:183], v[68:71], v[4:7]\n"
"v_exp_f32 v235, v235\n"
"v_mfma_f32_16x16x32_bf16 v[4:7], a[184:187], v[72:75], v[4:7]\n"
"ds_read_b128 a[140:143], v240 offset:33536\n"
"buffer_load_dwordx4 v[148:151], v252, s[64:67], s49 offen\n"
"v_cvt_pk_bf16_f32 v23, v234, v235\n"
"v_mfma_f32_16x16x32_bf16 v[4:7], a[188:191], v[76:79], v[4:7]\n"
"s_mov_b32 m0, s51\n"
"v_exp_f32 v0, v0\n"
"v_mfma_f32_16x16x32_bf16 a[96:99], a[192:195], v[20:23], a[96:99]\n"
"buffer_load_dwordx4 v210, s[24:27], s20 offen offset:0 lds\n"
"v_mfma_f32_16x16x32_bf16 a[100:103], a[196:199], v[20:23], a[100:103]\n"
"v_exp_f32 v1, v1\n"
"v_mfma_f32_16x16x32_bf16 a[104:107], a[200:203], v[20:23], a[104:107]\n"
"ds_read_b128 a[144:147], v240 offset:36864\n"
"v_cvt_pk_bf16_f32 v16, v0, v1\n"
"v_mfma_f32_16x16x32_bf16 a[108:111], a[204:207], v[20:23], a[108:111]\n"
"v_exp_f32 v2, v2\n"
"v_mfma_f32_16x16x32_bf16 a[112:115], a[208:211], v[20:23], a[112:115]\n"
"ds_read_b128 a[148:151], v240 offset:37120\n"
"v_add_f32 v44, v44, v0\n"
"v_mfma_f32_16x16x32_bf16 a[116:119], a[212:215], v[20:23], a[116:119]\n"
"v_exp_f32 v3, v3\n"
"v_mfma_f32_16x16x32_bf16 a[120:123], a[216:219], v[20:23], a[120:123]\n"
"ds_read_b128 a[152:155], v240 offset:37376\n"
"v_cvt_pk_bf16_f32 v17, v2, v3\n"
"v_mfma_f32_16x16x32_bf16 a[124:127], a[220:223], v[20:23], a[124:127]\n"
"v_exp_f32 v4, v4\n"
"v_mfma_f32_16x16x32_bf16 v[56:59], v[40:43], v[20:23], v[56:59]\n"
"ds_read_b128 a[156:159], v240 offset:37632\n"
"v_add_f32 v44, v44, v1\n"
"v_mfma_f32_16x16x32_bf16 v[8:11], a[160:163], v[80:83], v[28:31]\n"
"v_exp_f32 v5, v5\n"
"v_mfma_f32_16x16x32_bf16 v[8:11], a[164:167], v[84:87], v[8:11]\n"
"ds_read_b64_tr_b16 a[192:193], v242 offset:49152\n"
"v_cvt_pk_bf16_f32 v18, v4, v5\n"
"v_mfma_f32_16x16x32_bf16 v[8:11], a[168:171], v[88:91], v[8:11]\n"
"v_exp_f32 v6, v6\n"
"v_mfma_f32_16x16x32_bf16 v[8:11], a[172:175], v[92:95], v[8:11]\n"
"ds_read_b64_tr_b16 a[194:195], v242 offset:53248\n"
"v_add_f32 v44, v44, v2\n"
"v_mfma_f32_16x16x32_bf16 v[12:15], a[176:179], v[80:83], v[28:31]\n"
"v_exp_f32 v7, v7\n"
"v_mfma_f32_16x16x32_bf16 v[12:15], a[180:183], v[84:87], v[12:15]\n"
"s_mov_b32 m0, s51\n"
"ds_read_b64_tr_b16 a[196:197], v243 offset:49152\n"
"v_cvt_pk_bf16_f32 v19, v6, v7\n"
"v_mfma_f32_16x16x32_bf16 v[12:15], a[184:187], v[88:91], v[12:15]\n"
"buffer_load_dwordx4 v211, s[24:27], s20 offen offset:1024 lds\n"
"v_mfma_f32_16x16x32_bf16 v[12:15], a[188:191], v[92:95], v[12:15]\n"
"s_waitcnt lgkmcnt(11)\n"
"v_exp_f32 v8, v8\n"
"v_mfma_f32_16x16x32_bf16 a[0:3], a[224:227], v[16:19], a[0:3]\n"
"ds_read_b64_tr_b16 a[198:199], v243 offset:53248\n"
"v_add_f32 v44, v44, v3\n"
"v_mfma_f32_16x16x32_bf16 a[4:7], a[228:231], v[16:19], a[4:7]\n"
"v_exp_f32 v9, v9\n"
"v_mfma_f32_16x16x32_bf16 a[8:11], a[232:235], v[16:19], a[8:11]\n"
"ds_read_b64_tr_b16 a[200:201], v242 offset:49408\n"
"v_cvt_pk_bf16_f32 v20, v8, v9\n"
"v_mfma_f32_16x16x32_bf16 a[12:15], a[236:239], v[16:19], a[12:15]\n"
"v_exp_f32 v10, v10\n"
"v_mfma_f32_16x16x32_bf16 a[16:19], a[240:243], v[16:19], a[16:19]\n"
"ds_read_b64_tr_b16 a[202:203], v242 offset:53504\n"
"v_add_f32 v44, v44, v4\n"
"v_mfma_f32_16x16x32_bf16 a[20:23], a[244:247], v[16:19], a[20:23]\n"
"v_exp_f32 v11, v11\n"
"v_mfma_f32_16x16x32_bf16 a[24:27], a[248:251], v[16:19], a[24:27]\n"
"ds_read_b64_tr_b16 a[204:205], v243 offset:49408\n"
"v_cvt_pk_bf16_f32 v21, v10, v11\n"
"v_mfma_f32_16x16x32_bf16 a[28:31], a[252:255], v[16:19], a[28:31]\n"
"v_exp_f32 v12, v12\n"
"v_mfma_f32_16x16x32_bf16 v[220:223], a[160:163], v[96:99], v[32:35]\n"
"ds_read_b64_tr_b16 a[206:207], v243 offset:53504\n"
"v_add_f32 v44, v44, v5\n"
"v_mfma_f32_16x16x32_bf16 v[220:223], a[164:167], v[100:103], v[220:223]\n"
"v_exp_f32 v13, v13\n"
"v_mfma_f32_16x16x32_bf16 v[220:223], a[168:171], v[104:107], v[220:223]\n"
"ds_read_b64_tr_b16 a[208:209], v242 offset:49664\n"
"buffer_load_dwordx4 v[184:187], v252, s[68:71], s49 offen\n"
"v_cvt_pk_bf16_f32 v22, v12, v13\n"
"v_mfma_f32_16x16x32_bf16 v[220:223], a[172:175], v[108:111], v[220:223]\n"
"s_add_u32 s49, s49, 285696\n"
"v_exp_f32 v14, v14\n"
"v_mfma_f32_16x16x32_bf16 v[224:227], a[176:179], v[96:99], v[32:35]\n"
"ds_read_b64_tr_b16 a[210:211], v242 offset:53760\n"
"v_add_f32 v44, v44, v6\n"
"v_mfma_f32_16x16x32_bf16 v[224:227], a[180:183], v[100:103], v[224:227]\n"
"v_exp_f32 v15, v15\n"
"v_mfma_f32_16x16x32_bf16 v[224:227], a[184:187], v[104:107], v[224:227]\n"
"ds_read_b64_tr_b16 a[212:213], v243 offset:49664\n"
"v_cvt_pk_bf16_f32 v23, v14, v15\n"
"v_mfma_f32_16x16x32_bf16 v[224:227], a[188:191], v[108:111], v[224:227]\n"
"s_mov_b32 m0, s51\n"
"v_exp_f32 v220, v220\n"
"v_mfma_f32_16x16x32_bf16 a[32:35], a[224:227], v[20:23], a[32:35]\n"
"buffer_load_dwordx4 v212, s[24:27], s20 offen offset:2048 lds\n"
"v_mfma_f32_16x16x32_bf16 a[36:39], a[228:231], v[20:23], a[36:39]\n"
"v_exp_f32 v221, v221\n"
"v_mfma_f32_16x16x32_bf16 a[40:43], a[232:235], v[20:23], a[40:43]\n"
"ds_read_b64_tr_b16 a[214:215], v243 offset:53760\n"
"v_cvt_pk_bf16_f32 v16, v220, v221\n"
"v_mfma_f32_16x16x32_bf16 a[44:47], a[236:239], v[20:23], a[44:47]\n"
"v_exp_f32 v222, v222\n"
"v_mfma_f32_16x16x32_bf16 a[48:51], a[240:243], v[20:23], a[48:51]\n"
"ds_read_b64_tr_b16 a[216:217], v242 offset:49920\n"
"v_add_f32 v44, v44, v7\n"
"v_mfma_f32_16x16x32_bf16 a[52:55], a[244:247], v[20:23], a[52:55]\n"
"v_exp_f32 v223, v223\n"
"v_mfma_f32_16x16x32_bf16 a[56:59], a[248:251], v[20:23], a[56:59]\n"
"ds_read_b64_tr_b16 a[218:219], v242 offset:54016\n"
"v_cvt_pk_bf16_f32 v17, v222, v223\n"
"v_mfma_f32_16x16x32_bf16 a[60:63], a[252:255], v[20:23], a[60:63]\n"
"v_exp_f32 v224, v224\n"
"v_mfma_f32_16x16x32_bf16 v[48:51], v[40:43], v[20:23], v[48:51]\n"
"ds_read_b64_tr_b16 a[220:221], v243 offset:49920\n"
"v_add_f32 v52, v52, v220\n"
"v_mfma_f32_16x16x32_bf16 v[228:231], a[160:163], v[112:115], v[36:39]\n"
"v_exp_f32 v225, v225\n"
"v_mfma_f32_16x16x32_bf16 v[228:231], a[164:167], v[116:119], v[228:231]\n"
"ds_read_b64_tr_b16 a[222:223], v243 offset:54016\n"
"v_cvt_pk_bf16_f32 v18, v224, v225\n"
"v_mfma_f32_16x16x32_bf16 v[228:231], a[168:171], v[120:123], v[228:231]\n"
"v_exp_f32 v226, v226\n"
"v_mfma_f32_16x16x32_bf16 v[228:231], a[172:175], v[124:127], v[228:231]\n"
"v_add_f32 v52, v52, v221\n"
"v_add_f32 v52, v52, v222\n"
"v_mfma_f32_16x16x32_bf16 v[232:235], a[176:179], v[112:115], v[36:39]\n"
"v_exp_f32 v227, v227\n"
"v_mfma_f32_16x16x32_bf16 v[232:235], a[180:183], v[116:119], v[232:235]\n"
"s_mov_b32 m0, s51\n"
"v_cvt_pk_bf16_f32 v19, v226, v227\n"
"v_add_f32 v52, v52, v223\n"
"v_mfma_f32_16x16x32_bf16 v[232:235], a[184:187], v[120:123], v[232:235]\n"
"buffer_load_dwordx4 v213, s[24:27], s20 offen offset:3072 lds\n"
"v_mfma_f32_16x16x32_bf16 v[232:235], a[188:191], v[124:127], v[232:235]\n"
"v_exp_f32 v228, v228\n"
"v_mfma_f32_16x16x32_bf16 a[64:67], a[224:227], v[16:19], a[64:67]\n"
"v_add_f32 v52, v52, v224\n"
"v_add_f32 v52, v52, v225\n"
"v_mfma_f32_16x16x32_bf16 a[68:71], a[228:231], v[16:19], a[68:71]\n"
"v_exp_f32 v229, v229\n"
"v_mfma_f32_16x16x32_bf16 a[72:75], a[232:235], v[16:19], a[72:75]\n"
"v_cvt_pk_bf16_f32 v20, v228, v229\n"
"v_add_f32 v52, v52, v226\n"
"v_mfma_f32_16x16x32_bf16 a[76:79], a[236:239], v[16:19], a[76:79]\n"
"v_exp_f32 v230, v230\n"
"v_mfma_f32_16x16x32_bf16 a[80:83], a[240:243], v[16:19], a[80:83]\n"
"s_waitcnt lgkmcnt(8)\n"
"v_mfma_f32_16x16x32_bf16 a[84:87], a[244:247], v[16:19], a[84:87]\n"
"v_exp_f32 v231, v231\n"
"v_mfma_f32_16x16x32_bf16 a[88:91], a[248:251], v[16:19], a[88:91]\n"
"buffer_load_dwordx4 v[152:155], v252, s[64:67], s49 offen\n"
"v_cvt_pk_bf16_f32 v21, v230, v231\n"
"v_add_f32 v52, v52, v227\n"
"v_mfma_f32_16x16x32_bf16 a[92:95], a[252:255], v[16:19], a[92:95]\n"
"v_exp_f32 v232, v232\n"
"v_mfma_f32_16x16x32_bf16 v[0:3], a[128:131], v[64:67], v[24:27]\n"
"ds_read_b128 a[160:163], v240 offset:40960\n"
"v_mfma_f32_16x16x32_bf16 v[0:3], a[132:135], v[68:71], v[0:3]\n"
"v_exp_f32 v233, v233\n"
"v_mfma_f32_16x16x32_bf16 v[0:3], a[136:139], v[72:75], v[0:3]\n"
"ds_read_b128 a[164:167], v240 offset:41216\n"
"v_cvt_pk_bf16_f32 v22, v232, v233\n"
"v_mfma_f32_16x16x32_bf16 v[0:3], a[140:143], v[76:79], v[0:3]\n"
"v_exp_f32 v234, v234\n"
"v_mfma_f32_16x16x32_bf16 v[4:7], a[144:147], v[64:67], v[24:27]\n"
"ds_read_b128 a[168:171], v240 offset:41472\n"
"v_mfma_f32_16x16x32_bf16 v[4:7], a[148:151], v[68:71], v[4:7]\n"
"v_exp_f32 v235, v235\n"
"v_mfma_f32_16x16x32_bf16 v[4:7], a[152:155], v[72:75], v[4:7]\n"
"ds_read_b128 a[172:175], v240 offset:41728\n"
"v_cvt_pk_bf16_f32 v23, v234, v235\n"
"v_mfma_f32_16x16x32_bf16 v[4:7], a[156:159], v[76:79], v[4:7]\n"
"s_add_u32 m0, s51, 4096\n"
"v_exp_f32 v0, v0\n"
"v_mfma_f32_16x16x32_bf16 a[96:99], a[224:227], v[20:23], a[96:99]\n"
"buffer_load_dwordx4 v214, s[24:27], s20 offen offset:0 lds\n"
"v_mfma_f32_16x16x32_bf16 a[100:103], a[228:231], v[20:23], a[100:103]\n"
"v_exp_f32 v1, v1\n"
"v_mfma_f32_16x16x32_bf16 a[104:107], a[232:235], v[20:23], a[104:107]\n"
"ds_read_b128 a[176:179], v240 offset:45056\n"
"v_cvt_pk_bf16_f32 v16, v0, v1\n"
"v_mfma_f32_16x16x32_bf16 a[108:111], a[236:239], v[20:23], a[108:111]\n"
"v_exp_f32 v2, v2\n"
"v_mfma_f32_16x16x32_bf16 a[112:115], a[240:243], v[20:23], a[112:115]\n"
"ds_read_b128 a[180:183], v240 offset:45312\n"
"v_add_f32 v44, v44, v0\n"
"v_mfma_f32_16x16x32_bf16 a[116:119], a[244:247], v[20:23], a[116:119]\n"
"v_exp_f32 v3, v3\n"
"v_mfma_f32_16x16x32_bf16 a[120:123], a[248:251], v[20:23], a[120:123]\n"
"ds_read_b128 a[184:187], v240 offset:45568\n"
"v_cvt_pk_bf16_f32 v17, v2, v3\n"
"v_mfma_f32_16x16x32_bf16 a[124:127], a[252:255], v[20:23], a[124:127]\n"
"v_exp_f32 v4, v4\n"
"v_mfma_f32_16x16x32_bf16 v[56:59], v[40:43], v[20:23], v[56:59]\n"
"ds_read_b128 a[188:191], v240 offset:45824\n"
"v_add_f32 v44, v44, v1\n"
"v_mfma_f32_16x16x32_bf16 v[8:11], a[128:131], v[80:83], v[28:31]\n"
"v_exp_f32 v5, v5\n"
"v_mfma_f32_16x16x32_bf16 v[8:11], a[132:135], v[84:87], v[8:11]\n"
"ds_read_b64_tr_b16 a[224:225], v242 offset:57344\n"
"v_cvt_pk_bf16_f32 v18, v4, v5\n"
"v_mfma_f32_16x16x32_bf16 v[8:11], a[136:139], v[88:91], v[8:11]\n"
"v_exp_f32 v6, v6\n"
"v_mfma_f32_16x16x32_bf16 v[8:11], a[140:143], v[92:95], v[8:11]\n"
"ds_read_b64_tr_b16 a[226:227], v242 offset:61440\n"
"v_add_f32 v44, v44, v2\n"
"v_mfma_f32_16x16x32_bf16 v[12:15], a[144:147], v[80:83], v[28:31]\n"
"v_exp_f32 v7, v7\n"
"v_mfma_f32_16x16x32_bf16 v[12:15], a[148:151], v[84:87], v[12:15]\n"
"s_add_u32 m0, s51, 4096\n"
"ds_read_b64_tr_b16 a[228:229], v243 offset:57344\n"
"v_cvt_pk_bf16_f32 v19, v6, v7\n"
"v_mfma_f32_16x16x32_bf16 v[12:15], a[152:155], v[88:91], v[12:15]\n"
"buffer_load_dwordx4 v236, s[24:27], s20 offen offset:1024 lds\n"
"v_mfma_f32_16x16x32_bf16 v[12:15], a[156:159], v[92:95], v[12:15]\n"
"s_waitcnt lgkmcnt(11)\n"
"v_exp_f32 v8, v8\n"
"v_mfma_f32_16x16x32_bf16 a[0:3], a[192:195], v[16:19], a[0:3]\n"
"ds_read_b64_tr_b16 a[230:231], v243 offset:61440\n"
"v_add_f32 v44, v44, v3\n"
"v_mfma_f32_16x16x32_bf16 a[4:7], a[196:199], v[16:19], a[4:7]\n"
"v_exp_f32 v9, v9\n"
"v_mfma_f32_16x16x32_bf16 a[8:11], a[200:203], v[16:19], a[8:11]\n"
"ds_read_b64_tr_b16 a[232:233], v242 offset:57600\n"
"buffer_load_dwordx4 v[188:191], v252, s[68:71], s49 offen\n"
"v_cvt_pk_bf16_f32 v20, v8, v9\n"
"v_mfma_f32_16x16x32_bf16 a[12:15], a[204:207], v[16:19], a[12:15]\n"
"s_add_u32 s49, s49, 9216\n"
"v_exp_f32 v10, v10\n"
"v_mfma_f32_16x16x32_bf16 a[16:19], a[208:211], v[16:19], a[16:19]\n"
"ds_read_b64_tr_b16 a[234:235], v242 offset:61696\n"
"v_add_f32 v44, v44, v4\n"
"v_mfma_f32_16x16x32_bf16 a[20:23], a[212:215], v[16:19], a[20:23]\n"
"s_add_u32 m0, s51, 4096\n"
"v_exp_f32 v11, v11\n"
"v_mfma_f32_16x16x32_bf16 a[24:27], a[216:219], v[16:19], a[24:27]\n"
"buffer_load_dwordx4 v237, s[24:27], s20 offen offset:2048 lds\n"
"ds_read_b64_tr_b16 a[236:237], v243 offset:57600\n"
"v_cvt_pk_bf16_f32 v21, v10, v11\n"
"v_mfma_f32_16x16x32_bf16 a[28:31], a[220:223], v[16:19], a[28:31]\n"
"v_exp_f32 v12, v12\n"
"v_mfma_f32_16x16x32_bf16 v[220:223], a[128:131], v[96:99], v[32:35]\n"
"ds_read_b64_tr_b16 a[238:239], v243 offset:61696\n"
"v_add_f32 v44, v44, v5\n"
"v_mfma_f32_16x16x32_bf16 v[220:223], a[132:135], v[100:103], v[220:223]\n"
"v_exp_f32 v13, v13\n"
"v_mfma_f32_16x16x32_bf16 v[220:223], a[136:139], v[104:107], v[220:223]\n"
"ds_read_b64_tr_b16 a[240:241], v242 offset:57856\n"
"v_cvt_pk_bf16_f32 v22, v12, v13\n"
"v_mfma_f32_16x16x32_bf16 v[220:223], a[140:143], v[108:111], v[220:223]\n"
"v_exp_f32 v14, v14\n"
"v_mfma_f32_16x16x32_bf16 v[224:227], a[144:147], v[96:99], v[32:35]\n"
"ds_read_b64_tr_b16 a[242:243], v242 offset:61952\n"
"v_add_f32 v44, v44, v6\n"
"v_mfma_f32_16x16x32_bf16 v[224:227], a[148:151], v[100:103], v[224:227]\n"
"v_exp_f32 v15, v15\n"
"v_mfma_f32_16x16x32_bf16 v[224:227], a[152:155], v[104:107], v[224:227]\n"
"ds_read_b64_tr_b16 a[244:245], v243 offset:57856\n"
"v_cvt_pk_bf16_f32 v23, v14, v15\n"
"v_mfma_f32_16x16x32_bf16 v[224:227], a[156:159], v[108:111], v[224:227]\n"
"s_add_u32 m0, s51, 4096\n"
"v_exp_f32 v220, v220\n"
"v_mfma_f32_16x16x32_bf16 a[32:35], a[192:195], v[20:23], a[32:35]\n"
"buffer_load_dwordx4 v238, s[24:27], s20 offen offset:3072 lds\n"
"v_mfma_f32_16x16x32_bf16 a[36:39], a[196:199], v[20:23], a[36:39]\n"
"v_exp_f32 v221, v221\n"
"v_mfma_f32_16x16x32_bf16 a[40:43], a[200:203], v[20:23], a[40:43]\n"
"ds_read_b64_tr_b16 a[246:247], v243 offset:61952\n"
"v_cvt_pk_bf16_f32 v16, v220, v221\n"
"v_mfma_f32_16x16x32_bf16 a[44:47], a[204:207], v[20:23], a[44:47]\n"
"v_exp_f32 v222, v222\n"
"v_mfma_f32_16x16x32_bf16 a[48:51], a[208:211], v[20:23], a[48:51]\n"
"ds_read_b64_tr_b16 a[248:249], v242 offset:58112\n"
"v_add_f32 v44, v44, v7\n"
"v_mfma_f32_16x16x32_bf16 a[52:55], a[212:215], v[20:23], a[52:55]\n"
"v_exp_f32 v223, v223\n"
"v_mfma_f32_16x16x32_bf16 a[56:59], a[216:219], v[20:23], a[56:59]\n"
"ds_read_b64_tr_b16 a[250:251], v242 offset:62208\n"
"v_cvt_pk_bf16_f32 v17, v222, v223\n"
"v_mfma_f32_16x16x32_bf16 a[60:63], a[220:223], v[20:23], a[60:63]\n"
"v_exp_f32 v224, v224\n"
"v_mfma_f32_16x16x32_bf16 v[48:51], v[40:43], v[20:23], v[48:51]\n"
"ds_read_b64_tr_b16 a[252:253], v243 offset:58112\n"
"v_add_f32 v52, v52, v220\n"
"v_mfma_f32_16x16x32_bf16 v[228:231], a[128:131], v[112:115], v[36:39]\n"
"v_exp_f32 v225, v225\n"
"v_mfma_f32_16x16x32_bf16 v[228:231], a[132:135], v[116:119], v[228:231]\n"
"ds_read_b64_tr_b16 a[254:255], v243 offset:62208\n"
"v_cvt_pk_bf16_f32 v18, v224, v225\n"
"v_mfma_f32_16x16x32_bf16 v[228:231], a[136:139], v[120:123], v[228:231]\n"
"v_exp_f32 v226, v226\n"
"v_mfma_f32_16x16x32_bf16 v[228:231], a[140:143], v[124:127], v[228:231]\n"
"v_add_f32 v52, v52, v221\n"
"v_add_f32 v52, v52, v222\n"
"v_mfma_f32_16x16x32_bf16 v[232:235], a[144:147], v[112:115], v[36:39]\n"
"v_exp_f32 v227, v227\n"
"v_mfma_f32_16x16x32_bf16 v[232:235], a[148:151], v[116:119], v[232:235]\n"
"s_add_u32 m0, s51, 8192\n"
"buffer_load_dwordx4 v[156:159], v252, s[64:67], s49 offen\n"
"v_cvt_pk_bf16_f32 v19, v226, v227\n"
"v_add_f32 v52, v52, v223\n"
"v_mfma_f32_16x16x32_bf16 v[232:235], a[152:155], v[120:123], v[232:235]\n"
"buffer_load_dwordx4 v239, s[24:27], s20 offen offset:0 lds\n"
"v_mfma_f32_16x16x32_bf16 v[232:235], a[156:159], v[124:127], v[232:235]\n"
"v_exp_f32 v228, v228\n"
"v_mfma_f32_16x16x32_bf16 a[64:67], a[192:195], v[16:19], a[64:67]\n"
"v_add_f32 v52, v52, v224\n"
"v_add_f32 v52, v52, v225\n"
"v_mfma_f32_16x16x32_bf16 a[68:71], a[196:199], v[16:19], a[68:71]\n"
"v_exp_f32 v229, v229\n"
"v_mfma_f32_16x16x32_bf16 a[72:75], a[200:203], v[16:19], a[72:75]\n"
"v_cvt_pk_bf16_f32 v20, v228, v229\n"
"v_add_f32 v52, v52, v226\n"
"v_mfma_f32_16x16x32_bf16 a[76:79], a[204:207], v[16:19], a[76:79]\n"
"s_waitcnt vmcnt(25) lgkmcnt(0)\n"
"v_exp_f32 v230, v230\n"
"v_mfma_f32_16x16x32_bf16 a[80:83], a[208:211], v[16:19], a[80:83]\n"
"s_barrier\n"
"v_mfma_f32_16x16x32_bf16 a[84:87], a[212:215], v[16:19], a[84:87]\n"
"v_exp_f32 v231, v231\n"
"v_mfma_f32_16x16x32_bf16 a[88:91], a[216:219], v[16:19], a[88:91]\n"
"v_cvt_pk_bf16_f32 v21, v230, v231\n"
"v_add_f32 v52, v52, v227\n"
"v_mfma_f32_16x16x32_bf16 a[92:95], a[220:223], v[16:19], a[92:95]\n"
"v_exp_f32 v232, v232\n"
"v_mfma_f32_16x16x32_bf16 v[0:3], a[160:163], v[64:67], v[24:27]\n"
"ds_read_b128 a[128:131], v241 offset:0\n"
"v_mfma_f32_16x16x32_bf16 v[0:3], a[164:167], v[68:71], v[0:3]\n"
"v_exp_f32 v233, v233\n"
"v_mfma_f32_16x16x32_bf16 v[0:3], a[168:171], v[72:75], v[0:3]\n"
"ds_read_b128 a[132:135], v241 offset:256\n"
"v_cvt_pk_bf16_f32 v22, v232, v233\n"
"v_mfma_f32_16x16x32_bf16 v[0:3], a[172:175], v[76:79], v[0:3]\n"
"v_exp_f32 v234, v234\n"
"v_mfma_f32_16x16x32_bf16 v[4:7], a[176:179], v[64:67], v[24:27]\n"
"ds_read_b128 a[136:139], v241 offset:512\n"
"v_mfma_f32_16x16x32_bf16 v[4:7], a[180:183], v[68:71], v[4:7]\n"
"v_exp_f32 v235, v235\n"
"v_mfma_f32_16x16x32_bf16 v[4:7], a[184:187], v[72:75], v[4:7]\n"
"ds_read_b128 a[140:143], v241 offset:768\n"
"v_cvt_pk_bf16_f32 v23, v234, v235\n"
"v_mfma_f32_16x16x32_bf16 v[4:7], a[188:191], v[76:79], v[4:7]\n"
"s_add_u32 m0, s53, 36864\n"
"v_exp_f32 v0, v0\n"
"v_mfma_f32_16x16x32_bf16 a[96:99], a[192:195], v[20:23], a[96:99]\n"
"buffer_load_dwordx4 v210, s[24:27], s21 offen offset:0 lds\n"
"v_mfma_f32_16x16x32_bf16 a[100:103], a[196:199], v[20:23], a[100:103]\n"
"v_exp_f32 v1, v1\n"
"v_mfma_f32_16x16x32_bf16 a[104:107], a[200:203], v[20:23], a[104:107]\n"
"ds_read_b128 a[144:147], v241 offset:4096\n"
"v_cvt_pk_bf16_f32 v16, v0, v1\n"
"v_mfma_f32_16x16x32_bf16 a[108:111], a[204:207], v[20:23], a[108:111]\n"
"v_exp_f32 v2, v2\n"
"v_mfma_f32_16x16x32_bf16 a[112:115], a[208:211], v[20:23], a[112:115]\n"
"ds_read_b128 a[148:151], v241 offset:4352\n"
"v_add_f32 v44, v44, v0\n"
"v_mfma_f32_16x16x32_bf16 a[116:119], a[212:215], v[20:23], a[116:119]\n"
"v_exp_f32 v3, v3\n"
"v_mfma_f32_16x16x32_bf16 a[120:123], a[216:219], v[20:23], a[120:123]\n"
"ds_read_b128 a[152:155], v241 offset:4608\n"
"v_cvt_pk_bf16_f32 v17, v2, v3\n"
"v_mfma_f32_16x16x32_bf16 a[124:127], a[220:223], v[20:23], a[124:127]\n"
"v_exp_f32 v4, v4\n"
"v_mfma_f32_16x16x32_bf16 v[56:59], v[40:43], v[20:23], v[56:59]\n"
"ds_read_b128 a[156:159], v241 offset:4864\n"
"v_add_f32 v44, v44, v1\n"
"v_mfma_f32_16x16x32_bf16 v[8:11], a[160:163], v[80:83], v[28:31]\n"
"v_exp_f32 v5, v5\n"
"v_mfma_f32_16x16x32_bf16 v[8:11], a[164:167], v[84:87], v[8:11]\n"
"ds_read_b64_tr_b16 a[192:193], v244 offset:16384\n"
"buffer_load_dwordx4 v[192:195], v252, s[68:71], s49 offen\n"
"v_cvt_pk_bf16_f32 v18, v4, v5\n"
"v_mfma_f32_16x16x32_bf16 v[8:11], a[168:171], v[88:91], v[8:11]\n"
"s_add_u32 s49, s49, 285696\n"
"v_exp_f32 v6, v6\n"
"v_mfma_f32_16x16x32_bf16 v[8:11], a[172:175], v[92:95], v[8:11]\n"
"ds_read_b64_tr_b16 a[194:195], v244 offset:20480\n"
"v_add_f32 v44, v44, v2\n"
"v_mfma_f32_16x16x32_bf16 v[12:15], a[176:179], v[80:83], v[28:31]\n"
"v_exp_f32 v7, v7\n"
"v_mfma_f32_16x16x32_bf16 v[12:15], a[180:183], v[84:87], v[12:15]\n"
"s_add_u32 m0, s53, 36864\n"
"ds_read_b64_tr_b16 a[196:197], v245 offset:16384\n"
"v_cvt_pk_bf16_f32 v19, v6, v7\n"
"v_mfma_f32_16x16x32_bf16 v[12:15], a[184:187], v[88:91], v[12:15]\n"
"buffer_load_dwordx4 v211, s[24:27], s21 offen offset:1024 lds\n"
"v_mfma_f32_16x16x32_bf16 v[12:15], a[188:191], v[92:95], v[12:15]\n"
"s_waitcnt lgkmcnt(11)\n"
"v_exp_f32 v8, v8\n"
"v_mfma_f32_16x16x32_bf16 a[0:3], a[224:227], v[16:19], a[0:3]\n"
"ds_read_b64_tr_b16 a[198:199], v245 offset:20480\n"
"v_add_f32 v44, v44, v3\n"
"v_mfma_f32_16x16x32_bf16 a[4:7], a[228:231], v[16:19], a[4:7]\n"
"v_exp_f32 v9, v9\n"
"v_mfma_f32_16x16x32_bf16 a[8:11], a[232:235], v[16:19], a[8:11]\n"
"ds_read_b64_tr_b16 a[200:201], v244 offset:16640\n"
"v_cvt_pk_bf16_f32 v20, v8, v9\n"
"v_mfma_f32_16x16x32_bf16 a[12:15], a[236:239], v[16:19], a[12:15]\n"
"v_exp_f32 v10, v10\n"
"v_mfma_f32_16x16x32_bf16 a[16:19], a[240:243], v[16:19], a[16:19]\n"
"ds_read_b64_tr_b16 a[202:203], v244 offset:20736\n"
"v_add_f32 v44, v44, v4\n"
"v_mfma_f32_16x16x32_bf16 a[20:23], a[244:247], v[16:19], a[20:23]\n"
"v_exp_f32 v11, v11\n"
"v_mfma_f32_16x16x32_bf16 a[24:27], a[248:251], v[16:19], a[24:27]\n"
"ds_read_b64_tr_b16 a[204:205], v245 offset:16640\n"
"v_cvt_pk_bf16_f32 v21, v10, v11\n"
"v_mfma_f32_16x16x32_bf16 a[28:31], a[252:255], v[16:19], a[28:31]\n"
"v_exp_f32 v12, v12\n"
"v_mfma_f32_16x16x32_bf16 v[220:223], a[160:163], v[96:99], v[32:35]\n"
"ds_read_b64_tr_b16 a[206:207], v245 offset:20736\n"
"v_add_f32 v44, v44, v5\n"
"v_mfma_f32_16x16x32_bf16 v[220:223], a[164:167], v[100:103], v[220:223]\n"
"v_exp_f32 v13, v13\n"
"v_mfma_f32_16x16x32_bf16 v[220:223], a[168:171], v[104:107], v[220:223]\n"
"ds_read_b64_tr_b16 a[208:209], v244 offset:16896\n"
"v_cvt_pk_bf16_f32 v22, v12, v13\n"
"v_mfma_f32_16x16x32_bf16 v[220:223], a[172:175], v[108:111], v[220:223]\n"
"v_exp_f32 v14, v14\n"
"v_mfma_f32_16x16x32_bf16 v[224:227], a[176:179], v[96:99], v[32:35]\n"
"ds_read_b64_tr_b16 a[210:211], v244 offset:20992\n"
"v_add_f32 v44, v44, v6\n"
"v_mfma_f32_16x16x32_bf16 v[224:227], a[180:183], v[100:103], v[224:227]\n"
"v_exp_f32 v15, v15\n"
"v_mfma_f32_16x16x32_bf16 v[224:227], a[184:187], v[104:107], v[224:227]\n"
"ds_read_b64_tr_b16 a[212:213], v245 offset:16896\n"
"v_cvt_pk_bf16_f32 v23, v14, v15\n"
"v_mfma_f32_16x16x32_bf16 v[224:227], a[188:191], v[108:111], v[224:227]\n"
"s_add_u32 m0, s53, 36864\n"
"v_exp_f32 v220, v220\n"
"v_mfma_f32_16x16x32_bf16 a[32:35], a[224:227], v[20:23], a[32:35]\n"
"buffer_load_dwordx4 v212, s[24:27], s21 offen offset:2048 lds\n"
"v_mfma_f32_16x16x32_bf16 a[36:39], a[228:231], v[20:23], a[36:39]\n"
"v_exp_f32 v221, v221\n"
"v_mfma_f32_16x16x32_bf16 a[40:43], a[232:235], v[20:23], a[40:43]\n"
"ds_read_b64_tr_b16 a[214:215], v245 offset:20992\n"
"v_cvt_pk_bf16_f32 v16, v220, v221\n"
"v_mfma_f32_16x16x32_bf16 a[44:47], a[236:239], v[20:23], a[44:47]\n"
"v_exp_f32 v222, v222\n"
"v_mfma_f32_16x16x32_bf16 a[48:51], a[240:243], v[20:23], a[48:51]\n"
"ds_read_b64_tr_b16 a[216:217], v244 offset:17152\n"
"v_add_f32 v44, v44, v7\n"
"v_mfma_f32_16x16x32_bf16 a[52:55], a[244:247], v[20:23], a[52:55]\n"
"v_exp_f32 v223, v223\n"
"v_mfma_f32_16x16x32_bf16 a[56:59], a[248:251], v[20:23], a[56:59]\n"
"ds_read_b64_tr_b16 a[218:219], v244 offset:21248\n"
"buffer_load_dwordx4 v[160:163], v251, s[64:67], s49 offen\n"
"v_cvt_pk_bf16_f32 v17, v222, v223\n"
"v_mfma_f32_16x16x32_bf16 a[60:63], a[252:255], v[20:23], a[60:63]\n"
"v_exp_f32 v224, v224\n"
"v_mfma_f32_16x16x32_bf16 v[48:51], v[40:43], v[20:23], v[48:51]\n"
"ds_read_b64_tr_b16 a[220:221], v245 offset:17152\n"
"v_add_f32 v52, v52, v220\n"
"v_mfma_f32_16x16x32_bf16 v[228:231], a[160:163], v[112:115], v[36:39]\n"
"v_exp_f32 v225, v225\n"
"v_mfma_f32_16x16x32_bf16 v[228:231], a[164:167], v[116:119], v[228:231]\n"
"ds_read_b64_tr_b16 a[222:223], v245 offset:21248\n"
"v_cvt_pk_bf16_f32 v18, v224, v225\n"
"v_mfma_f32_16x16x32_bf16 v[228:231], a[168:171], v[120:123], v[228:231]\n"
"v_exp_f32 v226, v226\n"
"v_mfma_f32_16x16x32_bf16 v[228:231], a[172:175], v[124:127], v[228:231]\n"
"v_add_f32 v52, v52, v221\n"
"v_add_f32 v52, v52, v222\n"
"v_mfma_f32_16x16x32_bf16 v[232:235], a[176:179], v[112:115], v[36:39]\n"
"v_exp_f32 v227, v227\n"
"v_mfma_f32_16x16x32_bf16 v[232:235], a[180:183], v[116:119], v[232:235]\n"
"s_add_u32 m0, s53, 36864\n"
"v_cvt_pk_bf16_f32 v19, v226, v227\n"
"v_add_f32 v52, v52, v223\n"
"v_mfma_f32_16x16x32_bf16 v[232:235], a[184:187], v[120:123], v[232:235]\n"
"buffer_load_dwordx4 v213, s[24:27], s21 offen offset:3072 lds\n"
"v_mfma_f32_16x16x32_bf16 v[232:235], a[188:191], v[124:127], v[232:235]\n"
"v_exp_f32 v228, v228\n"
"v_mfma_f32_16x16x32_bf16 a[64:67], a[224:227], v[16:19], a[64:67]\n"
"v_add_f32 v52, v52, v224\n"
"v_add_f32 v52, v52, v225\n"
"v_mfma_f32_16x16x32_bf16 a[68:71], a[228:231], v[16:19], a[68:71]\n"
"v_exp_f32 v229, v229\n"
"v_mfma_f32_16x16x32_bf16 a[72:75], a[232:235], v[16:19], a[72:75]\n"
"v_cvt_pk_bf16_f32 v20, v228, v229\n"
"v_add_f32 v52, v52, v226\n"
"v_mfma_f32_16x16x32_bf16 a[76:79], a[236:239], v[16:19], a[76:79]\n"
"v_exp_f32 v230, v230\n"
"v_mfma_f32_16x16x32_bf16 a[80:83], a[240:243], v[16:19], a[80:83]\n"
"s_waitcnt lgkmcnt(8)\n"
"v_mfma_f32_16x16x32_bf16 a[84:87], a[244:247], v[16:19], a[84:87]\n"
"v_exp_f32 v231, v231\n"
"v_mfma_f32_16x16x32_bf16 a[88:91], a[248:251], v[16:19], a[88:91]\n"
"v_cvt_pk_bf16_f32 v21, v230, v231\n"
"v_add_f32 v52, v52, v227\n"
"v_mfma_f32_16x16x32_bf16 a[92:95], a[252:255], v[16:19], a[92:95]\n"
"v_exp_f32 v232, v232\n"
"v_mfma_f32_16x16x32_bf16 v[0:3], a[128:131], v[64:67], v[24:27]\n"
"ds_read_b128 a[160:163], v241 offset:8192\n"
"v_mfma_f32_16x16x32_bf16 v[0:3], a[132:135], v[68:71], v[0:3]\n"
"v_exp_f32 v233, v233\n"
"v_mfma_f32_16x16x32_bf16 v[0:3], a[136:139], v[72:75], v[0:3]\n"
"ds_read_b128 a[164:167], v241 offset:8448\n"
"v_cvt_pk_bf16_f32 v22, v232, v233\n"
"v_mfma_f32_16x16x32_bf16 v[0:3], a[140:143], v[76:79], v[0:3]\n"
"v_exp_f32 v234, v234\n"
"v_mfma_f32_16x16x32_bf16 v[4:7], a[144:147], v[64:67], v[24:27]\n"
"ds_read_b128 a[168:171], v241 offset:8704\n"
"v_mfma_f32_16x16x32_bf16 v[4:7], a[148:151], v[68:71], v[4:7]\n"
"v_exp_f32 v235, v235\n"
"v_mfma_f32_16x16x32_bf16 v[4:7], a[152:155], v[72:75], v[4:7]\n"
"ds_read_b128 a[172:175], v241 offset:8960\n"
"v_cvt_pk_bf16_f32 v23, v234, v235\n"
"v_mfma_f32_16x16x32_bf16 v[4:7], a[156:159], v[76:79], v[4:7]\n"
"s_add_u32 m0, s53, 40960\n"
"v_exp_f32 v0, v0\n"
"v_mfma_f32_16x16x32_bf16 a[96:99], a[224:227], v[20:23], a[96:99]\n"
"buffer_load_dwordx4 v214, s[24:27], s21 offen offset:0 lds\n"
"v_mfma_f32_16x16x32_bf16 a[100:103], a[228:231], v[20:23], a[100:103]\n"
"v_exp_f32 v1, v1\n"
"v_mfma_f32_16x16x32_bf16 a[104:107], a[232:235], v[20:23], a[104:107]\n"
"ds_read_b128 a[176:179], v241 offset:12288\n"
"buffer_load_dwordx4 v[196:199], v251, s[68:71], s49 offen\n"
"v_cvt_pk_bf16_f32 v16, v0, v1\n"
"v_mfma_f32_16x16x32_bf16 a[108:111], a[236:239], v[20:23], a[108:111]\n"
"s_lshl_b32 s62, s41, 6\n"
"v_exp_f32 v2, v2\n"
"v_mfma_f32_16x16x32_bf16 a[112:115], a[240:243], v[20:23], a[112:115]\n"
"ds_read_b128 a[180:183], v241 offset:12544\n"
"v_add_f32 v44, v44, v0\n"
"v_mfma_f32_16x16x32_bf16 a[116:119], a[244:247], v[20:23], a[116:119]\n"
"v_exp_f32 v3, v3\n"
"v_mfma_f32_16x16x32_bf16 a[120:123], a[248:251], v[20:23], a[120:123]\n"
"ds_read_b128 a[184:187], v241 offset:12800\n"
"v_cvt_pk_bf16_f32 v17, v2, v3\n"
"v_mfma_f32_16x16x32_bf16 a[124:127], a[252:255], v[20:23], a[124:127]\n"
"v_exp_f32 v4, v4\n"
"v_mfma_f32_16x16x32_bf16 v[56:59], v[40:43], v[20:23], v[56:59]\n"
"ds_read_b128 a[188:191], v241 offset:13056\n"
"v_add_f32 v44, v44, v1\n"
"v_mfma_f32_16x16x32_bf16 v[8:11], a[128:131], v[80:83], v[28:31]\n"
"v_exp_f32 v5, v5\n"
"v_mfma_f32_16x16x32_bf16 v[8:11], a[132:135], v[84:87], v[8:11]\n"
"ds_read_b64_tr_b16 a[224:225], v244 offset:24576\n"
"v_cvt_pk_bf16_f32 v18, v4, v5\n"
"v_add_u32 v253, s62, v248\n"
"v_mfma_f32_16x16x32_bf16 v[8:11], a[136:139], v[88:91], v[8:11]\n"
"v_exp_f32 v6, v6\n"
"v_mfma_f32_16x16x32_bf16 v[8:11], a[140:143], v[92:95], v[8:11]\n"
"ds_read_b64_tr_b16 a[226:227], v244 offset:28672\n"
"v_add_f32 v44, v44, v2\n"
"v_mfma_f32_16x16x32_bf16 v[12:15], a[144:147], v[80:83], v[28:31]\n"
"v_exp_f32 v7, v7\n"
"v_mfma_f32_16x16x32_bf16 v[12:15], a[148:151], v[84:87], v[12:15]\n"
"s_add_u32 m0, s53, 40960\n"
"ds_read_b64_tr_b16 a[228:229], v245 offset:24576\n"
"v_cvt_pk_bf16_f32 v19, v6, v7\n"
"v_mfma_f32_16x16x32_bf16 v[12:15], a[152:155], v[88:91], v[12:15]\n"
"buffer_load_dwordx4 v236, s[24:27], s21 offen offset:1024 lds\n"
"v_mov_b32 v255, 0x10000000\n"
"v_mfma_f32_16x16x32_bf16 v[12:15], a[156:159], v[92:95], v[12:15]\n"
"s_waitcnt lgkmcnt(11)\n"
"v_exp_f32 v8, v8\n"
"v_mfma_f32_16x16x32_bf16 a[0:3], a[192:195], v[16:19], a[0:3]\n"
"ds_read_b64_tr_b16 a[230:231], v245 offset:28672\n"
"v_add_f32 v44, v44, v3\n"
"v_mfma_f32_16x16x32_bf16 a[4:7], a[196:199], v[16:19], a[4:7]\n"
"v_exp_f32 v9, v9\n"
"v_mfma_f32_16x16x32_bf16 a[8:11], a[200:203], v[16:19], a[8:11]\n"
"ds_read_b64_tr_b16 a[232:233], v244 offset:24832\n"
"v_cvt_pk_bf16_f32 v20, v8, v9\n"
"v_mfma_f32_16x16x32_bf16 a[12:15], a[204:207], v[16:19], a[12:15]\n"
"v_exp_f32 v10, v10\n"
"v_mfma_f32_16x16x32_bf16 a[16:19], a[208:211], v[16:19], a[16:19]\n"
"ds_read_b64_tr_b16 a[234:235], v244 offset:28928\n"
"v_add_f32 v44, v44, v4\n"
"v_mfma_f32_16x16x32_bf16 a[20:23], a[212:215], v[16:19], a[20:23]\n"
"s_add_u32 m0, s53, 40960\n"
"v_exp_f32 v11, v11\n"
"v_cmp_gt_u32 vcc, 144, v253\n"
"v_mfma_f32_16x16x32_bf16 a[24:27], a[216:219], v[16:19], a[24:27]\n"
"buffer_load_dwordx4 v237, s[24:27], s21 offen offset:2048 lds\n"
"ds_read_b64_tr_b16 a[236:237], v245 offset:24832\n"
"v_cvt_pk_bf16_f32 v21, v10, v11\n"
"v_mfma_f32_16x16x32_bf16 a[28:31], a[220:223], v[16:19], a[28:31]\n"
"v_exp_f32 v12, v12\n"
"v_mfma_f32_16x16x32_bf16 v[220:223], a[128:131], v[96:99], v[32:35]\n"
"ds_read_b64_tr_b16 a[238:239], v245 offset:28928\n"
"v_add_f32 v44, v44, v5\n"
"v_mfma_f32_16x16x32_bf16 v[220:223], a[132:135], v[100:103], v[220:223]\n"
"v_exp_f32 v13, v13\n"
"v_mfma_f32_16x16x32_bf16 v[220:223], a[136:139], v[104:107], v[220:223]\n"
"ds_read_b64_tr_b16 a[240:241], v244 offset:25088\n"
"v_cvt_pk_bf16_f32 v22, v12, v13\n"
"v_mfma_f32_16x16x32_bf16 v[220:223], a[140:143], v[108:111], v[220:223]\n"
"v_exp_f32 v14, v14\n"
"v_mfma_f32_16x16x32_bf16 v[224:227], a[144:147], v[96:99], v[32:35]\n"
"ds_read_b64_tr_b16 a[242:243], v244 offset:29184\n"
"v_add_f32 v44, v44, v6\n"
"v_lshlrev_b32 v254, 3, v253\n"
"v_mfma_f32_16x16x32_bf16 v[224:227], a[148:151], v[100:103], v[224:227]\n"
"s_nop 0\n"
"v_exp_f32 v15, v15\n"
"v_mfma_f32_16x16x32_bf16 v[224:227], a[152:155], v[104:107], v[224:227]\n"
"ds_read_b64_tr_b16 a[244:245], v245 offset:25088\n"
"v_cvt_pk_bf16_f32 v23, v14, v15\n"
"v_mfma_f32_16x16x32_bf16 v[224:227], a[156:159], v[108:111], v[224:227]\n"
"s_add_u32 m0, s53, 40960\n"
"v_exp_f32 v220, v220\n"
"v_mfma_f32_16x16x32_bf16 a[32:35], a[192:195], v[20:23], a[32:35]\n"
"buffer_load_dwordx4 v238, s[24:27], s21 offen offset:3072 lds\n"
"v_cndmask_b32 v254, v255, v254, vcc\n"
"v_mfma_f32_16x16x32_bf16 a[36:39], a[196:199], v[20:23], a[36:39]\n"
"v_exp_f32 v221, v221\n"
"v_mfma_f32_16x16x32_bf16 a[40:43], a[200:203], v[20:23], a[40:43]\n"
"ds_read_b64_tr_b16 a[246:247], v245 offset:29184\n"
"v_cvt_pk_bf16_f32 v16, v220, v221\n"
"v_mfma_f32_16x16x32_bf16 a[44:47], a[204:207], v[20:23], a[44:47]\n"
"v_exp_f32 v222, v222\n"
"v_mfma_f32_16x16x32_bf16 a[48:51], a[208:211], v[20:23], a[48:51]\n"
"ds_read_b64_tr_b16 a[248:249], v244 offset:25344\n"
"v_add_f32 v44, v44, v7\n"
"v_mfma_f32_16x16x32_bf16 a[52:55], a[212:215], v[20:23], a[52:55]\n"
"v_exp_f32 v223, v223\n"
"v_mfma_f32_16x16x32_bf16 a[56:59], a[216:219], v[20:23], a[56:59]\n"
"ds_read_b64_tr_b16 a[250:251], v244 offset:29440\n"
"v_cvt_pk_bf16_f32 v17, v222, v223\n"
"v_mfma_f32_16x16x32_bf16 a[60:63], a[220:223], v[20:23], a[60:63]\n"
"v_exp_f32 v224, v224\n"
"v_lshlrev_b32 v216, 6, v248\n"
"v_mfma_f32_16x16x32_bf16 v[48:51], v[40:43], v[20:23], v[48:51]\n"
"ds_read_b64_tr_b16 a[252:253], v245 offset:25344\n"
"v_add_f32 v52, v52, v220\n"
"v_mfma_f32_16x16x32_bf16 v[228:231], a[128:131], v[112:115], v[36:39]\n"
"v_exp_f32 v225, v225\n"
"v_mfma_f32_16x16x32_bf16 v[228:231], a[132:135], v[116:119], v[228:231]\n"
"ds_read_b64_tr_b16 a[254:255], v245 offset:29440\n"
"v_cvt_pk_bf16_f32 v18, v224, v225\n"
"v_mfma_f32_16x16x32_bf16 v[228:231], a[136:139], v[120:123], v[228:231]\n"
"v_exp_f32 v226, v226\n"
"v_mfma_f32_16x16x32_bf16 v[228:231], a[140:143], v[124:127], v[228:231]\n"
"v_add_f32 v52, v52, v221\n"
"v_add_f32 v52, v52, v222\n"
"v_mfma_f32_16x16x32_bf16 v[232:235], a[144:147], v[112:115], v[36:39]\n"
"v_exp_f32 v227, v227\n"
"v_mfma_f32_16x16x32_bf16 v[232:235], a[148:151], v[116:119], v[232:235]\n"
"s_add_u32 m0, s53, 45056\n"
"v_cvt_pk_bf16_f32 v19, v226, v227\n"
"v_add_f32 v52, v52, v223\n"
"v_lshrrev_b32 v215, 3, v248\n"
"v_mfma_f32_16x16x32_bf16 v[232:235], a[152:155], v[120:123], v[232:235]\n"
"buffer_load_dwordx4 v239, s[24:27], s21 offen offset:0 lds\n"
"v_mul_u32_u24 v215, 1152, v215\n"
"v_mfma_f32_16x16x32_bf16 v[232:235], a[156:159], v[124:127], v[232:235]\n"
"v_exp_f32 v228, v228\n"
"v_mfma_f32_16x16x32_bf16 a[64:67], a[192:195], v[16:19], a[64:67]\n"
"v_add_f32 v52, v52, v224\n"
"v_add_f32 v52, v52, v225\n"
"v_mfma_f32_16x16x32_bf16 a[68:71], a[196:199], v[16:19], a[68:71]\n"
"v_exp_f32 v229, v229\n"
"v_mfma_f32_16x16x32_bf16 a[72:75], a[200:203], v[16:19], a[72:75]\n"
"v_cvt_pk_bf16_f32 v20, v228, v229\n"
"v_add_f32 v52, v52, v226\n"
"v_mfma_f32_16x16x32_bf16 a[76:79], a[204:207], v[16:19], a[76:79]\n"
"s_waitcnt vmcnt(26) lgkmcnt(0)\n"
"v_exp_f32 v230, v230\n"
"v_mfma_f32_16x16x32_bf16 a[80:83], a[208:211], v[16:19], a[80:83]\n"
"s_barrier\n"
"v_mfma_f32_16x16x32_bf16 a[84:87], a[212:215], v[16:19], a[84:87]\n"
"v_exp_f32 v231, v231\n"
"v_mfma_f32_16x16x32_bf16 a[88:91], a[216:219], v[16:19], a[88:91]\n"
"v_cvt_pk_bf16_f32 v21, v230, v231\n"
"v_add_f32 v52, v52, v227\n"
"v_and_b32 v217, 7, v248\n"
"v_mfma_f32_16x16x32_bf16 a[92:95], a[220:223], v[16:19], a[92:95]\n"
"v_exp_f32 v232, v232\n"
"v_mfma_f32_16x16x32_bf16 v[0:3], a[160:163], v[64:67], v[24:27]\n"
"ds_read_b128 a[128:131], v241 offset:32768\n"
"v_lshl_add_u32 v215, v217, 7, v215\n"
"v_mfma_f32_16x16x32_bf16 v[0:3], a[164:167], v[68:71], v[0:3]\n"
"s_and_b32 s46, s44, 7\n"
"v_exp_f32 v233, v233\n"
"v_mfma_f32_16x16x32_bf16 v[0:3], a[168:171], v[72:75], v[0:3]\n"
"s_lshl_b32 s46, s46, 2\n"
"ds_read_b128 a[132:135], v241 offset:33024\n"
"v_cvt_pk_bf16_f32 v22, v232, v233\n"
"v_mfma_f32_16x16x32_bf16 v[0:3], a[172:175], v[76:79], v[0:3]\n"
"s_add_u32 s46, s46, s41\n"
"v_exp_f32 v234, v234\n"
"v_mfma_f32_16x16x32_bf16 v[4:7], a[176:179], v[64:67], v[24:27]\n"
"s_mul_i32 s46, s46, 9216\n"
"ds_read_b128 a[136:139], v241 offset:33280\n"
"v_mfma_f32_16x16x32_bf16 v[4:7], a[180:183], v[68:71], v[4:7]\n"
"s_mul_i32 s62, s43, 294912\n"
"v_exp_f32 v235, v235\n"
"v_mfma_f32_16x16x32_bf16 v[4:7], a[184:187], v[72:75], v[4:7]\n"
"s_add_u32 s46, s46, s62\n"
"ds_read_b128 a[140:143], v241 offset:33536\n"
"v_cvt_pk_bf16_f32 v23, v234, v235\n"
"v_mfma_f32_16x16x32_bf16 v[4:7], a[188:191], v[76:79], v[4:7]\n"
"s_add_u32 m0, s53, 73728\n"
"v_exp_f32 v0, v0\n"
"v_mfma_f32_16x16x32_bf16 a[96:99], a[192:195], v[20:23], a[96:99]\n"
"buffer_load_dwordx4 v210, s[24:27], s22 offen offset:0 lds\n"
"v_mfma_f32_16x16x32_bf16 a[100:103], a[196:199], v[20:23], a[100:103]\n"
"v_exp_f32 v1, v1\n"
"v_mfma_f32_16x16x32_bf16 a[104:107], a[200:203], v[20:23], a[104:107]\n"
"ds_read_b128 a[144:147], v241 offset:36864\n"
"buffer_load_dword v250, v215, s[24:27], s46 offen offset:128\n"
"v_cvt_pk_bf16_f32 v16, v0, v1\n"
"v_mfma_f32_16x16x32_bf16 a[108:111], a[204:207], v[20:23], a[108:111]\n"
"v_exp_f32 v2, v2\n"
"v_mfma_f32_16x16x32_bf16 a[112:115], a[208:211], v[20:23], a[112:115]\n"
"ds_read_b128 a[148:151], v241 offset:37120\n"
"v_add_f32 v44, v44, v0\n"
"v_mfma_f32_16x16x32_bf16 a[116:119], a[212:215], v[20:23], a[116:119]\n"
"v_exp_f32 v3, v3\n"
"v_mfma_f32_16x16x32_bf16 a[120:123], a[216:219], v[20:23], a[120:123]\n"
"ds_read_b128 a[152:155], v241 offset:37376\n"
"v_cvt_pk_bf16_f32 v17, v2, v3\n"
"v_mfma_f32_16x16x32_bf16 a[124:127], a[220:223], v[20:23], a[124:127]\n"
"v_exp_f32 v4, v4\n"
"v_mfma_f32_16x16x32_bf16 v[56:59], v[40:43], v[20:23], v[56:59]\n"
"ds_read_b128 a[156:159], v241 offset:37632\n"
"v_add_f32 v44, v44, v1\n"
"v_mfma_f32_16x16x32_bf16 v[8:11], a[160:163], v[80:83], v[28:31]\n"
"v_exp_f32 v5, v5\n"
"v_mfma_f32_16x16x32_bf16 v[8:11], a[164:167], v[84:87], v[8:11]\n"
"ds_read_b64_tr_b16 a[192:193], v244 offset:49152\n"
"v_cvt_pk_bf16_f32 v18, v4, v5\n"
"v_mfma_f32_16x16x32_bf16 v[8:11], a[168:171], v[88:91], v[8:11]\n"
"v_exp_f32 v6, v6\n"
"v_mfma_f32_16x16x32_bf16 v[8:11], a[172:175], v[92:95], v[8:11]\n"
"ds_read_b64_tr_b16 a[194:195], v244 offset:53248\n"
"v_add_f32 v44, v44, v2\n"
"v_mfma_f32_16x16x32_bf16 v[12:15], a[176:179], v[80:83], v[28:31]\n"
"v_exp_f32 v7, v7\n"
"v_mfma_f32_16x16x32_bf16 v[12:15], a[180:183], v[84:87], v[12:15]\n"
"s_add_u32 m0, s53, 73728\n"
"ds_read_b64_tr_b16 a[196:197], v245 offset:49152\n"
"v_cvt_pk_bf16_f32 v19, v6, v7\n"
"v_mfma_f32_16x16x32_bf16 v[12:15], a[184:187], v[88:91], v[12:15]\n"
"buffer_load_dwordx4 v211, s[24:27], s22 offen offset:1024 lds\n"
"v_mfma_f32_16x16x32_bf16 v[12:15], a[188:191], v[92:95], v[12:15]\n"
"s_waitcnt lgkmcnt(11)\n"
"v_exp_f32 v8, v8\n"
"v_mfma_f32_16x16x32_bf16 a[0:3], a[224:227], v[16:19], a[0:3]\n"
"ds_read_b64_tr_b16 a[198:199], v245 offset:53248\n"
"v_add_f32 v44, v44, v3\n"
"v_mfma_f32_16x16x32_bf16 a[4:7], a[228:231], v[16:19], a[4:7]\n"
"v_exp_f32 v9, v9\n"
"v_mfma_f32_16x16x32_bf16 a[8:11], a[232:235], v[16:19], a[8:11]\n"
"ds_read_b64_tr_b16 a[200:201], v244 offset:49408\n"
"v_cvt_pk_bf16_f32 v20, v8, v9\n"
"v_mfma_f32_16x16x32_bf16 a[12:15], a[236:239], v[16:19], a[12:15]\n"
"v_exp_f32 v10, v10\n"
"v_mfma_f32_16x16x32_bf16 a[16:19], a[240:243], v[16:19], a[16:19]\n"
"ds_read_b64_tr_b16 a[202:203], v244 offset:53504\n"
"v_add_f32 v44, v44, v4\n"
"v_mfma_f32_16x16x32_bf16 a[20:23], a[244:247], v[16:19], a[20:23]\n"
"v_exp_f32 v11, v11\n"
"v_mfma_f32_16x16x32_bf16 a[24:27], a[248:251], v[16:19], a[24:27]\n"
"ds_read_b64_tr_b16 a[204:205], v245 offset:49408\n"
"v_cvt_pk_bf16_f32 v21, v10, v11\n"
"v_mfma_f32_16x16x32_bf16 a[28:31], a[252:255], v[16:19], a[28:31]\n"
"v_exp_f32 v12, v12\n"
"v_mfma_f32_16x16x32_bf16 v[220:223], a[160:163], v[96:99], v[32:35]\n"
"ds_read_b64_tr_b16 a[206:207], v245 offset:53504\n"
"v_add_f32 v44, v44, v5\n"
"v_mfma_f32_16x16x32_bf16 v[220:223], a[164:167], v[100:103], v[220:223]\n"
"v_exp_f32 v13, v13\n"
"v_mfma_f32_16x16x32_bf16 v[220:223], a[168:171], v[104:107], v[220:223]\n"
"ds_read_b64_tr_b16 a[208:209], v244 offset:49664\n"
"v_cvt_pk_bf16_f32 v22, v12, v13\n"
"v_mfma_f32_16x16x32_bf16 v[220:223], a[172:175], v[108:111], v[220:223]\n"
"v_exp_f32 v14, v14\n"
"v_mfma_f32_16x16x32_bf16 v[224:227], a[176:179], v[96:99], v[32:35]\n"
"ds_read_b64_tr_b16 a[210:211], v244 offset:53760\n"
"v_add_f32 v44, v44, v6\n"
"v_mfma_f32_16x16x32_bf16 v[224:227], a[180:183], v[100:103], v[224:227]\n"
"v_exp_f32 v15, v15\n"
"v_mfma_f32_16x16x32_bf16 v[224:227], a[184:187], v[104:107], v[224:227]\n"
"ds_read_b64_tr_b16 a[212:213], v245 offset:49664\n"
"buffer_load_dword v250, v215, s[24:27], s46 offen offset:192\n"
"v_cvt_pk_bf16_f32 v23, v14, v15\n"
"v_mfma_f32_16x16x32_bf16 v[224:227], a[188:191], v[108:111], v[224:227]\n"
"s_add_u32 m0, s53, 73728\n"
"v_exp_f32 v220, v220\n"
"v_mfma_f32_16x16x32_bf16 a[32:35], a[224:227], v[20:23], a[32:35]\n"
"buffer_load_dwordx4 v212, s[24:27], s22 offen offset:2048 lds\n"
"v_mfma_f32_16x16x32_bf16 a[36:39], a[228:231], v[20:23], a[36:39]\n"
"v_exp_f32 v221, v221\n"
"v_mfma_f32_16x16x32_bf16 a[40:43], a[232:235], v[20:23], a[40:43]\n"
"ds_read_b64_tr_b16 a[214:215], v245 offset:53760\n"
"v_cvt_pk_bf16_f32 v16, v220, v221\n"
"v_mfma_f32_16x16x32_bf16 a[44:47], a[236:239], v[20:23], a[44:47]\n"
"v_exp_f32 v222, v222\n"
"v_mfma_f32_16x16x32_bf16 a[48:51], a[240:243], v[20:23], a[48:51]\n"
"ds_read_b64_tr_b16 a[216:217], v244 offset:49920\n"
"v_add_f32 v44, v44, v7\n"
"v_mfma_f32_16x16x32_bf16 a[52:55], a[244:247], v[20:23], a[52:55]\n"
"v_exp_f32 v223, v223\n"
"v_mfma_f32_16x16x32_bf16 a[56:59], a[248:251], v[20:23], a[56:59]\n"
"ds_read_b64_tr_b16 a[218:219], v244 offset:54016\n"
"v_cvt_pk_bf16_f32 v17, v222, v223\n"
"v_mfma_f32_16x16x32_bf16 a[60:63], a[252:255], v[20:23], a[60:63]\n"
"v_exp_f32 v224, v224\n"
"v_mfma_f32_16x16x32_bf16 v[48:51], v[40:43], v[20:23], v[48:51]\n"
"ds_read_b64_tr_b16 a[220:221], v245 offset:49920\n"
"v_add_f32 v52, v52, v220\n"
"v_mfma_f32_16x16x32_bf16 v[228:231], a[160:163], v[112:115], v[36:39]\n"
"v_exp_f32 v225, v225\n"
"v_mfma_f32_16x16x32_bf16 v[228:231], a[164:167], v[116:119], v[228:231]\n"
"ds_read_b64_tr_b16 a[222:223], v245 offset:54016\n"
"v_cvt_pk_bf16_f32 v18, v224, v225\n"
"v_mfma_f32_16x16x32_bf16 v[228:231], a[168:171], v[120:123], v[228:231]\n"
"v_exp_f32 v226, v226\n"
"v_mfma_f32_16x16x32_bf16 v[228:231], a[172:175], v[124:127], v[228:231]\n"
"v_add_f32 v52, v52, v221\n"
"v_add_f32 v52, v52, v222\n"
"v_mfma_f32_16x16x32_bf16 v[232:235], a[176:179], v[112:115], v[36:39]\n"
"v_exp_f32 v227, v227\n"
"v_mfma_f32_16x16x32_bf16 v[232:235], a[180:183], v[116:119], v[232:235]\n"
"s_add_u32 m0, s53, 73728\n"
"v_cvt_pk_bf16_f32 v19, v226, v227\n"
"v_add_f32 v52, v52, v223\n"
"v_mfma_f32_16x16x32_bf16 v[232:235], a[184:187], v[120:123], v[232:235]\n"
"buffer_load_dwordx4 v213, s[24:27], s22 offen offset:3072 lds\n"
"v_mfma_f32_16x16x32_bf16 v[232:235], a[188:191], v[124:127], v[232:235]\n"
"v_exp_f32 v228, v228\n"
"v_mfma_f32_16x16x32_bf16 a[64:67], a[224:227], v[16:19], a[64:67]\n"
"v_add_f32 v52, v52, v224\n"
"v_add_f32 v52, v52, v225\n"
"v_mfma_f32_16x16x32_bf16 a[68:71], a[228:231], v[16:19], a[68:71]\n"
"v_exp_f32 v229, v229\n"
"v_mfma_f32_16x16x32_bf16 a[72:75], a[232:235], v[16:19], a[72:75]\n"
"v_cvt_pk_bf16_f32 v20, v228, v229\n"
"v_add_f32 v52, v52, v226\n"
"v_mfma_f32_16x16x32_bf16 a[76:79], a[236:239], v[16:19], a[76:79]\n"
"v_exp_f32 v230, v230\n"
"v_mfma_f32_16x16x32_bf16 a[80:83], a[240:243], v[16:19], a[80:83]\n"
"s_waitcnt lgkmcnt(8)\n"
"v_mfma_f32_16x16x32_bf16 a[84:87], a[244:247], v[16:19], a[84:87]\n"
"v_exp_f32 v231, v231\n"
"v_mfma_f32_16x16x32_bf16 a[88:91], a[248:251], v[16:19], a[88:91]\n"
"v_cvt_pk_bf16_f32 v21, v230, v231\n"
"v_add_f32 v52, v52, v227\n"
"v_mfma_f32_16x16x32_bf16 a[92:95], a[252:255], v[16:19], a[92:95]\n"
"v_exp_f32 v232, v232\n"
"v_mfma_f32_16x16x32_bf16 v[0:3], a[128:131], v[64:67], v[24:27]\n"
"ds_read_b128 a[160:163], v241 offset:40960\n"
"v_mfma_f32_16x16x32_bf16 v[0:3], a[132:135], v[68:71], v[0:3]\n"
"v_exp_f32 v233, v233\n"
"v_mfma_f32_16x16x32_bf16 v[0:3], a[136:139], v[72:75], v[0:3]\n"
"ds_read_b128 a[164:167], v241 offset:41216\n"
"v_cvt_pk_bf16_f32 v22, v232, v233\n"
"v_mfma_f32_16x16x32_bf16 v[0:3], a[140:143], v[76:79], v[0:3]\n"
"v_exp_f32 v234, v234\n"
"v_mfma_f32_16x16x32_bf16 v[4:7], a[144:147], v[64:67], v[24:27]\n"
"ds_read_b128 a[168:171], v241 offset:41472\n"
"v_mfma_f32_16x16x32_bf16 v[4:7], a[148:151], v[68:71], v[4:7]\n"
"v_exp_f32 v235, v235\n"
"v_mfma_f32_16x16x32_bf16 v[4:7], a[152:155], v[72:75], v[4:7]\n"
"ds_read_b128 a[172:175], v241 offset:41728\n"
"v_cvt_pk_bf16_f32 v23, v234, v235\n"
"v_mfma_f32_16x16x32_bf16 v[4:7], a[156:159], v[76:79], v[4:7]\n"
"s_add_u32 m0, s53, 77824\n"
"v_exp_f32 v0, v0\n"
"v_mfma_f32_16x16x32_bf16 a[96:99], a[224:227], v[20:23], a[96:99]\n"
"buffer_load_dwordx4 v214, s[24:27], s22 offen offset:0 lds\n"
"v_mfma_f32_16x16x32_bf16 a[100:103], a[228:231], v[20:23], a[100:103]\n"
"v_exp_f32 v1, v1\n"
"v_mfma_f32_16x16x32_bf16 a[104:107], a[232:235], v[20:23], a[104:107]\n"
"ds_read_b128 a[176:179], v241 offset:45056\n"
"v_cvt_pk_bf16_f32 v16, v0, v1\n"
"v_mfma_f32_16x16x32_bf16 a[108:111], a[236:239], v[20:23], a[108:111]\n"
"v_exp_f32 v2, v2\n"
"v_mfma_f32_16x16x32_bf16 a[112:115], a[240:243], v[20:23], a[112:115]\n"
"ds_read_b128 a[180:183], v241 offset:45312\n"
"v_add_f32 v44, v44, v0\n"
"v_mfma_f32_16x16x32_bf16 a[116:119], a[244:247], v[20:23], a[116:119]\n"
"v_exp_f32 v3, v3\n"
"v_mfma_f32_16x16x32_bf16 a[120:123], a[248:251], v[20:23], a[120:123]\n"
"ds_read_b128 a[184:187], v241 offset:45568\n"
"v_cvt_pk_bf16_f32 v17, v2, v3\n"
"v_mfma_f32_16x16x32_bf16 a[124:127], a[252:255], v[20:23], a[124:127]\n"
"v_exp_f32 v4, v4\n"
"v_mfma_f32_16x16x32_bf16 v[56:59], v[40:43], v[20:23], v[56:59]\n"
"ds_read_b128 a[188:191], v241 offset:45824\n"
"v_add_f32 v44, v44, v1\n"
"v_mfma_f32_16x16x32_bf16 v[8:11], a[128:131], v[80:83], v[28:31]\n"
"v_exp_f32 v5, v5\n"
"v_mfma_f32_16x16x32_bf16 v[8:11], a[132:135], v[84:87], v[8:11]\n"
"ds_read_b64_tr_b16 a[224:225], v244 offset:57344\n"
"v_cvt_pk_bf16_f32 v18, v4, v5\n"
"v_mfma_f32_16x16x32_bf16 v[8:11], a[136:139], v[88:91], v[8:11]\n"
"v_exp_f32 v6, v6\n"
"v_mfma_f32_16x16x32_bf16 v[8:11], a[140:143], v[92:95], v[8:11]\n"
"ds_read_b64_tr_b16 a[226:227], v244 offset:61440\n"
"v_add_f32 v44, v44, v2\n"
"v_mfma_f32_16x16x32_bf16 v[12:15], a[144:147], v[80:83], v[28:31]\n"
"v_exp_f32 v7, v7\n"
"v_mfma_f32_16x16x32_bf16 v[12:15], a[148:151], v[84:87], v[12:15]\n"
"s_add_u32 m0, s53, 77824\n"
"ds_read_b64_tr_b16 a[228:229], v245 offset:57344\n"
"v_cvt_pk_bf16_f32 v19, v6, v7\n"
"v_mfma_f32_16x16x32_bf16 v[12:15], a[152:155], v[88:91], v[12:15]\n"
"buffer_load_dwordx4 v236, s[24:27], s22 offen offset:1024 lds\n"
"v_mfma_f32_16x16x32_bf16 v[12:15], a[156:159], v[92:95], v[12:15]\n"
"s_waitcnt lgkmcnt(11)\n"
"v_exp_f32 v8, v8\n"
"v_mfma_f32_16x16x32_bf16 a[0:3], a[192:195], v[16:19], a[0:3]\n"
"ds_read_b64_tr_b16 a[230:231], v245 offset:61440\n"
"v_add_f32 v44, v44, v3\n"
"v_mfma_f32_16x16x32_bf16 a[4:7], a[196:199], v[16:19], a[4:7]\n"
"v_exp_f32 v9, v9\n"
"v_mfma_f32_16x16x32_bf16 a[8:11], a[200:203], v[16:19], a[8:11]\n"
"ds_read_b64_tr_b16 a[232:233], v244 offset:57600\n"
"v_cvt_pk_bf16_f32 v20, v8, v9\n"
"v_mfma_f32_16x16x32_bf16 a[12:15], a[204:207], v[16:19], a[12:15]\n"
"v_exp_f32 v10, v10\n"
"v_mfma_f32_16x16x32_bf16 a[16:19], a[208:211], v[16:19], a[16:19]\n"
"ds_read_b64_tr_b16 a[234:235], v244 offset:61696\n"
"v_add_f32 v44, v44, v4\n"
"v_mfma_f32_16x16x32_bf16 a[20:23], a[212:215], v[16:19], a[20:23]\n"
"s_add_u32 m0, s53, 77824\n"
"v_exp_f32 v11, v11\n"
"v_mfma_f32_16x16x32_bf16 a[24:27], a[216:219], v[16:19], a[24:27]\n"
"buffer_load_dwordx4 v237, s[24:27], s22 offen offset:2048 lds\n"
"ds_read_b64_tr_b16 a[236:237], v245 offset:57600\n"
"v_cvt_pk_bf16_f32 v21, v10, v11\n"
"v_mfma_f32_16x16x32_bf16 a[28:31], a[220:223], v[16:19], a[28:31]\n"
"v_exp_f32 v12, v12\n"
"v_mfma_f32_16x16x32_bf16 v[220:223], a[128:131], v[96:99], v[32:35]\n"
"ds_read_b64_tr_b16 a[238:239], v245 offset:61696\n"
"v_add_f32 v44, v44, v5\n"
"v_mfma_f32_16x16x32_bf16 v[220:223], a[132:135], v[100:103], v[220:223]\n"
"v_exp_f32 v13, v13\n"
"v_mfma_f32_16x16x32_bf16 v[220:223], a[136:139], v[104:107], v[220:223]\n"
"ds_read_b64_tr_b16 a[240:241], v244 offset:57856\n"
"v_cvt_pk_bf16_f32 v22, v12, v13\n"
"v_mfma_f32_16x16x32_bf16 v[220:223], a[140:143], v[108:111], v[220:223]\n"
"v_exp_f32 v14, v14\n"
"v_mfma_f32_16x16x32_bf16 v[224:227], a[144:147], v[96:99], v[32:35]\n"
"ds_read_b64_tr_b16 a[242:243], v244 offset:61952\n"
"v_add_f32 v44, v44, v6\n"
"v_mfma_f32_16x16x32_bf16 v[224:227], a[148:151], v[100:103], v[224:227]\n"
"v_exp_f32 v15, v15\n"
"v_mfma_f32_16x16x32_bf16 v[224:227], a[152:155], v[104:107], v[224:227]\n"
"ds_read_b64_tr_b16 a[244:245], v245 offset:57856\n"
"v_cvt_pk_bf16_f32 v23, v14, v15\n"
"v_mfma_f32_16x16x32_bf16 v[224:227], a[156:159], v[108:111], v[224:227]\n"
"s_add_u32 m0, s53, 77824\n"
"v_exp_f32 v220, v220\n"
"v_mfma_f32_16x16x32_bf16 a[32:35], a[192:195], v[20:23], a[32:35]\n"
"buffer_load_dwordx4 v238, s[24:27], s22 offen offset:3072 lds\n"
"v_mfma_f32_16x16x32_bf16 a[36:39], a[196:199], v[20:23], a[36:39]\n"
"v_exp_f32 v221, v221\n"
"v_mfma_f32_16x16x32_bf16 a[40:43], a[200:203], v[20:23], a[40:43]\n"
"ds_read_b64_tr_b16 a[246:247], v245 offset:61952\n"
"v_cvt_pk_bf16_f32 v16, v220, v221\n"
"v_mfma_f32_16x16x32_bf16 a[44:47], a[204:207], v[20:23], a[44:47]\n"
"v_exp_f32 v222, v222\n"
"v_mfma_f32_16x16x32_bf16 a[48:51], a[208:211], v[20:23], a[48:51]\n"
"ds_read_b64_tr_b16 a[248:249], v244 offset:58112\n"
"v_add_f32 v44, v44, v7\n"
"v_mfma_f32_16x16x32_bf16 a[52:55], a[212:215], v[20:23], a[52:55]\n"
"v_exp_f32 v223, v223\n"
"v_mfma_f32_16x16x32_bf16 a[56:59], a[216:219], v[20:23], a[56:59]\n"
"ds_read_b64_tr_b16 a[250:251], v244 offset:62208\n"
"v_cvt_pk_bf16_f32 v17, v222, v223\n"
"v_mfma_f32_16x16x32_bf16 a[60:63], a[220:223], v[20:23], a[60:63]\n"
"v_exp_f32 v224, v224\n"
"v_mfma_f32_16x16x32_bf16 v[48:51], v[40:43], v[20:23], v[48:51]\n"
"ds_read_b64_tr_b16 a[252:253], v245 offset:58112\n"
"v_add_f32 v52, v52, v220\n"
"v_mfma_f32_16x16x32_bf16 v[228:231], a[128:131], v[112:115], v[36:39]\n"
"v_exp_f32 v225, v225\n"
"v_mfma_f32_16x16x32_bf16 v[228:231], a[132:135], v[116:119], v[228:231]\n"
"ds_read_b64_tr_b16 a[254:255], v245 offset:62208\n"
"v_cvt_pk_bf16_f32 v18, v224, v225\n"
"v_mfma_f32_16x16x32_bf16 v[228:231], a[136:139], v[120:123], v[228:231]\n"
"v_exp_f32 v226, v226\n"
"v_mfma_f32_16x16x32_bf16 v[228:231], a[140:143], v[124:127], v[228:231]\n"
"v_add_f32 v52, v52, v221\n"
"v_add_f32 v52, v52, v222\n"
"v_mfma_f32_16x16x32_bf16 v[232:235], a[144:147], v[112:115], v[36:39]\n"
"v_exp_f32 v227, v227\n"
"v_mfma_f32_16x16x32_bf16 v[232:235], a[148:151], v[116:119], v[232:235]\n"
"s_add_u32 m0, s53, 81920\n"
"v_cvt_pk_bf16_f32 v19, v226, v227\n"
"v_add_f32 v52, v52, v223\n"
"v_mfma_f32_16x16x32_bf16 v[232:235], a[152:155], v[120:123], v[232:235]\n"
"buffer_load_dwordx4 v239, s[24:27], s22 offen offset:0 lds\n"
"v_mfma_f32_16x16x32_bf16 v[232:235], a[156:159], v[124:127], v[232:235]\n"
"v_exp_f32 v228, v228\n"
"v_mfma_f32_16x16x32_bf16 a[64:67], a[192:195], v[16:19], a[64:67]\n"
"v_add_f32 v52, v52, v224\n"
"v_add_f32 v52, v52, v225\n"
"v_mfma_f32_16x16x32_bf16 a[68:71], a[196:199], v[16:19], a[68:71]\n"
"v_exp_f32 v229, v229\n"
"v_mfma_f32_16x16x32_bf16 a[72:75], a[200:203], v[16:19], a[72:75]\n"
"v_cvt_pk_bf16_f32 v20, v228, v229\n"
"v_add_f32 v52, v52, v226\n"
"v_mfma_f32_16x16x32_bf16 a[76:79], a[204:207], v[16:19], a[76:79]\n"
"s_waitcnt vmcnt(23) lgkmcnt(0)\n"
"v_exp_f32 v230, v230\n"
"v_mfma_f32_16x16x32_bf16 a[80:83], a[208:211], v[16:19], a[80:83]\n"
"s_barrier\n"
"v_mfma_f32_16x16x32_bf16 a[84:87], a[212:215], v[16:19], a[84:87]\n"
"v_exp_f32 v231, v231\n"
"v_mfma_f32_16x16x32_bf16 a[88:91], a[216:219], v[16:19], a[88:91]\n"
"v_cvt_pk_bf16_f32 v21, v230, v231\n"
"v_add_f32 v52, v52, v227\n"
"v_mfma_f32_16x16x32_bf16 a[92:95], a[220:223], v[16:19], a[92:95]\n"
"v_exp_f32 v232, v232\n"
"v_mfma_f32_16x16x32_bf16 v[0:3], a[160:163], v[64:67], v[24:27]\n"
"ds_read_b64_tr_b16 a[128:129], v202 offset:0\n"
"v_mfma_f32_16x16x32_bf16 v[0:3], a[164:167], v[68:71], v[0:3]\n"
"s_add_u32 m0, s53, 110592\n"
"v_exp_f32 v233, v233\n"
"v_mfma_f32_16x16x32_bf16 v[0:3], a[168:171], v[72:75], v[0:3]\n"
"buffer_load_dwordx4 v210, s[24:27], s23 offen offset:0 lds\n"
"ds_read_b64_tr_b16 a[130:131], v202 offset:4608\n"
"v_cvt_pk_bf16_f32 v22, v232, v233\n"
"v_mfma_f32_16x16x32_bf16 v[0:3], a[172:175], v[76:79], v[0:3]\n"
"v_exp_f32 v234, v234\n"
"v_mfma_f32_16x16x32_bf16 v[4:7], a[176:179], v[64:67], v[24:27]\n"
"ds_read_b64_tr_b16 a[132:133], v202 offset:32\n"
"v_mfma_f32_16x16x32_bf16 v[4:7], a[180:183], v[68:71], v[4:7]\n"
"s_add_u32 m0, s53, 110592\n"
"v_exp_f32 v235, v235\n"
"v_mfma_f32_16x16x32_bf16 v[4:7], a[184:187], v[72:75], v[4:7]\n"
"buffer_load_dwordx4 v211, s[24:27], s23 offen offset:1024 lds\n"
"ds_read_b64_tr_b16 a[134:135], v202 offset:4640\n"
"v_cvt_pk_bf16_f32 v23, v234, v235\n"
"v_mfma_f32_16x16x32_bf16 v[4:7], a[188:191], v[76:79], v[4:7]\n"
"v_exp_f32 v0, v0\n"
"v_mfma_f32_16x16x32_bf16 a[96:99], a[192:195], v[20:23], a[96:99]\n"
"s_nop 0\n"
"v_mfma_f32_16x16x32_bf16 a[100:103], a[196:199], v[20:23], a[100:103]\n"
"s_add_u32 m0, s53, 110592\n"
"v_exp_f32 v1, v1\n"
"v_mfma_f32_16x16x32_bf16 a[104:107], a[200:203], v[20:23], a[104:107]\n"
"buffer_load_dwordx4 v212, s[24:27], s23 offen offset:2048 lds\n"
"ds_read_b64_tr_b16 a[136:137], v202 offset:64\n"
"v_cvt_pk_bf16_f32 v16, v0, v1\n"
"v_mfma_f32_16x16x32_bf16 a[108:111], a[204:207], v[20:23], a[108:111]\n"
"v_exp_f32 v2, v2\n"
"v_mfma_f32_16x16x32_bf16 a[112:115], a[208:211], v[20:23], a[112:115]\n"
"ds_read_b64_tr_b16 a[138:139], v202 offset:4672\n"
"v_add_f32 v44, v44, v0\n"
"v_mfma_f32_16x16x32_bf16 a[116:119], a[212:215], v[20:23], a[116:119]\n"
"s_add_u32 m0, s53, 110592\n"
"v_exp_f32 v3, v3\n"
"v_mfma_f32_16x16x32_bf16 a[120:123], a[216:219], v[20:23], a[120:123]\n"
"buffer_load_dwordx4 v213, s[24:27], s23 offen offset:3072 lds\n"
"ds_read_b64_tr_b16 a[140:141], v202 offset:96\n"
"v_cvt_pk_bf16_f32 v17, v2, v3\n"
"v_mfma_f32_16x16x32_bf16 a[124:127], a[220:223], v[20:23], a[124:127]\n"
"v_exp_f32 v4, v4\n"
"v_mfma_f32_16x16x32_bf16 v[56:59], v[40:43], v[20:23], v[56:59]\n"
"ds_read_b64_tr_b16 a[142:143], v202 offset:4704\n"
"v_add_f32 v44, v44, v1\n"
"v_mfma_f32_16x16x32_bf16 v[8:11], a[160:163], v[80:83], v[28:31]\n"
"s_add_u32 m0, s53, 114688\n"
"v_exp_f32 v5, v5\n"
"v_mfma_f32_16x16x32_bf16 v[8:11], a[164:167], v[84:87], v[8:11]\n"
"buffer_load_dwordx4 v214, s[24:27], s23 offen offset:0 lds\n"
"ds_read_b64_tr_b16 a[144:145], v202 offset:128\n"
"v_cvt_pk_bf16_f32 v18, v4, v5\n"
"v_mfma_f32_16x16x32_bf16 v[8:11], a[168:171], v[88:91], v[8:11]\n"
"v_exp_f32 v6, v6\n"
"v_mfma_f32_16x16x32_bf16 v[8:11], a[172:175], v[92:95], v[8:11]\n"
"ds_read_b64_tr_b16 a[146:147], v202 offset:4736\n"
"v_add_f32 v44, v44, v2\n"
"v_mfma_f32_16x16x32_bf16 v[12:15], a[176:179], v[80:83], v[28:31]\n"
"s_add_u32 m0, s53, 114688\n"
"v_exp_f32 v7, v7\n"
"v_mfma_f32_16x16x32_bf16 v[12:15], a[180:183], v[84:87], v[12:15]\n"
"buffer_load_dwordx4 v236, s[24:27], s23 offen offset:1024 lds\n"
"ds_read_b64_tr_b16 a[148:149], v202 offset:160\n"
"v_cvt_pk_bf16_f32 v19, v6, v7\n"
"v_mfma_f32_16x16x32_bf16 v[12:15], a[184:187], v[88:91], v[12:15]\n"
"s_nop 0\n"
"v_mfma_f32_16x16x32_bf16 v[12:15], a[188:191], v[92:95], v[12:15]\n"
"s_add_u32 m0, s53, 114688\n"
"v_exp_f32 v8, v8\n"
"v_mfma_f32_16x16x32_bf16 a[0:3], a[224:227], v[16:19], a[0:3]\n"
"buffer_load_dwordx4 v237, s[24:27], s23 offen offset:2048 lds\n"
"ds_read_b64_tr_b16 a[150:151], v202 offset:4768\n"
"v_add_f32 v44, v44, v3\n"
"v_mfma_f32_16x16x32_bf16 a[4:7], a[228:231], v[16:19], a[4:7]\n"
"v_exp_f32 v9, v9\n"
"v_mfma_f32_16x16x32_bf16 a[8:11], a[232:235], v[16:19], a[8:11]\n"
"ds_read_b64_tr_b16 a[152:153], v202 offset:192\n"
"v_cvt_pk_bf16_f32 v20, v8, v9\n"
"v_mfma_f32_16x16x32_bf16 a[12:15], a[236:239], v[16:19], a[12:15]\n"
"s_add_u32 m0, s53, 114688\n"
"v_exp_f32 v10, v10\n"
"v_mfma_f32_16x16x32_bf16 a[16:19], a[240:243], v[16:19], a[16:19]\n"
"buffer_load_dwordx4 v238, s[24:27], s23 offen offset:3072 lds\n"
"ds_read_b64_tr_b16 a[154:155], v202 offset:4800\n"
"v_add_f32 v44, v44, v4\n"
"v_mfma_f32_16x16x32_bf16 a[20:23], a[244:247], v[16:19], a[20:23]\n"
"v_exp_f32 v11, v11\n"
"v_mfma_f32_16x16x32_bf16 a[24:27], a[248:251], v[16:19], a[24:27]\n"
"ds_read_b64_tr_b16 a[156:157], v202 offset:224\n"
"v_cvt_pk_bf16_f32 v21, v10, v11\n"
"v_mfma_f32_16x16x32_bf16 a[28:31], a[252:255], v[16:19], a[28:31]\n"
"s_add_u32 m0, s53, 118784\n"
"v_exp_f32 v12, v12\n"
"v_mfma_f32_16x16x32_bf16 v[220:223], a[160:163], v[96:99], v[32:35]\n"
"buffer_load_dwordx4 v239, s[24:27], s23 offen offset:0 lds\n"
"ds_read_b64_tr_b16 a[158:159], v202 offset:4832\n"
"v_add_f32 v44, v44, v5\n"
"v_mfma_f32_16x16x32_bf16 v[220:223], a[164:167], v[100:103], v[220:223]\n"
"v_exp_f32 v13, v13\n"
"v_mfma_f32_16x16x32_bf16 v[220:223], a[168:171], v[104:107], v[220:223]\n"
"ds_read_b64_tr_b16 a[192:193], v205 offset:0\n"
"v_cvt_pk_bf16_f32 v22, v12, v13\n"
"v_mfma_f32_16x16x32_bf16 v[220:223], a[172:175], v[108:111], v[220:223]\n"
"v_exp_f32 v14, v14\n"
"v_mfma_f32_16x16x32_bf16 v[224:227], a[176:179], v[96:99], v[32:35]\n"
"ds_read_b64_tr_b16 a[194:195], v205 offset:4608\n"
"v_add_f32 v44, v44, v6\n"
"v_mfma_f32_16x16x32_bf16 v[224:227], a[180:183], v[100:103], v[224:227]\n"
"v_exp_f32 v15, v15\n"
"v_mfma_f32_16x16x32_bf16 v[224:227], a[184:187], v[104:107], v[224:227]\n"
"ds_read_b64_tr_b16 a[196:197], v202 offset:9216\n"
"v_cvt_pk_bf16_f32 v23, v14, v15\n"
"v_mfma_f32_16x16x32_bf16 v[224:227], a[188:191], v[108:111], v[224:227]\n"
"v_exp_f32 v220, v220\n"
"v_mfma_f32_16x16x32_bf16 a[32:35], a[224:227], v[20:23], a[32:35]\n"
"s_nop 0\n"
"v_mfma_f32_16x16x32_bf16 a[36:39], a[228:231], v[20:23], a[36:39]\n"
"v_exp_f32 v221, v221\n"
"v_mfma_f32_16x16x32_bf16 a[40:43], a[232:235], v[20:23], a[40:43]\n"
"ds_read_b64_tr_b16 a[198:199], v202 offset:13824\n"
"v_cvt_pk_bf16_f32 v16, v220, v221\n"
"v_mfma_f32_16x16x32_bf16 a[44:47], a[236:239], v[20:23], a[44:47]\n"
"v_exp_f32 v222, v222\n"
"v_mfma_f32_16x16x32_bf16 a[48:51], a[240:243], v[20:23], a[48:51]\n"
"v_add_f32 v44, v44, v7\n"
"v_mfma_f32_16x16x32_bf16 a[52:55], a[244:247], v[20:23], a[52:55]\n"
"v_exp_f32 v223, v223\n"
"s_add_u32 s46, s52, 4096\n"
"s_add_u32 s47, s52, 8192\n"
"s_add_u32 s48, s52, 12288\n"
"v_mov_b32 v45, v44\n"
"v_mfma_f32_16x16x32_bf16 a[56:59], a[248:251], v[20:23], a[56:59]\n"
"v_cvt_pk_bf16_f32 v17, v222, v223\n"
"s_nop 0\n"
"s_nop 0\n"
"v_permlane16_swap_b32 v44, v45\n"
"v_add_f32 v44, v44, v45\n"
"v_mfma_f32_16x16x32_bf16 a[60:63], a[252:255], v[20:23], a[60:63]\n"
"v_exp_f32 v224, v224\n"
"v_mov_b32 v45, v44\n"
"s_nop 0\n"
"s_nop 0\n"
"v_permlane32_swap_b32 v44, v45\n"
"v_mfma_f32_16x16x32_bf16 v[48:51], v[40:43], v[20:23], v[48:51]\n"
"v_add_f32 v52, v52, v220\n"
"v_add_f32 v44, v44, v45\n"
"v_rcp_f32 v46, v44\n"
"v_accvgpr_read_b32 v216, a0\n"
"v_accvgpr_read_b32 v217, a1\n"
"v_mfma_f32_16x16x32_bf16 v[228:231], a[160:163], v[112:115], v[36:39]\n"
"v_exp_f32 v225, v225\n"
"v_accvgpr_read_b32 v218, a2\n"
"v_accvgpr_read_b32 v219, a3\n"
"v_accvgpr_read_b32 v60, a4\n"
"v_accvgpr_read_b32 v61, a5\n"
"v_mfma_f32_16x16x32_bf16 v[228:231], a[164:167], v[116:119], v[228:231]\n"
"v_cvt_pk_bf16_f32 v18, v224, v225\n"
"v_accvgpr_read_b32 v62, a6\n"
"v_accvgpr_read_b32 v63, a7\n"
"v_mul_f32 v216, v216, v46\n"
"v_mul_f32 v217, v217, v46\n"
"v_mfma_f32_16x16x32_bf16 v[228:231], a[168:171], v[120:123], v[228:231]\n"
"v_exp_f32 v226, v226\n"
"v_mul_f32 v218, v218, v46\n"
"v_mul_f32 v219, v219, v46\n"
"v_mul_f32 v60, v60, v46\n"
"v_mul_f32 v61, v61, v46\n"
"v_mfma_f32_16x16x32_bf16 v[228:231], a[172:175], v[124:127], v[228:231]\n"
"v_add_f32 v52, v52, v221\n"
"v_add_f32 v52, v52, v222\n"
"v_mul_f32 v62, v62, v46\n"
"v_mul_f32 v63, v63, v46\n"
"v_cvt_pk_bf16_f32 v0, v216, v217\n"
"v_cvt_pk_bf16_f32 v1, v218, v219\n"
"v_mfma_f32_16x16x32_bf16 v[232:235], a[176:179], v[112:115], v[36:39]\n"
"v_exp_f32 v227, v227\n"
"v_cvt_pk_bf16_f32 v2, v60, v61\n"
"v_cvt_pk_bf16_f32 v3, v62, v63\n"
"buffer_store_dwordx4 v[0:3], v207, s[28:31], s52 offen offset:0 sc1\n"
"v_accvgpr_read_b32 v216, a8\n"
"v_mfma_f32_16x16x32_bf16 v[232:235], a[180:183], v[116:119], v[232:235]\n"
"v_cvt_pk_bf16_f32 v19, v226, v227\n"
"v_add_f32 v52, v52, v223\n"
"v_accvgpr_read_b32 v217, a9\n"
"v_accvgpr_read_b32 v218, a10\n"
"v_accvgpr_read_b32 v219, a11\n"
"v_accvgpr_read_b32 v60, a12\n"
"v_mfma_f32_16x16x32_bf16 v[232:235], a[184:187], v[120:123], v[232:235]\n"
"v_accvgpr_read_b32 v61, a13\n"
"v_accvgpr_read_b32 v62, a14\n"
"v_accvgpr_read_b32 v63, a15\n"
"v_mul_f32 v216, v216, v46\n"
"v_mfma_f32_16x16x32_bf16 v[232:235], a[188:191], v[124:127], v[232:235]\n"
"v_exp_f32 v228, v228\n"
"v_mul_f32 v217, v217, v46\n"
"v_mul_f32 v218, v218, v46\n"
"v_mul_f32 v219, v219, v46\n"
"v_mul_f32 v60, v60, v46\n"
"v_mfma_f32_16x16x32_bf16 a[64:67], a[224:227], v[16:19], a[64:67]\n"
"v_add_f32 v52, v52, v224\n"
"v_add_f32 v52, v52, v225\n"
"v_mul_f32 v61, v61, v46\n"
"v_mul_f32 v62, v62, v46\n"
"v_mul_f32 v63, v63, v46\n"
"v_cvt_pk_bf16_f32 v4, v216, v217\n"
"v_mfma_f32_16x16x32_bf16 a[68:71], a[228:231], v[16:19], a[68:71]\n"
"v_exp_f32 v229, v229\n"
"v_cvt_pk_bf16_f32 v5, v218, v219\n"
"v_cvt_pk_bf16_f32 v6, v60, v61\n"
"v_cvt_pk_bf16_f32 v7, v62, v63\n"
"buffer_store_dwordx4 v[4:7], v207, s[28:31], s52 offen offset:2048 sc1\n"
"v_mfma_f32_16x16x32_bf16 a[72:75], a[232:235], v[16:19], a[72:75]\n"
"v_cvt_pk_bf16_f32 v20, v228, v229\n"
"v_add_f32 v52, v52, v226\n"
"v_accvgpr_read_b32 v216, a16\n"
"v_accvgpr_read_b32 v217, a17\n"
"v_accvgpr_read_b32 v218, a18\n"
"v_accvgpr_read_b32 v219, a19\n"
"v_mfma_f32_16x16x32_bf16 a[76:79], a[236:239], v[16:19], a[76:79]\n"
"v_exp_f32 v230, v230\n"
"v_accvgpr_read_b32 v60, a20\n"
"v_accvgpr_read_b32 v61, a21\n"
"v_accvgpr_read_b32 v62, a22\n"
"v_accvgpr_read_b32 v63, a23\n"
"v_mfma_f32_16x16x32_bf16 a[80:83], a[240:243], v[16:19], a[80:83]\n"
"v_mul_f32 v216, v216, v46\n"
"v_mul_f32 v217, v217, v46\n"
"v_mul_f32 v218, v218, v46\n"
"v_mul_f32 v219, v219, v46\n"
"v_mfma_f32_16x16x32_bf16 a[84:87], a[244:247], v[16:19], a[84:87]\n"
"v_exp_f32 v231, v231\n"
"v_mul_f32 v60, v60, v46\n"
"v_mul_f32 v61, v61, v46\n"
"v_mul_f32 v62, v62, v46\n"
"v_mul_f32 v63, v63, v46\n"
"v_mfma_f32_16x16x32_bf16 a[88:91], a[248:251], v[16:19], a[88:91]\n"
"v_cvt_pk_bf16_f32 v21, v230, v231\n"
"v_add_f32 v52, v52, v227\n"
"v_cvt_pk_bf16_f32 v8, v216, v217\n"
"v_cvt_pk_bf16_f32 v9, v218, v219\n"
"v_cvt_pk_bf16_f32 v10, v60, v61\n"
"v_cvt_pk_bf16_f32 v11, v62, v63\n"
"v_mfma_f32_16x16x32_bf16 a[92:95], a[252:255], v[16:19], a[92:95]\n"
"v_exp_f32 v232, v232\n"
"buffer_store_dwordx4 v[8:11], v207, s[28:31], s46 offen offset:0 sc1\n"
"v_accvgpr_read_b32 v216, a24\n"
"v_accvgpr_read_b32 v217, a25\n"
"v_accvgpr_read_b32 v218, a26\n"
"v_exp_f32 v233, v233\n"
"v_accvgpr_read_b32 v219, a27\n"
"v_cvt_pk_bf16_f32 v22, v232, v233\n"
"v_accvgpr_read_b32 v60, a28\n"
"v_exp_f32 v234, v234\n"
"v_accvgpr_read_b32 v61, a29\n"
"v_exp_f32 v235, v235\n"
"v_accvgpr_read_b32 v62, a30\n"
"v_cvt_pk_bf16_f32 v23, v234, v235\n"
"v_accvgpr_read_b32 v63, a31\n"
"v_mul_f32 v216, v216, v46\n"
"v_mul_f32 v217, v217, v46\n"
"v_mfma_f32_16x16x32_bf16 a[96:99], a[224:227], v[20:23], a[96:99]\n"
"v_mul_f32 v218, v218, v46\n"
"v_mul_f32 v219, v219, v46\n"
"v_mfma_f32_16x16x32_bf16 a[100:103], a[228:231], v[20:23], a[100:103]\n"
"v_mul_f32 v60, v60, v46\n"
"v_mul_f32 v61, v61, v46\n"
"v_mfma_f32_16x16x32_bf16 a[104:107], a[232:235], v[20:23], a[104:107]\n"
"v_mul_f32 v62, v62, v46\n"
"v_mul_f32 v63, v63, v46\n"
"v_mfma_f32_16x16x32_bf16 a[108:111], a[236:239], v[20:23], a[108:111]\n"
"v_cvt_pk_bf16_f32 v12, v216, v217\n"
"v_cvt_pk_bf16_f32 v13, v218, v219\n"
"v_mfma_f32_16x16x32_bf16 a[112:115], a[240:243], v[20:23], a[112:115]\n"
"v_cvt_pk_bf16_f32 v14, v60, v61\n"
"v_cvt_pk_bf16_f32 v15, v62, v63\n"
"v_mfma_f32_16x16x32_bf16 a[116:119], a[244:247], v[20:23], a[116:119]\n"
"buffer_store_dwordx4 v[12:15], v207, s[28:31], s46 offen offset:2048 sc1\n"
"v_rcp_f32 v48, v49\n"
"v_mfma_f32_16x16x32_bf16 a[120:123], a[248:251], v[20:23], a[120:123]\n"
"v_accvgpr_read_b32 v216, a48\n"
"v_accvgpr_read_b32 v217, a49\n"
"v_mfma_f32_16x16x32_bf16 a[124:127], a[252:255], v[20:23], a[124:127]\n"
"v_accvgpr_read_b32 v218, a50\n"
"v_accvgpr_read_b32 v219, a51\n"
"v_mfma_f32_16x16x32_bf16 v[56:59], v[40:43], v[20:23], v[56:59]\n"
"v_accvgpr_read_b32 v60, a52\n"
"v_accvgpr_read_b32 v61, a53\n"
"v_accvgpr_read_b32 v62, a54\n"
"v_accvgpr_read_b32 v63, a55\n"
"v_mul_f32 v216, v216, v48\n"
"v_mul_f32 v217, v217, v48\n"
"v_mul_f32 v218, v218, v48\n"
"v_mul_f32 v219, v219, v48\n"
"v_mul_f32 v60, v60, v48\n"
"v_mul_f32 v61, v61, v48\n"
"v_mul_f32 v62, v62, v48\n"
"v_mul_f32 v63, v63, v48\n"
"v_cvt_pk_bf16_f32 v24, v216, v217\n"
"v_cvt_pk_bf16_f32 v25, v218, v219\n"
"v_cvt_pk_bf16_f32 v26, v60, v61\n"
"v_cvt_pk_bf16_f32 v27, v62, v63\n"
"buffer_store_dwordx4 v[24:27], v207, s[28:31], s48 offen offset:0 sc1\n"
"v_accvgpr_read_b32 v216, a56\n"
"v_accvgpr_read_b32 v217, a57\n"
"v_accvgpr_read_b32 v218, a58\n"
"v_accvgpr_read_b32 v219, a59\n"
"v_accvgpr_read_b32 v60, a60\n"
"v_accvgpr_read_b32 v61, a61\n"
"v_accvgpr_read_b32 v62, a62\n"
"v_accvgpr_read_b32 v63, a63\n"
"v_mul_f32 v216, v216, v48\n"
"v_mul_f32 v217, v217, v48\n"
"v_mul_f32 v218, v218, v48\n"
"v_mul_f32 v219, v219, v48\n"
"v_mul_f32 v60, v60, v48\n"
"v_mul_f32 v61, v61, v48\n"
"v_mul_f32 v62, v62, v48\n"
"v_mul_f32 v63, v63, v48\n"
"v_cvt_pk_bf16_f32 v28, v216, v217\n"
"v_cvt_pk_bf16_f32 v29, v218, v219\n"
"v_cvt_pk_bf16_f32 v30, v60, v61\n"
"v_cvt_pk_bf16_f32 v31, v62, v63\n"
"buffer_store_dwordx4 v[28:31], v207, s[28:31], s48 offen offset:2048 sc1\n"
"v_mov_b32 v226, v52\n"
"s_nop 0\n"
"s_nop 0\n"
"v_permlane16_swap_b32 v52, v226\n"
"v_add_f32 v52, v52, v226\n"
"v_mov_b32 v226, v52\n"
"s_nop 0\n"
"s_nop 0\n"
"v_permlane32_swap_b32 v52, v226\n"
"v_add_f32 v52, v52, v226\n"
"v_mov_b32 v219, 0x5f800000\n"
"s_nop 7\n"
"v_cmp_nle_f32 s[56:57], v44, v219\n"
"v_cmp_nle_f32 s[58:59], v49, v219\n"
"v_cmp_nle_f32 s[60:61], v52, v219\n"
"v_cmp_nle_f32 s[62:63], v56, v219\n"
"v_rcp_f32 v218, v44\n"
"v_rcp_f32 v220, v49\n"
"v_rcp_f32 v222, v52\n"
"v_rcp_f32 v224, v56\n"
"v_accvgpr_read_b32 v226, a32\n"
"v_accvgpr_read_b32 v227, a33\n"
"v_accvgpr_read_b32 v228, a34\n"
"v_accvgpr_read_b32 v229, a35\n"
"v_accvgpr_read_b32 v230, a36\n"
"v_accvgpr_read_b32 v231, a37\n"
"v_accvgpr_read_b32 v232, a38\n"
"v_accvgpr_read_b32 v233, a39\n"
"v_pk_mul_f32 v[226:227], v[226:227], v[220:221] op_sel_hi:[1,0]\n"
"v_pk_mul_f32 v[228:229], v[228:229], v[220:221] op_sel_hi:[1,0]\n"
"v_pk_mul_f32 v[230:231], v[230:231], v[220:221] op_sel_hi:[1,0]\n"
"v_pk_mul_f32 v[232:233], v[232:233], v[220:221] op_sel_hi:[1,0]\n"
"v_cvt_pk_bf16_f32 v16, v226, v227\n"
"v_cvt_pk_bf16_f32 v17, v228, v229\n"
"v_cvt_pk_bf16_f32 v18, v230, v231\n"
"v_cvt_pk_bf16_f32 v19, v232, v233\n"
"buffer_store_dwordx4 v[16:19], v207, s[28:31], s47 offen offset:0 sc1\n"
"s_or_b64 s[56:57], s[56:57], s[58:59]\n"
"s_or_b64 s[60:61], s[60:61], s[62:63]\n"
"s_or_b64 s[56:57], s[56:57], s[60:61]\n"
"s_cselect_b32 s56, 1, 0\n"
"s_cmp_eq_u32 s45, 0\n"
"s_cselect_b32 s56, s56, 0\n"
"s_lshl_b32 s57, s41, 2\n"
"s_add_u32 s57, s57, s40\n"
"s_add_u32 s57, s57, 163776\n"
"v_mov_b32 v234, s56\n"
"v_mov_b32 v235, s57\n"
"ds_write_b32 v235, v234\n"
"v_accvgpr_read_b32 v226, a40\n"
"v_accvgpr_read_b32 v227, a41\n"
"v_accvgpr_read_b32 v228, a42\n"
"v_accvgpr_read_b32 v229, a43\n"
"v_accvgpr_read_b32 v230, a44\n"
"v_accvgpr_read_b32 v231, a45\n"
"v_accvgpr_read_b32 v232, a46\n"
"v_accvgpr_read_b32 v233, a47\n"
"v_pk_mul_f32 v[226:227], v[226:227], v[220:221] op_sel_hi:[1,0]\n"
"v_pk_mul_f32 v[228:229], v[228:229], v[220:221] op_sel_hi:[1,0]\n"
"v_pk_mul_f32 v[230:231], v[230:231], v[220:221] op_sel_hi:[1,0]\n"
"v_pk_mul_f32 v[232:233], v[232:233], v[220:221] op_sel_hi:[1,0]\n"
"v_cvt_pk_bf16_f32 v20, v226, v227\n"
"v_cvt_pk_bf16_f32 v21, v228, v229\n"
"v_cvt_pk_bf16_f32 v22, v230, v231\n"
"v_cvt_pk_bf16_f32 v23, v232, v233\n"
"buffer_store_dwordx4 v[20:23], v207, s[28:31], s47 offen offset:2048 sc1\n"
"s_and_b32 s60, s55, 4294967292\n"
"s_mov_b32 s23, 9576448\n"
"s_lshl_b32 s62, s60, 6\n"
"v_and_b32 v215, 3, v248\n"
"v_lshlrev_b32 v215, 6, v215\n"
"v_add_u32 v215, s62, v215\n"
"v_min_u32 v251, 1, v253\n"
"v_lshlrev_b32 v251, 28, v251\n"
"s_lshl_b32 s62, s55, 6\n"
"v_add_u32 v251, s62, v251\n"
"v_mov_b32 v252, s85\n"
"v_accvgpr_read_b32 v226, a64\n"
"v_accvgpr_read_b32 v227, a65\n"
"v_accvgpr_read_b32 v228, a66\n"
"v_accvgpr_read_b32 v229, a67\n"
"v_accvgpr_read_b32 v230, a68\n"
"v_accvgpr_read_b32 v231, a69\n"
"v_accvgpr_read_b32 v232, a70\n"
"v_accvgpr_read_b32 v233, a71\n"
"v_pk_mul_f32 v[226:227], v[226:227], v[222:223] op_sel_hi:[1,0]\n"
"v_pk_mul_f32 v[228:229], v[228:229], v[222:223] op_sel_hi:[1,0]\n"
"v_pk_mul_f32 v[230:231], v[230:231], v[222:223] op_sel_hi:[1,0]\n"
"v_pk_mul_f32 v[232:233], v[232:233], v[222:223] op_sel_hi:[1,0]\n"
"v_cvt_pk_bf16_f32 v32, v226, v227\n"
"v_cvt_pk_bf16_f32 v33, v228, v229\n"
"v_cvt_pk_bf16_f32 v34, v230, v231\n"
"v_cvt_pk_bf16_f32 v35, v232, v233\n"
"buffer_store_dwordx4 v[32:35], v207, s[28:31], s52 offen offset:1024 sc1\n"
"s_add_u32 s61, s43, 1\n"
"s_and_b32 s61, s61, 3\n"
"s_add_u32 s61, s61, s60\n"
"s_lshl_b32 s61, s61, 2\n"
"s_add_u32 s61, s61, s41\n"
"s_lshl_b32 s61, s61, 14\n"
"s_add_u32 s20, s61, 9592832\n"
"v_accvgpr_read_b32 v226, a72\n"
"v_accvgpr_read_b32 v227, a73\n"
"v_accvgpr_read_b32 v228, a74\n"
"v_accvgpr_read_b32 v229, a75\n"
"v_accvgpr_read_b32 v230, a76\n"
"v_accvgpr_read_b32 v231, a77\n"
"v_accvgpr_read_b32 v232, a78\n"
"v_accvgpr_read_b32 v233, a79\n"
"v_pk_mul_f32 v[226:227], v[226:227], v[222:223] op_sel_hi:[1,0]\n"
"v_pk_mul_f32 v[228:229], v[228:229], v[222:223] op_sel_hi:[1,0]\n"
"v_pk_mul_f32 v[230:231], v[230:231], v[222:223] op_sel_hi:[1,0]\n"
"v_pk_mul_f32 v[232:233], v[232:233], v[222:223] op_sel_hi:[1,0]\n"
"v_cvt_pk_bf16_f32 v36, v226, v227\n"
"v_cvt_pk_bf16_f32 v37, v228, v229\n"
"v_cvt_pk_bf16_f32 v38, v230, v231\n"
"v_cvt_pk_bf16_f32 v39, v232, v233\n"
"buffer_store_dwordx4 v[36:39], v207, s[28:31], s52 offen offset:3072 sc1\n"
"s_add_u32 s61, s43, 2\n"
"s_and_b32 s61, s61, 3\n"
"s_add_u32 s61, s61, s60\n"
"s_lshl_b32 s61, s61, 2\n"
"s_add_u32 s61, s61, s41\n"
"s_lshl_b32 s61, s61, 14\n"
"s_add_u32 s21, s61, 9592832\n"
"v_accvgpr_read_b32 v226, a80\n"
"v_accvgpr_read_b32 v227, a81\n"
"v_accvgpr_read_b32 v228, a82\n"
"v_accvgpr_read_b32 v229, a83\n"
"v_accvgpr_read_b32 v230, a84\n"
"v_accvgpr_read_b32 v231, a85\n"
"v_accvgpr_read_b32 v232, a86\n"
"v_accvgpr_read_b32 v233, a87\n"
"v_pk_mul_f32 v[226:227], v[226:227], v[222:223] op_sel_hi:[1,0]\n"
"v_pk_mul_f32 v[228:229], v[228:229], v[222:223] op_sel_hi:[1,0]\n"
"v_pk_mul_f32 v[230:231], v[230:231], v[222:223] op_sel_hi:[1,0]\n"
"v_pk_mul_f32 v[232:233], v[232:233], v[222:223] op_sel_hi:[1,0]\n"
"v_cvt_pk_bf16_f32 v40, v226, v227\n"
"v_cvt_pk_bf16_f32 v41, v228, v229\n"
"v_cvt_pk_bf16_f32 v42, v230, v231\n"
"v_cvt_pk_bf16_f32 v43, v232, v233\n"
"buffer_store_dwordx4 v[40:43], v207, s[28:31], s46 offen offset:1024 sc1\n"
"s_add_u32 s61, s43, 3\n"
"s_and_b32 s61, s61, 3\n"
"s_add_u32 s61, s61, s60\n"
"s_lshl_b32 s61, s61, 2\n"
"s_add_u32 s61, s61, s41\n"
"s_lshl_b32 s61, s61, 14\n"
"s_add_u32 s22, s61, 9592832\n"
"v_accvgpr_read_b32 v226, a88\n"
"v_accvgpr_read_b32 v227, a89\n"
"v_accvgpr_read_b32 v228, a90\n"
"v_accvgpr_read_b32 v229, a91\n"
"v_accvgpr_read_b32 v230, a92\n"
"v_accvgpr_read_b32 v231, a93\n"
"v_accvgpr_read_b32 v232, a94\n"
"v_accvgpr_read_b32 v233, a95\n"
"v_pk_mul_f32 v[226:227], v[226:227], v[222:223] op_sel_hi:[1,0]\n"
"v_pk_mul_f32 v[228:229], v[228:229], v[222:223] op_sel_hi:[1,0]\n"
"v_pk_mul_f32 v[230:231], v[230:231], v[222:223] op_sel_hi:[1,0]\n"
"v_pk_mul_f32 v[232:233], v[232:233], v[222:223] op_sel_hi:[1,0]\n"
"v_cvt_pk_bf16_f32 v44, v226, v227\n"
"v_cvt_pk_bf16_f32 v45, v228, v229\n"
"v_cvt_pk_bf16_f32 v46, v230, v231\n"
"v_cvt_pk_bf16_f32 v47, v232, v233\n"
"buffer_store_dwordx4 v[44:47], v207, s[28:31], s46 offen offset:3072 sc1\n"
"s_waitcnt vmcnt(32) lgkmcnt(0)\n"
"s_barrier\n"
"ds_read_b32 v234, v209\n"
"v_accvgpr_read_b32 v226, a96\n"
"v_accvgpr_read_b32 v227, a97\n"
"v_accvgpr_read_b32 v228, a98\n"
"v_accvgpr_read_b32 v229, a99\n"
"v_accvgpr_read_b32 v230, a100\n"
"v_accvgpr_read_b32 v231, a101\n"
"v_accvgpr_read_b32 v232, a102\n"
"v_accvgpr_read_b32 v233, a103\n"
"v_pk_mul_f32 v[226:227], v[226:227], v[224:225] op_sel_hi:[1,0]\n"
"v_pk_mul_f32 v[228:229], v[228:229], v[224:225] op_sel_hi:[1,0]\n"
"v_pk_mul_f32 v[230:231], v[230:231], v[224:225] op_sel_hi:[1,0]\n"
"v_pk_mul_f32 v[232:233], v[232:233], v[224:225] op_sel_hi:[1,0]\n"
"v_cvt_pk_bf16_f32 v48, v226, v227\n"
"v_cvt_pk_bf16_f32 v49, v228, v229\n"
"v_cvt_pk_bf16_f32 v50, v230, v231\n"
"v_cvt_pk_bf16_f32 v51, v232, v233\n"
"buffer_store_dwordx4 v[48:51], v207, s[28:31], s47 offen offset:1024 sc1\n"
"v_accvgpr_read_b32 v226, a104\n"
"v_accvgpr_read_b32 v227, a105\n"
"v_accvgpr_read_b32 v228, a106\n"
"v_accvgpr_read_b32 v229, a107\n"
"v_accvgpr_read_b32 v230, a108\n"
"v_accvgpr_read_b32 v231, a109\n"
"v_accvgpr_read_b32 v232, a110\n"
"v_accvgpr_read_b32 v233, a111\n"
"v_pk_mul_f32 v[226:227], v[226:227], v[224:225] op_sel_hi:[1,0]\n"
"v_pk_mul_f32 v[228:229], v[228:229], v[224:225] op_sel_hi:[1,0]\n"
"v_pk_mul_f32 v[230:231], v[230:231], v[224:225] op_sel_hi:[1,0]\n"
"v_pk_mul_f32 v[232:233], v[232:233], v[224:225] op_sel_hi:[1,0]\n"
"v_cvt_pk_bf16_f32 v52, v226, v227\n"
"v_cvt_pk_bf16_f32 v53, v228, v229\n"
"v_cvt_pk_bf16_f32 v54, v230, v231\n"
"v_cvt_pk_bf16_f32 v55, v232, v233\n"
"buffer_store_dwordx4 v[52:55], v207, s[28:31], s47 offen offset:3072 sc1\n"
"v_accvgpr_read_b32 v226, a112\n"
"v_accvgpr_read_b32 v227, a113\n"
"v_accvgpr_read_b32 v228, a114\n"
"v_accvgpr_read_b32 v229, a115\n"
"v_accvgpr_read_b32 v230, a116\n"
"v_accvgpr_read_b32 v231, a117\n"
"v_accvgpr_read_b32 v232, a118\n"
"v_accvgpr_read_b32 v233, a119\n"
"v_pk_mul_f32 v[226:227], v[226:227], v[224:225] op_sel_hi:[1,0]\n"
"v_pk_mul_f32 v[228:229], v[228:229], v[224:225] op_sel_hi:[1,0]\n"
"v_pk_mul_f32 v[230:231], v[230:231], v[224:225] op_sel_hi:[1,0]\n"
"v_pk_mul_f32 v[232:233], v[232:233], v[224:225] op_sel_hi:[1,0]\n"
"v_cvt_pk_bf16_f32 v56, v226, v227\n"
"v_cvt_pk_bf16_f32 v57, v228, v229\n"
"v_cvt_pk_bf16_f32 v58, v230, v231\n"
"v_cvt_pk_bf16_f32 v59, v232, v233\n"
"buffer_store_dwordx4 v[56:59], v207, s[28:31], s48 offen offset:1024 sc1\n"
"v_accvgpr_read_b32 v226, a120\n"
"v_accvgpr_read_b32 v227, a121\n"
"v_accvgpr_read_b32 v228, a122\n"
"v_accvgpr_read_b32 v229, a123\n"
"v_accvgpr_read_b32 v230, a124\n"
"v_accvgpr_read_b32 v231, a125\n"
"v_accvgpr_read_b32 v232, a126\n"
"v_accvgpr_read_b32 v233, a127\n"
"v_pk_mul_f32 v[226:227], v[226:227], v[224:225] op_sel_hi:[1,0]\n"
"v_pk_mul_f32 v[228:229], v[228:229], v[224:225] op_sel_hi:[1,0]\n"
"v_pk_mul_f32 v[230:231], v[230:231], v[224:225] op_sel_hi:[1,0]\n"
"v_pk_mul_f32 v[232:233], v[232:233], v[224:225] op_sel_hi:[1,0]\n"
"v_cvt_pk_bf16_f32 v60, v226, v227\n"
"v_cvt_pk_bf16_f32 v61, v228, v229\n"
"v_cvt_pk_bf16_f32 v62, v230, v231\n"
"v_cvt_pk_bf16_f32 v63, v232, v233\n"
"buffer_store_dwordx4 v[60:63], v207, s[28:31], s48 offen offset:3072 sc1\n"
"s_waitcnt lgkmcnt(0)\n"
"v_cmp_ne_u32 vcc, 0, v234\n"
"s_nop 0\n"
"s_cbranch_vccnz AT_fb\n"
"v_mfma_f32_16x16x32_bf16 v[64:67], a[128:131], v[0:3], 0\n"
"ds_read_b64_tr_b16 a[200:201], v202 offset:9248\n"
"v_mfma_f32_16x16x32_bf16 v[68:71], a[128:131], v[32:35], 0\n"
"ds_read_b64_tr_b16 a[202:203], v202 offset:13856\n"
"v_mfma_f32_16x16x32_bf16 v[72:75], a[132:135], v[0:3], 0\n"
"s_mov_b32 s56, 41984\n"
"ds_read_b64_tr_b16 a[204:205], v202 offset:9280\n"
"v_mfma_f32_16x16x32_bf16 v[76:79], a[132:135], v[32:35], 0\n"
"s_mov_b32 s57, 46592\n"
"ds_read_b64_tr_b16 a[206:207], v202 offset:13888\n"
"v_mfma_f32_16x16x32_bf16 v[80:83], a[136:139], v[0:3], 0\n"
"s_mov_b32 s58, 51200\n"
"ds_read_b64_tr_b16 a[208:209], v202 offset:9312\n"
"v_mfma_f32_16x16x32_bf16 v[84:87], a[136:139], v[32:35], 0\n"
"s_mov_b32 s59, 55808\n"
"ds_read_b64_tr_b16 a[210:211], v202 offset:13920\n"
"v_mfma_f32_16x16x32_bf16 v[88:91], a[140:143], v[0:3], 0\n"
"ds_read_b64_tr_b16 a[212:213], v202 offset:9344\n"
"v_lshlrev_b32 v246, 2, v254\n"
"v_mfma_f32_16x16x32_bf16 v[92:95], a[140:143], v[32:35], 0\n"
"s_lshl_b32 s54, s44, 2\n"
"ds_read_b64_tr_b16 a[214:215], v202 offset:13952\n"
"v_mfma_f32_16x16x32_bf16 v[96:99], a[144:147], v[0:3], 0\n"
"s_add_u32 s54, s54, s41\n"
"ds_read_b64_tr_b16 a[216:217], v202 offset:9376\n"
"v_mfma_f32_16x16x32_bf16 v[100:103], a[144:147], v[32:35], 0\n"
"s_mul_i32 s54, s54, 36864\n"
"ds_read_b64_tr_b16 a[218:219], v202 offset:13984\n"
"v_mfma_f32_16x16x32_bf16 v[104:107], a[148:151], v[0:3], 0\n"
"s_lshr_b32 s62, s43, 2\n"
"ds_read_b64_tr_b16 a[220:221], v202 offset:9408\n"
"v_mfma_f32_16x16x32_bf16 v[108:111], a[148:151], v[32:35], 0\n"
"s_mul_i32 s62, s62, 2359296\n"
"ds_read_b64_tr_b16 a[222:223], v202 offset:14016\n"
"v_mfma_f32_16x16x32_bf16 v[112:115], a[152:155], v[0:3], 0\n"
"s_add_u32 s54, s54, s62\n"
"ds_read_b64_tr_b16 a[128:129], v202 offset:9440\n"
"v_mfma_f32_16x16x32_bf16 v[116:119], a[152:155], v[32:35], 0\n"
"s_add_u32 s54, s54, 139264\n"
"ds_read_b64_tr_b16 a[130:131], v202 offset:14048\n"
"v_mfma_f32_16x16x32_bf16 v[120:123], a[156:159], v[0:3], 0\n"
"s_bfe_u32 s62, s43, 0x10001\n"
"ds_read_b64_tr_b16 a[132:133], v205 offset:9216\n"
"v_mfma_f32_16x16x32_bf16 v[124:127], a[156:159], v[32:35], 0\n"
"s_mul_i32 s62, s62, 9216\n"
"ds_read_b64_tr_b16 a[134:135], v205 offset:13824\n"
"v_mfma_f32_16x16x32_bf16 v[226:229], a[192:195], v[0:3], 0\n"
"s_add_u32 s54, s54, s62\n"
"ds_read_b64_tr_b16 a[136:137], v202 offset:18432\n"
"v_mfma_f32_16x16x32_bf16 v[230:233], a[192:195], v[32:35], 0\n"
"s_and_b32 s62, s43, 1\n"
"ds_read_b64_tr_b16 a[138:139], v202 offset:23040\n"
"v_mfma_f32_16x16x32_bf16 v[64:67], a[196:199], v[4:7], v[64:67]\n"
"s_lshl_b32 s49, s62, 3\n"
"ds_read_b64_tr_b16 a[140:141], v202 offset:18464\n"
"v_mfma_f32_16x16x32_bf16 v[68:71], a[196:199], v[36:39], v[68:71]\n"
"s_waitcnt lgkmcnt(11)\n"
"s_add_u32 s49, s49, s54\n"
"ds_read_b64_tr_b16 a[142:143], v202 offset:23072\n"
"v_mfma_f32_16x16x32_bf16 v[72:75], a[200:203], v[4:7], v[72:75]\n"
"s_add_u32 s49, s49, 4096\n"
"ds_read_b64_tr_b16 a[144:145], v202 offset:18496\n"
"v_mfma_f32_16x16x32_bf16 v[76:79], a[200:203], v[36:39], v[76:79]\n"
"s_mul_i32 s62, s62, 5120\n"
"ds_read_b64_tr_b16 a[146:147], v202 offset:23104\n"
"v_mfma_f32_16x16x32_bf16 v[80:83], a[204:207], v[4:7], v[80:83]\n"
"s_add_u32 s54, s54, s62\n"
"ds_read_b64_tr_b16 a[148:149], v202 offset:18528\n"
"v_mfma_f32_16x16x32_bf16 v[84:87], a[204:207], v[36:39], v[84:87]\n"
"s_mul_i32 s46, s55, 9216\n"
"ds_read_b64_tr_b16 a[150:151], v202 offset:23136\n"
"v_mfma_f32_16x16x32_bf16 v[88:91], a[208:211], v[4:7], v[88:91]\n"
"s_min_u32 s62, s41, 2\n"
"ds_read_b64_tr_b16 a[152:153], v202 offset:18560\n"
"v_mfma_f32_16x16x32_bf16 v[92:95], a[208:211], v[36:39], v[92:95]\n"
"s_mul_i32 s62, s62, 3072\n"
"ds_read_b64_tr_b16 a[154:155], v202 offset:23168\n"
"v_mfma_f32_16x16x32_bf16 v[96:99], a[212:215], v[4:7], v[96:99]\n"
"s_add_u32 s46, s46, s62\n"
"ds_read_b64_tr_b16 a[156:157], v202 offset:18592\n"
"v_mfma_f32_16x16x32_bf16 v[100:103], a[212:215], v[36:39], v[100:103]\n"
"s_waitcnt lgkmcnt(11)\n"
"s_add_u32 s45, s40, s62\n"
"ds_read_b64_tr_b16 a[158:159], v202 offset:23200\n"
"v_mfma_f32_16x16x32_bf16 v[104:107], a[216:219], v[4:7], v[104:107]\n"
"s_lshr_b32 s47, s55, 1\n"
"ds_read_b64_tr_b16 a[192:193], v202 offset:18624\n"
"v_mfma_f32_16x16x32_bf16 v[108:111], a[216:219], v[36:39], v[108:111]\n"
"s_mul_i32 s47, s47, 18432\n"
"ds_read_b64_tr_b16 a[194:195], v202 offset:23232\n"
"v_mfma_f32_16x16x32_bf16 v[112:115], a[220:223], v[4:7], v[112:115]\n"
"s_and_b32 s62, s55, 1\n"
"ds_read_b64_tr_b16 a[196:197], v202 offset:18656\n"
"v_mfma_f32_16x16x32_bf16 v[116:119], a[220:223], v[36:39], v[116:119]\n"
"s_lshl_b32 s62, s62, 7\n"
"ds_read_b64_tr_b16 a[198:199], v202 offset:23264\n"
"v_mfma_f32_16x16x32_bf16 v[120:123], a[128:131], v[4:7], v[120:123]\n"
"s_add_u32 s47, s47, s62\n"
"ds_read_b64_tr_b16 a[200:201], v205 offset:18432\n"
"v_mfma_f32_16x16x32_bf16 v[124:127], a[128:131], v[36:39], v[124:127]\n"
"s_add_u32 s47, s47, 139264\n"
"ds_read_b64_tr_b16 a[202:203], v205 offset:23040\n"
"v_mfma_f32_16x16x32_bf16 v[226:229], a[132:135], v[4:7], v[226:229]\n"
"s_mul_i32 s48, s55, 73728\n"
"ds_read_b64_tr_b16 a[204:205], v200 offset:0\n"
"v_mfma_f32_16x16x32_bf16 v[230:233], a[132:135], v[36:39], v[230:233]\n"
"s_waitcnt lgkmcnt(11)\n"
"s_add_u32 s48, s48, 37888000\n"
"ds_read_b64_tr_b16 a[206:207], v200 offset:4608\n"
"v_mfma_f32_16x16x32_bf16 v[64:67], a[136:139], v[8:11], v[64:67]\n"
"s_add_u32 s62, s40, 119808\n"
"ds_read_b64_tr_b16 a[208:209], v200 offset:32\n"
"v_mfma_f32_16x16x32_bf16 v[68:71], a[136:139], v[40:43], v[68:71]\n"
"ds_read_b64_tr_b16 a[210:211], v200 offset:4640\n"
"v_and_b32 v206, 7, v248\n"
"v_mfma_f32_16x16x32_bf16 v[72:75], a[140:143], v[8:11], v[72:75]\n"
"ds_read_b64_tr_b16 a[212:213], v200 offset:64\n"
"v_lshrrev_b32 v255, 5, v253\n"
"v_mfma_f32_16x16x32_bf16 v[76:79], a[140:143], v[40:43], v[76:79]\n"
"ds_read_b64_tr_b16 a[214:215], v200 offset:4672\n"
"v_bfe_u32 v216, v253, 3, 2\n"
"v_mfma_f32_16x16x32_bf16 v[80:83], a[144:147], v[8:11], v[80:83]\n"
"ds_read_b64_tr_b16 a[216:217], v200 offset:96\n"
"v_lshlrev_b32 v241, 10, v255\n"
"v_mfma_f32_16x16x32_bf16 v[84:87], a[144:147], v[40:43], v[84:87]\n"
"ds_read_b64_tr_b16 a[218:219], v200 offset:4704\n"
"v_lshl_add_u32 v241, v216, 8, v241\n"
"v_mfma_f32_16x16x32_bf16 v[88:91], a[148:151], v[8:11], v[88:91]\n"
"ds_read_b64_tr_b16 a[220:221], v200 offset:128\n"
"v_lshl_add_u32 v241, v206, 4, v241\n"
"v_mfma_f32_16x16x32_bf16 v[92:95], a[148:151], v[40:43], v[92:95]\n"
"s_waitcnt lgkmcnt(11)\n"
"ds_read_b64_tr_b16 a[222:223], v200 offset:4736\n"
"v_add_u32 v242, 6144, v241\n"
"v_mfma_f32_16x16x32_bf16 v[96:99], a[152:155], v[8:11], v[96:99]\n"
"ds_read_b64_tr_b16 a[128:129], v200 offset:160\n"
"v_add_u32 v243, 12288, v241\n"
"v_mfma_f32_16x16x32_bf16 v[100:103], a[152:155], v[40:43], v[100:103]\n"
"ds_read_b64_tr_b16 a[130:131], v200 offset:4768\n"
"v_lshlrev_b32 v255, 5, v255\n"
"v_mfma_f32_16x16x32_bf16 v[104:107], a[156:159], v[8:11], v[104:107]\n"
"ds_read_b64_tr_b16 a[132:133], v200 offset:192\n"
"v_lshl_add_u32 v255, v216, 2, v255\n"
"v_mfma_f32_16x16x32_bf16 v[108:111], a[156:159], v[40:43], v[108:111]\n"
"ds_read_b64_tr_b16 a[134:135], v200 offset:4800\n"
"v_lshl_add_u32 v244, v255, 2, s62\n"
"v_mfma_f32_16x16x32_bf16 v[112:115], a[192:195], v[8:11], v[112:115]\n"
"ds_read_b64_tr_b16 a[136:137], v200 offset:224\n"
"v_mul_u32_u24 v206, 1152, v206\n"
"v_mfma_f32_16x16x32_bf16 v[116:119], a[192:195], v[40:43], v[116:119]\n"
"ds_read_b64_tr_b16 a[138:139], v200 offset:4832\n"
"v_lshl_add_u32 v240, v255, 1, v206\n"
"v_mfma_f32_16x16x32_bf16 v[120:123], a[196:199], v[8:11], v[120:123]\n"
"ds_read_b64_tr_b16 a[140:141], v203 offset:0\n"
"v_add_u32 v240, s40, v240\n"
"v_mfma_f32_16x16x32_bf16 v[124:127], a[196:199], v[40:43], v[124:127]\n"
"s_waitcnt lgkmcnt(11)\n"
"ds_read_b64_tr_b16 a[142:143], v203 offset:4608\n"
"v_add_u32 v245, v206, v255\n"
"v_mfma_f32_16x16x32_bf16 v[226:229], a[200:203], v[8:11], v[226:229]\n"
"ds_read_b64_tr_b16 a[144:145], v200 offset:9216\n"
"v_lshlrev_b32 v245, 3, v245\n"
"v_mfma_f32_16x16x32_bf16 v[230:233], a[200:203], v[40:43], v[230:233]\n"
"ds_read_b64_tr_b16 a[146:147], v200 offset:13824\n"
"v_lshl_add_u32 v247, v253, 4, s62\n"
"v_mfma_f32_16x16x32_bf16 v[64:67], a[204:207], v[12:15], v[64:67]\n"
"s_mul_i32 s62, s41, 7\n"
"ds_read_b64_tr_b16 a[148:149], v200 offset:9248\n"
"v_mfma_f32_16x16x32_bf16 v[68:71], a[204:207], v[44:47], v[68:71]\n"
"s_min_u32 s62, s62, 20\n"
"ds_read_b64_tr_b16 a[150:151], v200 offset:13856\n"
"v_mfma_f32_16x16x32_bf16 v[72:75], a[208:211], v[12:15], v[72:75]\n"
"s_lshl_b32 s62, s62, 10\n"
"ds_read_b64_tr_b16 a[152:153], v200 offset:9280\n"
"v_mfma_f32_16x16x32_bf16 v[76:79], a[208:211], v[44:47], v[76:79]\n"
"s_add_u32 s50, s50, s62\n"
"ds_read_b64_tr_b16 a[154:155], v200 offset:13888\n"
"v_mfma_f32_16x16x32_bf16 v[80:83], a[212:215], v[12:15], v[80:83]\n"
"s_add_u32 s84, s40, s62\n"
"ds_read_b64_tr_b16 a[156:157], v200 offset:9312\n"
"v_mfma_f32_16x16x32_bf16 v[84:87], a[212:215], v[44:47], v[84:87]\n"
"s_waitcnt lgkmcnt(11)\n"
"ds_read_b64_tr_b16 a[158:159], v200 offset:13920\n"
"v_mfma_f32_16x16x32_bf16 v[88:91], a[216:219], v[12:15], v[88:91]\n"
"ds_read_b64_tr_b16 a[192:193], v200 offset:9344\n"
"v_mfma_f32_16x16x32_bf16 v[92:95], a[216:219], v[44:47], v[92:95]\n"
"ds_read_b64_tr_b16 a[194:195], v200 offset:13952\n"
"v_mfma_f32_16x16x32_bf16 v[96:99], a[220:223], v[12:15], v[96:99]\n"
"ds_read_b64_tr_b16 a[196:197], v200 offset:9376\n"
"v_mfma_f32_16x16x32_bf16 v[100:103], a[220:223], v[44:47], v[100:103]\n"
"ds_read_b64_tr_b16 a[198:199], v200 offset:13984\n"
"v_mfma_f32_16x16x32_bf16 v[104:107], a[128:131], v[12:15], v[104:107]\n"
"ds_read_b64_tr_b16 a[200:201], v200 offset:9408\n"
"v_mfma_f32_16x16x32_bf16 v[108:111], a[128:131], v[44:47], v[108:111]\n"
"ds_read_b64_tr_b16 a[202:203], v200 offset:14016\n"
"v_mfma_f32_16x16x32_bf16 v[112:115], a[132:135], v[12:15], v[112:115]\n"
"ds_read_b64_tr_b16 a[204:205], v200 offset:9440\n"
"v_mfma_f32_16x16x32_bf16 v[116:119], a[132:135], v[44:47], v[116:119]\n"
"s_waitcnt lgkmcnt(11)\n"
"ds_read_b64_tr_b16 a[206:207], v200 offset:14048\n"
"v_mfma_f32_16x16x32_bf16 v[120:123], a[136:139], v[12:15], v[120:123]\n"
"ds_read_b64_tr_b16 a[208:209], v203 offset:9216\n"
"v_mfma_f32_16x16x32_bf16 v[124:127], a[136:139], v[44:47], v[124:127]\n"
"ds_read_b64_tr_b16 a[210:211], v203 offset:13824\n"
"v_mfma_f32_16x16x32_bf16 v[226:229], a[140:143], v[12:15], v[226:229]\n"
"ds_read_b64_tr_b16 a[212:213], v200 offset:18432\n"
"v_mfma_f32_16x16x32_bf16 v[230:233], a[140:143], v[44:47], v[230:233]\n"
"ds_read_b64_tr_b16 a[214:215], v200 offset:23040\n"
"v_mfma_f32_16x16x32_bf16 v[64:67], a[144:147], v[16:19], v[64:67]\n"
"ds_read_b64_tr_b16 a[216:217], v200 offset:18464\n"
"v_mfma_f32_16x16x32_bf16 v[68:71], a[144:147], v[48:51], v[68:71]\n"
"s_waitcnt vmcnt(0)\n"
"ds_read_b64_tr_b16 a[218:219], v200 offset:23072\n"
"v_mfma_f32_16x16x32_bf16 v[72:75], a[148:151], v[16:19], v[72:75]\n"
"s_barrier\n"
"ds_read_b64_tr_b16 a[220:221], v200 offset:18496\n"
"buffer_store_dword v252, v251, s[28:31], s23 offen sc1\n"
"v_mfma_f32_16x16x32_bf16 v[76:79], a[148:151], v[48:51], v[76:79]\n"
"s_waitcnt lgkmcnt(11)\n"
"ds_read_b64_tr_b16 a[222:223], v200 offset:23104\n"
"buffer_load_dwordx4 a[160:163], v246, s[28:31], s56 offen offset:0 sc1\n"
"v_mfma_f32_16x16x32_bf16 v[80:83], a[152:155], v[16:19], v[80:83]\n"
"s_mov_b32 m0, s51\n"
"ds_read_b64_tr_b16 a[128:129], v200 offset:18528\n"
"v_mfma_f32_16x16x32_bf16 v[84:87], a[152:155], v[48:51], v[84:87]\n"
"ds_read_b64_tr_b16 a[130:131], v200 offset:23136\n"
"buffer_load_dwordx4 v210, s[24:27], s36 offen offset:0 lds\n"
"v_mfma_f32_16x16x32_bf16 v[88:91], a[156:159], v[16:19], v[88:91]\n"
"ds_read_b64_tr_b16 a[132:133], v200 offset:18560\n"
"v_mfma_f32_16x16x32_bf16 v[92:95], a[156:159], v[48:51], v[92:95]\n"
"ds_read_b64_tr_b16 a[134:135], v200 offset:23168\n"
"v_mfma_f32_16x16x32_bf16 v[96:99], a[192:195], v[16:19], v[96:99]\n"
"ds_read_b64_tr_b16 a[136:137], v200 offset:18592\n"
"v_mfma_f32_16x16x32_bf16 v[100:103], a[192:195], v[48:51], v[100:103]\n"
"ds_read_b64_tr_b16 a[138:139], v200 offset:23200\n"
"buffer_load_dwordx4 a[164:167], v246, s[28:31], s56 offen offset:16 sc1\n"
"v_mfma_f32_16x16x32_bf16 v[104:107], a[196:199], v[16:19], v[104:107]\n"
"ds_read_b64_tr_b16 a[140:141], v200 offset:18624\n"
"v_mfma_f32_16x16x32_bf16 v[108:111], a[196:199], v[48:51], v[108:111]\n"
"s_waitcnt lgkmcnt(11)\n"
"s_mov_b32 m0, s51\n"
"ds_read_b64_tr_b16 a[142:143], v200 offset:23232\n"
"v_mfma_f32_16x16x32_bf16 v[112:115], a[200:203], v[16:19], v[112:115]\n"
"ds_read_b64_tr_b16 a[144:145], v200 offset:18656\n"
"buffer_load_dwordx4 v211, s[24:27], s36 offen offset:1024 lds\n"
"v_mfma_f32_16x16x32_bf16 v[116:119], a[200:203], v[48:51], v[116:119]\n"
"ds_read_b64_tr_b16 a[146:147], v200 offset:23264\n"
"v_mfma_f32_16x16x32_bf16 v[120:123], a[204:207], v[16:19], v[120:123]\n"
"ds_read_b64_tr_b16 a[148:149], v203 offset:18432\n"
"v_mfma_f32_16x16x32_bf16 v[124:127], a[204:207], v[48:51], v[124:127]\n"
"ds_read_b64_tr_b16 a[150:151], v203 offset:23040\n"
"buffer_load_dwordx4 a[168:171], v246, s[28:31], s57 offen offset:0 sc1\n"
"v_mfma_f32_16x16x32_bf16 v[226:229], a[208:211], v[16:19], v[226:229]\n"
"ds_read_b64_tr_b16 a[152:153], v200 offset:27648\n"
"v_mfma_f32_16x16x32_bf16 v[230:233], a[208:211], v[48:51], v[230:233]\n"
"ds_read_b64_tr_b16 a[154:155], v200 offset:32256\n"
"v_mfma_f32_16x16x32_bf16 v[64:67], a[212:215], v[20:23], v[64:67]\n"
"s_mov_b32 m0, s51\n"
"ds_read_b64_tr_b16 a[156:157], v200 offset:27680\n"
"v_mfma_f32_16x16x32_bf16 v[68:71], a[212:215], v[52:55], v[68:71]\n"
"s_waitcnt lgkmcnt(11)\n"
"ds_read_b64_tr_b16 a[158:159], v200 offset:32288\n"
"buffer_load_dwordx4 v212, s[24:27], s36 offen offset:2048 lds\n"
"v_mfma_f32_16x16x32_bf16 v[72:75], a[216:219], v[20:23], v[72:75]\n"
"ds_read_b64_tr_b16 a[192:193], v200 offset:27712\n"
"v_mfma_f32_16x16x32_bf16 v[76:79], a[216:219], v[52:55], v[76:79]\n"
"ds_read_b64_tr_b16 a[194:195], v200 offset:32320\n"
"buffer_load_dwordx4 a[172:175], v246, s[28:31], s57 offen offset:16 sc1\n"
"v_mfma_f32_16x16x32_bf16 v[80:83], a[220:223], v[20:23], v[80:83]\n"
"ds_read_b64_tr_b16 a[196:197], v200 offset:27744\n"
"v_mfma_f32_16x16x32_bf16 v[84:87], a[220:223], v[52:55], v[84:87]\n"
"ds_read_b64_tr_b16 a[198:199], v200 offset:32352\n"
"v_mfma_f32_16x16x32_bf16 v[88:91], a[128:131], v[20:23], v[88:91]\n"
"ds_read_b64_tr_b16 a[200:201], v200 offset:27776\n"
"v_mfma_f32_16x16x32_bf16 v[92:95], a[128:131], v[52:55], v[92:95]\n"
"s_mov_b32 m0, s51\n"
"ds_read_b64_tr_b16 a[202:203], v200 offset:32384\n"
"v_mfma_f32_16x16x32_bf16 v[96:99], a[132:135], v[20:23], v[96:99]\n"
"ds_read_b64_tr_b16 a[204:205], v200 offset:27808\n"
"buffer_load_dwordx4 v213, s[24:27], s36 offen offset:3072 lds\n"
"v_mfma_f32_16x16x32_bf16 v[100:103], a[132:135], v[52:55], v[100:103]\n"
"s_waitcnt lgkmcnt(11)\n"
"ds_read_b64_tr_b16 a[206:207], v200 offset:32416\n"
"buffer_load_dwordx4 a[176:179], v246, s[28:31], s58 offen offset:0 sc1\n"
"v_mfma_f32_16x16x32_bf16 v[104:107], a[136:139], v[20:23], v[104:107]\n"
"ds_read_b64_tr_b16 a[208:209], v200 offset:27840\n"
"v_mfma_f32_16x16x32_bf16 v[108:111], a[136:139], v[52:55], v[108:111]\n"
"ds_read_b64_tr_b16 a[210:211], v200 offset:32448\n"
"v_mfma_f32_16x16x32_bf16 v[112:115], a[140:143], v[20:23], v[112:115]\n"
"ds_read_b64_tr_b16 a[212:213], v200 offset:27872\n"
"v_mfma_f32_16x16x32_bf16 v[116:119], a[140:143], v[52:55], v[116:119]\n"
"ds_read_b64_tr_b16 a[214:215], v200 offset:32480\n"
"v_mfma_f32_16x16x32_bf16 v[120:123], a[144:147], v[20:23], v[120:123]\n"
"s_add_u32 m0, s51, 4096\n"
"ds_read_b64_tr_b16 a[216:217], v203 offset:27648\n"
"v_mfma_f32_16x16x32_bf16 v[124:127], a[144:147], v[52:55], v[124:127]\n"
"ds_read_b64_tr_b16 a[218:219], v203 offset:32256\n"
"buffer_load_dwordx4 v214, s[24:27], s36 offen offset:0 lds\n"
"v_mfma_f32_16x16x32_bf16 v[226:229], a[148:151], v[20:23], v[226:229]\n"
"ds_read_b64_tr_b16 a[220:221], v200 offset:36864\n"
"buffer_load_dwordx4 a[180:183], v246, s[28:31], s58 offen offset:16 sc1\n"
"v_mfma_f32_16x16x32_bf16 v[230:233], a[148:151], v[52:55], v[230:233]\n"
"s_waitcnt lgkmcnt(11)\n"
"ds_read_b64_tr_b16 a[222:223], v200 offset:41472\n"
"v_mfma_f32_16x16x32_bf16 v[64:67], a[152:155], v[24:27], v[64:67]\n"
"ds_read_b64_tr_b16 a[128:129], v200 offset:36896\n"
"v_mfma_f32_16x16x32_bf16 v[68:71], a[152:155], v[56:59], v[68:71]\n"
"ds_read_b64_tr_b16 a[130:131], v200 offset:41504\n"
"v_mfma_f32_16x16x32_bf16 v[72:75], a[156:159], v[24:27], v[72:75]\n"
"ds_read_b64_tr_b16 a[132:133], v200 offset:36928\n"
"v_mfma_f32_16x16x32_bf16 v[76:79], a[156:159], v[56:59], v[76:79]\n"
"s_add_u32 m0, s51, 4096\n"
"ds_read_b64_tr_b16 a[134:135], v200 offset:41536\n"
"v_mfma_f32_16x16x32_bf16 v[80:83], a[192:195], v[24:27], v[80:83]\n"
"ds_read_b64_tr_b16 a[136:137], v200 offset:36960\n"
"buffer_load_dwordx4 v236, s[24:27], s36 offen offset:1024 lds\n"
"v_mfma_f32_16x16x32_bf16 v[84:87], a[192:195], v[56:59], v[84:87]\n"
"ds_read_b64_tr_b16 a[138:139], v200 offset:41568\n"
"buffer_load_dwordx4 a[184:187], v246, s[28:31], s59 offen offset:0 sc1\n"
"v_mfma_f32_16x16x32_bf16 v[88:91], a[196:199], v[24:27], v[88:91]\n"
"ds_read_b64_tr_b16 a[140:141], v200 offset:36992\n"
"v_mfma_f32_16x16x32_bf16 v[92:95], a[196:199], v[56:59], v[92:95]\n"
"s_waitcnt lgkmcnt(11)\n"
"ds_read_b64_tr_b16 a[142:143], v200 offset:41600\n"
"v_mfma_f32_16x16x32_bf16 v[96:99], a[200:203], v[24:27], v[96:99]\n"
"ds_read_b64_tr_b16 a[144:145], v200 offset:37024\n"
"v_mfma_f32_16x16x32_bf16 v[100:103], a[200:203], v[56:59], v[100:103]\n"
"ds_read_b64_tr_b16 a[146:147], v200 offset:41632\n"
"v_mfma_f32_16x16x32_bf16 v[104:107], a[204:207], v[24:27], v[104:107]\n"
"s_add_u32 m0, s51, 4096\n"
"ds_read_b64_tr_b16 a[148:149], v200 offset:37056\n"
"v_mfma_f32_16x16x32_bf16 v[108:111], a[204:207], v[56:59], v[108:111]\n"
"ds_read_b64_tr_b16 a[150:151], v200 offset:41664\n"
"buffer_load_dwordx4 v237, s[24:27], s36 offen offset:2048 lds\n"
"v_mfma_f32_16x16x32_bf16 v[112:115], a[208:211], v[24:27], v[112:115]\n"
"ds_read_b64_tr_b16 a[152:153], v200 offset:37088\n"
"buffer_load_dwordx4 a[188:191], v246, s[28:31], s59 offen offset:16 sc1\n"
"v_mfma_f32_16x16x32_bf16 v[116:119], a[208:211], v[56:59], v[116:119]\n"
"ds_read_b64_tr_b16 a[154:155], v200 offset:41696\n"
"v_mfma_f32_16x16x32_bf16 v[120:123], a[212:215], v[24:27], v[120:123]\n"
"ds_read_b64_tr_b16 a[156:157], v203 offset:36864\n"
"v_mfma_f32_16x16x32_bf16 v[124:127], a[212:215], v[56:59], v[124:127]\n"
"s_waitcnt lgkmcnt(11)\n"
"ds_read_b64_tr_b16 a[158:159], v203 offset:41472\n"
"v_mfma_f32_16x16x32_bf16 v[226:229], a[216:219], v[24:27], v[226:229]\n"
"ds_read_b64_tr_b16 a[192:193], v200 offset:46080\n"
"v_mfma_f32_16x16x32_bf16 v[230:233], a[216:219], v[56:59], v[230:233]\n"
"s_add_u32 m0, s51, 4096\n"
"ds_read_b64_tr_b16 a[194:195], v200 offset:50688\n"
"v_mfma_f32_16x16x32_bf16 v[64:67], a[220:223], v[28:31], v[64:67]\n"
"ds_read_b64_tr_b16 a[196:197], v200 offset:46112\n"
"buffer_load_dwordx4 v238, s[24:27], s36 offen offset:3072 lds\n"
"v_mfma_f32_16x16x32_bf16 v[68:71], a[220:223], v[60:63], v[68:71]\n"
"ds_read_b64_tr_b16 a[198:199], v200 offset:50720\n"
"buffer_load_dwordx2 a[224:225], v254, s[80:83], 0 offen\n"
"v_mfma_f32_16x16x32_bf16 v[72:75], a[128:131], v[28:31], v[72:75]\n"
"ds_read_b64_tr_b16 a[200:201], v200 offset:46144\n"
"v_mfma_f32_16x16x32_bf16 v[76:79], a[128:131], v[60:63], v[76:79]\n"
"ds_read_b64_tr_b16 a[202:203], v200 offset:50752\n"
"v_mfma_f32_16x16x32_bf16 v[80:83], a[132:135], v[28:31], v[80:83]\n"
"ds_read_b64_tr_b16 a[204:205], v200 offset:46176\n"
"v_mfma_f32_16x16x32_bf16 v[84:87], a[132:135], v[60:63], v[84:87]\n"
"s_waitcnt lgkmcnt(11)\n"
"ds_read_b64_tr_b16 a[206:207], v200 offset:50784\n"
"v_mfma_f32_16x16x32_bf16 v[88:91], a[136:139], v[28:31], v[88:91]\n"
"s_add_u32 m0, s51, 8192\n"
"ds_read_b64_tr_b16 a[208:209], v200 offset:46208\n"
"v_mfma_f32_16x16x32_bf16 v[92:95], a[136:139], v[60:63], v[92:95]\n"
"ds_read_b64_tr_b16 a[210:211], v200 offset:50816\n"
"buffer_load_dwordx4 v239, s[24:27], s36 offen offset:0 lds\n"
"v_mfma_f32_16x16x32_bf16 v[96:99], a[140:143], v[28:31], v[96:99]\n"
"ds_read_b64_tr_b16 a[212:213], v200 offset:46240\n"
"v_mfma_f32_16x16x32_bf16 v[100:103], a[140:143], v[60:63], v[100:103]\n"
"ds_read_b64_tr_b16 a[214:215], v200 offset:50848\n"
"v_mfma_f32_16x16x32_bf16 v[104:107], a[144:147], v[28:31], v[104:107]\n"
"ds_read_b64_tr_b16 a[216:217], v200 offset:46272\n"
"v_mfma_f32_16x16x32_bf16 v[108:111], a[144:147], v[60:63], v[108:111]\n"
"ds_read_b64_tr_b16 a[218:219], v200 offset:50880\n"
"v_mfma_f32_16x16x32_bf16 v[112:115], a[148:151], v[28:31], v[112:115]\n"
"ds_read_b64_tr_b16 a[220:221], v200 offset:46304\n"
"v_mfma_f32_16x16x32_bf16 v[116:119], a[148:151], v[60:63], v[116:119]\n"
"s_waitcnt lgkmcnt(11)\n"
"ds_read_b64_tr_b16 a[222:223], v200 offset:50912\n"
"v_mfma_f32_16x16x32_bf16 v[120:123], a[152:155], v[28:31], v[120:123]\n"
"ds_read_b64_tr_b16 a[128:129], v203 offset:46080\n"
"buffer_load_dword v249, v215, s[28:31], s23 offen sc1\n"
"v_mfma_f32_16x16x32_bf16 v[124:127], a[152:155], v[60:63], v[124:127]\n"
"ds_read_b64_tr_b16 a[130:131], v203 offset:50688\n"
"v_mfma_f32_16x16x32_bf16 v[226:229], a[156:159], v[28:31], v[226:229]\n"
"ds_read_b64_tr_b16 a[132:133], v200 offset:55296\n"
"v_mfma_f32_16x16x32_bf16 v[230:233], a[156:159], v[60:63], v[230:233]\n"
"ds_read_b64_tr_b16 a[134:135], v200 offset:59904\n"
"s_barrier\n"
"s_waitcnt vmcnt(2)\n"
"s_mov_b32 s87, 0x80000\n"
"v_accvgpr_read_b32 v26, a160\n"
"v_accvgpr_read_b32 v27, a161\n"
"v_accvgpr_read_b32 v28, a162\n"
"v_accvgpr_read_b32 v29, a163\n"
"v_accvgpr_read_b32 v30, a164\n"
"v_accvgpr_read_b32 v31, a165\n"
"v_accvgpr_read_b32 v32, a166\n"
"v_accvgpr_read_b32 v33, a167\n"
"v_accvgpr_read_b32 v34, a168\n"
"v_accvgpr_read_b32 v35, a169\n"
"v_accvgpr_read_b32 v36, a170\n"
"v_accvgpr_read_b32 v37, a171\n"
"v_accvgpr_read_b32 v38, a172\n"
"v_accvgpr_read_b32 v39, a173\n"
"v_accvgpr_read_b32 v40, a174\n"
"v_accvgpr_read_b32 v41, a175\n"
"v_accvgpr_read_b32 v42, a176\n"
"v_accvgpr_read_b32 v43, a177\n"
"v_accvgpr_read_b32 v44, a178\n"
"v_accvgpr_read_b32 v45, a179\n"
"v_accvgpr_read_b32 v46, a180\n"
"v_accvgpr_read_b32 v47, a181\n"
"v_accvgpr_read_b32 v48, a182\n"
"v_accvgpr_read_b32 v49, a183\n"
"v_accvgpr_read_b32 v50, a184\n"
"v_accvgpr_read_b32 v51, a185\n"
"v_accvgpr_read_b32 v52, a186\n"
"v_accvgpr_read_b32 v53, a187\n"
"v_accvgpr_read_b32 v54, a188\n"
"v_accvgpr_read_b32 v55, a189\n"
"v_accvgpr_read_b32 v56, a190\n"
"v_accvgpr_read_b32 v57, a191\n"
"v_accvgpr_read_b32 v58, a224\n"
"v_accvgpr_read_b32 v59, a225\n"
"AT_tchk:\n"
"v_xor_b32 v24, s85, v27\n"
"v_xor_b32 v25, s85, v29\n"
"v_bitop3_b32 v24, v31, s85, v24 bitop3:0xbe\n"
"v_bitop3_b32 v25, v33, s85, v25 bitop3:0xbe\n"
"v_bitop3_b32 v24, v35, s85, v24 bitop3:0xbe\n"
"v_bitop3_b32 v25, v37, s85, v25 bitop3:0xbe\n"
"v_bitop3_b32 v24, v39, s85, v24 bitop3:0xbe\n"
"v_bitop3_b32 v25, v41, s85, v25 bitop3:0xbe\n"
"v_bitop3_b32 v24, v43, s85, v24 bitop3:0xbe\n"
"v_bitop3_b32 v25, v45, s85, v25 bitop3:0xbe\n"
"v_bitop3_b32 v24, v47, s85, v24 bitop3:0xbe\n"
"v_bitop3_b32 v25, v49, s85, v25 bitop3:0xbe\n"
"v_bitop3_b32 v24, v51, s85, v24 bitop3:0xbe\n"
"v_bitop3_b32 v25, v53, s85, v25 bitop3:0xbe\n"
"v_bitop3_b32 v24, v55, s85, v24 bitop3:0xbe\n"
"v_bitop3_b32 v25, v57, s85, v25 bitop3:0xbe\n"
"v_or_b32 v24, v24, v25\n"
"v_cmp_ne_u32 s[62:63], 0, v24\n"
"v_cmp_gt_u32 vcc, 144, v253\n"
"s_nop 1\n"
"s_and_b64 s[62:63], vcc, s[62:63]\n"
"s_cmp_eq_u64 s[62:63], 0\n"
"s_cbranch_scc1 AT_tok\n"
"s_sub_u32 s87, s87, 1\n"
"s_cmp_eq_u32 s87, 0\n"
"s_cbranch_scc1 AT_tok\n"
"s_sleep 2\n"
"buffer_load_dwordx4 v[26:29], v246, s[28:31], s56 offen offset:0 sc1\n"
"buffer_load_dwordx4 v[30:33], v246, s[28:31], s56 offen offset:16 sc1\n"
"buffer_load_dwordx4 v[34:37], v246, s[28:31], s57 offen offset:0 sc1\n"
"buffer_load_dwordx4 v[38:41], v246, s[28:31], s57 offen offset:16 sc1\n"
"buffer_load_dwordx4 v[42:45], v246, s[28:31], s58 offen offset:0 sc1\n"
"buffer_load_dwordx4 v[46:49], v246, s[28:31], s58 offen offset:16 sc1\n"
"buffer_load_dwordx4 v[50:53], v246, s[28:31], s59 offen offset:0 sc1\n"
"buffer_load_dwordx4 v[54:57], v246, s[28:31], s59 offen offset:16 sc1\n"
"s_waitcnt vmcnt(0)\n"
"s_branch AT_tchk\n"
"AT_tok:\n"
"s_waitcnt vmcnt(0)\n"
"v_cmp_ne_u32 vcc, s85, v249\n"
"s_nop 0\n"
"s_cbranch_vccnz AT_x1\n"
"AT_x2:\n"
"buffer_load_dwordx4 a[0:3], v207, s[28:31], s20 offen offset:0 sc1\n"
"buffer_load_dwordx4 a[4:7], v207, s[28:31], s20 offen offset:1024 sc1\n"
"buffer_load_dwordx4 a[8:11], v207, s[28:31], s21 offen offset:0 sc1\n"
"buffer_load_dwordx4 a[12:15], v207, s[28:31], s21 offen offset:1024 sc1\n"
"buffer_load_dwordx4 a[16:19], v207, s[28:31], s22 offen offset:0 sc1\n"
"buffer_load_dwordx4 a[20:23], v207, s[28:31], s22 offen offset:1024 sc1\n"
"buffer_load_dwordx4 a[24:27], v207, s[28:31], s20 offen offset:2048 sc1\n"
"buffer_load_dwordx4 a[28:31], v207, s[28:31], s20 offen offset:3072 sc1\n"
"s_add_u32 s20, s20, 4096\n"
"v_cmp_gt_u32 vcc, 144, v253\n"
"v_lshlrev_b32 v0, 16, v58\n"
"v_and_b32 v1, 0xffff0000, v58\n"
"v_lshlrev_b32 v2, 16, v59\n"
"v_and_b32 v3, 0xffff0000, v59\n"
"v_mov_b32 v4, v26\n"
"v_add_f32 v12, 1.0, v42\n"
"v_mov_b32 v16, v34\n"
"v_mov_b32 v20, v50\n"
"v_mul_f32 v8, v12, v0\n"
"v_mov_b32 v5, v28\n"
"v_add_f32 v13, 1.0, v44\n"
"v_mov_b32 v17, v36\n"
"v_mov_b32 v21, v52\n"
"v_mul_f32 v9, v13, v1\n"
"v_mov_b32 v6, v30\n"
"v_add_f32 v14, 1.0, v46\n"
"v_mov_b32 v18, v38\n"
"v_mov_b32 v22, v54\n"
"v_mul_f32 v10, v14, v2\n"
"v_mov_b32 v7, v32\n"
"v_add_f32 v15, 1.0, v48\n"
"v_mov_b32 v19, v40\n"
"v_mov_b32 v23, v56\n"
"v_mul_f32 v11, v15, v3\n"
"s_mov_b64 exec, vcc\n"
"ds_write_b128 v247, v[4:7] offset:0\n"
"ds_write_b128 v247, v[8:11] offset:2304\n"
"ds_write_b128 v247, v[12:15] offset:4608\n"
"ds_write_b128 v247, v[16:19] offset:6912\n"
"ds_write_b128 v247, v[20:23] offset:38912\n"
"s_mov_b64 exec, -1\n"
"v_add_f32 v24, v8, v9\n"
"v_mul_f32 v25, v16, v0\n"
"v_add_f32 v24, v24, v10\n"
"v_fmac_f32 v25, v17, v1\n"
"v_add_f32 v24, v24, v11\n"
"v_fmac_f32 v25, v18, v2\n"
"s_nop 0\n"
"v_fmac_f32 v25, v19, v3\n"
"s_nop 1\n"
"v_add_f32_dpp v24, v24, v24 row_ror:1 row_mask:0xf bank_mask:0xf\n"
"v_add_f32_dpp v25, v25, v25 row_ror:1 row_mask:0xf bank_mask:0xf\n"
"s_nop 0\n"
"v_add_f32_dpp v24, v24, v24 row_ror:2 row_mask:0xf bank_mask:0xf\n"
"v_add_f32_dpp v25, v25, v25 row_ror:2 row_mask:0xf bank_mask:0xf\n"
"s_nop 0\n"
"v_add_f32_dpp v24, v24, v24 row_ror:4 row_mask:0xf bank_mask:0xf\n"
"v_add_f32_dpp v25, v25, v25 row_ror:4 row_mask:0xf bank_mask:0xf\n"
"s_nop 0\n"
"v_add_f32_dpp v24, v24, v24 row_ror:8 row_mask:0xf bank_mask:0xf\n"
"v_add_f32_dpp v25, v25, v25 row_ror:8 row_mask:0xf bank_mask:0xf\n"
"s_nop 0\n"
"v_mov_b32 v26, v24\n"
"v_mov_b32 v27, v25\n"
"s_nop 0\n"
"v_permlane16_swap_b32 v24, v26\n"
"v_permlane16_swap_b32 v25, v27\n"
"v_add_f32 v24, v24, v26\n"
"v_add_f32 v25, v25, v27\n"
"v_mov_b32 v26, v24\n"
"v_mov_b32 v27, v25\n"
"s_nop 0\n"
"v_permlane32_swap_b32 v24, v26\n"
"v_permlane32_swap_b32 v25, v27\n"
"v_add_f32 v24, v24, v26\n"
"v_add_f32 v25, v25, v27\n"
"s_lshl_b32 s62, s41, 3\n"
"s_add_u32 s62, s62, s40\n"
"v_mov_b32 v26, s62\n"
"v_add_u32 v26, 161536, v26\n"
"s_mov_b64 exec, 1\n"
"ds_write_b64 v26, v[24:25]\n"
"s_mov_b64 exec, -1\n"
"s_waitcnt vmcnt(6)\n"
"v_mfma_f32_16x16x32_bf16 v[64:67], a[192:195], a[0:3], v[64:67]\n"
"ds_read_b64_tr_b16 a[136:137], v200 offset:55328\n"
"buffer_load_dwordx4 a[32:35], v207, s[28:31], s21 offen offset:2048 sc1\n"
"v_mfma_f32_16x16x32_bf16 v[68:71], a[192:195], a[4:7], v[68:71]\n"
"s_add_u32 m0, s53, 36864\n"
"ds_read_b64_tr_b16 a[138:139], v200 offset:59936\n"
"v_mfma_f32_16x16x32_bf16 v[72:75], a[196:199], a[0:3], v[72:75]\n"
"ds_read_b64_tr_b16 a[140:141], v200 offset:55360\n"
"buffer_load_dwordx4 v210, s[24:27], s37 offen offset:0 lds\n"
"v_mfma_f32_16x16x32_bf16 v[76:79], a[196:199], a[4:7], v[76:79]\n"
"s_waitcnt lgkmcnt(14)\n"
"ds_read_b64_tr_b16 a[142:143], v200 offset:59968\n"
"v_mfma_f32_16x16x32_bf16 v[80:83], a[200:203], a[0:3], v[80:83]\n"
"ds_read_b64_tr_b16 a[144:145], v200 offset:55392\n"
"v_mfma_f32_16x16x32_bf16 v[84:87], a[200:203], a[4:7], v[84:87]\n"
"ds_read_b64_tr_b16 a[146:147], v200 offset:60000\n"
"buffer_load_dwordx4 a[36:39], v207, s[28:31], s21 offen offset:3072 sc1\n"
"v_mfma_f32_16x16x32_bf16 v[88:91], a[204:207], a[0:3], v[88:91]\n"
"s_add_u32 s21, s21, 4096\n"
"ds_read_b64_tr_b16 a[148:149], v200 offset:55424\n"
"v_mfma_f32_16x16x32_bf16 v[92:95], a[204:207], a[4:7], v[92:95]\n"
"ds_read_b64_tr_b16 a[150:151], v200 offset:60032\n"
"v_mfma_f32_16x16x32_bf16 v[96:99], a[208:211], a[0:3], v[96:99]\n"
"s_add_u32 m0, s53, 36864\n"
"ds_read_b64_tr_b16 a[152:153], v200 offset:55456\n"
"v_mfma_f32_16x16x32_bf16 v[100:103], a[208:211], a[4:7], v[100:103]\n"
"ds_read_b64_tr_b16 a[154:155], v200 offset:60064\n"
"buffer_load_dwordx4 v211, s[24:27], s37 offen offset:1024 lds\n"
"v_mfma_f32_16x16x32_bf16 v[104:107], a[212:215], a[0:3], v[104:107]\n"
"ds_read_b64_tr_b16 a[156:157], v200 offset:55488\n"
"buffer_load_dwordx4 a[40:43], v207, s[28:31], s22 offen offset:2048 sc1\n"
"v_mfma_f32_16x16x32_bf16 v[108:111], a[212:215], a[4:7], v[108:111]\n"
"ds_read_b64_tr_b16 a[158:159], v200 offset:60096\n"
"v_mfma_f32_16x16x32_bf16 v[112:115], a[216:219], a[0:3], v[112:115]\n"
"ds_read_b64_tr_b16 a[192:193], v200 offset:55520\n"
"v_mfma_f32_16x16x32_bf16 v[116:119], a[216:219], a[4:7], v[116:119]\n"
"s_waitcnt lgkmcnt(11)\n"
"ds_read_b64_tr_b16 a[194:195], v200 offset:60128\n"
"v_mfma_f32_16x16x32_bf16 v[120:123], a[220:223], a[0:3], v[120:123]\n"
"ds_read_b64_tr_b16 a[196:197], v203 offset:55296\n"
"v_mfma_f32_16x16x32_bf16 v[124:127], a[220:223], a[4:7], v[124:127]\n"
"s_add_u32 m0, s53, 36864\n"
"ds_read_b64_tr_b16 a[198:199], v203 offset:59904\n"
"buffer_load_dwordx4 a[44:47], v207, s[28:31], s22 offen offset:3072 sc1\n"
"v_mfma_f32_16x16x32_bf16 v[226:229], a[128:131], a[0:3], v[226:229]\n"
"s_add_u32 s22, s22, 4096\n"
"ds_read_b64_tr_b16 a[200:201], v200 offset:64512\n"
"buffer_load_dwordx4 v212, s[24:27], s37 offen offset:2048 lds\n"
"v_mfma_f32_16x16x32_bf16 v[230:233], a[128:131], a[4:7], v[230:233]\n"
"s_waitcnt vmcnt(11)\n"
"ds_read_b64_tr_b16 a[202:203], v201 offset:3584\n"
"v_mfma_f32_16x16x32_bf16 v[64:67], a[132:135], a[8:11], v[64:67]\n"
"ds_read_b64_tr_b16 a[204:205], v200 offset:64544\n"
"v_mfma_f32_16x16x32_bf16 v[68:71], a[132:135], a[12:15], v[68:71]\n"
"ds_read_b64_tr_b16 a[206:207], v201 offset:3616\n"
"v_mfma_f32_16x16x32_bf16 v[72:75], a[136:139], a[8:11], v[72:75]\n"
"ds_read_b64_tr_b16 a[208:209], v200 offset:64576\n"
"buffer_load_dwordx4 a[48:51], v207, s[28:31], s20 offen offset:0 sc1\n"
"v_mfma_f32_16x16x32_bf16 v[76:79], a[136:139], a[12:15], v[76:79]\n"
"s_waitcnt lgkmcnt(11)\n"
"ds_read_b64_tr_b16 a[210:211], v201 offset:3648\n"
"v_mfma_f32_16x16x32_bf16 v[80:83], a[140:143], a[8:11], v[80:83]\n"
"s_add_u32 m0, s53, 36864\n"
"ds_read_b64_tr_b16 a[212:213], v200 offset:64608\n"
"v_mfma_f32_16x16x32_bf16 v[84:87], a[140:143], a[12:15], v[84:87]\n"
"ds_read_b64_tr_b16 a[214:215], v201 offset:3680\n"
"buffer_load_dwordx4 v213, s[24:27], s37 offen offset:3072 lds\n"
"v_mfma_f32_16x16x32_bf16 v[88:91], a[144:147], a[8:11], v[88:91]\n"
"ds_read_b64_tr_b16 a[216:217], v200 offset:64640\n"
"v_mfma_f32_16x16x32_bf16 v[92:95], a[144:147], a[12:15], v[92:95]\n"
"ds_read_b64_tr_b16 a[218:219], v201 offset:3712\n"
"buffer_load_dwordx4 a[52:55], v207, s[28:31], s20 offen offset:1024 sc1\n"
"v_mfma_f32_16x16x32_bf16 v[96:99], a[148:151], a[8:11], v[96:99]\n"
"ds_read_b64_tr_b16 a[220:221], v200 offset:64672\n"
"v_mfma_f32_16x16x32_bf16 v[100:103], a[148:151], a[12:15], v[100:103]\n"
"ds_read_b64_tr_b16 a[222:223], v201 offset:3744\n"
"v_mfma_f32_16x16x32_bf16 v[104:107], a[152:155], a[8:11], v[104:107]\n"
"ds_read_b64_tr_b16 a[128:129], v200 offset:64704\n"
"v_mfma_f32_16x16x32_bf16 v[108:111], a[152:155], a[12:15], v[108:111]\n"
"s_waitcnt lgkmcnt(11)\n"
"ds_read_b64_tr_b16 a[130:131], v201 offset:3776\n"
"v_mfma_f32_16x16x32_bf16 v[112:115], a[156:159], a[8:11], v[112:115]\n"
"s_add_u32 m0, s53, 40960\n"
"ds_read_b64_tr_b16 a[132:133], v200 offset:64736\n"
"buffer_load_dwordx4 a[56:59], v207, s[28:31], s21 offen offset:0 sc1\n"
"v_mfma_f32_16x16x32_bf16 v[116:119], a[156:159], a[12:15], v[116:119]\n"
"ds_read_b64_tr_b16 a[134:135], v201 offset:3808\n"
"buffer_load_dwordx4 v214, s[24:27], s37 offen offset:0 lds\n"
"v_mfma_f32_16x16x32_bf16 v[120:123], a[192:195], a[8:11], v[120:123]\n"
"ds_read_b64_tr_b16 a[136:137], v203 offset:64512\n"
"v_mfma_f32_16x16x32_bf16 v[124:127], a[192:195], a[12:15], v[124:127]\n"
"ds_read_b64_tr_b16 a[138:139], v204 offset:3584\n"
"v_mfma_f32_16x16x32_bf16 v[226:229], a[196:199], a[8:11], v[226:229]\n"
"ds_read_b64_tr_b16 a[140:141], v201 offset:8192\n"
"v_mfma_f32_16x16x32_bf16 v[230:233], a[196:199], a[12:15], v[230:233]\n"
"s_waitcnt vmcnt(14)\n"
"ds_read_b64_tr_b16 a[142:143], v201 offset:12800\n"
"v_mfma_f32_16x16x32_bf16 v[64:67], a[200:203], a[16:19], v[64:67]\n"
"ds_read_b64_tr_b16 a[144:145], v201 offset:8224\n"
"v_mfma_f32_16x16x32_bf16 v[68:71], a[200:203], a[20:23], v[68:71]\n"
"s_waitcnt lgkmcnt(11)\n"
"s_add_u32 m0, s53, 40960\n"
"ds_read_b64_tr_b16 a[146:147], v201 offset:12832\n"
"buffer_load_dwordx4 a[60:63], v207, s[28:31], s21 offen offset:1024 sc1\n"
"v_mfma_f32_16x16x32_bf16 v[72:75], a[204:207], a[16:19], v[72:75]\n"
"ds_read_b64_tr_b16 a[148:149], v201 offset:8256\n"
"buffer_load_dwordx4 v236, s[24:27], s37 offen offset:1024 lds\n"
"v_mfma_f32_16x16x32_bf16 v[76:79], a[204:207], a[20:23], v[76:79]\n"
"ds_read_b64_tr_b16 a[150:151], v201 offset:12864\n"
"v_mfma_f32_16x16x32_bf16 v[80:83], a[208:211], a[16:19], v[80:83]\n"
"ds_read_b64_tr_b16 a[152:153], v201 offset:8288\n"
"v_mfma_f32_16x16x32_bf16 v[84:87], a[208:211], a[20:23], v[84:87]\n"
"ds_read_b64_tr_b16 a[154:155], v201 offset:12896\n"
"v_mfma_f32_16x16x32_bf16 v[88:91], a[212:215], a[16:19], v[88:91]\n"
"ds_read_b64_tr_b16 a[156:157], v201 offset:8320\n"
"v_mfma_f32_16x16x32_bf16 v[92:95], a[212:215], a[20:23], v[92:95]\n"
"ds_read_b64_tr_b16 a[158:159], v201 offset:12928\n"
"v_mfma_f32_16x16x32_bf16 v[96:99], a[216:219], a[16:19], v[96:99]\n"
"s_add_u32 m0, s53, 40960\n"
"ds_read_b64_tr_b16 a[192:193], v201 offset:8352\n"
"buffer_load_dwordx4 a[64:67], v207, s[28:31], s22 offen offset:0 sc1\n"
"v_mfma_f32_16x16x32_bf16 v[100:103], a[216:219], a[20:23], v[100:103]\n"
"s_waitcnt lgkmcnt(11)\n"
"ds_read_b64_tr_b16 a[194:195], v201 offset:12960\n"
"buffer_load_dwordx4 v237, s[24:27], s37 offen offset:2048 lds\n"
"v_mfma_f32_16x16x32_bf16 v[104:107], a[220:223], a[16:19], v[104:107]\n"
"ds_read_b64_tr_b16 a[196:197], v201 offset:8384\n"
"v_mfma_f32_16x16x32_bf16 v[108:111], a[220:223], a[20:23], v[108:111]\n"
"ds_read_b64_tr_b16 a[198:199], v201 offset:12992\n"
"v_mfma_f32_16x16x32_bf16 v[112:115], a[128:131], a[16:19], v[112:115]\n"
"ds_read_b64_tr_b16 a[200:201], v201 offset:8416\n"
"v_mfma_f32_16x16x32_bf16 v[116:119], a[128:131], a[20:23], v[116:119]\n"
"ds_read_b64_tr_b16 a[202:203], v201 offset:13024\n"
"v_mfma_f32_16x16x32_bf16 v[120:123], a[132:135], a[16:19], v[120:123]\n"
"ds_read_b64_tr_b16 a[204:205], v204 offset:8192\n"
"v_mfma_f32_16x16x32_bf16 v[124:127], a[132:135], a[20:23], v[124:127]\n"
"s_add_u32 m0, s53, 40960\n"
"ds_read_b64_tr_b16 a[206:207], v204 offset:12800\n"
"buffer_load_dwordx4 a[68:71], v207, s[28:31], s22 offen offset:1024 sc1\n"
"v_mfma_f32_16x16x32_bf16 v[226:229], a[136:139], a[16:19], v[226:229]\n"
"ds_read_b64_tr_b16 a[208:209], v201 offset:17408\n"
"buffer_load_dwordx4 v238, s[24:27], s37 offen offset:3072 lds\n"
"v_mfma_f32_16x16x32_bf16 v[230:233], a[136:139], a[20:23], v[230:233]\n"
"s_waitcnt lgkmcnt(11)\n"
"s_waitcnt vmcnt(18)\n"
"ds_read_b64_tr_b16 a[210:211], v201 offset:22016\n"
"v_mfma_f32_16x16x32_bf16 v[64:67], a[140:143], a[24:27], v[64:67]\n"
"ds_read_b64_tr_b16 a[212:213], v201 offset:17440\n"
"v_mfma_f32_16x16x32_bf16 v[68:71], a[140:143], a[28:31], v[68:71]\n"
"ds_read_b64_tr_b16 a[214:215], v201 offset:22048\n"
"v_mfma_f32_16x16x32_bf16 v[72:75], a[144:147], a[24:27], v[72:75]\n"
"ds_read_b64_tr_b16 a[216:217], v201 offset:17472\n"
"v_mfma_f32_16x16x32_bf16 v[76:79], a[144:147], a[28:31], v[76:79]\n"
"ds_read_b64_tr_b16 a[218:219], v201 offset:22080\n"
"v_mfma_f32_16x16x32_bf16 v[80:83], a[148:151], a[24:27], v[80:83]\n"
"s_add_u32 m0, s53, 45056\n"
"ds_read_b64_tr_b16 a[220:221], v201 offset:17504\n"
"buffer_load_dwordx4 a[72:75], v207, s[28:31], s20 offen offset:2048 sc1\n"
"v_mfma_f32_16x16x32_bf16 v[84:87], a[148:151], a[28:31], v[84:87]\n"
"ds_read_b64_tr_b16 a[222:223], v201 offset:22112\n"
"buffer_load_dwordx4 v239, s[24:27], s37 offen offset:0 lds\n"
"v_mfma_f32_16x16x32_bf16 v[88:91], a[152:155], a[24:27], v[88:91]\n"
"ds_read_b64_tr_b16 a[128:129], v201 offset:17536\n"
"v_mfma_f32_16x16x32_bf16 v[92:95], a[152:155], a[28:31], v[92:95]\n"
"s_waitcnt lgkmcnt(11)\n"
"ds_read_b64_tr_b16 a[130:131], v201 offset:22144\n"
"v_mfma_f32_16x16x32_bf16 v[96:99], a[156:159], a[24:27], v[96:99]\n"
"ds_read_b64_tr_b16 a[132:133], v201 offset:17568\n"
"v_mfma_f32_16x16x32_bf16 v[100:103], a[156:159], a[28:31], v[100:103]\n"
"ds_read_b64_tr_b16 a[134:135], v201 offset:22176\n"
"v_mfma_f32_16x16x32_bf16 v[104:107], a[192:195], a[24:27], v[104:107]\n"
"ds_read_b64_tr_b16 a[136:137], v201 offset:17600\n"
"v_mfma_f32_16x16x32_bf16 v[108:111], a[192:195], a[28:31], v[108:111]\n"
"ds_read_b64_tr_b16 a[138:139], v201 offset:22208\n"
"buffer_load_dwordx4 a[76:79], v207, s[28:31], s20 offen offset:3072 sc1\n"
"v_mfma_f32_16x16x32_bf16 v[112:115], a[196:199], a[24:27], v[112:115]\n"
"s_add_u32 s20, s20, 4096\n"
"ds_read_b64_tr_b16 a[140:141], v201 offset:17632\n"
"v_mfma_f32_16x16x32_bf16 v[116:119], a[196:199], a[28:31], v[116:119]\n"
"ds_read_b64_tr_b16 a[142:143], v201 offset:22240\n"
"v_mfma_f32_16x16x32_bf16 v[120:123], a[200:203], a[24:27], v[120:123]\n"
"ds_read_b64_tr_b16 a[144:145], v204 offset:17408\n"
"v_mfma_f32_16x16x32_bf16 v[124:127], a[200:203], a[28:31], v[124:127]\n"
"s_waitcnt lgkmcnt(11)\n"
"ds_read_b64_tr_b16 a[146:147], v204 offset:22016\n"
"v_mfma_f32_16x16x32_bf16 v[226:229], a[204:207], a[24:27], v[226:229]\n"
"ds_read_b64_tr_b16 a[148:149], v201 offset:26624\n"
"v_mfma_f32_16x16x32_bf16 v[230:233], a[204:207], a[28:31], v[230:233]\n"
"s_waitcnt vmcnt(18)\n"
"ds_read_b64_tr_b16 a[150:151], v201 offset:31232\n"
"v_mfma_f32_16x16x32_bf16 v[64:67], a[208:211], a[32:35], v[64:67]\n"
"ds_read_b64_tr_b16 a[152:153], v201 offset:26656\n"
"buffer_load_dwordx4 a[80:83], v207, s[28:31], s21 offen offset:2048 sc1\n"
"v_mfma_f32_16x16x32_bf16 v[68:71], a[208:211], a[36:39], v[68:71]\n"
"s_waitcnt vmcnt(31)\n"
"ds_read_b64_tr_b16 a[154:155], v201 offset:31264\n"
"v_mfma_f32_16x16x32_bf16 v[72:75], a[212:215], a[32:35], v[72:75]\n"
"s_barrier\n"
"ds_read_b64_tr_b16 a[156:157], v201 offset:26688\n"
"v_mfma_f32_16x16x32_bf16 v[76:79], a[212:215], a[36:39], v[76:79]\n"
"ds_read_b64_tr_b16 a[158:159], v201 offset:31296\n"
"v_mfma_f32_16x16x32_bf16 v[80:83], a[216:219], a[32:35], v[80:83]\n"
"s_add_u32 m0, s53, 73728\n"
"ds_read_b64_tr_b16 a[192:193], v201 offset:26720\n"
"v_mfma_f32_16x16x32_bf16 v[84:87], a[216:219], a[36:39], v[84:87]\n"
"s_waitcnt lgkmcnt(11)\n"
"ds_read_b64_tr_b16 a[194:195], v201 offset:31328\n"
"buffer_load_dwordx4 v210, s[24:27], s38 offen offset:0 lds\n"
"v_mfma_f32_16x16x32_bf16 v[88:91], a[220:223], a[32:35], v[88:91]\n"
"ds_read_b64_tr_b16 a[196:197], v201 offset:26752\n"
"v_mfma_f32_16x16x32_bf16 v[92:95], a[220:223], a[36:39], v[92:95]\n"
"ds_read_b64_tr_b16 a[198:199], v201 offset:31360\n"
"buffer_load_dwordx4 a[84:87], v207, s[28:31], s21 offen offset:3072 sc1\n"
"v_mfma_f32_16x16x32_bf16 v[96:99], a[128:131], a[32:35], v[96:99]\n"
"s_add_u32 s21, s21, 4096\n"
"ds_read_b64_tr_b16 a[200:201], v201 offset:26784\n"
"v_mfma_f32_16x16x32_bf16 v[100:103], a[128:131], a[36:39], v[100:103]\n"
"ds_read_b64_tr_b16 a[202:203], v201 offset:31392\n"
"v_mfma_f32_16x16x32_bf16 v[104:107], a[132:135], a[32:35], v[104:107]\n"
"ds_read_b64_tr_b16 a[204:205], v201 offset:26816\n"
"v_mfma_f32_16x16x32_bf16 v[108:111], a[132:135], a[36:39], v[108:111]\n"
"s_add_u32 m0, s53, 73728\n"
"ds_read_b64_tr_b16 a[206:207], v201 offset:31424\n"
"v_mfma_f32_16x16x32_bf16 v[112:115], a[136:139], a[32:35], v[112:115]\n"
"ds_read_b64_tr_b16 a[208:209], v201 offset:26848\n"
"buffer_load_dwordx4 v211, s[24:27], s38 offen offset:1024 lds\n"
"v_mfma_f32_16x16x32_bf16 v[116:119], a[136:139], a[36:39], v[116:119]\n"
"s_waitcnt lgkmcnt(11)\n"
"ds_read_b64_tr_b16 a[210:211], v201 offset:31456\n"
"v_mfma_f32_16x16x32_bf16 v[120:123], a[140:143], a[32:35], v[120:123]\n"
"ds_read_b64_tr_b16 a[212:213], v204 offset:26624\n"
"buffer_load_dwordx4 a[88:91], v207, s[28:31], s22 offen offset:2048 sc1\n"
"v_mfma_f32_16x16x32_bf16 v[124:127], a[140:143], a[36:39], v[124:127]\n"
"ds_read_b64_tr_b16 a[214:215], v204 offset:31232\n"
"v_mfma_f32_16x16x32_bf16 v[226:229], a[144:147], a[32:35], v[226:229]\n"
"ds_read_b64_tr_b16 a[216:217], v201 offset:35840\n"
"v_mfma_f32_16x16x32_bf16 v[230:233], a[144:147], a[36:39], v[230:233]\n"
"s_waitcnt vmcnt(20)\n"
"ds_read_b64_tr_b16 a[218:219], v201 offset:40448\n"
"v_mfma_f32_16x16x32_bf16 v[64:67], a[148:151], a[40:43], v[64:67]\n"
"s_add_u32 m0, s53, 73728\n"
"ds_read_b64_tr_b16 a[220:221], v201 offset:35872\n"
"v_mfma_f32_16x16x32_bf16 v[68:71], a[148:151], a[44:47], v[68:71]\n"
"ds_read_b64_tr_b16 a[222:223], v201 offset:40480\n"
"buffer_load_dwordx4 v212, s[24:27], s38 offen offset:2048 lds\n"
"v_mfma_f32_16x16x32_bf16 v[72:75], a[152:155], a[40:43], v[72:75]\n"
"ds_read_b64_tr_b16 a[128:129], v201 offset:35904\n"
"v_mfma_f32_16x16x32_bf16 v[76:79], a[152:155], a[44:47], v[76:79]\n"
"s_waitcnt lgkmcnt(11)\n"
"ds_read_b64_tr_b16 a[130:131], v201 offset:40512\n"
"buffer_load_dwordx4 a[92:95], v207, s[28:31], s22 offen offset:3072 sc1\n"
"v_mfma_f32_16x16x32_bf16 v[80:83], a[156:159], a[40:43], v[80:83]\n"
"s_add_u32 s22, s22, 4096\n"
"ds_read_b64_tr_b16 a[132:133], v201 offset:35936\n"
"v_mfma_f32_16x16x32_bf16 v[84:87], a[156:159], a[44:47], v[84:87]\n"
"ds_read_b64_tr_b16 a[134:135], v201 offset:40544\n"
"v_mfma_f32_16x16x32_bf16 v[88:91], a[192:195], a[40:43], v[88:91]\n"
"ds_read_b64_tr_b16 a[136:137], v201 offset:35968\n"
"v_mfma_f32_16x16x32_bf16 v[92:95], a[192:195], a[44:47], v[92:95]\n"
"s_add_u32 m0, s53, 73728\n"
"ds_read_b64_tr_b16 a[138:139], v201 offset:40576\n"
"v_mfma_f32_16x16x32_bf16 v[96:99], a[196:199], a[40:43], v[96:99]\n"
"ds_read_b64_tr_b16 a[140:141], v201 offset:36000\n"
"buffer_load_dwordx4 v213, s[24:27], s38 offen offset:3072 lds\n"
"v_mfma_f32_16x16x32_bf16 v[100:103], a[196:199], a[44:47], v[100:103]\n"
"ds_read_b64_tr_b16 a[142:143], v201 offset:40608\n"
"v_mfma_f32_16x16x32_bf16 v[104:107], a[200:203], a[40:43], v[104:107]\n"
"ds_read_b64_tr_b16 a[144:145], v201 offset:36032\n"
"buffer_load_dwordx4 a[96:99], v207, s[28:31], s20 offen offset:0 sc1\n"
"v_mfma_f32_16x16x32_bf16 v[108:111], a[200:203], a[44:47], v[108:111]\n"
"s_waitcnt lgkmcnt(11)\n"
"ds_read_b64_tr_b16 a[146:147], v201 offset:40640\n"
"v_mfma_f32_16x16x32_bf16 v[112:115], a[204:207], a[40:43], v[112:115]\n"
"ds_read_b64_tr_b16 a[148:149], v201 offset:36064\n"
"v_mfma_f32_16x16x32_bf16 v[116:119], a[204:207], a[44:47], v[116:119]\n"
"ds_read_b64_tr_b16 a[150:151], v201 offset:40672\n"
"v_mfma_f32_16x16x32_bf16 v[120:123], a[208:211], a[40:43], v[120:123]\n"
"s_add_u32 m0, s53, 77824\n"
"ds_read_b64_tr_b16 a[152:153], v204 offset:35840\n"
"v_mfma_f32_16x16x32_bf16 v[124:127], a[208:211], a[44:47], v[124:127]\n"
"ds_read_b64_tr_b16 a[154:155], v204 offset:40448\n"
"buffer_load_dwordx4 v214, s[24:27], s38 offen offset:0 lds\n"
"v_mfma_f32_16x16x32_bf16 v[226:229], a[212:215], a[40:43], v[226:229]\n"
"ds_read_b64_tr_b16 a[156:157], v201 offset:45056\n"
"v_mfma_f32_16x16x32_bf16 v[230:233], a[212:215], a[44:47], v[230:233]\n"
"s_waitcnt vmcnt(21)\n"
"ds_read_b64_tr_b16 a[158:159], v201 offset:49664\n"
"buffer_load_dwordx4 a[100:103], v207, s[28:31], s20 offen offset:1024 sc1\n"
"v_mfma_f32_16x16x32_bf16 v[64:67], a[216:219], a[48:51], v[64:67]\n"
"ds_read_b64_tr_b16 a[192:193], v201 offset:45088\n"
"v_mfma_f32_16x16x32_bf16 v[68:71], a[216:219], a[52:55], v[68:71]\n"
"s_waitcnt lgkmcnt(11)\n"
"ds_read_b64_tr_b16 a[194:195], v201 offset:49696\n"
"v_mfma_f32_16x16x32_bf16 v[72:75], a[220:223], a[48:51], v[72:75]\n"
"ds_read_b64_tr_b16 a[196:197], v201 offset:45120\n"
"v_mfma_f32_16x16x32_bf16 v[76:79], a[220:223], a[52:55], v[76:79]\n"
"s_add_u32 m0, s53, 77824\n"
"ds_read_b64_tr_b16 a[198:199], v201 offset:49728\n"
"v_mfma_f32_16x16x32_bf16 v[80:83], a[128:131], a[48:51], v[80:83]\n"
"ds_read_b64_tr_b16 a[200:201], v201 offset:45152\n"
"buffer_load_dwordx4 v236, s[24:27], s38 offen offset:1024 lds\n"
"v_mfma_f32_16x16x32_bf16 v[84:87], a[128:131], a[52:55], v[84:87]\n"
"ds_read_b64_tr_b16 a[202:203], v201 offset:49760\n"
"v_mfma_f32_16x16x32_bf16 v[88:91], a[132:135], a[48:51], v[88:91]\n"
"ds_read_b64_tr_b16 a[204:205], v201 offset:45184\n"
"buffer_load_dwordx4 a[104:107], v207, s[28:31], s21 offen offset:0 sc1\n"
"v_mfma_f32_16x16x32_bf16 v[92:95], a[132:135], a[52:55], v[92:95]\n"
"ds_read_b64_tr_b16 a[206:207], v201 offset:49792\n"
"v_mfma_f32_16x16x32_bf16 v[96:99], a[136:139], a[48:51], v[96:99]\n"
"ds_read_b64_tr_b16 a[208:209], v201 offset:45216\n"
"v_mfma_f32_16x16x32_bf16 v[100:103], a[136:139], a[52:55], v[100:103]\n"
"s_waitcnt lgkmcnt(11)\n"
"ds_read_b64_tr_b16 a[210:211], v201 offset:49824\n"
"v_mfma_f32_16x16x32_bf16 v[104:107], a[140:143], a[48:51], v[104:107]\n"
"s_add_u32 m0, s53, 77824\n"
"ds_read_b64_tr_b16 a[212:213], v201 offset:45248\n"
"v_mfma_f32_16x16x32_bf16 v[108:111], a[140:143], a[52:55], v[108:111]\n"
"ds_read_b64_tr_b16 a[214:215], v201 offset:49856\n"
"buffer_load_dwordx4 v237, s[24:27], s38 offen offset:2048 lds\n"
"v_mfma_f32_16x16x32_bf16 v[112:115], a[144:147], a[48:51], v[112:115]\n"
"ds_read_b64_tr_b16 a[216:217], v201 offset:45280\n"
"v_mfma_f32_16x16x32_bf16 v[116:119], a[144:147], a[52:55], v[116:119]\n"
"ds_read_b64_tr_b16 a[218:219], v201 offset:49888\n"
"buffer_load_dwordx4 a[108:111], v207, s[28:31], s21 offen offset:1024 sc1\n"
"v_mfma_f32_16x16x32_bf16 v[120:123], a[148:151], a[48:51], v[120:123]\n"
"ds_read_b64_tr_b16 a[220:221], v204 offset:45056\n"
"v_mfma_f32_16x16x32_bf16 v[124:127], a[148:151], a[52:55], v[124:127]\n"
"ds_read_b64_tr_b16 a[222:223], v204 offset:49664\n"
"v_mfma_f32_16x16x32_bf16 v[226:229], a[152:155], a[48:51], v[226:229]\n"
"ds_read_b64_tr_b16 a[128:129], v202 offset:0\n"
"v_mfma_f32_16x16x32_bf16 v[230:233], a[152:155], a[52:55], v[230:233]\n"
"s_waitcnt lgkmcnt(11)\n"
"s_waitcnt vmcnt(23)\n"
"ds_read_b64_tr_b16 a[130:131], v202 offset:4608\n"
"v_mfma_f32_16x16x32_bf16 v[64:67], a[156:159], a[56:59], v[64:67]\n"
"s_add_u32 m0, s53, 77824\n"
"ds_read_b64_tr_b16 a[132:133], v202 offset:32\n"
"v_mfma_f32_16x16x32_bf16 v[68:71], a[156:159], a[60:63], v[68:71]\n"
"ds_read_b64_tr_b16 a[134:135], v202 offset:4640\n"
"buffer_load_dwordx4 v238, s[24:27], s38 offen offset:3072 lds\n"
"v_mfma_f32_16x16x32_bf16 v[72:75], a[192:195], a[56:59], v[72:75]\n"
"ds_read_b64_tr_b16 a[136:137], v202 offset:64\n"
"buffer_load_dwordx4 a[112:115], v207, s[28:31], s22 offen offset:0 sc1\n"
"v_mfma_f32_16x16x32_bf16 v[76:79], a[192:195], a[60:63], v[76:79]\n"
"ds_read_b64_tr_b16 a[138:139], v202 offset:4672\n"
"v_mfma_f32_16x16x32_bf16 v[80:83], a[196:199], a[56:59], v[80:83]\n"
"ds_read_b64_tr_b16 a[140:141], v202 offset:96\n"
"v_mfma_f32_16x16x32_bf16 v[84:87], a[196:199], a[60:63], v[84:87]\n"
"ds_read_b64_tr_b16 a[142:143], v202 offset:4704\n"
"v_mfma_f32_16x16x32_bf16 v[88:91], a[200:203], a[56:59], v[88:91]\n"
"ds_read_b64_tr_b16 a[144:145], v202 offset:128\n"
"v_mfma_f32_16x16x32_bf16 v[92:95], a[200:203], a[60:63], v[92:95]\n"
"s_waitcnt lgkmcnt(11)\n"
"s_add_u32 m0, s53, 81920\n"
"ds_read_b64_tr_b16 a[146:147], v202 offset:4736\n"
"v_mfma_f32_16x16x32_bf16 v[96:99], a[204:207], a[56:59], v[96:99]\n"
"ds_read_b64_tr_b16 a[148:149], v202 offset:160\n"
"buffer_load_dwordx4 v239, s[24:27], s38 offen offset:0 lds\n"
"v_mfma_f32_16x16x32_bf16 v[100:103], a[204:207], a[60:63], v[100:103]\n"
"ds_read_b64_tr_b16 a[150:151], v202 offset:4768\n"
"buffer_load_dwordx4 a[116:119], v207, s[28:31], s22 offen offset:1024 sc1\n"
"v_mfma_f32_16x16x32_bf16 v[104:107], a[208:211], a[56:59], v[104:107]\n"
"ds_read_b64_tr_b16 a[152:153], v202 offset:192\n"
"v_mfma_f32_16x16x32_bf16 v[108:111], a[208:211], a[60:63], v[108:111]\n"
"ds_read_b64_tr_b16 a[154:155], v202 offset:4800\n"
"v_mfma_f32_16x16x32_bf16 v[112:115], a[212:215], a[56:59], v[112:115]\n"
"ds_read_b64_tr_b16 a[156:157], v202 offset:224\n"
"v_mfma_f32_16x16x32_bf16 v[116:119], a[212:215], a[60:63], v[116:119]\n"
"ds_read_b64_tr_b16 a[158:159], v202 offset:4832\n"
"v_mfma_f32_16x16x32_bf16 v[120:123], a[216:219], a[56:59], v[120:123]\n"
"ds_read_b64_tr_b16 a[192:193], v205 offset:0\n"
"v_mfma_f32_16x16x32_bf16 v[124:127], a[216:219], a[60:63], v[124:127]\n"
"s_waitcnt lgkmcnt(11)\n"
"ds_read_b64_tr_b16 a[194:195], v205 offset:4608\n"
"v_mfma_f32_16x16x32_bf16 v[226:229], a[220:223], a[56:59], v[226:229]\n"
"ds_read_b64_tr_b16 a[196:197], v202 offset:9216\n"
"buffer_load_dwordx4 a[120:123], v207, s[28:31], s20 offen offset:2048 sc1\n"
"v_mfma_f32_16x16x32_bf16 v[230:233], a[220:223], a[60:63], v[230:233]\n"
"s_waitcnt vmcnt(24)\n"
"ds_read_b64_tr_b16 a[198:199], v202 offset:13824\n"
"v_mfma_f32_16x16x32_bf16 v[64:67], a[128:131], a[64:67], v[64:67]\n"
"ds_read_b64_tr_b16 a[200:201], v202 offset:9248\n"
"v_mfma_f32_16x16x32_bf16 v[68:71], a[128:131], a[68:71], v[68:71]\n"
"s_waitcnt vmcnt(21)\n"
"ds_read_b64_tr_b16 a[202:203], v202 offset:13856\n"
"v_mfma_f32_16x16x32_bf16 v[72:75], a[132:135], a[64:67], v[72:75]\n"
"s_barrier\n"
"ds_read_b64_tr_b16 a[204:205], v202 offset:9280\n"
"v_mfma_f32_16x16x32_bf16 v[76:79], a[132:135], a[68:71], v[76:79]\n"
"ds_read_b64_tr_b16 a[206:207], v202 offset:13888\n"
"v_mfma_f32_16x16x32_bf16 v[80:83], a[136:139], a[64:67], v[80:83]\n"
"ds_read_b64_tr_b16 a[208:209], v202 offset:9312\n"
"v_mfma_f32_16x16x32_bf16 v[84:87], a[136:139], a[68:71], v[84:87]\n"
"s_waitcnt lgkmcnt(11)\n"
"s_add_u32 m0, s53, 110592\n"
"ds_read_b64_tr_b16 a[210:211], v202 offset:13920\n"
"buffer_load_dwordx4 a[124:127], v207, s[28:31], s20 offen offset:3072 sc1\n"
"v_mfma_f32_16x16x32_bf16 v[88:91], a[140:143], a[64:67], v[88:91]\n"
"s_add_u32 s20, s20, 4096\n"
"ds_read_b64_tr_b16 a[212:213], v202 offset:9344\n"
"buffer_load_dwordx4 v210, s[24:27], s39 offen offset:0 lds\n"
"v_mfma_f32_16x16x32_bf16 v[92:95], a[140:143], a[68:71], v[92:95]\n"
"ds_read_b64_tr_b16 a[214:215], v202 offset:13952\n"
"v_mfma_f32_16x16x32_bf16 v[96:99], a[144:147], a[64:67], v[96:99]\n"
"ds_read_b64_tr_b16 a[216:217], v202 offset:9376\n"
"v_mfma_f32_16x16x32_bf16 v[100:103], a[144:147], a[68:71], v[100:103]\n"
"ds_read_b64_tr_b16 a[218:219], v202 offset:13984\n"
"v_mfma_f32_16x16x32_bf16 v[104:107], a[148:151], a[64:67], v[104:107]\n"
"ds_read_b64_tr_b16 a[220:221], v202 offset:9408\n"
"v_mfma_f32_16x16x32_bf16 v[108:111], a[148:151], a[68:71], v[108:111]\n"
"ds_read_b64_tr_b16 a[222:223], v202 offset:14016\n"
"v_mfma_f32_16x16x32_bf16 v[112:115], a[152:155], a[64:67], v[112:115]\n"
"s_add_u32 m0, s53, 110592\n"
"ds_read_b64_tr_b16 a[128:129], v202 offset:9440\n"
"buffer_load_dwordx4 v[0:3], v207, s[28:31], s21 offen offset:2048 sc1\n"
"v_mfma_f32_16x16x32_bf16 v[116:119], a[152:155], a[68:71], v[116:119]\n"
"s_waitcnt lgkmcnt(11)\n"
"ds_read_b64_tr_b16 a[130:131], v202 offset:14048\n"
"buffer_load_dwordx4 v211, s[24:27], s39 offen offset:1024 lds\n"
"v_mfma_f32_16x16x32_bf16 v[120:123], a[156:159], a[64:67], v[120:123]\n"
"ds_read_b64_tr_b16 a[132:133], v205 offset:9216\n"
"v_mfma_f32_16x16x32_bf16 v[124:127], a[156:159], a[68:71], v[124:127]\n"
"ds_read_b64_tr_b16 a[134:135], v205 offset:13824\n"
"v_mfma_f32_16x16x32_bf16 v[226:229], a[192:195], a[64:67], v[226:229]\n"
"ds_read_b64_tr_b16 a[136:137], v202 offset:18432\n"
"v_mfma_f32_16x16x32_bf16 v[230:233], a[192:195], a[68:71], v[230:233]\n"
"s_waitcnt vmcnt(24)\n"
"ds_read_b64_tr_b16 a[138:139], v202 offset:23040\n"
"v_mfma_f32_16x16x32_bf16 v[64:67], a[196:199], a[72:75], v[64:67]\n"
"ds_read_b64_tr_b16 a[140:141], v202 offset:18464\n"
"v_mfma_f32_16x16x32_bf16 v[68:71], a[196:199], a[76:79], v[68:71]\n"
"s_add_u32 m0, s53, 110592\n"
"ds_read_b64_tr_b16 a[142:143], v202 offset:23072\n"
"buffer_load_dwordx4 v[4:7], v207, s[28:31], s21 offen offset:3072 sc1\n"
"v_mfma_f32_16x16x32_bf16 v[72:75], a[200:203], a[72:75], v[72:75]\n"
"s_add_u32 s21, s21, 4096\n"
"ds_read_b64_tr_b16 a[144:145], v202 offset:18496\n"
"buffer_load_dwordx4 v212, s[24:27], s39 offen offset:2048 lds\n"
"v_mfma_f32_16x16x32_bf16 v[76:79], a[200:203], a[76:79], v[76:79]\n"
"s_waitcnt lgkmcnt(11)\n"
"ds_read_b64_tr_b16 a[146:147], v202 offset:23104\n"
"v_mfma_f32_16x16x32_bf16 v[80:83], a[204:207], a[72:75], v[80:83]\n"
"ds_read_b64_tr_b16 a[148:149], v202 offset:18528\n"
"v_mfma_f32_16x16x32_bf16 v[84:87], a[204:207], a[76:79], v[84:87]\n"
"ds_read_b64_tr_b16 a[150:151], v202 offset:23136\n"
"v_mfma_f32_16x16x32_bf16 v[88:91], a[208:211], a[72:75], v[88:91]\n"
"ds_read_b64_tr_b16 a[152:153], v202 offset:18560\n"
"v_mfma_f32_16x16x32_bf16 v[92:95], a[208:211], a[76:79], v[92:95]\n"
"ds_read_b64_tr_b16 a[154:155], v202 offset:23168\n"
"v_mfma_f32_16x16x32_bf16 v[96:99], a[212:215], a[72:75], v[96:99]\n"
"s_add_u32 m0, s53, 110592\n"
"ds_read_b64_tr_b16 a[156:157], v202 offset:18592\n"
"buffer_load_dwordx4 v[8:11], v207, s[28:31], s22 offen offset:2048 sc1\n"
"v_mfma_f32_16x16x32_bf16 v[100:103], a[212:215], a[76:79], v[100:103]\n"
"ds_read_b64_tr_b16 a[158:159], v202 offset:23200\n"
"buffer_load_dwordx4 v213, s[24:27], s39 offen offset:3072 lds\n"
"v_mfma_f32_16x16x32_bf16 v[104:107], a[216:219], a[72:75], v[104:107]\n"
"ds_read_b64_tr_b16 a[192:193], v202 offset:18624\n"
"v_mfma_f32_16x16x32_bf16 v[108:111], a[216:219], a[76:79], v[108:111]\n"
"s_waitcnt lgkmcnt(11)\n"
"ds_read_b64_tr_b16 a[194:195], v202 offset:23232\n"
"v_mfma_f32_16x16x32_bf16 v[112:115], a[220:223], a[72:75], v[112:115]\n"
"ds_read_b64_tr_b16 a[196:197], v202 offset:18656\n"
"v_mfma_f32_16x16x32_bf16 v[116:119], a[220:223], a[76:79], v[116:119]\n"
"ds_read_b64_tr_b16 a[198:199], v202 offset:23264\n"
"v_mfma_f32_16x16x32_bf16 v[120:123], a[128:131], a[72:75], v[120:123]\n"
"ds_read_b64_tr_b16 a[200:201], v205 offset:18432\n"
"v_mfma_f32_16x16x32_bf16 v[124:127], a[128:131], a[76:79], v[124:127]\n"
"s_add_u32 m0, s53, 114688\n"
"ds_read_b64_tr_b16 a[202:203], v205 offset:23040\n"
"buffer_load_dwordx4 v[12:15], v207, s[28:31], s22 offen offset:3072 sc1\n"
"v_mfma_f32_16x16x32_bf16 v[226:229], a[132:135], a[72:75], v[226:229]\n"
"s_add_u32 s22, s22, 4096\n"
"ds_read_b64_tr_b16 a[204:205], v200 offset:0\n"
"buffer_load_dwordx4 v214, s[24:27], s39 offen offset:0 lds\n"
"v_mfma_f32_16x16x32_bf16 v[230:233], a[132:135], a[76:79], v[230:233]\n"
"s_waitcnt vmcnt(27)\n"
"ds_read_b64_tr_b16 a[206:207], v200 offset:4608\n"
"v_mfma_f32_16x16x32_bf16 v[64:67], a[136:139], a[80:83], v[64:67]\n"
"ds_read_b64_tr_b16 a[208:209], v200 offset:32\n"
"v_mfma_f32_16x16x32_bf16 v[68:71], a[136:139], a[84:87], v[68:71]\n"
"s_waitcnt lgkmcnt(11)\n"
"ds_read_b64_tr_b16 a[210:211], v200 offset:4640\n"
"v_mfma_f32_16x16x32_bf16 v[72:75], a[140:143], a[80:83], v[72:75]\n"
"ds_read_b64_tr_b16 a[212:213], v200 offset:64\n"
"v_mfma_f32_16x16x32_bf16 v[76:79], a[140:143], a[84:87], v[76:79]\n"
"ds_read_b64_tr_b16 a[214:215], v200 offset:4672\n"
"v_mfma_f32_16x16x32_bf16 v[80:83], a[144:147], a[80:83], v[80:83]\n"
"s_add_u32 m0, s53, 114688\n"
"ds_read_b64_tr_b16 a[216:217], v200 offset:96\n"
"buffer_load_dwordx4 v[16:19], v207, s[28:31], s20 offen offset:0 sc1\n"
"v_mfma_f32_16x16x32_bf16 v[84:87], a[144:147], a[84:87], v[84:87]\n"
"ds_read_b64_tr_b16 a[218:219], v200 offset:4704\n"
"buffer_load_dwordx4 v236, s[24:27], s39 offen offset:1024 lds\n"
"v_mfma_f32_16x16x32_bf16 v[88:91], a[148:151], a[80:83], v[88:91]\n"
"ds_read_b64_tr_b16 a[220:221], v200 offset:128\n"
"v_mfma_f32_16x16x32_bf16 v[92:95], a[148:151], a[84:87], v[92:95]\n"
"ds_read_b64_tr_b16 a[222:223], v200 offset:4736\n"
"v_mfma_f32_16x16x32_bf16 v[96:99], a[152:155], a[80:83], v[96:99]\n"
"ds_read_b64_tr_b16 a[128:129], v200 offset:160\n"
"v_mfma_f32_16x16x32_bf16 v[100:103], a[152:155], a[84:87], v[100:103]\n"
"s_waitcnt lgkmcnt(11)\n"
"ds_read_b64_tr_b16 a[130:131], v200 offset:4768\n"
"v_mfma_f32_16x16x32_bf16 v[104:107], a[156:159], a[80:83], v[104:107]\n"
"ds_read_b64_tr_b16 a[132:133], v200 offset:192\n"
"v_mfma_f32_16x16x32_bf16 v[108:111], a[156:159], a[84:87], v[108:111]\n"
"s_add_u32 m0, s53, 114688\n"
"ds_read_b64_tr_b16 a[134:135], v200 offset:4800\n"
"buffer_load_dwordx4 v[20:23], v207, s[28:31], s20 offen offset:1024 sc1\n"
"v_mfma_f32_16x16x32_bf16 v[112:115], a[192:195], a[80:83], v[112:115]\n"
"ds_read_b64_tr_b16 a[136:137], v200 offset:224\n"
"buffer_load_dwordx4 v237, s[24:27], s39 offen offset:2048 lds\n"
"v_mfma_f32_16x16x32_bf16 v[116:119], a[192:195], a[84:87], v[116:119]\n"
"ds_read_b64_tr_b16 a[138:139], v200 offset:4832\n"
"v_mfma_f32_16x16x32_bf16 v[120:123], a[196:199], a[80:83], v[120:123]\n"
"ds_read_b64_tr_b16 a[140:141], v203 offset:0\n"
"v_mfma_f32_16x16x32_bf16 v[124:127], a[196:199], a[84:87], v[124:127]\n"
"ds_read_b64_tr_b16 a[142:143], v203 offset:4608\n"
"v_mfma_f32_16x16x32_bf16 v[226:229], a[200:203], a[80:83], v[226:229]\n"
"ds_read_b64_tr_b16 a[144:145], v200 offset:9216\n"
"v_mfma_f32_16x16x32_bf16 v[230:233], a[200:203], a[84:87], v[230:233]\n"
"s_waitcnt lgkmcnt(11)\n"
"s_waitcnt vmcnt(27)\n"
"ds_read_b64_tr_b16 a[146:147], v200 offset:13824\n"
"v_mfma_f32_16x16x32_bf16 v[64:67], a[204:207], a[88:91], v[64:67]\n"
"s_add_u32 m0, s53, 114688\n"
"ds_read_b64_tr_b16 a[148:149], v200 offset:9248\n"
"buffer_load_dwordx4 v[24:27], v207, s[28:31], s21 offen offset:0 sc1\n"
"v_mfma_f32_16x16x32_bf16 v[68:71], a[204:207], a[92:95], v[68:71]\n"
"ds_read_b64_tr_b16 a[150:151], v200 offset:13856\n"
"buffer_load_dwordx4 v238, s[24:27], s39 offen offset:3072 lds\n"
"v_mfma_f32_16x16x32_bf16 v[72:75], a[208:211], a[88:91], v[72:75]\n"
"ds_read_b64_tr_b16 a[152:153], v200 offset:9280\n"
"v_mfma_f32_16x16x32_bf16 v[76:79], a[208:211], a[92:95], v[76:79]\n"
"ds_read_b64_tr_b16 a[154:155], v200 offset:13888\n"
"v_mfma_f32_16x16x32_bf16 v[80:83], a[212:215], a[88:91], v[80:83]\n"
"ds_read_b64_tr_b16 a[156:157], v200 offset:9312\n"
"v_mfma_f32_16x16x32_bf16 v[84:87], a[212:215], a[92:95], v[84:87]\n"
"ds_read_b64_tr_b16 a[158:159], v200 offset:13920\n"
"v_mfma_f32_16x16x32_bf16 v[88:91], a[216:219], a[88:91], v[88:91]\n"
"ds_read_b64_tr_b16 a[192:193], v200 offset:9344\n"
"v_mfma_f32_16x16x32_bf16 v[92:95], a[216:219], a[92:95], v[92:95]\n"
"s_waitcnt lgkmcnt(11)\n"
"s_add_u32 m0, s53, 118784\n"
"ds_read_b64_tr_b16 a[194:195], v200 offset:13952\n"
"buffer_load_dwordx4 v[28:31], v207, s[28:31], s21 offen offset:1024 sc1\n"
"v_mfma_f32_16x16x32_bf16 v[96:99], a[220:223], a[88:91], v[96:99]\n"
"ds_read_b64_tr_b16 a[196:197], v200 offset:9376\n"
"buffer_load_dwordx4 v239, s[24:27], s39 offen offset:0 lds\n"
"v_mfma_f32_16x16x32_bf16 v[100:103], a[220:223], a[92:95], v[100:103]\n"
"ds_read_b64_tr_b16 a[198:199], v200 offset:13984\n"
"v_mfma_f32_16x16x32_bf16 v[104:107], a[128:131], a[88:91], v[104:107]\n"
"ds_read_b64_tr_b16 a[200:201], v200 offset:9408\n"
"v_mfma_f32_16x16x32_bf16 v[108:111], a[128:131], a[92:95], v[108:111]\n"
"ds_read_b64_tr_b16 a[202:203], v200 offset:14016\n"
"v_mfma_f32_16x16x32_bf16 v[112:115], a[132:135], a[88:91], v[112:115]\n"
"ds_read_b64_tr_b16 a[204:205], v200 offset:9440\n"
"v_mfma_f32_16x16x32_bf16 v[116:119], a[132:135], a[92:95], v[116:119]\n"
"ds_read_b64_tr_b16 a[206:207], v200 offset:14048\n"
"v_mfma_f32_16x16x32_bf16 v[120:123], a[136:139], a[88:91], v[120:123]\n"
"ds_read_b64_tr_b16 a[208:209], v203 offset:9216\n"
"buffer_load_dwordx4 v[32:35], v207, s[28:31], s22 offen offset:0 sc1\n"
"v_mfma_f32_16x16x32_bf16 v[124:127], a[136:139], a[92:95], v[124:127]\n"
"s_waitcnt lgkmcnt(11)\n"
"ds_read_b64_tr_b16 a[210:211], v203 offset:13824\n"
"v_mfma_f32_16x16x32_bf16 v[226:229], a[140:143], a[88:91], v[226:229]\n"
"ds_read_b64_tr_b16 a[212:213], v200 offset:18432\n"
"v_mfma_f32_16x16x32_bf16 v[230:233], a[140:143], a[92:95], v[230:233]\n"
"s_waitcnt vmcnt(28)\n"
"ds_read_b64_tr_b16 a[214:215], v200 offset:23040\n"
"v_mfma_f32_16x16x32_bf16 v[64:67], a[144:147], a[96:99], v[64:67]\n"
"ds_read_b64_tr_b16 a[216:217], v200 offset:18464\n"
"v_mfma_f32_16x16x32_bf16 v[68:71], a[144:147], a[100:103], v[68:71]\n"
"ds_read_b64_tr_b16 a[218:219], v200 offset:23072\n"
"v_mfma_f32_16x16x32_bf16 v[72:75], a[148:151], a[96:99], v[72:75]\n"
"ds_read_b64_tr_b16 a[220:221], v200 offset:18496\n"
"v_mfma_f32_16x16x32_bf16 v[76:79], a[148:151], a[100:103], v[76:79]\n"
"ds_read_b64_tr_b16 a[222:223], v200 offset:23104\n"
"buffer_load_dwordx4 v[36:39], v207, s[28:31], s22 offen offset:1024 sc1\n"
"v_mfma_f32_16x16x32_bf16 v[80:83], a[152:155], a[96:99], v[80:83]\n"
"ds_read_b64_tr_b16 a[128:129], v200 offset:18528\n"
"v_mfma_f32_16x16x32_bf16 v[84:87], a[152:155], a[100:103], v[84:87]\n"
"s_waitcnt lgkmcnt(11)\n"
"ds_read_b64_tr_b16 a[130:131], v200 offset:23136\n"
"v_mfma_f32_16x16x32_bf16 v[88:91], a[156:159], a[96:99], v[88:91]\n"
"ds_read_b64_tr_b16 a[132:133], v200 offset:18560\n"
"v_mfma_f32_16x16x32_bf16 v[92:95], a[156:159], a[100:103], v[92:95]\n"
"ds_read_b64_tr_b16 a[134:135], v200 offset:23168\n"
"v_mfma_f32_16x16x32_bf16 v[96:99], a[192:195], a[96:99], v[96:99]\n"
"ds_read_b64_tr_b16 a[136:137], v200 offset:18592\n"
"v_mfma_f32_16x16x32_bf16 v[100:103], a[192:195], a[100:103], v[100:103]\n"
"ds_read_b64_tr_b16 a[138:139], v200 offset:23200\n"
"v_mfma_f32_16x16x32_bf16 v[104:107], a[196:199], a[96:99], v[104:107]\n"
"ds_read_b64_tr_b16 a[140:141], v200 offset:18624\n"
"buffer_load_dwordx4 v[40:43], v207, s[28:31], s20 offen offset:2048 sc1\n"
"v_mfma_f32_16x16x32_bf16 v[108:111], a[196:199], a[100:103], v[108:111]\n"
"ds_read_b64_tr_b16 a[142:143], v200 offset:23232\n"
"v_mfma_f32_16x16x32_bf16 v[112:115], a[200:203], a[96:99], v[112:115]\n"
"ds_read_b64_tr_b16 a[144:145], v200 offset:18656\n"
"v_mfma_f32_16x16x32_bf16 v[116:119], a[200:203], a[100:103], v[116:119]\n"
"s_waitcnt lgkmcnt(11)\n"
"ds_read_b64_tr_b16 a[146:147], v200 offset:23264\n"
"v_mfma_f32_16x16x32_bf16 v[120:123], a[204:207], a[96:99], v[120:123]\n"
"ds_read_b64_tr_b16 a[148:149], v203 offset:18432\n"
"v_mfma_f32_16x16x32_bf16 v[124:127], a[204:207], a[100:103], v[124:127]\n"
"ds_read_b64_tr_b16 a[150:151], v203 offset:23040\n"
"v_mfma_f32_16x16x32_bf16 v[226:229], a[208:211], a[96:99], v[226:229]\n"
"ds_read_b64_tr_b16 a[152:153], v200 offset:27648\n"
"v_mfma_f32_16x16x32_bf16 v[230:233], a[208:211], a[100:103], v[230:233]\n"
"s_waitcnt vmcnt(26)\n"
"ds_read_b64_tr_b16 a[154:155], v200 offset:32256\n"
"buffer_load_dwordx4 v[44:47], v207, s[28:31], s20 offen offset:3072 sc1\n"
"v_mfma_f32_16x16x32_bf16 v[64:67], a[212:215], a[104:107], v[64:67]\n"
"s_add_u32 s20, s20, 4096\n"
"ds_read_b64_tr_b16 a[156:157], v200 offset:27680\n"
"v_mfma_f32_16x16x32_bf16 v[68:71], a[212:215], a[108:111], v[68:71]\n"
"ds_read_b64_tr_b16 a[158:159], v200 offset:32288\n"
"v_mfma_f32_16x16x32_bf16 v[72:75], a[216:219], a[104:107], v[72:75]\n"
"ds_read_b64_tr_b16 a[192:193], v200 offset:27712\n"
"v_mfma_f32_16x16x32_bf16 v[76:79], a[216:219], a[108:111], v[76:79]\n"
"s_waitcnt lgkmcnt(11)\n"
"ds_read_b64_tr_b16 a[194:195], v200 offset:32320\n"
"v_mfma_f32_16x16x32_bf16 v[80:83], a[220:223], a[104:107], v[80:83]\n"
"ds_read_b64_tr_b16 a[196:197], v200 offset:27744\n"
"v_mfma_f32_16x16x32_bf16 v[84:87], a[220:223], a[108:111], v[84:87]\n"
"ds_read_b64_tr_b16 a[198:199], v200 offset:32352\n"
"v_mfma_f32_16x16x32_bf16 v[88:91], a[128:131], a[104:107], v[88:91]\n"
"ds_read_b64_tr_b16 a[200:201], v200 offset:27776\n"
"buffer_load_dwordx4 v[48:51], v207, s[28:31], s21 offen offset:2048 sc1\n"
"v_mfma_f32_16x16x32_bf16 v[92:95], a[128:131], a[108:111], v[92:95]\n"
"ds_read_b64_tr_b16 a[202:203], v200 offset:32384\n"
"v_mfma_f32_16x16x32_bf16 v[96:99], a[132:135], a[104:107], v[96:99]\n"
"ds_read_b64_tr_b16 a[204:205], v200 offset:27808\n"
"v_mfma_f32_16x16x32_bf16 v[100:103], a[132:135], a[108:111], v[100:103]\n"
"ds_read_b64_tr_b16 a[206:207], v200 offset:32416\n"
"v_mfma_f32_16x16x32_bf16 v[104:107], a[136:139], a[104:107], v[104:107]\n"
"ds_read_b64_tr_b16 a[208:209], v200 offset:27840\n"
"v_mfma_f32_16x16x32_bf16 v[108:111], a[136:139], a[108:111], v[108:111]\n"
"s_waitcnt lgkmcnt(11)\n"
"ds_read_b64_tr_b16 a[210:211], v200 offset:32448\n"
"v_mfma_f32_16x16x32_bf16 v[112:115], a[140:143], a[104:107], v[112:115]\n"
"ds_read_b64_tr_b16 a[212:213], v200 offset:27872\n"
"v_mfma_f32_16x16x32_bf16 v[116:119], a[140:143], a[108:111], v[116:119]\n"
"ds_read_b64_tr_b16 a[214:215], v200 offset:32480\n"
"buffer_load_dwordx4 v[52:55], v207, s[28:31], s21 offen offset:3072 sc1\n"
"v_mfma_f32_16x16x32_bf16 v[120:123], a[144:147], a[104:107], v[120:123]\n"
"s_add_u32 s21, s21, 4096\n"
"ds_read_b64_tr_b16 a[216:217], v203 offset:27648\n"
"v_mfma_f32_16x16x32_bf16 v[124:127], a[144:147], a[108:111], v[124:127]\n"
"ds_read_b64_tr_b16 a[218:219], v203 offset:32256\n"
"v_mfma_f32_16x16x32_bf16 v[226:229], a[148:151], a[104:107], v[226:229]\n"
"ds_read_b64_tr_b16 a[220:221], v200 offset:36864\n"
"v_mfma_f32_16x16x32_bf16 v[230:233], a[148:151], a[108:111], v[230:233]\n"
"s_waitcnt vmcnt(25)\n"
"ds_read_b64_tr_b16 a[222:223], v200 offset:41472\n"
"v_mfma_f32_16x16x32_bf16 v[64:67], a[152:155], a[112:115], v[64:67]\n"
"ds_read_b64_tr_b16 a[128:129], v200 offset:36896\n"
"v_mfma_f32_16x16x32_bf16 v[68:71], a[152:155], a[116:119], v[68:71]\n"
"s_waitcnt lgkmcnt(11)\n"
"ds_read_b64_tr_b16 a[130:131], v200 offset:41504\n"
"v_mfma_f32_16x16x32_bf16 v[72:75], a[156:159], a[112:115], v[72:75]\n"
"ds_read_b64_tr_b16 a[132:133], v200 offset:36928\n"
"buffer_load_dwordx4 v[56:59], v207, s[28:31], s22 offen offset:2048 sc1\n"
"v_mfma_f32_16x16x32_bf16 v[76:79], a[156:159], a[116:119], v[76:79]\n"
"ds_read_b64_tr_b16 a[134:135], v200 offset:41536\n"
"v_mfma_f32_16x16x32_bf16 v[80:83], a[192:195], a[112:115], v[80:83]\n"
"ds_read_b64_tr_b16 a[136:137], v200 offset:36960\n"
"v_mfma_f32_16x16x32_bf16 v[84:87], a[192:195], a[116:119], v[84:87]\n"
"ds_read_b64_tr_b16 a[138:139], v200 offset:41568\n"
"v_mfma_f32_16x16x32_bf16 v[88:91], a[196:199], a[112:115], v[88:91]\n"
"ds_read_b64_tr_b16 a[140:141], v200 offset:36992\n"
"v_mfma_f32_16x16x32_bf16 v[92:95], a[196:199], a[116:119], v[92:95]\n"
"ds_read_b64_tr_b16 a[142:143], v200 offset:41600\n"
"v_mfma_f32_16x16x32_bf16 v[96:99], a[200:203], a[112:115], v[96:99]\n"
"ds_read_b64_tr_b16 a[144:145], v200 offset:37024\n"
"v_mfma_f32_16x16x32_bf16 v[100:103], a[200:203], a[116:119], v[100:103]\n"
"s_waitcnt lgkmcnt(11)\n"
"ds_read_b64_tr_b16 a[146:147], v200 offset:41632\n"
"buffer_load_dwordx4 v[60:63], v207, s[28:31], s22 offen offset:3072 sc1\n"
"v_mfma_f32_16x16x32_bf16 v[104:107], a[204:207], a[112:115], v[104:107]\n"
"s_add_u32 s22, s22, 4096\n"
"ds_read_b64_tr_b16 a[148:149], v200 offset:37056\n"
"v_mfma_f32_16x16x32_bf16 v[108:111], a[204:207], a[116:119], v[108:111]\n"
"s_waitcnt vmcnt(8)\n"
"ds_read_b64_tr_b16 a[150:151], v200 offset:41664\n"
"v_mfma_f32_16x16x32_bf16 v[112:115], a[208:211], a[112:115], v[112:115]\n"
"s_barrier\n"
"ds_read_b64_tr_b16 a[152:153], v200 offset:37088\n"
"v_mfma_f32_16x16x32_bf16 v[116:119], a[208:211], a[116:119], v[116:119]\n"
"ds_read_b64_tr_b16 a[154:155], v200 offset:41696\n"
"v_mfma_f32_16x16x32_bf16 v[120:123], a[212:215], a[112:115], v[120:123]\n"
"ds_read_b64_tr_b16 a[156:157], v203 offset:36864\n"
"v_mfma_f32_16x16x32_bf16 v[124:127], a[212:215], a[116:119], v[124:127]\n"
"ds_read_b64_tr_b16 a[158:159], v203 offset:41472\n"
"v_mfma_f32_16x16x32_bf16 v[226:229], a[216:219], a[112:115], v[226:229]\n"
"ds_read_b64_tr_b16 a[192:193], v203 offset:46080\n"
"v_mfma_f32_16x16x32_bf16 v[230:233], a[216:219], a[116:119], v[230:233]\n"
"s_waitcnt lgkmcnt(11)\n"
"s_waitcnt vmcnt(25)\n"
"ds_read_b64_tr_b16 a[194:195], v203 offset:50688\n"
"v_mfma_f32_16x16x32_bf16 v[64:67], a[220:223], a[120:123], v[64:67]\n"
"ds_read_b64_tr_b16 a[196:197], v203 offset:55296\n"
"v_mfma_f32_16x16x32_bf16 v[68:71], a[220:223], a[124:127], v[68:71]\n"
"ds_read_b64_tr_b16 a[198:199], v203 offset:59904\n"
"v_mfma_f32_16x16x32_bf16 v[72:75], a[128:131], a[120:123], v[72:75]\n"
"ds_read_b64_tr_b16 a[200:201], v203 offset:64512\n"
"v_mfma_f32_16x16x32_bf16 v[76:79], a[128:131], a[124:127], v[76:79]\n"
"ds_read_b64_tr_b16 a[202:203], v204 offset:3584\n"
"v_mfma_f32_16x16x32_bf16 v[80:83], a[132:135], a[120:123], v[80:83]\n"
"ds_read_b64_tr_b16 a[204:205], v204 offset:8192\n"
"v_mfma_f32_16x16x32_bf16 v[84:87], a[132:135], a[124:127], v[84:87]\n"
"ds_read_b64_tr_b16 a[206:207], v204 offset:12800\n"
"v_mfma_f32_16x16x32_bf16 v[88:91], a[136:139], a[120:123], v[88:91]\n"
"ds_read_b64_tr_b16 a[208:209], v204 offset:17408\n"
"v_mfma_f32_16x16x32_bf16 v[92:95], a[136:139], a[124:127], v[92:95]\n"
"s_waitcnt lgkmcnt(11)\n"
"ds_read_b64_tr_b16 a[210:211], v204 offset:22016\n"
"v_mfma_f32_16x16x32_bf16 v[96:99], a[140:143], a[120:123], v[96:99]\n"
"ds_read_b64_tr_b16 a[212:213], v204 offset:26624\n"
"v_mfma_f32_16x16x32_bf16 v[100:103], a[140:143], a[124:127], v[100:103]\n"
"ds_read_b64_tr_b16 a[214:215], v204 offset:31232\n"
"v_mfma_f32_16x16x32_bf16 v[104:107], a[144:147], a[120:123], v[104:107]\n"
"ds_read_b64_tr_b16 a[216:217], v204 offset:35840\n"
"v_mfma_f32_16x16x32_bf16 v[108:111], a[144:147], a[124:127], v[108:111]\n"
"ds_read_b64_tr_b16 a[218:219], v204 offset:40448\n"
"v_mfma_f32_16x16x32_bf16 v[112:115], a[148:151], a[120:123], v[112:115]\n"
"ds_read_b64_tr_b16 a[220:221], v204 offset:45056\n"
"v_mfma_f32_16x16x32_bf16 v[116:119], a[148:151], a[124:127], v[116:119]\n"
"ds_read_b64_tr_b16 a[222:223], v204 offset:49664\n"
"v_mfma_f32_16x16x32_bf16 v[120:123], a[152:155], a[120:123], v[120:123]\n"
"ds_read_b64_tr_b16 a[128:129], v200 offset:46080\n"
"v_mfma_f32_16x16x32_bf16 v[124:127], a[152:155], a[124:127], v[124:127]\n"
"s_waitcnt lgkmcnt(11)\n"
"s_mov_b32 m0, s45\n"
"ds_read_b64_tr_b16 a[130:131], v200 offset:50688\n"
"v_mfma_f32_16x16x32_bf16 v[226:229], a[156:159], a[120:123], v[226:229]\n"
"ds_read_b64_tr_b16 a[132:133], v200 offset:55296\n"
"buffer_load_dwordx4 v207, s[16:19], s46 offen offset:0 lds\n"
"v_mfma_f32_16x16x32_bf16 v[230:233], a[156:159], a[124:127], v[230:233]\n"
"s_waitcnt vmcnt(22)\n"
"ds_read_b64_tr_b16 a[134:135], v200 offset:59904\n"
"v_mfma_f32_16x16x32_bf16 v[226:229], a[192:195], v[0:3], v[226:229]\n"
"ds_read_b64_tr_b16 a[136:137], v200 offset:64512\n"
"v_mfma_f32_16x16x32_bf16 v[230:233], a[192:195], v[4:7], v[230:233]\n"
"s_waitcnt vmcnt(18)\n"
"ds_read_b64_tr_b16 a[138:139], v201 offset:3584\n"
"v_mfma_f32_16x16x32_bf16 v[226:229], a[196:199], v[8:11], v[226:229]\n"
"s_mov_b32 m0, s45\n"
"ds_read_b64_tr_b16 a[140:141], v201 offset:8192\n"
"v_accvgpr_write_b32 a48, v128\n"
"v_mfma_f32_16x16x32_bf16 v[230:233], a[196:199], v[12:15], v[230:233]\n"
"s_waitcnt vmcnt(14)\n"
"ds_read_b64_tr_b16 a[142:143], v201 offset:12800\n"
"buffer_load_dwordx4 v207, s[16:19], s46 offen offset:1024 lds\n"
"v_mfma_f32_16x16x32_bf16 v[226:229], a[200:203], v[16:19], v[226:229]\n"
"ds_read_b64_tr_b16 a[144:145], v201 offset:17408\n"
"v_accvgpr_write_b32 a49, v129\n"
"v_mfma_f32_16x16x32_bf16 v[230:233], a[200:203], v[20:23], v[230:233]\n"
"s_waitcnt lgkmcnt(11)\n"
"s_waitcnt vmcnt(11)\n"
"ds_read_b64_tr_b16 a[146:147], v201 offset:22016\n"
"v_accvgpr_write_b32 a50, v130\n"
"v_mfma_f32_16x16x32_bf16 v[226:229], a[204:207], v[24:27], v[226:229]\n"
"ds_read_b64_tr_b16 a[148:149], v201 offset:26624\n"
"v_accvgpr_write_b32 a51, v131\n"
"v_mfma_f32_16x16x32_bf16 v[230:233], a[204:207], v[28:31], v[230:233]\n"
"s_waitcnt vmcnt(8)\n"
"ds_read_b64_tr_b16 a[150:151], v201 offset:31232\n"
"v_accvgpr_write_b32 a52, v132\n"
"v_mfma_f32_16x16x32_bf16 v[226:229], a[208:211], v[32:35], v[226:229]\n"
"s_mov_b32 m0, s45\n"
"ds_read_b64_tr_b16 a[152:153], v201 offset:35840\n"
"v_accvgpr_write_b32 a53, v133\n"
"v_mfma_f32_16x16x32_bf16 v[230:233], a[208:211], v[36:39], v[230:233]\n"
"s_waitcnt vmcnt(6)\n"
"ds_read_b64_tr_b16 a[154:155], v201 offset:40448\n"
"buffer_load_dwordx4 v207, s[16:19], s46 offen offset:2048 lds\n"
"v_mfma_f32_16x16x32_bf16 v[226:229], a[212:215], v[40:43], v[226:229]\n"
"ds_read_b64_tr_b16 a[156:157], v201 offset:45056\n"
"v_accvgpr_write_b32 a54, v134\n"
"v_mfma_f32_16x16x32_bf16 v[230:233], a[212:215], v[44:47], v[230:233]\n"
"s_waitcnt vmcnt(5)\n"
"ds_read_b64_tr_b16 a[158:159], v201 offset:49664\n"
"v_accvgpr_write_b32 a55, v135\n"
"v_mfma_f32_16x16x32_bf16 v[226:229], a[216:219], v[48:51], v[226:229]\n"
"ds_read_b64_tr_b16 a[192:193], v200 offset:46112\n"
"v_accvgpr_write_b32 a56, v136\n"
"v_mfma_f32_16x16x32_bf16 v[230:233], a[216:219], v[52:55], v[230:233]\n"
"s_waitcnt lgkmcnt(11)\n"
"s_waitcnt vmcnt(3)\n"
"ds_read_b64_tr_b16 a[194:195], v200 offset:50720\n"
"v_accvgpr_write_b32 a57, v137\n"
"v_mfma_f32_16x16x32_bf16 v[226:229], a[220:223], v[56:59], v[226:229]\n"
"ds_read_b64_tr_b16 a[196:197], v200 offset:55328\n"
"v_accvgpr_write_b32 a58, v138\n"
"v_mfma_f32_16x16x32_bf16 v[230:233], a[220:223], v[60:63], v[230:233]\n"
"ds_read_b64_tr_b16 a[198:199], v200 offset:59936\n"
"v_accvgpr_write_b32 a59, v139\n"
"v_mfma_f32_16x16x32_bf16 v[64:67], a[128:131], v[0:3], v[64:67]\n"
"ds_read_b64_tr_b16 a[200:201], v200 offset:64544\n"
"v_accvgpr_write_b32 a60, v140\n"
"v_mfma_f32_16x16x32_bf16 v[68:71], a[128:131], v[4:7], v[68:71]\n"
"s_add_u32 m0, s84, 131072\n"
"ds_read_b64_tr_b16 a[202:203], v201 offset:3616\n"
"v_accvgpr_write_b32 a61, v141\n"
"v_mfma_f32_16x16x32_bf16 v[64:67], a[132:135], v[8:11], v[64:67]\n"
"ds_read_b64_tr_b16 a[204:205], v201 offset:8224\n"
"buffer_load_dwordx4 v207, s[72:75], s50 offen offset:0 lds\n"
"v_mfma_f32_16x16x32_bf16 v[68:71], a[132:135], v[12:15], v[68:71]\n"
"ds_read_b64_tr_b16 a[206:207], v201 offset:12832\n"
"v_cvt_pk_bf16_f32 v218, v226, v227\n"
"v_mfma_f32_16x16x32_bf16 v[64:67], a[136:139], v[16:19], v[64:67]\n"
"ds_read_b64_tr_b16 a[208:209], v201 offset:17440\n"
"v_cvt_pk_bf16_f32 v219, v228, v229\n"
"v_mfma_f32_16x16x32_bf16 v[68:71], a[136:139], v[20:23], v[68:71]\n"
"s_waitcnt lgkmcnt(11)\n"
"ds_read_b64_tr_b16 a[210:211], v201 offset:22048\n"
"v_cvt_pk_bf16_f32 v222, v230, v231\n"
"v_mfma_f32_16x16x32_bf16 v[64:67], a[140:143], v[24:27], v[64:67]\n"
"ds_read_b64_tr_b16 a[212:213], v201 offset:26656\n"
"v_cvt_pk_bf16_f32 v223, v232, v233\n"
"v_mfma_f32_16x16x32_bf16 v[68:71], a[140:143], v[28:31], v[68:71]\n"
"ds_read_b64_tr_b16 a[214:215], v201 offset:31264\n"
"v_accvgpr_write_b32 a62, v142\n"
"v_mfma_f32_16x16x32_bf16 v[64:67], a[144:147], v[32:35], v[64:67]\n"
"s_add_u32 m0, s84, 131072\n"
"ds_read_b64_tr_b16 a[216:217], v201 offset:35872\n"
"buffer_store_dwordx2 v[218:219], v207, s[28:31], s49 offen sc1\n"
"v_mfma_f32_16x16x32_bf16 v[68:71], a[144:147], v[36:39], v[68:71]\n"
"ds_read_b64_tr_b16 a[218:219], v201 offset:40480\n"
"buffer_load_dwordx4 v207, s[72:75], s50 offen offset:1024 lds\n"
"v_mfma_f32_16x16x32_bf16 v[64:67], a[148:151], v[40:43], v[64:67]\n"
"ds_read_b64_tr_b16 a[220:221], v201 offset:45088\n"
"buffer_store_dwordx2 v[222:223], v208, s[28:31], s49 offen sc1\n"
"v_mfma_f32_16x16x32_bf16 v[68:71], a[148:151], v[44:47], v[68:71]\n"
"ds_read_b64_tr_b16 a[222:223], v201 offset:49696\n"
"v_accvgpr_write_b32 a63, v143\n"
"v_mfma_f32_16x16x32_bf16 v[64:67], a[152:155], v[48:51], v[64:67]\n"
"ds_read_b64_tr_b16 a[128:129], v200 offset:46144\n"
"v_accvgpr_write_b32 a64, v144\n"
"v_mfma_f32_16x16x32_bf16 v[68:71], a[152:155], v[52:55], v[68:71]\n"
"s_waitcnt lgkmcnt(11)\n"
"ds_read_b64_tr_b16 a[130:131], v200 offset:50752\n"
"v_accvgpr_write_b32 a65, v145\n"
"v_mfma_f32_16x16x32_bf16 v[64:67], a[156:159], v[56:59], v[64:67]\n"
"s_add_u32 m0, s84, 131072\n"
"ds_read_b64_tr_b16 a[132:133], v200 offset:55360\n"
"v_accvgpr_write_b32 a66, v146\n"
"v_mfma_f32_16x16x32_bf16 v[68:71], a[156:159], v[60:63], v[68:71]\n"
"ds_read_b64_tr_b16 a[134:135], v200 offset:59968\n"
"buffer_load_dwordx4 v207, s[72:75], s50 offen offset:2048 lds\n"
"v_mfma_f32_16x16x32_bf16 v[72:75], a[192:195], v[0:3], v[72:75]\n"
"ds_read_b64_tr_b16 a[136:137], v200 offset:64576\n"
"v_accvgpr_write_b32 a67, v147\n"
"v_mfma_f32_16x16x32_bf16 v[76:79], a[192:195], v[4:7], v[76:79]\n"
"ds_read_b64_tr_b16 a[138:139], v201 offset:3648\n"
"v_accvgpr_write_b32 a68, v148\n"
"v_mfma_f32_16x16x32_bf16 v[72:75], a[196:199], v[8:11], v[72:75]\n"
"ds_read_b64_tr_b16 a[140:141], v201 offset:8256\n"
"v_accvgpr_write_b32 a69, v149\n"
"v_mfma_f32_16x16x32_bf16 v[76:79], a[196:199], v[12:15], v[76:79]\n"
"ds_read_b64_tr_b16 a[142:143], v201 offset:12864\n"
"v_cvt_pk_bf16_f32 v218, v64, v65\n"
"v_mfma_f32_16x16x32_bf16 v[72:75], a[200:203], v[16:19], v[72:75]\n"
"ds_read_b64_tr_b16 a[144:145], v201 offset:17472\n"
"v_cvt_pk_bf16_f32 v219, v66, v67\n"
"v_mfma_f32_16x16x32_bf16 v[76:79], a[200:203], v[20:23], v[76:79]\n"
"s_waitcnt lgkmcnt(11)\n"
"ds_read_b64_tr_b16 a[146:147], v201 offset:22080\n"
"v_cvt_pk_bf16_f32 v222, v68, v69\n"
"v_mfma_f32_16x16x32_bf16 v[72:75], a[204:207], v[24:27], v[72:75]\n"
"s_add_u32 m0, s84, 131072\n"
"ds_read_b64_tr_b16 a[148:149], v201 offset:26688\n"
"v_cvt_pk_bf16_f32 v223, v70, v71\n"
"v_mfma_f32_16x16x32_bf16 v[76:79], a[204:207], v[28:31], v[76:79]\n"
"ds_read_b64_tr_b16 a[150:151], v201 offset:31296\n"
"buffer_load_dwordx4 v207, s[72:75], s50 offen offset:3072 lds\n"
"v_mfma_f32_16x16x32_bf16 v[72:75], a[208:211], v[32:35], v[72:75]\n"
"ds_read_b64_tr_b16 a[152:153], v201 offset:35904\n"
"v_accvgpr_write_b32 a70, v150\n"
"v_mfma_f32_16x16x32_bf16 v[76:79], a[208:211], v[36:39], v[76:79]\n"
"ds_read_b64_tr_b16 a[154:155], v201 offset:40512\n"
"v_accvgpr_write_b32 a71, v151\n"
"v_mfma_f32_16x16x32_bf16 v[72:75], a[212:215], v[40:43], v[72:75]\n"
"ds_read_b64_tr_b16 a[156:157], v201 offset:45120\n"
"v_accvgpr_write_b32 a72, v152\n"
"v_mfma_f32_16x16x32_bf16 v[76:79], a[212:215], v[44:47], v[76:79]\n"
"ds_read_b64_tr_b16 a[158:159], v201 offset:49728\n"
"v_accvgpr_write_b32 a73, v153\n"
"v_mfma_f32_16x16x32_bf16 v[72:75], a[216:219], v[48:51], v[72:75]\n"
"s_add_u32 m0, s84, 135168\n"
"s_add_u32 s50, s50, 4096\n"
"ds_read_b64_tr_b16 a[192:193], v200 offset:46176\n"
"v_accvgpr_write_b32 a74, v154\n"
"v_mfma_f32_16x16x32_bf16 v[76:79], a[216:219], v[52:55], v[76:79]\n"
"s_waitcnt lgkmcnt(11)\n"
"ds_read_b64_tr_b16 a[194:195], v200 offset:50784\n"
"buffer_load_dwordx4 v207, s[72:75], s50 offen offset:0 lds\n"
"v_mfma_f32_16x16x32_bf16 v[72:75], a[220:223], v[56:59], v[72:75]\n"
"ds_read_b64_tr_b16 a[196:197], v200 offset:55392\n"
"v_accvgpr_write_b32 a75, v155\n"
"v_mfma_f32_16x16x32_bf16 v[76:79], a[220:223], v[60:63], v[76:79]\n"
"ds_read_b64_tr_b16 a[198:199], v200 offset:60000\n"
"v_accvgpr_write_b32 a76, v156\n"
"v_mfma_f32_16x16x32_bf16 v[80:83], a[128:131], v[0:3], v[80:83]\n"
"ds_read_b64_tr_b16 a[200:201], v200 offset:64608\n"
"v_accvgpr_write_b32 a77, v157\n"
"v_mfma_f32_16x16x32_bf16 v[84:87], a[128:131], v[4:7], v[84:87]\n"
"ds_read_b64_tr_b16 a[202:203], v201 offset:3680\n"
"v_accvgpr_write_b32 a78, v158\n"
"v_mfma_f32_16x16x32_bf16 v[80:83], a[132:135], v[8:11], v[80:83]\n"
"ds_read_b64_tr_b16 a[204:205], v201 offset:8288\n"
"v_accvgpr_write_b32 a79, v159\n"
"v_mfma_f32_16x16x32_bf16 v[84:87], a[132:135], v[12:15], v[84:87]\n"
"ds_read_b64_tr_b16 a[206:207], v201 offset:12896\n"
"v_cvt_pk_bf16_f32 v220, v72, v73\n"
"v_mfma_f32_16x16x32_bf16 v[80:83], a[136:139], v[16:19], v[80:83]\n"
"ds_read_b64_tr_b16 a[208:209], v201 offset:17504\n"
"v_cvt_pk_bf16_f32 v221, v74, v75\n"
"v_mfma_f32_16x16x32_bf16 v[84:87], a[136:139], v[20:23], v[84:87]\n"
"s_waitcnt lgkmcnt(11)\n"
"ds_read_b64_tr_b16 a[210:211], v201 offset:22112\n"
"v_cvt_pk_bf16_f32 v224, v76, v77\n"
"v_mfma_f32_16x16x32_bf16 v[80:83], a[140:143], v[24:27], v[80:83]\n"
"s_add_u32 m0, s84, 135168\n"
"ds_read_b64_tr_b16 a[212:213], v201 offset:26720\n"
"v_cvt_pk_bf16_f32 v225, v78, v79\n"
"v_mfma_f32_16x16x32_bf16 v[84:87], a[140:143], v[28:31], v[84:87]\n"
"ds_read_b64_tr_b16 a[214:215], v201 offset:31328\n"
"buffer_load_dwordx4 v207, s[72:75], s50 offen offset:1024 lds\n"
"v_mfma_f32_16x16x32_bf16 v[80:83], a[144:147], v[32:35], v[80:83]\n"
"ds_read_b64_tr_b16 a[216:217], v201 offset:35936\n"
"buffer_store_dwordx4 v[218:221], v207, s[28:31], s54 offen offset:0 sc1\n"
"v_mfma_f32_16x16x32_bf16 v[84:87], a[144:147], v[36:39], v[84:87]\n"
"ds_read_b64_tr_b16 a[218:219], v201 offset:40544\n"
"v_accvgpr_write_b32 a80, v160\n"
"v_mfma_f32_16x16x32_bf16 v[80:83], a[148:151], v[40:43], v[80:83]\n"
"ds_read_b64_tr_b16 a[220:221], v201 offset:45152\n"
"buffer_store_dwordx4 v[222:225], v208, s[28:31], s54 offen offset:0 sc1\n"
"v_mfma_f32_16x16x32_bf16 v[84:87], a[148:151], v[44:47], v[84:87]\n"
"ds_read_b64_tr_b16 a[222:223], v201 offset:49760\n"
"v_accvgpr_write_b32 a81, v161\n"
"v_mfma_f32_16x16x32_bf16 v[80:83], a[152:155], v[48:51], v[80:83]\n"
"s_add_u32 m0, s84, 135168\n"
"ds_read_b64_tr_b16 a[128:129], v200 offset:46208\n"
"v_accvgpr_write_b32 a82, v162\n"
"v_mfma_f32_16x16x32_bf16 v[84:87], a[152:155], v[52:55], v[84:87]\n"
"s_waitcnt lgkmcnt(11)\n"
"ds_read_b64_tr_b16 a[130:131], v200 offset:50816\n"
"buffer_load_dwordx4 v207, s[72:75], s50 offen offset:2048 lds\n"
"v_mfma_f32_16x16x32_bf16 v[80:83], a[156:159], v[56:59], v[80:83]\n"
"ds_read_b64_tr_b16 a[132:133], v200 offset:55424\n"
"v_accvgpr_write_b32 a83, v163\n"
"v_mfma_f32_16x16x32_bf16 v[84:87], a[156:159], v[60:63], v[84:87]\n"
"ds_read_b64_tr_b16 a[134:135], v200 offset:60032\n"
"v_accvgpr_write_b32 a84, v164\n"
"v_mfma_f32_16x16x32_bf16 v[88:91], a[192:195], v[0:3], v[88:91]\n"
"ds_read_b64_tr_b16 a[136:137], v200 offset:64640\n"
"v_accvgpr_write_b32 a85, v165\n"
"v_mfma_f32_16x16x32_bf16 v[92:95], a[192:195], v[4:7], v[92:95]\n"
"ds_read_b64_tr_b16 a[138:139], v201 offset:3712\n"
"v_accvgpr_write_b32 a86, v166\n"
"v_mfma_f32_16x16x32_bf16 v[88:91], a[196:199], v[8:11], v[88:91]\n"
"ds_read_b64_tr_b16 a[140:141], v201 offset:8320\n"
"v_accvgpr_write_b32 a87, v167\n"
"v_mfma_f32_16x16x32_bf16 v[92:95], a[196:199], v[12:15], v[92:95]\n"
"ds_read_b64_tr_b16 a[142:143], v201 offset:12928\n"
"v_cvt_pk_bf16_f32 v218, v80, v81\n"
"v_mfma_f32_16x16x32_bf16 v[88:91], a[200:203], v[16:19], v[88:91]\n"
"ds_read_b64_tr_b16 a[144:145], v201 offset:17536\n"
"v_cvt_pk_bf16_f32 v219, v82, v83\n"
"v_mfma_f32_16x16x32_bf16 v[92:95], a[200:203], v[20:23], v[92:95]\n"
"s_waitcnt lgkmcnt(11)\n"
"ds_read_b64_tr_b16 a[146:147], v201 offset:22144\n"
"v_cvt_pk_bf16_f32 v222, v84, v85\n"
"v_mfma_f32_16x16x32_bf16 v[88:91], a[204:207], v[24:27], v[88:91]\n"
"ds_read_b64_tr_b16 a[148:149], v201 offset:26752\n"
"v_cvt_pk_bf16_f32 v223, v86, v87\n"
"v_mfma_f32_16x16x32_bf16 v[92:95], a[204:207], v[28:31], v[92:95]\n"
"ds_read_b64_tr_b16 a[150:151], v201 offset:31360\n"
"v_accvgpr_write_b32 a88, v168\n"
"v_mfma_f32_16x16x32_bf16 v[88:91], a[208:211], v[32:35], v[88:91]\n"
"ds_read_b64_tr_b16 a[152:153], v201 offset:35968\n"
"v_accvgpr_write_b32 a89, v169\n"
"v_mfma_f32_16x16x32_bf16 v[92:95], a[208:211], v[36:39], v[92:95]\n"
"ds_read_b64_tr_b16 a[154:155], v201 offset:40576\n"
"v_accvgpr_write_b32 a90, v170\n"
"v_mfma_f32_16x16x32_bf16 v[88:91], a[212:215], v[40:43], v[88:91]\n"
"ds_read_b64_tr_b16 a[156:157], v201 offset:45184\n"
"v_accvgpr_write_b32 a91, v171\n"
"v_mfma_f32_16x16x32_bf16 v[92:95], a[212:215], v[44:47], v[92:95]\n"
"ds_read_b64_tr_b16 a[158:159], v201 offset:49792\n"
"v_accvgpr_write_b32 a92, v172\n"
"v_mfma_f32_16x16x32_bf16 v[88:91], a[216:219], v[48:51], v[88:91]\n"
"ds_read_b64_tr_b16 a[192:193], v200 offset:46240\n"
"v_accvgpr_write_b32 a93, v173\n"
"v_mfma_f32_16x16x32_bf16 v[92:95], a[216:219], v[52:55], v[92:95]\n"
"s_waitcnt lgkmcnt(11)\n"
"ds_read_b64_tr_b16 a[194:195], v200 offset:50848\n"
"v_accvgpr_write_b32 a94, v174\n"
"v_mfma_f32_16x16x32_bf16 v[88:91], a[220:223], v[56:59], v[88:91]\n"
"ds_read_b64_tr_b16 a[196:197], v200 offset:55456\n"
"v_accvgpr_write_b32 a95, v175\n"
"v_mfma_f32_16x16x32_bf16 v[92:95], a[220:223], v[60:63], v[92:95]\n"
"ds_read_b64_tr_b16 a[198:199], v200 offset:60064\n"
"v_accvgpr_write_b32 a96, v176\n"
"v_mfma_f32_16x16x32_bf16 v[96:99], a[128:131], v[0:3], v[96:99]\n"
"ds_read_b64_tr_b16 a[200:201], v200 offset:64672\n"
"v_accvgpr_write_b32 a97, v177\n"
"v_mfma_f32_16x16x32_bf16 v[100:103], a[128:131], v[4:7], v[100:103]\n"
"ds_read_b64_tr_b16 a[202:203], v201 offset:3744\n"
"v_accvgpr_write_b32 a98, v178\n"
"v_mfma_f32_16x16x32_bf16 v[96:99], a[132:135], v[8:11], v[96:99]\n"
"ds_read_b64_tr_b16 a[204:205], v201 offset:8352\n"
"v_accvgpr_write_b32 a99, v179\n"
"v_mfma_f32_16x16x32_bf16 v[100:103], a[132:135], v[12:15], v[100:103]\n"
"ds_read_b64_tr_b16 a[206:207], v201 offset:12960\n"
"v_cvt_pk_bf16_f32 v220, v88, v89\n"
"v_mfma_f32_16x16x32_bf16 v[96:99], a[136:139], v[16:19], v[96:99]\n"
"ds_read_b64_tr_b16 a[208:209], v201 offset:17568\n"
"v_cvt_pk_bf16_f32 v221, v90, v91\n"
"v_mfma_f32_16x16x32_bf16 v[100:103], a[136:139], v[20:23], v[100:103]\n"
"s_waitcnt lgkmcnt(11)\n"
"ds_read_b64_tr_b16 a[210:211], v201 offset:22176\n"
"v_cvt_pk_bf16_f32 v224, v92, v93\n"
"v_mfma_f32_16x16x32_bf16 v[96:99], a[140:143], v[24:27], v[96:99]\n"
"ds_read_b64_tr_b16 a[212:213], v201 offset:26784\n"
"v_cvt_pk_bf16_f32 v225, v94, v95\n"
"v_mfma_f32_16x16x32_bf16 v[100:103], a[140:143], v[28:31], v[100:103]\n"
"ds_read_b64_tr_b16 a[214:215], v201 offset:31392\n"
"v_accvgpr_write_b32 a100, v180\n"
"v_mfma_f32_16x16x32_bf16 v[96:99], a[144:147], v[32:35], v[96:99]\n"
"ds_read_b64_tr_b16 a[216:217], v201 offset:36000\n"
"buffer_store_dwordx4 v[218:221], v207, s[28:31], s54 offen offset:1024 sc1\n"
"v_mfma_f32_16x16x32_bf16 v[100:103], a[144:147], v[36:39], v[100:103]\n"
"ds_read_b64_tr_b16 a[218:219], v201 offset:40608\n"
"buffer_store_dword v252, v251, s[28:31], 0 offen sc1\n"
"v_mfma_f32_16x16x32_bf16 v[96:99], a[148:151], v[40:43], v[96:99]\n"
"ds_read_b64_tr_b16 a[220:221], v201 offset:45216\n"
"buffer_store_dwordx4 v[222:225], v208, s[28:31], s54 offen offset:1024 sc1\n"
"v_mfma_f32_16x16x32_bf16 v[100:103], a[148:151], v[44:47], v[100:103]\n"
"ds_read_b64_tr_b16 a[222:223], v201 offset:49824\n"
"v_accvgpr_write_b32 a101, v181\n"
"v_mfma_f32_16x16x32_bf16 v[96:99], a[152:155], v[48:51], v[96:99]\n"
"ds_read_b64_tr_b16 a[128:129], v200 offset:46272\n"
"v_accvgpr_write_b32 a102, v182\n"
"v_mfma_f32_16x16x32_bf16 v[100:103], a[152:155], v[52:55], v[100:103]\n"
"s_waitcnt lgkmcnt(11)\n"
"ds_read_b64_tr_b16 a[130:131], v200 offset:50880\n"
"v_accvgpr_write_b32 a103, v183\n"
"v_mfma_f32_16x16x32_bf16 v[96:99], a[156:159], v[56:59], v[96:99]\n"
"ds_read_b64_tr_b16 a[132:133], v200 offset:55488\n"
"v_accvgpr_write_b32 a104, v184\n"
"v_mfma_f32_16x16x32_bf16 v[100:103], a[156:159], v[60:63], v[100:103]\n"
"ds_read_b64_tr_b16 a[134:135], v200 offset:60096\n"
"v_accvgpr_write_b32 a105, v185\n"
"v_mfma_f32_16x16x32_bf16 v[104:107], a[192:195], v[0:3], v[104:107]\n"
"ds_read_b64_tr_b16 a[136:137], v200 offset:64704\n"
"v_accvgpr_write_b32 a106, v186\n"
"v_mfma_f32_16x16x32_bf16 v[108:111], a[192:195], v[4:7], v[108:111]\n"
"ds_read_b64_tr_b16 a[138:139], v201 offset:3776\n"
"v_accvgpr_write_b32 a107, v187\n"
"v_mfma_f32_16x16x32_bf16 v[104:107], a[196:199], v[8:11], v[104:107]\n"
"ds_read_b64_tr_b16 a[140:141], v201 offset:8384\n"
"v_accvgpr_write_b32 a108, v188\n"
"v_mfma_f32_16x16x32_bf16 v[108:111], a[196:199], v[12:15], v[108:111]\n"
"ds_read_b64_tr_b16 a[142:143], v201 offset:12992\n"
"v_cvt_pk_bf16_f32 v218, v96, v97\n"
"v_mfma_f32_16x16x32_bf16 v[104:107], a[200:203], v[16:19], v[104:107]\n"
"ds_read_b64_tr_b16 a[144:145], v201 offset:17600\n"
"v_cvt_pk_bf16_f32 v219, v98, v99\n"
"v_mfma_f32_16x16x32_bf16 v[108:111], a[200:203], v[20:23], v[108:111]\n"
"s_waitcnt lgkmcnt(11)\n"
"ds_read_b64_tr_b16 a[146:147], v201 offset:22208\n"
"v_cvt_pk_bf16_f32 v222, v100, v101\n"
"v_mfma_f32_16x16x32_bf16 v[104:107], a[204:207], v[24:27], v[104:107]\n"
"ds_read_b64_tr_b16 a[148:149], v201 offset:26816\n"
"v_cvt_pk_bf16_f32 v223, v102, v103\n"
"v_mfma_f32_16x16x32_bf16 v[108:111], a[204:207], v[28:31], v[108:111]\n"
"s_waitcnt vmcnt(14)\n"
"ds_read_b64_tr_b16 a[150:151], v201 offset:31424\n"
"v_accvgpr_write_b32 a109, v189\n"
"v_mfma_f32_16x16x32_bf16 v[104:107], a[208:211], v[32:35], v[104:107]\n"
"s_barrier\n"
"ds_read_b64_tr_b16 a[152:153], v201 offset:36032\n"
"v_accvgpr_write_b32 a110, v190\n"
"v_mfma_f32_16x16x32_bf16 v[108:111], a[208:211], v[36:39], v[108:111]\n"
"ds_read_b64_tr_b16 a[154:155], v201 offset:40640\n"
"v_accvgpr_write_b32 a111, v191\n"
"v_mfma_f32_16x16x32_bf16 v[104:107], a[212:215], v[40:43], v[104:107]\n"
"ds_read_b64_tr_b16 a[156:157], v201 offset:45248\n"
"v_accvgpr_write_b32 a112, v192\n"
"v_mfma_f32_16x16x32_bf16 v[108:111], a[212:215], v[44:47], v[108:111]\n"
"ds_read_b64_tr_b16 a[158:159], v201 offset:49856\n"
"v_accvgpr_write_b32 a113, v193\n"
"v_mfma_f32_16x16x32_bf16 v[104:107], a[216:219], v[48:51], v[104:107]\n"
"ds_read_b64_tr_b16 a[192:193], v200 offset:46304\n"
"v_accvgpr_write_b32 a114, v194\n"
"v_mfma_f32_16x16x32_bf16 v[108:111], a[216:219], v[52:55], v[108:111]\n"
"s_waitcnt lgkmcnt(11)\n"
"ds_read_b64_tr_b16 a[194:195], v200 offset:50912\n"
"v_accvgpr_write_b32 a115, v195\n"
"v_mfma_f32_16x16x32_bf16 v[104:107], a[220:223], v[56:59], v[104:107]\n"
"ds_read_b64_tr_b16 a[196:197], v200 offset:55520\n"
"v_accvgpr_write_b32 a116, v196\n"
"v_mfma_f32_16x16x32_bf16 v[108:111], a[220:223], v[60:63], v[108:111]\n"
"ds_read_b64_tr_b16 a[198:199], v200 offset:60128\n"
"v_accvgpr_write_b32 a117, v197\n"
"v_mfma_f32_16x16x32_bf16 v[112:115], a[128:131], v[0:3], v[112:115]\n"
"ds_read_b64_tr_b16 a[200:201], v200 offset:64736\n"
"v_accvgpr_write_b32 a118, v198\n"
"v_mfma_f32_16x16x32_bf16 v[116:119], a[128:131], v[4:7], v[116:119]\n"
"ds_read_b64_tr_b16 a[202:203], v201 offset:3808\n"
"v_accvgpr_write_b32 a119, v199\n"
"v_mfma_f32_16x16x32_bf16 v[112:115], a[132:135], v[8:11], v[112:115]\n"
"ds_read_b64_tr_b16 a[204:205], v201 offset:8416\n"
"v_mfma_f32_16x16x32_bf16 v[116:119], a[132:135], v[12:15], v[116:119]\n"
"ds_read_b64_tr_b16 a[206:207], v201 offset:13024\n"
"v_cvt_pk_bf16_f32 v220, v104, v105\n"
"v_mfma_f32_16x16x32_bf16 v[112:115], a[136:139], v[16:19], v[112:115]\n"
"ds_read_b64_tr_b16 a[208:209], v201 offset:17632\n"
"v_cvt_pk_bf16_f32 v221, v106, v107\n"
"v_mfma_f32_16x16x32_bf16 v[116:119], a[136:139], v[20:23], v[116:119]\n"
"s_waitcnt lgkmcnt(11)\n"
"ds_read_b64_tr_b16 a[210:211], v201 offset:22240\n"
"v_cvt_pk_bf16_f32 v224, v108, v109\n"
"v_mfma_f32_16x16x32_bf16 v[112:115], a[140:143], v[24:27], v[112:115]\n"
"ds_read_b64_tr_b16 a[212:213], v201 offset:26848\n"
"v_cvt_pk_bf16_f32 v225, v110, v111\n"
"v_mfma_f32_16x16x32_bf16 v[116:119], a[140:143], v[28:31], v[116:119]\n"
"ds_read_b64_tr_b16 a[214:215], v201 offset:31456\n"
"v_mfma_f32_16x16x32_bf16 v[112:115], a[144:147], v[32:35], v[112:115]\n"
"ds_read_b64_tr_b16 a[216:217], v201 offset:36064\n"
"buffer_store_dwordx4 v[218:221], v207, s[28:31], s54 offen offset:2048 sc1\n"
"v_mfma_f32_16x16x32_bf16 v[116:119], a[144:147], v[36:39], v[116:119]\n"
"ds_read_b64_tr_b16 a[218:219], v201 offset:40672\n"
"v_mfma_f32_16x16x32_bf16 v[112:115], a[148:151], v[40:43], v[112:115]\n"
"ds_read_b64_tr_b16 a[220:221], v201 offset:45280\n"
"buffer_store_dwordx4 v[222:225], v208, s[28:31], s54 offen offset:2048 sc1\n"
"v_mfma_f32_16x16x32_bf16 v[116:119], a[148:151], v[44:47], v[116:119]\n"
"ds_read_b64_tr_b16 a[222:223], v201 offset:49888\n"
"v_mfma_f32_16x16x32_bf16 v[112:115], a[152:155], v[48:51], v[112:115]\n"
"s_nop 0\n"
"v_mfma_f32_16x16x32_bf16 v[116:119], a[152:155], v[52:55], v[116:119]\n"
"s_waitcnt lgkmcnt(10)\n"
"v_mfma_f32_16x16x32_bf16 v[112:115], a[156:159], v[56:59], v[112:115]\n"
"s_nop 0\n"
"v_mfma_f32_16x16x32_bf16 v[116:119], a[156:159], v[60:63], v[116:119]\n"
"s_nop 0\n"
"v_mfma_f32_16x16x32_bf16 v[120:123], a[192:195], v[0:3], v[120:123]\n"
"s_nop 0\n"
"v_mfma_f32_16x16x32_bf16 v[124:127], a[192:195], v[4:7], v[124:127]\n"
"s_nop 0\n"
"v_mfma_f32_16x16x32_bf16 v[120:123], a[196:199], v[8:11], v[120:123]\n"
"s_nop 0\n"
"v_mfma_f32_16x16x32_bf16 v[124:127], a[196:199], v[12:15], v[124:127]\n"
"v_cvt_pk_bf16_f32 v218, v112, v113\n"
"v_mfma_f32_16x16x32_bf16 v[120:123], a[200:203], v[16:19], v[120:123]\n"
"v_cvt_pk_bf16_f32 v219, v114, v115\n"
"v_mfma_f32_16x16x32_bf16 v[124:127], a[200:203], v[20:23], v[124:127]\n"
"s_waitcnt lgkmcnt(2)\n"
"v_cvt_pk_bf16_f32 v222, v116, v117\n"
"v_mfma_f32_16x16x32_bf16 v[120:123], a[204:207], v[24:27], v[120:123]\n"
"v_cvt_pk_bf16_f32 v223, v118, v119\n"
"v_mfma_f32_16x16x32_bf16 v[124:127], a[204:207], v[28:31], v[124:127]\n"
"s_nop 0\n"
"v_mfma_f32_16x16x32_bf16 v[120:123], a[208:211], v[32:35], v[120:123]\n"
"s_nop 0\n"
"v_mfma_f32_16x16x32_bf16 v[124:127], a[208:211], v[36:39], v[124:127]\n"
"s_nop 0\n"
"v_mfma_f32_16x16x32_bf16 v[120:123], a[212:215], v[40:43], v[120:123]\n"
"s_nop 0\n"
"v_mfma_f32_16x16x32_bf16 v[124:127], a[212:215], v[44:47], v[124:127]\n"
"s_nop 0\n"
"v_mfma_f32_16x16x32_bf16 v[120:123], a[216:219], v[48:51], v[120:123]\n"
"s_nop 0\n"
"v_mfma_f32_16x16x32_bf16 v[124:127], a[216:219], v[52:55], v[124:127]\n"
"s_waitcnt lgkmcnt(0)\n"
"v_mfma_f32_16x16x32_bf16 v[120:123], a[220:223], v[56:59], v[120:123]\n"
"s_nop 0\n"
"v_mfma_f32_16x16x32_bf16 v[124:127], a[220:223], v[60:63], v[124:127]\n"
"s_nop 0\n"
"s_nop 1\n"
"s_nop 1\n"
"s_nop 1\n"
"s_nop 1\n"
"v_cvt_pk_bf16_f32 v220, v120, v121\n"
"s_nop 1\n"
"v_cvt_pk_bf16_f32 v221, v122, v123\n"
"s_nop 1\n"
"v_cvt_pk_bf16_f32 v224, v124, v125\n"
"s_nop 1\n"
"v_cvt_pk_bf16_f32 v225, v126, v127\n"
"s_nop 1\n"
"s_nop 1\n"
"buffer_store_dwordx4 v[218:221], v207, s[28:31], s54 offen offset:3072 sc1\n"
"s_nop 1\n"
"s_nop 1\n"
"buffer_store_dwordx4 v[222:225], v208, s[28:31], s54 offen offset:3072 sc1\n"
"s_nop 1\n"
"s_add_u32 s56, s40, 161024\n"
"s_nop 1\n"
"v_mov_b32 v227, s56\n"
"v_mov_b32 v224, 0x3f80\n"
"v_mov_b32 v225, 0x3f800000\n"
"v_and_b32 v226, 7, v248\n"
"v_lshl_add_u32 v226, v226, 4, v227\n"
"s_mov_b32 s14, 0xfffff800\n"
"s_cmp_lg_u32 s41, 3\n"
"s_cbranch_scc1 AT_A0\n"
"s_mov_b64 s[8:9], 0\n"
"s_mul_i32 s63, s41, 9216\n"
"s_add_u32 s63, s63, s40\n"
"s_add_u32 s63, s63, 9216\n"
"v_lshrrev_b32 v185, 2, v248\n"
"v_and_b32 v186, 3, v248\n"
"v_mul_u32_u24 v187, 96, v185\n"
"v_lshl_add_u32 v188, v186, 4, s63\n"
"v_lshl_add_u32 v189, v187, 1, v188\n"
"v_add_u32 v188, v187, v188\n"
"v_lshrrev_b32 v190, 4, v248\n"
"v_mul_u32_u24 v191, 384, v190\n"
"v_add_u32 v187, v187, v191\n"
"v_lshl_add_u32 v187, v186, 3, v187\n"
"v_add_u32 v187, s63, v187\n"
"s_mov_b32 exec_lo, 0x77777777\n"
"s_mov_b32 exec_hi, 0x77777777\n"
"ds_write_b128 v189, a[48:51] offset:0\n"
"ds_write_b128 v189, a[84:87] offset:48\n"
"ds_write_b128 v189, a[52:55] offset:96\n"
"ds_write_b128 v189, a[88:91] offset:144\n"
"ds_write_b128 v189, a[56:59] offset:3072\n"
"ds_write_b128 v189, a[92:95] offset:3120\n"
"ds_write_b128 v189, a[60:63] offset:3168\n"
"ds_write_b128 v189, a[96:99] offset:3216\n"
"ds_write_b128 v189, a[64:67] offset:6144\n"
"ds_write_b128 v189, a[100:103] offset:6192\n"
"ds_write_b128 v189, a[68:71] offset:6240\n"
"ds_write_b128 v189, a[104:107] offset:6288\n"
"s_mov_b64 exec, -1\n"
"ds_read_b64_tr_b16 a[128:129], v187 offset:0\n"
"ds_read_b64_tr_b16 a[130:131], v187 offset:384\n"
"ds_read_b64_tr_b16 a[132:133], v187 offset:32\n"
"ds_read_b64_tr_b16 a[134:135], v187 offset:416\n"
"ds_read_b64_tr_b16 a[136:137], v187 offset:64\n"
"ds_read_b64_tr_b16 a[138:139], v187 offset:448\n"
"ds_read_b64_tr_b16 a[140:141], v187 offset:3072\n"
"ds_read_b64_tr_b16 a[142:143], v187 offset:3456\n"
"ds_read_b64_tr_b16 a[144:145], v187 offset:3104\n"
"ds_read_b64_tr_b16 a[146:147], v187 offset:3488\n"
"ds_read_b64_tr_b16 a[148:149], v187 offset:3136\n"
"ds_read_b64_tr_b16 a[150:151], v187 offset:3520\n"
"ds_read_b64_tr_b16 a[152:153], v187 offset:6144\n"
"ds_read_b64_tr_b16 a[154:155], v187 offset:6528\n"
"ds_read_b64_tr_b16 a[156:157], v187 offset:6176\n"
"ds_read_b64_tr_b16 a[158:159], v187 offset:6560\n"
"ds_read_b64_tr_b16 a[160:161], v187 offset:6208\n"
"ds_read_b64_tr_b16 a[162:163], v187 offset:6592\n"
"s_mov_b32 exec_lo, 0x77777777\n"
"s_mov_b32 exec_hi, 0x77777777\n"
"ds_write_b128 v189, a[72:75] offset:0\n"
"ds_write_b128 v189, a[108:111] offset:48\n"
"ds_write_b128 v189, a[76:79] offset:96\n"
"ds_write_b128 v189, a[112:115] offset:144\n"
"ds_write_b128 v188, a[80:83] offset:3072\n"
"ds_write_b128 v188, a[116:119] offset:3120\n"
"s_mov_b64 exec, -1\n"
"ds_read_b64_tr_b16 a[164:165], v187 offset:0\n"
"ds_read_b64_tr_b16 a[166:167], v187 offset:384\n"
"ds_read_b64_tr_b16 a[168:169], v187 offset:32\n"
"ds_read_b64_tr_b16 a[170:171], v187 offset:416\n"
"ds_read_b64_tr_b16 a[172:173], v187 offset:64\n"
"ds_read_b64_tr_b16 a[174:175], v187 offset:448\n"
"ds_read_b64_tr_b16 a[176:177], v187 offset:3072\n"
"ds_read_b64_tr_b16 a[178:179], v187 offset:3456\n"
"ds_read_b64_tr_b16 a[180:181], v187 offset:3104\n"
"ds_read_b64_tr_b16 a[182:183], v187 offset:3488\n"
"ds_read_b64_tr_b16 a[184:185], v187 offset:3136\n"
"ds_read_b64_tr_b16 a[186:187], v187 offset:3520\n"
"v_and_b32 v241, 1, v248\n"
"v_mul_u32_u24 v241, 1152, v241\n"
"v_lshl_add_u32 v241, v190, 4, v241\n"
"s_mul_i32 s63, s41, 288\n"
"v_add_u32 v241, s63, v241\n"
"v_and_b32 v242, 10, v248\n"
"v_lshl_add_u32 v241, v242, 26, v241\n"
"v_and_b32 v242, 32, v248\n"
"v_lshlrev_b32 v242, 25, v242\n"
"s_add_u32 s63, s63, s40\n"
"s_add_u32 s63, s63, 131072\n"
"v_cmp_eq_u32 vcc, 3, v190\n"
"v_and_b32 v185, 3, v185\n"
"v_lshlrev_b32 v191, 3, v190\n"
"v_cndmask_b32_e64 v191, v191, 0, vcc\n"
"v_add_u32 v191, v191, v185\n"
"v_mul_u32_u24 v191, 1152, v191\n"
"v_lshl_add_u32 v191, v186, 3, v191\n"
"v_add_u32 v255, s63, v191\n"
"v_lshrrev_b32 v243, 3, v248\n"
"v_mul_u32_u24 v243, 24576, v243\n"
"v_and_b32 v247, 7, v248\n"
"v_lshl_add_u32 v243, v247, 4, v243\n"
"v_mul_u32_u24 v224, 24576, v190\n"
"v_and_b32 v246, 15, v248\n"
"v_lshl_add_u32 v224, v246, 3, v224\n"
"v_or_b32 v224, v242, v224\n"
"v_subrev_u32 v252, 65536, v227\n"
"s_mul_i32 s63, s41, 384\n"
"v_mul_u32_u24 v244, 192, v190\n"
"v_lshl_add_u32 v244, v246, 2, v244\n"
"v_add3_u32 v244, v244, v252, s63\n"
"v_cmp_lt_u32 vcc, 23, v248\n"
"v_lshl_add_u32 v249, v248, 2, v252\n"
"v_add_u32 v185, 96, v249\n"
"v_cndmask_b32 v249, v249, v185, vcc\n"
"s_lshl_b32 s63, s41, 9\n"
"v_add_u32 v252, s63, v252\n"
"v_lshl_add_u32 v251, v248, 1, v252\n"
"v_add_u32 v185, 208, v251\n"
"v_cndmask_b32 v251, v251, v185, vcc\n"
"v_lshl_add_u32 v185, v248, 3, v252\n"
"v_lshl_add_u32 v252, v247, 6, v252\n"
"v_lshl_add_u32 v252, v190, 4, v252\n"
"s_min_u32 s63, s41, 2\n"
"s_mul_i32 s63, s63, 192\n"
"v_add_u32 v183, s63, v248\n"
"v_mul_u32_u24 v183, 911, v183\n"
"s_lshl_b32 s63, s55, 3\n"
"v_lshrrev_b32 v180, 16, v183\n"
"v_add_u32 v180, s63, v180\n"
"v_add_u32 v183, 58304, v183\n"
"v_lshrrev_b32 v181, 16, v183\n"
"v_add_u32 v181, s63, v181\n"
"v_add_u32 v183, 58304, v183\n"
"v_lshrrev_b32 v182, 16, v183\n"
"v_add_u32 v182, s63, v182\n"
"v_lshlrev_b32 v250, 4, v248\n"
"v_add_u32 v184, s45, v250\n"
"s_cmp_eq_u32 s41, 3\n"
"s_cselect_b32 s63, 0x10000000, 0\n"
"v_or_b32 v250, s63, v250\n"
"ds_read_b128 v[212:215], v227 offset:512\n"
"ds_read_b128 v[216:219], v227 offset:528\n"
"s_lshl_b32 s56, s55, 3\n"
"s_sub_u32 s56, 2047, s56\n"
"v_and_b32 v220, 7, v248\n"
"v_sub_u32 v220, s56, v220\n"
"s_waitcnt lgkmcnt(0)\n"
"v_add_f32 v212, v212, v214\n"
"v_add_f32 v213, v213, v215\n"
"v_add_f32 v212, v212, v216\n"
"v_add_f32 v213, v213, v217\n"
"v_mov_b32 v145, s85\n"
"v_mov_b32 v147, s85\n"
"s_mul_i32 s56, s55, 64\n"
"s_add_u32 s56, s56, 26370048\n"
"v_mov_b32 v148, 0\n"
"v_mul_u32_u24 v149, 64, v248\n"
"s_mov_b32 s57, 26370048\n"
"s_mov_b32 s58, 26374144\n"
"s_mov_b32 s59, 26378240\n"
"s_mov_b32 s60, 26382336\n"
"s_mov_b32 s87, 0x80000\n"
"s_waitcnt vmcnt(0)\n"
"s_barrier\n"
"ds_read_b128 v[200:203], v226 offset:0\n"
"ds_read_b128 v[204:207], v226 offset:128\n"
"ds_read_b128 v[208:211], v226 offset:256\n"
"s_waitcnt lgkmcnt(1)\n"
"v_add_f32 v200, v200, v204\n"
"v_add_f32 v201, v201, v205\n"
"v_add_f32 v202, v202, v206\n"
"s_waitcnt lgkmcnt(0)\n"
"v_add_f32 v200, v200, v208\n"
"v_add_f32 v201, v201, v209\n"
"v_add_f32 v202, v202, v210\n"
"v_mul_f32 v200, 0x3ae38e39, v200\n"
"v_mul_f32 v201, 0x3ae38e39, v201\n"
"v_fma_f32 v201, -v200, v200, v201\n"
"v_max_f32 v201, 0, v201\n"
"v_add_f32 v201, 0x3727c5ac, v201\n"
"v_rsq_f32 v192, v201\n"
"v_fma_f32 v202, -v200, v212, v202\n"
"v_fma_f32 v196, v192, v202, v213\n"
"v_ashrrev_i32 v203, 31, v196\n"
"v_or_b32 v203, 0x80000000, v203\n"
"v_xor_b32 v196, v196, v203\n"
"v_and_or_b32 v196, v196, s14, v220\n"
"v_mul_f32 v194, v200, v192\n"
"v_sub_f32 v194, 0, v194\n"
"v_mov_b32_dpp v203, v196 row_ror:1 row_mask:0xf bank_mask:0xf\n"
"v_min_u32 v198, v196, v203\n"
"v_max_u32 v197, v196, v203\n"
"s_nop 0\n"
"v_mov_b32_dpp v204, v198 row_ror:2 row_mask:0xf bank_mask:0xf\n"
"v_mov_b32_dpp v203, v197 row_ror:2 row_mask:0xf bank_mask:0xf\n"
"v_min_u32 v205, v197, v203\n"
"v_max_u32 v197, v197, v203\n"
"v_max3_u32 v198, v198, v204, v205\n"
"s_nop 0\n"
"v_mov_b32_dpp v203, v197 row_ror:4 row_mask:0xf bank_mask:0xf\n"
"v_mov_b32_dpp v204, v198 row_ror:4 row_mask:0xf bank_mask:0xf\n"
"v_min_u32 v205, v197, v203\n"
"v_max_u32 v197, v197, v203\n"
"v_max3_u32 v198, v198, v204, v205\n"
"v_mov_b32 v144, v197\n"
"v_mov_b32 v146, v198\n"
"s_mov_b64 exec, 1\n"
"buffer_store_dwordx4 v[144:147], v148, s[28:31], s56 offen sc1\n"
"s_mov_b64 exec, -1\n"
"AT_S2:\n"
"buffer_load_dwordx4 v[152:155], v149, s[28:31], s57 offen sc1\n"
"buffer_load_dwordx4 v[156:159], v149, s[28:31], s58 offen sc1\n"
"buffer_load_dwordx4 v[160:163], v149, s[28:31], s59 offen sc1\n"
"buffer_load_dwordx4 v[164:167], v149, s[28:31], s60 offen sc1\n"
"s_waitcnt vmcnt(0)\n"
"v_xor_b32 v150, s85, v153\n"
"v_xor_b32 v151, s85, v155\n"
"v_bitop3_b32 v150, v157, s85, v150 bitop3:0xbe\n"
"v_bitop3_b32 v151, v159, s85, v151 bitop3:0xbe\n"
"v_bitop3_b32 v150, v161, s85, v150 bitop3:0xbe\n"
"v_bitop3_b32 v151, v163, s85, v151 bitop3:0xbe\n"
"v_bitop3_b32 v150, v165, s85, v150 bitop3:0xbe\n"
"v_bitop3_b32 v151, v167, s85, v151 bitop3:0xbe\n"
"v_or_b32 v150, v150, v151\n"
"v_cmp_ne_u32 vcc, 0, v150\n"
"s_nop 0\n"
"s_cbranch_vccz AT_S3\n"
"s_sub_u32 s87, s87, 1\n"
"s_cmp_eq_u32 s87, 0\n"
"s_cbranch_scc1 AT_S3\n"
"s_sleep 1\n"
"s_branch AT_S2\n"
"AT_S3:\n"
"v_min_u32 v205, v152, v156\n"
"v_max_u32 v152, v152, v156\n"
"v_max3_u32 v154, v154, v158, v205\n"
"v_min_u32 v205, v160, v164\n"
"v_max_u32 v160, v160, v164\n"
"v_max3_u32 v162, v162, v166, v205\n"
"v_min_u32 v205, v152, v160\n"
"v_max_u32 v152, v152, v160\n"
"v_max3_u32 v154, v154, v162, v205\n"
"s_nop 0\n"
"v_mov_b32_dpp v156, v152 row_ror:1 row_mask:0xf bank_mask:0xf\n"
"v_mov_b32_dpp v158, v154 row_ror:1 row_mask:0xf bank_mask:0xf\n"
"v_min_u32 v205, v152, v156\n"
"v_max_u32 v152, v152, v156\n"
"v_max3_u32 v154, v154, v158, v205\n"
"s_nop 0\n"
"v_mov_b32_dpp v156, v152 row_ror:2 row_mask:0xf bank_mask:0xf\n"
"v_mov_b32_dpp v158, v154 row_ror:2 row_mask:0xf bank_mask:0xf\n"
"v_min_u32 v205, v152, v156\n"
"v_max_u32 v152, v152, v156\n"
"v_max3_u32 v154, v154, v158, v205\n"
"s_nop 0\n"
"v_mov_b32_dpp v156, v152 row_ror:4 row_mask:0xf bank_mask:0xf\n"
"v_mov_b32_dpp v158, v154 row_ror:4 row_mask:0xf bank_mask:0xf\n"
"v_min_u32 v205, v152, v156\n"
"v_max_u32 v152, v152, v156\n"
"v_max3_u32 v154, v154, v158, v205\n"
"s_nop 0\n"
"v_mov_b32_dpp v156, v152 row_ror:8 row_mask:0xf bank_mask:0xf\n"
"v_mov_b32_dpp v158, v154 row_ror:8 row_mask:0xf bank_mask:0xf\n"
"v_min_u32 v205, v152, v156\n"
"v_max_u32 v152, v152, v156\n"
"v_max3_u32 v154, v154, v158, v205\n"
"v_mov_b32 v156, v152\n"
"v_mov_b32 v158, v154\n"
"s_nop 0\n"
"v_permlane16_swap_b32 v152, v156\n"
"v_permlane16_swap_b32 v154, v158\n"
"v_min_u32 v205, v152, v156\n"
"v_max_u32 v152, v152, v156\n"
"v_max3_u32 v154, v154, v158, v205\n"
"v_mov_b32 v156, v152\n"
"v_mov_b32 v158, v154\n"
"s_nop 0\n"
"v_permlane32_swap_b32 v152, v156\n"
"v_permlane32_swap_b32 v154, v158\n"
"v_min_u32 v205, v152, v156\n"
"v_max_u32 v152, v152, v156\n"
"v_max3_u32 v154, v154, v158, v205\n"
"v_mov_b32 v153, v154\n"
"s_mov_b64 exec, 1\n"
"ds_write_b64 v227, v[152:153] offset:656\n"
"s_mov_b64 exec, -1\n"
"s_branch AT_B3\n"
"AT_A0:\n"
"v_and_b32 v192, 15, v248\n"
"v_lshlrev_b32 v192, 6, v192\n"
"s_lshl_b32 s57, s44, 10\n"
"buffer_load_dword v193, v192, s[28:31], s57 offen sc1\n"
"ds_read_b64 v[228:229], v240 offset:0\n"
"ds_read_b64 v[230:231], v240 offset:32\n"
"ds_read_b64 v[232:233], v240 offset:384\n"
"ds_read_b64 v[234:235], v240 offset:416\n"
"ds_read_b64 v[236:237], v240 offset:768\n"
"ds_read_b64 v[238:239], v240 offset:800\n"
"ds_read_b128 v[96:99], v244 offset:0\n"
"ds_read_b128 v[100:103], v244 offset:64\n"
"ds_read_b128 v[104:107], v244 offset:768\n"
"ds_read_b128 v[108:111], v244 offset:832\n"
"ds_read_b128 v[112:115], v244 offset:1536\n"
"ds_read_b128 v[116:119], v244 offset:1600\n"
"ds_read_b128 v[120:123], v244 offset:2304\n"
"ds_read_b128 v[124:127], v244 offset:2368\n"
"ds_read_b128 v[128:131], v244 offset:3072\n"
"ds_read_b128 v[132:135], v244 offset:3136\n"
"ds_read_b128 v[136:139], v244 offset:3840\n"
"ds_read_b128 v[140:143], v244 offset:3904\n"
"v_mov_b32 v48, 0\n"
"v_mov_b32 v49, 0\n"
"v_mov_b32 v50, 0\n"
"v_mov_b32 v51, 0\n"
"v_mov_b32 v52, 0\n"
"v_mov_b32 v53, 0\n"
"v_mov_b32 v54, 0\n"
"v_mov_b32 v55, 0\n"
"v_mov_b32 v56, 0\n"
"v_mov_b32 v57, 0\n"
"v_mov_b32 v58, 0\n"
"v_mov_b32 v59, 0\n"
"v_mov_b32 v60, 0\n"
"v_mov_b32 v61, 0\n"
"v_mov_b32 v62, 0\n"
"v_mov_b32 v63, 0\n"
"v_mov_b32 v64, 0\n"
"v_mov_b32 v65, 0\n"
"v_mov_b32 v66, 0\n"
"v_mov_b32 v67, 0\n"
"v_mov_b32 v68, 0\n"
"v_mov_b32 v69, 0\n"
"v_mov_b32 v70, 0\n"
"v_mov_b32 v71, 0\n"
"v_mov_b32 v72, 0\n"
"v_mov_b32 v73, 0\n"
"v_mov_b32 v74, 0\n"
"v_mov_b32 v75, 0\n"
"v_mov_b32 v76, 0\n"
"v_mov_b32 v77, 0\n"
"v_mov_b32 v78, 0\n"
"v_mov_b32 v79, 0\n"
"v_mov_b32 v80, 0\n"
"v_mov_b32 v81, 0\n"
"v_mov_b32 v82, 0\n"
"v_mov_b32 v83, 0\n"
"v_mov_b32 v84, 0\n"
"v_mov_b32 v85, 0\n"
"v_mov_b32 v86, 0\n"
"v_mov_b32 v87, 0\n"
"v_mov_b32 v88, 0\n"
"v_mov_b32 v89, 0\n"
"v_mov_b32 v90, 0\n"
"v_mov_b32 v91, 0\n"
"v_mov_b32 v92, 0\n"
"v_mov_b32 v93, 0\n"
"v_mov_b32 v94, 0\n"
"v_mov_b32 v95, 0\n"
"ds_read_b128 v[212:215], v227 offset:512\n"
"ds_read_b128 v[216:219], v227 offset:528\n"
"s_lshl_b32 s56, s55, 3\n"
"s_sub_u32 s56, 2047, s56\n"
"v_and_b32 v220, 7, v248\n"
"v_sub_u32 v220, s56, v220\n"
"s_waitcnt lgkmcnt(0)\n"
"v_add_f32 v212, v212, v214\n"
"v_add_f32 v213, v213, v215\n"
"v_add_f32 v212, v212, v216\n"
"v_add_f32 v213, v213, v217\n"
"s_mul_i32 s56, s41, 1536\n"
"s_add_u32 s56, s56, s40\n"
"s_add_u32 s56, s56, 49152\n"
"v_and_b32 v168, 32, v248\n"
"v_lshl_add_u32 v168, v168, 2, s56\n"
"v_bfe_u32 v169, v248, 3, 2\n"
"v_lshl_add_u32 v168, v169, 4, v168\n"
"v_lshl_add_u32 v169, v248, 5, s56\n"
"v_lshrrev_b32 v170, 3, v248\n"
"v_mul_u32_u24 v171, 43, v170\n"
"v_lshrrev_b32 v171, 7, v171\n"
"v_mul_u32_u24 v172, 3, v171\n"
"v_sub_u32 v170, v170, v172\n"
"v_mul_u32_u24 v170, 384, v170\n"
"v_and_b32 v172, 7, v248\n"
"v_lshl_add_u32 v170, v172, 4, v170\n"
"v_and_b32 v171, 2, v171\n"
"v_lshl_add_u32 v170, v171, 27, v170\n"
"s_lshl_b32 s56, s41, 7\n"
"v_add_u32 v170, s56, v170\n"
"s_mov_b32 s52, 0\n"
"s_mov_b32 s87, 0x80000\n"
"v_lshlrev_b32 v144, 16, v228\n"
"v_and_b32 v145, 0xffff0000, v228\n"
"v_lshlrev_b32 v146, 16, v229\n"
"v_and_b32 v147, 0xffff0000, v229\n"
"v_lshlrev_b32 v148, 16, v230\n"
"v_and_b32 v149, 0xffff0000, v230\n"
"v_lshlrev_b32 v150, 16, v231\n"
"v_and_b32 v151, 0xffff0000, v231\n"
"v_lshlrev_b32 v152, 16, v232\n"
"v_and_b32 v153, 0xffff0000, v232\n"
"v_lshlrev_b32 v154, 16, v233\n"
"v_and_b32 v155, 0xffff0000, v233\n"
"v_lshlrev_b32 v156, 16, v234\n"
"v_and_b32 v157, 0xffff0000, v234\n"
"v_lshlrev_b32 v158, 16, v235\n"
"v_and_b32 v159, 0xffff0000, v235\n"
"v_lshlrev_b32 v160, 16, v236\n"
"v_and_b32 v161, 0xffff0000, v236\n"
"v_lshlrev_b32 v162, 16, v237\n"
"v_and_b32 v163, 0xffff0000, v237\n"
"v_lshlrev_b32 v164, 16, v238\n"
"v_and_b32 v165, 0xffff0000, v238\n"
"v_lshlrev_b32 v166, 16, v239\n"
"v_and_b32 v167, 0xffff0000, v239\n"
"s_waitcnt vmcnt(0)\n"
"AT_A1:\n"
"v_cmp_eq_u32 vcc, s85, v193\n"
"s_sub_u32 s87, s87, 1\n"
"s_lshr_b32 s53, vcc_lo, 1\n"
"s_and_b32 s53, s53, vcc_lo\n"
"s_lshr_b32 s59, s53, 2\n"
"s_and_b32 s53, s53, s59\n"
"s_and_b32 s53, s53, 0x1111\n"
"s_cmp_lt_i32 s87, 0\n"
"s_cselect_b32 s53, 0x1111, s53\n"
"s_andn2_b32 s53, s53, s52\n"
"s_cmp_lg_u32 s53, 0\n"
"s_cbranch_scc1 AT_A2\n"
"s_sleep 2\n"
"AT_A1b:\n"
"buffer_load_dword v193, v192, s[28:31], s57 offen sc1\n"
"s_waitcnt vmcnt(0)\n"
"s_branch AT_A1\n"
"AT_A2:\n"
"s_bcnt1_i32_b32 s54, s53\n"
"s_bitcmp1_b32 s53, 0\n"
"s_cbranch_scc0 AT_Ai0\n"
"s_mov_b32 s56, s47\n"
"buffer_load_dwordx4 v[0:3], v241, s[28:31], s56 offen sc1\n"
"buffer_load_dwordx4 v[4:7], v242, s[28:31], s56 offen sc1\n"
"buffer_load_dwordx4 v[8:11], v243, s[28:31], s56 offen sc1\n"
"AT_Ai0:\n"
"s_bitcmp1_b32 s53, 4\n"
"s_cbranch_scc0 AT_Ai1\n"
"s_add_u32 s56, s47, 2359296\n"
"buffer_load_dwordx4 v[12:15], v241, s[28:31], s56 offen sc1\n"
"buffer_load_dwordx4 v[16:19], v242, s[28:31], s56 offen sc1\n"
"buffer_load_dwordx4 v[20:23], v243, s[28:31], s56 offen sc1\n"
"AT_Ai1:\n"
"s_bitcmp1_b32 s53, 8\n"
"s_cbranch_scc0 AT_Ai2\n"
"s_add_u32 s56, s47, 4718592\n"
"buffer_load_dwordx4 v[24:27], v241, s[28:31], s56 offen sc1\n"
"buffer_load_dwordx4 v[28:31], v242, s[28:31], s56 offen sc1\n"
"buffer_load_dwordx4 v[32:35], v243, s[28:31], s56 offen sc1\n"
"AT_Ai2:\n"
"s_bitcmp1_b32 s53, 12\n"
"s_cbranch_scc0 AT_Ai3\n"
"s_add_u32 s56, s47, 7077888\n"
"buffer_load_dwordx4 v[36:39], v241, s[28:31], s56 offen sc1\n"
"buffer_load_dwordx4 v[40:43], v242, s[28:31], s56 offen sc1\n"
"buffer_load_dwordx4 v[44:47], v243, s[28:31], s56 offen sc1\n"
"AT_Ai3:\n"
"s_bitcmp1_b32 s53, 0\n"
"s_cbranch_scc0 AT_Ar0\n"
"s_sub_u32 s54, s54, 1\n"
"s_cmp_eq_u32 s54, 0\n"
"s_cbranch_scc1 AT_Aw0_0\n"
"s_cmp_eq_u32 s54, 1\n"
"s_cbranch_scc1 AT_Aw0_1\n"
"s_cmp_eq_u32 s54, 2\n"
"s_cbranch_scc1 AT_Aw0_2\n"
"s_waitcnt vmcnt(9)\n"
"s_branch AT_Ax0\n"
"AT_Aw0_2:\n"
"s_waitcnt vmcnt(6)\n"
"s_branch AT_Ax0\n"
"AT_Aw0_1:\n"
"s_waitcnt vmcnt(3)\n"
"s_branch AT_Ax0\n"
"AT_Aw0_0:\n"
"s_waitcnt vmcnt(0)\n"
"AT_Ax0:\n"
"v_max3_u32 v194, v0, v1, v2\n"
"v_max3_u32 v195, v3, v4, v5\n"
"v_max3_u32 v194, v194, v6, v7\n"
"v_max3_u32 v195, v195, v8, v9\n"
"v_max3_u32 v194, v194, v10, v11\n"
"v_max_u32 v194, v194, v195\n"
"v_cmp_eq_u32 vcc, -1, v194\n"
"s_cmp_lt_i32 s87, 0\n"
"s_cselect_b64 vcc, 0, vcc\n"
"s_cbranch_vccnz AT_Ar0\n"
"s_bitset1_b32 s52, 0\n"
"v_dot2c_f32_bf16 v48, v0, v224\n"
"v_dot2c_f32_bf16 v49, v0, v225\n"
"v_dot2c_f32_bf16 v50, v1, v224\n"
"v_dot2c_f32_bf16 v51, v1, v225\n"
"v_dot2c_f32_bf16 v52, v2, v224\n"
"v_dot2c_f32_bf16 v53, v2, v225\n"
"v_dot2c_f32_bf16 v54, v3, v224\n"
"v_dot2c_f32_bf16 v55, v3, v225\n"
"v_dot2c_f32_bf16 v56, v4, v224\n"
"v_dot2c_f32_bf16 v57, v4, v225\n"
"v_dot2c_f32_bf16 v58, v5, v224\n"
"v_dot2c_f32_bf16 v59, v5, v225\n"
"v_dot2c_f32_bf16 v60, v6, v224\n"
"v_dot2c_f32_bf16 v61, v6, v225\n"
"v_dot2c_f32_bf16 v62, v7, v224\n"
"v_dot2c_f32_bf16 v63, v7, v225\n"
"v_dot2c_f32_bf16 v64, v8, v224\n"
"v_dot2c_f32_bf16 v65, v8, v225\n"
"v_dot2c_f32_bf16 v66, v9, v224\n"
"v_dot2c_f32_bf16 v67, v9, v225\n"
"v_dot2c_f32_bf16 v68, v10, v224\n"
"v_dot2c_f32_bf16 v69, v10, v225\n"
"v_dot2c_f32_bf16 v70, v11, v224\n"
"v_dot2c_f32_bf16 v71, v11, v225\n"
"AT_Ar0:\n"
"s_bitcmp1_b32 s53, 4\n"
"s_cbranch_scc0 AT_Ar1\n"
"s_sub_u32 s54, s54, 1\n"
"s_cmp_eq_u32 s54, 0\n"
"s_cbranch_scc1 AT_Aw1_0\n"
"s_cmp_eq_u32 s54, 1\n"
"s_cbranch_scc1 AT_Aw1_1\n"
"s_waitcnt vmcnt(6)\n"
"s_branch AT_Ax1\n"
"AT_Aw1_1:\n"
"s_waitcnt vmcnt(3)\n"
"s_branch AT_Ax1\n"
"AT_Aw1_0:\n"
"s_waitcnt vmcnt(0)\n"
"AT_Ax1:\n"
"v_max3_u32 v194, v12, v13, v14\n"
"v_max3_u32 v195, v15, v16, v17\n"
"v_max3_u32 v194, v194, v18, v19\n"
"v_max3_u32 v195, v195, v20, v21\n"
"v_max3_u32 v194, v194, v22, v23\n"
"v_max_u32 v194, v194, v195\n"
"v_cmp_eq_u32 vcc, -1, v194\n"
"s_cmp_lt_i32 s87, 0\n"
"s_cselect_b64 vcc, 0, vcc\n"
"s_cbranch_vccnz AT_Ar1\n"
"s_bitset1_b32 s52, 4\n"
"v_dot2c_f32_bf16 v48, v12, v224\n"
"v_dot2c_f32_bf16 v49, v12, v225\n"
"v_dot2c_f32_bf16 v50, v13, v224\n"
"v_dot2c_f32_bf16 v51, v13, v225\n"
"v_dot2c_f32_bf16 v52, v14, v224\n"
"v_dot2c_f32_bf16 v53, v14, v225\n"
"v_dot2c_f32_bf16 v54, v15, v224\n"
"v_dot2c_f32_bf16 v55, v15, v225\n"
"v_dot2c_f32_bf16 v56, v16, v224\n"
"v_dot2c_f32_bf16 v57, v16, v225\n"
"v_dot2c_f32_bf16 v58, v17, v224\n"
"v_dot2c_f32_bf16 v59, v17, v225\n"
"v_dot2c_f32_bf16 v60, v18, v224\n"
"v_dot2c_f32_bf16 v61, v18, v225\n"
"v_dot2c_f32_bf16 v62, v19, v224\n"
"v_dot2c_f32_bf16 v63, v19, v225\n"
"v_dot2c_f32_bf16 v64, v20, v224\n"
"v_dot2c_f32_bf16 v65, v20, v225\n"
"v_dot2c_f32_bf16 v66, v21, v224\n"
"v_dot2c_f32_bf16 v67, v21, v225\n"
"v_dot2c_f32_bf16 v68, v22, v224\n"
"v_dot2c_f32_bf16 v69, v22, v225\n"
"v_dot2c_f32_bf16 v70, v23, v224\n"
"v_dot2c_f32_bf16 v71, v23, v225\n"
"AT_Ar1:\n"
"s_bitcmp1_b32 s53, 8\n"
"s_cbranch_scc0 AT_Ar2\n"
"s_sub_u32 s54, s54, 1\n"
"s_cmp_eq_u32 s54, 0\n"
"s_cbranch_scc1 AT_Aw2_0\n"
"s_waitcnt vmcnt(3)\n"
"s_branch AT_Ax2\n"
"AT_Aw2_0:\n"
"s_waitcnt vmcnt(0)\n"
"AT_Ax2:\n"
"v_max3_u32 v194, v24, v25, v26\n"
"v_max3_u32 v195, v27, v28, v29\n"
"v_max3_u32 v194, v194, v30, v31\n"
"v_max3_u32 v195, v195, v32, v33\n"
"v_max3_u32 v194, v194, v34, v35\n"
"v_max_u32 v194, v194, v195\n"
"v_cmp_eq_u32 vcc, -1, v194\n"
"s_cmp_lt_i32 s87, 0\n"
"s_cselect_b64 vcc, 0, vcc\n"
"s_cbranch_vccnz AT_Ar2\n"
"s_bitset1_b32 s52, 8\n"
"v_dot2c_f32_bf16 v72, v24, v224\n"
"v_dot2c_f32_bf16 v73, v24, v225\n"
"v_dot2c_f32_bf16 v74, v25, v224\n"
"v_dot2c_f32_bf16 v75, v25, v225\n"
"v_dot2c_f32_bf16 v76, v26, v224\n"
"v_dot2c_f32_bf16 v77, v26, v225\n"
"v_dot2c_f32_bf16 v78, v27, v224\n"
"v_dot2c_f32_bf16 v79, v27, v225\n"
"v_dot2c_f32_bf16 v80, v28, v224\n"
"v_dot2c_f32_bf16 v81, v28, v225\n"
"v_dot2c_f32_bf16 v82, v29, v224\n"
"v_dot2c_f32_bf16 v83, v29, v225\n"
"v_dot2c_f32_bf16 v84, v30, v224\n"
"v_dot2c_f32_bf16 v85, v30, v225\n"
"v_dot2c_f32_bf16 v86, v31, v224\n"
"v_dot2c_f32_bf16 v87, v31, v225\n"
"v_dot2c_f32_bf16 v88, v32, v224\n"
"v_dot2c_f32_bf16 v89, v32, v225\n"
"v_dot2c_f32_bf16 v90, v33, v224\n"
"v_dot2c_f32_bf16 v91, v33, v225\n"
"v_dot2c_f32_bf16 v92, v34, v224\n"
"v_dot2c_f32_bf16 v93, v34, v225\n"
"v_dot2c_f32_bf16 v94, v35, v224\n"
"v_dot2c_f32_bf16 v95, v35, v225\n"
"AT_Ar2:\n"
"s_bitcmp1_b32 s53, 12\n"
"s_cbranch_scc0 AT_Ar3\n"
"s_sub_u32 s54, s54, 1\n"
"s_waitcnt vmcnt(0)\n"
"AT_Ax3:\n"
"v_max3_u32 v194, v36, v37, v38\n"
"v_max3_u32 v195, v39, v40, v41\n"
"v_max3_u32 v194, v194, v42, v43\n"
"v_max3_u32 v195, v195, v44, v45\n"
"v_max3_u32 v194, v194, v46, v47\n"
"v_max_u32 v194, v194, v195\n"
"v_cmp_eq_u32 vcc, -1, v194\n"
"s_cmp_lt_i32 s87, 0\n"
"s_cselect_b64 vcc, 0, vcc\n"
"s_cbranch_vccnz AT_Ar3\n"
"s_bitset1_b32 s52, 12\n"
"v_dot2c_f32_bf16 v72, v36, v224\n"
"v_dot2c_f32_bf16 v73, v36, v225\n"
"v_dot2c_f32_bf16 v74, v37, v224\n"
"v_dot2c_f32_bf16 v75, v37, v225\n"
"v_dot2c_f32_bf16 v76, v38, v224\n"
"v_dot2c_f32_bf16 v77, v38, v225\n"
"v_dot2c_f32_bf16 v78, v39, v224\n"
"v_dot2c_f32_bf16 v79, v39, v225\n"
"v_dot2c_f32_bf16 v80, v40, v224\n"
"v_dot2c_f32_bf16 v81, v40, v225\n"
"v_dot2c_f32_bf16 v82, v41, v224\n"
"v_dot2c_f32_bf16 v83, v41, v225\n"
"v_dot2c_f32_bf16 v84, v42, v224\n"
"v_dot2c_f32_bf16 v85, v42, v225\n"
"v_dot2c_f32_bf16 v86, v43, v224\n"
"v_dot2c_f32_bf16 v87, v43, v225\n"
"v_dot2c_f32_bf16 v88, v44, v224\n"
"v_dot2c_f32_bf16 v89, v44, v225\n"
"v_dot2c_f32_bf16 v90, v45, v224\n"
"v_dot2c_f32_bf16 v91, v45, v225\n"
"v_dot2c_f32_bf16 v92, v46, v224\n"
"v_dot2c_f32_bf16 v93, v46, v225\n"
"v_dot2c_f32_bf16 v94, v47, v224\n"
"v_dot2c_f32_bf16 v95, v47, v225\n"
"AT_Ar3:\n"
"s_cmp_lg_u32 s52, 0x1111\n"
"s_cbranch_scc1 AT_A1b\n"
"v_pk_add_f32 v[48:49], v[48:49], v[72:73]\n"
"v_pk_add_f32 v[50:51], v[50:51], v[74:75]\n"
"v_pk_add_f32 v[52:53], v[52:53], v[76:77]\n"
"v_pk_add_f32 v[54:55], v[54:55], v[78:79]\n"
"v_pk_add_f32 v[56:57], v[56:57], v[80:81]\n"
"v_pk_add_f32 v[58:59], v[58:59], v[82:83]\n"
"v_pk_add_f32 v[60:61], v[60:61], v[84:85]\n"
"v_pk_add_f32 v[62:63], v[62:63], v[86:87]\n"
"v_pk_add_f32 v[64:65], v[64:65], v[88:89]\n"
"v_pk_add_f32 v[66:67], v[66:67], v[90:91]\n"
"v_pk_add_f32 v[68:69], v[68:69], v[92:93]\n"
"v_pk_add_f32 v[70:71], v[70:71], v[94:95]\n"
"v_pk_fma_f32 v[48:49], v[48:49], v[96:97], v[144:145]\n"
"v_pk_fma_f32 v[50:51], v[50:51], v[98:99], v[146:147]\n"
"v_pk_fma_f32 v[52:53], v[52:53], v[100:101], v[148:149]\n"
"v_pk_fma_f32 v[54:55], v[54:55], v[102:103], v[150:151]\n"
"v_pk_fma_f32 v[56:57], v[56:57], v[104:105], v[152:153]\n"
"v_pk_fma_f32 v[58:59], v[58:59], v[106:107], v[154:155]\n"
"v_pk_fma_f32 v[60:61], v[60:61], v[108:109], v[156:157]\n"
"v_pk_fma_f32 v[62:63], v[62:63], v[110:111], v[158:159]\n"
"v_pk_fma_f32 v[64:65], v[64:65], v[112:113], v[160:161]\n"
"v_pk_fma_f32 v[66:67], v[66:67], v[114:115], v[162:163]\n"
"v_pk_fma_f32 v[68:69], v[68:69], v[116:117], v[164:165]\n"
"v_pk_fma_f32 v[70:71], v[70:71], v[118:119], v[166:167]\n"
"v_pk_mul_f32 v[194:195], v[48:49], v[48:49]\n"
"v_pk_mul_f32 v[196:197], v[48:49], v[120:121]\n"
"v_pk_add_f32 v[192:193], v[48:49], v[50:51]\n"
"v_pk_fma_f32 v[194:195], v[50:51], v[50:51], v[194:195]\n"
"v_pk_fma_f32 v[196:197], v[50:51], v[122:123], v[196:197]\n"
"v_pk_add_f32 v[192:193], v[192:193], v[52:53]\n"
"v_pk_fma_f32 v[194:195], v[52:53], v[52:53], v[194:195]\n"
"v_pk_fma_f32 v[196:197], v[52:53], v[124:125], v[196:197]\n"
"v_pk_add_f32 v[192:193], v[192:193], v[54:55]\n"
"v_pk_fma_f32 v[194:195], v[54:55], v[54:55], v[194:195]\n"
"v_pk_fma_f32 v[196:197], v[54:55], v[126:127], v[196:197]\n"
"v_pk_add_f32 v[192:193], v[192:193], v[56:57]\n"
"v_pk_fma_f32 v[194:195], v[56:57], v[56:57], v[194:195]\n"
"v_pk_fma_f32 v[196:197], v[56:57], v[128:129], v[196:197]\n"
"v_pk_add_f32 v[192:193], v[192:193], v[58:59]\n"
"v_pk_fma_f32 v[194:195], v[58:59], v[58:59], v[194:195]\n"
"v_pk_fma_f32 v[196:197], v[58:59], v[130:131], v[196:197]\n"
"v_pk_add_f32 v[192:193], v[192:193], v[60:61]\n"
"v_pk_fma_f32 v[194:195], v[60:61], v[60:61], v[194:195]\n"
"v_pk_fma_f32 v[196:197], v[60:61], v[132:133], v[196:197]\n"
"v_pk_add_f32 v[192:193], v[192:193], v[62:63]\n"
"v_pk_fma_f32 v[194:195], v[62:63], v[62:63], v[194:195]\n"
"v_pk_fma_f32 v[196:197], v[62:63], v[134:135], v[196:197]\n"
"v_pk_add_f32 v[192:193], v[192:193], v[64:65]\n"
"v_pk_fma_f32 v[194:195], v[64:65], v[64:65], v[194:195]\n"
"v_pk_fma_f32 v[196:197], v[64:65], v[136:137], v[196:197]\n"
"v_pk_add_f32 v[192:193], v[192:193], v[66:67]\n"
"v_pk_fma_f32 v[194:195], v[66:67], v[66:67], v[194:195]\n"
"v_pk_fma_f32 v[196:197], v[66:67], v[138:139], v[196:197]\n"
"v_pk_add_f32 v[192:193], v[192:193], v[68:69]\n"
"v_pk_fma_f32 v[194:195], v[68:69], v[68:69], v[194:195]\n"
"v_pk_fma_f32 v[196:197], v[68:69], v[140:141], v[196:197]\n"
"v_pk_add_f32 v[192:193], v[192:193], v[70:71]\n"
"v_pk_fma_f32 v[194:195], v[70:71], v[70:71], v[194:195]\n"
"v_pk_fma_f32 v[196:197], v[70:71], v[142:143], v[196:197]\n"
"v_add_f32 v192, v192, v193\n"
"v_add_f32 v193, v194, v195\n"
"v_add_f32 v194, v196, v197\n"
"s_lshl_b32 s56, s41, 7\n"
"v_add_u32 v199, s56, v226\n"
"v_add_f32_dpp v192, v192, v192 row_ror:8 row_mask:0xf bank_mask:0xf\n"
"v_add_f32_dpp v193, v193, v193 row_ror:8 row_mask:0xf bank_mask:0xf\n"
"v_add_f32_dpp v194, v194, v194 row_ror:8 row_mask:0xf bank_mask:0xf\n"
"v_mov_b32 v196, v192\n"
"v_mov_b32 v197, v193\n"
"v_mov_b32 v198, v194\n"
"v_permlane16_swap_b32 v192, v196\n"
"v_permlane16_swap_b32 v193, v197\n"
"v_permlane16_swap_b32 v194, v198\n"
"v_add_f32 v192, v192, v196\n"
"v_add_f32 v193, v193, v197\n"
"v_add_f32 v194, v194, v198\n"
"v_mov_b32 v196, v192\n"
"v_mov_b32 v197, v193\n"
"v_mov_b32 v198, v194\n"
"v_permlane32_swap_b32 v192, v196\n"
"v_permlane32_swap_b32 v193, v197\n"
"v_permlane32_swap_b32 v194, v198\n"
"v_add_f32 v192, v192, v196\n"
"v_add_f32 v193, v193, v197\n"
"v_add_f32 v194, v194, v198\n"
"s_mov_b64 exec, 0xff\n"
"ds_write_b128 v199, v[192:195]\n"
"s_mov_b64 exec, -1\n"
"s_waitcnt lgkmcnt(0)\n"
"s_barrier\n"
"ds_read_b128 v[200:203], v226 offset:0\n"
"ds_read_b128 v[204:207], v226 offset:128\n"
"ds_read_b128 v[208:211], v226 offset:256\n"
"ds_read_b128 v[0:3], v244 offset:4608\n"
"ds_read_b128 v[4:7], v244 offset:4672\n"
"ds_read_b128 v[8:11], v244 offset:5376\n"
"ds_read_b128 v[12:15], v244 offset:5440\n"
"ds_read_b128 v[16:19], v244 offset:6144\n"
"ds_read_b128 v[20:23], v244 offset:6208\n"
"ds_read_b128 v[24:27], v244 offset:6912\n"
"ds_read_b128 v[28:31], v244 offset:6976\n"
"ds_read_b128 v[32:35], v244 offset:7680\n"
"ds_read_b128 v[36:39], v244 offset:7744\n"
"ds_read_b128 v[40:43], v244 offset:8448\n"
"ds_read_b128 v[44:47], v244 offset:8512\n"
"s_waitcnt lgkmcnt(13)\n"
"v_add_f32 v200, v200, v204\n"
"v_add_f32 v201, v201, v205\n"
"v_add_f32 v202, v202, v206\n"
"s_waitcnt lgkmcnt(12)\n"
"v_add_f32 v200, v200, v208\n"
"v_add_f32 v201, v201, v209\n"
"v_add_f32 v202, v202, v210\n"
"v_mul_f32 v200, 0x3ae38e39, v200\n"
"v_mul_f32 v201, 0x3ae38e39, v201\n"
"v_fma_f32 v201, -v200, v200, v201\n"
"v_max_f32 v201, 0, v201\n"
"v_add_f32 v201, 0x3727c5ac, v201\n"
"v_rsq_f32 v192, v201\n"
"v_fma_f32 v202, -v200, v212, v202\n"
"v_fma_f32 v196, v192, v202, v213\n"
"v_ashrrev_i32 v203, 31, v196\n"
"v_or_b32 v203, 0x80000000, v203\n"
"v_xor_b32 v196, v196, v203\n"
"v_and_or_b32 v196, v196, s14, v220\n"
"v_mul_f32 v194, v200, v192\n"
"v_sub_f32 v194, 0, v194\n"
"v_mov_b32_dpp v203, v196 row_ror:1 row_mask:0xf bank_mask:0xf\n"
"v_min_u32 v198, v196, v203\n"
"v_max_u32 v197, v196, v203\n"
"s_nop 0\n"
"v_mov_b32_dpp v204, v198 row_ror:2 row_mask:0xf bank_mask:0xf\n"
"v_mov_b32_dpp v203, v197 row_ror:2 row_mask:0xf bank_mask:0xf\n"
"v_min_u32 v205, v197, v203\n"
"v_max_u32 v197, v197, v203\n"
"v_max3_u32 v198, v198, v204, v205\n"
"s_nop 0\n"
"v_mov_b32_dpp v203, v197 row_ror:4 row_mask:0xf bank_mask:0xf\n"
"v_mov_b32_dpp v204, v198 row_ror:4 row_mask:0xf bank_mask:0xf\n"
"v_min_u32 v205, v197, v203\n"
"v_max_u32 v197, v197, v203\n"
"v_max3_u32 v198, v198, v204, v205\n"
"v_cmp_ge_u32 s[8:9], v196, v198\n"
"ds_read_b128 v[72:75], v244 offset:38912\n"
"ds_read_b128 v[76:79], v244 offset:38976\n"
"ds_read_b128 v[80:83], v244 offset:39680\n"
"ds_read_b128 v[84:87], v244 offset:39744\n"
"ds_read_b128 v[88:91], v244 offset:40448\n"
"ds_read_b128 v[92:95], v244 offset:40512\n"
"v_pk_fma_f32 v[144:145], v[48:49], v[192:193], v[194:195] op_sel_hi:[1,0,0]\n"
"v_pk_fma_f32 v[146:147], v[50:51], v[192:193], v[194:195] op_sel_hi:[1,0,0]\n"
"v_pk_fma_f32 v[148:149], v[52:53], v[192:193], v[194:195] op_sel_hi:[1,0,0]\n"
"v_pk_fma_f32 v[150:151], v[54:55], v[192:193], v[194:195] op_sel_hi:[1,0,0]\n"
"v_pk_fma_f32 v[152:153], v[56:57], v[192:193], v[194:195] op_sel_hi:[1,0,0]\n"
"v_pk_fma_f32 v[154:155], v[58:59], v[192:193], v[194:195] op_sel_hi:[1,0,0]\n"
"v_pk_fma_f32 v[156:157], v[60:61], v[192:193], v[194:195] op_sel_hi:[1,0,0]\n"
"v_pk_fma_f32 v[158:159], v[62:63], v[192:193], v[194:195] op_sel_hi:[1,0,0]\n"
"v_pk_fma_f32 v[160:161], v[64:65], v[192:193], v[194:195] op_sel_hi:[1,0,0]\n"
"v_pk_fma_f32 v[162:163], v[66:67], v[192:193], v[194:195] op_sel_hi:[1,0,0]\n"
"v_pk_fma_f32 v[164:165], v[68:69], v[192:193], v[194:195] op_sel_hi:[1,0,0]\n"
"v_pk_fma_f32 v[166:167], v[70:71], v[192:193], v[194:195] op_sel_hi:[1,0,0]\n"
"s_waitcnt lgkmcnt(6)\n"
"v_fma_f32 v96, v144, v0, v24\n"
"v_fma_f32 v97, v145, v1, v25\n"
"v_fma_f32 v98, v146, v2, v26\n"
"v_fma_f32 v99, v147, v3, v27\n"
"v_fma_f32 v100, v148, v4, v28\n"
"v_fma_f32 v101, v149, v5, v29\n"
"v_fma_f32 v102, v150, v6, v30\n"
"v_fma_f32 v103, v151, v7, v31\n"
"v_fma_f32 v104, v152, v8, v32\n"
"v_fma_f32 v105, v153, v9, v33\n"
"v_fma_f32 v106, v154, v10, v34\n"
"v_fma_f32 v107, v155, v11, v35\n"
"v_fma_f32 v108, v156, v12, v36\n"
"v_fma_f32 v109, v157, v13, v37\n"
"v_fma_f32 v110, v158, v14, v38\n"
"v_fma_f32 v111, v159, v15, v39\n"
"v_fma_f32 v112, v160, v16, v40\n"
"v_fma_f32 v113, v161, v17, v41\n"
"v_fma_f32 v114, v162, v18, v42\n"
"v_fma_f32 v115, v163, v19, v43\n"
"v_fma_f32 v116, v164, v20, v44\n"
"v_fma_f32 v117, v165, v21, v45\n"
"v_fma_f32 v118, v166, v22, v46\n"
"v_fma_f32 v119, v167, v23, v47\n"
"v_cmp_ne_u32 vcc, v196, v197\n"
"v_add_u32 v135, 768, v168\n"
"v_cmp_gt_u32 s[58:59], 24, v248\n"
"v_cndmask_b32 v134, v168, v135, vcc\n"
"s_mov_b64 exec, s[8:9]\n"
"ds_write_b128 v134, v[96:99] offset:0\n"
"ds_write_b128 v134, v[100:103] offset:64\n"
"ds_write_b128 v134, v[104:107] offset:256\n"
"ds_write_b128 v134, v[108:111] offset:320\n"
"ds_write_b128 v134, v[112:115] offset:512\n"
"ds_write_b128 v134, v[116:119] offset:576\n"
"s_mov_b64 exec, -1\n"
"ds_read_b128 v[136:139], v169 offset:0\n"
"ds_read_b128 v[140:143], v169 offset:16\n"
"v_cndmask_b32 v135, v198, v197, s[58:59]\n"
"v_not_b32 v135, v135\n"
"v_and_b32 v135, 7, v135\n"
"v_mul_u32_u24 v135, 2304, v135\n"
"v_add_u32 v135, v135, v170\n"
"s_mul_i32 s60, s55, 18432\n"
"s_add_u32 s60, s60, 26402816\n"
"s_waitcnt lgkmcnt(0)\n"
"v_cvt_pk_bf16_f32 v120, v136, v137\n"
"v_cvt_pk_bf16_f32 v121, v138, v139\n"
"v_cvt_pk_bf16_f32 v122, v140, v141\n"
"v_cvt_pk_bf16_f32 v123, v142, v143\n"
"buffer_store_dwordx4 v[120:123], v135, s[28:31], s60 offen sc1\n"
"v_lshlrev_b32 v128, 16, v120\n"
"v_and_b32 v129, 0xffff0000, v120\n"
"v_sub_f32 v136, v136, v128\n"
"v_sub_f32 v137, v137, v129\n"
"v_lshlrev_b32 v128, 16, v121\n"
"v_and_b32 v129, 0xffff0000, v121\n"
"v_sub_f32 v138, v138, v128\n"
"v_sub_f32 v139, v139, v129\n"
"v_lshlrev_b32 v128, 16, v122\n"
"v_and_b32 v129, 0xffff0000, v122\n"
"v_sub_f32 v140, v140, v128\n"
"v_sub_f32 v141, v141, v129\n"
"v_lshlrev_b32 v128, 16, v123\n"
"v_and_b32 v129, 0xffff0000, v123\n"
"v_sub_f32 v142, v142, v128\n"
"v_sub_f32 v143, v143, v129\n"
"v_cvt_pk_bf16_f32 v124, v136, v137\n"
"v_cvt_pk_bf16_f32 v125, v138, v139\n"
"v_cvt_pk_bf16_f32 v126, v140, v141\n"
"v_cvt_pk_bf16_f32 v127, v142, v143\n"
"buffer_store_dwordx4 v[124:127], v135, s[28:31], s60 offen offset:1152 sc1\n"
"v_fma_f32 v200, v72, v96, v48\n"
"v_fma_f32 v201, v73, v97, v49\n"
"v_fma_f32 v202, v74, v98, v50\n"
"v_fma_f32 v203, v75, v99, v51\n"
"v_fma_f32 v204, v76, v100, v52\n"
"v_fma_f32 v205, v77, v101, v53\n"
"v_fma_f32 v206, v78, v102, v54\n"
"v_fma_f32 v207, v79, v103, v55\n"
"v_fma_f32 v208, v80, v104, v56\n"
"v_fma_f32 v209, v81, v105, v57\n"
"v_fma_f32 v210, v82, v106, v58\n"
"v_fma_f32 v211, v83, v107, v59\n"
"v_fma_f32 v212, v84, v108, v60\n"
"v_fma_f32 v213, v85, v109, v61\n"
"v_fma_f32 v214, v86, v110, v62\n"
"v_fma_f32 v215, v87, v111, v63\n"
"v_fma_f32 v216, v88, v112, v64\n"
"v_fma_f32 v217, v89, v113, v65\n"
"v_fma_f32 v218, v90, v114, v66\n"
"v_fma_f32 v219, v91, v115, v67\n"
"v_fma_f32 v220, v92, v116, v68\n"
"v_fma_f32 v221, v93, v117, v69\n"
"v_fma_f32 v222, v94, v118, v70\n"
"v_fma_f32 v223, v95, v119, v71\n"
"v_cvt_pk_bf16_f32 v228, v200, v201\n"
"v_cvt_pk_bf16_f32 v229, v202, v203\n"
"v_cvt_pk_bf16_f32 v230, v204, v205\n"
"v_cvt_pk_bf16_f32 v231, v206, v207\n"
"v_cvt_pk_bf16_f32 v232, v208, v209\n"
"v_cvt_pk_bf16_f32 v233, v210, v211\n"
"v_cvt_pk_bf16_f32 v234, v212, v213\n"
"v_cvt_pk_bf16_f32 v235, v214, v215\n"
"v_cvt_pk_bf16_f32 v236, v216, v217\n"
"v_cvt_pk_bf16_f32 v237, v218, v219\n"
"v_cvt_pk_bf16_f32 v238, v220, v221\n"
"v_cvt_pk_bf16_f32 v239, v222, v223\n"
"ds_write_b64 v240, v[228:229] offset:0\n"
"ds_write_b64 v240, v[230:231] offset:32\n"
"ds_write_b64 v240, v[232:233] offset:384\n"
"ds_write_b64 v240, v[234:235] offset:416\n"
"ds_write_b64 v240, v[236:237] offset:768\n"
"ds_write_b64 v240, v[238:239] offset:800\n"
"s_mov_b64 s[8:9], -1\n"
"v_mov_b32 v225, v196\n"
"s_mul_i32 s63, s41, 9216\n"
"s_add_u32 s63, s63, s40\n"
"s_add_u32 s63, s63, 9216\n"
"v_lshrrev_b32 v185, 2, v248\n"
"v_and_b32 v186, 3, v248\n"
"v_mul_u32_u24 v187, 96, v185\n"
"v_lshl_add_u32 v188, v186, 4, s63\n"
"v_lshl_add_u32 v189, v187, 1, v188\n"
"v_add_u32 v188, v187, v188\n"
"v_lshrrev_b32 v190, 4, v248\n"
"v_mul_u32_u24 v191, 384, v190\n"
"v_add_u32 v187, v187, v191\n"
"v_lshl_add_u32 v187, v186, 3, v187\n"
"v_add_u32 v187, s63, v187\n"
"s_mov_b32 exec_lo, 0x77777777\n"
"s_mov_b32 exec_hi, 0x77777777\n"
"ds_write_b128 v189, a[48:51] offset:0\n"
"ds_write_b128 v189, a[84:87] offset:48\n"
"ds_write_b128 v189, a[52:55] offset:96\n"
"ds_write_b128 v189, a[88:91] offset:144\n"
"ds_write_b128 v189, a[56:59] offset:3072\n"
"ds_write_b128 v189, a[92:95] offset:3120\n"
"ds_write_b128 v189, a[60:63] offset:3168\n"
"ds_write_b128 v189, a[96:99] offset:3216\n"
"ds_write_b128 v189, a[64:67] offset:6144\n"
"ds_write_b128 v189, a[100:103] offset:6192\n"
"ds_write_b128 v189, a[68:71] offset:6240\n"
"ds_write_b128 v189, a[104:107] offset:6288\n"
"s_mov_b64 exec, -1\n"
"ds_read_b64_tr_b16 a[128:129], v187 offset:0\n"
"ds_read_b64_tr_b16 a[130:131], v187 offset:384\n"
"ds_read_b64_tr_b16 a[132:133], v187 offset:32\n"
"ds_read_b64_tr_b16 a[134:135], v187 offset:416\n"
"ds_read_b64_tr_b16 a[136:137], v187 offset:64\n"
"ds_read_b64_tr_b16 a[138:139], v187 offset:448\n"
"ds_read_b64_tr_b16 a[140:141], v187 offset:3072\n"
"ds_read_b64_tr_b16 a[142:143], v187 offset:3456\n"
"ds_read_b64_tr_b16 a[144:145], v187 offset:3104\n"
"ds_read_b64_tr_b16 a[146:147], v187 offset:3488\n"
"ds_read_b64_tr_b16 a[148:149], v187 offset:3136\n"
"ds_read_b64_tr_b16 a[150:151], v187 offset:3520\n"
"ds_read_b64_tr_b16 a[152:153], v187 offset:6144\n"
"ds_read_b64_tr_b16 a[154:155], v187 offset:6528\n"
"ds_read_b64_tr_b16 a[156:157], v187 offset:6176\n"
"ds_read_b64_tr_b16 a[158:159], v187 offset:6560\n"
"ds_read_b64_tr_b16 a[160:161], v187 offset:6208\n"
"ds_read_b64_tr_b16 a[162:163], v187 offset:6592\n"
"s_mov_b32 exec_lo, 0x77777777\n"
"s_mov_b32 exec_hi, 0x77777777\n"
"ds_write_b128 v189, a[72:75] offset:0\n"
"ds_write_b128 v189, a[108:111] offset:48\n"
"ds_write_b128 v189, a[76:79] offset:96\n"
"ds_write_b128 v189, a[112:115] offset:144\n"
"ds_write_b128 v188, a[80:83] offset:3072\n"
"ds_write_b128 v188, a[116:119] offset:3120\n"
"s_mov_b64 exec, -1\n"
"ds_read_b64_tr_b16 a[164:165], v187 offset:0\n"
"ds_read_b64_tr_b16 a[166:167], v187 offset:384\n"
"ds_read_b64_tr_b16 a[168:169], v187 offset:32\n"
"ds_read_b64_tr_b16 a[170:171], v187 offset:416\n"
"ds_read_b64_tr_b16 a[172:173], v187 offset:64\n"
"ds_read_b64_tr_b16 a[174:175], v187 offset:448\n"
"ds_read_b64_tr_b16 a[176:177], v187 offset:3072\n"
"ds_read_b64_tr_b16 a[178:179], v187 offset:3456\n"
"ds_read_b64_tr_b16 a[180:181], v187 offset:3104\n"
"ds_read_b64_tr_b16 a[182:183], v187 offset:3488\n"
"ds_read_b64_tr_b16 a[184:185], v187 offset:3136\n"
"ds_read_b64_tr_b16 a[186:187], v187 offset:3520\n"
"v_and_b32 v241, 1, v248\n"
"v_mul_u32_u24 v241, 1152, v241\n"
"v_lshl_add_u32 v241, v190, 4, v241\n"
"s_mul_i32 s63, s41, 288\n"
"v_add_u32 v241, s63, v241\n"
"v_and_b32 v242, 10, v248\n"
"v_lshl_add_u32 v241, v242, 26, v241\n"
"v_and_b32 v242, 32, v248\n"
"v_lshlrev_b32 v242, 25, v242\n"
"s_add_u32 s63, s63, s40\n"
"s_add_u32 s63, s63, 131072\n"
"v_cmp_eq_u32 vcc, 3, v190\n"
"v_and_b32 v185, 3, v185\n"
"v_lshlrev_b32 v191, 3, v190\n"
"v_cndmask_b32_e64 v191, v191, 0, vcc\n"
"v_add_u32 v191, v191, v185\n"
"v_mul_u32_u24 v191, 1152, v191\n"
"v_lshl_add_u32 v191, v186, 3, v191\n"
"v_add_u32 v255, s63, v191\n"
"v_lshrrev_b32 v243, 3, v248\n"
"v_mul_u32_u24 v243, 24576, v243\n"
"v_and_b32 v247, 7, v248\n"
"v_lshl_add_u32 v243, v247, 4, v243\n"
"v_mul_u32_u24 v224, 24576, v190\n"
"v_and_b32 v246, 15, v248\n"
"v_lshl_add_u32 v224, v246, 3, v224\n"
"v_or_b32 v224, v242, v224\n"
"v_subrev_u32 v252, 65536, v227\n"
"s_mul_i32 s63, s41, 384\n"
"v_mul_u32_u24 v244, 192, v190\n"
"v_lshl_add_u32 v244, v246, 2, v244\n"
"v_add3_u32 v244, v244, v252, s63\n"
"v_cmp_lt_u32 vcc, 23, v248\n"
"v_lshl_add_u32 v249, v248, 2, v252\n"
"v_add_u32 v185, 96, v249\n"
"v_cndmask_b32 v249, v249, v185, vcc\n"
"s_lshl_b32 s63, s41, 9\n"
"v_add_u32 v252, s63, v252\n"
"v_lshl_add_u32 v251, v248, 1, v252\n"
"v_add_u32 v185, 208, v251\n"
"v_cndmask_b32 v251, v251, v185, vcc\n"
"v_lshl_add_u32 v185, v248, 3, v252\n"
"v_lshl_add_u32 v252, v247, 6, v252\n"
"v_lshl_add_u32 v252, v190, 4, v252\n"
"s_min_u32 s63, s41, 2\n"
"s_mul_i32 s63, s63, 192\n"
"v_add_u32 v183, s63, v248\n"
"v_mul_u32_u24 v183, 911, v183\n"
"s_lshl_b32 s63, s55, 3\n"
"v_lshrrev_b32 v180, 16, v183\n"
"v_add_u32 v180, s63, v180\n"
"v_add_u32 v183, 58304, v183\n"
"v_lshrrev_b32 v181, 16, v183\n"
"v_add_u32 v181, s63, v181\n"
"v_add_u32 v183, 58304, v183\n"
"v_lshrrev_b32 v182, 16, v183\n"
"v_add_u32 v182, s63, v182\n"
"v_lshlrev_b32 v250, 4, v248\n"
"v_add_u32 v184, s45, v250\n"
"s_cmp_eq_u32 s41, 3\n"
"s_cselect_b32 s63, 0x10000000, 0\n"
"v_or_b32 v250, s63, v250\n"
"AT_B3:\n"
"s_waitcnt lgkmcnt(0)\n"
"s_barrier\n"
"ds_read_b64 v[246:247], v227 offset:656\n"
"v_and_b32 v186, 4, v248\n"
"v_cmp_eq_u32 vcc, 0, v186\n"
"s_mov_b32 s56, 26402816\n"
"s_waitcnt lgkmcnt(0)\n"
"v_cndmask_b32 v186, v247, v246, vcc\n"
"v_readfirstlane_b32 s10, v246\n"
"v_not_b32 v186, v186\n"
"v_readfirstlane_b32 s11, v247\n"
"v_and_b32 v186, 0x7ff, v186\n"
"v_mul_u32_u24 v186, 2304, v186\n"
"v_add_u32 v241, v241, v186\n"
"v_or_b32 v242, v242, v241\n"
"s_andn2_b32 s12, 0x7ff, s10\n"
"s_andn2_b32 s13, 0x7ff, s11\n"
"s_cmp_ge_u32 s42, 192\n"
"s_cbranch_scc1 AT_F0\n"
"buffer_load_dwordx4 v[0:3], v241, s[28:31], s56 offen offset:0 sc1\n"
"buffer_load_dwordx4 v[4:7], v241, s[28:31], s56 offen offset:64 sc1\n"
"buffer_load_dwordx4 v[8:11], v241, s[28:31], s56 offen offset:128 sc1\n"
"buffer_load_dwordx4 v[12:15], v241, s[28:31], s56 offen offset:192 sc1\n"
"buffer_load_dwordx4 v[16:19], v242, s[28:31], s56 offen offset:256 sc1\n"
"v_mov_b32 v20, 0\n"
"v_mov_b32 v21, 0\n"
"ds_write_b64 v185, v[20:21] offset:4096\n"
"ds_read_b64_tr_b16 a[192:193], v255 offset:0\n"
"ds_read_b64_tr_b16 a[194:195], v255 offset:4608\n"
"ds_read_b64_tr_b16 a[196:197], v255 offset:32\n"
"ds_read_b64_tr_b16 a[198:199], v255 offset:4640\n"
"ds_read_b64_tr_b16 a[200:201], v255 offset:64\n"
"ds_read_b64_tr_b16 a[202:203], v255 offset:4672\n"
"ds_read_b64_tr_b16 a[204:205], v255 offset:96\n"
"ds_read_b64_tr_b16 a[206:207], v255 offset:4704\n"
"ds_read_b64_tr_b16 a[208:209], v255 offset:128\n"
"ds_read_b64_tr_b16 a[210:211], v255 offset:4736\n"
"ds_read_b64_tr_b16 a[212:213], v255 offset:160\n"
"ds_read_b64_tr_b16 a[214:215], v255 offset:4768\n"
"ds_read_b64_tr_b16 a[216:217], v255 offset:192\n"
"ds_read_b64_tr_b16 a[218:219], v255 offset:4800\n"
"ds_read_b64_tr_b16 a[220:221], v255 offset:224\n"
"ds_read_b64_tr_b16 a[222:223], v255 offset:4832\n"
"ds_read_b64_tr_b16 a[224:225], v255 offset:256\n"
"ds_read_b64_tr_b16 a[226:227], v255 offset:4864\n"
"s_mul_i32 s57, s41, 442368\n"
"s_lshl_b32 s58, s42, 7\n"
"s_add_u32 s57, s57, s58\n"
"s_add_u32 s57, s57, 56762368\n"
"s_add_u32 s62, s57, 196608\n"
"s_add_u32 s63, s57, 393216\n"
"v_mov_b32 v85, s85\n"
"v_mov_b32 v87, s85\n"
"v_mov_b32 v89, s85\n"
"v_mov_b32 v91, s85\n"
"v_mov_b32 v93, s85\n"
"s_mov_b32 s87, 0x80000\n"
"s_waitcnt vmcnt(0)\n"
"AT_A6:\n"
"v_max3_u32 v22, v0, v1, v2\n"
"v_max3_u32 v23, v3, v4, v5\n"
"v_max3_u32 v22, v22, v6, v7\n"
"v_max3_u32 v23, v23, v8, v9\n"
"v_max3_u32 v22, v22, v10, v11\n"
"v_max3_u32 v23, v23, v12, v13\n"
"v_max3_u32 v22, v22, v14, v15\n"
"v_max3_u32 v23, v23, v16, v17\n"
"v_max3_u32 v22, v22, v18, v19\n"
"v_max_u32 v22, v22, v23\n"
"v_cmp_eq_u32 vcc, -1, v22\n"
"s_nop 0\n"
"s_cbranch_vccz AT_A7\n"
"s_sub_u32 s87, s87, 1\n"
"s_cmp_eq_u32 s87, 0\n"
"s_cbranch_scc1 AT_A7\n"
"s_sleep 1\n"
"buffer_load_dwordx4 v[0:3], v241, s[28:31], s56 offen offset:0 sc1\n"
"buffer_load_dwordx4 v[4:7], v241, s[28:31], s56 offen offset:64 sc1\n"
"buffer_load_dwordx4 v[8:11], v241, s[28:31], s56 offen offset:128 sc1\n"
"buffer_load_dwordx4 v[12:15], v241, s[28:31], s56 offen offset:192 sc1\n"
"buffer_load_dwordx4 v[16:19], v242, s[28:31], s56 offen offset:256 sc1\n"
"s_waitcnt vmcnt(0)\n"
"s_branch AT_A6\n"
"AT_A7:\n"
"v_mfma_f32_16x16x32_bf16 v[24:27], v[0:3], a[128:131], 0\n"
"v_mfma_f32_16x16x32_bf16 v[28:31], v[0:3], a[132:135], 0\n"
"v_mfma_f32_16x16x32_bf16 v[32:35], v[0:3], a[136:139], 0\n"
"v_mfma_f32_16x16x32_bf16 v[24:27], v[4:7], a[140:143], v[24:27]\n"
"v_mfma_f32_16x16x32_bf16 v[28:31], v[4:7], a[144:147], v[28:31]\n"
"v_mfma_f32_16x16x32_bf16 v[32:35], v[4:7], a[148:151], v[32:35]\n"
"v_mfma_f32_16x16x32_bf16 v[24:27], v[8:11], a[152:155], v[24:27]\n"
"v_mfma_f32_16x16x32_bf16 v[28:31], v[8:11], a[156:159], v[28:31]\n"
"v_mfma_f32_16x16x32_bf16 v[32:35], v[8:11], a[160:163], v[32:35]\n"
"v_mfma_f32_16x16x32_bf16 v[24:27], v[12:15], a[164:167], v[24:27]\n"
"v_mfma_f32_16x16x32_bf16 v[28:31], v[12:15], a[168:171], v[28:31]\n"
"v_mfma_f32_16x16x32_bf16 v[32:35], v[12:15], a[172:175], v[32:35]\n"
"v_mfma_f32_16x16x32_bf16 v[24:27], v[16:19], a[176:179], v[24:27]\n"
"v_mfma_f32_16x16x32_bf16 v[28:31], v[16:19], a[180:183], v[28:31]\n"
"v_mfma_f32_16x16x32_bf16 v[32:35], v[16:19], a[184:187], v[32:35]\n"
"s_nop 5\n"
"v_add_f32 v24, v24, v25\n"
"v_add_f32 v28, v28, v29\n"
"v_add_f32 v32, v32, v33\n"
"s_mov_b32 exec_hi, 0\n"
"ds_write_b32 v244, v24 offset:2048\n"
"ds_write_b32 v244, v28 offset:2112\n"
"ds_write_b32 v244, v32 offset:2176\n"
"s_mov_b64 exec, -1\n"
"v_cmp_eq_u32 vcc, s10, v225\n"
"v_cmp_eq_u32 s[58:59], s11, v225\n"
"s_nop 0\n"
"s_or_b64 s[58:59], s[58:59], vcc\n"
"s_and_b64 exec, s[8:9], s[58:59]\n"
"s_cbranch_execz AT_A5\n"
"s_add_u32 s60, s48, 4608\n"
"v_mov_b32 v101, s85\n"
"v_mov_b32 v103, s85\n"
"v_mov_b32 v105, s85\n"
"v_mov_b32 v107, s85\n"
"v_mov_b32 v109, s85\n"
"v_mov_b32 v111, s85\n"
"v_mov_b32 v113, s85\n"
"v_mov_b32 v115, s85\n"
"v_mov_b32 v100, v200\n"
"v_mov_b32 v102, v201\n"
"buffer_store_dwordx4 v[100:103], v245, s[28:31], s60 offen offset:0 sc1\n"
"v_mov_b32 v104, v202\n"
"v_mov_b32 v106, v203\n"
"buffer_store_dwordx4 v[104:107], v245, s[28:31], s60 offen offset:16 sc1\n"
"v_mov_b32 v108, v204\n"
"v_mov_b32 v110, v205\n"
"buffer_store_dwordx4 v[108:111], v245, s[28:31], s60 offen offset:128 sc1\n"
"v_mov_b32 v112, v206\n"
"v_mov_b32 v114, v207\n"
"buffer_store_dwordx4 v[112:115], v245, s[28:31], s60 offen offset:144 sc1\n"
"v_mov_b32 v100, v208\n"
"v_mov_b32 v102, v209\n"
"buffer_store_dwordx4 v[100:103], v245, s[28:31], s60 offen offset:1536 sc1\n"
"v_mov_b32 v104, v210\n"
"v_mov_b32 v106, v211\n"
"buffer_store_dwordx4 v[104:107], v245, s[28:31], s60 offen offset:1552 sc1\n"
"v_mov_b32 v108, v212\n"
"v_mov_b32 v110, v213\n"
"buffer_store_dwordx4 v[108:111], v245, s[28:31], s60 offen offset:1664 sc1\n"
"v_mov_b32 v112, v214\n"
"v_mov_b32 v114, v215\n"
"buffer_store_dwordx4 v[112:115], v245, s[28:31], s60 offen offset:1680 sc1\n"
"v_mov_b32 v100, v216\n"
"v_mov_b32 v102, v217\n"
"buffer_store_dwordx4 v[100:103], v245, s[28:31], s60 offen offset:3072 sc1\n"
"v_mov_b32 v104, v218\n"
"v_mov_b32 v106, v219\n"
"buffer_store_dwordx4 v[104:107], v245, s[28:31], s60 offen offset:3088 sc1\n"
"v_mov_b32 v108, v220\n"
"v_mov_b32 v110, v221\n"
"buffer_store_dwordx4 v[108:111], v245, s[28:31], s60 offen offset:3200 sc1\n"
"v_mov_b32 v112, v222\n"
"v_mov_b32 v114, v223\n"
"buffer_store_dwordx4 v[112:115], v245, s[28:31], s60 offen offset:3216 sc1\n"
"AT_A5:\n"
"s_mov_b64 exec, -1\n"
"s_waitcnt lgkmcnt(0)\n"
"s_barrier\n"
"ds_read_b32 v0, v249 offset:2048\n"
"ds_read_b32 v1, v249 offset:2144\n"
"ds_read_b32 v2, v249 offset:2432\n"
"ds_read_b32 v3, v249 offset:2528\n"
"ds_read_b32 v4, v249 offset:2816\n"
"ds_read_b32 v5, v249 offset:2912\n"
"ds_read_b32 v6, v249 offset:3200\n"
"ds_read_b32 v7, v249 offset:3296\n"
"v_cmp_gt_u32 s[58:59], 32, v248\n"
"s_waitcnt lgkmcnt(5)\n"
"v_add_f32 v0, v0, v2\n"
"s_waitcnt lgkmcnt(1)\n"
"v_add_f32 v4, v4, v6\n"
"v_add_f32 v1, v1, v3\n"
"v_add_f32 v0, v0, v4\n"
"s_waitcnt lgkmcnt(0)\n"
"v_add_f32 v5, v5, v7\n"
"v_mul_f32 v2, 0xbfb8aa3b, v0\n"
"v_add_f32 v1, v1, v5\n"
"v_exp_f32 v2, v2\n"
"v_mul_f32 v0, v0, v1\n"
"v_add_f32 v2, 1.0, v2\n"
"v_rcp_f32 v2, v2\n"
"s_mov_b32 exec_hi, 0xffff\n"
"v_mul_f32 v0, v0, v2\n"
"v_cvt_pk_bf16_f32 v1, v0, v0\n"
"ds_write_b16 v251, v1 offset:4096\n"
"v_and_b32 v2, 0xffff0000, v1\n"
"v_sub_f32 v0, v0, v2\n"
"v_cvt_pk_bf16_f32 v0, v0, v0\n"
"ds_write_b16 v251, v0 offset:4160\n"
"s_mov_b64 exec, -1\n"
"ds_read_b128 v[8:11], v252 offset:4096\n"
"s_waitcnt lgkmcnt(0)\n"
"v_mfma_f32_16x16x32_bf16 v[40:43], v[8:11], a[192:195], 0\n"
"v_mfma_f32_16x16x32_bf16 v[44:47], v[8:11], a[196:199], 0\n"
"v_mfma_f32_16x16x32_bf16 v[48:51], v[8:11], a[200:203], 0\n"
"v_mfma_f32_16x16x32_bf16 v[52:55], v[8:11], a[204:207], 0\n"
"v_mfma_f32_16x16x32_bf16 v[56:59], v[8:11], a[208:211], 0\n"
"v_mfma_f32_16x16x32_bf16 v[60:63], v[8:11], a[212:215], 0\n"
"v_mfma_f32_16x16x32_bf16 v[64:67], v[8:11], a[216:219], 0\n"
"v_mfma_f32_16x16x32_bf16 v[68:71], v[8:11], a[220:223], 0\n"
"v_mfma_f32_16x16x32_bf16 v[72:75], v[8:11], a[224:227], 0\n"
"v_add_f32 v40, v40, v41\n"
"v_add_f32 v44, v44, v45\n"
"v_add_f32 v48, v48, v49\n"
"v_add_f32 v52, v52, v53\n"
"v_cndmask_b32 v84, v48, v40, s[58:59]\n"
"v_cndmask_b32 v86, v52, v44, s[58:59]\n"
"buffer_store_dwordx4 v[84:87], v243, s[28:31], s57 offen sc1\n"
"v_add_f32 v56, v56, v57\n"
"v_add_f32 v60, v60, v61\n"
"v_add_f32 v64, v64, v65\n"
"v_add_f32 v68, v68, v69\n"
"v_cndmask_b32 v88, v64, v56, s[58:59]\n"
"v_cndmask_b32 v90, v68, v60, s[58:59]\n"
"buffer_store_dwordx4 v[88:91], v243, s[28:31], s62 offen sc1\n"
"v_add_f32 v92, v72, v73\n"
"buffer_store_dwordx2 v[92:93], v224, s[28:31], s63 offen sc1\n"
"ds_read_b128 v[228:231], v184 offset:0\n"
"ds_read_b128 v[232:235], v184 offset:1024\n"
"ds_read_b128 v[236:239], v184 offset:2048\n"
"v_cmp_ne_u32 vcc, s12, v180\n"
"v_cmp_ne_u32 s[60:61], s13, v180\n"
"s_waitcnt lgkmcnt(2)\n"
"s_and_b64 exec, vcc, s[60:61]\n"
"buffer_store_dwordx4 v[228:231], v250, s[76:79], s46 offen offset:0 sc1\n"
"s_mov_b64 exec, -1\n"
"v_cmp_ne_u32 vcc, s12, v181\n"
"v_cmp_ne_u32 s[60:61], s13, v181\n"
"s_waitcnt lgkmcnt(1)\n"
"s_and_b64 exec, vcc, s[60:61]\n"
"buffer_store_dwordx4 v[232:235], v250, s[76:79], s46 offen offset:1024 sc1\n"
"s_mov_b64 exec, -1\n"
"v_cmp_ne_u32 vcc, s12, v182\n"
"v_cmp_ne_u32 s[60:61], s13, v182\n"
"s_waitcnt lgkmcnt(0)\n"
"s_and_b64 exec, vcc, s[60:61]\n"
"buffer_store_dwordx4 v[236:239], v250, s[76:79], s46 offen offset:2048 sc1\n"
"s_mov_b64 exec, -1\n"
"s_branch AT_F1\n"
"AT_F0:\n"
"v_cmp_eq_u32 vcc, s10, v225\n"
"v_cmp_eq_u32 s[58:59], s11, v225\n"
"s_nop 0\n"
"s_or_b64 s[58:59], s[58:59], vcc\n"
"s_and_b64 exec, s[8:9], s[58:59]\n"
"s_cbranch_execz AT_F0b\n"
"s_add_u32 s60, s48, 4608\n"
"v_mov_b32 v101, s85\n"
"v_mov_b32 v103, s85\n"
"v_mov_b32 v105, s85\n"
"v_mov_b32 v107, s85\n"
"v_mov_b32 v109, s85\n"
"v_mov_b32 v111, s85\n"
"v_mov_b32 v113, s85\n"
"v_mov_b32 v115, s85\n"
"v_mov_b32 v100, v200\n"
"v_mov_b32 v102, v201\n"
"buffer_store_dwordx4 v[100:103], v245, s[28:31], s60 offen offset:0 sc1\n"
"v_mov_b32 v104, v202\n"
"v_mov_b32 v106, v203\n"
"buffer_store_dwordx4 v[104:107], v245, s[28:31], s60 offen offset:16 sc1\n"
"v_mov_b32 v108, v204\n"
"v_mov_b32 v110, v205\n"
"buffer_store_dwordx4 v[108:111], v245, s[28:31], s60 offen offset:128 sc1\n"
"v_mov_b32 v112, v206\n"
"v_mov_b32 v114, v207\n"
"buffer_store_dwordx4 v[112:115], v245, s[28:31], s60 offen offset:144 sc1\n"
"v_mov_b32 v100, v208\n"
"v_mov_b32 v102, v209\n"
"buffer_store_dwordx4 v[100:103], v245, s[28:31], s60 offen offset:1536 sc1\n"
"v_mov_b32 v104, v210\n"
"v_mov_b32 v106, v211\n"
"buffer_store_dwordx4 v[104:107], v245, s[28:31], s60 offen offset:1552 sc1\n"
"v_mov_b32 v108, v212\n"
"v_mov_b32 v110, v213\n"
"buffer_store_dwordx4 v[108:111], v245, s[28:31], s60 offen offset:1664 sc1\n"
"v_mov_b32 v112, v214\n"
"v_mov_b32 v114, v215\n"
"buffer_store_dwordx4 v[112:115], v245, s[28:31], s60 offen offset:1680 sc1\n"
"v_mov_b32 v100, v216\n"
"v_mov_b32 v102, v217\n"
"buffer_store_dwordx4 v[100:103], v245, s[28:31], s60 offen offset:3072 sc1\n"
"v_mov_b32 v104, v218\n"
"v_mov_b32 v106, v219\n"
"buffer_store_dwordx4 v[104:107], v245, s[28:31], s60 offen offset:3088 sc1\n"
"v_mov_b32 v108, v220\n"
"v_mov_b32 v110, v221\n"
"buffer_store_dwordx4 v[108:111], v245, s[28:31], s60 offen offset:3200 sc1\n"
"v_mov_b32 v112, v222\n"
"v_mov_b32 v114, v223\n"
"buffer_store_dwordx4 v[112:115], v245, s[28:31], s60 offen offset:3216 sc1\n"
"AT_F0b:\n"
"s_mov_b64 exec, -1\n"
"ds_read_b128 v[228:231], v184 offset:0\n"
"ds_read_b128 v[232:235], v184 offset:1024\n"
"ds_read_b128 v[236:239], v184 offset:2048\n"
"v_cmp_ne_u32 vcc, s12, v180\n"
"v_cmp_ne_u32 s[60:61], s13, v180\n"
"s_waitcnt lgkmcnt(2)\n"
"s_and_b64 exec, vcc, s[60:61]\n"
"buffer_store_dwordx4 v[228:231], v250, s[76:79], s46 offen offset:0 sc1\n"
"s_mov_b64 exec, -1\n"
"v_cmp_ne_u32 vcc, s12, v181\n"
"v_cmp_ne_u32 s[60:61], s13, v181\n"
"s_waitcnt lgkmcnt(1)\n"
"s_and_b64 exec, vcc, s[60:61]\n"
"buffer_store_dwordx4 v[232:235], v250, s[76:79], s46 offen offset:1024 sc1\n"
"s_mov_b64 exec, -1\n"
"v_cmp_ne_u32 vcc, s12, v182\n"
"v_cmp_ne_u32 s[60:61], s13, v182\n"
"s_waitcnt lgkmcnt(0)\n"
"s_and_b64 exec, vcc, s[60:61]\n"
"buffer_store_dwordx4 v[236:239], v250, s[76:79], s46 offen offset:2048 sc1\n"
"s_mov_b64 exec, -1\n"
"s_sleep 28\n"
"AT_F1:\n"
"s_cmp_lt_u32 s42, 184\n"
"s_cbranch_scc1 AT_end\n"
"s_sub_u32 s56, s42, 184\n"
"s_mul_i32 s57, s56, 57\n"
"s_lshr_b32 s57, s57, 10\n"
"s_mul_i32 s58, s57, 144\n"
"s_mul_i32 s57, s57, 18\n"
"s_sub_u32 s57, s56, s57\n"
"s_lshr_b32 s61, s57, 2\n"
"s_lshl_b32 s61, s61, 5\n"
"s_add_u32 s58, s58, s61\n"
"s_and_b32 s61, s57, 1\n"
"s_lshl_b32 s62, s61, 3\n"
"s_bfe_u32 s63, s57, 0x10001\n"
"s_cmp_ge_u32 s57, 16\n"
"s_cselect_b32 s62, 0, s62\n"
"s_cselect_b32 s63, s61, s63\n"
"s_cselect_b64 vcc, -1, 0\n"
"s_add_u32 s58, s58, s62\n"
"s_cmp_eq_u32 s63, 1\n"
"s_cselect_b32 s59, s13, s12\n"
"s_cselect_b32 s60, s11, s10\n"
"s_mul_i32 s56, s56, 24576\n"
"s_add_u32 s56, s56, 56762368\n"
"s_mul_i32 s61, s59, 1152\n"
"s_add_u32 s61, s61, s58\n"
"s_lshl_b32 s61, s61, 3\n"
"s_add_u32 s61, s61, 37892608\n"
"v_lshlrev_b32 v30, 4, v253\n"
"v_add_u32 v31, 4096, v30\n"
"v_add_u32 v32, 8192, v30\n"
"v_add_u32 v33, 12288, v30\n"
"v_add_u32 v34, 16384, v30\n"
"v_add_u32 v35, 20480, v30\n"
"v_and_b32 v36, 15, v248\n"
"v_lshlrev_b32 v37, 3, v36\n"
"v_and_b32 v41, 1, v36\n"
"v_lshrrev_b32 v42, 1, v36\n"
"v_lshl_add_u32 v41, v41, 4, v42\n"
"v_cndmask_b32 v41, v41, v36, vcc\n"
"v_lshlrev_b32 v42, 3, v41\n"
"s_lshl_b32 s62, s58, 2\n"
"s_add_u32 s62, s62, s40\n"
"s_add_u32 s62, s62, 158720\n"
"v_lshl_add_u32 v38, v41, 2, s62\n"
"ds_read_b32 v39, v38\n"
"s_mov_b32 s87, 0x80000\n"
"AT_F2:\n"
"buffer_load_dwordx4 v[0:3], v30, s[28:31], s56 offen sc1\n"
"buffer_load_dwordx4 v[4:7], v31, s[28:31], s56 offen sc1\n"
"buffer_load_dwordx4 v[8:11], v32, s[28:31], s56 offen sc1\n"
"buffer_load_dwordx4 v[12:15], v33, s[28:31], s56 offen sc1\n"
"buffer_load_dwordx4 v[16:19], v34, s[28:31], s56 offen sc1\n"
"buffer_load_dwordx4 v[20:23], v35, s[28:31], s56 offen sc1\n"
"buffer_load_dwordx2 v[24:25], v42, s[28:31], s61 offen sc1\n"
"s_and_b32 s62, s60, s14\n"
"s_ashr_i32 s63, s62, 31\n"
"s_not_b32 s63, s63\n"
"s_and_b32 s63, s63, s14\n"
"s_or_b32 s63, s63, 0x80000000\n"
"s_xor_b32 s62, s62, s63\n"
"v_mov_b32 v40, s62\n"
"v_mul_f32 v40, 0xbfb8aa3b, v40\n"
"v_exp_f32 v40, v40\n"
"s_nop 0\n"
"v_add_f32 v40, 1.0, v40\n"
"v_rcp_f32 v40, v40\n"
"s_waitcnt vmcnt(0) lgkmcnt(0)\n"
"v_xor_b32 v26, s85, v1\n"
"v_xor_b32 v27, s85, v3\n"
"v_bitop3_b32 v26, v5, s85, v26 bitop3:0xbe\n"
"v_bitop3_b32 v27, v7, s85, v27 bitop3:0xbe\n"
"v_bitop3_b32 v26, v9, s85, v26 bitop3:0xbe\n"
"v_bitop3_b32 v27, v11, s85, v27 bitop3:0xbe\n"
"v_bitop3_b32 v26, v13, s85, v26 bitop3:0xbe\n"
"v_bitop3_b32 v27, v15, s85, v27 bitop3:0xbe\n"
"v_bitop3_b32 v26, v17, s85, v26 bitop3:0xbe\n"
"v_bitop3_b32 v27, v19, s85, v27 bitop3:0xbe\n"
"v_bitop3_b32 v26, v21, s85, v26 bitop3:0xbe\n"
"v_bitop3_b32 v27, v23, s85, v27 bitop3:0xbe\n"
"v_bitop3_b32 v26, v25, s85, v26 bitop3:0xbe\n"
"v_or_b32 v26, v26, v27\n"
"v_cmp_ne_u32 vcc, 0, v26\n"
"s_nop 0\n"
"s_cbranch_vccz AT_F3\n"
"s_sub_u32 s87, s87, 1\n"
"s_cmp_eq_u32 s87, 0\n"
"s_cbranch_scc1 AT_F3\n"
"s_sleep 2\n"
"s_branch AT_F2\n"
"AT_F3:\n"
"v_add_f32 v0, v0, v4\n"
"v_add_f32 v2, v2, v6\n"
"v_add_f32 v8, v8, v12\n"
"v_add_f32 v10, v10, v14\n"
"v_add_f32 v16, v16, v20\n"
"v_add_f32 v18, v18, v22\n"
"v_add_f32 v0, v0, v8\n"
"v_add_f32 v2, v2, v10\n"
"v_add_f32 v0, v0, v16\n"
"v_add_f32 v1, v2, v18\n"
"v_mul_f32 v40, v40, v39\n"
"s_lshl_b32 s62, s41, 6\n"
"v_add3_u32 v28, v37, v227, s62\n"
"v_add_f32_dpp v0, v0, v0 row_ror:8 row_mask:0xf bank_mask:0xf\n"
"v_add_f32_dpp v1, v1, v1 row_ror:8 row_mask:0xf bank_mask:0xf\n"
"s_nop 0\n"
"v_mov_b32 v2, v0\n"
"v_mov_b32 v3, v1\n"
"s_nop 0\n"
"v_permlane16_swap_b32 v0, v2\n"
"v_permlane16_swap_b32 v1, v3\n"
"v_add_f32 v0, v0, v2\n"
"v_add_f32 v1, v1, v3\n"
"v_mov_b32 v2, v0\n"
"v_mov_b32 v3, v1\n"
"s_nop 0\n"
"v_permlane32_swap_b32 v0, v2\n"
"v_permlane32_swap_b32 v1, v3\n"
"v_add_f32 v0, v0, v2\n"
"v_add_f32 v1, v1, v3\n"
"s_mov_b64 exec, 0xff\n"
"ds_write_b64 v28, v[0:1] offset:1024\n"
"s_mov_b64 exec, -1\n"
"v_lshl_add_u32 v28, v36, 2, v227\n"
"s_waitcnt lgkmcnt(0)\n"
"s_barrier\n"
"s_cmp_lg_u32 s41, 0\n"
"s_cbranch_scc1 AT_end\n"
"ds_read_b32 v0, v28 offset:1024\n"
"ds_read_b32 v1, v28 offset:1088\n"
"ds_read_b32 v2, v28 offset:1152\n"
"ds_read_b32 v3, v28 offset:1216\n"
"s_mul_i32 s61, s59, 576\n"
"s_add_u32 s61, s61, s58\n"
"s_lshl_b32 s61, s61, 1\n"
"v_lshlrev_b32 v5, 1, v41\n"
"s_waitcnt lgkmcnt(0)\n"
"v_add_f32 v0, v0, v1\n"
"v_add_f32 v2, v2, v3\n"
"v_add_f32 v0, v0, v2\n"
"v_fma_f32 v0, v0, v40, v24\n"
"v_cvt_pk_bf16_f32 v0, v0, v0\n"
"s_mov_b64 exec, 0xffff\n"
"buffer_store_short v0, v5, s[76:79], s61 offen sc1\n"
"s_branch AT_end\n"
"AT_x1:\n"
"s_mov_b32 s87, 0x80000\n"
"AT_x3:\n"
"buffer_load_dword v249, v215, s[28:31], s23 offen sc1\n"
"s_waitcnt vmcnt(0)\n"
"v_cmp_ne_u32 vcc, s85, v249\n"
"s_sub_u32 s87, s87, 1\n"
"s_cbranch_vccz AT_x2\n"
"s_cmp_eq_u32 s87, 0\n"
"s_cbranch_scc1 AT_x2\n"
"s_sleep 1\n"
"s_branch AT_x3\n"
"AT_fb:\n"
"s_waitcnt vmcnt(0) lgkmcnt(0)\n"
"s_barrier\n"
"s_mov_b32 s45, 1\n"
"s_mov_b32 s87, 32\n"
"s_lshl_b32 s84, s41, 12\n"
"s_lshl_b32 s56, s43, 19\n"
"s_add_u32 s84, s84, s56\n"
"v_mov_b32 v160, 0xff800000\n"
"v_mov_b32 v161, 0xff800000\n"
"v_mov_b32 v162, 0xff800000\n"
"v_mov_b32 v163, 0xff800000\n"
"AT_fbl:\n"
"s_mov_b32 m0, s86\n"
"s_nop 0\n"
"buffer_load_dwordx4 v246, s[8:11], s84 offen offset:0 lds\n"
"buffer_load_dwordx4 v246, s[8:11], s84 offen offset:1024 lds\n"
"buffer_load_dwordx4 v247, s[8:11], s84 offen offset:2048 lds\n"
"buffer_load_dwordx4 v247, s[8:11], s84 offen offset:3072 lds\n"
"s_add_u32 s84, s84, 16384\n"
"s_waitcnt vmcnt(0)\n"
"s_barrier\n"
"ds_read_b128 a[128:131], v240 offset:0\n"
"ds_read_b128 a[132:135], v240 offset:256\n"
"ds_read_b128 a[136:139], v240 offset:512\n"
"ds_read_b128 a[140:143], v240 offset:768\n"
"ds_read_b128 a[144:147], v240 offset:4096\n"
"ds_read_b128 a[148:151], v240 offset:4352\n"
"ds_read_b128 a[152:155], v240 offset:4608\n"
"ds_read_b128 a[156:159], v240 offset:4864\n"
"s_waitcnt lgkmcnt(0)\n"
"v_mfma_f32_16x16x32_bf16 v[128:131], a[128:131], v[64:67], 0\n"
"v_mfma_f32_16x16x32_bf16 v[128:131], a[132:135], v[68:71], v[128:131]\n"
"v_mfma_f32_16x16x32_bf16 v[128:131], a[136:139], v[72:75], v[128:131]\n"
"v_mfma_f32_16x16x32_bf16 v[128:131], a[140:143], v[76:79], v[128:131]\n"
"v_mfma_f32_16x16x32_bf16 v[132:135], a[144:147], v[64:67], 0\n"
"v_mfma_f32_16x16x32_bf16 v[132:135], a[148:151], v[68:71], v[132:135]\n"
"v_mfma_f32_16x16x32_bf16 v[132:135], a[152:155], v[72:75], v[132:135]\n"
"v_mfma_f32_16x16x32_bf16 v[132:135], a[156:159], v[76:79], v[132:135]\n"
"v_mfma_f32_16x16x32_bf16 v[136:139], a[128:131], v[80:83], 0\n"
"v_mfma_f32_16x16x32_bf16 v[136:139], a[132:135], v[84:87], v[136:139]\n"
"v_mfma_f32_16x16x32_bf16 v[136:139], a[136:139], v[88:91], v[136:139]\n"
"v_mfma_f32_16x16x32_bf16 v[136:139], a[140:143], v[92:95], v[136:139]\n"
"v_mfma_f32_16x16x32_bf16 v[140:143], a[144:147], v[80:83], 0\n"
"v_mfma_f32_16x16x32_bf16 v[140:143], a[148:151], v[84:87], v[140:143]\n"
"v_mfma_f32_16x16x32_bf16 v[140:143], a[152:155], v[88:91], v[140:143]\n"
"v_mfma_f32_16x16x32_bf16 v[140:143], a[156:159], v[92:95], v[140:143]\n"
"v_mfma_f32_16x16x32_bf16 v[144:147], a[128:131], v[96:99], 0\n"
"v_mfma_f32_16x16x32_bf16 v[144:147], a[132:135], v[100:103], v[144:147]\n"
"v_mfma_f32_16x16x32_bf16 v[144:147], a[136:139], v[104:107], v[144:147]\n"
"v_mfma_f32_16x16x32_bf16 v[144:147], a[140:143], v[108:111], v[144:147]\n"
"v_mfma_f32_16x16x32_bf16 v[148:151], a[144:147], v[96:99], 0\n"
"v_mfma_f32_16x16x32_bf16 v[148:151], a[148:151], v[100:103], v[148:151]\n"
"v_mfma_f32_16x16x32_bf16 v[148:151], a[152:155], v[104:107], v[148:151]\n"
"v_mfma_f32_16x16x32_bf16 v[148:151], a[156:159], v[108:111], v[148:151]\n"
"v_mfma_f32_16x16x32_bf16 v[152:155], a[128:131], v[112:115], 0\n"
"v_mfma_f32_16x16x32_bf16 v[152:155], a[132:135], v[116:119], v[152:155]\n"
"v_mfma_f32_16x16x32_bf16 v[152:155], a[136:139], v[120:123], v[152:155]\n"
"v_mfma_f32_16x16x32_bf16 v[152:155], a[140:143], v[124:127], v[152:155]\n"
"v_mfma_f32_16x16x32_bf16 v[156:159], a[144:147], v[112:115], 0\n"
"v_mfma_f32_16x16x32_bf16 v[156:159], a[148:151], v[116:119], v[156:159]\n"
"v_mfma_f32_16x16x32_bf16 v[156:159], a[152:155], v[120:123], v[156:159]\n"
"v_mfma_f32_16x16x32_bf16 v[156:159], a[156:159], v[124:127], v[156:159]\n"
"s_nop 7\n"
"v_max3_f32 v160, v160, v128, v129\n"
"v_max3_f32 v160, v160, v130, v131\n"
"v_max3_f32 v160, v160, v132, v133\n"
"v_max3_f32 v160, v160, v134, v135\n"
"v_max3_f32 v161, v161, v136, v137\n"
"v_max3_f32 v161, v161, v138, v139\n"
"v_max3_f32 v161, v161, v140, v141\n"
"v_max3_f32 v161, v161, v142, v143\n"
"v_max3_f32 v162, v162, v144, v145\n"
"v_max3_f32 v162, v162, v146, v147\n"
"v_max3_f32 v162, v162, v148, v149\n"
"v_max3_f32 v162, v162, v150, v151\n"
"v_max3_f32 v163, v163, v152, v153\n"
"v_max3_f32 v163, v163, v154, v155\n"
"v_max3_f32 v163, v163, v156, v157\n"
"v_max3_f32 v163, v163, v158, v159\n"
"ds_read_b128 a[128:131], v240 offset:8192\n"
"ds_read_b128 a[132:135], v240 offset:8448\n"
"ds_read_b128 a[136:139], v240 offset:8704\n"
"ds_read_b128 a[140:143], v240 offset:8960\n"
"ds_read_b128 a[144:147], v240 offset:12288\n"
"ds_read_b128 a[148:151], v240 offset:12544\n"
"ds_read_b128 a[152:155], v240 offset:12800\n"
"ds_read_b128 a[156:159], v240 offset:13056\n"
"s_waitcnt lgkmcnt(0)\n"
"v_mfma_f32_16x16x32_bf16 v[128:131], a[128:131], v[64:67], 0\n"
"v_mfma_f32_16x16x32_bf16 v[128:131], a[132:135], v[68:71], v[128:131]\n"
"v_mfma_f32_16x16x32_bf16 v[128:131], a[136:139], v[72:75], v[128:131]\n"
"v_mfma_f32_16x16x32_bf16 v[128:131], a[140:143], v[76:79], v[128:131]\n"
"v_mfma_f32_16x16x32_bf16 v[132:135], a[144:147], v[64:67], 0\n"
"v_mfma_f32_16x16x32_bf16 v[132:135], a[148:151], v[68:71], v[132:135]\n"
"v_mfma_f32_16x16x32_bf16 v[132:135], a[152:155], v[72:75], v[132:135]\n"
"v_mfma_f32_16x16x32_bf16 v[132:135], a[156:159], v[76:79], v[132:135]\n"
"v_mfma_f32_16x16x32_bf16 v[136:139], a[128:131], v[80:83], 0\n"
"v_mfma_f32_16x16x32_bf16 v[136:139], a[132:135], v[84:87], v[136:139]\n"
"v_mfma_f32_16x16x32_bf16 v[136:139], a[136:139], v[88:91], v[136:139]\n"
"v_mfma_f32_16x16x32_bf16 v[136:139], a[140:143], v[92:95], v[136:139]\n"
"v_mfma_f32_16x16x32_bf16 v[140:143], a[144:147], v[80:83], 0\n"
"v_mfma_f32_16x16x32_bf16 v[140:143], a[148:151], v[84:87], v[140:143]\n"
"v_mfma_f32_16x16x32_bf16 v[140:143], a[152:155], v[88:91], v[140:143]\n"
"v_mfma_f32_16x16x32_bf16 v[140:143], a[156:159], v[92:95], v[140:143]\n"
"v_mfma_f32_16x16x32_bf16 v[144:147], a[128:131], v[96:99], 0\n"
"v_mfma_f32_16x16x32_bf16 v[144:147], a[132:135], v[100:103], v[144:147]\n"
"v_mfma_f32_16x16x32_bf16 v[144:147], a[136:139], v[104:107], v[144:147]\n"
"v_mfma_f32_16x16x32_bf16 v[144:147], a[140:143], v[108:111], v[144:147]\n"
"v_mfma_f32_16x16x32_bf16 v[148:151], a[144:147], v[96:99], 0\n"
"v_mfma_f32_16x16x32_bf16 v[148:151], a[148:151], v[100:103], v[148:151]\n"
"v_mfma_f32_16x16x32_bf16 v[148:151], a[152:155], v[104:107], v[148:151]\n"
"v_mfma_f32_16x16x32_bf16 v[148:151], a[156:159], v[108:111], v[148:151]\n"
"v_mfma_f32_16x16x32_bf16 v[152:155], a[128:131], v[112:115], 0\n"
"v_mfma_f32_16x16x32_bf16 v[152:155], a[132:135], v[116:119], v[152:155]\n"
"v_mfma_f32_16x16x32_bf16 v[152:155], a[136:139], v[120:123], v[152:155]\n"
"v_mfma_f32_16x16x32_bf16 v[152:155], a[140:143], v[124:127], v[152:155]\n"
"v_mfma_f32_16x16x32_bf16 v[156:159], a[144:147], v[112:115], 0\n"
"v_mfma_f32_16x16x32_bf16 v[156:159], a[148:151], v[116:119], v[156:159]\n"
"v_mfma_f32_16x16x32_bf16 v[156:159], a[152:155], v[120:123], v[156:159]\n"
"v_mfma_f32_16x16x32_bf16 v[156:159], a[156:159], v[124:127], v[156:159]\n"
"s_nop 7\n"
"v_max3_f32 v160, v160, v128, v129\n"
"v_max3_f32 v160, v160, v130, v131\n"
"v_max3_f32 v160, v160, v132, v133\n"
"v_max3_f32 v160, v160, v134, v135\n"
"v_max3_f32 v161, v161, v136, v137\n"
"v_max3_f32 v161, v161, v138, v139\n"
"v_max3_f32 v161, v161, v140, v141\n"
"v_max3_f32 v161, v161, v142, v143\n"
"v_max3_f32 v162, v162, v144, v145\n"
"v_max3_f32 v162, v162, v146, v147\n"
"v_max3_f32 v162, v162, v148, v149\n"
"v_max3_f32 v162, v162, v150, v151\n"
"v_max3_f32 v163, v163, v152, v153\n"
"v_max3_f32 v163, v163, v154, v155\n"
"v_max3_f32 v163, v163, v156, v157\n"
"v_max3_f32 v163, v163, v158, v159\n"
"s_barrier\n"
"s_sub_u32 s87, s87, 1\n"
"s_cmp_lg_u32 s87, 0\n"
"s_cbranch_scc1 AT_fbl\n"
"v_mov_b32 v164, v160\n"
"s_nop 1\n"
"v_permlane32_swap_b32 v160, v164\n"
"v_max_f32 v160, v160, v164\n"
"v_mov_b32 v164, v160\n"
"s_nop 1\n"
"v_permlane16_swap_b32 v160, v164\n"
"v_max_f32 v160, v160, v164\n"
"v_sub_f32 v24, 0, v160\n"
"v_sub_f32 v25, 0, v160\n"
"v_sub_f32 v26, 0, v160\n"
"v_sub_f32 v27, 0, v160\n"
"v_mov_b32 v164, v161\n"
"s_nop 1\n"
"v_permlane32_swap_b32 v161, v164\n"
"v_max_f32 v161, v161, v164\n"
"v_mov_b32 v164, v161\n"
"s_nop 1\n"
"v_permlane16_swap_b32 v161, v164\n"
"v_max_f32 v161, v161, v164\n"
"v_sub_f32 v28, 0, v161\n"
"v_sub_f32 v29, 0, v161\n"
"v_sub_f32 v30, 0, v161\n"
"v_sub_f32 v31, 0, v161\n"
"v_mov_b32 v164, v162\n"
"s_nop 1\n"
"v_permlane32_swap_b32 v162, v164\n"
"v_max_f32 v162, v162, v164\n"
"v_mov_b32 v164, v162\n"
"s_nop 1\n"
"v_permlane16_swap_b32 v162, v164\n"
"v_max_f32 v162, v162, v164\n"
"v_sub_f32 v32, 0, v162\n"
"v_sub_f32 v33, 0, v162\n"
"v_sub_f32 v34, 0, v162\n"
"v_sub_f32 v35, 0, v162\n"
"v_mov_b32 v164, v163\n"
"s_nop 1\n"
"v_permlane32_swap_b32 v163, v164\n"
"v_max_f32 v163, v163, v164\n"
"v_mov_b32 v164, v163\n"
"s_nop 1\n"
"v_permlane16_swap_b32 v163, v164\n"
"v_max_f32 v163, v163, v164\n"
"v_sub_f32 v36, 0, v163\n"
"v_sub_f32 v37, 0, v163\n"
"v_sub_f32 v38, 0, v163\n"
"v_sub_f32 v39, 0, v163\n"
"s_and_b32 s62, s42, 7\n"
"s_mul_i32 s46, s62, 663552\n"
"s_lshr_b32 s47, s43, 2\n"
"s_mul_i32 s47, s47, 1179648\n"
"s_getpc_b64 s[32:33]\n"
"AT_pc2:\n"
"s_sub_u32 s32, s32, AT_pc2-AT_start\n"
"s_subb_u32 s33, s33, 0\n"
"s_and_b32 s33, s33, 0xffff\n"
"s_mov_b32 s34, AT_kend-AT_start\n"
"s_mov_b32 s35, 0x20000\n"
"s_lshr_b32 s62, s42, 3\n"
"s_lshl_b32 s62, s62, 2\n"
"s_add_u32 s62, s62, s41\n"
"s_and_b32 s62, s62, 31\n"
"s_lshl_b32 s48, s62, 12\n"
"s_lshl_b32 s54, s44, 2\n"
"s_add_u32 s54, s54, s41\n"
"s_mul_i32 s54, s54, 36864\n"
"s_lshr_b32 s62, s43, 2\n"
"s_mul_i32 s62, s62, 2359296\n"
"s_add_u32 s54, s54, s62\n"
"s_add_u32 s54, s54, 139264\n"
"s_bfe_u32 s62, s43, 0x10001\n"
"s_mul_i32 s62, s62, 9216\n"
"s_add_u32 s54, s54, s62\n"
"s_and_b32 s62, s43, 1\n"
"s_lshl_b32 s49, s62, 3\n"
"s_add_u32 s49, s49, s54\n"
"s_add_u32 s49, s49, 4096\n"
"s_mul_i32 s62, s62, 5120\n"
"s_add_u32 s54, s54, s62\n"
"s_and_b32 s62, s43, 1\n"
"s_lshl_b32 s62, s62, 10\n"
"s_sub_u32 s54, s54, s62\n"
"s_add_u32 s49, s54, 4096\n"
"s_lshl_b32 s52, s44, 4\n"
"s_add_u32 s52, s52, s43\n"
"s_mul_i32 s52, s52, 18432\n"
"s_mul_i32 s62, s41, 5\n"
"s_min_u32 s62, s62, 13\n"
"s_lshl_b32 s62, s62, 10\n"
"s_add_u32 s52, s52, s62\n"
"s_add_u32 s52, s52, 26402816\n"
"s_add_u32 s53, s52, 4096\n"
"v_mov_b32 v40, 0x3f803f80\n"
"v_mov_b32 v41, 0x3f803f80\n"
"v_mov_b32 v42, 0x3f803f80\n"
"v_mov_b32 v43, 0x3f803f80\n"
"v_mov_b32 v20, 0\n"
"v_mov_b32 v21, 0\n"
"v_mov_b32 v22, 0\n"
"v_mov_b32 v23, 0\n"
"v_mov_b32 v44, 0\n"
"v_mov_b32 v52, 0\n"
"v_mov_b32 v216, -1\n"
"v_mov_b32 v217, -1\n"
"v_mov_b32 v218, -1\n"
"v_mov_b32 v219, -1\n"
"v_lshl_add_u32 v255, v248, 6, s48\n"
"v_lshlrev_b32 v207, 4, v248\n"
"v_add_u32 v208, 18432, v207\n"
"s_lshl_b32 s84, s41, 12\n"
"s_lshl_b32 s56, s43, 19\n"
"s_add_u32 s84, s84, s56\n"
"v_mov_b32 v251, 0x40000000\n"
"v_mov_b32 v252, 0x40000000\n"
"v_mov_b32 v253, 0x40000000\n"
"v_add_f32 v148, 0xc2480000, v161\n"
"v_add_f32 v149, 0xc2480000, v161\n"
"v_add_f32 v150, 0xc2480000, v161\n"
"v_add_f32 v151, 0xc2480000, v161\n"
"v_add_f32 v156, 0xc2480000, v163\n"
"v_add_f32 v157, 0xc2480000, v163\n"
"v_add_f32 v158, 0xc2480000, v163\n"
"v_add_f32 v159, 0xc2480000, v163\n"
"s_mov_b32 m0, s86\n"
"s_nop 0\n"
"buffer_load_dwordx4 v246, s[8:11], s84 offen offset:0 lds\n"
"buffer_load_dwordx4 v246, s[8:11], s84 offen offset:1024 lds\n"
"buffer_load_dwordx4 v247, s[8:11], s84 offen offset:2048 lds\n"
"buffer_load_dwordx4 v247, s[8:11], s84 offen offset:3072 lds\n"
"s_add_u32 m0, s86, 16384\n"
"s_nop 0\n"
"buffer_load_dwordx4 v246, s[12:15], s84 offen offset:0 lds\n"
"buffer_load_dwordx4 v247, s[12:15], s84 offen offset:1024 lds\n"
"buffer_load_dwordx4 v246, s[12:15], s84 offen offset:2048 lds\n"
"buffer_load_dwordx4 v247, s[12:15], s84 offen offset:3072 lds\n"
"s_add_u32 s84, s84, 16384\n"
"s_add_u32 m0, s86, 32768\n"
"s_nop 0\n"
"buffer_load_dwordx4 v246, s[8:11], s84 offen offset:0 lds\n"
"buffer_load_dwordx4 v246, s[8:11], s84 offen offset:1024 lds\n"
"buffer_load_dwordx4 v247, s[8:11], s84 offen offset:2048 lds\n"
"buffer_load_dwordx4 v247, s[8:11], s84 offen offset:3072 lds\n"
"s_add_u32 m0, s86, 49152\n"
"s_nop 0\n"
"buffer_load_dwordx4 v246, s[12:15], s84 offen offset:0 lds\n"
"buffer_load_dwordx4 v247, s[12:15], s84 offen offset:1024 lds\n"
"buffer_load_dwordx4 v246, s[12:15], s84 offen offset:2048 lds\n"
"buffer_load_dwordx4 v247, s[12:15], s84 offen offset:3072 lds\n"
"s_add_u32 s84, s84, 16384\n"
"s_add_u32 m0, s86, 65536\n"
"s_nop 0\n"
"buffer_load_dwordx4 v246, s[8:11], s84 offen offset:0 lds\n"
"buffer_load_dwordx4 v246, s[8:11], s84 offen offset:1024 lds\n"
"buffer_load_dwordx4 v247, s[8:11], s84 offen offset:2048 lds\n"
"buffer_load_dwordx4 v247, s[8:11], s84 offen offset:3072 lds\n"
"s_add_u32 m0, s86, 81920\n"
"s_nop 0\n"
"buffer_load_dwordx4 v246, s[12:15], s84 offen offset:0 lds\n"
"buffer_load_dwordx4 v247, s[12:15], s84 offen offset:1024 lds\n"
"buffer_load_dwordx4 v246, s[12:15], s84 offen offset:2048 lds\n"
"buffer_load_dwordx4 v247, s[12:15], s84 offen offset:3072 lds\n"
"s_add_u32 s84, s84, 16384\n"
"s_waitcnt vmcnt(16)\n"
"s_barrier\n"
"ds_read_b128 a[128:131], v240 offset:0\n"
"ds_read_b128 a[132:135], v240 offset:256\n"
"ds_read_b128 a[136:139], v240 offset:512\n"
"ds_read_b128 a[140:143], v240 offset:768\n"
"ds_read_b128 a[144:147], v240 offset:4096\n"
"ds_read_b128 a[148:151], v240 offset:4352\n"
"ds_read_b128 a[152:155], v240 offset:4608\n"
"ds_read_b128 a[156:159], v240 offset:4864\n"
"ds_read_b64_tr_b16 a[192:193], v242 offset:16384\n"
"ds_read_b64_tr_b16 a[194:195], v242 offset:20480\n"
"ds_read_b64_tr_b16 a[196:197], v243 offset:16384\n"
"ds_read_b64_tr_b16 a[198:199], v243 offset:20480\n"
"ds_read_b64_tr_b16 a[200:201], v242 offset:16640\n"
"ds_read_b64_tr_b16 a[202:203], v242 offset:20736\n"
"ds_read_b64_tr_b16 a[204:205], v243 offset:16640\n"
"ds_read_b64_tr_b16 a[206:207], v243 offset:20736\n"
"ds_read_b64_tr_b16 a[208:209], v242 offset:16896\n"
"ds_read_b64_tr_b16 a[210:211], v242 offset:20992\n"
"ds_read_b64_tr_b16 a[212:213], v243 offset:16896\n"
"ds_read_b64_tr_b16 a[214:215], v243 offset:20992\n"
"ds_read_b64_tr_b16 a[216:217], v242 offset:17152\n"
"ds_read_b64_tr_b16 a[218:219], v242 offset:21248\n"
"ds_read_b64_tr_b16 a[220:221], v243 offset:17152\n"
"ds_read_b64_tr_b16 a[222:223], v243 offset:21248\n"
"s_waitcnt lgkmcnt(0)\n"
"s_branch AT_main\n"
"AT_end:\n"
"AT_kend:\n"
  :: [kp] "s"(__builtin_amdgcn_kernarg_segment_ptr()), [blk] "s"(blockIdx.x), [wid] "s"(wid), [lds] "s"(ldsb) : "v0", "v1", "v2", "v3", "v4", "v5", "v6", "v7", "v8", "v9", "v10", "v11", "v12", "v13", "v14", "v15", "v16", "v17", "v18", "v19", "v20", "v21", "v22", "v23", "v24", "v25", "v26", "v27", "v28", "v29", "v30", "v31", "v32", "v33", "v34", "v35", "v36", "v37", "v38", "v39", "v40", "v41", "v42", "v43", "v44", "v45", "v46", "v47", "v48", "v49", "v50", "v51", "v52", "v53", "v54", "v55", "v56", "v57", "v58", "v59", "v60", "v61", "v62", "v63", "v64", "v65", "v66", "v67", "v68", "v69", "v70", "v71", "v72", "v73", "v74", "v75", "v76", "v77", "v78", "v79", "v80", "v81", "v82", "v83", "v84", "v85", "v86", "v87", "v88", "v89", "v90", "v91", "v92", "v93", "v94", "v95", "v96", "v97", "v98", "v99", "v100", "v101", "v102", "v103", "v104", "v105", "v106", "v107", "v108", "v109", "v110", "v111", "v112", "v113", "v114", "v115", "v116", "v117", "v118", "v119", "v120", "v121", "v122", "v123", "v124", "v125", "v126", "v127", "v128", "v129", "v130", "v131", "v132", "v133", "v134", "v135", "v136", "v137", "v138", "v139", "v140", "v141", "v142", "v143", "v144", "v145", "v146", "v147", "v148", "v149", "v150", "v151", "v152", "v153", "v154", "v155", "v156", "v157", "v158", "v159", "v160", "v161", "v162", "v163", "v164", "v165", "v166", "v167", "v168", "v169", "v170", "v171", "v172", "v173", "v174", "v175", "v176", "v177", "v178", "v179", "v180", "v181", "v182", "v183", "v184", "v185", "v186", "v187", "v188", "v189", "v190", "v191", "v192", "v193", "v194", "v195", "v196", "v197", "v198", "v199", "v200", "v201", "v202", "v203", "v204", "v205", "v206", "v207", "v208", "v209", "v210", "v211", "v212", "v213", "v214", "v215", "v216", "v217", "v218", "v219", "v220", "v221", "v222", "v223", "v224", "v225", "v226", "v227", "v228", "v229", "v230", "v231", "v232", "v233", "v234", "v235", "v236", "v237", "v238", "v239", "v240", "v241", "v242", "v243", "v244", "v245", "v246", "v247", "v248", "v249", "v250", "v251", "v252", "v253", "v254", "v255", "a0", "a1", "a2", "a3", "a4", "a5", "a6", "a7", "a8", "a9", "a10", "a11", "a12", "a13", "a14", "a15", "a16", "a17", "a18", "a19", "a20", "a21", "a22", "a23", "a24", "a25", "a26", "a27", "a28", "a29", "a30", "a31", "a32", "a33", "a34", "a35", "a36", "a37", "a38", "a39", "a40", "a41", "a42", "a43", "a44", "a45", "a46", "a47", "a48", "a49", "a50", "a51", "a52", "a53", "a54", "a55", "a56", "a57", "a58", "a59", "a60", "a61", "a62", "a63", "a64", "a65", "a66", "a67", "a68", "a69", "a70", "a71", "a72", "a73", "a74", "a75", "a76", "a77", "a78", "a79", "a80", "a81", "a82", "a83", "a84", "a85", "a86", "a87", "a88", "a89", "a90", "a91", "a92", "a93", "a94", "a95", "a96", "a97", "a98", "a99", "a100", "a101", "a102", "a103", "a104", "a105", "a106", "a107", "a108", "a109", "a110", "a111", "a112", "a113", "a114", "a115", "a116", "a117", "a118", "a119", "a120", "a121", "a122", "a123", "a124", "a125", "a126", "a127", "a128", "a129", "a130", "a131", "a132", "a133", "a134", "a135", "a136", "a137", "a138", "a139", "a140", "a141", "a142", "a143", "a144", "a145", "a146", "a147", "a148", "a149", "a150", "a151", "a152", "a153", "a154", "a155", "a156", "a157", "a158", "a159", "a160", "a161", "a162", "a163", "a164", "a165", "a166", "a167", "a168", "a169", "a170", "a171", "a172", "a173", "a174", "a175", "a176", "a177", "a178", "a179", "a180", "a181", "a182", "a183", "a184", "a185", "a186", "a187", "a188", "a189", "a190", "a191", "a192", "a193", "a194", "a195", "a196", "a197", "a198", "a199", "a200", "a201", "a202", "a203", "a204", "a205", "a206", "a207", "a208", "a209", "a210", "a211", "a212", "a213", "a214", "a215", "a216", "a217", "a218", "a219", "a220", "a221", "a222", "a223", "a224", "a225", "a226", "a227", "a228", "a229", "a230", "a231", "a232", "a233", "a234", "a235", "a236", "a237", "a238", "a239", "a240", "a241", "a242", "a243", "a244", "a245", "a246", "a247", "a248", "a249", "a250", "a251", "a252", "a253", "a254", "a255", "s8", "s9", "s10", "s11", "s12", "s13", "s14", "s15", "s16", "s17", "s18", "s19", "s20", "s21", "s22", "s23", "s24", "s25", "s26", "s27", "s28", "s29", "s30", "s31", "s32", "s33", "s34", "s35", "s36", "s37", "s38", "s39", "s40", "s41", "s42", "s43", "s44", "s45", "s46", "s47", "s48", "s49", "s50", "s51", "s52", "s53", "s54", "s55", "s56", "s57", "s58", "s59", "s60", "s61", "s62", "s63", "s64", "s65", "s66", "s67", "s68", "s69", "s70", "s71", "s72", "s73", "s74", "s75", "s76", "s77", "s78", "s79", "s80", "s81", "s82", "s83", "s84", "s85", "s86", "s87", "s88", "s89", "s90", "s91", "vcc", "scc", "memory");
}

extern "C" void kernel_launch(void* const* d_in, const int* in_sizes, int n_in, void* d_out, int out_size, void* d_ws, size_t ws_size,
                              hipStream_t stream) {
  const unsigned F = 0x20000u;
  KArgs a;
  a.k = {d_in[1], HKV * SQ * 256u, F}; a.v = {d_in[2], HKV * SQ * 256u, F}; a.x = {d_in[0], SQ * DM * 2u, F}; a.wq = {d_in[3], DM * 4096u * 2u, F};
  a.wo = {d_in[4], DM * 4096u * 2u, F}; a.ws = {d_ws, (unsigned)WS_END, F}; a.c = {d_in[10], DM * 2u, F}; a.wmod = {d_in[11], DM * 6u * DM * 2u, F};
  a.wg = {d_in[7], DM * FF * 2u, F}; a.wu = {d_in[8], DM * FF * 2u, F}; a.wd = {d_in[9], DM * FF * 2u, F}; a.out = {d_out, SQ * DM * 2u, F}; a.wr = {d_in[6], DM * 2u, F};
  (void)hipFuncSetAttribute((const void*)k_main, hipFuncAttributeMaxDynamicSharedMemorySize, 163840);
  k_main<<<256, 256, 163840, stream>>>(a);
}
```

```cpp
#include <hip/hip_runtime.h>
#include <stdint.h>


constexpr int SQ = 2048, DM = 576, HKV = 16, FF = 4608;

constexpr size_t WS_FLAG = 0;
constexpr size_t WS_R    = 20480;
constexpr size_t WS_GRAN = 32768;
constexpr size_t WS_EPOCH = 65536;
constexpr size_t WS_ST   = 69632;
constexpr size_t WS_PART = 139264;
constexpr size_t WS_ROW  = WS_PART + (size_t)HKV * SQ * DM * 2;
constexpr size_t WS_Y    = WS_ROW + (size_t)SQ * DM * 16;
constexpr size_t WS_END  = WS_Y + 72 * 192 * 128;

struct Buf { const void* p; unsigned bytes; unsigned fmt; };
struct KArgs { Buf k, v, x, wq, wo, ws, c, wmod, wg, wu, wd, out, wr; };

__global__ __launch_bounds__(256, 1) __attribute__((aligned(4096))) void k_main(KArgs args) {
  extern __shared__ __attribute__((aligned(16))) char lds[];
  const unsigned wid = __builtin_amdgcn_readfirstlane(threadIdx.x >> 6);
  const unsigned ldsb = (unsigned)(size_t)lds;
  asm volatile(
"AT_start:\n"
"s_load_dwordx16 s[24:39], %[kp], 64\n"
"s_load_dwordx16 s[8:23], %[kp], 0\n"
"s_load_dwordx16 s[64:79], %[kp], 128\n"
"s_load_dwordx4 s[80:83], %[kp], 192\n"
"s_mov_b32 s42, %[blk]\n"
"s_mov_b32 s41, %[wid]\n"
"s_mov_b32 s40, %[lds]\n"
"s_mov_b32 s45, 0\n"
"v_mbcnt_lo_u32_b32 v248, -1, 0\n"
"v_mbcnt_hi_u32_b32 v248, -1, v248\n"
"s_and_b32 s56, s42, 3\n"
"s_lshr_b32 s57, s42, 3\n"
"s_and_b32 s58, s57, 3\n"
"s_lshl_b32 s56, s56, 2\n"
"s_add_u32 s43, s56, s58\n"
"s_lshr_b32 s44, s57, 2\n"
"s_and_b32 s56, s42, 7\n"
"s_lshr_b32 s56, s56, 2\n"
"s_lshl_b32 s56, s56, 3\n"
"s_add_u32 s44, s44, s56\n"
"s_lshr_b32 s56, s42, 5\n"
"s_lshl_b32 s56, s56, 3\n"
"s_and_b32 s58, s42, 7\n"
"s_add_u32 s56, s56, s58\n"
"s_lshl_b32 s56, s56, 2\n"
"s_and_b32 s57, s57, 3\n"
"s_add_u32 s55, s56, s57\n"
"v_lshrrev_b32 v226, 4, v248\n"
"v_bfe_u32 v227, v248, 2, 2\n"
"v_and_b32 v228, 3, v248\n"
"v_and_b32 v229, 15, v248\n"
"v_lshrrev_b32 v230, 3, v248\n"
"v_and_b32 v231, 7, v248\n"
"s_lshl_b32 s56, s41, 6\n"
"v_add_u32 v234, s56, v248\n"
"v_lshrrev_b32 v232, 1, v234\n"
"v_and_b32 v233, 1, v234\n"
"v_lshlrev_b32 v236, 1, v232\n"
"v_mul_u32_u24 v232, 6912, v232\n"
"v_lshl_add_u32 v235, v233, 4, v232\n"
"s_lshl_b32 s3, s55, 5\n"
"s_add_u32 s4, s3, 884736\n"
"s_add_u32 s5, s3, 1769472\n"
"s_add_u32 s6, s3, 2654208\n"
"s_add_u32 s7, s3, 3538944\n"
"v_lshrrev_b32 v232, 1, v230\n"
"v_xor_b32 v232, v232, v231\n"
"v_mul_u32_u24 v233, 1152, v230\n"
"v_lshl_add_u32 v222, v232, 4, v233\n"
"v_xor_b32 v232, 4, v232\n"
"v_lshl_add_u32 v223, v232, 4, v233\n"
"s_lshl_b32 s61, s44, 2\n"
"s_add_u32 s61, s61, s41\n"
"s_mul_i32 s46, s61, 36864\n"
"s_add_u32 s47, s46, 8192\n"
"s_add_u32 s48, s46, 16384\n"
"s_add_u32 s49, s46, 24576\n"
"s_mul_i32 s63, s41, 36864\n"
"s_add_u32 s63, s63, s40\n"
"s_getpc_b64 s[84:85]\n"
"AT_pc0:\n"
"s_sub_u32 s84, s84, AT_pc0-AT_start\n"
"s_subb_u32 s85, s85, 0\n"
"s_and_b32 s85, s85, 0xffff\n"
"s_mov_b32 s86, AT_kend-AT_start\n"
"s_mov_b32 s87, 0x20000\n"
"s_cmp_ge_u32 s42, 224\n"
"s_cselect_b32 s86, s86, 0\n"
"s_lshr_b32 s52, s42, 3\n"
"s_lshl_b32 s52, s52, 2\n"
"s_add_u32 s52, s52, s41\n"
"s_and_b32 s52, s52, 15\n"
"s_lshl_b32 s52, s52, 12\n"
"v_lshlrev_b32 v214, 6, v248\n"
"buffer_load_dword v250, v214, s[84:87], s52 offen\n"
"s_add_u32 s52, s52, 65536\n"
"buffer_load_dword v250, v214, s[84:87], s52 offen\n"
"s_waitcnt lgkmcnt(0)\n"
"s_load_dword s85, s[28:29], 65536\n"
"s_cmp_lt_u32 s55, 216\n"
"s_cselect_b32 s38, s38, 0\n"
"buffer_load_ushort v164, v236, s[32:35], 0 offen offset:0\n"
"buffer_load_ushort v165, v236, s[32:35], 0 offen offset:256\n"
"buffer_load_ushort v166, v236, s[32:35], 0 offen offset:512\n"
"buffer_load_ushort v167, v236, s[32:35], 0 offen offset:768\n"
"buffer_load_ushort v168, v236, s[32:35], 0 offen offset:1024\n"
"buffer_load_dwordx4 v[144:147], v235, s[36:39], s3 offen\n"
"buffer_load_dwordx4 v[148:151], v235, s[36:39], s4 offen\n"
"buffer_load_dwordx4 v[152:155], v235, s[36:39], s5 offen\n"
"buffer_load_dwordx4 v[156:159], v235, s[36:39], s6 offen\n"
"buffer_load_dwordx4 v[160:163], v235, s[36:39], s7 offen\n"
"s_mov_b32 m0, s63\n"
"s_nop 0\n"
"buffer_load_dwordx4 v222, s[16:19], s46 offen offset:0 lds\n"
"buffer_load_dwordx4 v223, s[16:19], s47 offen offset:1024 lds\n"
"buffer_load_dwordx4 v222, s[16:19], s48 offen offset:2048 lds\n"
"buffer_load_dwordx4 v223, s[16:19], s49 offen offset:3072 lds\n"
"s_add_u32 m0, s63, 3968\n"
"s_nop 0\n"
"buffer_load_dwordx4 v222, s[16:19], s46 offen offset:128 lds\n"
"buffer_load_dwordx4 v223, s[16:19], s47 offen offset:1152 lds\n"
"buffer_load_dwordx4 v222, s[16:19], s48 offen offset:2176 lds\n"
"buffer_load_dwordx4 v223, s[16:19], s49 offen offset:3200 lds\n"
"s_lshl_b32 s56, s41, 12\n"
"s_add_u32 s86, s40, s56\n"
"s_lshl_b32 s84, s41, 12\n"
"s_lshl_b32 s56, s43, 19\n"
"s_add_u32 s84, s84, s56\n"
"s_lshl_b32 s50, s41, 17\n"
"s_lshl_b32 s56, s43, 9\n"
"s_add_u32 s50, s50, s56\n"
"s_lshl_b32 s56, s41, 11\n"
"s_add_u32 s51, s40, s56\n"
"s_mov_b32 s61, 32768\n"
"s_mov_b32 s62, 37376\n"
"v_lshlrev_b32 v232, 13, v227\n"
"s_add_u32 m0, s63, 7936\n"
"v_lshl_add_u32 v232, v226, 6, v232\n"
"buffer_load_dwordx4 v222, s[16:19], s46 offen offset:256 lds\n"
"v_lshl_add_u32 v217, v228, 4, v232\n"
"v_and_b32 v232, 2, v227\n"
"v_xor_b32 v232, v232, v226\n"
"v_lshlrev_b32 v233, 10, v227\n"
"v_lshl_add_u32 v233, v228, 6, v233\n"
"v_lshl_add_u32 v233, v232, 4, v233\n"
"v_add_u32 v240, s40, v233\n"
"buffer_load_dwordx4 v223, s[16:19], s47 offen offset:1280 lds\n"
"v_add_u32 v241, 0x10000, v240\n"
"v_lshlrev_b32 v233, 10, v226\n"
"v_lshl_add_u32 v233, v227, 6, v233\n"
"v_lshl_add_u32 v233, v228, 3, v233\n"
"v_add_u32 v233, s40, v233\n"
"v_and_b32 v232, 1, v226\n"
"v_lshl_add_u32 v242, v232, 5, v233\n"
"buffer_load_dwordx4 v222, s[16:19], s48 offen offset:2304 lds\n"
"v_xor_b32 v243, 32, v242\n"
"v_add_u32 v244, 0x10000, v242\n"
"v_add_u32 v245, 0x10000, v243\n"
"v_lshlrev_b32 v233, 8, v227\n"
"v_lshl_add_u32 v233, v226, 6, v233\n"
"v_lshl_add_u32 v246, v228, 4, v233\n"
"v_xor_b32 v247, 32, v246\n"
"buffer_load_dwordx4 v223, s[16:19], s49 offen offset:3328 lds\n"
"v_mul_u32_u24 v233, 16392, v232\n"
"v_lshrrev_b32 v232, 1, v226\n"
"v_lshl_add_u32 v233, v232, 11, v233\n"
"v_lshl_add_u32 v233, v227, 6, v233\n"
"v_lshl_add_u32 v233, v228, 4, v233\n"
"v_add_u32 v218, s40, v233\n"
"s_add_u32 m0, s63, 11904\n"
"v_add_u32 v219, 65568, v218\n"
"buffer_load_dwordx4 v222, s[16:19], s46 offen offset:384 lds\n"
"v_lshrrev_b32 v232, 1, v229\n"
"v_xor_b32 v232, v232, v226\n"
"v_lshlrev_b32 v233, 7, v229\n"
"v_lshl_add_u32 v233, v232, 4, v233\n"
"v_add_u32 v220, s63, v233\n"
"v_xor_b32 v221, 64, v220\n"
"s_add_u32 s56, s40, 148480\n"
"v_lshl_add_u32 v224, v226, 4, s56\n"
"v_lshrrev_b32 v232, 3, v234\n"
"v_lshlrev_b32 v233, 8, v232\n"
"v_and_b32 v232, 1, v234\n"
"v_lshl_add_u32 v233, v232, 6, v233\n"
"v_bfe_u32 v232, v234, 1, 2\n"
"v_lshl_add_u32 v233, v232, 4, v233\n"
"buffer_load_dwordx4 v223, s[16:19], s47 offen offset:1408 lds\n"
"v_add_u32 v216, s56, v233\n"
"v_lshlrev_b32 v225, 5, v234\n"
"v_mov_b32 v232, 0x40000000\n"
"v_cmp_gt_u32 vcc, 144, v234\n"
"s_nop 1\n"
"v_cndmask_b32 v225, v232, v225, vcc\n"
"s_lshl_b32 s56, s41, 8\n"
"s_add_u32 s57, s40, 147456\n"
"s_add_u32 s56, s56, s57\n"
"v_and_b32 v232, 1, v248\n"
"v_lshlrev_b32 v232, 5, v232\n"
"v_lshl_add_u32 v232, v226, 6, v232\n"
"v_add_u32 v238, s56, v232\n"
"v_lshlrev_b32 v232, 8, v226\n"
"buffer_load_dwordx4 v222, s[16:19], s48 offen offset:2432 lds\n"
"v_lshl_add_u32 v232, v229, 2, v232\n"
"v_add_u32 v239, s57, v232\n"
"v_lshlrev_b32 v232, 6, v228\n"
"v_lshl_add_u32 v232, v226, 4, v232\n"
"v_add_u32 v254, s40, v232\n"
"s_lshr_b32 s57, s42, 3\n"
"s_lshl_b32 s57, s57, 2\n"
"s_add_u32 s57, s57, s41\n"
"s_sub_u32 s57, s57, 128\n"
"s_and_b32 s58, s42, 7\n"
"s_mul_i32 s59, s57, 9216\n"
"s_mul_i32 s60, s58, 1152\n"
"s_add_u32 s59, s59, s60\n"
"v_lshlrev_b32 v252, 6, v248\n"
"buffer_load_dwordx4 v223, s[16:19], s49 offen offset:3456 lds\n"
"v_add_u32 v253, 0x40000000, v252\n"
"v_cmp_gt_u32 vcc, 18, v248\n"
"s_nop 1\n"
"v_cndmask_b32 v251, v253, v252, vcc\n"
"v_cmp_gt_u32 vcc, 36, v248\n"
"s_nop 1\n"
"v_cndmask_b32 v253, v253, v252, vcc\n"
"s_mul_i32 s60, s57, 1152\n"
"s_lshl_b32 s57, s60, 1\n"
"v_add_u32 v252, s60, v251\n"
"v_add_u32 v251, s59, v251\n"
"v_add_u32 v253, s57, v253\n"
"v_mov_b32 v237, 0xffff0000\n"
"s_add_u32 m0, s63, 15872\n"
"v_mov_b32 v232, 0x3f803f80\n"
"buffer_load_dwordx4 v222, s[16:19], s46 offen offset:512 lds\n"
"v_accvgpr_write_b32 a0, v232\n"
"v_accvgpr_write_b32 a1, v232\n"
"v_accvgpr_write_b32 a2, v232\n"
"v_accvgpr_write_b32 a3, v232\n"
"s_waitcnt vmcnt(22)\n"
"v_lshlrev_b32 v164, 16, v164\n"
"v_mul_f32 v184, 0xbfb8aa3b, v164\n"
"v_lshlrev_b32 v165, 16, v165\n"
"v_mul_f32 v185, 0xbfb8aa3b, v165\n"
"v_lshlrev_b32 v166, 16, v166\n"
"v_mul_f32 v186, 0xbfb8aa3b, v166\n"
"v_lshlrev_b32 v167, 16, v167\n"
"v_mul_f32 v187, 0xbfb8aa3b, v167\n"
"v_lshlrev_b32 v168, 16, v168\n"
"v_mul_f32 v188, 0xbfb8aa3b, v168\n"
"v_exp_f32 v184, v184\n"
"buffer_load_dwordx4 v223, s[16:19], s47 offen offset:1536 lds\n"
"v_exp_f32 v185, v185\n"
"v_exp_f32 v186, v186\n"
"v_exp_f32 v187, v187\n"
"v_exp_f32 v188, v188\n"
"v_add_f32 v184, 1.0, v184\n"
"v_add_f32 v185, 1.0, v185\n"
"v_add_f32 v186, 1.0, v186\n"
"v_add_f32 v187, 1.0, v187\n"
"v_add_f32 v188, 1.0, v188\n"
"v_rcp_f32 v184, v184\n"
"v_rcp_f32 v185, v185\n"
"v_rcp_f32 v186, v186\n"
"buffer_load_dwordx4 v222, s[16:19], s48 offen offset:2560 lds\n"
"v_rcp_f32 v187, v187\n"
"v_rcp_f32 v188, v188\n"
"v_mul_f32 v164, v164, v184\n"
"v_mul_f32 v165, v165, v185\n"
"v_mul_f32 v166, v166, v186\n"
"v_mul_f32 v167, v167, v187\n"
"v_mul_f32 v168, v168, v188\n"
"v_cvt_pk_bf16_f32 v164, v164, v164\n"
"v_and_b32 v164, v237, v164\n"
"v_cvt_pk_bf16_f32 v165, v165, v165\n"
"v_and_b32 v165, v237, v165\n"
"v_cvt_pk_bf16_f32 v166, v166, v166\n"
"buffer_load_dwordx4 v223, s[16:19], s49 offen offset:3584 lds\n"
"v_and_b32 v166, v237, v166\n"
"v_cvt_pk_bf16_f32 v167, v167, v167\n"
"v_and_b32 v167, v237, v167\n"
"v_cvt_pk_bf16_f32 v168, v168, v168\n"
"v_and_b32 v168, v237, v168\n"
"s_waitcnt vmcnt(24)\n"
"v_lshlrev_b32 v184, 16, v144\n"
"v_and_b32 v185, v237, v144\n"
"v_lshlrev_b32 v186, 16, v145\n"
"v_and_b32 v187, v237, v145\n"
"v_lshlrev_b32 v188, 16, v146\n"
"s_add_u32 m0, s63, 19840\n"
"v_and_b32 v189, v237, v146\n"
"buffer_load_dwordx4 v222, s[16:19], s46 offen offset:640 lds\n"
"v_lshlrev_b32 v190, 16, v147\n"
"v_and_b32 v191, v237, v147\n"
"v_mul_f32 v176, v164, v184\n"
"v_mul_f32 v177, v164, v185\n"
"v_mul_f32 v178, v164, v186\n"
"v_mul_f32 v179, v164, v187\n"
"v_mul_f32 v180, v164, v188\n"
"v_mul_f32 v181, v164, v189\n"
"v_mul_f32 v182, v164, v190\n"
"v_mul_f32 v183, v164, v191\n"
"s_waitcnt vmcnt(24)\n"
"v_lshlrev_b32 v184, 16, v148\n"
"buffer_load_dwordx4 v223, s[16:19], s47 offen offset:1664 lds\n"
"v_and_b32 v185, v237, v148\n"
"v_lshlrev_b32 v186, 16, v149\n"
"v_and_b32 v187, v237, v149\n"
"v_lshlrev_b32 v188, 16, v150\n"
"v_and_b32 v189, v237, v150\n"
"v_lshlrev_b32 v190, 16, v151\n"
"v_and_b32 v191, v237, v151\n"
"v_fmac_f32 v176, v165, v184\n"
"v_fmac_f32 v177, v165, v185\n"
"v_fmac_f32 v178, v165, v186\n"
"v_fmac_f32 v179, v165, v187\n"
"v_fmac_f32 v180, v165, v188\n"
"buffer_load_dwordx4 v222, s[16:19], s48 offen offset:2688 lds\n"
"v_fmac_f32 v181, v165, v189\n"
"v_fmac_f32 v182, v165, v190\n"
"v_fmac_f32 v183, v165, v191\n"
"s_waitcnt vmcnt(25)\n"
"v_lshlrev_b32 v184, 16, v152\n"
"v_and_b32 v185, v237, v152\n"
"v_lshlrev_b32 v186, 16, v153\n"
"v_and_b32 v187, v237, v153\n"
"v_lshlrev_b32 v188, 16, v154\n"
"v_and_b32 v189, v237, v154\n"
"v_lshlrev_b32 v190, 16, v155\n"
"v_and_b32 v191, v237, v155\n"
"buffer_load_dwordx4 v223, s[16:19], s49 offen offset:3712 lds\n"
"v_fmac_f32 v176, v166, v184\n"
"v_fmac_f32 v177, v166, v185\n"
"v_fmac_f32 v178, v166, v186\n"
"v_fmac_f32 v179, v166, v187\n"
"v_fmac_f32 v180, v166, v188\n"
"v_fmac_f32 v181, v166, v189\n"
"v_fmac_f32 v182, v166, v190\n"
"v_fmac_f32 v183, v166, v191\n"
"s_waitcnt vmcnt(25)\n"
"v_lshlrev_b32 v184, 16, v156\n"
"v_and_b32 v185, v237, v156\n"
"s_add_u32 m0, s63, 23808\n"
"v_lshlrev_b32 v186, 16, v157\n"
"buffer_load_dwordx4 v222, s[16:19], s46 offen offset:768 lds\n"
"v_and_b32 v187, v237, v157\n"
"v_lshlrev_b32 v188, 16, v158\n"
"v_and_b32 v189, v237, v158\n"
"v_lshlrev_b32 v190, 16, v159\n"
"v_and_b32 v191, v237, v159\n"
"v_fmac_f32 v176, v167, v184\n"
"v_fmac_f32 v177, v167, v185\n"
"v_fmac_f32 v178, v167, v186\n"
"v_fmac_f32 v179, v167, v187\n"
"v_fmac_f32 v180, v167, v188\n"
"v_fmac_f32 v181, v167, v189\n"
"v_fmac_f32 v182, v167, v190\n"
"buffer_load_dwordx4 v223, s[16:19], s47 offen offset:1792 lds\n"
"v_fmac_f32 v183, v167, v191\n"
"s_waitcnt vmcnt(26)\n"
"v_lshlrev_b32 v184, 16, v160\n"
"v_and_b32 v185, v237, v160\n"
"v_lshlrev_b32 v186, 16, v161\n"
"v_and_b32 v187, v237, v161\n"
"v_lshlrev_b32 v188, 16, v162\n"
"v_and_b32 v189, v237, v162\n"
"v_lshlrev_b32 v190, 16, v163\n"
"v_and_b32 v191, v237, v163\n"
"v_fmac_f32 v176, v168, v184\n"
"v_fmac_f32 v177, v168, v185\n"
"buffer_load_dwordx4 v222, s[16:19], s48 offen offset:2816 lds\n"
"v_fmac_f32 v178, v168, v186\n"
"v_fmac_f32 v179, v168, v187\n"
"v_fmac_f32 v180, v168, v188\n"
"v_fmac_f32 v181, v168, v189\n"
"v_fmac_f32 v182, v168, v190\n"
"v_fmac_f32 v183, v168, v191\n"
"v_add_f32_dpp v176, v176, v176 row_ror:2 row_mask:0xf bank_mask:0xf\n"
"v_add_f32_dpp v177, v177, v177 row_ror:2 row_mask:0xf bank_mask:0xf\n"
"v_add_f32_dpp v178, v178, v178 row_ror:2 row_mask:0xf bank_mask:0xf\n"
"v_add_f32_dpp v179, v179, v179 row_ror:2 row_mask:0xf bank_mask:0xf\n"
"v_add_f32_dpp v180, v180, v180 row_ror:2 row_mask:0xf bank_mask:0xf\n"
"v_add_f32_dpp v181, v181, v181 row_ror:2 row_mask:0xf bank_mask:0xf\n"
"buffer_load_dwordx4 v223, s[16:19], s49 offen offset:3840 lds\n"
"v_add_f32_dpp v182, v182, v182 row_ror:2 row_mask:0xf bank_mask:0xf\n"
"v_add_f32_dpp v183, v183, v183 row_ror:2 row_mask:0xf bank_mask:0xf\n"
"v_add_f32_dpp v176, v176, v176 row_ror:4 row_mask:0xf bank_mask:0xf\n"
"v_add_f32_dpp v177, v177, v177 row_ror:4 row_mask:0xf bank_mask:0xf\n"
"v_add_f32_dpp v178, v178, v178 row_ror:4 row_mask:0xf bank_mask:0xf\n"
"v_add_f32_dpp v179, v179, v179 row_ror:4 row_mask:0xf bank_mask:0xf\n"
"v_add_f32_dpp v180, v180, v180 row_ror:4 row_mask:0xf bank_mask:0xf\n"
"v_add_f32_dpp v181, v181, v181 row_ror:4 row_mask:0xf bank_mask:0xf\n"
"v_add_f32_dpp v182, v182, v182 row_ror:4 row_mask:0xf bank_mask:0xf\n"
"v_add_f32_dpp v183, v183, v183 row_ror:4 row_mask:0xf bank_mask:0xf\n"
"v_add_f32_dpp v176, v176, v176 row_ror:8 row_mask:0xf bank_mask:0xf\n"
"s_add_u32 m0, s63, 27776\n"
"v_add_f32_dpp v177, v177, v177 row_ror:8 row_mask:0xf bank_mask:0xf\n"
"buffer_load_dwordx4 v222, s[16:19], s46 offen offset:896 lds\n"
"v_add_f32_dpp v178, v178, v178 row_ror:8 row_mask:0xf bank_mask:0xf\n"
"v_add_f32_dpp v179, v179, v179 row_ror:8 row_mask:0xf bank_mask:0xf\n"
"v_add_f32_dpp v180, v180, v180 row_ror:8 row_mask:0xf bank_mask:0xf\n"
"v_add_f32_dpp v181, v181, v181 row_ror:8 row_mask:0xf bank_mask:0xf\n"
"v_add_f32_dpp v182, v182, v182 row_ror:8 row_mask:0xf bank_mask:0xf\n"
"v_add_f32_dpp v183, v183, v183 row_ror:8 row_mask:0xf bank_mask:0xf\n"
"s_mov_b32 exec_lo, 0x00030003\n"
"s_mov_b32 exec_hi, 0x00030003\n"
"ds_write_b128 v238, v[176:179]\n"
"ds_write_b128 v238, v[180:183] offset:16\n"
"s_mov_b64 exec, -1\n"
"s_waitcnt lgkmcnt(0)\n"
"s_barrier\n"
"ds_read_b32 v184, v239 offset:0\n"
"ds_read_b32 v185, v239 offset:64\n"
"ds_read_b32 v186, v239 offset:128\n"
"ds_read_b32 v187, v239 offset:192\n"
"s_add_u32 s85, s85, 1\n"
"s_lshl_b32 s56, s55, 7\n"
"s_add_u32 s56, s56, 32768\n"
"s_lshl_b32 s58, s41, 2\n"
"s_lshl_b32 s58, 15, s58\n"
"v_lshlrev_b32 v190, 3, v248\n"
"v_mov_b32 v189, s85\n"
"s_waitcnt lgkmcnt(2)\n"
"v_add_f32 v184, v184, v185\n"
"s_waitcnt lgkmcnt(0)\n"
"v_add_f32 v186, v186, v187\n"
"v_add_f32 v184, v184, v186\n"
"v_mov_b32 v185, v184\n"
"s_nop 1\n"
"v_permlane32_swap_b32 v184, v185\n"
"v_add_f32 v184, v184, v185\n"
"v_mov_b32 v185, v184\n"
"s_nop 1\n"
"v_permlane16_swap_b32 v184, v185\n"
"v_add_f32 v184, v184, v185\n"
"v_cvt_pk_bf16_f32 v188, v184, v184\n"
"v_and_b32 v188, v237, v188\n"
"s_mov_b32 exec_lo, s58\n"
"s_mov_b32 exec_hi, 0\n"
"buffer_store_dwordx2 v[188:189], v190, s[28:31], s56 offen sc1\n"
"s_mov_b64 exec, -1\n"
"s_waitcnt vmcnt(26)\n"
"ds_read_b128 v[0:3], v220 offset:0\n"
"ds_read_b128 v[4:7], v220 offset:2048\n"
"ds_read_b128 v[8:11], v221 offset:0\n"
"ds_read_b128 v[12:15], v221 offset:2048\n"
"s_waitcnt vmcnt(22)\n"
"ds_read_b128 v[16:19], v220 offset:4096\n"
"ds_read_b128 v[20:23], v220 offset:6144\n"
"ds_read_b128 v[24:27], v221 offset:4096\n"
"ds_read_b128 v[28:31], v221 offset:6144\n"
"s_waitcnt lgkmcnt(7)\n"
"v_mfma_f32_16x16x32_bf16 v[192:195], a[0:3], v[0:3], 0\n"
"v_mfma_f32_16x16x32_bf16 v[200:203], v[0:3], v[0:3], 0\n"
"s_waitcnt lgkmcnt(6)\n"
"v_mfma_f32_16x16x32_bf16 v[196:199], a[0:3], v[4:7], 0\n"
"v_mfma_f32_16x16x32_bf16 v[204:207], v[4:7], v[4:7], 0\n"
"s_waitcnt lgkmcnt(5)\n"
"v_mfma_f32_16x16x32_bf16 v[192:195], a[0:3], v[8:11], v[192:195]\n"
"v_mfma_f32_16x16x32_bf16 v[200:203], v[8:11], v[8:11], v[200:203]\n"
"s_waitcnt lgkmcnt(4)\n"
"v_mfma_f32_16x16x32_bf16 v[196:199], a[0:3], v[12:15], v[196:199]\n"
"v_mfma_f32_16x16x32_bf16 v[204:207], v[12:15], v[12:15], v[204:207]\n"
"s_waitcnt lgkmcnt(3)\n"
"s_waitcnt vmcnt(18)\n"
"ds_read_b128 v[32:35], v220 offset:8192\n"
"ds_read_b128 v[36:39], v220 offset:10240\n"
"ds_read_b128 v[40:43], v221 offset:8192\n"
"ds_read_b128 v[44:47], v221 offset:10240\n"
"v_mfma_f32_16x16x32_bf16 v[192:195], a[0:3], v[16:19], v[192:195]\n"
"v_lshlrev_b32 v144, 16, v0\n"
"v_and_b32 v0, v237, v0\n"
"v_mfma_f32_16x16x32_bf16 v[200:203], v[16:19], v[16:19], v[200:203]\n"
"s_waitcnt lgkmcnt(6)\n"
"v_lshlrev_b32 v145, 16, v1\n"
"v_and_b32 v1, v237, v1\n"
"v_mfma_f32_16x16x32_bf16 v[196:199], a[0:3], v[20:23], v[196:199]\n"
"v_lshlrev_b32 v146, 16, v2\n"
"v_and_b32 v2, v237, v2\n"
"v_mfma_f32_16x16x32_bf16 v[204:207], v[20:23], v[20:23], v[204:207]\n"
"s_waitcnt lgkmcnt(4)\n"
"v_lshlrev_b32 v147, 16, v3\n"
"v_and_b32 v3, v237, v3\n"
"v_mfma_f32_16x16x32_bf16 v[192:195], a[0:3], v[24:27], v[192:195]\n"
"v_lshlrev_b32 v148, 16, v4\n"
"v_and_b32 v4, v237, v4\n"
"v_mfma_f32_16x16x32_bf16 v[200:203], v[24:27], v[24:27], v[200:203]\n"
"v_lshlrev_b32 v149, 16, v5\n"
"v_and_b32 v5, v237, v5\n"
"v_mfma_f32_16x16x32_bf16 v[196:199], a[0:3], v[28:31], v[196:199]\n"
"v_lshlrev_b32 v150, 16, v6\n"
"v_and_b32 v6, v237, v6\n"
"v_mfma_f32_16x16x32_bf16 v[204:207], v[28:31], v[28:31], v[204:207]\n"
"s_waitcnt lgkmcnt(3)\n"
"v_lshlrev_b32 v151, 16, v7\n"
"v_and_b32 v7, v237, v7\n"
"s_waitcnt vmcnt(14)\n"
"ds_read_b128 v[48:51], v220 offset:12288\n"
"ds_read_b128 v[52:55], v220 offset:14336\n"
"ds_read_b128 v[56:59], v221 offset:12288\n"
"ds_read_b128 v[60:63], v221 offset:14336\n"
"v_mfma_f32_16x16x32_bf16 v[192:195], a[0:3], v[32:35], v[192:195]\n"
"v_lshlrev_b32 v152, 16, v8\n"
"v_and_b32 v8, v237, v8\n"
"v_mfma_f32_16x16x32_bf16 v[200:203], v[32:35], v[32:35], v[200:203]\n"
"s_waitcnt lgkmcnt(6)\n"
"v_lshlrev_b32 v153, 16, v9\n"
"v_and_b32 v9, v237, v9\n"
"v_mfma_f32_16x16x32_bf16 v[196:199], a[0:3], v[36:39], v[196:199]\n"
"v_lshlrev_b32 v154, 16, v10\n"
"v_and_b32 v10, v237, v10\n"
"v_mfma_f32_16x16x32_bf16 v[204:207], v[36:39], v[36:39], v[204:207]\n"
"s_waitcnt lgkmcnt(4)\n"
"v_lshlrev_b32 v155, 16, v11\n"
"v_and_b32 v11, v237, v11\n"
"v_mfma_f32_16x16x32_bf16 v[192:195], a[0:3], v[40:43], v[192:195]\n"
"v_lshlrev_b32 v156, 16, v12\n"
"v_and_b32 v12, v237, v12\n"
"v_mfma_f32_16x16x32_bf16 v[200:203], v[40:43], v[40:43], v[200:203]\n"
"v_lshlrev_b32 v157, 16, v13\n"
"v_and_b32 v13, v237, v13\n"
"v_mfma_f32_16x16x32_bf16 v[196:199], a[0:3], v[44:47], v[196:199]\n"
"v_lshlrev_b32 v158, 16, v14\n"
"v_and_b32 v14, v237, v14\n"
"v_mfma_f32_16x16x32_bf16 v[204:207], v[44:47], v[44:47], v[204:207]\n"
"s_waitcnt lgkmcnt(3)\n"
"v_lshlrev_b32 v159, 16, v15\n"
"v_and_b32 v15, v237, v15\n"
"buffer_load_dwordx4 v223, s[16:19], s47 offen offset:1920 lds\n"
"buffer_load_dwordx4 v222, s[16:19], s48 offen offset:2944 lds\n"
"buffer_load_dwordx4 v223, s[16:19], s49 offen offset:3968 lds\n"
"s_waitcnt vmcnt(13)\n"
"ds_read_b128 v[64:67], v220 offset:16384\n"
"ds_read_b128 v[68:71], v220 offset:18432\n"
"ds_read_b128 v[72:75], v221 offset:16384\n"
"ds_read_b128 v[76:79], v221 offset:18432\n"
"v_mfma_f32_16x16x32_bf16 v[192:195], a[0:3], v[48:51], v[192:195]\n"
"v_lshlrev_b32 v160, 16, v16\n"
"v_and_b32 v16, v237, v16\n"
"v_mfma_f32_16x16x32_bf16 v[200:203], v[48:51], v[48:51], v[200:203]\n"
"s_waitcnt lgkmcnt(6)\n"
"v_lshlrev_b32 v161, 16, v17\n"
"v_and_b32 v17, v237, v17\n"
"v_mfma_f32_16x16x32_bf16 v[196:199], a[0:3], v[52:55], v[196:199]\n"
"v_lshlrev_b32 v162, 16, v18\n"
"v_and_b32 v18, v237, v18\n"
"v_mfma_f32_16x16x32_bf16 v[204:207], v[52:55], v[52:55], v[204:207]\n"
"s_waitcnt lgkmcnt(4)\n"
"v_lshlrev_b32 v163, 16, v19\n"
"v_and_b32 v19, v237, v19\n"
"v_mfma_f32_16x16x32_bf16 v[192:195], a[0:3], v[56:59], v[192:195]\n"
"v_lshlrev_b32 v164, 16, v20\n"
"v_and_b32 v20, v237, v20\n"
"v_mfma_f32_16x16x32_bf16 v[200:203], v[56:59], v[56:59], v[200:203]\n"
"v_lshlrev_b32 v165, 16, v21\n"
"v_and_b32 v21, v237, v21\n"
"v_mfma_f32_16x16x32_bf16 v[196:199], a[0:3], v[60:63], v[196:199]\n"
"v_lshlrev_b32 v166, 16, v22\n"
"v_and_b32 v22, v237, v22\n"
"v_mfma_f32_16x16x32_bf16 v[204:207], v[60:63], v[60:63], v[204:207]\n"
"s_waitcnt lgkmcnt(3)\n"
"v_lshlrev_b32 v167, 16, v23\n"
"v_and_b32 v23, v237, v23\n"
"s_add_u32 m0, s63, 31744\n"
"s_nop 0\n"
"buffer_load_dwordx4 v222, s[16:19], s46 offen offset:1024 lds\n"
"buffer_load_dwordx4 v223, s[16:19], s47 offen offset:2048 lds\n"
"buffer_load_dwordx4 v222, s[16:19], s48 offen offset:3072 lds\n"
"s_add_u32 s49, s49, 128\n"
"s_add_u32 m0, s63, 31872\n"
"s_nop 0\n"
"buffer_load_dwordx4 v223, s[16:19], s49 offen offset:3968 lds\n"
"s_waitcnt vmcnt(13)\n"
"ds_read_b128 v[80:83], v220 offset:20480\n"
"ds_read_b128 v[84:87], v220 offset:22528\n"
"ds_read_b128 v[88:91], v221 offset:20480\n"
"ds_read_b128 v[92:95], v221 offset:22528\n"
"v_mfma_f32_16x16x32_bf16 v[192:195], a[0:3], v[64:67], v[192:195]\n"
"v_lshlrev_b32 v168, 16, v24\n"
"v_and_b32 v24, v237, v24\n"
"v_mfma_f32_16x16x32_bf16 v[200:203], v[64:67], v[64:67], v[200:203]\n"
"s_waitcnt lgkmcnt(6)\n"
"v_lshlrev_b32 v169, 16, v25\n"
"v_and_b32 v25, v237, v25\n"
"v_mfma_f32_16x16x32_bf16 v[196:199], a[0:3], v[68:71], v[196:199]\n"
"v_lshlrev_b32 v170, 16, v26\n"
"v_and_b32 v26, v237, v26\n"
"v_mfma_f32_16x16x32_bf16 v[204:207], v[68:71], v[68:71], v[204:207]\n"
"s_waitcnt lgkmcnt(4)\n"
"v_lshlrev_b32 v171, 16, v27\n"
"v_and_b32 v27, v237, v27\n"
"v_mfma_f32_16x16x32_bf16 v[192:195], a[0:3], v[72:75], v[192:195]\n"
"v_lshlrev_b32 v172, 16, v28\n"
"v_and_b32 v28, v237, v28\n"
"v_mfma_f32_16x16x32_bf16 v[200:203], v[72:75], v[72:75], v[200:203]\n"
"v_lshlrev_b32 v173, 16, v29\n"
"v_and_b32 v29, v237, v29\n"
"v_mfma_f32_16x16x32_bf16 v[196:199], a[0:3], v[76:79], v[196:199]\n"
"v_lshlrev_b32 v174, 16, v30\n"
"v_and_b32 v30, v237, v30\n"
"v_mfma_f32_16x16x32_bf16 v[204:207], v[76:79], v[76:79], v[204:207]\n"
"s_waitcnt lgkmcnt(3)\n"
"v_lshlrev_b32 v175, 16, v31\n"
"v_and_b32 v31, v237, v31\n"
"s_waitcnt vmcnt(9)\n"
"ds_read_b128 v[96:99], v220 offset:24576\n"
"ds_read_b128 v[100:103], v220 offset:26624\n"
"ds_read_b128 v[104:107], v221 offset:24576\n"
"ds_read_b128 v[108:111], v221 offset:26624\n"
"v_mfma_f32_16x16x32_bf16 v[192:195], a[0:3], v[80:83], v[192:195]\n"
"v_lshlrev_b32 v176, 16, v32\n"
"v_and_b32 v32, v237, v32\n"
"v_mfma_f32_16x16x32_bf16 v[200:203], v[80:83], v[80:83], v[200:203]\n"
"s_waitcnt lgkmcnt(6)\n"
"v_lshlrev_b32 v177, 16, v33\n"
"v_and_b32 v33, v237, v33\n"
"v_mfma_f32_16x16x32_bf16 v[196:199], a[0:3], v[84:87], v[196:199]\n"
"v_lshlrev_b32 v178, 16, v34\n"
"v_and_b32 v34, v237, v34\n"
"v_mfma_f32_16x16x32_bf16 v[204:207], v[84:87], v[84:87], v[204:207]\n"
"s_waitcnt lgkmcnt(4)\n"
"v_lshlrev_b32 v179, 16, v35\n"
"v_and_b32 v35, v237, v35\n"
"v_mfma_f32_16x16x32_bf16 v[192:195], a[0:3], v[88:91], v[192:195]\n"
"v_lshlrev_b32 v180, 16, v36\n"
"v_and_b32 v36, v237, v36\n"
"v_mfma_f32_16x16x32_bf16 v[200:203], v[88:91], v[88:91], v[200:203]\n"
"v_lshlrev_b32 v181, 16, v37\n"
"v_and_b32 v37, v237, v37\n"
"v_mfma_f32_16x16x32_bf16 v[196:199], a[0:3], v[92:95], v[196:199]\n"
"v_lshlrev_b32 v182, 16, v38\n"
"v_and_b32 v38, v237, v38\n"
"v_mfma_f32_16x16x32_bf16 v[204:207], v[92:95], v[92:95], v[204:207]\n"
"v_lshlrev_b32 v183, 16, v39\n"
"v_and_b32 v39, v237, v39\n"
"s_mov_b64 exec, 0\n"
"s_cmp_eq_u32 s41, 0\n"
"s_cbranch_scc1 AT_hw0_0\n"
"s_cmp_eq_u32 s41, 1\n"
"s_cbranch_scc1 AT_hw0_1\n"
"s_cmp_eq_u32 s41, 2\n"
"s_cbranch_scc1 AT_hw0_2\n"
"s_call_b64 %[kp], AT_pg5\n"
"s_call_b64 %[kp], AT_pg9\n"
"s_call_b64 %[kp], AT_pg13\n"
"s_branch AT_hx0\n"
"AT_pg20:\n"
"AT_hr:\n"
"s_setpc_b64 %[kp]\n"
"AT_hw0_0:\n"
"s_call_b64 %[kp], AT_pg2\n"
"s_call_b64 %[kp], AT_pg6\n"
"s_call_b64 %[kp], AT_pg10\n"
"s_call_b64 %[kp], AT_pg14\n"
"s_branch AT_hx0\n"
"AT_hw0_1:\n"
"s_call_b64 %[kp], AT_pg3\n"
"s_call_b64 %[kp], AT_pg7\n"
"s_call_b64 %[kp], AT_pg11\n"
"s_branch AT_hx0\n"
"AT_hw0_2:\n"
"s_call_b64 %[kp], AT_pg4\n"
"s_call_b64 %[kp], AT_pg8\n"
"s_call_b64 %[kp], AT_pg12\n"
"AT_hx0:\n"
"s_mov_b64 exec, -1\n"
"s_waitcnt vmcnt(4)\n"
"ds_read_b128 v[112:115], v220 offset:28672\n"
"ds_read_b128 v[116:119], v220 offset:30720\n"
"ds_read_b128 v[120:123], v221 offset:28672\n"
"ds_read_b128 v[124:127], v221 offset:30720\n"
"s_waitcnt lgkmcnt(7)\n"
"v_mfma_f32_16x16x32_bf16 v[192:195], a[0:3], v[96:99], v[192:195]\n"
"v_lshlrev_b32 v184, 16, v40\n"
"v_and_b32 v40, v237, v40\n"
"v_mfma_f32_16x16x32_bf16 v[200:203], v[96:99], v[96:99], v[200:203]\n"
"s_waitcnt lgkmcnt(6)\n"
"v_lshlrev_b32 v185, 16, v41\n"
"v_and_b32 v41, v237, v41\n"
"v_mfma_f32_16x16x32_bf16 v[196:199], a[0:3], v[100:103], v[196:199]\n"
"v_lshlrev_b32 v186, 16, v42\n"
"v_and_b32 v42, v237, v42\n"
"v_mfma_f32_16x16x32_bf16 v[204:207], v[100:103], v[100:103], v[204:207]\n"
"s_waitcnt lgkmcnt(4)\n"
"v_lshlrev_b32 v187, 16, v43\n"
"v_and_b32 v43, v237, v43\n"
"v_mfma_f32_16x16x32_bf16 v[192:195], a[0:3], v[104:107], v[192:195]\n"
"v_lshlrev_b32 v188, 16, v44\n"
"v_and_b32 v44, v237, v44\n"
"v_mfma_f32_16x16x32_bf16 v[200:203], v[104:107], v[104:107], v[200:203]\n"
"v_lshlrev_b32 v189, 16, v45\n"
"v_and_b32 v45, v237, v45\n"
"v_mfma_f32_16x16x32_bf16 v[196:199], a[0:3], v[108:111], v[196:199]\n"
"v_lshlrev_b32 v190, 16, v46\n"
"v_and_b32 v46, v237, v46\n"
"v_mfma_f32_16x16x32_bf16 v[204:207], v[108:111], v[108:111], v[204:207]\n"
"s_waitcnt lgkmcnt(3)\n"
"v_lshlrev_b32 v191, 16, v47\n"
"v_and_b32 v47, v237, v47\n"
"s_waitcnt vmcnt(0)\n"
"ds_read_b128 v[128:131], v220 offset:32768\n"
"ds_read_b128 v[132:135], v220 offset:34816\n"
"ds_read_b128 v[136:139], v221 offset:32768\n"
"ds_read_b128 v[140:143], v221 offset:34816\n"
"v_mfma_f32_16x16x32_bf16 v[192:195], a[0:3], v[112:115], v[192:195]\n"
"v_mfma_f32_16x16x32_bf16 v[200:203], v[112:115], v[112:115], v[200:203]\n"
"s_waitcnt lgkmcnt(6)\n"
"v_mfma_f32_16x16x32_bf16 v[196:199], a[0:3], v[116:119], v[196:199]\n"
"v_mfma_f32_16x16x32_bf16 v[204:207], v[116:119], v[116:119], v[204:207]\n"
"s_waitcnt lgkmcnt(4)\n"
"v_mfma_f32_16x16x32_bf16 v[192:195], a[0:3], v[120:123], v[192:195]\n"
"v_mfma_f32_16x16x32_bf16 v[200:203], v[120:123], v[120:123], v[200:203]\n"
"v_mfma_f32_16x16x32_bf16 v[196:199], a[0:3], v[124:127], v[196:199]\n"
"v_mfma_f32_16x16x32_bf16 v[204:207], v[124:127], v[124:127], v[204:207]\n"
"s_waitcnt lgkmcnt(0)\n"
"s_barrier\n"
"v_mfma_f32_16x16x32_bf16 v[192:195], a[0:3], v[128:131], v[192:195]\n"
"v_cmp_eq_u32 s[56:57], 1, v228\n"
"v_cmp_eq_u32 s[58:59], 2, v228\n"
"v_mfma_f32_16x16x32_bf16 v[200:203], v[128:131], v[128:131], v[200:203]\n"
"v_cmp_eq_u32 vcc, 3, v228\n"
"v_mfma_f32_16x16x32_bf16 v[196:199], a[0:3], v[132:135], v[196:199]\n"
"v_mfma_f32_16x16x32_bf16 v[204:207], v[132:135], v[132:135], v[204:207]\n"
"v_mfma_f32_16x16x32_bf16 v[192:195], a[0:3], v[136:139], v[192:195]\n"
"v_mfma_f32_16x16x32_bf16 v[200:203], v[136:139], v[136:139], v[200:203]\n"
"v_mfma_f32_16x16x32_bf16 v[196:199], a[0:3], v[140:143], v[196:199]\n"
"v_mfma_f32_16x16x32_bf16 v[204:207], v[140:143], v[140:143], v[204:207]\n"
"s_add_u32 m0, s51, 98352\n"
"s_nop 0\n"
"buffer_load_dwordx4 v217, s[20:23], s50 offen offset:0 lds\n"
"s_add_u32 m0, s51, 106288\n"
"s_nop 0\n"
"buffer_load_dwordx4 v217, s[20:23], s50 offen offset:256 lds\n"
"s_add_u32 s50, s50, 32768\n"
"s_add_u32 m0, s51, 99376\n"
"s_nop 0\n"
"buffer_load_dwordx4 v217, s[20:23], s50 offen offset:0 lds\n"
"s_add_u32 m0, s51, 107312\n"
"s_nop 0\n"
"buffer_load_dwordx4 v217, s[20:23], s50 offen offset:256 lds\n"
"s_add_u32 s50, s50, 32768\n"
"s_nop 1\n"
"v_cndmask_b32 v200, v200, v201, s[56:57]\n"
"v_cndmask_b32 v200, v200, v202, s[58:59]\n"
"v_cndmask_b32 v200, v200, v203, vcc\n"
"v_cndmask_b32 v204, v204, v205, s[56:57]\n"
"v_cndmask_b32 v204, v204, v206, s[58:59]\n"
"v_cndmask_b32 v204, v204, v207, vcc\n"
"v_cmp_eq_u32 vcc, v226, v227\n"
"v_mul_f32 v209, 0x3ae38e39, v192\n"
"v_mul_f32 v213, 0x3ae38e39, v196\n"
"AT_pg1:\n"
"s_cbranch_execz AT_hr\n"
"v_cndmask_b32 v210, 0, v200, vcc\n"
"v_cndmask_b32 v214, 0, v204, vcc\n"
"v_mov_b32 v192, v210\n"
"v_mov_b32 v193, v214\n"
"v_mul_f32 v211, v209, v209\n"
"v_permlane32_swap_b32 v210, v192\n"
"v_permlane32_swap_b32 v214, v193\n"
"v_add_f32 v210, v210, v192\n"
"v_add_f32 v214, v214, v193\n"
"v_mov_b32 v192, v210\n"
"v_mov_b32 v193, v214\n"
"v_mul_f32 v215, v213, v213\n"
"v_permlane16_swap_b32 v210, v192\n"
"v_permlane16_swap_b32 v214, v193\n"
"v_add_f32 v210, v210, v192\n"
"v_add_f32 v214, v214, v193\n"
"v_mov_b32 v192, 0x3ae38e39\n"
"v_fma_f32 v210, v210, v192, -v211\n"
"v_fma_f32 v214, v214, v192, -v215\n"
"v_max_f32 v210, 0, v210\n"
"v_max_f32 v214, 0, v214\n"
"v_add_f32 v210, 0x3727c5ac, v210\n"
"v_add_f32 v214, 0x3727c5ac, v214\n"
"v_rsq_f32 v208, v210\n"
"v_rsq_f32 v212, v214\n"
"v_mul_f32_e64 v210, -v209, v208\n"
"v_mul_f32_e64 v214, -v213, v212\n"
"v_pk_fma_f32 v[144:145], v[144:145], v[208:209], v[210:211] op_sel_hi:[1,0,0]\n"
"v_pk_fma_f32 v[146:147], v[146:147], v[208:209], v[210:211] op_sel_hi:[1,0,0]\n"
"v_pk_fma_f32 v[0:1], v[0:1], v[208:209], v[210:211] op_sel_hi:[1,0,0]\n"
"v_pk_fma_f32 v[2:3], v[2:3], v[208:209], v[210:211] op_sel_hi:[1,0,0]\n"
"v_cvt_pk_f16_f32 v0, v144, v0\n"
"v_cvt_pk_f16_f32 v1, v145, v1\n"
"v_cvt_pk_f16_f32 v2, v146, v2\n"
"v_cvt_pk_f16_f32 v3, v147, v3\n"
"v_pk_fma_f32 v[148:149], v[148:149], v[212:213], v[214:215] op_sel_hi:[1,0,0]\n"
"v_pk_fma_f32 v[150:151], v[150:151], v[212:213], v[214:215] op_sel_hi:[1,0,0]\n"
"v_pk_fma_f32 v[4:5], v[4:5], v[212:213], v[214:215] op_sel_hi:[1,0,0]\n"
"v_pk_fma_f32 v[6:7], v[6:7], v[212:213], v[214:215] op_sel_hi:[1,0,0]\n"
"v_cvt_pk_f16_f32 v4, v148, v4\n"
"v_cvt_pk_f16_f32 v5, v149, v5\n"
"v_cvt_pk_f16_f32 v6, v150, v6\n"
"v_cvt_pk_f16_f32 v7, v151, v7\n"
"v_pk_fma_f32 v[152:153], v[152:153], v[208:209], v[210:211] op_sel_hi:[1,0,0]\n"
"v_pk_fma_f32 v[154:155], v[154:155], v[208:209], v[210:211] op_sel_hi:[1,0,0]\n"
"v_pk_fma_f32 v[8:9], v[8:9], v[208:209], v[210:211] op_sel_hi:[1,0,0]\n"
"s_add_u32 m0, s51, 114744\n"
"v_pk_fma_f32 v[10:11], v[10:11], v[208:209], v[210:211] op_sel_hi:[1,0,0]\n"
"buffer_load_dwordx4 v217, s[20:23], s50 offen offset:0 lds\n"
"v_cvt_pk_f16_f32 v8, v152, v8\n"
"v_cvt_pk_f16_f32 v9, v153, v9\n"
"v_cvt_pk_f16_f32 v10, v154, v10\n"
"v_cvt_pk_f16_f32 v11, v155, v11\n"
"v_pk_fma_f32 v[156:157], v[156:157], v[212:213], v[214:215] op_sel_hi:[1,0,0]\n"
"v_pk_fma_f32 v[158:159], v[158:159], v[212:213], v[214:215] op_sel_hi:[1,0,0]\n"
"v_pk_fma_f32 v[12:13], v[12:13], v[212:213], v[214:215] op_sel_hi:[1,0,0]\n"
"v_pk_fma_f32 v[14:15], v[14:15], v[212:213], v[214:215] op_sel_hi:[1,0,0]\n"
"v_cvt_pk_f16_f32 v12, v156, v12\n"
"v_cvt_pk_f16_f32 v13, v157, v13\n"
"v_cvt_pk_f16_f32 v14, v158, v14\n"
"v_cvt_pk_f16_f32 v15, v159, v15\n"
"v_pk_fma_f32 v[160:161], v[160:161], v[208:209], v[210:211] op_sel_hi:[1,0,0]\n"
"v_pk_fma_f32 v[162:163], v[162:163], v[208:209], v[210:211] op_sel_hi:[1,0,0]\n"
"v_pk_fma_f32 v[16:17], v[16:17], v[208:209], v[210:211] op_sel_hi:[1,0,0]\n"
"v_pk_fma_f32 v[18:19], v[18:19], v[208:209], v[210:211] op_sel_hi:[1,0,0]\n"
"v_cvt_pk_f16_f32 v16, v160, v16\n"
"v_cvt_pk_f16_f32 v17, v161, v17\n"
"v_cvt_pk_f16_f32 v18, v162, v18\n"
"v_cvt_pk_f16_f32 v19, v163, v19\n"
"v_pk_fma_f32 v[164:165], v[164:165], v[212:213], v[214:215] op_sel_hi:[1,0,0]\n"
"v_pk_fma_f32 v[166:167], v[166:167], v[212:213], v[214:215] op_sel_hi:[1,0,0]\n"
"v_pk_fma_f32 v[20:21], v[20:21], v[212:213], v[214:215] op_sel_hi:[1,0,0]\n"
"v_pk_fma_f32 v[22:23], v[22:23], v[212:213], v[214:215] op_sel_hi:[1,0,0]\n"
"v_cvt_pk_f16_f32 v20, v164, v20\n"
"v_cvt_pk_f16_f32 v21, v165, v21\n"
"v_cvt_pk_f16_f32 v22, v166, v22\n"
"v_cvt_pk_f16_f32 v23, v167, v23\n"
"v_pk_fma_f32 v[168:169], v[168:169], v[208:209], v[210:211] op_sel_hi:[1,0,0]\n"
"v_pk_fma_f32 v[170:171], v[170:171], v[208:209], v[210:211] op_sel_hi:[1,0,0]\n"
"v_pk_fma_f32 v[24:25], v[24:25], v[208:209], v[210:211] op_sel_hi:[1,0,0]\n"
"s_add_u32 m0, s51, 122680\n"
"v_pk_fma_f32 v[26:27], v[26:27], v[208:209], v[210:211] op_sel_hi:[1,0,0]\n"
"buffer_load_dwordx4 v217, s[20:23], s50 offen offset:256 lds\n"
"s_add_u32 s50, s50, 32768\n"
"v_cvt_pk_f16_f32 v24, v168, v24\n"
"v_cvt_pk_f16_f32 v25, v169, v25\n"
"v_cvt_pk_f16_f32 v26, v170, v26\n"
"v_cvt_pk_f16_f32 v27, v171, v27\n"
"v_pk_fma_f32 v[172:173], v[172:173], v[212:213], v[214:215] op_sel_hi:[1,0,0]\n"
"v_pk_fma_f32 v[174:175], v[174:175], v[212:213], v[214:215] op_sel_hi:[1,0,0]\n"
"v_pk_fma_f32 v[28:29], v[28:29], v[212:213], v[214:215] op_sel_hi:[1,0,0]\n"
"v_pk_fma_f32 v[30:31], v[30:31], v[212:213], v[214:215] op_sel_hi:[1,0,0]\n"
"v_cvt_pk_f16_f32 v28, v172, v28\n"
"v_cvt_pk_f16_f32 v29, v173, v29\n"
"v_cvt_pk_f16_f32 v30, v174, v30\n"
"v_cvt_pk_f16_f32 v31, v175, v31\n"
"buffer_load_dwordx4 v[144:147], v225, s[28:31], s61 offen offset:0 sc1\n"
"buffer_load_dwordx4 v[148:151], v225, s[28:31], s61 offen offset:16 sc1\n"
"buffer_load_dwordx4 v[152:155], v225, s[28:31], s62 offen offset:0 sc1\n"
"buffer_load_dwordx4 v[156:159], v225, s[28:31], s62 offen offset:16 sc1\n"
"v_pk_fma_f32 v[176:177], v[176:177], v[208:209], v[210:211] op_sel_hi:[1,0,0]\n"
"v_pk_fma_f32 v[178:179], v[178:179], v[208:209], v[210:211] op_sel_hi:[1,0,0]\n"
"v_pk_fma_f32 v[32:33], v[32:33], v[208:209], v[210:211] op_sel_hi:[1,0,0]\n"
"v_pk_fma_f32 v[34:35], v[34:35], v[208:209], v[210:211] op_sel_hi:[1,0,0]\n"
"v_cvt_pk_f16_f32 v32, v176, v32\n"
"v_cvt_pk_f16_f32 v33, v177, v33\n"
"v_cvt_pk_f16_f32 v34, v178, v34\n"
"v_cvt_pk_f16_f32 v35, v179, v35\n"
"v_pk_fma_f32 v[180:181], v[180:181], v[212:213], v[214:215] op_sel_hi:[1,0,0]\n"
"v_pk_fma_f32 v[182:183], v[182:183], v[212:213], v[214:215] op_sel_hi:[1,0,0]\n"
"v_pk_fma_f32 v[36:37], v[36:37], v[212:213], v[214:215] op_sel_hi:[1,0,0]\n"
"v_pk_fma_f32 v[38:39], v[38:39], v[212:213], v[214:215] op_sel_hi:[1,0,0]\n"
"v_cvt_pk_f16_f32 v36, v180, v36\n"
"v_cvt_pk_f16_f32 v37, v181, v37\n"
"v_cvt_pk_f16_f32 v38, v182, v38\n"
"v_cvt_pk_f16_f32 v39, v183, v39\n"
"v_pk_fma_f32 v[184:185], v[184:185], v[208:209], v[210:211] op_sel_hi:[1,0,0]\n"
"v_pk_fma_f32 v[186:187], v[186:187], v[208:209], v[210:211] op_sel_hi:[1,0,0]\n"
"v_pk_fma_f32 v[40:41], v[40:41], v[208:209], v[210:211] op_sel_hi:[1,0,0]\n"
"s_add_u32 m0, s51, 115768\n"
"v_pk_fma_f32 v[42:43], v[42:43], v[208:209], v[210:211] op_sel_hi:[1,0,0]\n"
"buffer_load_dwordx4 v217, s[20:23], s50 offen offset:0 lds\n"
"v_cvt_pk_f16_f32 v40, v184, v40\n"
"v_cvt_pk_f16_f32 v41, v185, v41\n"
"v_cvt_pk_f16_f32 v42, v186, v42\n"
"v_cvt_pk_f16_f32 v43, v187, v43\n"
"v_pk_fma_f32 v[188:189], v[188:189], v[212:213], v[214:215] op_sel_hi:[1,0,0]\n"
"v_pk_fma_f32 v[190:191], v[190:191], v[212:213], v[214:215] op_sel_hi:[1,0,0]\n"
"v_pk_fma_f32 v[44:45], v[44:45], v[212:213], v[214:215] op_sel_hi:[1,0,0]\n"
"v_pk_fma_f32 v[46:47], v[46:47], v[212:213], v[214:215] op_sel_hi:[1,0,0]\n"
"v_cvt_pk_f16_f32 v44, v188, v44\n"
"v_cvt_pk_f16_f32 v45, v189, v45\n"
"v_cvt_pk_f16_f32 v46, v190, v46\n"
"v_cvt_pk_f16_f32 v47, v191, v47\n"
"v_lshlrev_b32 v176, 16, v48\n"
"v_and_b32 v177, v237, v48\n"
"v_lshlrev_b32 v178, 16, v49\n"
"v_and_b32 v179, v237, v49\n"
"v_lshlrev_b32 v180, 16, v50\n"
"v_and_b32 v181, v237, v50\n"
"v_lshlrev_b32 v182, 16, v51\n"
"v_and_b32 v183, v237, v51\n"
"v_pk_fma_f32 v[176:177], v[176:177], v[208:209], v[210:211] op_sel_hi:[1,0,0]\n"
"v_pk_fma_f32 v[178:179], v[178:179], v[208:209], v[210:211] op_sel_hi:[1,0,0]\n"
"v_pk_fma_f32 v[180:181], v[180:181], v[208:209], v[210:211] op_sel_hi:[1,0,0]\n"
"v_pk_fma_f32 v[182:183], v[182:183], v[208:209], v[210:211] op_sel_hi:[1,0,0]\n"
"v_cvt_pk_f16_f32 v48, v176, v177\n"
"v_cvt_pk_f16_f32 v49, v178, v179\n"
"v_cvt_pk_f16_f32 v50, v180, v181\n"
"v_cvt_pk_f16_f32 v51, v182, v183\n"
"v_lshlrev_b32 v184, 16, v52\n"
"v_and_b32 v185, v237, v52\n"
"v_lshlrev_b32 v186, 16, v53\n"
"s_add_u32 m0, s51, 123704\n"
"v_and_b32 v187, v237, v53\n"
"buffer_load_dwordx4 v217, s[20:23], s50 offen offset:256 lds\n"
"s_add_u32 s50, s50, 425984\n"
"v_lshlrev_b32 v188, 16, v54\n"
"v_and_b32 v189, v237, v54\n"
"v_lshlrev_b32 v190, 16, v55\n"
"v_and_b32 v191, v237, v55\n"
"v_pk_fma_f32 v[184:185], v[184:185], v[212:213], v[214:215] op_sel_hi:[1,0,0]\n"
"v_pk_fma_f32 v[186:187], v[186:187], v[212:213], v[214:215] op_sel_hi:[1,0,0]\n"
"v_pk_fma_f32 v[188:189], v[188:189], v[212:213], v[214:215] op_sel_hi:[1,0,0]\n"
"v_pk_fma_f32 v[190:191], v[190:191], v[212:213], v[214:215] op_sel_hi:[1,0,0]\n"
"v_cvt_pk_f16_f32 v52, v184, v185\n"
"v_cvt_pk_f16_f32 v53, v186, v187\n"
"v_cvt_pk_f16_f32 v54, v188, v189\n"
"v_cvt_pk_f16_f32 v55, v190, v191\n"
"v_lshlrev_b32 v176, 16, v56\n"
"v_and_b32 v177, v237, v56\n"
"v_lshlrev_b32 v178, 16, v57\n"
"v_and_b32 v179, v237, v57\n"
"v_lshlrev_b32 v180, 16, v58\n"
"v_and_b32 v181, v237, v58\n"
"v_lshlrev_b32 v182, 16, v59\n"
"s_mov_b32 m0, s51\n"
"v_and_b32 v183, v237, v59\n"
"buffer_load_dwordx4 v217, s[20:23], s50 offen offset:0 lds\n"
"v_pk_fma_f32 v[176:177], v[176:177], v[208:209], v[210:211] op_sel_hi:[1,0,0]\n"
"v_pk_fma_f32 v[178:179], v[178:179], v[208:209], v[210:211] op_sel_hi:[1,0,0]\n"
"v_pk_fma_f32 v[180:181], v[180:181], v[208:209], v[210:211] op_sel_hi:[1,0,0]\n"
"v_pk_fma_f32 v[182:183], v[182:183], v[208:209], v[210:211] op_sel_hi:[1,0,0]\n"
"v_cvt_pk_f16_f32 v56, v176, v177\n"
"v_cvt_pk_f16_f32 v57, v178, v179\n"
"v_cvt_pk_f16_f32 v58, v180, v181\n"
"v_cvt_pk_f16_f32 v59, v182, v183\n"
"v_lshlrev_b32 v184, 16, v60\n"
"v_and_b32 v185, v237, v60\n"
"v_lshlrev_b32 v186, 16, v61\n"
"v_and_b32 v187, v237, v61\n"
"v_lshlrev_b32 v188, 16, v62\n"
"v_and_b32 v189, v237, v62\n"
"v_lshlrev_b32 v190, 16, v63\n"
"v_and_b32 v191, v237, v63\n"
"v_pk_fma_f32 v[184:185], v[184:185], v[212:213], v[214:215] op_sel_hi:[1,0,0]\n"
"v_pk_fma_f32 v[186:187], v[186:187], v[212:213], v[214:215] op_sel_hi:[1,0,0]\n"
"v_pk_fma_f32 v[188:189], v[188:189], v[212:213], v[214:215] op_sel_hi:[1,0,0]\n"
"s_add_u32 m0, s51, 7936\n"
"v_pk_fma_f32 v[190:191], v[190:191], v[212:213], v[214:215] op_sel_hi:[1,0,0]\n"
"buffer_load_dwordx4 v217, s[20:23], s50 offen offset:256 lds\n"
"s_add_u32 s50, s50, 32768\n"
"v_cvt_pk_f16_f32 v60, v184, v185\n"
"v_cvt_pk_f16_f32 v61, v186, v187\n"
"v_cvt_pk_f16_f32 v62, v188, v189\n"
"v_cvt_pk_f16_f32 v63, v190, v191\n"
"v_lshlrev_b32 v176, 16, v64\n"
"v_and_b32 v177, v237, v64\n"
"v_lshlrev_b32 v178, 16, v65\n"
"v_and_b32 v179, v237, v65\n"
"v_lshlrev_b32 v180, 16, v66\n"
"v_and_b32 v181, v237, v66\n"
"v_lshlrev_b32 v182, 16, v67\n"
"v_and_b32 v183, v237, v67\n"
"v_pk_fma_f32 v[176:177], v[176:177], v[208:209], v[210:211] op_sel_hi:[1,0,0]\n"
"v_pk_fma_f32 v[178:179], v[178:179], v[208:209], v[210:211] op_sel_hi:[1,0,0]\n"
"v_pk_fma_f32 v[180:181], v[180:181], v[208:209], v[210:211] op_sel_hi:[1,0,0]\n"
"v_pk_fma_f32 v[182:183], v[182:183], v[208:209], v[210:211] op_sel_hi:[1,0,0]\n"
"v_cvt_pk_f16_f32 v64, v176, v177\n"
"v_cvt_pk_f16_f32 v65, v178, v179\n"
"v_cvt_pk_f16_f32 v66, v180, v181\n"
"v_cvt_pk_f16_f32 v67, v182, v183\n"
"v_lshlrev_b32 v184, 16, v68\n"
"v_and_b32 v185, v237, v68\n"
"v_lshlrev_b32 v186, 16, v69\n"
"s_add_u32 m0, s51, 1024\n"
"v_and_b32 v187, v237, v69\n"
"buffer_load_dwordx4 v217, s[20:23], s50 offen offset:0 lds\n"
"v_lshlrev_b32 v188, 16, v70\n"
"v_and_b32 v189, v237, v70\n"
"v_lshlrev_b32 v190, 16, v71\n"
"v_and_b32 v191, v237, v71\n"
"v_pk_fma_f32 v[184:185], v[184:185], v[212:213], v[214:215] op_sel_hi:[1,0,0]\n"
"v_pk_fma_f32 v[186:187], v[186:187], v[212:213], v[214:215] op_sel_hi:[1,0,0]\n"
"v_pk_fma_f32 v[188:189], v[188:189], v[212:213], v[214:215] op_sel_hi:[1,0,0]\n"
"v_pk_fma_f32 v[190:191], v[190:191], v[212:213], v[214:215] op_sel_hi:[1,0,0]\n"
"v_cvt_pk_f16_f32 v68, v184, v185\n"
"v_cvt_pk_f16_f32 v69, v186, v187\n"
"v_cvt_pk_f16_f32 v70, v188, v189\n"
"v_cvt_pk_f16_f32 v71, v190, v191\n"
"v_lshlrev_b32 v176, 16, v72\n"
"v_and_b32 v177, v237, v72\n"
"v_lshlrev_b32 v178, 16, v73\n"
"v_and_b32 v179, v237, v73\n"
"v_lshlrev_b32 v180, 16, v74\n"
"v_and_b32 v181, v237, v74\n"
"v_lshlrev_b32 v182, 16, v75\n"
"s_add_u32 m0, s51, 8960\n"
"v_and_b32 v183, v237, v75\n"
"buffer_load_dwordx4 v217, s[20:23], s50 offen offset:256 lds\n"
"s_add_u32 s50, s50, 32768\n"
"v_pk_fma_f32 v[176:177], v[176:177], v[208:209], v[210:211] op_sel_hi:[1,0,0]\n"
"v_pk_fma_f32 v[178:179], v[178:179], v[208:209], v[210:211] op_sel_hi:[1,0,0]\n"
"v_pk_fma_f32 v[180:181], v[180:181], v[208:209], v[210:211] op_sel_hi:[1,0,0]\n"
"v_pk_fma_f32 v[182:183], v[182:183], v[208:209], v[210:211] op_sel_hi:[1,0,0]\n"
"v_cvt_pk_f16_f32 v72, v176, v177\n"
"v_cvt_pk_f16_f32 v73, v178, v179\n"
"v_cvt_pk_f16_f32 v74, v180, v181\n"
"v_cvt_pk_f16_f32 v75, v182, v183\n"
"v_lshlrev_b32 v184, 16, v76\n"
"v_and_b32 v185, v237, v76\n"
"v_lshlrev_b32 v186, 16, v77\n"
"v_and_b32 v187, v237, v77\n"
"v_lshlrev_b32 v188, 16, v78\n"
"v_and_b32 v189, v237, v78\n"
"v_lshlrev_b32 v190, 16, v79\n"
"v_and_b32 v191, v237, v79\n"
"v_pk_fma_f32 v[184:185], v[184:185], v[212:213], v[214:215] op_sel_hi:[1,0,0]\n"
"v_pk_fma_f32 v[186:187], v[186:187], v[212:213], v[214:215] op_sel_hi:[1,0,0]\n"
"v_pk_fma_f32 v[188:189], v[188:189], v[212:213], v[214:215] op_sel_hi:[1,0,0]\n"
"s_add_u32 m0, s51, 16392\n"
"v_pk_fma_f32 v[190:191], v[190:191], v[212:213], v[214:215] op_sel_hi:[1,0,0]\n"
"buffer_load_dwordx4 v217, s[20:23], s50 offen offset:0 lds\n"
"v_cvt_pk_f16_f32 v76, v184, v185\n"
"v_cvt_pk_f16_f32 v77, v186, v187\n"
"v_cvt_pk_f16_f32 v78, v188, v189\n"
"v_cvt_pk_f16_f32 v79, v190, v191\n"
"v_lshlrev_b32 v176, 16, v80\n"
"v_and_b32 v177, v237, v80\n"
"v_lshlrev_b32 v178, 16, v81\n"
"v_and_b32 v179, v237, v81\n"
"v_lshlrev_b32 v180, 16, v82\n"
"v_and_b32 v181, v237, v82\n"
"v_lshlrev_b32 v182, 16, v83\n"
"v_and_b32 v183, v237, v83\n"
"v_pk_fma_f32 v[176:177], v[176:177], v[208:209], v[210:211] op_sel_hi:[1,0,0]\n"
"v_pk_fma_f32 v[178:179], v[178:179], v[208:209], v[210:211] op_sel_hi:[1,0,0]\n"
"v_pk_fma_f32 v[180:181], v[180:181], v[208:209], v[210:211] op_sel_hi:[1,0,0]\n"
"v_pk_fma_f32 v[182:183], v[182:183], v[208:209], v[210:211] op_sel_hi:[1,0,0]\n"
"v_cvt_pk_f16_f32 v80, v176, v177\n"
"v_cvt_pk_f16_f32 v81, v178, v179\n"
"v_cvt_pk_f16_f32 v82, v180, v181\n"
"v_cvt_pk_f16_f32 v83, v182, v183\n"
"v_lshlrev_b32 v184, 16, v84\n"
"v_and_b32 v185, v237, v84\n"
"v_lshlrev_b32 v186, 16, v85\n"
"s_add_u32 m0, s51, 24328\n"
"v_and_b32 v187, v237, v85\n"
"buffer_load_dwordx4 v217, s[20:23], s50 offen offset:256 lds\n"
"s_add_u32 s50, s50, 32768\n"
"v_lshlrev_b32 v188, 16, v86\n"
"v_and_b32 v189, v237, v86\n"
"v_lshlrev_b32 v190, 16, v87\n"
"v_and_b32 v191, v237, v87\n"
"v_pk_fma_f32 v[184:185], v[184:185], v[212:213], v[214:215] op_sel_hi:[1,0,0]\n"
"v_pk_fma_f32 v[186:187], v[186:187], v[212:213], v[214:215] op_sel_hi:[1,0,0]\n"
"v_pk_fma_f32 v[188:189], v[188:189], v[212:213], v[214:215] op_sel_hi:[1,0,0]\n"
"v_pk_fma_f32 v[190:191], v[190:191], v[212:213], v[214:215] op_sel_hi:[1,0,0]\n"
"v_cvt_pk_f16_f32 v84, v184, v185\n"
"v_cvt_pk_f16_f32 v85, v186, v187\n"
"v_cvt_pk_f16_f32 v86, v188, v189\n"
"v_cvt_pk_f16_f32 v87, v190, v191\n"
"v_lshlrev_b32 v176, 16, v88\n"
"v_and_b32 v177, v237, v88\n"
"v_lshlrev_b32 v178, 16, v89\n"
"v_and_b32 v179, v237, v89\n"
"v_lshlrev_b32 v180, 16, v90\n"
"v_and_b32 v181, v237, v90\n"
"v_lshlrev_b32 v182, 16, v91\n"
"s_add_u32 m0, s51, 17416\n"
"v_and_b32 v183, v237, v91\n"
"buffer_load_dwordx4 v217, s[20:23], s50 offen offset:0 lds\n"
"v_pk_fma_f32 v[176:177], v[176:177], v[208:209], v[210:211] op_sel_hi:[1,0,0]\n"
"v_pk_fma_f32 v[178:179], v[178:179], v[208:209], v[210:211] op_sel_hi:[1,0,0]\n"
"v_pk_fma_f32 v[180:181], v[180:181], v[208:209], v[210:211] op_sel_hi:[1,0,0]\n"
"v_pk_fma_f32 v[182:183], v[182:183], v[208:209], v[210:211] op_sel_hi:[1,0,0]\n"
"v_cvt_pk_f16_f32 v88, v176, v177\n"
"v_cvt_pk_f16_f32 v89, v178, v179\n"
"v_cvt_pk_f16_f32 v90, v180, v181\n"
"v_cvt_pk_f16_f32 v91, v182, v183\n"
"v_lshlrev_b32 v184, 16, v92\n"
"v_and_b32 v185, v237, v92\n"
"v_lshlrev_b32 v186, 16, v93\n"
"v_and_b32 v187, v237, v93\n"
"v_lshlrev_b32 v188, 16, v94\n"
"v_and_b32 v189, v237, v94\n"
"v_lshlrev_b32 v190, 16, v95\n"
"v_and_b32 v191, v237, v95\n"
"v_pk_fma_f32 v[184:185], v[184:185], v[212:213], v[214:215] op_sel_hi:[1,0,0]\n"
"v_pk_fma_f32 v[186:187], v[186:187], v[212:213], v[214:215] op_sel_hi:[1,0,0]\n"
"v_pk_fma_f32 v[188:189], v[188:189], v[212:213], v[214:215] op_sel_hi:[1,0,0]\n"
"s_add_u32 m0, s51, 25352\n"
"v_pk_fma_f32 v[190:191], v[190:191], v[212:213], v[214:215] op_sel_hi:[1,0,0]\n"
"buffer_load_dwordx4 v217, s[20:23], s50 offen offset:256 lds\n"
"s_add_u32 s50, s50, 425984\n"
"v_cvt_pk_f16_f32 v92, v184, v185\n"
"v_cvt_pk_f16_f32 v93, v186, v187\n"
"v_cvt_pk_f16_f32 v94, v188, v189\n"
"v_cvt_pk_f16_f32 v95, v190, v191\n"
"v_lshlrev_b32 v176, 16, v96\n"
"v_and_b32 v177, v237, v96\n"
"v_lshlrev_b32 v178, 16, v97\n"
"v_and_b32 v179, v237, v97\n"
"v_lshlrev_b32 v180, 16, v98\n"
"v_and_b32 v181, v237, v98\n"
"v_lshlrev_b32 v182, 16, v99\n"
"v_and_b32 v183, v237, v99\n"
"v_pk_fma_f32 v[176:177], v[176:177], v[208:209], v[210:211] op_sel_hi:[1,0,0]\n"
"v_pk_fma_f32 v[178:179], v[178:179], v[208:209], v[210:211] op_sel_hi:[1,0,0]\n"
"v_pk_fma_f32 v[180:181], v[180:181], v[208:209], v[210:211] op_sel_hi:[1,0,0]\n"
"v_pk_fma_f32 v[182:183], v[182:183], v[208:209], v[210:211] op_sel_hi:[1,0,0]\n"
"v_cvt_pk_f16_f32 v96, v176, v177\n"
"v_cvt_pk_f16_f32 v97, v178, v179\n"
"v_cvt_pk_f16_f32 v98, v180, v181\n"
"v_cvt_pk_f16_f32 v99, v182, v183\n"
"v_lshlrev_b32 v184, 16, v100\n"
"v_and_b32 v185, v237, v100\n"
"v_lshlrev_b32 v186, 16, v101\n"
"s_add_u32 m0, s51, 32784\n"
"v_and_b32 v187, v237, v101\n"
"buffer_load_dwordx4 v217, s[20:23], s50 offen offset:0 lds\n"
"v_lshlrev_b32 v188, 16, v102\n"
"v_and_b32 v189, v237, v102\n"
"v_lshlrev_b32 v190, 16, v103\n"
"v_and_b32 v191, v237, v103\n"
"v_pk_fma_f32 v[184:185], v[184:185], v[212:213], v[214:215] op_sel_hi:[1,0,0]\n"
"v_pk_fma_f32 v[186:187], v[186:187], v[212:213], v[214:215] op_sel_hi:[1,0,0]\n"
"v_pk_fma_f32 v[188:189], v[188:189], v[212:213], v[214:215] op_sel_hi:[1,0,0]\n"
"v_pk_fma_f32 v[190:191], v[190:191], v[212:213], v[214:215] op_sel_hi:[1,0,0]\n"
"v_cvt_pk_f16_f32 v100, v184, v185\n"
"v_cvt_pk_f16_f32 v101, v186, v187\n"
"v_cvt_pk_f16_f32 v102, v188, v189\n"
"v_cvt_pk_f16_f32 v103, v190, v191\n"
"v_lshlrev_b32 v176, 16, v104\n"
"v_and_b32 v177, v237, v104\n"
"v_lshlrev_b32 v178, 16, v105\n"
"v_and_b32 v179, v237, v105\n"
"v_lshlrev_b32 v180, 16, v106\n"
"v_and_b32 v181, v237, v106\n"
"v_lshlrev_b32 v182, 16, v107\n"
"s_add_u32 m0, s51, 40720\n"
"v_and_b32 v183, v237, v107\n"
"buffer_load_dwordx4 v217, s[20:23], s50 offen offset:256 lds\n"
"s_add_u32 s50, s50, 32768\n"
"v_pk_fma_f32 v[176:177], v[176:177], v[208:209], v[210:211] op_sel_hi:[1,0,0]\n"
"v_pk_fma_f32 v[178:179], v[178:179], v[208:209], v[210:211] op_sel_hi:[1,0,0]\n"
"v_pk_fma_f32 v[180:181], v[180:181], v[208:209], v[210:211] op_sel_hi:[1,0,0]\n"
"v_pk_fma_f32 v[182:183], v[182:183], v[208:209], v[210:211] op_sel_hi:[1,0,0]\n"
"v_cvt_pk_f16_f32 v104, v176, v177\n"
"v_cvt_pk_f16_f32 v105, v178, v179\n"
"v_cvt_pk_f16_f32 v106, v180, v181\n"
"v_cvt_pk_f16_f32 v107, v182, v183\n"
"v_lshlrev_b32 v184, 16, v108\n"
"v_and_b32 v185, v237, v108\n"
"v_lshlrev_b32 v186, 16, v109\n"
"v_and_b32 v187, v237, v109\n"
"v_lshlrev_b32 v188, 16, v110\n"
"v_and_b32 v189, v237, v110\n"
"v_lshlrev_b32 v190, 16, v111\n"
"v_and_b32 v191, v237, v111\n"
"v_pk_fma_f32 v[184:185], v[184:185], v[212:213], v[214:215] op_sel_hi:[1,0,0]\n"
"v_pk_fma_f32 v[186:187], v[186:187], v[212:213], v[214:215] op_sel_hi:[1,0,0]\n"
"v_pk_fma_f32 v[188:189], v[188:189], v[212:213], v[214:215] op_sel_hi:[1,0,0]\n"
"s_add_u32 m0, s51, 33808\n"
"v_pk_fma_f32 v[190:191], v[190:191], v[212:213], v[214:215] op_sel_hi:[1,0,0]\n"
"buffer_load_dwordx4 v217, s[20:23], s50 offen offset:0 lds\n"
"v_cvt_pk_f16_f32 v108, v184, v185\n"
"v_cvt_pk_f16_f32 v109, v186, v187\n"
"v_cvt_pk_f16_f32 v110, v188, v189\n"
"v_cvt_pk_f16_f32 v111, v190, v191\n"
"s_mov_b32 s87, 0x80000\n"
"s_waitcnt vmcnt(13)\n"
"AT_gchk:\n"
"v_xor_b32 v192, s85, v145\n"
"v_xor_b32 v193, s85, v147\n"
"v_bitop3_b32 v192, v149, s85, v192 bitop3:0xbe\n"
"v_bitop3_b32 v193, v151, s85, v193 bitop3:0xbe\n"
"v_bitop3_b32 v192, v153, s85, v192 bitop3:0xbe\n"
"v_bitop3_b32 v193, v155, s85, v193 bitop3:0xbe\n"
"v_bitop3_b32 v192, v157, s85, v192 bitop3:0xbe\n"
"v_bitop3_b32 v193, v159, s85, v193 bitop3:0xbe\n"
"v_or_b32 v192, v192, v193\n"
"v_cmp_ne_u32 s[56:57], 0, v192\n"
"v_cmp_gt_u32 vcc, 144, v234\n"
"s_nop 1\n"
"s_and_b64 vcc, vcc, s[56:57]\n"
"s_cbranch_vccz AT_gok\n"
"s_sub_u32 s87, s87, 1\n"
"s_cmp_eq_u32 s87, 0\n"
"s_cbranch_scc1 AT_gok\n"
"s_sleep 2\n"
"buffer_load_dwordx4 v[144:147], v225, s[28:31], s61 offen offset:0 sc1\n"
"buffer_load_dwordx4 v[148:151], v225, s[28:31], s61 offen offset:16 sc1\n"
"buffer_load_dwordx4 v[152:155], v225, s[28:31], s62 offen offset:0 sc1\n"
"buffer_load_dwordx4 v[156:159], v225, s[28:31], s62 offen offset:16 sc1\n"
"s_waitcnt vmcnt(0)\n"
"s_branch AT_gchk\n"
"AT_gok:\n"
"v_mov_b32 v236, 0x3e0293ee\n"
"v_mul_f32 v192, v236, v144\n"
"v_fma_f32 v196, v152, v236, v236\n"
"v_mul_f32 v193, v236, v146\n"
"v_fma_f32 v197, v154, v236, v236\n"
"v_mul_f32 v194, v236, v148\n"
"v_fma_f32 v198, v156, v236, v236\n"
"v_mul_f32 v195, v236, v150\n"
"v_fma_f32 v199, v158, v236, v236\n"
"ds_write_b128 v216, v[196:199]\n"
"ds_write_b128 v216, v[192:195] offset:128\n"
"v_lshlrev_b32 v176, 16, v112\n"
"v_and_b32 v177, v237, v112\n"
"v_lshlrev_b32 v178, 16, v113\n"
"v_and_b32 v179, v237, v113\n"
"v_lshlrev_b32 v180, 16, v114\n"
"v_and_b32 v181, v237, v114\n"
"v_lshlrev_b32 v182, 16, v115\n"
"v_and_b32 v183, v237, v115\n"
"v_pk_fma_f32 v[176:177], v[176:177], v[208:209], v[210:211] op_sel_hi:[1,0,0]\n"
"v_pk_fma_f32 v[178:179], v[178:179], v[208:209], v[210:211] op_sel_hi:[1,0,0]\n"
"v_pk_fma_f32 v[180:181], v[180:181], v[208:209], v[210:211] op_sel_hi:[1,0,0]\n"
"v_pk_fma_f32 v[182:183], v[182:183], v[208:209], v[210:211] op_sel_hi:[1,0,0]\n"
"v_cvt_pk_f16_f32 v112, v176, v177\n"
"v_cvt_pk_f16_f32 v113, v178, v179\n"
"v_cvt_pk_f16_f32 v114, v180, v181\n"
"v_cvt_pk_f16_f32 v115, v182, v183\n"
"v_lshlrev_b32 v184, 16, v116\n"
"v_and_b32 v185, v237, v116\n"
"v_lshlrev_b32 v186, 16, v117\n"
"s_add_u32 m0, s51, 41744\n"
"v_and_b32 v187, v237, v117\n"
"buffer_load_dwordx4 v217, s[20:23], s50 offen offset:256 lds\n"
"s_add_u32 s50, s50, 32768\n"
"v_lshlrev_b32 v188, 16, v118\n"
"v_and_b32 v189, v237, v118\n"
"v_lshlrev_b32 v190, 16, v119\n"
"v_and_b32 v191, v237, v119\n"
"v_pk_fma_f32 v[184:185], v[184:185], v[212:213], v[214:215] op_sel_hi:[1,0,0]\n"
"v_pk_fma_f32 v[186:187], v[186:187], v[212:213], v[214:215] op_sel_hi:[1,0,0]\n"
"v_pk_fma_f32 v[188:189], v[188:189], v[212:213], v[214:215] op_sel_hi:[1,0,0]\n"
"v_pk_fma_f32 v[190:191], v[190:191], v[212:213], v[214:215] op_sel_hi:[1,0,0]\n"
"v_cvt_pk_f16_f32 v116, v184, v185\n"
"v_cvt_pk_f16_f32 v117, v186, v187\n"
"v_cvt_pk_f16_f32 v118, v188, v189\n"
"v_cvt_pk_f16_f32 v119, v190, v191\n"
"v_lshlrev_b32 v176, 16, v120\n"
"v_and_b32 v177, v237, v120\n"
"v_lshlrev_b32 v178, 16, v121\n"
"v_and_b32 v179, v237, v121\n"
"v_lshlrev_b32 v180, 16, v122\n"
"v_and_b32 v181, v237, v122\n"
"v_lshlrev_b32 v182, 16, v123\n"
"s_add_u32 m0, s51, 49176\n"
"v_and_b32 v183, v237, v123\n"
"buffer_load_dwordx4 v217, s[20:23], s50 offen offset:0 lds\n"
"v_pk_fma_f32 v[176:177], v[176:177], v[208:209], v[210:211] op_sel_hi:[1,0,0]\n"
"v_pk_fma_f32 v[178:179], v[178:179], v[208:209], v[210:211] op_sel_hi:[1,0,0]\n"
"v_pk_fma_f32 v[180:181], v[180:181], v[208:209], v[210:211] op_sel_hi:[1,0,0]\n"
"v_pk_fma_f32 v[182:183], v[182:183], v[208:209], v[210:211] op_sel_hi:[1,0,0]\n"
"v_cvt_pk_f16_f32 v120, v176, v177\n"
"v_cvt_pk_f16_f32 v121, v178, v179\n"
"v_cvt_pk_f16_f32 v122, v180, v181\n"
"v_cvt_pk_f16_f32 v123, v182, v183\n"
"v_lshlrev_b32 v184, 16, v124\n"
"v_and_b32 v185, v237, v124\n"
"v_lshlrev_b32 v186, 16, v125\n"
"v_and_b32 v187, v237, v125\n"
"v_lshlrev_b32 v188, 16, v126\n"
"v_and_b32 v189, v237, v126\n"
"v_lshlrev_b32 v190, 16, v127\n"
"v_and_b32 v191, v237, v127\n"
"v_pk_fma_f32 v[184:185], v[184:185], v[212:213], v[214:215] op_sel_hi:[1,0,0]\n"
"v_pk_fma_f32 v[186:187], v[186:187], v[212:213], v[214:215] op_sel_hi:[1,0,0]\n"
"v_pk_fma_f32 v[188:189], v[188:189], v[212:213], v[214:215] op_sel_hi:[1,0,0]\n"
"s_add_u32 m0, s51, 57112\n"
"v_pk_fma_f32 v[190:191], v[190:191], v[212:213], v[214:215] op_sel_hi:[1,0,0]\n"
"buffer_load_dwordx4 v217, s[20:23], s50 offen offset:256 lds\n"
"s_add_u32 s50, s50, 32768\n"
"v_cvt_pk_f16_f32 v124, v184, v185\n"
"v_cvt_pk_f16_f32 v125, v186, v187\n"
"v_cvt_pk_f16_f32 v126, v188, v189\n"
"v_cvt_pk_f16_f32 v127, v190, v191\n"
"s_waitcnt vmcnt(14) lgkmcnt(0)\n"
"s_barrier\n"
"ds_read_b128 v[144:147], v224 offset:0\n"
"ds_read_b128 v[148:151], v224 offset:64\n"
"ds_read_b128 v[152:155], v224 offset:128\n"
"ds_read_b128 v[156:159], v224 offset:192\n"
"ds_read_b128 v[160:163], v224 offset:256\n"
"ds_read_b128 v[164:167], v224 offset:320\n"
"ds_read_b128 v[168:171], v224 offset:384\n"
"ds_read_b128 v[172:175], v224 offset:448\n"
"v_lshlrev_b32 v176, 16, v128\n"
"v_and_b32 v177, v237, v128\n"
"v_lshlrev_b32 v178, 16, v129\n"
"v_and_b32 v179, v237, v129\n"
"v_lshlrev_b32 v180, 16, v130\n"
"v_and_b32 v181, v237, v130\n"
"v_lshlrev_b32 v182, 16, v131\n"
"v_and_b32 v183, v237, v131\n"
"v_pk_fma_f32 v[176:177], v[176:177], v[208:209], v[210:211] op_sel_hi:[1,0,0]\n"
"v_pk_fma_f32 v[178:179], v[178:179], v[208:209], v[210:211] op_sel_hi:[1,0,0]\n"
"v_pk_fma_f32 v[180:181], v[180:181], v[208:209], v[210:211] op_sel_hi:[1,0,0]\n"
"v_pk_fma_f32 v[182:183], v[182:183], v[208:209], v[210:211] op_sel_hi:[1,0,0]\n"
"v_cvt_pk_f16_f32 v128, v176, v177\n"
"v_cvt_pk_f16_f32 v129, v178, v179\n"
"v_cvt_pk_f16_f32 v130, v180, v181\n"
"v_cvt_pk_f16_f32 v131, v182, v183\n"
"v_lshlrev_b32 v184, 16, v132\n"
"v_and_b32 v185, v237, v132\n"
"v_lshlrev_b32 v186, 16, v133\n"
"s_add_u32 m0, s51, 50200\n"
"v_and_b32 v187, v237, v133\n"
"buffer_load_dwordx4 v217, s[20:23], s50 offen offset:0 lds\n"
"v_lshlrev_b32 v188, 16, v134\n"
"v_and_b32 v189, v237, v134\n"
"v_lshlrev_b32 v190, 16, v135\n"
"v_and_b32 v191, v237, v135\n"
"v_pk_fma_f32 v[184:185], v[184:185], v[212:213], v[214:215] op_sel_hi:[1,0,0]\n"
"v_pk_fma_f32 v[186:187], v[186:187], v[212:213], v[214:215] op_sel_hi:[1,0,0]\n"
"v_pk_fma_f32 v[188:189], v[188:189], v[212:213], v[214:215] op_sel_hi:[1,0,0]\n"
"v_pk_fma_f32 v[190:191], v[190:191], v[212:213], v[214:215] op_sel_hi:[1,0,0]\n"
"v_cvt_pk_f16_f32 v132, v184, v185\n"
"v_cvt_pk_f16_f32 v133, v186, v187\n"
"v_cvt_pk_f16_f32 v134, v188, v189\n"
"v_cvt_pk_f16_f32 v135, v190, v191\n"
"v_lshlrev_b32 v176, 16, v136\n"
"v_and_b32 v177, v237, v136\n"
"v_lshlrev_b32 v178, 16, v137\n"
"v_and_b32 v179, v237, v137\n"
"v_lshlrev_b32 v180, 16, v138\n"
"v_and_b32 v181, v237, v138\n"
"v_lshlrev_b32 v182, 16, v139\n"
"s_add_u32 m0, s51, 58136\n"
"v_and_b32 v183, v237, v139\n"
"buffer_load_dwordx4 v217, s[20:23], s50 offen offset:256 lds\n"
"s_add_u32 s50, s50, 425984\n"
"v_pk_fma_f32 v[176:177], v[176:177], v[208:209], v[210:211] op_sel_hi:[1,0,0]\n"
"v_pk_fma_f32 v[178:179], v[178:179], v[208:209], v[210:211] op_sel_hi:[1,0,0]\n"
"v_pk_fma_f32 v[180:181], v[180:181], v[208:209], v[210:211] op_sel_hi:[1,0,0]\n"
"v_pk_fma_f32 v[182:183], v[182:183], v[208:209], v[210:211] op_sel_hi:[1,0,0]\n"
"v_cvt_pk_f16_f32 v136, v176, v177\n"
"v_cvt_pk_f16_f32 v137, v178, v179\n"
"v_cvt_pk_f16_f32 v138, v180, v181\n"
"v_cvt_pk_f16_f32 v139, v182, v183\n"
"v_lshlrev_b32 v184, 16, v140\n"
"v_and_b32 v185, v237, v140\n"
"v_lshlrev_b32 v186, 16, v141\n"
"v_and_b32 v187, v237, v141\n"
"v_lshlrev_b32 v188, 16, v142\n"
"v_and_b32 v189, v237, v142\n"
"v_lshlrev_b32 v190, 16, v143\n"
"v_and_b32 v191, v237, v143\n"
"v_pk_fma_f32 v[184:185], v[184:185], v[212:213], v[214:215] op_sel_hi:[1,0,0]\n"
"v_pk_fma_f32 v[186:187], v[186:187], v[212:213], v[214:215] op_sel_hi:[1,0,0]\n"
"v_pk_fma_f32 v[188:189], v[188:189], v[212:213], v[214:215] op_sel_hi:[1,0,0]\n"
"v_pk_fma_f32 v[190:191], v[190:191], v[212:213], v[214:215] op_sel_hi:[1,0,0]\n"
"v_cvt_pk_f16_f32 v140, v184, v185\n"
"v_cvt_pk_f16_f32 v141, v186, v187\n"
"v_cvt_pk_f16_f32 v142, v188, v189\n"
"v_cvt_pk_f16_f32 v143, v190, v191\n"
"ds_read_b64_tr_b16 a[160:161], v219 offset:32784\n"
"s_waitcnt lgkmcnt(6)\n"
"v_fma_mix_f32 v176, v0, v144, v152 op_sel:[0,0,0] op_sel_hi:[1,0,0]\n"
"v_fma_mix_f32 v177, v0, v145, v153 op_sel:[1,0,0] op_sel_hi:[1,0,0]\n"
"v_fma_mix_f32 v178, v1, v146, v154 op_sel:[0,0,0] op_sel_hi:[1,0,0]\n"
"ds_read_b64_tr_b16 a[162:163], v219 offset:33808\n"
"v_fma_mix_f32 v179, v1, v147, v155 op_sel:[1,0,0] op_sel_hi:[1,0,0]\n"
"s_waitcnt lgkmcnt(6)\n"
"v_fma_mix_f32 v180, v2, v148, v156 op_sel:[0,0,0] op_sel_hi:[1,0,0]\n"
"v_fma_mix_f32 v181, v2, v149, v157 op_sel:[1,0,0] op_sel_hi:[1,0,0]\n"
"ds_read_b64_tr_b16 a[164:165], v219 offset:36880\n"
"v_fma_mix_f32 v182, v3, v150, v158 op_sel:[0,0,0] op_sel_hi:[1,0,0]\n"
"v_fma_mix_f32 v183, v3, v151, v159 op_sel:[1,0,0] op_sel_hi:[1,0,0]\n"
"v_cvt_pk_bf16_f32 v0, v176, v177\n"
"ds_read_b64_tr_b16 a[166:167], v219 offset:37904\n"
"v_cvt_pk_bf16_f32 v1, v178, v179\n"
"v_cvt_pk_bf16_f32 v2, v180, v181\n"
"v_cvt_pk_bf16_f32 v3, v182, v183\n"
"ds_read_b64_tr_b16 a[168:169], v219 offset:32792\n"
"v_fma_mix_f32 v184, v4, v144, v152 op_sel:[0,0,0] op_sel_hi:[1,0,0]\n"
"v_fma_mix_f32 v185, v4, v145, v153 op_sel:[1,0,0] op_sel_hi:[1,0,0]\n"
"AT_pg2:\n"
"s_cbranch_execz AT_hr\n"
"v_fma_mix_f32 v186, v5, v146, v154 op_sel:[0,0,0] op_sel_hi:[1,0,0]\n"
"ds_read_b64_tr_b16 a[170:171], v219 offset:33816\n"
"v_fma_mix_f32 v187, v5, v147, v155 op_sel:[1,0,0] op_sel_hi:[1,0,0]\n"
"v_fma_mix_f32 v188, v6, v148, v156 op_sel:[0,0,0] op_sel_hi:[1,0,0]\n"
"v_fma_mix_f32 v189, v6, v149, v157 op_sel:[1,0,0] op_sel_hi:[1,0,0]\n"
"ds_read_b64_tr_b16 a[172:173], v219 offset:36888\n"
"v_fma_mix_f32 v190, v7, v150, v158 op_sel:[0,0,0] op_sel_hi:[1,0,0]\n"
"v_fma_mix_f32 v191, v7, v151, v159 op_sel:[1,0,0] op_sel_hi:[1,0,0]\n"
"v_cvt_pk_bf16_f32 v4, v184, v185\n"
"ds_read_b64_tr_b16 a[174:175], v219 offset:37912\n"
"ds_read_b128 v[144:147], v224 offset:512\n"
"v_cvt_pk_bf16_f32 v5, v186, v187\n"
"v_cvt_pk_bf16_f32 v6, v188, v189\n"
"ds_read_b64_tr_b16 a[176:177], v219 offset:33040\n"
"v_cvt_pk_bf16_f32 v7, v190, v191\n"
"s_waitcnt lgkmcnt(11)\n"
"v_fma_mix_f32 v176, v8, v160, v168 op_sel:[0,0,0] op_sel_hi:[1,0,0]\n"
"v_fma_mix_f32 v177, v8, v161, v169 op_sel:[1,0,0] op_sel_hi:[1,0,0]\n"
"ds_read_b64_tr_b16 a[178:179], v219 offset:34064\n"
"v_fma_mix_f32 v178, v9, v162, v170 op_sel:[0,0,0] op_sel_hi:[1,0,0]\n"
"v_fma_mix_f32 v179, v9, v163, v171 op_sel:[1,0,0] op_sel_hi:[1,0,0]\n"
"s_waitcnt lgkmcnt(11)\n"
"v_fma_mix_f32 v180, v10, v164, v172 op_sel:[0,0,0] op_sel_hi:[1,0,0]\n"
"ds_read_b64_tr_b16 a[180:181], v219 offset:37136\n"
"ds_read_b128 v[148:151], v224 offset:576\n"
"v_fma_mix_f32 v181, v10, v165, v173 op_sel:[1,0,0] op_sel_hi:[1,0,0]\n"
"v_fma_mix_f32 v182, v11, v166, v174 op_sel:[0,0,0] op_sel_hi:[1,0,0]\n"
"ds_read_b64_tr_b16 a[182:183], v219 offset:38160\n"
"v_fma_mix_f32 v183, v11, v167, v175 op_sel:[1,0,0] op_sel_hi:[1,0,0]\n"
"v_cvt_pk_bf16_f32 v8, v176, v177\n"
"v_cvt_pk_bf16_f32 v9, v178, v179\n"
"ds_read_b64_tr_b16 a[184:185], v219 offset:33048\n"
"v_cvt_pk_bf16_f32 v10, v180, v181\n"
"v_cvt_pk_bf16_f32 v11, v182, v183\n"
"v_fma_mix_f32 v184, v12, v160, v168 op_sel:[0,0,0] op_sel_hi:[1,0,0]\n"
"ds_read_b64_tr_b16 a[186:187], v219 offset:34072\n"
"ds_read_b128 v[152:155], v224 offset:640\n"
"v_fma_mix_f32 v185, v12, v161, v169 op_sel:[1,0,0] op_sel_hi:[1,0,0]\n"
"v_fma_mix_f32 v186, v13, v162, v170 op_sel:[0,0,0] op_sel_hi:[1,0,0]\n"
"ds_read_b64_tr_b16 a[188:189], v219 offset:37144\n"
"v_fma_mix_f32 v187, v13, v163, v171 op_sel:[1,0,0] op_sel_hi:[1,0,0]\n"
"v_fma_mix_f32 v188, v14, v164, v172 op_sel:[0,0,0] op_sel_hi:[1,0,0]\n"
"v_fma_mix_f32 v189, v14, v165, v173 op_sel:[1,0,0] op_sel_hi:[1,0,0]\n"
"ds_read_b64_tr_b16 a[190:191], v219 offset:38168\n"
"v_fma_mix_f32 v190, v15, v166, v174 op_sel:[0,0,0] op_sel_hi:[1,0,0]\n"
"v_fma_mix_f32 v191, v15, v167, v175 op_sel:[1,0,0] op_sel_hi:[1,0,0]\n"
"v_cvt_pk_bf16_f32 v12, v184, v185\n"
"ds_read_b64_tr_b16 a[224:225], v219 offset:33296\n"
"ds_read_b128 v[156:159], v224 offset:704\n"
"ds_read_b128 v[160:163], v224 offset:768\n"
"v_cvt_pk_bf16_f32 v13, v186, v187\n"
"ds_read_b64_tr_b16 a[226:227], v219 offset:34320\n"
"v_cvt_pk_bf16_f32 v14, v188, v189\n"
"v_cvt_pk_bf16_f32 v15, v190, v191\n"
"s_waitcnt lgkmcnt(6)\n"
"v_fma_mix_f32 v176, v16, v144, v152 op_sel:[0,0,0] op_sel_hi:[1,0,0]\n"
"ds_read_b64_tr_b16 a[228:229], v219 offset:37392\n"
"v_fma_mix_f32 v177, v16, v145, v153 op_sel:[1,0,0] op_sel_hi:[1,0,0]\n"
"v_fma_mix_f32 v178, v17, v146, v154 op_sel:[0,0,0] op_sel_hi:[1,0,0]\n"
"v_fma_mix_f32 v179, v17, v147, v155 op_sel:[1,0,0] op_sel_hi:[1,0,0]\n"
"ds_read_b64_tr_b16 a[230:231], v219 offset:38416\n"
"ds_read_b128 v[164:167], v224 offset:832\n"
"s_waitcnt lgkmcnt(5)\n"
"v_fma_mix_f32 v180, v18, v148, v156 op_sel:[0,0,0] op_sel_hi:[1,0,0]\n"
"v_fma_mix_f32 v181, v18, v149, v157 op_sel:[1,0,0] op_sel_hi:[1,0,0]\n"
"v_mfma_f32_16x16x32_bf16 a[0:3], a[160:163], v[0:3], 0\n"
"ds_read_b64_tr_b16 a[232:233], v219 offset:33304\n"
"v_fma_mix_f32 v182, v19, v150, v158 op_sel:[0,0,0] op_sel_hi:[1,0,0]\n"
"v_mfma_f32_16x16x32_bf16 a[0:3], a[164:167], v[8:11], a[0:3]\n"
"s_waitcnt vmcnt(8)\n"
"ds_read_b64_tr_b16 a[234:235], v219 offset:34328\n"
"v_fma_mix_f32 v183, v19, v151, v159 op_sel:[1,0,0] op_sel_hi:[1,0,0]\n"
"v_mfma_f32_16x16x32_bf16 a[4:7], a[160:163], v[4:7], 0\n"
"s_barrier\n"
"ds_read_b64_tr_b16 a[236:237], v219 offset:37400\n"
"ds_read_b128 v[168:171], v224 offset:896\n"
"v_mfma_f32_16x16x32_bf16 a[4:7], a[164:167], v[12:15], a[4:7]\n"
"ds_read_b64_tr_b16 a[238:239], v219 offset:38424\n"
"v_cvt_pk_bf16_f32 v16, v176, v177\n"
"v_mfma_f32_16x16x32_bf16 a[8:11], a[168:171], v[0:3], 0\n"
"s_add_u32 m0, s51, 65568\n"
"ds_read_b64_tr_b16 a[240:241], v219 offset:33552\n"
"v_cvt_pk_bf16_f32 v17, v178, v179\n"
"v_mfma_f32_16x16x32_bf16 a[8:11], a[172:175], v[8:11], a[8:11]\n"
"ds_read_b64_tr_b16 a[242:243], v219 offset:34576\n"
"buffer_load_dwordx4 v217, s[20:23], s50 offen offset:0 lds\n"
"v_mfma_f32_16x16x32_bf16 a[12:15], a[168:171], v[4:7], 0\n"
"ds_read_b64_tr_b16 a[244:245], v219 offset:37648\n"
"ds_read_b128 v[172:175], v224 offset:960\n"
"v_mfma_f32_16x16x32_bf16 a[12:15], a[172:175], v[12:15], a[12:15]\n"
"ds_read_b64_tr_b16 a[246:247], v219 offset:38672\n"
"v_cvt_pk_bf16_f32 v18, v180, v181\n"
"v_mfma_f32_16x16x32_bf16 a[16:19], a[176:179], v[0:3], 0\n"
"ds_read_b64_tr_b16 a[248:249], v219 offset:33560\n"
"v_cvt_pk_bf16_f32 v19, v182, v183\n"
"v_mfma_f32_16x16x32_bf16 a[16:19], a[180:183], v[8:11], a[16:19]\n"
"ds_read_b64_tr_b16 a[250:251], v219 offset:34584\n"
"v_fma_mix_f32 v184, v20, v144, v152 op_sel:[0,0,0] op_sel_hi:[1,0,0]\n"
"v_mfma_f32_16x16x32_bf16 a[20:23], a[176:179], v[4:7], 0\n"
"s_add_u32 m0, s51, 73504\n"
"ds_read_b64_tr_b16 a[252:253], v219 offset:37656\n"
"v_fma_mix_f32 v185, v20, v145, v153 op_sel:[1,0,0] op_sel_hi:[1,0,0]\n"
"v_mfma_f32_16x16x32_bf16 a[20:23], a[180:183], v[12:15], a[20:23]\n"
"ds_read_b64_tr_b16 a[254:255], v219 offset:38680\n"
"buffer_load_dwordx4 v217, s[20:23], s50 offen offset:256 lds\n"
"v_mfma_f32_16x16x32_bf16 a[24:27], a[184:187], v[0:3], 0\n"
"s_add_u32 s50, s50, 32768\n"
"ds_read_b64_tr_b16 a[160:161], v219 offset:40976\n"
"v_fma_mix_f32 v186, v21, v146, v154 op_sel:[0,0,0] op_sel_hi:[1,0,0]\n"
"v_mfma_f32_16x16x32_bf16 a[24:27], a[188:191], v[8:11], a[24:27]\n"
"ds_read_b64_tr_b16 a[162:163], v219 offset:42000\n"
"v_fma_mix_f32 v187, v21, v147, v155 op_sel:[1,0,0] op_sel_hi:[1,0,0]\n"
"v_mfma_f32_16x16x32_bf16 a[28:31], a[184:187], v[4:7], 0\n"
"ds_read_b64_tr_b16 a[164:165], v219 offset:45072\n"
"v_fma_mix_f32 v188, v22, v148, v156 op_sel:[0,0,0] op_sel_hi:[1,0,0]\n"
"v_mfma_f32_16x16x32_bf16 a[28:31], a[188:191], v[12:15], a[28:31]\n"
"s_waitcnt lgkmcnt(12)\n"
"ds_read_b64_tr_b16 a[166:167], v219 offset:46096\n"
"v_fma_mix_f32 v189, v22, v149, v157 op_sel:[1,0,0] op_sel_hi:[1,0,0]\n"
"v_mfma_f32_16x16x32_bf16 a[32:35], a[224:227], v[0:3], 0\n"
"s_add_u32 m0, s51, 66592\n"
"ds_read_b64_tr_b16 a[168:169], v219 offset:40984\n"
"v_fma_mix_f32 v190, v23, v150, v158 op_sel:[0,0,0] op_sel_hi:[1,0,0]\n"
"v_mfma_f32_16x16x32_bf16 a[32:35], a[228:231], v[8:11], a[32:35]\n"
"ds_read_b64_tr_b16 a[170:171], v219 offset:42008\n"
"buffer_load_dwordx4 v217, s[20:23], s50 offen offset:0 lds\n"
"v_mfma_f32_16x16x32_bf16 a[36:39], a[224:227], v[4:7], 0\n"
"ds_read_b64_tr_b16 a[172:173], v219 offset:45080\n"
"v_fma_mix_f32 v191, v23, v151, v159 op_sel:[1,0,0] op_sel_hi:[1,0,0]\n"
"v_mfma_f32_16x16x32_bf16 a[36:39], a[228:231], v[12:15], a[36:39]\n"
"ds_read_b64_tr_b16 a[174:175], v219 offset:46104\n"
"ds_read_b128 v[144:147], v224 offset:1024\n"
"v_mfma_f32_16x16x32_bf16 a[40:43], a[232:235], v[0:3], 0\n"
"ds_read_b64_tr_b16 a[176:177], v219 offset:41232\n"
"v_cvt_pk_bf16_f32 v20, v184, v185\n"
"v_mfma_f32_16x16x32_bf16 a[40:43], a[236:239], v[8:11], a[40:43]\n"
"ds_read_b64_tr_b16 a[178:179], v219 offset:42256\n"
"v_cvt_pk_bf16_f32 v21, v186, v187\n"
"v_mfma_f32_16x16x32_bf16 a[44:47], a[232:235], v[4:7], 0\n"
"s_add_u32 m0, s51, 74528\n"
"ds_read_b64_tr_b16 a[180:181], v219 offset:45328\n"
"ds_read_b128 v[148:151], v224 offset:1088\n"
"v_mfma_f32_16x16x32_bf16 a[44:47], a[236:239], v[12:15], a[44:47]\n"
"s_waitcnt lgkmcnt(13)\n"
"ds_read_b64_tr_b16 a[182:183], v219 offset:46352\n"
"buffer_load_dwordx4 v217, s[20:23], s50 offen offset:256 lds\n"
"v_mfma_f32_16x16x32_bf16 a[48:51], a[240:243], v[0:3], 0\n"
"s_add_u32 s50, s50, 32768\n"
"ds_read_b64_tr_b16 a[184:185], v219 offset:41240\n"
"v_cvt_pk_bf16_f32 v22, v188, v189\n"
"v_mfma_f32_16x16x32_bf16 a[48:51], a[244:247], v[8:11], a[48:51]\n"
"ds_read_b64_tr_b16 a[186:187], v219 offset:42264\n"
"ds_read_b128 v[152:155], v224 offset:1152\n"
"v_mfma_f32_16x16x32_bf16 a[52:55], a[240:243], v[4:7], 0\n"
"ds_read_b64_tr_b16 a[188:189], v219 offset:45336\n"
"v_cvt_pk_bf16_f32 v23, v190, v191\n"
"v_mfma_f32_16x16x32_bf16 a[52:55], a[244:247], v[12:15], a[52:55]\n"
"ds_read_b64_tr_b16 a[190:191], v219 offset:46360\n"
"v_fma_mix_f32 v176, v24, v160, v168 op_sel:[0,0,0] op_sel_hi:[1,0,0]\n"
"v_mfma_f32_16x16x32_bf16 a[56:59], a[248:251], v[0:3], 0\n"
"s_add_u32 m0, s51, 81960\n"
"ds_read_b64_tr_b16 a[224:225], v219 offset:41488\n"
"ds_read_b128 v[156:159], v224 offset:1216\n"
"v_mfma_f32_16x16x32_bf16 a[56:59], a[252:255], v[8:11], a[56:59]\n"
"ds_read_b64_tr_b16 a[226:227], v219 offset:42512\n"
"buffer_load_dwordx4 v217, s[20:23], s50 offen offset:0 lds\n"
"v_mfma_f32_16x16x32_bf16 a[60:63], a[248:251], v[4:7], 0\n"
"ds_read_b64_tr_b16 a[228:229], v219 offset:45584\n"
"v_fma_mix_f32 v177, v24, v161, v169 op_sel:[1,0,0] op_sel_hi:[1,0,0]\n"
"v_mfma_f32_16x16x32_bf16 a[60:63], a[252:255], v[12:15], a[60:63]\n"
"s_waitcnt lgkmcnt(14)\n"
"ds_read_b64_tr_b16 a[230:231], v219 offset:46608\n"
"v_fma_mix_f32 v178, v25, v162, v170 op_sel:[0,0,0] op_sel_hi:[1,0,0]\n"
"v_mfma_f32_16x16x32_bf16 a[64:67], a[160:163], v[0:3], 0\n"
"ds_read_b64_tr_b16 a[232:233], v219 offset:41496\n"
"v_fma_mix_f32 v179, v25, v163, v171 op_sel:[1,0,0] op_sel_hi:[1,0,0]\n"
"v_mfma_f32_16x16x32_bf16 a[64:67], a[164:167], v[8:11], a[64:67]\n"
"ds_read_b64_tr_b16 a[234:235], v219 offset:42520\n"
"v_fma_mix_f32 v180, v26, v164, v172 op_sel:[0,0,0] op_sel_hi:[1,0,0]\n"
"v_mfma_f32_16x16x32_bf16 a[68:71], a[160:163], v[4:7], 0\n"
"s_add_u32 m0, s51, 89896\n"
"ds_read_b64_tr_b16 a[236:237], v219 offset:45592\n"
"v_fma_mix_f32 v181, v26, v165, v173 op_sel:[1,0,0] op_sel_hi:[1,0,0]\n"
"v_mfma_f32_16x16x32_bf16 a[68:71], a[164:167], v[12:15], a[68:71]\n"
"ds_read_b64_tr_b16 a[238:239], v219 offset:46616\n"
"buffer_load_dwordx4 v217, s[20:23], s50 offen offset:256 lds\n"
"v_mfma_f32_16x16x32_bf16 a[72:75], a[168:171], v[0:3], 0\n"
"s_add_u32 s50, s50, 32768\n"
"ds_read_b64_tr_b16 a[240:241], v219 offset:41744\n"
"v_fma_mix_f32 v182, v27, v166, v174 op_sel:[0,0,0] op_sel_hi:[1,0,0]\n"
"v_mfma_f32_16x16x32_bf16 a[72:75], a[172:175], v[8:11], a[72:75]\n"
"ds_read_b64_tr_b16 a[242:243], v219 offset:42768\n"
"v_fma_mix_f32 v183, v27, v167, v175 op_sel:[1,0,0] op_sel_hi:[1,0,0]\n"
"v_mfma_f32_16x16x32_bf16 a[76:79], a[168:171], v[4:7], 0\n"
"ds_read_b64_tr_b16 a[244:245], v219 offset:45840\n"
"v_cvt_pk_bf16_f32 v24, v176, v177\n"
"v_mfma_f32_16x16x32_bf16 a[76:79], a[172:175], v[12:15], a[76:79]\n"
"s_waitcnt lgkmcnt(12)\n"
"ds_read_b64_tr_b16 a[246:247], v219 offset:46864\n"
"v_cvt_pk_bf16_f32 v25, v178, v179\n"
"v_mfma_f32_16x16x32_bf16 a[80:83], a[176:179], v[0:3], 0\n"
"s_add_u32 m0, s51, 82984\n"
"ds_read_b64_tr_b16 a[248:249], v219 offset:41752\n"
"v_cvt_pk_bf16_f32 v26, v180, v181\n"
"v_mfma_f32_16x16x32_bf16 a[80:83], a[180:183], v[8:11], a[80:83]\n"
"ds_read_b64_tr_b16 a[250:251], v219 offset:42776\n"
"buffer_load_dwordx4 v217, s[20:23], s50 offen offset:0 lds\n"
"v_mfma_f32_16x16x32_bf16 a[84:87], a[176:179], v[4:7], 0\n"
"ds_read_b64_tr_b16 a[252:253], v219 offset:45848\n"
"v_cvt_pk_bf16_f32 v27, v182, v183\n"
"v_mfma_f32_16x16x32_bf16 a[84:87], a[180:183], v[12:15], a[84:87]\n"
"ds_read_b64_tr_b16 a[254:255], v219 offset:46872\n"
"v_fma_mix_f32 v184, v28, v160, v168 op_sel:[0,0,0] op_sel_hi:[1,0,0]\n"
"v_mfma_f32_16x16x32_bf16 v[236:239], a[184:187], v[0:3], 0\n"
"ds_read_b64_tr_b16 a[160:161], v218 offset:0\n"
"v_fma_mix_f32 v185, v28, v161, v169 op_sel:[1,0,0] op_sel_hi:[1,0,0]\n"
"v_fma_mix_f32 v186, v29, v162, v170 op_sel:[0,0,0] op_sel_hi:[1,0,0]\n"
"v_mfma_f32_16x16x32_bf16 v[236:239], a[188:191], v[8:11], v[236:239]\n"
"ds_read_b64_tr_b16 a[162:163], v218 offset:1024\n"
"v_fma_mix_f32 v187, v29, v163, v171 op_sel:[1,0,0] op_sel_hi:[1,0,0]\n"
"v_mfma_f32_16x16x32_bf16 v[232:235], a[184:187], v[4:7], 0\n"
"s_add_u32 m0, s51, 90920\n"
"ds_read_b64_tr_b16 a[164:165], v218 offset:4096\n"
"v_fma_mix_f32 v188, v30, v164, v172 op_sel:[0,0,0] op_sel_hi:[1,0,0]\n"
"v_mfma_f32_16x16x32_bf16 v[232:235], a[188:191], v[12:15], v[232:235]\n"
"s_waitcnt lgkmcnt(11)\n"
"buffer_load_dwordx4 v217, s[20:23], s50 offen offset:256 lds\n"
"ds_read_b64_tr_b16 a[166:167], v218 offset:5120\n"
"v_mfma_f32_16x16x32_bf16 v[228:231], a[224:227], v[0:3], 0\n"
"s_add_u32 s50, s50, 425984\n"
"ds_read_b64_tr_b16 a[168:169], v218 offset:8\n"
"v_fma_mix_f32 v189, v30, v165, v173 op_sel:[1,0,0] op_sel_hi:[1,0,0]\n"
"v_mfma_f32_16x16x32_bf16 v[228:231], a[228:231], v[8:11], v[228:231]\n"
"ds_read_b64_tr_b16 a[170:171], v218 offset:1032\n"
"v_fma_mix_f32 v190, v31, v166, v174 op_sel:[0,0,0] op_sel_hi:[1,0,0]\n"
"v_mfma_f32_16x16x32_bf16 v[220:223], a[224:227], v[4:7], 0\n"
"ds_read_b64_tr_b16 a[172:173], v218 offset:4104\n"
"v_fma_mix_f32 v191, v31, v167, v175 op_sel:[1,0,0] op_sel_hi:[1,0,0]\n"
"v_mfma_f32_16x16x32_bf16 v[220:223], a[228:231], v[12:15], v[220:223]\n"
"ds_read_b64_tr_b16 a[174:175], v218 offset:5128\n"
"ds_read_b128 v[160:163], v224 offset:1280\n"
"v_mfma_f32_16x16x32_bf16 v[212:215], a[232:235], v[0:3], 0\n"
"ds_read_b64_tr_b16 a[176:177], v218 offset:256\n"
"v_cvt_pk_bf16_f32 v28, v184, v185\n"
"v_mfma_f32_16x16x32_bf16 v[212:215], a[236:239], v[8:11], v[212:215]\n"
"ds_read_b64_tr_b16 a[178:179], v218 offset:1280\n"
"v_cvt_pk_bf16_f32 v29, v186, v187\n"
"v_mfma_f32_16x16x32_bf16 v[208:211], a[232:235], v[4:7], 0\n"
"ds_read_b64_tr_b16 a[180:181], v218 offset:4352\n"
"ds_read_b128 v[164:167], v224 offset:1344\n"
"v_mfma_f32_16x16x32_bf16 v[208:211], a[236:239], v[12:15], v[208:211]\n"
"s_waitcnt lgkmcnt(13)\n"
"ds_read_b64_tr_b16 a[182:183], v218 offset:5376\n"
"v_cvt_pk_bf16_f32 v30, v188, v189\n"
"v_mfma_f32_16x16x32_bf16 v[204:207], a[240:243], v[0:3], 0\n"
"ds_read_b64_tr_b16 a[184:185], v218 offset:264\n"
"v_cvt_pk_bf16_f32 v31, v190, v191\n"
"v_mfma_f32_16x16x32_bf16 v[204:207], a[244:247], v[8:11], v[204:207]\n"
"ds_read_b64_tr_b16 a[186:187], v218 offset:1288\n"
"ds_read_b128 v[168:171], v224 offset:1408\n"
"v_mfma_f32_16x16x32_bf16 v[200:203], a[240:243], v[4:7], 0\n"
"ds_read_b64_tr_b16 a[188:189], v218 offset:4360\n"
"v_fma_mix_f32 v176, v32, v144, v152 op_sel:[0,0,0] op_sel_hi:[1,0,0]\n"
"v_mfma_f32_16x16x32_bf16 v[200:203], a[244:247], v[12:15], v[200:203]\n"
"ds_read_b64_tr_b16 a[190:191], v218 offset:5384\n"
"v_fma_mix_f32 v177, v32, v145, v153 op_sel:[1,0,0] op_sel_hi:[1,0,0]\n"
"v_mfma_f32_16x16x32_bf16 v[196:199], a[248:251], v[0:3], 0\n"
"ds_read_b64_tr_b16 a[224:225], v218 offset:512\n"
"ds_read_b128 v[172:175], v224 offset:1472\n"
"v_mfma_f32_16x16x32_bf16 v[196:199], a[252:255], v[8:11], v[196:199]\n"
"ds_read_b64_tr_b16 a[226:227], v218 offset:1536\n"
"v_fma_mix_f32 v178, v33, v146, v154 op_sel:[0,0,0] op_sel_hi:[1,0,0]\n"
"v_mfma_f32_16x16x32_bf16 v[192:195], a[248:251], v[4:7], 0\n"
"ds_read_b64_tr_b16 a[228:229], v218 offset:4608\n"
"v_fma_mix_f32 v179, v33, v147, v155 op_sel:[1,0,0] op_sel_hi:[1,0,0]\n"
"v_mfma_f32_16x16x32_bf16 v[192:195], a[252:255], v[12:15], v[192:195]\n"
"s_waitcnt lgkmcnt(14)\n"
"ds_read_b64_tr_b16 a[230:231], v218 offset:5632\n"
"v_fma_mix_f32 v180, v34, v148, v156 op_sel:[0,0,0] op_sel_hi:[1,0,0]\n"
"v_mfma_f32_16x16x32_bf16 a[0:3], a[160:163], v[16:19], a[0:3]\n"
"ds_read_b64_tr_b16 a[232:233], v218 offset:520\n"
"v_fma_mix_f32 v181, v34, v149, v157 op_sel:[1,0,0] op_sel_hi:[1,0,0]\n"
"v_mfma_f32_16x16x32_bf16 a[0:3], a[164:167], v[24:27], a[0:3]\n"
"s_waitcnt vmcnt(8)\n"
"ds_read_b64_tr_b16 a[234:235], v218 offset:1544\n"
"v_fma_mix_f32 v182, v35, v150, v158 op_sel:[0,0,0] op_sel_hi:[1,0,0]\n"
"v_mfma_f32_16x16x32_bf16 a[4:7], a[160:163], v[20:23], a[4:7]\n"
"s_barrier\n"
"ds_read_b64_tr_b16 a[236:237], v218 offset:4616\n"
"v_fma_mix_f32 v183, v35, v151, v159 op_sel:[1,0,0] op_sel_hi:[1,0,0]\n"
"v_mfma_f32_16x16x32_bf16 a[4:7], a[164:167], v[28:31], a[4:7]\n"
"ds_read_b64_tr_b16 a[238:239], v218 offset:5640\n"
"v_cvt_pk_bf16_f32 v32, v176, v177\n"
"v_mfma_f32_16x16x32_bf16 a[8:11], a[168:171], v[16:19], a[8:11]\n"
"s_add_u32 m0, s51, 98352\n"
"ds_read_b64_tr_b16 a[240:241], v218 offset:768\n"
"v_cvt_pk_bf16_f32 v33, v178, v179\n"
"v_mfma_f32_16x16x32_bf16 a[8:11], a[172:175], v[24:27], a[8:11]\n"
"ds_read_b64_tr_b16 a[242:243], v218 offset:1792\n"
"buffer_load_dwordx4 v217, s[20:23], s50 offen offset:0 lds\n"
"v_mfma_f32_16x16x32_bf16 a[12:15], a[168:171], v[20:23], a[12:15]\n"
"ds_read_b64_tr_b16 a[244:245], v218 offset:4864\n"
"v_cvt_pk_bf16_f32 v34, v180, v181\n"
"v_mfma_f32_16x16x32_bf16 a[12:15], a[172:175], v[28:31], a[12:15]\n"
"s_waitcnt lgkmcnt(12)\n"
"ds_read_b64_tr_b16 a[246:247], v218 offset:5888\n"
"v_cvt_pk_bf16_f32 v35, v182, v183\n"
"v_mfma_f32_16x16x32_bf16 a[16:19], a[176:179], v[16:19], a[16:19]\n"
"ds_read_b64_tr_b16 a[248:249], v218 offset:776\n"
"v_fma_mix_f32 v184, v36, v144, v152 op_sel:[0,0,0] op_sel_hi:[1,0,0]\n"
"v_mfma_f32_16x16x32_bf16 a[16:19], a[180:183], v[24:27], a[16:19]\n"
"ds_read_b64_tr_b16 a[250:251], v218 offset:1800\n"
"v_fma_mix_f32 v185, v36, v145, v153 op_sel:[1,0,0] op_sel_hi:[1,0,0]\n"
"v_mfma_f32_16x16x32_bf16 a[20:23], a[176:179], v[20:23], a[20:23]\n"
"s_add_u32 m0, s51, 106288\n"
"ds_read_b64_tr_b16 a[252:253], v218 offset:4872\n"
"v_fma_mix_f32 v186, v37, v146, v154 op_sel:[0,0,0] op_sel_hi:[1,0,0]\n"
"v_mfma_f32_16x16x32_bf16 a[20:23], a[180:183], v[28:31], a[20:23]\n"
"ds_read_b64_tr_b16 a[254:255], v218 offset:5896\n"
"buffer_load_dwordx4 v217, s[20:23], s50 offen offset:256 lds\n"
"v_mfma_f32_16x16x32_bf16 a[24:27], a[184:187], v[16:19], a[24:27]\n"
"s_add_u32 s50, s50, 32768\n"
"ds_read_b64_tr_b16 a[160:161], v218 offset:8192\n"
"v_fma_mix_f32 v187, v37, v147, v155 op_sel:[1,0,0] op_sel_hi:[1,0,0]\n"
"v_mfma_f32_16x16x32_bf16 a[24:27], a[188:191], v[24:27], a[24:27]\n"
"ds_read_b64_tr_b16 a[162:163], v218 offset:9216\n"
"v_fma_mix_f32 v188, v38, v148, v156 op_sel:[0,0,0] op_sel_hi:[1,0,0]\n"
"v_mfma_f32_16x16x32_bf16 a[28:31], a[184:187], v[20:23], a[28:31]\n"
"ds_read_b64_tr_b16 a[164:165], v218 offset:12288\n"
"v_fma_mix_f32 v189, v38, v149, v157 op_sel:[1,0,0] op_sel_hi:[1,0,0]\n"
"v_mfma_f32_16x16x32_bf16 a[28:31], a[188:191], v[28:31], a[28:31]\n"
"s_waitcnt lgkmcnt(11)\n"
"ds_read_b64_tr_b16 a[166:167], v218 offset:13312\n"
"v_fma_mix_f32 v190, v39, v150, v158 op_sel:[0,0,0] op_sel_hi:[1,0,0]\n"
"v_mfma_f32_16x16x32_bf16 a[32:35], a[224:227], v[16:19], a[32:35]\n"
"s_add_u32 m0, s51, 99376\n"
"ds_read_b64_tr_b16 a[168:169], v218 offset:8200\n"
"v_fma_mix_f32 v191, v39, v151, v159 op_sel:[1,0,0] op_sel_hi:[1,0,0]\n"
"v_mfma_f32_16x16x32_bf16 a[32:35], a[228:231], v[24:27], a[32:35]\n"
"s_and_b32 s62, s42, 7\n"
"ds_read_b64_tr_b16 a[170:171], v218 offset:9224\n"
"buffer_load_dwordx4 v217, s[20:23], s50 offen offset:0 lds\n"
"v_mfma_f32_16x16x32_bf16 a[36:39], a[224:227], v[20:23], a[36:39]\n"
"ds_read_b64_tr_b16 a[172:173], v218 offset:12296\n"
"ds_read_b128 v[144:147], v224 offset:1536\n"
"v_mfma_f32_16x16x32_bf16 a[36:39], a[228:231], v[28:31], a[36:39]\n"
"s_mul_i32 s46, s62, 663552\n"
"ds_read_b64_tr_b16 a[174:175], v218 offset:13320\n"
"v_cvt_pk_bf16_f32 v36, v184, v185\n"
"v_mfma_f32_16x16x32_bf16 a[40:43], a[232:235], v[16:19], a[40:43]\n"
"ds_read_b64_tr_b16 a[176:177], v218 offset:8448\n"
"v_cvt_pk_bf16_f32 v37, v186, v187\n"
"v_mfma_f32_16x16x32_bf16 a[40:43], a[236:239], v[24:27], a[40:43]\n"
"s_lshr_b32 s47, s43, 2\n"
"ds_read_b64_tr_b16 a[178:179], v218 offset:9472\n"
"ds_read_b128 v[148:151], v224 offset:1600\n"
"v_mfma_f32_16x16x32_bf16 a[44:47], a[232:235], v[20:23], a[44:47]\n"
"s_add_u32 m0, s51, 107312\n"
"ds_read_b64_tr_b16 a[180:181], v218 offset:12544\n"
"v_cvt_pk_bf16_f32 v38, v188, v189\n"
"v_mfma_f32_16x16x32_bf16 a[44:47], a[236:239], v[28:31], a[44:47]\n"
"s_waitcnt lgkmcnt(13)\n"
"s_mul_i32 s47, s47, 1179648\n"
"ds_read_b64_tr_b16 a[182:183], v218 offset:13568\n"
"buffer_load_dwordx4 v217, s[20:23], s50 offen offset:256 lds\n"
"v_mfma_f32_16x16x32_bf16 a[48:51], a[240:243], v[16:19], a[48:51]\n"
"s_add_u32 s50, s50, 32768\n"
"ds_read_b64_tr_b16 a[184:185], v218 offset:8456\n"
"ds_read_b128 v[152:155], v224 offset:1664\n"
"v_mfma_f32_16x16x32_bf16 a[48:51], a[244:247], v[24:27], a[48:51]\n"
"s_getpc_b64 s[32:33]\n"
"AT_pc1:\n"
"s_sub_u32 s32, s32, AT_pc1-AT_start\n"
"s_subb_u32 s33, s33, 0\n"
"ds_read_b64_tr_b16 a[186:187], v218 offset:9480\n"
"v_cvt_pk_bf16_f32 v39, v190, v191\n"
"v_mfma_f32_16x16x32_bf16 a[52:55], a[240:243], v[20:23], a[52:55]\n"
"ds_read_b64_tr_b16 a[188:189], v218 offset:12552\n"
"v_fma_mix_f32 v176, v40, v160, v168 op_sel:[0,0,0] op_sel_hi:[1,0,0]\n"
"v_mfma_f32_16x16x32_bf16 a[52:55], a[244:247], v[28:31], a[52:55]\n"
"s_and_b32 s33, s33, 0xffff\n"
"ds_read_b64_tr_b16 a[190:191], v218 offset:13576\n"
"ds_read_b128 v[156:159], v224 offset:1728\n"
"v_mfma_f32_16x16x32_bf16 a[56:59], a[248:251], v[16:19], a[56:59]\n"
"s_add_u32 m0, s51, 114744\n"
"ds_read_b64_tr_b16 a[224:225], v218 offset:8704\n"
"v_fma_mix_f32 v177, v40, v161, v169 op_sel:[1,0,0] op_sel_hi:[1,0,0]\n"
"v_mfma_f32_16x16x32_bf16 a[56:59], a[252:255], v[24:27], a[56:59]\n"
"s_mov_b32 s34, AT_kend-AT_start\n"
"ds_read_b64_tr_b16 a[226:227], v218 offset:9728\n"
"buffer_load_dwordx4 v217, s[20:23], s50 offen offset:0 lds\n"
"v_mfma_f32_16x16x32_bf16 a[60:63], a[248:251], v[20:23], a[60:63]\n"
"ds_read_b64_tr_b16 a[228:229], v218 offset:12800\n"
"v_fma_mix_f32 v178, v41, v162, v170 op_sel:[0,0,0] op_sel_hi:[1,0,0]\n"
"v_mfma_f32_16x16x32_bf16 a[60:63], a[252:255], v[28:31], a[60:63]\n"
"s_waitcnt lgkmcnt(14)\n"
"s_mov_b32 s35, 0x20000\n"
"ds_read_b64_tr_b16 a[230:231], v218 offset:13824\n"
"v_fma_mix_f32 v179, v41, v163, v171 op_sel:[1,0,0] op_sel_hi:[1,0,0]\n"
"v_mfma_f32_16x16x32_bf16 a[64:67], a[160:163], v[16:19], a[64:67]\n"
"ds_read_b64_tr_b16 a[232:233], v218 offset:8712\n"
"v_fma_mix_f32 v180, v42, v164, v172 op_sel:[0,0,0] op_sel_hi:[1,0,0]\n"
"v_mfma_f32_16x16x32_bf16 a[64:67], a[164:167], v[24:27], a[64:67]\n"
"s_lshr_b32 s62, s42, 3\n"
"ds_read_b64_tr_b16 a[234:235], v218 offset:9736\n"
"v_fma_mix_f32 v181, v42, v165, v173 op_sel:[1,0,0] op_sel_hi:[1,0,0]\n"
"v_mfma_f32_16x16x32_bf16 a[68:71], a[160:163], v[20:23], a[68:71]\n"
"s_add_u32 m0, s51, 122680\n"
"ds_read_b64_tr_b16 a[236:237], v218 offset:12808\n"
"v_fma_mix_f32 v182, v43, v166, v174 op_sel:[0,0,0] op_sel_hi:[1,0,0]\n"
"v_mfma_f32_16x16x32_bf16 a[68:71], a[164:167], v[28:31], a[68:71]\n"
"s_lshl_b32 s62, s62, 2\n"
"ds_read_b64_tr_b16 a[238:239], v218 offset:13832\n"
"buffer_load_dwordx4 v217, s[20:23], s50 offen offset:256 lds\n"
"v_mfma_f32_16x16x32_bf16 a[72:75], a[168:171], v[16:19], a[72:75]\n"
"s_add_u32 s50, s50, 32768\n"
"ds_read_b64_tr_b16 a[240:241], v218 offset:8960\n"
"v_fma_mix_f32 v183, v43, v167, v175 op_sel:[1,0,0] op_sel_hi:[1,0,0]\n"
"v_mfma_f32_16x16x32_bf16 a[72:75], a[172:175], v[24:27], a[72:75]\n"
"s_add_u32 s62, s62, s41\n"
"ds_read_b64_tr_b16 a[242:243], v218 offset:9984\n"
"v_cvt_pk_bf16_f32 v40, v176, v177\n"
"v_mfma_f32_16x16x32_bf16 a[76:79], a[168:171], v[20:23], a[76:79]\n"
"ds_read_b64_tr_b16 a[244:245], v218 offset:13056\n"
"v_cvt_pk_bf16_f32 v41, v178, v179\n"
"v_mfma_f32_16x16x32_bf16 a[76:79], a[172:175], v[28:31], a[76:79]\n"
"s_waitcnt lgkmcnt(11)\n"
"s_and_b32 s62, s62, 31\n"
"ds_read_b64_tr_b16 a[246:247], v218 offset:14080\n"
"v_cvt_pk_bf16_f32 v42, v180, v181\n"
"v_mfma_f32_16x16x32_bf16 a[80:83], a[176:179], v[16:19], a[80:83]\n"
"s_add_u32 m0, s51, 115768\n"
"ds_read_b64_tr_b16 a[248:249], v218 offset:8968\n"
"v_cvt_pk_bf16_f32 v43, v182, v183\n"
"v_mfma_f32_16x16x32_bf16 a[80:83], a[180:183], v[24:27], a[80:83]\n"
"s_lshl_b32 s48, s62, 12\n"
"ds_read_b64_tr_b16 a[250:251], v218 offset:9992\n"
"buffer_load_dwordx4 v217, s[20:23], s50 offen offset:0 lds\n"
"v_mfma_f32_16x16x32_bf16 a[84:87], a[176:179], v[20:23], a[84:87]\n"
"ds_read_b64_tr_b16 a[252:253], v218 offset:13064\n"
"v_fma_mix_f32 v184, v44, v160, v168 op_sel:[0,0,0] op_sel_hi:[1,0,0]\n"
"v_mfma_f32_16x16x32_bf16 a[84:87], a[180:183], v[28:31], a[84:87]\n"
"s_lshl_b32 s54, s44, 2\n"
"ds_read_b64_tr_b16 a[254:255], v218 offset:14088\n"
"v_fma_mix_f32 v185, v44, v161, v169 op_sel:[1,0,0] op_sel_hi:[1,0,0]\n"
"v_mfma_f32_16x16x32_bf16 v[236:239], a[184:187], v[16:19], v[236:239]\n"
"ds_read_b64_tr_b16 a[160:161], v218 offset:32784\n"
"v_fma_mix_f32 v186, v45, v162, v170 op_sel:[0,0,0] op_sel_hi:[1,0,0]\n"
"v_fma_mix_f32 v187, v45, v163, v171 op_sel:[1,0,0] op_sel_hi:[1,0,0]\n"
"v_mfma_f32_16x16x32_bf16 v[236:239], a[188:191], v[24:27], v[236:239]\n"
"s_add_u32 s54, s54, s41\n"
"ds_read_b64_tr_b16 a[162:163], v218 offset:33808\n"
"v_fma_mix_f32 v188, v46, v164, v172 op_sel:[0,0,0] op_sel_hi:[1,0,0]\n"
"v_mfma_f32_16x16x32_bf16 v[232:235], a[184:187], v[20:23], v[232:235]\n"
"s_add_u32 m0, s51, 123704\n"
"ds_read_b64_tr_b16 a[164:165], v218 offset:36880\n"
"v_fma_mix_f32 v189, v46, v165, v173 op_sel:[1,0,0] op_sel_hi:[1,0,0]\n"
"v_mfma_f32_16x16x32_bf16 v[232:235], a[188:191], v[28:31], v[232:235]\n"
"s_waitcnt lgkmcnt(11)\n"
"s_mul_i32 s54, s54, 36864\n"
"buffer_load_dwordx4 v217, s[20:23], s50 offen offset:256 lds\n"
"ds_read_b64_tr_b16 a[166:167], v218 offset:37904\n"
"v_mfma_f32_16x16x32_bf16 v[228:231], a[224:227], v[16:19], v[228:231]\n"
"s_add_u32 s50, s50, 425984\n"
"ds_read_b64_tr_b16 a[168:169], v218 offset:32792\n"
"v_fma_mix_f32 v190, v47, v166, v174 op_sel:[0,0,0] op_sel_hi:[1,0,0]\n"
"v_mfma_f32_16x16x32_bf16 v[228:231], a[228:231], v[24:27], v[228:231]\n"
"s_lshr_b32 s62, s43, 2\n"
"ds_read_b64_tr_b16 a[170:171], v218 offset:33816\n"
"v_fma_mix_f32 v191, v47, v167, v175 op_sel:[1,0,0] op_sel_hi:[1,0,0]\n"
"v_mfma_f32_16x16x32_bf16 v[220:223], a[224:227], v[20:23], v[220:223]\n"
"ds_read_b64_tr_b16 a[172:173], v218 offset:36888\n"
"ds_read_b128 v[160:163], v224 offset:1792\n"
"v_mfma_f32_16x16x32_bf16 v[220:223], a[228:231], v[28:31], v[220:223]\n"
"s_mul_i32 s62, s62, 2359296\n"
"ds_read_b64_tr_b16 a[174:175], v218 offset:37912\n"
"v_cvt_pk_bf16_f32 v44, v184, v185\n"
"v_mfma_f32_16x16x32_bf16 v[212:215], a[232:235], v[16:19], v[212:215]\n"
"ds_read_b64_tr_b16 a[176:177], v218 offset:33040\n"
"v_cvt_pk_bf16_f32 v45, v186, v187\n"
"v_mfma_f32_16x16x32_bf16 v[212:215], a[236:239], v[24:27], v[212:215]\n"
"s_add_u32 s54, s54, s62\n"
"ds_read_b64_tr_b16 a[178:179], v218 offset:34064\n"
"ds_read_b128 v[164:167], v224 offset:1856\n"
"v_mfma_f32_16x16x32_bf16 v[208:211], a[232:235], v[20:23], v[208:211]\n"
"ds_read_b64_tr_b16 a[180:181], v218 offset:37136\n"
"v_cvt_pk_bf16_f32 v46, v188, v189\n"
"v_mfma_f32_16x16x32_bf16 v[208:211], a[236:239], v[28:31], v[208:211]\n"
"s_waitcnt lgkmcnt(13)\n"
"s_add_u32 s54, s54, 139264\n"
"ds_read_b64_tr_b16 a[182:183], v218 offset:38160\n"
"v_cvt_pk_bf16_f32 v47, v190, v191\n"
"v_mfma_f32_16x16x32_bf16 v[204:207], a[240:243], v[16:19], v[204:207]\n"
"ds_read_b64_tr_b16 a[184:185], v218 offset:33048\n"
"ds_read_b128 v[168:171], v224 offset:1920\n"
"v_mfma_f32_16x16x32_bf16 v[204:207], a[244:247], v[24:27], v[204:207]\n"
"s_bfe_u32 s62, s43, 0x10001\n"
"ds_read_b64_tr_b16 a[186:187], v218 offset:34072\n"
"v_fma_mix_f32 v176, v48, v144, v152 op_sel:[0,0,0] op_sel_hi:[1,0,0]\n"
"v_mfma_f32_16x16x32_bf16 v[200:203], a[240:243], v[20:23], v[200:203]\n"
"ds_read_b64_tr_b16 a[188:189], v218 offset:37144\n"
"v_fma_mix_f32 v177, v48, v145, v153 op_sel:[1,0,0] op_sel_hi:[1,0,0]\n"
"v_mfma_f32_16x16x32_bf16 v[200:203], a[244:247], v[28:31], v[200:203]\n"
"s_mul_i32 s62, s62, 9216\n"
"ds_read_b64_tr_b16 a[190:191], v218 offset:38168\n"
"ds_read_b128 v[172:175], v224 offset:1984\n"
"v_mfma_f32_16x16x32_bf16 v[196:199], a[248:251], v[16:19], v[196:199]\n"
"ds_read_b64_tr_b16 a[224:225], v218 offset:33296\n"
"v_fma_mix_f32 v178, v49, v146, v154 op_sel:[0,0,0] op_sel_hi:[1,0,0]\n"
"v_mfma_f32_16x16x32_bf16 v[196:199], a[252:255], v[24:27], v[196:199]\n"
"s_add_u32 s54, s54, s62\n"
"ds_read_b64_tr_b16 a[226:227], v218 offset:34320\n"
"v_fma_mix_f32 v179, v49, v147, v155 op_sel:[1,0,0] op_sel_hi:[1,0,0]\n"
"v_mfma_f32_16x16x32_bf16 v[192:195], a[248:251], v[20:23], v[192:195]\n"
"ds_read_b64_tr_b16 a[228:229], v218 offset:37392\n"
"v_fma_mix_f32 v180, v50, v148, v156 op_sel:[0,0,0] op_sel_hi:[1,0,0]\n"
"v_mfma_f32_16x16x32_bf16 v[192:195], a[252:255], v[28:31], v[192:195]\n"
"s_waitcnt lgkmcnt(14)\n"
"s_and_b32 s62, s43, 1\n"
"ds_read_b64_tr_b16 a[230:231], v218 offset:38416\n"
"v_fma_mix_f32 v181, v50, v149, v157 op_sel:[1,0,0] op_sel_hi:[1,0,0]\n"
"v_mfma_f32_16x16x32_bf16 a[0:3], a[160:163], v[32:35], a[0:3]\n"
"ds_read_b64_tr_b16 a[232:233], v218 offset:33304\n"
"v_fma_mix_f32 v182, v51, v150, v158 op_sel:[0,0,0] op_sel_hi:[1,0,0]\n"
"v_mfma_f32_16x16x32_bf16 a[0:3], a[164:167], v[40:43], a[0:3]\n"
"s_lshl_b32 s49, s62, 3\n"
"ds_read_b64_tr_b16 a[234:235], v218 offset:34328\n"
"v_fma_mix_f32 v183, v51, v151, v159 op_sel:[1,0,0] op_sel_hi:[1,0,0]\n"
"v_mfma_f32_16x16x32_bf16 a[4:7], a[160:163], v[36:39], a[4:7]\n"
"s_waitcnt vmcnt(8)\n"
"s_barrier\n"
"ds_read_b64_tr_b16 a[236:237], v218 offset:37400\n"
"v_cvt_pk_bf16_f32 v48, v176, v177\n"
"v_mfma_f32_16x16x32_bf16 a[4:7], a[164:167], v[44:47], a[4:7]\n"
"s_add_u32 s49, s49, s54\n"
"ds_read_b64_tr_b16 a[238:239], v218 offset:38424\n"
"v_cvt_pk_bf16_f32 v49, v178, v179\n"
"v_mfma_f32_16x16x32_bf16 a[8:11], a[168:171], v[32:35], a[8:11]\n"
"s_mov_b32 m0, s51\n"
"AT_pg3:\n"
"s_cbranch_execz AT_hr\n"
"ds_read_b64_tr_b16 a[240:241], v218 offset:33552\n"
"v_cvt_pk_bf16_f32 v50, v180, v181\n"
"v_mfma_f32_16x16x32_bf16 a[8:11], a[172:175], v[40:43], a[8:11]\n"
"s_add_u32 s49, s49, 4096\n"
"ds_read_b64_tr_b16 a[242:243], v218 offset:34576\n"
"buffer_load_dwordx4 v217, s[20:23], s50 offen offset:0 lds\n"
"v_mfma_f32_16x16x32_bf16 a[12:15], a[168:171], v[36:39], a[12:15]\n"
"ds_read_b64_tr_b16 a[244:245], v218 offset:37648\n"
"v_cvt_pk_bf16_f32 v51, v182, v183\n"
"v_mfma_f32_16x16x32_bf16 a[12:15], a[172:175], v[44:47], a[12:15]\n"
"s_waitcnt lgkmcnt(11)\n"
"s_mul_i32 s62, s62, 5120\n"
"ds_read_b64_tr_b16 a[246:247], v218 offset:38672\n"
"v_fma_mix_f32 v184, v52, v144, v152 op_sel:[0,0,0] op_sel_hi:[1,0,0]\n"
"v_mfma_f32_16x16x32_bf16 a[16:19], a[176:179], v[32:35], a[16:19]\n"
"ds_read_b64_tr_b16 a[248:249], v218 offset:33560\n"
"v_fma_mix_f32 v185, v52, v145, v153 op_sel:[1,0,0] op_sel_hi:[1,0,0]\n"
"v_mfma_f32_16x16x32_bf16 a[16:19], a[180:183], v[40:43], a[16:19]\n"
"s_add_u32 s54, s54, s62\n"
"ds_read_b64_tr_b16 a[250:251], v218 offset:34584\n"
"v_fma_mix_f32 v186, v53, v146, v154 op_sel:[0,0,0] op_sel_hi:[1,0,0]\n"
"v_mfma_f32_16x16x32_bf16 a[20:23], a[176:179], v[36:39], a[20:23]\n"
"s_add_u32 m0, s51, 7936\n"
"ds_read_b64_tr_b16 a[252:253], v218 offset:37656\n"
"v_fma_mix_f32 v187, v53, v147, v155 op_sel:[1,0,0] op_sel_hi:[1,0,0]\n"
"v_mfma_f32_16x16x32_bf16 a[20:23], a[180:183], v[44:47], a[20:23]\n"
"s_and_b32 s62, s43, 1\n"
"ds_read_b64_tr_b16 a[254:255], v218 offset:38680\n"
"buffer_load_dwordx4 v217, s[20:23], s50 offen offset:256 lds\n"
"v_mfma_f32_16x16x32_bf16 a[24:27], a[184:187], v[32:35], a[24:27]\n"
"s_add_u32 s50, s50, 32768\n"
"ds_read_b64_tr_b16 a[160:161], v218 offset:40976\n"
"v_fma_mix_f32 v188, v54, v148, v156 op_sel:[0,0,0] op_sel_hi:[1,0,0]\n"
"v_mfma_f32_16x16x32_bf16 a[24:27], a[188:191], v[40:43], a[24:27]\n"
"s_lshl_b32 s62, s62, 10\n"
"ds_read_b64_tr_b16 a[162:163], v218 offset:42000\n"
"v_fma_mix_f32 v189, v54, v149, v157 op_sel:[1,0,0] op_sel_hi:[1,0,0]\n"
"v_mfma_f32_16x16x32_bf16 a[28:31], a[184:187], v[36:39], a[28:31]\n"
"ds_read_b64_tr_b16 a[164:165], v218 offset:45072\n"
"v_fma_mix_f32 v190, v55, v150, v158 op_sel:[0,0,0] op_sel_hi:[1,0,0]\n"
"v_mfma_f32_16x16x32_bf16 a[28:31], a[188:191], v[44:47], a[28:31]\n"
"s_waitcnt lgkmcnt(11)\n"
"s_sub_u32 s54, s54, s62\n"
"ds_read_b64_tr_b16 a[166:167], v218 offset:46096\n"
"v_fma_mix_f32 v191, v55, v151, v159 op_sel:[1,0,0] op_sel_hi:[1,0,0]\n"
"v_mfma_f32_16x16x32_bf16 a[32:35], a[224:227], v[32:35], a[32:35]\n"
"s_add_u32 m0, s51, 1024\n"
"ds_read_b64_tr_b16 a[168:169], v218 offset:40984\n"
"ds_read_b128 v[144:147], v224 offset:2048\n"
"v_mfma_f32_16x16x32_bf16 a[32:35], a[228:231], v[40:43], a[32:35]\n"
"s_add_u32 s49, s54, 4096\n"
"ds_read_b64_tr_b16 a[170:171], v218 offset:42008\n"
"buffer_load_dwordx4 v217, s[20:23], s50 offen offset:0 lds\n"
"v_mfma_f32_16x16x32_bf16 a[36:39], a[224:227], v[36:39], a[36:39]\n"
"ds_read_b64_tr_b16 a[172:173], v218 offset:45080\n"
"v_cvt_pk_bf16_f32 v52, v184, v185\n"
"v_mfma_f32_16x16x32_bf16 a[36:39], a[228:231], v[44:47], a[36:39]\n"
"s_lshl_b32 s52, s44, 4\n"
"ds_read_b64_tr_b16 a[174:175], v218 offset:46104\n"
"ds_read_b128 v[148:151], v224 offset:2112\n"
"v_mfma_f32_16x16x32_bf16 a[40:43], a[232:235], v[32:35], a[40:43]\n"
"ds_read_b64_tr_b16 a[176:177], v218 offset:41232\n"
"v_cvt_pk_bf16_f32 v53, v186, v187\n"
"v_mfma_f32_16x16x32_bf16 a[40:43], a[236:239], v[40:43], a[40:43]\n"
"s_add_u32 s52, s52, s43\n"
"ds_read_b64_tr_b16 a[178:179], v218 offset:42256\n"
"v_cvt_pk_bf16_f32 v54, v188, v189\n"
"v_mfma_f32_16x16x32_bf16 a[44:47], a[232:235], v[36:39], a[44:47]\n"
"s_add_u32 m0, s51, 8960\n"
"ds_read_b64_tr_b16 a[180:181], v218 offset:45328\n"
"ds_read_b128 v[152:155], v224 offset:2176\n"
"v_mfma_f32_16x16x32_bf16 a[44:47], a[236:239], v[44:47], a[44:47]\n"
"s_waitcnt lgkmcnt(14)\n"
"s_mul_i32 s52, s52, 18432\n"
"ds_read_b64_tr_b16 a[182:183], v218 offset:46352\n"
"buffer_load_dwordx4 v217, s[20:23], s50 offen offset:256 lds\n"
"v_mfma_f32_16x16x32_bf16 a[48:51], a[240:243], v[32:35], a[48:51]\n"
"s_add_u32 s50, s50, 32768\n"
"ds_read_b64_tr_b16 a[184:185], v218 offset:41240\n"
"v_cvt_pk_bf16_f32 v55, v190, v191\n"
"v_mfma_f32_16x16x32_bf16 a[48:51], a[244:247], v[40:43], a[48:51]\n"
"s_mul_i32 s62, s41, 5\n"
"ds_read_b64_tr_b16 a[186:187], v218 offset:42264\n"
"ds_read_b128 v[156:159], v224 offset:2240\n"
"v_mfma_f32_16x16x32_bf16 a[52:55], a[240:243], v[36:39], a[52:55]\n"
"ds_read_b64_tr_b16 a[188:189], v218 offset:45336\n"
"v_fma_mix_f32 v176, v56, v160, v168 op_sel:[0,0,0] op_sel_hi:[1,0,0]\n"
"v_mfma_f32_16x16x32_bf16 a[52:55], a[244:247], v[44:47], a[52:55]\n"
"s_min_u32 s62, s62, 13\n"
"ds_read_b64_tr_b16 a[190:191], v218 offset:46360\n"
"v_fma_mix_f32 v177, v56, v161, v169 op_sel:[1,0,0] op_sel_hi:[1,0,0]\n"
"v_mfma_f32_16x16x32_bf16 a[56:59], a[248:251], v[32:35], a[56:59]\n"
"s_add_u32 m0, s51, 16392\n"
"ds_read_b64_tr_b16 a[224:225], v218 offset:41488\n"
"v_fma_mix_f32 v178, v57, v162, v170 op_sel:[0,0,0] op_sel_hi:[1,0,0]\n"
"v_mfma_f32_16x16x32_bf16 a[56:59], a[252:255], v[40:43], a[56:59]\n"
"s_lshl_b32 s62, s62, 10\n"
"ds_read_b64_tr_b16 a[226:227], v218 offset:42512\n"
"buffer_load_dwordx4 v217, s[20:23], s50 offen offset:0 lds\n"
"v_mfma_f32_16x16x32_bf16 a[60:63], a[248:251], v[36:39], a[60:63]\n"
"ds_read_b64_tr_b16 a[228:229], v218 offset:45584\n"
"v_fma_mix_f32 v179, v57, v163, v171 op_sel:[1,0,0] op_sel_hi:[1,0,0]\n"
"v_mfma_f32_16x16x32_bf16 a[60:63], a[252:255], v[44:47], a[60:63]\n"
"s_waitcnt lgkmcnt(13)\n"
"s_add_u32 s52, s52, s62\n"
"ds_read_b64_tr_b16 a[230:231], v218 offset:46608\n"
"v_fma_mix_f32 v180, v58, v164, v172 op_sel:[0,0,0] op_sel_hi:[1,0,0]\n"
"v_mfma_f32_16x16x32_bf16 a[64:67], a[160:163], v[32:35], a[64:67]\n"
"ds_read_b64_tr_b16 a[232:233], v218 offset:41496\n"
"v_fma_mix_f32 v181, v58, v165, v173 op_sel:[1,0,0] op_sel_hi:[1,0,0]\n"
"v_mfma_f32_16x16x32_bf16 a[64:67], a[164:167], v[40:43], a[64:67]\n"
"s_add_u32 s52, s52, 26402816\n"
"ds_read_b64_tr_b16 a[234:235], v218 offset:42520\n"
"v_fma_mix_f32 v182, v59, v166, v174 op_sel:[0,0,0] op_sel_hi:[1,0,0]\n"
"v_mfma_f32_16x16x32_bf16 a[68:71], a[160:163], v[36:39], a[68:71]\n"
"s_add_u32 m0, s51, 24328\n"
"ds_read_b64_tr_b16 a[236:237], v218 offset:45592\n"
"v_fma_mix_f32 v183, v59, v167, v175 op_sel:[1,0,0] op_sel_hi:[1,0,0]\n"
"v_mfma_f32_16x16x32_bf16 a[68:71], a[164:167], v[44:47], a[68:71]\n"
"s_add_u32 s53, s52, 4096\n"
"ds_read_b64_tr_b16 a[238:239], v218 offset:46616\n"
"buffer_load_dwordx4 v217, s[20:23], s50 offen offset:256 lds\n"
"v_mfma_f32_16x16x32_bf16 a[72:75], a[168:171], v[32:35], a[72:75]\n"
"s_add_u32 s50, s50, 32768\n"
"ds_read_b64_tr_b16 a[240:241], v218 offset:41744\n"
"v_cvt_pk_bf16_f32 v56, v176, v177\n"
"v_mfma_f32_16x16x32_bf16 a[72:75], a[172:175], v[40:43], a[72:75]\n"
"ds_read_b64_tr_b16 a[242:243], v218 offset:42768\n"
"v_cvt_pk_bf16_f32 v57, v178, v179\n"
"v_mfma_f32_16x16x32_bf16 a[76:79], a[168:171], v[36:39], a[76:79]\n"
"ds_read_b64_tr_b16 a[244:245], v218 offset:45840\n"
"v_cvt_pk_bf16_f32 v58, v180, v181\n"
"v_mfma_f32_16x16x32_bf16 a[76:79], a[172:175], v[44:47], a[76:79]\n"
"s_waitcnt lgkmcnt(11)\n"
"ds_read_b64_tr_b16 a[246:247], v218 offset:46864\n"
"v_cvt_pk_bf16_f32 v59, v182, v183\n"
"v_mfma_f32_16x16x32_bf16 a[80:83], a[176:179], v[32:35], a[80:83]\n"
"s_add_u32 m0, s51, 17416\n"
"ds_read_b64_tr_b16 a[248:249], v218 offset:41752\n"
"v_fma_mix_f32 v184, v60, v160, v168 op_sel:[0,0,0] op_sel_hi:[1,0,0]\n"
"v_mfma_f32_16x16x32_bf16 a[80:83], a[180:183], v[40:43], a[80:83]\n"
"ds_read_b64_tr_b16 a[250:251], v218 offset:42776\n"
"buffer_load_dwordx4 v217, s[20:23], s50 offen offset:0 lds\n"
"v_mfma_f32_16x16x32_bf16 a[84:87], a[176:179], v[36:39], a[84:87]\n"
"ds_read_b64_tr_b16 a[252:253], v218 offset:45848\n"
"v_fma_mix_f32 v185, v60, v161, v169 op_sel:[1,0,0] op_sel_hi:[1,0,0]\n"
"v_mfma_f32_16x16x32_bf16 a[84:87], a[180:183], v[44:47], a[84:87]\n"
"ds_read_b64_tr_b16 a[254:255], v218 offset:46872\n"
"v_fma_mix_f32 v186, v61, v162, v170 op_sel:[0,0,0] op_sel_hi:[1,0,0]\n"
"v_mfma_f32_16x16x32_bf16 v[236:239], a[184:187], v[32:35], v[236:239]\n"
"ds_read_b64_tr_b16 a[160:161], v219 offset:0\n"
"v_fma_mix_f32 v187, v61, v163, v171 op_sel:[1,0,0] op_sel_hi:[1,0,0]\n"
"v_fma_mix_f32 v188, v62, v164, v172 op_sel:[0,0,0] op_sel_hi:[1,0,0]\n"
"v_mfma_f32_16x16x32_bf16 v[236:239], a[188:191], v[40:43], v[236:239]\n"
"ds_read_b64_tr_b16 a[162:163], v219 offset:1024\n"
"v_fma_mix_f32 v189, v62, v165, v173 op_sel:[1,0,0] op_sel_hi:[1,0,0]\n"
"v_mfma_f32_16x16x32_bf16 v[232:235], a[184:187], v[36:39], v[232:235]\n"
"s_add_u32 m0, s51, 25352\n"
"ds_read_b64_tr_b16 a[164:165], v219 offset:4096\n"
"v_fma_mix_f32 v190, v63, v166, v174 op_sel:[0,0,0] op_sel_hi:[1,0,0]\n"
"v_mfma_f32_16x16x32_bf16 v[232:235], a[188:191], v[44:47], v[232:235]\n"
"s_waitcnt lgkmcnt(11)\n"
"buffer_load_dwordx4 v217, s[20:23], s50 offen offset:256 lds\n"
"ds_read_b64_tr_b16 a[166:167], v219 offset:5120\n"
"v_mfma_f32_16x16x32_bf16 v[228:231], a[224:227], v[32:35], v[228:231]\n"
"s_add_u32 s50, s50, 425984\n"
"ds_read_b64_tr_b16 a[168:169], v219 offset:8\n"
"v_fma_mix_f32 v191, v63, v167, v175 op_sel:[1,0,0] op_sel_hi:[1,0,0]\n"
"v_mfma_f32_16x16x32_bf16 v[228:231], a[228:231], v[40:43], v[228:231]\n"
"ds_read_b64_tr_b16 a[170:171], v219 offset:1032\n"
"ds_read_b128 v[160:163], v224 offset:2304\n"
"v_mfma_f32_16x16x32_bf16 v[220:223], a[224:227], v[36:39], v[220:223]\n"
"ds_read_b64_tr_b16 a[172:173], v219 offset:4104\n"
"v_cvt_pk_bf16_f32 v60, v184, v185\n"
"v_mfma_f32_16x16x32_bf16 v[220:223], a[228:231], v[44:47], v[220:223]\n"
"ds_read_b64_tr_b16 a[174:175], v219 offset:5128\n"
"v_cvt_pk_bf16_f32 v61, v186, v187\n"
"v_mfma_f32_16x16x32_bf16 v[212:215], a[232:235], v[32:35], v[212:215]\n"
"ds_read_b64_tr_b16 a[176:177], v219 offset:256\n"
"ds_read_b128 v[164:167], v224 offset:2368\n"
"v_mfma_f32_16x16x32_bf16 v[212:215], a[236:239], v[40:43], v[212:215]\n"
"ds_read_b64_tr_b16 a[178:179], v219 offset:1280\n"
"v_cvt_pk_bf16_f32 v62, v188, v189\n"
"v_mfma_f32_16x16x32_bf16 v[208:211], a[232:235], v[36:39], v[208:211]\n"
"ds_read_b64_tr_b16 a[180:181], v219 offset:4352\n"
"v_cvt_pk_bf16_f32 v63, v190, v191\n"
"v_mfma_f32_16x16x32_bf16 v[208:211], a[236:239], v[44:47], v[208:211]\n"
"s_waitcnt lgkmcnt(13)\n"
"ds_read_b64_tr_b16 a[182:183], v219 offset:5376\n"
"ds_read_b128 v[168:171], v224 offset:2432\n"
"v_mfma_f32_16x16x32_bf16 v[204:207], a[240:243], v[32:35], v[204:207]\n"
"ds_read_b64_tr_b16 a[184:185], v219 offset:264\n"
"v_fma_mix_f32 v176, v64, v144, v152 op_sel:[0,0,0] op_sel_hi:[1,0,0]\n"
"v_mfma_f32_16x16x32_bf16 v[204:207], a[244:247], v[40:43], v[204:207]\n"
"ds_read_b64_tr_b16 a[186:187], v219 offset:1288\n"
"v_fma_mix_f32 v177, v64, v145, v153 op_sel:[1,0,0] op_sel_hi:[1,0,0]\n"
"v_mfma_f32_16x16x32_bf16 v[200:203], a[240:243], v[36:39], v[200:203]\n"
"ds_read_b64_tr_b16 a[188:189], v219 offset:4360\n"
"ds_read_b128 v[172:175], v224 offset:2496\n"
"v_mfma_f32_16x16x32_bf16 v[200:203], a[244:247], v[44:47], v[200:203]\n"
"ds_read_b64_tr_b16 a[190:191], v219 offset:5384\n"
"v_fma_mix_f32 v178, v65, v146, v154 op_sel:[0,0,0] op_sel_hi:[1,0,0]\n"
"v_mfma_f32_16x16x32_bf16 v[196:199], a[248:251], v[32:35], v[196:199]\n"
"ds_read_b64_tr_b16 a[224:225], v219 offset:512\n"
"v_fma_mix_f32 v179, v65, v147, v155 op_sel:[1,0,0] op_sel_hi:[1,0,0]\n"
"v_mfma_f32_16x16x32_bf16 v[196:199], a[252:255], v[40:43], v[196:199]\n"
"ds_read_b64_tr_b16 a[226:227], v219 offset:1536\n"
"v_fma_mix_f32 v180, v66, v148, v156 op_sel:[0,0,0] op_sel_hi:[1,0,0]\n"
"v_mfma_f32_16x16x32_bf16 v[192:195], a[248:251], v[36:39], v[192:195]\n"
"ds_read_b64_tr_b16 a[228:229], v219 offset:4608\n"
"v_fma_mix_f32 v181, v66, v149, v157 op_sel:[1,0,0] op_sel_hi:[1,0,0]\n"
"v_mfma_f32_16x16x32_bf16 v[192:195], a[252:255], v[44:47], v[192:195]\n"
"s_waitcnt lgkmcnt(14)\n"
"ds_read_b64_tr_b16 a[230:231], v219 offset:5632\n"
"v_fma_mix_f32 v182, v67, v150, v158 op_sel:[0,0,0] op_sel_hi:[1,0,0]\n"
"v_mfma_f32_16x16x32_bf16 a[0:3], a[160:163], v[48:51], a[0:3]\n"
"ds_read_b64_tr_b16 a[232:233], v219 offset:520\n"
"v_fma_mix_f32 v183, v67, v151, v159 op_sel:[1,0,0] op_sel_hi:[1,0,0]\n"
"v_mfma_f32_16x16x32_bf16 a[0:3], a[164:167], v[56:59], a[0:3]\n"
"s_waitcnt vmcnt(8)\n"
"ds_read_b64_tr_b16 a[234:235], v219 offset:1544\n"
"v_cvt_pk_bf16_f32 v64, v176, v177\n"
"v_mfma_f32_16x16x32_bf16 a[4:7], a[160:163], v[52:55], a[4:7]\n"
"s_barrier\n"
"ds_read_b64_tr_b16 a[236:237], v219 offset:4616\n"
"v_cvt_pk_bf16_f32 v65, v178, v179\n"
"v_mfma_f32_16x16x32_bf16 a[4:7], a[164:167], v[60:63], a[4:7]\n"
"ds_read_b64_tr_b16 a[238:239], v219 offset:5640\n"
"v_cvt_pk_bf16_f32 v66, v180, v181\n"
"v_mfma_f32_16x16x32_bf16 a[8:11], a[168:171], v[48:51], a[8:11]\n"
"s_add_u32 m0, s51, 32784\n"
"ds_read_b64_tr_b16 a[240:241], v219 offset:768\n"
"v_cvt_pk_bf16_f32 v67, v182, v183\n"
"v_mfma_f32_16x16x32_bf16 a[8:11], a[172:175], v[56:59], a[8:11]\n"
"ds_read_b64_tr_b16 a[242:243], v219 offset:1792\n"
"buffer_load_dwordx4 v217, s[20:23], s50 offen offset:0 lds\n"
"v_mfma_f32_16x16x32_bf16 a[12:15], a[168:171], v[52:55], a[12:15]\n"
"ds_read_b64_tr_b16 a[244:245], v219 offset:4864\n"
"v_fma_mix_f32 v184, v68, v144, v152 op_sel:[0,0,0] op_sel_hi:[1,0,0]\n"
"v_mfma_f32_16x16x32_bf16 a[12:15], a[172:175], v[60:63], a[12:15]\n"
"s_waitcnt lgkmcnt(11)\n"
"ds_read_b64_tr_b16 a[246:247], v219 offset:5888\n"
"v_fma_mix_f32 v185, v68, v145, v153 op_sel:[1,0,0] op_sel_hi:[1,0,0]\n"
"v_mfma_f32_16x16x32_bf16 a[16:19], a[176:179], v[48:51], a[16:19]\n"
"ds_read_b64_tr_b16 a[248:249], v219 offset:776\n"
"v_fma_mix_f32 v186, v69, v146, v154 op_sel:[0,0,0] op_sel_hi:[1,0,0]\n"
"v_mfma_f32_16x16x32_bf16 a[16:19], a[180:183], v[56:59], a[16:19]\n"
"ds_read_b64_tr_b16 a[250:251], v219 offset:1800\n"
"v_fma_mix_f32 v187, v69, v147, v155 op_sel:[1,0,0] op_sel_hi:[1,0,0]\n"
"v_mfma_f32_16x16x32_bf16 a[20:23], a[176:179], v[52:55], a[20:23]\n"
"s_add_u32 m0, s51, 40720\n"
"ds_read_b64_tr_b16 a[252:253], v219 offset:4872\n"
"v_fma_mix_f32 v188, v70, v148, v156 op_sel:[0,0,0] op_sel_hi:[1,0,0]\n"
"v_mfma_f32_16x16x32_bf16 a[20:23], a[180:183], v[60:63], a[20:23]\n"
"ds_read_b64_tr_b16 a[254:255], v219 offset:5896\n"
"buffer_load_dwordx4 v217, s[20:23], s50 offen offset:256 lds\n"
"v_mfma_f32_16x16x32_bf16 a[24:27], a[184:187], v[48:51], a[24:27]\n"
"s_add_u32 s50, s50, 32768\n"
"ds_read_b64_tr_b16 a[160:161], v219 offset:8192\n"
"v_fma_mix_f32 v189, v70, v149, v157 op_sel:[1,0,0] op_sel_hi:[1,0,0]\n"
"v_mfma_f32_16x16x32_bf16 a[24:27], a[188:191], v[56:59], a[24:27]\n"
"ds_read_b64_tr_b16 a[162:163], v219 offset:9216\n"
"v_fma_mix_f32 v190, v71, v150, v158 op_sel:[0,0,0] op_sel_hi:[1,0,0]\n"
"v_mfma_f32_16x16x32_bf16 a[28:31], a[184:187], v[52:55], a[28:31]\n"
"ds_read_b64_tr_b16 a[164:165], v219 offset:12288\n"
"v_fma_mix_f32 v191, v71, v151, v159 op_sel:[1,0,0] op_sel_hi:[1,0,0]\n"
"v_mfma_f32_16x16x32_bf16 a[28:31], a[188:191], v[60:63], a[28:31]\n"
"s_waitcnt lgkmcnt(11)\n"
"ds_read_b64_tr_b16 a[166:167], v219 offset:13312\n"
"ds_read_b128 v[144:147], v224 offset:2560\n"
"v_mfma_f32_16x16x32_bf16 a[32:35], a[224:227], v[48:51], a[32:35]\n"
"s_add_u32 m0, s51, 33808\n"
"ds_read_b64_tr_b16 a[168:169], v219 offset:8200\n"
"v_cvt_pk_bf16_f32 v68, v184, v185\n"
"v_mfma_f32_16x16x32_bf16 a[32:35], a[228:231], v[56:59], a[32:35]\n"
"ds_read_b64_tr_b16 a[170:171], v219 offset:9224\n"
"buffer_load_dwordx4 v217, s[20:23], s50 offen offset:0 lds\n"
"v_mfma_f32_16x16x32_bf16 a[36:39], a[224:227], v[52:55], a[36:39]\n"
"ds_read_b64_tr_b16 a[172:173], v219 offset:12296\n"
"ds_read_b128 v[148:151], v224 offset:2624\n"
"v_mfma_f32_16x16x32_bf16 a[36:39], a[228:231], v[60:63], a[36:39]\n"
"ds_read_b64_tr_b16 a[174:175], v219 offset:13320\n"
"v_cvt_pk_bf16_f32 v69, v186, v187\n"
"v_mfma_f32_16x16x32_bf16 a[40:43], a[232:235], v[48:51], a[40:43]\n"
"ds_read_b64_tr_b16 a[176:177], v219 offset:8448\n"
"v_cvt_pk_bf16_f32 v70, v188, v189\n"
"v_mfma_f32_16x16x32_bf16 a[40:43], a[236:239], v[56:59], a[40:43]\n"
"ds_read_b64_tr_b16 a[178:179], v219 offset:9472\n"
"ds_read_b128 v[152:155], v224 offset:2688\n"
"v_mfma_f32_16x16x32_bf16 a[44:47], a[232:235], v[52:55], a[44:47]\n"
"s_add_u32 m0, s51, 41744\n"
"ds_read_b64_tr_b16 a[180:181], v219 offset:12544\n"
"v_cvt_pk_bf16_f32 v71, v190, v191\n"
"v_mfma_f32_16x16x32_bf16 a[44:47], a[236:239], v[60:63], a[44:47]\n"
"s_waitcnt lgkmcnt(14)\n"
"ds_read_b64_tr_b16 a[182:183], v219 offset:13568\n"
"buffer_load_dwordx4 v217, s[20:23], s50 offen offset:256 lds\n"
"v_mfma_f32_16x16x32_bf16 a[48:51], a[240:243], v[48:51], a[48:51]\n"
"s_add_u32 s50, s50, 32768\n"
"ds_read_b64_tr_b16 a[184:185], v219 offset:8456\n"
"ds_read_b128 v[156:159], v224 offset:2752\n"
"v_mfma_f32_16x16x32_bf16 a[48:51], a[244:247], v[56:59], a[48:51]\n"
"ds_read_b64_tr_b16 a[186:187], v219 offset:9480\n"
"v_fma_mix_f32 v176, v72, v160, v168 op_sel:[0,0,0] op_sel_hi:[1,0,0]\n"
"v_mfma_f32_16x16x32_bf16 a[52:55], a[240:243], v[52:55], a[52:55]\n"
"ds_read_b64_tr_b16 a[188:189], v219 offset:12552\n"
"v_fma_mix_f32 v177, v72, v161, v169 op_sel:[1,0,0] op_sel_hi:[1,0,0]\n"
"v_mfma_f32_16x16x32_bf16 a[52:55], a[244:247], v[60:63], a[52:55]\n"
"ds_read_b64_tr_b16 a[190:191], v219 offset:13576\n"
"v_fma_mix_f32 v178, v73, v162, v170 op_sel:[0,0,0] op_sel_hi:[1,0,0]\n"
"v_mfma_f32_16x16x32_bf16 a[56:59], a[248:251], v[48:51], a[56:59]\n"
"s_add_u32 m0, s51, 49176\n"
"ds_read_b64_tr_b16 a[224:225], v219 offset:8704\n"
"v_fma_mix_f32 v179, v73, v163, v171 op_sel:[1,0,0] op_sel_hi:[1,0,0]\n"
"v_mfma_f32_16x16x32_bf16 a[56:59], a[252:255], v[56:59], a[56:59]\n"
"ds_read_b64_tr_b16 a[226:227], v219 offset:9728\n"
"buffer_load_dwordx4 v217, s[20:23], s50 offen offset:0 lds\n"
"v_mfma_f32_16x16x32_bf16 a[60:63], a[248:251], v[52:55], a[60:63]\n"
"ds_read_b64_tr_b16 a[228:229], v219 offset:12800\n"
"v_fma_mix_f32 v180, v74, v164, v172 op_sel:[0,0,0] op_sel_hi:[1,0,0]\n"
"v_mfma_f32_16x16x32_bf16 a[60:63], a[252:255], v[60:63], a[60:63]\n"
"s_waitcnt lgkmcnt(13)\n"
"ds_read_b64_tr_b16 a[230:231], v219 offset:13824\n"
"v_fma_mix_f32 v181, v74, v165, v173 op_sel:[1,0,0] op_sel_hi:[1,0,0]\n"
"v_mfma_f32_16x16x32_bf16 a[64:67], a[160:163], v[48:51], a[64:67]\n"
"ds_read_b64_tr_b16 a[232:233], v219 offset:8712\n"
"v_fma_mix_f32 v182, v75, v166, v174 op_sel:[0,0,0] op_sel_hi:[1,0,0]\n"
"v_mfma_f32_16x16x32_bf16 a[64:67], a[164:167], v[56:59], a[64:67]\n"
"ds_read_b64_tr_b16 a[234:235], v219 offset:9736\n"
"v_fma_mix_f32 v183, v75, v167, v175 op_sel:[1,0,0] op_sel_hi:[1,0,0]\n"
"v_mfma_f32_16x16x32_bf16 a[68:71], a[160:163], v[52:55], a[68:71]\n"
"s_add_u32 m0, s51, 57112\n"
"ds_read_b64_tr_b16 a[236:237], v219 offset:12808\n"
"v_cvt_pk_bf16_f32 v72, v176, v177\n"
"v_mfma_f32_16x16x32_bf16 a[68:71], a[164:167], v[60:63], a[68:71]\n"
"ds_read_b64_tr_b16 a[238:239], v219 offset:13832\n"
"buffer_load_dwordx4 v217, s[20:23], s50 offen offset:256 lds\n"
"v_mfma_f32_16x16x32_bf16 a[72:75], a[168:171], v[48:51], a[72:75]\n"
"s_add_u32 s50, s50, 32768\n"
"ds_read_b64_tr_b16 a[240:241], v219 offset:8960\n"
"v_cvt_pk_bf16_f32 v73, v178, v179\n"
"v_mfma_f32_16x16x32_bf16 a[72:75], a[172:175], v[56:59], a[72:75]\n"
"ds_read_b64_tr_b16 a[242:243], v219 offset:9984\n"
"v_cvt_pk_bf16_f32 v74, v180, v181\n"
"v_mfma_f32_16x16x32_bf16 a[76:79], a[168:171], v[52:55], a[76:79]\n"
"ds_read_b64_tr_b16 a[244:245], v219 offset:13056\n"
"v_cvt_pk_bf16_f32 v75, v182, v183\n"
"v_mfma_f32_16x16x32_bf16 a[76:79], a[172:175], v[60:63], a[76:79]\n"
"s_waitcnt lgkmcnt(11)\n"
"ds_read_b64_tr_b16 a[246:247], v219 offset:14080\n"
"v_fma_mix_f32 v184, v76, v160, v168 op_sel:[0,0,0] op_sel_hi:[1,0,0]\n"
"v_mfma_f32_16x16x32_bf16 a[80:83], a[176:179], v[48:51], a[80:83]\n"
"s_add_u32 m0, s51, 50200\n"
"ds_read_b64_tr_b16 a[248:249], v219 offset:8968\n"
"v_fma_mix_f32 v185, v76, v161, v169 op_sel:[1,0,0] op_sel_hi:[1,0,0]\n"
"v_mfma_f32_16x16x32_bf16 a[80:83], a[180:183], v[56:59], a[80:83]\n"
"ds_read_b64_tr_b16 a[250:251], v219 offset:9992\n"
"buffer_load_dwordx4 v217, s[20:23], s50 offen offset:0 lds\n"
"v_mfma_f32_16x16x32_bf16 a[84:87], a[176:179], v[52:55], a[84:87]\n"
"ds_read_b64_tr_b16 a[252:253], v219 offset:13064\n"
"v_fma_mix_f32 v186, v77, v162, v170 op_sel:[0,0,0] op_sel_hi:[1,0,0]\n"
"v_mfma_f32_16x16x32_bf16 a[84:87], a[180:183], v[60:63], a[84:87]\n"
"ds_read_b64_tr_b16 a[254:255], v219 offset:14088\n"
"v_fma_mix_f32 v187, v77, v163, v171 op_sel:[1,0,0] op_sel_hi:[1,0,0]\n"
"v_mfma_f32_16x16x32_bf16 v[236:239], a[184:187], v[48:51], v[236:239]\n"
"ds_read_b64_tr_b16 a[160:161], v219 offset:32784\n"
"v_fma_mix_f32 v188, v78, v164, v172 op_sel:[0,0,0] op_sel_hi:[1,0,0]\n"
"v_fma_mix_f32 v189, v78, v165, v173 op_sel:[1,0,0] op_sel_hi:[1,0,0]\n"
"v_mfma_f32_16x16x32_bf16 v[236:239], a[188:191], v[56:59], v[236:239]\n"
"ds_read_b64_tr_b16 a[162:163], v219 offset:33808\n"
"v_fma_mix_f32 v190, v79, v166, v174 op_sel:[0,0,0] op_sel_hi:[1,0,0]\n"
"v_mfma_f32_16x16x32_bf16 v[232:235], a[184:187], v[52:55], v[232:235]\n"
"s_add_u32 m0, s51, 58136\n"
"ds_read_b64_tr_b16 a[164:165], v219 offset:36880\n"
"v_fma_mix_f32 v191, v79, v167, v175 op_sel:[1,0,0] op_sel_hi:[1,0,0]\n"
"v_mfma_f32_16x16x32_bf16 v[232:235], a[188:191], v[60:63], v[232:235]\n"
"s_waitcnt lgkmcnt(11)\n"
"buffer_load_dwordx4 v217, s[20:23], s50 offen offset:256 lds\n"
"ds_read_b64_tr_b16 a[166:167], v219 offset:37904\n"
"v_mfma_f32_16x16x32_bf16 v[228:231], a[224:227], v[48:51], v[228:231]\n"
"s_add_u32 s50, s50, 425984\n"
"ds_read_b64_tr_b16 a[168:169], v219 offset:32792\n"
"ds_read_b128 v[160:163], v224 offset:2816\n"
"v_mfma_f32_16x16x32_bf16 v[228:231], a[228:231], v[56:59], v[228:231]\n"
"ds_read_b64_tr_b16 a[170:171], v219 offset:33816\n"
"v_cvt_pk_bf16_f32 v76, v184, v185\n"
"v_mfma_f32_16x16x32_bf16 v[220:223], a[224:227], v[52:55], v[220:223]\n"
"ds_read_b64_tr_b16 a[172:173], v219 offset:36888\n"
"v_cvt_pk_bf16_f32 v77, v186, v187\n"
"v_mfma_f32_16x16x32_bf16 v[220:223], a[228:231], v[60:63], v[220:223]\n"
"ds_read_b64_tr_b16 a[174:175], v219 offset:37912\n"
"ds_read_b128 v[164:167], v224 offset:2880\n"
"v_mfma_f32_16x16x32_bf16 v[212:215], a[232:235], v[48:51], v[212:215]\n"
"ds_read_b64_tr_b16 a[176:177], v219 offset:33040\n"
"v_cvt_pk_bf16_f32 v78, v188, v189\n"
"v_mfma_f32_16x16x32_bf16 v[212:215], a[236:239], v[56:59], v[212:215]\n"
"ds_read_b64_tr_b16 a[178:179], v219 offset:34064\n"
"v_cvt_pk_bf16_f32 v79, v190, v191\n"
"v_mfma_f32_16x16x32_bf16 v[208:211], a[232:235], v[52:55], v[208:211]\n"
"ds_read_b64_tr_b16 a[180:181], v219 offset:37136\n"
"ds_read_b128 v[168:171], v224 offset:2944\n"
"v_mfma_f32_16x16x32_bf16 v[208:211], a[236:239], v[60:63], v[208:211]\n"
"s_waitcnt lgkmcnt(14)\n"
"ds_read_b64_tr_b16 a[182:183], v219 offset:38160\n"
"v_fma_mix_f32 v176, v80, v144, v152 op_sel:[0,0,0] op_sel_hi:[1,0,0]\n"
"v_mfma_f32_16x16x32_bf16 v[204:207], a[240:243], v[48:51], v[204:207]\n"
"ds_read_b64_tr_b16 a[184:185], v219 offset:33048\n"
"v_fma_mix_f32 v177, v80, v145, v153 op_sel:[1,0,0] op_sel_hi:[1,0,0]\n"
"v_mfma_f32_16x16x32_bf16 v[204:207], a[244:247], v[56:59], v[204:207]\n"
"ds_read_b64_tr_b16 a[186:187], v219 offset:34072\n"
"ds_read_b128 v[172:175], v224 offset:3008\n"
"v_mfma_f32_16x16x32_bf16 v[200:203], a[240:243], v[52:55], v[200:203]\n"
"ds_read_b64_tr_b16 a[188:189], v219 offset:37144\n"
"v_fma_mix_f32 v178, v81, v146, v154 op_sel:[0,0,0] op_sel_hi:[1,0,0]\n"
"v_mfma_f32_16x16x32_bf16 v[200:203], a[244:247], v[60:63], v[200:203]\n"
"ds_read_b64_tr_b16 a[190:191], v219 offset:38168\n"
"v_fma_mix_f32 v179, v81, v147, v155 op_sel:[1,0,0] op_sel_hi:[1,0,0]\n"
"v_mfma_f32_16x16x32_bf16 v[196:199], a[248:251], v[48:51], v[196:199]\n"
"ds_read_b64_tr_b16 a[224:225], v219 offset:33296\n"
"v_fma_mix_f32 v180, v82, v148, v156 op_sel:[0,0,0] op_sel_hi:[1,0,0]\n"
"v_mfma_f32_16x16x32_bf16 v[196:199], a[252:255], v[56:59], v[196:199]\n"
"ds_read_b64_tr_b16 a[226:227], v219 offset:34320\n"
"v_fma_mix_f32 v181, v82, v149, v157 op_sel:[1,0,0] op_sel_hi:[1,0,0]\n"
"v_mfma_f32_16x16x32_bf16 v[192:195], a[248:251], v[52:55], v[192:195]\n"
"ds_read_b64_tr_b16 a[228:229], v219 offset:37392\n"
"v_fma_mix_f32 v182, v83, v150, v158 op_sel:[0,0,0] op_sel_hi:[1,0,0]\n"
"v_mfma_f32_16x16x32_bf16 v[192:195], a[252:255], v[60:63], v[192:195]\n"
"s_waitcnt lgkmcnt(13)\n"
"ds_read_b64_tr_b16 a[230:231], v219 offset:38416\n"
"v_fma_mix_f32 v183, v83, v151, v159 op_sel:[1,0,0] op_sel_hi:[1,0,0]\n"
"v_mfma_f32_16x16x32_bf16 a[0:3], a[160:163], v[64:67], a[0:3]\n"
"ds_read_b64_tr_b16 a[232:233], v219 offset:33304\n"
"v_cvt_pk_bf16_f32 v80, v176, v177\n"
"v_mfma_f32_16x16x32_bf16 a[0:3], a[164:167], v[72:75], a[0:3]\n"
"s_waitcnt vmcnt(8)\n"
"ds_read_b64_tr_b16 a[234:235], v219 offset:34328\n"
"v_cvt_pk_bf16_f32 v81, v178, v179\n"
"v_mfma_f32_16x16x32_bf16 a[4:7], a[160:163], v[68:71], a[4:7]\n"
"s_barrier\n"
"ds_read_b64_tr_b16 a[236:237], v219 offset:37400\n"
"v_cvt_pk_bf16_f32 v82, v180, v181\n"
"v_mfma_f32_16x16x32_bf16 a[4:7], a[164:167], v[76:79], a[4:7]\n"
"ds_read_b64_tr_b16 a[238:239], v219 offset:38424\n"
"v_cvt_pk_bf16_f32 v83, v182, v183\n"
"v_mfma_f32_16x16x32_bf16 a[8:11], a[168:171], v[64:67], a[8:11]\n"
"s_add_u32 m0, s51, 65568\n"
"ds_read_b64_tr_b16 a[240:241], v219 offset:33552\n"
"v_fma_mix_f32 v184, v84, v144, v152 op_sel:[0,0,0] op_sel_hi:[1,0,0]\n"
"v_mfma_f32_16x16x32_bf16 a[8:11], a[172:175], v[72:75], a[8:11]\n"
"ds_read_b64_tr_b16 a[242:243], v219 offset:34576\n"
"buffer_load_dwordx4 v217, s[20:23], s50 offen offset:0 lds\n"
"v_mfma_f32_16x16x32_bf16 a[12:15], a[168:171], v[68:71], a[12:15]\n"
"ds_read_b64_tr_b16 a[244:245], v219 offset:37648\n"
"v_fma_mix_f32 v185, v84, v145, v153 op_sel:[1,0,0] op_sel_hi:[1,0,0]\n"
"v_mfma_f32_16x16x32_bf16 a[12:15], a[172:175], v[76:79], a[12:15]\n"
"s_waitcnt lgkmcnt(11)\n"
"ds_read_b64_tr_b16 a[246:247], v219 offset:38672\n"
"v_fma_mix_f32 v186, v85, v146, v154 op_sel:[0,0,0] op_sel_hi:[1,0,0]\n"
"v_mfma_f32_16x16x32_bf16 a[16:19], a[176:179], v[64:67], a[16:19]\n"
"ds_read_b64_tr_b16 a[248:249], v219 offset:33560\n"
"v_fma_mix_f32 v187, v85, v147, v155 op_sel:[1,0,0] op_sel_hi:[1,0,0]\n"
"v_mfma_f32_16x16x32_bf16 a[16:19], a[180:183], v[72:75], a[16:19]\n"
"ds_read_b64_tr_b16 a[250:251], v219 offset:34584\n"
"v_fma_mix_f32 v188, v86, v148, v156 op_sel:[0,0,0] op_sel_hi:[1,0,0]\n"
"v_mfma_f32_16x16x32_bf16 a[20:23], a[176:179], v[68:71], a[20:23]\n"
"s_add_u32 m0, s51, 73504\n"
"ds_read_b64_tr_b16 a[252:253], v219 offset:37656\n"
"v_fma_mix_f32 v189, v86, v149, v157 op_sel:[1,0,0] op_sel_hi:[1,0,0]\n"
"v_mfma_f32_16x16x32_bf16 a[20:23], a[180:183], v[76:79], a[20:23]\n"
"ds_read_b64_tr_b16 a[254:255], v219 offset:38680\n"
"buffer_load_dwordx4 v217, s[20:23], s50 offen offset:256 lds\n"
"v_mfma_f32_16x16x32_bf16 a[24:27], a[184:187], v[64:67], a[24:27]\n"
"s_add_u32 s50, s50, 32768\n"
"ds_read_b64_tr_b16 a[160:161], v219 offset:40976\n"
"v_fma_mix_f32 v190, v87, v150, v158 op_sel:[0,0,0] op_sel_hi:[1,0,0]\n"
"v_mfma_f32_16x16x32_bf16 a[24:27], a[188:191], v[72:75], a[24:27]\n"
"ds_read_b64_tr_b16 a[162:163], v219 offset:42000\n"
"v_fma_mix_f32 v191, v87, v151, v159 op_sel:[1,0,0] op_sel_hi:[1,0,0]\n"
"v_mfma_f32_16x16x32_bf16 a[28:31], a[184:187], v[68:71], a[28:31]\n"
"ds_read_b64_tr_b16 a[164:165], v219 offset:45072\n"
"ds_read_b128 v[144:147], v224 offset:3072\n"
"v_mfma_f32_16x16x32_bf16 a[28:31], a[188:191], v[76:79], a[28:31]\n"
"s_waitcnt lgkmcnt(12)\n"
"ds_read_b64_tr_b16 a[166:167], v219 offset:46096\n"
"v_cvt_pk_bf16_f32 v84, v184, v185\n"
"v_mfma_f32_16x16x32_bf16 a[32:35], a[224:227], v[64:67], a[32:35]\n"
"s_add_u32 m0, s51, 66592\n"
"ds_read_b64_tr_b16 a[168:169], v219 offset:40984\n"
"v_cvt_pk_bf16_f32 v85, v186, v187\n"
"v_mfma_f32_16x16x32_bf16 a[32:35], a[228:231], v[72:75], a[32:35]\n"
"ds_read_b64_tr_b16 a[170:171], v219 offset:42008\n"
"buffer_load_dwordx4 v217, s[20:23], s50 offen offset:0 lds\n"
"v_mfma_f32_16x16x32_bf16 a[36:39], a[224:227], v[68:71], a[36:39]\n"
"ds_read_b64_tr_b16 a[172:173], v219 offset:45080\n"
"ds_read_b128 v[148:151], v224 offset:3136\n"
"v_mfma_f32_16x16x32_bf16 a[36:39], a[228:231], v[76:79], a[36:39]\n"
"ds_read_b64_tr_b16 a[174:175], v219 offset:46104\n"
"v_cvt_pk_bf16_f32 v86, v188, v189\n"
"v_mfma_f32_16x16x32_bf16 a[40:43], a[232:235], v[64:67], a[40:43]\n"
"ds_read_b64_tr_b16 a[176:177], v219 offset:41232\n"
"v_cvt_pk_bf16_f32 v87, v190, v191\n"
"v_mfma_f32_16x16x32_bf16 a[40:43], a[236:239], v[72:75], a[40:43]\n"
"ds_read_b64_tr_b16 a[178:179], v219 offset:42256\n"
"ds_read_b128 v[152:155], v224 offset:3200\n"
"v_mfma_f32_16x16x32_bf16 a[44:47], a[232:235], v[68:71], a[44:47]\n"
"s_add_u32 m0, s51, 74528\n"
"ds_read_b64_tr_b16 a[180:181], v219 offset:45328\n"
"v_fma_mix_f32 v176, v88, v160, v168 op_sel:[0,0,0] op_sel_hi:[1,0,0]\n"
"v_mfma_f32_16x16x32_bf16 a[44:47], a[236:239], v[76:79], a[44:47]\n"
"s_waitcnt lgkmcnt(14)\n"
"ds_read_b64_tr_b16 a[182:183], v219 offset:46352\n"
"buffer_load_dwordx4 v217, s[20:23], s50 offen offset:256 lds\n"
"v_mfma_f32_16x16x32_bf16 a[48:51], a[240:243], v[64:67], a[48:51]\n"
"s_add_u32 s50, s50, 32768\n"
"ds_read_b64_tr_b16 a[184:185], v219 offset:41240\n"
"ds_read_b128 v[156:159], v224 offset:3264\n"
"v_mfma_f32_16x16x32_bf16 a[48:51], a[244:247], v[72:75], a[48:51]\n"
"ds_read_b64_tr_b16 a[186:187], v219 offset:42264\n"
"v_fma_mix_f32 v177, v88, v161, v169 op_sel:[1,0,0] op_sel_hi:[1,0,0]\n"
"v_mfma_f32_16x16x32_bf16 a[52:55], a[240:243], v[68:71], a[52:55]\n"
"ds_read_b64_tr_b16 a[188:189], v219 offset:45336\n"
"v_fma_mix_f32 v178, v89, v162, v170 op_sel:[0,0,0] op_sel_hi:[1,0,0]\n"
"v_mfma_f32_16x16x32_bf16 a[52:55], a[244:247], v[76:79], a[52:55]\n"
"AT_pg4:\n"
"s_cbranch_execz AT_hr\n"
"ds_read_b64_tr_b16 a[190:191], v219 offset:46360\n"
"v_fma_mix_f32 v179, v89, v163, v171 op_sel:[1,0,0] op_sel_hi:[1,0,0]\n"
"v_mfma_f32_16x16x32_bf16 a[56:59], a[248:251], v[64:67], a[56:59]\n"
"s_add_u32 m0, s51, 81960\n"
"ds_read_b64_tr_b16 a[224:225], v219 offset:41488\n"
"v_fma_mix_f32 v180, v90, v164, v172 op_sel:[0,0,0] op_sel_hi:[1,0,0]\n"
"v_mfma_f32_16x16x32_bf16 a[56:59], a[252:255], v[72:75], a[56:59]\n"
"ds_read_b64_tr_b16 a[226:227], v219 offset:42512\n"
"buffer_load_dwordx4 v217, s[20:23], s50 offen offset:0 lds\n"
"v_mfma_f32_16x16x32_bf16 a[60:63], a[248:251], v[68:71], a[60:63]\n"
"ds_read_b64_tr_b16 a[228:229], v219 offset:45584\n"
"v_fma_mix_f32 v181, v90, v165, v173 op_sel:[1,0,0] op_sel_hi:[1,0,0]\n"
"v_mfma_f32_16x16x32_bf16 a[60:63], a[252:255], v[76:79], a[60:63]\n"
"s_waitcnt lgkmcnt(13)\n"
"ds_read_b64_tr_b16 a[230:231], v219 offset:46608\n"
"v_fma_mix_f32 v182, v91, v166, v174 op_sel:[0,0,0] op_sel_hi:[1,0,0]\n"
"v_mfma_f32_16x16x32_bf16 a[64:67], a[160:163], v[64:67], a[64:67]\n"
"ds_read_b64_tr_b16 a[232:233], v219 offset:41496\n"
"v_fma_mix_f32 v183, v91, v167, v175 op_sel:[1,0,0] op_sel_hi:[1,0,0]\n"
"v_mfma_f32_16x16x32_bf16 a[64:67], a[164:167], v[72:75], a[64:67]\n"
"ds_read_b64_tr_b16 a[234:235], v219 offset:42520\n"
"v_cvt_pk_bf16_f32 v88, v176, v177\n"
"v_mfma_f32_16x16x32_bf16 a[68:71], a[160:163], v[68:71], a[68:71]\n"
"s_add_u32 m0, s51, 89896\n"
"ds_read_b64_tr_b16 a[236:237], v219 offset:45592\n"
"v_cvt_pk_bf16_f32 v89, v178, v179\n"
"v_mfma_f32_16x16x32_bf16 a[68:71], a[164:167], v[76:79], a[68:71]\n"
"ds_read_b64_tr_b16 a[238:239], v219 offset:46616\n"
"buffer_load_dwordx4 v217, s[20:23], s50 offen offset:256 lds\n"
"v_mfma_f32_16x16x32_bf16 a[72:75], a[168:171], v[64:67], a[72:75]\n"
"s_add_u32 s50, s50, 32768\n"
"ds_read_b64_tr_b16 a[240:241], v219 offset:41744\n"
"v_cvt_pk_bf16_f32 v90, v180, v181\n"
"v_mfma_f32_16x16x32_bf16 a[72:75], a[172:175], v[72:75], a[72:75]\n"
"ds_read_b64_tr_b16 a[242:243], v219 offset:42768\n"
"v_cvt_pk_bf16_f32 v91, v182, v183\n"
"v_mfma_f32_16x16x32_bf16 a[76:79], a[168:171], v[68:71], a[76:79]\n"
"ds_read_b64_tr_b16 a[244:245], v219 offset:45840\n"
"v_fma_mix_f32 v184, v92, v160, v168 op_sel:[0,0,0] op_sel_hi:[1,0,0]\n"
"v_mfma_f32_16x16x32_bf16 a[76:79], a[172:175], v[76:79], a[76:79]\n"
"s_waitcnt lgkmcnt(11)\n"
"ds_read_b64_tr_b16 a[246:247], v219 offset:46864\n"
"v_fma_mix_f32 v185, v92, v161, v169 op_sel:[1,0,0] op_sel_hi:[1,0,0]\n"
"v_mfma_f32_16x16x32_bf16 a[80:83], a[176:179], v[64:67], a[80:83]\n"
"s_add_u32 m0, s51, 82984\n"
"ds_read_b64_tr_b16 a[248:249], v219 offset:41752\n"
"v_fma_mix_f32 v186, v93, v162, v170 op_sel:[0,0,0] op_sel_hi:[1,0,0]\n"
"v_mfma_f32_16x16x32_bf16 a[80:83], a[180:183], v[72:75], a[80:83]\n"
"ds_read_b64_tr_b16 a[250:251], v219 offset:42776\n"
"buffer_load_dwordx4 v217, s[20:23], s50 offen offset:0 lds\n"
"v_mfma_f32_16x16x32_bf16 a[84:87], a[176:179], v[68:71], a[84:87]\n"
"ds_read_b64_tr_b16 a[252:253], v219 offset:45848\n"
"v_fma_mix_f32 v187, v93, v163, v171 op_sel:[1,0,0] op_sel_hi:[1,0,0]\n"
"v_mfma_f32_16x16x32_bf16 a[84:87], a[180:183], v[76:79], a[84:87]\n"
"ds_read_b64_tr_b16 a[254:255], v219 offset:46872\n"
"v_fma_mix_f32 v188, v94, v164, v172 op_sel:[0,0,0] op_sel_hi:[1,0,0]\n"
"v_mfma_f32_16x16x32_bf16 v[236:239], a[184:187], v[64:67], v[236:239]\n"
"ds_read_b64_tr_b16 a[160:161], v218 offset:0\n"
"v_fma_mix_f32 v189, v94, v165, v173 op_sel:[1,0,0] op_sel_hi:[1,0,0]\n"
"v_fma_mix_f32 v190, v95, v166, v174 op_sel:[0,0,0] op_sel_hi:[1,0,0]\n"
"v_mfma_f32_16x16x32_bf16 v[236:239], a[188:191], v[72:75], v[236:239]\n"
"ds_read_b64_tr_b16 a[162:163], v218 offset:1024\n"
"v_fma_mix_f32 v191, v95, v167, v175 op_sel:[1,0,0] op_sel_hi:[1,0,0]\n"
"v_mfma_f32_16x16x32_bf16 v[232:235], a[184:187], v[68:71], v[232:235]\n"
"s_add_u32 m0, s51, 90920\n"
"ds_read_b64_tr_b16 a[164:165], v218 offset:4096\n"
"ds_read_b128 v[160:163], v224 offset:3328\n"
"v_mfma_f32_16x16x32_bf16 v[232:235], a[188:191], v[76:79], v[232:235]\n"
"s_waitcnt lgkmcnt(12)\n"
"buffer_load_dwordx4 v217, s[20:23], s50 offen offset:256 lds\n"
"ds_read_b64_tr_b16 a[166:167], v218 offset:5120\n"
"v_mfma_f32_16x16x32_bf16 v[228:231], a[224:227], v[64:67], v[228:231]\n"
"s_add_u32 s50, s50, 425984\n"
"ds_read_b64_tr_b16 a[168:169], v218 offset:8\n"
"v_cvt_pk_bf16_f32 v92, v184, v185\n"
"v_mfma_f32_16x16x32_bf16 v[228:231], a[228:231], v[72:75], v[228:231]\n"
"ds_read_b64_tr_b16 a[170:171], v218 offset:1032\n"
"ds_read_b128 v[164:167], v224 offset:3392\n"
"v_mfma_f32_16x16x32_bf16 v[220:223], a[224:227], v[68:71], v[220:223]\n"
"ds_read_b64_tr_b16 a[172:173], v218 offset:4104\n"
"v_cvt_pk_bf16_f32 v93, v186, v187\n"
"v_mfma_f32_16x16x32_bf16 v[220:223], a[228:231], v[76:79], v[220:223]\n"
"ds_read_b64_tr_b16 a[174:175], v218 offset:5128\n"
"v_cvt_pk_bf16_f32 v94, v188, v189\n"
"v_mfma_f32_16x16x32_bf16 v[212:215], a[232:235], v[64:67], v[212:215]\n"
"ds_read_b64_tr_b16 a[176:177], v218 offset:256\n"
"ds_read_b128 v[168:171], v224 offset:3456\n"
"v_mfma_f32_16x16x32_bf16 v[212:215], a[236:239], v[72:75], v[212:215]\n"
"ds_read_b64_tr_b16 a[178:179], v218 offset:1280\n"
"v_cvt_pk_bf16_f32 v95, v190, v191\n"
"v_mfma_f32_16x16x32_bf16 v[208:211], a[232:235], v[68:71], v[208:211]\n"
"ds_read_b64_tr_b16 a[180:181], v218 offset:4352\n"
"v_fma_mix_f32 v176, v96, v144, v152 op_sel:[0,0,0] op_sel_hi:[1,0,0]\n"
"v_mfma_f32_16x16x32_bf16 v[208:211], a[236:239], v[76:79], v[208:211]\n"
"s_waitcnt lgkmcnt(14)\n"
"ds_read_b64_tr_b16 a[182:183], v218 offset:5376\n"
"ds_read_b128 v[172:175], v224 offset:3520\n"
"v_mfma_f32_16x16x32_bf16 v[204:207], a[240:243], v[64:67], v[204:207]\n"
"ds_read_b64_tr_b16 a[184:185], v218 offset:264\n"
"v_fma_mix_f32 v177, v96, v145, v153 op_sel:[1,0,0] op_sel_hi:[1,0,0]\n"
"v_mfma_f32_16x16x32_bf16 v[204:207], a[244:247], v[72:75], v[204:207]\n"
"ds_read_b64_tr_b16 a[186:187], v218 offset:1288\n"
"v_fma_mix_f32 v178, v97, v146, v154 op_sel:[0,0,0] op_sel_hi:[1,0,0]\n"
"v_mfma_f32_16x16x32_bf16 v[200:203], a[240:243], v[68:71], v[200:203]\n"
"ds_read_b64_tr_b16 a[188:189], v218 offset:4360\n"
"v_fma_mix_f32 v179, v97, v147, v155 op_sel:[1,0,0] op_sel_hi:[1,0,0]\n"
"v_mfma_f32_16x16x32_bf16 v[200:203], a[244:247], v[76:79], v[200:203]\n"
"ds_read_b64_tr_b16 a[190:191], v218 offset:5384\n"
"v_fma_mix_f32 v180, v98, v148, v156 op_sel:[0,0,0] op_sel_hi:[1,0,0]\n"
"v_mfma_f32_16x16x32_bf16 v[196:199], a[248:251], v[64:67], v[196:199]\n"
"ds_read_b64_tr_b16 a[224:225], v218 offset:512\n"
"v_fma_mix_f32 v181, v98, v149, v157 op_sel:[1,0,0] op_sel_hi:[1,0,0]\n"
"v_mfma_f32_16x16x32_bf16 v[196:199], a[252:255], v[72:75], v[196:199]\n"
"ds_read_b64_tr_b16 a[226:227], v218 offset:1536\n"
"v_fma_mix_f32 v182, v99, v150, v158 op_sel:[0,0,0] op_sel_hi:[1,0,0]\n"
"v_mfma_f32_16x16x32_bf16 v[192:195], a[248:251], v[68:71], v[192:195]\n"
"ds_read_b64_tr_b16 a[228:229], v218 offset:4608\n"
"v_fma_mix_f32 v183, v99, v151, v159 op_sel:[1,0,0] op_sel_hi:[1,0,0]\n"
"v_mfma_f32_16x16x32_bf16 v[192:195], a[252:255], v[76:79], v[192:195]\n"
"s_waitcnt lgkmcnt(13)\n"
"ds_read_b64_tr_b16 a[230:231], v218 offset:5632\n"
"v_cvt_pk_bf16_f32 v96, v176, v177\n"
"v_mfma_f32_16x16x32_bf16 a[0:3], a[160:163], v[80:83], a[0:3]\n"
"ds_read_b64_tr_b16 a[232:233], v218 offset:520\n"
"v_cvt_pk_bf16_f32 v97, v178, v179\n"
"v_mfma_f32_16x16x32_bf16 a[0:3], a[164:167], v[88:91], a[0:3]\n"
"s_waitcnt vmcnt(8)\n"
"ds_read_b64_tr_b16 a[234:235], v218 offset:1544\n"
"v_cvt_pk_bf16_f32 v98, v180, v181\n"
"v_mfma_f32_16x16x32_bf16 a[4:7], a[160:163], v[84:87], a[4:7]\n"
"s_barrier\n"
"ds_read_b64_tr_b16 a[236:237], v218 offset:4616\n"
"v_cvt_pk_bf16_f32 v99, v182, v183\n"
"v_mfma_f32_16x16x32_bf16 a[4:7], a[164:167], v[92:95], a[4:7]\n"
"ds_read_b64_tr_b16 a[238:239], v218 offset:5640\n"
"v_fma_mix_f32 v184, v100, v144, v152 op_sel:[0,0,0] op_sel_hi:[1,0,0]\n"
"v_mfma_f32_16x16x32_bf16 a[8:11], a[168:171], v[80:83], a[8:11]\n"
"s_add_u32 m0, s51, 98352\n"
"ds_read_b64_tr_b16 a[240:241], v218 offset:768\n"
"v_fma_mix_f32 v185, v100, v145, v153 op_sel:[1,0,0] op_sel_hi:[1,0,0]\n"
"v_mfma_f32_16x16x32_bf16 a[8:11], a[172:175], v[88:91], a[8:11]\n"
"ds_read_b64_tr_b16 a[242:243], v218 offset:1792\n"
"buffer_load_dwordx4 v217, s[20:23], s50 offen offset:0 lds\n"
"v_mfma_f32_16x16x32_bf16 a[12:15], a[168:171], v[84:87], a[12:15]\n"
"ds_read_b64_tr_b16 a[244:245], v218 offset:4864\n"
"v_fma_mix_f32 v186, v101, v146, v154 op_sel:[0,0,0] op_sel_hi:[1,0,0]\n"
"v_mfma_f32_16x16x32_bf16 a[12:15], a[172:175], v[92:95], a[12:15]\n"
"s_waitcnt lgkmcnt(11)\n"
"ds_read_b64_tr_b16 a[246:247], v218 offset:5888\n"
"v_fma_mix_f32 v187, v101, v147, v155 op_sel:[1,0,0] op_sel_hi:[1,0,0]\n"
"v_mfma_f32_16x16x32_bf16 a[16:19], a[176:179], v[80:83], a[16:19]\n"
"ds_read_b64_tr_b16 a[248:249], v218 offset:776\n"
"v_fma_mix_f32 v188, v102, v148, v156 op_sel:[0,0,0] op_sel_hi:[1,0,0]\n"
"v_mfma_f32_16x16x32_bf16 a[16:19], a[180:183], v[88:91], a[16:19]\n"
"ds_read_b64_tr_b16 a[250:251], v218 offset:1800\n"
"v_fma_mix_f32 v189, v102, v149, v157 op_sel:[1,0,0] op_sel_hi:[1,0,0]\n"
"v_mfma_f32_16x16x32_bf16 a[20:23], a[176:179], v[84:87], a[20:23]\n"
"s_add_u32 m0, s51, 106288\n"
"ds_read_b64_tr_b16 a[252:253], v218 offset:4872\n"
"v_fma_mix_f32 v190, v103, v150, v158 op_sel:[0,0,0] op_sel_hi:[1,0,0]\n"
"v_mfma_f32_16x16x32_bf16 a[20:23], a[180:183], v[92:95], a[20:23]\n"
"ds_read_b64_tr_b16 a[254:255], v218 offset:5896\n"
"buffer_load_dwordx4 v217, s[20:23], s50 offen offset:256 lds\n"
"v_mfma_f32_16x16x32_bf16 a[24:27], a[184:187], v[80:83], a[24:27]\n"
"s_add_u32 s50, s50, 32768\n"
"ds_read_b64_tr_b16 a[160:161], v218 offset:8192\n"
"v_fma_mix_f32 v191, v103, v151, v159 op_sel:[1,0,0] op_sel_hi:[1,0,0]\n"
"v_mfma_f32_16x16x32_bf16 a[24:27], a[188:191], v[88:91], a[24:27]\n"
"ds_read_b64_tr_b16 a[162:163], v218 offset:9216\n"
"ds_read_b128 v[144:147], v224 offset:3584\n"
"v_mfma_f32_16x16x32_bf16 a[28:31], a[184:187], v[84:87], a[28:31]\n"
"ds_read_b64_tr_b16 a[164:165], v218 offset:12288\n"
"v_cvt_pk_bf16_f32 v100, v184, v185\n"
"v_mfma_f32_16x16x32_bf16 a[28:31], a[188:191], v[92:95], a[28:31]\n"
"s_waitcnt lgkmcnt(12)\n"
"ds_read_b64_tr_b16 a[166:167], v218 offset:13312\n"
"v_cvt_pk_bf16_f32 v101, v186, v187\n"
"v_mfma_f32_16x16x32_bf16 a[32:35], a[224:227], v[80:83], a[32:35]\n"
"s_add_u32 m0, s51, 99376\n"
"ds_read_b64_tr_b16 a[168:169], v218 offset:8200\n"
"ds_read_b128 v[148:151], v224 offset:3648\n"
"v_mfma_f32_16x16x32_bf16 a[32:35], a[228:231], v[88:91], a[32:35]\n"
"ds_read_b64_tr_b16 a[170:171], v218 offset:9224\n"
"buffer_load_dwordx4 v217, s[20:23], s50 offen offset:0 lds\n"
"v_mfma_f32_16x16x32_bf16 a[36:39], a[224:227], v[84:87], a[36:39]\n"
"ds_read_b64_tr_b16 a[172:173], v218 offset:12296\n"
"v_cvt_pk_bf16_f32 v102, v188, v189\n"
"v_mfma_f32_16x16x32_bf16 a[36:39], a[228:231], v[92:95], a[36:39]\n"
"ds_read_b64_tr_b16 a[174:175], v218 offset:13320\n"
"ds_read_b128 v[152:155], v224 offset:3712\n"
"v_mfma_f32_16x16x32_bf16 a[40:43], a[232:235], v[80:83], a[40:43]\n"
"ds_read_b64_tr_b16 a[176:177], v218 offset:8448\n"
"v_cvt_pk_bf16_f32 v103, v190, v191\n"
"v_mfma_f32_16x16x32_bf16 a[40:43], a[236:239], v[88:91], a[40:43]\n"
"ds_read_b64_tr_b16 a[178:179], v218 offset:9472\n"
"v_fma_mix_f32 v176, v104, v160, v168 op_sel:[0,0,0] op_sel_hi:[1,0,0]\n"
"v_mfma_f32_16x16x32_bf16 a[44:47], a[232:235], v[84:87], a[44:47]\n"
"s_add_u32 m0, s51, 107312\n"
"ds_read_b64_tr_b16 a[180:181], v218 offset:12544\n"
"ds_read_b128 v[156:159], v224 offset:3776\n"
"v_mfma_f32_16x16x32_bf16 a[44:47], a[236:239], v[92:95], a[44:47]\n"
"s_waitcnt lgkmcnt(14)\n"
"ds_read_b64_tr_b16 a[182:183], v218 offset:13568\n"
"buffer_load_dwordx4 v217, s[20:23], s50 offen offset:256 lds\n"
"v_mfma_f32_16x16x32_bf16 a[48:51], a[240:243], v[80:83], a[48:51]\n"
"s_add_u32 s50, s50, 32768\n"
"ds_read_b64_tr_b16 a[184:185], v218 offset:8456\n"
"v_fma_mix_f32 v177, v104, v161, v169 op_sel:[1,0,0] op_sel_hi:[1,0,0]\n"
"v_mfma_f32_16x16x32_bf16 a[48:51], a[244:247], v[88:91], a[48:51]\n"
"ds_read_b64_tr_b16 a[186:187], v218 offset:9480\n"
"v_fma_mix_f32 v178, v105, v162, v170 op_sel:[0,0,0] op_sel_hi:[1,0,0]\n"
"v_mfma_f32_16x16x32_bf16 a[52:55], a[240:243], v[84:87], a[52:55]\n"
"ds_read_b64_tr_b16 a[188:189], v218 offset:12552\n"
"v_fma_mix_f32 v179, v105, v163, v171 op_sel:[1,0,0] op_sel_hi:[1,0,0]\n"
"v_mfma_f32_16x16x32_bf16 a[52:55], a[244:247], v[92:95], a[52:55]\n"
"ds_read_b64_tr_b16 a[190:191], v218 offset:13576\n"
"v_fma_mix_f32 v180, v106, v164, v172 op_sel:[0,0,0] op_sel_hi:[1,0,0]\n"
"v_mfma_f32_16x16x32_bf16 a[56:59], a[248:251], v[80:83], a[56:59]\n"
"s_add_u32 m0, s51, 114744\n"
"ds_read_b64_tr_b16 a[224:225], v218 offset:8704\n"
"v_fma_mix_f32 v181, v106, v165, v173 op_sel:[1,0,0] op_sel_hi:[1,0,0]\n"
"v_mfma_f32_16x16x32_bf16 a[56:59], a[252:255], v[88:91], a[56:59]\n"
"ds_read_b64_tr_b16 a[226:227], v218 offset:9728\n"
"buffer_load_dwordx4 v217, s[20:23], s50 offen offset:0 lds\n"
"v_mfma_f32_16x16x32_bf16 a[60:63], a[248:251], v[84:87], a[60:63]\n"
"ds_read_b64_tr_b16 a[228:229], v218 offset:12800\n"
"v_fma_mix_f32 v182, v107, v166, v174 op_sel:[0,0,0] op_sel_hi:[1,0,0]\n"
"v_mfma_f32_16x16x32_bf16 a[60:63], a[252:255], v[92:95], a[60:63]\n"
"s_waitcnt lgkmcnt(12)\n"
"ds_read_b64_tr_b16 a[230:231], v218 offset:13824\n"
"v_fma_mix_f32 v183, v107, v167, v175 op_sel:[1,0,0] op_sel_hi:[1,0,0]\n"
"v_mfma_f32_16x16x32_bf16 a[64:67], a[160:163], v[80:83], a[64:67]\n"
"ds_read_b64_tr_b16 a[232:233], v218 offset:8712\n"
"v_cvt_pk_bf16_f32 v104, v176, v177\n"
"v_mfma_f32_16x16x32_bf16 a[64:67], a[164:167], v[88:91], a[64:67]\n"
"ds_read_b64_tr_b16 a[234:235], v218 offset:9736\n"
"v_cvt_pk_bf16_f32 v105, v178, v179\n"
"v_mfma_f32_16x16x32_bf16 a[68:71], a[160:163], v[84:87], a[68:71]\n"
"s_add_u32 m0, s51, 122680\n"
"ds_read_b64_tr_b16 a[236:237], v218 offset:12808\n"
"v_cvt_pk_bf16_f32 v106, v180, v181\n"
"v_mfma_f32_16x16x32_bf16 a[68:71], a[164:167], v[92:95], a[68:71]\n"
"ds_read_b64_tr_b16 a[238:239], v218 offset:13832\n"
"buffer_load_dwordx4 v217, s[20:23], s50 offen offset:256 lds\n"
"v_mfma_f32_16x16x32_bf16 a[72:75], a[168:171], v[80:83], a[72:75]\n"
"s_add_u32 s50, s50, 32768\n"
"ds_read_b64_tr_b16 a[240:241], v218 offset:8960\n"
"v_cvt_pk_bf16_f32 v107, v182, v183\n"
"v_mfma_f32_16x16x32_bf16 a[72:75], a[172:175], v[88:91], a[72:75]\n"
"ds_read_b64_tr_b16 a[242:243], v218 offset:9984\n"
"v_fma_mix_f32 v184, v108, v160, v168 op_sel:[0,0,0] op_sel_hi:[1,0,0]\n"
"v_mfma_f32_16x16x32_bf16 a[76:79], a[168:171], v[84:87], a[76:79]\n"
"ds_read_b64_tr_b16 a[244:245], v218 offset:13056\n"
"v_fma_mix_f32 v185, v108, v161, v169 op_sel:[1,0,0] op_sel_hi:[1,0,0]\n"
"v_mfma_f32_16x16x32_bf16 a[76:79], a[172:175], v[92:95], a[76:79]\n"
"s_waitcnt lgkmcnt(11)\n"
"ds_read_b64_tr_b16 a[246:247], v218 offset:14080\n"
"v_fma_mix_f32 v186, v109, v162, v170 op_sel:[0,0,0] op_sel_hi:[1,0,0]\n"
"v_mfma_f32_16x16x32_bf16 a[80:83], a[176:179], v[80:83], a[80:83]\n"
"s_add_u32 m0, s51, 115768\n"
"ds_read_b64_tr_b16 a[248:249], v218 offset:8968\n"
"v_fma_mix_f32 v187, v109, v163, v171 op_sel:[1,0,0] op_sel_hi:[1,0,0]\n"
"v_mfma_f32_16x16x32_bf16 a[80:83], a[180:183], v[88:91], a[80:83]\n"
"ds_read_b64_tr_b16 a[250:251], v218 offset:9992\n"
"buffer_load_dwordx4 v217, s[20:23], s50 offen offset:0 lds\n"
"v_mfma_f32_16x16x32_bf16 a[84:87], a[176:179], v[84:87], a[84:87]\n"
"ds_read_b64_tr_b16 a[252:253], v218 offset:13064\n"
"v_fma_mix_f32 v188, v110, v164, v172 op_sel:[0,0,0] op_sel_hi:[1,0,0]\n"
"v_mfma_f32_16x16x32_bf16 a[84:87], a[180:183], v[92:95], a[84:87]\n"
"ds_read_b64_tr_b16 a[254:255], v218 offset:14088\n"
"v_fma_mix_f32 v189, v110, v165, v173 op_sel:[1,0,0] op_sel_hi:[1,0,0]\n"
"v_mfma_f32_16x16x32_bf16 v[236:239], a[184:187], v[80:83], v[236:239]\n"
"ds_read_b64_tr_b16 a[160:161], v218 offset:32784\n"
"v_fma_mix_f32 v190, v111, v166, v174 op_sel:[0,0,0] op_sel_hi:[1,0,0]\n"
"v_fma_mix_f32 v191, v111, v167, v175 op_sel:[1,0,0] op_sel_hi:[1,0,0]\n"
"v_mfma_f32_16x16x32_bf16 v[236:239], a[188:191], v[88:91], v[236:239]\n"
"ds_read_b64_tr_b16 a[162:163], v218 offset:33808\n"
"ds_read_b128 v[160:163], v224 offset:3840\n"
"v_mfma_f32_16x16x32_bf16 v[232:235], a[184:187], v[84:87], v[232:235]\n"
"s_add_u32 m0, s51, 123704\n"
"ds_read_b64_tr_b16 a[164:165], v218 offset:36880\n"
"v_cvt_pk_bf16_f32 v108, v184, v185\n"
"v_mfma_f32_16x16x32_bf16 v[232:235], a[188:191], v[92:95], v[232:235]\n"
"s_waitcnt lgkmcnt(12)\n"
"buffer_load_dwordx4 v217, s[20:23], s50 offen offset:256 lds\n"
"ds_read_b64_tr_b16 a[166:167], v218 offset:37904\n"
"v_mfma_f32_16x16x32_bf16 v[228:231], a[224:227], v[80:83], v[228:231]\n"
"s_add_u32 s50, s50, 425984\n"
"ds_read_b64_tr_b16 a[168:169], v218 offset:32792\n"
"ds_read_b128 v[164:167], v224 offset:3904\n"
"v_mfma_f32_16x16x32_bf16 v[228:231], a[228:231], v[88:91], v[228:231]\n"
"ds_read_b64_tr_b16 a[170:171], v218 offset:33816\n"
"v_cvt_pk_bf16_f32 v109, v186, v187\n"
"v_mfma_f32_16x16x32_bf16 v[220:223], a[224:227], v[84:87], v[220:223]\n"
"ds_read_b64_tr_b16 a[172:173], v218 offset:36888\n"
"v_cvt_pk_bf16_f32 v110, v188, v189\n"
"v_mfma_f32_16x16x32_bf16 v[220:223], a[228:231], v[92:95], v[220:223]\n"
"ds_read_b64_tr_b16 a[174:175], v218 offset:37912\n"
"ds_read_b128 v[168:171], v224 offset:3968\n"
"v_mfma_f32_16x16x32_bf16 v[212:215], a[232:235], v[80:83], v[212:215]\n"
"ds_read_b64_tr_b16 a[176:177], v218 offset:33040\n"
"v_cvt_pk_bf16_f32 v111, v190, v191\n"
"v_mfma_f32_16x16x32_bf16 v[212:215], a[236:239], v[88:91], v[212:215]\n"
"ds_read_b64_tr_b16 a[178:179], v218 offset:34064\n"
"v_fma_mix_f32 v176, v112, v144, v152 op_sel:[0,0,0] op_sel_hi:[1,0,0]\n"
"v_mfma_f32_16x16x32_bf16 v[208:211], a[232:235], v[84:87], v[208:211]\n"
"ds_read_b64_tr_b16 a[180:181], v218 offset:37136\n"
"ds_read_b128 v[172:175], v224 offset:4032\n"
"v_mfma_f32_16x16x32_bf16 v[208:211], a[236:239], v[92:95], v[208:211]\n"
"s_waitcnt lgkmcnt(14)\n"
"ds_read_b64_tr_b16 a[182:183], v218 offset:38160\n"
"v_fma_mix_f32 v177, v112, v145, v153 op_sel:[1,0,0] op_sel_hi:[1,0,0]\n"
"v_mfma_f32_16x16x32_bf16 v[204:207], a[240:243], v[80:83], v[204:207]\n"
"ds_read_b64_tr_b16 a[184:185], v218 offset:33048\n"
"v_fma_mix_f32 v178, v113, v146, v154 op_sel:[0,0,0] op_sel_hi:[1,0,0]\n"
"v_mfma_f32_16x16x32_bf16 v[204:207], a[244:247], v[88:91], v[204:207]\n"
"ds_read_b64_tr_b16 a[186:187], v218 offset:34072\n"
"v_fma_mix_f32 v179, v113, v147, v155 op_sel:[1,0,0] op_sel_hi:[1,0,0]\n"
"v_mfma_f32_16x16x32_bf16 v[200:203], a[240:243], v[84:87], v[200:203]\n"
"ds_read_b64_tr_b16 a[188:189], v218 offset:37144\n"
"v_fma_mix_f32 v180, v114, v148, v156 op_sel:[0,0,0] op_sel_hi:[1,0,0]\n"
"v_mfma_f32_16x16x32_bf16 v[200:203], a[244:247], v[92:95], v[200:203]\n"
"ds_read_b64_tr_b16 a[190:191], v218 offset:38168\n"
"v_fma_mix_f32 v181, v114, v149, v157 op_sel:[1,0,0] op_sel_hi:[1,0,0]\n"
"v_mfma_f32_16x16x32_bf16 v[196:199], a[248:251], v[80:83], v[196:199]\n"
"ds_read_b64_tr_b16 a[224:225], v218 offset:33296\n"
"v_fma_mix_f32 v182, v115, v150, v158 op_sel:[0,0,0] op_sel_hi:[1,0,0]\n"
"v_mfma_f32_16x16x32_bf16 v[196:199], a[252:255], v[88:91], v[196:199]\n"
"ds_read_b64_tr_b16 a[226:227], v218 offset:34320\n"
"v_fma_mix_f32 v183, v115, v151, v159 op_sel:[1,0,0] op_sel_hi:[1,0,0]\n"
"v_mfma_f32_16x16x32_bf16 v[192:195], a[248:251], v[84:87], v[192:195]\n"
"ds_read_b64_tr_b16 a[228:229], v218 offset:37392\n"
"v_cvt_pk_bf16_f32 v112, v176, v177\n"
"v_mfma_f32_16x16x32_bf16 v[192:195], a[252:255], v[92:95], v[192:195]\n"
"s_waitcnt lgkmcnt(12)\n"
"ds_read_b64_tr_b16 a[230:231], v218 offset:38416\n"
"v_cvt_pk_bf16_f32 v113, v178, v179\n"
"v_mfma_f32_16x16x32_bf16 a[0:3], a[160:163], v[96:99], a[0:3]\n"
"ds_read_b64_tr_b16 a[232:233], v218 offset:33304\n"
"v_cvt_pk_bf16_f32 v114, v180, v181\n"
"v_mfma_f32_16x16x32_bf16 a[0:3], a[164:167], v[104:107], a[0:3]\n"
"s_waitcnt vmcnt(8)\n"
"ds_read_b64_tr_b16 a[234:235], v218 offset:34328\n"
"v_cvt_pk_bf16_f32 v115, v182, v183\n"
"v_mfma_f32_16x16x32_bf16 a[4:7], a[160:163], v[100:103], a[4:7]\n"
"s_barrier\n"
"ds_read_b64_tr_b16 a[236:237], v218 offset:37400\n"
"v_fma_mix_f32 v184, v116, v144, v152 op_sel:[0,0,0] op_sel_hi:[1,0,0]\n"
"v_mfma_f32_16x16x32_bf16 a[4:7], a[164:167], v[108:111], a[4:7]\n"
"ds_read_b64_tr_b16 a[238:239], v218 offset:38424\n"
"v_fma_mix_f32 v185, v116, v145, v153 op_sel:[1,0,0] op_sel_hi:[1,0,0]\n"
"v_mfma_f32_16x16x32_bf16 a[8:11], a[168:171], v[96:99], a[8:11]\n"
"s_mov_b32 m0, s86\n"
"ds_read_b64_tr_b16 a[240:241], v218 offset:33552\n"
"v_fma_mix_f32 v186, v117, v146, v154 op_sel:[0,0,0] op_sel_hi:[1,0,0]\n"
"v_mfma_f32_16x16x32_bf16 a[8:11], a[172:175], v[104:107], a[8:11]\n"
"ds_read_b64_tr_b16 a[242:243], v218 offset:34576\n"
"buffer_load_dwordx4 v246, s[8:11], s84 offen offset:0 lds\n"
"v_mfma_f32_16x16x32_bf16 a[12:15], a[168:171], v[100:103], a[12:15]\n"
"ds_read_b64_tr_b16 a[244:245], v218 offset:37648\n"
"v_fma_mix_f32 v187, v117, v147, v155 op_sel:[1,0,0] op_sel_hi:[1,0,0]\n"
"v_mfma_f32_16x16x32_bf16 a[12:15], a[172:175], v[108:111], a[12:15]\n"
"s_waitcnt lgkmcnt(11)\n"
"ds_read_b64_tr_b16 a[246:247], v218 offset:38672\n"
"v_fma_mix_f32 v188, v118, v148, v156 op_sel:[0,0,0] op_sel_hi:[1,0,0]\n"
"v_mfma_f32_16x16x32_bf16 a[16:19], a[176:179], v[96:99], a[16:19]\n"
"ds_read_b64_tr_b16 a[248:249], v218 offset:33560\n"
"v_fma_mix_f32 v189, v118, v149, v157 op_sel:[1,0,0] op_sel_hi:[1,0,0]\n"
"v_mfma_f32_16x16x32_bf16 a[16:19], a[180:183], v[104:107], a[16:19]\n"
"ds_read_b64_tr_b16 a[250:251], v218 offset:34584\n"
"v_fma_mix_f32 v190, v119, v150, v158 op_sel:[0,0,0] op_sel_hi:[1,0,0]\n"
"v_mfma_f32_16x16x32_bf16 a[20:23], a[176:179], v[100:103], a[20:23]\n"
"ds_read_b64_tr_b16 a[252:253], v218 offset:37656\n"
"v_fma_mix_f32 v191, v119, v151, v159 op_sel:[1,0,0] op_sel_hi:[1,0,0]\n"
"v_mfma_f32_16x16x32_bf16 a[20:23], a[180:183], v[108:111], a[20:23]\n"
"ds_read_b64_tr_b16 a[254:255], v218 offset:38680\n"
"buffer_load_dwordx4 v246, s[8:11], s84 offen offset:1024 lds\n"
"v_mfma_f32_16x16x32_bf16 a[24:27], a[184:187], v[96:99], a[24:27]\n"
"ds_read_b64_tr_b16 a[160:161], v218 offset:40976\n"
"ds_read_b128 v[144:147], v224 offset:4096\n"
"v_mfma_f32_16x16x32_bf16 a[24:27], a[188:191], v[104:107], a[24:27]\n"
"ds_read_b64_tr_b16 a[162:163], v218 offset:42000\n"
"v_cvt_pk_bf16_f32 v116, v184, v185\n"
"v_mfma_f32_16x16x32_bf16 a[28:31], a[184:187], v[100:103], a[28:31]\n"
"ds_read_b64_tr_b16 a[164:165], v218 offset:45072\n"
"v_cvt_pk_bf16_f32 v117, v186, v187\n"
"v_mfma_f32_16x16x32_bf16 a[28:31], a[188:191], v[108:111], a[28:31]\n"
"s_waitcnt lgkmcnt(12)\n"
"ds_read_b64_tr_b16 a[166:167], v218 offset:46096\n"
"ds_read_b128 v[148:151], v224 offset:4160\n"
"v_mfma_f32_16x16x32_bf16 a[32:35], a[224:227], v[96:99], a[32:35]\n"
"ds_read_b64_tr_b16 a[168:169], v218 offset:40984\n"
"v_cvt_pk_bf16_f32 v118, v188, v189\n"
"v_mfma_f32_16x16x32_bf16 a[32:35], a[228:231], v[104:107], a[32:35]\n"
"ds_read_b64_tr_b16 a[170:171], v218 offset:42008\n"
"buffer_load_dwordx4 v247, s[8:11], s84 offen offset:2048 lds\n"
"v_mfma_f32_16x16x32_bf16 a[36:39], a[224:227], v[100:103], a[36:39]\n"
"ds_read_b64_tr_b16 a[172:173], v218 offset:45080\n"
"ds_read_b128 v[152:155], v224 offset:4224\n"
"v_mfma_f32_16x16x32_bf16 a[36:39], a[228:231], v[108:111], a[36:39]\n"
"ds_read_b64_tr_b16 a[174:175], v218 offset:46104\n"
"v_cvt_pk_bf16_f32 v119, v190, v191\n"
"v_mfma_f32_16x16x32_bf16 a[40:43], a[232:235], v[96:99], a[40:43]\n"
"ds_read_b64_tr_b16 a[176:177], v218 offset:41232\n"
"v_fma_mix_f32 v176, v120, v160, v168 op_sel:[0,0,0] op_sel_hi:[1,0,0]\n"
"v_mfma_f32_16x16x32_bf16 a[40:43], a[236:239], v[104:107], a[40:43]\n"
"ds_read_b64_tr_b16 a[178:179], v218 offset:42256\n"
"ds_read_b128 v[156:159], v224 offset:4288\n"
"v_mfma_f32_16x16x32_bf16 a[44:47], a[232:235], v[100:103], a[44:47]\n"
"ds_read_b64_tr_b16 a[180:181], v218 offset:45328\n"
"v_fma_mix_f32 v177, v120, v161, v169 op_sel:[1,0,0] op_sel_hi:[1,0,0]\n"
"v_mfma_f32_16x16x32_bf16 a[44:47], a[236:239], v[108:111], a[44:47]\n"
"s_waitcnt lgkmcnt(14)\n"
"ds_read_b64_tr_b16 a[182:183], v218 offset:46352\n"
"buffer_load_dwordx4 v247, s[8:11], s84 offen offset:3072 lds\n"
"v_mfma_f32_16x16x32_bf16 a[48:51], a[240:243], v[96:99], a[48:51]\n"
"ds_read_b64_tr_b16 a[184:185], v218 offset:41240\n"
"v_fma_mix_f32 v178, v121, v162, v170 op_sel:[0,0,0] op_sel_hi:[1,0,0]\n"
"v_mfma_f32_16x16x32_bf16 a[48:51], a[244:247], v[104:107], a[48:51]\n"
"ds_read_b64_tr_b16 a[186:187], v218 offset:42264\n"
"v_fma_mix_f32 v179, v121, v163, v171 op_sel:[1,0,0] op_sel_hi:[1,0,0]\n"
"v_mfma_f32_16x16x32_bf16 a[52:55], a[240:243], v[100:103], a[52:55]\n"
"ds_read_b64_tr_b16 a[188:189], v218 offset:45336\n"
"v_fma_mix_f32 v180, v122, v164, v172 op_sel:[0,0,0] op_sel_hi:[1,0,0]\n"
"v_mfma_f32_16x16x32_bf16 a[52:55], a[244:247], v[108:111], a[52:55]\n"
"ds_read_b64_tr_b16 a[190:191], v218 offset:46360\n"
"v_fma_mix_f32 v181, v122, v165, v173 op_sel:[1,0,0] op_sel_hi:[1,0,0]\n"
"v_mfma_f32_16x16x32_bf16 a[56:59], a[248:251], v[96:99], a[56:59]\n"
"s_add_u32 m0, s86, 16384\n"
"ds_read_b64_tr_b16 a[224:225], v218 offset:41488\n"
"v_fma_mix_f32 v182, v123, v166, v174 op_sel:[0,0,0] op_sel_hi:[1,0,0]\n"
"v_mfma_f32_16x16x32_bf16 a[56:59], a[252:255], v[104:107], a[56:59]\n"
"ds_read_b64_tr_b16 a[226:227], v218 offset:42512\n"
"buffer_load_dwordx4 v246, s[12:15], s84 offen offset:0 lds\n"
"v_mfma_f32_16x16x32_bf16 a[60:63], a[248:251], v[100:103], a[60:63]\n"
"ds_read_b64_tr_b16 a[228:229], v218 offset:45584\n"
"v_fma_mix_f32 v183, v123, v167, v175 op_sel:[1,0,0] op_sel_hi:[1,0,0]\n"
"v_mfma_f32_16x16x32_bf16 a[60:63], a[252:255], v[108:111], a[60:63]\n"
"s_waitcnt lgkmcnt(12)\n"
"ds_read_b64_tr_b16 a[230:231], v218 offset:46608\n"
"v_cvt_pk_bf16_f32 v120, v176, v177\n"
"v_mfma_f32_16x16x32_bf16 a[64:67], a[160:163], v[96:99], a[64:67]\n"
"ds_read_b64_tr_b16 a[232:233], v218 offset:41496\n"
"v_cvt_pk_bf16_f32 v121, v178, v179\n"
"v_mfma_f32_16x16x32_bf16 a[64:67], a[164:167], v[104:107], a[64:67]\n"
"ds_read_b64_tr_b16 a[234:235], v218 offset:42520\n"
"v_cvt_pk_bf16_f32 v122, v180, v181\n"
"v_mfma_f32_16x16x32_bf16 a[68:71], a[160:163], v[100:103], a[68:71]\n"
"ds_read_b64_tr_b16 a[236:237], v218 offset:45592\n"
"v_cvt_pk_bf16_f32 v123, v182, v183\n"
"v_mfma_f32_16x16x32_bf16 a[68:71], a[164:167], v[108:111], a[68:71]\n"
"ds_read_b64_tr_b16 a[238:239], v218 offset:46616\n"
"buffer_load_dwordx4 v247, s[12:15], s84 offen offset:1024 lds\n"
"v_mfma_f32_16x16x32_bf16 a[72:75], a[168:171], v[96:99], a[72:75]\n"
"ds_read_b64_tr_b16 a[240:241], v218 offset:41744\n"
"v_fma_mix_f32 v184, v124, v160, v168 op_sel:[0,0,0] op_sel_hi:[1,0,0]\n"
"v_mfma_f32_16x16x32_bf16 a[72:75], a[172:175], v[104:107], a[72:75]\n"
"ds_read_b64_tr_b16 a[242:243], v218 offset:42768\n"
"v_fma_mix_f32 v185, v124, v161, v169 op_sel:[1,0,0] op_sel_hi:[1,0,0]\n"
"v_mfma_f32_16x16x32_bf16 a[76:79], a[168:171], v[100:103], a[76:79]\n"
"ds_read_b64_tr_b16 a[244:245], v218 offset:45840\n"
"v_fma_mix_f32 v186, v125, v162, v170 op_sel:[0,0,0] op_sel_hi:[1,0,0]\n"
"v_mfma_f32_16x16x32_bf16 a[76:79], a[172:175], v[108:111], a[76:79]\n"
"s_waitcnt lgkmcnt(11)\n"
"ds_read_b64_tr_b16 a[246:247], v218 offset:46864\n"
"v_fma_mix_f32 v187, v125, v163, v171 op_sel:[1,0,0] op_sel_hi:[1,0,0]\n"
"v_mfma_f32_16x16x32_bf16 a[80:83], a[176:179], v[96:99], a[80:83]\n"
"ds_read_b64_tr_b16 a[248:249], v218 offset:41752\n"
"v_fma_mix_f32 v188, v126, v164, v172 op_sel:[0,0,0] op_sel_hi:[1,0,0]\n"
"v_mfma_f32_16x16x32_bf16 a[80:83], a[180:183], v[104:107], a[80:83]\n"
"ds_read_b64_tr_b16 a[250:251], v218 offset:42776\n"
"buffer_load_dwordx4 v246, s[12:15], s84 offen offset:2048 lds\n"
"v_mfma_f32_16x16x32_bf16 a[84:87], a[176:179], v[100:103], a[84:87]\n"
"ds_read_b64_tr_b16 a[252:253], v218 offset:45848\n"
"v_fma_mix_f32 v189, v126, v165, v173 op_sel:[1,0,0] op_sel_hi:[1,0,0]\n"
"v_mfma_f32_16x16x32_bf16 a[84:87], a[180:183], v[108:111], a[84:87]\n"
"ds_read_b64_tr_b16 a[254:255], v218 offset:46872\n"
"v_fma_mix_f32 v190, v127, v166, v174 op_sel:[0,0,0] op_sel_hi:[1,0,0]\n"
"v_mfma_f32_16x16x32_bf16 v[236:239], a[184:187], v[96:99], v[236:239]\n"
"ds_read_b64_tr_b16 a[160:161], v219 offset:0\n"
"v_fma_mix_f32 v191, v127, v167, v175 op_sel:[1,0,0] op_sel_hi:[1,0,0]\n"
"v_cvt_pk_bf16_f32 v124, v184, v185\n"
"v_mfma_f32_16x16x32_bf16 v[236:239], a[188:191], v[104:107], v[236:239]\n"
"ds_read_b64_tr_b16 a[162:163], v219 offset:1024\n"
"ds_read_b128 v[160:163], v224 offset:4352\n"
"v_mfma_f32_16x16x32_bf16 v[232:235], a[184:187], v[100:103], v[232:235]\n"
"ds_read_b64_tr_b16 a[164:165], v219 offset:4096\n"
"v_cvt_pk_bf16_f32 v125, v186, v187\n"
"v_mfma_f32_16x16x32_bf16 v[232:235], a[188:191], v[108:111], v[232:235]\n"
"s_waitcnt lgkmcnt(12)\n"
"buffer_load_dwordx4 v247, s[12:15], s84 offen offset:3072 lds\n"
"ds_read_b64_tr_b16 a[166:167], v219 offset:5120\n"
"v_mfma_f32_16x16x32_bf16 v[228:231], a[224:227], v[96:99], v[228:231]\n"
"s_add_u32 s84, s84, 16384\n"
"ds_read_b64_tr_b16 a[168:169], v219 offset:8\n"
"ds_read_b128 v[164:167], v224 offset:4416\n"
"v_mfma_f32_16x16x32_bf16 v[228:231], a[228:231], v[104:107], v[228:231]\n"
"ds_read_b64_tr_b16 a[170:171], v219 offset:1032\n"
"v_cvt_pk_bf16_f32 v126, v188, v189\n"
"v_mfma_f32_16x16x32_bf16 v[220:223], a[224:227], v[100:103], v[220:223]\n"
"ds_read_b64_tr_b16 a[172:173], v219 offset:4104\n"
"v_cvt_pk_bf16_f32 v127, v190, v191\n"
"v_mfma_f32_16x16x32_bf16 v[220:223], a[228:231], v[108:111], v[220:223]\n"
"ds_read_b64_tr_b16 a[174:175], v219 offset:5128\n"
"ds_read_b128 v[168:171], v224 offset:4480\n"
"v_mfma_f32_16x16x32_bf16 v[212:215], a[232:235], v[96:99], v[212:215]\n"
"ds_read_b64_tr_b16 a[176:177], v219 offset:256\n"
"v_fma_mix_f32 v176, v128, v144, v152 op_sel:[0,0,0] op_sel_hi:[1,0,0]\n"
"v_mfma_f32_16x16x32_bf16 v[212:215], a[236:239], v[104:107], v[212:215]\n"
"ds_read_b64_tr_b16 a[178:179], v219 offset:1280\n"
"v_fma_mix_f32 v177, v128, v145, v153 op_sel:[1,0,0] op_sel_hi:[1,0,0]\n"
"v_mfma_f32_16x16x32_bf16 v[208:211], a[232:235], v[100:103], v[208:211]\n"
"ds_read_b64_tr_b16 a[180:181], v219 offset:4352\n"
"ds_read_b128 v[172:175], v224 offset:4544\n"
"v_mfma_f32_16x16x32_bf16 v[208:211], a[236:239], v[108:111], v[208:211]\n"
"s_waitcnt lgkmcnt(14)\n"
"ds_read_b64_tr_b16 a[182:183], v219 offset:5376\n"
"v_fma_mix_f32 v178, v129, v146, v154 op_sel:[0,0,0] op_sel_hi:[1,0,0]\n"
"v_mfma_f32_16x16x32_bf16 v[204:207], a[240:243], v[96:99], v[204:207]\n"
"ds_read_b64_tr_b16 a[184:185], v219 offset:264\n"
"v_fma_mix_f32 v179, v129, v147, v155 op_sel:[1,0,0] op_sel_hi:[1,0,0]\n"
"v_mfma_f32_16x16x32_bf16 v[204:207], a[244:247], v[104:107], v[204:207]\n"
"ds_read_b64_tr_b16 a[186:187], v219 offset:1288\n"
"v_fma_mix_f32 v180, v130, v148, v156 op_sel:[0,0,0] op_sel_hi:[1,0,0]\n"
"v_mfma_f32_16x16x32_bf16 v[200:203], a[240:243], v[100:103], v[200:203]\n"
"AT_pg5:\n"
"s_cbranch_execz AT_hr\n"
"ds_read_b64_tr_b16 a[188:189], v219 offset:4360\n"
"v_fma_mix_f32 v181, v130, v149, v157 op_sel:[1,0,0] op_sel_hi:[1,0,0]\n"
"v_mfma_f32_16x16x32_bf16 v[200:203], a[244:247], v[108:111], v[200:203]\n"
"ds_read_b64_tr_b16 a[190:191], v219 offset:5384\n"
"v_fma_mix_f32 v182, v131, v150, v158 op_sel:[0,0,0] op_sel_hi:[1,0,0]\n"
"v_mfma_f32_16x16x32_bf16 v[196:199], a[248:251], v[96:99], v[196:199]\n"
"ds_read_b64_tr_b16 a[224:225], v219 offset:512\n"
"v_fma_mix_f32 v183, v131, v151, v159 op_sel:[1,0,0] op_sel_hi:[1,0,0]\n"
"v_mfma_f32_16x16x32_bf16 v[196:199], a[252:255], v[104:107], v[196:199]\n"
"ds_read_b64_tr_b16 a[226:227], v219 offset:1536\n"
"v_cvt_pk_bf16_f32 v128, v176, v177\n"
"v_mfma_f32_16x16x32_bf16 v[192:195], a[248:251], v[100:103], v[192:195]\n"
"ds_read_b64_tr_b16 a[228:229], v219 offset:4608\n"
"v_cvt_pk_bf16_f32 v129, v178, v179\n"
"v_mfma_f32_16x16x32_bf16 v[192:195], a[252:255], v[108:111], v[192:195]\n"
"s_waitcnt lgkmcnt(12)\n"
"ds_read_b64_tr_b16 a[230:231], v219 offset:5632\n"
"v_cvt_pk_bf16_f32 v130, v180, v181\n"
"v_mfma_f32_16x16x32_bf16 a[0:3], a[160:163], v[112:115], a[0:3]\n"
"ds_read_b64_tr_b16 a[232:233], v219 offset:520\n"
"v_cvt_pk_bf16_f32 v131, v182, v183\n"
"v_mfma_f32_16x16x32_bf16 a[0:3], a[164:167], v[120:123], a[0:3]\n"
"s_waitcnt vmcnt(8)\n"
"ds_read_b64_tr_b16 a[234:235], v219 offset:1544\n"
"v_fma_mix_f32 v184, v132, v144, v152 op_sel:[0,0,0] op_sel_hi:[1,0,0]\n"
"v_mfma_f32_16x16x32_bf16 a[4:7], a[160:163], v[116:119], a[4:7]\n"
"s_barrier\n"
"ds_read_b64_tr_b16 a[236:237], v219 offset:4616\n"
"v_fma_mix_f32 v185, v132, v145, v153 op_sel:[1,0,0] op_sel_hi:[1,0,0]\n"
"v_mfma_f32_16x16x32_bf16 a[4:7], a[164:167], v[124:127], a[4:7]\n"
"ds_read_b64_tr_b16 a[238:239], v219 offset:5640\n"
"v_fma_mix_f32 v186, v133, v146, v154 op_sel:[0,0,0] op_sel_hi:[1,0,0]\n"
"v_mfma_f32_16x16x32_bf16 a[8:11], a[168:171], v[112:115], a[8:11]\n"
"s_add_u32 m0, s86, 32768\n"
"ds_read_b64_tr_b16 a[240:241], v219 offset:768\n"
"v_fma_mix_f32 v187, v133, v147, v155 op_sel:[1,0,0] op_sel_hi:[1,0,0]\n"
"v_mfma_f32_16x16x32_bf16 a[8:11], a[172:175], v[120:123], a[8:11]\n"
"ds_read_b64_tr_b16 a[242:243], v219 offset:1792\n"
"buffer_load_dwordx4 v246, s[8:11], s84 offen offset:0 lds\n"
"v_mfma_f32_16x16x32_bf16 a[12:15], a[168:171], v[116:119], a[12:15]\n"
"ds_read_b64_tr_b16 a[244:245], v219 offset:4864\n"
"v_fma_mix_f32 v188, v134, v148, v156 op_sel:[0,0,0] op_sel_hi:[1,0,0]\n"
"v_mfma_f32_16x16x32_bf16 a[12:15], a[172:175], v[124:127], a[12:15]\n"
"s_waitcnt lgkmcnt(11)\n"
"ds_read_b64_tr_b16 a[246:247], v219 offset:5888\n"
"v_fma_mix_f32 v189, v134, v149, v157 op_sel:[1,0,0] op_sel_hi:[1,0,0]\n"
"v_mfma_f32_16x16x32_bf16 a[16:19], a[176:179], v[112:115], a[16:19]\n"
"ds_read_b64_tr_b16 a[248:249], v219 offset:776\n"
"v_fma_mix_f32 v190, v135, v150, v158 op_sel:[0,0,0] op_sel_hi:[1,0,0]\n"
"v_mfma_f32_16x16x32_bf16 a[16:19], a[180:183], v[120:123], a[16:19]\n"
"ds_read_b64_tr_b16 a[250:251], v219 offset:1800\n"
"v_fma_mix_f32 v191, v135, v151, v159 op_sel:[1,0,0] op_sel_hi:[1,0,0]\n"
"v_mfma_f32_16x16x32_bf16 a[20:23], a[176:179], v[116:119], a[20:23]\n"
"ds_read_b64_tr_b16 a[252:253], v219 offset:4872\n"
"v_cvt_pk_bf16_f32 v132, v184, v185\n"
"v_mfma_f32_16x16x32_bf16 a[20:23], a[180:183], v[124:127], a[20:23]\n"
"ds_read_b64_tr_b16 a[254:255], v219 offset:5896\n"
"buffer_load_dwordx4 v246, s[8:11], s84 offen offset:1024 lds\n"
"v_mfma_f32_16x16x32_bf16 a[24:27], a[184:187], v[112:115], a[24:27]\n"
"ds_read_b64_tr_b16 a[160:161], v219 offset:8192\n"
"v_cvt_pk_bf16_f32 v133, v186, v187\n"
"v_mfma_f32_16x16x32_bf16 a[24:27], a[188:191], v[120:123], a[24:27]\n"
"ds_read_b64_tr_b16 a[162:163], v219 offset:9216\n"
"v_cvt_pk_bf16_f32 v134, v188, v189\n"
"v_mfma_f32_16x16x32_bf16 a[28:31], a[184:187], v[116:119], a[28:31]\n"
"ds_read_b64_tr_b16 a[164:165], v219 offset:12288\n"
"v_cvt_pk_bf16_f32 v135, v190, v191\n"
"v_mfma_f32_16x16x32_bf16 a[28:31], a[188:191], v[124:127], a[28:31]\n"
"s_waitcnt lgkmcnt(11)\n"
"ds_read_b64_tr_b16 a[166:167], v219 offset:13312\n"
"v_fma_mix_f32 v176, v136, v160, v168 op_sel:[0,0,0] op_sel_hi:[1,0,0]\n"
"v_mfma_f32_16x16x32_bf16 a[32:35], a[224:227], v[112:115], a[32:35]\n"
"ds_read_b64_tr_b16 a[168:169], v219 offset:8200\n"
"v_fma_mix_f32 v177, v136, v161, v169 op_sel:[1,0,0] op_sel_hi:[1,0,0]\n"
"v_mfma_f32_16x16x32_bf16 a[32:35], a[228:231], v[120:123], a[32:35]\n"
"ds_read_b64_tr_b16 a[170:171], v219 offset:9224\n"
"buffer_load_dwordx4 v247, s[8:11], s84 offen offset:2048 lds\n"
"v_mfma_f32_16x16x32_bf16 a[36:39], a[224:227], v[116:119], a[36:39]\n"
"ds_read_b64_tr_b16 a[172:173], v219 offset:12296\n"
"v_fma_mix_f32 v178, v137, v162, v170 op_sel:[0,0,0] op_sel_hi:[1,0,0]\n"
"v_mfma_f32_16x16x32_bf16 a[36:39], a[228:231], v[124:127], a[36:39]\n"
"ds_read_b64_tr_b16 a[174:175], v219 offset:13320\n"
"v_fma_mix_f32 v179, v137, v163, v171 op_sel:[1,0,0] op_sel_hi:[1,0,0]\n"
"v_mfma_f32_16x16x32_bf16 a[40:43], a[232:235], v[112:115], a[40:43]\n"
"ds_read_b64_tr_b16 a[176:177], v219 offset:8448\n"
"v_fma_mix_f32 v180, v138, v164, v172 op_sel:[0,0,0] op_sel_hi:[1,0,0]\n"
"v_mfma_f32_16x16x32_bf16 a[40:43], a[236:239], v[120:123], a[40:43]\n"
"ds_read_b64_tr_b16 a[178:179], v219 offset:9472\n"
"v_fma_mix_f32 v181, v138, v165, v173 op_sel:[1,0,0] op_sel_hi:[1,0,0]\n"
"v_mfma_f32_16x16x32_bf16 a[44:47], a[232:235], v[116:119], a[44:47]\n"
"ds_read_b64_tr_b16 a[180:181], v219 offset:12544\n"
"v_fma_mix_f32 v182, v139, v166, v174 op_sel:[0,0,0] op_sel_hi:[1,0,0]\n"
"v_mfma_f32_16x16x32_bf16 a[44:47], a[236:239], v[124:127], a[44:47]\n"
"s_waitcnt lgkmcnt(11)\n"
"ds_read_b64_tr_b16 a[182:183], v219 offset:13568\n"
"buffer_load_dwordx4 v247, s[8:11], s84 offen offset:3072 lds\n"
"v_mfma_f32_16x16x32_bf16 a[48:51], a[240:243], v[112:115], a[48:51]\n"
"ds_read_b64_tr_b16 a[184:185], v219 offset:8456\n"
"v_fma_mix_f32 v183, v139, v167, v175 op_sel:[1,0,0] op_sel_hi:[1,0,0]\n"
"v_mfma_f32_16x16x32_bf16 a[48:51], a[244:247], v[120:123], a[48:51]\n"
"ds_read_b64_tr_b16 a[186:187], v219 offset:9480\n"
"v_cvt_pk_bf16_f32 v136, v176, v177\n"
"v_mfma_f32_16x16x32_bf16 a[52:55], a[240:243], v[116:119], a[52:55]\n"
"ds_read_b64_tr_b16 a[188:189], v219 offset:12552\n"
"v_cvt_pk_bf16_f32 v137, v178, v179\n"
"v_mfma_f32_16x16x32_bf16 a[52:55], a[244:247], v[124:127], a[52:55]\n"
"ds_read_b64_tr_b16 a[190:191], v219 offset:13576\n"
"v_cvt_pk_bf16_f32 v138, v180, v181\n"
"v_mfma_f32_16x16x32_bf16 a[56:59], a[248:251], v[112:115], a[56:59]\n"
"s_add_u32 m0, s86, 49152\n"
"ds_read_b64_tr_b16 a[224:225], v219 offset:8704\n"
"v_cvt_pk_bf16_f32 v139, v182, v183\n"
"v_mfma_f32_16x16x32_bf16 a[56:59], a[252:255], v[120:123], a[56:59]\n"
"ds_read_b64_tr_b16 a[226:227], v219 offset:9728\n"
"buffer_load_dwordx4 v246, s[12:15], s84 offen offset:0 lds\n"
"v_mfma_f32_16x16x32_bf16 a[60:63], a[248:251], v[116:119], a[60:63]\n"
"ds_read_b64_tr_b16 a[228:229], v219 offset:12800\n"
"v_fma_mix_f32 v184, v140, v160, v168 op_sel:[0,0,0] op_sel_hi:[1,0,0]\n"
"v_mfma_f32_16x16x32_bf16 a[60:63], a[252:255], v[124:127], a[60:63]\n"
"s_waitcnt lgkmcnt(11)\n"
"ds_read_b64_tr_b16 a[230:231], v219 offset:13824\n"
"v_fma_mix_f32 v185, v140, v161, v169 op_sel:[1,0,0] op_sel_hi:[1,0,0]\n"
"v_mfma_f32_16x16x32_bf16 a[64:67], a[160:163], v[112:115], a[64:67]\n"
"ds_read_b64_tr_b16 a[232:233], v219 offset:8712\n"
"v_fma_mix_f32 v186, v141, v162, v170 op_sel:[0,0,0] op_sel_hi:[1,0,0]\n"
"v_mfma_f32_16x16x32_bf16 a[64:67], a[164:167], v[120:123], a[64:67]\n"
"ds_read_b64_tr_b16 a[234:235], v219 offset:9736\n"
"v_fma_mix_f32 v187, v141, v163, v171 op_sel:[1,0,0] op_sel_hi:[1,0,0]\n"
"v_mfma_f32_16x16x32_bf16 a[68:71], a[160:163], v[116:119], a[68:71]\n"
"ds_read_b64_tr_b16 a[236:237], v219 offset:12808\n"
"v_fma_mix_f32 v188, v142, v164, v172 op_sel:[0,0,0] op_sel_hi:[1,0,0]\n"
"v_mfma_f32_16x16x32_bf16 a[68:71], a[164:167], v[124:127], a[68:71]\n"
"ds_read_b64_tr_b16 a[238:239], v219 offset:13832\n"
"buffer_load_dwordx4 v247, s[12:15], s84 offen offset:1024 lds\n"
"v_mfma_f32_16x16x32_bf16 a[72:75], a[168:171], v[112:115], a[72:75]\n"
"ds_read_b64_tr_b16 a[240:241], v219 offset:8960\n"
"v_fma_mix_f32 v189, v142, v165, v173 op_sel:[1,0,0] op_sel_hi:[1,0,0]\n"
"v_mfma_f32_16x16x32_bf16 a[72:75], a[172:175], v[120:123], a[72:75]\n"
"ds_read_b64_tr_b16 a[242:243], v219 offset:9984\n"
"v_fma_mix_f32 v190, v143, v166, v174 op_sel:[0,0,0] op_sel_hi:[1,0,0]\n"
"v_mfma_f32_16x16x32_bf16 a[76:79], a[168:171], v[116:119], a[76:79]\n"
"ds_read_b64_tr_b16 a[244:245], v219 offset:13056\n"
"v_fma_mix_f32 v191, v143, v167, v175 op_sel:[1,0,0] op_sel_hi:[1,0,0]\n"
"v_mfma_f32_16x16x32_bf16 a[76:79], a[172:175], v[124:127], a[76:79]\n"
"s_waitcnt lgkmcnt(11)\n"
"ds_read_b64_tr_b16 a[246:247], v219 offset:14080\n"
"v_cvt_pk_bf16_f32 v140, v184, v185\n"
"v_mfma_f32_16x16x32_bf16 a[80:83], a[176:179], v[112:115], a[80:83]\n"
"ds_read_b64_tr_b16 a[248:249], v219 offset:8968\n"
"v_cvt_pk_bf16_f32 v141, v186, v187\n"
"v_mfma_f32_16x16x32_bf16 a[80:83], a[180:183], v[120:123], a[80:83]\n"
"ds_read_b64_tr_b16 a[250:251], v219 offset:9992\n"
"buffer_load_dwordx4 v246, s[12:15], s84 offen offset:2048 lds\n"
"v_mfma_f32_16x16x32_bf16 a[84:87], a[176:179], v[116:119], a[84:87]\n"
"ds_read_b64_tr_b16 a[252:253], v219 offset:13064\n"
"v_cvt_pk_bf16_f32 v142, v188, v189\n"
"v_mfma_f32_16x16x32_bf16 a[84:87], a[180:183], v[124:127], a[84:87]\n"
"ds_read_b64_tr_b16 a[254:255], v219 offset:14088\n"
"v_cvt_pk_bf16_f32 v143, v190, v191\n"
"v_mfma_f32_16x16x32_bf16 v[236:239], a[184:187], v[112:115], v[236:239]\n"
"ds_read_b64_tr_b16 a[160:161], v219 offset:32784\n"
"v_mfma_f32_16x16x32_bf16 v[236:239], a[188:191], v[120:123], v[236:239]\n"
"ds_read_b64_tr_b16 a[162:163], v219 offset:33808\n"
"v_mfma_f32_16x16x32_bf16 v[232:235], a[184:187], v[116:119], v[232:235]\n"
"ds_read_b64_tr_b16 a[164:165], v219 offset:36880\n"
"v_mfma_f32_16x16x32_bf16 v[232:235], a[188:191], v[124:127], v[232:235]\n"
"s_waitcnt lgkmcnt(11)\n"
"buffer_load_dwordx4 v247, s[12:15], s84 offen offset:3072 lds\n"
"ds_read_b64_tr_b16 a[166:167], v219 offset:37904\n"
"v_mfma_f32_16x16x32_bf16 v[228:231], a[224:227], v[112:115], v[228:231]\n"
"s_add_u32 s84, s84, 16384\n"
"ds_read_b64_tr_b16 a[168:169], v219 offset:32792\n"
"v_mfma_f32_16x16x32_bf16 v[228:231], a[228:231], v[120:123], v[228:231]\n"
"ds_read_b64_tr_b16 a[170:171], v219 offset:33816\n"
"v_mfma_f32_16x16x32_bf16 v[220:223], a[224:227], v[116:119], v[220:223]\n"
"ds_read_b64_tr_b16 a[172:173], v219 offset:36888\n"
"v_mfma_f32_16x16x32_bf16 v[220:223], a[228:231], v[124:127], v[220:223]\n"
"ds_read_b64_tr_b16 a[174:175], v219 offset:37912\n"
"v_mfma_f32_16x16x32_bf16 v[212:215], a[232:235], v[112:115], v[212:215]\n"
"ds_read_b64_tr_b16 a[176:177], v219 offset:33040\n"
"v_mfma_f32_16x16x32_bf16 v[212:215], a[236:239], v[120:123], v[212:215]\n"
"ds_read_b64_tr_b16 a[178:179], v219 offset:34064\n"
"v_mfma_f32_16x16x32_bf16 v[208:211], a[232:235], v[116:119], v[208:211]\n"
"ds_read_b64_tr_b16 a[180:181], v219 offset:37136\n"
"v_mfma_f32_16x16x32_bf16 v[208:211], a[236:239], v[124:127], v[208:211]\n"
"s_waitcnt lgkmcnt(11)\n"
"ds_read_b64_tr_b16 a[182:183], v219 offset:38160\n"
"v_mfma_f32_16x16x32_bf16 v[204:207], a[240:243], v[112:115], v[204:207]\n"
"ds_read_b64_tr_b16 a[184:185], v219 offset:33048\n"
"v_mfma_f32_16x16x32_bf16 v[204:207], a[244:247], v[120:123], v[204:207]\n"
"ds_read_b64_tr_b16 a[186:187], v219 offset:34072\n"
"v_mfma_f32_16x16x32_bf16 v[200:203], a[240:243], v[116:119], v[200:203]\n"
"ds_read_b64_tr_b16 a[188:189], v219 offset:37144\n"
"v_mfma_f32_16x16x32_bf16 v[200:203], a[244:247], v[124:127], v[200:203]\n"
"ds_read_b64_tr_b16 a[190:191], v219 offset:38168\n"
"v_mfma_f32_16x16x32_bf16 v[196:199], a[248:251], v[112:115], v[196:199]\n"
"ds_read_b64_tr_b16 a[224:225], v219 offset:33296\n"
"v_mfma_f32_16x16x32_bf16 v[196:199], a[252:255], v[120:123], v[196:199]\n"
"ds_read_b64_tr_b16 a[226:227], v219 offset:34320\n"
"v_mfma_f32_16x16x32_bf16 v[192:195], a[248:251], v[116:119], v[192:195]\n"
"ds_read_b64_tr_b16 a[228:229], v219 offset:37392\n"
"v_mfma_f32_16x16x32_bf16 v[192:195], a[252:255], v[124:127], v[192:195]\n"
"s_waitcnt lgkmcnt(11)\n"
"ds_read_b64_tr_b16 a[230:231], v219 offset:38416\n"
"v_mfma_f32_16x16x32_bf16 a[0:3], a[160:163], v[128:131], a[0:3]\n"
"ds_read_b64_tr_b16 a[232:233], v219 offset:33304\n"
"v_mfma_f32_16x16x32_bf16 a[0:3], a[164:167], v[136:139], a[0:3]\n"
"s_waitcnt vmcnt(8)\n"
"ds_read_b64_tr_b16 a[234:235], v219 offset:34328\n"
"v_mfma_f32_16x16x32_bf16 a[4:7], a[160:163], v[132:135], a[4:7]\n"
"s_barrier\n"
"ds_read_b64_tr_b16 a[236:237], v219 offset:37400\n"
"v_mfma_f32_16x16x32_bf16 a[4:7], a[164:167], v[140:143], a[4:7]\n"
"ds_read_b64_tr_b16 a[238:239], v219 offset:38424\n"
"v_mfma_f32_16x16x32_bf16 a[8:11], a[168:171], v[128:131], a[8:11]\n"
"s_add_u32 m0, s86, 65536\n"
"ds_read_b64_tr_b16 a[240:241], v219 offset:33552\n"
"v_mfma_f32_16x16x32_bf16 a[8:11], a[172:175], v[136:139], a[8:11]\n"
"ds_read_b64_tr_b16 a[242:243], v219 offset:34576\n"
"buffer_load_dwordx4 v246, s[8:11], s84 offen offset:0 lds\n"
"v_mfma_f32_16x16x32_bf16 a[12:15], a[168:171], v[132:135], a[12:15]\n"
"ds_read_b64_tr_b16 a[244:245], v219 offset:37648\n"
"v_accvgpr_read_b32 v176, a0\n"
"v_mfma_f32_16x16x32_bf16 a[12:15], a[172:175], v[140:143], a[12:15]\n"
"s_waitcnt lgkmcnt(11)\n"
"ds_read_b64_tr_b16 a[246:247], v219 offset:38672\n"
"v_accvgpr_read_b32 v177, a1\n"
"v_mfma_f32_16x16x32_bf16 a[16:19], a[176:179], v[128:131], a[16:19]\n"
"ds_read_b64_tr_b16 a[248:249], v219 offset:33560\n"
"v_accvgpr_read_b32 v178, a2\n"
"v_mfma_f32_16x16x32_bf16 a[16:19], a[180:183], v[136:139], a[16:19]\n"
"ds_read_b64_tr_b16 a[250:251], v219 offset:34584\n"
"v_accvgpr_read_b32 v179, a3\n"
"v_mfma_f32_16x16x32_bf16 a[20:23], a[176:179], v[132:135], a[20:23]\n"
"ds_read_b64_tr_b16 a[252:253], v219 offset:37656\n"
"v_cvt_pk_bf16_f32 v64, v176, v177\n"
"v_mfma_f32_16x16x32_bf16 a[20:23], a[180:183], v[140:143], a[20:23]\n"
"ds_read_b64_tr_b16 a[254:255], v219 offset:38680\n"
"buffer_load_dwordx4 v246, s[8:11], s84 offen offset:1024 lds\n"
"v_mfma_f32_16x16x32_bf16 a[24:27], a[184:187], v[128:131], a[24:27]\n"
"ds_read_b64_tr_b16 a[160:161], v219 offset:40976\n"
"v_cvt_pk_bf16_f32 v65, v178, v179\n"
"v_mfma_f32_16x16x32_bf16 a[24:27], a[188:191], v[136:139], a[24:27]\n"
"ds_read_b64_tr_b16 a[162:163], v219 offset:42000\n"
"v_accvgpr_read_b32 v180, a4\n"
"v_mfma_f32_16x16x32_bf16 a[28:31], a[184:187], v[132:135], a[28:31]\n"
"ds_read_b64_tr_b16 a[164:165], v219 offset:45072\n"
"v_accvgpr_read_b32 v181, a5\n"
"v_mfma_f32_16x16x32_bf16 a[28:31], a[188:191], v[140:143], a[28:31]\n"
"s_waitcnt lgkmcnt(11)\n"
"ds_read_b64_tr_b16 a[166:167], v219 offset:46096\n"
"v_accvgpr_read_b32 v182, a6\n"
"v_mfma_f32_16x16x32_bf16 a[32:35], a[224:227], v[128:131], a[32:35]\n"
"ds_read_b64_tr_b16 a[168:169], v219 offset:40984\n"
"v_accvgpr_read_b32 v183, a7\n"
"v_mfma_f32_16x16x32_bf16 a[32:35], a[228:231], v[136:139], a[32:35]\n"
"ds_read_b64_tr_b16 a[170:171], v219 offset:42008\n"
"buffer_load_dwordx4 v247, s[8:11], s84 offen offset:2048 lds\n"
"v_mfma_f32_16x16x32_bf16 a[36:39], a[224:227], v[132:135], a[36:39]\n"
"ds_read_b64_tr_b16 a[172:173], v219 offset:45080\n"
"v_cvt_pk_bf16_f32 v96, v180, v181\n"
"v_mfma_f32_16x16x32_bf16 a[36:39], a[228:231], v[140:143], a[36:39]\n"
"ds_read_b64_tr_b16 a[174:175], v219 offset:46104\n"
"v_cvt_pk_bf16_f32 v97, v182, v183\n"
"v_mfma_f32_16x16x32_bf16 a[40:43], a[232:235], v[128:131], a[40:43]\n"
"ds_read_b64_tr_b16 a[176:177], v219 offset:41232\n"
"v_accvgpr_read_b32 v184, a8\n"
"v_mfma_f32_16x16x32_bf16 a[40:43], a[236:239], v[136:139], a[40:43]\n"
"ds_read_b64_tr_b16 a[178:179], v219 offset:42256\n"
"v_accvgpr_read_b32 v185, a9\n"
"v_mfma_f32_16x16x32_bf16 a[44:47], a[232:235], v[132:135], a[44:47]\n"
"ds_read_b64_tr_b16 a[180:181], v219 offset:45328\n"
"v_accvgpr_read_b32 v186, a10\n"
"v_mfma_f32_16x16x32_bf16 a[44:47], a[236:239], v[140:143], a[44:47]\n"
"s_waitcnt lgkmcnt(11)\n"
"ds_read_b64_tr_b16 a[182:183], v219 offset:46352\n"
"buffer_load_dwordx4 v247, s[8:11], s84 offen offset:3072 lds\n"
"v_mfma_f32_16x16x32_bf16 a[48:51], a[240:243], v[128:131], a[48:51]\n"
"ds_read_b64_tr_b16 a[184:185], v219 offset:41240\n"
"v_accvgpr_read_b32 v187, a11\n"
"v_mfma_f32_16x16x32_bf16 a[48:51], a[244:247], v[136:139], a[48:51]\n"
"ds_read_b64_tr_b16 a[186:187], v219 offset:42264\n"
"v_cvt_pk_bf16_f32 v66, v184, v185\n"
"v_mfma_f32_16x16x32_bf16 a[52:55], a[240:243], v[132:135], a[52:55]\n"
"ds_read_b64_tr_b16 a[188:189], v219 offset:45336\n"
"v_cvt_pk_bf16_f32 v67, v186, v187\n"
"v_mfma_f32_16x16x32_bf16 a[52:55], a[244:247], v[140:143], a[52:55]\n"
"ds_read_b64_tr_b16 a[190:191], v219 offset:46360\n"
"v_accvgpr_read_b32 v188, a12\n"
"v_mfma_f32_16x16x32_bf16 a[56:59], a[248:251], v[128:131], a[56:59]\n"
"s_add_u32 m0, s86, 81920\n"
"ds_read_b64_tr_b16 a[224:225], v219 offset:41488\n"
"v_accvgpr_read_b32 v189, a13\n"
"v_mfma_f32_16x16x32_bf16 a[56:59], a[252:255], v[136:139], a[56:59]\n"
"ds_read_b64_tr_b16 a[226:227], v219 offset:42512\n"
"buffer_load_dwordx4 v246, s[12:15], s84 offen offset:0 lds\n"
"v_mfma_f32_16x16x32_bf16 a[60:63], a[248:251], v[132:135], a[60:63]\n"
"ds_read_b64_tr_b16 a[228:229], v219 offset:45584\n"
"v_accvgpr_read_b32 v190, a14\n"
"v_mfma_f32_16x16x32_bf16 a[60:63], a[252:255], v[140:143], a[60:63]\n"
"s_waitcnt lgkmcnt(11)\n"
"ds_read_b64_tr_b16 a[230:231], v219 offset:46608\n"
"v_accvgpr_read_b32 v191, a15\n"
"v_mfma_f32_16x16x32_bf16 a[64:67], a[160:163], v[128:131], a[64:67]\n"
"ds_read_b64_tr_b16 a[232:233], v219 offset:41496\n"
"v_cvt_pk_bf16_f32 v98, v188, v189\n"
"v_mfma_f32_16x16x32_bf16 a[64:67], a[164:167], v[136:139], a[64:67]\n"
"ds_read_b64_tr_b16 a[234:235], v219 offset:42520\n"
"v_cvt_pk_bf16_f32 v99, v190, v191\n"
"v_mfma_f32_16x16x32_bf16 a[68:71], a[160:163], v[132:135], a[68:71]\n"
"ds_read_b64_tr_b16 a[236:237], v219 offset:45592\n"
"v_accvgpr_read_b32 v176, a16\n"
"v_mfma_f32_16x16x32_bf16 a[68:71], a[164:167], v[140:143], a[68:71]\n"
"ds_read_b64_tr_b16 a[238:239], v219 offset:46616\n"
"buffer_load_dwordx4 v247, s[12:15], s84 offen offset:1024 lds\n"
"v_mfma_f32_16x16x32_bf16 a[72:75], a[168:171], v[128:131], a[72:75]\n"
"ds_read_b64_tr_b16 a[240:241], v219 offset:41744\n"
"v_accvgpr_read_b32 v177, a17\n"
"v_mfma_f32_16x16x32_bf16 a[72:75], a[172:175], v[136:139], a[72:75]\n"
"ds_read_b64_tr_b16 a[242:243], v219 offset:42768\n"
"v_accvgpr_read_b32 v178, a18\n"
"v_mfma_f32_16x16x32_bf16 a[76:79], a[168:171], v[132:135], a[76:79]\n"
"ds_read_b64_tr_b16 a[244:245], v219 offset:45840\n"
"v_accvgpr_read_b32 v179, a19\n"
"v_mfma_f32_16x16x32_bf16 a[76:79], a[172:175], v[140:143], a[76:79]\n"
"s_waitcnt lgkmcnt(11)\n"
"ds_read_b64_tr_b16 a[246:247], v219 offset:46864\n"
"v_cvt_pk_bf16_f32 v68, v176, v177\n"
"v_mfma_f32_16x16x32_bf16 a[80:83], a[176:179], v[128:131], a[80:83]\n"
"ds_read_b64_tr_b16 a[248:249], v219 offset:41752\n"
"v_cvt_pk_bf16_f32 v69, v178, v179\n"
"v_mfma_f32_16x16x32_bf16 a[80:83], a[180:183], v[136:139], a[80:83]\n"
"ds_read_b64_tr_b16 a[250:251], v219 offset:42776\n"
"buffer_load_dwordx4 v246, s[12:15], s84 offen offset:2048 lds\n"
"v_mfma_f32_16x16x32_bf16 a[84:87], a[176:179], v[132:135], a[84:87]\n"
"ds_read_b64_tr_b16 a[252:253], v219 offset:45848\n"
"v_accvgpr_read_b32 v180, a20\n"
"v_mfma_f32_16x16x32_bf16 a[84:87], a[180:183], v[140:143], a[84:87]\n"
"ds_read_b64_tr_b16 a[254:255], v219 offset:46872\n"
"v_accvgpr_read_b32 v181, a21\n"
"v_mfma_f32_16x16x32_bf16 v[236:239], a[184:187], v[128:131], v[236:239]\n"
"ds_read_b128 a[160:163], v254 offset:0\n"
"v_accvgpr_read_b32 v182, a22\n"
"v_mfma_f32_16x16x32_bf16 v[236:239], a[188:191], v[136:139], v[236:239]\n"
"ds_read_b128 a[164:167], v254 offset:256\n"
"v_accvgpr_read_b32 v183, a23\n"
"v_mfma_f32_16x16x32_bf16 v[232:235], a[184:187], v[132:135], v[232:235]\n"
"ds_read_b128 a[168:171], v254 offset:512\n"
"v_cvt_pk_bf16_f32 v100, v180, v181\n"
"v_mfma_f32_16x16x32_bf16 v[232:235], a[188:191], v[140:143], v[232:235]\n"
"s_waitcnt lgkmcnt(11)\n"
"buffer_load_dwordx4 v247, s[12:15], s84 offen offset:3072 lds\n"
"ds_read_b128 a[172:175], v254 offset:768\n"
"v_mfma_f32_16x16x32_bf16 v[228:231], a[224:227], v[128:131], v[228:231]\n"
"s_add_u32 s84, s84, 16384\n"
"ds_read_b128 a[128:131], v240 offset:0\n"
"v_cvt_pk_bf16_f32 v101, v182, v183\n"
"v_mfma_f32_16x16x32_bf16 v[228:231], a[228:231], v[136:139], v[228:231]\n"
"ds_read_b128 a[132:135], v240 offset:256\n"
"v_accvgpr_read_b32 v184, a24\n"
"v_mfma_f32_16x16x32_bf16 v[220:223], a[224:227], v[132:135], v[220:223]\n"
"ds_read_b128 a[136:139], v240 offset:512\n"
"v_accvgpr_read_b32 v185, a25\n"
"v_mfma_f32_16x16x32_bf16 v[220:223], a[228:231], v[140:143], v[220:223]\n"
"ds_read_b128 a[140:143], v240 offset:768\n"
"v_accvgpr_read_b32 v186, a26\n"
"v_mfma_f32_16x16x32_bf16 v[212:215], a[232:235], v[128:131], v[212:215]\n"
"ds_read_b128 a[144:147], v240 offset:4096\n"
"v_accvgpr_read_b32 v187, a27\n"
"v_mfma_f32_16x16x32_bf16 v[212:215], a[236:239], v[136:139], v[212:215]\n"
"ds_read_b128 a[148:151], v240 offset:4352\n"
"v_cvt_pk_bf16_f32 v70, v184, v185\n"
"v_mfma_f32_16x16x32_bf16 v[208:211], a[232:235], v[132:135], v[208:211]\n"
"ds_read_b128 a[152:155], v240 offset:4608\n"
"v_cvt_pk_bf16_f32 v71, v186, v187\n"
"v_mfma_f32_16x16x32_bf16 v[208:211], a[236:239], v[140:143], v[208:211]\n"
"s_waitcnt lgkmcnt(11)\n"
"ds_read_b128 a[156:159], v240 offset:4864\n"
"v_accvgpr_read_b32 v188, a28\n"
"v_mfma_f32_16x16x32_bf16 v[204:207], a[240:243], v[128:131], v[204:207]\n"
"ds_read_b64_tr_b16 a[192:193], v242 offset:16384\n"
"v_accvgpr_read_b32 v189, a29\n"
"v_mfma_f32_16x16x32_bf16 v[204:207], a[244:247], v[136:139], v[204:207]\n"
"ds_read_b64_tr_b16 a[194:195], v242 offset:20480\n"
"v_accvgpr_read_b32 v190, a30\n"
"v_mfma_f32_16x16x32_bf16 v[200:203], a[240:243], v[132:135], v[200:203]\n"
"ds_read_b64_tr_b16 a[196:197], v243 offset:16384\n"
"v_accvgpr_read_b32 v191, a31\n"
"v_mfma_f32_16x16x32_bf16 v[200:203], a[244:247], v[140:143], v[200:203]\n"
"ds_read_b64_tr_b16 a[198:199], v243 offset:20480\n"
"v_cvt_pk_bf16_f32 v102, v188, v189\n"
"v_mfma_f32_16x16x32_bf16 v[196:199], a[248:251], v[128:131], v[196:199]\n"
"ds_read_b64_tr_b16 a[200:201], v242 offset:16640\n"
"v_cvt_pk_bf16_f32 v103, v190, v191\n"
"v_mfma_f32_16x16x32_bf16 v[196:199], a[252:255], v[136:139], v[196:199]\n"
"ds_read_b64_tr_b16 a[202:203], v242 offset:20736\n"
"v_accvgpr_read_b32 v176, a32\n"
"v_mfma_f32_16x16x32_bf16 v[192:195], a[248:251], v[132:135], v[192:195]\n"
"ds_read_b64_tr_b16 a[204:205], v243 offset:16640\n"
"v_accvgpr_read_b32 v177, a33\n"
"v_mfma_f32_16x16x32_bf16 v[192:195], a[252:255], v[140:143], v[192:195]\n"
"ds_read_b64_tr_b16 a[206:207], v243 offset:20736\n"
"v_accvgpr_read_b32 v178, a34\n"
"s_barrier\n"
"ds_read_b64_tr_b16 a[208:209], v242 offset:16896\n"
"v_accvgpr_read_b32 v179, a35\n"
"v_cvt_pk_bf16_f32 v72, v176, v177\n"
"v_cvt_pk_bf16_f32 v73, v178, v179\n"
"ds_read_b64_tr_b16 a[210:211], v242 offset:20992\n"
"v_accvgpr_read_b32 v180, a36\n"
"v_accvgpr_read_b32 v181, a37\n"
"v_accvgpr_read_b32 v182, a38\n"
"ds_read_b64_tr_b16 a[212:213], v243 offset:16896\n"
"v_accvgpr_read_b32 v183, a39\n"
"v_cvt_pk_bf16_f32 v104, v180, v181\n"
"v_cvt_pk_bf16_f32 v105, v182, v183\n"
"ds_read_b64_tr_b16 a[214:215], v243 offset:20992\n"
"v_accvgpr_read_b32 v184, a40\n"
"v_accvgpr_read_b32 v185, a41\n"
"v_accvgpr_read_b32 v186, a42\n"
"ds_read_b64_tr_b16 a[216:217], v242 offset:17152\n"
"v_accvgpr_read_b32 v187, a43\n"
"v_cvt_pk_bf16_f32 v74, v184, v185\n"
"v_cvt_pk_bf16_f32 v75, v186, v187\n"
"ds_read_b64_tr_b16 a[218:219], v242 offset:21248\n"
"v_accvgpr_read_b32 v188, a44\n"
"v_accvgpr_read_b32 v189, a45\n"
"v_accvgpr_read_b32 v190, a46\n"
"ds_read_b64_tr_b16 a[220:221], v243 offset:17152\n"
"v_accvgpr_read_b32 v191, a47\n"
"v_cvt_pk_bf16_f32 v106, v188, v189\n"
"v_cvt_pk_bf16_f32 v107, v190, v191\n"
"ds_read_b64_tr_b16 a[222:223], v243 offset:21248\n"
"v_accvgpr_read_b32 v176, a48\n"
"v_accvgpr_read_b32 v177, a49\n"
"v_accvgpr_read_b32 v178, a50\n"
"v_accvgpr_read_b32 v179, a51\n"
"v_cvt_pk_bf16_f32 v76, v176, v177\n"
"v_cvt_pk_bf16_f32 v77, v178, v179\n"
"v_accvgpr_read_b32 v180, a52\n"
"v_accvgpr_read_b32 v181, a53\n"
"v_accvgpr_read_b32 v182, a54\n"
"v_accvgpr_read_b32 v183, a55\n"
"v_cvt_pk_bf16_f32 v108, v180, v181\n"
"v_cvt_pk_bf16_f32 v109, v182, v183\n"
"v_accvgpr_read_b32 v184, a56\n"
"v_accvgpr_read_b32 v185, a57\n"
"v_accvgpr_read_b32 v186, a58\n"
"v_accvgpr_read_b32 v187, a59\n"
"v_cvt_pk_bf16_f32 v78, v184, v185\n"
"v_cvt_pk_bf16_f32 v79, v186, v187\n"
"v_accvgpr_read_b32 v188, a60\n"
"v_accvgpr_read_b32 v189, a61\n"
"v_accvgpr_read_b32 v190, a62\n"
"v_accvgpr_read_b32 v191, a63\n"
"v_cvt_pk_bf16_f32 v110, v188, v189\n"
"v_cvt_pk_bf16_f32 v111, v190, v191\n"
"v_accvgpr_read_b32 v176, a64\n"
"v_accvgpr_read_b32 v177, a65\n"
"v_accvgpr_read_b32 v178, a66\n"
"s_nop 1\n"
"v_accvgpr_read_b32 v179, a67\n"
"v_cvt_pk_bf16_f32 v80, v176, v177\n"
"v_cvt_pk_bf16_f32 v81, v178, v179\n"
"v_accvgpr_read_b32 v180, a68\n"
"s_waitcnt lgkmcnt(14)\n"
"v_mfma_f32_16x16x32_bf16 v[144:147], a[160:163], v[64:67], 0\n"
"v_accvgpr_read_b32 v181, a69\n"
"v_accvgpr_read_b32 v182, a70\n"
"v_mfma_f32_16x16x32_bf16 v[144:147], a[164:167], v[68:71], v[144:147]\n"
"v_accvgpr_read_b32 v183, a71\n"
"v_cvt_pk_bf16_f32 v112, v180, v181\n"
"v_mfma_f32_16x16x32_bf16 v[144:147], a[168:171], v[72:75], v[144:147]\n"
"v_cvt_pk_bf16_f32 v113, v182, v183\n"
"v_accvgpr_read_b32 v184, a72\n"
"v_mfma_f32_16x16x32_bf16 v[144:147], a[172:175], v[76:79], v[144:147]\n"
"v_accvgpr_read_b32 v185, a73\n"
"v_accvgpr_read_b32 v186, a74\n"
"v_mfma_f32_16x16x32_bf16 v[152:155], a[160:163], v[96:99], 0\n"
"v_accvgpr_read_b32 v187, a75\n"
"v_cvt_pk_bf16_f32 v82, v184, v185\n"
"v_mfma_f32_16x16x32_bf16 v[152:155], a[164:167], v[100:103], v[152:155]\n"
"v_cvt_pk_bf16_f32 v83, v186, v187\n"
"v_accvgpr_read_b32 v188, a76\n"
"v_mfma_f32_16x16x32_bf16 v[152:155], a[168:171], v[104:107], v[152:155]\n"
"v_accvgpr_read_b32 v189, a77\n"
"v_accvgpr_read_b32 v190, a78\n"
"v_mfma_f32_16x16x32_bf16 v[152:155], a[172:175], v[108:111], v[152:155]\n"
"v_accvgpr_read_b32 v191, a79\n"
"v_cvt_pk_bf16_f32 v114, v188, v189\n"
"v_cvt_pk_bf16_f32 v115, v190, v191\n"
"v_accvgpr_read_b32 v176, a80\n"
"v_accvgpr_read_b32 v177, a81\n"
"v_accvgpr_read_b32 v178, a82\n"
"v_accvgpr_read_b32 v179, a83\n"
"v_cvt_pk_bf16_f32 v84, v176, v177\n"
"v_cvt_pk_bf16_f32 v85, v178, v179\n"
"v_accvgpr_read_b32 v180, a84\n"
"v_accvgpr_read_b32 v181, a85\n"
"v_accvgpr_read_b32 v182, a86\n"
"v_accvgpr_read_b32 v183, a87\n"
"v_cvt_pk_bf16_f32 v116, v180, v181\n"
"v_cvt_pk_bf16_f32 v117, v182, v183\n"
"v_cvt_pk_bf16_f32 v86, v236, v237\n"
"v_cvt_pk_bf16_f32 v87, v238, v239\n"
"v_cvt_pk_bf16_f32 v118, v232, v233\n"
"v_cvt_pk_bf16_f32 v119, v234, v235\n"
"v_cvt_pk_bf16_f32 v88, v228, v229\n"
"v_cvt_pk_bf16_f32 v89, v230, v231\n"
"v_cvt_pk_bf16_f32 v120, v220, v221\n"
"v_cvt_pk_bf16_f32 v121, v222, v223\n"
"v_cvt_pk_bf16_f32 v90, v212, v213\n"
"v_cvt_pk_bf16_f32 v91, v214, v215\n"
"v_cvt_pk_bf16_f32 v122, v208, v209\n"
"v_cvt_pk_bf16_f32 v123, v210, v211\n"
"v_cvt_pk_bf16_f32 v92, v204, v205\n"
"v_cvt_pk_bf16_f32 v93, v206, v207\n"
"v_cvt_pk_bf16_f32 v124, v200, v201\n"
"v_cvt_pk_bf16_f32 v125, v202, v203\n"
"v_cvt_pk_bf16_f32 v94, v196, v197\n"
"v_cvt_pk_bf16_f32 v95, v198, v199\n"
"v_cvt_pk_bf16_f32 v126, v192, v193\n"
"AT_pg6:\n"
"s_cbranch_execz AT_hr\n"
"v_cvt_pk_bf16_f32 v127, v194, v195\n"
"v_max3_f32 v144, v144, v145, v146\n"
"v_max_f32 v144, v144, v147\n"
"v_sub_f32 v24, 0xc2480000, v144\n"
"v_sub_f32 v25, 0xc2480000, v144\n"
"v_sub_f32 v26, 0xc2480000, v144\n"
"v_sub_f32 v27, 0xc2480000, v144\n"
"s_nop 1\n"
"v_mfma_f32_16x16x32_bf16 v[148:151], a[160:163], v[80:83], 0\n"
"v_max3_f32 v152, v152, v153, v154\n"
"v_max_f32 v152, v152, v155\n"
"v_mfma_f32_16x16x32_bf16 v[148:151], a[164:167], v[84:87], v[148:151]\n"
"v_sub_f32 v32, 0xc2480000, v152\n"
"v_sub_f32 v33, 0xc2480000, v152\n"
"v_mfma_f32_16x16x32_bf16 v[148:151], a[168:171], v[88:91], v[148:151]\n"
"v_sub_f32 v34, 0xc2480000, v152\n"
"v_sub_f32 v35, 0xc2480000, v152\n"
"v_mfma_f32_16x16x32_bf16 v[148:151], a[172:175], v[92:95], v[148:151]\n"
"v_mov_b32 v40, 0x3f803f80\n"
"v_mov_b32 v41, 0x3f803f80\n"
"v_mfma_f32_16x16x32_bf16 v[156:159], a[160:163], v[112:115], 0\n"
"v_mov_b32 v42, 0x3f803f80\n"
"v_mov_b32 v43, 0x3f803f80\n"
"v_mfma_f32_16x16x32_bf16 v[156:159], a[164:167], v[116:119], v[156:159]\n"
"v_mov_b32 v20, 0\n"
"v_mov_b32 v21, 0\n"
"v_mfma_f32_16x16x32_bf16 v[156:159], a[168:171], v[120:123], v[156:159]\n"
"v_mov_b32 v22, 0\n"
"v_mov_b32 v23, 0\n"
"v_mfma_f32_16x16x32_bf16 v[156:159], a[172:175], v[124:127], v[156:159]\n"
"v_mov_b32 v44, 0\n"
"v_mov_b32 v52, 0\n"
"v_mov_b32 v216, -1\n"
"v_mov_b32 v217, -1\n"
"v_mov_b32 v218, -1\n"
"v_mov_b32 v219, -1\n"
"v_lshl_add_u32 v255, v248, 6, s48\n"
"v_lshlrev_b32 v207, 4, v248\n"
"v_add_u32 v208, 18432, v207\n"
"s_waitcnt lgkmcnt(0)\n"
"AT_main:\n"
"s_or_b32 s56, s42, s41\n"
"s_cmp_lg_u32 s56, 0\n"
"s_cbranch_scc1 AT_h1\n"
"v_lshlrev_b32 v214, 7, v248\n"
"s_mov_b32 s57, 32768\n"
"s_mov_b32 s58, 40960\n"
"s_mov_b32 s59, 49152\n"
"s_mov_b32 s60, 57344\n"
"buffer_load_dword v210, v214, s[28:31], s57 offen offset:4 sc1\n"
"buffer_load_dword v211, v214, s[28:31], s58 offen offset:4 sc1\n"
"buffer_load_dword v212, v214, s[28:31], s59 offen offset:4 sc1\n"
"buffer_load_dword v213, v214, s[28:31], s60 offen offset:4 sc1\n"
"AT_h1:\n"
"v_mfma_f32_16x16x32_bf16 v[0:3], a[128:131], v[64:67], v[24:27]\n"
"s_nop 0\n"
"ds_read_b128 a[160:163], v240 offset:8192\n"
"v_mfma_f32_16x16x32_bf16 v[0:3], a[132:135], v[68:71], v[0:3]\n"
"s_add_u32 m0, s86, 98304\n"
"v_mfma_f32_16x16x32_bf16 v[0:3], a[136:139], v[72:75], v[0:3]\n"
"s_nop 0\n"
"buffer_load_dwordx4 v246, s[8:11], s84 offen offset:0 lds\n"
"ds_read_b128 a[164:167], v240 offset:8448\n"
"v_mfma_f32_16x16x32_bf16 v[0:3], a[140:143], v[76:79], v[0:3]\n"
"s_nop 0\n"
"v_mfma_f32_16x16x32_bf16 v[4:7], a[144:147], v[64:67], v[24:27]\n"
"ds_read_b128 a[168:171], v240 offset:8704\n"
"v_max3_f32 v148, v148, v149, v150\n"
"v_mfma_f32_16x16x32_bf16 v[4:7], a[148:151], v[68:71], v[4:7]\n"
"v_max_f32 v148, v148, v151\n"
"v_sub_f32 v28, 0xc2480000, v148\n"
"v_mfma_f32_16x16x32_bf16 v[4:7], a[152:155], v[72:75], v[4:7]\n"
"ds_read_b128 a[172:175], v240 offset:8960\n"
"v_sub_f32 v29, 0xc2480000, v148\n"
"v_mfma_f32_16x16x32_bf16 v[4:7], a[156:159], v[76:79], v[4:7]\n"
"v_exp_f32 v0, v0\n"
"v_sub_f32 v30, 0xc2480000, v148\n"
"v_mfma_f32_16x16x32_bf16 a[96:99], v[40:43], v[20:23], 0\n"
"buffer_load_dwordx4 v246, s[8:11], s84 offen offset:1024 lds\n"
"v_sub_f32 v31, 0xc2480000, v148\n"
"v_mfma_f32_16x16x32_bf16 a[100:103], v[40:43], v[20:23], 0\n"
"v_exp_f32 v1, v1\n"
"v_max3_f32 v156, v156, v157, v158\n"
"v_mfma_f32_16x16x32_bf16 a[104:107], v[40:43], v[20:23], 0\n"
"ds_read_b128 a[176:179], v240 offset:12288\n"
"v_cvt_pk_bf16_f32 v16, v0, v1\n"
"v_max_f32 v156, v156, v159\n"
"v_mfma_f32_16x16x32_bf16 a[108:111], v[40:43], v[20:23], 0\n"
"v_exp_f32 v2, v2\n"
"v_sub_f32 v36, 0xc2480000, v156\n"
"v_mfma_f32_16x16x32_bf16 a[112:115], v[40:43], v[20:23], 0\n"
"ds_read_b128 a[180:183], v240 offset:12544\n"
"v_add_f32 v44, v44, v0\n"
"v_sub_f32 v37, 0xc2480000, v156\n"
"v_mfma_f32_16x16x32_bf16 a[116:119], v[40:43], v[20:23], 0\n"
"v_exp_f32 v3, v3\n"
"v_sub_f32 v38, 0xc2480000, v156\n"
"v_mfma_f32_16x16x32_bf16 a[120:123], v[40:43], v[20:23], 0\n"
"ds_read_b128 a[184:187], v240 offset:12800\n"
"v_cvt_pk_bf16_f32 v17, v2, v3\n"
"v_sub_f32 v39, 0xc2480000, v156\n"
"v_mfma_f32_16x16x32_bf16 a[124:127], v[40:43], v[20:23], 0\n"
"s_nop 0\n"
"v_exp_f32 v4, v4\n"
"v_mfma_f32_16x16x32_bf16 v[56:59], v[40:43], v[20:23], 0\n"
"buffer_load_dwordx4 v247, s[8:11], s84 offen offset:2048 lds\n"
"ds_read_b128 a[188:191], v240 offset:13056\n"
"v_add_f32 v44, v44, v1\n"
"v_mfma_f32_16x16x32_bf16 v[8:11], a[128:131], v[80:83], v[28:31]\n"
"s_nop 0\n"
"v_exp_f32 v5, v5\n"
"v_mfma_f32_16x16x32_bf16 v[8:11], a[132:135], v[84:87], v[8:11]\n"
"ds_read_b64_tr_b16 a[224:225], v242 offset:24576\n"
"v_cvt_pk_bf16_f32 v18, v4, v5\n"
"v_mfma_f32_16x16x32_bf16 v[8:11], a[136:139], v[88:91], v[8:11]\n"
"s_nop 0\n"
"v_exp_f32 v6, v6\n"
"v_mfma_f32_16x16x32_bf16 v[8:11], a[140:143], v[92:95], v[8:11]\n"
"ds_read_b64_tr_b16 a[226:227], v242 offset:28672\n"
"v_add_f32 v44, v44, v2\n"
"v_mfma_f32_16x16x32_bf16 v[12:15], a[144:147], v[80:83], v[28:31]\n"
"s_nop 0\n"
"v_exp_f32 v7, v7\n"
"v_mfma_f32_16x16x32_bf16 v[12:15], a[148:151], v[84:87], v[12:15]\n"
"ds_read_b64_tr_b16 a[228:229], v243 offset:24576\n"
"v_cvt_pk_bf16_f32 v19, v6, v7\n"
"v_mfma_f32_16x16x32_bf16 v[12:15], a[152:155], v[88:91], v[12:15]\n"
"s_nop 0\n"
"buffer_load_dwordx4 v247, s[8:11], s84 offen offset:3072 lds\n"
"v_mfma_f32_16x16x32_bf16 v[12:15], a[156:159], v[92:95], v[12:15]\n"
"s_nop 0\n"
"v_exp_f32 v8, v8\n"
"v_mfma_f32_16x16x32_bf16 a[0:3], a[192:195], v[16:19], 0\n"
"ds_read_b64_tr_b16 a[230:231], v243 offset:28672\n"
"v_add_f32 v44, v44, v3\n"
"v_mfma_f32_16x16x32_bf16 a[4:7], a[196:199], v[16:19], 0\n"
"s_nop 0\n"
"v_exp_f32 v9, v9\n"
"v_mfma_f32_16x16x32_bf16 a[8:11], a[200:203], v[16:19], 0\n"
"ds_read_b64_tr_b16 a[232:233], v242 offset:24832\n"
"v_cvt_pk_bf16_f32 v20, v8, v9\n"
"v_mfma_f32_16x16x32_bf16 a[12:15], a[204:207], v[16:19], 0\n"
"s_nop 0\n"
"v_exp_f32 v10, v10\n"
"v_mfma_f32_16x16x32_bf16 a[16:19], a[208:211], v[16:19], 0\n"
"ds_read_b64_tr_b16 a[234:235], v242 offset:28928\n"
"v_add_f32 v44, v44, v4\n"
"v_mfma_f32_16x16x32_bf16 a[20:23], a[212:215], v[16:19], 0\n"
"s_add_u32 m0, s86, 114688\n"
"v_exp_f32 v11, v11\n"
"v_mfma_f32_16x16x32_bf16 a[24:27], a[216:219], v[16:19], 0\n"
"buffer_load_dwordx4 v246, s[12:15], s84 offen offset:0 lds\n"
"ds_read_b64_tr_b16 a[236:237], v243 offset:24832\n"
"v_cvt_pk_bf16_f32 v21, v10, v11\n"
"v_mfma_f32_16x16x32_bf16 a[28:31], a[220:223], v[16:19], 0\n"
"s_nop 0\n"
"v_exp_f32 v12, v12\n"
"v_mfma_f32_16x16x32_bf16 v[220:223], a[128:131], v[96:99], v[32:35]\n"
"ds_read_b64_tr_b16 a[238:239], v243 offset:28928\n"
"v_add_f32 v44, v44, v5\n"
"v_mfma_f32_16x16x32_bf16 v[220:223], a[132:135], v[100:103], v[220:223]\n"
"s_nop 0\n"
"v_exp_f32 v13, v13\n"
"v_mfma_f32_16x16x32_bf16 v[220:223], a[136:139], v[104:107], v[220:223]\n"
"ds_read_b64_tr_b16 a[240:241], v242 offset:25088\n"
"v_cvt_pk_bf16_f32 v22, v12, v13\n"
"v_mfma_f32_16x16x32_bf16 v[220:223], a[140:143], v[108:111], v[220:223]\n"
"s_nop 0\n"
"v_exp_f32 v14, v14\n"
"v_mfma_f32_16x16x32_bf16 v[224:227], a[144:147], v[96:99], v[32:35]\n"
"ds_read_b64_tr_b16 a[242:243], v242 offset:29184\n"
"v_add_f32 v44, v44, v6\n"
"v_mfma_f32_16x16x32_bf16 v[224:227], a[148:151], v[100:103], v[224:227]\n"
"s_nop 0\n"
"v_exp_f32 v15, v15\n"
"v_mfma_f32_16x16x32_bf16 v[224:227], a[152:155], v[104:107], v[224:227]\n"
"ds_read_b64_tr_b16 a[244:245], v243 offset:25088\n"
"v_cvt_pk_bf16_f32 v23, v14, v15\n"
"v_mfma_f32_16x16x32_bf16 v[224:227], a[156:159], v[108:111], v[224:227]\n"
"s_nop 0\n"
"v_exp_f32 v220, v220\n"
"v_mfma_f32_16x16x32_bf16 a[32:35], a[192:195], v[20:23], 0\n"
"s_nop 0\n"
"buffer_load_dwordx4 v247, s[12:15], s84 offen offset:1024 lds\n"
"v_mfma_f32_16x16x32_bf16 a[36:39], a[196:199], v[20:23], 0\n"
"s_nop 0\n"
"v_exp_f32 v221, v221\n"
"v_mfma_f32_16x16x32_bf16 a[40:43], a[200:203], v[20:23], 0\n"
"ds_read_b64_tr_b16 a[246:247], v243 offset:29184\n"
"v_cvt_pk_bf16_f32 v16, v220, v221\n"
"v_mfma_f32_16x16x32_bf16 a[44:47], a[204:207], v[20:23], 0\n"
"s_nop 0\n"
"v_exp_f32 v222, v222\n"
"v_mfma_f32_16x16x32_bf16 a[48:51], a[208:211], v[20:23], 0\n"
"ds_read_b64_tr_b16 a[248:249], v242 offset:25344\n"
"v_add_f32 v44, v44, v7\n"
"v_mfma_f32_16x16x32_bf16 a[52:55], a[212:215], v[20:23], 0\n"
"s_nop 0\n"
"v_exp_f32 v223, v223\n"
"v_mfma_f32_16x16x32_bf16 a[56:59], a[216:219], v[20:23], 0\n"
"ds_read_b64_tr_b16 a[250:251], v242 offset:29440\n"
"v_cvt_pk_bf16_f32 v17, v222, v223\n"
"v_mfma_f32_16x16x32_bf16 a[60:63], a[220:223], v[20:23], 0\n"
"s_nop 0\n"
"v_exp_f32 v224, v224\n"
"v_mfma_f32_16x16x32_bf16 v[48:51], v[40:43], v[20:23], 0\n"
"buffer_load_dwordx4 v246, s[12:15], s84 offen offset:2048 lds\n"
"ds_read_b64_tr_b16 a[252:253], v243 offset:25344\n"
"v_add_f32 v52, v52, v220\n"
"v_mfma_f32_16x16x32_bf16 v[228:231], a[128:131], v[112:115], v[36:39]\n"
"s_nop 0\n"
"v_exp_f32 v225, v225\n"
"v_mfma_f32_16x16x32_bf16 v[228:231], a[132:135], v[116:119], v[228:231]\n"
"ds_read_b64_tr_b16 a[254:255], v243 offset:29440\n"
"v_cvt_pk_bf16_f32 v18, v224, v225\n"
"v_mfma_f32_16x16x32_bf16 v[228:231], a[136:139], v[120:123], v[228:231]\n"
"s_nop 0\n"
"v_exp_f32 v226, v226\n"
"v_mfma_f32_16x16x32_bf16 v[228:231], a[140:143], v[124:127], v[228:231]\n"
"v_add_f32 v52, v52, v221\n"
"v_add_f32 v52, v52, v222\n"
"v_mfma_f32_16x16x32_bf16 v[232:235], a[144:147], v[112:115], v[36:39]\n"
"s_nop 0\n"
"v_exp_f32 v227, v227\n"
"v_mfma_f32_16x16x32_bf16 v[232:235], a[148:151], v[116:119], v[232:235]\n"
"v_cvt_pk_bf16_f32 v19, v226, v227\n"
"v_add_f32 v52, v52, v223\n"
"v_mfma_f32_16x16x32_bf16 v[232:235], a[152:155], v[120:123], v[232:235]\n"
"s_nop 0\n"
"buffer_load_dwordx4 v247, s[12:15], s84 offen offset:3072 lds\n"
"v_mfma_f32_16x16x32_bf16 v[232:235], a[156:159], v[124:127], v[232:235]\n"
"s_add_u32 s84, s84, 16384\n"
"v_exp_f32 v228, v228\n"
"v_mfma_f32_16x16x32_bf16 a[64:67], a[192:195], v[16:19], 0\n"
"v_add_f32 v52, v52, v224\n"
"v_add_f32 v52, v52, v225\n"
"v_mfma_f32_16x16x32_bf16 a[68:71], a[196:199], v[16:19], 0\n"
"s_nop 0\n"
"v_exp_f32 v229, v229\n"
"v_mfma_f32_16x16x32_bf16 a[72:75], a[200:203], v[16:19], 0\n"
"v_cvt_pk_bf16_f32 v20, v228, v229\n"
"v_add_f32 v52, v52, v226\n"
"v_mfma_f32_16x16x32_bf16 a[76:79], a[204:207], v[16:19], 0\n"
"s_waitcnt vmcnt(16) lgkmcnt(0)\n"
"v_exp_f32 v230, v230\n"
"v_mfma_f32_16x16x32_bf16 a[80:83], a[208:211], v[16:19], 0\n"
"s_barrier\n"
"v_mfma_f32_16x16x32_bf16 a[84:87], a[212:215], v[16:19], 0\n"
"s_nop 0\n"
"v_exp_f32 v231, v231\n"
"v_mfma_f32_16x16x32_bf16 a[88:91], a[216:219], v[16:19], 0\n"
"v_cvt_pk_bf16_f32 v21, v230, v231\n"
"v_add_f32 v52, v52, v227\n"
"v_mfma_f32_16x16x32_bf16 a[92:95], a[220:223], v[16:19], 0\n"
"s_nop 0\n"
"v_exp_f32 v232, v232\n"
"s_mov_b32 s87, 6\n"
".p2align 6\n"
"AT_loop:\n"
"v_mfma_f32_16x16x32_bf16 v[0:3], a[160:163], v[64:67], v[24:27]\n"
"ds_read_b128 a[128:131], v240 offset:32768\n"
"buffer_load_dword v250, v251, s[64:67], 0 offen\n"
"v_mfma_f32_16x16x32_bf16 v[0:3], a[164:167], v[68:71], v[0:3]\n"
"v_exp_f32 v233, v233\n"
"v_mfma_f32_16x16x32_bf16 v[0:3], a[168:171], v[72:75], v[0:3]\n"
"ds_read_b128 a[132:135], v240 offset:33024\n"
"v_cvt_pk_bf16_f32 v22, v232, v233\n"
"v_mfma_f32_16x16x32_bf16 v[0:3], a[172:175], v[76:79], v[0:3]\n"
"v_exp_f32 v234, v234\n"
"v_mfma_f32_16x16x32_bf16 v[4:7], a[176:179], v[64:67], v[24:27]\n"
"ds_read_b128 a[136:139], v240 offset:33280\n"
"v_mfma_f32_16x16x32_bf16 v[4:7], a[180:183], v[68:71], v[4:7]\n"
"v_exp_f32 v235, v235\n"
"v_mfma_f32_16x16x32_bf16 v[4:7], a[184:187], v[72:75], v[4:7]\n"
"ds_read_b128 a[140:143], v240 offset:33536\n"
"v_cvt_pk_bf16_f32 v23, v234, v235\n"
"v_mfma_f32_16x16x32_bf16 v[4:7], a[188:191], v[76:79], v[4:7]\n"
"s_mov_b32 m0, s86\n"
"v_exp_f32 v0, v0\n"
"v_mfma_f32_16x16x32_bf16 a[96:99], a[192:195], v[20:23], a[96:99]\n"
"buffer_load_dwordx4 v246, s[8:11], s84 offen offset:0 lds\n"
"v_mfma_f32_16x16x32_bf16 a[100:103], a[196:199], v[20:23], a[100:103]\n"
"v_exp_f32 v1, v1\n"
"v_mfma_f32_16x16x32_bf16 a[104:107], a[200:203], v[20:23], a[104:107]\n"
"ds_read_b128 a[144:147], v240 offset:36864\n"
"v_cvt_pk_bf16_f32 v16, v0, v1\n"
"v_mfma_f32_16x16x32_bf16 a[108:111], a[204:207], v[20:23], a[108:111]\n"
"v_exp_f32 v2, v2\n"
"v_mfma_f32_16x16x32_bf16 a[112:115], a[208:211], v[20:23], a[112:115]\n"
"ds_read_b128 a[148:151], v240 offset:37120\n"
"v_add_f32 v44, v44, v0\n"
"v_mfma_f32_16x16x32_bf16 a[116:119], a[212:215], v[20:23], a[116:119]\n"
"v_exp_f32 v3, v3\n"
"v_mfma_f32_16x16x32_bf16 a[120:123], a[216:219], v[20:23], a[120:123]\n"
"ds_read_b128 a[152:155], v240 offset:37376\n"
"v_cvt_pk_bf16_f32 v17, v2, v3\n"
"v_mfma_f32_16x16x32_bf16 a[124:127], a[220:223], v[20:23], a[124:127]\n"
"v_exp_f32 v4, v4\n"
"v_mfma_f32_16x16x32_bf16 v[56:59], v[40:43], v[20:23], v[56:59]\n"
"ds_read_b128 a[156:159], v240 offset:37632\n"
"v_add_f32 v44, v44, v1\n"
"v_mfma_f32_16x16x32_bf16 v[8:11], a[160:163], v[80:83], v[28:31]\n"
"v_exp_f32 v5, v5\n"
"v_mfma_f32_16x16x32_bf16 v[8:11], a[164:167], v[84:87], v[8:11]\n"
"ds_read_b64_tr_b16 a[192:193], v242 offset:49152\n"
"v_cvt_pk_bf16_f32 v18, v4, v5\n"
"v_mfma_f32_16x16x32_bf16 v[8:11], a[168:171], v[88:91], v[8:11]\n"
"v_exp_f32 v6, v6\n"
"v_mfma_f32_16x16x32_bf16 v[8:11], a[172:175], v[92:95], v[8:11]\n"
"ds_read_b64_tr_b16 a[194:195], v242 offset:53248\n"
"v_add_f32 v44, v44, v2\n"
"v_mfma_f32_16x16x32_bf16 v[12:15], a[176:179], v[80:83], v[28:31]\n"
"v_exp_f32 v7, v7\n"
"v_mfma_f32_16x16x32_bf16 v[12:15], a[180:183], v[84:87], v[12:15]\n"
"ds_read_b64_tr_b16 a[196:197], v243 offset:49152\n"
"v_cvt_pk_bf16_f32 v19, v6, v7\n"
"v_mfma_f32_16x16x32_bf16 v[12:15], a[184:187], v[88:91], v[12:15]\n"
"buffer_load_dwordx4 v246, s[8:11], s84 offen offset:1024 lds\n"
"v_mfma_f32_16x16x32_bf16 v[12:15], a[188:191], v[92:95], v[12:15]\n"
"s_waitcnt lgkmcnt(11)\n"
"v_exp_f32 v8, v8\n"
"v_mfma_f32_16x16x32_bf16 a[0:3], a[224:227], v[16:19], a[0:3]\n"
"ds_read_b64_tr_b16 a[198:199], v243 offset:53248\n"
"v_add_f32 v44, v44, v3\n"
"v_mfma_f32_16x16x32_bf16 a[4:7], a[228:231], v[16:19], a[4:7]\n"
"v_exp_f32 v9, v9\n"
"v_mfma_f32_16x16x32_bf16 a[8:11], a[232:235], v[16:19], a[8:11]\n"
"ds_read_b64_tr_b16 a[200:201], v242 offset:49408\n"
"v_cvt_pk_bf16_f32 v20, v8, v9\n"
"v_mfma_f32_16x16x32_bf16 a[12:15], a[236:239], v[16:19], a[12:15]\n"
"v_exp_f32 v10, v10\n"
"v_mfma_f32_16x16x32_bf16 a[16:19], a[240:243], v[16:19], a[16:19]\n"
"ds_read_b64_tr_b16 a[202:203], v242 offset:53504\n"
"v_add_f32 v44, v44, v4\n"
"v_mfma_f32_16x16x32_bf16 a[20:23], a[244:247], v[16:19], a[20:23]\n"
"v_exp_f32 v11, v11\n"
"v_mfma_f32_16x16x32_bf16 a[24:27], a[248:251], v[16:19], a[24:27]\n"
"ds_read_b64_tr_b16 a[204:205], v243 offset:49408\n"
"v_cvt_pk_bf16_f32 v21, v10, v11\n"
"v_mfma_f32_16x16x32_bf16 a[28:31], a[252:255], v[16:19], a[28:31]\n"
"v_exp_f32 v12, v12\n"
"v_mfma_f32_16x16x32_bf16 v[220:223], a[160:163], v[96:99], v[32:35]\n"
"ds_read_b64_tr_b16 a[206:207], v243 offset:53504\n"
"v_add_f32 v44, v44, v5\n"
"v_mfma_f32_16x16x32_bf16 v[220:223], a[164:167], v[100:103], v[220:223]\n"
"v_exp_f32 v13, v13\n"
"v_mfma_f32_16x16x32_bf16 v[220:223], a[168:171], v[104:107], v[220:223]\n"
"ds_read_b64_tr_b16 a[208:209], v242 offset:49664\n"
"v_cvt_pk_bf16_f32 v22, v12, v13\n"
"v_mfma_f32_16x16x32_bf16 v[220:223], a[172:175], v[108:111], v[220:223]\n"
"v_exp_f32 v14, v14\n"
"v_mfma_f32_16x16x32_bf16 v[224:227], a[176:179], v[96:99], v[32:35]\n"
"ds_read_b64_tr_b16 a[210:211], v242 offset:53760\n"
"v_add_f32 v44, v44, v6\n"
"v_mfma_f32_16x16x32_bf16 v[224:227], a[180:183], v[100:103], v[224:227]\n"
"v_exp_f32 v15, v15\n"
"v_mfma_f32_16x16x32_bf16 v[224:227], a[184:187], v[104:107], v[224:227]\n"
"ds_read_b64_tr_b16 a[212:213], v243 offset:49664\n"
"v_cvt_pk_bf16_f32 v23, v14, v15\n"
"v_mfma_f32_16x16x32_bf16 v[224:227], a[188:191], v[108:111], v[224:227]\n"
"v_exp_f32 v220, v220\n"
"v_mfma_f32_16x16x32_bf16 a[32:35], a[224:227], v[20:23], a[32:35]\n"
"buffer_load_dwordx4 v247, s[8:11], s84 offen offset:2048 lds\n"
"v_mfma_f32_16x16x32_bf16 a[36:39], a[228:231], v[20:23], a[36:39]\n"
"v_exp_f32 v221, v221\n"
"v_mfma_f32_16x16x32_bf16 a[40:43], a[232:235], v[20:23], a[40:43]\n"
"ds_read_b64_tr_b16 a[214:215], v243 offset:53760\n"
"v_cvt_pk_bf16_f32 v16, v220, v221\n"
"v_mfma_f32_16x16x32_bf16 a[44:47], a[236:239], v[20:23], a[44:47]\n"
"v_exp_f32 v222, v222\n"
"v_mfma_f32_16x16x32_bf16 a[48:51], a[240:243], v[20:23], a[48:51]\n"
"ds_read_b64_tr_b16 a[216:217], v242 offset:49920\n"
"v_add_f32 v44, v44, v7\n"
"v_mfma_f32_16x16x32_bf16 a[52:55], a[244:247], v[20:23], a[52:55]\n"
"v_exp_f32 v223, v223\n"
"v_mfma_f32_16x16x32_bf16 a[56:59], a[248:251], v[20:23], a[56:59]\n"
"ds_read_b64_tr_b16 a[218:219], v242 offset:54016\n"
"v_cvt_pk_bf16_f32 v17, v222, v223\n"
"v_mfma_f32_16x16x32_bf16 a[60:63], a[252:255], v[20:23], a[60:63]\n"
"v_exp_f32 v224, v224\n"
"v_mfma_f32_16x16x32_bf16 v[48:51], v[40:43], v[20:23], v[48:51]\n"
"ds_read_b64_tr_b16 a[220:221], v243 offset:49920\n"
"v_add_f32 v52, v52, v220\n"
"v_mfma_f32_16x16x32_bf16 v[228:231], a[160:163], v[112:115], v[36:39]\n"
"v_exp_f32 v225, v225\n"
"v_mfma_f32_16x16x32_bf16 v[228:231], a[164:167], v[116:119], v[228:231]\n"
"ds_read_b64_tr_b16 a[222:223], v243 offset:54016\n"
"v_cvt_pk_bf16_f32 v18, v224, v225\n"
"v_mfma_f32_16x16x32_bf16 v[228:231], a[168:171], v[120:123], v[228:231]\n"
"v_exp_f32 v226, v226\n"
"v_mfma_f32_16x16x32_bf16 v[228:231], a[172:175], v[124:127], v[228:231]\n"
"v_add_f32 v52, v52, v221\n"
"v_add_f32 v52, v52, v222\n"
"v_mfma_f32_16x16x32_bf16 v[232:235], a[176:179], v[112:115], v[36:39]\n"
"v_exp_f32 v227, v227\n"
"v_mfma_f32_16x16x32_bf16 v[232:235], a[180:183], v[116:119], v[232:235]\n"
"v_cvt_pk_bf16_f32 v19, v226, v227\n"
"v_add_f32 v52, v52, v223\n"
"v_mfma_f32_16x16x32_bf16 v[232:235], a[184:187], v[120:123], v[232:235]\n"
"buffer_load_dwordx4 v247, s[8:11], s84 offen offset:3072 lds\n"
"v_mfma_f32_16x16x32_bf16 v[232:235], a[188:191], v[124:127], v[232:235]\n"
"v_exp_f32 v228, v228\n"
"v_mfma_f32_16x16x32_bf16 a[64:67], a[224:227], v[16:19], a[64:67]\n"
"v_add_f32 v52, v52, v224\n"
"v_add_f32 v52, v52, v225\n"
"v_mfma_f32_16x16x32_bf16 a[68:71], a[228:231], v[16:19], a[68:71]\n"
"v_exp_f32 v229, v229\n"
"v_mfma_f32_16x16x32_bf16 a[72:75], a[232:235], v[16:19], a[72:75]\n"
"v_cvt_pk_bf16_f32 v20, v228, v229\n"
"v_add_f32 v52, v52, v226\n"
"v_mfma_f32_16x16x32_bf16 a[76:79], a[236:239], v[16:19], a[76:79]\n"
"v_exp_f32 v230, v230\n"
"v_mfma_f32_16x16x32_bf16 a[80:83], a[240:243], v[16:19], a[80:83]\n"
"s_waitcnt lgkmcnt(8)\n"
"buffer_load_dword v250, v255, s[32:35], 0 offen\n"
"v_mfma_f32_16x16x32_bf16 a[84:87], a[244:247], v[16:19], a[84:87]\n"
"v_exp_f32 v231, v231\n"
"v_mfma_f32_16x16x32_bf16 a[88:91], a[248:251], v[16:19], a[88:91]\n"
"v_cvt_pk_bf16_f32 v21, v230, v231\n"
"v_add_f32 v52, v52, v227\n"
"v_mfma_f32_16x16x32_bf16 a[92:95], a[252:255], v[16:19], a[92:95]\n"
"v_exp_f32 v232, v232\n"
"v_mfma_f32_16x16x32_bf16 v[0:3], a[128:131], v[64:67], v[24:27]\n"
"ds_read_b128 a[160:163], v240 offset:40960\n"
"v_mfma_f32_16x16x32_bf16 v[0:3], a[132:135], v[68:71], v[0:3]\n"
"v_exp_f32 v233, v233\n"
"v_mfma_f32_16x16x32_bf16 v[0:3], a[136:139], v[72:75], v[0:3]\n"
"ds_read_b128 a[164:167], v240 offset:41216\n"
"v_cvt_pk_bf16_f32 v22, v232, v233\n"
"v_mfma_f32_16x16x32_bf16 v[0:3], a[140:143], v[76:79], v[0:3]\n"
"v_exp_f32 v234, v234\n"
"v_mfma_f32_16x16x32_bf16 v[4:7], a[144:147], v[64:67], v[24:27]\n"
"ds_read_b128 a[168:171], v240 offset:41472\n"
"v_mfma_f32_16x16x32_bf16 v[4:7], a[148:151], v[68:71], v[4:7]\n"
"v_exp_f32 v235, v235\n"
"v_mfma_f32_16x16x32_bf16 v[4:7], a[152:155], v[72:75], v[4:7]\n"
"ds_read_b128 a[172:175], v240 offset:41728\n"
"v_cvt_pk_bf16_f32 v23, v234, v235\n"
"v_mfma_f32_16x16x32_bf16 v[4:7], a[156:159], v[76:79], v[4:7]\n"
"s_add_u32 m0, s86, 16384\n"
"v_exp_f32 v0, v0\n"
"v_mfma_f32_16x16x32_bf16 a[96:99], a[224:227], v[20:23], a[96:99]\n"
"buffer_load_dwordx4 v246, s[12:15], s84 offen offset:0 lds\n"
"v_mfma_f32_16x16x32_bf16 a[100:103], a[228:231], v[20:23], a[100:103]\n"
"v_exp_f32 v1, v1\n"
"v_mfma_f32_16x16x32_bf16 a[104:107], a[232:235], v[20:23], a[104:107]\n"
"ds_read_b128 a[176:179], v240 offset:45056\n"
"v_cvt_pk_bf16_f32 v16, v0, v1\n"
"v_mfma_f32_16x16x32_bf16 a[108:111], a[236:239], v[20:23], a[108:111]\n"
"v_exp_f32 v2, v2\n"
"v_mfma_f32_16x16x32_bf16 a[112:115], a[240:243], v[20:23], a[112:115]\n"
"ds_read_b128 a[180:183], v240 offset:45312\n"
"v_add_f32 v44, v44, v0\n"
"v_mfma_f32_16x16x32_bf16 a[116:119], a[244:247], v[20:23], a[116:119]\n"
"v_exp_f32 v3, v3\n"
"v_mfma_f32_16x16x32_bf16 a[120:123], a[248:251], v[20:23], a[120:123]\n"
"ds_read_b128 a[184:187], v240 offset:45568\n"
"v_cvt_pk_bf16_f32 v17, v2, v3\n"
"v_mfma_f32_16x16x32_bf16 a[124:127], a[252:255], v[20:23], a[124:127]\n"
"v_exp_f32 v4, v4\n"
"v_mfma_f32_16x16x32_bf16 v[56:59], v[40:43], v[20:23], v[56:59]\n"
"ds_read_b128 a[188:191], v240 offset:45824\n"
"v_add_f32 v44, v44, v1\n"
"v_mfma_f32_16x16x32_bf16 v[8:11], a[128:131], v[80:83], v[28:31]\n"
"v_exp_f32 v5, v5\n"
"v_mfma_f32_16x16x32_bf16 v[8:11], a[132:135], v[84:87], v[8:11]\n"
"ds_read_b64_tr_b16 a[224:225], v242 offset:57344\n"
"v_cvt_pk_bf16_f32 v18, v4, v5\n"
"v_mfma_f32_16x16x32_bf16 v[8:11], a[136:139], v[88:91], v[8:11]\n"
"v_exp_f32 v6, v6\n"
"v_mfma_f32_16x16x32_bf16 v[8:11], a[140:143], v[92:95], v[8:11]\n"
"ds_read_b64_tr_b16 a[226:227], v242 offset:61440\n"
"v_add_f32 v44, v44, v2\n"
"v_mfma_f32_16x16x32_bf16 v[12:15], a[144:147], v[80:83], v[28:31]\n"
"v_exp_f32 v7, v7\n"
"v_mfma_f32_16x16x32_bf16 v[12:15], a[148:151], v[84:87], v[12:15]\n"
"ds_read_b64_tr_b16 a[228:229], v243 offset:57344\n"
"v_cvt_pk_bf16_f32 v19, v6, v7\n"
"v_mfma_f32_16x16x32_bf16 v[12:15], a[152:155], v[88:91], v[12:15]\n"
"buffer_load_dwordx4 v247, s[12:15], s84 offen offset:1024 lds\n"
"v_mfma_f32_16x16x32_bf16 v[12:15], a[156:159], v[92:95], v[12:15]\n"
"s_waitcnt lgkmcnt(11)\n"
"v_exp_f32 v8, v8\n"
"v_mfma_f32_16x16x32_bf16 a[0:3], a[192:195], v[16:19], a[0:3]\n"
"ds_read_b64_tr_b16 a[230:231], v243 offset:61440\n"
"v_add_f32 v44, v44, v3\n"
"v_mfma_f32_16x16x32_bf16 a[4:7], a[196:199], v[16:19], a[4:7]\n"
"v_exp_f32 v9, v9\n"
"v_mfma_f32_16x16x32_bf16 a[8:11], a[200:203], v[16:19], a[8:11]\n"
"ds_read_b64_tr_b16 a[232:233], v242 offset:57600\n"
"v_cvt_pk_bf16_f32 v20, v8, v9\n"
"v_mfma_f32_16x16x32_bf16 a[12:15], a[204:207], v[16:19], a[12:15]\n"
"v_exp_f32 v10, v10\n"
"v_mfma_f32_16x16x32_bf16 a[16:19], a[208:211], v[16:19], a[16:19]\n"
"ds_read_b64_tr_b16 a[234:235], v242 offset:61696\n"
"v_add_f32 v44, v44, v4\n"
"v_mfma_f32_16x16x32_bf16 a[20:23], a[212:215], v[16:19], a[20:23]\n"
"v_exp_f32 v11, v11\n"
"v_mfma_f32_16x16x32_bf16 a[24:27], a[216:219], v[16:19], a[24:27]\n"
"ds_read_b64_tr_b16 a[236:237], v243 offset:57600\n"
"v_cvt_pk_bf16_f32 v21, v10, v11\n"
"v_mfma_f32_16x16x32_bf16 a[28:31], a[220:223], v[16:19], a[28:31]\n"
"v_exp_f32 v12, v12\n"
"v_mfma_f32_16x16x32_bf16 v[220:223], a[128:131], v[96:99], v[32:35]\n"
"ds_read_b64_tr_b16 a[238:239], v243 offset:61696\n"
"v_add_f32 v44, v44, v5\n"
"v_mfma_f32_16x16x32_bf16 v[220:223], a[132:135], v[100:103], v[220:223]\n"
"v_exp_f32 v13, v13\n"
"v_mfma_f32_16x16x32_bf16 v[220:223], a[136:139], v[104:107], v[220:223]\n"
"ds_read_b64_tr_b16 a[240:241], v242 offset:57856\n"
"v_cvt_pk_bf16_f32 v22, v12, v13\n"
"v_mfma_f32_16x16x32_bf16 v[220:223], a[140:143], v[108:111], v[220:223]\n"
"v_exp_f32 v14, v14\n"
"v_mfma_f32_16x16x32_bf16 v[224:227], a[144:147], v[96:99], v[32:35]\n"
"ds_read_b64_tr_b16 a[242:243], v242 offset:61952\n"
"v_add_f32 v44, v44, v6\n"
"v_mfma_f32_16x16x32_bf16 v[224:227], a[148:151], v[100:103], v[224:227]\n"
"v_exp_f32 v15, v15\n"
"v_mfma_f32_16x16x32_bf16 v[224:227], a[152:155], v[104:107], v[224:227]\n"
"ds_read_b64_tr_b16 a[244:245], v243 offset:57856\n"
"v_cvt_pk_bf16_f32 v23, v14, v15\n"
"v_mfma_f32_16x16x32_bf16 v[224:227], a[156:159], v[108:111], v[224:227]\n"
"v_exp_f32 v220, v220\n"
"v_mfma_f32_16x16x32_bf16 a[32:35], a[192:195], v[20:23], a[32:35]\n"
"buffer_load_dwordx4 v246, s[12:15], s84 offen offset:2048 lds\n"
"v_mfma_f32_16x16x32_bf16 a[36:39], a[196:199], v[20:23], a[36:39]\n"
"v_exp_f32 v221, v221\n"
"v_mfma_f32_16x16x32_bf16 a[40:43], a[200:203], v[20:23], a[40:43]\n"
"ds_read_b64_tr_b16 a[246:247], v243 offset:61952\n"
"v_cvt_pk_bf16_f32 v16, v220, v221\n"
"v_mfma_f32_16x16x32_bf16 a[44:47], a[204:207], v[20:23], a[44:47]\n"
"v_exp_f32 v222, v222\n"
"v_mfma_f32_16x16x32_bf16 a[48:51], a[208:211], v[20:23], a[48:51]\n"
"ds_read_b64_tr_b16 a[248:249], v242 offset:58112\n"
"v_add_f32 v44, v44, v7\n"
"v_mfma_f32_16x16x32_bf16 a[52:55], a[212:215], v[20:23], a[52:55]\n"
"v_exp_f32 v223, v223\n"
"v_mfma_f32_16x16x32_bf16 a[56:59], a[216:219], v[20:23], a[56:59]\n"
"ds_read_b64_tr_b16 a[250:251], v242 offset:62208\n"
"v_cvt_pk_bf16_f32 v17, v222, v223\n"
"v_mfma_f32_16x16x32_bf16 a[60:63], a[220:223], v[20:23], a[60:63]\n"
"v_exp_f32 v224, v224\n"
"v_mfma_f32_16x16x32_bf16 v[48:51], v[40:43], v[20:23], v[48:51]\n"
"ds_read_b64_tr_b16 a[252:253], v243 offset:58112\n"
"v_add_f32 v52, v52, v220\n"
"v_mfma_f32_16x16x32_bf16 v[228:231], a[128:131], v[112:115], v[36:39]\n"
"v_exp_f32 v225, v225\n"
"v_mfma_f32_16x16x32_bf16 v[228:231], a[132:135], v[116:119], v[228:231]\n"
"ds_read_b64_tr_b16 a[254:255], v243 offset:62208\n"
"v_cvt_pk_bf16_f32 v18, v224, v225\n"
"v_mfma_f32_16x16x32_bf16 v[228:231], a[136:139], v[120:123], v[228:231]\n"
"v_exp_f32 v226, v226\n"
"v_mfma_f32_16x16x32_bf16 v[228:231], a[140:143], v[124:127], v[228:231]\n"
"v_add_f32 v52, v52, v221\n"
"v_add_f32 v52, v52, v222\n"
"v_mfma_f32_16x16x32_bf16 v[232:235], a[144:147], v[112:115], v[36:39]\n"
"v_exp_f32 v227, v227\n"
"v_mfma_f32_16x16x32_bf16 v[232:235], a[148:151], v[116:119], v[232:235]\n"
"v_cvt_pk_bf16_f32 v19, v226, v227\n"
"v_add_f32 v52, v52, v223\n"
"v_mfma_f32_16x16x32_bf16 v[232:235], a[152:155], v[120:123], v[232:235]\n"
"buffer_load_dwordx4 v247, s[12:15], s84 offen offset:3072 lds\n"
"v_mfma_f32_16x16x32_bf16 v[232:235], a[156:159], v[124:127], v[232:235]\n"
"s_add_u32 s84, s84, 16384\n"
"v_exp_f32 v228, v228\n"
"v_mfma_f32_16x16x32_bf16 a[64:67], a[192:195], v[16:19], a[64:67]\n"
"v_add_f32 v52, v52, v224\n"
"v_add_f32 v52, v52, v225\n"
"v_mfma_f32_16x16x32_bf16 a[68:71], a[196:199], v[16:19], a[68:71]\n"
"v_exp_f32 v229, v229\n"
"v_mfma_f32_16x16x32_bf16 a[72:75], a[200:203], v[16:19], a[72:75]\n"
"v_cvt_pk_bf16_f32 v20, v228, v229\n"
"v_add_f32 v52, v52, v226\n"
"v_mfma_f32_16x16x32_bf16 a[76:79], a[204:207], v[16:19], a[76:79]\n"
"s_waitcnt vmcnt(18) lgkmcnt(0)\n"
"v_exp_f32 v230, v230\n"
"v_mfma_f32_16x16x32_bf16 a[80:83], a[208:211], v[16:19], a[80:83]\n"
"s_barrier\n"
"v_mfma_f32_16x16x32_bf16 a[84:87], a[212:215], v[16:19], a[84:87]\n"
"v_exp_f32 v231, v231\n"
"v_mfma_f32_16x16x32_bf16 a[88:91], a[216:219], v[16:19], a[88:91]\n"
"v_cvt_pk_bf16_f32 v21, v230, v231\n"
"v_add_f32 v52, v52, v227\n"
"v_mfma_f32_16x16x32_bf16 a[92:95], a[220:223], v[16:19], a[92:95]\n"
"v_exp_f32 v232, v232\n"
"v_mfma_f32_16x16x32_bf16 v[0:3], a[160:163], v[64:67], v[24:27]\n"
"ds_read_b128 a[128:131], v241 offset:0\n"
"buffer_store_dwordx4 v[216:219], v207, s[28:31], s54 offen sc1\n"
"v_mfma_f32_16x16x32_bf16 v[0:3], a[164:167], v[68:71], v[0:3]\n"
"v_exp_f32 v233, v233\n"
"v_mfma_f32_16x16x32_bf16 v[0:3], a[168:171], v[72:75], v[0:3]\n"
"ds_read_b128 a[132:135], v241 offset:256\n"
"v_cvt_pk_bf16_f32 v22, v232, v233\n"
"v_mfma_f32_16x16x32_bf16 v[0:3], a[172:175], v[76:79], v[0:3]\n"
"v_exp_f32 v234, v234\n"
"v_mfma_f32_16x16x32_bf16 v[4:7], a[176:179], v[64:67], v[24:27]\n"
"ds_read_b128 a[136:139], v241 offset:512\n"
"v_mfma_f32_16x16x32_bf16 v[4:7], a[180:183], v[68:71], v[4:7]\n"
"v_exp_f32 v235, v235\n"
"v_mfma_f32_16x16x32_bf16 v[4:7], a[184:187], v[72:75], v[4:7]\n"
"ds_read_b128 a[140:143], v241 offset:768\n"
"v_cvt_pk_bf16_f32 v23, v234, v235\n"
"v_mfma_f32_16x16x32_bf16 v[4:7], a[188:191], v[76:79], v[4:7]\n"
"s_add_u32 m0, s86, 32768\n"
"v_exp_f32 v0, v0\n"
"v_mfma_f32_16x16x32_bf16 a[96:99], a[192:195], v[20:23], a[96:99]\n"
"buffer_load_dwordx4 v246, s[8:11], s84 offen offset:0 lds\n"
"v_mfma_f32_16x16x32_bf16 a[100:103], a[196:199], v[20:23], a[100:103]\n"
"v_exp_f32 v1, v1\n"
"v_mfma_f32_16x16x32_bf16 a[104:107], a[200:203], v[20:23], a[104:107]\n"
"ds_read_b128 a[144:147], v241 offset:4096\n"
"v_cvt_pk_bf16_f32 v16, v0, v1\n"
"v_mfma_f32_16x16x32_bf16 a[108:111], a[204:207], v[20:23], a[108:111]\n"
"v_exp_f32 v2, v2\n"
"v_mfma_f32_16x16x32_bf16 a[112:115], a[208:211], v[20:23], a[112:115]\n"
"ds_read_b128 a[148:151], v241 offset:4352\n"
"v_add_f32 v44, v44, v0\n"
"v_mfma_f32_16x16x32_bf16 a[116:119], a[212:215], v[20:23], a[116:119]\n"
"AT_pg7:\n"
"s_cbranch_execz AT_hr\n"
"v_exp_f32 v3, v3\n"
"v_mfma_f32_16x16x32_bf16 a[120:123], a[216:219], v[20:23], a[120:123]\n"
"ds_read_b128 a[152:155], v241 offset:4608\n"
"v_cvt_pk_bf16_f32 v17, v2, v3\n"
"v_mfma_f32_16x16x32_bf16 a[124:127], a[220:223], v[20:23], a[124:127]\n"
"v_exp_f32 v4, v4\n"
"v_mfma_f32_16x16x32_bf16 v[56:59], v[40:43], v[20:23], v[56:59]\n"
"ds_read_b128 a[156:159], v241 offset:4864\n"
"v_add_f32 v44, v44, v1\n"
"v_mfma_f32_16x16x32_bf16 v[8:11], a[160:163], v[80:83], v[28:31]\n"
"v_exp_f32 v5, v5\n"
"v_mfma_f32_16x16x32_bf16 v[8:11], a[164:167], v[84:87], v[8:11]\n"
"ds_read_b64_tr_b16 a[192:193], v244 offset:16384\n"
"v_cvt_pk_bf16_f32 v18, v4, v5\n"
"v_mfma_f32_16x16x32_bf16 v[8:11], a[168:171], v[88:91], v[8:11]\n"
"v_exp_f32 v6, v6\n"
"v_mfma_f32_16x16x32_bf16 v[8:11], a[172:175], v[92:95], v[8:11]\n"
"ds_read_b64_tr_b16 a[194:195], v244 offset:20480\n"
"v_add_f32 v44, v44, v2\n"
"v_mfma_f32_16x16x32_bf16 v[12:15], a[176:179], v[80:83], v[28:31]\n"
"v_exp_f32 v7, v7\n"
"v_mfma_f32_16x16x32_bf16 v[12:15], a[180:183], v[84:87], v[12:15]\n"
"ds_read_b64_tr_b16 a[196:197], v245 offset:16384\n"
"v_cvt_pk_bf16_f32 v19, v6, v7\n"
"v_mfma_f32_16x16x32_bf16 v[12:15], a[184:187], v[88:91], v[12:15]\n"
"buffer_load_dwordx4 v246, s[8:11], s84 offen offset:1024 lds\n"
"v_mfma_f32_16x16x32_bf16 v[12:15], a[188:191], v[92:95], v[12:15]\n"
"s_waitcnt lgkmcnt(11)\n"
"v_exp_f32 v8, v8\n"
"v_mfma_f32_16x16x32_bf16 a[0:3], a[224:227], v[16:19], a[0:3]\n"
"ds_read_b64_tr_b16 a[198:199], v245 offset:20480\n"
"v_add_f32 v44, v44, v3\n"
"v_mfma_f32_16x16x32_bf16 a[4:7], a[228:231], v[16:19], a[4:7]\n"
"v_exp_f32 v9, v9\n"
"v_mfma_f32_16x16x32_bf16 a[8:11], a[232:235], v[16:19], a[8:11]\n"
"ds_read_b64_tr_b16 a[200:201], v244 offset:16640\n"
"v_cvt_pk_bf16_f32 v20, v8, v9\n"
"v_mfma_f32_16x16x32_bf16 a[12:15], a[236:239], v[16:19], a[12:15]\n"
"v_exp_f32 v10, v10\n"
"v_mfma_f32_16x16x32_bf16 a[16:19], a[240:243], v[16:19], a[16:19]\n"
"ds_read_b64_tr_b16 a[202:203], v244 offset:20736\n"
"v_add_f32 v44, v44, v4\n"
"v_mfma_f32_16x16x32_bf16 a[20:23], a[244:247], v[16:19], a[20:23]\n"
"v_exp_f32 v11, v11\n"
"v_mfma_f32_16x16x32_bf16 a[24:27], a[248:251], v[16:19], a[24:27]\n"
"ds_read_b64_tr_b16 a[204:205], v245 offset:16640\n"
"v_cvt_pk_bf16_f32 v21, v10, v11\n"
"v_mfma_f32_16x16x32_bf16 a[28:31], a[252:255], v[16:19], a[28:31]\n"
"v_exp_f32 v12, v12\n"
"v_mfma_f32_16x16x32_bf16 v[220:223], a[160:163], v[96:99], v[32:35]\n"
"ds_read_b64_tr_b16 a[206:207], v245 offset:20736\n"
"v_add_f32 v44, v44, v5\n"
"v_mfma_f32_16x16x32_bf16 v[220:223], a[164:167], v[100:103], v[220:223]\n"
"v_exp_f32 v13, v13\n"
"v_mfma_f32_16x16x32_bf16 v[220:223], a[168:171], v[104:107], v[220:223]\n"
"ds_read_b64_tr_b16 a[208:209], v244 offset:16896\n"
"v_cvt_pk_bf16_f32 v22, v12, v13\n"
"v_mfma_f32_16x16x32_bf16 v[220:223], a[172:175], v[108:111], v[220:223]\n"
"v_exp_f32 v14, v14\n"
"v_mfma_f32_16x16x32_bf16 v[224:227], a[176:179], v[96:99], v[32:35]\n"
"ds_read_b64_tr_b16 a[210:211], v244 offset:20992\n"
"v_add_f32 v44, v44, v6\n"
"v_mfma_f32_16x16x32_bf16 v[224:227], a[180:183], v[100:103], v[224:227]\n"
"v_exp_f32 v15, v15\n"
"v_mfma_f32_16x16x32_bf16 v[224:227], a[184:187], v[104:107], v[224:227]\n"
"ds_read_b64_tr_b16 a[212:213], v245 offset:16896\n"
"v_cvt_pk_bf16_f32 v23, v14, v15\n"
"v_mfma_f32_16x16x32_bf16 v[224:227], a[188:191], v[108:111], v[224:227]\n"
"v_exp_f32 v220, v220\n"
"v_mfma_f32_16x16x32_bf16 a[32:35], a[224:227], v[20:23], a[32:35]\n"
"buffer_load_dwordx4 v247, s[8:11], s84 offen offset:2048 lds\n"
"v_mfma_f32_16x16x32_bf16 a[36:39], a[228:231], v[20:23], a[36:39]\n"
"v_exp_f32 v221, v221\n"
"v_mfma_f32_16x16x32_bf16 a[40:43], a[232:235], v[20:23], a[40:43]\n"
"ds_read_b64_tr_b16 a[214:215], v245 offset:20992\n"
"v_cvt_pk_bf16_f32 v16, v220, v221\n"
"v_mfma_f32_16x16x32_bf16 a[44:47], a[236:239], v[20:23], a[44:47]\n"
"v_exp_f32 v222, v222\n"
"v_mfma_f32_16x16x32_bf16 a[48:51], a[240:243], v[20:23], a[48:51]\n"
"ds_read_b64_tr_b16 a[216:217], v244 offset:17152\n"
"v_add_f32 v44, v44, v7\n"
"v_mfma_f32_16x16x32_bf16 a[52:55], a[244:247], v[20:23], a[52:55]\n"
"v_exp_f32 v223, v223\n"
"v_mfma_f32_16x16x32_bf16 a[56:59], a[248:251], v[20:23], a[56:59]\n"
"ds_read_b64_tr_b16 a[218:219], v244 offset:21248\n"
"v_cvt_pk_bf16_f32 v17, v222, v223\n"
"v_mfma_f32_16x16x32_bf16 a[60:63], a[252:255], v[20:23], a[60:63]\n"
"v_exp_f32 v224, v224\n"
"v_mfma_f32_16x16x32_bf16 v[48:51], v[40:43], v[20:23], v[48:51]\n"
"ds_read_b64_tr_b16 a[220:221], v245 offset:17152\n"
"v_add_f32 v52, v52, v220\n"
"v_mfma_f32_16x16x32_bf16 v[228:231], a[160:163], v[112:115], v[36:39]\n"
"v_exp_f32 v225, v225\n"
"v_mfma_f32_16x16x32_bf16 v[228:231], a[164:167], v[116:119], v[228:231]\n"
"ds_read_b64_tr_b16 a[222:223], v245 offset:21248\n"
"v_cvt_pk_bf16_f32 v18, v224, v225\n"
"v_mfma_f32_16x16x32_bf16 v[228:231], a[168:171], v[120:123], v[228:231]\n"
"v_exp_f32 v226, v226\n"
"v_mfma_f32_16x16x32_bf16 v[228:231], a[172:175], v[124:127], v[228:231]\n"
"v_add_f32 v52, v52, v221\n"
"v_add_f32 v52, v52, v222\n"
"v_mfma_f32_16x16x32_bf16 v[232:235], a[176:179], v[112:115], v[36:39]\n"
"v_exp_f32 v227, v227\n"
"v_mfma_f32_16x16x32_bf16 v[232:235], a[180:183], v[116:119], v[232:235]\n"
"v_cvt_pk_bf16_f32 v19, v226, v227\n"
"v_add_f32 v52, v52, v223\n"
"v_mfma_f32_16x16x32_bf16 v[232:235], a[184:187], v[120:123], v[232:235]\n"
"buffer_load_dwordx4 v247, s[8:11], s84 offen offset:3072 lds\n"
"v_mfma_f32_16x16x32_bf16 v[232:235], a[188:191], v[124:127], v[232:235]\n"
"v_exp_f32 v228, v228\n"
"v_mfma_f32_16x16x32_bf16 a[64:67], a[224:227], v[16:19], a[64:67]\n"
"v_add_f32 v52, v52, v224\n"
"v_add_f32 v52, v52, v225\n"
"v_mfma_f32_16x16x32_bf16 a[68:71], a[228:231], v[16:19], a[68:71]\n"
"v_exp_f32 v229, v229\n"
"v_mfma_f32_16x16x32_bf16 a[72:75], a[232:235], v[16:19], a[72:75]\n"
"v_cvt_pk_bf16_f32 v20, v228, v229\n"
"v_add_f32 v52, v52, v226\n"
"v_mfma_f32_16x16x32_bf16 a[76:79], a[236:239], v[16:19], a[76:79]\n"
"v_exp_f32 v230, v230\n"
"v_mfma_f32_16x16x32_bf16 a[80:83], a[240:243], v[16:19], a[80:83]\n"
"s_waitcnt lgkmcnt(8)\n"
"buffer_load_dword v250, v251, s[68:71], 0 offen\n"
"v_mfma_f32_16x16x32_bf16 a[84:87], a[244:247], v[16:19], a[84:87]\n"
"v_exp_f32 v231, v231\n"
"v_mfma_f32_16x16x32_bf16 a[88:91], a[248:251], v[16:19], a[88:91]\n"
"v_cvt_pk_bf16_f32 v21, v230, v231\n"
"v_add_f32 v52, v52, v227\n"
"v_mfma_f32_16x16x32_bf16 a[92:95], a[252:255], v[16:19], a[92:95]\n"
"v_exp_f32 v232, v232\n"
"v_mfma_f32_16x16x32_bf16 v[0:3], a[128:131], v[64:67], v[24:27]\n"
"ds_read_b128 a[160:163], v241 offset:8192\n"
"v_add_u32 v251, 1179648, v251\n"
"v_mfma_f32_16x16x32_bf16 v[0:3], a[132:135], v[68:71], v[0:3]\n"
"v_exp_f32 v233, v233\n"
"v_mfma_f32_16x16x32_bf16 v[0:3], a[136:139], v[72:75], v[0:3]\n"
"ds_read_b128 a[164:167], v241 offset:8448\n"
"v_cvt_pk_bf16_f32 v22, v232, v233\n"
"v_mfma_f32_16x16x32_bf16 v[0:3], a[140:143], v[76:79], v[0:3]\n"
"v_exp_f32 v234, v234\n"
"v_mfma_f32_16x16x32_bf16 v[4:7], a[144:147], v[64:67], v[24:27]\n"
"ds_read_b128 a[168:171], v241 offset:8704\n"
"v_mfma_f32_16x16x32_bf16 v[4:7], a[148:151], v[68:71], v[4:7]\n"
"v_exp_f32 v235, v235\n"
"v_mfma_f32_16x16x32_bf16 v[4:7], a[152:155], v[72:75], v[4:7]\n"
"ds_read_b128 a[172:175], v241 offset:8960\n"
"v_cvt_pk_bf16_f32 v23, v234, v235\n"
"v_mfma_f32_16x16x32_bf16 v[4:7], a[156:159], v[76:79], v[4:7]\n"
"s_add_u32 m0, s86, 49152\n"
"v_exp_f32 v0, v0\n"
"v_mfma_f32_16x16x32_bf16 a[96:99], a[224:227], v[20:23], a[96:99]\n"
"buffer_load_dwordx4 v246, s[12:15], s84 offen offset:0 lds\n"
"v_mfma_f32_16x16x32_bf16 a[100:103], a[228:231], v[20:23], a[100:103]\n"
"v_exp_f32 v1, v1\n"
"v_mfma_f32_16x16x32_bf16 a[104:107], a[232:235], v[20:23], a[104:107]\n"
"ds_read_b128 a[176:179], v241 offset:12288\n"
"v_cvt_pk_bf16_f32 v16, v0, v1\n"
"v_mfma_f32_16x16x32_bf16 a[108:111], a[236:239], v[20:23], a[108:111]\n"
"v_exp_f32 v2, v2\n"
"v_mfma_f32_16x16x32_bf16 a[112:115], a[240:243], v[20:23], a[112:115]\n"
"ds_read_b128 a[180:183], v241 offset:12544\n"
"v_add_f32 v44, v44, v0\n"
"v_mfma_f32_16x16x32_bf16 a[116:119], a[244:247], v[20:23], a[116:119]\n"
"v_exp_f32 v3, v3\n"
"v_mfma_f32_16x16x32_bf16 a[120:123], a[248:251], v[20:23], a[120:123]\n"
"ds_read_b128 a[184:187], v241 offset:12800\n"
"v_cvt_pk_bf16_f32 v17, v2, v3\n"
"v_mfma_f32_16x16x32_bf16 a[124:127], a[252:255], v[20:23], a[124:127]\n"
"v_exp_f32 v4, v4\n"
"v_mfma_f32_16x16x32_bf16 v[56:59], v[40:43], v[20:23], v[56:59]\n"
"ds_read_b128 a[188:191], v241 offset:13056\n"
"v_add_f32 v44, v44, v1\n"
"v_mfma_f32_16x16x32_bf16 v[8:11], a[128:131], v[80:83], v[28:31]\n"
"v_exp_f32 v5, v5\n"
"v_mfma_f32_16x16x32_bf16 v[8:11], a[132:135], v[84:87], v[8:11]\n"
"ds_read_b64_tr_b16 a[224:225], v244 offset:24576\n"
"v_cvt_pk_bf16_f32 v18, v4, v5\n"
"v_mfma_f32_16x16x32_bf16 v[8:11], a[136:139], v[88:91], v[8:11]\n"
"v_exp_f32 v6, v6\n"
"v_mfma_f32_16x16x32_bf16 v[8:11], a[140:143], v[92:95], v[8:11]\n"
"ds_read_b64_tr_b16 a[226:227], v244 offset:28672\n"
"v_add_f32 v44, v44, v2\n"
"v_mfma_f32_16x16x32_bf16 v[12:15], a[144:147], v[80:83], v[28:31]\n"
"v_exp_f32 v7, v7\n"
"v_mfma_f32_16x16x32_bf16 v[12:15], a[148:151], v[84:87], v[12:15]\n"
"ds_read_b64_tr_b16 a[228:229], v245 offset:24576\n"
"v_cvt_pk_bf16_f32 v19, v6, v7\n"
"v_mfma_f32_16x16x32_bf16 v[12:15], a[152:155], v[88:91], v[12:15]\n"
"buffer_load_dwordx4 v247, s[12:15], s84 offen offset:1024 lds\n"
"v_mfma_f32_16x16x32_bf16 v[12:15], a[156:159], v[92:95], v[12:15]\n"
"s_waitcnt lgkmcnt(11)\n"
"v_exp_f32 v8, v8\n"
"v_mfma_f32_16x16x32_bf16 a[0:3], a[192:195], v[16:19], a[0:3]\n"
"ds_read_b64_tr_b16 a[230:231], v245 offset:28672\n"
"v_add_f32 v44, v44, v3\n"
"v_mfma_f32_16x16x32_bf16 a[4:7], a[196:199], v[16:19], a[4:7]\n"
"v_exp_f32 v9, v9\n"
"v_mfma_f32_16x16x32_bf16 a[8:11], a[200:203], v[16:19], a[8:11]\n"
"ds_read_b64_tr_b16 a[232:233], v244 offset:24832\n"
"v_cvt_pk_bf16_f32 v20, v8, v9\n"
"v_mfma_f32_16x16x32_bf16 a[12:15], a[204:207], v[16:19], a[12:15]\n"
"v_exp_f32 v10, v10\n"
"v_mfma_f32_16x16x32_bf16 a[16:19], a[208:211], v[16:19], a[16:19]\n"
"ds_read_b64_tr_b16 a[234:235], v244 offset:28928\n"
"v_add_f32 v44, v44, v4\n"
"v_mfma_f32_16x16x32_bf16 a[20:23], a[212:215], v[16:19], a[20:23]\n"
"v_exp_f32 v11, v11\n"
"v_mfma_f32_16x16x32_bf16 a[24:27], a[216:219], v[16:19], a[24:27]\n"
"ds_read_b64_tr_b16 a[236:237], v245 offset:24832\n"
"v_cvt_pk_bf16_f32 v21, v10, v11\n"
"v_mfma_f32_16x16x32_bf16 a[28:31], a[220:223], v[16:19], a[28:31]\n"
"v_exp_f32 v12, v12\n"
"v_mfma_f32_16x16x32_bf16 v[220:223], a[128:131], v[96:99], v[32:35]\n"
"ds_read_b64_tr_b16 a[238:239], v245 offset:28928\n"
"v_add_f32 v44, v44, v5\n"
"v_mfma_f32_16x16x32_bf16 v[220:223], a[132:135], v[100:103], v[220:223]\n"
"v_exp_f32 v13, v13\n"
"v_mfma_f32_16x16x32_bf16 v[220:223], a[136:139], v[104:107], v[220:223]\n"
"ds_read_b64_tr_b16 a[240:241], v244 offset:25088\n"
"v_cvt_pk_bf16_f32 v22, v12, v13\n"
"v_mfma_f32_16x16x32_bf16 v[220:223], a[140:143], v[108:111], v[220:223]\n"
"v_exp_f32 v14, v14\n"
"v_mfma_f32_16x16x32_bf16 v[224:227], a[144:147], v[96:99], v[32:35]\n"
"ds_read_b64_tr_b16 a[242:243], v244 offset:29184\n"
"v_add_f32 v44, v44, v6\n"
"v_mfma_f32_16x16x32_bf16 v[224:227], a[148:151], v[100:103], v[224:227]\n"
"v_exp_f32 v15, v15\n"
"v_mfma_f32_16x16x32_bf16 v[224:227], a[152:155], v[104:107], v[224:227]\n"
"ds_read_b64_tr_b16 a[244:245], v245 offset:25088\n"
"v_cvt_pk_bf16_f32 v23, v14, v15\n"
"v_mfma_f32_16x16x32_bf16 v[224:227], a[156:159], v[108:111], v[224:227]\n"
"v_exp_f32 v220, v220\n"
"v_mfma_f32_16x16x32_bf16 a[32:35], a[192:195], v[20:23], a[32:35]\n"
"buffer_load_dwordx4 v246, s[12:15], s84 offen offset:2048 lds\n"
"v_mfma_f32_16x16x32_bf16 a[36:39], a[196:199], v[20:23], a[36:39]\n"
"v_exp_f32 v221, v221\n"
"v_mfma_f32_16x16x32_bf16 a[40:43], a[200:203], v[20:23], a[40:43]\n"
"ds_read_b64_tr_b16 a[246:247], v245 offset:29184\n"
"v_cvt_pk_bf16_f32 v16, v220, v221\n"
"v_mfma_f32_16x16x32_bf16 a[44:47], a[204:207], v[20:23], a[44:47]\n"
"v_exp_f32 v222, v222\n"
"v_mfma_f32_16x16x32_bf16 a[48:51], a[208:211], v[20:23], a[48:51]\n"
"ds_read_b64_tr_b16 a[248:249], v244 offset:25344\n"
"v_add_f32 v44, v44, v7\n"
"v_mfma_f32_16x16x32_bf16 a[52:55], a[212:215], v[20:23], a[52:55]\n"
"v_exp_f32 v223, v223\n"
"v_mfma_f32_16x16x32_bf16 a[56:59], a[216:219], v[20:23], a[56:59]\n"
"ds_read_b64_tr_b16 a[250:251], v244 offset:29440\n"
"v_cvt_pk_bf16_f32 v17, v222, v223\n"
"v_mfma_f32_16x16x32_bf16 a[60:63], a[220:223], v[20:23], a[60:63]\n"
"v_exp_f32 v224, v224\n"
"v_mfma_f32_16x16x32_bf16 v[48:51], v[40:43], v[20:23], v[48:51]\n"
"ds_read_b64_tr_b16 a[252:253], v245 offset:25344\n"
"v_add_f32 v52, v52, v220\n"
"v_mfma_f32_16x16x32_bf16 v[228:231], a[128:131], v[112:115], v[36:39]\n"
"v_exp_f32 v225, v225\n"
"v_mfma_f32_16x16x32_bf16 v[228:231], a[132:135], v[116:119], v[228:231]\n"
"ds_read_b64_tr_b16 a[254:255], v245 offset:29440\n"
"v_cvt_pk_bf16_f32 v18, v224, v225\n"
"v_mfma_f32_16x16x32_bf16 v[228:231], a[136:139], v[120:123], v[228:231]\n"
"v_exp_f32 v226, v226\n"
"v_mfma_f32_16x16x32_bf16 v[228:231], a[140:143], v[124:127], v[228:231]\n"
"v_add_f32 v52, v52, v221\n"
"v_add_f32 v52, v52, v222\n"
"v_mfma_f32_16x16x32_bf16 v[232:235], a[144:147], v[112:115], v[36:39]\n"
"v_exp_f32 v227, v227\n"
"v_mfma_f32_16x16x32_bf16 v[232:235], a[148:151], v[116:119], v[232:235]\n"
"v_cvt_pk_bf16_f32 v19, v226, v227\n"
"v_add_f32 v52, v52, v223\n"
"v_mfma_f32_16x16x32_bf16 v[232:235], a[152:155], v[120:123], v[232:235]\n"
"buffer_load_dwordx4 v247, s[12:15], s84 offen offset:3072 lds\n"
"v_mfma_f32_16x16x32_bf16 v[232:235], a[156:159], v[124:127], v[232:235]\n"
"s_add_u32 s84, s84, 16384\n"
"v_exp_f32 v228, v228\n"
"v_mfma_f32_16x16x32_bf16 a[64:67], a[192:195], v[16:19], a[64:67]\n"
"v_add_f32 v52, v52, v224\n"
"v_add_f32 v52, v52, v225\n"
"v_mfma_f32_16x16x32_bf16 a[68:71], a[196:199], v[16:19], a[68:71]\n"
"v_exp_f32 v229, v229\n"
"v_mfma_f32_16x16x32_bf16 a[72:75], a[200:203], v[16:19], a[72:75]\n"
"v_cvt_pk_bf16_f32 v20, v228, v229\n"
"v_add_f32 v52, v52, v226\n"
"v_mfma_f32_16x16x32_bf16 a[76:79], a[204:207], v[16:19], a[76:79]\n"
"s_waitcnt vmcnt(20) lgkmcnt(0)\n"
"v_exp_f32 v230, v230\n"
"v_mfma_f32_16x16x32_bf16 a[80:83], a[208:211], v[16:19], a[80:83]\n"
"s_barrier\n"
"v_mfma_f32_16x16x32_bf16 a[84:87], a[212:215], v[16:19], a[84:87]\n"
"v_exp_f32 v231, v231\n"
"v_mfma_f32_16x16x32_bf16 a[88:91], a[216:219], v[16:19], a[88:91]\n"
"v_cvt_pk_bf16_f32 v21, v230, v231\n"
"v_add_f32 v52, v52, v227\n"
"v_mfma_f32_16x16x32_bf16 a[92:95], a[220:223], v[16:19], a[92:95]\n"
"v_exp_f32 v232, v232\n"
"v_mfma_f32_16x16x32_bf16 v[0:3], a[160:163], v[64:67], v[24:27]\n"
"ds_read_b128 a[128:131], v241 offset:32768\n"
"buffer_store_dwordx4 v[216:219], v207, s[28:31], s52 offen sc1\n"
"v_mfma_f32_16x16x32_bf16 v[0:3], a[164:167], v[68:71], v[0:3]\n"
"s_add_u32 s52, s52, 1024\n"
"v_exp_f32 v233, v233\n"
"v_mfma_f32_16x16x32_bf16 v[0:3], a[168:171], v[72:75], v[0:3]\n"
"s_min_u32 s52, s52, s53\n"
"ds_read_b128 a[132:135], v241 offset:33024\n"
"v_cvt_pk_bf16_f32 v22, v232, v233\n"
"v_mfma_f32_16x16x32_bf16 v[0:3], a[172:175], v[76:79], v[0:3]\n"
"v_exp_f32 v234, v234\n"
"v_mfma_f32_16x16x32_bf16 v[4:7], a[176:179], v[64:67], v[24:27]\n"
"ds_read_b128 a[136:139], v241 offset:33280\n"
"v_mfma_f32_16x16x32_bf16 v[4:7], a[180:183], v[68:71], v[4:7]\n"
"v_exp_f32 v235, v235\n"
"v_mfma_f32_16x16x32_bf16 v[4:7], a[184:187], v[72:75], v[4:7]\n"
"ds_read_b128 a[140:143], v241 offset:33536\n"
"v_cvt_pk_bf16_f32 v23, v234, v235\n"
"v_mfma_f32_16x16x32_bf16 v[4:7], a[188:191], v[76:79], v[4:7]\n"
"s_add_u32 m0, s86, 65536\n"
"v_exp_f32 v0, v0\n"
"v_mfma_f32_16x16x32_bf16 a[96:99], a[192:195], v[20:23], a[96:99]\n"
"buffer_load_dwordx4 v246, s[8:11], s84 offen offset:0 lds\n"
"v_mfma_f32_16x16x32_bf16 a[100:103], a[196:199], v[20:23], a[100:103]\n"
"v_exp_f32 v1, v1\n"
"v_mfma_f32_16x16x32_bf16 a[104:107], a[200:203], v[20:23], a[104:107]\n"
"ds_read_b128 a[144:147], v241 offset:36864\n"
"v_cvt_pk_bf16_f32 v16, v0, v1\n"
"v_mfma_f32_16x16x32_bf16 a[108:111], a[204:207], v[20:23], a[108:111]\n"
"v_exp_f32 v2, v2\n"
"v_mfma_f32_16x16x32_bf16 a[112:115], a[208:211], v[20:23], a[112:115]\n"
"ds_read_b128 a[148:151], v241 offset:37120\n"
"v_add_f32 v44, v44, v0\n"
"v_mfma_f32_16x16x32_bf16 a[116:119], a[212:215], v[20:23], a[116:119]\n"
"v_exp_f32 v3, v3\n"
"v_mfma_f32_16x16x32_bf16 a[120:123], a[216:219], v[20:23], a[120:123]\n"
"ds_read_b128 a[152:155], v241 offset:37376\n"
"v_cvt_pk_bf16_f32 v17, v2, v3\n"
"v_mfma_f32_16x16x32_bf16 a[124:127], a[220:223], v[20:23], a[124:127]\n"
"v_exp_f32 v4, v4\n"
"v_mfma_f32_16x16x32_bf16 v[56:59], v[40:43], v[20:23], v[56:59]\n"
"ds_read_b128 a[156:159], v241 offset:37632\n"
"v_add_f32 v44, v44, v1\n"
"v_mfma_f32_16x16x32_bf16 v[8:11], a[160:163], v[80:83], v[28:31]\n"
"v_exp_f32 v5, v5\n"
"v_mfma_f32_16x16x32_bf16 v[8:11], a[164:167], v[84:87], v[8:11]\n"
"ds_read_b64_tr_b16 a[192:193], v244 offset:49152\n"
"v_cvt_pk_bf16_f32 v18, v4, v5\n"
"v_mfma_f32_16x16x32_bf16 v[8:11], a[168:171], v[88:91], v[8:11]\n"
"v_exp_f32 v6, v6\n"
"v_mfma_f32_16x16x32_bf16 v[8:11], a[172:175], v[92:95], v[8:11]\n"
"ds_read_b64_tr_b16 a[194:195], v244 offset:53248\n"
"v_add_f32 v44, v44, v2\n"
"v_mfma_f32_16x16x32_bf16 v[12:15], a[176:179], v[80:83], v[28:31]\n"
"v_exp_f32 v7, v7\n"
"v_mfma_f32_16x16x32_bf16 v[12:15], a[180:183], v[84:87], v[12:15]\n"
"ds_read_b64_tr_b16 a[196:197], v245 offset:49152\n"
"v_cvt_pk_bf16_f32 v19, v6, v7\n"
"v_mfma_f32_16x16x32_bf16 v[12:15], a[184:187], v[88:91], v[12:15]\n"
"buffer_load_dwordx4 v246, s[8:11], s84 offen offset:1024 lds\n"
"v_mfma_f32_16x16x32_bf16 v[12:15], a[188:191], v[92:95], v[12:15]\n"
"s_waitcnt lgkmcnt(11)\n"
"v_exp_f32 v8, v8\n"
"v_mfma_f32_16x16x32_bf16 a[0:3], a[224:227], v[16:19], a[0:3]\n"
"ds_read_b64_tr_b16 a[198:199], v245 offset:53248\n"
"v_add_f32 v44, v44, v3\n"
"v_mfma_f32_16x16x32_bf16 a[4:7], a[228:231], v[16:19], a[4:7]\n"
"v_exp_f32 v9, v9\n"
"v_mfma_f32_16x16x32_bf16 a[8:11], a[232:235], v[16:19], a[8:11]\n"
"ds_read_b64_tr_b16 a[200:201], v244 offset:49408\n"
"v_cvt_pk_bf16_f32 v20, v8, v9\n"
"v_mfma_f32_16x16x32_bf16 a[12:15], a[236:239], v[16:19], a[12:15]\n"
"v_exp_f32 v10, v10\n"
"v_mfma_f32_16x16x32_bf16 a[16:19], a[240:243], v[16:19], a[16:19]\n"
"ds_read_b64_tr_b16 a[202:203], v244 offset:53504\n"
"v_add_f32 v44, v44, v4\n"
"v_mfma_f32_16x16x32_bf16 a[20:23], a[244:247], v[16:19], a[20:23]\n"
"v_exp_f32 v11, v11\n"
"v_mfma_f32_16x16x32_bf16 a[24:27], a[248:251], v[16:19], a[24:27]\n"
"ds_read_b64_tr_b16 a[204:205], v245 offset:49408\n"
"v_cvt_pk_bf16_f32 v21, v10, v11\n"
"v_mfma_f32_16x16x32_bf16 a[28:31], a[252:255], v[16:19], a[28:31]\n"
"v_exp_f32 v12, v12\n"
"v_mfma_f32_16x16x32_bf16 v[220:223], a[160:163], v[96:99], v[32:35]\n"
"ds_read_b64_tr_b16 a[206:207], v245 offset:53504\n"
"v_add_f32 v44, v44, v5\n"
"v_mfma_f32_16x16x32_bf16 v[220:223], a[164:167], v[100:103], v[220:223]\n"
"v_exp_f32 v13, v13\n"
"v_mfma_f32_16x16x32_bf16 v[220:223], a[168:171], v[104:107], v[220:223]\n"
"ds_read_b64_tr_b16 a[208:209], v244 offset:49664\n"
"v_cvt_pk_bf16_f32 v22, v12, v13\n"
"v_mfma_f32_16x16x32_bf16 v[220:223], a[172:175], v[108:111], v[220:223]\n"
"v_exp_f32 v14, v14\n"
"v_mfma_f32_16x16x32_bf16 v[224:227], a[176:179], v[96:99], v[32:35]\n"
"ds_read_b64_tr_b16 a[210:211], v244 offset:53760\n"
"v_add_f32 v44, v44, v6\n"
"v_mfma_f32_16x16x32_bf16 v[224:227], a[180:183], v[100:103], v[224:227]\n"
"v_exp_f32 v15, v15\n"
"v_mfma_f32_16x16x32_bf16 v[224:227], a[184:187], v[104:107], v[224:227]\n"
"ds_read_b64_tr_b16 a[212:213], v245 offset:49664\n"
"v_cvt_pk_bf16_f32 v23, v14, v15\n"
"v_mfma_f32_16x16x32_bf16 v[224:227], a[188:191], v[108:111], v[224:227]\n"
"v_exp_f32 v220, v220\n"
"v_mfma_f32_16x16x32_bf16 a[32:35], a[224:227], v[20:23], a[32:35]\n"
"buffer_load_dwordx4 v247, s[8:11], s84 offen offset:2048 lds\n"
"v_mfma_f32_16x16x32_bf16 a[36:39], a[228:231], v[20:23], a[36:39]\n"
"v_exp_f32 v221, v221\n"
"v_mfma_f32_16x16x32_bf16 a[40:43], a[232:235], v[20:23], a[40:43]\n"
"ds_read_b64_tr_b16 a[214:215], v245 offset:53760\n"
"v_cvt_pk_bf16_f32 v16, v220, v221\n"
"v_mfma_f32_16x16x32_bf16 a[44:47], a[236:239], v[20:23], a[44:47]\n"
"v_exp_f32 v222, v222\n"
"v_mfma_f32_16x16x32_bf16 a[48:51], a[240:243], v[20:23], a[48:51]\n"
"ds_read_b64_tr_b16 a[216:217], v244 offset:49920\n"
"v_add_f32 v44, v44, v7\n"
"v_mfma_f32_16x16x32_bf16 a[52:55], a[244:247], v[20:23], a[52:55]\n"
"v_exp_f32 v223, v223\n"
"v_mfma_f32_16x16x32_bf16 a[56:59], a[248:251], v[20:23], a[56:59]\n"
"ds_read_b64_tr_b16 a[218:219], v244 offset:54016\n"
"v_cvt_pk_bf16_f32 v17, v222, v223\n"
"v_mfma_f32_16x16x32_bf16 a[60:63], a[252:255], v[20:23], a[60:63]\n"
"v_exp_f32 v224, v224\n"
"v_mfma_f32_16x16x32_bf16 v[48:51], v[40:43], v[20:23], v[48:51]\n"
"ds_read_b64_tr_b16 a[220:221], v245 offset:49920\n"
"v_add_f32 v52, v52, v220\n"
"v_mfma_f32_16x16x32_bf16 v[228:231], a[160:163], v[112:115], v[36:39]\n"
"v_exp_f32 v225, v225\n"
"v_mfma_f32_16x16x32_bf16 v[228:231], a[164:167], v[116:119], v[228:231]\n"
"ds_read_b64_tr_b16 a[222:223], v245 offset:54016\n"
"v_cvt_pk_bf16_f32 v18, v224, v225\n"
"v_mfma_f32_16x16x32_bf16 v[228:231], a[168:171], v[120:123], v[228:231]\n"
"v_exp_f32 v226, v226\n"
"v_mfma_f32_16x16x32_bf16 v[228:231], a[172:175], v[124:127], v[228:231]\n"
"v_add_f32 v52, v52, v221\n"
"v_add_f32 v52, v52, v222\n"
"v_mfma_f32_16x16x32_bf16 v[232:235], a[176:179], v[112:115], v[36:39]\n"
"v_exp_f32 v227, v227\n"
"v_mfma_f32_16x16x32_bf16 v[232:235], a[180:183], v[116:119], v[232:235]\n"
"v_cvt_pk_bf16_f32 v19, v226, v227\n"
"v_add_f32 v52, v52, v223\n"
"v_mfma_f32_16x16x32_bf16 v[232:235], a[184:187], v[120:123], v[232:235]\n"
"buffer_load_dwordx4 v247, s[8:11], s84 offen offset:3072 lds\n"
"v_mfma_f32_16x16x32_bf16 v[232:235], a[188:191], v[124:127], v[232:235]\n"
"v_exp_f32 v228, v228\n"
"v_mfma_f32_16x16x32_bf16 a[64:67], a[224:227], v[16:19], a[64:67]\n"
"v_add_f32 v52, v52, v224\n"
"v_add_f32 v52, v52, v225\n"
"v_mfma_f32_16x16x32_bf16 a[68:71], a[228:231], v[16:19], a[68:71]\n"
"v_exp_f32 v229, v229\n"
"v_mfma_f32_16x16x32_bf16 a[72:75], a[232:235], v[16:19], a[72:75]\n"
"v_cvt_pk_bf16_f32 v20, v228, v229\n"
"v_add_f32 v52, v52, v226\n"
"v_mfma_f32_16x16x32_bf16 a[76:79], a[236:239], v[16:19], a[76:79]\n"
"v_exp_f32 v230, v230\n"
"v_mfma_f32_16x16x32_bf16 a[80:83], a[240:243], v[16:19], a[80:83]\n"
"s_waitcnt lgkmcnt(8)\n"
"buffer_load_dword v250, v252, s[72:75], s46 offen\n"
"v_mfma_f32_16x16x32_bf16 a[84:87], a[244:247], v[16:19], a[84:87]\n"
"v_exp_f32 v231, v231\n"
"v_mfma_f32_16x16x32_bf16 a[88:91], a[248:251], v[16:19], a[88:91]\n"
"v_cvt_pk_bf16_f32 v21, v230, v231\n"
"v_add_f32 v52, v52, v227\n"
"v_mfma_f32_16x16x32_bf16 a[92:95], a[252:255], v[16:19], a[92:95]\n"
"v_exp_f32 v232, v232\n"
"v_mfma_f32_16x16x32_bf16 v[0:3], a[128:131], v[64:67], v[24:27]\n"
"ds_read_b128 a[160:163], v241 offset:40960\n"
"v_add_u32 v252, 147456, v252\n"
"v_mfma_f32_16x16x32_bf16 v[0:3], a[132:135], v[68:71], v[0:3]\n"
"v_exp_f32 v233, v233\n"
"v_mfma_f32_16x16x32_bf16 v[0:3], a[136:139], v[72:75], v[0:3]\n"
"ds_read_b128 a[164:167], v241 offset:41216\n"
"v_cvt_pk_bf16_f32 v22, v232, v233\n"
"v_mfma_f32_16x16x32_bf16 v[0:3], a[140:143], v[76:79], v[0:3]\n"
"v_exp_f32 v234, v234\n"
"v_mfma_f32_16x16x32_bf16 v[4:7], a[144:147], v[64:67], v[24:27]\n"
"ds_read_b128 a[168:171], v241 offset:41472\n"
"v_mfma_f32_16x16x32_bf16 v[4:7], a[148:151], v[68:71], v[4:7]\n"
"v_exp_f32 v235, v235\n"
"v_mfma_f32_16x16x32_bf16 v[4:7], a[152:155], v[72:75], v[4:7]\n"
"ds_read_b128 a[172:175], v241 offset:41728\n"
"v_cvt_pk_bf16_f32 v23, v234, v235\n"
"v_mfma_f32_16x16x32_bf16 v[4:7], a[156:159], v[76:79], v[4:7]\n"
"s_add_u32 m0, s86, 81920\n"
"v_exp_f32 v0, v0\n"
"v_mfma_f32_16x16x32_bf16 a[96:99], a[224:227], v[20:23], a[96:99]\n"
"buffer_load_dwordx4 v246, s[12:15], s84 offen offset:0 lds\n"
"v_mfma_f32_16x16x32_bf16 a[100:103], a[228:231], v[20:23], a[100:103]\n"
"v_exp_f32 v1, v1\n"
"v_mfma_f32_16x16x32_bf16 a[104:107], a[232:235], v[20:23], a[104:107]\n"
"ds_read_b128 a[176:179], v241 offset:45056\n"
"v_cvt_pk_bf16_f32 v16, v0, v1\n"
"v_mfma_f32_16x16x32_bf16 a[108:111], a[236:239], v[20:23], a[108:111]\n"
"v_exp_f32 v2, v2\n"
"v_mfma_f32_16x16x32_bf16 a[112:115], a[240:243], v[20:23], a[112:115]\n"
"ds_read_b128 a[180:183], v241 offset:45312\n"
"v_add_f32 v44, v44, v0\n"
"v_mfma_f32_16x16x32_bf16 a[116:119], a[244:247], v[20:23], a[116:119]\n"
"v_exp_f32 v3, v3\n"
"v_mfma_f32_16x16x32_bf16 a[120:123], a[248:251], v[20:23], a[120:123]\n"
"ds_read_b128 a[184:187], v241 offset:45568\n"
"v_cvt_pk_bf16_f32 v17, v2, v3\n"
"v_mfma_f32_16x16x32_bf16 a[124:127], a[252:255], v[20:23], a[124:127]\n"
"v_exp_f32 v4, v4\n"
"v_mfma_f32_16x16x32_bf16 v[56:59], v[40:43], v[20:23], v[56:59]\n"
"ds_read_b128 a[188:191], v241 offset:45824\n"
"v_add_f32 v44, v44, v1\n"
"v_mfma_f32_16x16x32_bf16 v[8:11], a[128:131], v[80:83], v[28:31]\n"
"v_exp_f32 v5, v5\n"
"v_mfma_f32_16x16x32_bf16 v[8:11], a[132:135], v[84:87], v[8:11]\n"
"ds_read_b64_tr_b16 a[224:225], v244 offset:57344\n"
"v_cvt_pk_bf16_f32 v18, v4, v5\n"
"v_mfma_f32_16x16x32_bf16 v[8:11], a[136:139], v[88:91], v[8:11]\n"
"v_exp_f32 v6, v6\n"
"v_mfma_f32_16x16x32_bf16 v[8:11], a[140:143], v[92:95], v[8:11]\n"
"ds_read_b64_tr_b16 a[226:227], v244 offset:61440\n"
"v_add_f32 v44, v44, v2\n"
"v_mfma_f32_16x16x32_bf16 v[12:15], a[144:147], v[80:83], v[28:31]\n"
"v_exp_f32 v7, v7\n"
"v_mfma_f32_16x16x32_bf16 v[12:15], a[148:151], v[84:87], v[12:15]\n"
"ds_read_b64_tr_b16 a[228:229], v245 offset:57344\n"
"v_cvt_pk_bf16_f32 v19, v6, v7\n"
"v_mfma_f32_16x16x32_bf16 v[12:15], a[152:155], v[88:91], v[12:15]\n"
"buffer_load_dwordx4 v247, s[12:15], s84 offen offset:1024 lds\n"
"v_mfma_f32_16x16x32_bf16 v[12:15], a[156:159], v[92:95], v[12:15]\n"
"s_waitcnt lgkmcnt(11)\n"
"v_exp_f32 v8, v8\n"
"v_mfma_f32_16x16x32_bf16 a[0:3], a[192:195], v[16:19], a[0:3]\n"
"ds_read_b64_tr_b16 a[230:231], v245 offset:61440\n"
"v_add_f32 v44, v44, v3\n"
"v_mfma_f32_16x16x32_bf16 a[4:7], a[196:199], v[16:19], a[4:7]\n"
"v_exp_f32 v9, v9\n"
"v_mfma_f32_16x16x32_bf16 a[8:11], a[200:203], v[16:19], a[8:11]\n"
"ds_read_b64_tr_b16 a[232:233], v244 offset:57600\n"
"v_cvt_pk_bf16_f32 v20, v8, v9\n"
"v_mfma_f32_16x16x32_bf16 a[12:15], a[204:207], v[16:19], a[12:15]\n"
"v_exp_f32 v10, v10\n"
"v_mfma_f32_16x16x32_bf16 a[16:19], a[208:211], v[16:19], a[16:19]\n"
"ds_read_b64_tr_b16 a[234:235], v244 offset:61696\n"
"v_add_f32 v44, v44, v4\n"
"v_mfma_f32_16x16x32_bf16 a[20:23], a[212:215], v[16:19], a[20:23]\n"
"v_exp_f32 v11, v11\n"
"v_mfma_f32_16x16x32_bf16 a[24:27], a[216:219], v[16:19], a[24:27]\n"
"ds_read_b64_tr_b16 a[236:237], v245 offset:57600\n"
"v_cvt_pk_bf16_f32 v21, v10, v11\n"
"v_mfma_f32_16x16x32_bf16 a[28:31], a[220:223], v[16:19], a[28:31]\n"
"v_exp_f32 v12, v12\n"
"v_mfma_f32_16x16x32_bf16 v[220:223], a[128:131], v[96:99], v[32:35]\n"
"ds_read_b64_tr_b16 a[238:239], v245 offset:61696\n"
"v_add_f32 v44, v44, v5\n"
"v_mfma_f32_16x16x32_bf16 v[220:223], a[132:135], v[100:103], v[220:223]\n"
"v_exp_f32 v13, v13\n"
"v_mfma_f32_16x16x32_bf16 v[220:223], a[136:139], v[104:107], v[220:223]\n"
"ds_read_b64_tr_b16 a[240:241], v244 offset:57856\n"
"v_cvt_pk_bf16_f32 v22, v12, v13\n"
"v_mfma_f32_16x16x32_bf16 v[220:223], a[140:143], v[108:111], v[220:223]\n"
"v_exp_f32 v14, v14\n"
"v_mfma_f32_16x16x32_bf16 v[224:227], a[144:147], v[96:99], v[32:35]\n"
"ds_read_b64_tr_b16 a[242:243], v244 offset:61952\n"
"v_add_f32 v44, v44, v6\n"
"v_mfma_f32_16x16x32_bf16 v[224:227], a[148:151], v[100:103], v[224:227]\n"
"v_exp_f32 v15, v15\n"
"v_mfma_f32_16x16x32_bf16 v[224:227], a[152:155], v[104:107], v[224:227]\n"
"ds_read_b64_tr_b16 a[244:245], v245 offset:57856\n"
"v_cvt_pk_bf16_f32 v23, v14, v15\n"
"v_mfma_f32_16x16x32_bf16 v[224:227], a[156:159], v[108:111], v[224:227]\n"
"v_exp_f32 v220, v220\n"
"v_mfma_f32_16x16x32_bf16 a[32:35], a[192:195], v[20:23], a[32:35]\n"
"buffer_load_dwordx4 v246, s[12:15], s84 offen offset:2048 lds\n"
"v_mfma_f32_16x16x32_bf16 a[36:39], a[196:199], v[20:23], a[36:39]\n"
"v_exp_f32 v221, v221\n"
"v_mfma_f32_16x16x32_bf16 a[40:43], a[200:203], v[20:23], a[40:43]\n"
"ds_read_b64_tr_b16 a[246:247], v245 offset:61952\n"
"v_cvt_pk_bf16_f32 v16, v220, v221\n"
"v_mfma_f32_16x16x32_bf16 a[44:47], a[204:207], v[20:23], a[44:47]\n"
"v_exp_f32 v222, v222\n"
"v_mfma_f32_16x16x32_bf16 a[48:51], a[208:211], v[20:23], a[48:51]\n"
"ds_read_b64_tr_b16 a[248:249], v244 offset:58112\n"
"v_add_f32 v44, v44, v7\n"
"v_mfma_f32_16x16x32_bf16 a[52:55], a[212:215], v[20:23], a[52:55]\n"
"v_exp_f32 v223, v223\n"
"v_mfma_f32_16x16x32_bf16 a[56:59], a[216:219], v[20:23], a[56:59]\n"
"ds_read_b64_tr_b16 a[250:251], v244 offset:62208\n"
"v_cvt_pk_bf16_f32 v17, v222, v223\n"
"v_mfma_f32_16x16x32_bf16 a[60:63], a[220:223], v[20:23], a[60:63]\n"
"v_exp_f32 v224, v224\n"
"v_mfma_f32_16x16x32_bf16 v[48:51], v[40:43], v[20:23], v[48:51]\n"
"ds_read_b64_tr_b16 a[252:253], v245 offset:58112\n"
"v_add_f32 v52, v52, v220\n"
"v_mfma_f32_16x16x32_bf16 v[228:231], a[128:131], v[112:115], v[36:39]\n"
"v_exp_f32 v225, v225\n"
"v_mfma_f32_16x16x32_bf16 v[228:231], a[132:135], v[116:119], v[228:231]\n"
"ds_read_b64_tr_b16 a[254:255], v245 offset:62208\n"
"v_cvt_pk_bf16_f32 v18, v224, v225\n"
"v_mfma_f32_16x16x32_bf16 v[228:231], a[136:139], v[120:123], v[228:231]\n"
"v_exp_f32 v226, v226\n"
"v_mfma_f32_16x16x32_bf16 v[228:231], a[140:143], v[124:127], v[228:231]\n"
"v_add_f32 v52, v52, v221\n"
"v_add_f32 v52, v52, v222\n"
"v_mfma_f32_16x16x32_bf16 v[232:235], a[144:147], v[112:115], v[36:39]\n"
"v_exp_f32 v227, v227\n"
"v_mfma_f32_16x16x32_bf16 v[232:235], a[148:151], v[116:119], v[232:235]\n"
"v_cvt_pk_bf16_f32 v19, v226, v227\n"
"v_add_f32 v52, v52, v223\n"
"v_mfma_f32_16x16x32_bf16 v[232:235], a[152:155], v[120:123], v[232:235]\n"
"buffer_load_dwordx4 v247, s[12:15], s84 offen offset:3072 lds\n"
"v_mfma_f32_16x16x32_bf16 v[232:235], a[156:159], v[124:127], v[232:235]\n"
"s_add_u32 s84, s84, 16384\n"
"v_exp_f32 v228, v228\n"
"v_mfma_f32_16x16x32_bf16 a[64:67], a[192:195], v[16:19], a[64:67]\n"
"AT_pg8:\n"
"s_cbranch_execz AT_hr\n"
"v_add_f32 v52, v52, v224\n"
"v_add_f32 v52, v52, v225\n"
"v_mfma_f32_16x16x32_bf16 a[68:71], a[196:199], v[16:19], a[68:71]\n"
"v_exp_f32 v229, v229\n"
"v_mfma_f32_16x16x32_bf16 a[72:75], a[200:203], v[16:19], a[72:75]\n"
"v_cvt_pk_bf16_f32 v20, v228, v229\n"
"v_add_f32 v52, v52, v226\n"
"v_mfma_f32_16x16x32_bf16 a[76:79], a[204:207], v[16:19], a[76:79]\n"
"s_waitcnt vmcnt(20) lgkmcnt(0)\n"
"v_exp_f32 v230, v230\n"
"v_mfma_f32_16x16x32_bf16 a[80:83], a[208:211], v[16:19], a[80:83]\n"
"s_barrier\n"
"v_mfma_f32_16x16x32_bf16 a[84:87], a[212:215], v[16:19], a[84:87]\n"
"v_exp_f32 v231, v231\n"
"v_mfma_f32_16x16x32_bf16 a[88:91], a[216:219], v[16:19], a[88:91]\n"
"v_cvt_pk_bf16_f32 v21, v230, v231\n"
"v_add_f32 v52, v52, v227\n"
"v_mfma_f32_16x16x32_bf16 a[92:95], a[220:223], v[16:19], a[92:95]\n"
"v_exp_f32 v232, v232\n"
"v_mfma_f32_16x16x32_bf16 v[0:3], a[160:163], v[64:67], v[24:27]\n"
"ds_read_b128 a[128:131], v240 offset:0\n"
"buffer_store_dwordx4 v[216:219], v208, s[28:31], s54 offen sc1\n"
"v_mfma_f32_16x16x32_bf16 v[0:3], a[164:167], v[68:71], v[0:3]\n"
"s_add_u32 s54, s54, 1024\n"
"v_exp_f32 v233, v233\n"
"v_mfma_f32_16x16x32_bf16 v[0:3], a[168:171], v[72:75], v[0:3]\n"
"s_min_u32 s54, s54, s49\n"
"ds_read_b128 a[132:135], v240 offset:256\n"
"v_cvt_pk_bf16_f32 v22, v232, v233\n"
"v_mfma_f32_16x16x32_bf16 v[0:3], a[172:175], v[76:79], v[0:3]\n"
"v_exp_f32 v234, v234\n"
"v_mfma_f32_16x16x32_bf16 v[4:7], a[176:179], v[64:67], v[24:27]\n"
"ds_read_b128 a[136:139], v240 offset:512\n"
"v_mfma_f32_16x16x32_bf16 v[4:7], a[180:183], v[68:71], v[4:7]\n"
"v_exp_f32 v235, v235\n"
"v_mfma_f32_16x16x32_bf16 v[4:7], a[184:187], v[72:75], v[4:7]\n"
"ds_read_b128 a[140:143], v240 offset:768\n"
"v_cvt_pk_bf16_f32 v23, v234, v235\n"
"v_mfma_f32_16x16x32_bf16 v[4:7], a[188:191], v[76:79], v[4:7]\n"
"s_add_u32 m0, s86, 98304\n"
"v_exp_f32 v0, v0\n"
"v_mfma_f32_16x16x32_bf16 a[96:99], a[192:195], v[20:23], a[96:99]\n"
"buffer_load_dwordx4 v246, s[8:11], s84 offen offset:0 lds\n"
"v_mfma_f32_16x16x32_bf16 a[100:103], a[196:199], v[20:23], a[100:103]\n"
"v_exp_f32 v1, v1\n"
"v_mfma_f32_16x16x32_bf16 a[104:107], a[200:203], v[20:23], a[104:107]\n"
"ds_read_b128 a[144:147], v240 offset:4096\n"
"v_cvt_pk_bf16_f32 v16, v0, v1\n"
"v_mfma_f32_16x16x32_bf16 a[108:111], a[204:207], v[20:23], a[108:111]\n"
"v_exp_f32 v2, v2\n"
"v_mfma_f32_16x16x32_bf16 a[112:115], a[208:211], v[20:23], a[112:115]\n"
"ds_read_b128 a[148:151], v240 offset:4352\n"
"v_add_f32 v44, v44, v0\n"
"v_mfma_f32_16x16x32_bf16 a[116:119], a[212:215], v[20:23], a[116:119]\n"
"v_exp_f32 v3, v3\n"
"v_mfma_f32_16x16x32_bf16 a[120:123], a[216:219], v[20:23], a[120:123]\n"
"ds_read_b128 a[152:155], v240 offset:4608\n"
"v_cvt_pk_bf16_f32 v17, v2, v3\n"
"v_mfma_f32_16x16x32_bf16 a[124:127], a[220:223], v[20:23], a[124:127]\n"
"v_exp_f32 v4, v4\n"
"v_mfma_f32_16x16x32_bf16 v[56:59], v[40:43], v[20:23], v[56:59]\n"
"ds_read_b128 a[156:159], v240 offset:4864\n"
"v_add_f32 v44, v44, v1\n"
"v_mfma_f32_16x16x32_bf16 v[8:11], a[160:163], v[80:83], v[28:31]\n"
"v_exp_f32 v5, v5\n"
"v_mfma_f32_16x16x32_bf16 v[8:11], a[164:167], v[84:87], v[8:11]\n"
"ds_read_b64_tr_b16 a[192:193], v242 offset:16384\n"
"v_cvt_pk_bf16_f32 v18, v4, v5\n"
"v_mfma_f32_16x16x32_bf16 v[8:11], a[168:171], v[88:91], v[8:11]\n"
"v_exp_f32 v6, v6\n"
"v_mfma_f32_16x16x32_bf16 v[8:11], a[172:175], v[92:95], v[8:11]\n"
"ds_read_b64_tr_b16 a[194:195], v242 offset:20480\n"
"v_add_f32 v44, v44, v2\n"
"v_mfma_f32_16x16x32_bf16 v[12:15], a[176:179], v[80:83], v[28:31]\n"
"v_exp_f32 v7, v7\n"
"v_mfma_f32_16x16x32_bf16 v[12:15], a[180:183], v[84:87], v[12:15]\n"
"ds_read_b64_tr_b16 a[196:197], v243 offset:16384\n"
"v_cvt_pk_bf16_f32 v19, v6, v7\n"
"v_mfma_f32_16x16x32_bf16 v[12:15], a[184:187], v[88:91], v[12:15]\n"
"buffer_load_dwordx4 v246, s[8:11], s84 offen offset:1024 lds\n"
"v_mfma_f32_16x16x32_bf16 v[12:15], a[188:191], v[92:95], v[12:15]\n"
"s_waitcnt lgkmcnt(11)\n"
"v_exp_f32 v8, v8\n"
"v_mfma_f32_16x16x32_bf16 a[0:3], a[224:227], v[16:19], a[0:3]\n"
"ds_read_b64_tr_b16 a[198:199], v243 offset:20480\n"
"v_add_f32 v44, v44, v3\n"
"v_mfma_f32_16x16x32_bf16 a[4:7], a[228:231], v[16:19], a[4:7]\n"
"v_exp_f32 v9, v9\n"
"v_mfma_f32_16x16x32_bf16 a[8:11], a[232:235], v[16:19], a[8:11]\n"
"ds_read_b64_tr_b16 a[200:201], v242 offset:16640\n"
"v_cvt_pk_bf16_f32 v20, v8, v9\n"
"v_mfma_f32_16x16x32_bf16 a[12:15], a[236:239], v[16:19], a[12:15]\n"
"v_exp_f32 v10, v10\n"
"v_mfma_f32_16x16x32_bf16 a[16:19], a[240:243], v[16:19], a[16:19]\n"
"ds_read_b64_tr_b16 a[202:203], v242 offset:20736\n"
"v_add_f32 v44, v44, v4\n"
"v_mfma_f32_16x16x32_bf16 a[20:23], a[244:247], v[16:19], a[20:23]\n"
"v_exp_f32 v11, v11\n"
"v_mfma_f32_16x16x32_bf16 a[24:27], a[248:251], v[16:19], a[24:27]\n"
"ds_read_b64_tr_b16 a[204:205], v243 offset:16640\n"
"v_cvt_pk_bf16_f32 v21, v10, v11\n"
"v_mfma_f32_16x16x32_bf16 a[28:31], a[252:255], v[16:19], a[28:31]\n"
"v_exp_f32 v12, v12\n"
"v_mfma_f32_16x16x32_bf16 v[220:223], a[160:163], v[96:99], v[32:35]\n"
"ds_read_b64_tr_b16 a[206:207], v243 offset:20736\n"
"v_add_f32 v44, v44, v5\n"
"v_mfma_f32_16x16x32_bf16 v[220:223], a[164:167], v[100:103], v[220:223]\n"
"v_exp_f32 v13, v13\n"
"v_mfma_f32_16x16x32_bf16 v[220:223], a[168:171], v[104:107], v[220:223]\n"
"ds_read_b64_tr_b16 a[208:209], v242 offset:16896\n"
"v_cvt_pk_bf16_f32 v22, v12, v13\n"
"v_mfma_f32_16x16x32_bf16 v[220:223], a[172:175], v[108:111], v[220:223]\n"
"v_exp_f32 v14, v14\n"
"v_mfma_f32_16x16x32_bf16 v[224:227], a[176:179], v[96:99], v[32:35]\n"
"ds_read_b64_tr_b16 a[210:211], v242 offset:20992\n"
"v_add_f32 v44, v44, v6\n"
"v_mfma_f32_16x16x32_bf16 v[224:227], a[180:183], v[100:103], v[224:227]\n"
"v_exp_f32 v15, v15\n"
"v_mfma_f32_16x16x32_bf16 v[224:227], a[184:187], v[104:107], v[224:227]\n"
"ds_read_b64_tr_b16 a[212:213], v243 offset:16896\n"
"v_cvt_pk_bf16_f32 v23, v14, v15\n"
"v_mfma_f32_16x16x32_bf16 v[224:227], a[188:191], v[108:111], v[224:227]\n"
"v_exp_f32 v220, v220\n"
"v_mfma_f32_16x16x32_bf16 a[32:35], a[224:227], v[20:23], a[32:35]\n"
"buffer_load_dwordx4 v247, s[8:11], s84 offen offset:2048 lds\n"
"v_mfma_f32_16x16x32_bf16 a[36:39], a[228:231], v[20:23], a[36:39]\n"
"v_exp_f32 v221, v221\n"
"v_mfma_f32_16x16x32_bf16 a[40:43], a[232:235], v[20:23], a[40:43]\n"
"ds_read_b64_tr_b16 a[214:215], v243 offset:20992\n"
"v_cvt_pk_bf16_f32 v16, v220, v221\n"
"v_mfma_f32_16x16x32_bf16 a[44:47], a[236:239], v[20:23], a[44:47]\n"
"v_exp_f32 v222, v222\n"
"v_mfma_f32_16x16x32_bf16 a[48:51], a[240:243], v[20:23], a[48:51]\n"
"ds_read_b64_tr_b16 a[216:217], v242 offset:17152\n"
"v_add_f32 v44, v44, v7\n"
"v_mfma_f32_16x16x32_bf16 a[52:55], a[244:247], v[20:23], a[52:55]\n"
"v_exp_f32 v223, v223\n"
"v_mfma_f32_16x16x32_bf16 a[56:59], a[248:251], v[20:23], a[56:59]\n"
"ds_read_b64_tr_b16 a[218:219], v242 offset:21248\n"
"v_cvt_pk_bf16_f32 v17, v222, v223\n"
"v_mfma_f32_16x16x32_bf16 a[60:63], a[252:255], v[20:23], a[60:63]\n"
"v_exp_f32 v224, v224\n"
"v_mfma_f32_16x16x32_bf16 v[48:51], v[40:43], v[20:23], v[48:51]\n"
"ds_read_b64_tr_b16 a[220:221], v243 offset:17152\n"
"v_add_f32 v52, v52, v220\n"
"v_mfma_f32_16x16x32_bf16 v[228:231], a[160:163], v[112:115], v[36:39]\n"
"v_exp_f32 v225, v225\n"
"v_mfma_f32_16x16x32_bf16 v[228:231], a[164:167], v[116:119], v[228:231]\n"
"ds_read_b64_tr_b16 a[222:223], v243 offset:21248\n"
"v_cvt_pk_bf16_f32 v18, v224, v225\n"
"v_mfma_f32_16x16x32_bf16 v[228:231], a[168:171], v[120:123], v[228:231]\n"
"v_exp_f32 v226, v226\n"
"v_mfma_f32_16x16x32_bf16 v[228:231], a[172:175], v[124:127], v[228:231]\n"
"v_add_f32 v52, v52, v221\n"
"v_add_f32 v52, v52, v222\n"
"v_mfma_f32_16x16x32_bf16 v[232:235], a[176:179], v[112:115], v[36:39]\n"
"v_exp_f32 v227, v227\n"
"v_mfma_f32_16x16x32_bf16 v[232:235], a[180:183], v[116:119], v[232:235]\n"
"v_cvt_pk_bf16_f32 v19, v226, v227\n"
"v_add_f32 v52, v52, v223\n"
"v_mfma_f32_16x16x32_bf16 v[232:235], a[184:187], v[120:123], v[232:235]\n"
"buffer_load_dwordx4 v247, s[8:11], s84 offen offset:3072 lds\n"
"v_mfma_f32_16x16x32_bf16 v[232:235], a[188:191], v[124:127], v[232:235]\n"
"v_exp_f32 v228, v228\n"
"v_mfma_f32_16x16x32_bf16 a[64:67], a[224:227], v[16:19], a[64:67]\n"
"v_add_f32 v52, v52, v224\n"
"v_add_f32 v52, v52, v225\n"
"v_mfma_f32_16x16x32_bf16 a[68:71], a[228:231], v[16:19], a[68:71]\n"
"v_exp_f32 v229, v229\n"
"v_mfma_f32_16x16x32_bf16 a[72:75], a[232:235], v[16:19], a[72:75]\n"
"v_cvt_pk_bf16_f32 v20, v228, v229\n"
"v_add_f32 v52, v52, v226\n"
"v_mfma_f32_16x16x32_bf16 a[76:79], a[236:239], v[16:19], a[76:79]\n"
"v_exp_f32 v230, v230\n"
"v_mfma_f32_16x16x32_bf16 a[80:83], a[240:243], v[16:19], a[80:83]\n"
"s_waitcnt lgkmcnt(8)\n"
"buffer_load_dword v250, v253, s[24:27], s47 offen\n"
"v_mfma_f32_16x16x32_bf16 a[84:87], a[244:247], v[16:19], a[84:87]\n"
"v_exp_f32 v231, v231\n"
"v_mfma_f32_16x16x32_bf16 a[88:91], a[248:251], v[16:19], a[88:91]\n"
"v_cvt_pk_bf16_f32 v21, v230, v231\n"
"v_add_f32 v52, v52, v227\n"
"v_mfma_f32_16x16x32_bf16 a[92:95], a[252:255], v[16:19], a[92:95]\n"
"v_exp_f32 v232, v232\n"
"v_mfma_f32_16x16x32_bf16 v[0:3], a[128:131], v[64:67], v[24:27]\n"
"ds_read_b128 a[160:163], v240 offset:8192\n"
"v_add_u32 v253, 294912, v253\n"
"v_mfma_f32_16x16x32_bf16 v[0:3], a[132:135], v[68:71], v[0:3]\n"
"v_exp_f32 v233, v233\n"
"v_mfma_f32_16x16x32_bf16 v[0:3], a[136:139], v[72:75], v[0:3]\n"
"ds_read_b128 a[164:167], v240 offset:8448\n"
"v_cvt_pk_bf16_f32 v22, v232, v233\n"
"v_mfma_f32_16x16x32_bf16 v[0:3], a[140:143], v[76:79], v[0:3]\n"
"v_exp_f32 v234, v234\n"
"v_mfma_f32_16x16x32_bf16 v[4:7], a[144:147], v[64:67], v[24:27]\n"
"ds_read_b128 a[168:171], v240 offset:8704\n"
"v_mfma_f32_16x16x32_bf16 v[4:7], a[148:151], v[68:71], v[4:7]\n"
"v_exp_f32 v235, v235\n"
"v_mfma_f32_16x16x32_bf16 v[4:7], a[152:155], v[72:75], v[4:7]\n"
"ds_read_b128 a[172:175], v240 offset:8960\n"
"v_cvt_pk_bf16_f32 v23, v234, v235\n"
"v_mfma_f32_16x16x32_bf16 v[4:7], a[156:159], v[76:79], v[4:7]\n"
"s_add_u32 m0, s86, 114688\n"
"v_exp_f32 v0, v0\n"
"v_mfma_f32_16x16x32_bf16 a[96:99], a[224:227], v[20:23], a[96:99]\n"
"buffer_load_dwordx4 v246, s[12:15], s84 offen offset:0 lds\n"
"v_mfma_f32_16x16x32_bf16 a[100:103], a[228:231], v[20:23], a[100:103]\n"
"v_exp_f32 v1, v1\n"
"v_mfma_f32_16x16x32_bf16 a[104:107], a[232:235], v[20:23], a[104:107]\n"
"ds_read_b128 a[176:179], v240 offset:12288\n"
"v_cvt_pk_bf16_f32 v16, v0, v1\n"
"v_mfma_f32_16x16x32_bf16 a[108:111], a[236:239], v[20:23], a[108:111]\n"
"v_exp_f32 v2, v2\n"
"v_mfma_f32_16x16x32_bf16 a[112:115], a[240:243], v[20:23], a[112:115]\n"
"ds_read_b128 a[180:183], v240 offset:12544\n"
"v_add_f32 v44, v44, v0\n"
"v_mfma_f32_16x16x32_bf16 a[116:119], a[244:247], v[20:23], a[116:119]\n"
"v_exp_f32 v3, v3\n"
"v_mfma_f32_16x16x32_bf16 a[120:123], a[248:251], v[20:23], a[120:123]\n"
"ds_read_b128 a[184:187], v240 offset:12800\n"
"v_cvt_pk_bf16_f32 v17, v2, v3\n"
"v_mfma_f32_16x16x32_bf16 a[124:127], a[252:255], v[20:23], a[124:127]\n"
"v_exp_f32 v4, v4\n"
"v_mfma_f32_16x16x32_bf16 v[56:59], v[40:43], v[20:23], v[56:59]\n"
"ds_read_b128 a[188:191], v240 offset:13056\n"
"v_add_f32 v44, v44, v1\n"
"v_mfma_f32_16x16x32_bf16 v[8:11], a[128:131], v[80:83], v[28:31]\n"
"v_exp_f32 v5, v5\n"
"v_mfma_f32_16x16x32_bf16 v[8:11], a[132:135], v[84:87], v[8:11]\n"
"ds_read_b64_tr_b16 a[224:225], v242 offset:24576\n"
"v_cvt_pk_bf16_f32 v18, v4, v5\n"
"v_mfma_f32_16x16x32_bf16 v[8:11], a[136:139], v[88:91], v[8:11]\n"
"v_exp_f32 v6, v6\n"
"v_mfma_f32_16x16x32_bf16 v[8:11], a[140:143], v[92:95], v[8:11]\n"
"ds_read_b64_tr_b16 a[226:227], v242 offset:28672\n"
"v_add_f32 v44, v44, v2\n"
"v_mfma_f32_16x16x32_bf16 v[12:15], a[144:147], v[80:83], v[28:31]\n"
"v_exp_f32 v7, v7\n"
"v_mfma_f32_16x16x32_bf16 v[12:15], a[148:151], v[84:87], v[12:15]\n"
"ds_read_b64_tr_b16 a[228:229], v243 offset:24576\n"
"v_cvt_pk_bf16_f32 v19, v6, v7\n"
"v_mfma_f32_16x16x32_bf16 v[12:15], a[152:155], v[88:91], v[12:15]\n"
"buffer_load_dwordx4 v247, s[12:15], s84 offen offset:1024 lds\n"
"v_mfma_f32_16x16x32_bf16 v[12:15], a[156:159], v[92:95], v[12:15]\n"
"s_waitcnt lgkmcnt(11)\n"
"v_exp_f32 v8, v8\n"
"v_mfma_f32_16x16x32_bf16 a[0:3], a[192:195], v[16:19], a[0:3]\n"
"ds_read_b64_tr_b16 a[230:231], v243 offset:28672\n"
"v_add_f32 v44, v44, v3\n"
"v_mfma_f32_16x16x32_bf16 a[4:7], a[196:199], v[16:19], a[4:7]\n"
"v_exp_f32 v9, v9\n"
"v_mfma_f32_16x16x32_bf16 a[8:11], a[200:203], v[16:19], a[8:11]\n"
"ds_read_b64_tr_b16 a[232:233], v242 offset:24832\n"
"v_cvt_pk_bf16_f32 v20, v8, v9\n"
"v_mfma_f32_16x16x32_bf16 a[12:15], a[204:207], v[16:19], a[12:15]\n"
"v_exp_f32 v10, v10\n"
"v_mfma_f32_16x16x32_bf16 a[16:19], a[208:211], v[16:19], a[16:19]\n"
"ds_read_b64_tr_b16 a[234:235], v242 offset:28928\n"
"v_add_f32 v44, v44, v4\n"
"v_mfma_f32_16x16x32_bf16 a[20:23], a[212:215], v[16:19], a[20:23]\n"
"v_exp_f32 v11, v11\n"
"v_mfma_f32_16x16x32_bf16 a[24:27], a[216:219], v[16:19], a[24:27]\n"
"ds_read_b64_tr_b16 a[236:237], v243 offset:24832\n"
"v_cvt_pk_bf16_f32 v21, v10, v11\n"
"v_mfma_f32_16x16x32_bf16 a[28:31], a[220:223], v[16:19], a[28:31]\n"
"v_exp_f32 v12, v12\n"
"v_mfma_f32_16x16x32_bf16 v[220:223], a[128:131], v[96:99], v[32:35]\n"
"ds_read_b64_tr_b16 a[238:239], v243 offset:28928\n"
"v_add_f32 v44, v44, v5\n"
"v_mfma_f32_16x16x32_bf16 v[220:223], a[132:135], v[100:103], v[220:223]\n"
"v_exp_f32 v13, v13\n"
"v_mfma_f32_16x16x32_bf16 v[220:223], a[136:139], v[104:107], v[220:223]\n"
"ds_read_b64_tr_b16 a[240:241], v242 offset:25088\n"
"v_cvt_pk_bf16_f32 v22, v12, v13\n"
"v_mfma_f32_16x16x32_bf16 v[220:223], a[140:143], v[108:111], v[220:223]\n"
"v_exp_f32 v14, v14\n"
"v_mfma_f32_16x16x32_bf16 v[224:227], a[144:147], v[96:99], v[32:35]\n"
"ds_read_b64_tr_b16 a[242:243], v242 offset:29184\n"
"v_add_f32 v44, v44, v6\n"
"v_mfma_f32_16x16x32_bf16 v[224:227], a[148:151], v[100:103], v[224:227]\n"
"v_exp_f32 v15, v15\n"
"v_mfma_f32_16x16x32_bf16 v[224:227], a[152:155], v[104:107], v[224:227]\n"
"ds_read_b64_tr_b16 a[244:245], v243 offset:25088\n"
"v_cvt_pk_bf16_f32 v23, v14, v15\n"
"v_mfma_f32_16x16x32_bf16 v[224:227], a[156:159], v[108:111], v[224:227]\n"
"v_exp_f32 v220, v220\n"
"v_mfma_f32_16x16x32_bf16 a[32:35], a[192:195], v[20:23], a[32:35]\n"
"buffer_load_dwordx4 v246, s[12:15], s84 offen offset:2048 lds\n"
"v_mfma_f32_16x16x32_bf16 a[36:39], a[196:199], v[20:23], a[36:39]\n"
"v_exp_f32 v221, v221\n"
"v_mfma_f32_16x16x32_bf16 a[40:43], a[200:203], v[20:23], a[40:43]\n"
"ds_read_b64_tr_b16 a[246:247], v243 offset:29184\n"
"v_cvt_pk_bf16_f32 v16, v220, v221\n"
"v_mfma_f32_16x16x32_bf16 a[44:47], a[204:207], v[20:23], a[44:47]\n"
"v_exp_f32 v222, v222\n"
"v_mfma_f32_16x16x32_bf16 a[48:51], a[208:211], v[20:23], a[48:51]\n"
"ds_read_b64_tr_b16 a[248:249], v242 offset:25344\n"
"v_add_f32 v44, v44, v7\n"
"v_mfma_f32_16x16x32_bf16 a[52:55], a[212:215], v[20:23], a[52:55]\n"
"v_exp_f32 v223, v223\n"
"v_mfma_f32_16x16x32_bf16 a[56:59], a[216:219], v[20:23], a[56:59]\n"
"ds_read_b64_tr_b16 a[250:251], v242 offset:29440\n"
"v_cvt_pk_bf16_f32 v17, v222, v223\n"
"v_mfma_f32_16x16x32_bf16 a[60:63], a[220:223], v[20:23], a[60:63]\n"
"v_exp_f32 v224, v224\n"
"v_mfma_f32_16x16x32_bf16 v[48:51], v[40:43], v[20:23], v[48:51]\n"
"ds_read_b64_tr_b16 a[252:253], v243 offset:25344\n"
"v_add_f32 v52, v52, v220\n"
"v_mfma_f32_16x16x32_bf16 v[228:231], a[128:131], v[112:115], v[36:39]\n"
"v_exp_f32 v225, v225\n"
"v_mfma_f32_16x16x32_bf16 v[228:231], a[132:135], v[116:119], v[228:231]\n"
"ds_read_b64_tr_b16 a[254:255], v243 offset:29440\n"
"v_cvt_pk_bf16_f32 v18, v224, v225\n"
"v_mfma_f32_16x16x32_bf16 v[228:231], a[136:139], v[120:123], v[228:231]\n"
"v_exp_f32 v226, v226\n"
"v_mfma_f32_16x16x32_bf16 v[228:231], a[140:143], v[124:127], v[228:231]\n"
"v_add_f32 v52, v52, v221\n"
"v_add_f32 v52, v52, v222\n"
"v_mfma_f32_16x16x32_bf16 v[232:235], a[144:147], v[112:115], v[36:39]\n"
"v_exp_f32 v227, v227\n"
"v_mfma_f32_16x16x32_bf16 v[232:235], a[148:151], v[116:119], v[232:235]\n"
"v_cvt_pk_bf16_f32 v19, v226, v227\n"
"v_add_f32 v52, v52, v223\n"
"v_mfma_f32_16x16x32_bf16 v[232:235], a[152:155], v[120:123], v[232:235]\n"
"buffer_load_dwordx4 v247, s[12:15], s84 offen offset:3072 lds\n"
"v_mfma_f32_16x16x32_bf16 v[232:235], a[156:159], v[124:127], v[232:235]\n"
"s_add_u32 s84, s84, 16384\n"
"v_exp_f32 v228, v228\n"
"v_mfma_f32_16x16x32_bf16 a[64:67], a[192:195], v[16:19], a[64:67]\n"
"s_sub_u32 s87, s87, 1\n"
"v_add_f32 v52, v52, v224\n"
"v_add_f32 v52, v52, v225\n"
"v_mfma_f32_16x16x32_bf16 a[68:71], a[196:199], v[16:19], a[68:71]\n"
"s_cmp_lg_u32 s87, 0\n"
"v_exp_f32 v229, v229\n"
"v_mfma_f32_16x16x32_bf16 a[72:75], a[200:203], v[16:19], a[72:75]\n"
"v_cvt_pk_bf16_f32 v20, v228, v229\n"
"v_add_f32 v52, v52, v226\n"
"v_mfma_f32_16x16x32_bf16 a[76:79], a[204:207], v[16:19], a[76:79]\n"
"s_waitcnt vmcnt(20) lgkmcnt(0)\n"
"v_exp_f32 v230, v230\n"
"v_mfma_f32_16x16x32_bf16 a[80:83], a[208:211], v[16:19], a[80:83]\n"
"s_barrier\n"
"v_mfma_f32_16x16x32_bf16 a[84:87], a[212:215], v[16:19], a[84:87]\n"
"v_exp_f32 v231, v231\n"
"v_mfma_f32_16x16x32_bf16 a[88:91], a[216:219], v[16:19], a[88:91]\n"
"v_cvt_pk_bf16_f32 v21, v230, v231\n"
"v_add_f32 v52, v52, v227\n"
"v_mfma_f32_16x16x32_bf16 a[92:95], a[220:223], v[16:19], a[92:95]\n"
"v_exp_f32 v232, v232\n"
"s_cbranch_scc1 AT_loop\n"
"s_cmp_lg_u32 s56, 0\n"
"s_cbranch_scc1 AT_h3\n"
"s_mov_b32 s61, 0x80000\n"
"AT_h2:\n"
"v_xor_b32 v210, s85, v210\n"
"v_xor_b32 v211, s85, v211\n"
"v_xor_b32 v212, s85, v212\n"
"v_xor_b32 v213, s85, v213\n"
"v_or3_b32 v210, v210, v211, v212\n"
"v_or_b32 v210, v210, v213\n"
"v_cmp_ne_u32 vcc, 0, v210\n"
"s_sub_u32 s61, s61, 1\n"
"s_cbranch_vccz AT_h4\n"
"s_cmp_eq_u32 s61, 0\n"
"s_cbranch_scc1 AT_h4\n"
"s_sleep 2\n"
"buffer_load_dword v210, v214, s[28:31], s57 offen offset:4 sc1\n"
"buffer_load_dword v211, v214, s[28:31], s58 offen offset:4 sc1\n"
"buffer_load_dword v212, v214, s[28:31], s59 offen offset:4 sc1\n"
"buffer_load_dword v213, v214, s[28:31], s60 offen offset:4 sc1\n"
"s_waitcnt vmcnt(0)\n"
"s_branch AT_h2\n"
"AT_h4:\n"
"v_mov_b32 v210, s85\n"
"v_mov_b32 v211, 65536\n"
"s_mov_b64 exec, 1\n"
"buffer_store_dword v210, v211, s[28:31], 0 offen sc1\n"
"s_mov_b64 exec, -1\n"
"AT_h3:\n"
"v_mfma_f32_16x16x32_bf16 v[0:3], a[160:163], v[64:67], v[24:27]\n"
"ds_read_b128 a[128:131], v240 offset:32768\n"
"buffer_load_dword v250, v255, s[32:35], 0 offen\n"
"v_mfma_f32_16x16x32_bf16 v[0:3], a[164:167], v[68:71], v[0:3]\n"
"v_exp_f32 v233, v233\n"
"v_mfma_f32_16x16x32_bf16 v[0:3], a[168:171], v[72:75], v[0:3]\n"
"ds_read_b128 a[132:135], v240 offset:33024\n"
"v_cvt_pk_bf16_f32 v22, v232, v233\n"
"v_mfma_f32_16x16x32_bf16 v[0:3], a[172:175], v[76:79], v[0:3]\n"
"v_exp_f32 v234, v234\n"
"v_mfma_f32_16x16x32_bf16 v[4:7], a[176:179], v[64:67], v[24:27]\n"
"ds_read_b128 a[136:139], v240 offset:33280\n"
"v_lshlrev_b32 v216, 6, v248\n"
"v_mfma_f32_16x16x32_bf16 v[4:7], a[180:183], v[68:71], v[4:7]\n"
"s_and_b32 s48, s42, 7\n"
"v_exp_f32 v235, v235\n"
"v_mfma_f32_16x16x32_bf16 v[4:7], a[184:187], v[72:75], v[4:7]\n"
"s_mul_i32 s48, s48, 576\n"
"ds_read_b128 a[140:143], v240 offset:33536\n"
"v_cvt_pk_bf16_f32 v23, v234, v235\n"
"v_mfma_f32_16x16x32_bf16 v[4:7], a[188:191], v[76:79], v[4:7]\n"
"s_mov_b32 m0, s86\n"
"v_exp_f32 v0, v0\n"
"v_mfma_f32_16x16x32_bf16 a[96:99], a[192:195], v[20:23], a[96:99]\n"
"s_lshr_b32 s62, s42, 3\n"
"buffer_load_dwordx4 v246, s[8:11], s84 offen offset:0 lds\n"
"v_mfma_f32_16x16x32_bf16 a[100:103], a[196:199], v[20:23], a[100:103]\n"
"s_mul_i32 s62, s62, 24\n"
"v_exp_f32 v1, v1\n"
"v_mfma_f32_16x16x32_bf16 a[104:107], a[200:203], v[20:23], a[104:107]\n"
"s_add_u32 s48, s48, s62\n"
"ds_read_b128 a[144:147], v240 offset:36864\n"
"v_cvt_pk_bf16_f32 v16, v0, v1\n"
"v_mfma_f32_16x16x32_bf16 a[108:111], a[204:207], v[20:23], a[108:111]\n"
"s_cmp_lt_u32 s42, 192\n"
"s_cselect_b32 s74, s74, 0\n"
"s_cselect_b32 s66, s66, 0\n"
"s_cselect_b32 s70, s70, 0\n"
"v_exp_f32 v2, v2\n"
"v_mfma_f32_16x16x32_bf16 a[112:115], a[208:211], v[20:23], a[112:115]\n"
"s_mul_i32 s50, s48, 1152\n"
"ds_read_b128 a[148:151], v240 offset:37120\n"
"v_add_f32 v44, v44, v0\n"
"v_mfma_f32_16x16x32_bf16 a[116:119], a[212:215], v[20:23], a[116:119]\n"
"s_lshl_b32 s62, s41, 13\n"
"v_exp_f32 v3, v3\n"
"v_mfma_f32_16x16x32_bf16 a[120:123], a[216:219], v[20:23], a[120:123]\n"
"s_add_u32 s46, s50, s62\n"
"ds_read_b128 a[152:155], v240 offset:37376\n"
"v_cvt_pk_bf16_f32 v17, v2, v3\n"
"v_mfma_f32_16x16x32_bf16 a[124:127], a[220:223], v[20:23], a[124:127]\n"
"s_add_u32 s47, s46, 4096\n"
"v_exp_f32 v4, v4\n"
"v_mfma_f32_16x16x32_bf16 v[56:59], v[40:43], v[20:23], v[56:59]\n"
"ds_read_b128 a[156:159], v240 offset:37632\n"
"v_add_f32 v44, v44, v1\n"
"v_mfma_f32_16x16x32_bf16 v[8:11], a[160:163], v[80:83], v[28:31]\n"
"v_exp_f32 v5, v5\n"
"v_mfma_f32_16x16x32_bf16 v[8:11], a[164:167], v[84:87], v[8:11]\n"
"ds_read_b64_tr_b16 a[192:193], v242 offset:49152\n"
"v_cvt_pk_bf16_f32 v18, v4, v5\n"
"v_mfma_f32_16x16x32_bf16 v[8:11], a[168:171], v[88:91], v[8:11]\n"
"v_exp_f32 v6, v6\n"
"v_mfma_f32_16x16x32_bf16 v[8:11], a[172:175], v[92:95], v[8:11]\n"
"ds_read_b64_tr_b16 a[194:195], v242 offset:53248\n"
"v_add_f32 v44, v44, v2\n"
"v_mfma_f32_16x16x32_bf16 v[12:15], a[176:179], v[80:83], v[28:31]\n"
"v_exp_f32 v7, v7\n"
"v_mfma_f32_16x16x32_bf16 v[12:15], a[180:183], v[84:87], v[12:15]\n"
"ds_read_b64_tr_b16 a[196:197], v243 offset:49152\n"
"v_cvt_pk_bf16_f32 v19, v6, v7\n"
"v_mfma_f32_16x16x32_bf16 v[12:15], a[184:187], v[88:91], v[12:15]\n"
"buffer_load_dwordx4 v246, s[8:11], s84 offen offset:1024 lds\n"
"v_mfma_f32_16x16x32_bf16 v[12:15], a[188:191], v[92:95], v[12:15]\n"
"s_waitcnt lgkmcnt(11)\n"
"v_exp_f32 v8, v8\n"
"v_mfma_f32_16x16x32_bf16 a[0:3], a[224:227], v[16:19], a[0:3]\n"
"ds_read_b64_tr_b16 a[198:199], v243 offset:53248\n"
"v_add_f32 v44, v44, v3\n"
"v_mfma_f32_16x16x32_bf16 a[4:7], a[228:231], v[16:19], a[4:7]\n"
"v_exp_f32 v9, v9\n"
"v_mfma_f32_16x16x32_bf16 a[8:11], a[232:235], v[16:19], a[8:11]\n"
"ds_read_b64_tr_b16 a[200:201], v242 offset:49408\n"
"v_cvt_pk_bf16_f32 v20, v8, v9\n"
"v_mfma_f32_16x16x32_bf16 a[12:15], a[236:239], v[16:19], a[12:15]\n"
"v_exp_f32 v10, v10\n"
"v_mfma_f32_16x16x32_bf16 a[16:19], a[240:243], v[16:19], a[16:19]\n"
"ds_read_b64_tr_b16 a[202:203], v242 offset:53504\n"
"buffer_load_dword v250, v216, s[72:75], s46 offen\n"
"v_add_f32 v44, v44, v4\n"
"v_mfma_f32_16x16x32_bf16 a[20:23], a[244:247], v[16:19], a[20:23]\n"
"v_exp_f32 v11, v11\n"
"v_mfma_f32_16x16x32_bf16 a[24:27], a[248:251], v[16:19], a[24:27]\n"
"ds_read_b64_tr_b16 a[204:205], v243 offset:49408\n"
"v_cvt_pk_bf16_f32 v21, v10, v11\n"
"v_mfma_f32_16x16x32_bf16 a[28:31], a[252:255], v[16:19], a[28:31]\n"
"v_exp_f32 v12, v12\n"
"v_mfma_f32_16x16x32_bf16 v[220:223], a[160:163], v[96:99], v[32:35]\n"
"ds_read_b64_tr_b16 a[206:207], v243 offset:53504\n"
"v_add_f32 v44, v44, v5\n"
"v_mfma_f32_16x16x32_bf16 v[220:223], a[164:167], v[100:103], v[220:223]\n"
"v_exp_f32 v13, v13\n"
"v_mfma_f32_16x16x32_bf16 v[220:223], a[168:171], v[104:107], v[220:223]\n"
"ds_read_b64_tr_b16 a[208:209], v242 offset:49664\n"
"v_cvt_pk_bf16_f32 v22, v12, v13\n"
"v_mfma_f32_16x16x32_bf16 v[220:223], a[172:175], v[108:111], v[220:223]\n"
"v_exp_f32 v14, v14\n"
"v_mfma_f32_16x16x32_bf16 v[224:227], a[176:179], v[96:99], v[32:35]\n"
"ds_read_b64_tr_b16 a[210:211], v242 offset:53760\n"
"v_add_f32 v44, v44, v6\n"
"v_mfma_f32_16x16x32_bf16 v[224:227], a[180:183], v[100:103], v[224:227]\n"
"v_exp_f32 v15, v15\n"
"v_mfma_f32_16x16x32_bf16 v[224:227], a[184:187], v[104:107], v[224:227]\n"
"ds_read_b64_tr_b16 a[212:213], v243 offset:49664\n"
"v_cvt_pk_bf16_f32 v23, v14, v15\n"
"v_mfma_f32_16x16x32_bf16 v[224:227], a[188:191], v[108:111], v[224:227]\n"
"v_exp_f32 v220, v220\n"
"v_mfma_f32_16x16x32_bf16 a[32:35], a[224:227], v[20:23], a[32:35]\n"
"buffer_load_dwordx4 v247, s[8:11], s84 offen offset:2048 lds\n"
"v_mfma_f32_16x16x32_bf16 a[36:39], a[228:231], v[20:23], a[36:39]\n"
"v_exp_f32 v221, v221\n"
"v_mfma_f32_16x16x32_bf16 a[40:43], a[232:235], v[20:23], a[40:43]\n"
"ds_read_b64_tr_b16 a[214:215], v243 offset:53760\n"
"v_cvt_pk_bf16_f32 v16, v220, v221\n"
"v_mfma_f32_16x16x32_bf16 a[44:47], a[236:239], v[20:23], a[44:47]\n"
"v_exp_f32 v222, v222\n"
"v_mfma_f32_16x16x32_bf16 a[48:51], a[240:243], v[20:23], a[48:51]\n"
"ds_read_b64_tr_b16 a[216:217], v242 offset:49920\n"
"v_add_f32 v44, v44, v7\n"
"v_mfma_f32_16x16x32_bf16 a[52:55], a[244:247], v[20:23], a[52:55]\n"
"v_exp_f32 v223, v223\n"
"v_mfma_f32_16x16x32_bf16 a[56:59], a[248:251], v[20:23], a[56:59]\n"
"ds_read_b64_tr_b16 a[218:219], v242 offset:54016\n"
"v_cvt_pk_bf16_f32 v17, v222, v223\n"
"v_mfma_f32_16x16x32_bf16 a[60:63], a[252:255], v[20:23], a[60:63]\n"
"v_exp_f32 v224, v224\n"
"v_mfma_f32_16x16x32_bf16 v[48:51], v[40:43], v[20:23], v[48:51]\n"
"ds_read_b64_tr_b16 a[220:221], v243 offset:49920\n"
"v_add_f32 v52, v52, v220\n"
"v_mfma_f32_16x16x32_bf16 v[228:231], a[160:163], v[112:115], v[36:39]\n"
"v_exp_f32 v225, v225\n"
"v_mfma_f32_16x16x32_bf16 v[228:231], a[164:167], v[116:119], v[228:231]\n"
"ds_read_b64_tr_b16 a[222:223], v243 offset:54016\n"
"v_cvt_pk_bf16_f32 v18, v224, v225\n"
"v_mfma_f32_16x16x32_bf16 v[228:231], a[168:171], v[120:123], v[228:231]\n"
"v_exp_f32 v226, v226\n"
"v_mfma_f32_16x16x32_bf16 v[228:231], a[172:175], v[124:127], v[228:231]\n"
"v_add_f32 v52, v52, v221\n"
"v_add_f32 v52, v52, v222\n"
"v_mfma_f32_16x16x32_bf16 v[232:235], a[176:179], v[112:115], v[36:39]\n"
"v_exp_f32 v227, v227\n"
"v_mfma_f32_16x16x32_bf16 v[232:235], a[180:183], v[116:119], v[232:235]\n"
"v_cvt_pk_bf16_f32 v19, v226, v227\n"
"v_add_f32 v52, v52, v223\n"
"v_mfma_f32_16x16x32_bf16 v[232:235], a[184:187], v[120:123], v[232:235]\n"
"buffer_load_dwordx4 v247, s[8:11], s84 offen offset:3072 lds\n"
"v_mfma_f32_16x16x32_bf16 v[232:235], a[188:191], v[124:127], v[232:235]\n"
"v_exp_f32 v228, v228\n"
"v_mfma_f32_16x16x32_bf16 a[64:67], a[224:227], v[16:19], a[64:67]\n"
"buffer_load_dword v250, v216, s[72:75], s47 offen\n"
"v_add_f32 v52, v52, v224\n"
"v_add_f32 v52, v52, v225\n"
"v_mfma_f32_16x16x32_bf16 a[68:71], a[228:231], v[16:19], a[68:71]\n"
"v_exp_f32 v229, v229\n"
"v_mfma_f32_16x16x32_bf16 a[72:75], a[232:235], v[16:19], a[72:75]\n"
"v_cvt_pk_bf16_f32 v20, v228, v229\n"
"v_add_f32 v52, v52, v226\n"
"v_mfma_f32_16x16x32_bf16 a[76:79], a[236:239], v[16:19], a[76:79]\n"
"v_exp_f32 v230, v230\n"
"v_mfma_f32_16x16x32_bf16 a[80:83], a[240:243], v[16:19], a[80:83]\n"
"s_waitcnt lgkmcnt(8)\n"
"v_lshrrev_b32 v251, 2, v248\n"
"v_mfma_f32_16x16x32_bf16 a[84:87], a[244:247], v[16:19], a[84:87]\n"
"v_exp_f32 v231, v231\n"
"v_mfma_f32_16x16x32_bf16 a[88:91], a[248:251], v[16:19], a[88:91]\n"
"v_cvt_pk_bf16_f32 v21, v230, v231\n"
"v_add_f32 v52, v52, v227\n"
"v_mfma_f32_16x16x32_bf16 a[92:95], a[252:255], v[16:19], a[92:95]\n"
"v_exp_f32 v232, v232\n"
"v_mfma_f32_16x16x32_bf16 v[0:3], a[128:131], v[64:67], v[24:27]\n"
"ds_read_b128 a[160:163], v240 offset:40960\n"
"v_mul_u32_u24 v251, 9216, v251\n"
"v_mfma_f32_16x16x32_bf16 v[0:3], a[132:135], v[68:71], v[0:3]\n"
"v_exp_f32 v233, v233\n"
"v_mfma_f32_16x16x32_bf16 v[0:3], a[136:139], v[72:75], v[0:3]\n"
"ds_read_b128 a[164:167], v240 offset:41216\n"
"v_cvt_pk_bf16_f32 v22, v232, v233\n"
"v_mfma_f32_16x16x32_bf16 v[0:3], a[140:143], v[76:79], v[0:3]\n"
"v_exp_f32 v234, v234\n"
"v_mfma_f32_16x16x32_bf16 v[4:7], a[144:147], v[64:67], v[24:27]\n"
"ds_read_b128 a[168:171], v240 offset:41472\n"
"v_and_b32 v217, 3, v248\n"
"v_mfma_f32_16x16x32_bf16 v[4:7], a[148:151], v[68:71], v[4:7]\n"
"v_exp_f32 v235, v235\n"
"v_mfma_f32_16x16x32_bf16 v[4:7], a[152:155], v[72:75], v[4:7]\n"
"ds_read_b128 a[172:175], v240 offset:41728\n"
"v_cvt_pk_bf16_f32 v23, v234, v235\n"
"v_mfma_f32_16x16x32_bf16 v[4:7], a[156:159], v[76:79], v[4:7]\n"
"s_add_u32 m0, s86, 16384\n"
"v_exp_f32 v0, v0\n"
"v_mfma_f32_16x16x32_bf16 a[96:99], a[224:227], v[20:23], a[96:99]\n"
"buffer_load_dwordx4 v246, s[12:15], s84 offen offset:0 lds\n"
"v_add_u32 v255, 1, v217\n"
"v_mfma_f32_16x16x32_bf16 a[100:103], a[228:231], v[20:23], a[100:103]\n"
"v_exp_f32 v1, v1\n"
"v_mfma_f32_16x16x32_bf16 a[104:107], a[232:235], v[20:23], a[104:107]\n"
"ds_read_b128 a[176:179], v240 offset:45056\n"
"v_cvt_pk_bf16_f32 v16, v0, v1\n"
"v_mfma_f32_16x16x32_bf16 a[108:111], a[236:239], v[20:23], a[108:111]\n"
"v_exp_f32 v2, v2\n"
"v_mfma_f32_16x16x32_bf16 a[112:115], a[240:243], v[20:23], a[112:115]\n"
"ds_read_b128 a[180:183], v240 offset:45312\n"
"v_add_f32 v44, v44, v0\n"
"v_mfma_f32_16x16x32_bf16 a[116:119], a[244:247], v[20:23], a[116:119]\n"
"v_exp_f32 v3, v3\n"
"v_mfma_f32_16x16x32_bf16 a[120:123], a[248:251], v[20:23], a[120:123]\n"
"ds_read_b128 a[184:187], v240 offset:45568\n"
"v_cvt_pk_bf16_f32 v17, v2, v3\n"
"v_mfma_f32_16x16x32_bf16 a[124:127], a[252:255], v[20:23], a[124:127]\n"
"v_exp_f32 v4, v4\n"
"v_and_b32 v255, 4, v255\n"
"v_mfma_f32_16x16x32_bf16 v[56:59], v[40:43], v[20:23], v[56:59]\n"
"ds_read_b128 a[188:191], v240 offset:45824\n"
"v_add_f32 v44, v44, v1\n"
"v_mfma_f32_16x16x32_bf16 v[8:11], a[128:131], v[80:83], v[28:31]\n"
"v_exp_f32 v5, v5\n"
"v_mfma_f32_16x16x32_bf16 v[8:11], a[132:135], v[84:87], v[8:11]\n"
"ds_read_b64_tr_b16 a[224:225], v242 offset:57344\n"
"v_cvt_pk_bf16_f32 v18, v4, v5\n"
"v_mfma_f32_16x16x32_bf16 v[8:11], a[136:139], v[88:91], v[8:11]\n"
"v_exp_f32 v6, v6\n"
"v_mfma_f32_16x16x32_bf16 v[8:11], a[140:143], v[92:95], v[8:11]\n"
"ds_read_b64_tr_b16 a[226:227], v242 offset:61440\n"
"v_add_f32 v44, v44, v2\n"
"v_mfma_f32_16x16x32_bf16 v[12:15], a[144:147], v[80:83], v[28:31]\n"
"v_exp_f32 v7, v7\n"
"v_mfma_f32_16x16x32_bf16 v[12:15], a[148:151], v[84:87], v[12:15]\n"
"ds_read_b64_tr_b16 a[228:229], v243 offset:57344\n"
"v_cvt_pk_bf16_f32 v19, v6, v7\n"
"v_lshlrev_b32 v255, 26, v255\n"
"v_mfma_f32_16x16x32_bf16 v[12:15], a[152:155], v[88:91], v[12:15]\n"
"AT_pg9:\n"
"s_cbranch_execz AT_hr\n"
"buffer_load_dwordx4 v247, s[12:15], s84 offen offset:1024 lds\n"
"v_lshl_add_u32 v217, v217, 4, v255\n"
"v_mfma_f32_16x16x32_bf16 v[12:15], a[156:159], v[92:95], v[12:15]\n"
"s_waitcnt lgkmcnt(11)\n"
"v_exp_f32 v8, v8\n"
"v_mfma_f32_16x16x32_bf16 a[0:3], a[192:195], v[16:19], a[0:3]\n"
"ds_read_b64_tr_b16 a[230:231], v243 offset:61440\n"
"v_add_f32 v44, v44, v3\n"
"v_mfma_f32_16x16x32_bf16 a[4:7], a[196:199], v[16:19], a[4:7]\n"
"v_exp_f32 v9, v9\n"
"v_mfma_f32_16x16x32_bf16 a[8:11], a[200:203], v[16:19], a[8:11]\n"
"ds_read_b64_tr_b16 a[232:233], v242 offset:57600\n"
"v_cvt_pk_bf16_f32 v20, v8, v9\n"
"v_mfma_f32_16x16x32_bf16 a[12:15], a[204:207], v[16:19], a[12:15]\n"
"v_exp_f32 v10, v10\n"
"v_mfma_f32_16x16x32_bf16 a[16:19], a[208:211], v[16:19], a[16:19]\n"
"ds_read_b64_tr_b16 a[234:235], v242 offset:61696\n"
"v_add_f32 v44, v44, v4\n"
"v_mfma_f32_16x16x32_bf16 a[20:23], a[212:215], v[16:19], a[20:23]\n"
"v_exp_f32 v11, v11\n"
"v_lshl_add_u32 v252, v251, 1, v217\n"
"v_mfma_f32_16x16x32_bf16 a[24:27], a[216:219], v[16:19], a[24:27]\n"
"ds_read_b64_tr_b16 a[236:237], v243 offset:57600\n"
"v_cvt_pk_bf16_f32 v21, v10, v11\n"
"v_mfma_f32_16x16x32_bf16 a[28:31], a[220:223], v[16:19], a[28:31]\n"
"v_exp_f32 v12, v12\n"
"v_mfma_f32_16x16x32_bf16 v[220:223], a[128:131], v[96:99], v[32:35]\n"
"ds_read_b64_tr_b16 a[238:239], v243 offset:61696\n"
"v_add_f32 v44, v44, v5\n"
"v_mfma_f32_16x16x32_bf16 v[220:223], a[132:135], v[100:103], v[220:223]\n"
"v_exp_f32 v13, v13\n"
"v_mfma_f32_16x16x32_bf16 v[220:223], a[136:139], v[104:107], v[220:223]\n"
"ds_read_b64_tr_b16 a[240:241], v242 offset:57856\n"
"v_cvt_pk_bf16_f32 v22, v12, v13\n"
"v_mfma_f32_16x16x32_bf16 v[220:223], a[140:143], v[108:111], v[220:223]\n"
"v_exp_f32 v14, v14\n"
"v_mfma_f32_16x16x32_bf16 v[224:227], a[144:147], v[96:99], v[32:35]\n"
"ds_read_b64_tr_b16 a[242:243], v242 offset:61952\n"
"v_add_f32 v44, v44, v6\n"
"v_add_u32 v251, v251, v217\n"
"v_mfma_f32_16x16x32_bf16 v[224:227], a[148:151], v[100:103], v[224:227]\n"
"s_mul_i32 s62, s41, 1327104\n"
"v_exp_f32 v15, v15\n"
"v_mfma_f32_16x16x32_bf16 v[224:227], a[152:155], v[104:107], v[224:227]\n"
"s_lshl_b32 s49, s48, 1\n"
"ds_read_b64_tr_b16 a[244:245], v243 offset:57856\n"
"v_cvt_pk_bf16_f32 v23, v14, v15\n"
"v_mfma_f32_16x16x32_bf16 v[224:227], a[156:159], v[108:111], v[224:227]\n"
"s_add_u32 s49, s49, s62\n"
"v_exp_f32 v220, v220\n"
"v_mfma_f32_16x16x32_bf16 a[32:35], a[192:195], v[20:23], a[32:35]\n"
"buffer_load_dwordx4 v246, s[12:15], s84 offen offset:2048 lds\n"
"v_mfma_f32_16x16x32_bf16 a[36:39], a[196:199], v[20:23], a[36:39]\n"
"v_exp_f32 v221, v221\n"
"v_mfma_f32_16x16x32_bf16 a[40:43], a[200:203], v[20:23], a[40:43]\n"
"ds_read_b64_tr_b16 a[246:247], v243 offset:61952\n"
"buffer_load_dwordx4 v[128:131], v252, s[64:67], s49 offen\n"
"v_cvt_pk_bf16_f32 v16, v220, v221\n"
"v_mfma_f32_16x16x32_bf16 a[44:47], a[204:207], v[20:23], a[44:47]\n"
"v_exp_f32 v222, v222\n"
"v_mfma_f32_16x16x32_bf16 a[48:51], a[208:211], v[20:23], a[48:51]\n"
"ds_read_b64_tr_b16 a[248:249], v242 offset:58112\n"
"v_add_f32 v44, v44, v7\n"
"v_mfma_f32_16x16x32_bf16 a[52:55], a[212:215], v[20:23], a[52:55]\n"
"v_exp_f32 v223, v223\n"
"v_mfma_f32_16x16x32_bf16 a[56:59], a[216:219], v[20:23], a[56:59]\n"
"ds_read_b64_tr_b16 a[250:251], v242 offset:62208\n"
"v_cvt_pk_bf16_f32 v17, v222, v223\n"
"v_mfma_f32_16x16x32_bf16 a[60:63], a[220:223], v[20:23], a[60:63]\n"
"v_exp_f32 v224, v224\n"
"v_mfma_f32_16x16x32_bf16 v[48:51], v[40:43], v[20:23], v[48:51]\n"
"ds_read_b64_tr_b16 a[252:253], v243 offset:58112\n"
"v_add_f32 v52, v52, v220\n"
"v_mfma_f32_16x16x32_bf16 v[228:231], a[128:131], v[112:115], v[36:39]\n"
"v_exp_f32 v225, v225\n"
"v_mul_u32_u24 v249, 3641, v248\n"
"v_mfma_f32_16x16x32_bf16 v[228:231], a[132:135], v[116:119], v[228:231]\n"
"s_movk_i32 s60, 864\n"
"ds_read_b64_tr_b16 a[254:255], v243 offset:62208\n"
"v_cvt_pk_bf16_f32 v18, v224, v225\n"
"v_mfma_f32_16x16x32_bf16 v[228:231], a[136:139], v[120:123], v[228:231]\n"
"v_exp_f32 v226, v226\n"
"v_mfma_f32_16x16x32_bf16 v[228:231], a[140:143], v[124:127], v[228:231]\n"
"v_add_f32 v52, v52, v221\n"
"v_add_f32 v52, v52, v222\n"
"v_mfma_f32_16x16x32_bf16 v[232:235], a[144:147], v[112:115], v[36:39]\n"
"v_exp_f32 v227, v227\n"
"v_mfma_f32_16x16x32_bf16 v[232:235], a[148:151], v[116:119], v[232:235]\n"
"v_cvt_pk_bf16_f32 v19, v226, v227\n"
"v_add_f32 v52, v52, v223\n"
"v_mfma_f32_16x16x32_bf16 v[232:235], a[152:155], v[120:123], v[232:235]\n"
"buffer_load_dwordx4 v247, s[12:15], s84 offen offset:3072 lds\n"
"v_add_u32 v206, 4096, v207\n"
"v_mfma_f32_16x16x32_bf16 v[232:235], a[156:159], v[124:127], v[232:235]\n"
"s_add_u32 s84, s84, 16384\n"
"v_exp_f32 v228, v228\n"
"v_mfma_f32_16x16x32_bf16 a[64:67], a[192:195], v[16:19], a[64:67]\n"
"v_add_f32 v52, v52, v224\n"
"v_add_f32 v52, v52, v225\n"
"v_mfma_f32_16x16x32_bf16 a[68:71], a[196:199], v[16:19], a[68:71]\n"
"v_exp_f32 v229, v229\n"
"v_mfma_f32_16x16x32_bf16 a[72:75], a[200:203], v[16:19], a[72:75]\n"
"v_cvt_pk_bf16_f32 v20, v228, v229\n"
"v_add_f32 v52, v52, v226\n"
"v_mfma_f32_16x16x32_bf16 a[76:79], a[204:207], v[16:19], a[76:79]\n"
"s_waitcnt vmcnt(20) lgkmcnt(0)\n"
"v_exp_f32 v230, v230\n"
"v_mfma_f32_16x16x32_bf16 a[80:83], a[208:211], v[16:19], a[80:83]\n"
"s_barrier\n"
"v_mfma_f32_16x16x32_bf16 a[84:87], a[212:215], v[16:19], a[84:87]\n"
"v_exp_f32 v231, v231\n"
"v_mfma_f32_16x16x32_bf16 a[88:91], a[216:219], v[16:19], a[88:91]\n"
"v_cvt_pk_bf16_f32 v21, v230, v231\n"
"v_add_f32 v52, v52, v227\n"
"v_lshrrev_b32 v217, 16, v249\n"
"v_mfma_f32_16x16x32_bf16 a[92:95], a[220:223], v[16:19], a[92:95]\n"
"v_exp_f32 v232, v232\n"
"v_mfma_f32_16x16x32_bf16 v[0:3], a[160:163], v[64:67], v[24:27]\n"
"ds_read_b128 a[128:131], v241 offset:0\n"
"v_mad_u32_u24 v210, v217, s60, v207\n"
"v_mfma_f32_16x16x32_bf16 v[0:3], a[164:167], v[68:71], v[0:3]\n"
"v_exp_f32 v233, v233\n"
"v_mfma_f32_16x16x32_bf16 v[0:3], a[168:171], v[72:75], v[0:3]\n"
"ds_read_b128 a[132:135], v241 offset:256\n"
"v_cvt_pk_bf16_f32 v22, v232, v233\n"
"v_mfma_f32_16x16x32_bf16 v[0:3], a[172:175], v[76:79], v[0:3]\n"
"v_exp_f32 v234, v234\n"
"v_mfma_f32_16x16x32_bf16 v[4:7], a[176:179], v[64:67], v[24:27]\n"
"ds_read_b128 a[136:139], v241 offset:512\n"
"v_add_u32 v249, 233024, v249\n"
"v_mfma_f32_16x16x32_bf16 v[4:7], a[180:183], v[68:71], v[4:7]\n"
"v_exp_f32 v235, v235\n"
"v_mfma_f32_16x16x32_bf16 v[4:7], a[184:187], v[72:75], v[4:7]\n"
"ds_read_b128 a[140:143], v241 offset:768\n"
"v_cvt_pk_bf16_f32 v23, v234, v235\n"
"v_mfma_f32_16x16x32_bf16 v[4:7], a[188:191], v[76:79], v[4:7]\n"
"s_add_u32 m0, s86, 32768\n"
"v_exp_f32 v0, v0\n"
"v_mfma_f32_16x16x32_bf16 a[96:99], a[192:195], v[20:23], a[96:99]\n"
"buffer_load_dwordx4 v246, s[8:11], s84 offen offset:0 lds\n"
"v_lshrrev_b32 v217, 16, v249\n"
"v_mfma_f32_16x16x32_bf16 a[100:103], a[196:199], v[20:23], a[100:103]\n"
"v_exp_f32 v1, v1\n"
"v_mfma_f32_16x16x32_bf16 a[104:107], a[200:203], v[20:23], a[104:107]\n"
"ds_read_b128 a[144:147], v241 offset:4096\n"
"v_cvt_pk_bf16_f32 v16, v0, v1\n"
"v_mfma_f32_16x16x32_bf16 a[108:111], a[204:207], v[20:23], a[108:111]\n"
"v_exp_f32 v2, v2\n"
"v_mfma_f32_16x16x32_bf16 a[112:115], a[208:211], v[20:23], a[112:115]\n"
"ds_read_b128 a[148:151], v241 offset:4352\n"
"v_add_f32 v44, v44, v0\n"
"v_mfma_f32_16x16x32_bf16 a[116:119], a[212:215], v[20:23], a[116:119]\n"
"v_exp_f32 v3, v3\n"
"v_mfma_f32_16x16x32_bf16 a[120:123], a[216:219], v[20:23], a[120:123]\n"
"ds_read_b128 a[152:155], v241 offset:4608\n"
"v_cvt_pk_bf16_f32 v17, v2, v3\n"
"v_mfma_f32_16x16x32_bf16 a[124:127], a[220:223], v[20:23], a[124:127]\n"
"v_exp_f32 v4, v4\n"
"v_mad_u32_u24 v211, v217, s60, v207\n"
"v_mfma_f32_16x16x32_bf16 v[56:59], v[40:43], v[20:23], v[56:59]\n"
"ds_read_b128 a[156:159], v241 offset:4864\n"
"v_add_f32 v44, v44, v1\n"
"v_mfma_f32_16x16x32_bf16 v[8:11], a[160:163], v[80:83], v[28:31]\n"
"v_exp_f32 v5, v5\n"
"v_mfma_f32_16x16x32_bf16 v[8:11], a[164:167], v[84:87], v[8:11]\n"
"ds_read_b64_tr_b16 a[192:193], v244 offset:16384\n"
"v_cvt_pk_bf16_f32 v18, v4, v5\n"
"v_mfma_f32_16x16x32_bf16 v[8:11], a[168:171], v[88:91], v[8:11]\n"
"v_exp_f32 v6, v6\n"
"v_mfma_f32_16x16x32_bf16 v[8:11], a[172:175], v[92:95], v[8:11]\n"
"ds_read_b64_tr_b16 a[194:195], v244 offset:20480\n"
"v_add_f32 v44, v44, v2\n"
"v_mfma_f32_16x16x32_bf16 v[12:15], a[176:179], v[80:83], v[28:31]\n"
"v_exp_f32 v7, v7\n"
"v_mfma_f32_16x16x32_bf16 v[12:15], a[180:183], v[84:87], v[12:15]\n"
"ds_read_b64_tr_b16 a[196:197], v245 offset:16384\n"
"v_cvt_pk_bf16_f32 v19, v6, v7\n"
"v_add_u32 v249, 233024, v249\n"
"v_mfma_f32_16x16x32_bf16 v[12:15], a[184:187], v[88:91], v[12:15]\n"
"buffer_load_dwordx4 v246, s[8:11], s84 offen offset:1024 lds\n"
"v_lshrrev_b32 v217, 16, v249\n"
"v_mfma_f32_16x16x32_bf16 v[12:15], a[188:191], v[92:95], v[12:15]\n"
"s_waitcnt lgkmcnt(11)\n"
"v_exp_f32 v8, v8\n"
"v_mfma_f32_16x16x32_bf16 a[0:3], a[224:227], v[16:19], a[0:3]\n"
"ds_read_b64_tr_b16 a[198:199], v245 offset:20480\n"
"v_add_f32 v44, v44, v3\n"
"v_mfma_f32_16x16x32_bf16 a[4:7], a[228:231], v[16:19], a[4:7]\n"
"v_exp_f32 v9, v9\n"
"v_mfma_f32_16x16x32_bf16 a[8:11], a[232:235], v[16:19], a[8:11]\n"
"ds_read_b64_tr_b16 a[200:201], v244 offset:16640\n"
"v_cvt_pk_bf16_f32 v20, v8, v9\n"
"v_mfma_f32_16x16x32_bf16 a[12:15], a[236:239], v[16:19], a[12:15]\n"
"v_exp_f32 v10, v10\n"
"v_mfma_f32_16x16x32_bf16 a[16:19], a[240:243], v[16:19], a[16:19]\n"
"ds_read_b64_tr_b16 a[202:203], v244 offset:20736\n"
"v_add_f32 v44, v44, v4\n"
"v_mfma_f32_16x16x32_bf16 a[20:23], a[244:247], v[16:19], a[20:23]\n"
"v_exp_f32 v11, v11\n"
"v_mad_u32_u24 v212, v217, s60, v207\n"
"v_mfma_f32_16x16x32_bf16 a[24:27], a[248:251], v[16:19], a[24:27]\n"
"ds_read_b64_tr_b16 a[204:205], v245 offset:16640\n"
"v_cvt_pk_bf16_f32 v21, v10, v11\n"
"v_mfma_f32_16x16x32_bf16 a[28:31], a[252:255], v[16:19], a[28:31]\n"
"v_exp_f32 v12, v12\n"
"v_mfma_f32_16x16x32_bf16 v[220:223], a[160:163], v[96:99], v[32:35]\n"
"ds_read_b64_tr_b16 a[206:207], v245 offset:20736\n"
"v_add_f32 v44, v44, v5\n"
"v_mfma_f32_16x16x32_bf16 v[220:223], a[164:167], v[100:103], v[220:223]\n"
"v_exp_f32 v13, v13\n"
"v_mfma_f32_16x16x32_bf16 v[220:223], a[168:171], v[104:107], v[220:223]\n"
"ds_read_b64_tr_b16 a[208:209], v244 offset:16896\n"
"v_cvt_pk_bf16_f32 v22, v12, v13\n"
"v_mfma_f32_16x16x32_bf16 v[220:223], a[172:175], v[108:111], v[220:223]\n"
"v_exp_f32 v14, v14\n"
"v_mfma_f32_16x16x32_bf16 v[224:227], a[176:179], v[96:99], v[32:35]\n"
"ds_read_b64_tr_b16 a[210:211], v244 offset:20992\n"
"v_add_f32 v44, v44, v6\n"
"v_add_u32 v249, 233024, v249\n"
"v_mfma_f32_16x16x32_bf16 v[224:227], a[180:183], v[100:103], v[224:227]\n"
"v_exp_f32 v15, v15\n"
"v_mfma_f32_16x16x32_bf16 v[224:227], a[184:187], v[104:107], v[224:227]\n"
"ds_read_b64_tr_b16 a[212:213], v245 offset:16896\n"
"v_cvt_pk_bf16_f32 v23, v14, v15\n"
"v_mfma_f32_16x16x32_bf16 v[224:227], a[188:191], v[108:111], v[224:227]\n"
"v_exp_f32 v220, v220\n"
"v_mfma_f32_16x16x32_bf16 a[32:35], a[224:227], v[20:23], a[32:35]\n"
"buffer_load_dwordx4 v247, s[8:11], s84 offen offset:2048 lds\n"
"v_lshrrev_b32 v217, 16, v249\n"
"v_mfma_f32_16x16x32_bf16 a[36:39], a[228:231], v[20:23], a[36:39]\n"
"v_exp_f32 v221, v221\n"
"v_mfma_f32_16x16x32_bf16 a[40:43], a[232:235], v[20:23], a[40:43]\n"
"ds_read_b64_tr_b16 a[214:215], v245 offset:20992\n"
"v_cvt_pk_bf16_f32 v16, v220, v221\n"
"v_mfma_f32_16x16x32_bf16 a[44:47], a[236:239], v[20:23], a[44:47]\n"
"v_exp_f32 v222, v222\n"
"v_mfma_f32_16x16x32_bf16 a[48:51], a[240:243], v[20:23], a[48:51]\n"
"ds_read_b64_tr_b16 a[216:217], v244 offset:17152\n"
"v_add_f32 v44, v44, v7\n"
"v_mfma_f32_16x16x32_bf16 a[52:55], a[244:247], v[20:23], a[52:55]\n"
"v_exp_f32 v223, v223\n"
"v_mfma_f32_16x16x32_bf16 a[56:59], a[248:251], v[20:23], a[56:59]\n"
"ds_read_b64_tr_b16 a[218:219], v244 offset:21248\n"
"v_cvt_pk_bf16_f32 v17, v222, v223\n"
"v_mfma_f32_16x16x32_bf16 a[60:63], a[252:255], v[20:23], a[60:63]\n"
"v_exp_f32 v224, v224\n"
"v_mad_u32_u24 v213, v217, s60, v207\n"
"v_mfma_f32_16x16x32_bf16 v[48:51], v[40:43], v[20:23], v[48:51]\n"
"ds_read_b64_tr_b16 a[220:221], v245 offset:17152\n"
"v_add_f32 v52, v52, v220\n"
"v_mfma_f32_16x16x32_bf16 v[228:231], a[160:163], v[112:115], v[36:39]\n"
"v_exp_f32 v225, v225\n"
"v_mfma_f32_16x16x32_bf16 v[228:231], a[164:167], v[116:119], v[228:231]\n"
"ds_read_b64_tr_b16 a[222:223], v245 offset:21248\n"
"v_cvt_pk_bf16_f32 v18, v224, v225\n"
"v_mfma_f32_16x16x32_bf16 v[228:231], a[168:171], v[120:123], v[228:231]\n"
"v_exp_f32 v226, v226\n"
"v_mfma_f32_16x16x32_bf16 v[228:231], a[172:175], v[124:127], v[228:231]\n"
"v_add_f32 v52, v52, v221\n"
"v_add_f32 v52, v52, v222\n"
"v_mfma_f32_16x16x32_bf16 v[232:235], a[176:179], v[112:115], v[36:39]\n"
"v_exp_f32 v227, v227\n"
"v_mfma_f32_16x16x32_bf16 v[232:235], a[180:183], v[116:119], v[232:235]\n"
"v_cvt_pk_bf16_f32 v19, v226, v227\n"
"v_add_f32 v52, v52, v223\n"
"v_add_u32 v249, 233024, v249\n"
"v_mfma_f32_16x16x32_bf16 v[232:235], a[184:187], v[120:123], v[232:235]\n"
"buffer_load_dwordx4 v247, s[8:11], s84 offen offset:3072 lds\n"
"v_lshrrev_b32 v217, 16, v249\n"
"v_mfma_f32_16x16x32_bf16 v[232:235], a[188:191], v[124:127], v[232:235]\n"
"v_exp_f32 v228, v228\n"
"v_mfma_f32_16x16x32_bf16 a[64:67], a[224:227], v[16:19], a[64:67]\n"
"buffer_load_dwordx4 v[164:167], v252, s[68:71], s49 offen\n"
"v_add_f32 v52, v52, v224\n"
"v_add_f32 v52, v52, v225\n"
"v_mfma_f32_16x16x32_bf16 a[68:71], a[228:231], v[16:19], a[68:71]\n"
"v_exp_f32 v229, v229\n"
"v_mfma_f32_16x16x32_bf16 a[72:75], a[232:235], v[16:19], a[72:75]\n"
"v_cvt_pk_bf16_f32 v20, v228, v229\n"
"v_add_f32 v52, v52, v226\n"
"v_mfma_f32_16x16x32_bf16 a[76:79], a[236:239], v[16:19], a[76:79]\n"
"v_exp_f32 v230, v230\n"
"v_mfma_f32_16x16x32_bf16 a[80:83], a[240:243], v[16:19], a[80:83]\n"
"s_waitcnt lgkmcnt(8)\n"
"v_mad_u32_u24 v214, v217, s60, v206\n"
"v_mfma_f32_16x16x32_bf16 a[84:87], a[244:247], v[16:19], a[84:87]\n"
"v_exp_f32 v231, v231\n"
"v_mfma_f32_16x16x32_bf16 a[88:91], a[248:251], v[16:19], a[88:91]\n"
"v_cvt_pk_bf16_f32 v21, v230, v231\n"
"v_add_f32 v52, v52, v227\n"
"v_mfma_f32_16x16x32_bf16 a[92:95], a[252:255], v[16:19], a[92:95]\n"
"v_exp_f32 v232, v232\n"
"v_mfma_f32_16x16x32_bf16 v[0:3], a[128:131], v[64:67], v[24:27]\n"
"ds_read_b128 a[160:163], v241 offset:8192\n"
"v_add_u32 v249, 233024, v249\n"
"v_mfma_f32_16x16x32_bf16 v[0:3], a[132:135], v[68:71], v[0:3]\n"
"v_exp_f32 v233, v233\n"
"v_mfma_f32_16x16x32_bf16 v[0:3], a[136:139], v[72:75], v[0:3]\n"
"ds_read_b128 a[164:167], v241 offset:8448\n"
"v_cvt_pk_bf16_f32 v22, v232, v233\n"
"v_mfma_f32_16x16x32_bf16 v[0:3], a[140:143], v[76:79], v[0:3]\n"
"v_exp_f32 v234, v234\n"
"v_mfma_f32_16x16x32_bf16 v[4:7], a[144:147], v[64:67], v[24:27]\n"
"ds_read_b128 a[168:171], v241 offset:8704\n"
"v_lshrrev_b32 v217, 16, v249\n"
"v_mfma_f32_16x16x32_bf16 v[4:7], a[148:151], v[68:71], v[4:7]\n"
"v_exp_f32 v235, v235\n"
"v_mfma_f32_16x16x32_bf16 v[4:7], a[152:155], v[72:75], v[4:7]\n"
"ds_read_b128 a[172:175], v241 offset:8960\n"
"v_cvt_pk_bf16_f32 v23, v234, v235\n"
"v_mfma_f32_16x16x32_bf16 v[4:7], a[156:159], v[76:79], v[4:7]\n"
"s_add_u32 m0, s86, 49152\n"
"v_exp_f32 v0, v0\n"
"v_mfma_f32_16x16x32_bf16 a[96:99], a[224:227], v[20:23], a[96:99]\n"
"buffer_load_dwordx4 v246, s[12:15], s84 offen offset:0 lds\n"
"v_mad_u32_u24 v236, v217, s60, v206\n"
"v_mfma_f32_16x16x32_bf16 a[100:103], a[228:231], v[20:23], a[100:103]\n"
"v_exp_f32 v1, v1\n"
"v_mfma_f32_16x16x32_bf16 a[104:107], a[232:235], v[20:23], a[104:107]\n"
"ds_read_b128 a[176:179], v241 offset:12288\n"
"v_cvt_pk_bf16_f32 v16, v0, v1\n"
"v_mfma_f32_16x16x32_bf16 a[108:111], a[236:239], v[20:23], a[108:111]\n"
"v_exp_f32 v2, v2\n"
"v_mfma_f32_16x16x32_bf16 a[112:115], a[240:243], v[20:23], a[112:115]\n"
"ds_read_b128 a[180:183], v241 offset:12544\n"
"v_add_f32 v44, v44, v0\n"
"v_mfma_f32_16x16x32_bf16 a[116:119], a[244:247], v[20:23], a[116:119]\n"
"v_exp_f32 v3, v3\n"
"v_mfma_f32_16x16x32_bf16 a[120:123], a[248:251], v[20:23], a[120:123]\n"
"ds_read_b128 a[184:187], v241 offset:12800\n"
"v_cvt_pk_bf16_f32 v17, v2, v3\n"
"v_mfma_f32_16x16x32_bf16 a[124:127], a[252:255], v[20:23], a[124:127]\n"
"v_exp_f32 v4, v4\n"
"v_add_u32 v249, 233024, v249\n"
"v_mfma_f32_16x16x32_bf16 v[56:59], v[40:43], v[20:23], v[56:59]\n"
"ds_read_b128 a[188:191], v241 offset:13056\n"
"v_add_f32 v44, v44, v1\n"
"v_mfma_f32_16x16x32_bf16 v[8:11], a[128:131], v[80:83], v[28:31]\n"
"v_exp_f32 v5, v5\n"
"v_mfma_f32_16x16x32_bf16 v[8:11], a[132:135], v[84:87], v[8:11]\n"
"ds_read_b64_tr_b16 a[224:225], v244 offset:24576\n"
"v_cvt_pk_bf16_f32 v18, v4, v5\n"
"v_mfma_f32_16x16x32_bf16 v[8:11], a[136:139], v[88:91], v[8:11]\n"
"v_exp_f32 v6, v6\n"
"v_mfma_f32_16x16x32_bf16 v[8:11], a[140:143], v[92:95], v[8:11]\n"
"ds_read_b64_tr_b16 a[226:227], v244 offset:28672\n"
"v_add_f32 v44, v44, v2\n"
"v_mfma_f32_16x16x32_bf16 v[12:15], a[144:147], v[80:83], v[28:31]\n"
"v_exp_f32 v7, v7\n"
"v_mfma_f32_16x16x32_bf16 v[12:15], a[148:151], v[84:87], v[12:15]\n"
"ds_read_b64_tr_b16 a[228:229], v245 offset:24576\n"
"v_cvt_pk_bf16_f32 v19, v6, v7\n"
"v_lshrrev_b32 v217, 16, v249\n"
"v_mfma_f32_16x16x32_bf16 v[12:15], a[152:155], v[88:91], v[12:15]\n"
"buffer_load_dwordx4 v247, s[12:15], s84 offen offset:1024 lds\n"
"v_mad_u32_u24 v237, v217, s60, v206\n"
"v_mfma_f32_16x16x32_bf16 v[12:15], a[156:159], v[92:95], v[12:15]\n"
"s_waitcnt lgkmcnt(11)\n"
"v_exp_f32 v8, v8\n"
"v_mfma_f32_16x16x32_bf16 a[0:3], a[192:195], v[16:19], a[0:3]\n"
"ds_read_b64_tr_b16 a[230:231], v245 offset:28672\n"
"v_add_f32 v44, v44, v3\n"
"v_mfma_f32_16x16x32_bf16 a[4:7], a[196:199], v[16:19], a[4:7]\n"
"v_exp_f32 v9, v9\n"
"v_mfma_f32_16x16x32_bf16 a[8:11], a[200:203], v[16:19], a[8:11]\n"
"ds_read_b64_tr_b16 a[232:233], v244 offset:24832\n"
"v_cvt_pk_bf16_f32 v20, v8, v9\n"
"v_mfma_f32_16x16x32_bf16 a[12:15], a[204:207], v[16:19], a[12:15]\n"
"v_exp_f32 v10, v10\n"
"v_mfma_f32_16x16x32_bf16 a[16:19], a[208:211], v[16:19], a[16:19]\n"
"ds_read_b64_tr_b16 a[234:235], v244 offset:28928\n"
"v_add_f32 v44, v44, v4\n"
"v_mfma_f32_16x16x32_bf16 a[20:23], a[212:215], v[16:19], a[20:23]\n"
"v_exp_f32 v11, v11\n"
"v_add_u32 v249, 233024, v249\n"
"v_mfma_f32_16x16x32_bf16 a[24:27], a[216:219], v[16:19], a[24:27]\n"
"ds_read_b64_tr_b16 a[236:237], v245 offset:24832\n"
"v_cvt_pk_bf16_f32 v21, v10, v11\n"
"v_mfma_f32_16x16x32_bf16 a[28:31], a[220:223], v[16:19], a[28:31]\n"
"v_exp_f32 v12, v12\n"
"v_mfma_f32_16x16x32_bf16 v[220:223], a[128:131], v[96:99], v[32:35]\n"
"ds_read_b64_tr_b16 a[238:239], v245 offset:28928\n"
"v_add_f32 v44, v44, v5\n"
"v_mfma_f32_16x16x32_bf16 v[220:223], a[132:135], v[100:103], v[220:223]\n"
"v_exp_f32 v13, v13\n"
"v_mfma_f32_16x16x32_bf16 v[220:223], a[136:139], v[104:107], v[220:223]\n"
"ds_read_b64_tr_b16 a[240:241], v244 offset:25088\n"
"v_cvt_pk_bf16_f32 v22, v12, v13\n"
"v_mfma_f32_16x16x32_bf16 v[220:223], a[140:143], v[108:111], v[220:223]\n"
"v_exp_f32 v14, v14\n"
"v_mfma_f32_16x16x32_bf16 v[224:227], a[144:147], v[96:99], v[32:35]\n"
"ds_read_b64_tr_b16 a[242:243], v244 offset:29184\n"
"v_add_f32 v44, v44, v6\n"
"v_lshrrev_b32 v217, 16, v249\n"
"v_mfma_f32_16x16x32_bf16 v[224:227], a[148:151], v[100:103], v[224:227]\n"
"v_exp_f32 v15, v15\n"
"v_mfma_f32_16x16x32_bf16 v[224:227], a[152:155], v[104:107], v[224:227]\n"
"ds_read_b64_tr_b16 a[244:245], v245 offset:25088\n"
"v_cvt_pk_bf16_f32 v23, v14, v15\n"
"v_mfma_f32_16x16x32_bf16 v[224:227], a[156:159], v[108:111], v[224:227]\n"
"v_exp_f32 v220, v220\n"
"v_mfma_f32_16x16x32_bf16 a[32:35], a[192:195], v[20:23], a[32:35]\n"
"buffer_load_dwordx4 v246, s[12:15], s84 offen offset:2048 lds\n"
"v_mad_u32_u24 v238, v217, s60, v206\n"
"v_mfma_f32_16x16x32_bf16 a[36:39], a[196:199], v[20:23], a[36:39]\n"
"v_exp_f32 v221, v221\n"
"v_mfma_f32_16x16x32_bf16 a[40:43], a[200:203], v[20:23], a[40:43]\n"
"ds_read_b64_tr_b16 a[246:247], v245 offset:29184\n"
"v_cvt_pk_bf16_f32 v16, v220, v221\n"
"v_mfma_f32_16x16x32_bf16 a[44:47], a[204:207], v[20:23], a[44:47]\n"
"v_exp_f32 v222, v222\n"
"v_mfma_f32_16x16x32_bf16 a[48:51], a[208:211], v[20:23], a[48:51]\n"
"ds_read_b64_tr_b16 a[248:249], v244 offset:25344\n"
"v_add_f32 v44, v44, v7\n"
"v_mfma_f32_16x16x32_bf16 a[52:55], a[212:215], v[20:23], a[52:55]\n"
"v_exp_f32 v223, v223\n"
"v_mfma_f32_16x16x32_bf16 a[56:59], a[216:219], v[20:23], a[56:59]\n"
"ds_read_b64_tr_b16 a[250:251], v244 offset:29440\n"
"v_cvt_pk_bf16_f32 v17, v222, v223\n"
"v_mfma_f32_16x16x32_bf16 a[60:63], a[220:223], v[20:23], a[60:63]\n"
"v_exp_f32 v224, v224\n"
"v_add_u32 v206, 4096, v206\n"
"v_mfma_f32_16x16x32_bf16 v[48:51], v[40:43], v[20:23], v[48:51]\n"
"ds_read_b64_tr_b16 a[252:253], v245 offset:25344\n"
"v_add_f32 v52, v52, v220\n"
"v_mfma_f32_16x16x32_bf16 v[228:231], a[128:131], v[112:115], v[36:39]\n"
"v_exp_f32 v225, v225\n"
"v_mfma_f32_16x16x32_bf16 v[228:231], a[132:135], v[116:119], v[228:231]\n"
"ds_read_b64_tr_b16 a[254:255], v245 offset:29440\n"
"v_cvt_pk_bf16_f32 v18, v224, v225\n"
"v_mfma_f32_16x16x32_bf16 v[228:231], a[136:139], v[120:123], v[228:231]\n"
"v_exp_f32 v226, v226\n"
"v_mfma_f32_16x16x32_bf16 v[228:231], a[140:143], v[124:127], v[228:231]\n"
"v_add_f32 v52, v52, v221\n"
"v_add_f32 v52, v52, v222\n"
"v_mfma_f32_16x16x32_bf16 v[232:235], a[144:147], v[112:115], v[36:39]\n"
"v_exp_f32 v227, v227\n"
"v_mfma_f32_16x16x32_bf16 v[232:235], a[148:151], v[116:119], v[232:235]\n"
"v_cvt_pk_bf16_f32 v19, v226, v227\n"
"v_add_f32 v52, v52, v223\n"
"v_add_u32 v249, 233024, v249\n"
"v_mfma_f32_16x16x32_bf16 v[232:235], a[152:155], v[120:123], v[232:235]\n"
"buffer_load_dwordx4 v247, s[12:15], s84 offen offset:3072 lds\n"
"v_lshrrev_b32 v217, 16, v249\n"
"v_mfma_f32_16x16x32_bf16 v[232:235], a[156:159], v[124:127], v[232:235]\n"
"s_add_u32 s84, s84, 16384\n"
"v_exp_f32 v228, v228\n"
"v_mfma_f32_16x16x32_bf16 a[64:67], a[192:195], v[16:19], a[64:67]\n"
"v_add_f32 v52, v52, v224\n"
"v_add_f32 v52, v52, v225\n"
"v_mfma_f32_16x16x32_bf16 a[68:71], a[196:199], v[16:19], a[68:71]\n"
"v_exp_f32 v229, v229\n"
"v_mfma_f32_16x16x32_bf16 a[72:75], a[200:203], v[16:19], a[72:75]\n"
"v_cvt_pk_bf16_f32 v20, v228, v229\n"
"v_add_f32 v52, v52, v226\n"
"v_mfma_f32_16x16x32_bf16 a[76:79], a[204:207], v[16:19], a[76:79]\n"
"s_waitcnt vmcnt(21) lgkmcnt(0)\n"
"v_exp_f32 v230, v230\n"
"v_mfma_f32_16x16x32_bf16 a[80:83], a[208:211], v[16:19], a[80:83]\n"
"s_barrier\n"
"v_mfma_f32_16x16x32_bf16 a[84:87], a[212:215], v[16:19], a[84:87]\n"
"v_exp_f32 v231, v231\n"
"v_mfma_f32_16x16x32_bf16 a[88:91], a[216:219], v[16:19], a[88:91]\n"
"v_cvt_pk_bf16_f32 v21, v230, v231\n"
"v_add_f32 v52, v52, v227\n"
"v_mad_u32_u24 v239, v217, s60, v206\n"
"v_mfma_f32_16x16x32_bf16 a[92:95], a[220:223], v[16:19], a[92:95]\n"
"s_and_b32 s56, s43, 3\n"
"v_exp_f32 v232, v232\n"
"v_mfma_f32_16x16x32_bf16 v[0:3], a[160:163], v[64:67], v[24:27]\n"
"s_lshr_b32 s57, s43, 2\n"
"ds_read_b128 a[128:131], v241 offset:32768\n"
"v_mfma_f32_16x16x32_bf16 v[0:3], a[164:167], v[68:71], v[0:3]\n"
"s_add_u32 s49, s49, 9216\n"
"v_exp_f32 v233, v233\n"
"v_mfma_f32_16x16x32_bf16 v[0:3], a[168:171], v[72:75], v[0:3]\n"
"ds_read_b128 a[132:135], v241 offset:33024\n"
"buffer_load_dwordx4 v[132:135], v252, s[64:67], s49 offen\n"
"v_cvt_pk_bf16_f32 v22, v232, v233\n"
"v_mfma_f32_16x16x32_bf16 v[0:3], a[172:175], v[76:79], v[0:3]\n"
"s_mul_i32 s57, s57, 1179648\n"
"v_exp_f32 v234, v234\n"
"v_mfma_f32_16x16x32_bf16 v[4:7], a[176:179], v[64:67], v[24:27]\n"
"s_mul_i32 s62, s56, 288\n"
"ds_read_b128 a[136:139], v241 offset:33280\n"
"v_mfma_f32_16x16x32_bf16 v[4:7], a[180:183], v[68:71], v[4:7]\n"
"s_add_u32 s57, s57, s62\n"
"v_exp_f32 v235, v235\n"
"v_mfma_f32_16x16x32_bf16 v[4:7], a[184:187], v[72:75], v[4:7]\n"
"s_lshl_b32 s58, s56, 3\n"
"ds_read_b128 a[140:143], v241 offset:33536\n"
"v_cvt_pk_bf16_f32 v23, v234, v235\n"
"v_mfma_f32_16x16x32_bf16 v[4:7], a[188:191], v[76:79], v[4:7]\n"
"s_add_u32 m0, s86, 65536\n"
"v_exp_f32 v0, v0\n"
"v_mfma_f32_16x16x32_bf16 a[96:99], a[192:195], v[20:23], a[96:99]\n"
"s_add_u32 s58, s58, s41\n"
"buffer_load_dwordx4 v246, s[8:11], s84 offen offset:0 lds\n"
"v_mfma_f32_16x16x32_bf16 a[100:103], a[196:199], v[20:23], a[100:103]\n"
"s_add_u32 s58, s58, 0\n"
"v_exp_f32 v1, v1\n"
"v_mfma_f32_16x16x32_bf16 a[104:107], a[200:203], v[20:23], a[104:107]\n"
"s_mul_i32 s58, s58, 36864\n"
"ds_read_b128 a[144:147], v241 offset:36864\n"
"v_cvt_pk_bf16_f32 v16, v0, v1\n"
"v_mfma_f32_16x16x32_bf16 a[108:111], a[204:207], v[20:23], a[108:111]\n"
"s_add_u32 s20, s58, s57\n"
"v_exp_f32 v2, v2\n"
"v_mfma_f32_16x16x32_bf16 a[112:115], a[208:211], v[20:23], a[112:115]\n"
"s_lshl_b32 s58, s56, 3\n"
"ds_read_b128 a[148:151], v241 offset:37120\n"
"v_add_f32 v44, v44, v0\n"
"v_mfma_f32_16x16x32_bf16 a[116:119], a[212:215], v[20:23], a[116:119]\n"
"s_add_u32 s58, s58, s41\n"
"v_exp_f32 v3, v3\n"
"v_mfma_f32_16x16x32_bf16 a[120:123], a[216:219], v[20:23], a[120:123]\n"
"s_add_u32 s58, s58, 4\n"
"ds_read_b128 a[152:155], v241 offset:37376\n"
"v_cvt_pk_bf16_f32 v17, v2, v3\n"
"v_mfma_f32_16x16x32_bf16 a[124:127], a[220:223], v[20:23], a[124:127]\n"
"s_mul_i32 s58, s58, 36864\n"
"v_exp_f32 v4, v4\n"
"v_mfma_f32_16x16x32_bf16 v[56:59], v[40:43], v[20:23], v[56:59]\n"
"s_add_u32 s21, s58, s57\n"
"ds_read_b128 a[156:159], v241 offset:37632\n"
"v_add_f32 v44, v44, v1\n"
"v_mfma_f32_16x16x32_bf16 v[8:11], a[160:163], v[80:83], v[28:31]\n"
"s_add_u32 s58, s41, 0\n"
"v_exp_f32 v5, v5\n"
"v_mfma_f32_16x16x32_bf16 v[8:11], a[164:167], v[84:87], v[8:11]\n"
"s_mul_i32 s59, s58, 43\n"
"ds_read_b64_tr_b16 a[192:193], v244 offset:49152\n"
"v_cvt_pk_bf16_f32 v18, v4, v5\n"
"v_mfma_f32_16x16x32_bf16 v[8:11], a[168:171], v[88:91], v[8:11]\n"
"s_lshr_b32 s59, s59, 7\n"
"v_exp_f32 v6, v6\n"
"v_mfma_f32_16x16x32_bf16 v[8:11], a[172:175], v[92:95], v[8:11]\n"
"ds_read_b64_tr_b16 a[194:195], v244 offset:53248\n"
"v_add_f32 v44, v44, v2\n"
"v_mfma_f32_16x16x32_bf16 v[12:15], a[176:179], v[80:83], v[28:31]\n"
"v_exp_f32 v7, v7\n"
"v_mfma_f32_16x16x32_bf16 v[12:15], a[180:183], v[84:87], v[12:15]\n"
"ds_read_b64_tr_b16 a[196:197], v245 offset:49152\n"
"v_cvt_pk_bf16_f32 v19, v6, v7\n"
"v_mfma_f32_16x16x32_bf16 v[12:15], a[184:187], v[88:91], v[12:15]\n"
"buffer_load_dwordx4 v246, s[8:11], s84 offen offset:1024 lds\n"
"v_mfma_f32_16x16x32_bf16 v[12:15], a[188:191], v[92:95], v[12:15]\n"
"s_waitcnt lgkmcnt(11)\n"
"v_exp_f32 v8, v8\n"
"v_mfma_f32_16x16x32_bf16 a[0:3], a[224:227], v[16:19], a[0:3]\n"
"ds_read_b64_tr_b16 a[198:199], v245 offset:53248\n"
"v_add_f32 v44, v44, v3\n"
"v_mfma_f32_16x16x32_bf16 a[4:7], a[228:231], v[16:19], a[4:7]\n"
"v_exp_f32 v9, v9\n"
"v_mfma_f32_16x16x32_bf16 a[8:11], a[232:235], v[16:19], a[8:11]\n"
"ds_read_b64_tr_b16 a[200:201], v244 offset:49408\n"
"v_cvt_pk_bf16_f32 v20, v8, v9\n"
"v_mfma_f32_16x16x32_bf16 a[12:15], a[236:239], v[16:19], a[12:15]\n"
"v_exp_f32 v10, v10\n"
"v_mfma_f32_16x16x32_bf16 a[16:19], a[240:243], v[16:19], a[16:19]\n"
"ds_read_b64_tr_b16 a[202:203], v244 offset:53504\n"
"v_add_f32 v44, v44, v4\n"
"v_mfma_f32_16x16x32_bf16 a[20:23], a[244:247], v[16:19], a[20:23]\n"
"v_exp_f32 v11, v11\n"
"v_mfma_f32_16x16x32_bf16 a[24:27], a[248:251], v[16:19], a[24:27]\n"
"ds_read_b64_tr_b16 a[204:205], v245 offset:49408\n"
"buffer_load_dwordx4 v[168:171], v252, s[68:71], s49 offen\n"
"v_cvt_pk_bf16_f32 v21, v10, v11\n"
"v_mfma_f32_16x16x32_bf16 a[28:31], a[252:255], v[16:19], a[28:31]\n"
"s_mul_i32 s60, s59, 3\n"
"v_exp_f32 v12, v12\n"
"v_mfma_f32_16x16x32_bf16 v[220:223], a[160:163], v[96:99], v[32:35]\n"
"s_sub_u32 s58, s58, s60\n"
"ds_read_b64_tr_b16 a[206:207], v245 offset:53504\n"
"v_add_f32 v44, v44, v5\n"
"v_mfma_f32_16x16x32_bf16 v[220:223], a[164:167], v[100:103], v[220:223]\n"
"s_add_u32 s58, s58, s56\n"
"v_exp_f32 v13, v13\n"
"v_mfma_f32_16x16x32_bf16 v[220:223], a[168:171], v[104:107], v[220:223]\n"
"s_add_u32 s58, s58, 1\n"
"ds_read_b64_tr_b16 a[208:209], v244 offset:49664\n"
"v_cvt_pk_bf16_f32 v22, v12, v13\n"
"v_mfma_f32_16x16x32_bf16 v[220:223], a[172:175], v[108:111], v[220:223]\n"
"s_and_b32 s58, s58, 3\n"
"v_exp_f32 v14, v14\n"
"v_mfma_f32_16x16x32_bf16 v[224:227], a[176:179], v[96:99], v[32:35]\n"
"s_lshl_b32 s58, s58, 3\n"
"ds_read_b64_tr_b16 a[210:211], v244 offset:53760\n"
"v_add_f32 v44, v44, v6\n"
"v_mfma_f32_16x16x32_bf16 v[224:227], a[180:183], v[100:103], v[224:227]\n"
"s_add_u32 s58, s58, s59\n"
"v_exp_f32 v15, v15\n"
"v_mfma_f32_16x16x32_bf16 v[224:227], a[184:187], v[104:107], v[224:227]\n"
"s_mul_i32 s58, s58, 36864\n"
"ds_read_b64_tr_b16 a[212:213], v245 offset:49664\n"
"v_cvt_pk_bf16_f32 v23, v14, v15\n"
"v_mfma_f32_16x16x32_bf16 v[224:227], a[188:191], v[108:111], v[224:227]\n"
"s_add_u32 s22, s58, s57\n"
"v_exp_f32 v220, v220\n"
"v_mfma_f32_16x16x32_bf16 a[32:35], a[224:227], v[20:23], a[32:35]\n"
"s_add_u32 s58, s41, 4\n"
"buffer_load_dwordx4 v247, s[8:11], s84 offen offset:2048 lds\n"
"v_mfma_f32_16x16x32_bf16 a[36:39], a[228:231], v[20:23], a[36:39]\n"
"s_mul_i32 s59, s58, 43\n"
"v_exp_f32 v221, v221\n"
"v_mfma_f32_16x16x32_bf16 a[40:43], a[232:235], v[20:23], a[40:43]\n"
"s_lshr_b32 s59, s59, 7\n"
"ds_read_b64_tr_b16 a[214:215], v245 offset:53760\n"
"v_cvt_pk_bf16_f32 v16, v220, v221\n"
"v_mfma_f32_16x16x32_bf16 a[44:47], a[236:239], v[20:23], a[44:47]\n"
"s_mul_i32 s60, s59, 3\n"
"v_exp_f32 v222, v222\n"
"v_mfma_f32_16x16x32_bf16 a[48:51], a[240:243], v[20:23], a[48:51]\n"
"s_sub_u32 s58, s58, s60\n"
"ds_read_b64_tr_b16 a[216:217], v244 offset:49920\n"
"v_add_f32 v44, v44, v7\n"
"v_mfma_f32_16x16x32_bf16 a[52:55], a[244:247], v[20:23], a[52:55]\n"
"s_add_u32 s58, s58, s56\n"
"v_exp_f32 v223, v223\n"
"v_mfma_f32_16x16x32_bf16 a[56:59], a[248:251], v[20:23], a[56:59]\n"
"s_add_u32 s58, s58, 1\n"
"ds_read_b64_tr_b16 a[218:219], v244 offset:54016\n"
"v_cvt_pk_bf16_f32 v17, v222, v223\n"
"AT_pg10:\n"
"s_cbranch_execz AT_hr\n"
"v_mfma_f32_16x16x32_bf16 a[60:63], a[252:255], v[20:23], a[60:63]\n"
"s_add_u32 s49, s49, 285696\n"
"v_exp_f32 v224, v224\n"
"v_mfma_f32_16x16x32_bf16 v[48:51], v[40:43], v[20:23], v[48:51]\n"
"ds_read_b64_tr_b16 a[220:221], v245 offset:49920\n"
"v_add_f32 v52, v52, v220\n"
"v_mfma_f32_16x16x32_bf16 v[228:231], a[160:163], v[112:115], v[36:39]\n"
"v_exp_f32 v225, v225\n"
"v_mfma_f32_16x16x32_bf16 v[228:231], a[164:167], v[116:119], v[228:231]\n"
"ds_read_b64_tr_b16 a[222:223], v245 offset:54016\n"
"v_cvt_pk_bf16_f32 v18, v224, v225\n"
"v_mfma_f32_16x16x32_bf16 v[228:231], a[168:171], v[120:123], v[228:231]\n"
"v_exp_f32 v226, v226\n"
"v_mfma_f32_16x16x32_bf16 v[228:231], a[172:175], v[124:127], v[228:231]\n"
"v_add_f32 v52, v52, v221\n"
"v_add_f32 v52, v52, v222\n"
"v_mfma_f32_16x16x32_bf16 v[232:235], a[176:179], v[112:115], v[36:39]\n"
"v_exp_f32 v227, v227\n"
"v_mfma_f32_16x16x32_bf16 v[232:235], a[180:183], v[116:119], v[232:235]\n"
"v_cvt_pk_bf16_f32 v19, v226, v227\n"
"v_add_f32 v52, v52, v223\n"
"v_mfma_f32_16x16x32_bf16 v[232:235], a[184:187], v[120:123], v[232:235]\n"
"buffer_load_dwordx4 v247, s[8:11], s84 offen offset:3072 lds\n"
"v_mfma_f32_16x16x32_bf16 v[232:235], a[188:191], v[124:127], v[232:235]\n"
"v_exp_f32 v228, v228\n"
"v_mfma_f32_16x16x32_bf16 a[64:67], a[224:227], v[16:19], a[64:67]\n"
"v_add_f32 v52, v52, v224\n"
"v_add_f32 v52, v52, v225\n"
"v_mfma_f32_16x16x32_bf16 a[68:71], a[228:231], v[16:19], a[68:71]\n"
"v_exp_f32 v229, v229\n"
"v_mfma_f32_16x16x32_bf16 a[72:75], a[232:235], v[16:19], a[72:75]\n"
"buffer_load_dwordx4 v[136:139], v252, s[64:67], s49 offen\n"
"v_cvt_pk_bf16_f32 v20, v228, v229\n"
"v_add_f32 v52, v52, v226\n"
"v_mfma_f32_16x16x32_bf16 a[76:79], a[236:239], v[16:19], a[76:79]\n"
"s_and_b32 s58, s58, 3\n"
"v_exp_f32 v230, v230\n"
"v_mfma_f32_16x16x32_bf16 a[80:83], a[240:243], v[16:19], a[80:83]\n"
"s_waitcnt lgkmcnt(8)\n"
"v_mfma_f32_16x16x32_bf16 a[84:87], a[244:247], v[16:19], a[84:87]\n"
"s_lshl_b32 s58, s58, 3\n"
"v_exp_f32 v231, v231\n"
"v_mfma_f32_16x16x32_bf16 a[88:91], a[248:251], v[16:19], a[88:91]\n"
"s_add_u32 s58, s58, s59\n"
"v_cvt_pk_bf16_f32 v21, v230, v231\n"
"v_add_f32 v52, v52, v227\n"
"v_mfma_f32_16x16x32_bf16 a[92:95], a[252:255], v[16:19], a[92:95]\n"
"s_mul_i32 s58, s58, 36864\n"
"v_exp_f32 v232, v232\n"
"v_mfma_f32_16x16x32_bf16 v[0:3], a[128:131], v[64:67], v[24:27]\n"
"s_add_u32 s23, s58, s57\n"
"ds_read_b128 a[160:163], v241 offset:40960\n"
"v_mfma_f32_16x16x32_bf16 v[0:3], a[132:135], v[68:71], v[0:3]\n"
"s_add_u32 s58, s41, 8\n"
"v_exp_f32 v233, v233\n"
"v_mfma_f32_16x16x32_bf16 v[0:3], a[136:139], v[72:75], v[0:3]\n"
"s_mul_i32 s59, s58, 43\n"
"ds_read_b128 a[164:167], v241 offset:41216\n"
"v_cvt_pk_bf16_f32 v22, v232, v233\n"
"v_mfma_f32_16x16x32_bf16 v[0:3], a[140:143], v[76:79], v[0:3]\n"
"s_lshr_b32 s59, s59, 7\n"
"v_exp_f32 v234, v234\n"
"v_mfma_f32_16x16x32_bf16 v[4:7], a[144:147], v[64:67], v[24:27]\n"
"s_mul_i32 s60, s59, 3\n"
"ds_read_b128 a[168:171], v241 offset:41472\n"
"v_mfma_f32_16x16x32_bf16 v[4:7], a[148:151], v[68:71], v[4:7]\n"
"s_sub_u32 s58, s58, s60\n"
"v_exp_f32 v235, v235\n"
"v_mfma_f32_16x16x32_bf16 v[4:7], a[152:155], v[72:75], v[4:7]\n"
"s_add_u32 s58, s58, s56\n"
"ds_read_b128 a[172:175], v241 offset:41728\n"
"v_cvt_pk_bf16_f32 v23, v234, v235\n"
"v_mfma_f32_16x16x32_bf16 v[4:7], a[156:159], v[76:79], v[4:7]\n"
"s_add_u32 m0, s86, 81920\n"
"v_exp_f32 v0, v0\n"
"v_mfma_f32_16x16x32_bf16 a[96:99], a[224:227], v[20:23], a[96:99]\n"
"s_add_u32 s58, s58, 1\n"
"buffer_load_dwordx4 v246, s[12:15], s84 offen offset:0 lds\n"
"v_mfma_f32_16x16x32_bf16 a[100:103], a[228:231], v[20:23], a[100:103]\n"
"s_and_b32 s58, s58, 3\n"
"v_exp_f32 v1, v1\n"
"v_mfma_f32_16x16x32_bf16 a[104:107], a[232:235], v[20:23], a[104:107]\n"
"s_lshl_b32 s58, s58, 3\n"
"ds_read_b128 a[176:179], v241 offset:45056\n"
"v_cvt_pk_bf16_f32 v16, v0, v1\n"
"v_mfma_f32_16x16x32_bf16 a[108:111], a[236:239], v[20:23], a[108:111]\n"
"s_add_u32 s58, s58, s59\n"
"v_exp_f32 v2, v2\n"
"v_mfma_f32_16x16x32_bf16 a[112:115], a[240:243], v[20:23], a[112:115]\n"
"s_mul_i32 s58, s58, 36864\n"
"ds_read_b128 a[180:183], v241 offset:45312\n"
"v_add_f32 v44, v44, v0\n"
"v_mfma_f32_16x16x32_bf16 a[116:119], a[244:247], v[20:23], a[116:119]\n"
"v_exp_f32 v3, v3\n"
"v_mfma_f32_16x16x32_bf16 a[120:123], a[248:251], v[20:23], a[120:123]\n"
"ds_read_b128 a[184:187], v241 offset:45568\n"
"v_cvt_pk_bf16_f32 v17, v2, v3\n"
"v_mfma_f32_16x16x32_bf16 a[124:127], a[252:255], v[20:23], a[124:127]\n"
"v_exp_f32 v4, v4\n"
"v_mfma_f32_16x16x32_bf16 v[56:59], v[40:43], v[20:23], v[56:59]\n"
"ds_read_b128 a[188:191], v241 offset:45824\n"
"v_add_f32 v44, v44, v1\n"
"v_mfma_f32_16x16x32_bf16 v[8:11], a[128:131], v[80:83], v[28:31]\n"
"v_exp_f32 v5, v5\n"
"v_mfma_f32_16x16x32_bf16 v[8:11], a[132:135], v[84:87], v[8:11]\n"
"ds_read_b64_tr_b16 a[224:225], v244 offset:57344\n"
"v_cvt_pk_bf16_f32 v18, v4, v5\n"
"v_mfma_f32_16x16x32_bf16 v[8:11], a[136:139], v[88:91], v[8:11]\n"
"v_exp_f32 v6, v6\n"
"v_mfma_f32_16x16x32_bf16 v[8:11], a[140:143], v[92:95], v[8:11]\n"
"ds_read_b64_tr_b16 a[226:227], v244 offset:61440\n"
"v_add_f32 v44, v44, v2\n"
"v_mfma_f32_16x16x32_bf16 v[12:15], a[144:147], v[80:83], v[28:31]\n"
"v_exp_f32 v7, v7\n"
"v_mfma_f32_16x16x32_bf16 v[12:15], a[148:151], v[84:87], v[12:15]\n"
"ds_read_b64_tr_b16 a[228:229], v245 offset:57344\n"
"buffer_load_dwordx4 v[172:175], v252, s[68:71], s49 offen\n"
"v_cvt_pk_bf16_f32 v19, v6, v7\n"
"v_mfma_f32_16x16x32_bf16 v[12:15], a[152:155], v[88:91], v[12:15]\n"
"s_add_u32 s36, s58, s57\n"
"buffer_load_dwordx4 v247, s[12:15], s84 offen offset:1024 lds\n"
"v_mfma_f32_16x16x32_bf16 v[12:15], a[156:159], v[92:95], v[12:15]\n"
"s_waitcnt lgkmcnt(11)\n"
"v_exp_f32 v8, v8\n"
"v_mfma_f32_16x16x32_bf16 a[0:3], a[192:195], v[16:19], a[0:3]\n"
"s_add_u32 s58, s41, 12\n"
"ds_read_b64_tr_b16 a[230:231], v245 offset:61440\n"
"v_add_f32 v44, v44, v3\n"
"v_mfma_f32_16x16x32_bf16 a[4:7], a[196:199], v[16:19], a[4:7]\n"
"s_mul_i32 s59, s58, 43\n"
"v_exp_f32 v9, v9\n"
"v_mfma_f32_16x16x32_bf16 a[8:11], a[200:203], v[16:19], a[8:11]\n"
"s_lshr_b32 s59, s59, 7\n"
"ds_read_b64_tr_b16 a[232:233], v244 offset:57600\n"
"v_cvt_pk_bf16_f32 v20, v8, v9\n"
"v_mfma_f32_16x16x32_bf16 a[12:15], a[204:207], v[16:19], a[12:15]\n"
"s_mul_i32 s60, s59, 3\n"
"v_exp_f32 v10, v10\n"
"v_mfma_f32_16x16x32_bf16 a[16:19], a[208:211], v[16:19], a[16:19]\n"
"s_sub_u32 s58, s58, s60\n"
"ds_read_b64_tr_b16 a[234:235], v244 offset:61696\n"
"v_add_f32 v44, v44, v4\n"
"v_mfma_f32_16x16x32_bf16 a[20:23], a[212:215], v[16:19], a[20:23]\n"
"s_add_u32 s58, s58, s56\n"
"v_exp_f32 v11, v11\n"
"v_mfma_f32_16x16x32_bf16 a[24:27], a[216:219], v[16:19], a[24:27]\n"
"s_add_u32 s58, s58, 1\n"
"ds_read_b64_tr_b16 a[236:237], v245 offset:57600\n"
"v_cvt_pk_bf16_f32 v21, v10, v11\n"
"v_mfma_f32_16x16x32_bf16 a[28:31], a[220:223], v[16:19], a[28:31]\n"
"s_and_b32 s58, s58, 3\n"
"v_exp_f32 v12, v12\n"
"v_mfma_f32_16x16x32_bf16 v[220:223], a[128:131], v[96:99], v[32:35]\n"
"s_lshl_b32 s58, s58, 3\n"
"ds_read_b64_tr_b16 a[238:239], v245 offset:61696\n"
"v_add_f32 v44, v44, v5\n"
"v_mfma_f32_16x16x32_bf16 v[220:223], a[132:135], v[100:103], v[220:223]\n"
"s_add_u32 s58, s58, s59\n"
"v_exp_f32 v13, v13\n"
"v_mfma_f32_16x16x32_bf16 v[220:223], a[136:139], v[104:107], v[220:223]\n"
"s_mul_i32 s58, s58, 36864\n"
"ds_read_b64_tr_b16 a[240:241], v244 offset:57856\n"
"v_cvt_pk_bf16_f32 v22, v12, v13\n"
"v_mfma_f32_16x16x32_bf16 v[220:223], a[140:143], v[108:111], v[220:223]\n"
"s_add_u32 s37, s58, s57\n"
"v_exp_f32 v14, v14\n"
"v_mfma_f32_16x16x32_bf16 v[224:227], a[144:147], v[96:99], v[32:35]\n"
"s_add_u32 s58, s41, 16\n"
"ds_read_b64_tr_b16 a[242:243], v244 offset:61952\n"
"v_add_f32 v44, v44, v6\n"
"v_mfma_f32_16x16x32_bf16 v[224:227], a[148:151], v[100:103], v[224:227]\n"
"s_mul_i32 s59, s58, 43\n"
"v_exp_f32 v15, v15\n"
"v_mfma_f32_16x16x32_bf16 v[224:227], a[152:155], v[104:107], v[224:227]\n"
"s_lshr_b32 s59, s59, 7\n"
"ds_read_b64_tr_b16 a[244:245], v245 offset:57856\n"
"v_cvt_pk_bf16_f32 v23, v14, v15\n"
"v_mfma_f32_16x16x32_bf16 v[224:227], a[156:159], v[108:111], v[224:227]\n"
"s_add_u32 s49, s49, 9216\n"
"v_exp_f32 v220, v220\n"
"v_mfma_f32_16x16x32_bf16 a[32:35], a[192:195], v[20:23], a[32:35]\n"
"buffer_load_dwordx4 v246, s[12:15], s84 offen offset:2048 lds\n"
"v_mfma_f32_16x16x32_bf16 a[36:39], a[196:199], v[20:23], a[36:39]\n"
"v_exp_f32 v221, v221\n"
"v_mfma_f32_16x16x32_bf16 a[40:43], a[200:203], v[20:23], a[40:43]\n"
"ds_read_b64_tr_b16 a[246:247], v245 offset:61952\n"
"v_cvt_pk_bf16_f32 v16, v220, v221\n"
"v_mfma_f32_16x16x32_bf16 a[44:47], a[204:207], v[20:23], a[44:47]\n"
"v_exp_f32 v222, v222\n"
"v_mfma_f32_16x16x32_bf16 a[48:51], a[208:211], v[20:23], a[48:51]\n"
"ds_read_b64_tr_b16 a[248:249], v244 offset:58112\n"
"v_add_f32 v44, v44, v7\n"
"v_mfma_f32_16x16x32_bf16 a[52:55], a[212:215], v[20:23], a[52:55]\n"
"v_exp_f32 v223, v223\n"
"v_mfma_f32_16x16x32_bf16 a[56:59], a[216:219], v[20:23], a[56:59]\n"
"ds_read_b64_tr_b16 a[250:251], v244 offset:62208\n"
"v_cvt_pk_bf16_f32 v17, v222, v223\n"
"v_mfma_f32_16x16x32_bf16 a[60:63], a[220:223], v[20:23], a[60:63]\n"
"v_exp_f32 v224, v224\n"
"v_mfma_f32_16x16x32_bf16 v[48:51], v[40:43], v[20:23], v[48:51]\n"
"ds_read_b64_tr_b16 a[252:253], v245 offset:58112\n"
"v_add_f32 v52, v52, v220\n"
"v_mfma_f32_16x16x32_bf16 v[228:231], a[128:131], v[112:115], v[36:39]\n"
"v_exp_f32 v225, v225\n"
"v_mfma_f32_16x16x32_bf16 v[228:231], a[132:135], v[116:119], v[228:231]\n"
"ds_read_b64_tr_b16 a[254:255], v245 offset:62208\n"
"buffer_load_dwordx4 v[140:143], v252, s[64:67], s49 offen\n"
"v_cvt_pk_bf16_f32 v18, v224, v225\n"
"v_mfma_f32_16x16x32_bf16 v[228:231], a[136:139], v[120:123], v[228:231]\n"
"s_mul_i32 s60, s59, 3\n"
"v_exp_f32 v226, v226\n"
"v_mfma_f32_16x16x32_bf16 v[228:231], a[140:143], v[124:127], v[228:231]\n"
"s_sub_u32 s58, s58, s60\n"
"v_add_f32 v52, v52, v221\n"
"v_add_f32 v52, v52, v222\n"
"v_mfma_f32_16x16x32_bf16 v[232:235], a[144:147], v[112:115], v[36:39]\n"
"s_add_u32 s58, s58, s56\n"
"v_exp_f32 v227, v227\n"
"v_mfma_f32_16x16x32_bf16 v[232:235], a[148:151], v[116:119], v[232:235]\n"
"s_add_u32 s58, s58, 1\n"
"v_cvt_pk_bf16_f32 v19, v226, v227\n"
"v_add_f32 v52, v52, v223\n"
"v_mfma_f32_16x16x32_bf16 v[232:235], a[152:155], v[120:123], v[232:235]\n"
"s_and_b32 s58, s58, 3\n"
"buffer_load_dwordx4 v247, s[12:15], s84 offen offset:3072 lds\n"
"v_mfma_f32_16x16x32_bf16 v[232:235], a[156:159], v[124:127], v[232:235]\n"
"s_add_u32 s84, s84, 16384\n"
"v_exp_f32 v228, v228\n"
"v_mfma_f32_16x16x32_bf16 a[64:67], a[192:195], v[16:19], a[64:67]\n"
"s_lshl_b32 s58, s58, 3\n"
"v_add_f32 v52, v52, v224\n"
"v_add_f32 v52, v52, v225\n"
"v_mfma_f32_16x16x32_bf16 a[68:71], a[196:199], v[16:19], a[68:71]\n"
"s_add_u32 s58, s58, s59\n"
"v_exp_f32 v229, v229\n"
"v_mfma_f32_16x16x32_bf16 a[72:75], a[200:203], v[16:19], a[72:75]\n"
"s_mul_i32 s58, s58, 36864\n"
"v_cvt_pk_bf16_f32 v20, v228, v229\n"
"v_add_f32 v52, v52, v226\n"
"v_mfma_f32_16x16x32_bf16 a[76:79], a[204:207], v[16:19], a[76:79]\n"
"s_waitcnt vmcnt(22) lgkmcnt(0)\n"
"v_exp_f32 v230, v230\n"
"v_mfma_f32_16x16x32_bf16 a[80:83], a[208:211], v[16:19], a[80:83]\n"
"s_barrier\n"
"v_mfma_f32_16x16x32_bf16 a[84:87], a[212:215], v[16:19], a[84:87]\n"
"s_add_u32 s38, s58, s57\n"
"v_exp_f32 v231, v231\n"
"v_mfma_f32_16x16x32_bf16 a[88:91], a[216:219], v[16:19], a[88:91]\n"
"s_add_u32 s58, s41, 20\n"
"v_cvt_pk_bf16_f32 v21, v230, v231\n"
"v_add_f32 v52, v52, v227\n"
"v_mfma_f32_16x16x32_bf16 a[92:95], a[220:223], v[16:19], a[92:95]\n"
"s_mul_i32 s59, s58, 43\n"
"v_exp_f32 v232, v232\n"
"v_mfma_f32_16x16x32_bf16 v[0:3], a[160:163], v[64:67], v[24:27]\n"
"s_lshr_b32 s59, s59, 7\n"
"ds_read_b128 a[128:131], v240 offset:0\n"
"v_mfma_f32_16x16x32_bf16 v[0:3], a[164:167], v[68:71], v[0:3]\n"
"s_mul_i32 s60, s59, 3\n"
"v_exp_f32 v233, v233\n"
"v_mfma_f32_16x16x32_bf16 v[0:3], a[168:171], v[72:75], v[0:3]\n"
"s_sub_u32 s58, s58, s60\n"
"ds_read_b128 a[132:135], v240 offset:256\n"
"v_cvt_pk_bf16_f32 v22, v232, v233\n"
"v_mfma_f32_16x16x32_bf16 v[0:3], a[172:175], v[76:79], v[0:3]\n"
"s_add_u32 s58, s58, s56\n"
"v_exp_f32 v234, v234\n"
"v_mfma_f32_16x16x32_bf16 v[4:7], a[176:179], v[64:67], v[24:27]\n"
"s_add_u32 s58, s58, 1\n"
"ds_read_b128 a[136:139], v240 offset:512\n"
"v_mfma_f32_16x16x32_bf16 v[4:7], a[180:183], v[68:71], v[4:7]\n"
"v_exp_f32 v235, v235\n"
"v_mfma_f32_16x16x32_bf16 v[4:7], a[184:187], v[72:75], v[4:7]\n"
"ds_read_b128 a[140:143], v240 offset:768\n"
"v_cvt_pk_bf16_f32 v23, v234, v235\n"
"v_mfma_f32_16x16x32_bf16 v[4:7], a[188:191], v[76:79], v[4:7]\n"
"s_add_u32 m0, s86, 98304\n"
"v_exp_f32 v0, v0\n"
"v_mfma_f32_16x16x32_bf16 a[96:99], a[192:195], v[20:23], a[96:99]\n"
"buffer_load_dwordx4 v246, s[8:11], s84 offen offset:0 lds\n"
"v_mfma_f32_16x16x32_bf16 a[100:103], a[196:199], v[20:23], a[100:103]\n"
"v_exp_f32 v1, v1\n"
"v_mfma_f32_16x16x32_bf16 a[104:107], a[200:203], v[20:23], a[104:107]\n"
"ds_read_b128 a[144:147], v240 offset:4096\n"
"v_cvt_pk_bf16_f32 v16, v0, v1\n"
"v_mfma_f32_16x16x32_bf16 a[108:111], a[204:207], v[20:23], a[108:111]\n"
"v_exp_f32 v2, v2\n"
"v_mfma_f32_16x16x32_bf16 a[112:115], a[208:211], v[20:23], a[112:115]\n"
"ds_read_b128 a[148:151], v240 offset:4352\n"
"v_add_f32 v44, v44, v0\n"
"v_mfma_f32_16x16x32_bf16 a[116:119], a[212:215], v[20:23], a[116:119]\n"
"v_exp_f32 v3, v3\n"
"v_mfma_f32_16x16x32_bf16 a[120:123], a[216:219], v[20:23], a[120:123]\n"
"ds_read_b128 a[152:155], v240 offset:4608\n"
"buffer_load_dwordx4 v[176:179], v252, s[68:71], s49 offen\n"
"v_cvt_pk_bf16_f32 v17, v2, v3\n"
"v_mfma_f32_16x16x32_bf16 a[124:127], a[220:223], v[20:23], a[124:127]\n"
"s_and_b32 s58, s58, 3\n"
"v_exp_f32 v4, v4\n"
"v_mfma_f32_16x16x32_bf16 v[56:59], v[40:43], v[20:23], v[56:59]\n"
"s_lshl_b32 s58, s58, 3\n"
"ds_read_b128 a[156:159], v240 offset:4864\n"
"v_add_f32 v44, v44, v1\n"
"v_mfma_f32_16x16x32_bf16 v[8:11], a[160:163], v[80:83], v[28:31]\n"
"s_add_u32 s58, s58, s59\n"
"v_exp_f32 v5, v5\n"
"v_mfma_f32_16x16x32_bf16 v[8:11], a[164:167], v[84:87], v[8:11]\n"
"s_mul_i32 s58, s58, 36864\n"
"ds_read_b64_tr_b16 a[192:193], v242 offset:16384\n"
"v_cvt_pk_bf16_f32 v18, v4, v5\n"
"v_mfma_f32_16x16x32_bf16 v[8:11], a[168:171], v[88:91], v[8:11]\n"
"s_add_u32 s39, s58, s57\n"
"v_exp_f32 v6, v6\n"
"v_mfma_f32_16x16x32_bf16 v[8:11], a[172:175], v[92:95], v[8:11]\n"
"s_mul_i32 s58, s41, 9216\n"
"ds_read_b64_tr_b16 a[194:195], v242 offset:20480\n"
"v_add_f32 v44, v44, v2\n"
"v_mfma_f32_16x16x32_bf16 v[12:15], a[176:179], v[80:83], v[28:31]\n"
"s_add_u32 s58, s58, s40\n"
"v_exp_f32 v7, v7\n"
"v_mfma_f32_16x16x32_bf16 v[12:15], a[180:183], v[84:87], v[12:15]\n"
"s_sub_u32 s53, s58, 27648\n"
"ds_read_b64_tr_b16 a[196:197], v243 offset:16384\n"
"v_cvt_pk_bf16_f32 v19, v6, v7\n"
"v_mfma_f32_16x16x32_bf16 v[12:15], a[184:187], v[88:91], v[12:15]\n"
"s_add_u32 s58, s58, 131072\n"
"buffer_load_dwordx4 v246, s[8:11], s84 offen offset:1024 lds\n"
"v_mfma_f32_16x16x32_bf16 v[12:15], a[188:191], v[92:95], v[12:15]\n"
"s_waitcnt lgkmcnt(11)\n"
"v_exp_f32 v8, v8\n"
"v_mfma_f32_16x16x32_bf16 a[0:3], a[224:227], v[16:19], a[0:3]\n"
"s_cmp_lt_u32 s41, 3\n"
"s_cselect_b32 s51, s58, s40\n"
"ds_read_b64_tr_b16 a[198:199], v243 offset:20480\n"
"v_add_f32 v44, v44, v3\n"
"v_mfma_f32_16x16x32_bf16 a[4:7], a[228:231], v[16:19], a[4:7]\n"
"s_nop 0\n"
"v_exp_f32 v9, v9\n"
"v_mfma_f32_16x16x32_bf16 a[8:11], a[232:235], v[16:19], a[8:11]\n"
"s_and_b32 s58, s56, 1\n"
"ds_read_b64_tr_b16 a[200:201], v242 offset:16640\n"
"v_cvt_pk_bf16_f32 v20, v8, v9\n"
"v_mfma_f32_16x16x32_bf16 a[12:15], a[236:239], v[16:19], a[12:15]\n"
"s_lshl_b32 s59, s58, 5\n"
"v_exp_f32 v10, v10\n"
"v_mfma_f32_16x16x32_bf16 a[16:19], a[240:243], v[16:19], a[16:19]\n"
"s_add_u32 s59, s59, s40\n"
"ds_read_b64_tr_b16 a[202:203], v242 offset:20736\n"
"v_add_f32 v44, v44, v4\n"
"v_mfma_f32_16x16x32_bf16 a[20:23], a[244:247], v[16:19], a[20:23]\n"
"s_xor_b32 s60, s58, 1\n"
"v_exp_f32 v11, v11\n"
"v_mfma_f32_16x16x32_bf16 a[24:27], a[248:251], v[16:19], a[24:27]\n"
"s_lshl_b32 s60, s60, 8\n"
"ds_read_b64_tr_b16 a[204:205], v243 offset:16640\n"
"v_cvt_pk_bf16_f32 v21, v10, v11\n"
"v_mfma_f32_16x16x32_bf16 a[28:31], a[252:255], v[16:19], a[28:31]\n"
"s_add_u32 s49, s49, 285696\n"
"v_exp_f32 v12, v12\n"
"v_mfma_f32_16x16x32_bf16 v[220:223], a[160:163], v[96:99], v[32:35]\n"
"ds_read_b64_tr_b16 a[206:207], v243 offset:20736\n"
"v_add_f32 v44, v44, v5\n"
"v_mfma_f32_16x16x32_bf16 v[220:223], a[164:167], v[100:103], v[220:223]\n"
"v_exp_f32 v13, v13\n"
"v_mfma_f32_16x16x32_bf16 v[220:223], a[168:171], v[104:107], v[220:223]\n"
"ds_read_b64_tr_b16 a[208:209], v242 offset:16896\n"
"v_cvt_pk_bf16_f32 v22, v12, v13\n"
"v_mfma_f32_16x16x32_bf16 v[220:223], a[172:175], v[108:111], v[220:223]\n"
"v_exp_f32 v14, v14\n"
"v_mfma_f32_16x16x32_bf16 v[224:227], a[176:179], v[96:99], v[32:35]\n"
"ds_read_b64_tr_b16 a[210:211], v242 offset:20992\n"
"v_add_f32 v44, v44, v6\n"
"v_mfma_f32_16x16x32_bf16 v[224:227], a[180:183], v[100:103], v[224:227]\n"
"v_exp_f32 v15, v15\n"
"v_mfma_f32_16x16x32_bf16 v[224:227], a[184:187], v[104:107], v[224:227]\n"
"ds_read_b64_tr_b16 a[212:213], v243 offset:16896\n"
"v_cvt_pk_bf16_f32 v23, v14, v15\n"
"v_mfma_f32_16x16x32_bf16 v[224:227], a[188:191], v[108:111], v[224:227]\n"
"v_exp_f32 v220, v220\n"
"v_mfma_f32_16x16x32_bf16 a[32:35], a[224:227], v[20:23], a[32:35]\n"
"buffer_load_dwordx4 v247, s[8:11], s84 offen offset:2048 lds\n"
"v_mfma_f32_16x16x32_bf16 a[36:39], a[228:231], v[20:23], a[36:39]\n"
"v_exp_f32 v221, v221\n"
"v_mfma_f32_16x16x32_bf16 a[40:43], a[232:235], v[20:23], a[40:43]\n"
"ds_read_b64_tr_b16 a[214:215], v243 offset:20992\n"
"buffer_load_dwordx4 v[144:147], v252, s[64:67], s49 offen\n"
"v_cvt_pk_bf16_f32 v16, v220, v221\n"
"v_mfma_f32_16x16x32_bf16 a[44:47], a[236:239], v[20:23], a[44:47]\n"
"s_add_u32 s60, s60, s40\n"
"v_exp_f32 v222, v222\n"
"v_mfma_f32_16x16x32_bf16 a[48:51], a[240:243], v[20:23], a[48:51]\n"
"ds_read_b64_tr_b16 a[216:217], v242 offset:17152\n"
"v_add_f32 v44, v44, v7\n"
"v_mfma_f32_16x16x32_bf16 a[52:55], a[244:247], v[20:23], a[52:55]\n"
"v_exp_f32 v223, v223\n"
"v_mfma_f32_16x16x32_bf16 a[56:59], a[248:251], v[20:23], a[56:59]\n"
"ds_read_b64_tr_b16 a[218:219], v242 offset:21248\n"
"v_cvt_pk_bf16_f32 v17, v222, v223\n"
"v_mfma_f32_16x16x32_bf16 a[60:63], a[252:255], v[20:23], a[60:63]\n"
"v_exp_f32 v224, v224\n"
"v_mfma_f32_16x16x32_bf16 v[48:51], v[40:43], v[20:23], v[48:51]\n"
"ds_read_b64_tr_b16 a[220:221], v243 offset:17152\n"
"v_add_f32 v52, v52, v220\n"
"v_mfma_f32_16x16x32_bf16 v[228:231], a[160:163], v[112:115], v[36:39]\n"
"v_exp_f32 v225, v225\n"
"v_mfma_f32_16x16x32_bf16 v[228:231], a[164:167], v[116:119], v[228:231]\n"
"ds_read_b64_tr_b16 a[222:223], v243 offset:21248\n"
"v_cvt_pk_bf16_f32 v18, v224, v225\n"
"v_lshrrev_b32 v217, 2, v248\n"
"v_mfma_f32_16x16x32_bf16 v[228:231], a[168:171], v[120:123], v[228:231]\n"
"v_exp_f32 v226, v226\n"
"v_mfma_f32_16x16x32_bf16 v[228:231], a[172:175], v[124:127], v[228:231]\n"
"v_add_f32 v52, v52, v221\n"
"v_add_f32 v52, v52, v222\n"
"v_mfma_f32_16x16x32_bf16 v[232:235], a[176:179], v[112:115], v[36:39]\n"
"v_exp_f32 v227, v227\n"
"v_mfma_f32_16x16x32_bf16 v[232:235], a[180:183], v[116:119], v[232:235]\n"
"v_cvt_pk_bf16_f32 v19, v226, v227\n"
"v_add_f32 v52, v52, v223\n"
"v_mfma_f32_16x16x32_bf16 v[232:235], a[184:187], v[120:123], v[232:235]\n"
"buffer_load_dwordx4 v247, s[8:11], s84 offen offset:3072 lds\n"
"v_mul_u32_u24 v203, 288, v217\n"
"v_mfma_f32_16x16x32_bf16 v[232:235], a[188:191], v[124:127], v[232:235]\n"
"v_exp_f32 v228, v228\n"
"v_mfma_f32_16x16x32_bf16 a[64:67], a[224:227], v[16:19], a[64:67]\n"
"v_add_f32 v52, v52, v224\n"
"v_add_f32 v52, v52, v225\n"
"v_mfma_f32_16x16x32_bf16 a[68:71], a[228:231], v[16:19], a[68:71]\n"
"v_exp_f32 v229, v229\n"
"v_mfma_f32_16x16x32_bf16 a[72:75], a[232:235], v[16:19], a[72:75]\n"
"v_cvt_pk_bf16_f32 v20, v228, v229\n"
"v_add_f32 v52, v52, v226\n"
"v_mfma_f32_16x16x32_bf16 a[76:79], a[236:239], v[16:19], a[76:79]\n"
"v_exp_f32 v230, v230\n"
"v_mfma_f32_16x16x32_bf16 a[80:83], a[240:243], v[16:19], a[80:83]\n"
"s_waitcnt lgkmcnt(8)\n"
"v_and_b32 v217, 3, v248\n"
"v_mfma_f32_16x16x32_bf16 a[84:87], a[244:247], v[16:19], a[84:87]\n"
"v_exp_f32 v231, v231\n"
"v_mfma_f32_16x16x32_bf16 a[88:91], a[248:251], v[16:19], a[88:91]\n"
"v_cvt_pk_bf16_f32 v21, v230, v231\n"
"v_add_f32 v52, v52, v227\n"
"v_mfma_f32_16x16x32_bf16 a[92:95], a[252:255], v[16:19], a[92:95]\n"
"v_exp_f32 v232, v232\n"
"v_mfma_f32_16x16x32_bf16 v[0:3], a[128:131], v[64:67], v[24:27]\n"
"ds_read_b128 a[160:163], v240 offset:8192\n"
"v_lshl_add_u32 v203, v217, 3, v203\n"
"v_mfma_f32_16x16x32_bf16 v[0:3], a[132:135], v[68:71], v[0:3]\n"
"v_exp_f32 v233, v233\n"
"v_mfma_f32_16x16x32_bf16 v[0:3], a[136:139], v[72:75], v[0:3]\n"
"ds_read_b128 a[164:167], v240 offset:8448\n"
"v_cvt_pk_bf16_f32 v22, v232, v233\n"
"v_mfma_f32_16x16x32_bf16 v[0:3], a[140:143], v[76:79], v[0:3]\n"
"v_exp_f32 v234, v234\n"
"v_mfma_f32_16x16x32_bf16 v[4:7], a[144:147], v[64:67], v[24:27]\n"
"ds_read_b128 a[168:171], v240 offset:8704\n"
"v_add_u32 v200, s59, v203\n"
"v_mfma_f32_16x16x32_bf16 v[4:7], a[148:151], v[68:71], v[4:7]\n"
"v_exp_f32 v235, v235\n"
"v_mfma_f32_16x16x32_bf16 v[4:7], a[152:155], v[72:75], v[4:7]\n"
"ds_read_b128 a[172:175], v240 offset:8960\n"
"v_cvt_pk_bf16_f32 v23, v234, v235\n"
"v_mfma_f32_16x16x32_bf16 v[4:7], a[156:159], v[76:79], v[4:7]\n"
"s_add_u32 m0, s86, 114688\n"
"v_exp_f32 v0, v0\n"
"v_mfma_f32_16x16x32_bf16 a[96:99], a[224:227], v[20:23], a[96:99]\n"
"buffer_load_dwordx4 v246, s[12:15], s84 offen offset:0 lds\n"
"v_add_u32 v203, s60, v203\n"
"v_mfma_f32_16x16x32_bf16 a[100:103], a[228:231], v[20:23], a[100:103]\n"
"v_exp_f32 v1, v1\n"
"v_mfma_f32_16x16x32_bf16 a[104:107], a[232:235], v[20:23], a[104:107]\n"
"ds_read_b128 a[176:179], v240 offset:12288\n"
"v_cvt_pk_bf16_f32 v16, v0, v1\n"
"v_mfma_f32_16x16x32_bf16 a[108:111], a[236:239], v[20:23], a[108:111]\n"
"v_exp_f32 v2, v2\n"
"v_mfma_f32_16x16x32_bf16 a[112:115], a[240:243], v[20:23], a[112:115]\n"
"ds_read_b128 a[180:183], v240 offset:12544\n"
"v_add_f32 v44, v44, v0\n"
"v_mfma_f32_16x16x32_bf16 a[116:119], a[244:247], v[20:23], a[116:119]\n"
"v_exp_f32 v3, v3\n"
"v_mfma_f32_16x16x32_bf16 a[120:123], a[248:251], v[20:23], a[120:123]\n"
"ds_read_b128 a[184:187], v240 offset:12800\n"
"v_cvt_pk_bf16_f32 v17, v2, v3\n"
"v_mfma_f32_16x16x32_bf16 a[124:127], a[252:255], v[20:23], a[124:127]\n"
"v_exp_f32 v4, v4\n"
"v_add_u32 v201, 65536, v200\n"
"v_mfma_f32_16x16x32_bf16 v[56:59], v[40:43], v[20:23], v[56:59]\n"
"ds_read_b128 a[188:191], v240 offset:13056\n"
"v_add_f32 v44, v44, v1\n"
"v_mfma_f32_16x16x32_bf16 v[8:11], a[128:131], v[80:83], v[28:31]\n"
"v_exp_f32 v5, v5\n"
"v_mfma_f32_16x16x32_bf16 v[8:11], a[132:135], v[84:87], v[8:11]\n"
"ds_read_b64_tr_b16 a[224:225], v242 offset:24576\n"
"v_cvt_pk_bf16_f32 v18, v4, v5\n"
"v_mfma_f32_16x16x32_bf16 v[8:11], a[136:139], v[88:91], v[8:11]\n"
"v_exp_f32 v6, v6\n"
"v_mfma_f32_16x16x32_bf16 v[8:11], a[140:143], v[92:95], v[8:11]\n"
"ds_read_b64_tr_b16 a[226:227], v242 offset:28672\n"
"v_add_f32 v44, v44, v2\n"
"v_mfma_f32_16x16x32_bf16 v[12:15], a[144:147], v[80:83], v[28:31]\n"
"v_exp_f32 v7, v7\n"
"v_mfma_f32_16x16x32_bf16 v[12:15], a[148:151], v[84:87], v[12:15]\n"
"ds_read_b64_tr_b16 a[228:229], v243 offset:24576\n"
"v_cvt_pk_bf16_f32 v19, v6, v7\n"
"v_add_u32 v204, 65536, v203\n"
"v_mfma_f32_16x16x32_bf16 v[12:15], a[152:155], v[88:91], v[12:15]\n"
"buffer_load_dwordx4 v247, s[12:15], s84 offen offset:1024 lds\n"
"v_add_u32 v202, 131072, v200\n"
"v_mfma_f32_16x16x32_bf16 v[12:15], a[156:159], v[92:95], v[12:15]\n"
"s_waitcnt lgkmcnt(11)\n"
"v_exp_f32 v8, v8\n"
"v_mfma_f32_16x16x32_bf16 a[0:3], a[192:195], v[16:19], a[0:3]\n"
"ds_read_b64_tr_b16 a[230:231], v243 offset:28672\n"
"v_add_f32 v44, v44, v3\n"
"v_mfma_f32_16x16x32_bf16 a[4:7], a[196:199], v[16:19], a[4:7]\n"
"v_exp_f32 v9, v9\n"
"v_mfma_f32_16x16x32_bf16 a[8:11], a[200:203], v[16:19], a[8:11]\n"
"ds_read_b64_tr_b16 a[232:233], v242 offset:24832\n"
"v_cvt_pk_bf16_f32 v20, v8, v9\n"
"v_mfma_f32_16x16x32_bf16 a[12:15], a[204:207], v[16:19], a[12:15]\n"
"v_exp_f32 v10, v10\n"
"v_mfma_f32_16x16x32_bf16 a[16:19], a[208:211], v[16:19], a[16:19]\n"
"ds_read_b64_tr_b16 a[234:235], v242 offset:28928\n"
"v_add_f32 v44, v44, v4\n"
"v_mfma_f32_16x16x32_bf16 a[20:23], a[212:215], v[16:19], a[20:23]\n"
"v_exp_f32 v11, v11\n"
"v_add_u32 v205, 131072, v203\n"
"v_mfma_f32_16x16x32_bf16 a[24:27], a[216:219], v[16:19], a[24:27]\n"
"s_add_u32 s62, s40, 163776\n"
"ds_read_b64_tr_b16 a[236:237], v243 offset:24832\n"
"v_cvt_pk_bf16_f32 v21, v10, v11\n"
"v_mfma_f32_16x16x32_bf16 a[28:31], a[220:223], v[16:19], a[28:31]\n"
"v_exp_f32 v12, v12\n"
"v_mfma_f32_16x16x32_bf16 v[220:223], a[128:131], v[96:99], v[32:35]\n"
"ds_read_b64_tr_b16 a[238:239], v243 offset:28928\n"
"v_add_f32 v44, v44, v5\n"
"v_mfma_f32_16x16x32_bf16 v[220:223], a[132:135], v[100:103], v[220:223]\n"
"v_exp_f32 v13, v13\n"
"v_mfma_f32_16x16x32_bf16 v[220:223], a[136:139], v[104:107], v[220:223]\n"
"ds_read_b64_tr_b16 a[240:241], v242 offset:25088\n"
"v_cvt_pk_bf16_f32 v22, v12, v13\n"
"v_mfma_f32_16x16x32_bf16 v[220:223], a[140:143], v[108:111], v[220:223]\n"
"v_exp_f32 v14, v14\n"
"v_mfma_f32_16x16x32_bf16 v[224:227], a[144:147], v[96:99], v[32:35]\n"
"ds_read_b64_tr_b16 a[242:243], v242 offset:29184\n"
"v_add_f32 v44, v44, v6\n"
"v_mfma_f32_16x16x32_bf16 v[224:227], a[148:151], v[100:103], v[224:227]\n"
"v_exp_f32 v15, v15\n"
"v_lshl_add_u32 v209, v217, 2, s62\n"
"v_mfma_f32_16x16x32_bf16 v[224:227], a[152:155], v[104:107], v[224:227]\n"
"s_lshl_b32 s55, s44, 4\n"
"ds_read_b64_tr_b16 a[244:245], v243 offset:25088\n"
"v_cvt_pk_bf16_f32 v23, v14, v15\n"
"v_mfma_f32_16x16x32_bf16 v[224:227], a[156:159], v[108:111], v[224:227]\n"
"s_add_u32 s55, s55, s43\n"
"v_exp_f32 v220, v220\n"
"v_mfma_f32_16x16x32_bf16 a[32:35], a[192:195], v[20:23], a[32:35]\n"
"s_lshl_b32 s52, s55, 2\n"
"buffer_load_dwordx4 v246, s[12:15], s84 offen offset:2048 lds\n"
"v_mfma_f32_16x16x32_bf16 a[36:39], a[196:199], v[20:23], a[36:39]\n"
"v_exp_f32 v221, v221\n"
"v_mfma_f32_16x16x32_bf16 a[40:43], a[200:203], v[20:23], a[40:43]\n"
"ds_read_b64_tr_b16 a[246:247], v243 offset:29184\n"
"buffer_load_dwordx4 v[180:183], v252, s[68:71], s49 offen\n"
"v_cvt_pk_bf16_f32 v16, v220, v221\n"
"v_mfma_f32_16x16x32_bf16 a[44:47], a[204:207], v[20:23], a[44:47]\n"
"s_add_u32 s52, s52, s41\n"
"v_exp_f32 v222, v222\n"
"v_mfma_f32_16x16x32_bf16 a[48:51], a[208:211], v[20:23], a[48:51]\n"
"s_lshl_b32 s52, s52, 14\n"
"ds_read_b64_tr_b16 a[248:249], v242 offset:25344\n"
"v_add_f32 v44, v44, v7\n"
"v_mfma_f32_16x16x32_bf16 a[52:55], a[212:215], v[20:23], a[52:55]\n"
"s_add_u32 s52, s52, 9592832\n"
"v_exp_f32 v223, v223\n"
"v_mfma_f32_16x16x32_bf16 a[56:59], a[216:219], v[20:23], a[56:59]\n"
"s_add_u32 s49, s49, 9216\n"
"ds_read_b64_tr_b16 a[250:251], v242 offset:29440\n"
"v_cvt_pk_bf16_f32 v17, v222, v223\n"
"v_mfma_f32_16x16x32_bf16 a[60:63], a[220:223], v[20:23], a[60:63]\n"
"v_exp_f32 v224, v224\n"
"v_mfma_f32_16x16x32_bf16 v[48:51], v[40:43], v[20:23], v[48:51]\n"
"ds_read_b64_tr_b16 a[252:253], v243 offset:25344\n"
"v_add_f32 v52, v52, v220\n"
"v_mfma_f32_16x16x32_bf16 v[228:231], a[128:131], v[112:115], v[36:39]\n"
"v_exp_f32 v225, v225\n"
"v_mfma_f32_16x16x32_bf16 v[228:231], a[132:135], v[116:119], v[228:231]\n"
"ds_read_b64_tr_b16 a[254:255], v243 offset:29440\n"
"v_cvt_pk_bf16_f32 v18, v224, v225\n"
"v_mfma_f32_16x16x32_bf16 v[228:231], a[136:139], v[120:123], v[228:231]\n"
"v_exp_f32 v226, v226\n"
"v_mfma_f32_16x16x32_bf16 v[228:231], a[140:143], v[124:127], v[228:231]\n"
"v_add_f32 v52, v52, v221\n"
"v_add_f32 v52, v52, v222\n"
"v_mfma_f32_16x16x32_bf16 v[232:235], a[144:147], v[112:115], v[36:39]\n"
"v_exp_f32 v227, v227\n"
"v_mfma_f32_16x16x32_bf16 v[232:235], a[148:151], v[116:119], v[232:235]\n"
"v_cvt_pk_bf16_f32 v19, v226, v227\n"
"v_add_f32 v52, v52, v223\n"
"v_mfma_f32_16x16x32_bf16 v[232:235], a[152:155], v[120:123], v[232:235]\n"
"buffer_load_dwordx4 v247, s[12:15], s84 offen offset:3072 lds\n"
"v_mfma_f32_16x16x32_bf16 v[232:235], a[156:159], v[124:127], v[232:235]\n"
"s_add_u32 s84, s84, 16384\n"
"v_exp_f32 v228, v228\n"
"v_mfma_f32_16x16x32_bf16 a[64:67], a[192:195], v[16:19], a[64:67]\n"
"v_add_f32 v52, v52, v224\n"
"v_add_f32 v52, v52, v225\n"
"v_mfma_f32_16x16x32_bf16 a[68:71], a[196:199], v[16:19], a[68:71]\n"
"v_exp_f32 v229, v229\n"
"v_mfma_f32_16x16x32_bf16 a[72:75], a[200:203], v[16:19], a[72:75]\n"
"v_cvt_pk_bf16_f32 v20, v228, v229\n"
"v_add_f32 v52, v52, v226\n"
"v_mfma_f32_16x16x32_bf16 a[76:79], a[204:207], v[16:19], a[76:79]\n"
"s_waitcnt vmcnt(24) lgkmcnt(0)\n"
"v_exp_f32 v230, v230\n"
"v_mfma_f32_16x16x32_bf16 a[80:83], a[208:211], v[16:19], a[80:83]\n"
"s_barrier\n"
"v_mfma_f32_16x16x32_bf16 a[84:87], a[212:215], v[16:19], a[84:87]\n"
"v_exp_f32 v231, v231\n"
"v_mfma_f32_16x16x32_bf16 a[88:91], a[216:219], v[16:19], a[88:91]\n"
"v_cvt_pk_bf16_f32 v21, v230, v231\n"
"v_add_f32 v52, v52, v227\n"
"v_mfma_f32_16x16x32_bf16 a[92:95], a[220:223], v[16:19], a[92:95]\n"
"v_exp_f32 v232, v232\n"
"v_mfma_f32_16x16x32_bf16 v[0:3], a[160:163], v[64:67], v[24:27]\n"
"AT_pg11:\n"
"s_cbranch_execz AT_hr\n"
"ds_read_b128 a[128:131], v240 offset:32768\n"
"v_mfma_f32_16x16x32_bf16 v[0:3], a[164:167], v[68:71], v[0:3]\n"
"v_exp_f32 v233, v233\n"
"v_mfma_f32_16x16x32_bf16 v[0:3], a[168:171], v[72:75], v[0:3]\n"
"ds_read_b128 a[132:135], v240 offset:33024\n"
"v_cvt_pk_bf16_f32 v22, v232, v233\n"
"v_mfma_f32_16x16x32_bf16 v[0:3], a[172:175], v[76:79], v[0:3]\n"
"v_exp_f32 v234, v234\n"
"v_mfma_f32_16x16x32_bf16 v[4:7], a[176:179], v[64:67], v[24:27]\n"
"ds_read_b128 a[136:139], v240 offset:33280\n"
"v_mfma_f32_16x16x32_bf16 v[4:7], a[180:183], v[68:71], v[4:7]\n"
"v_exp_f32 v235, v235\n"
"v_mfma_f32_16x16x32_bf16 v[4:7], a[184:187], v[72:75], v[4:7]\n"
"ds_read_b128 a[140:143], v240 offset:33536\n"
"buffer_load_dwordx4 v[148:151], v252, s[64:67], s49 offen\n"
"v_cvt_pk_bf16_f32 v23, v234, v235\n"
"v_mfma_f32_16x16x32_bf16 v[4:7], a[188:191], v[76:79], v[4:7]\n"
"s_mov_b32 m0, s51\n"
"v_exp_f32 v0, v0\n"
"v_mfma_f32_16x16x32_bf16 a[96:99], a[192:195], v[20:23], a[96:99]\n"
"buffer_load_dwordx4 v210, s[24:27], s20 offen offset:0 lds\n"
"v_mfma_f32_16x16x32_bf16 a[100:103], a[196:199], v[20:23], a[100:103]\n"
"v_exp_f32 v1, v1\n"
"v_mfma_f32_16x16x32_bf16 a[104:107], a[200:203], v[20:23], a[104:107]\n"
"ds_read_b128 a[144:147], v240 offset:36864\n"
"v_cvt_pk_bf16_f32 v16, v0, v1\n"
"v_mfma_f32_16x16x32_bf16 a[108:111], a[204:207], v[20:23], a[108:111]\n"
"v_exp_f32 v2, v2\n"
"v_mfma_f32_16x16x32_bf16 a[112:115], a[208:211], v[20:23], a[112:115]\n"
"ds_read_b128 a[148:151], v240 offset:37120\n"
"v_add_f32 v44, v44, v0\n"
"v_mfma_f32_16x16x32_bf16 a[116:119], a[212:215], v[20:23], a[116:119]\n"
"v_exp_f32 v3, v3\n"
"v_mfma_f32_16x16x32_bf16 a[120:123], a[216:219], v[20:23], a[120:123]\n"
"ds_read_b128 a[152:155], v240 offset:37376\n"
"v_cvt_pk_bf16_f32 v17, v2, v3\n"
"v_mfma_f32_16x16x32_bf16 a[124:127], a[220:223], v[20:23], a[124:127]\n"
"v_exp_f32 v4, v4\n"
"v_mfma_f32_16x16x32_bf16 v[56:59], v[40:43], v[20:23], v[56:59]\n"
"ds_read_b128 a[156:159], v240 offset:37632\n"
"v_add_f32 v44, v44, v1\n"
"v_mfma_f32_16x16x32_bf16 v[8:11], a[160:163], v[80:83], v[28:31]\n"
"v_exp_f32 v5, v5\n"
"v_mfma_f32_16x16x32_bf16 v[8:11], a[164:167], v[84:87], v[8:11]\n"
"ds_read_b64_tr_b16 a[192:193], v242 offset:49152\n"
"v_cvt_pk_bf16_f32 v18, v4, v5\n"
"v_mfma_f32_16x16x32_bf16 v[8:11], a[168:171], v[88:91], v[8:11]\n"
"v_exp_f32 v6, v6\n"
"v_mfma_f32_16x16x32_bf16 v[8:11], a[172:175], v[92:95], v[8:11]\n"
"ds_read_b64_tr_b16 a[194:195], v242 offset:53248\n"
"v_add_f32 v44, v44, v2\n"
"v_mfma_f32_16x16x32_bf16 v[12:15], a[176:179], v[80:83], v[28:31]\n"
"v_exp_f32 v7, v7\n"
"v_mfma_f32_16x16x32_bf16 v[12:15], a[180:183], v[84:87], v[12:15]\n"
"s_mov_b32 m0, s51\n"
"ds_read_b64_tr_b16 a[196:197], v243 offset:49152\n"
"v_cvt_pk_bf16_f32 v19, v6, v7\n"
"v_mfma_f32_16x16x32_bf16 v[12:15], a[184:187], v[88:91], v[12:15]\n"
"buffer_load_dwordx4 v211, s[24:27], s20 offen offset:1024 lds\n"
"v_mfma_f32_16x16x32_bf16 v[12:15], a[188:191], v[92:95], v[12:15]\n"
"s_waitcnt lgkmcnt(11)\n"
"v_exp_f32 v8, v8\n"
"v_mfma_f32_16x16x32_bf16 a[0:3], a[224:227], v[16:19], a[0:3]\n"
"ds_read_b64_tr_b16 a[198:199], v243 offset:53248\n"
"v_add_f32 v44, v44, v3\n"
"v_mfma_f32_16x16x32_bf16 a[4:7], a[228:231], v[16:19], a[4:7]\n"
"v_exp_f32 v9, v9\n"
"v_mfma_f32_16x16x32_bf16 a[8:11], a[232:235], v[16:19], a[8:11]\n"
"ds_read_b64_tr_b16 a[200:201], v242 offset:49408\n"
"v_cvt_pk_bf16_f32 v20, v8, v9\n"
"v_mfma_f32_16x16x32_bf16 a[12:15], a[236:239], v[16:19], a[12:15]\n"
"v_exp_f32 v10, v10\n"
"v_mfma_f32_16x16x32_bf16 a[16:19], a[240:243], v[16:19], a[16:19]\n"
"ds_read_b64_tr_b16 a[202:203], v242 offset:53504\n"
"v_add_f32 v44, v44, v4\n"
"v_mfma_f32_16x16x32_bf16 a[20:23], a[244:247], v[16:19], a[20:23]\n"
"v_exp_f32 v11, v11\n"
"v_mfma_f32_16x16x32_bf16 a[24:27], a[248:251], v[16:19], a[24:27]\n"
"ds_read_b64_tr_b16 a[204:205], v243 offset:49408\n"
"v_cvt_pk_bf16_f32 v21, v10, v11\n"
"v_mfma_f32_16x16x32_bf16 a[28:31], a[252:255], v[16:19], a[28:31]\n"
"v_exp_f32 v12, v12\n"
"v_mfma_f32_16x16x32_bf16 v[220:223], a[160:163], v[96:99], v[32:35]\n"
"ds_read_b64_tr_b16 a[206:207], v243 offset:53504\n"
"v_add_f32 v44, v44, v5\n"
"v_mfma_f32_16x16x32_bf16 v[220:223], a[164:167], v[100:103], v[220:223]\n"
"v_exp_f32 v13, v13\n"
"v_mfma_f32_16x16x32_bf16 v[220:223], a[168:171], v[104:107], v[220:223]\n"
"ds_read_b64_tr_b16 a[208:209], v242 offset:49664\n"
"buffer_load_dwordx4 v[184:187], v252, s[68:71], s49 offen\n"
"v_cvt_pk_bf16_f32 v22, v12, v13\n"
"v_mfma_f32_16x16x32_bf16 v[220:223], a[172:175], v[108:111], v[220:223]\n"
"s_add_u32 s49, s49, 285696\n"
"v_exp_f32 v14, v14\n"
"v_mfma_f32_16x16x32_bf16 v[224:227], a[176:179], v[96:99], v[32:35]\n"
"ds_read_b64_tr_b16 a[210:211], v242 offset:53760\n"
"v_add_f32 v44, v44, v6\n"
"v_mfma_f32_16x16x32_bf16 v[224:227], a[180:183], v[100:103], v[224:227]\n"
"v_exp_f32 v15, v15\n"
"v_mfma_f32_16x16x32_bf16 v[224:227], a[184:187], v[104:107], v[224:227]\n"
"ds_read_b64_tr_b16 a[212:213], v243 offset:49664\n"
"v_cvt_pk_bf16_f32 v23, v14, v15\n"
"v_mfma_f32_16x16x32_bf16 v[224:227], a[188:191], v[108:111], v[224:227]\n"
"s_mov_b32 m0, s51\n"
"v_exp_f32 v220, v220\n"
"v_mfma_f32_16x16x32_bf16 a[32:35], a[224:227], v[20:23], a[32:35]\n"
"buffer_load_dwordx4 v212, s[24:27], s20 offen offset:2048 lds\n"
"v_mfma_f32_16x16x32_bf16 a[36:39], a[228:231], v[20:23], a[36:39]\n"
"v_exp_f32 v221, v221\n"
"v_mfma_f32_16x16x32_bf16 a[40:43], a[232:235], v[20:23], a[40:43]\n"
"ds_read_b64_tr_b16 a[214:215], v243 offset:53760\n"
"v_cvt_pk_bf16_f32 v16, v220, v221\n"
"v_mfma_f32_16x16x32_bf16 a[44:47], a[236:239], v[20:23], a[44:47]\n"
"v_exp_f32 v222, v222\n"
"v_mfma_f32_16x16x32_bf16 a[48:51], a[240:243], v[20:23], a[48:51]\n"
"ds_read_b64_tr_b16 a[216:217], v242 offset:49920\n"
"v_add_f32 v44, v44, v7\n"
"v_mfma_f32_16x16x32_bf16 a[52:55], a[244:247], v[20:23], a[52:55]\n"
"v_exp_f32 v223, v223\n"
"v_mfma_f32_16x16x32_bf16 a[56:59], a[248:251], v[20:23], a[56:59]\n"
"ds_read_b64_tr_b16 a[218:219], v242 offset:54016\n"
"v_cvt_pk_bf16_f32 v17, v222, v223\n"
"v_mfma_f32_16x16x32_bf16 a[60:63], a[252:255], v[20:23], a[60:63]\n"
"v_exp_f32 v224, v224\n"
"v_mfma_f32_16x16x32_bf16 v[48:51], v[40:43], v[20:23], v[48:51]\n"
"ds_read_b64_tr_b16 a[220:221], v243 offset:49920\n"
"v_add_f32 v52, v52, v220\n"
"v_mfma_f32_16x16x32_bf16 v[228:231], a[160:163], v[112:115], v[36:39]\n"
"v_exp_f32 v225, v225\n"
"v_mfma_f32_16x16x32_bf16 v[228:231], a[164:167], v[116:119], v[228:231]\n"
"ds_read_b64_tr_b16 a[222:223], v243 offset:54016\n"
"v_cvt_pk_bf16_f32 v18, v224, v225\n"
"v_mfma_f32_16x16x32_bf16 v[228:231], a[168:171], v[120:123], v[228:231]\n"
"v_exp_f32 v226, v226\n"
"v_mfma_f32_16x16x32_bf16 v[228:231], a[172:175], v[124:127], v[228:231]\n"
"v_add_f32 v52, v52, v221\n"
"v_add_f32 v52, v52, v222\n"
"v_mfma_f32_16x16x32_bf16 v[232:235], a[176:179], v[112:115], v[36:39]\n"
"v_exp_f32 v227, v227\n"
"v_mfma_f32_16x16x32_bf16 v[232:235], a[180:183], v[116:119], v[232:235]\n"
"s_mov_b32 m0, s51\n"
"v_cvt_pk_bf16_f32 v19, v226, v227\n"
"v_add_f32 v52, v52, v223\n"
"v_mfma_f32_16x16x32_bf16 v[232:235], a[184:187], v[120:123], v[232:235]\n"
"buffer_load_dwordx4 v213, s[24:27], s20 offen offset:3072 lds\n"
"v_mfma_f32_16x16x32_bf16 v[232:235], a[188:191], v[124:127], v[232:235]\n"
"v_exp_f32 v228, v228\n"
"v_mfma_f32_16x16x32_bf16 a[64:67], a[224:227], v[16:19], a[64:67]\n"
"v_add_f32 v52, v52, v224\n"
"v_add_f32 v52, v52, v225\n"
"v_mfma_f32_16x16x32_bf16 a[68:71], a[228:231], v[16:19], a[68:71]\n"
"v_exp_f32 v229, v229\n"
"v_mfma_f32_16x16x32_bf16 a[72:75], a[232:235], v[16:19], a[72:75]\n"
"v_cvt_pk_bf16_f32 v20, v228, v229\n"
"v_add_f32 v52, v52, v226\n"
"v_mfma_f32_16x16x32_bf16 a[76:79], a[236:239], v[16:19], a[76:79]\n"
"v_exp_f32 v230, v230\n"
"v_mfma_f32_16x16x32_bf16 a[80:83], a[240:243], v[16:19], a[80:83]\n"
"s_waitcnt lgkmcnt(8)\n"
"v_mfma_f32_16x16x32_bf16 a[84:87], a[244:247], v[16:19], a[84:87]\n"
"v_exp_f32 v231, v231\n"
"v_mfma_f32_16x16x32_bf16 a[88:91], a[248:251], v[16:19], a[88:91]\n"
"buffer_load_dwordx4 v[152:155], v252, s[64:67], s49 offen\n"
"v_cvt_pk_bf16_f32 v21, v230, v231\n"
"v_add_f32 v52, v52, v227\n"
"v_mfma_f32_16x16x32_bf16 a[92:95], a[252:255], v[16:19], a[92:95]\n"
"v_exp_f32 v232, v232\n"
"v_mfma_f32_16x16x32_bf16 v[0:3], a[128:131], v[64:67], v[24:27]\n"
"ds_read_b128 a[160:163], v240 offset:40960\n"
"v_mfma_f32_16x16x32_bf16 v[0:3], a[132:135], v[68:71], v[0:3]\n"
"v_exp_f32 v233, v233\n"
"v_mfma_f32_16x16x32_bf16 v[0:3], a[136:139], v[72:75], v[0:3]\n"
"ds_read_b128 a[164:167], v240 offset:41216\n"
"v_cvt_pk_bf16_f32 v22, v232, v233\n"
"v_mfma_f32_16x16x32_bf16 v[0:3], a[140:143], v[76:79], v[0:3]\n"
"v_exp_f32 v234, v234\n"
"v_mfma_f32_16x16x32_bf16 v[4:7], a[144:147], v[64:67], v[24:27]\n"
"ds_read_b128 a[168:171], v240 offset:41472\n"
"v_mfma_f32_16x16x32_bf16 v[4:7], a[148:151], v[68:71], v[4:7]\n"
"v_exp_f32 v235, v235\n"
"v_mfma_f32_16x16x32_bf16 v[4:7], a[152:155], v[72:75], v[4:7]\n"
"ds_read_b128 a[172:175], v240 offset:41728\n"
"v_cvt_pk_bf16_f32 v23, v234, v235\n"
"v_mfma_f32_16x16x32_bf16 v[4:7], a[156:159], v[76:79], v[4:7]\n"
"s_add_u32 m0, s51, 4096\n"
"v_exp_f32 v0, v0\n"
"v_mfma_f32_16x16x32_bf16 a[96:99], a[224:227], v[20:23], a[96:99]\n"
"buffer_load_dwordx4 v214, s[24:27], s20 offen offset:0 lds\n"
"v_mfma_f32_16x16x32_bf16 a[100:103], a[228:231], v[20:23], a[100:103]\n"
"v_exp_f32 v1, v1\n"
"v_mfma_f32_16x16x32_bf16 a[104:107], a[232:235], v[20:23], a[104:107]\n"
"ds_read_b128 a[176:179], v240 offset:45056\n"
"v_cvt_pk_bf16_f32 v16, v0, v1\n"
"v_mfma_f32_16x16x32_bf16 a[108:111], a[236:239], v[20:23], a[108:111]\n"
"v_exp_f32 v2, v2\n"
"v_mfma_f32_16x16x32_bf16 a[112:115], a[240:243], v[20:23], a[112:115]\n"
"ds_read_b128 a[180:183], v240 offset:45312\n"
"v_add_f32 v44, v44, v0\n"
"v_mfma_f32_16x16x32_bf16 a[116:119], a[244:247], v[20:23], a[116:119]\n"
"v_exp_f32 v3, v3\n"
"v_mfma_f32_16x16x32_bf16 a[120:123], a[248:251], v[20:23], a[120:123]\n"
"ds_read_b128 a[184:187], v240 offset:45568\n"
"v_cvt_pk_bf16_f32 v17, v2, v3\n"
"v_mfma_f32_16x16x32_bf16 a[124:127], a[252:255], v[20:23], a[124:127]\n"
"v_exp_f32 v4, v4\n"
"v_mfma_f32_16x16x32_bf16 v[56:59], v[40:43], v[20:23], v[56:59]\n"
"ds_read_b128 a[188:191], v240 offset:45824\n"
"v_add_f32 v44, v44, v1\n"
"v_mfma_f32_16x16x32_bf16 v[8:11], a[128:131], v[80:83], v[28:31]\n"
"v_exp_f32 v5, v5\n"
"v_mfma_f32_16x16x32_bf16 v[8:11], a[132:135], v[84:87], v[8:11]\n"
"ds_read_b64_tr_b16 a[224:225], v242 offset:57344\n"
"v_cvt_pk_bf16_f32 v18, v4, v5\n"
"v_mfma_f32_16x16x32_bf16 v[8:11], a[136:139], v[88:91], v[8:11]\n"
"v_exp_f32 v6, v6\n"
"v_mfma_f32_16x16x32_bf16 v[8:11], a[140:143], v[92:95], v[8:11]\n"
"ds_read_b64_tr_b16 a[226:227], v242 offset:61440\n"
"v_add_f32 v44, v44, v2\n"
"v_mfma_f32_16x16x32_bf16 v[12:15], a[144:147], v[80:83], v[28:31]\n"
"v_exp_f32 v7, v7\n"
"v_mfma_f32_16x16x32_bf16 v[12:15], a[148:151], v[84:87], v[12:15]\n"
"s_add_u32 m0, s51, 4096\n"
"ds_read_b64_tr_b16 a[228:229], v243 offset:57344\n"
"v_cvt_pk_bf16_f32 v19, v6, v7\n"
"v_mfma_f32_16x16x32_bf16 v[12:15], a[152:155], v[88:91], v[12:15]\n"
"buffer_load_dwordx4 v236, s[24:27], s20 offen offset:1024 lds\n"
"v_mfma_f32_16x16x32_bf16 v[12:15], a[156:159], v[92:95], v[12:15]\n"
"s_waitcnt lgkmcnt(11)\n"
"v_exp_f32 v8, v8\n"
"v_mfma_f32_16x16x32_bf16 a[0:3], a[192:195], v[16:19], a[0:3]\n"
"ds_read_b64_tr_b16 a[230:231], v243 offset:61440\n"
"v_add_f32 v44, v44, v3\n"
"v_mfma_f32_16x16x32_bf16 a[4:7], a[196:199], v[16:19], a[4:7]\n"
"v_exp_f32 v9, v9\n"
"v_mfma_f32_16x16x32_bf16 a[8:11], a[200:203], v[16:19], a[8:11]\n"
"ds_read_b64_tr_b16 a[232:233], v242 offset:57600\n"
"buffer_load_dwordx4 v[188:191], v252, s[68:71], s49 offen\n"
"v_cvt_pk_bf16_f32 v20, v8, v9\n"
"v_mfma_f32_16x16x32_bf16 a[12:15], a[204:207], v[16:19], a[12:15]\n"
"s_add_u32 s49, s49, 9216\n"
"v_exp_f32 v10, v10\n"
"v_mfma_f32_16x16x32_bf16 a[16:19], a[208:211], v[16:19], a[16:19]\n"
"ds_read_b64_tr_b16 a[234:235], v242 offset:61696\n"
"v_add_f32 v44, v44, v4\n"
"v_mfma_f32_16x16x32_bf16 a[20:23], a[212:215], v[16:19], a[20:23]\n"
"s_add_u32 m0, s51, 4096\n"
"v_exp_f32 v11, v11\n"
"v_mfma_f32_16x16x32_bf16 a[24:27], a[216:219], v[16:19], a[24:27]\n"
"buffer_load_dwordx4 v237, s[24:27], s20 offen offset:2048 lds\n"
"ds_read_b64_tr_b16 a[236:237], v243 offset:57600\n"
"v_cvt_pk_bf16_f32 v21, v10, v11\n"
"v_mfma_f32_16x16x32_bf16 a[28:31], a[220:223], v[16:19], a[28:31]\n"
"v_exp_f32 v12, v12\n"
"v_mfma_f32_16x16x32_bf16 v[220:223], a[128:131], v[96:99], v[32:35]\n"
"ds_read_b64_tr_b16 a[238:239], v243 offset:61696\n"
"v_add_f32 v44, v44, v5\n"
"v_mfma_f32_16x16x32_bf16 v[220:223], a[132:135], v[100:103], v[220:223]\n"
"v_exp_f32 v13, v13\n"
"v_mfma_f32_16x16x32_bf16 v[220:223], a[136:139], v[104:107], v[220:223]\n"
"ds_read_b64_tr_b16 a[240:241], v242 offset:57856\n"
"v_cvt_pk_bf16_f32 v22, v12, v13\n"
"v_mfma_f32_16x16x32_bf16 v[220:223], a[140:143], v[108:111], v[220:223]\n"
"v_exp_f32 v14, v14\n"
"v_mfma_f32_16x16x32_bf16 v[224:227], a[144:147], v[96:99], v[32:35]\n"
"ds_read_b64_tr_b16 a[242:243], v242 offset:61952\n"
"v_add_f32 v44, v44, v6\n"
"v_mfma_f32_16x16x32_bf16 v[224:227], a[148:151], v[100:103], v[224:227]\n"
"v_exp_f32 v15, v15\n"
"v_mfma_f32_16x16x32_bf16 v[224:227], a[152:155], v[104:107], v[224:227]\n"
"ds_read_b64_tr_b16 a[244:245], v243 offset:57856\n"
"v_cvt_pk_bf16_f32 v23, v14, v15\n"
"v_mfma_f32_16x16x32_bf16 v[224:227], a[156:159], v[108:111], v[224:227]\n"
"s_add_u32 m0, s51, 4096\n"
"v_exp_f32 v220, v220\n"
"v_mfma_f32_16x16x32_bf16 a[32:35], a[192:195], v[20:23], a[32:35]\n"
"buffer_load_dwordx4 v238, s[24:27], s20 offen offset:3072 lds\n"
"v_mfma_f32_16x16x32_bf16 a[36:39], a[196:199], v[20:23], a[36:39]\n"
"v_exp_f32 v221, v221\n"
"v_mfma_f32_16x16x32_bf16 a[40:43], a[200:203], v[20:23], a[40:43]\n"
"ds_read_b64_tr_b16 a[246:247], v243 offset:61952\n"
"v_cvt_pk_bf16_f32 v16, v220, v221\n"
"v_mfma_f32_16x16x32_bf16 a[44:47], a[204:207], v[20:23], a[44:47]\n"
"v_exp_f32 v222, v222\n"
"v_mfma_f32_16x16x32_bf16 a[48:51], a[208:211], v[20:23], a[48:51]\n"
"ds_read_b64_tr_b16 a[248:249], v242 offset:58112\n"
"v_add_f32 v44, v44, v7\n"
"v_mfma_f32_16x16x32_bf16 a[52:55], a[212:215], v[20:23], a[52:55]\n"
"v_exp_f32 v223, v223\n"
"v_mfma_f32_16x16x32_bf16 a[56:59], a[216:219], v[20:23], a[56:59]\n"
"ds_read_b64_tr_b16 a[250:251], v242 offset:62208\n"
"v_cvt_pk_bf16_f32 v17, v222, v223\n"
"v_mfma_f32_16x16x32_bf16 a[60:63], a[220:223], v[20:23], a[60:63]\n"
"v_exp_f32 v224, v224\n"
"v_mfma_f32_16x16x32_bf16 v[48:51], v[40:43], v[20:23], v[48:51]\n"
"ds_read_b64_tr_b16 a[252:253], v243 offset:58112\n"
"v_add_f32 v52, v52, v220\n"
"v_mfma_f32_16x16x32_bf16 v[228:231], a[128:131], v[112:115], v[36:39]\n"
"v_exp_f32 v225, v225\n"
"v_mfma_f32_16x16x32_bf16 v[228:231], a[132:135], v[116:119], v[228:231]\n"
"ds_read_b64_tr_b16 a[254:255], v243 offset:62208\n"
"v_cvt_pk_bf16_f32 v18, v224, v225\n"
"v_mfma_f32_16x16x32_bf16 v[228:231], a[136:139], v[120:123], v[228:231]\n"
"v_exp_f32 v226, v226\n"
"v_mfma_f32_16x16x32_bf16 v[228:231], a[140:143], v[124:127], v[228:231]\n"
"v_add_f32 v52, v52, v221\n"
"v_add_f32 v52, v52, v222\n"
"v_mfma_f32_16x16x32_bf16 v[232:235], a[144:147], v[112:115], v[36:39]\n"
"v_exp_f32 v227, v227\n"
"v_mfma_f32_16x16x32_bf16 v[232:235], a[148:151], v[116:119], v[232:235]\n"
"s_add_u32 m0, s51, 8192\n"
"buffer_load_dwordx4 v[156:159], v252, s[64:67], s49 offen\n"
"v_cvt_pk_bf16_f32 v19, v226, v227\n"
"v_add_f32 v52, v52, v223\n"
"v_mfma_f32_16x16x32_bf16 v[232:235], a[152:155], v[120:123], v[232:235]\n"
"buffer_load_dwordx4 v239, s[24:27], s20 offen offset:0 lds\n"
"v_mfma_f32_16x16x32_bf16 v[232:235], a[156:159], v[124:127], v[232:235]\n"
"v_exp_f32 v228, v228\n"
"v_mfma_f32_16x16x32_bf16 a[64:67], a[192:195], v[16:19], a[64:67]\n"
"v_add_f32 v52, v52, v224\n"
"v_add_f32 v52, v52, v225\n"
"v_mfma_f32_16x16x32_bf16 a[68:71], a[196:199], v[16:19], a[68:71]\n"
"v_exp_f32 v229, v229\n"
"v_mfma_f32_16x16x32_bf16 a[72:75], a[200:203], v[16:19], a[72:75]\n"
"v_cvt_pk_bf16_f32 v20, v228, v229\n"
"v_add_f32 v52, v52, v226\n"
"v_mfma_f32_16x16x32_bf16 a[76:79], a[204:207], v[16:19], a[76:79]\n"
"s_waitcnt vmcnt(25) lgkmcnt(0)\n"
"v_exp_f32 v230, v230\n"
"v_mfma_f32_16x16x32_bf16 a[80:83], a[208:211], v[16:19], a[80:83]\n"
"s_barrier\n"
"v_mfma_f32_16x16x32_bf16 a[84:87], a[212:215], v[16:19], a[84:87]\n"
"v_exp_f32 v231, v231\n"
"v_mfma_f32_16x16x32_bf16 a[88:91], a[216:219], v[16:19], a[88:91]\n"
"v_cvt_pk_bf16_f32 v21, v230, v231\n"
"v_add_f32 v52, v52, v227\n"
"v_mfma_f32_16x16x32_bf16 a[92:95], a[220:223], v[16:19], a[92:95]\n"
"v_exp_f32 v232, v232\n"
"v_mfma_f32_16x16x32_bf16 v[0:3], a[160:163], v[64:67], v[24:27]\n"
"ds_read_b128 a[128:131], v241 offset:0\n"
"v_mfma_f32_16x16x32_bf16 v[0:3], a[164:167], v[68:71], v[0:3]\n"
"v_exp_f32 v233, v233\n"
"v_mfma_f32_16x16x32_bf16 v[0:3], a[168:171], v[72:75], v[0:3]\n"
"ds_read_b128 a[132:135], v241 offset:256\n"
"v_cvt_pk_bf16_f32 v22, v232, v233\n"
"v_mfma_f32_16x16x32_bf16 v[0:3], a[172:175], v[76:79], v[0:3]\n"
"v_exp_f32 v234, v234\n"
"v_mfma_f32_16x16x32_bf16 v[4:7], a[176:179], v[64:67], v[24:27]\n"
"ds_read_b128 a[136:139], v241 offset:512\n"
"v_mfma_f32_16x16x32_bf16 v[4:7], a[180:183], v[68:71], v[4:7]\n"
"v_exp_f32 v235, v235\n"
"v_mfma_f32_16x16x32_bf16 v[4:7], a[184:187], v[72:75], v[4:7]\n"
"ds_read_b128 a[140:143], v241 offset:768\n"
"v_cvt_pk_bf16_f32 v23, v234, v235\n"
"v_mfma_f32_16x16x32_bf16 v[4:7], a[188:191], v[76:79], v[4:7]\n"
"s_add_u32 m0, s53, 36864\n"
"v_exp_f32 v0, v0\n"
"v_mfma_f32_16x16x32_bf16 a[96:99], a[192:195], v[20:23], a[96:99]\n"
"buffer_load_dwordx4 v210, s[24:27], s21 offen offset:0 lds\n"
"v_mfma_f32_16x16x32_bf16 a[100:103], a[196:199], v[20:23], a[100:103]\n"
"v_exp_f32 v1, v1\n"
"v_mfma_f32_16x16x32_bf16 a[104:107], a[200:203], v[20:23], a[104:107]\n"
"ds_read_b128 a[144:147], v241 offset:4096\n"
"v_cvt_pk_bf16_f32 v16, v0, v1\n"
"v_mfma_f32_16x16x32_bf16 a[108:111], a[204:207], v[20:23], a[108:111]\n"
"v_exp_f32 v2, v2\n"
"v_mfma_f32_16x16x32_bf16 a[112:115], a[208:211], v[20:23], a[112:115]\n"
"ds_read_b128 a[148:151], v241 offset:4352\n"
"v_add_f32 v44, v44, v0\n"
"v_mfma_f32_16x16x32_bf16 a[116:119], a[212:215], v[20:23], a[116:119]\n"
"v_exp_f32 v3, v3\n"
"v_mfma_f32_16x16x32_bf16 a[120:123], a[216:219], v[20:23], a[120:123]\n"
"ds_read_b128 a[152:155], v241 offset:4608\n"
"v_cvt_pk_bf16_f32 v17, v2, v3\n"
"v_mfma_f32_16x16x32_bf16 a[124:127], a[220:223], v[20:23], a[124:127]\n"
"v_exp_f32 v4, v4\n"
"v_mfma_f32_16x16x32_bf16 v[56:59], v[40:43], v[20:23], v[56:59]\n"
"ds_read_b128 a[156:159], v241 offset:4864\n"
"v_add_f32 v44, v44, v1\n"
"v_mfma_f32_16x16x32_bf16 v[8:11], a[160:163], v[80:83], v[28:31]\n"
"v_exp_f32 v5, v5\n"
"v_mfma_f32_16x16x32_bf16 v[8:11], a[164:167], v[84:87], v[8:11]\n"
"ds_read_b64_tr_b16 a[192:193], v244 offset:16384\n"
"buffer_load_dwordx4 v[192:195], v252, s[68:71], s49 offen\n"
"v_cvt_pk_bf16_f32 v18, v4, v5\n"
"v_mfma_f32_16x16x32_bf16 v[8:11], a[168:171], v[88:91], v[8:11]\n"
"s_add_u32 s49, s49, 285696\n"
"v_exp_f32 v6, v6\n"
"v_mfma_f32_16x16x32_bf16 v[8:11], a[172:175], v[92:95], v[8:11]\n"
"ds_read_b64_tr_b16 a[194:195], v244 offset:20480\n"
"v_add_f32 v44, v44, v2\n"
"v_mfma_f32_16x16x32_bf16 v[12:15], a[176:179], v[80:83], v[28:31]\n"
"v_exp_f32 v7, v7\n"
"v_mfma_f32_16x16x32_bf16 v[12:15], a[180:183], v[84:87], v[12:15]\n"
"s_add_u32 m0, s53, 36864\n"
"ds_read_b64_tr_b16 a[196:197], v245 offset:16384\n"
"v_cvt_pk_bf16_f32 v19, v6, v7\n"
"v_mfma_f32_16x16x32_bf16 v[12:15], a[184:187], v[88:91], v[12:15]\n"
"buffer_load_dwordx4 v211, s[24:27], s21 offen offset:1024 lds\n"
"v_mfma_f32_16x16x32_bf16 v[12:15], a[188:191], v[92:95], v[12:15]\n"
"s_waitcnt lgkmcnt(11)\n"
"v_exp_f32 v8, v8\n"
"v_mfma_f32_16x16x32_bf16 a[0:3], a[224:227], v[16:19], a[0:3]\n"
"ds_read_b64_tr_b16 a[198:199], v245 offset:20480\n"
"v_add_f32 v44, v44, v3\n"
"v_mfma_f32_16x16x32_bf16 a[4:7], a[228:231], v[16:19], a[4:7]\n"
"v_exp_f32 v9, v9\n"
"v_mfma_f32_16x16x32_bf16 a[8:11], a[232:235], v[16:19], a[8:11]\n"
"ds_read_b64_tr_b16 a[200:201], v244 offset:16640\n"
"v_cvt_pk_bf16_f32 v20, v8, v9\n"
"v_mfma_f32_16x16x32_bf16 a[12:15], a[236:239], v[16:19], a[12:15]\n"
"v_exp_f32 v10, v10\n"
"v_mfma_f32_16x16x32_bf16 a[16:19], a[240:243], v[16:19], a[16:19]\n"
"ds_read_b64_tr_b16 a[202:203], v244 offset:20736\n"
"v_add_f32 v44, v44, v4\n"
"v_mfma_f32_16x16x32_bf16 a[20:23], a[244:247], v[16:19], a[20:23]\n"
"v_exp_f32 v11, v11\n"
"v_mfma_f32_16x16x32_bf16 a[24:27], a[248:251], v[16:19], a[24:27]\n"
"ds_read_b64_tr_b16 a[204:205], v245 offset:16640\n"
"v_cvt_pk_bf16_f32 v21, v10, v11\n"
"v_mfma_f32_16x16x32_bf16 a[28:31], a[252:255], v[16:19], a[28:31]\n"
"v_exp_f32 v12, v12\n"
"v_mfma_f32_16x16x32_bf16 v[220:223], a[160:163], v[96:99], v[32:35]\n"
"ds_read_b64_tr_b16 a[206:207], v245 offset:20736\n"
"v_add_f32 v44, v44, v5\n"
"v_mfma_f32_16x16x32_bf16 v[220:223], a[164:167], v[100:103], v[220:223]\n"
"v_exp_f32 v13, v13\n"
"v_mfma_f32_16x16x32_bf16 v[220:223], a[168:171], v[104:107], v[220:223]\n"
"ds_read_b64_tr_b16 a[208:209], v244 offset:16896\n"
"v_cvt_pk_bf16_f32 v22, v12, v13\n"
"v_mfma_f32_16x16x32_bf16 v[220:223], a[172:175], v[108:111], v[220:223]\n"
"v_exp_f32 v14, v14\n"
"v_mfma_f32_16x16x32_bf16 v[224:227], a[176:179], v[96:99], v[32:35]\n"
"ds_read_b64_tr_b16 a[210:211], v244 offset:20992\n"
"v_add_f32 v44, v44, v6\n"
"v_mfma_f32_16x16x32_bf16 v[224:227], a[180:183], v[100:103], v[224:227]\n"
"v_exp_f32 v15, v15\n"
"v_mfma_f32_16x16x32_bf16 v[224:227], a[184:187], v[104:107], v[224:227]\n"
"ds_read_b64_tr_b16 a[212:213], v245 offset:16896\n"
"v_cvt_pk_bf16_f32 v23, v14, v15\n"
"v_mfma_f32_16x16x32_bf16 v[224:227], a[188:191], v[108:111], v[224:227]\n"
"s_add_u32 m0, s53, 36864\n"
"v_exp_f32 v220, v220\n"
"v_mfma_f32_16x16x32_bf16 a[32:35], a[224:227], v[20:23], a[32:35]\n"
"buffer_load_dwordx4 v212, s[24:27], s21 offen offset:2048 lds\n"
"v_mfma_f32_16x16x32_bf16 a[36:39], a[228:231], v[20:23], a[36:39]\n"
"v_exp_f32 v221, v221\n"
"v_mfma_f32_16x16x32_bf16 a[40:43], a[232:235], v[20:23], a[40:43]\n"
"ds_read_b64_tr_b16 a[214:215], v245 offset:20992\n"
"v_cvt_pk_bf16_f32 v16, v220, v221\n"
"v_mfma_f32_16x16x32_bf16 a[44:47], a[236:239], v[20:23], a[44:47]\n"
"v_exp_f32 v222, v222\n"
"v_mfma_f32_16x16x32_bf16 a[48:51], a[240:243], v[20:23], a[48:51]\n"
"ds_read_b64_tr_b16 a[216:217], v244 offset:17152\n"
"v_add_f32 v44, v44, v7\n"
"v_mfma_f32_16x16x32_bf16 a[52:55], a[244:247], v[20:23], a[52:55]\n"
"v_exp_f32 v223, v223\n"
"v_mfma_f32_16x16x32_bf16 a[56:59], a[248:251], v[20:23], a[56:59]\n"
"ds_read_b64_tr_b16 a[218:219], v244 offset:21248\n"
"buffer_load_dwordx4 v[160:163], v251, s[64:67], s49 offen\n"
"v_cvt_pk_bf16_f32 v17, v222, v223\n"
"v_mfma_f32_16x16x32_bf16 a[60:63], a[252:255], v[20:23], a[60:63]\n"
"v_exp_f32 v224, v224\n"
"v_mfma_f32_16x16x32_bf16 v[48:51], v[40:43], v[20:23], v[48:51]\n"
"ds_read_b64_tr_b16 a[220:221], v245 offset:17152\n"
"v_add_f32 v52, v52, v220\n"
"v_mfma_f32_16x16x32_bf16 v[228:231], a[160:163], v[112:115], v[36:39]\n"
"v_exp_f32 v225, v225\n"
"v_mfma_f32_16x16x32_bf16 v[228:231], a[164:167], v[116:119], v[228:231]\n"
"ds_read_b64_tr_b16 a[222:223], v245 offset:21248\n"
"v_cvt_pk_bf16_f32 v18, v224, v225\n"
"v_mfma_f32_16x16x32_bf16 v[228:231], a[168:171], v[120:123], v[228:231]\n"
"v_exp_f32 v226, v226\n"
"v_mfma_f32_16x16x32_bf16 v[228:231], a[172:175], v[124:127], v[228:231]\n"
"v_add_f32 v52, v52, v221\n"
"v_add_f32 v52, v52, v222\n"
"v_mfma_f32_16x16x32_bf16 v[232:235], a[176:179], v[112:115], v[36:39]\n"
"v_exp_f32 v227, v227\n"
"v_mfma_f32_16x16x32_bf16 v[232:235], a[180:183], v[116:119], v[232:235]\n"
"s_add_u32 m0, s53, 36864\n"
"v_cvt_pk_bf16_f32 v19, v226, v227\n"
"v_add_f32 v52, v52, v223\n"
"v_mfma_f32_16x16x32_bf16 v[232:235], a[184:187], v[120:123], v[232:235]\n"
"buffer_load_dwordx4 v213, s[24:27], s21 offen offset:3072 lds\n"
"v_mfma_f32_16x16x32_bf16 v[232:235], a[188:191], v[124:127], v[232:235]\n"
"v_exp_f32 v228, v228\n"
"v_mfma_f32_16x16x32_bf16 a[64:67], a[224:227], v[16:19], a[64:67]\n"
"v_add_f32 v52, v52, v224\n"
"v_add_f32 v52, v52, v225\n"
"v_mfma_f32_16x16x32_bf16 a[68:71], a[228:231], v[16:19], a[68:71]\n"
"v_exp_f32 v229, v229\n"
"v_mfma_f32_16x16x32_bf16 a[72:75], a[232:235], v[16:19], a[72:75]\n"
"v_cvt_pk_bf16_f32 v20, v228, v229\n"
"v_add_f32 v52, v52, v226\n"
"v_mfma_f32_16x16x32_bf16 a[76:79], a[236:239], v[16:19], a[76:79]\n"
"v_exp_f32 v230, v230\n"
"v_mfma_f32_16x16x32_bf16 a[80:83], a[240:243], v[16:19], a[80:83]\n"
"s_waitcnt lgkmcnt(8)\n"
"v_mfma_f32_16x16x32_bf16 a[84:87], a[244:247], v[16:19], a[84:87]\n"
"v_exp_f32 v231, v231\n"
"v_mfma_f32_16x16x32_bf16 a[88:91], a[248:251], v[16:19], a[88:91]\n"
"v_cvt_pk_bf16_f32 v21, v230, v231\n"
"v_add_f32 v52, v52, v227\n"
"v_mfma_f32_16x16x32_bf16 a[92:95], a[252:255], v[16:19], a[92:95]\n"
"v_exp_f32 v232, v232\n"
"v_mfma_f32_16x16x32_bf16 v[0:3], a[128:131], v[64:67], v[24:27]\n"
"ds_read_b128 a[160:163], v241 offset:8192\n"
"v_mfma_f32_16x16x32_bf16 v[0:3], a[132:135], v[68:71], v[0:3]\n"
"v_exp_f32 v233, v233\n"
"v_mfma_f32_16x16x32_bf16 v[0:3], a[136:139], v[72:75], v[0:3]\n"
"ds_read_b128 a[164:167], v241 offset:8448\n"
"v_cvt_pk_bf16_f32 v22, v232, v233\n"
"v_mfma_f32_16x16x32_bf16 v[0:3], a[140:143], v[76:79], v[0:3]\n"
"v_exp_f32 v234, v234\n"
"v_mfma_f32_16x16x32_bf16 v[4:7], a[144:147], v[64:67], v[24:27]\n"
"ds_read_b128 a[168:171], v241 offset:8704\n"
"v_mfma_f32_16x16x32_bf16 v[4:7], a[148:151], v[68:71], v[4:7]\n"
"v_exp_f32 v235, v235\n"
"v_mfma_f32_16x16x32_bf16 v[4:7], a[152:155], v[72:75], v[4:7]\n"
"ds_read_b128 a[172:175], v241 offset:8960\n"
"v_cvt_pk_bf16_f32 v23, v234, v235\n"
"v_mfma_f32_16x16x32_bf16 v[4:7], a[156:159], v[76:79], v[4:7]\n"
"s_add_u32 m0, s53, 40960\n"
"v_exp_f32 v0, v0\n"
"v_mfma_f32_16x16x32_bf16 a[96:99], a[224:227], v[20:23], a[96:99]\n"
"buffer_load_dwordx4 v214, s[24:27], s21 offen offset:0 lds\n"
"v_mfma_f32_16x16x32_bf16 a[100:103], a[228:231], v[20:23], a[100:103]\n"
"v_exp_f32 v1, v1\n"
"v_mfma_f32_16x16x32_bf16 a[104:107], a[232:235], v[20:23], a[104:107]\n"
"ds_read_b128 a[176:179], v241 offset:12288\n"
"buffer_load_dwordx4 v[196:199], v251, s[68:71], s49 offen\n"
"v_cvt_pk_bf16_f32 v16, v0, v1\n"
"v_mfma_f32_16x16x32_bf16 a[108:111], a[236:239], v[20:23], a[108:111]\n"
"s_lshl_b32 s62, s41, 6\n"
"v_exp_f32 v2, v2\n"
"v_mfma_f32_16x16x32_bf16 a[112:115], a[240:243], v[20:23], a[112:115]\n"
"ds_read_b128 a[180:183], v241 offset:12544\n"
"v_add_f32 v44, v44, v0\n"
"v_mfma_f32_16x16x32_bf16 a[116:119], a[244:247], v[20:23], a[116:119]\n"
"v_exp_f32 v3, v3\n"
"v_mfma_f32_16x16x32_bf16 a[120:123], a[248:251], v[20:23], a[120:123]\n"
"ds_read_b128 a[184:187], v241 offset:12800\n"
"v_cvt_pk_bf16_f32 v17, v2, v3\n"
"v_mfma_f32_16x16x32_bf16 a[124:127], a[252:255], v[20:23], a[124:127]\n"
"v_exp_f32 v4, v4\n"
"v_mfma_f32_16x16x32_bf16 v[56:59], v[40:43], v[20:23], v[56:59]\n"
"ds_read_b128 a[188:191], v241 offset:13056\n"
"v_add_f32 v44, v44, v1\n"
"v_mfma_f32_16x16x32_bf16 v[8:11], a[128:131], v[80:83], v[28:31]\n"
"v_exp_f32 v5, v5\n"
"v_mfma_f32_16x16x32_bf16 v[8:11], a[132:135], v[84:87], v[8:11]\n"
"ds_read_b64_tr_b16 a[224:225], v244 offset:24576\n"
"v_cvt_pk_bf16_f32 v18, v4, v5\n"
"v_add_u32 v253, s62, v248\n"
"v_mfma_f32_16x16x32_bf16 v[8:11], a[136:139], v[88:91], v[8:11]\n"
"v_exp_f32 v6, v6\n"
"v_mfma_f32_16x16x32_bf16 v[8:11], a[140:143], v[92:95], v[8:11]\n"
"ds_read_b64_tr_b16 a[226:227], v244 offset:28672\n"
"v_add_f32 v44, v44, v2\n"
"v_mfma_f32_16x16x32_bf16 v[12:15], a[144:147], v[80:83], v[28:31]\n"
"v_exp_f32 v7, v7\n"
"v_mfma_f32_16x16x32_bf16 v[12:15], a[148:151], v[84:87], v[12:15]\n"
"s_add_u32 m0, s53, 40960\n"
"ds_read_b64_tr_b16 a[228:229], v245 offset:24576\n"
"v_cvt_pk_bf16_f32 v19, v6, v7\n"
"v_mfma_f32_16x16x32_bf16 v[12:15], a[152:155], v[88:91], v[12:15]\n"
"buffer_load_dwordx4 v236, s[24:27], s21 offen offset:1024 lds\n"
"v_mov_b32 v255, 0x10000000\n"
"v_mfma_f32_16x16x32_bf16 v[12:15], a[156:159], v[92:95], v[12:15]\n"
"s_waitcnt lgkmcnt(11)\n"
"v_exp_f32 v8, v8\n"
"v_mfma_f32_16x16x32_bf16 a[0:3], a[192:195], v[16:19], a[0:3]\n"
"ds_read_b64_tr_b16 a[230:231], v245 offset:28672\n"
"v_add_f32 v44, v44, v3\n"
"v_mfma_f32_16x16x32_bf16 a[4:7], a[196:199], v[16:19], a[4:7]\n"
"v_exp_f32 v9, v9\n"
"v_mfma_f32_16x16x32_bf16 a[8:11], a[200:203], v[16:19], a[8:11]\n"
"ds_read_b64_tr_b16 a[232:233], v244 offset:24832\n"
"v_cvt_pk_bf16_f32 v20, v8, v9\n"
"v_mfma_f32_16x16x32_bf16 a[12:15], a[204:207], v[16:19], a[12:15]\n"
"v_exp_f32 v10, v10\n"
"v_mfma_f32_16x16x32_bf16 a[16:19], a[208:211], v[16:19], a[16:19]\n"
"ds_read_b64_tr_b16 a[234:235], v244 offset:28928\n"
"v_add_f32 v44, v44, v4\n"
"v_mfma_f32_16x16x32_bf16 a[20:23], a[212:215], v[16:19], a[20:23]\n"
"s_add_u32 m0, s53, 40960\n"
"v_exp_f32 v11, v11\n"
"v_cmp_gt_u32 vcc, 144, v253\n"
"v_mfma_f32_16x16x32_bf16 a[24:27], a[216:219], v[16:19], a[24:27]\n"
"buffer_load_dwordx4 v237, s[24:27], s21 offen offset:2048 lds\n"
"ds_read_b64_tr_b16 a[236:237], v245 offset:24832\n"
"v_cvt_pk_bf16_f32 v21, v10, v11\n"
"v_mfma_f32_16x16x32_bf16 a[28:31], a[220:223], v[16:19], a[28:31]\n"
"v_exp_f32 v12, v12\n"
"v_mfma_f32_16x16x32_bf16 v[220:223], a[128:131], v[96:99], v[32:35]\n"
"ds_read_b64_tr_b16 a[238:239], v245 offset:28928\n"
"v_add_f32 v44, v44, v5\n"
"v_mfma_f32_16x16x32_bf16 v[220:223], a[132:135], v[100:103], v[220:223]\n"
"v_exp_f32 v13, v13\n"
"v_mfma_f32_16x16x32_bf16 v[220:223], a[136:139], v[104:107], v[220:223]\n"
"ds_read_b64_tr_b16 a[240:241], v244 offset:25088\n"
"v_cvt_pk_bf16_f32 v22, v12, v13\n"
"v_mfma_f32_16x16x32_bf16 v[220:223], a[140:143], v[108:111], v[220:223]\n"
"AT_pg12:\n"
"s_cbranch_execz AT_hr\n"
"v_exp_f32 v14, v14\n"
"v_mfma_f32_16x16x32_bf16 v[224:227], a[144:147], v[96:99], v[32:35]\n"
"ds_read_b64_tr_b16 a[242:243], v244 offset:29184\n"
"v_add_f32 v44, v44, v6\n"
"v_lshlrev_b32 v254, 3, v253\n"
"v_mfma_f32_16x16x32_bf16 v[224:227], a[148:151], v[100:103], v[224:227]\n"
"s_nop 0\n"
"v_exp_f32 v15, v15\n"
"v_mfma_f32_16x16x32_bf16 v[224:227], a[152:155], v[104:107], v[224:227]\n"
"ds_read_b64_tr_b16 a[244:245], v245 offset:25088\n"
"v_cvt_pk_bf16_f32 v23, v14, v15\n"
"v_mfma_f32_16x16x32_bf16 v[224:227], a[156:159], v[108:111], v[224:227]\n"
"s_add_u32 m0, s53, 40960\n"
"v_exp_f32 v220, v220\n"
"v_mfma_f32_16x16x32_bf16 a[32:35], a[192:195], v[20:23], a[32:35]\n"
"buffer_load_dwordx4 v238, s[24:27], s21 offen offset:3072 lds\n"
"v_cndmask_b32 v254, v255, v254, vcc\n"
"v_mfma_f32_16x16x32_bf16 a[36:39], a[196:199], v[20:23], a[36:39]\n"
"v_exp_f32 v221, v221\n"
"v_mfma_f32_16x16x32_bf16 a[40:43], a[200:203], v[20:23], a[40:43]\n"
"ds_read_b64_tr_b16 a[246:247], v245 offset:29184\n"
"v_cvt_pk_bf16_f32 v16, v220, v221\n"
"v_mfma_f32_16x16x32_bf16 a[44:47], a[204:207], v[20:23], a[44:47]\n"
"v_exp_f32 v222, v222\n"
"v_mfma_f32_16x16x32_bf16 a[48:51], a[208:211], v[20:23], a[48:51]\n"
"ds_read_b64_tr_b16 a[248:249], v244 offset:25344\n"
"v_add_f32 v44, v44, v7\n"
"v_mfma_f32_16x16x32_bf16 a[52:55], a[212:215], v[20:23], a[52:55]\n"
"v_exp_f32 v223, v223\n"
"v_mfma_f32_16x16x32_bf16 a[56:59], a[216:219], v[20:23], a[56:59]\n"
"ds_read_b64_tr_b16 a[250:251], v244 offset:29440\n"
"v_cvt_pk_bf16_f32 v17, v222, v223\n"
"v_mfma_f32_16x16x32_bf16 a[60:63], a[220:223], v[20:23], a[60:63]\n"
"v_exp_f32 v224, v224\n"
"v_lshlrev_b32 v216, 6, v248\n"
"v_mfma_f32_16x16x32_bf16 v[48:51], v[40:43], v[20:23], v[48:51]\n"
"ds_read_b64_tr_b16 a[252:253], v245 offset:25344\n"
"v_add_f32 v52, v52, v220\n"
"v_mfma_f32_16x16x32_bf16 v[228:231], a[128:131], v[112:115], v[36:39]\n"
"v_exp_f32 v225, v225\n"
"v_mfma_f32_16x16x32_bf16 v[228:231], a[132:135], v[116:119], v[228:231]\n"
"ds_read_b64_tr_b16 a[254:255], v245 offset:29440\n"
"v_cvt_pk_bf16_f32 v18, v224, v225\n"
"v_mfma_f32_16x16x32_bf16 v[228:231], a[136:139], v[120:123], v[228:231]\n"
"v_exp_f32 v226, v226\n"
"v_mfma_f32_16x16x32_bf16 v[228:231], a[140:143], v[124:127], v[228:231]\n"
"v_add_f32 v52, v52, v221\n"
"v_add_f32 v52, v52, v222\n"
"v_mfma_f32_16x16x32_bf16 v[232:235], a[144:147], v[112:115], v[36:39]\n"
"v_exp_f32 v227, v227\n"
"v_mfma_f32_16x16x32_bf16 v[232:235], a[148:151], v[116:119], v[232:235]\n"
"s_add_u32 m0, s53, 45056\n"
"v_cvt_pk_bf16_f32 v19, v226, v227\n"
"v_add_f32 v52, v52, v223\n"
"v_lshrrev_b32 v215, 3, v248\n"
"v_mfma_f32_16x16x32_bf16 v[232:235], a[152:155], v[120:123], v[232:235]\n"
"buffer_load_dwordx4 v239, s[24:27], s21 offen offset:0 lds\n"
"v_mul_u32_u24 v215, 1152, v215\n"
"v_mfma_f32_16x16x32_bf16 v[232:235], a[156:159], v[124:127], v[232:235]\n"
"v_exp_f32 v228, v228\n"
"v_mfma_f32_16x16x32_bf16 a[64:67], a[192:195], v[16:19], a[64:67]\n"
"v_add_f32 v52, v52, v224\n"
"v_add_f32 v52, v52, v225\n"
"v_mfma_f32_16x16x32_bf16 a[68:71], a[196:199], v[16:19], a[68:71]\n"
"v_exp_f32 v229, v229\n"
"v_mfma_f32_16x16x32_bf16 a[72:75], a[200:203], v[16:19], a[72:75]\n"
"v_cvt_pk_bf16_f32 v20, v228, v229\n"
"v_add_f32 v52, v52, v226\n"
"v_mfma_f32_16x16x32_bf16 a[76:79], a[204:207], v[16:19], a[76:79]\n"
"s_waitcnt vmcnt(26) lgkmcnt(0)\n"
"v_exp_f32 v230, v230\n"
"v_mfma_f32_16x16x32_bf16 a[80:83], a[208:211], v[16:19], a[80:83]\n"
"s_barrier\n"
"v_mfma_f32_16x16x32_bf16 a[84:87], a[212:215], v[16:19], a[84:87]\n"
"v_exp_f32 v231, v231\n"
"v_mfma_f32_16x16x32_bf16 a[88:91], a[216:219], v[16:19], a[88:91]\n"
"v_cvt_pk_bf16_f32 v21, v230, v231\n"
"v_add_f32 v52, v52, v227\n"
"v_and_b32 v217, 7, v248\n"
"v_mfma_f32_16x16x32_bf16 a[92:95], a[220:223], v[16:19], a[92:95]\n"
"v_exp_f32 v232, v232\n"
"v_mfma_f32_16x16x32_bf16 v[0:3], a[160:163], v[64:67], v[24:27]\n"
"ds_read_b128 a[128:131], v241 offset:32768\n"
"v_lshl_add_u32 v215, v217, 7, v215\n"
"v_mfma_f32_16x16x32_bf16 v[0:3], a[164:167], v[68:71], v[0:3]\n"
"s_and_b32 s46, s44, 7\n"
"v_exp_f32 v233, v233\n"
"v_mfma_f32_16x16x32_bf16 v[0:3], a[168:171], v[72:75], v[0:3]\n"
"s_lshl_b32 s46, s46, 2\n"
"ds_read_b128 a[132:135], v241 offset:33024\n"
"v_cvt_pk_bf16_f32 v22, v232, v233\n"
"v_mfma_f32_16x16x32_bf16 v[0:3], a[172:175], v[76:79], v[0:3]\n"
"s_add_u32 s46, s46, s41\n"
"v_exp_f32 v234, v234\n"
"v_mfma_f32_16x16x32_bf16 v[4:7], a[176:179], v[64:67], v[24:27]\n"
"s_mul_i32 s46, s46, 9216\n"
"ds_read_b128 a[136:139], v241 offset:33280\n"
"v_mfma_f32_16x16x32_bf16 v[4:7], a[180:183], v[68:71], v[4:7]\n"
"s_mul_i32 s62, s43, 294912\n"
"v_exp_f32 v235, v235\n"
"v_mfma_f32_16x16x32_bf16 v[4:7], a[184:187], v[72:75], v[4:7]\n"
"s_add_u32 s46, s46, s62\n"
"ds_read_b128 a[140:143], v241 offset:33536\n"
"v_cvt_pk_bf16_f32 v23, v234, v235\n"
"v_mfma_f32_16x16x32_bf16 v[4:7], a[188:191], v[76:79], v[4:7]\n"
"s_add_u32 m0, s53, 73728\n"
"v_exp_f32 v0, v0\n"
"v_mfma_f32_16x16x32_bf16 a[96:99], a[192:195], v[20:23], a[96:99]\n"
"buffer_load_dwordx4 v210, s[24:27], s22 offen offset:0 lds\n"
"v_mfma_f32_16x16x32_bf16 a[100:103], a[196:199], v[20:23], a[100:103]\n"
"v_exp_f32 v1, v1\n"
"v_mfma_f32_16x16x32_bf16 a[104:107], a[200:203], v[20:23], a[104:107]\n"
"ds_read_b128 a[144:147], v241 offset:36864\n"
"buffer_load_dword v250, v215, s[24:27], s46 offen offset:128\n"
"v_cvt_pk_bf16_f32 v16, v0, v1\n"
"v_mfma_f32_16x16x32_bf16 a[108:111], a[204:207], v[20:23], a[108:111]\n"
"v_exp_f32 v2, v2\n"
"v_mfma_f32_16x16x32_bf16 a[112:115], a[208:211], v[20:23], a[112:115]\n"
"ds_read_b128 a[148:151], v241 offset:37120\n"
"v_add_f32 v44, v44, v0\n"
"v_mfma_f32_16x16x32_bf16 a[116:119], a[212:215], v[20:23], a[116:119]\n"
"v_exp_f32 v3, v3\n"
"v_mfma_f32_16x16x32_bf16 a[120:123], a[216:219], v[20:23], a[120:123]\n"
"ds_read_b128 a[152:155], v241 offset:37376\n"
"v_cvt_pk_bf16_f32 v17, v2, v3\n"
"v_mfma_f32_16x16x32_bf16 a[124:127], a[220:223], v[20:23], a[124:127]\n"
"v_exp_f32 v4, v4\n"
"v_mfma_f32_16x16x32_bf16 v[56:59], v[40:43], v[20:23], v[56:59]\n"
"ds_read_b128 a[156:159], v241 offset:37632\n"
"v_add_f32 v44, v44, v1\n"
"v_mfma_f32_16x16x32_bf16 v[8:11], a[160:163], v[80:83], v[28:31]\n"
"v_exp_f32 v5, v5\n"
"v_mfma_f32_16x16x32_bf16 v[8:11], a[164:167], v[84:87], v[8:11]\n"
"ds_read_b64_tr_b16 a[192:193], v244 offset:49152\n"
"v_cvt_pk_bf16_f32 v18, v4, v5\n"
"v_mfma_f32_16x16x32_bf16 v[8:11], a[168:171], v[88:91], v[8:11]\n"
"v_exp_f32 v6, v6\n"
"v_mfma_f32_16x16x32_bf16 v[8:11], a[172:175], v[92:95], v[8:11]\n"
"ds_read_b64_tr_b16 a[194:195], v244 offset:53248\n"
"v_add_f32 v44, v44, v2\n"
"v_mfma_f32_16x16x32_bf16 v[12:15], a[176:179], v[80:83], v[28:31]\n"
"v_exp_f32 v7, v7\n"
"v_mfma_f32_16x16x32_bf16 v[12:15], a[180:183], v[84:87], v[12:15]\n"
"s_add_u32 m0, s53, 73728\n"
"ds_read_b64_tr_b16 a[196:197], v245 offset:49152\n"
"v_cvt_pk_bf16_f32 v19, v6, v7\n"
"v_mfma_f32_16x16x32_bf16 v[12:15], a[184:187], v[88:91], v[12:15]\n"
"buffer_load_dwordx4 v211, s[24:27], s22 offen offset:1024 lds\n"
"v_mfma_f32_16x16x32_bf16 v[12:15], a[188:191], v[92:95], v[12:15]\n"
"s_waitcnt lgkmcnt(11)\n"
"v_exp_f32 v8, v8\n"
"v_mfma_f32_16x16x32_bf16 a[0:3], a[224:227], v[16:19], a[0:3]\n"
"ds_read_b64_tr_b16 a[198:199], v245 offset:53248\n"
"v_add_f32 v44, v44, v3\n"
"v_mfma_f32_16x16x32_bf16 a[4:7], a[228:231], v[16:19], a[4:7]\n"
"v_exp_f32 v9, v9\n"
"v_mfma_f32_16x16x32_bf16 a[8:11], a[232:235], v[16:19], a[8:11]\n"
"ds_read_b64_tr_b16 a[200:201], v244 offset:49408\n"
"v_cvt_pk_bf16_f32 v20, v8, v9\n"
"v_mfma_f32_16x16x32_bf16 a[12:15], a[236:239], v[16:19], a[12:15]\n"
"v_exp_f32 v10, v10\n"
"v_mfma_f32_16x16x32_bf16 a[16:19], a[240:243], v[16:19], a[16:19]\n"
"ds_read_b64_tr_b16 a[202:203], v244 offset:53504\n"
"v_add_f32 v44, v44, v4\n"
"v_mfma_f32_16x16x32_bf16 a[20:23], a[244:247], v[16:19], a[20:23]\n"
"v_exp_f32 v11, v11\n"
"v_mfma_f32_16x16x32_bf16 a[24:27], a[248:251], v[16:19], a[24:27]\n"
"ds_read_b64_tr_b16 a[204:205], v245 offset:49408\n"
"v_cvt_pk_bf16_f32 v21, v10, v11\n"
"v_mfma_f32_16x16x32_bf16 a[28:31], a[252:255], v[16:19], a[28:31]\n"
"v_exp_f32 v12, v12\n"
"v_mfma_f32_16x16x32_bf16 v[220:223], a[160:163], v[96:99], v[32:35]\n"
"ds_read_b64_tr_b16 a[206:207], v245 offset:53504\n"
"v_add_f32 v44, v44, v5\n"
"v_mfma_f32_16x16x32_bf16 v[220:223], a[164:167], v[100:103], v[220:223]\n"
"v_exp_f32 v13, v13\n"
"v_mfma_f32_16x16x32_bf16 v[220:223], a[168:171], v[104:107], v[220:223]\n"
"ds_read_b64_tr_b16 a[208:209], v244 offset:49664\n"
"v_cvt_pk_bf16_f32 v22, v12, v13\n"
"v_mfma_f32_16x16x32_bf16 v[220:223], a[172:175], v[108:111], v[220:223]\n"
"v_exp_f32 v14, v14\n"
"v_mfma_f32_16x16x32_bf16 v[224:227], a[176:179], v[96:99], v[32:35]\n"
"ds_read_b64_tr_b16 a[210:211], v244 offset:53760\n"
"v_add_f32 v44, v44, v6\n"
"v_mfma_f32_16x16x32_bf16 v[224:227], a[180:183], v[100:103], v[224:227]\n"
"v_exp_f32 v15, v15\n"
"v_mfma_f32_16x16x32_bf16 v[224:227], a[184:187], v[104:107], v[224:227]\n"
"ds_read_b64_tr_b16 a[212:213], v245 offset:49664\n"
"buffer_load_dword v250, v215, s[24:27], s46 offen offset:192\n"
"v_cvt_pk_bf16_f32 v23, v14, v15\n"
"v_mfma_f32_16x16x32_bf16 v[224:227], a[188:191], v[108:111], v[224:227]\n"
"s_add_u32 m0, s53, 73728\n"
"v_exp_f32 v220, v220\n"
"v_mfma_f32_16x16x32_bf16 a[32:35], a[224:227], v[20:23], a[32:35]\n"
"buffer_load_dwordx4 v212, s[24:27], s22 offen offset:2048 lds\n"
"v_mfma_f32_16x16x32_bf16 a[36:39], a[228:231], v[20:23], a[36:39]\n"
"v_exp_f32 v221, v221\n"
"v_mfma_f32_16x16x32_bf16 a[40:43], a[232:235], v[20:23], a[40:43]\n"
"ds_read_b64_tr_b16 a[214:215], v245 offset:53760\n"
"v_cvt_pk_bf16_f32 v16, v220, v221\n"
"v_mfma_f32_16x16x32_bf16 a[44:47], a[236:239], v[20:23], a[44:47]\n"
"v_exp_f32 v222, v222\n"
"v_mfma_f32_16x16x32_bf16 a[48:51], a[240:243], v[20:23], a[48:51]\n"
"ds_read_b64_tr_b16 a[216:217], v244 offset:49920\n"
"v_add_f32 v44, v44, v7\n"
"v_mfma_f32_16x16x32_bf16 a[52:55], a[244:247], v[20:23], a[52:55]\n"
"v_exp_f32 v223, v223\n"
"v_mfma_f32_16x16x32_bf16 a[56:59], a[248:251], v[20:23], a[56:59]\n"
"ds_read_b64_tr_b16 a[218:219], v244 offset:54016\n"
"v_cvt_pk_bf16_f32 v17, v222, v223\n"
"v_mfma_f32_16x16x32_bf16 a[60:63], a[252:255], v[20:23], a[60:63]\n"
"v_exp_f32 v224, v224\n"
"v_mfma_f32_16x16x32_bf16 v[48:51], v[40:43], v[20:23], v[48:51]\n"
"ds_read_b64_tr_b16 a[220:221], v245 offset:49920\n"
"v_add_f32 v52, v52, v220\n"
"v_mfma_f32_16x16x32_bf16 v[228:231], a[160:163], v[112:115], v[36:39]\n"
"v_exp_f32 v225, v225\n"
"v_mfma_f32_16x16x32_bf16 v[228:231], a[164:167], v[116:119], v[228:231]\n"
"ds_read_b64_tr_b16 a[222:223], v245 offset:54016\n"
"v_cvt_pk_bf16_f32 v18, v224, v225\n"
"v_mfma_f32_16x16x32_bf16 v[228:231], a[168:171], v[120:123], v[228:231]\n"
"v_exp_f32 v226, v226\n"
"v_mfma_f32_16x16x32_bf16 v[228:231], a[172:175], v[124:127], v[228:231]\n"
"v_add_f32 v52, v52, v221\n"
"v_add_f32 v52, v52, v222\n"
"v_mfma_f32_16x16x32_bf16 v[232:235], a[176:179], v[112:115], v[36:39]\n"
"v_exp_f32 v227, v227\n"
"v_mfma_f32_16x16x32_bf16 v[232:235], a[180:183], v[116:119], v[232:235]\n"
"s_add_u32 m0, s53, 73728\n"
"v_cvt_pk_bf16_f32 v19, v226, v227\n"
"v_add_f32 v52, v52, v223\n"
"v_mfma_f32_16x16x32_bf16 v[232:235], a[184:187], v[120:123], v[232:235]\n"
"buffer_load_dwordx4 v213, s[24:27], s22 offen offset:3072 lds\n"
"v_mfma_f32_16x16x32_bf16 v[232:235], a[188:191], v[124:127], v[232:235]\n"
"v_exp_f32 v228, v228\n"
"v_mfma_f32_16x16x32_bf16 a[64:67], a[224:227], v[16:19], a[64:67]\n"
"v_add_f32 v52, v52, v224\n"
"v_add_f32 v52, v52, v225\n"
"v_mfma_f32_16x16x32_bf16 a[68:71], a[228:231], v[16:19], a[68:71]\n"
"v_exp_f32 v229, v229\n"
"v_mfma_f32_16x16x32_bf16 a[72:75], a[232:235], v[16:19], a[72:75]\n"
"v_cvt_pk_bf16_f32 v20, v228, v229\n"
"v_add_f32 v52, v52, v226\n"
"v_mfma_f32_16x16x32_bf16 a[76:79], a[236:239], v[16:19], a[76:79]\n"
"v_exp_f32 v230, v230\n"
"v_mfma_f32_16x16x32_bf16 a[80:83], a[240:243], v[16:19], a[80:83]\n"
"s_waitcnt lgkmcnt(8)\n"
"v_mfma_f32_16x16x32_bf16 a[84:87], a[244:247], v[16:19], a[84:87]\n"
"v_exp_f32 v231, v231\n"
"v_mfma_f32_16x16x32_bf16 a[88:91], a[248:251], v[16:19], a[88:91]\n"
"v_cvt_pk_bf16_f32 v21, v230, v231\n"
"v_add_f32 v52, v52, v227\n"
"v_mfma_f32_16x16x32_bf16 a[92:95], a[252:255], v[16:19], a[92:95]\n"
"v_exp_f32 v232, v232\n"
"v_mfma_f32_16x16x32_bf16 v[0:3], a[128:131], v[64:67], v[24:27]\n"
"ds_read_b128 a[160:163], v241 offset:40960\n"
"v_mfma_f32_16x16x32_bf16 v[0:3], a[132:135], v[68:71], v[0:3]\n"
"v_exp_f32 v233, v233\n"
"v_mfma_f32_16x16x32_bf16 v[0:3], a[136:139], v[72:75], v[0:3]\n"
"ds_read_b128 a[164:167], v241 offset:41216\n"
"v_cvt_pk_bf16_f32 v22, v232, v233\n"
"v_mfma_f32_16x16x32_bf16 v[0:3], a[140:143], v[76:79], v[0:3]\n"
"v_exp_f32 v234, v234\n"
"v_mfma_f32_16x16x32_bf16 v[4:7], a[144:147], v[64:67], v[24:27]\n"
"ds_read_b128 a[168:171], v241 offset:41472\n"
"v_mfma_f32_16x16x32_bf16 v[4:7], a[148:151], v[68:71], v[4:7]\n"
"v_exp_f32 v235, v235\n"
"v_mfma_f32_16x16x32_bf16 v[4:7], a[152:155], v[72:75], v[4:7]\n"
"ds_read_b128 a[172:175], v241 offset:41728\n"
"v_cvt_pk_bf16_f32 v23, v234, v235\n"
"v_mfma_f32_16x16x32_bf16 v[4:7], a[156:159], v[76:79], v[4:7]\n"
"s_add_u32 m0, s53, 77824\n"
"v_exp_f32 v0, v0\n"
"v_mfma_f32_16x16x32_bf16 a[96:99], a[224:227], v[20:23], a[96:99]\n"
"buffer_load_dwordx4 v214, s[24:27], s22 offen offset:0 lds\n"
"v_mfma_f32_16x16x32_bf16 a[100:103], a[228:231], v[20:23], a[100:103]\n"
"v_exp_f32 v1, v1\n"
"v_mfma_f32_16x16x32_bf16 a[104:107], a[232:235], v[20:23], a[104:107]\n"
"ds_read_b128 a[176:179], v241 offset:45056\n"
"v_cvt_pk_bf16_f32 v16, v0, v1\n"
"v_mfma_f32_16x16x32_bf16 a[108:111], a[236:239], v[20:23], a[108:111]\n"
"v_exp_f32 v2, v2\n"
"v_mfma_f32_16x16x32_bf16 a[112:115], a[240:243], v[20:23], a[112:115]\n"
"ds_read_b128 a[180:183], v241 offset:45312\n"
"v_add_f32 v44, v44, v0\n"
"v_mfma_f32_16x16x32_bf16 a[116:119], a[244:247], v[20:23], a[116:119]\n"
"v_exp_f32 v3, v3\n"
"v_mfma_f32_16x16x32_bf16 a[120:123], a[248:251], v[20:23], a[120:123]\n"
"ds_read_b128 a[184:187], v241 offset:45568\n"
"v_cvt_pk_bf16_f32 v17, v2, v3\n"
"v_mfma_f32_16x16x32_bf16 a[124:127], a[252:255], v[20:23], a[124:127]\n"
"v_exp_f32 v4, v4\n"
"v_mfma_f32_16x16x32_bf16 v[56:59], v[40:43], v[20:23], v[56:59]\n"
"ds_read_b128 a[188:191], v241 offset:45824\n"
"v_add_f32 v44, v44, v1\n"
"v_mfma_f32_16x16x32_bf16 v[8:11], a[128:131], v[80:83], v[28:31]\n"
"v_exp_f32 v5, v5\n"
"v_mfma_f32_16x16x32_bf16 v[8:11], a[132:135], v[84:87], v[8:11]\n"
"ds_read_b64_tr_b16 a[224:225], v244 offset:57344\n"
"v_cvt_pk_bf16_f32 v18, v4, v5\n"
"v_mfma_f32_16x16x32_bf16 v[8:11], a[136:139], v[88:91], v[8:11]\n"
"v_exp_f32 v6, v6\n"
"v_mfma_f32_16x16x32_bf16 v[8:11], a[140:143], v[92:95], v[8:11]\n"
"ds_read_b64_tr_b16 a[226:227], v244 offset:61440\n"
"v_add_f32 v44, v44, v2\n"
"v_mfma_f32_16x16x32_bf16 v[12:15], a[144:147], v[80:83], v[28:31]\n"
"v_exp_f32 v7, v7\n"
"v_mfma_f32_16x16x32_bf16 v[12:15], a[148:151], v[84:87], v[12:15]\n"
"s_add_u32 m0, s53, 77824\n"
"ds_read_b64_tr_b16 a[228:229], v245 offset:57344\n"
"v_cvt_pk_bf16_f32 v19, v6, v7\n"
"v_mfma_f32_16x16x32_bf16 v[12:15], a[152:155], v[88:91], v[12:15]\n"
"buffer_load_dwordx4 v236, s[24:27], s22 offen offset:1024 lds\n"
"v_mfma_f32_16x16x32_bf16 v[12:15], a[156:159], v[92:95], v[12:15]\n"
"s_waitcnt lgkmcnt(11)\n"
"v_exp_f32 v8, v8\n"
"v_mfma_f32_16x16x32_bf16 a[0:3], a[192:195], v[16:19], a[0:3]\n"
"ds_read_b64_tr_b16 a[230:231], v245 offset:61440\n"
"v_add_f32 v44, v44, v3\n"
"v_mfma_f32_16x16x32_bf16 a[4:7], a[196:199], v[16:19], a[4:7]\n"
"v_exp_f32 v9, v9\n"
"v_mfma_f32_16x16x32_bf16 a[8:11], a[200:203], v[16:19], a[8:11]\n"
"ds_read_b64_tr_b16 a[232:233], v244 offset:57600\n"
"v_cvt_pk_bf16_f32 v20, v8, v9\n"
"v_mfma_f32_16x16x32_bf16 a[12:15], a[204:207], v[16:19], a[12:15]\n"
"v_exp_f32 v10, v10\n"
"v_mfma_f32_16x16x32_bf16 a[16:19], a[208:211], v[16:19], a[16:19]\n"
"ds_read_b64_tr_b16 a[234:235], v244 offset:61696\n"
"v_add_f32 v44, v44, v4\n"
"v_mfma_f32_16x16x32_bf16 a[20:23], a[212:215], v[16:19], a[20:23]\n"
"s_add_u32 m0, s53, 77824\n"
"v_exp_f32 v11, v11\n"
"v_mfma_f32_16x16x32_bf16 a[24:27], a[216:219], v[16:19], a[24:27]\n"
"buffer_load_dwordx4 v237, s[24:27], s22 offen offset:2048 lds\n"
"ds_read_b64_tr_b16 a[236:237], v245 offset:57600\n"
"v_cvt_pk_bf16_f32 v21, v10, v11\n"
"v_mfma_f32_16x16x32_bf16 a[28:31], a[220:223], v[16:19], a[28:31]\n"
"v_exp_f32 v12, v12\n"
"v_mfma_f32_16x16x32_bf16 v[220:223], a[128:131], v[96:99], v[32:35]\n"
"ds_read_b64_tr_b16 a[238:239], v245 offset:61696\n"
"v_add_f32 v44, v44, v5\n"
"v_mfma_f32_16x16x32_bf16 v[220:223], a[132:135], v[100:103], v[220:223]\n"
"v_exp_f32 v13, v13\n"
"v_mfma_f32_16x16x32_bf16 v[220:223], a[136:139], v[104:107], v[220:223]\n"
"ds_read_b64_tr_b16 a[240:241], v244 offset:57856\n"
"v_cvt_pk_bf16_f32 v22, v12, v13\n"
"v_mfma_f32_16x16x32_bf16 v[220:223], a[140:143], v[108:111], v[220:223]\n"
"v_exp_f32 v14, v14\n"
"v_mfma_f32_16x16x32_bf16 v[224:227], a[144:147], v[96:99], v[32:35]\n"
"ds_read_b64_tr_b16 a[242:243], v244 offset:61952\n"
"v_add_f32 v44, v44, v6\n"
"v_mfma_f32_16x16x32_bf16 v[224:227], a[148:151], v[100:103], v[224:227]\n"
"v_exp_f32 v15, v15\n"
"v_mfma_f32_16x16x32_bf16 v[224:227], a[152:155], v[104:107], v[224:227]\n"
"ds_read_b64_tr_b16 a[244:245], v245 offset:57856\n"
"v_cvt_pk_bf16_f32 v23, v14, v15\n"
"v_mfma_f32_16x16x32_bf16 v[224:227], a[156:159], v[108:111], v[224:227]\n"
"s_add_u32 m0, s53, 77824\n"
"v_exp_f32 v220, v220\n"
"v_mfma_f32_16x16x32_bf16 a[32:35], a[192:195], v[20:23], a[32:35]\n"
"buffer_load_dwordx4 v238, s[24:27], s22 offen offset:3072 lds\n"
"v_mfma_f32_16x16x32_bf16 a[36:39], a[196:199], v[20:23], a[36:39]\n"
"v_exp_f32 v221, v221\n"
"v_mfma_f32_16x16x32_bf16 a[40:43], a[200:203], v[20:23], a[40:43]\n"
"ds_read_b64_tr_b16 a[246:247], v245 offset:61952\n"
"v_cvt_pk_bf16_f32 v16, v220, v221\n"
"v_mfma_f32_16x16x32_bf16 a[44:47], a[204:207], v[20:23], a[44:47]\n"
"v_exp_f32 v222, v222\n"
"v_mfma_f32_16x16x32_bf16 a[48:51], a[208:211], v[20:23], a[48:51]\n"
"ds_read_b64_tr_b16 a[248:249], v244 offset:58112\n"
"v_add_f32 v44, v44, v7\n"
"v_mfma_f32_16x16x32_bf16 a[52:55], a[212:215], v[20:23], a[52:55]\n"
"v_exp_f32 v223, v223\n"
"v_mfma_f32_16x16x32_bf16 a[56:59], a[216:219], v[20:23], a[56:59]\n"
"ds_read_b64_tr_b16 a[250:251], v244 offset:62208\n"
"v_cvt_pk_bf16_f32 v17, v222, v223\n"
"v_mfma_f32_16x16x32_bf16 a[60:63], a[220:223], v[20:23], a[60:63]\n"
"v_exp_f32 v224, v224\n"
"v_mfma_f32_16x16x32_bf16 v[48:51], v[40:43], v[20:23], v[48:51]\n"
"ds_read_b64_tr_b16 a[252:253], v245 offset:58112\n"
"v_add_f32 v52, v52, v220\n"
"v_mfma_f32_16x16x32_bf16 v[228:231], a[128:131], v[112:115], v[36:39]\n"
"v_exp_f32 v225, v225\n"
"v_mfma_f32_16x16x32_bf16 v[228:231], a[132:135], v[116:119], v[228:231]\n"
"ds_read_b64_tr_b16 a[254:255], v245 offset:62208\n"
"v_cvt_pk_bf16_f32 v18, v224, v225\n"
"v_mfma_f32_16x16x32_bf16 v[228:231], a[136:139], v[120:123], v[228:231]\n"
"v_exp_f32 v226, v226\n"
"v_mfma_f32_16x16x32_bf16 v[228:231], a[140:143], v[124:127], v[228:231]\n"
"v_add_f32 v52, v52, v221\n"
"v_add_f32 v52, v52, v222\n"
"v_mfma_f32_16x16x32_bf16 v[232:235], a[144:147], v[112:115], v[36:39]\n"
"v_exp_f32 v227, v227\n"
"v_mfma_f32_16x16x32_bf16 v[232:235], a[148:151], v[116:119], v[232:235]\n"
"s_add_u32 m0, s53, 81920\n"
"v_cvt_pk_bf16_f32 v19, v226, v227\n"
"v_add_f32 v52, v52, v223\n"
"v_mfma_f32_16x16x32_bf16 v[232:235], a[152:155], v[120:123], v[232:235]\n"
"buffer_load_dwordx4 v239, s[24:27], s22 offen offset:0 lds\n"
"v_mfma_f32_16x16x32_bf16 v[232:235], a[156:159], v[124:127], v[232:235]\n"
"v_exp_f32 v228, v228\n"
"v_mfma_f32_16x16x32_bf16 a[64:67], a[192:195], v[16:19], a[64:67]\n"
"v_add_f32 v52, v52, v224\n"
"v_add_f32 v52, v52, v225\n"
"v_mfma_f32_16x16x32_bf16 a[68:71], a[196:199], v[16:19], a[68:71]\n"
"v_exp_f32 v229, v229\n"
"v_mfma_f32_16x16x32_bf16 a[72:75], a[200:203], v[16:19], a[72:75]\n"
"v_cvt_pk_bf16_f32 v20, v228, v229\n"
"v_add_f32 v52, v52, v226\n"
"v_mfma_f32_16x16x32_bf16 a[76:79], a[204:207], v[16:19], a[76:79]\n"
"s_waitcnt vmcnt(23) lgkmcnt(0)\n"
"v_exp_f32 v230, v230\n"
"v_mfma_f32_16x16x32_bf16 a[80:83], a[208:211], v[16:19], a[80:83]\n"
"s_barrier\n"
"v_mfma_f32_16x16x32_bf16 a[84:87], a[212:215], v[16:19], a[84:87]\n"
"v_exp_f32 v231, v231\n"
"v_mfma_f32_16x16x32_bf16 a[88:91], a[216:219], v[16:19], a[88:91]\n"
"v_cvt_pk_bf16_f32 v21, v230, v231\n"
"v_add_f32 v52, v52, v227\n"
"v_mfma_f32_16x16x32_bf16 a[92:95], a[220:223], v[16:19], a[92:95]\n"
"v_exp_f32 v232, v232\n"
"v_mfma_f32_16x16x32_bf16 v[0:3], a[160:163], v[64:67], v[24:27]\n"
"ds_read_b64_tr_b16 a[128:129], v202 offset:0\n"
"v_mfma_f32_16x16x32_bf16 v[0:3], a[164:167], v[68:71], v[0:3]\n"
"s_add_u32 m0, s53, 110592\n"
"v_exp_f32 v233, v233\n"
"v_mfma_f32_16x16x32_bf16 v[0:3], a[168:171], v[72:75], v[0:3]\n"
"buffer_load_dwordx4 v210, s[24:27], s23 offen offset:0 lds\n"
"ds_read_b64_tr_b16 a[130:131], v202 offset:4608\n"
"v_cvt_pk_bf16_f32 v22, v232, v233\n"
"v_mfma_f32_16x16x32_bf16 v[0:3], a[172:175], v[76:79], v[0:3]\n"
"v_exp_f32 v234, v234\n"
"v_mfma_f32_16x16x32_bf16 v[4:7], a[176:179], v[64:67], v[24:27]\n"
"ds_read_b64_tr_b16 a[132:133], v202 offset:32\n"
"v_mfma_f32_16x16x32_bf16 v[4:7], a[180:183], v[68:71], v[4:7]\n"
"s_add_u32 m0, s53, 110592\n"
"v_exp_f32 v235, v235\n"
"v_mfma_f32_16x16x32_bf16 v[4:7], a[184:187], v[72:75], v[4:7]\n"
"buffer_load_dwordx4 v211, s[24:27], s23 offen offset:1024 lds\n"
"ds_read_b64_tr_b16 a[134:135], v202 offset:4640\n"
"v_cvt_pk_bf16_f32 v23, v234, v235\n"
"v_mfma_f32_16x16x32_bf16 v[4:7], a[188:191], v[76:79], v[4:7]\n"
"v_exp_f32 v0, v0\n"
"v_mfma_f32_16x16x32_bf16 a[96:99], a[192:195], v[20:23], a[96:99]\n"
"s_nop 0\n"
"v_mfma_f32_16x16x32_bf16 a[100:103], a[196:199], v[20:23], a[100:103]\n"
"s_add_u32 m0, s53, 110592\n"
"v_exp_f32 v1, v1\n"
"v_mfma_f32_16x16x32_bf16 a[104:107], a[200:203], v[20:23], a[104:107]\n"
"buffer_load_dwordx4 v212, s[24:27], s23 offen offset:2048 lds\n"
"ds_read_b64_tr_b16 a[136:137], v202 offset:64\n"
"v_cvt_pk_bf16_f32 v16, v0, v1\n"
"v_mfma_f32_16x16x32_bf16 a[108:111], a[204:207], v[20:23], a[108:111]\n"
"v_exp_f32 v2, v2\n"
"v_mfma_f32_16x16x32_bf16 a[112:115], a[208:211], v[20:23], a[112:115]\n"
"ds_read_b64_tr_b16 a[138:139], v202 offset:4672\n"
"v_add_f32 v44, v44, v0\n"
"v_mfma_f32_16x16x32_bf16 a[116:119], a[212:215], v[20:23], a[116:119]\n"
"s_add_u32 m0, s53, 110592\n"
"v_exp_f32 v3, v3\n"
"v_mfma_f32_16x16x32_bf16 a[120:123], a[216:219], v[20:23], a[120:123]\n"
"buffer_load_dwordx4 v213, s[24:27], s23 offen offset:3072 lds\n"
"ds_read_b64_tr_b16 a[140:141], v202 offset:96\n"
"v_cvt_pk_bf16_f32 v17, v2, v3\n"
"v_mfma_f32_16x16x32_bf16 a[124:127], a[220:223], v[20:23], a[124:127]\n"
"v_exp_f32 v4, v4\n"
"v_mfma_f32_16x16x32_bf16 v[56:59], v[40:43], v[20:23], v[56:59]\n"
"ds_read_b64_tr_b16 a[142:143], v202 offset:4704\n"
"v_add_f32 v44, v44, v1\n"
"v_mfma_f32_16x16x32_bf16 v[8:11], a[160:163], v[80:83], v[28:31]\n"
"s_add_u32 m0, s53, 114688\n"
"v_exp_f32 v5, v5\n"
"v_mfma_f32_16x16x32_bf16 v[8:11], a[164:167], v[84:87], v[8:11]\n"
"buffer_load_dwordx4 v214, s[24:27], s23 offen offset:0 lds\n"
"ds_read_b64_tr_b16 a[144:145], v202 offset:128\n"
"v_cvt_pk_bf16_f32 v18, v4, v5\n"
"v_mfma_f32_16x16x32_bf16 v[8:11], a[168:171], v[88:91], v[8:11]\n"
"v_exp_f32 v6, v6\n"
"v_mfma_f32_16x16x32_bf16 v[8:11], a[172:175], v[92:95], v[8:11]\n"
"ds_read_b64_tr_b16 a[146:147], v202 offset:4736\n"
"v_add_f32 v44, v44, v2\n"
"v_mfma_f32_16x16x32_bf16 v[12:15], a[176:179], v[80:83], v[28:31]\n"
"s_add_u32 m0, s53, 114688\n"
"v_exp_f32 v7, v7\n"
"v_mfma_f32_16x16x32_bf16 v[12:15], a[180:183], v[84:87], v[12:15]\n"
"buffer_load_dwordx4 v236, s[24:27], s23 offen offset:1024 lds\n"
"ds_read_b64_tr_b16 a[148:149], v202 offset:160\n"
"v_cvt_pk_bf16_f32 v19, v6, v7\n"
"v_mfma_f32_16x16x32_bf16 v[12:15], a[184:187], v[88:91], v[12:15]\n"
"s_nop 0\n"
"v_mfma_f32_16x16x32_bf16 v[12:15], a[188:191], v[92:95], v[12:15]\n"
"s_add_u32 m0, s53, 114688\n"
"v_exp_f32 v8, v8\n"
"v_mfma_f32_16x16x32_bf16 a[0:3], a[224:227], v[16:19], a[0:3]\n"
"buffer_load_dwordx4 v237, s[24:27], s23 offen offset:2048 lds\n"
"ds_read_b64_tr_b16 a[150:151], v202 offset:4768\n"
"v_add_f32 v44, v44, v3\n"
"v_mfma_f32_16x16x32_bf16 a[4:7], a[228:231], v[16:19], a[4:7]\n"
"v_exp_f32 v9, v9\n"
"v_mfma_f32_16x16x32_bf16 a[8:11], a[232:235], v[16:19], a[8:11]\n"
"ds_read_b64_tr_b16 a[152:153], v202 offset:192\n"
"v_cvt_pk_bf16_f32 v20, v8, v9\n"
"v_mfma_f32_16x16x32_bf16 a[12:15], a[236:239], v[16:19], a[12:15]\n"
"s_add_u32 m0, s53, 114688\n"
"v_exp_f32 v10, v10\n"
"v_mfma_f32_16x16x32_bf16 a[16:19], a[240:243], v[16:19], a[16:19]\n"
"buffer_load_dwordx4 v238, s[24:27], s23 offen offset:3072 lds\n"
"ds_read_b64_tr_b16 a[154:155], v202 offset:4800\n"
"v_add_f32 v44, v44, v4\n"
"v_mfma_f32_16x16x32_bf16 a[20:23], a[244:247], v[16:19], a[20:23]\n"
"v_exp_f32 v11, v11\n"
"v_mfma_f32_16x16x32_bf16 a[24:27], a[248:251], v[16:19], a[24:27]\n"
"ds_read_b64_tr_b16 a[156:157], v202 offset:224\n"
"v_cvt_pk_bf16_f32 v21, v10, v11\n"
"v_mfma_f32_16x16x32_bf16 a[28:31], a[252:255], v[16:19], a[28:31]\n"
"s_add_u32 m0, s53, 118784\n"
"v_exp_f32 v12, v12\n"
"v_mfma_f32_16x16x32_bf16 v[220:223], a[160:163], v[96:99], v[32:35]\n"
"buffer_load_dwordx4 v239, s[24:27], s23 offen offset:0 lds\n"
"ds_read_b64_tr_b16 a[158:159], v202 offset:4832\n"
"v_add_f32 v44, v44, v5\n"
"v_mfma_f32_16x16x32_bf16 v[220:223], a[164:167], v[100:103], v[220:223]\n"
"v_exp_f32 v13, v13\n"
"v_mfma_f32_16x16x32_bf16 v[220:223], a[168:171], v[104:107], v[220:223]\n"
"ds_read_b64_tr_b16 a[192:193], v205 offset:0\n"
"v_cvt_pk_bf16_f32 v22, v12, v13\n"
"v_mfma_f32_16x16x32_bf16 v[220:223], a[172:175], v[108:111], v[220:223]\n"
"v_exp_f32 v14, v14\n"
"v_mfma_f32_16x16x32_bf16 v[224:227], a[176:179], v[96:99], v[32:35]\n"
"ds_read_b64_tr_b16 a[194:195], v205 offset:4608\n"
"v_add_f32 v44, v44, v6\n"
"v_mfma_f32_16x16x32_bf16 v[224:227], a[180:183], v[100:103], v[224:227]\n"
"v_exp_f32 v15, v15\n"
"v_mfma_f32_16x16x32_bf16 v[224:227], a[184:187], v[104:107], v[224:227]\n"
"ds_read_b64_tr_b16 a[196:197], v202 offset:9216\n"
"v_cvt_pk_bf16_f32 v23, v14, v15\n"
"v_mfma_f32_16x16x32_bf16 v[224:227], a[188:191], v[108:111], v[224:227]\n"
"v_exp_f32 v220, v220\n"
"v_mfma_f32_16x16x32_bf16 a[32:35], a[224:227], v[20:23], a[32:35]\n"
"s_nop 0\n"
"v_mfma_f32_16x16x32_bf16 a[36:39], a[228:231], v[20:23], a[36:39]\n"
"v_exp_f32 v221, v221\n"
"v_mfma_f32_16x16x32_bf16 a[40:43], a[232:235], v[20:23], a[40:43]\n"
"ds_read_b64_tr_b16 a[198:199], v202 offset:13824\n"
"v_cvt_pk_bf16_f32 v16, v220, v221\n"
"v_mfma_f32_16x16x32_bf16 a[44:47], a[236:239], v[20:23], a[44:47]\n"
"v_exp_f32 v222, v222\n"
"v_mfma_f32_16x16x32_bf16 a[48:51], a[240:243], v[20:23], a[48:51]\n"
"v_add_f32 v44, v44, v7\n"
"v_mfma_f32_16x16x32_bf16 a[52:55], a[244:247], v[20:23], a[52:55]\n"
"v_exp_f32 v223, v223\n"
"s_add_u32 s46, s52, 4096\n"
"s_add_u32 s47, s52, 8192\n"
"s_add_u32 s48, s52, 12288\n"
"v_mov_b32 v45, v44\n"
"v_mfma_f32_16x16x32_bf16 a[56:59], a[248:251], v[20:23], a[56:59]\n"
"v_cvt_pk_bf16_f32 v17, v222, v223\n"
"s_nop 0\n"
"s_nop 0\n"
"v_permlane16_swap_b32 v44, v45\n"
"v_add_f32 v44, v44, v45\n"
"v_mfma_f32_16x16x32_bf16 a[60:63], a[252:255], v[20:23], a[60:63]\n"
"v_exp_f32 v224, v224\n"
"v_mov_b32 v45, v44\n"
"s_nop 0\n"
"s_nop 0\n"
"v_permlane32_swap_b32 v44, v45\n"
"v_mfma_f32_16x16x32_bf16 v[48:51], v[40:43], v[20:23], v[48:51]\n"
"v_add_f32 v52, v52, v220\n"
"v_add_f32 v44, v44, v45\n"
"v_rcp_f32 v46, v44\n"
"v_accvgpr_read_b32 v216, a0\n"
"v_accvgpr_read_b32 v217, a1\n"
"v_mfma_f32_16x16x32_bf16 v[228:231], a[160:163], v[112:115], v[36:39]\n"
"v_exp_f32 v225, v225\n"
"v_accvgpr_read_b32 v218, a2\n"
"v_accvgpr_read_b32 v219, a3\n"
"v_accvgpr_read_b32 v60, a4\n"
"v_accvgpr_read_b32 v61, a5\n"
"v_mfma_f32_16x16x32_bf16 v[228:231], a[164:167], v[116:119], v[228:231]\n"
"v_cvt_pk_bf16_f32 v18, v224, v225\n"
"v_accvgpr_read_b32 v62, a6\n"
"v_accvgpr_read_b32 v63, a7\n"
"v_mul_f32 v216, v216, v46\n"
"v_mul_f32 v217, v217, v46\n"
"v_mfma_f32_16x16x32_bf16 v[228:231], a[168:171], v[120:123], v[228:231]\n"
"v_exp_f32 v226, v226\n"
"v_mul_f32 v218, v218, v46\n"
"v_mul_f32 v219, v219, v46\n"
"v_mul_f32 v60, v60, v46\n"
"v_mul_f32 v61, v61, v46\n"
"v_mfma_f32_16x16x32_bf16 v[228:231], a[172:175], v[124:127], v[228:231]\n"
"v_add_f32 v52, v52, v221\n"
"v_add_f32 v52, v52, v222\n"
"v_mul_f32 v62, v62, v46\n"
"v_mul_f32 v63, v63, v46\n"
"v_cvt_pk_bf16_f32 v0, v216, v217\n"
"v_cvt_pk_bf16_f32 v1, v218, v219\n"
"v_mfma_f32_16x16x32_bf16 v[232:235], a[176:179], v[112:115], v[36:39]\n"
"v_exp_f32 v227, v227\n"
"v_cvt_pk_bf16_f32 v2, v60, v61\n"
"v_cvt_pk_bf16_f32 v3, v62, v63\n"
"buffer_store_dwordx4 v[0:3], v207, s[28:31], s52 offen offset:0 sc1\n"
"v_accvgpr_read_b32 v216, a8\n"
"v_mfma_f32_16x16x32_bf16 v[232:235], a[180:183], v[116:119], v[232:235]\n"
"v_cvt_pk_bf16_f32 v19, v226, v227\n"
"v_add_f32 v52, v52, v223\n"
"v_accvgpr_read_b32 v217, a9\n"
"v_accvgpr_read_b32 v218, a10\n"
"v_accvgpr_read_b32 v219, a11\n"
"v_accvgpr_read_b32 v60, a12\n"
"v_mfma_f32_16x16x32_bf16 v[232:235], a[184:187], v[120:123], v[232:235]\n"
"AT_pg13:\n"
"s_cbranch_execz AT_hr\n"
"v_accvgpr_read_b32 v61, a13\n"
"v_accvgpr_read_b32 v62, a14\n"
"v_accvgpr_read_b32 v63, a15\n"
"v_mul_f32 v216, v216, v46\n"
"v_mfma_f32_16x16x32_bf16 v[232:235], a[188:191], v[124:127], v[232:235]\n"
"v_exp_f32 v228, v228\n"
"v_mul_f32 v217, v217, v46\n"
"v_mul_f32 v218, v218, v46\n"
"v_mul_f32 v219, v219, v46\n"
"v_mul_f32 v60, v60, v46\n"
"v_mfma_f32_16x16x32_bf16 a[64:67], a[224:227], v[16:19], a[64:67]\n"
"v_add_f32 v52, v52, v224\n"
"v_add_f32 v52, v52, v225\n"
"v_mul_f32 v61, v61, v46\n"
"v_mul_f32 v62, v62, v46\n"
"v_mul_f32 v63, v63, v46\n"
"v_cvt_pk_bf16_f32 v4, v216, v217\n"
"v_mfma_f32_16x16x32_bf16 a[68:71], a[228:231], v[16:19], a[68:71]\n"
"v_exp_f32 v229, v229\n"
"v_cvt_pk_bf16_f32 v5, v218, v219\n"
"v_cvt_pk_bf16_f32 v6, v60, v61\n"
"v_cvt_pk_bf16_f32 v7, v62, v63\n"
"buffer_store_dwordx4 v[4:7], v207, s[28:31], s52 offen offset:2048 sc1\n"
"v_mfma_f32_16x16x32_bf16 a[72:75], a[232:235], v[16:19], a[72:75]\n"
"v_cvt_pk_bf16_f32 v20, v228, v229\n"
"v_add_f32 v52, v52, v226\n"
"v_accvgpr_read_b32 v216, a16\n"
"v_accvgpr_read_b32 v217, a17\n"
"v_accvgpr_read_b32 v218, a18\n"
"v_accvgpr_read_b32 v219, a19\n"
"v_mfma_f32_16x16x32_bf16 a[76:79], a[236:239], v[16:19], a[76:79]\n"
"v_exp_f32 v230, v230\n"
"v_accvgpr_read_b32 v60, a20\n"
"v_accvgpr_read_b32 v61, a21\n"
"v_accvgpr_read_b32 v62, a22\n"
"v_accvgpr_read_b32 v63, a23\n"
"v_mfma_f32_16x16x32_bf16 a[80:83], a[240:243], v[16:19], a[80:83]\n"
"v_mul_f32 v216, v216, v46\n"
"v_mul_f32 v217, v217, v46\n"
"v_mul_f32 v218, v218, v46\n"
"v_mul_f32 v219, v219, v46\n"
"v_mfma_f32_16x16x32_bf16 a[84:87], a[244:247], v[16:19], a[84:87]\n"
"v_exp_f32 v231, v231\n"
"v_mul_f32 v60, v60, v46\n"
"v_mul_f32 v61, v61, v46\n"
"v_mul_f32 v62, v62, v46\n"
"v_mul_f32 v63, v63, v46\n"
"v_mfma_f32_16x16x32_bf16 a[88:91], a[248:251], v[16:19], a[88:91]\n"
"v_cvt_pk_bf16_f32 v21, v230, v231\n"
"v_add_f32 v52, v52, v227\n"
"v_cvt_pk_bf16_f32 v8, v216, v217\n"
"v_cvt_pk_bf16_f32 v9, v218, v219\n"
"v_cvt_pk_bf16_f32 v10, v60, v61\n"
"v_cvt_pk_bf16_f32 v11, v62, v63\n"
"v_mfma_f32_16x16x32_bf16 a[92:95], a[252:255], v[16:19], a[92:95]\n"
"v_exp_f32 v232, v232\n"
"buffer_store_dwordx4 v[8:11], v207, s[28:31], s46 offen offset:0 sc1\n"
"v_accvgpr_read_b32 v216, a24\n"
"v_accvgpr_read_b32 v217, a25\n"
"v_accvgpr_read_b32 v218, a26\n"
"v_exp_f32 v233, v233\n"
"v_accvgpr_read_b32 v219, a27\n"
"v_cvt_pk_bf16_f32 v22, v232, v233\n"
"v_accvgpr_read_b32 v60, a28\n"
"v_exp_f32 v234, v234\n"
"v_accvgpr_read_b32 v61, a29\n"
"v_exp_f32 v235, v235\n"
"v_accvgpr_read_b32 v62, a30\n"
"v_cvt_pk_bf16_f32 v23, v234, v235\n"
"v_accvgpr_read_b32 v63, a31\n"
"v_mul_f32 v216, v216, v46\n"
"v_mul_f32 v217, v217, v46\n"
"v_mfma_f32_16x16x32_bf16 a[96:99], a[224:227], v[20:23], a[96:99]\n"
"v_mul_f32 v218, v218, v46\n"
"v_mul_f32 v219, v219, v46\n"
"v_mfma_f32_16x16x32_bf16 a[100:103], a[228:231], v[20:23], a[100:103]\n"
"v_mul_f32 v60, v60, v46\n"
"v_mul_f32 v61, v61, v46\n"
"v_mfma_f32_16x16x32_bf16 a[104:107], a[232:235], v[20:23], a[104:107]\n"
"v_mul_f32 v62, v62, v46\n"
"v_mul_f32 v63, v63, v46\n"
"v_mfma_f32_16x16x32_bf16 a[108:111], a[236:239], v[20:23], a[108:111]\n"
"v_cvt_pk_bf16_f32 v12, v216, v217\n"
"v_cvt_pk_bf16_f32 v13, v218, v219\n"
"v_mfma_f32_16x16x32_bf16 a[112:115], a[240:243], v[20:23], a[112:115]\n"
"v_cvt_pk_bf16_f32 v14, v60, v61\n"
"v_cvt_pk_bf16_f32 v15, v62, v63\n"
"v_mfma_f32_16x16x32_bf16 a[116:119], a[244:247], v[20:23], a[116:119]\n"
"buffer_store_dwordx4 v[12:15], v207, s[28:31], s46 offen offset:2048 sc1\n"
"v_rcp_f32 v48, v49\n"
"v_mfma_f32_16x16x32_bf16 a[120:123], a[248:251], v[20:23], a[120:123]\n"
"v_accvgpr_read_b32 v216, a48\n"
"v_accvgpr_read_b32 v217, a49\n"
"v_mfma_f32_16x16x32_bf16 a[124:127], a[252:255], v[20:23], a[124:127]\n"
"v_accvgpr_read_b32 v218, a50\n"
"v_accvgpr_read_b32 v219, a51\n"
"v_mfma_f32_16x16x32_bf16 v[56:59], v[40:43], v[20:23], v[56:59]\n"
"v_accvgpr_read_b32 v60, a52\n"
"v_accvgpr_read_b32 v61, a53\n"
"v_accvgpr_read_b32 v62, a54\n"
"v_accvgpr_read_b32 v63, a55\n"
"v_mul_f32 v216, v216, v48\n"
"v_mul_f32 v217, v217, v48\n"
"v_mul_f32 v218, v218, v48\n"
"v_mul_f32 v219, v219, v48\n"
"v_mul_f32 v60, v60, v48\n"
"v_mul_f32 v61, v61, v48\n"
"v_mul_f32 v62, v62, v48\n"
"v_mul_f32 v63, v63, v48\n"
"v_cvt_pk_bf16_f32 v24, v216, v217\n"
"v_cvt_pk_bf16_f32 v25, v218, v219\n"
"v_cvt_pk_bf16_f32 v26, v60, v61\n"
"v_cvt_pk_bf16_f32 v27, v62, v63\n"
"buffer_store_dwordx4 v[24:27], v207, s[28:31], s48 offen offset:0 sc1\n"
"v_accvgpr_read_b32 v216, a56\n"
"v_accvgpr_read_b32 v217, a57\n"
"v_accvgpr_read_b32 v218, a58\n"
"v_accvgpr_read_b32 v219, a59\n"
"v_accvgpr_read_b32 v60, a60\n"
"v_accvgpr_read_b32 v61, a61\n"
"v_accvgpr_read_b32 v62, a62\n"
"v_accvgpr_read_b32 v63, a63\n"
"v_mul_f32 v216, v216, v48\n"
"v_mul_f32 v217, v217, v48\n"
"v_mul_f32 v218, v218, v48\n"
"v_mul_f32 v219, v219, v48\n"
"v_mul_f32 v60, v60, v48\n"
"v_mul_f32 v61, v61, v48\n"
"v_mul_f32 v62, v62, v48\n"
"v_mul_f32 v63, v63, v48\n"
"v_cvt_pk_bf16_f32 v28, v216, v217\n"
"v_cvt_pk_bf16_f32 v29, v218, v219\n"
"v_cvt_pk_bf16_f32 v30, v60, v61\n"
"v_cvt_pk_bf16_f32 v31, v62, v63\n"
"buffer_store_dwordx4 v[28:31], v207, s[28:31], s48 offen offset:2048 sc1\n"
"v_mov_b32 v226, v52\n"
"s_nop 0\n"
"s_nop 0\n"
"v_permlane16_swap_b32 v52, v226\n"
"v_add_f32 v52, v52, v226\n"
"v_mov_b32 v226, v52\n"
"s_nop 0\n"
"s_nop 0\n"
"v_permlane32_swap_b32 v52, v226\n"
"v_add_f32 v52, v52, v226\n"
"v_mov_b32 v219, 0x5f800000\n"
"s_nop 7\n"
"v_cmp_nle_f32 s[56:57], v44, v219\n"
"v_cmp_nle_f32 s[58:59], v49, v219\n"
"v_cmp_nle_f32 s[60:61], v52, v219\n"
"v_cmp_nle_f32 s[62:63], v56, v219\n"
"v_rcp_f32 v218, v44\n"
"v_rcp_f32 v220, v49\n"
"v_rcp_f32 v222, v52\n"
"v_rcp_f32 v224, v56\n"
"v_accvgpr_read_b32 v226, a32\n"
"v_accvgpr_read_b32 v227, a33\n"
"v_accvgpr_read_b32 v228, a34\n"
"v_accvgpr_read_b32 v229, a35\n"
"v_accvgpr_read_b32 v230, a36\n"
"v_accvgpr_read_b32 v231, a37\n"
"v_accvgpr_read_b32 v232, a38\n"
"v_accvgpr_read_b32 v233, a39\n"
"v_pk_mul_f32 v[226:227], v[226:227], v[220:221] op_sel_hi:[1,0]\n"
"v_pk_mul_f32 v[228:229], v[228:229], v[220:221] op_sel_hi:[1,0]\n"
"v_pk_mul_f32 v[230:231], v[230:231], v[220:221] op_sel_hi:[1,0]\n"
"v_pk_mul_f32 v[232:233], v[232:233], v[220:221] op_sel_hi:[1,0]\n"
"v_cvt_pk_bf16_f32 v16, v226, v227\n"
"v_cvt_pk_bf16_f32 v17, v228, v229\n"
"v_cvt_pk_bf16_f32 v18, v230, v231\n"
"v_cvt_pk_bf16_f32 v19, v232, v233\n"
"buffer_store_dwordx4 v[16:19], v207, s[28:31], s47 offen offset:0 sc1\n"
"s_or_b64 s[56:57], s[56:57], s[58:59]\n"
"s_or_b64 s[60:61], s[60:61], s[62:63]\n"
"s_or_b64 s[56:57], s[56:57], s[60:61]\n"
"s_cselect_b32 s56, 1, 0\n"
"s_cmp_eq_u32 s45, 0\n"
"s_cselect_b32 s56, s56, 0\n"
"s_lshl_b32 s57, s41, 2\n"
"s_add_u32 s57, s57, s40\n"
"s_add_u32 s57, s57, 163776\n"
"v_mov_b32 v234, s56\n"
"v_mov_b32 v235, s57\n"
"ds_write_b32 v235, v234\n"
"v_accvgpr_read_b32 v226, a40\n"
"v_accvgpr_read_b32 v227, a41\n"
"v_accvgpr_read_b32 v228, a42\n"
"v_accvgpr_read_b32 v229, a43\n"
"v_accvgpr_read_b32 v230, a44\n"
"v_accvgpr_read_b32 v231, a45\n"
"v_accvgpr_read_b32 v232, a46\n"
"v_accvgpr_read_b32 v233, a47\n"
"v_pk_mul_f32 v[226:227], v[226:227], v[220:221] op_sel_hi:[1,0]\n"
"v_pk_mul_f32 v[228:229], v[228:229], v[220:221] op_sel_hi:[1,0]\n"
"v_pk_mul_f32 v[230:231], v[230:231], v[220:221] op_sel_hi:[1,0]\n"
"v_pk_mul_f32 v[232:233], v[232:233], v[220:221] op_sel_hi:[1,0]\n"
"v_cvt_pk_bf16_f32 v20, v226, v227\n"
"v_cvt_pk_bf16_f32 v21, v228, v229\n"
"v_cvt_pk_bf16_f32 v22, v230, v231\n"
"v_cvt_pk_bf16_f32 v23, v232, v233\n"
"buffer_store_dwordx4 v[20:23], v207, s[28:31], s47 offen offset:2048 sc1\n"
"s_and_b32 s60, s55, 4294967292\n"
"s_mov_b32 s23, 9576448\n"
"s_lshl_b32 s62, s60, 6\n"
"v_and_b32 v215, 3, v248\n"
"v_lshlrev_b32 v215, 6, v215\n"
"v_add_u32 v215, s62, v215\n"
"v_min_u32 v251, 1, v253\n"
"v_lshlrev_b32 v251, 28, v251\n"
"s_lshl_b32 s62, s55, 6\n"
"v_add_u32 v251, s62, v251\n"
"v_mov_b32 v252, s85\n"
"v_accvgpr_read_b32 v226, a64\n"
"v_accvgpr_read_b32 v227, a65\n"
"v_accvgpr_read_b32 v228, a66\n"
"v_accvgpr_read_b32 v229, a67\n"
"v_accvgpr_read_b32 v230, a68\n"
"v_accvgpr_read_b32 v231, a69\n"
"v_accvgpr_read_b32 v232, a70\n"
"v_accvgpr_read_b32 v233, a71\n"
"v_pk_mul_f32 v[226:227], v[226:227], v[222:223] op_sel_hi:[1,0]\n"
"v_pk_mul_f32 v[228:229], v[228:229], v[222:223] op_sel_hi:[1,0]\n"
"v_pk_mul_f32 v[230:231], v[230:231], v[222:223] op_sel_hi:[1,0]\n"
"v_pk_mul_f32 v[232:233], v[232:233], v[222:223] op_sel_hi:[1,0]\n"
"v_cvt_pk_bf16_f32 v32, v226, v227\n"
"v_cvt_pk_bf16_f32 v33, v228, v229\n"
"v_cvt_pk_bf16_f32 v34, v230, v231\n"
"v_cvt_pk_bf16_f32 v35, v232, v233\n"
"buffer_store_dwordx4 v[32:35], v207, s[28:31], s52 offen offset:1024 sc1\n"
"s_add_u32 s61, s43, 1\n"
"s_and_b32 s61, s61, 3\n"
"s_add_u32 s61, s61, s60\n"
"s_lshl_b32 s61, s61, 2\n"
"s_add_u32 s61, s61, s41\n"
"s_lshl_b32 s61, s61, 14\n"
"s_add_u32 s20, s61, 9592832\n"
"v_accvgpr_read_b32 v226, a72\n"
"v_accvgpr_read_b32 v227, a73\n"
"v_accvgpr_read_b32 v228, a74\n"
"v_accvgpr_read_b32 v229, a75\n"
"v_accvgpr_read_b32 v230, a76\n"
"v_accvgpr_read_b32 v231, a77\n"
"v_accvgpr_read_b32 v232, a78\n"
"v_accvgpr_read_b32 v233, a79\n"
"v_pk_mul_f32 v[226:227], v[226:227], v[222:223] op_sel_hi:[1,0]\n"
"v_pk_mul_f32 v[228:229], v[228:229], v[222:223] op_sel_hi:[1,0]\n"
"v_pk_mul_f32 v[230:231], v[230:231], v[222:223] op_sel_hi:[1,0]\n"
"v_pk_mul_f32 v[232:233], v[232:233], v[222:223] op_sel_hi:[1,0]\n"
"v_cvt_pk_bf16_f32 v36, v226, v227\n"
"v_cvt_pk_bf16_f32 v37, v228, v229\n"
"v_cvt_pk_bf16_f32 v38, v230, v231\n"
"v_cvt_pk_bf16_f32 v39, v232, v233\n"
"buffer_store_dwordx4 v[36:39], v207, s[28:31], s52 offen offset:3072 sc1\n"
"s_add_u32 s61, s43, 2\n"
"s_and_b32 s61, s61, 3\n"
"s_add_u32 s61, s61, s60\n"
"s_lshl_b32 s61, s61, 2\n"
"s_add_u32 s61, s61, s41\n"
"s_lshl_b32 s61, s61, 14\n"
"s_add_u32 s21, s61, 9592832\n"
"v_accvgpr_read_b32 v226, a80\n"
"v_accvgpr_read_b32 v227, a81\n"
"v_accvgpr_read_b32 v228, a82\n"
"v_accvgpr_read_b32 v229, a83\n"
"v_accvgpr_read_b32 v230, a84\n"
"v_accvgpr_read_b32 v231, a85\n"
"v_accvgpr_read_b32 v232, a86\n"
"v_accvgpr_read_b32 v233, a87\n"
"v_pk_mul_f32 v[226:227], v[226:227], v[222:223] op_sel_hi:[1,0]\n"
"v_pk_mul_f32 v[228:229], v[228:229], v[222:223] op_sel_hi:[1,0]\n"
"v_pk_mul_f32 v[230:231], v[230:231], v[222:223] op_sel_hi:[1,0]\n"
"v_pk_mul_f32 v[232:233], v[232:233], v[222:223] op_sel_hi:[1,0]\n"
"v_cvt_pk_bf16_f32 v40, v226, v227\n"
"v_cvt_pk_bf16_f32 v41, v228, v229\n"
"v_cvt_pk_bf16_f32 v42, v230, v231\n"
"v_cvt_pk_bf16_f32 v43, v232, v233\n"
"buffer_store_dwordx4 v[40:43], v207, s[28:31], s46 offen offset:1024 sc1\n"
"s_add_u32 s61, s43, 3\n"
"s_and_b32 s61, s61, 3\n"
"s_add_u32 s61, s61, s60\n"
"s_lshl_b32 s61, s61, 2\n"
"s_add_u32 s61, s61, s41\n"
"s_lshl_b32 s61, s61, 14\n"
"s_add_u32 s22, s61, 9592832\n"
"v_accvgpr_read_b32 v226, a88\n"
"v_accvgpr_read_b32 v227, a89\n"
"v_accvgpr_read_b32 v228, a90\n"
"v_accvgpr_read_b32 v229, a91\n"
"v_accvgpr_read_b32 v230, a92\n"
"v_accvgpr_read_b32 v231, a93\n"
"v_accvgpr_read_b32 v232, a94\n"
"v_accvgpr_read_b32 v233, a95\n"
"v_pk_mul_f32 v[226:227], v[226:227], v[222:223] op_sel_hi:[1,0]\n"
"v_pk_mul_f32 v[228:229], v[228:229], v[222:223] op_sel_hi:[1,0]\n"
"v_pk_mul_f32 v[230:231], v[230:231], v[222:223] op_sel_hi:[1,0]\n"
"v_pk_mul_f32 v[232:233], v[232:233], v[222:223] op_sel_hi:[1,0]\n"
"v_cvt_pk_bf16_f32 v44, v226, v227\n"
"v_cvt_pk_bf16_f32 v45, v228, v229\n"
"v_cvt_pk_bf16_f32 v46, v230, v231\n"
"v_cvt_pk_bf16_f32 v47, v232, v233\n"
"buffer_store_dwordx4 v[44:47], v207, s[28:31], s46 offen offset:3072 sc1\n"
"s_waitcnt vmcnt(32) lgkmcnt(0)\n"
"s_barrier\n"
"ds_read_b32 v234, v209\n"
"v_accvgpr_read_b32 v226, a96\n"
"v_accvgpr_read_b32 v227, a97\n"
"v_accvgpr_read_b32 v228, a98\n"
"v_accvgpr_read_b32 v229, a99\n"
"v_accvgpr_read_b32 v230, a100\n"
"v_accvgpr_read_b32 v231, a101\n"
"v_accvgpr_read_b32 v232, a102\n"
"v_accvgpr_read_b32 v233, a103\n"
"v_pk_mul_f32 v[226:227], v[226:227], v[224:225] op_sel_hi:[1,0]\n"
"v_pk_mul_f32 v[228:229], v[228:229], v[224:225] op_sel_hi:[1,0]\n"
"v_pk_mul_f32 v[230:231], v[230:231], v[224:225] op_sel_hi:[1,0]\n"
"v_pk_mul_f32 v[232:233], v[232:233], v[224:225] op_sel_hi:[1,0]\n"
"v_cvt_pk_bf16_f32 v48, v226, v227\n"
"v_cvt_pk_bf16_f32 v49, v228, v229\n"
"v_cvt_pk_bf16_f32 v50, v230, v231\n"
"v_cvt_pk_bf16_f32 v51, v232, v233\n"
"buffer_store_dwordx4 v[48:51], v207, s[28:31], s47 offen offset:1024 sc1\n"
"v_accvgpr_read_b32 v226, a104\n"
"v_accvgpr_read_b32 v227, a105\n"
"v_accvgpr_read_b32 v228, a106\n"
"v_accvgpr_read_b32 v229, a107\n"
"v_accvgpr_read_b32 v230, a108\n"
"v_accvgpr_read_b32 v231, a109\n"
"v_accvgpr_read_b32 v232, a110\n"
"v_accvgpr_read_b32 v233, a111\n"
"v_pk_mul_f32 v[226:227], v[226:227], v[224:225] op_sel_hi:[1,0]\n"
"v_pk_mul_f32 v[228:229], v[228:229], v[224:225] op_sel_hi:[1,0]\n"
"v_pk_mul_f32 v[230:231], v[230:231], v[224:225] op_sel_hi:[1,0]\n"
"v_pk_mul_f32 v[232:233], v[232:233], v[224:225] op_sel_hi:[1,0]\n"
"v_cvt_pk_bf16_f32 v52, v226, v227\n"
"v_cvt_pk_bf16_f32 v53, v228, v229\n"
"v_cvt_pk_bf16_f32 v54, v230, v231\n"
"v_cvt_pk_bf16_f32 v55, v232, v233\n"
"buffer_store_dwordx4 v[52:55], v207, s[28:31], s47 offen offset:3072 sc1\n"
"v_accvgpr_read_b32 v226, a112\n"
"v_accvgpr_read_b32 v227, a113\n"
"v_accvgpr_read_b32 v228, a114\n"
"v_accvgpr_read_b32 v229, a115\n"
"v_accvgpr_read_b32 v230, a116\n"
"v_accvgpr_read_b32 v231, a117\n"
"v_accvgpr_read_b32 v232, a118\n"
"v_accvgpr_read_b32 v233, a119\n"
"v_pk_mul_f32 v[226:227], v[226:227], v[224:225] op_sel_hi:[1,0]\n"
"v_pk_mul_f32 v[228:229], v[228:229], v[224:225] op_sel_hi:[1,0]\n"
"v_pk_mul_f32 v[230:231], v[230:231], v[224:225] op_sel_hi:[1,0]\n"
"v_pk_mul_f32 v[232:233], v[232:233], v[224:225] op_sel_hi:[1,0]\n"
"v_cvt_pk_bf16_f32 v56, v226, v227\n"
"v_cvt_pk_bf16_f32 v57, v228, v229\n"
"v_cvt_pk_bf16_f32 v58, v230, v231\n"
"v_cvt_pk_bf16_f32 v59, v232, v233\n"
"buffer_store_dwordx4 v[56:59], v207, s[28:31], s48 offen offset:1024 sc1\n"
"v_accvgpr_read_b32 v226, a120\n"
"v_accvgpr_read_b32 v227, a121\n"
"v_accvgpr_read_b32 v228, a122\n"
"v_accvgpr_read_b32 v229, a123\n"
"v_accvgpr_read_b32 v230, a124\n"
"v_accvgpr_read_b32 v231, a125\n"
"v_accvgpr_read_b32 v232, a126\n"
"v_accvgpr_read_b32 v233, a127\n"
"v_pk_mul_f32 v[226:227], v[226:227], v[224:225] op_sel_hi:[1,0]\n"
"v_pk_mul_f32 v[228:229], v[228:229], v[224:225] op_sel_hi:[1,0]\n"
"v_pk_mul_f32 v[230:231], v[230:231], v[224:225] op_sel_hi:[1,0]\n"
"v_pk_mul_f32 v[232:233], v[232:233], v[224:225] op_sel_hi:[1,0]\n"
"v_cvt_pk_bf16_f32 v60, v226, v227\n"
"v_cvt_pk_bf16_f32 v61, v228, v229\n"
"v_cvt_pk_bf16_f32 v62, v230, v231\n"
"v_cvt_pk_bf16_f32 v63, v232, v233\n"
"buffer_store_dwordx4 v[60:63], v207, s[28:31], s48 offen offset:3072 sc1\n"
"s_waitcnt lgkmcnt(0)\n"
"v_cmp_ne_u32 vcc, 0, v234\n"
"s_nop 0\n"
"s_cbranch_vccnz AT_fb\n"
"v_mfma_f32_16x16x32_bf16 v[64:67], a[128:131], v[0:3], 0\n"
"ds_read_b64_tr_b16 a[200:201], v202 offset:9248\n"
"v_mfma_f32_16x16x32_bf16 v[68:71], a[128:131], v[32:35], 0\n"
"ds_read_b64_tr_b16 a[202:203], v202 offset:13856\n"
"v_mfma_f32_16x16x32_bf16 v[72:75], a[132:135], v[0:3], 0\n"
"s_mov_b32 s56, 41984\n"
"ds_read_b64_tr_b16 a[204:205], v202 offset:9280\n"
"v_mfma_f32_16x16x32_bf16 v[76:79], a[132:135], v[32:35], 0\n"
"s_mov_b32 s57, 46592\n"
"ds_read_b64_tr_b16 a[206:207], v202 offset:13888\n"
"v_mfma_f32_16x16x32_bf16 v[80:83], a[136:139], v[0:3], 0\n"
"s_mov_b32 s58, 51200\n"
"ds_read_b64_tr_b16 a[208:209], v202 offset:9312\n"
"v_mfma_f32_16x16x32_bf16 v[84:87], a[136:139], v[32:35], 0\n"
"s_mov_b32 s59, 55808\n"
"ds_read_b64_tr_b16 a[210:211], v202 offset:13920\n"
"v_mfma_f32_16x16x32_bf16 v[88:91], a[140:143], v[0:3], 0\n"
"ds_read_b64_tr_b16 a[212:213], v202 offset:9344\n"
"v_lshlrev_b32 v246, 2, v254\n"
"v_mfma_f32_16x16x32_bf16 v[92:95], a[140:143], v[32:35], 0\n"
"s_lshl_b32 s54, s44, 2\n"
"ds_read_b64_tr_b16 a[214:215], v202 offset:13952\n"
"v_mfma_f32_16x16x32_bf16 v[96:99], a[144:147], v[0:3], 0\n"
"s_add_u32 s54, s54, s41\n"
"ds_read_b64_tr_b16 a[216:217], v202 offset:9376\n"
"v_mfma_f32_16x16x32_bf16 v[100:103], a[144:147], v[32:35], 0\n"
"s_mul_i32 s54, s54, 36864\n"
"ds_read_b64_tr_b16 a[218:219], v202 offset:13984\n"
"v_mfma_f32_16x16x32_bf16 v[104:107], a[148:151], v[0:3], 0\n"
"s_lshr_b32 s62, s43, 2\n"
"ds_read_b64_tr_b16 a[220:221], v202 offset:9408\n"
"v_mfma_f32_16x16x32_bf16 v[108:111], a[148:151], v[32:35], 0\n"
"s_mul_i32 s62, s62, 2359296\n"
"ds_read_b64_tr_b16 a[222:223], v202 offset:14016\n"
"v_mfma_f32_16x16x32_bf16 v[112:115], a[152:155], v[0:3], 0\n"
"s_add_u32 s54, s54, s62\n"
"ds_read_b64_tr_b16 a[128:129], v202 offset:9440\n"
"v_mfma_f32_16x16x32_bf16 v[116:119], a[152:155], v[32:35], 0\n"
"s_add_u32 s54, s54, 139264\n"
"ds_read_b64_tr_b16 a[130:131], v202 offset:14048\n"
"v_mfma_f32_16x16x32_bf16 v[120:123], a[156:159], v[0:3], 0\n"
"s_bfe_u32 s62, s43, 0x10001\n"
"ds_read_b64_tr_b16 a[132:133], v205 offset:9216\n"
"v_mfma_f32_16x16x32_bf16 v[124:127], a[156:159], v[32:35], 0\n"
"s_mul_i32 s62, s62, 9216\n"
"ds_read_b64_tr_b16 a[134:135], v205 offset:13824\n"
"v_mfma_f32_16x16x32_bf16 v[226:229], a[192:195], v[0:3], 0\n"
"s_add_u32 s54, s54, s62\n"
"ds_read_b64_tr_b16 a[136:137], v202 offset:18432\n"
"v_mfma_f32_16x16x32_bf16 v[230:233], a[192:195], v[32:35], 0\n"
"s_and_b32 s62, s43, 1\n"
"ds_read_b64_tr_b16 a[138:139], v202 offset:23040\n"
"v_mfma_f32_16x16x32_bf16 v[64:67], a[196:199], v[4:7], v[64:67]\n"
"s_lshl_b32 s49, s62, 3\n"
"ds_read_b64_tr_b16 a[140:141], v202 offset:18464\n"
"v_mfma_f32_16x16x32_bf16 v[68:71], a[196:199], v[36:39], v[68:71]\n"
"s_waitcnt lgkmcnt(11)\n"
"s_add_u32 s49, s49, s54\n"
"ds_read_b64_tr_b16 a[142:143], v202 offset:23072\n"
"v_mfma_f32_16x16x32_bf16 v[72:75], a[200:203], v[4:7], v[72:75]\n"
"s_add_u32 s49, s49, 4096\n"
"ds_read_b64_tr_b16 a[144:145], v202 offset:18496\n"
"v_mfma_f32_16x16x32_bf16 v[76:79], a[200:203], v[36:39], v[76:79]\n"
"s_mul_i32 s62, s62, 5120\n"
"ds_read_b64_tr_b16 a[146:147], v202 offset:23104\n"
"v_mfma_f32_16x16x32_bf16 v[80:83], a[204:207], v[4:7], v[80:83]\n"
"s_add_u32 s54, s54, s62\n"
"ds_read_b64_tr_b16 a[148:149], v202 offset:18528\n"
"v_mfma_f32_16x16x32_bf16 v[84:87], a[204:207], v[36:39], v[84:87]\n"
"s_mul_i32 s46, s55, 9216\n"
"ds_read_b64_tr_b16 a[150:151], v202 offset:23136\n"
"v_mfma_f32_16x16x32_bf16 v[88:91], a[208:211], v[4:7], v[88:91]\n"
"s_min_u32 s62, s41, 2\n"
"ds_read_b64_tr_b16 a[152:153], v202 offset:18560\n"
"v_mfma_f32_16x16x32_bf16 v[92:95], a[208:211], v[36:39], v[92:95]\n"
"s_mul_i32 s62, s62, 3072\n"
"ds_read_b64_tr_b16 a[154:155], v202 offset:23168\n"
"v_mfma_f32_16x16x32_bf16 v[96:99], a[212:215], v[4:7], v[96:99]\n"
"s_add_u32 s46, s46, s62\n"
"ds_read_b64_tr_b16 a[156:157], v202 offset:18592\n"
"v_mfma_f32_16x16x32_bf16 v[100:103], a[212:215], v[36:39], v[100:103]\n"
"s_waitcnt lgkmcnt(11)\n"
"s_add_u32 s45, s40, s62\n"
"ds_read_b64_tr_b16 a[158:159], v202 offset:23200\n"
"v_mfma_f32_16x16x32_bf16 v[104:107], a[216:219], v[4:7], v[104:107]\n"
"s_lshr_b32 s47, s55, 1\n"
"ds_read_b64_tr_b16 a[192:193], v202 offset:18624\n"
"v_mfma_f32_16x16x32_bf16 v[108:111], a[216:219], v[36:39], v[108:111]\n"
"s_mul_i32 s47, s47, 18432\n"
"ds_read_b64_tr_b16 a[194:195], v202 offset:23232\n"
"v_mfma_f32_16x16x32_bf16 v[112:115], a[220:223], v[4:7], v[112:115]\n"
"s_and_b32 s62, s55, 1\n"
"ds_read_b64_tr_b16 a[196:197], v202 offset:18656\n"
"v_mfma_f32_16x16x32_bf16 v[116:119], a[220:223], v[36:39], v[116:119]\n"
"s_lshl_b32 s62, s62, 7\n"
"ds_read_b64_tr_b16 a[198:199], v202 offset:23264\n"
"v_mfma_f32_16x16x32_bf16 v[120:123], a[128:131], v[4:7], v[120:123]\n"
"s_add_u32 s47, s47, s62\n"
"ds_read_b64_tr_b16 a[200:201], v205 offset:18432\n"
"v_mfma_f32_16x16x32_bf16 v[124:127], a[128:131], v[36:39], v[124:127]\n"
"s_add_u32 s47, s47, 139264\n"
"ds_read_b64_tr_b16 a[202:203], v205 offset:23040\n"
"v_mfma_f32_16x16x32_bf16 v[226:229], a[132:135], v[4:7], v[226:229]\n"
"s_mul_i32 s48, s55, 73728\n"
"ds_read_b64_tr_b16 a[204:205], v200 offset:0\n"
"v_mfma_f32_16x16x32_bf16 v[230:233], a[132:135], v[36:39], v[230:233]\n"
"s_waitcnt lgkmcnt(11)\n"
"s_add_u32 s48, s48, 37888000\n"
"ds_read_b64_tr_b16 a[206:207], v200 offset:4608\n"
"v_mfma_f32_16x16x32_bf16 v[64:67], a[136:139], v[8:11], v[64:67]\n"
"s_add_u32 s62, s40, 119808\n"
"ds_read_b64_tr_b16 a[208:209], v200 offset:32\n"
"v_mfma_f32_16x16x32_bf16 v[68:71], a[136:139], v[40:43], v[68:71]\n"
"ds_read_b64_tr_b16 a[210:211], v200 offset:4640\n"
"v_and_b32 v206, 7, v248\n"
"v_mfma_f32_16x16x32_bf16 v[72:75], a[140:143], v[8:11], v[72:75]\n"
"ds_read_b64_tr_b16 a[212:213], v200 offset:64\n"
"v_lshrrev_b32 v255, 5, v253\n"
"v_mfma_f32_16x16x32_bf16 v[76:79], a[140:143], v[40:43], v[76:79]\n"
"ds_read_b64_tr_b16 a[214:215], v200 offset:4672\n"
"v_bfe_u32 v216, v253, 3, 2\n"
"v_mfma_f32_16x16x32_bf16 v[80:83], a[144:147], v[8:11], v[80:83]\n"
"ds_read_b64_tr_b16 a[216:217], v200 offset:96\n"
"v_lshlrev_b32 v241, 10, v255\n"
"v_mfma_f32_16x16x32_bf16 v[84:87], a[144:147], v[40:43], v[84:87]\n"
"ds_read_b64_tr_b16 a[218:219], v200 offset:4704\n"
"v_lshl_add_u32 v241, v216, 8, v241\n"
"v_mfma_f32_16x16x32_bf16 v[88:91], a[148:151], v[8:11], v[88:91]\n"
"ds_read_b64_tr_b16 a[220:221], v200 offset:128\n"
"v_lshl_add_u32 v241, v206, 4, v241\n"
"v_mfma_f32_16x16x32_bf16 v[92:95], a[148:151], v[40:43], v[92:95]\n"
"s_waitcnt lgkmcnt(11)\n"
"ds_read_b64_tr_b16 a[222:223], v200 offset:4736\n"
"v_add_u32 v242, 6144, v241\n"
"v_mfma_f32_16x16x32_bf16 v[96:99], a[152:155], v[8:11], v[96:99]\n"
"ds_read_b64_tr_b16 a[128:129], v200 offset:160\n"
"v_add_u32 v243, 12288, v241\n"
"v_mfma_f32_16x16x32_bf16 v[100:103], a[152:155], v[40:43], v[100:103]\n"
"ds_read_b64_tr_b16 a[130:131], v200 offset:4768\n"
"v_lshlrev_b32 v255, 5, v255\n"
"v_mfma_f32_16x16x32_bf16 v[104:107], a[156:159], v[8:11], v[104:107]\n"
"ds_read_b64_tr_b16 a[132:133], v200 offset:192\n"
"v_lshl_add_u32 v255, v216, 2, v255\n"
"v_mfma_f32_16x16x32_bf16 v[108:111], a[156:159], v[40:43], v[108:111]\n"
"ds_read_b64_tr_b16 a[134:135], v200 offset:4800\n"
"v_lshl_add_u32 v244, v255, 2, s62\n"
"v_mfma_f32_16x16x32_bf16 v[112:115], a[192:195], v[8:11], v[112:115]\n"
"ds_read_b64_tr_b16 a[136:137], v200 offset:224\n"
"v_mul_u32_u24 v206, 1152, v206\n"
"v_mfma_f32_16x16x32_bf16 v[116:119], a[192:195], v[40:43], v[116:119]\n"
"ds_read_b64_tr_b16 a[138:139], v200 offset:4832\n"
"v_lshl_add_u32 v240, v255, 1, v206\n"
"v_mfma_f32_16x16x32_bf16 v[120:123], a[196:199], v[8:11], v[120:123]\n"
"ds_read_b64_tr_b16 a[140:141], v203 offset:0\n"
"v_add_u32 v240, s40, v240\n"
"v_mfma_f32_16x16x32_bf16 v[124:127], a[196:199], v[40:43], v[124:127]\n"
"s_waitcnt lgkmcnt(11)\n"
"ds_read_b64_tr_b16 a[142:143], v203 offset:4608\n"
"v_add_u32 v245, v206, v255\n"
"v_mfma_f32_16x16x32_bf16 v[226:229], a[200:203], v[8:11], v[226:229]\n"
"ds_read_b64_tr_b16 a[144:145], v200 offset:9216\n"
"v_lshlrev_b32 v245, 3, v245\n"
"v_mfma_f32_16x16x32_bf16 v[230:233], a[200:203], v[40:43], v[230:233]\n"
"ds_read_b64_tr_b16 a[146:147], v200 offset:13824\n"
"v_lshl_add_u32 v247, v253, 4, s62\n"
"v_mfma_f32_16x16x32_bf16 v[64:67], a[204:207], v[12:15], v[64:67]\n"
"s_mul_i32 s62, s41, 7\n"
"ds_read_b64_tr_b16 a[148:149], v200 offset:9248\n"
"v_mfma_f32_16x16x32_bf16 v[68:71], a[204:207], v[44:47], v[68:71]\n"
"s_min_u32 s62, s62, 20\n"
"ds_read_b64_tr_b16 a[150:151], v200 offset:13856\n"
"v_mfma_f32_16x16x32_bf16 v[72:75], a[208:211], v[12:15], v[72:75]\n"
"s_lshl_b32 s62, s62, 10\n"
"ds_read_b64_tr_b16 a[152:153], v200 offset:9280\n"
"v_mfma_f32_16x16x32_bf16 v[76:79], a[208:211], v[44:47], v[76:79]\n"
"s_add_u32 s50, s50, s62\n"
"ds_read_b64_tr_b16 a[154:155], v200 offset:13888\n"
"v_mfma_f32_16x16x32_bf16 v[80:83], a[212:215], v[12:15], v[80:83]\n"
"s_add_u32 s84, s40, s62\n"
"ds_read_b64_tr_b16 a[156:157], v200 offset:9312\n"
"v_mfma_f32_16x16x32_bf16 v[84:87], a[212:215], v[44:47], v[84:87]\n"
"s_waitcnt lgkmcnt(11)\n"
"ds_read_b64_tr_b16 a[158:159], v200 offset:13920\n"
"v_mfma_f32_16x16x32_bf16 v[88:91], a[216:219], v[12:15], v[88:91]\n"
"ds_read_b64_tr_b16 a[192:193], v200 offset:9344\n"
"v_mfma_f32_16x16x32_bf16 v[92:95], a[216:219], v[44:47], v[92:95]\n"
"ds_read_b64_tr_b16 a[194:195], v200 offset:13952\n"
"v_mfma_f32_16x16x32_bf16 v[96:99], a[220:223], v[12:15], v[96:99]\n"
"ds_read_b64_tr_b16 a[196:197], v200 offset:9376\n"
"v_mfma_f32_16x16x32_bf16 v[100:103], a[220:223], v[44:47], v[100:103]\n"
"ds_read_b64_tr_b16 a[198:199], v200 offset:13984\n"
"v_mfma_f32_16x16x32_bf16 v[104:107], a[128:131], v[12:15], v[104:107]\n"
"ds_read_b64_tr_b16 a[200:201], v200 offset:9408\n"
"v_mfma_f32_16x16x32_bf16 v[108:111], a[128:131], v[44:47], v[108:111]\n"
"ds_read_b64_tr_b16 a[202:203], v200 offset:14016\n"
"v_mfma_f32_16x16x32_bf16 v[112:115], a[132:135], v[12:15], v[112:115]\n"
"ds_read_b64_tr_b16 a[204:205], v200 offset:9440\n"
"v_mfma_f32_16x16x32_bf16 v[116:119], a[132:135], v[44:47], v[116:119]\n"
"s_waitcnt lgkmcnt(11)\n"
"ds_read_b64_tr_b16 a[206:207], v200 offset:14048\n"
"v_mfma_f32_16x16x32_bf16 v[120:123], a[136:139], v[12:15], v[120:123]\n"
"ds_read_b64_tr_b16 a[208:209], v203 offset:9216\n"
"v_mfma_f32_16x16x32_bf16 v[124:127], a[136:139], v[44:47], v[124:127]\n"
"ds_read_b64_tr_b16 a[210:211], v203 offset:13824\n"
"v_mfma_f32_16x16x32_bf16 v[226:229], a[140:143], v[12:15], v[226:229]\n"
"ds_read_b64_tr_b16 a[212:213], v200 offset:18432\n"
"v_mfma_f32_16x16x32_bf16 v[230:233], a[140:143], v[44:47], v[230:233]\n"
"ds_read_b64_tr_b16 a[214:215], v200 offset:23040\n"
"v_mfma_f32_16x16x32_bf16 v[64:67], a[144:147], v[16:19], v[64:67]\n"
"AT_pg14:\n"
"s_cbranch_execz AT_hr\n"
"ds_read_b64_tr_b16 a[216:217], v200 offset:18464\n"
"v_mfma_f32_16x16x32_bf16 v[68:71], a[144:147], v[48:51], v[68:71]\n"
"s_waitcnt vmcnt(0)\n"
"ds_read_b64_tr_b16 a[218:219], v200 offset:23072\n"
"v_mfma_f32_16x16x32_bf16 v[72:75], a[148:151], v[16:19], v[72:75]\n"
"s_barrier\n"
"ds_read_b64_tr_b16 a[220:221], v200 offset:18496\n"
"buffer_store_dword v252, v251, s[28:31], s23 offen sc1\n"
"v_mfma_f32_16x16x32_bf16 v[76:79], a[148:151], v[48:51], v[76:79]\n"
"s_waitcnt lgkmcnt(11)\n"
"ds_read_b64_tr_b16 a[222:223], v200 offset:23104\n"
"buffer_load_dwordx4 a[160:163], v246, s[28:31], s56 offen offset:0 sc1\n"
"v_mfma_f32_16x16x32_bf16 v[80:83], a[152:155], v[16:19], v[80:83]\n"
"s_mov_b32 m0, s51\n"
"ds_read_b64_tr_b16 a[128:129], v200 offset:18528\n"
"v_mfma_f32_16x16x32_bf16 v[84:87], a[152:155], v[48:51], v[84:87]\n"
"ds_read_b64_tr_b16 a[130:131], v200 offset:23136\n"
"buffer_load_dwordx4 v210, s[24:27], s36 offen offset:0 lds\n"
"v_mfma_f32_16x16x32_bf16 v[88:91], a[156:159], v[16:19], v[88:91]\n"
"ds_read_b64_tr_b16 a[132:133], v200 offset:18560\n"
"v_mfma_f32_16x16x32_bf16 v[92:95], a[156:159], v[48:51], v[92:95]\n"
"ds_read_b64_tr_b16 a[134:135], v200 offset:23168\n"
"v_mfma_f32_16x16x32_bf16 v[96:99], a[192:195], v[16:19], v[96:99]\n"
"ds_read_b64_tr_b16 a[136:137], v200 offset:18592\n"
"v_mfma_f32_16x16x32_bf16 v[100:103], a[192:195], v[48:51], v[100:103]\n"
"ds_read_b64_tr_b16 a[138:139], v200 offset:23200\n"
"buffer_load_dwordx4 a[164:167], v246, s[28:31], s56 offen offset:16 sc1\n"
"v_mfma_f32_16x16x32_bf16 v[104:107], a[196:199], v[16:19], v[104:107]\n"
"ds_read_b64_tr_b16 a[140:141], v200 offset:18624\n"
"v_mfma_f32_16x16x32_bf16 v[108:111], a[196:199], v[48:51], v[108:111]\n"
"s_waitcnt lgkmcnt(11)\n"
"s_mov_b32 m0, s51\n"
"ds_read_b64_tr_b16 a[142:143], v200 offset:23232\n"
"v_mfma_f32_16x16x32_bf16 v[112:115], a[200:203], v[16:19], v[112:115]\n"
"ds_read_b64_tr_b16 a[144:145], v200 offset:18656\n"
"buffer_load_dwordx4 v211, s[24:27], s36 offen offset:1024 lds\n"
"v_mfma_f32_16x16x32_bf16 v[116:119], a[200:203], v[48:51], v[116:119]\n"
"ds_read_b64_tr_b16 a[146:147], v200 offset:23264\n"
"v_mfma_f32_16x16x32_bf16 v[120:123], a[204:207], v[16:19], v[120:123]\n"
"ds_read_b64_tr_b16 a[148:149], v203 offset:18432\n"
"v_mfma_f32_16x16x32_bf16 v[124:127], a[204:207], v[48:51], v[124:127]\n"
"ds_read_b64_tr_b16 a[150:151], v203 offset:23040\n"
"buffer_load_dwordx4 a[168:171], v246, s[28:31], s57 offen offset:0 sc1\n"
"v_mfma_f32_16x16x32_bf16 v[226:229], a[208:211], v[16:19], v[226:229]\n"
"ds_read_b64_tr_b16 a[152:153], v200 offset:27648\n"
"v_mfma_f32_16x16x32_bf16 v[230:233], a[208:211], v[48:51], v[230:233]\n"
"ds_read_b64_tr_b16 a[154:155], v200 offset:32256\n"
"v_mfma_f32_16x16x32_bf16 v[64:67], a[212:215], v[20:23], v[64:67]\n"
"s_mov_b32 m0, s51\n"
"ds_read_b64_tr_b16 a[156:157], v200 offset:27680\n"
"v_mfma_f32_16x16x32_bf16 v[68:71], a[212:215], v[52:55], v[68:71]\n"
"s_waitcnt lgkmcnt(11)\n"
"ds_read_b64_tr_b16 a[158:159], v200 offset:32288\n"
"buffer_load_dwordx4 v212, s[24:27], s36 offen offset:2048 lds\n"
"v_mfma_f32_16x16x32_bf16 v[72:75], a[216:219], v[20:23], v[72:75]\n"
"ds_read_b64_tr_b16 a[192:193], v200 offset:27712\n"
"v_mfma_f32_16x16x32_bf16 v[76:79], a[216:219], v[52:55], v[76:79]\n"
"ds_read_b64_tr_b16 a[194:195], v200 offset:32320\n"
"buffer_load_dwordx4 a[172:175], v246, s[28:31], s57 offen offset:16 sc1\n"
"v_mfma_f32_16x16x32_bf16 v[80:83], a[220:223], v[20:23], v[80:83]\n"
"ds_read_b64_tr_b16 a[196:197], v200 offset:27744\n"
"v_mfma_f32_16x16x32_bf16 v[84:87], a[220:223], v[52:55], v[84:87]\n"
"ds_read_b64_tr_b16 a[198:199], v200 offset:32352\n"
"v_mfma_f32_16x16x32_bf16 v[88:91], a[128:131], v[20:23], v[88:91]\n"
"ds_read_b64_tr_b16 a[200:201], v200 offset:27776\n"
"v_mfma_f32_16x16x32_bf16 v[92:95], a[128:131], v[52:55], v[92:95]\n"
"s_mov_b32 m0, s51\n"
"ds_read_b64_tr_b16 a[202:203], v200 offset:32384\n"
"v_mfma_f32_16x16x32_bf16 v[96:99], a[132:135], v[20:23], v[96:99]\n"
"ds_read_b64_tr_b16 a[204:205], v200 offset:27808\n"
"buffer_load_dwordx4 v213, s[24:27], s36 offen offset:3072 lds\n"
"v_mfma_f32_16x16x32_bf16 v[100:103], a[132:135], v[52:55], v[100:103]\n"
"s_waitcnt lgkmcnt(11)\n"
"ds_read_b64_tr_b16 a[206:207], v200 offset:32416\n"
"buffer_load_dwordx4 a[176:179], v246, s[28:31], s58 offen offset:0 sc1\n"
"v_mfma_f32_16x16x32_bf16 v[104:107], a[136:139], v[20:23], v[104:107]\n"
"ds_read_b64_tr_b16 a[208:209], v200 offset:27840\n"
"v_mfma_f32_16x16x32_bf16 v[108:111], a[136:139], v[52:55], v[108:111]\n"
"ds_read_b64_tr_b16 a[210:211], v200 offset:32448\n"
"v_mfma_f32_16x16x32_bf16 v[112:115], a[140:143], v[20:23], v[112:115]\n"
"ds_read_b64_tr_b16 a[212:213], v200 offset:27872\n"
"v_mfma_f32_16x16x32_bf16 v[116:119], a[140:143], v[52:55], v[116:119]\n"
"ds_read_b64_tr_b16 a[214:215], v200 offset:32480\n"
"v_mfma_f32_16x16x32_bf16 v[120:123], a[144:147], v[20:23], v[120:123]\n"
"s_add_u32 m0, s51, 4096\n"
"ds_read_b64_tr_b16 a[216:217], v203 offset:27648\n"
"v_mfma_f32_16x16x32_bf16 v[124:127], a[144:147], v[52:55], v[124:127]\n"
"ds_read_b64_tr_b16 a[218:219], v203 offset:32256\n"
"buffer_load_dwordx4 v214, s[24:27], s36 offen offset:0 lds\n"
"v_mfma_f32_16x16x32_bf16 v[226:229], a[148:151], v[20:23], v[226:229]\n"
"ds_read_b64_tr_b16 a[220:221], v200 offset:36864\n"
"buffer_load_dwordx4 a[180:183], v246, s[28:31], s58 offen offset:16 sc1\n"
"v_mfma_f32_16x16x32_bf16 v[230:233], a[148:151], v[52:55], v[230:233]\n"
"s_waitcnt lgkmcnt(11)\n"
"ds_read_b64_tr_b16 a[222:223], v200 offset:41472\n"
"v_mfma_f32_16x16x32_bf16 v[64:67], a[152:155], v[24:27], v[64:67]\n"
"ds_read_b64_tr_b16 a[128:129], v200 offset:36896\n"
"v_mfma_f32_16x16x32_bf16 v[68:71], a[152:155], v[56:59], v[68:71]\n"
"ds_read_b64_tr_b16 a[130:131], v200 offset:41504\n"
"v_mfma_f32_16x16x32_bf16 v[72:75], a[156:159], v[24:27], v[72:75]\n"
"ds_read_b64_tr_b16 a[132:133], v200 offset:36928\n"
"v_mfma_f32_16x16x32_bf16 v[76:79], a[156:159], v[56:59], v[76:79]\n"
"s_add_u32 m0, s51, 4096\n"
"ds_read_b64_tr_b16 a[134:135], v200 offset:41536\n"
"v_mfma_f32_16x16x32_bf16 v[80:83], a[192:195], v[24:27], v[80:83]\n"
"ds_read_b64_tr_b16 a[136:137], v200 offset:36960\n"
"buffer_load_dwordx4 v236, s[24:27], s36 offen offset:1024 lds\n"
"v_mfma_f32_16x16x32_bf16 v[84:87], a[192:195], v[56:59], v[84:87]\n"
"ds_read_b64_tr_b16 a[138:139], v200 offset:41568\n"
"buffer_load_dwordx4 a[184:187], v246, s[28:31], s59 offen offset:0 sc1\n"
"v_mfma_f32_16x16x32_bf16 v[88:91], a[196:199], v[24:27], v[88:91]\n"
"ds_read_b64_tr_b16 a[140:141], v200 offset:36992\n"
"v_mfma_f32_16x16x32_bf16 v[92:95], a[196:199], v[56:59], v[92:95]\n"
"s_waitcnt lgkmcnt(11)\n"
"ds_read_b64_tr_b16 a[142:143], v200 offset:41600\n"
"v_mfma_f32_16x16x32_bf16 v[96:99], a[200:203], v[24:27], v[96:99]\n"
"ds_read_b64_tr_b16 a[144:145], v200 offset:37024\n"
"v_mfma_f32_16x16x32_bf16 v[100:103], a[200:203], v[56:59], v[100:103]\n"
"ds_read_b64_tr_b16 a[146:147], v200 offset:41632\n"
"v_mfma_f32_16x16x32_bf16 v[104:107], a[204:207], v[24:27], v[104:107]\n"
"s_add_u32 m0, s51, 4096\n"
"ds_read_b64_tr_b16 a[148:149], v200 offset:37056\n"
"v_mfma_f32_16x16x32_bf16 v[108:111], a[204:207], v[56:59], v[108:111]\n"
"ds_read_b64_tr_b16 a[150:151], v200 offset:41664\n"
"buffer_load_dwordx4 v237, s[24:27], s36 offen offset:2048 lds\n"
"v_mfma_f32_16x16x32_bf16 v[112:115], a[208:211], v[24:27], v[112:115]\n"
"ds_read_b64_tr_b16 a[152:153], v200 offset:37088\n"
"buffer_load_dwordx4 a[188:191], v246, s[28:31], s59 offen offset:16 sc1\n"
"v_mfma_f32_16x16x32_bf16 v[116:119], a[208:211], v[56:59], v[116:119]\n"
"ds_read_b64_tr_b16 a[154:155], v200 offset:41696\n"
"v_mfma_f32_16x16x32_bf16 v[120:123], a[212:215], v[24:27], v[120:123]\n"
"ds_read_b64_tr_b16 a[156:157], v203 offset:36864\n"
"v_mfma_f32_16x16x32_bf16 v[124:127], a[212:215], v[56:59], v[124:127]\n"
"s_waitcnt lgkmcnt(11)\n"
"ds_read_b64_tr_b16 a[158:159], v203 offset:41472\n"
"v_mfma_f32_16x16x32_bf16 v[226:229], a[216:219], v[24:27], v[226:229]\n"
"ds_read_b64_tr_b16 a[192:193], v200 offset:46080\n"
"v_mfma_f32_16x16x32_bf16 v[230:233], a[216:219], v[56:59], v[230:233]\n"
"s_add_u32 m0, s51, 4096\n"
"ds_read_b64_tr_b16 a[194:195], v200 offset:50688\n"
"v_mfma_f32_16x16x32_bf16 v[64:67], a[220:223], v[28:31], v[64:67]\n"
"ds_read_b64_tr_b16 a[196:197], v200 offset:46112\n"
"buffer_load_dwordx4 v238, s[24:27], s36 offen offset:3072 lds\n"
"v_mfma_f32_16x16x32_bf16 v[68:71], a[220:223], v[60:63], v[68:71]\n"
"ds_read_b64_tr_b16 a[198:199], v200 offset:50720\n"
"buffer_load_dwordx2 a[224:225], v254, s[80:83], 0 offen\n"
"v_mfma_f32_16x16x32_bf16 v[72:75], a[128:131], v[28:31], v[72:75]\n"
"ds_read_b64_tr_b16 a[200:201], v200 offset:46144\n"
"v_mfma_f32_16x16x32_bf16 v[76:79], a[128:131], v[60:63], v[76:79]\n"
"ds_read_b64_tr_b16 a[202:203], v200 offset:50752\n"
"v_mfma_f32_16x16x32_bf16 v[80:83], a[132:135], v[28:31], v[80:83]\n"
"ds_read_b64_tr_b16 a[204:205], v200 offset:46176\n"
"v_mfma_f32_16x16x32_bf16 v[84:87], a[132:135], v[60:63], v[84:87]\n"
"s_waitcnt lgkmcnt(11)\n"
"ds_read_b64_tr_b16 a[206:207], v200 offset:50784\n"
"v_mfma_f32_16x16x32_bf16 v[88:91], a[136:139], v[28:31], v[88:91]\n"
"s_add_u32 m0, s51, 8192\n"
"ds_read_b64_tr_b16 a[208:209], v200 offset:46208\n"
"v_mfma_f32_16x16x32_bf16 v[92:95], a[136:139], v[60:63], v[92:95]\n"
"ds_read_b64_tr_b16 a[210:211], v200 offset:50816\n"
"buffer_load_dwordx4 v239, s[24:27], s36 offen offset:0 lds\n"
"v_mfma_f32_16x16x32_bf16 v[96:99], a[140:143], v[28:31], v[96:99]\n"
"ds_read_b64_tr_b16 a[212:213], v200 offset:46240\n"
"v_mfma_f32_16x16x32_bf16 v[100:103], a[140:143], v[60:63], v[100:103]\n"
"ds_read_b64_tr_b16 a[214:215], v200 offset:50848\n"
"v_mfma_f32_16x16x32_bf16 v[104:107], a[144:147], v[28:31], v[104:107]\n"
"ds_read_b64_tr_b16 a[216:217], v200 offset:46272\n"
"v_mfma_f32_16x16x32_bf16 v[108:111], a[144:147], v[60:63], v[108:111]\n"
"ds_read_b64_tr_b16 a[218:219], v200 offset:50880\n"
"v_mfma_f32_16x16x32_bf16 v[112:115], a[148:151], v[28:31], v[112:115]\n"
"ds_read_b64_tr_b16 a[220:221], v200 offset:46304\n"
"v_mfma_f32_16x16x32_bf16 v[116:119], a[148:151], v[60:63], v[116:119]\n"
"s_waitcnt lgkmcnt(11)\n"
"ds_read_b64_tr_b16 a[222:223], v200 offset:50912\n"
"v_mfma_f32_16x16x32_bf16 v[120:123], a[152:155], v[28:31], v[120:123]\n"
"ds_read_b64_tr_b16 a[128:129], v203 offset:46080\n"
"buffer_load_dword v249, v215, s[28:31], s23 offen sc1\n"
"v_mfma_f32_16x16x32_bf16 v[124:127], a[152:155], v[60:63], v[124:127]\n"
"ds_read_b64_tr_b16 a[130:131], v203 offset:50688\n"
"v_mfma_f32_16x16x32_bf16 v[226:229], a[156:159], v[28:31], v[226:229]\n"
"ds_read_b64_tr_b16 a[132:133], v200 offset:55296\n"
"v_mfma_f32_16x16x32_bf16 v[230:233], a[156:159], v[60:63], v[230:233]\n"
"ds_read_b64_tr_b16 a[134:135], v200 offset:59904\n"
"s_barrier\n"
"s_waitcnt vmcnt(2)\n"
"s_mov_b32 s87, 0x80000\n"
"v_accvgpr_read_b32 v26, a160\n"
"v_accvgpr_read_b32 v27, a161\n"
"v_accvgpr_read_b32 v28, a162\n"
"v_accvgpr_read_b32 v29, a163\n"
"v_accvgpr_read_b32 v30, a164\n"
"v_accvgpr_read_b32 v31, a165\n"
"v_accvgpr_read_b32 v32, a166\n"
"v_accvgpr_read_b32 v33, a167\n"
"v_accvgpr_read_b32 v34, a168\n"
"v_accvgpr_read_b32 v35, a169\n"
"v_accvgpr_read_b32 v36, a170\n"
"v_accvgpr_read_b32 v37, a171\n"
"v_accvgpr_read_b32 v38, a172\n"
"v_accvgpr_read_b32 v39, a173\n"
"v_accvgpr_read_b32 v40, a174\n"
"v_accvgpr_read_b32 v41, a175\n"
"v_accvgpr_read_b32 v42, a176\n"
"v_accvgpr_read_b32 v43, a177\n"
"v_accvgpr_read_b32 v44, a178\n"
"v_accvgpr_read_b32 v45, a179\n"
"v_accvgpr_read_b32 v46, a180\n"
"v_accvgpr_read_b32 v47, a181\n"
"v_accvgpr_read_b32 v48, a182\n"
"v_accvgpr_read_b32 v49, a183\n"
"v_accvgpr_read_b32 v50, a184\n"
"v_accvgpr_read_b32 v51, a185\n"
"v_accvgpr_read_b32 v52, a186\n"
"v_accvgpr_read_b32 v53, a187\n"
"v_accvgpr_read_b32 v54, a188\n"
"v_accvgpr_read_b32 v55, a189\n"
"v_accvgpr_read_b32 v56, a190\n"
"v_accvgpr_read_b32 v57, a191\n"
"v_accvgpr_read_b32 v58, a224\n"
"v_accvgpr_read_b32 v59, a225\n"
"AT_tchk:\n"
"v_xor_b32 v24, s85, v27\n"
"v_xor_b32 v25, s85, v29\n"
"v_bitop3_b32 v24, v31, s85, v24 bitop3:0xbe\n"
"v_bitop3_b32 v25, v33, s85, v25 bitop3:0xbe\n"
"v_bitop3_b32 v24, v35, s85, v24 bitop3:0xbe\n"
"v_bitop3_b32 v25, v37, s85, v25 bitop3:0xbe\n"
"v_bitop3_b32 v24, v39, s85, v24 bitop3:0xbe\n"
"v_bitop3_b32 v25, v41, s85, v25 bitop3:0xbe\n"
"v_bitop3_b32 v24, v43, s85, v24 bitop3:0xbe\n"
"v_bitop3_b32 v25, v45, s85, v25 bitop3:0xbe\n"
"v_bitop3_b32 v24, v47, s85, v24 bitop3:0xbe\n"
"v_bitop3_b32 v25, v49, s85, v25 bitop3:0xbe\n"
"v_bitop3_b32 v24, v51, s85, v24 bitop3:0xbe\n"
"v_bitop3_b32 v25, v53, s85, v25 bitop3:0xbe\n"
"v_bitop3_b32 v24, v55, s85, v24 bitop3:0xbe\n"
"v_bitop3_b32 v25, v57, s85, v25 bitop3:0xbe\n"
"v_or_b32 v24, v24, v25\n"
"v_cmp_ne_u32 s[62:63], 0, v24\n"
"v_cmp_gt_u32 vcc, 144, v253\n"
"s_nop 1\n"
"s_and_b64 s[62:63], vcc, s[62:63]\n"
"s_cmp_eq_u64 s[62:63], 0\n"
"s_cbranch_scc1 AT_tok\n"
"s_sub_u32 s87, s87, 1\n"
"s_cmp_eq_u32 s87, 0\n"
"s_cbranch_scc1 AT_tok\n"
"s_sleep 2\n"
"buffer_load_dwordx4 v[26:29], v246, s[28:31], s56 offen offset:0 sc1\n"
"buffer_load_dwordx4 v[30:33], v246, s[28:31], s56 offen offset:16 sc1\n"
"buffer_load_dwordx4 v[34:37], v246, s[28:31], s57 offen offset:0 sc1\n"
"buffer_load_dwordx4 v[38:41], v246, s[28:31], s57 offen offset:16 sc1\n"
"buffer_load_dwordx4 v[42:45], v246, s[28:31], s58 offen offset:0 sc1\n"
"buffer_load_dwordx4 v[46:49], v246, s[28:31], s58 offen offset:16 sc1\n"
"buffer_load_dwordx4 v[50:53], v246, s[28:31], s59 offen offset:0 sc1\n"
"buffer_load_dwordx4 v[54:57], v246, s[28:31], s59 offen offset:16 sc1\n"
"s_waitcnt vmcnt(0)\n"
"s_branch AT_tchk\n"
"AT_tok:\n"
"s_waitcnt vmcnt(0)\n"
"v_cmp_ne_u32 vcc, s85, v249\n"
"s_nop 0\n"
"s_cbranch_vccnz AT_x1\n"
"AT_x2:\n"
"buffer_load_dwordx4 a[0:3], v207, s[28:31], s20 offen offset:0 sc1\n"
"buffer_load_dwordx4 a[4:7], v207, s[28:31], s20 offen offset:1024 sc1\n"
"buffer_load_dwordx4 a[8:11], v207, s[28:31], s21 offen offset:0 sc1\n"
"buffer_load_dwordx4 a[12:15], v207, s[28:31], s21 offen offset:1024 sc1\n"
"buffer_load_dwordx4 a[16:19], v207, s[28:31], s22 offen offset:0 sc1\n"
"buffer_load_dwordx4 a[20:23], v207, s[28:31], s22 offen offset:1024 sc1\n"
"buffer_load_dwordx4 a[24:27], v207, s[28:31], s20 offen offset:2048 sc1\n"
"buffer_load_dwordx4 a[28:31], v207, s[28:31], s20 offen offset:3072 sc1\n"
"s_add_u32 s20, s20, 4096\n"
"v_cmp_gt_u32 vcc, 144, v253\n"
"v_lshlrev_b32 v0, 16, v58\n"
"v_and_b32 v1, 0xffff0000, v58\n"
"v_lshlrev_b32 v2, 16, v59\n"
"v_and_b32 v3, 0xffff0000, v59\n"
"v_mov_b32 v4, v26\n"
"v_add_f32 v12, 1.0, v42\n"
"v_mov_b32 v16, v34\n"
"v_mov_b32 v20, v50\n"
"v_mul_f32 v8, v12, v0\n"
"v_mov_b32 v5, v28\n"
"v_add_f32 v13, 1.0, v44\n"
"v_mov_b32 v17, v36\n"
"v_mov_b32 v21, v52\n"
"v_mul_f32 v9, v13, v1\n"
"v_mov_b32 v6, v30\n"
"v_add_f32 v14, 1.0, v46\n"
"v_mov_b32 v18, v38\n"
"v_mov_b32 v22, v54\n"
"v_mul_f32 v10, v14, v2\n"
"v_mov_b32 v7, v32\n"
"v_add_f32 v15, 1.0, v48\n"
"v_mov_b32 v19, v40\n"
"v_mov_b32 v23, v56\n"
"v_mul_f32 v11, v15, v3\n"
"s_mov_b64 exec, vcc\n"
"ds_write_b128 v247, v[4:7] offset:0\n"
"ds_write_b128 v247, v[8:11] offset:2304\n"
"ds_write_b128 v247, v[12:15] offset:4608\n"
"ds_write_b128 v247, v[16:19] offset:6912\n"
"ds_write_b128 v247, v[20:23] offset:38912\n"
"s_mov_b64 exec, -1\n"
"v_add_f32 v24, v8, v9\n"
"v_mul_f32 v25, v16, v0\n"
"v_add_f32 v24, v24, v10\n"
"v_fmac_f32 v25, v17, v1\n"
"v_add_f32 v24, v24, v11\n"
"v_fmac_f32 v25, v18, v2\n"
"s_nop 0\n"
"v_fmac_f32 v25, v19, v3\n"
"s_nop 1\n"
"v_add_f32_dpp v24, v24, v24 row_ror:1 row_mask:0xf bank_mask:0xf\n"
"v_add_f32_dpp v25, v25, v25 row_ror:1 row_mask:0xf bank_mask:0xf\n"
"s_nop 0\n"
"v_add_f32_dpp v24, v24, v24 row_ror:2 row_mask:0xf bank_mask:0xf\n"
"v_add_f32_dpp v25, v25, v25 row_ror:2 row_mask:0xf bank_mask:0xf\n"
"s_nop 0\n"
"v_add_f32_dpp v24, v24, v24 row_ror:4 row_mask:0xf bank_mask:0xf\n"
"v_add_f32_dpp v25, v25, v25 row_ror:4 row_mask:0xf bank_mask:0xf\n"
"s_nop 0\n"
"v_add_f32_dpp v24, v24, v24 row_ror:8 row_mask:0xf bank_mask:0xf\n"
"v_add_f32_dpp v25, v25, v25 row_ror:8 row_mask:0xf bank_mask:0xf\n"
"s_nop 0\n"
"v_mov_b32 v26, v24\n"
"v_mov_b32 v27, v25\n"
"s_nop 0\n"
"v_permlane16_swap_b32 v24, v26\n"
"v_permlane16_swap_b32 v25, v27\n"
"v_add_f32 v24, v24, v26\n"
"v_add_f32 v25, v25, v27\n"
"v_mov_b32 v26, v24\n"
"v_mov_b32 v27, v25\n"
"s_nop 0\n"
"v_permlane32_swap_b32 v24, v26\n"
"v_permlane32_swap_b32 v25, v27\n"
"v_add_f32 v24, v24, v26\n"
"v_add_f32 v25, v25, v27\n"
"s_lshl_b32 s62, s41, 3\n"
"s_add_u32 s62, s62, s40\n"
"v_mov_b32 v26, s62\n"
"v_add_u32 v26, 161536, v26\n"
"s_mov_b64 exec, 1\n"
"ds_write_b64 v26, v[24:25]\n"
"s_mov_b64 exec, -1\n"
"s_mov_b64 exec, 0\n"
"s_cmp_eq_u32 s41, 0\n"
"s_cbranch_scc1 AT_hw1_0\n"
"s_cmp_eq_u32 s41, 1\n"
"s_cbranch_scc1 AT_hw1_1\n"
"s_cmp_eq_u32 s41, 2\n"
"s_cbranch_scc1 AT_hw1_2\n"
"s_call_b64 %[kp], AT_pgA0\n"
"s_branch AT_hx1\n"
"AT_hw1_0:\n"
"s_call_b64 %[kp], AT_pg15\n"
"s_branch AT_hx1\n"
"AT_hw1_1:\n"
"s_call_b64 %[kp], AT_pg16\n"
"s_branch AT_hx1\n"
"AT_hw1_2:\n"
"s_call_b64 %[kp], AT_pg17\n"
"AT_hx1:\n"
"s_mov_b64 exec, -1\n"
"s_waitcnt vmcnt(6)\n"
"v_mfma_f32_16x16x32_bf16 v[64:67], a[192:195], a[0:3], v[64:67]\n"
"ds_read_b64_tr_b16 a[136:137], v200 offset:55328\n"
"buffer_load_dwordx4 a[32:35], v207, s[28:31], s21 offen offset:2048 sc1\n"
"v_mfma_f32_16x16x32_bf16 v[68:71], a[192:195], a[4:7], v[68:71]\n"
"s_add_u32 m0, s53, 36864\n"
"ds_read_b64_tr_b16 a[138:139], v200 offset:59936\n"
"v_mfma_f32_16x16x32_bf16 v[72:75], a[196:199], a[0:3], v[72:75]\n"
"ds_read_b64_tr_b16 a[140:141], v200 offset:55360\n"
"buffer_load_dwordx4 v210, s[24:27], s37 offen offset:0 lds\n"
"v_mfma_f32_16x16x32_bf16 v[76:79], a[196:199], a[4:7], v[76:79]\n"
"s_waitcnt lgkmcnt(14)\n"
"ds_read_b64_tr_b16 a[142:143], v200 offset:59968\n"
"v_mfma_f32_16x16x32_bf16 v[80:83], a[200:203], a[0:3], v[80:83]\n"
"ds_read_b64_tr_b16 a[144:145], v200 offset:55392\n"
"v_mfma_f32_16x16x32_bf16 v[84:87], a[200:203], a[4:7], v[84:87]\n"
"ds_read_b64_tr_b16 a[146:147], v200 offset:60000\n"
"buffer_load_dwordx4 a[36:39], v207, s[28:31], s21 offen offset:3072 sc1\n"
"v_mfma_f32_16x16x32_bf16 v[88:91], a[204:207], a[0:3], v[88:91]\n"
"s_add_u32 s21, s21, 4096\n"
"ds_read_b64_tr_b16 a[148:149], v200 offset:55424\n"
"v_mfma_f32_16x16x32_bf16 v[92:95], a[204:207], a[4:7], v[92:95]\n"
"ds_read_b64_tr_b16 a[150:151], v200 offset:60032\n"
"v_mfma_f32_16x16x32_bf16 v[96:99], a[208:211], a[0:3], v[96:99]\n"
"s_add_u32 m0, s53, 36864\n"
"ds_read_b64_tr_b16 a[152:153], v200 offset:55456\n"
"v_mfma_f32_16x16x32_bf16 v[100:103], a[208:211], a[4:7], v[100:103]\n"
"ds_read_b64_tr_b16 a[154:155], v200 offset:60064\n"
"buffer_load_dwordx4 v211, s[24:27], s37 offen offset:1024 lds\n"
"v_mfma_f32_16x16x32_bf16 v[104:107], a[212:215], a[0:3], v[104:107]\n"
"ds_read_b64_tr_b16 a[156:157], v200 offset:55488\n"
"buffer_load_dwordx4 a[40:43], v207, s[28:31], s22 offen offset:2048 sc1\n"
"v_mfma_f32_16x16x32_bf16 v[108:111], a[212:215], a[4:7], v[108:111]\n"
"ds_read_b64_tr_b16 a[158:159], v200 offset:60096\n"
"v_mfma_f32_16x16x32_bf16 v[112:115], a[216:219], a[0:3], v[112:115]\n"
"ds_read_b64_tr_b16 a[192:193], v200 offset:55520\n"
"v_mfma_f32_16x16x32_bf16 v[116:119], a[216:219], a[4:7], v[116:119]\n"
"s_waitcnt lgkmcnt(11)\n"
"ds_read_b64_tr_b16 a[194:195], v200 offset:60128\n"
"v_mfma_f32_16x16x32_bf16 v[120:123], a[220:223], a[0:3], v[120:123]\n"
"ds_read_b64_tr_b16 a[196:197], v203 offset:55296\n"
"v_mfma_f32_16x16x32_bf16 v[124:127], a[220:223], a[4:7], v[124:127]\n"
"s_add_u32 m0, s53, 36864\n"
"ds_read_b64_tr_b16 a[198:199], v203 offset:59904\n"
"buffer_load_dwordx4 a[44:47], v207, s[28:31], s22 offen offset:3072 sc1\n"
"v_mfma_f32_16x16x32_bf16 v[226:229], a[128:131], a[0:3], v[226:229]\n"
"s_add_u32 s22, s22, 4096\n"
"ds_read_b64_tr_b16 a[200:201], v200 offset:64512\n"
"buffer_load_dwordx4 v212, s[24:27], s37 offen offset:2048 lds\n"
"v_mfma_f32_16x16x32_bf16 v[230:233], a[128:131], a[4:7], v[230:233]\n"
"s_waitcnt vmcnt(11)\n"
"ds_read_b64_tr_b16 a[202:203], v201 offset:3584\n"
"v_mfma_f32_16x16x32_bf16 v[64:67], a[132:135], a[8:11], v[64:67]\n"
"ds_read_b64_tr_b16 a[204:205], v200 offset:64544\n"
"v_mfma_f32_16x16x32_bf16 v[68:71], a[132:135], a[12:15], v[68:71]\n"
"ds_read_b64_tr_b16 a[206:207], v201 offset:3616\n"
"v_mfma_f32_16x16x32_bf16 v[72:75], a[136:139], a[8:11], v[72:75]\n"
"ds_read_b64_tr_b16 a[208:209], v200 offset:64576\n"
"buffer_load_dwordx4 a[48:51], v207, s[28:31], s20 offen offset:0 sc1\n"
"v_mfma_f32_16x16x32_bf16 v[76:79], a[136:139], a[12:15], v[76:79]\n"
"s_waitcnt lgkmcnt(11)\n"
"ds_read_b64_tr_b16 a[210:211], v201 offset:3648\n"
"v_mfma_f32_16x16x32_bf16 v[80:83], a[140:143], a[8:11], v[80:83]\n"
"s_add_u32 m0, s53, 36864\n"
"ds_read_b64_tr_b16 a[212:213], v200 offset:64608\n"
"v_mfma_f32_16x16x32_bf16 v[84:87], a[140:143], a[12:15], v[84:87]\n"
"ds_read_b64_tr_b16 a[214:215], v201 offset:3680\n"
"buffer_load_dwordx4 v213, s[24:27], s37 offen offset:3072 lds\n"
"v_mfma_f32_16x16x32_bf16 v[88:91], a[144:147], a[8:11], v[88:91]\n"
"ds_read_b64_tr_b16 a[216:217], v200 offset:64640\n"
"v_mfma_f32_16x16x32_bf16 v[92:95], a[144:147], a[12:15], v[92:95]\n"
"ds_read_b64_tr_b16 a[218:219], v201 offset:3712\n"
"buffer_load_dwordx4 a[52:55], v207, s[28:31], s20 offen offset:1024 sc1\n"
"v_mfma_f32_16x16x32_bf16 v[96:99], a[148:151], a[8:11], v[96:99]\n"
"ds_read_b64_tr_b16 a[220:221], v200 offset:64672\n"
"v_mfma_f32_16x16x32_bf16 v[100:103], a[148:151], a[12:15], v[100:103]\n"
"ds_read_b64_tr_b16 a[222:223], v201 offset:3744\n"
"v_mfma_f32_16x16x32_bf16 v[104:107], a[152:155], a[8:11], v[104:107]\n"
"ds_read_b64_tr_b16 a[128:129], v200 offset:64704\n"
"v_mfma_f32_16x16x32_bf16 v[108:111], a[152:155], a[12:15], v[108:111]\n"
"s_waitcnt lgkmcnt(11)\n"
"ds_read_b64_tr_b16 a[130:131], v201 offset:3776\n"
"v_mfma_f32_16x16x32_bf16 v[112:115], a[156:159], a[8:11], v[112:115]\n"
"s_add_u32 m0, s53, 40960\n"
"ds_read_b64_tr_b16 a[132:133], v200 offset:64736\n"
"buffer_load_dwordx4 a[56:59], v207, s[28:31], s21 offen offset:0 sc1\n"
"v_mfma_f32_16x16x32_bf16 v[116:119], a[156:159], a[12:15], v[116:119]\n"
"ds_read_b64_tr_b16 a[134:135], v201 offset:3808\n"
"buffer_load_dwordx4 v214, s[24:27], s37 offen offset:0 lds\n"
"v_mfma_f32_16x16x32_bf16 v[120:123], a[192:195], a[8:11], v[120:123]\n"
"ds_read_b64_tr_b16 a[136:137], v203 offset:64512\n"
"v_mfma_f32_16x16x32_bf16 v[124:127], a[192:195], a[12:15], v[124:127]\n"
"ds_read_b64_tr_b16 a[138:139], v204 offset:3584\n"
"v_mfma_f32_16x16x32_bf16 v[226:229], a[196:199], a[8:11], v[226:229]\n"
"ds_read_b64_tr_b16 a[140:141], v201 offset:8192\n"
"v_mfma_f32_16x16x32_bf16 v[230:233], a[196:199], a[12:15], v[230:233]\n"
"s_waitcnt vmcnt(14)\n"
"ds_read_b64_tr_b16 a[142:143], v201 offset:12800\n"
"v_mfma_f32_16x16x32_bf16 v[64:67], a[200:203], a[16:19], v[64:67]\n"
"ds_read_b64_tr_b16 a[144:145], v201 offset:8224\n"
"v_mfma_f32_16x16x32_bf16 v[68:71], a[200:203], a[20:23], v[68:71]\n"
"s_waitcnt lgkmcnt(11)\n"
"s_add_u32 m0, s53, 40960\n"
"ds_read_b64_tr_b16 a[146:147], v201 offset:12832\n"
"buffer_load_dwordx4 a[60:63], v207, s[28:31], s21 offen offset:1024 sc1\n"
"v_mfma_f32_16x16x32_bf16 v[72:75], a[204:207], a[16:19], v[72:75]\n"
"ds_read_b64_tr_b16 a[148:149], v201 offset:8256\n"
"buffer_load_dwordx4 v236, s[24:27], s37 offen offset:1024 lds\n"
"v_mfma_f32_16x16x32_bf16 v[76:79], a[204:207], a[20:23], v[76:79]\n"
"ds_read_b64_tr_b16 a[150:151], v201 offset:12864\n"
"v_mfma_f32_16x16x32_bf16 v[80:83], a[208:211], a[16:19], v[80:83]\n"
"ds_read_b64_tr_b16 a[152:153], v201 offset:8288\n"
"v_mfma_f32_16x16x32_bf16 v[84:87], a[208:211], a[20:23], v[84:87]\n"
"ds_read_b64_tr_b16 a[154:155], v201 offset:12896\n"
"v_mfma_f32_16x16x32_bf16 v[88:91], a[212:215], a[16:19], v[88:91]\n"
"ds_read_b64_tr_b16 a[156:157], v201 offset:8320\n"
"v_mfma_f32_16x16x32_bf16 v[92:95], a[212:215], a[20:23], v[92:95]\n"
"ds_read_b64_tr_b16 a[158:159], v201 offset:12928\n"
"v_mfma_f32_16x16x32_bf16 v[96:99], a[216:219], a[16:19], v[96:99]\n"
"s_add_u32 m0, s53, 40960\n"
"ds_read_b64_tr_b16 a[192:193], v201 offset:8352\n"
"buffer_load_dwordx4 a[64:67], v207, s[28:31], s22 offen offset:0 sc1\n"
"v_mfma_f32_16x16x32_bf16 v[100:103], a[216:219], a[20:23], v[100:103]\n"
"s_waitcnt lgkmcnt(11)\n"
"ds_read_b64_tr_b16 a[194:195], v201 offset:12960\n"
"buffer_load_dwordx4 v237, s[24:27], s37 offen offset:2048 lds\n"
"v_mfma_f32_16x16x32_bf16 v[104:107], a[220:223], a[16:19], v[104:107]\n"
"ds_read_b64_tr_b16 a[196:197], v201 offset:8384\n"
"v_mfma_f32_16x16x32_bf16 v[108:111], a[220:223], a[20:23], v[108:111]\n"
"ds_read_b64_tr_b16 a[198:199], v201 offset:12992\n"
"v_mfma_f32_16x16x32_bf16 v[112:115], a[128:131], a[16:19], v[112:115]\n"
"ds_read_b64_tr_b16 a[200:201], v201 offset:8416\n"
"v_mfma_f32_16x16x32_bf16 v[116:119], a[128:131], a[20:23], v[116:119]\n"
"ds_read_b64_tr_b16 a[202:203], v201 offset:13024\n"
"v_mfma_f32_16x16x32_bf16 v[120:123], a[132:135], a[16:19], v[120:123]\n"
"ds_read_b64_tr_b16 a[204:205], v204 offset:8192\n"
"v_mfma_f32_16x16x32_bf16 v[124:127], a[132:135], a[20:23], v[124:127]\n"
"s_add_u32 m0, s53, 40960\n"
"ds_read_b64_tr_b16 a[206:207], v204 offset:12800\n"
"buffer_load_dwordx4 a[68:71], v207, s[28:31], s22 offen offset:1024 sc1\n"
"v_mfma_f32_16x16x32_bf16 v[226:229], a[136:139], a[16:19], v[226:229]\n"
"ds_read_b64_tr_b16 a[208:209], v201 offset:17408\n"
"buffer_load_dwordx4 v238, s[24:27], s37 offen offset:3072 lds\n"
"v_mfma_f32_16x16x32_bf16 v[230:233], a[136:139], a[20:23], v[230:233]\n"
"s_waitcnt lgkmcnt(11)\n"
"s_waitcnt vmcnt(18)\n"
"ds_read_b64_tr_b16 a[210:211], v201 offset:22016\n"
"v_mfma_f32_16x16x32_bf16 v[64:67], a[140:143], a[24:27], v[64:67]\n"
"ds_read_b64_tr_b16 a[212:213], v201 offset:17440\n"
"v_mfma_f32_16x16x32_bf16 v[68:71], a[140:143], a[28:31], v[68:71]\n"
"ds_read_b64_tr_b16 a[214:215], v201 offset:22048\n"
"v_mfma_f32_16x16x32_bf16 v[72:75], a[144:147], a[24:27], v[72:75]\n"
"ds_read_b64_tr_b16 a[216:217], v201 offset:17472\n"
"v_mfma_f32_16x16x32_bf16 v[76:79], a[144:147], a[28:31], v[76:79]\n"
"ds_read_b64_tr_b16 a[218:219], v201 offset:22080\n"
"v_mfma_f32_16x16x32_bf16 v[80:83], a[148:151], a[24:27], v[80:83]\n"
"s_add_u32 m0, s53, 45056\n"
"ds_read_b64_tr_b16 a[220:221], v201 offset:17504\n"
"buffer_load_dwordx4 a[72:75], v207, s[28:31], s20 offen offset:2048 sc1\n"
"v_mfma_f32_16x16x32_bf16 v[84:87], a[148:151], a[28:31], v[84:87]\n"
"ds_read_b64_tr_b16 a[222:223], v201 offset:22112\n"
"buffer_load_dwordx4 v239, s[24:27], s37 offen offset:0 lds\n"
"v_mfma_f32_16x16x32_bf16 v[88:91], a[152:155], a[24:27], v[88:91]\n"
"ds_read_b64_tr_b16 a[128:129], v201 offset:17536\n"
"v_mfma_f32_16x16x32_bf16 v[92:95], a[152:155], a[28:31], v[92:95]\n"
"s_waitcnt lgkmcnt(11)\n"
"ds_read_b64_tr_b16 a[130:131], v201 offset:22144\n"
"v_mfma_f32_16x16x32_bf16 v[96:99], a[156:159], a[24:27], v[96:99]\n"
"ds_read_b64_tr_b16 a[132:133], v201 offset:17568\n"
"v_mfma_f32_16x16x32_bf16 v[100:103], a[156:159], a[28:31], v[100:103]\n"
"ds_read_b64_tr_b16 a[134:135], v201 offset:22176\n"
"v_mfma_f32_16x16x32_bf16 v[104:107], a[192:195], a[24:27], v[104:107]\n"
"ds_read_b64_tr_b16 a[136:137], v201 offset:17600\n"
"v_mfma_f32_16x16x32_bf16 v[108:111], a[192:195], a[28:31], v[108:111]\n"
"ds_read_b64_tr_b16 a[138:139], v201 offset:22208\n"
"buffer_load_dwordx4 a[76:79], v207, s[28:31], s20 offen offset:3072 sc1\n"
"v_mfma_f32_16x16x32_bf16 v[112:115], a[196:199], a[24:27], v[112:115]\n"
"s_add_u32 s20, s20, 4096\n"
"ds_read_b64_tr_b16 a[140:141], v201 offset:17632\n"
"v_mfma_f32_16x16x32_bf16 v[116:119], a[196:199], a[28:31], v[116:119]\n"
"ds_read_b64_tr_b16 a[142:143], v201 offset:22240\n"
"v_mfma_f32_16x16x32_bf16 v[120:123], a[200:203], a[24:27], v[120:123]\n"
"ds_read_b64_tr_b16 a[144:145], v204 offset:17408\n"
"v_mfma_f32_16x16x32_bf16 v[124:127], a[200:203], a[28:31], v[124:127]\n"
"s_waitcnt lgkmcnt(11)\n"
"ds_read_b64_tr_b16 a[146:147], v204 offset:22016\n"
"v_mfma_f32_16x16x32_bf16 v[226:229], a[204:207], a[24:27], v[226:229]\n"
"ds_read_b64_tr_b16 a[148:149], v201 offset:26624\n"
"v_mfma_f32_16x16x32_bf16 v[230:233], a[204:207], a[28:31], v[230:233]\n"
"s_waitcnt vmcnt(18)\n"
"ds_read_b64_tr_b16 a[150:151], v201 offset:31232\n"
"v_mfma_f32_16x16x32_bf16 v[64:67], a[208:211], a[32:35], v[64:67]\n"
"ds_read_b64_tr_b16 a[152:153], v201 offset:26656\n"
"buffer_load_dwordx4 a[80:83], v207, s[28:31], s21 offen offset:2048 sc1\n"
"v_mfma_f32_16x16x32_bf16 v[68:71], a[208:211], a[36:39], v[68:71]\n"
"s_waitcnt vmcnt(31)\n"
"ds_read_b64_tr_b16 a[154:155], v201 offset:31264\n"
"v_mfma_f32_16x16x32_bf16 v[72:75], a[212:215], a[32:35], v[72:75]\n"
"s_barrier\n"
"ds_read_b64_tr_b16 a[156:157], v201 offset:26688\n"
"v_mfma_f32_16x16x32_bf16 v[76:79], a[212:215], a[36:39], v[76:79]\n"
"ds_read_b64_tr_b16 a[158:159], v201 offset:31296\n"
"v_mfma_f32_16x16x32_bf16 v[80:83], a[216:219], a[32:35], v[80:83]\n"
"s_add_u32 m0, s53, 73728\n"
"ds_read_b64_tr_b16 a[192:193], v201 offset:26720\n"
"v_mfma_f32_16x16x32_bf16 v[84:87], a[216:219], a[36:39], v[84:87]\n"
"s_waitcnt lgkmcnt(11)\n"
"ds_read_b64_tr_b16 a[194:195], v201 offset:31328\n"
"buffer_load_dwordx4 v210, s[24:27], s38 offen offset:0 lds\n"
"v_mfma_f32_16x16x32_bf16 v[88:91], a[220:223], a[32:35], v[88:91]\n"
"ds_read_b64_tr_b16 a[196:197], v201 offset:26752\n"
"v_mfma_f32_16x16x32_bf16 v[92:95], a[220:223], a[36:39], v[92:95]\n"
"ds_read_b64_tr_b16 a[198:199], v201 offset:31360\n"
"buffer_load_dwordx4 a[84:87], v207, s[28:31], s21 offen offset:3072 sc1\n"
"v_mfma_f32_16x16x32_bf16 v[96:99], a[128:131], a[32:35], v[96:99]\n"
"s_add_u32 s21, s21, 4096\n"
"ds_read_b64_tr_b16 a[200:201], v201 offset:26784\n"
"v_mfma_f32_16x16x32_bf16 v[100:103], a[128:131], a[36:39], v[100:103]\n"
"AT_pg15:\n"
"s_cbranch_execz AT_hr\n"
"ds_read_b64_tr_b16 a[202:203], v201 offset:31392\n"
"v_mfma_f32_16x16x32_bf16 v[104:107], a[132:135], a[32:35], v[104:107]\n"
"ds_read_b64_tr_b16 a[204:205], v201 offset:26816\n"
"v_mfma_f32_16x16x32_bf16 v[108:111], a[132:135], a[36:39], v[108:111]\n"
"s_add_u32 m0, s53, 73728\n"
"ds_read_b64_tr_b16 a[206:207], v201 offset:31424\n"
"v_mfma_f32_16x16x32_bf16 v[112:115], a[136:139], a[32:35], v[112:115]\n"
"ds_read_b64_tr_b16 a[208:209], v201 offset:26848\n"
"buffer_load_dwordx4 v211, s[24:27], s38 offen offset:1024 lds\n"
"v_mfma_f32_16x16x32_bf16 v[116:119], a[136:139], a[36:39], v[116:119]\n"
"s_waitcnt lgkmcnt(11)\n"
"ds_read_b64_tr_b16 a[210:211], v201 offset:31456\n"
"v_mfma_f32_16x16x32_bf16 v[120:123], a[140:143], a[32:35], v[120:123]\n"
"ds_read_b64_tr_b16 a[212:213], v204 offset:26624\n"
"buffer_load_dwordx4 a[88:91], v207, s[28:31], s22 offen offset:2048 sc1\n"
"v_mfma_f32_16x16x32_bf16 v[124:127], a[140:143], a[36:39], v[124:127]\n"
"ds_read_b64_tr_b16 a[214:215], v204 offset:31232\n"
"v_mfma_f32_16x16x32_bf16 v[226:229], a[144:147], a[32:35], v[226:229]\n"
"ds_read_b64_tr_b16 a[216:217], v201 offset:35840\n"
"v_mfma_f32_16x16x32_bf16 v[230:233], a[144:147], a[36:39], v[230:233]\n"
"s_waitcnt vmcnt(20)\n"
"ds_read_b64_tr_b16 a[218:219], v201 offset:40448\n"
"v_mfma_f32_16x16x32_bf16 v[64:67], a[148:151], a[40:43], v[64:67]\n"
"s_add_u32 m0, s53, 73728\n"
"ds_read_b64_tr_b16 a[220:221], v201 offset:35872\n"
"v_mfma_f32_16x16x32_bf16 v[68:71], a[148:151], a[44:47], v[68:71]\n"
"ds_read_b64_tr_b16 a[222:223], v201 offset:40480\n"
"buffer_load_dwordx4 v212, s[24:27], s38 offen offset:2048 lds\n"
"v_mfma_f32_16x16x32_bf16 v[72:75], a[152:155], a[40:43], v[72:75]\n"
"ds_read_b64_tr_b16 a[128:129], v201 offset:35904\n"
"v_mfma_f32_16x16x32_bf16 v[76:79], a[152:155], a[44:47], v[76:79]\n"
"s_waitcnt lgkmcnt(11)\n"
"ds_read_b64_tr_b16 a[130:131], v201 offset:40512\n"
"buffer_load_dwordx4 a[92:95], v207, s[28:31], s22 offen offset:3072 sc1\n"
"v_mfma_f32_16x16x32_bf16 v[80:83], a[156:159], a[40:43], v[80:83]\n"
"s_add_u32 s22, s22, 4096\n"
"ds_read_b64_tr_b16 a[132:133], v201 offset:35936\n"
"v_mfma_f32_16x16x32_bf16 v[84:87], a[156:159], a[44:47], v[84:87]\n"
"ds_read_b64_tr_b16 a[134:135], v201 offset:40544\n"
"v_mfma_f32_16x16x32_bf16 v[88:91], a[192:195], a[40:43], v[88:91]\n"
"ds_read_b64_tr_b16 a[136:137], v201 offset:35968\n"
"v_mfma_f32_16x16x32_bf16 v[92:95], a[192:195], a[44:47], v[92:95]\n"
"s_add_u32 m0, s53, 73728\n"
"ds_read_b64_tr_b16 a[138:139], v201 offset:40576\n"
"v_mfma_f32_16x16x32_bf16 v[96:99], a[196:199], a[40:43], v[96:99]\n"
"ds_read_b64_tr_b16 a[140:141], v201 offset:36000\n"
"buffer_load_dwordx4 v213, s[24:27], s38 offen offset:3072 lds\n"
"v_mfma_f32_16x16x32_bf16 v[100:103], a[196:199], a[44:47], v[100:103]\n"
"ds_read_b64_tr_b16 a[142:143], v201 offset:40608\n"
"v_mfma_f32_16x16x32_bf16 v[104:107], a[200:203], a[40:43], v[104:107]\n"
"ds_read_b64_tr_b16 a[144:145], v201 offset:36032\n"
"buffer_load_dwordx4 a[96:99], v207, s[28:31], s20 offen offset:0 sc1\n"
"v_mfma_f32_16x16x32_bf16 v[108:111], a[200:203], a[44:47], v[108:111]\n"
"s_waitcnt lgkmcnt(11)\n"
"ds_read_b64_tr_b16 a[146:147], v201 offset:40640\n"
"v_mfma_f32_16x16x32_bf16 v[112:115], a[204:207], a[40:43], v[112:115]\n"
"ds_read_b64_tr_b16 a[148:149], v201 offset:36064\n"
"v_mfma_f32_16x16x32_bf16 v[116:119], a[204:207], a[44:47], v[116:119]\n"
"ds_read_b64_tr_b16 a[150:151], v201 offset:40672\n"
"v_mfma_f32_16x16x32_bf16 v[120:123], a[208:211], a[40:43], v[120:123]\n"
"s_add_u32 m0, s53, 77824\n"
"ds_read_b64_tr_b16 a[152:153], v204 offset:35840\n"
"v_mfma_f32_16x16x32_bf16 v[124:127], a[208:211], a[44:47], v[124:127]\n"
"ds_read_b64_tr_b16 a[154:155], v204 offset:40448\n"
"buffer_load_dwordx4 v214, s[24:27], s38 offen offset:0 lds\n"
"v_mfma_f32_16x16x32_bf16 v[226:229], a[212:215], a[40:43], v[226:229]\n"
"ds_read_b64_tr_b16 a[156:157], v201 offset:45056\n"
"v_mfma_f32_16x16x32_bf16 v[230:233], a[212:215], a[44:47], v[230:233]\n"
"s_waitcnt vmcnt(21)\n"
"ds_read_b64_tr_b16 a[158:159], v201 offset:49664\n"
"buffer_load_dwordx4 a[100:103], v207, s[28:31], s20 offen offset:1024 sc1\n"
"v_mfma_f32_16x16x32_bf16 v[64:67], a[216:219], a[48:51], v[64:67]\n"
"ds_read_b64_tr_b16 a[192:193], v201 offset:45088\n"
"v_mfma_f32_16x16x32_bf16 v[68:71], a[216:219], a[52:55], v[68:71]\n"
"s_waitcnt lgkmcnt(11)\n"
"ds_read_b64_tr_b16 a[194:195], v201 offset:49696\n"
"v_mfma_f32_16x16x32_bf16 v[72:75], a[220:223], a[48:51], v[72:75]\n"
"ds_read_b64_tr_b16 a[196:197], v201 offset:45120\n"
"v_mfma_f32_16x16x32_bf16 v[76:79], a[220:223], a[52:55], v[76:79]\n"
"s_add_u32 m0, s53, 77824\n"
"ds_read_b64_tr_b16 a[198:199], v201 offset:49728\n"
"v_mfma_f32_16x16x32_bf16 v[80:83], a[128:131], a[48:51], v[80:83]\n"
"ds_read_b64_tr_b16 a[200:201], v201 offset:45152\n"
"buffer_load_dwordx4 v236, s[24:27], s38 offen offset:1024 lds\n"
"v_mfma_f32_16x16x32_bf16 v[84:87], a[128:131], a[52:55], v[84:87]\n"
"ds_read_b64_tr_b16 a[202:203], v201 offset:49760\n"
"v_mfma_f32_16x16x32_bf16 v[88:91], a[132:135], a[48:51], v[88:91]\n"
"ds_read_b64_tr_b16 a[204:205], v201 offset:45184\n"
"buffer_load_dwordx4 a[104:107], v207, s[28:31], s21 offen offset:0 sc1\n"
"v_mfma_f32_16x16x32_bf16 v[92:95], a[132:135], a[52:55], v[92:95]\n"
"ds_read_b64_tr_b16 a[206:207], v201 offset:49792\n"
"v_mfma_f32_16x16x32_bf16 v[96:99], a[136:139], a[48:51], v[96:99]\n"
"ds_read_b64_tr_b16 a[208:209], v201 offset:45216\n"
"v_mfma_f32_16x16x32_bf16 v[100:103], a[136:139], a[52:55], v[100:103]\n"
"s_waitcnt lgkmcnt(11)\n"
"ds_read_b64_tr_b16 a[210:211], v201 offset:49824\n"
"v_mfma_f32_16x16x32_bf16 v[104:107], a[140:143], a[48:51], v[104:107]\n"
"s_add_u32 m0, s53, 77824\n"
"ds_read_b64_tr_b16 a[212:213], v201 offset:45248\n"
"v_mfma_f32_16x16x32_bf16 v[108:111], a[140:143], a[52:55], v[108:111]\n"
"ds_read_b64_tr_b16 a[214:215], v201 offset:49856\n"
"buffer_load_dwordx4 v237, s[24:27], s38 offen offset:2048 lds\n"
"v_mfma_f32_16x16x32_bf16 v[112:115], a[144:147], a[48:51], v[112:115]\n"
"ds_read_b64_tr_b16 a[216:217], v201 offset:45280\n"
"v_mfma_f32_16x16x32_bf16 v[116:119], a[144:147], a[52:55], v[116:119]\n"
"ds_read_b64_tr_b16 a[218:219], v201 offset:49888\n"
"buffer_load_dwordx4 a[108:111], v207, s[28:31], s21 offen offset:1024 sc1\n"
"v_mfma_f32_16x16x32_bf16 v[120:123], a[148:151], a[48:51], v[120:123]\n"
"ds_read_b64_tr_b16 a[220:221], v204 offset:45056\n"
"v_mfma_f32_16x16x32_bf16 v[124:127], a[148:151], a[52:55], v[124:127]\n"
"ds_read_b64_tr_b16 a[222:223], v204 offset:49664\n"
"v_mfma_f32_16x16x32_bf16 v[226:229], a[152:155], a[48:51], v[226:229]\n"
"ds_read_b64_tr_b16 a[128:129], v202 offset:0\n"
"v_mfma_f32_16x16x32_bf16 v[230:233], a[152:155], a[52:55], v[230:233]\n"
"s_waitcnt lgkmcnt(11)\n"
"s_waitcnt vmcnt(23)\n"
"ds_read_b64_tr_b16 a[130:131], v202 offset:4608\n"
"v_mfma_f32_16x16x32_bf16 v[64:67], a[156:159], a[56:59], v[64:67]\n"
"s_add_u32 m0, s53, 77824\n"
"ds_read_b64_tr_b16 a[132:133], v202 offset:32\n"
"v_mfma_f32_16x16x32_bf16 v[68:71], a[156:159], a[60:63], v[68:71]\n"
"ds_read_b64_tr_b16 a[134:135], v202 offset:4640\n"
"buffer_load_dwordx4 v238, s[24:27], s38 offen offset:3072 lds\n"
"v_mfma_f32_16x16x32_bf16 v[72:75], a[192:195], a[56:59], v[72:75]\n"
"ds_read_b64_tr_b16 a[136:137], v202 offset:64\n"
"buffer_load_dwordx4 a[112:115], v207, s[28:31], s22 offen offset:0 sc1\n"
"v_mfma_f32_16x16x32_bf16 v[76:79], a[192:195], a[60:63], v[76:79]\n"
"ds_read_b64_tr_b16 a[138:139], v202 offset:4672\n"
"v_mfma_f32_16x16x32_bf16 v[80:83], a[196:199], a[56:59], v[80:83]\n"
"ds_read_b64_tr_b16 a[140:141], v202 offset:96\n"
"v_mfma_f32_16x16x32_bf16 v[84:87], a[196:199], a[60:63], v[84:87]\n"
"ds_read_b64_tr_b16 a[142:143], v202 offset:4704\n"
"v_mfma_f32_16x16x32_bf16 v[88:91], a[200:203], a[56:59], v[88:91]\n"
"ds_read_b64_tr_b16 a[144:145], v202 offset:128\n"
"v_mfma_f32_16x16x32_bf16 v[92:95], a[200:203], a[60:63], v[92:95]\n"
"s_waitcnt lgkmcnt(11)\n"
"s_add_u32 m0, s53, 81920\n"
"ds_read_b64_tr_b16 a[146:147], v202 offset:4736\n"
"v_mfma_f32_16x16x32_bf16 v[96:99], a[204:207], a[56:59], v[96:99]\n"
"ds_read_b64_tr_b16 a[148:149], v202 offset:160\n"
"buffer_load_dwordx4 v239, s[24:27], s38 offen offset:0 lds\n"
"v_mfma_f32_16x16x32_bf16 v[100:103], a[204:207], a[60:63], v[100:103]\n"
"ds_read_b64_tr_b16 a[150:151], v202 offset:4768\n"
"buffer_load_dwordx4 a[116:119], v207, s[28:31], s22 offen offset:1024 sc1\n"
"v_mfma_f32_16x16x32_bf16 v[104:107], a[208:211], a[56:59], v[104:107]\n"
"ds_read_b64_tr_b16 a[152:153], v202 offset:192\n"
"v_mfma_f32_16x16x32_bf16 v[108:111], a[208:211], a[60:63], v[108:111]\n"
"ds_read_b64_tr_b16 a[154:155], v202 offset:4800\n"
"v_mfma_f32_16x16x32_bf16 v[112:115], a[212:215], a[56:59], v[112:115]\n"
"ds_read_b64_tr_b16 a[156:157], v202 offset:224\n"
"v_mfma_f32_16x16x32_bf16 v[116:119], a[212:215], a[60:63], v[116:119]\n"
"ds_read_b64_tr_b16 a[158:159], v202 offset:4832\n"
"v_mfma_f32_16x16x32_bf16 v[120:123], a[216:219], a[56:59], v[120:123]\n"
"ds_read_b64_tr_b16 a[192:193], v205 offset:0\n"
"v_mfma_f32_16x16x32_bf16 v[124:127], a[216:219], a[60:63], v[124:127]\n"
"s_waitcnt lgkmcnt(11)\n"
"ds_read_b64_tr_b16 a[194:195], v205 offset:4608\n"
"v_mfma_f32_16x16x32_bf16 v[226:229], a[220:223], a[56:59], v[226:229]\n"
"ds_read_b64_tr_b16 a[196:197], v202 offset:9216\n"
"buffer_load_dwordx4 a[120:123], v207, s[28:31], s20 offen offset:2048 sc1\n"
"v_mfma_f32_16x16x32_bf16 v[230:233], a[220:223], a[60:63], v[230:233]\n"
"s_waitcnt vmcnt(24)\n"
"ds_read_b64_tr_b16 a[198:199], v202 offset:13824\n"
"v_mfma_f32_16x16x32_bf16 v[64:67], a[128:131], a[64:67], v[64:67]\n"
"ds_read_b64_tr_b16 a[200:201], v202 offset:9248\n"
"v_mfma_f32_16x16x32_bf16 v[68:71], a[128:131], a[68:71], v[68:71]\n"
"s_waitcnt vmcnt(21)\n"
"ds_read_b64_tr_b16 a[202:203], v202 offset:13856\n"
"v_mfma_f32_16x16x32_bf16 v[72:75], a[132:135], a[64:67], v[72:75]\n"
"s_barrier\n"
"ds_read_b64_tr_b16 a[204:205], v202 offset:9280\n"
"v_mfma_f32_16x16x32_bf16 v[76:79], a[132:135], a[68:71], v[76:79]\n"
"ds_read_b64_tr_b16 a[206:207], v202 offset:13888\n"
"v_mfma_f32_16x16x32_bf16 v[80:83], a[136:139], a[64:67], v[80:83]\n"
"ds_read_b64_tr_b16 a[208:209], v202 offset:9312\n"
"v_mfma_f32_16x16x32_bf16 v[84:87], a[136:139], a[68:71], v[84:87]\n"
"s_waitcnt lgkmcnt(11)\n"
"s_add_u32 m0, s53, 110592\n"
"ds_read_b64_tr_b16 a[210:211], v202 offset:13920\n"
"buffer_load_dwordx4 a[124:127], v207, s[28:31], s20 offen offset:3072 sc1\n"
"v_mfma_f32_16x16x32_bf16 v[88:91], a[140:143], a[64:67], v[88:91]\n"
"s_add_u32 s20, s20, 4096\n"
"ds_read_b64_tr_b16 a[212:213], v202 offset:9344\n"
"buffer_load_dwordx4 v210, s[24:27], s39 offen offset:0 lds\n"
"v_mfma_f32_16x16x32_bf16 v[92:95], a[140:143], a[68:71], v[92:95]\n"
"ds_read_b64_tr_b16 a[214:215], v202 offset:13952\n"
"v_mfma_f32_16x16x32_bf16 v[96:99], a[144:147], a[64:67], v[96:99]\n"
"ds_read_b64_tr_b16 a[216:217], v202 offset:9376\n"
"v_mfma_f32_16x16x32_bf16 v[100:103], a[144:147], a[68:71], v[100:103]\n"
"ds_read_b64_tr_b16 a[218:219], v202 offset:13984\n"
"v_mfma_f32_16x16x32_bf16 v[104:107], a[148:151], a[64:67], v[104:107]\n"
"ds_read_b64_tr_b16 a[220:221], v202 offset:9408\n"
"v_mfma_f32_16x16x32_bf16 v[108:111], a[148:151], a[68:71], v[108:111]\n"
"ds_read_b64_tr_b16 a[222:223], v202 offset:14016\n"
"v_mfma_f32_16x16x32_bf16 v[112:115], a[152:155], a[64:67], v[112:115]\n"
"s_add_u32 m0, s53, 110592\n"
"ds_read_b64_tr_b16 a[128:129], v202 offset:9440\n"
"buffer_load_dwordx4 v[0:3], v207, s[28:31], s21 offen offset:2048 sc1\n"
"v_mfma_f32_16x16x32_bf16 v[116:119], a[152:155], a[68:71], v[116:119]\n"
"s_waitcnt lgkmcnt(11)\n"
"ds_read_b64_tr_b16 a[130:131], v202 offset:14048\n"
"buffer_load_dwordx4 v211, s[24:27], s39 offen offset:1024 lds\n"
"v_mfma_f32_16x16x32_bf16 v[120:123], a[156:159], a[64:67], v[120:123]\n"
"ds_read_b64_tr_b16 a[132:133], v205 offset:9216\n"
"v_mfma_f32_16x16x32_bf16 v[124:127], a[156:159], a[68:71], v[124:127]\n"
"ds_read_b64_tr_b16 a[134:135], v205 offset:13824\n"
"v_mfma_f32_16x16x32_bf16 v[226:229], a[192:195], a[64:67], v[226:229]\n"
"ds_read_b64_tr_b16 a[136:137], v202 offset:18432\n"
"v_mfma_f32_16x16x32_bf16 v[230:233], a[192:195], a[68:71], v[230:233]\n"
"s_waitcnt vmcnt(24)\n"
"ds_read_b64_tr_b16 a[138:139], v202 offset:23040\n"
"v_mfma_f32_16x16x32_bf16 v[64:67], a[196:199], a[72:75], v[64:67]\n"
"ds_read_b64_tr_b16 a[140:141], v202 offset:18464\n"
"v_mfma_f32_16x16x32_bf16 v[68:71], a[196:199], a[76:79], v[68:71]\n"
"s_add_u32 m0, s53, 110592\n"
"ds_read_b64_tr_b16 a[142:143], v202 offset:23072\n"
"buffer_load_dwordx4 v[4:7], v207, s[28:31], s21 offen offset:3072 sc1\n"
"v_mfma_f32_16x16x32_bf16 v[72:75], a[200:203], a[72:75], v[72:75]\n"
"s_add_u32 s21, s21, 4096\n"
"ds_read_b64_tr_b16 a[144:145], v202 offset:18496\n"
"buffer_load_dwordx4 v212, s[24:27], s39 offen offset:2048 lds\n"
"v_mfma_f32_16x16x32_bf16 v[76:79], a[200:203], a[76:79], v[76:79]\n"
"s_waitcnt lgkmcnt(11)\n"
"ds_read_b64_tr_b16 a[146:147], v202 offset:23104\n"
"v_mfma_f32_16x16x32_bf16 v[80:83], a[204:207], a[72:75], v[80:83]\n"
"ds_read_b64_tr_b16 a[148:149], v202 offset:18528\n"
"v_mfma_f32_16x16x32_bf16 v[84:87], a[204:207], a[76:79], v[84:87]\n"
"ds_read_b64_tr_b16 a[150:151], v202 offset:23136\n"
"v_mfma_f32_16x16x32_bf16 v[88:91], a[208:211], a[72:75], v[88:91]\n"
"ds_read_b64_tr_b16 a[152:153], v202 offset:18560\n"
"v_mfma_f32_16x16x32_bf16 v[92:95], a[208:211], a[76:79], v[92:95]\n"
"ds_read_b64_tr_b16 a[154:155], v202 offset:23168\n"
"v_mfma_f32_16x16x32_bf16 v[96:99], a[212:215], a[72:75], v[96:99]\n"
"s_add_u32 m0, s53, 110592\n"
"ds_read_b64_tr_b16 a[156:157], v202 offset:18592\n"
"buffer_load_dwordx4 v[8:11], v207, s[28:31], s22 offen offset:2048 sc1\n"
"v_mfma_f32_16x16x32_bf16 v[100:103], a[212:215], a[76:79], v[100:103]\n"
"ds_read_b64_tr_b16 a[158:159], v202 offset:23200\n"
"buffer_load_dwordx4 v213, s[24:27], s39 offen offset:3072 lds\n"
"v_mfma_f32_16x16x32_bf16 v[104:107], a[216:219], a[72:75], v[104:107]\n"
"ds_read_b64_tr_b16 a[192:193], v202 offset:18624\n"
"v_mfma_f32_16x16x32_bf16 v[108:111], a[216:219], a[76:79], v[108:111]\n"
"s_waitcnt lgkmcnt(11)\n"
"ds_read_b64_tr_b16 a[194:195], v202 offset:23232\n"
"v_mfma_f32_16x16x32_bf16 v[112:115], a[220:223], a[72:75], v[112:115]\n"
"ds_read_b64_tr_b16 a[196:197], v202 offset:18656\n"
"v_mfma_f32_16x16x32_bf16 v[116:119], a[220:223], a[76:79], v[116:119]\n"
"ds_read_b64_tr_b16 a[198:199], v202 offset:23264\n"
"v_mfma_f32_16x16x32_bf16 v[120:123], a[128:131], a[72:75], v[120:123]\n"
"ds_read_b64_tr_b16 a[200:201], v205 offset:18432\n"
"v_mfma_f32_16x16x32_bf16 v[124:127], a[128:131], a[76:79], v[124:127]\n"
"s_add_u32 m0, s53, 114688\n"
"ds_read_b64_tr_b16 a[202:203], v205 offset:23040\n"
"buffer_load_dwordx4 v[12:15], v207, s[28:31], s22 offen offset:3072 sc1\n"
"v_mfma_f32_16x16x32_bf16 v[226:229], a[132:135], a[72:75], v[226:229]\n"
"s_add_u32 s22, s22, 4096\n"
"ds_read_b64_tr_b16 a[204:205], v200 offset:0\n"
"buffer_load_dwordx4 v214, s[24:27], s39 offen offset:0 lds\n"
"v_mfma_f32_16x16x32_bf16 v[230:233], a[132:135], a[76:79], v[230:233]\n"
"s_waitcnt vmcnt(27)\n"
"ds_read_b64_tr_b16 a[206:207], v200 offset:4608\n"
"v_mfma_f32_16x16x32_bf16 v[64:67], a[136:139], a[80:83], v[64:67]\n"
"ds_read_b64_tr_b16 a[208:209], v200 offset:32\n"
"v_mfma_f32_16x16x32_bf16 v[68:71], a[136:139], a[84:87], v[68:71]\n"
"s_waitcnt lgkmcnt(11)\n"
"ds_read_b64_tr_b16 a[210:211], v200 offset:4640\n"
"v_mfma_f32_16x16x32_bf16 v[72:75], a[140:143], a[80:83], v[72:75]\n"
"ds_read_b64_tr_b16 a[212:213], v200 offset:64\n"
"v_mfma_f32_16x16x32_bf16 v[76:79], a[140:143], a[84:87], v[76:79]\n"
"ds_read_b64_tr_b16 a[214:215], v200 offset:4672\n"
"v_mfma_f32_16x16x32_bf16 v[80:83], a[144:147], a[80:83], v[80:83]\n"
"s_add_u32 m0, s53, 114688\n"
"ds_read_b64_tr_b16 a[216:217], v200 offset:96\n"
"buffer_load_dwordx4 v[16:19], v207, s[28:31], s20 offen offset:0 sc1\n"
"v_mfma_f32_16x16x32_bf16 v[84:87], a[144:147], a[84:87], v[84:87]\n"
"ds_read_b64_tr_b16 a[218:219], v200 offset:4704\n"
"buffer_load_dwordx4 v236, s[24:27], s39 offen offset:1024 lds\n"
"v_mfma_f32_16x16x32_bf16 v[88:91], a[148:151], a[80:83], v[88:91]\n"
"ds_read_b64_tr_b16 a[220:221], v200 offset:128\n"
"v_mfma_f32_16x16x32_bf16 v[92:95], a[148:151], a[84:87], v[92:95]\n"
"ds_read_b64_tr_b16 a[222:223], v200 offset:4736\n"
"v_mfma_f32_16x16x32_bf16 v[96:99], a[152:155], a[80:83], v[96:99]\n"
"ds_read_b64_tr_b16 a[128:129], v200 offset:160\n"
"v_mfma_f32_16x16x32_bf16 v[100:103], a[152:155], a[84:87], v[100:103]\n"
"s_waitcnt lgkmcnt(11)\n"
"ds_read_b64_tr_b16 a[130:131], v200 offset:4768\n"
"v_mfma_f32_16x16x32_bf16 v[104:107], a[156:159], a[80:83], v[104:107]\n"
"ds_read_b64_tr_b16 a[132:133], v200 offset:192\n"
"v_mfma_f32_16x16x32_bf16 v[108:111], a[156:159], a[84:87], v[108:111]\n"
"s_add_u32 m0, s53, 114688\n"
"ds_read_b64_tr_b16 a[134:135], v200 offset:4800\n"
"buffer_load_dwordx4 v[20:23], v207, s[28:31], s20 offen offset:1024 sc1\n"
"v_mfma_f32_16x16x32_bf16 v[112:115], a[192:195], a[80:83], v[112:115]\n"
"ds_read_b64_tr_b16 a[136:137], v200 offset:224\n"
"buffer_load_dwordx4 v237, s[24:27], s39 offen offset:2048 lds\n"
"v_mfma_f32_16x16x32_bf16 v[116:119], a[192:195], a[84:87], v[116:119]\n"
"ds_read_b64_tr_b16 a[138:139], v200 offset:4832\n"
"v_mfma_f32_16x16x32_bf16 v[120:123], a[196:199], a[80:83], v[120:123]\n"
"ds_read_b64_tr_b16 a[140:141], v203 offset:0\n"
"v_mfma_f32_16x16x32_bf16 v[124:127], a[196:199], a[84:87], v[124:127]\n"
"ds_read_b64_tr_b16 a[142:143], v203 offset:4608\n"
"v_mfma_f32_16x16x32_bf16 v[226:229], a[200:203], a[80:83], v[226:229]\n"
"ds_read_b64_tr_b16 a[144:145], v200 offset:9216\n"
"v_mfma_f32_16x16x32_bf16 v[230:233], a[200:203], a[84:87], v[230:233]\n"
"s_waitcnt lgkmcnt(11)\n"
"s_waitcnt vmcnt(27)\n"
"ds_read_b64_tr_b16 a[146:147], v200 offset:13824\n"
"v_mfma_f32_16x16x32_bf16 v[64:67], a[204:207], a[88:91], v[64:67]\n"
"s_add_u32 m0, s53, 114688\n"
"ds_read_b64_tr_b16 a[148:149], v200 offset:9248\n"
"buffer_load_dwordx4 v[24:27], v207, s[28:31], s21 offen offset:0 sc1\n"
"v_mfma_f32_16x16x32_bf16 v[68:71], a[204:207], a[92:95], v[68:71]\n"
"ds_read_b64_tr_b16 a[150:151], v200 offset:13856\n"
"buffer_load_dwordx4 v238, s[24:27], s39 offen offset:3072 lds\n"
"v_mfma_f32_16x16x32_bf16 v[72:75], a[208:211], a[88:91], v[72:75]\n"
"ds_read_b64_tr_b16 a[152:153], v200 offset:9280\n"
"v_mfma_f32_16x16x32_bf16 v[76:79], a[208:211], a[92:95], v[76:79]\n"
"ds_read_b64_tr_b16 a[154:155], v200 offset:13888\n"
"v_mfma_f32_16x16x32_bf16 v[80:83], a[212:215], a[88:91], v[80:83]\n"
"ds_read_b64_tr_b16 a[156:157], v200 offset:9312\n"
"v_mfma_f32_16x16x32_bf16 v[84:87], a[212:215], a[92:95], v[84:87]\n"
"ds_read_b64_tr_b16 a[158:159], v200 offset:13920\n"
"v_mfma_f32_16x16x32_bf16 v[88:91], a[216:219], a[88:91], v[88:91]\n"
"ds_read_b64_tr_b16 a[192:193], v200 offset:9344\n"
"v_mfma_f32_16x16x32_bf16 v[92:95], a[216:219], a[92:95], v[92:95]\n"
"s_waitcnt lgkmcnt(11)\n"
"s_add_u32 m0, s53, 118784\n"
"ds_read_b64_tr_b16 a[194:195], v200 offset:13952\n"
"buffer_load_dwordx4 v[28:31], v207, s[28:31], s21 offen offset:1024 sc1\n"
"v_mfma_f32_16x16x32_bf16 v[96:99], a[220:223], a[88:91], v[96:99]\n"
"ds_read_b64_tr_b16 a[196:197], v200 offset:9376\n"
"buffer_load_dwordx4 v239, s[24:27], s39 offen offset:0 lds\n"
"v_mfma_f32_16x16x32_bf16 v[100:103], a[220:223], a[92:95], v[100:103]\n"
"ds_read_b64_tr_b16 a[198:199], v200 offset:13984\n"
"v_mfma_f32_16x16x32_bf16 v[104:107], a[128:131], a[88:91], v[104:107]\n"
"ds_read_b64_tr_b16 a[200:201], v200 offset:9408\n"
"v_mfma_f32_16x16x32_bf16 v[108:111], a[128:131], a[92:95], v[108:111]\n"
"ds_read_b64_tr_b16 a[202:203], v200 offset:14016\n"
"v_mfma_f32_16x16x32_bf16 v[112:115], a[132:135], a[88:91], v[112:115]\n"
"ds_read_b64_tr_b16 a[204:205], v200 offset:9440\n"
"v_mfma_f32_16x16x32_bf16 v[116:119], a[132:135], a[92:95], v[116:119]\n"
"ds_read_b64_tr_b16 a[206:207], v200 offset:14048\n"
"v_mfma_f32_16x16x32_bf16 v[120:123], a[136:139], a[88:91], v[120:123]\n"
"ds_read_b64_tr_b16 a[208:209], v203 offset:9216\n"
"buffer_load_dwordx4 v[32:35], v207, s[28:31], s22 offen offset:0 sc1\n"
"v_mfma_f32_16x16x32_bf16 v[124:127], a[136:139], a[92:95], v[124:127]\n"
"s_waitcnt lgkmcnt(11)\n"
"ds_read_b64_tr_b16 a[210:211], v203 offset:13824\n"
"v_mfma_f32_16x16x32_bf16 v[226:229], a[140:143], a[88:91], v[226:229]\n"
"ds_read_b64_tr_b16 a[212:213], v200 offset:18432\n"
"v_mfma_f32_16x16x32_bf16 v[230:233], a[140:143], a[92:95], v[230:233]\n"
"s_waitcnt vmcnt(28)\n"
"ds_read_b64_tr_b16 a[214:215], v200 offset:23040\n"
"v_mfma_f32_16x16x32_bf16 v[64:67], a[144:147], a[96:99], v[64:67]\n"
"ds_read_b64_tr_b16 a[216:217], v200 offset:18464\n"
"v_mfma_f32_16x16x32_bf16 v[68:71], a[144:147], a[100:103], v[68:71]\n"
"ds_read_b64_tr_b16 a[218:219], v200 offset:23072\n"
"v_mfma_f32_16x16x32_bf16 v[72:75], a[148:151], a[96:99], v[72:75]\n"
"ds_read_b64_tr_b16 a[220:221], v200 offset:18496\n"
"v_mfma_f32_16x16x32_bf16 v[76:79], a[148:151], a[100:103], v[76:79]\n"
"ds_read_b64_tr_b16 a[222:223], v200 offset:23104\n"
"buffer_load_dwordx4 v[36:39], v207, s[28:31], s22 offen offset:1024 sc1\n"
"v_mfma_f32_16x16x32_bf16 v[80:83], a[152:155], a[96:99], v[80:83]\n"
"ds_read_b64_tr_b16 a[128:129], v200 offset:18528\n"
"v_mfma_f32_16x16x32_bf16 v[84:87], a[152:155], a[100:103], v[84:87]\n"
"s_waitcnt lgkmcnt(11)\n"
"ds_read_b64_tr_b16 a[130:131], v200 offset:23136\n"
"v_mfma_f32_16x16x32_bf16 v[88:91], a[156:159], a[96:99], v[88:91]\n"
"ds_read_b64_tr_b16 a[132:133], v200 offset:18560\n"
"v_mfma_f32_16x16x32_bf16 v[92:95], a[156:159], a[100:103], v[92:95]\n"
"ds_read_b64_tr_b16 a[134:135], v200 offset:23168\n"
"v_mfma_f32_16x16x32_bf16 v[96:99], a[192:195], a[96:99], v[96:99]\n"
"ds_read_b64_tr_b16 a[136:137], v200 offset:18592\n"
"v_mfma_f32_16x16x32_bf16 v[100:103], a[192:195], a[100:103], v[100:103]\n"
"ds_read_b64_tr_b16 a[138:139], v200 offset:23200\n"
"v_mfma_f32_16x16x32_bf16 v[104:107], a[196:199], a[96:99], v[104:107]\n"
"ds_read_b64_tr_b16 a[140:141], v200 offset:18624\n"
"buffer_load_dwordx4 v[40:43], v207, s[28:31], s20 offen offset:2048 sc1\n"
"v_mfma_f32_16x16x32_bf16 v[108:111], a[196:199], a[100:103], v[108:111]\n"
"ds_read_b64_tr_b16 a[142:143], v200 offset:23232\n"
"v_mfma_f32_16x16x32_bf16 v[112:115], a[200:203], a[96:99], v[112:115]\n"
"ds_read_b64_tr_b16 a[144:145], v200 offset:18656\n"
"v_mfma_f32_16x16x32_bf16 v[116:119], a[200:203], a[100:103], v[116:119]\n"
"s_waitcnt lgkmcnt(11)\n"
"ds_read_b64_tr_b16 a[146:147], v200 offset:23264\n"
"v_mfma_f32_16x16x32_bf16 v[120:123], a[204:207], a[96:99], v[120:123]\n"
"ds_read_b64_tr_b16 a[148:149], v203 offset:18432\n"
"v_mfma_f32_16x16x32_bf16 v[124:127], a[204:207], a[100:103], v[124:127]\n"
"ds_read_b64_tr_b16 a[150:151], v203 offset:23040\n"
"v_mfma_f32_16x16x32_bf16 v[226:229], a[208:211], a[96:99], v[226:229]\n"
"ds_read_b64_tr_b16 a[152:153], v200 offset:27648\n"
"v_mfma_f32_16x16x32_bf16 v[230:233], a[208:211], a[100:103], v[230:233]\n"
"s_waitcnt vmcnt(26)\n"
"ds_read_b64_tr_b16 a[154:155], v200 offset:32256\n"
"buffer_load_dwordx4 v[44:47], v207, s[28:31], s20 offen offset:3072 sc1\n"
"v_mfma_f32_16x16x32_bf16 v[64:67], a[212:215], a[104:107], v[64:67]\n"
"s_add_u32 s20, s20, 4096\n"
"ds_read_b64_tr_b16 a[156:157], v200 offset:27680\n"
"v_mfma_f32_16x16x32_bf16 v[68:71], a[212:215], a[108:111], v[68:71]\n"
"ds_read_b64_tr_b16 a[158:159], v200 offset:32288\n"
"v_mfma_f32_16x16x32_bf16 v[72:75], a[216:219], a[104:107], v[72:75]\n"
"ds_read_b64_tr_b16 a[192:193], v200 offset:27712\n"
"v_mfma_f32_16x16x32_bf16 v[76:79], a[216:219], a[108:111], v[76:79]\n"
"s_waitcnt lgkmcnt(11)\n"
"ds_read_b64_tr_b16 a[194:195], v200 offset:32320\n"
"v_mfma_f32_16x16x32_bf16 v[80:83], a[220:223], a[104:107], v[80:83]\n"
"ds_read_b64_tr_b16 a[196:197], v200 offset:27744\n"
"v_mfma_f32_16x16x32_bf16 v[84:87], a[220:223], a[108:111], v[84:87]\n"
"ds_read_b64_tr_b16 a[198:199], v200 offset:32352\n"
"v_mfma_f32_16x16x32_bf16 v[88:91], a[128:131], a[104:107], v[88:91]\n"
"ds_read_b64_tr_b16 a[200:201], v200 offset:27776\n"
"buffer_load_dwordx4 v[48:51], v207, s[28:31], s21 offen offset:2048 sc1\n"
"v_mfma_f32_16x16x32_bf16 v[92:95], a[128:131], a[108:111], v[92:95]\n"
"ds_read_b64_tr_b16 a[202:203], v200 offset:32384\n"
"v_mfma_f32_16x16x32_bf16 v[96:99], a[132:135], a[104:107], v[96:99]\n"
"ds_read_b64_tr_b16 a[204:205], v200 offset:27808\n"
"v_mfma_f32_16x16x32_bf16 v[100:103], a[132:135], a[108:111], v[100:103]\n"
"ds_read_b64_tr_b16 a[206:207], v200 offset:32416\n"
"v_mfma_f32_16x16x32_bf16 v[104:107], a[136:139], a[104:107], v[104:107]\n"
"ds_read_b64_tr_b16 a[208:209], v200 offset:27840\n"
"v_mfma_f32_16x16x32_bf16 v[108:111], a[136:139], a[108:111], v[108:111]\n"
"s_waitcnt lgkmcnt(11)\n"
"ds_read_b64_tr_b16 a[210:211], v200 offset:32448\n"
"v_mfma_f32_16x16x32_bf16 v[112:115], a[140:143], a[104:107], v[112:115]\n"
"ds_read_b64_tr_b16 a[212:213], v200 offset:27872\n"
"v_mfma_f32_16x16x32_bf16 v[116:119], a[140:143], a[108:111], v[116:119]\n"
"ds_read_b64_tr_b16 a[214:215], v200 offset:32480\n"
"buffer_load_dwordx4 v[52:55], v207, s[28:31], s21 offen offset:3072 sc1\n"
"v_mfma_f32_16x16x32_bf16 v[120:123], a[144:147], a[104:107], v[120:123]\n"
"s_add_u32 s21, s21, 4096\n"
"ds_read_b64_tr_b16 a[216:217], v203 offset:27648\n"
"v_mfma_f32_16x16x32_bf16 v[124:127], a[144:147], a[108:111], v[124:127]\n"
"ds_read_b64_tr_b16 a[218:219], v203 offset:32256\n"
"v_mfma_f32_16x16x32_bf16 v[226:229], a[148:151], a[104:107], v[226:229]\n"
"ds_read_b64_tr_b16 a[220:221], v200 offset:36864\n"
"v_mfma_f32_16x16x32_bf16 v[230:233], a[148:151], a[108:111], v[230:233]\n"
"s_waitcnt vmcnt(25)\n"
"ds_read_b64_tr_b16 a[222:223], v200 offset:41472\n"
"v_mfma_f32_16x16x32_bf16 v[64:67], a[152:155], a[112:115], v[64:67]\n"
"ds_read_b64_tr_b16 a[128:129], v200 offset:36896\n"
"v_mfma_f32_16x16x32_bf16 v[68:71], a[152:155], a[116:119], v[68:71]\n"
"s_waitcnt lgkmcnt(11)\n"
"ds_read_b64_tr_b16 a[130:131], v200 offset:41504\n"
"v_mfma_f32_16x16x32_bf16 v[72:75], a[156:159], a[112:115], v[72:75]\n"
"ds_read_b64_tr_b16 a[132:133], v200 offset:36928\n"
"buffer_load_dwordx4 v[56:59], v207, s[28:31], s22 offen offset:2048 sc1\n"
"v_mfma_f32_16x16x32_bf16 v[76:79], a[156:159], a[116:119], v[76:79]\n"
"ds_read_b64_tr_b16 a[134:135], v200 offset:41536\n"
"v_mfma_f32_16x16x32_bf16 v[80:83], a[192:195], a[112:115], v[80:83]\n"
"ds_read_b64_tr_b16 a[136:137], v200 offset:36960\n"
"v_mfma_f32_16x16x32_bf16 v[84:87], a[192:195], a[116:119], v[84:87]\n"
"ds_read_b64_tr_b16 a[138:139], v200 offset:41568\n"
"v_mfma_f32_16x16x32_bf16 v[88:91], a[196:199], a[112:115], v[88:91]\n"
"ds_read_b64_tr_b16 a[140:141], v200 offset:36992\n"
"v_mfma_f32_16x16x32_bf16 v[92:95], a[196:199], a[116:119], v[92:95]\n"
"ds_read_b64_tr_b16 a[142:143], v200 offset:41600\n"
"v_mfma_f32_16x16x32_bf16 v[96:99], a[200:203], a[112:115], v[96:99]\n"
"ds_read_b64_tr_b16 a[144:145], v200 offset:37024\n"
"v_mfma_f32_16x16x32_bf16 v[100:103], a[200:203], a[116:119], v[100:103]\n"
"s_waitcnt lgkmcnt(11)\n"
"ds_read_b64_tr_b16 a[146:147], v200 offset:41632\n"
"buffer_load_dwordx4 v[60:63], v207, s[28:31], s22 offen offset:3072 sc1\n"
"v_mfma_f32_16x16x32_bf16 v[104:107], a[204:207], a[112:115], v[104:107]\n"
"s_add_u32 s22, s22, 4096\n"
"ds_read_b64_tr_b16 a[148:149], v200 offset:37056\n"
"v_mfma_f32_16x16x32_bf16 v[108:111], a[204:207], a[116:119], v[108:111]\n"
"s_waitcnt vmcnt(8)\n"
"ds_read_b64_tr_b16 a[150:151], v200 offset:41664\n"
"v_mfma_f32_16x16x32_bf16 v[112:115], a[208:211], a[112:115], v[112:115]\n"
"s_barrier\n"
"ds_read_b64_tr_b16 a[152:153], v200 offset:37088\n"
"v_mfma_f32_16x16x32_bf16 v[116:119], a[208:211], a[116:119], v[116:119]\n"
"ds_read_b64_tr_b16 a[154:155], v200 offset:41696\n"
"v_mfma_f32_16x16x32_bf16 v[120:123], a[212:215], a[112:115], v[120:123]\n"
"ds_read_b64_tr_b16 a[156:157], v203 offset:36864\n"
"v_mfma_f32_16x16x32_bf16 v[124:127], a[212:215], a[116:119], v[124:127]\n"
"ds_read_b64_tr_b16 a[158:159], v203 offset:41472\n"
"v_mfma_f32_16x16x32_bf16 v[226:229], a[216:219], a[112:115], v[226:229]\n"
"ds_read_b64_tr_b16 a[192:193], v203 offset:46080\n"
"v_mfma_f32_16x16x32_bf16 v[230:233], a[216:219], a[116:119], v[230:233]\n"
"s_waitcnt lgkmcnt(11)\n"
"s_waitcnt vmcnt(25)\n"
"ds_read_b64_tr_b16 a[194:195], v203 offset:50688\n"
"v_mfma_f32_16x16x32_bf16 v[64:67], a[220:223], a[120:123], v[64:67]\n"
"ds_read_b64_tr_b16 a[196:197], v203 offset:55296\n"
"v_mfma_f32_16x16x32_bf16 v[68:71], a[220:223], a[124:127], v[68:71]\n"
"ds_read_b64_tr_b16 a[198:199], v203 offset:59904\n"
"v_mfma_f32_16x16x32_bf16 v[72:75], a[128:131], a[120:123], v[72:75]\n"
"ds_read_b64_tr_b16 a[200:201], v203 offset:64512\n"
"v_mfma_f32_16x16x32_bf16 v[76:79], a[128:131], a[124:127], v[76:79]\n"
"ds_read_b64_tr_b16 a[202:203], v204 offset:3584\n"
"v_mfma_f32_16x16x32_bf16 v[80:83], a[132:135], a[120:123], v[80:83]\n"
"ds_read_b64_tr_b16 a[204:205], v204 offset:8192\n"
"v_mfma_f32_16x16x32_bf16 v[84:87], a[132:135], a[124:127], v[84:87]\n"
"ds_read_b64_tr_b16 a[206:207], v204 offset:12800\n"
"v_mfma_f32_16x16x32_bf16 v[88:91], a[136:139], a[120:123], v[88:91]\n"
"ds_read_b64_tr_b16 a[208:209], v204 offset:17408\n"
"v_mfma_f32_16x16x32_bf16 v[92:95], a[136:139], a[124:127], v[92:95]\n"
"s_waitcnt lgkmcnt(11)\n"
"ds_read_b64_tr_b16 a[210:211], v204 offset:22016\n"
"v_mfma_f32_16x16x32_bf16 v[96:99], a[140:143], a[120:123], v[96:99]\n"
"ds_read_b64_tr_b16 a[212:213], v204 offset:26624\n"
"v_mfma_f32_16x16x32_bf16 v[100:103], a[140:143], a[124:127], v[100:103]\n"
"ds_read_b64_tr_b16 a[214:215], v204 offset:31232\n"
"v_mfma_f32_16x16x32_bf16 v[104:107], a[144:147], a[120:123], v[104:107]\n"
"ds_read_b64_tr_b16 a[216:217], v204 offset:35840\n"
"v_mfma_f32_16x16x32_bf16 v[108:111], a[144:147], a[124:127], v[108:111]\n"
"ds_read_b64_tr_b16 a[218:219], v204 offset:40448\n"
"v_mfma_f32_16x16x32_bf16 v[112:115], a[148:151], a[120:123], v[112:115]\n"
"ds_read_b64_tr_b16 a[220:221], v204 offset:45056\n"
"v_mfma_f32_16x16x32_bf16 v[116:119], a[148:151], a[124:127], v[116:119]\n"
"ds_read_b64_tr_b16 a[222:223], v204 offset:49664\n"
"v_mfma_f32_16x16x32_bf16 v[120:123], a[152:155], a[120:123], v[120:123]\n"
"ds_read_b64_tr_b16 a[128:129], v200 offset:46080\n"
"v_mfma_f32_16x16x32_bf16 v[124:127], a[152:155], a[124:127], v[124:127]\n"
"s_waitcnt lgkmcnt(11)\n"
"s_mov_b32 m0, s45\n"
"ds_read_b64_tr_b16 a[130:131], v200 offset:50688\n"
"v_mfma_f32_16x16x32_bf16 v[226:229], a[156:159], a[120:123], v[226:229]\n"
"ds_read_b64_tr_b16 a[132:133], v200 offset:55296\n"
"buffer_load_dwordx4 v207, s[16:19], s46 offen offset:0 lds\n"
"v_mfma_f32_16x16x32_bf16 v[230:233], a[156:159], a[124:127], v[230:233]\n"
"s_waitcnt vmcnt(22)\n"
"ds_read_b64_tr_b16 a[134:135], v200 offset:59904\n"
"v_mfma_f32_16x16x32_bf16 v[226:229], a[192:195], v[0:3], v[226:229]\n"
"ds_read_b64_tr_b16 a[136:137], v200 offset:64512\n"
"v_mfma_f32_16x16x32_bf16 v[230:233], a[192:195], v[4:7], v[230:233]\n"
"s_waitcnt vmcnt(18)\n"
"ds_read_b64_tr_b16 a[138:139], v201 offset:3584\n"
"v_mfma_f32_16x16x32_bf16 v[226:229], a[196:199], v[8:11], v[226:229]\n"
"s_mov_b32 m0, s45\n"
"ds_read_b64_tr_b16 a[140:141], v201 offset:8192\n"
"v_accvgpr_write_b32 a48, v128\n"
"v_mfma_f32_16x16x32_bf16 v[230:233], a[196:199], v[12:15], v[230:233]\n"
"s_waitcnt vmcnt(14)\n"
"AT_pg16:\n"
"s_cbranch_execz AT_hr\n"
"ds_read_b64_tr_b16 a[142:143], v201 offset:12800\n"
"buffer_load_dwordx4 v207, s[16:19], s46 offen offset:1024 lds\n"
"v_mfma_f32_16x16x32_bf16 v[226:229], a[200:203], v[16:19], v[226:229]\n"
"ds_read_b64_tr_b16 a[144:145], v201 offset:17408\n"
"v_accvgpr_write_b32 a49, v129\n"
"v_mfma_f32_16x16x32_bf16 v[230:233], a[200:203], v[20:23], v[230:233]\n"
"s_waitcnt lgkmcnt(11)\n"
"s_waitcnt vmcnt(11)\n"
"ds_read_b64_tr_b16 a[146:147], v201 offset:22016\n"
"v_accvgpr_write_b32 a50, v130\n"
"v_mfma_f32_16x16x32_bf16 v[226:229], a[204:207], v[24:27], v[226:229]\n"
"ds_read_b64_tr_b16 a[148:149], v201 offset:26624\n"
"v_accvgpr_write_b32 a51, v131\n"
"v_mfma_f32_16x16x32_bf16 v[230:233], a[204:207], v[28:31], v[230:233]\n"
"s_waitcnt vmcnt(8)\n"
"ds_read_b64_tr_b16 a[150:151], v201 offset:31232\n"
"v_accvgpr_write_b32 a52, v132\n"
"v_mfma_f32_16x16x32_bf16 v[226:229], a[208:211], v[32:35], v[226:229]\n"
"s_mov_b32 m0, s45\n"
"ds_read_b64_tr_b16 a[152:153], v201 offset:35840\n"
"v_accvgpr_write_b32 a53, v133\n"
"v_mfma_f32_16x16x32_bf16 v[230:233], a[208:211], v[36:39], v[230:233]\n"
"s_waitcnt vmcnt(6)\n"
"ds_read_b64_tr_b16 a[154:155], v201 offset:40448\n"
"buffer_load_dwordx4 v207, s[16:19], s46 offen offset:2048 lds\n"
"v_mfma_f32_16x16x32_bf16 v[226:229], a[212:215], v[40:43], v[226:229]\n"
"ds_read_b64_tr_b16 a[156:157], v201 offset:45056\n"
"v_accvgpr_write_b32 a54, v134\n"
"v_mfma_f32_16x16x32_bf16 v[230:233], a[212:215], v[44:47], v[230:233]\n"
"s_waitcnt vmcnt(5)\n"
"ds_read_b64_tr_b16 a[158:159], v201 offset:49664\n"
"v_accvgpr_write_b32 a55, v135\n"
"v_mfma_f32_16x16x32_bf16 v[226:229], a[216:219], v[48:51], v[226:229]\n"
"ds_read_b64_tr_b16 a[192:193], v200 offset:46112\n"
"v_accvgpr_write_b32 a56, v136\n"
"v_mfma_f32_16x16x32_bf16 v[230:233], a[216:219], v[52:55], v[230:233]\n"
"s_waitcnt lgkmcnt(11)\n"
"s_waitcnt vmcnt(3)\n"
"ds_read_b64_tr_b16 a[194:195], v200 offset:50720\n"
"v_accvgpr_write_b32 a57, v137\n"
"v_mfma_f32_16x16x32_bf16 v[226:229], a[220:223], v[56:59], v[226:229]\n"
"ds_read_b64_tr_b16 a[196:197], v200 offset:55328\n"
"v_accvgpr_write_b32 a58, v138\n"
"v_mfma_f32_16x16x32_bf16 v[230:233], a[220:223], v[60:63], v[230:233]\n"
"ds_read_b64_tr_b16 a[198:199], v200 offset:59936\n"
"v_accvgpr_write_b32 a59, v139\n"
"v_mfma_f32_16x16x32_bf16 v[64:67], a[128:131], v[0:3], v[64:67]\n"
"ds_read_b64_tr_b16 a[200:201], v200 offset:64544\n"
"v_accvgpr_write_b32 a60, v140\n"
"v_mfma_f32_16x16x32_bf16 v[68:71], a[128:131], v[4:7], v[68:71]\n"
"s_add_u32 m0, s84, 131072\n"
"ds_read_b64_tr_b16 a[202:203], v201 offset:3616\n"
"v_accvgpr_write_b32 a61, v141\n"
"v_mfma_f32_16x16x32_bf16 v[64:67], a[132:135], v[8:11], v[64:67]\n"
"ds_read_b64_tr_b16 a[204:205], v201 offset:8224\n"
"buffer_load_dwordx4 v207, s[72:75], s50 offen offset:0 lds\n"
"v_mfma_f32_16x16x32_bf16 v[68:71], a[132:135], v[12:15], v[68:71]\n"
"ds_read_b64_tr_b16 a[206:207], v201 offset:12832\n"
"v_cvt_pk_bf16_f32 v218, v226, v227\n"
"v_mfma_f32_16x16x32_bf16 v[64:67], a[136:139], v[16:19], v[64:67]\n"
"ds_read_b64_tr_b16 a[208:209], v201 offset:17440\n"
"v_cvt_pk_bf16_f32 v219, v228, v229\n"
"v_mfma_f32_16x16x32_bf16 v[68:71], a[136:139], v[20:23], v[68:71]\n"
"s_waitcnt lgkmcnt(11)\n"
"ds_read_b64_tr_b16 a[210:211], v201 offset:22048\n"
"v_cvt_pk_bf16_f32 v222, v230, v231\n"
"v_mfma_f32_16x16x32_bf16 v[64:67], a[140:143], v[24:27], v[64:67]\n"
"ds_read_b64_tr_b16 a[212:213], v201 offset:26656\n"
"v_cvt_pk_bf16_f32 v223, v232, v233\n"
"v_mfma_f32_16x16x32_bf16 v[68:71], a[140:143], v[28:31], v[68:71]\n"
"ds_read_b64_tr_b16 a[214:215], v201 offset:31264\n"
"v_accvgpr_write_b32 a62, v142\n"
"v_mfma_f32_16x16x32_bf16 v[64:67], a[144:147], v[32:35], v[64:67]\n"
"s_add_u32 m0, s84, 131072\n"
"ds_read_b64_tr_b16 a[216:217], v201 offset:35872\n"
"buffer_store_dwordx2 v[218:219], v207, s[28:31], s49 offen sc1\n"
"v_mfma_f32_16x16x32_bf16 v[68:71], a[144:147], v[36:39], v[68:71]\n"
"ds_read_b64_tr_b16 a[218:219], v201 offset:40480\n"
"buffer_load_dwordx4 v207, s[72:75], s50 offen offset:1024 lds\n"
"v_mfma_f32_16x16x32_bf16 v[64:67], a[148:151], v[40:43], v[64:67]\n"
"ds_read_b64_tr_b16 a[220:221], v201 offset:45088\n"
"buffer_store_dwordx2 v[222:223], v208, s[28:31], s49 offen sc1\n"
"v_mfma_f32_16x16x32_bf16 v[68:71], a[148:151], v[44:47], v[68:71]\n"
"ds_read_b64_tr_b16 a[222:223], v201 offset:49696\n"
"v_accvgpr_write_b32 a63, v143\n"
"v_mfma_f32_16x16x32_bf16 v[64:67], a[152:155], v[48:51], v[64:67]\n"
"ds_read_b64_tr_b16 a[128:129], v200 offset:46144\n"
"v_accvgpr_write_b32 a64, v144\n"
"v_mfma_f32_16x16x32_bf16 v[68:71], a[152:155], v[52:55], v[68:71]\n"
"s_waitcnt lgkmcnt(11)\n"
"ds_read_b64_tr_b16 a[130:131], v200 offset:50752\n"
"v_accvgpr_write_b32 a65, v145\n"
"v_mfma_f32_16x16x32_bf16 v[64:67], a[156:159], v[56:59], v[64:67]\n"
"s_add_u32 m0, s84, 131072\n"
"ds_read_b64_tr_b16 a[132:133], v200 offset:55360\n"
"v_accvgpr_write_b32 a66, v146\n"
"v_mfma_f32_16x16x32_bf16 v[68:71], a[156:159], v[60:63], v[68:71]\n"
"ds_read_b64_tr_b16 a[134:135], v200 offset:59968\n"
"buffer_load_dwordx4 v207, s[72:75], s50 offen offset:2048 lds\n"
"v_mfma_f32_16x16x32_bf16 v[72:75], a[192:195], v[0:3], v[72:75]\n"
"ds_read_b64_tr_b16 a[136:137], v200 offset:64576\n"
"v_accvgpr_write_b32 a67, v147\n"
"v_mfma_f32_16x16x32_bf16 v[76:79], a[192:195], v[4:7], v[76:79]\n"
"ds_read_b64_tr_b16 a[138:139], v201 offset:3648\n"
"v_accvgpr_write_b32 a68, v148\n"
"v_mfma_f32_16x16x32_bf16 v[72:75], a[196:199], v[8:11], v[72:75]\n"
"ds_read_b64_tr_b16 a[140:141], v201 offset:8256\n"
"v_accvgpr_write_b32 a69, v149\n"
"v_mfma_f32_16x16x32_bf16 v[76:79], a[196:199], v[12:15], v[76:79]\n"
"ds_read_b64_tr_b16 a[142:143], v201 offset:12864\n"
"v_cvt_pk_bf16_f32 v218, v64, v65\n"
"v_mfma_f32_16x16x32_bf16 v[72:75], a[200:203], v[16:19], v[72:75]\n"
"ds_read_b64_tr_b16 a[144:145], v201 offset:17472\n"
"v_cvt_pk_bf16_f32 v219, v66, v67\n"
"v_mfma_f32_16x16x32_bf16 v[76:79], a[200:203], v[20:23], v[76:79]\n"
"s_waitcnt lgkmcnt(11)\n"
"ds_read_b64_tr_b16 a[146:147], v201 offset:22080\n"
"v_cvt_pk_bf16_f32 v222, v68, v69\n"
"v_mfma_f32_16x16x32_bf16 v[72:75], a[204:207], v[24:27], v[72:75]\n"
"s_add_u32 m0, s84, 131072\n"
"ds_read_b64_tr_b16 a[148:149], v201 offset:26688\n"
"v_cvt_pk_bf16_f32 v223, v70, v71\n"
"v_mfma_f32_16x16x32_bf16 v[76:79], a[204:207], v[28:31], v[76:79]\n"
"ds_read_b64_tr_b16 a[150:151], v201 offset:31296\n"
"buffer_load_dwordx4 v207, s[72:75], s50 offen offset:3072 lds\n"
"v_mfma_f32_16x16x32_bf16 v[72:75], a[208:211], v[32:35], v[72:75]\n"
"ds_read_b64_tr_b16 a[152:153], v201 offset:35904\n"
"v_accvgpr_write_b32 a70, v150\n"
"v_mfma_f32_16x16x32_bf16 v[76:79], a[208:211], v[36:39], v[76:79]\n"
"ds_read_b64_tr_b16 a[154:155], v201 offset:40512\n"
"v_accvgpr_write_b32 a71, v151\n"
"v_mfma_f32_16x16x32_bf16 v[72:75], a[212:215], v[40:43], v[72:75]\n"
"ds_read_b64_tr_b16 a[156:157], v201 offset:45120\n"
"v_accvgpr_write_b32 a72, v152\n"
"v_mfma_f32_16x16x32_bf16 v[76:79], a[212:215], v[44:47], v[76:79]\n"
"ds_read_b64_tr_b16 a[158:159], v201 offset:49728\n"
"v_accvgpr_write_b32 a73, v153\n"
"v_mfma_f32_16x16x32_bf16 v[72:75], a[216:219], v[48:51], v[72:75]\n"
"s_add_u32 m0, s84, 135168\n"
"s_add_u32 s50, s50, 4096\n"
"ds_read_b64_tr_b16 a[192:193], v200 offset:46176\n"
"v_accvgpr_write_b32 a74, v154\n"
"v_mfma_f32_16x16x32_bf16 v[76:79], a[216:219], v[52:55], v[76:79]\n"
"s_waitcnt lgkmcnt(11)\n"
"ds_read_b64_tr_b16 a[194:195], v200 offset:50784\n"
"buffer_load_dwordx4 v207, s[72:75], s50 offen offset:0 lds\n"
"v_mfma_f32_16x16x32_bf16 v[72:75], a[220:223], v[56:59], v[72:75]\n"
"ds_read_b64_tr_b16 a[196:197], v200 offset:55392\n"
"v_accvgpr_write_b32 a75, v155\n"
"v_mfma_f32_16x16x32_bf16 v[76:79], a[220:223], v[60:63], v[76:79]\n"
"ds_read_b64_tr_b16 a[198:199], v200 offset:60000\n"
"v_accvgpr_write_b32 a76, v156\n"
"v_mfma_f32_16x16x32_bf16 v[80:83], a[128:131], v[0:3], v[80:83]\n"
"ds_read_b64_tr_b16 a[200:201], v200 offset:64608\n"
"v_accvgpr_write_b32 a77, v157\n"
"v_mfma_f32_16x16x32_bf16 v[84:87], a[128:131], v[4:7], v[84:87]\n"
"ds_read_b64_tr_b16 a[202:203], v201 offset:3680\n"
"v_accvgpr_write_b32 a78, v158\n"
"v_mfma_f32_16x16x32_bf16 v[80:83], a[132:135], v[8:11], v[80:83]\n"
"ds_read_b64_tr_b16 a[204:205], v201 offset:8288\n"
"v_accvgpr_write_b32 a79, v159\n"
"v_mfma_f32_16x16x32_bf16 v[84:87], a[132:135], v[12:15], v[84:87]\n"
"ds_read_b64_tr_b16 a[206:207], v201 offset:12896\n"
"v_cvt_pk_bf16_f32 v220, v72, v73\n"
"v_mfma_f32_16x16x32_bf16 v[80:83], a[136:139], v[16:19], v[80:83]\n"
"ds_read_b64_tr_b16 a[208:209], v201 offset:17504\n"
"v_cvt_pk_bf16_f32 v221, v74, v75\n"
"v_mfma_f32_16x16x32_bf16 v[84:87], a[136:139], v[20:23], v[84:87]\n"
"s_waitcnt lgkmcnt(11)\n"
"ds_read_b64_tr_b16 a[210:211], v201 offset:22112\n"
"v_cvt_pk_bf16_f32 v224, v76, v77\n"
"v_mfma_f32_16x16x32_bf16 v[80:83], a[140:143], v[24:27], v[80:83]\n"
"s_add_u32 m0, s84, 135168\n"
"ds_read_b64_tr_b16 a[212:213], v201 offset:26720\n"
"v_cvt_pk_bf16_f32 v225, v78, v79\n"
"v_mfma_f32_16x16x32_bf16 v[84:87], a[140:143], v[28:31], v[84:87]\n"
"ds_read_b64_tr_b16 a[214:215], v201 offset:31328\n"
"buffer_load_dwordx4 v207, s[72:75], s50 offen offset:1024 lds\n"
"v_mfma_f32_16x16x32_bf16 v[80:83], a[144:147], v[32:35], v[80:83]\n"
"ds_read_b64_tr_b16 a[216:217], v201 offset:35936\n"
"buffer_store_dwordx4 v[218:221], v207, s[28:31], s54 offen offset:0 sc1\n"
"v_mfma_f32_16x16x32_bf16 v[84:87], a[144:147], v[36:39], v[84:87]\n"
"ds_read_b64_tr_b16 a[218:219], v201 offset:40544\n"
"v_accvgpr_write_b32 a80, v160\n"
"v_mfma_f32_16x16x32_bf16 v[80:83], a[148:151], v[40:43], v[80:83]\n"
"ds_read_b64_tr_b16 a[220:221], v201 offset:45152\n"
"buffer_store_dwordx4 v[222:225], v208, s[28:31], s54 offen offset:0 sc1\n"
"v_mfma_f32_16x16x32_bf16 v[84:87], a[148:151], v[44:47], v[84:87]\n"
"ds_read_b64_tr_b16 a[222:223], v201 offset:49760\n"
"v_accvgpr_write_b32 a81, v161\n"
"v_mfma_f32_16x16x32_bf16 v[80:83], a[152:155], v[48:51], v[80:83]\n"
"s_add_u32 m0, s84, 135168\n"
"ds_read_b64_tr_b16 a[128:129], v200 offset:46208\n"
"v_accvgpr_write_b32 a82, v162\n"
"v_mfma_f32_16x16x32_bf16 v[84:87], a[152:155], v[52:55], v[84:87]\n"
"s_waitcnt lgkmcnt(11)\n"
"ds_read_b64_tr_b16 a[130:131], v200 offset:50816\n"
"buffer_load_dwordx4 v207, s[72:75], s50 offen offset:2048 lds\n"
"v_mfma_f32_16x16x32_bf16 v[80:83], a[156:159], v[56:59], v[80:83]\n"
"ds_read_b64_tr_b16 a[132:133], v200 offset:55424\n"
"v_accvgpr_write_b32 a83, v163\n"
"v_mfma_f32_16x16x32_bf16 v[84:87], a[156:159], v[60:63], v[84:87]\n"
"ds_read_b64_tr_b16 a[134:135], v200 offset:60032\n"
"v_accvgpr_write_b32 a84, v164\n"
"v_mfma_f32_16x16x32_bf16 v[88:91], a[192:195], v[0:3], v[88:91]\n"
"ds_read_b64_tr_b16 a[136:137], v200 offset:64640\n"
"v_accvgpr_write_b32 a85, v165\n"
"v_mfma_f32_16x16x32_bf16 v[92:95], a[192:195], v[4:7], v[92:95]\n"
"ds_read_b64_tr_b16 a[138:139], v201 offset:3712\n"
"v_accvgpr_write_b32 a86, v166\n"
"v_mfma_f32_16x16x32_bf16 v[88:91], a[196:199], v[8:11], v[88:91]\n"
"ds_read_b64_tr_b16 a[140:141], v201 offset:8320\n"
"v_accvgpr_write_b32 a87, v167\n"
"v_mfma_f32_16x16x32_bf16 v[92:95], a[196:199], v[12:15], v[92:95]\n"
"ds_read_b64_tr_b16 a[142:143], v201 offset:12928\n"
"v_cvt_pk_bf16_f32 v218, v80, v81\n"
"v_mfma_f32_16x16x32_bf16 v[88:91], a[200:203], v[16:19], v[88:91]\n"
"ds_read_b64_tr_b16 a[144:145], v201 offset:17536\n"
"v_cvt_pk_bf16_f32 v219, v82, v83\n"
"v_mfma_f32_16x16x32_bf16 v[92:95], a[200:203], v[20:23], v[92:95]\n"
"s_waitcnt lgkmcnt(11)\n"
"ds_read_b64_tr_b16 a[146:147], v201 offset:22144\n"
"v_cvt_pk_bf16_f32 v222, v84, v85\n"
"v_mfma_f32_16x16x32_bf16 v[88:91], a[204:207], v[24:27], v[88:91]\n"
"ds_read_b64_tr_b16 a[148:149], v201 offset:26752\n"
"v_cvt_pk_bf16_f32 v223, v86, v87\n"
"v_mfma_f32_16x16x32_bf16 v[92:95], a[204:207], v[28:31], v[92:95]\n"
"ds_read_b64_tr_b16 a[150:151], v201 offset:31360\n"
"v_accvgpr_write_b32 a88, v168\n"
"v_mfma_f32_16x16x32_bf16 v[88:91], a[208:211], v[32:35], v[88:91]\n"
"ds_read_b64_tr_b16 a[152:153], v201 offset:35968\n"
"v_accvgpr_write_b32 a89, v169\n"
"v_mfma_f32_16x16x32_bf16 v[92:95], a[208:211], v[36:39], v[92:95]\n"
"ds_read_b64_tr_b16 a[154:155], v201 offset:40576\n"
"v_accvgpr_write_b32 a90, v170\n"
"v_mfma_f32_16x16x32_bf16 v[88:91], a[212:215], v[40:43], v[88:91]\n"
"ds_read_b64_tr_b16 a[156:157], v201 offset:45184\n"
"v_accvgpr_write_b32 a91, v171\n"
"v_mfma_f32_16x16x32_bf16 v[92:95], a[212:215], v[44:47], v[92:95]\n"
"ds_read_b64_tr_b16 a[158:159], v201 offset:49792\n"
"v_accvgpr_write_b32 a92, v172\n"
"v_mfma_f32_16x16x32_bf16 v[88:91], a[216:219], v[48:51], v[88:91]\n"
"ds_read_b64_tr_b16 a[192:193], v200 offset:46240\n"
"v_accvgpr_write_b32 a93, v173\n"
"v_mfma_f32_16x16x32_bf16 v[92:95], a[216:219], v[52:55], v[92:95]\n"
"s_waitcnt lgkmcnt(11)\n"
"ds_read_b64_tr_b16 a[194:195], v200 offset:50848\n"
"v_accvgpr_write_b32 a94, v174\n"
"v_mfma_f32_16x16x32_bf16 v[88:91], a[220:223], v[56:59], v[88:91]\n"
"ds_read_b64_tr_b16 a[196:197], v200 offset:55456\n"
"v_accvgpr_write_b32 a95, v175\n"
"v_mfma_f32_16x16x32_bf16 v[92:95], a[220:223], v[60:63], v[92:95]\n"
"ds_read_b64_tr_b16 a[198:199], v200 offset:60064\n"
"v_accvgpr_write_b32 a96, v176\n"
"v_mfma_f32_16x16x32_bf16 v[96:99], a[128:131], v[0:3], v[96:99]\n"
"ds_read_b64_tr_b16 a[200:201], v200 offset:64672\n"
"v_accvgpr_write_b32 a97, v177\n"
"v_mfma_f32_16x16x32_bf16 v[100:103], a[128:131], v[4:7], v[100:103]\n"
"ds_read_b64_tr_b16 a[202:203], v201 offset:3744\n"
"v_accvgpr_write_b32 a98, v178\n"
"v_mfma_f32_16x16x32_bf16 v[96:99], a[132:135], v[8:11], v[96:99]\n"
"ds_read_b64_tr_b16 a[204:205], v201 offset:8352\n"
"v_accvgpr_write_b32 a99, v179\n"
"v_mfma_f32_16x16x32_bf16 v[100:103], a[132:135], v[12:15], v[100:103]\n"
"ds_read_b64_tr_b16 a[206:207], v201 offset:12960\n"
"v_cvt_pk_bf16_f32 v220, v88, v89\n"
"v_mfma_f32_16x16x32_bf16 v[96:99], a[136:139], v[16:19], v[96:99]\n"
"ds_read_b64_tr_b16 a[208:209], v201 offset:17568\n"
"v_cvt_pk_bf16_f32 v221, v90, v91\n"
"v_mfma_f32_16x16x32_bf16 v[100:103], a[136:139], v[20:23], v[100:103]\n"
"s_waitcnt lgkmcnt(11)\n"
"ds_read_b64_tr_b16 a[210:211], v201 offset:22176\n"
"v_cvt_pk_bf16_f32 v224, v92, v93\n"
"v_mfma_f32_16x16x32_bf16 v[96:99], a[140:143], v[24:27], v[96:99]\n"
"ds_read_b64_tr_b16 a[212:213], v201 offset:26784\n"
"v_cvt_pk_bf16_f32 v225, v94, v95\n"
"v_mfma_f32_16x16x32_bf16 v[100:103], a[140:143], v[28:31], v[100:103]\n"
"ds_read_b64_tr_b16 a[214:215], v201 offset:31392\n"
"v_accvgpr_write_b32 a100, v180\n"
"v_mfma_f32_16x16x32_bf16 v[96:99], a[144:147], v[32:35], v[96:99]\n"
"ds_read_b64_tr_b16 a[216:217], v201 offset:36000\n"
"buffer_store_dwordx4 v[218:221], v207, s[28:31], s54 offen offset:1024 sc1\n"
"v_mfma_f32_16x16x32_bf16 v[100:103], a[144:147], v[36:39], v[100:103]\n"
"ds_read_b64_tr_b16 a[218:219], v201 offset:40608\n"
"buffer_store_dword v252, v251, s[28:31], 0 offen sc1\n"
"v_mfma_f32_16x16x32_bf16 v[96:99], a[148:151], v[40:43], v[96:99]\n"
"ds_read_b64_tr_b16 a[220:221], v201 offset:45216\n"
"buffer_store_dwordx4 v[222:225], v208, s[28:31], s54 offen offset:1024 sc1\n"
"v_mfma_f32_16x16x32_bf16 v[100:103], a[148:151], v[44:47], v[100:103]\n"
"ds_read_b64_tr_b16 a[222:223], v201 offset:49824\n"
"v_accvgpr_write_b32 a101, v181\n"
"v_mfma_f32_16x16x32_bf16 v[96:99], a[152:155], v[48:51], v[96:99]\n"
"ds_read_b64_tr_b16 a[128:129], v200 offset:46272\n"
"v_accvgpr_write_b32 a102, v182\n"
"v_mfma_f32_16x16x32_bf16 v[100:103], a[152:155], v[52:55], v[100:103]\n"
"s_waitcnt lgkmcnt(11)\n"
"ds_read_b64_tr_b16 a[130:131], v200 offset:50880\n"
"v_accvgpr_write_b32 a103, v183\n"
"v_mfma_f32_16x16x32_bf16 v[96:99], a[156:159], v[56:59], v[96:99]\n"
"ds_read_b64_tr_b16 a[132:133], v200 offset:55488\n"
"v_accvgpr_write_b32 a104, v184\n"
"v_mfma_f32_16x16x32_bf16 v[100:103], a[156:159], v[60:63], v[100:103]\n"
"ds_read_b64_tr_b16 a[134:135], v200 offset:60096\n"
"v_accvgpr_write_b32 a105, v185\n"
"v_mfma_f32_16x16x32_bf16 v[104:107], a[192:195], v[0:3], v[104:107]\n"
"ds_read_b64_tr_b16 a[136:137], v200 offset:64704\n"
"v_accvgpr_write_b32 a106, v186\n"
"v_mfma_f32_16x16x32_bf16 v[108:111], a[192:195], v[4:7], v[108:111]\n"
"ds_read_b64_tr_b16 a[138:139], v201 offset:3776\n"
"v_accvgpr_write_b32 a107, v187\n"
"v_mfma_f32_16x16x32_bf16 v[104:107], a[196:199], v[8:11], v[104:107]\n"
"ds_read_b64_tr_b16 a[140:141], v201 offset:8384\n"
"v_accvgpr_write_b32 a108, v188\n"
"v_mfma_f32_16x16x32_bf16 v[108:111], a[196:199], v[12:15], v[108:111]\n"
"ds_read_b64_tr_b16 a[142:143], v201 offset:12992\n"
"v_cvt_pk_bf16_f32 v218, v96, v97\n"
"v_mfma_f32_16x16x32_bf16 v[104:107], a[200:203], v[16:19], v[104:107]\n"
"ds_read_b64_tr_b16 a[144:145], v201 offset:17600\n"
"v_cvt_pk_bf16_f32 v219, v98, v99\n"
"v_mfma_f32_16x16x32_bf16 v[108:111], a[200:203], v[20:23], v[108:111]\n"
"s_waitcnt lgkmcnt(11)\n"
"ds_read_b64_tr_b16 a[146:147], v201 offset:22208\n"
"v_cvt_pk_bf16_f32 v222, v100, v101\n"
"v_mfma_f32_16x16x32_bf16 v[104:107], a[204:207], v[24:27], v[104:107]\n"
"ds_read_b64_tr_b16 a[148:149], v201 offset:26816\n"
"v_cvt_pk_bf16_f32 v223, v102, v103\n"
"v_mfma_f32_16x16x32_bf16 v[108:111], a[204:207], v[28:31], v[108:111]\n"
"s_waitcnt vmcnt(14)\n"
"ds_read_b64_tr_b16 a[150:151], v201 offset:31424\n"
"v_accvgpr_write_b32 a109, v189\n"
"v_mfma_f32_16x16x32_bf16 v[104:107], a[208:211], v[32:35], v[104:107]\n"
"s_barrier\n"
"ds_read_b64_tr_b16 a[152:153], v201 offset:36032\n"
"v_accvgpr_write_b32 a110, v190\n"
"v_mfma_f32_16x16x32_bf16 v[108:111], a[208:211], v[36:39], v[108:111]\n"
"ds_read_b64_tr_b16 a[154:155], v201 offset:40640\n"
"v_accvgpr_write_b32 a111, v191\n"
"v_mfma_f32_16x16x32_bf16 v[104:107], a[212:215], v[40:43], v[104:107]\n"
"ds_read_b64_tr_b16 a[156:157], v201 offset:45248\n"
"v_accvgpr_write_b32 a112, v192\n"
"v_mfma_f32_16x16x32_bf16 v[108:111], a[212:215], v[44:47], v[108:111]\n"
"ds_read_b64_tr_b16 a[158:159], v201 offset:49856\n"
"v_accvgpr_write_b32 a113, v193\n"
"v_mfma_f32_16x16x32_bf16 v[104:107], a[216:219], v[48:51], v[104:107]\n"
"ds_read_b64_tr_b16 a[192:193], v200 offset:46304\n"
"v_accvgpr_write_b32 a114, v194\n"
"v_mfma_f32_16x16x32_bf16 v[108:111], a[216:219], v[52:55], v[108:111]\n"
"s_waitcnt lgkmcnt(11)\n"
"ds_read_b64_tr_b16 a[194:195], v200 offset:50912\n"
"v_accvgpr_write_b32 a115, v195\n"
"v_mfma_f32_16x16x32_bf16 v[104:107], a[220:223], v[56:59], v[104:107]\n"
"ds_read_b64_tr_b16 a[196:197], v200 offset:55520\n"
"v_accvgpr_write_b32 a116, v196\n"
"v_mfma_f32_16x16x32_bf16 v[108:111], a[220:223], v[60:63], v[108:111]\n"
"ds_read_b64_tr_b16 a[198:199], v200 offset:60128\n"
"v_accvgpr_write_b32 a117, v197\n"
"v_mfma_f32_16x16x32_bf16 v[112:115], a[128:131], v[0:3], v[112:115]\n"
"ds_read_b64_tr_b16 a[200:201], v200 offset:64736\n"
"v_accvgpr_write_b32 a118, v198\n"
"v_mfma_f32_16x16x32_bf16 v[116:119], a[128:131], v[4:7], v[116:119]\n"
"ds_read_b64_tr_b16 a[202:203], v201 offset:3808\n"
"v_accvgpr_write_b32 a119, v199\n"
"v_mfma_f32_16x16x32_bf16 v[112:115], a[132:135], v[8:11], v[112:115]\n"
"ds_read_b64_tr_b16 a[204:205], v201 offset:8416\n"
"v_mfma_f32_16x16x32_bf16 v[116:119], a[132:135], v[12:15], v[116:119]\n"
"ds_read_b64_tr_b16 a[206:207], v201 offset:13024\n"
"v_cvt_pk_bf16_f32 v220, v104, v105\n"
"v_mfma_f32_16x16x32_bf16 v[112:115], a[136:139], v[16:19], v[112:115]\n"
"ds_read_b64_tr_b16 a[208:209], v201 offset:17632\n"
"v_cvt_pk_bf16_f32 v221, v106, v107\n"
"v_mfma_f32_16x16x32_bf16 v[116:119], a[136:139], v[20:23], v[116:119]\n"
"s_waitcnt lgkmcnt(11)\n"
"ds_read_b64_tr_b16 a[210:211], v201 offset:22240\n"
"v_cvt_pk_bf16_f32 v224, v108, v109\n"
"v_mfma_f32_16x16x32_bf16 v[112:115], a[140:143], v[24:27], v[112:115]\n"
"ds_read_b64_tr_b16 a[212:213], v201 offset:26848\n"
"v_cvt_pk_bf16_f32 v225, v110, v111\n"
"v_mfma_f32_16x16x32_bf16 v[116:119], a[140:143], v[28:31], v[116:119]\n"
"ds_read_b64_tr_b16 a[214:215], v201 offset:31456\n"
"v_mfma_f32_16x16x32_bf16 v[112:115], a[144:147], v[32:35], v[112:115]\n"
"ds_read_b64_tr_b16 a[216:217], v201 offset:36064\n"
"buffer_store_dwordx4 v[218:221], v207, s[28:31], s54 offen offset:2048 sc1\n"
"v_mfma_f32_16x16x32_bf16 v[116:119], a[144:147], v[36:39], v[116:119]\n"
"ds_read_b64_tr_b16 a[218:219], v201 offset:40672\n"
"v_mfma_f32_16x16x32_bf16 v[112:115], a[148:151], v[40:43], v[112:115]\n"
"ds_read_b64_tr_b16 a[220:221], v201 offset:45280\n"
"buffer_store_dwordx4 v[222:225], v208, s[28:31], s54 offen offset:2048 sc1\n"
"v_mfma_f32_16x16x32_bf16 v[116:119], a[148:151], v[44:47], v[116:119]\n"
"ds_read_b64_tr_b16 a[222:223], v201 offset:49888\n"
"v_mfma_f32_16x16x32_bf16 v[112:115], a[152:155], v[48:51], v[112:115]\n"
"s_nop 0\n"
"v_mfma_f32_16x16x32_bf16 v[116:119], a[152:155], v[52:55], v[116:119]\n"
"s_waitcnt lgkmcnt(10)\n"
"v_mfma_f32_16x16x32_bf16 v[112:115], a[156:159], v[56:59], v[112:115]\n"
"s_nop 0\n"
"v_mfma_f32_16x16x32_bf16 v[116:119], a[156:159], v[60:63], v[116:119]\n"
"s_nop 0\n"
"v_mfma_f32_16x16x32_bf16 v[120:123], a[192:195], v[0:3], v[120:123]\n"
"s_nop 0\n"
"v_mfma_f32_16x16x32_bf16 v[124:127], a[192:195], v[4:7], v[124:127]\n"
"s_nop 0\n"
"v_mfma_f32_16x16x32_bf16 v[120:123], a[196:199], v[8:11], v[120:123]\n"
"s_nop 0\n"
"v_mfma_f32_16x16x32_bf16 v[124:127], a[196:199], v[12:15], v[124:127]\n"
"v_cvt_pk_bf16_f32 v218, v112, v113\n"
"v_mfma_f32_16x16x32_bf16 v[120:123], a[200:203], v[16:19], v[120:123]\n"
"v_cvt_pk_bf16_f32 v219, v114, v115\n"
"v_mfma_f32_16x16x32_bf16 v[124:127], a[200:203], v[20:23], v[124:127]\n"
"s_waitcnt lgkmcnt(2)\n"
"v_cvt_pk_bf16_f32 v222, v116, v117\n"
"v_mfma_f32_16x16x32_bf16 v[120:123], a[204:207], v[24:27], v[120:123]\n"
"v_cvt_pk_bf16_f32 v223, v118, v119\n"
"v_mfma_f32_16x16x32_bf16 v[124:127], a[204:207], v[28:31], v[124:127]\n"
"s_nop 0\n"
"v_mfma_f32_16x16x32_bf16 v[120:123], a[208:211], v[32:35], v[120:123]\n"
"s_nop 0\n"
"v_mfma_f32_16x16x32_bf16 v[124:127], a[208:211], v[36:39], v[124:127]\n"
"s_nop 0\n"
"v_mfma_f32_16x16x32_bf16 v[120:123], a[212:215], v[40:43], v[120:123]\n"
"s_nop 0\n"
"v_mfma_f32_16x16x32_bf16 v[124:127], a[212:215], v[44:47], v[124:127]\n"
"s_nop 0\n"
"v_mfma_f32_16x16x32_bf16 v[120:123], a[216:219], v[48:51], v[120:123]\n"
"s_nop 0\n"
"v_mfma_f32_16x16x32_bf16 v[124:127], a[216:219], v[52:55], v[124:127]\n"
"s_waitcnt lgkmcnt(0)\n"
"v_mfma_f32_16x16x32_bf16 v[120:123], a[220:223], v[56:59], v[120:123]\n"
"s_nop 0\n"
"v_mfma_f32_16x16x32_bf16 v[124:127], a[220:223], v[60:63], v[124:127]\n"
"s_nop 0\n"
"s_nop 1\n"
"s_nop 1\n"
"s_nop 1\n"
"s_nop 1\n"
"v_cvt_pk_bf16_f32 v220, v120, v121\n"
"s_nop 1\n"
"v_cvt_pk_bf16_f32 v221, v122, v123\n"
"s_nop 1\n"
"v_cvt_pk_bf16_f32 v224, v124, v125\n"
"s_nop 1\n"
"v_cvt_pk_bf16_f32 v225, v126, v127\n"
"s_nop 1\n"
"s_nop 1\n"
"buffer_store_dwordx4 v[218:221], v207, s[28:31], s54 offen offset:3072 sc1\n"
"s_nop 1\n"
"s_nop 1\n"
"buffer_store_dwordx4 v[222:225], v208, s[28:31], s54 offen offset:3072 sc1\n"
"s_nop 1\n"
"s_add_u32 s56, s40, 161024\n"
"s_nop 1\n"
"v_mov_b32 v227, s56\n"
"v_mov_b32 v224, 0x3f80\n"
"v_mov_b32 v225, 0x3f800000\n"
"v_and_b32 v226, 7, v248\n"
"v_lshl_add_u32 v226, v226, 4, v227\n"
"s_mov_b32 s14, 0xfffff800\n"
"s_cmp_lg_u32 s41, 3\n"
"s_cbranch_scc1 AT_A0\n"
"s_mov_b64 s[8:9], 0\n"
"s_mov_b64 exec, 0\n"
"s_call_b64 %[kp], AT_pg18\n"
"s_mov_b64 exec, -1\n"
"s_mul_i32 s63, s41, 9216\n"
"s_add_u32 s63, s63, s40\n"
"s_add_u32 s63, s63, 9216\n"
"v_lshrrev_b32 v185, 2, v248\n"
"v_and_b32 v186, 3, v248\n"
"v_mul_u32_u24 v187, 96, v185\n"
"v_lshl_add_u32 v188, v186, 4, s63\n"
"v_lshl_add_u32 v189, v187, 1, v188\n"
"v_add_u32 v188, v187, v188\n"
"v_lshrrev_b32 v190, 4, v248\n"
"v_mul_u32_u24 v191, 384, v190\n"
"v_add_u32 v187, v187, v191\n"
"v_lshl_add_u32 v187, v186, 3, v187\n"
"v_add_u32 v187, s63, v187\n"
"s_mov_b32 exec_lo, 0x77777777\n"
"s_mov_b32 exec_hi, 0x77777777\n"
"ds_write_b128 v189, a[48:51] offset:0\n"
"ds_write_b128 v189, a[84:87] offset:48\n"
"ds_write_b128 v189, a[52:55] offset:96\n"
"ds_write_b128 v189, a[88:91] offset:144\n"
"ds_write_b128 v189, a[56:59] offset:3072\n"
"ds_write_b128 v189, a[92:95] offset:3120\n"
"ds_write_b128 v189, a[60:63] offset:3168\n"
"ds_write_b128 v189, a[96:99] offset:3216\n"
"ds_write_b128 v189, a[64:67] offset:6144\n"
"ds_write_b128 v189, a[100:103] offset:6192\n"
"ds_write_b128 v189, a[68:71] offset:6240\n"
"ds_write_b128 v189, a[104:107] offset:6288\n"
"s_mov_b64 exec, -1\n"
"ds_read_b64_tr_b16 a[128:129], v187 offset:0\n"
"ds_read_b64_tr_b16 a[130:131], v187 offset:384\n"
"ds_read_b64_tr_b16 a[132:133], v187 offset:32\n"
"ds_read_b64_tr_b16 a[134:135], v187 offset:416\n"
"ds_read_b64_tr_b16 a[136:137], v187 offset:64\n"
"ds_read_b64_tr_b16 a[138:139], v187 offset:448\n"
"ds_read_b64_tr_b16 a[140:141], v187 offset:3072\n"
"ds_read_b64_tr_b16 a[142:143], v187 offset:3456\n"
"ds_read_b64_tr_b16 a[144:145], v187 offset:3104\n"
"ds_read_b64_tr_b16 a[146:147], v187 offset:3488\n"
"ds_read_b64_tr_b16 a[148:149], v187 offset:3136\n"
"ds_read_b64_tr_b16 a[150:151], v187 offset:3520\n"
"ds_read_b64_tr_b16 a[152:153], v187 offset:6144\n"
"ds_read_b64_tr_b16 a[154:155], v187 offset:6528\n"
"ds_read_b64_tr_b16 a[156:157], v187 offset:6176\n"
"ds_read_b64_tr_b16 a[158:159], v187 offset:6560\n"
"ds_read_b64_tr_b16 a[160:161], v187 offset:6208\n"
"ds_read_b64_tr_b16 a[162:163], v187 offset:6592\n"
"s_mov_b32 exec_lo, 0x77777777\n"
"s_mov_b32 exec_hi, 0x77777777\n"
"ds_write_b128 v189, a[72:75] offset:0\n"
"ds_write_b128 v189, a[108:111] offset:48\n"
"ds_write_b128 v189, a[76:79] offset:96\n"
"ds_write_b128 v189, a[112:115] offset:144\n"
"ds_write_b128 v188, a[80:83] offset:3072\n"
"ds_write_b128 v188, a[116:119] offset:3120\n"
"s_mov_b64 exec, -1\n"
"ds_read_b64_tr_b16 a[164:165], v187 offset:0\n"
"ds_read_b64_tr_b16 a[166:167], v187 offset:384\n"
"ds_read_b64_tr_b16 a[168:169], v187 offset:32\n"
"ds_read_b64_tr_b16 a[170:171], v187 offset:416\n"
"ds_read_b64_tr_b16 a[172:173], v187 offset:64\n"
"ds_read_b64_tr_b16 a[174:175], v187 offset:448\n"
"ds_read_b64_tr_b16 a[176:177], v187 offset:3072\n"
"ds_read_b64_tr_b16 a[178:179], v187 offset:3456\n"
"ds_read_b64_tr_b16 a[180:181], v187 offset:3104\n"
"ds_read_b64_tr_b16 a[182:183], v187 offset:3488\n"
"ds_read_b64_tr_b16 a[184:185], v187 offset:3136\n"
"ds_read_b64_tr_b16 a[186:187], v187 offset:3520\n"
"v_and_b32 v241, 1, v248\n"
"v_mul_u32_u24 v241, 1152, v241\n"
"v_lshl_add_u32 v241, v190, 4, v241\n"
"s_mul_i32 s63, s41, 288\n"
"v_add_u32 v241, s63, v241\n"
"v_and_b32 v242, 10, v248\n"
"v_lshl_add_u32 v241, v242, 26, v241\n"
"v_and_b32 v242, 32, v248\n"
"v_lshlrev_b32 v242, 25, v242\n"
"s_add_u32 s63, s63, s40\n"
"s_add_u32 s63, s63, 131072\n"
"v_cmp_eq_u32 vcc, 3, v190\n"
"v_and_b32 v185, 3, v185\n"
"v_lshlrev_b32 v191, 3, v190\n"
"v_cndmask_b32_e64 v191, v191, 0, vcc\n"
"v_add_u32 v191, v191, v185\n"
"AT_pg17:\n"
"s_cbranch_execz AT_hr\n"
"v_mul_u32_u24 v191, 1152, v191\n"
"v_lshl_add_u32 v191, v186, 3, v191\n"
"v_add_u32 v255, s63, v191\n"
"v_lshrrev_b32 v243, 3, v248\n"
"v_mul_u32_u24 v243, 24576, v243\n"
"v_and_b32 v247, 7, v248\n"
"v_lshl_add_u32 v243, v247, 4, v243\n"
"v_mul_u32_u24 v224, 24576, v190\n"
"v_and_b32 v246, 15, v248\n"
"v_lshl_add_u32 v224, v246, 3, v224\n"
"v_or_b32 v224, v242, v224\n"
"v_subrev_u32 v252, 65536, v227\n"
"s_mul_i32 s63, s41, 384\n"
"v_mul_u32_u24 v244, 192, v190\n"
"v_lshl_add_u32 v244, v246, 2, v244\n"
"v_add3_u32 v244, v244, v252, s63\n"
"v_cmp_lt_u32 vcc, 23, v248\n"
"v_lshl_add_u32 v249, v248, 2, v252\n"
"v_add_u32 v185, 96, v249\n"
"v_cndmask_b32 v249, v249, v185, vcc\n"
"s_lshl_b32 s63, s41, 9\n"
"v_add_u32 v252, s63, v252\n"
"v_lshl_add_u32 v251, v248, 1, v252\n"
"v_add_u32 v185, 208, v251\n"
"v_cndmask_b32 v251, v251, v185, vcc\n"
"v_lshl_add_u32 v185, v248, 3, v252\n"
"v_lshl_add_u32 v252, v247, 6, v252\n"
"v_lshl_add_u32 v252, v190, 4, v252\n"
"s_min_u32 s63, s41, 2\n"
"s_mul_i32 s63, s63, 192\n"
"v_add_u32 v183, s63, v248\n"
"v_mul_u32_u24 v183, 911, v183\n"
"s_lshl_b32 s63, s55, 3\n"
"v_lshrrev_b32 v180, 16, v183\n"
"v_add_u32 v180, s63, v180\n"
"v_add_u32 v183, 58304, v183\n"
"v_lshrrev_b32 v181, 16, v183\n"
"v_add_u32 v181, s63, v181\n"
"v_add_u32 v183, 58304, v183\n"
"v_lshrrev_b32 v182, 16, v183\n"
"v_add_u32 v182, s63, v182\n"
"v_lshlrev_b32 v250, 4, v248\n"
"v_add_u32 v184, s45, v250\n"
"s_cmp_eq_u32 s41, 3\n"
"s_cselect_b32 s63, 0x10000000, 0\n"
"v_or_b32 v250, s63, v250\n"
"ds_read_b128 v[212:215], v227 offset:512\n"
"ds_read_b128 v[216:219], v227 offset:528\n"
"s_lshl_b32 s56, s55, 3\n"
"s_sub_u32 s56, 2047, s56\n"
"v_and_b32 v220, 7, v248\n"
"v_sub_u32 v220, s56, v220\n"
"s_waitcnt lgkmcnt(0)\n"
"v_add_f32 v212, v212, v214\n"
"v_add_f32 v213, v213, v215\n"
"v_add_f32 v212, v212, v216\n"
"v_add_f32 v213, v213, v217\n"
"v_mov_b32 v145, s85\n"
"v_mov_b32 v147, s85\n"
"s_mul_i32 s56, s55, 64\n"
"s_add_u32 s56, s56, 26370048\n"
"v_mov_b32 v148, 0\n"
"v_mul_u32_u24 v149, 64, v248\n"
"s_mov_b32 s57, 26370048\n"
"s_mov_b32 s58, 26374144\n"
"s_mov_b32 s59, 26378240\n"
"s_mov_b32 s60, 26382336\n"
"s_mov_b32 s87, 0x80000\n"
"s_waitcnt vmcnt(0)\n"
"s_barrier\n"
"ds_read_b128 v[200:203], v226 offset:0\n"
"ds_read_b128 v[204:207], v226 offset:128\n"
"ds_read_b128 v[208:211], v226 offset:256\n"
"s_waitcnt lgkmcnt(1)\n"
"v_add_f32 v200, v200, v204\n"
"v_add_f32 v201, v201, v205\n"
"v_add_f32 v202, v202, v206\n"
"s_waitcnt lgkmcnt(0)\n"
"v_add_f32 v200, v200, v208\n"
"v_add_f32 v201, v201, v209\n"
"v_add_f32 v202, v202, v210\n"
"v_mul_f32 v200, 0x3ae38e39, v200\n"
"v_mul_f32 v201, 0x3ae38e39, v201\n"
"v_fma_f32 v201, -v200, v200, v201\n"
"v_max_f32 v201, 0, v201\n"
"v_add_f32 v201, 0x3727c5ac, v201\n"
"v_rsq_f32 v192, v201\n"
"v_fma_f32 v202, -v200, v212, v202\n"
"v_fma_f32 v196, v192, v202, v213\n"
"v_ashrrev_i32 v203, 31, v196\n"
"v_or_b32 v203, 0x80000000, v203\n"
"v_xor_b32 v196, v196, v203\n"
"v_and_or_b32 v196, v196, s14, v220\n"
"v_mul_f32 v194, v200, v192\n"
"v_sub_f32 v194, 0, v194\n"
"v_mov_b32_dpp v203, v196 row_ror:1 row_mask:0xf bank_mask:0xf\n"
"v_min_u32 v198, v196, v203\n"
"v_max_u32 v197, v196, v203\n"
"s_nop 0\n"
"v_mov_b32_dpp v204, v198 row_ror:2 row_mask:0xf bank_mask:0xf\n"
"v_mov_b32_dpp v203, v197 row_ror:2 row_mask:0xf bank_mask:0xf\n"
"v_min_u32 v205, v197, v203\n"
"v_max_u32 v197, v197, v203\n"
"v_max3_u32 v198, v198, v204, v205\n"
"s_nop 0\n"
"v_mov_b32_dpp v203, v197 row_ror:4 row_mask:0xf bank_mask:0xf\n"
"v_mov_b32_dpp v204, v198 row_ror:4 row_mask:0xf bank_mask:0xf\n"
"v_min_u32 v205, v197, v203\n"
"v_max_u32 v197, v197, v203\n"
"v_max3_u32 v198, v198, v204, v205\n"
"v_mov_b32 v144, v197\n"
"v_mov_b32 v146, v198\n"
"s_mov_b64 exec, 1\n"
"buffer_store_dwordx4 v[144:147], v148, s[28:31], s56 offen sc1\n"
"s_mov_b64 exec, -1\n"
"AT_S2:\n"
"buffer_load_dwordx4 v[152:155], v149, s[28:31], s57 offen sc1\n"
"buffer_load_dwordx4 v[156:159], v149, s[28:31], s58 offen sc1\n"
"buffer_load_dwordx4 v[160:163], v149, s[28:31], s59 offen sc1\n"
"buffer_load_dwordx4 v[164:167], v149, s[28:31], s60 offen sc1\n"
"s_waitcnt vmcnt(0)\n"
"v_xor_b32 v150, s85, v153\n"
"v_xor_b32 v151, s85, v155\n"
"v_bitop3_b32 v150, v157, s85, v150 bitop3:0xbe\n"
"v_bitop3_b32 v151, v159, s85, v151 bitop3:0xbe\n"
"v_bitop3_b32 v150, v161, s85, v150 bitop3:0xbe\n"
"v_bitop3_b32 v151, v163, s85, v151 bitop3:0xbe\n"
"v_bitop3_b32 v150, v165, s85, v150 bitop3:0xbe\n"
"v_bitop3_b32 v151, v167, s85, v151 bitop3:0xbe\n"
"v_or_b32 v150, v150, v151\n"
"v_cmp_ne_u32 vcc, 0, v150\n"
"s_nop 0\n"
"s_cbranch_vccz AT_S3\n"
"s_sub_u32 s87, s87, 1\n"
"s_cmp_eq_u32 s87, 0\n"
"s_cbranch_scc1 AT_S3\n"
"s_sleep 1\n"
"s_branch AT_S2\n"
"AT_S3:\n"
"v_min_u32 v205, v152, v156\n"
"v_max_u32 v152, v152, v156\n"
"v_max3_u32 v154, v154, v158, v205\n"
"v_min_u32 v205, v160, v164\n"
"v_max_u32 v160, v160, v164\n"
"v_max3_u32 v162, v162, v166, v205\n"
"v_min_u32 v205, v152, v160\n"
"v_max_u32 v152, v152, v160\n"
"v_max3_u32 v154, v154, v162, v205\n"
"s_nop 0\n"
"v_mov_b32_dpp v156, v152 row_ror:1 row_mask:0xf bank_mask:0xf\n"
"v_mov_b32_dpp v158, v154 row_ror:1 row_mask:0xf bank_mask:0xf\n"
"v_min_u32 v205, v152, v156\n"
"v_max_u32 v152, v152, v156\n"
"v_max3_u32 v154, v154, v158, v205\n"
"s_nop 0\n"
"v_mov_b32_dpp v156, v152 row_ror:2 row_mask:0xf bank_mask:0xf\n"
"v_mov_b32_dpp v158, v154 row_ror:2 row_mask:0xf bank_mask:0xf\n"
"v_min_u32 v205, v152, v156\n"
"v_max_u32 v152, v152, v156\n"
"v_max3_u32 v154, v154, v158, v205\n"
"s_nop 0\n"
"v_mov_b32_dpp v156, v152 row_ror:4 row_mask:0xf bank_mask:0xf\n"
"v_mov_b32_dpp v158, v154 row_ror:4 row_mask:0xf bank_mask:0xf\n"
"v_min_u32 v205, v152, v156\n"
"v_max_u32 v152, v152, v156\n"
"v_max3_u32 v154, v154, v158, v205\n"
"s_nop 0\n"
"v_mov_b32_dpp v156, v152 row_ror:8 row_mask:0xf bank_mask:0xf\n"
"v_mov_b32_dpp v158, v154 row_ror:8 row_mask:0xf bank_mask:0xf\n"
"v_min_u32 v205, v152, v156\n"
"v_max_u32 v152, v152, v156\n"
"v_max3_u32 v154, v154, v158, v205\n"
"v_mov_b32 v156, v152\n"
"v_mov_b32 v158, v154\n"
"s_nop 0\n"
"v_permlane16_swap_b32 v152, v156\n"
"v_permlane16_swap_b32 v154, v158\n"
"v_min_u32 v205, v152, v156\n"
"v_max_u32 v152, v152, v156\n"
"v_max3_u32 v154, v154, v158, v205\n"
"v_mov_b32 v156, v152\n"
"v_mov_b32 v158, v154\n"
"s_nop 0\n"
"v_permlane32_swap_b32 v152, v156\n"
"v_permlane32_swap_b32 v154, v158\n"
"v_min_u32 v205, v152, v156\n"
"v_max_u32 v152, v152, v156\n"
"v_max3_u32 v154, v154, v158, v205\n"
"v_mov_b32 v153, v154\n"
"s_mov_b64 exec, 1\n"
"ds_write_b64 v227, v[152:153] offset:656\n"
"s_mov_b64 exec, -1\n"
"s_branch AT_B3\n"
"AT_A0:\n"
"AT_pgA0:\n"
"s_cbranch_execz AT_hr\n"
"v_and_b32 v192, 15, v248\n"
"v_lshlrev_b32 v192, 6, v192\n"
"s_lshl_b32 s57, s44, 10\n"
"buffer_load_dword v193, v192, s[28:31], s57 offen sc1\n"
"ds_read_b64 v[228:229], v240 offset:0\n"
"ds_read_b64 v[230:231], v240 offset:32\n"
"ds_read_b64 v[232:233], v240 offset:384\n"
"ds_read_b64 v[234:235], v240 offset:416\n"
"ds_read_b64 v[236:237], v240 offset:768\n"
"ds_read_b64 v[238:239], v240 offset:800\n"
"ds_read_b128 v[96:99], v244 offset:0\n"
"ds_read_b128 v[100:103], v244 offset:64\n"
"ds_read_b128 v[104:107], v244 offset:768\n"
"ds_read_b128 v[108:111], v244 offset:832\n"
"ds_read_b128 v[112:115], v244 offset:1536\n"
"ds_read_b128 v[116:119], v244 offset:1600\n"
"ds_read_b128 v[120:123], v244 offset:2304\n"
"ds_read_b128 v[124:127], v244 offset:2368\n"
"ds_read_b128 v[128:131], v244 offset:3072\n"
"ds_read_b128 v[132:135], v244 offset:3136\n"
"ds_read_b128 v[136:139], v244 offset:3840\n"
"ds_read_b128 v[140:143], v244 offset:3904\n"
"v_mov_b32 v48, 0\n"
"v_mov_b32 v49, 0\n"
"v_mov_b32 v50, 0\n"
"v_mov_b32 v51, 0\n"
"v_mov_b32 v52, 0\n"
"v_mov_b32 v53, 0\n"
"v_mov_b32 v54, 0\n"
"v_mov_b32 v55, 0\n"
"v_mov_b32 v56, 0\n"
"v_mov_b32 v57, 0\n"
"v_mov_b32 v58, 0\n"
"v_mov_b32 v59, 0\n"
"v_mov_b32 v60, 0\n"
"v_mov_b32 v61, 0\n"
"v_mov_b32 v62, 0\n"
"v_mov_b32 v63, 0\n"
"v_mov_b32 v64, 0\n"
"v_mov_b32 v65, 0\n"
"v_mov_b32 v66, 0\n"
"v_mov_b32 v67, 0\n"
"v_mov_b32 v68, 0\n"
"v_mov_b32 v69, 0\n"
"v_mov_b32 v70, 0\n"
"v_mov_b32 v71, 0\n"
"v_mov_b32 v72, 0\n"
"v_mov_b32 v73, 0\n"
"v_mov_b32 v74, 0\n"
"v_mov_b32 v75, 0\n"
"v_mov_b32 v76, 0\n"
"v_mov_b32 v77, 0\n"
"v_mov_b32 v78, 0\n"
"v_mov_b32 v79, 0\n"
"v_mov_b32 v80, 0\n"
"v_mov_b32 v81, 0\n"
"v_mov_b32 v82, 0\n"
"v_mov_b32 v83, 0\n"
"v_mov_b32 v84, 0\n"
"v_mov_b32 v85, 0\n"
"v_mov_b32 v86, 0\n"
"v_mov_b32 v87, 0\n"
"v_mov_b32 v88, 0\n"
"v_mov_b32 v89, 0\n"
"v_mov_b32 v90, 0\n"
"v_mov_b32 v91, 0\n"
"v_mov_b32 v92, 0\n"
"v_mov_b32 v93, 0\n"
"v_mov_b32 v94, 0\n"
"v_mov_b32 v95, 0\n"
"ds_read_b128 v[212:215], v227 offset:512\n"
"ds_read_b128 v[216:219], v227 offset:528\n"
"s_lshl_b32 s56, s55, 3\n"
"s_sub_u32 s56, 2047, s56\n"
"v_and_b32 v220, 7, v248\n"
"v_sub_u32 v220, s56, v220\n"
"s_waitcnt lgkmcnt(0)\n"
"v_add_f32 v212, v212, v214\n"
"v_add_f32 v213, v213, v215\n"
"v_add_f32 v212, v212, v216\n"
"v_add_f32 v213, v213, v217\n"
"s_mul_i32 s56, s41, 1536\n"
"s_add_u32 s56, s56, s40\n"
"s_add_u32 s56, s56, 49152\n"
"v_and_b32 v168, 32, v248\n"
"v_lshl_add_u32 v168, v168, 2, s56\n"
"v_bfe_u32 v169, v248, 3, 2\n"
"v_lshl_add_u32 v168, v169, 4, v168\n"
"v_lshl_add_u32 v169, v248, 5, s56\n"
"v_lshrrev_b32 v170, 3, v248\n"
"v_mul_u32_u24 v171, 43, v170\n"
"v_lshrrev_b32 v171, 7, v171\n"
"v_mul_u32_u24 v172, 3, v171\n"
"v_sub_u32 v170, v170, v172\n"
"v_mul_u32_u24 v170, 384, v170\n"
"v_and_b32 v172, 7, v248\n"
"v_lshl_add_u32 v170, v172, 4, v170\n"
"v_and_b32 v171, 2, v171\n"
"v_lshl_add_u32 v170, v171, 27, v170\n"
"s_lshl_b32 s56, s41, 7\n"
"v_add_u32 v170, s56, v170\n"
"s_mov_b32 s52, 0\n"
"s_mov_b32 s87, 0x80000\n"
"v_lshlrev_b32 v144, 16, v228\n"
"v_and_b32 v145, 0xffff0000, v228\n"
"v_lshlrev_b32 v146, 16, v229\n"
"v_and_b32 v147, 0xffff0000, v229\n"
"v_lshlrev_b32 v148, 16, v230\n"
"v_and_b32 v149, 0xffff0000, v230\n"
"v_lshlrev_b32 v150, 16, v231\n"
"v_and_b32 v151, 0xffff0000, v231\n"
"v_lshlrev_b32 v152, 16, v232\n"
"v_and_b32 v153, 0xffff0000, v232\n"
"v_lshlrev_b32 v154, 16, v233\n"
"v_and_b32 v155, 0xffff0000, v233\n"
"v_lshlrev_b32 v156, 16, v234\n"
"v_and_b32 v157, 0xffff0000, v234\n"
"v_lshlrev_b32 v158, 16, v235\n"
"v_and_b32 v159, 0xffff0000, v235\n"
"v_lshlrev_b32 v160, 16, v236\n"
"v_and_b32 v161, 0xffff0000, v236\n"
"v_lshlrev_b32 v162, 16, v237\n"
"v_and_b32 v163, 0xffff0000, v237\n"
"v_lshlrev_b32 v164, 16, v238\n"
"v_and_b32 v165, 0xffff0000, v238\n"
"v_lshlrev_b32 v166, 16, v239\n"
"v_and_b32 v167, 0xffff0000, v239\n"
"s_waitcnt vmcnt(0)\n"
"AT_A1:\n"
"v_cmp_eq_u32 vcc, s85, v193\n"
"s_sub_u32 s87, s87, 1\n"
"s_lshr_b32 s53, vcc_lo, 1\n"
"s_and_b32 s53, s53, vcc_lo\n"
"s_lshr_b32 s59, s53, 2\n"
"s_and_b32 s53, s53, s59\n"
"s_and_b32 s53, s53, 0x1111\n"
"s_cmp_lt_i32 s87, 0\n"
"s_cselect_b32 s53, 0x1111, s53\n"
"s_andn2_b32 s53, s53, s52\n"
"s_cmp_lg_u32 s53, 0\n"
"s_cbranch_scc1 AT_A2\n"
"s_sleep 2\n"
"AT_A1b:\n"
"buffer_load_dword v193, v192, s[28:31], s57 offen sc1\n"
"s_waitcnt vmcnt(0)\n"
"s_branch AT_A1\n"
"AT_A2:\n"
"s_bcnt1_i32_b32 s54, s53\n"
"s_bitcmp1_b32 s53, 0\n"
"s_cbranch_scc0 AT_Ai0\n"
"s_mov_b32 s56, s47\n"
"buffer_load_dwordx4 v[0:3], v241, s[28:31], s56 offen sc1\n"
"buffer_load_dwordx4 v[4:7], v242, s[28:31], s56 offen sc1\n"
"buffer_load_dwordx4 v[8:11], v243, s[28:31], s56 offen sc1\n"
"AT_Ai0:\n"
"s_bitcmp1_b32 s53, 4\n"
"s_cbranch_scc0 AT_Ai1\n"
"s_add_u32 s56, s47, 2359296\n"
"buffer_load_dwordx4 v[12:15], v241, s[28:31], s56 offen sc1\n"
"buffer_load_dwordx4 v[16:19], v242, s[28:31], s56 offen sc1\n"
"buffer_load_dwordx4 v[20:23], v243, s[28:31], s56 offen sc1\n"
"AT_Ai1:\n"
"s_bitcmp1_b32 s53, 8\n"
"s_cbranch_scc0 AT_Ai2\n"
"s_add_u32 s56, s47, 4718592\n"
"buffer_load_dwordx4 v[24:27], v241, s[28:31], s56 offen sc1\n"
"buffer_load_dwordx4 v[28:31], v242, s[28:31], s56 offen sc1\n"
"buffer_load_dwordx4 v[32:35], v243, s[28:31], s56 offen sc1\n"
"AT_Ai2:\n"
"s_bitcmp1_b32 s53, 12\n"
"s_cbranch_scc0 AT_Ai3\n"
"s_add_u32 s56, s47, 7077888\n"
"buffer_load_dwordx4 v[36:39], v241, s[28:31], s56 offen sc1\n"
"buffer_load_dwordx4 v[40:43], v242, s[28:31], s56 offen sc1\n"
"buffer_load_dwordx4 v[44:47], v243, s[28:31], s56 offen sc1\n"
"AT_Ai3:\n"
"s_bitcmp1_b32 s53, 0\n"
"s_cbranch_scc0 AT_Ar0\n"
"s_sub_u32 s54, s54, 1\n"
"s_cmp_eq_u32 s54, 0\n"
"s_cbranch_scc1 AT_Aw0_0\n"
"s_cmp_eq_u32 s54, 1\n"
"s_cbranch_scc1 AT_Aw0_1\n"
"s_cmp_eq_u32 s54, 2\n"
"s_cbranch_scc1 AT_Aw0_2\n"
"s_waitcnt vmcnt(9)\n"
"s_branch AT_Ax0\n"
"AT_Aw0_2:\n"
"s_waitcnt vmcnt(6)\n"
"s_branch AT_Ax0\n"
"AT_Aw0_1:\n"
"s_waitcnt vmcnt(3)\n"
"s_branch AT_Ax0\n"
"AT_Aw0_0:\n"
"s_waitcnt vmcnt(0)\n"
"AT_Ax0:\n"
"v_max3_u32 v194, v0, v1, v2\n"
"v_max3_u32 v195, v3, v4, v5\n"
"v_max3_u32 v194, v194, v6, v7\n"
"v_max3_u32 v195, v195, v8, v9\n"
"v_max3_u32 v194, v194, v10, v11\n"
"v_max_u32 v194, v194, v195\n"
"v_cmp_eq_u32 vcc, -1, v194\n"
"s_cmp_lt_i32 s87, 0\n"
"s_cselect_b64 vcc, 0, vcc\n"
"s_cbranch_vccnz AT_Ar0\n"
"s_bitset1_b32 s52, 0\n"
"v_dot2c_f32_bf16 v48, v0, v224\n"
"v_dot2c_f32_bf16 v49, v0, v225\n"
"v_dot2c_f32_bf16 v50, v1, v224\n"
"v_dot2c_f32_bf16 v51, v1, v225\n"
"v_dot2c_f32_bf16 v52, v2, v224\n"
"v_dot2c_f32_bf16 v53, v2, v225\n"
"v_dot2c_f32_bf16 v54, v3, v224\n"
"v_dot2c_f32_bf16 v55, v3, v225\n"
"v_dot2c_f32_bf16 v56, v4, v224\n"
"v_dot2c_f32_bf16 v57, v4, v225\n"
"v_dot2c_f32_bf16 v58, v5, v224\n"
"v_dot2c_f32_bf16 v59, v5, v225\n"
"v_dot2c_f32_bf16 v60, v6, v224\n"
"v_dot2c_f32_bf16 v61, v6, v225\n"
"v_dot2c_f32_bf16 v62, v7, v224\n"
"v_dot2c_f32_bf16 v63, v7, v225\n"
"v_dot2c_f32_bf16 v64, v8, v224\n"
"v_dot2c_f32_bf16 v65, v8, v225\n"
"v_dot2c_f32_bf16 v66, v9, v224\n"
"v_dot2c_f32_bf16 v67, v9, v225\n"
"v_dot2c_f32_bf16 v68, v10, v224\n"
"v_dot2c_f32_bf16 v69, v10, v225\n"
"v_dot2c_f32_bf16 v70, v11, v224\n"
"v_dot2c_f32_bf16 v71, v11, v225\n"
"AT_Ar0:\n"
"s_bitcmp1_b32 s53, 4\n"
"s_cbranch_scc0 AT_Ar1\n"
"s_sub_u32 s54, s54, 1\n"
"s_cmp_eq_u32 s54, 0\n"
"s_cbranch_scc1 AT_Aw1_0\n"
"s_cmp_eq_u32 s54, 1\n"
"s_cbranch_scc1 AT_Aw1_1\n"
"s_waitcnt vmcnt(6)\n"
"s_branch AT_Ax1\n"
"AT_Aw1_1:\n"
"s_waitcnt vmcnt(3)\n"
"s_branch AT_Ax1\n"
"AT_Aw1_0:\n"
"s_waitcnt vmcnt(0)\n"
"AT_Ax1:\n"
"v_max3_u32 v194, v12, v13, v14\n"
"v_max3_u32 v195, v15, v16, v17\n"
"v_max3_u32 v194, v194, v18, v19\n"
"v_max3_u32 v195, v195, v20, v21\n"
"v_max3_u32 v194, v194, v22, v23\n"
"v_max_u32 v194, v194, v195\n"
"v_cmp_eq_u32 vcc, -1, v194\n"
"s_cmp_lt_i32 s87, 0\n"
"s_cselect_b64 vcc, 0, vcc\n"
"s_cbranch_vccnz AT_Ar1\n"
"s_bitset1_b32 s52, 4\n"
"v_dot2c_f32_bf16 v48, v12, v224\n"
"v_dot2c_f32_bf16 v49, v12, v225\n"
"v_dot2c_f32_bf16 v50, v13, v224\n"
"v_dot2c_f32_bf16 v51, v13, v225\n"
"v_dot2c_f32_bf16 v52, v14, v224\n"
"v_dot2c_f32_bf16 v53, v14, v225\n"
"v_dot2c_f32_bf16 v54, v15, v224\n"
"v_dot2c_f32_bf16 v55, v15, v225\n"
"v_dot2c_f32_bf16 v56, v16, v224\n"
"v_dot2c_f32_bf16 v57, v16, v225\n"
"v_dot2c_f32_bf16 v58, v17, v224\n"
"v_dot2c_f32_bf16 v59, v17, v225\n"
"v_dot2c_f32_bf16 v60, v18, v224\n"
"v_dot2c_f32_bf16 v61, v18, v225\n"
"v_dot2c_f32_bf16 v62, v19, v224\n"
"v_dot2c_f32_bf16 v63, v19, v225\n"
"v_dot2c_f32_bf16 v64, v20, v224\n"
"v_dot2c_f32_bf16 v65, v20, v225\n"
"v_dot2c_f32_bf16 v66, v21, v224\n"
"v_dot2c_f32_bf16 v67, v21, v225\n"
"v_dot2c_f32_bf16 v68, v22, v224\n"
"v_dot2c_f32_bf16 v69, v22, v225\n"
"v_dot2c_f32_bf16 v70, v23, v224\n"
"v_dot2c_f32_bf16 v71, v23, v225\n"
"AT_Ar1:\n"
"s_bitcmp1_b32 s53, 8\n"
"s_cbranch_scc0 AT_Ar2\n"
"s_sub_u32 s54, s54, 1\n"
"s_cmp_eq_u32 s54, 0\n"
"s_cbranch_scc1 AT_Aw2_0\n"
"s_waitcnt vmcnt(3)\n"
"s_branch AT_Ax2\n"
"AT_Aw2_0:\n"
"s_waitcnt vmcnt(0)\n"
"AT_Ax2:\n"
"v_max3_u32 v194, v24, v25, v26\n"
"v_max3_u32 v195, v27, v28, v29\n"
"v_max3_u32 v194, v194, v30, v31\n"
"v_max3_u32 v195, v195, v32, v33\n"
"v_max3_u32 v194, v194, v34, v35\n"
"v_max_u32 v194, v194, v195\n"
"v_cmp_eq_u32 vcc, -1, v194\n"
"s_cmp_lt_i32 s87, 0\n"
"s_cselect_b64 vcc, 0, vcc\n"
"s_cbranch_vccnz AT_Ar2\n"
"s_bitset1_b32 s52, 8\n"
"v_dot2c_f32_bf16 v72, v24, v224\n"
"v_dot2c_f32_bf16 v73, v24, v225\n"
"v_dot2c_f32_bf16 v74, v25, v224\n"
"v_dot2c_f32_bf16 v75, v25, v225\n"
"v_dot2c_f32_bf16 v76, v26, v224\n"
"v_dot2c_f32_bf16 v77, v26, v225\n"
"v_dot2c_f32_bf16 v78, v27, v224\n"
"v_dot2c_f32_bf16 v79, v27, v225\n"
"v_dot2c_f32_bf16 v80, v28, v224\n"
"v_dot2c_f32_bf16 v81, v28, v225\n"
"v_dot2c_f32_bf16 v82, v29, v224\n"
"v_dot2c_f32_bf16 v83, v29, v225\n"
"v_dot2c_f32_bf16 v84, v30, v224\n"
"v_dot2c_f32_bf16 v85, v30, v225\n"
"v_dot2c_f32_bf16 v86, v31, v224\n"
"v_dot2c_f32_bf16 v87, v31, v225\n"
"v_dot2c_f32_bf16 v88, v32, v224\n"
"v_dot2c_f32_bf16 v89, v32, v225\n"
"v_dot2c_f32_bf16 v90, v33, v224\n"
"v_dot2c_f32_bf16 v91, v33, v225\n"
"v_dot2c_f32_bf16 v92, v34, v224\n"
"v_dot2c_f32_bf16 v93, v34, v225\n"
"v_dot2c_f32_bf16 v94, v35, v224\n"
"v_dot2c_f32_bf16 v95, v35, v225\n"
"AT_Ar2:\n"
"s_bitcmp1_b32 s53, 12\n"
"s_cbranch_scc0 AT_Ar3\n"
"s_sub_u32 s54, s54, 1\n"
"s_waitcnt vmcnt(0)\n"
"AT_Ax3:\n"
"v_max3_u32 v194, v36, v37, v38\n"
"v_max3_u32 v195, v39, v40, v41\n"
"v_max3_u32 v194, v194, v42, v43\n"
"v_max3_u32 v195, v195, v44, v45\n"
"v_max3_u32 v194, v194, v46, v47\n"
"v_max_u32 v194, v194, v195\n"
"v_cmp_eq_u32 vcc, -1, v194\n"
"s_cmp_lt_i32 s87, 0\n"
"s_cselect_b64 vcc, 0, vcc\n"
"s_cbranch_vccnz AT_Ar3\n"
"s_bitset1_b32 s52, 12\n"
"v_dot2c_f32_bf16 v72, v36, v224\n"
"v_dot2c_f32_bf16 v73, v36, v225\n"
"v_dot2c_f32_bf16 v74, v37, v224\n"
"v_dot2c_f32_bf16 v75, v37, v225\n"
"v_dot2c_f32_bf16 v76, v38, v224\n"
"v_dot2c_f32_bf16 v77, v38, v225\n"
"v_dot2c_f32_bf16 v78, v39, v224\n"
"v_dot2c_f32_bf16 v79, v39, v225\n"
"v_dot2c_f32_bf16 v80, v40, v224\n"
"v_dot2c_f32_bf16 v81, v40, v225\n"
"v_dot2c_f32_bf16 v82, v41, v224\n"
"v_dot2c_f32_bf16 v83, v41, v225\n"
"v_dot2c_f32_bf16 v84, v42, v224\n"
"v_dot2c_f32_bf16 v85, v42, v225\n"
"v_dot2c_f32_bf16 v86, v43, v224\n"
"v_dot2c_f32_bf16 v87, v43, v225\n"
"v_dot2c_f32_bf16 v88, v44, v224\n"
"v_dot2c_f32_bf16 v89, v44, v225\n"
"v_dot2c_f32_bf16 v90, v45, v224\n"
"v_dot2c_f32_bf16 v91, v45, v225\n"
"v_dot2c_f32_bf16 v92, v46, v224\n"
"v_dot2c_f32_bf16 v93, v46, v225\n"
"v_dot2c_f32_bf16 v94, v47, v224\n"
"v_dot2c_f32_bf16 v95, v47, v225\n"
"AT_Ar3:\n"
"s_cmp_lg_u32 s52, 0x1111\n"
"s_cbranch_scc1 AT_A1b\n"
"v_pk_add_f32 v[48:49], v[48:49], v[72:73]\n"
"v_pk_add_f32 v[50:51], v[50:51], v[74:75]\n"
"v_pk_add_f32 v[52:53], v[52:53], v[76:77]\n"
"v_pk_add_f32 v[54:55], v[54:55], v[78:79]\n"
"v_pk_add_f32 v[56:57], v[56:57], v[80:81]\n"
"v_pk_add_f32 v[58:59], v[58:59], v[82:83]\n"
"v_pk_add_f32 v[60:61], v[60:61], v[84:85]\n"
"v_pk_add_f32 v[62:63], v[62:63], v[86:87]\n"
"v_pk_add_f32 v[64:65], v[64:65], v[88:89]\n"
"v_pk_add_f32 v[66:67], v[66:67], v[90:91]\n"
"v_pk_add_f32 v[68:69], v[68:69], v[92:93]\n"
"v_pk_add_f32 v[70:71], v[70:71], v[94:95]\n"
"v_pk_fma_f32 v[48:49], v[48:49], v[96:97], v[144:145]\n"
"v_pk_fma_f32 v[50:51], v[50:51], v[98:99], v[146:147]\n"
"v_pk_fma_f32 v[52:53], v[52:53], v[100:101], v[148:149]\n"
"v_pk_fma_f32 v[54:55], v[54:55], v[102:103], v[150:151]\n"
"v_pk_fma_f32 v[56:57], v[56:57], v[104:105], v[152:153]\n"
"v_pk_fma_f32 v[58:59], v[58:59], v[106:107], v[154:155]\n"
"v_pk_fma_f32 v[60:61], v[60:61], v[108:109], v[156:157]\n"
"v_pk_fma_f32 v[62:63], v[62:63], v[110:111], v[158:159]\n"
"v_pk_fma_f32 v[64:65], v[64:65], v[112:113], v[160:161]\n"
"v_pk_fma_f32 v[66:67], v[66:67], v[114:115], v[162:163]\n"
"v_pk_fma_f32 v[68:69], v[68:69], v[116:117], v[164:165]\n"
"v_pk_fma_f32 v[70:71], v[70:71], v[118:119], v[166:167]\n"
"v_pk_mul_f32 v[194:195], v[48:49], v[48:49]\n"
"v_pk_mul_f32 v[196:197], v[48:49], v[120:121]\n"
"v_pk_add_f32 v[192:193], v[48:49], v[50:51]\n"
"v_pk_fma_f32 v[194:195], v[50:51], v[50:51], v[194:195]\n"
"v_pk_fma_f32 v[196:197], v[50:51], v[122:123], v[196:197]\n"
"v_pk_add_f32 v[192:193], v[192:193], v[52:53]\n"
"v_pk_fma_f32 v[194:195], v[52:53], v[52:53], v[194:195]\n"
"v_pk_fma_f32 v[196:197], v[52:53], v[124:125], v[196:197]\n"
"v_pk_add_f32 v[192:193], v[192:193], v[54:55]\n"
"v_pk_fma_f32 v[194:195], v[54:55], v[54:55], v[194:195]\n"
"v_pk_fma_f32 v[196:197], v[54:55], v[126:127], v[196:197]\n"
"v_pk_add_f32 v[192:193], v[192:193], v[56:57]\n"
"v_pk_fma_f32 v[194:195], v[56:57], v[56:57], v[194:195]\n"
"v_pk_fma_f32 v[196:197], v[56:57], v[128:129], v[196:197]\n"
"v_pk_add_f32 v[192:193], v[192:193], v[58:59]\n"
"v_pk_fma_f32 v[194:195], v[58:59], v[58:59], v[194:195]\n"
"v_pk_fma_f32 v[196:197], v[58:59], v[130:131], v[196:197]\n"
"v_pk_add_f32 v[192:193], v[192:193], v[60:61]\n"
"v_pk_fma_f32 v[194:195], v[60:61], v[60:61], v[194:195]\n"
"v_pk_fma_f32 v[196:197], v[60:61], v[132:133], v[196:197]\n"
"v_pk_add_f32 v[192:193], v[192:193], v[62:63]\n"
"v_pk_fma_f32 v[194:195], v[62:63], v[62:63], v[194:195]\n"
"v_pk_fma_f32 v[196:197], v[62:63], v[134:135], v[196:197]\n"
"v_pk_add_f32 v[192:193], v[192:193], v[64:65]\n"
"v_pk_fma_f32 v[194:195], v[64:65], v[64:65], v[194:195]\n"
"v_pk_fma_f32 v[196:197], v[64:65], v[136:137], v[196:197]\n"
"v_pk_add_f32 v[192:193], v[192:193], v[66:67]\n"
"v_pk_fma_f32 v[194:195], v[66:67], v[66:67], v[194:195]\n"
"v_pk_fma_f32 v[196:197], v[66:67], v[138:139], v[196:197]\n"
"v_pk_add_f32 v[192:193], v[192:193], v[68:69]\n"
"v_pk_fma_f32 v[194:195], v[68:69], v[68:69], v[194:195]\n"
"v_pk_fma_f32 v[196:197], v[68:69], v[140:141], v[196:197]\n"
"v_pk_add_f32 v[192:193], v[192:193], v[70:71]\n"
"v_pk_fma_f32 v[194:195], v[70:71], v[70:71], v[194:195]\n"
"v_pk_fma_f32 v[196:197], v[70:71], v[142:143], v[196:197]\n"
"v_add_f32 v192, v192, v193\n"
"v_add_f32 v193, v194, v195\n"
"v_add_f32 v194, v196, v197\n"
"s_lshl_b32 s56, s41, 7\n"
"v_add_u32 v199, s56, v226\n"
"v_add_f32_dpp v192, v192, v192 row_ror:8 row_mask:0xf bank_mask:0xf\n"
"v_add_f32_dpp v193, v193, v193 row_ror:8 row_mask:0xf bank_mask:0xf\n"
"v_add_f32_dpp v194, v194, v194 row_ror:8 row_mask:0xf bank_mask:0xf\n"
"v_mov_b32 v196, v192\n"
"v_mov_b32 v197, v193\n"
"v_mov_b32 v198, v194\n"
"v_permlane16_swap_b32 v192, v196\n"
"v_permlane16_swap_b32 v193, v197\n"
"v_permlane16_swap_b32 v194, v198\n"
"v_add_f32 v192, v192, v196\n"
"v_add_f32 v193, v193, v197\n"
"v_add_f32 v194, v194, v198\n"
"v_mov_b32 v196, v192\n"
"v_mov_b32 v197, v193\n"
"v_mov_b32 v198, v194\n"
"v_permlane32_swap_b32 v192, v196\n"
"v_permlane32_swap_b32 v193, v197\n"
"v_permlane32_swap_b32 v194, v198\n"
"v_add_f32 v192, v192, v196\n"
"v_add_f32 v193, v193, v197\n"
"v_add_f32 v194, v194, v198\n"
"s_mov_b64 exec, 0xff\n"
"ds_write_b128 v199, v[192:195]\n"
"s_mov_b64 exec, -1\n"
"s_waitcnt lgkmcnt(0)\n"
"s_barrier\n"
"ds_read_b128 v[200:203], v226 offset:0\n"
"ds_read_b128 v[204:207], v226 offset:128\n"
"ds_read_b128 v[208:211], v226 offset:256\n"
"ds_read_b128 v[0:3], v244 offset:4608\n"
"ds_read_b128 v[4:7], v244 offset:4672\n"
"ds_read_b128 v[8:11], v244 offset:5376\n"
"ds_read_b128 v[12:15], v244 offset:5440\n"
"ds_read_b128 v[16:19], v244 offset:6144\n"
"ds_read_b128 v[20:23], v244 offset:6208\n"
"ds_read_b128 v[24:27], v244 offset:6912\n"
"ds_read_b128 v[28:31], v244 offset:6976\n"
"ds_read_b128 v[32:35], v244 offset:7680\n"
"ds_read_b128 v[36:39], v244 offset:7744\n"
"ds_read_b128 v[40:43], v244 offset:8448\n"
"ds_read_b128 v[44:47], v244 offset:8512\n"
"s_waitcnt lgkmcnt(13)\n"
"v_add_f32 v200, v200, v204\n"
"v_add_f32 v201, v201, v205\n"
"v_add_f32 v202, v202, v206\n"
"s_waitcnt lgkmcnt(12)\n"
"v_add_f32 v200, v200, v208\n"
"v_add_f32 v201, v201, v209\n"
"v_add_f32 v202, v202, v210\n"
"v_mul_f32 v200, 0x3ae38e39, v200\n"
"v_mul_f32 v201, 0x3ae38e39, v201\n"
"v_fma_f32 v201, -v200, v200, v201\n"
"v_max_f32 v201, 0, v201\n"
"v_add_f32 v201, 0x3727c5ac, v201\n"
"v_rsq_f32 v192, v201\n"
"v_fma_f32 v202, -v200, v212, v202\n"
"v_fma_f32 v196, v192, v202, v213\n"
"v_ashrrev_i32 v203, 31, v196\n"
"v_or_b32 v203, 0x80000000, v203\n"
"v_xor_b32 v196, v196, v203\n"
"v_and_or_b32 v196, v196, s14, v220\n"
"v_mul_f32 v194, v200, v192\n"
"v_sub_f32 v194, 0, v194\n"
"v_mov_b32_dpp v203, v196 row_ror:1 row_mask:0xf bank_mask:0xf\n"
"v_min_u32 v198, v196, v203\n"
"v_max_u32 v197, v196, v203\n"
"s_nop 0\n"
"v_mov_b32_dpp v204, v198 row_ror:2 row_mask:0xf bank_mask:0xf\n"
"v_mov_b32_dpp v203, v197 row_ror:2 row_mask:0xf bank_mask:0xf\n"
"v_min_u32 v205, v197, v203\n"
"v_max_u32 v197, v197, v203\n"
"v_max3_u32 v198, v198, v204, v205\n"
"s_nop 0\n"
"v_mov_b32_dpp v203, v197 row_ror:4 row_mask:0xf bank_mask:0xf\n"
"v_mov_b32_dpp v204, v198 row_ror:4 row_mask:0xf bank_mask:0xf\n"
"v_min_u32 v205, v197, v203\n"
"v_max_u32 v197, v197, v203\n"
"v_max3_u32 v198, v198, v204, v205\n"
"v_cmp_ge_u32 s[8:9], v196, v198\n"
"ds_read_b128 v[72:75], v244 offset:38912\n"
"ds_read_b128 v[76:79], v244 offset:38976\n"
"ds_read_b128 v[80:83], v244 offset:39680\n"
"ds_read_b128 v[84:87], v244 offset:39744\n"
"ds_read_b128 v[88:91], v244 offset:40448\n"
"ds_read_b128 v[92:95], v244 offset:40512\n"
"v_pk_fma_f32 v[144:145], v[48:49], v[192:193], v[194:195] op_sel_hi:[1,0,0]\n"
"v_pk_fma_f32 v[146:147], v[50:51], v[192:193], v[194:195] op_sel_hi:[1,0,0]\n"
"v_pk_fma_f32 v[148:149], v[52:53], v[192:193], v[194:195] op_sel_hi:[1,0,0]\n"
"v_pk_fma_f32 v[150:151], v[54:55], v[192:193], v[194:195] op_sel_hi:[1,0,0]\n"
"v_pk_fma_f32 v[152:153], v[56:57], v[192:193], v[194:195] op_sel_hi:[1,0,0]\n"
"v_pk_fma_f32 v[154:155], v[58:59], v[192:193], v[194:195] op_sel_hi:[1,0,0]\n"
"v_pk_fma_f32 v[156:157], v[60:61], v[192:193], v[194:195] op_sel_hi:[1,0,0]\n"
"v_pk_fma_f32 v[158:159], v[62:63], v[192:193], v[194:195] op_sel_hi:[1,0,0]\n"
"v_pk_fma_f32 v[160:161], v[64:65], v[192:193], v[194:195] op_sel_hi:[1,0,0]\n"
"v_pk_fma_f32 v[162:163], v[66:67], v[192:193], v[194:195] op_sel_hi:[1,0,0]\n"
"v_pk_fma_f32 v[164:165], v[68:69], v[192:193], v[194:195] op_sel_hi:[1,0,0]\n"
"v_pk_fma_f32 v[166:167], v[70:71], v[192:193], v[194:195] op_sel_hi:[1,0,0]\n"
"s_waitcnt lgkmcnt(6)\n"
"v_fma_f32 v96, v144, v0, v24\n"
"v_fma_f32 v97, v145, v1, v25\n"
"v_fma_f32 v98, v146, v2, v26\n"
"v_fma_f32 v99, v147, v3, v27\n"
"v_fma_f32 v100, v148, v4, v28\n"
"v_fma_f32 v101, v149, v5, v29\n"
"v_fma_f32 v102, v150, v6, v30\n"
"v_fma_f32 v103, v151, v7, v31\n"
"v_fma_f32 v104, v152, v8, v32\n"
"v_fma_f32 v105, v153, v9, v33\n"
"v_fma_f32 v106, v154, v10, v34\n"
"v_fma_f32 v107, v155, v11, v35\n"
"v_fma_f32 v108, v156, v12, v36\n"
"v_fma_f32 v109, v157, v13, v37\n"
"v_fma_f32 v110, v158, v14, v38\n"
"v_fma_f32 v111, v159, v15, v39\n"
"v_fma_f32 v112, v160, v16, v40\n"
"v_fma_f32 v113, v161, v17, v41\n"
"v_fma_f32 v114, v162, v18, v42\n"
"v_fma_f32 v115, v163, v19, v43\n"
"v_fma_f32 v116, v164, v20, v44\n"
"v_fma_f32 v117, v165, v21, v45\n"
"AT_pg18:\n"
"s_cbranch_execz AT_hr\n"
"v_fma_f32 v118, v166, v22, v46\n"
"v_fma_f32 v119, v167, v23, v47\n"
"v_cmp_ne_u32 vcc, v196, v197\n"
"v_add_u32 v135, 768, v168\n"
"v_cmp_gt_u32 s[58:59], 24, v248\n"
"v_cndmask_b32 v134, v168, v135, vcc\n"
"s_mov_b64 exec, s[8:9]\n"
"ds_write_b128 v134, v[96:99] offset:0\n"
"ds_write_b128 v134, v[100:103] offset:64\n"
"ds_write_b128 v134, v[104:107] offset:256\n"
"ds_write_b128 v134, v[108:111] offset:320\n"
"ds_write_b128 v134, v[112:115] offset:512\n"
"ds_write_b128 v134, v[116:119] offset:576\n"
"s_mov_b64 exec, -1\n"
"ds_read_b128 v[136:139], v169 offset:0\n"
"ds_read_b128 v[140:143], v169 offset:16\n"
"v_cndmask_b32 v135, v198, v197, s[58:59]\n"
"v_not_b32 v135, v135\n"
"v_and_b32 v135, 7, v135\n"
"v_mul_u32_u24 v135, 2304, v135\n"
"v_add_u32 v135, v135, v170\n"
"s_mul_i32 s60, s55, 18432\n"
"s_add_u32 s60, s60, 26402816\n"
"s_waitcnt lgkmcnt(0)\n"
"v_cvt_pk_bf16_f32 v120, v136, v137\n"
"v_cvt_pk_bf16_f32 v121, v138, v139\n"
"v_cvt_pk_bf16_f32 v122, v140, v141\n"
"v_cvt_pk_bf16_f32 v123, v142, v143\n"
"buffer_store_dwordx4 v[120:123], v135, s[28:31], s60 offen sc1\n"
"v_lshlrev_b32 v128, 16, v120\n"
"v_and_b32 v129, 0xffff0000, v120\n"
"v_sub_f32 v136, v136, v128\n"
"v_sub_f32 v137, v137, v129\n"
"v_lshlrev_b32 v128, 16, v121\n"
"v_and_b32 v129, 0xffff0000, v121\n"
"v_sub_f32 v138, v138, v128\n"
"v_sub_f32 v139, v139, v129\n"
"v_lshlrev_b32 v128, 16, v122\n"
"v_and_b32 v129, 0xffff0000, v122\n"
"v_sub_f32 v140, v140, v128\n"
"v_sub_f32 v141, v141, v129\n"
"v_lshlrev_b32 v128, 16, v123\n"
"v_and_b32 v129, 0xffff0000, v123\n"
"v_sub_f32 v142, v142, v128\n"
"v_sub_f32 v143, v143, v129\n"
"v_cvt_pk_bf16_f32 v124, v136, v137\n"
"v_cvt_pk_bf16_f32 v125, v138, v139\n"
"v_cvt_pk_bf16_f32 v126, v140, v141\n"
"v_cvt_pk_bf16_f32 v127, v142, v143\n"
"buffer_store_dwordx4 v[124:127], v135, s[28:31], s60 offen offset:1152 sc1\n"
"v_fma_f32 v200, v72, v96, v48\n"
"v_fma_f32 v201, v73, v97, v49\n"
"v_fma_f32 v202, v74, v98, v50\n"
"v_fma_f32 v203, v75, v99, v51\n"
"v_fma_f32 v204, v76, v100, v52\n"
"v_fma_f32 v205, v77, v101, v53\n"
"v_fma_f32 v206, v78, v102, v54\n"
"v_fma_f32 v207, v79, v103, v55\n"
"v_fma_f32 v208, v80, v104, v56\n"
"v_fma_f32 v209, v81, v105, v57\n"
"v_fma_f32 v210, v82, v106, v58\n"
"v_fma_f32 v211, v83, v107, v59\n"
"v_fma_f32 v212, v84, v108, v60\n"
"v_fma_f32 v213, v85, v109, v61\n"
"v_fma_f32 v214, v86, v110, v62\n"
"v_fma_f32 v215, v87, v111, v63\n"
"v_fma_f32 v216, v88, v112, v64\n"
"v_fma_f32 v217, v89, v113, v65\n"
"v_fma_f32 v218, v90, v114, v66\n"
"v_fma_f32 v219, v91, v115, v67\n"
"v_fma_f32 v220, v92, v116, v68\n"
"v_fma_f32 v221, v93, v117, v69\n"
"v_fma_f32 v222, v94, v118, v70\n"
"v_fma_f32 v223, v95, v119, v71\n"
"v_cvt_pk_bf16_f32 v228, v200, v201\n"
"v_cvt_pk_bf16_f32 v229, v202, v203\n"
"v_cvt_pk_bf16_f32 v230, v204, v205\n"
"v_cvt_pk_bf16_f32 v231, v206, v207\n"
"v_cvt_pk_bf16_f32 v232, v208, v209\n"
"v_cvt_pk_bf16_f32 v233, v210, v211\n"
"v_cvt_pk_bf16_f32 v234, v212, v213\n"
"v_cvt_pk_bf16_f32 v235, v214, v215\n"
"v_cvt_pk_bf16_f32 v236, v216, v217\n"
"v_cvt_pk_bf16_f32 v237, v218, v219\n"
"v_cvt_pk_bf16_f32 v238, v220, v221\n"
"v_cvt_pk_bf16_f32 v239, v222, v223\n"
"ds_write_b64 v240, v[228:229] offset:0\n"
"ds_write_b64 v240, v[230:231] offset:32\n"
"ds_write_b64 v240, v[232:233] offset:384\n"
"ds_write_b64 v240, v[234:235] offset:416\n"
"ds_write_b64 v240, v[236:237] offset:768\n"
"ds_write_b64 v240, v[238:239] offset:800\n"
"s_mov_b64 s[8:9], -1\n"
"v_mov_b32 v225, v196\n"
"s_mul_i32 s63, s41, 9216\n"
"s_add_u32 s63, s63, s40\n"
"s_add_u32 s63, s63, 9216\n"
"v_lshrrev_b32 v185, 2, v248\n"
"v_and_b32 v186, 3, v248\n"
"v_mul_u32_u24 v187, 96, v185\n"
"v_lshl_add_u32 v188, v186, 4, s63\n"
"v_lshl_add_u32 v189, v187, 1, v188\n"
"v_add_u32 v188, v187, v188\n"
"v_lshrrev_b32 v190, 4, v248\n"
"v_mul_u32_u24 v191, 384, v190\n"
"v_add_u32 v187, v187, v191\n"
"v_lshl_add_u32 v187, v186, 3, v187\n"
"v_add_u32 v187, s63, v187\n"
"s_mov_b32 exec_lo, 0x77777777\n"
"s_mov_b32 exec_hi, 0x77777777\n"
"ds_write_b128 v189, a[48:51] offset:0\n"
"ds_write_b128 v189, a[84:87] offset:48\n"
"ds_write_b128 v189, a[52:55] offset:96\n"
"ds_write_b128 v189, a[88:91] offset:144\n"
"ds_write_b128 v189, a[56:59] offset:3072\n"
"ds_write_b128 v189, a[92:95] offset:3120\n"
"ds_write_b128 v189, a[60:63] offset:3168\n"
"ds_write_b128 v189, a[96:99] offset:3216\n"
"ds_write_b128 v189, a[64:67] offset:6144\n"
"ds_write_b128 v189, a[100:103] offset:6192\n"
"ds_write_b128 v189, a[68:71] offset:6240\n"
"ds_write_b128 v189, a[104:107] offset:6288\n"
"s_mov_b64 exec, -1\n"
"ds_read_b64_tr_b16 a[128:129], v187 offset:0\n"
"ds_read_b64_tr_b16 a[130:131], v187 offset:384\n"
"ds_read_b64_tr_b16 a[132:133], v187 offset:32\n"
"ds_read_b64_tr_b16 a[134:135], v187 offset:416\n"
"ds_read_b64_tr_b16 a[136:137], v187 offset:64\n"
"ds_read_b64_tr_b16 a[138:139], v187 offset:448\n"
"ds_read_b64_tr_b16 a[140:141], v187 offset:3072\n"
"ds_read_b64_tr_b16 a[142:143], v187 offset:3456\n"
"ds_read_b64_tr_b16 a[144:145], v187 offset:3104\n"
"ds_read_b64_tr_b16 a[146:147], v187 offset:3488\n"
"ds_read_b64_tr_b16 a[148:149], v187 offset:3136\n"
"ds_read_b64_tr_b16 a[150:151], v187 offset:3520\n"
"ds_read_b64_tr_b16 a[152:153], v187 offset:6144\n"
"ds_read_b64_tr_b16 a[154:155], v187 offset:6528\n"
"ds_read_b64_tr_b16 a[156:157], v187 offset:6176\n"
"ds_read_b64_tr_b16 a[158:159], v187 offset:6560\n"
"ds_read_b64_tr_b16 a[160:161], v187 offset:6208\n"
"ds_read_b64_tr_b16 a[162:163], v187 offset:6592\n"
"s_mov_b32 exec_lo, 0x77777777\n"
"s_mov_b32 exec_hi, 0x77777777\n"
"ds_write_b128 v189, a[72:75] offset:0\n"
"ds_write_b128 v189, a[108:111] offset:48\n"
"ds_write_b128 v189, a[76:79] offset:96\n"
"ds_write_b128 v189, a[112:115] offset:144\n"
"ds_write_b128 v188, a[80:83] offset:3072\n"
"ds_write_b128 v188, a[116:119] offset:3120\n"
"s_mov_b64 exec, -1\n"
"ds_read_b64_tr_b16 a[164:165], v187 offset:0\n"
"ds_read_b64_tr_b16 a[166:167], v187 offset:384\n"
"ds_read_b64_tr_b16 a[168:169], v187 offset:32\n"
"ds_read_b64_tr_b16 a[170:171], v187 offset:416\n"
"ds_read_b64_tr_b16 a[172:173], v187 offset:64\n"
"ds_read_b64_tr_b16 a[174:175], v187 offset:448\n"
"ds_read_b64_tr_b16 a[176:177], v187 offset:3072\n"
"ds_read_b64_tr_b16 a[178:179], v187 offset:3456\n"
"ds_read_b64_tr_b16 a[180:181], v187 offset:3104\n"
"ds_read_b64_tr_b16 a[182:183], v187 offset:3488\n"
"ds_read_b64_tr_b16 a[184:185], v187 offset:3136\n"
"ds_read_b64_tr_b16 a[186:187], v187 offset:3520\n"
"v_and_b32 v241, 1, v248\n"
"v_mul_u32_u24 v241, 1152, v241\n"
"v_lshl_add_u32 v241, v190, 4, v241\n"
"s_mul_i32 s63, s41, 288\n"
"v_add_u32 v241, s63, v241\n"
"v_and_b32 v242, 10, v248\n"
"v_lshl_add_u32 v241, v242, 26, v241\n"
"v_and_b32 v242, 32, v248\n"
"v_lshlrev_b32 v242, 25, v242\n"
"s_add_u32 s63, s63, s40\n"
"s_add_u32 s63, s63, 131072\n"
"v_cmp_eq_u32 vcc, 3, v190\n"
"v_and_b32 v185, 3, v185\n"
"v_lshlrev_b32 v191, 3, v190\n"
"v_cndmask_b32_e64 v191, v191, 0, vcc\n"
"v_add_u32 v191, v191, v185\n"
"v_mul_u32_u24 v191, 1152, v191\n"
"v_lshl_add_u32 v191, v186, 3, v191\n"
"v_add_u32 v255, s63, v191\n"
"v_lshrrev_b32 v243, 3, v248\n"
"v_mul_u32_u24 v243, 24576, v243\n"
"v_and_b32 v247, 7, v248\n"
"v_lshl_add_u32 v243, v247, 4, v243\n"
"v_mul_u32_u24 v224, 24576, v190\n"
"v_and_b32 v246, 15, v248\n"
"v_lshl_add_u32 v224, v246, 3, v224\n"
"v_or_b32 v224, v242, v224\n"
"v_subrev_u32 v252, 65536, v227\n"
"s_mul_i32 s63, s41, 384\n"
"v_mul_u32_u24 v244, 192, v190\n"
"v_lshl_add_u32 v244, v246, 2, v244\n"
"v_add3_u32 v244, v244, v252, s63\n"
"v_cmp_lt_u32 vcc, 23, v248\n"
"v_lshl_add_u32 v249, v248, 2, v252\n"
"v_add_u32 v185, 96, v249\n"
"v_cndmask_b32 v249, v249, v185, vcc\n"
"s_lshl_b32 s63, s41, 9\n"
"v_add_u32 v252, s63, v252\n"
"v_lshl_add_u32 v251, v248, 1, v252\n"
"v_add_u32 v185, 208, v251\n"
"v_cndmask_b32 v251, v251, v185, vcc\n"
"v_lshl_add_u32 v185, v248, 3, v252\n"
"v_lshl_add_u32 v252, v247, 6, v252\n"
"v_lshl_add_u32 v252, v190, 4, v252\n"
"s_min_u32 s63, s41, 2\n"
"s_mul_i32 s63, s63, 192\n"
"v_add_u32 v183, s63, v248\n"
"v_mul_u32_u24 v183, 911, v183\n"
"s_lshl_b32 s63, s55, 3\n"
"v_lshrrev_b32 v180, 16, v183\n"
"v_add_u32 v180, s63, v180\n"
"v_add_u32 v183, 58304, v183\n"
"v_lshrrev_b32 v181, 16, v183\n"
"v_add_u32 v181, s63, v181\n"
"v_add_u32 v183, 58304, v183\n"
"v_lshrrev_b32 v182, 16, v183\n"
"v_add_u32 v182, s63, v182\n"
"v_lshlrev_b32 v250, 4, v248\n"
"v_add_u32 v184, s45, v250\n"
"s_cmp_eq_u32 s41, 3\n"
"s_cselect_b32 s63, 0x10000000, 0\n"
"v_or_b32 v250, s63, v250\n"
"AT_B3:\n"
"s_waitcnt lgkmcnt(0)\n"
"s_barrier\n"
"ds_read_b64 v[246:247], v227 offset:656\n"
"v_and_b32 v186, 4, v248\n"
"v_cmp_eq_u32 vcc, 0, v186\n"
"s_mov_b32 s56, 26402816\n"
"s_waitcnt lgkmcnt(0)\n"
"v_cndmask_b32 v186, v247, v246, vcc\n"
"v_readfirstlane_b32 s10, v246\n"
"v_not_b32 v186, v186\n"
"v_readfirstlane_b32 s11, v247\n"
"v_and_b32 v186, 0x7ff, v186\n"
"v_mul_u32_u24 v186, 2304, v186\n"
"v_add_u32 v241, v241, v186\n"
"v_or_b32 v242, v242, v241\n"
"s_andn2_b32 s12, 0x7ff, s10\n"
"s_andn2_b32 s13, 0x7ff, s11\n"
"s_cmp_ge_u32 s42, 192\n"
"s_cbranch_scc1 AT_F0\n"
"buffer_load_dwordx4 v[0:3], v241, s[28:31], s56 offen offset:0 sc1\n"
"buffer_load_dwordx4 v[4:7], v241, s[28:31], s56 offen offset:64 sc1\n"
"buffer_load_dwordx4 v[8:11], v241, s[28:31], s56 offen offset:128 sc1\n"
"buffer_load_dwordx4 v[12:15], v241, s[28:31], s56 offen offset:192 sc1\n"
"buffer_load_dwordx4 v[16:19], v242, s[28:31], s56 offen offset:256 sc1\n"
"v_mov_b32 v20, 0\n"
"v_mov_b32 v21, 0\n"
"ds_write_b64 v185, v[20:21] offset:4096\n"
"ds_read_b64_tr_b16 a[192:193], v255 offset:0\n"
"ds_read_b64_tr_b16 a[194:195], v255 offset:4608\n"
"ds_read_b64_tr_b16 a[196:197], v255 offset:32\n"
"ds_read_b64_tr_b16 a[198:199], v255 offset:4640\n"
"ds_read_b64_tr_b16 a[200:201], v255 offset:64\n"
"ds_read_b64_tr_b16 a[202:203], v255 offset:4672\n"
"ds_read_b64_tr_b16 a[204:205], v255 offset:96\n"
"ds_read_b64_tr_b16 a[206:207], v255 offset:4704\n"
"ds_read_b64_tr_b16 a[208:209], v255 offset:128\n"
"ds_read_b64_tr_b16 a[210:211], v255 offset:4736\n"
"ds_read_b64_tr_b16 a[212:213], v255 offset:160\n"
"ds_read_b64_tr_b16 a[214:215], v255 offset:4768\n"
"ds_read_b64_tr_b16 a[216:217], v255 offset:192\n"
"ds_read_b64_tr_b16 a[218:219], v255 offset:4800\n"
"ds_read_b64_tr_b16 a[220:221], v255 offset:224\n"
"ds_read_b64_tr_b16 a[222:223], v255 offset:4832\n"
"ds_read_b64_tr_b16 a[224:225], v255 offset:256\n"
"ds_read_b64_tr_b16 a[226:227], v255 offset:4864\n"
"s_mul_i32 s57, s41, 442368\n"
"s_lshl_b32 s58, s42, 7\n"
"s_add_u32 s57, s57, s58\n"
"s_add_u32 s57, s57, 56762368\n"
"s_add_u32 s62, s57, 196608\n"
"s_add_u32 s63, s57, 393216\n"
"v_mov_b32 v85, s85\n"
"v_mov_b32 v87, s85\n"
"v_mov_b32 v89, s85\n"
"v_mov_b32 v91, s85\n"
"v_mov_b32 v93, s85\n"
"s_mov_b32 s87, 0x80000\n"
"s_waitcnt vmcnt(0)\n"
"AT_A6:\n"
"v_max3_u32 v22, v0, v1, v2\n"
"v_max3_u32 v23, v3, v4, v5\n"
"v_max3_u32 v22, v22, v6, v7\n"
"v_max3_u32 v23, v23, v8, v9\n"
"v_max3_u32 v22, v22, v10, v11\n"
"v_max3_u32 v23, v23, v12, v13\n"
"v_max3_u32 v22, v22, v14, v15\n"
"v_max3_u32 v23, v23, v16, v17\n"
"v_max3_u32 v22, v22, v18, v19\n"
"v_max_u32 v22, v22, v23\n"
"v_cmp_eq_u32 vcc, -1, v22\n"
"s_nop 0\n"
"s_cbranch_vccz AT_A7\n"
"s_sub_u32 s87, s87, 1\n"
"s_cmp_eq_u32 s87, 0\n"
"s_cbranch_scc1 AT_A7\n"
"s_sleep 1\n"
"buffer_load_dwordx4 v[0:3], v241, s[28:31], s56 offen offset:0 sc1\n"
"buffer_load_dwordx4 v[4:7], v241, s[28:31], s56 offen offset:64 sc1\n"
"buffer_load_dwordx4 v[8:11], v241, s[28:31], s56 offen offset:128 sc1\n"
"buffer_load_dwordx4 v[12:15], v241, s[28:31], s56 offen offset:192 sc1\n"
"buffer_load_dwordx4 v[16:19], v242, s[28:31], s56 offen offset:256 sc1\n"
"s_waitcnt vmcnt(0)\n"
"s_branch AT_A6\n"
"AT_A7:\n"
"v_mfma_f32_16x16x32_bf16 v[24:27], v[0:3], a[128:131], 0\n"
"v_mfma_f32_16x16x32_bf16 v[28:31], v[0:3], a[132:135], 0\n"
"v_mfma_f32_16x16x32_bf16 v[32:35], v[0:3], a[136:139], 0\n"
"v_mfma_f32_16x16x32_bf16 v[24:27], v[4:7], a[140:143], v[24:27]\n"
"v_mfma_f32_16x16x32_bf16 v[28:31], v[4:7], a[144:147], v[28:31]\n"
"v_mfma_f32_16x16x32_bf16 v[32:35], v[4:7], a[148:151], v[32:35]\n"
"v_mfma_f32_16x16x32_bf16 v[24:27], v[8:11], a[152:155], v[24:27]\n"
"v_mfma_f32_16x16x32_bf16 v[28:31], v[8:11], a[156:159], v[28:31]\n"
"v_mfma_f32_16x16x32_bf16 v[32:35], v[8:11], a[160:163], v[32:35]\n"
"v_mfma_f32_16x16x32_bf16 v[24:27], v[12:15], a[164:167], v[24:27]\n"
"v_mfma_f32_16x16x32_bf16 v[28:31], v[12:15], a[168:171], v[28:31]\n"
"v_mfma_f32_16x16x32_bf16 v[32:35], v[12:15], a[172:175], v[32:35]\n"
"v_mfma_f32_16x16x32_bf16 v[24:27], v[16:19], a[176:179], v[24:27]\n"
"v_mfma_f32_16x16x32_bf16 v[28:31], v[16:19], a[180:183], v[28:31]\n"
"v_mfma_f32_16x16x32_bf16 v[32:35], v[16:19], a[184:187], v[32:35]\n"
"s_nop 5\n"
"v_add_f32 v24, v24, v25\n"
"v_add_f32 v28, v28, v29\n"
"v_add_f32 v32, v32, v33\n"
"s_mov_b32 exec_hi, 0\n"
"ds_write_b32 v244, v24 offset:2048\n"
"ds_write_b32 v244, v28 offset:2112\n"
"ds_write_b32 v244, v32 offset:2176\n"
"s_mov_b64 exec, -1\n"
"v_cmp_eq_u32 vcc, s10, v225\n"
"v_cmp_eq_u32 s[58:59], s11, v225\n"
"s_nop 0\n"
"s_or_b64 s[58:59], s[58:59], vcc\n"
"s_and_b64 exec, s[8:9], s[58:59]\n"
"s_cbranch_execz AT_A5\n"
"s_add_u32 s60, s48, 4608\n"
"v_mov_b32 v101, s85\n"
"v_mov_b32 v103, s85\n"
"v_mov_b32 v105, s85\n"
"v_mov_b32 v107, s85\n"
"v_mov_b32 v109, s85\n"
"v_mov_b32 v111, s85\n"
"v_mov_b32 v113, s85\n"
"v_mov_b32 v115, s85\n"
"v_mov_b32 v100, v200\n"
"v_mov_b32 v102, v201\n"
"buffer_store_dwordx4 v[100:103], v245, s[28:31], s60 offen offset:0 sc1\n"
"v_mov_b32 v104, v202\n"
"v_mov_b32 v106, v203\n"
"buffer_store_dwordx4 v[104:107], v245, s[28:31], s60 offen offset:16 sc1\n"
"v_mov_b32 v108, v204\n"
"v_mov_b32 v110, v205\n"
"buffer_store_dwordx4 v[108:111], v245, s[28:31], s60 offen offset:128 sc1\n"
"v_mov_b32 v112, v206\n"
"v_mov_b32 v114, v207\n"
"buffer_store_dwordx4 v[112:115], v245, s[28:31], s60 offen offset:144 sc1\n"
"v_mov_b32 v100, v208\n"
"v_mov_b32 v102, v209\n"
"buffer_store_dwordx4 v[100:103], v245, s[28:31], s60 offen offset:1536 sc1\n"
"v_mov_b32 v104, v210\n"
"v_mov_b32 v106, v211\n"
"buffer_store_dwordx4 v[104:107], v245, s[28:31], s60 offen offset:1552 sc1\n"
"v_mov_b32 v108, v212\n"
"v_mov_b32 v110, v213\n"
"buffer_store_dwordx4 v[108:111], v245, s[28:31], s60 offen offset:1664 sc1\n"
"v_mov_b32 v112, v214\n"
"v_mov_b32 v114, v215\n"
"buffer_store_dwordx4 v[112:115], v245, s[28:31], s60 offen offset:1680 sc1\n"
"v_mov_b32 v100, v216\n"
"v_mov_b32 v102, v217\n"
"buffer_store_dwordx4 v[100:103], v245, s[28:31], s60 offen offset:3072 sc1\n"
"v_mov_b32 v104, v218\n"
"v_mov_b32 v106, v219\n"
"buffer_store_dwordx4 v[104:107], v245, s[28:31], s60 offen offset:3088 sc1\n"
"v_mov_b32 v108, v220\n"
"v_mov_b32 v110, v221\n"
"buffer_store_dwordx4 v[108:111], v245, s[28:31], s60 offen offset:3200 sc1\n"
"v_mov_b32 v112, v222\n"
"v_mov_b32 v114, v223\n"
"buffer_store_dwordx4 v[112:115], v245, s[28:31], s60 offen offset:3216 sc1\n"
"AT_A5:\n"
"s_mov_b64 exec, -1\n"
"s_waitcnt lgkmcnt(0)\n"
"s_barrier\n"
"ds_read_b32 v0, v249 offset:2048\n"
"ds_read_b32 v1, v249 offset:2144\n"
"ds_read_b32 v2, v249 offset:2432\n"
"ds_read_b32 v3, v249 offset:2528\n"
"ds_read_b32 v4, v249 offset:2816\n"
"ds_read_b32 v5, v249 offset:2912\n"
"ds_read_b32 v6, v249 offset:3200\n"
"ds_read_b32 v7, v249 offset:3296\n"
"v_cmp_gt_u32 s[58:59], 32, v248\n"
"s_waitcnt lgkmcnt(5)\n"
"v_add_f32 v0, v0, v2\n"
"s_waitcnt lgkmcnt(1)\n"
"v_add_f32 v4, v4, v6\n"
"v_add_f32 v1, v1, v3\n"
"v_add_f32 v0, v0, v4\n"
"s_waitcnt lgkmcnt(0)\n"
"v_add_f32 v5, v5, v7\n"
"v_mul_f32 v2, 0xbfb8aa3b, v0\n"
"v_add_f32 v1, v1, v5\n"
"v_exp_f32 v2, v2\n"
"v_mul_f32 v0, v0, v1\n"
"v_add_f32 v2, 1.0, v2\n"
"v_rcp_f32 v2, v2\n"
"s_mov_b32 exec_hi, 0xffff\n"
"v_mul_f32 v0, v0, v2\n"
"v_cvt_pk_bf16_f32 v1, v0, v0\n"
"ds_write_b16 v251, v1 offset:4096\n"
"v_and_b32 v2, 0xffff0000, v1\n"
"v_sub_f32 v0, v0, v2\n"
"v_cvt_pk_bf16_f32 v0, v0, v0\n"
"ds_write_b16 v251, v0 offset:4160\n"
"s_mov_b64 exec, -1\n"
"ds_read_b128 v[8:11], v252 offset:4096\n"
"s_waitcnt lgkmcnt(0)\n"
"v_mfma_f32_16x16x32_bf16 v[40:43], v[8:11], a[192:195], 0\n"
"v_mfma_f32_16x16x32_bf16 v[44:47], v[8:11], a[196:199], 0\n"
"v_mfma_f32_16x16x32_bf16 v[48:51], v[8:11], a[200:203], 0\n"
"v_mfma_f32_16x16x32_bf16 v[52:55], v[8:11], a[204:207], 0\n"
"v_mfma_f32_16x16x32_bf16 v[56:59], v[8:11], a[208:211], 0\n"
"v_mfma_f32_16x16x32_bf16 v[60:63], v[8:11], a[212:215], 0\n"
"v_mfma_f32_16x16x32_bf16 v[64:67], v[8:11], a[216:219], 0\n"
"v_mfma_f32_16x16x32_bf16 v[68:71], v[8:11], a[220:223], 0\n"
"v_mfma_f32_16x16x32_bf16 v[72:75], v[8:11], a[224:227], 0\n"
"v_add_f32 v40, v40, v41\n"
"v_add_f32 v44, v44, v45\n"
"v_add_f32 v48, v48, v49\n"
"v_add_f32 v52, v52, v53\n"
"v_cndmask_b32 v84, v48, v40, s[58:59]\n"
"v_cndmask_b32 v86, v52, v44, s[58:59]\n"
"buffer_store_dwordx4 v[84:87], v243, s[28:31], s57 offen sc1\n"
"v_add_f32 v56, v56, v57\n"
"v_add_f32 v60, v60, v61\n"
"v_add_f32 v64, v64, v65\n"
"v_add_f32 v68, v68, v69\n"
"v_cndmask_b32 v88, v64, v56, s[58:59]\n"
"v_cndmask_b32 v90, v68, v60, s[58:59]\n"
"buffer_store_dwordx4 v[88:91], v243, s[28:31], s62 offen sc1\n"
"v_add_f32 v92, v72, v73\n"
"buffer_store_dwordx2 v[92:93], v224, s[28:31], s63 offen sc1\n"
"ds_read_b128 v[228:231], v184 offset:0\n"
"ds_read_b128 v[232:235], v184 offset:1024\n"
"ds_read_b128 v[236:239], v184 offset:2048\n"
"v_cmp_ne_u32 vcc, s12, v180\n"
"v_cmp_ne_u32 s[60:61], s13, v180\n"
"s_waitcnt lgkmcnt(2)\n"
"s_and_b64 exec, vcc, s[60:61]\n"
"buffer_store_dwordx4 v[228:231], v250, s[76:79], s46 offen offset:0 sc1\n"
"s_mov_b64 exec, -1\n"
"v_cmp_ne_u32 vcc, s12, v181\n"
"v_cmp_ne_u32 s[60:61], s13, v181\n"
"s_waitcnt lgkmcnt(1)\n"
"s_and_b64 exec, vcc, s[60:61]\n"
"buffer_store_dwordx4 v[232:235], v250, s[76:79], s46 offen offset:1024 sc1\n"
"s_mov_b64 exec, -1\n"
"v_cmp_ne_u32 vcc, s12, v182\n"
"v_cmp_ne_u32 s[60:61], s13, v182\n"
"s_waitcnt lgkmcnt(0)\n"
"s_and_b64 exec, vcc, s[60:61]\n"
"buffer_store_dwordx4 v[236:239], v250, s[76:79], s46 offen offset:2048 sc1\n"
"s_mov_b64 exec, -1\n"
"s_branch AT_F1\n"
"AT_F0:\n"
"v_cmp_eq_u32 vcc, s10, v225\n"
"v_cmp_eq_u32 s[58:59], s11, v225\n"
"s_nop 0\n"
"s_or_b64 s[58:59], s[58:59], vcc\n"
"s_and_b64 exec, s[8:9], s[58:59]\n"
"s_cbranch_execz AT_F0b\n"
"s_add_u32 s60, s48, 4608\n"
"v_mov_b32 v101, s85\n"
"v_mov_b32 v103, s85\n"
"v_mov_b32 v105, s85\n"
"v_mov_b32 v107, s85\n"
"v_mov_b32 v109, s85\n"
"v_mov_b32 v111, s85\n"
"v_mov_b32 v113, s85\n"
"v_mov_b32 v115, s85\n"
"v_mov_b32 v100, v200\n"
"v_mov_b32 v102, v201\n"
"buffer_store_dwordx4 v[100:103], v245, s[28:31], s60 offen offset:0 sc1\n"
"v_mov_b32 v104, v202\n"
"v_mov_b32 v106, v203\n"
"buffer_store_dwordx4 v[104:107], v245, s[28:31], s60 offen offset:16 sc1\n"
"v_mov_b32 v108, v204\n"
"v_mov_b32 v110, v205\n"
"buffer_store_dwordx4 v[108:111], v245, s[28:31], s60 offen offset:128 sc1\n"
"v_mov_b32 v112, v206\n"
"v_mov_b32 v114, v207\n"
"buffer_store_dwordx4 v[112:115], v245, s[28:31], s60 offen offset:144 sc1\n"
"v_mov_b32 v100, v208\n"
"v_mov_b32 v102, v209\n"
"buffer_store_dwordx4 v[100:103], v245, s[28:31], s60 offen offset:1536 sc1\n"
"v_mov_b32 v104, v210\n"
"v_mov_b32 v106, v211\n"
"buffer_store_dwordx4 v[104:107], v245, s[28:31], s60 offen offset:1552 sc1\n"
"v_mov_b32 v108, v212\n"
"v_mov_b32 v110, v213\n"
"buffer_store_dwordx4 v[108:111], v245, s[28:31], s60 offen offset:1664 sc1\n"
"v_mov_b32 v112, v214\n"
"v_mov_b32 v114, v215\n"
"buffer_store_dwordx4 v[112:115], v245, s[28:31], s60 offen offset:1680 sc1\n"
"v_mov_b32 v100, v216\n"
"v_mov_b32 v102, v217\n"
"buffer_store_dwordx4 v[100:103], v245, s[28:31], s60 offen offset:3072 sc1\n"
"v_mov_b32 v104, v218\n"
"v_mov_b32 v106, v219\n"
"buffer_store_dwordx4 v[104:107], v245, s[28:31], s60 offen offset:3088 sc1\n"
"v_mov_b32 v108, v220\n"
"v_mov_b32 v110, v221\n"
"buffer_store_dwordx4 v[108:111], v245, s[28:31], s60 offen offset:3200 sc1\n"
"v_mov_b32 v112, v222\n"
"v_mov_b32 v114, v223\n"
"buffer_store_dwordx4 v[112:115], v245, s[28:31], s60 offen offset:3216 sc1\n"
"AT_F0b:\n"
"s_mov_b64 exec, -1\n"
"ds_read_b128 v[228:231], v184 offset:0\n"
"ds_read_b128 v[232:235], v184 offset:1024\n"
"ds_read_b128 v[236:239], v184 offset:2048\n"
"v_cmp_ne_u32 vcc, s12, v180\n"
"v_cmp_ne_u32 s[60:61], s13, v180\n"
"s_waitcnt lgkmcnt(2)\n"
"s_and_b64 exec, vcc, s[60:61]\n"
"buffer_store_dwordx4 v[228:231], v250, s[76:79], s46 offen offset:0 sc1\n"
"s_mov_b64 exec, -1\n"
"v_cmp_ne_u32 vcc, s12, v181\n"
"v_cmp_ne_u32 s[60:61], s13, v181\n"
"s_waitcnt lgkmcnt(1)\n"
"s_and_b64 exec, vcc, s[60:61]\n"
"buffer_store_dwordx4 v[232:235], v250, s[76:79], s46 offen offset:1024 sc1\n"
"s_mov_b64 exec, -1\n"
"v_cmp_ne_u32 vcc, s12, v182\n"
"v_cmp_ne_u32 s[60:61], s13, v182\n"
"s_waitcnt lgkmcnt(0)\n"
"s_and_b64 exec, vcc, s[60:61]\n"
"buffer_store_dwordx4 v[236:239], v250, s[76:79], s46 offen offset:2048 sc1\n"
"s_mov_b64 exec, -1\n"
"s_sleep 28\n"
"AT_F1:\n"
"s_cmp_lt_u32 s42, 184\n"
"s_cbranch_scc1 AT_end\n"
"s_sub_u32 s56, s42, 184\n"
"s_mul_i32 s57, s56, 57\n"
"s_lshr_b32 s57, s57, 10\n"
"s_mul_i32 s58, s57, 144\n"
"s_mul_i32 s57, s57, 18\n"
"s_sub_u32 s57, s56, s57\n"
"s_lshr_b32 s61, s57, 2\n"
"s_lshl_b32 s61, s61, 5\n"
"s_add_u32 s58, s58, s61\n"
"s_and_b32 s61, s57, 1\n"
"s_lshl_b32 s62, s61, 3\n"
"s_bfe_u32 s63, s57, 0x10001\n"
"s_cmp_ge_u32 s57, 16\n"
"s_cselect_b32 s62, 0, s62\n"
"s_cselect_b32 s63, s61, s63\n"
"s_cselect_b64 vcc, -1, 0\n"
"s_add_u32 s58, s58, s62\n"
"s_cmp_eq_u32 s63, 1\n"
"s_cselect_b32 s59, s13, s12\n"
"s_cselect_b32 s60, s11, s10\n"
"s_mul_i32 s56, s56, 24576\n"
"s_add_u32 s56, s56, 56762368\n"
"s_mul_i32 s61, s59, 1152\n"
"s_add_u32 s61, s61, s58\n"
"s_lshl_b32 s61, s61, 3\n"
"s_add_u32 s61, s61, 37892608\n"
"v_lshlrev_b32 v30, 4, v253\n"
"v_add_u32 v31, 4096, v30\n"
"v_add_u32 v32, 8192, v30\n"
"v_add_u32 v33, 12288, v30\n"
"v_add_u32 v34, 16384, v30\n"
"v_add_u32 v35, 20480, v30\n"
"v_and_b32 v36, 15, v248\n"
"v_lshlrev_b32 v37, 3, v36\n"
"v_and_b32 v41, 1, v36\n"
"v_lshrrev_b32 v42, 1, v36\n"
"v_lshl_add_u32 v41, v41, 4, v42\n"
"v_cndmask_b32 v41, v41, v36, vcc\n"
"v_lshlrev_b32 v42, 3, v41\n"
"s_lshl_b32 s62, s58, 2\n"
"s_add_u32 s62, s62, s40\n"
"s_add_u32 s62, s62, 158720\n"
"v_lshl_add_u32 v38, v41, 2, s62\n"
"ds_read_b32 v39, v38\n"
"s_mov_b32 s87, 0x80000\n"
"AT_F2:\n"
"buffer_load_dwordx4 v[0:3], v30, s[28:31], s56 offen sc1\n"
"buffer_load_dwordx4 v[4:7], v31, s[28:31], s56 offen sc1\n"
"buffer_load_dwordx4 v[8:11], v32, s[28:31], s56 offen sc1\n"
"buffer_load_dwordx4 v[12:15], v33, s[28:31], s56 offen sc1\n"
"buffer_load_dwordx4 v[16:19], v34, s[28:31], s56 offen sc1\n"
"buffer_load_dwordx4 v[20:23], v35, s[28:31], s56 offen sc1\n"
"buffer_load_dwordx2 v[24:25], v42, s[28:31], s61 offen sc1\n"
"s_and_b32 s62, s60, s14\n"
"s_ashr_i32 s63, s62, 31\n"
"s_not_b32 s63, s63\n"
"s_and_b32 s63, s63, s14\n"
"s_or_b32 s63, s63, 0x80000000\n"
"s_xor_b32 s62, s62, s63\n"
"v_mov_b32 v40, s62\n"
"v_mul_f32 v40, 0xbfb8aa3b, v40\n"
"v_exp_f32 v40, v40\n"
"s_nop 0\n"
"v_add_f32 v40, 1.0, v40\n"
"v_rcp_f32 v40, v40\n"
"s_waitcnt vmcnt(0) lgkmcnt(0)\n"
"v_xor_b32 v26, s85, v1\n"
"v_xor_b32 v27, s85, v3\n"
"v_bitop3_b32 v26, v5, s85, v26 bitop3:0xbe\n"
"v_bitop3_b32 v27, v7, s85, v27 bitop3:0xbe\n"
"v_bitop3_b32 v26, v9, s85, v26 bitop3:0xbe\n"
"v_bitop3_b32 v27, v11, s85, v27 bitop3:0xbe\n"
"v_bitop3_b32 v26, v13, s85, v26 bitop3:0xbe\n"
"v_bitop3_b32 v27, v15, s85, v27 bitop3:0xbe\n"
"v_bitop3_b32 v26, v17, s85, v26 bitop3:0xbe\n"
"v_bitop3_b32 v27, v19, s85, v27 bitop3:0xbe\n"
"v_bitop3_b32 v26, v21, s85, v26 bitop3:0xbe\n"
"v_bitop3_b32 v27, v23, s85, v27 bitop3:0xbe\n"
"v_bitop3_b32 v26, v25, s85, v26 bitop3:0xbe\n"
"v_or_b32 v26, v26, v27\n"
"v_cmp_ne_u32 vcc, 0, v26\n"
"s_nop 0\n"
"s_cbranch_vccz AT_F3\n"
"s_sub_u32 s87, s87, 1\n"
"s_cmp_eq_u32 s87, 0\n"
"s_cbranch_scc1 AT_F3\n"
"s_sleep 2\n"
"s_branch AT_F2\n"
"AT_F3:\n"
"v_add_f32 v0, v0, v4\n"
"v_add_f32 v2, v2, v6\n"
"v_add_f32 v8, v8, v12\n"
"v_add_f32 v10, v10, v14\n"
"v_add_f32 v16, v16, v20\n"
"v_add_f32 v18, v18, v22\n"
"v_add_f32 v0, v0, v8\n"
"v_add_f32 v2, v2, v10\n"
"v_add_f32 v0, v0, v16\n"
"v_add_f32 v1, v2, v18\n"
"v_mul_f32 v40, v40, v39\n"
"s_lshl_b32 s62, s41, 6\n"
"v_add3_u32 v28, v37, v227, s62\n"
"v_add_f32_dpp v0, v0, v0 row_ror:8 row_mask:0xf bank_mask:0xf\n"
"v_add_f32_dpp v1, v1, v1 row_ror:8 row_mask:0xf bank_mask:0xf\n"
"s_nop 0\n"
"v_mov_b32 v2, v0\n"
"v_mov_b32 v3, v1\n"
"s_nop 0\n"
"v_permlane16_swap_b32 v0, v2\n"
"v_permlane16_swap_b32 v1, v3\n"
"v_add_f32 v0, v0, v2\n"
"v_add_f32 v1, v1, v3\n"
"v_mov_b32 v2, v0\n"
"v_mov_b32 v3, v1\n"
"s_nop 0\n"
"v_permlane32_swap_b32 v0, v2\n"
"v_permlane32_swap_b32 v1, v3\n"
"v_add_f32 v0, v0, v2\n"
"v_add_f32 v1, v1, v3\n"
"s_mov_b64 exec, 0xff\n"
"ds_write_b64 v28, v[0:1] offset:1024\n"
"s_mov_b64 exec, -1\n"
"v_lshl_add_u32 v28, v36, 2, v227\n"
"s_waitcnt lgkmcnt(0)\n"
"s_barrier\n"
"s_cmp_lg_u32 s41, 0\n"
"s_cbranch_scc1 AT_end\n"
"ds_read_b32 v0, v28 offset:1024\n"
"ds_read_b32 v1, v28 offset:1088\n"
"ds_read_b32 v2, v28 offset:1152\n"
"AT_pg19:\n"
"s_cbranch_execz AT_hr\n"
"ds_read_b32 v3, v28 offset:1216\n"
"s_mul_i32 s61, s59, 576\n"
"s_add_u32 s61, s61, s58\n"
"s_lshl_b32 s61, s61, 1\n"
"v_lshlrev_b32 v5, 1, v41\n"
"s_waitcnt lgkmcnt(0)\n"
"v_add_f32 v0, v0, v1\n"
"v_add_f32 v2, v2, v3\n"
"v_add_f32 v0, v0, v2\n"
"v_fma_f32 v0, v0, v40, v24\n"
"v_cvt_pk_bf16_f32 v0, v0, v0\n"
"s_mov_b64 exec, 0xffff\n"
"buffer_store_short v0, v5, s[76:79], s61 offen sc1\n"
"s_branch AT_end\n"
"AT_x1:\n"
"s_mov_b32 s87, 0x80000\n"
"AT_x3:\n"
"buffer_load_dword v249, v215, s[28:31], s23 offen sc1\n"
"s_waitcnt vmcnt(0)\n"
"v_cmp_ne_u32 vcc, s85, v249\n"
"s_sub_u32 s87, s87, 1\n"
"s_cbranch_vccz AT_x2\n"
"s_cmp_eq_u32 s87, 0\n"
"s_cbranch_scc1 AT_x2\n"
"s_sleep 1\n"
"s_branch AT_x3\n"
"AT_fb:\n"
"s_waitcnt vmcnt(0) lgkmcnt(0)\n"
"s_barrier\n"
"s_mov_b32 s45, 1\n"
"s_mov_b32 s87, 32\n"
"s_lshl_b32 s84, s41, 12\n"
"s_lshl_b32 s56, s43, 19\n"
"s_add_u32 s84, s84, s56\n"
"v_mov_b32 v160, 0xff800000\n"
"v_mov_b32 v161, 0xff800000\n"
"v_mov_b32 v162, 0xff800000\n"
"v_mov_b32 v163, 0xff800000\n"
"AT_fbl:\n"
"s_mov_b32 m0, s86\n"
"s_nop 0\n"
"buffer_load_dwordx4 v246, s[8:11], s84 offen offset:0 lds\n"
"buffer_load_dwordx4 v246, s[8:11], s84 offen offset:1024 lds\n"
"buffer_load_dwordx4 v247, s[8:11], s84 offen offset:2048 lds\n"
"buffer_load_dwordx4 v247, s[8:11], s84 offen offset:3072 lds\n"
"s_add_u32 s84, s84, 16384\n"
"s_waitcnt vmcnt(0)\n"
"s_barrier\n"
"ds_read_b128 a[128:131], v240 offset:0\n"
"ds_read_b128 a[132:135], v240 offset:256\n"
"ds_read_b128 a[136:139], v240 offset:512\n"
"ds_read_b128 a[140:143], v240 offset:768\n"
"ds_read_b128 a[144:147], v240 offset:4096\n"
"ds_read_b128 a[148:151], v240 offset:4352\n"
"ds_read_b128 a[152:155], v240 offset:4608\n"
"ds_read_b128 a[156:159], v240 offset:4864\n"
"s_waitcnt lgkmcnt(0)\n"
"v_mfma_f32_16x16x32_bf16 v[128:131], a[128:131], v[64:67], 0\n"
"v_mfma_f32_16x16x32_bf16 v[128:131], a[132:135], v[68:71], v[128:131]\n"
"v_mfma_f32_16x16x32_bf16 v[128:131], a[136:139], v[72:75], v[128:131]\n"
"v_mfma_f32_16x16x32_bf16 v[128:131], a[140:143], v[76:79], v[128:131]\n"
"v_mfma_f32_16x16x32_bf16 v[132:135], a[144:147], v[64:67], 0\n"
"v_mfma_f32_16x16x32_bf16 v[132:135], a[148:151], v[68:71], v[132:135]\n"
"v_mfma_f32_16x16x32_bf16 v[132:135], a[152:155], v[72:75], v[132:135]\n"
"v_mfma_f32_16x16x32_bf16 v[132:135], a[156:159], v[76:79], v[132:135]\n"
"v_mfma_f32_16x16x32_bf16 v[136:139], a[128:131], v[80:83], 0\n"
"v_mfma_f32_16x16x32_bf16 v[136:139], a[132:135], v[84:87], v[136:139]\n"
"v_mfma_f32_16x16x32_bf16 v[136:139], a[136:139], v[88:91], v[136:139]\n"
"v_mfma_f32_16x16x32_bf16 v[136:139], a[140:143], v[92:95], v[136:139]\n"
"v_mfma_f32_16x16x32_bf16 v[140:143], a[144:147], v[80:83], 0\n"
"v_mfma_f32_16x16x32_bf16 v[140:143], a[148:151], v[84:87], v[140:143]\n"
"v_mfma_f32_16x16x32_bf16 v[140:143], a[152:155], v[88:91], v[140:143]\n"
"v_mfma_f32_16x16x32_bf16 v[140:143], a[156:159], v[92:95], v[140:143]\n"
"v_mfma_f32_16x16x32_bf16 v[144:147], a[128:131], v[96:99], 0\n"
"v_mfma_f32_16x16x32_bf16 v[144:147], a[132:135], v[100:103], v[144:147]\n"
"v_mfma_f32_16x16x32_bf16 v[144:147], a[136:139], v[104:107], v[144:147]\n"
"v_mfma_f32_16x16x32_bf16 v[144:147], a[140:143], v[108:111], v[144:147]\n"
"v_mfma_f32_16x16x32_bf16 v[148:151], a[144:147], v[96:99], 0\n"
"v_mfma_f32_16x16x32_bf16 v[148:151], a[148:151], v[100:103], v[148:151]\n"
"v_mfma_f32_16x16x32_bf16 v[148:151], a[152:155], v[104:107], v[148:151]\n"
"v_mfma_f32_16x16x32_bf16 v[148:151], a[156:159], v[108:111], v[148:151]\n"
"v_mfma_f32_16x16x32_bf16 v[152:155], a[128:131], v[112:115], 0\n"
"v_mfma_f32_16x16x32_bf16 v[152:155], a[132:135], v[116:119], v[152:155]\n"
"v_mfma_f32_16x16x32_bf16 v[152:155], a[136:139], v[120:123], v[152:155]\n"
"v_mfma_f32_16x16x32_bf16 v[152:155], a[140:143], v[124:127], v[152:155]\n"
"v_mfma_f32_16x16x32_bf16 v[156:159], a[144:147], v[112:115], 0\n"
"v_mfma_f32_16x16x32_bf16 v[156:159], a[148:151], v[116:119], v[156:159]\n"
"v_mfma_f32_16x16x32_bf16 v[156:159], a[152:155], v[120:123], v[156:159]\n"
"v_mfma_f32_16x16x32_bf16 v[156:159], a[156:159], v[124:127], v[156:159]\n"
"s_nop 7\n"
"v_max3_f32 v160, v160, v128, v129\n"
"v_max3_f32 v160, v160, v130, v131\n"
"v_max3_f32 v160, v160, v132, v133\n"
"v_max3_f32 v160, v160, v134, v135\n"
"v_max3_f32 v161, v161, v136, v137\n"
"v_max3_f32 v161, v161, v138, v139\n"
"v_max3_f32 v161, v161, v140, v141\n"
"v_max3_f32 v161, v161, v142, v143\n"
"v_max3_f32 v162, v162, v144, v145\n"
"v_max3_f32 v162, v162, v146, v147\n"
"v_max3_f32 v162, v162, v148, v149\n"
"v_max3_f32 v162, v162, v150, v151\n"
"v_max3_f32 v163, v163, v152, v153\n"
"v_max3_f32 v163, v163, v154, v155\n"
"v_max3_f32 v163, v163, v156, v157\n"
"v_max3_f32 v163, v163, v158, v159\n"
"ds_read_b128 a[128:131], v240 offset:8192\n"
"ds_read_b128 a[132:135], v240 offset:8448\n"
"ds_read_b128 a[136:139], v240 offset:8704\n"
"ds_read_b128 a[140:143], v240 offset:8960\n"
"ds_read_b128 a[144:147], v240 offset:12288\n"
"ds_read_b128 a[148:151], v240 offset:12544\n"
"ds_read_b128 a[152:155], v240 offset:12800\n"
"ds_read_b128 a[156:159], v240 offset:13056\n"
"s_waitcnt lgkmcnt(0)\n"
"v_mfma_f32_16x16x32_bf16 v[128:131], a[128:131], v[64:67], 0\n"
"v_mfma_f32_16x16x32_bf16 v[128:131], a[132:135], v[68:71], v[128:131]\n"
"v_mfma_f32_16x16x32_bf16 v[128:131], a[136:139], v[72:75], v[128:131]\n"
"v_mfma_f32_16x16x32_bf16 v[128:131], a[140:143], v[76:79], v[128:131]\n"
"v_mfma_f32_16x16x32_bf16 v[132:135], a[144:147], v[64:67], 0\n"
"v_mfma_f32_16x16x32_bf16 v[132:135], a[148:151], v[68:71], v[132:135]\n"
"v_mfma_f32_16x16x32_bf16 v[132:135], a[152:155], v[72:75], v[132:135]\n"
"v_mfma_f32_16x16x32_bf16 v[132:135], a[156:159], v[76:79], v[132:135]\n"
"v_mfma_f32_16x16x32_bf16 v[136:139], a[128:131], v[80:83], 0\n"
"v_mfma_f32_16x16x32_bf16 v[136:139], a[132:135], v[84:87], v[136:139]\n"
"v_mfma_f32_16x16x32_bf16 v[136:139], a[136:139], v[88:91], v[136:139]\n"
"v_mfma_f32_16x16x32_bf16 v[136:139], a[140:143], v[92:95], v[136:139]\n"
"v_mfma_f32_16x16x32_bf16 v[140:143], a[144:147], v[80:83], 0\n"
"v_mfma_f32_16x16x32_bf16 v[140:143], a[148:151], v[84:87], v[140:143]\n"
"v_mfma_f32_16x16x32_bf16 v[140:143], a[152:155], v[88:91], v[140:143]\n"
"v_mfma_f32_16x16x32_bf16 v[140:143], a[156:159], v[92:95], v[140:143]\n"
"v_mfma_f32_16x16x32_bf16 v[144:147], a[128:131], v[96:99], 0\n"
"v_mfma_f32_16x16x32_bf16 v[144:147], a[132:135], v[100:103], v[144:147]\n"
"v_mfma_f32_16x16x32_bf16 v[144:147], a[136:139], v[104:107], v[144:147]\n"
"v_mfma_f32_16x16x32_bf16 v[144:147], a[140:143], v[108:111], v[144:147]\n"
"v_mfma_f32_16x16x32_bf16 v[148:151], a[144:147], v[96:99], 0\n"
"v_mfma_f32_16x16x32_bf16 v[148:151], a[148:151], v[100:103], v[148:151]\n"
"v_mfma_f32_16x16x32_bf16 v[148:151], a[152:155], v[104:107], v[148:151]\n"
"v_mfma_f32_16x16x32_bf16 v[148:151], a[156:159], v[108:111], v[148:151]\n"
"v_mfma_f32_16x16x32_bf16 v[152:155], a[128:131], v[112:115], 0\n"
"v_mfma_f32_16x16x32_bf16 v[152:155], a[132:135], v[116:119], v[152:155]\n"
"v_mfma_f32_16x16x32_bf16 v[152:155], a[136:139], v[120:123], v[152:155]\n"
"v_mfma_f32_16x16x32_bf16 v[152:155], a[140:143], v[124:127], v[152:155]\n"
"v_mfma_f32_16x16x32_bf16 v[156:159], a[144:147], v[112:115], 0\n"
"v_mfma_f32_16x16x32_bf16 v[156:159], a[148:151], v[116:119], v[156:159]\n"
"v_mfma_f32_16x16x32_bf16 v[156:159], a[152:155], v[120:123], v[156:159]\n"
"v_mfma_f32_16x16x32_bf16 v[156:159], a[156:159], v[124:127], v[156:159]\n"
"s_nop 7\n"
"v_max3_f32 v160, v160, v128, v129\n"
"v_max3_f32 v160, v160, v130, v131\n"
"v_max3_f32 v160, v160, v132, v133\n"
"v_max3_f32 v160, v160, v134, v135\n"
"v_max3_f32 v161, v161, v136, v137\n"
"v_max3_f32 v161, v161, v138, v139\n"
"v_max3_f32 v161, v161, v140, v141\n"
"v_max3_f32 v161, v161, v142, v143\n"
"v_max3_f32 v162, v162, v144, v145\n"
"v_max3_f32 v162, v162, v146, v147\n"
"v_max3_f32 v162, v162, v148, v149\n"
"v_max3_f32 v162, v162, v150, v151\n"
"v_max3_f32 v163, v163, v152, v153\n"
"v_max3_f32 v163, v163, v154, v155\n"
"v_max3_f32 v163, v163, v156, v157\n"
"v_max3_f32 v163, v163, v158, v159\n"
"s_barrier\n"
"s_sub_u32 s87, s87, 1\n"
"s_cmp_lg_u32 s87, 0\n"
"s_cbranch_scc1 AT_fbl\n"
"v_mov_b32 v164, v160\n"
"s_nop 1\n"
"v_permlane32_swap_b32 v160, v164\n"
"v_max_f32 v160, v160, v164\n"
"v_mov_b32 v164, v160\n"
"s_nop 1\n"
"v_permlane16_swap_b32 v160, v164\n"
"v_max_f32 v160, v160, v164\n"
"v_sub_f32 v24, 0, v160\n"
"v_sub_f32 v25, 0, v160\n"
"v_sub_f32 v26, 0, v160\n"
"v_sub_f32 v27, 0, v160\n"
"v_mov_b32 v164, v161\n"
"s_nop 1\n"
"v_permlane32_swap_b32 v161, v164\n"
"v_max_f32 v161, v161, v164\n"
"v_mov_b32 v164, v161\n"
"s_nop 1\n"
"v_permlane16_swap_b32 v161, v164\n"
"v_max_f32 v161, v161, v164\n"
"v_sub_f32 v28, 0, v161\n"
"v_sub_f32 v29, 0, v161\n"
"v_sub_f32 v30, 0, v161\n"
"v_sub_f32 v31, 0, v161\n"
"v_mov_b32 v164, v162\n"
"s_nop 1\n"
"v_permlane32_swap_b32 v162, v164\n"
"v_max_f32 v162, v162, v164\n"
"v_mov_b32 v164, v162\n"
"s_nop 1\n"
"v_permlane16_swap_b32 v162, v164\n"
"v_max_f32 v162, v162, v164\n"
"v_sub_f32 v32, 0, v162\n"
"v_sub_f32 v33, 0, v162\n"
"v_sub_f32 v34, 0, v162\n"
"v_sub_f32 v35, 0, v162\n"
"v_mov_b32 v164, v163\n"
"s_nop 1\n"
"v_permlane32_swap_b32 v163, v164\n"
"v_max_f32 v163, v163, v164\n"
"v_mov_b32 v164, v163\n"
"s_nop 1\n"
"v_permlane16_swap_b32 v163, v164\n"
"v_max_f32 v163, v163, v164\n"
"v_sub_f32 v36, 0, v163\n"
"v_sub_f32 v37, 0, v163\n"
"v_sub_f32 v38, 0, v163\n"
"v_sub_f32 v39, 0, v163\n"
"s_and_b32 s62, s42, 7\n"
"s_mul_i32 s46, s62, 663552\n"
"s_lshr_b32 s47, s43, 2\n"
"s_mul_i32 s47, s47, 1179648\n"
"s_getpc_b64 s[32:33]\n"
"AT_pc2:\n"
"s_sub_u32 s32, s32, AT_pc2-AT_start\n"
"s_subb_u32 s33, s33, 0\n"
"s_and_b32 s33, s33, 0xffff\n"
"s_mov_b32 s34, AT_kend-AT_start\n"
"s_mov_b32 s35, 0x20000\n"
"s_lshr_b32 s62, s42, 3\n"
"s_lshl_b32 s62, s62, 2\n"
"s_add_u32 s62, s62, s41\n"
"s_and_b32 s62, s62, 31\n"
"s_lshl_b32 s48, s62, 12\n"
"s_lshl_b32 s54, s44, 2\n"
"s_add_u32 s54, s54, s41\n"
"s_mul_i32 s54, s54, 36864\n"
"s_lshr_b32 s62, s43, 2\n"
"s_mul_i32 s62, s62, 2359296\n"
"s_add_u32 s54, s54, s62\n"
"s_add_u32 s54, s54, 139264\n"
"s_bfe_u32 s62, s43, 0x10001\n"
"s_mul_i32 s62, s62, 9216\n"
"s_add_u32 s54, s54, s62\n"
"s_and_b32 s62, s43, 1\n"
"s_lshl_b32 s49, s62, 3\n"
"s_add_u32 s49, s49, s54\n"
"s_add_u32 s49, s49, 4096\n"
"s_mul_i32 s62, s62, 5120\n"
"s_add_u32 s54, s54, s62\n"
"s_and_b32 s62, s43, 1\n"
"s_lshl_b32 s62, s62, 10\n"
"s_sub_u32 s54, s54, s62\n"
"s_add_u32 s49, s54, 4096\n"
"s_lshl_b32 s52, s44, 4\n"
"s_add_u32 s52, s52, s43\n"
"s_mul_i32 s52, s52, 18432\n"
"s_mul_i32 s62, s41, 5\n"
"s_min_u32 s62, s62, 13\n"
"s_lshl_b32 s62, s62, 10\n"
"s_add_u32 s52, s52, s62\n"
"s_add_u32 s52, s52, 26402816\n"
"s_add_u32 s53, s52, 4096\n"
"v_mov_b32 v40, 0x3f803f80\n"
"v_mov_b32 v41, 0x3f803f80\n"
"v_mov_b32 v42, 0x3f803f80\n"
"v_mov_b32 v43, 0x3f803f80\n"
"v_mov_b32 v20, 0\n"
"v_mov_b32 v21, 0\n"
"v_mov_b32 v22, 0\n"
"v_mov_b32 v23, 0\n"
"v_mov_b32 v44, 0\n"
"v_mov_b32 v52, 0\n"
"v_mov_b32 v216, -1\n"
"v_mov_b32 v217, -1\n"
"v_mov_b32 v218, -1\n"
"v_mov_b32 v219, -1\n"
"v_lshl_add_u32 v255, v248, 6, s48\n"
"v_lshlrev_b32 v207, 4, v248\n"
"v_add_u32 v208, 18432, v207\n"
"s_lshl_b32 s84, s41, 12\n"
"s_lshl_b32 s56, s43, 19\n"
"s_add_u32 s84, s84, s56\n"
"v_mov_b32 v251, 0x40000000\n"
"v_mov_b32 v252, 0x40000000\n"
"v_mov_b32 v253, 0x40000000\n"
"v_add_f32 v148, 0xc2480000, v161\n"
"v_add_f32 v149, 0xc2480000, v161\n"
"v_add_f32 v150, 0xc2480000, v161\n"
"v_add_f32 v151, 0xc2480000, v161\n"
"v_add_f32 v156, 0xc2480000, v163\n"
"v_add_f32 v157, 0xc2480000, v163\n"
"v_add_f32 v158, 0xc2480000, v163\n"
"v_add_f32 v159, 0xc2480000, v163\n"
"s_mov_b32 m0, s86\n"
"s_nop 0\n"
"buffer_load_dwordx4 v246, s[8:11], s84 offen offset:0 lds\n"
"buffer_load_dwordx4 v246, s[8:11], s84 offen offset:1024 lds\n"
"buffer_load_dwordx4 v247, s[8:11], s84 offen offset:2048 lds\n"
"buffer_load_dwordx4 v247, s[8:11], s84 offen offset:3072 lds\n"
"s_add_u32 m0, s86, 16384\n"
"s_nop 0\n"
"buffer_load_dwordx4 v246, s[12:15], s84 offen offset:0 lds\n"
"buffer_load_dwordx4 v247, s[12:15], s84 offen offset:1024 lds\n"
"buffer_load_dwordx4 v246, s[12:15], s84 offen offset:2048 lds\n"
"buffer_load_dwordx4 v247, s[12:15], s84 offen offset:3072 lds\n"
"s_add_u32 s84, s84, 16384\n"
"s_add_u32 m0, s86, 32768\n"
"s_nop 0\n"
"buffer_load_dwordx4 v246, s[8:11], s84 offen offset:0 lds\n"
"buffer_load_dwordx4 v246, s[8:11], s84 offen offset:1024 lds\n"
"buffer_load_dwordx4 v247, s[8:11], s84 offen offset:2048 lds\n"
"buffer_load_dwordx4 v247, s[8:11], s84 offen offset:3072 lds\n"
"s_add_u32 m0, s86, 49152\n"
"s_nop 0\n"
"buffer_load_dwordx4 v246, s[12:15], s84 offen offset:0 lds\n"
"buffer_load_dwordx4 v247, s[12:15], s84 offen offset:1024 lds\n"
"buffer_load_dwordx4 v246, s[12:15], s84 offen offset:2048 lds\n"
"buffer_load_dwordx4 v247, s[12:15], s84 offen offset:3072 lds\n"
"s_add_u32 s84, s84, 16384\n"
"s_add_u32 m0, s86, 65536\n"
"s_nop 0\n"
"buffer_load_dwordx4 v246, s[8:11], s84 offen offset:0 lds\n"
"buffer_load_dwordx4 v246, s[8:11], s84 offen offset:1024 lds\n"
"buffer_load_dwordx4 v247, s[8:11], s84 offen offset:2048 lds\n"
"buffer_load_dwordx4 v247, s[8:11], s84 offen offset:3072 lds\n"
"s_add_u32 m0, s86, 81920\n"
"s_nop 0\n"
"buffer_load_dwordx4 v246, s[12:15], s84 offen offset:0 lds\n"
"buffer_load_dwordx4 v247, s[12:15], s84 offen offset:1024 lds\n"
"buffer_load_dwordx4 v246, s[12:15], s84 offen offset:2048 lds\n"
"buffer_load_dwordx4 v247, s[12:15], s84 offen offset:3072 lds\n"
"s_add_u32 s84, s84, 16384\n"
"s_waitcnt vmcnt(16)\n"
"s_barrier\n"
"ds_read_b128 a[128:131], v240 offset:0\n"
"ds_read_b128 a[132:135], v240 offset:256\n"
"ds_read_b128 a[136:139], v240 offset:512\n"
"ds_read_b128 a[140:143], v240 offset:768\n"
"ds_read_b128 a[144:147], v240 offset:4096\n"
"ds_read_b128 a[148:151], v240 offset:4352\n"
"ds_read_b128 a[152:155], v240 offset:4608\n"
"ds_read_b128 a[156:159], v240 offset:4864\n"
"ds_read_b64_tr_b16 a[192:193], v242 offset:16384\n"
"ds_read_b64_tr_b16 a[194:195], v242 offset:20480\n"
"ds_read_b64_tr_b16 a[196:197], v243 offset:16384\n"
"ds_read_b64_tr_b16 a[198:199], v243 offset:20480\n"
"ds_read_b64_tr_b16 a[200:201], v242 offset:16640\n"
"ds_read_b64_tr_b16 a[202:203], v242 offset:20736\n"
"ds_read_b64_tr_b16 a[204:205], v243 offset:16640\n"
"ds_read_b64_tr_b16 a[206:207], v243 offset:20736\n"
"ds_read_b64_tr_b16 a[208:209], v242 offset:16896\n"
"ds_read_b64_tr_b16 a[210:211], v242 offset:20992\n"
"ds_read_b64_tr_b16 a[212:213], v243 offset:16896\n"
"ds_read_b64_tr_b16 a[214:215], v243 offset:20992\n"
"ds_read_b64_tr_b16 a[216:217], v242 offset:17152\n"
"ds_read_b64_tr_b16 a[218:219], v242 offset:21248\n"
"ds_read_b64_tr_b16 a[220:221], v243 offset:17152\n"
"ds_read_b64_tr_b16 a[222:223], v243 offset:21248\n"
"s_waitcnt lgkmcnt(0)\n"
"s_branch AT_main\n"
"AT_end:\n"
"AT_kend:\n"
  :: [kp] "s"(__builtin_amdgcn_kernarg_segment_ptr()), [blk] "s"(blockIdx.x), [wid] "s"(wid), [lds] "s"(ldsb) : "v0", "v1", "v2", "v3", "v4", "v5", "v6", "v7", "v8", "v9", "v10", "v11", "v12", "v13", "v14", "v15", "v16", "v17", "v18", "v19", "v20", "v21", "v22", "v23", "v24", "v25", "v26", "v27", "v28", "v29", "v30", "v31", "v32", "v33", "v34", "v35", "v36", "v37", "v38", "v39", "v40", "v41", "v42", "v43", "v44", "v45", "v46", "v47", "v48", "v49", "v50", "v51", "v52", "v53", "v54", "v55", "v56", "v57", "v58", "v59", "v60", "v61", "v62", "v63", "v64", "v65", "v66", "v67", "v68", "v69", "v70", "v71", "v72", "v73", "v74", "v75", "v76", "v77", "v78", "v79", "v80", "v81", "v82", "v83", "v84", "v85", "v86", "v87", "v88", "v89", "v90", "v91", "v92", "v93", "v94", "v95", "v96", "v97", "v98", "v99", "v100", "v101", "v102", "v103", "v104", "v105", "v106", "v107", "v108", "v109", "v110", "v111", "v112", "v113", "v114", "v115", "v116", "v117", "v118", "v119", "v120", "v121", "v122", "v123", "v124", "v125", "v126", "v127", "v128", "v129", "v130", "v131", "v132", "v133", "v134", "v135", "v136", "v137", "v138", "v139", "v140", "v141", "v142", "v143", "v144", "v145", "v146", "v147", "v148", "v149", "v150", "v151", "v152", "v153", "v154", "v155", "v156", "v157", "v158", "v159", "v160", "v161", "v162", "v163", "v164", "v165", "v166", "v167", "v168", "v169", "v170", "v171", "v172", "v173", "v174", "v175", "v176", "v177", "v178", "v179", "v180", "v181", "v182", "v183", "v184", "v185", "v186", "v187", "v188", "v189", "v190", "v191", "v192", "v193", "v194", "v195", "v196", "v197", "v198", "v199", "v200", "v201", "v202", "v203", "v204", "v205", "v206", "v207", "v208", "v209", "v210", "v211", "v212", "v213", "v214", "v215", "v216", "v217", "v218", "v219", "v220", "v221", "v222", "v223", "v224", "v225", "v226", "v227", "v228", "v229", "v230", "v231", "v232", "v233", "v234", "v235", "v236", "v237", "v238", "v239", "v240", "v241", "v242", "v243", "v244", "v245", "v246", "v247", "v248", "v249", "v250", "v251", "v252", "v253", "v254", "v255", "a0", "a1", "a2", "a3", "a4", "a5", "a6", "a7", "a8", "a9", "a10", "a11", "a12", "a13", "a14", "a15", "a16", "a17", "a18", "a19", "a20", "a21", "a22", "a23", "a24", "a25", "a26", "a27", "a28", "a29", "a30", "a31", "a32", "a33", "a34", "a35", "a36", "a37", "a38", "a39", "a40", "a41", "a42", "a43", "a44", "a45", "a46", "a47", "a48", "a49", "a50", "a51", "a52", "a53", "a54", "a55", "a56", "a57", "a58", "a59", "a60", "a61", "a62", "a63", "a64", "a65", "a66", "a67", "a68", "a69", "a70", "a71", "a72", "a73", "a74", "a75", "a76", "a77", "a78", "a79", "a80", "a81", "a82", "a83", "a84", "a85", "a86", "a87", "a88", "a89", "a90", "a91", "a92", "a93", "a94", "a95", "a96", "a97", "a98", "a99", "a100", "a101", "a102", "a103", "a104", "a105", "a106", "a107", "a108", "a109", "a110", "a111", "a112", "a113", "a114", "a115", "a116", "a117", "a118", "a119", "a120", "a121", "a122", "a123", "a124", "a125", "a126", "a127", "a128", "a129", "a130", "a131", "a132", "a133", "a134", "a135", "a136", "a137", "a138", "a139", "a140", "a141", "a142", "a143", "a144", "a145", "a146", "a147", "a148", "a149", "a150", "a151", "a152", "a153", "a154", "a155", "a156", "a157", "a158", "a159", "a160", "a161", "a162", "a163", "a164", "a165", "a166", "a167", "a168", "a169", "a170", "a171", "a172", "a173", "a174", "a175", "a176", "a177", "a178", "a179", "a180", "a181", "a182", "a183", "a184", "a185", "a186", "a187", "a188", "a189", "a190", "a191", "a192", "a193", "a194", "a195", "a196", "a197", "a198", "a199", "a200", "a201", "a202", "a203", "a204", "a205", "a206", "a207", "a208", "a209", "a210", "a211", "a212", "a213", "a214", "a215", "a216", "a217", "a218", "a219", "a220", "a221", "a222", "a223", "a224", "a225", "a226", "a227", "a228", "a229", "a230", "a231", "a232", "a233", "a234", "a235", "a236", "a237", "a238", "a239", "a240", "a241", "a242", "a243", "a244", "a245", "a246", "a247", "a248", "a249", "a250", "a251", "a252", "a253", "a254", "a255", "s8", "s9", "s10", "s11", "s12", "s13", "s14", "s15", "s16", "s17", "s18", "s19", "s20", "s21", "s22", "s23", "s24", "s25", "s26", "s27", "s28", "s29", "s30", "s31", "s32", "s33", "s34", "s35", "s36", "s37", "s38", "s39", "s40", "s41", "s42", "s43", "s44", "s45", "s46", "s47", "s48", "s49", "s50", "s51", "s52", "s53", "s54", "s55", "s56", "s57", "s58", "s59", "s60", "s61", "s62", "s63", "s64", "s65", "s66", "s67", "s68", "s69", "s70", "s71", "s72", "s73", "s74", "s75", "s76", "s77", "s78", "s79", "s80", "s81", "s82", "s83", "s84", "s85", "s86", "s87", "s88", "s89", "s90", "s91", "vcc", "scc", "memory");
}

extern "C" void kernel_launch(void* const* d_in, const int* in_sizes, int n_in, void* d_out, int out_size, void* d_ws, size_t ws_size,
                              hipStream_t stream) {
  const unsigned F = 0x20000u;
  KArgs a;
  a.k = {d_in[1], HKV * SQ * 256u, F}; a.v = {d_in[2], HKV * SQ * 256u, F}; a.x = {d_in[0], SQ * DM * 2u, F}; a.wq = {d_in[3], DM * 4096u * 2u, F};
  a.wo = {d_in[4], DM * 4096u * 2u, F}; a.ws = {d_ws, (unsigned)WS_END, F}; a.c = {d_in[10], DM * 2u, F}; a.wmod = {d_in[11], DM * 6u * DM * 2u, F};
  a.wg = {d_in[7], DM * FF * 2u, F}; a.wu = {d_in[8], DM * FF * 2u, F}; a.wd = {d_in[9], DM * FF * 2u, F}; a.out = {d_out, SQ * DM * 2u, F}; a.wr = {d_in[6], DM * 2u, F};
  (void)hipFuncSetAttribute((const void*)k_main, hipFuncAttributeMaxDynamicSharedMemorySize, 163840);
  k_main<<<256, 256, 163840, stream>>>(a);
}
```

```cpp
#include <hip/hip_runtime.h>
#include <stdint.h>


constexpr int SQ = 2048, DM = 576, HKV = 16, FF = 4608;

constexpr size_t WS_FLAG = 0;
constexpr size_t WS_R    = 20480;
constexpr size_t WS_GRAN = 32768;
constexpr size_t WS_EPOCH = 65536;
constexpr size_t WS_ST   = 69632;
constexpr size_t WS_PART = 139264;
constexpr size_t WS_ROW  = WS_PART + (size_t)HKV * SQ * DM * 2;
constexpr size_t WS_Y    = WS_ROW + (size_t)SQ * DM * 16;
constexpr size_t WS_END  = WS_Y + 72 * 192 * 128;

struct Buf { const void* p; unsigned bytes; unsigned fmt; };
struct KArgs { Buf k, v, x, wq, wo, ws, c, wmod, wg, wu, wd, out, wr; };

__global__ __launch_bounds__(256, 1) __attribute__((aligned(4096))) void k_main(KArgs args) {
  extern __shared__ __attribute__((aligned(16))) char lds[];
  const unsigned wid = __builtin_amdgcn_readfirstlane(threadIdx.x >> 6);
  const unsigned ldsb = (unsigned)(size_t)lds;
  asm volatile(
"AT_start:\n"
"s_load_dwordx16 s[24:39], %[kp], 64\n"
"s_load_dwordx16 s[8:23], %[kp], 0\n"
"s_load_dwordx16 s[64:79], %[kp], 128\n"
"s_load_dwordx4 s[80:83], %[kp], 192\n"
"s_mov_b32 s42, %[blk]\n"
"s_mov_b32 s41, %[wid]\n"
"s_mov_b32 s40, %[lds]\n"
"s_mov_b32 s45, 0\n"
"v_mbcnt_lo_u32_b32 v248, -1, 0\n"
"v_mbcnt_hi_u32_b32 v248, -1, v248\n"
"s_and_b32 s56, s42, 3\n"
"s_lshr_b32 s57, s42, 3\n"
"s_and_b32 s58, s57, 3\n"
"s_lshl_b32 s56, s56, 2\n"
"s_add_u32 s43, s56, s58\n"
"s_lshr_b32 s44, s57, 2\n"
"s_and_b32 s56, s42, 7\n"
"s_lshr_b32 s56, s56, 2\n"
"s_lshl_b32 s56, s56, 3\n"
"s_add_u32 s44, s44, s56\n"
"s_lshr_b32 s56, s42, 5\n"
"s_lshl_b32 s56, s56, 3\n"
"s_and_b32 s58, s42, 7\n"
"s_add_u32 s56, s56, s58\n"
"s_lshl_b32 s56, s56, 2\n"
"s_and_b32 s57, s57, 3\n"
"s_add_u32 s55, s56, s57\n"
"v_lshrrev_b32 v226, 4, v248\n"
"v_bfe_u32 v227, v248, 2, 2\n"
"v_and_b32 v228, 3, v248\n"
"v_and_b32 v229, 15, v248\n"
"v_lshrrev_b32 v230, 3, v248\n"
"v_and_b32 v231, 7, v248\n"
"s_lshl_b32 s56, s41, 6\n"
"v_add_u32 v234, s56, v248\n"
"v_lshrrev_b32 v232, 1, v234\n"
"v_and_b32 v233, 1, v234\n"
"v_lshlrev_b32 v236, 1, v232\n"
"v_mul_u32_u24 v232, 6912, v232\n"
"v_lshl_add_u32 v235, v233, 4, v232\n"
"s_lshl_b32 s7, s55, 5\n"
"s_add_u32 s84, s55, 0\n"
"s_and_b32 s84, s84, 3\n"
"s_mul_i32 s3, s84, 884736\n"
"s_add_u32 s3, s3, s7\n"
"s_lshl_b32 s84, s84, 8\n"
"s_add_u32 s86, s55, 1\n"
"s_and_b32 s86, s86, 3\n"
"s_mul_i32 s4, s86, 884736\n"
"s_add_u32 s4, s4, s7\n"
"s_lshl_b32 s86, s86, 8\n"
"s_add_u32 s87, s55, 2\n"
"s_and_b32 s87, s87, 3\n"
"s_mul_i32 s5, s87, 884736\n"
"s_add_u32 s5, s5, s7\n"
"s_lshl_b32 s87, s87, 8\n"
"s_add_u32 s54, s55, 3\n"
"s_and_b32 s54, s54, 3\n"
"s_mul_i32 s6, s54, 884736\n"
"s_add_u32 s6, s6, s7\n"
"s_lshl_b32 s54, s54, 8\n"
"s_add_u32 s7, s7, 3538944\n"
"s_lshl_b32 s63, s44, 4\n"
"s_add_u32 s63, s63, s43\n"
"s_mul_i32 s46, s63, 9216\n"
"s_cmp_eq_u32 s41, 3\n"
"s_cselect_b32 s61, 0x10000000, 0\n"
"v_lshrrev_b32 v220, 3, v234\n"
"v_mul_u32_u24 v233, 1152, v231\n"
"v_lshl_add_u32 v233, v220, 4, v233\n"
"v_or_b32 v222, s61, v233\n"
"s_waitcnt lgkmcnt(0)\n"
"s_load_dword s85, s[28:29], 65536\n"
"s_cmp_lt_u32 s55, 216\n"
"s_cselect_b32 s38, s38, 0\n"
"buffer_load_dwordx4 v[192:195], v222, s[16:19], s46 offen offset:0\n"
"buffer_load_dwordx4 v[196:199], v222, s[16:19], s46 offen offset:384\n"
"buffer_load_dwordx4 v[200:203], v222, s[16:19], s46 offen offset:768\n"
"s_mul_i32 s56, s63, 9216\n"
"s_add_u32 s56, s56, 31137792\n"
"v_mov_b32 v214, 0\n"
"buffer_load_dword v250, v214, s[28:31], s56 offen\n"
"buffer_load_dword v250, v214, s[28:31], 0 offen offset:32\n"
"buffer_load_dwordx4 v[144:147], v235, s[36:39], s3 offen\n"
"buffer_load_ushort v164, v236, s[32:35], s84 offen\n"
"buffer_load_ushort v165, v236, s[32:35], s86 offen\n"
"buffer_load_ushort v166, v236, s[32:35], s87 offen\n"
"buffer_load_ushort v167, v236, s[32:35], s54 offen\n"
"buffer_load_ushort v168, v236, s[32:35], 0 offen offset:1024\n"
"s_lshl_b32 s50, s41, 17\n"
"s_lshl_b32 s56, s43, 9\n"
"s_add_u32 s50, s50, s56\n"
"s_lshl_b32 s56, s41, 11\n"
"s_add_u32 s51, s40, s56\n"
"v_lshlrev_b32 v232, 13, v227\n"
"v_lshl_add_u32 v232, v226, 6, v232\n"
"v_lshl_add_u32 v217, v228, 4, v232\n"
"v_lshlrev_b32 v232, 8, v220\n"
"v_lshl_add_u32 v232, v231, 4, v232\n"
"v_or_b32 v223, s61, v232\n"
"s_lshr_b32 s47, s63, 1\n"
"s_mul_i32 s47, s47, 18432\n"
"s_and_b32 s56, s63, 1\n"
"s_lshl_b32 s56, s56, 7\n"
"s_add_u32 s47, s47, s56\n"
"s_add_u32 s47, s47, 31137792\n"
"s_add_u32 s48, s47, 6144\n"
"s_add_u32 s49, s47, 12288\n"
"s_add_u32 s56, s40, 146944\n"
"v_lshl_add_u32 v220, v231, 3, s56\n"
"s_lshl_b32 s56, s41, 6\n"
"v_add_u32 v221, s56, v220\n"
"v_min_u32 v212, 1, v234\n"
"v_lshlrev_b32 v212, 28, v212\n"
"s_lshl_b32 s56, s63, 6\n"
"s_add_u32 s56, s56, 31121408\n"
"v_add_u32 v212, s56, v212\n"
"s_lshl_b32 s56, s44, 2\n"
"s_add_u32 s56, s56, s41\n"
"s_mul_i32 s2, s56, 36864\n"
"s_add_u32 s2, s2, 31137792\n"
"s_lshl_b32 s56, s56, 8\n"
"s_add_u32 s56, s56, 31121408\n"
"v_lshl_add_u32 v213, v228, 6, s56\n"
"v_lshlrev_b32 v249, 4, v248\n"
"v_add_u32 v255, 18432, v249\n"
"v_mov_b32 v215, 0x3f803f80\n"
"s_lshl_b32 s56, s41, 12\n"
"s_add_u32 s86, s40, s56\n"
"s_lshl_b32 s84, s41, 12\n"
"s_lshl_b32 s56, s43, 19\n"
"s_add_u32 s84, s84, s56\n"
"s_mov_b32 s61, 32768\n"
"s_mov_b32 s62, 37376\n"
"v_and_b32 v232, 2, v227\n"
"v_xor_b32 v232, v232, v226\n"
"v_lshlrev_b32 v233, 10, v227\n"
"v_lshl_add_u32 v233, v228, 6, v233\n"
"v_lshl_add_u32 v233, v232, 4, v233\n"
"v_add_u32 v240, s40, v233\n"
"v_add_u32 v241, 0x10000, v240\n"
"v_lshlrev_b32 v233, 10, v226\n"
"v_lshl_add_u32 v233, v227, 6, v233\n"
"v_lshl_add_u32 v233, v228, 3, v233\n"
"v_add_u32 v233, s40, v233\n"
"v_and_b32 v232, 1, v226\n"
"v_lshl_add_u32 v242, v232, 5, v233\n"
"v_xor_b32 v243, 32, v242\n"
"v_add_u32 v244, 0x10000, v242\n"
"v_add_u32 v245, 0x10000, v243\n"
"v_lshlrev_b32 v233, 8, v227\n"
"v_lshl_add_u32 v233, v226, 6, v233\n"
"v_lshl_add_u32 v246, v228, 4, v233\n"
"v_xor_b32 v247, 32, v246\n"
"v_mul_u32_u24 v233, 16392, v232\n"
"v_lshrrev_b32 v232, 1, v226\n"
"v_lshl_add_u32 v233, v232, 11, v233\n"
"v_lshl_add_u32 v233, v227, 6, v233\n"
"v_lshl_add_u32 v233, v228, 4, v233\n"
"v_add_u32 v218, s40, v233\n"
"v_add_u32 v219, 65568, v218\n"
"s_add_u32 s56, s40, 148480\n"
"v_lshl_add_u32 v224, v226, 4, s56\n"
"v_lshrrev_b32 v232, 3, v234\n"
"v_lshlrev_b32 v233, 8, v232\n"
"v_and_b32 v232, 1, v234\n"
"v_lshl_add_u32 v233, v232, 6, v233\n"
"v_bfe_u32 v232, v234, 1, 2\n"
"v_lshl_add_u32 v233, v232, 4, v233\n"
"v_add_u32 v216, s56, v233\n"
"v_lshlrev_b32 v225, 5, v234\n"
"v_mov_b32 v232, 0x40000000\n"
"v_cmp_gt_u32 vcc, 144, v234\n"
"s_nop 1\n"
"v_cndmask_b32 v225, v232, v225, vcc\n"
"s_lshl_b32 s56, s41, 8\n"
"s_add_u32 s57, s40, 147456\n"
"s_add_u32 s56, s56, s57\n"
"v_and_b32 v232, 1, v248\n"
"v_lshlrev_b32 v232, 5, v232\n"
"v_lshl_add_u32 v232, v226, 6, v232\n"
"v_add_u32 v238, s56, v232\n"
"v_lshlrev_b32 v232, 8, v226\n"
"v_lshl_add_u32 v232, v229, 2, v232\n"
"v_add_u32 v239, s57, v232\n"
"v_lshlrev_b32 v232, 6, v228\n"
"v_lshl_add_u32 v232, v226, 4, v232\n"
"v_add_u32 v254, s40, v232\n"
"s_lshr_b32 s57, s42, 3\n"
"s_lshl_b32 s57, s57, 2\n"
"s_add_u32 s57, s57, s41\n"
"s_sub_u32 s57, s57, 128\n"
"s_and_b32 s58, s42, 7\n"
"s_mul_i32 s59, s57, 9216\n"
"s_mul_i32 s60, s58, 1152\n"
"s_add_u32 s59, s59, s60\n"
"v_lshlrev_b32 v252, 6, v248\n"
"v_add_u32 v253, 0x40000000, v252\n"
"v_cmp_gt_u32 vcc, 18, v248\n"
"s_nop 1\n"
"v_cndmask_b32 v251, v253, v252, vcc\n"
"v_cmp_gt_u32 vcc, 36, v248\n"
"s_nop 1\n"
"v_cndmask_b32 v253, v253, v252, vcc\n"
"s_mul_i32 s60, s57, 1152\n"
"s_lshl_b32 s57, s60, 1\n"
"v_add_u32 v252, s60, v251\n"
"v_add_u32 v251, s59, v251\n"
"v_add_u32 v253, s57, v253\n"
"v_mov_b32 v237, 0xffff0000\n"
"buffer_load_dwordx4 v[148:151], v235, s[36:39], s4 offen\n"
"buffer_load_dwordx4 v[152:155], v235, s[36:39], s5 offen\n"
"buffer_load_dwordx4 v[156:159], v235, s[36:39], s6 offen\n"
"buffer_load_dwordx4 v[160:163], v235, s[36:39], s7 offen\n"
"s_waitcnt vmcnt(12)\n"
"v_dot2_f32_bf16 v204, v192, v215, 0\n"
"v_dot2_f32_bf16 v205, v192, v192, 0\n"
"v_dot2_f32_bf16 v206, v193, v215, 0\n"
"v_dot2_f32_bf16 v207, v193, v193, 0\n"
"v_dot2_f32_bf16 v204, v194, v215, v204\n"
"v_dot2_f32_bf16 v205, v194, v194, v205\n"
"v_dot2_f32_bf16 v206, v195, v215, v206\n"
"v_dot2_f32_bf16 v207, v195, v195, v207\n"
"v_dot2_f32_bf16 v204, v196, v215, v204\n"
"v_dot2_f32_bf16 v205, v196, v196, v205\n"
"v_dot2_f32_bf16 v206, v197, v215, v206\n"
"v_dot2_f32_bf16 v207, v197, v197, v207\n"
"v_dot2_f32_bf16 v204, v198, v215, v204\n"
"v_dot2_f32_bf16 v205, v198, v198, v205\n"
"v_dot2_f32_bf16 v206, v199, v215, v206\n"
"v_dot2_f32_bf16 v207, v199, v199, v207\n"
"v_dot2_f32_bf16 v204, v200, v215, v204\n"
"v_dot2_f32_bf16 v205, v200, v200, v205\n"
"v_dot2_f32_bf16 v206, v201, v215, v206\n"
"v_dot2_f32_bf16 v207, v201, v201, v207\n"
"v_dot2_f32_bf16 v204, v202, v215, v204\n"
"v_dot2_f32_bf16 v205, v202, v202, v205\n"
"v_dot2_f32_bf16 v206, v203, v215, v206\n"
"v_dot2_f32_bf16 v207, v203, v203, v207\n"
"s_nop 2\n"
"v_add_f32 v204, v204, v206\n"
"v_add_f32 v205, v205, v207\n"
"s_nop 0\n"
"v_add_f32_dpp v204, v204, v204 row_ror:8 row_mask:0xf bank_mask:0xf\n"
"v_add_f32_dpp v205, v205, v205 row_ror:8 row_mask:0xf bank_mask:0xf\n"
"s_nop 0\n"
"v_mov_b32 v206, v204\n"
"v_mov_b32 v207, v205\n"
"s_nop 0\n"
"v_permlane16_swap_b32 v204, v206\n"
"v_permlane16_swap_b32 v205, v207\n"
"v_add_f32 v204, v204, v206\n"
"v_add_f32 v205, v205, v207\n"
"v_mov_b32 v206, v204\n"
"v_mov_b32 v207, v205\n"
"s_nop 0\n"
"v_permlane32_swap_b32 v204, v206\n"
"v_permlane32_swap_b32 v205, v207\n"
"v_add_f32 v204, v204, v206\n"
"v_add_f32 v205, v205, v207\n"
"s_mov_b64 exec, 0xff\n"
"ds_write_b64 v221, v[204:205]\n"
"s_mov_b64 exec, -1\n"
"v_lshlrev_b32 v0, 16, v192\n"
"v_and_b32 v1, v237, v192\n"
"v_lshlrev_b32 v2, 16, v193\n"
"v_and_b32 v3, v237, v193\n"
"v_lshlrev_b32 v4, 16, v194\n"
"v_and_b32 v5, v237, v194\n"
"v_lshlrev_b32 v6, 16, v195\n"
"v_and_b32 v7, v237, v195\n"
"v_lshlrev_b32 v8, 16, v196\n"
"v_and_b32 v9, v237, v196\n"
"v_lshlrev_b32 v10, 16, v197\n"
"v_and_b32 v11, v237, v197\n"
"v_lshlrev_b32 v12, 16, v198\n"
"v_and_b32 v13, v237, v198\n"
"v_lshlrev_b32 v14, 16, v199\n"
"v_and_b32 v15, v237, v199\n"
"v_lshlrev_b32 v16, 16, v200\n"
"v_and_b32 v17, v237, v200\n"
"v_lshlrev_b32 v18, 16, v201\n"
"v_and_b32 v19, v237, v201\n"
"v_lshlrev_b32 v20, 16, v202\n"
"v_and_b32 v21, v237, v202\n"
"v_lshlrev_b32 v22, 16, v203\n"
"v_and_b32 v23, v237, v203\n"
"s_waitcnt lgkmcnt(0)\n"
"s_barrier\n"
"ds_read_b64 v[204:205], v220 offset:0\n"
"ds_read_b64 v[206:207], v220 offset:64\n"
"ds_read_b64 v[208:209], v220 offset:128\n"
"s_waitcnt lgkmcnt(1)\n"
"v_add_f32 v204, v204, v206\n"
"v_add_f32 v205, v205, v207\n"
"s_waitcnt lgkmcnt(0)\n"
"v_add_f32 v204, v204, v208\n"
"v_add_f32 v205, v205, v209\n"
"v_mul_f32 v204, 0x3ae38e39, v204\n"
"v_mul_f32 v205, 0x3ae38e39, v205\n"
"v_fma_f32 v205, -v204, v204, v205\n"
"v_max_f32 v205, 0, v205\n"
"v_add_f32 v205, 0x3727c5ac, v205\n"
"v_rsq_f32 v206, v205\n"
"s_nop 0\n"
"v_mul_f32_e64 v208, -v204, v206\n"
"v_pk_fma_f32 v[0:1], v[0:1], v[206:207], v[208:209] op_sel_hi:[1,0,0]\n"
"v_pk_fma_f32 v[2:3], v[2:3], v[206:207], v[208:209] op_sel_hi:[1,0,0]\n"
"v_pk_fma_f32 v[4:5], v[4:5], v[206:207], v[208:209] op_sel_hi:[1,0,0]\n"
"v_pk_fma_f32 v[6:7], v[6:7], v[206:207], v[208:209] op_sel_hi:[1,0,0]\n"
"v_cvt_pk_f16_f32 v192, v0, v1\n"
"v_cvt_pk_f16_f32 v193, v2, v3\n"
"v_cvt_pk_f16_f32 v194, v4, v5\n"
"v_cvt_pk_f16_f32 v195, v6, v7\n"
"buffer_store_dwordx4 v[192:195], v223, s[28:31], s47 offen sc1\n"
"v_pk_fma_f32 v[8:9], v[8:9], v[206:207], v[208:209] op_sel_hi:[1,0,0]\n"
"v_pk_fma_f32 v[10:11], v[10:11], v[206:207], v[208:209] op_sel_hi:[1,0,0]\n"
"v_pk_fma_f32 v[12:13], v[12:13], v[206:207], v[208:209] op_sel_hi:[1,0,0]\n"
"v_pk_fma_f32 v[14:15], v[14:15], v[206:207], v[208:209] op_sel_hi:[1,0,0]\n"
"v_cvt_pk_f16_f32 v196, v8, v9\n"
"v_cvt_pk_f16_f32 v197, v10, v11\n"
"v_cvt_pk_f16_f32 v198, v12, v13\n"
"v_cvt_pk_f16_f32 v199, v14, v15\n"
"buffer_store_dwordx4 v[196:199], v223, s[28:31], s48 offen sc1\n"
"v_pk_fma_f32 v[16:17], v[16:17], v[206:207], v[208:209] op_sel_hi:[1,0,0]\n"
"v_pk_fma_f32 v[18:19], v[18:19], v[206:207], v[208:209] op_sel_hi:[1,0,0]\n"
"v_pk_fma_f32 v[20:21], v[20:21], v[206:207], v[208:209] op_sel_hi:[1,0,0]\n"
"v_pk_fma_f32 v[22:23], v[22:23], v[206:207], v[208:209] op_sel_hi:[1,0,0]\n"
"v_cvt_pk_f16_f32 v200, v16, v17\n"
"v_cvt_pk_f16_f32 v201, v18, v19\n"
"v_cvt_pk_f16_f32 v202, v20, v21\n"
"v_cvt_pk_f16_f32 v203, v22, v23\n"
"buffer_store_dwordx4 v[200:203], v223, s[28:31], s49 offen sc1\n"
"s_waitcnt vmcnt(7)\n"
"v_lshlrev_b32 v164, 16, v164\n"
"v_mul_f32 v184, 0xbfb8aa3b, v164\n"
"v_lshlrev_b32 v165, 16, v165\n"
"v_mul_f32 v185, 0xbfb8aa3b, v165\n"
"s_add_u32 m0, s51, 98352\n"
"v_lshlrev_b32 v166, 16, v166\n"
"buffer_load_dwordx4 v217, s[20:23], s50 offen offset:0 lds\n"
"v_mul_f32 v186, 0xbfb8aa3b, v166\n"
"v_lshlrev_b32 v167, 16, v167\n"
"v_mul_f32 v187, 0xbfb8aa3b, v167\n"
"v_lshlrev_b32 v168, 16, v168\n"
"v_mul_f32 v188, 0xbfb8aa3b, v168\n"
"s_add_u32 m0, s51, 106288\n"
"v_exp_f32 v184, v184\n"
"buffer_load_dwordx4 v217, s[20:23], s50 offen offset:256 lds\n"
"s_add_u32 s50, s50, 32768\n"
"v_exp_f32 v185, v185\n"
"v_exp_f32 v186, v186\n"
"v_exp_f32 v187, v187\n"
"v_exp_f32 v188, v188\n"
"v_add_f32 v184, 1.0, v184\n"
"s_add_u32 m0, s51, 99376\n"
"v_add_f32 v185, 1.0, v185\n"
"buffer_load_dwordx4 v217, s[20:23], s50 offen offset:0 lds\n"
"v_add_f32 v186, 1.0, v186\n"
"v_add_f32 v187, 1.0, v187\n"
"v_add_f32 v188, 1.0, v188\n"
"v_rcp_f32 v184, v184\n"
"v_rcp_f32 v185, v185\n"
"s_add_u32 m0, s51, 107312\n"
"v_rcp_f32 v186, v186\n"
"buffer_load_dwordx4 v217, s[20:23], s50 offen offset:256 lds\n"
"s_add_u32 s50, s50, 32768\n"
"v_rcp_f32 v187, v187\n"
"v_rcp_f32 v188, v188\n"
"v_mul_f32 v164, v164, v184\n"
"v_mul_f32 v165, v165, v185\n"
"v_mul_f32 v166, v166, v186\n"
"v_mul_f32 v167, v167, v187\n"
"v_mul_f32 v168, v168, v188\n"
"v_cvt_pk_bf16_f32 v164, v164, v164\n"
"v_and_b32 v164, v237, v164\n"
"v_cvt_pk_bf16_f32 v165, v165, v165\n"
"v_and_b32 v165, v237, v165\n"
"v_cvt_pk_bf16_f32 v166, v166, v166\n"
"v_and_b32 v166, v237, v166\n"
"v_cvt_pk_bf16_f32 v167, v167, v167\n"
"v_and_b32 v167, v237, v167\n"
"v_cvt_pk_bf16_f32 v168, v168, v168\n"
"v_and_b32 v168, v237, v168\n"
"s_waitcnt vmcnt(16)\n"
"v_lshlrev_b32 v184, 16, v144\n"
"v_and_b32 v185, v237, v144\n"
"v_lshlrev_b32 v186, 16, v145\n"
"v_and_b32 v187, v237, v145\n"
"v_lshlrev_b32 v188, 16, v146\n"
"v_and_b32 v189, v237, v146\n"
"v_lshlrev_b32 v190, 16, v147\n"
"v_and_b32 v191, v237, v147\n"
"v_mul_f32 v176, v164, v184\n"
"v_mul_f32 v177, v164, v185\n"
"v_mul_f32 v178, v164, v186\n"
"v_mul_f32 v179, v164, v187\n"
"v_mul_f32 v180, v164, v188\n"
"v_mul_f32 v181, v164, v189\n"
"v_mul_f32 v182, v164, v190\n"
"v_mul_f32 v183, v164, v191\n"
"s_waitcnt vmcnt(10)\n"
"v_lshlrev_b32 v184, 16, v148\n"
"v_and_b32 v185, v237, v148\n"
"v_lshlrev_b32 v186, 16, v149\n"
"v_and_b32 v187, v237, v149\n"
"v_lshlrev_b32 v188, 16, v150\n"
"v_and_b32 v189, v237, v150\n"
"v_lshlrev_b32 v190, 16, v151\n"
"v_and_b32 v191, v237, v151\n"
"v_fmac_f32 v176, v165, v184\n"
"v_fmac_f32 v177, v165, v185\n"
"v_fmac_f32 v178, v165, v186\n"
"v_fmac_f32 v179, v165, v187\n"
"v_fmac_f32 v180, v165, v188\n"
"v_fmac_f32 v181, v165, v189\n"
"v_fmac_f32 v182, v165, v190\n"
"v_fmac_f32 v183, v165, v191\n"
"s_waitcnt vmcnt(9)\n"
"v_lshlrev_b32 v184, 16, v152\n"
"v_and_b32 v185, v237, v152\n"
"v_lshlrev_b32 v186, 16, v153\n"
"v_and_b32 v187, v237, v153\n"
"v_lshlrev_b32 v188, 16, v154\n"
"v_and_b32 v189, v237, v154\n"
"v_lshlrev_b32 v190, 16, v155\n"
"v_and_b32 v191, v237, v155\n"
"v_fmac_f32 v176, v166, v184\n"
"v_fmac_f32 v177, v166, v185\n"
"v_fmac_f32 v178, v166, v186\n"
"v_fmac_f32 v179, v166, v187\n"
"v_fmac_f32 v180, v166, v188\n"
"v_fmac_f32 v181, v166, v189\n"
"v_fmac_f32 v182, v166, v190\n"
"v_fmac_f32 v183, v166, v191\n"
"s_waitcnt vmcnt(8)\n"
"v_lshlrev_b32 v184, 16, v156\n"
"v_and_b32 v185, v237, v156\n"
"v_lshlrev_b32 v186, 16, v157\n"
"v_and_b32 v187, v237, v157\n"
"v_lshlrev_b32 v188, 16, v158\n"
"v_and_b32 v189, v237, v158\n"
"v_lshlrev_b32 v190, 16, v159\n"
"v_and_b32 v191, v237, v159\n"
"v_fmac_f32 v176, v167, v184\n"
"v_fmac_f32 v177, v167, v185\n"
"v_fmac_f32 v178, v167, v186\n"
"v_fmac_f32 v179, v167, v187\n"
"v_fmac_f32 v180, v167, v188\n"
"v_fmac_f32 v181, v167, v189\n"
"v_fmac_f32 v182, v167, v190\n"
"v_fmac_f32 v183, v167, v191\n"
"s_waitcnt vmcnt(7)\n"
"v_lshlrev_b32 v184, 16, v160\n"
"v_and_b32 v185, v237, v160\n"
"v_lshlrev_b32 v186, 16, v161\n"
"v_and_b32 v187, v237, v161\n"
"v_lshlrev_b32 v188, 16, v162\n"
"v_and_b32 v189, v237, v162\n"
"v_lshlrev_b32 v190, 16, v163\n"
"v_and_b32 v191, v237, v163\n"
"v_fmac_f32 v176, v168, v184\n"
"v_fmac_f32 v177, v168, v185\n"
"v_fmac_f32 v178, v168, v186\n"
"v_fmac_f32 v179, v168, v187\n"
"v_fmac_f32 v180, v168, v188\n"
"v_fmac_f32 v181, v168, v189\n"
"v_fmac_f32 v182, v168, v190\n"
"v_fmac_f32 v183, v168, v191\n"
"v_add_f32_dpp v176, v176, v176 row_ror:2 row_mask:0xf bank_mask:0xf\n"
"v_add_f32_dpp v177, v177, v177 row_ror:2 row_mask:0xf bank_mask:0xf\n"
"v_add_f32_dpp v178, v178, v178 row_ror:2 row_mask:0xf bank_mask:0xf\n"
"v_add_f32_dpp v179, v179, v179 row_ror:2 row_mask:0xf bank_mask:0xf\n"
"v_add_f32_dpp v180, v180, v180 row_ror:2 row_mask:0xf bank_mask:0xf\n"
"v_add_f32_dpp v181, v181, v181 row_ror:2 row_mask:0xf bank_mask:0xf\n"
"v_add_f32_dpp v182, v182, v182 row_ror:2 row_mask:0xf bank_mask:0xf\n"
"v_add_f32_dpp v183, v183, v183 row_ror:2 row_mask:0xf bank_mask:0xf\n"
"v_add_f32_dpp v176, v176, v176 row_ror:4 row_mask:0xf bank_mask:0xf\n"
"v_add_f32_dpp v177, v177, v177 row_ror:4 row_mask:0xf bank_mask:0xf\n"
"v_add_f32_dpp v178, v178, v178 row_ror:4 row_mask:0xf bank_mask:0xf\n"
"v_add_f32_dpp v179, v179, v179 row_ror:4 row_mask:0xf bank_mask:0xf\n"
"v_add_f32_dpp v180, v180, v180 row_ror:4 row_mask:0xf bank_mask:0xf\n"
"v_add_f32_dpp v181, v181, v181 row_ror:4 row_mask:0xf bank_mask:0xf\n"
"v_add_f32_dpp v182, v182, v182 row_ror:4 row_mask:0xf bank_mask:0xf\n"
"v_add_f32_dpp v183, v183, v183 row_ror:4 row_mask:0xf bank_mask:0xf\n"
"v_add_f32_dpp v176, v176, v176 row_ror:8 row_mask:0xf bank_mask:0xf\n"
"v_add_f32_dpp v177, v177, v177 row_ror:8 row_mask:0xf bank_mask:0xf\n"
"v_add_f32_dpp v178, v178, v178 row_ror:8 row_mask:0xf bank_mask:0xf\n"
"v_add_f32_dpp v179, v179, v179 row_ror:8 row_mask:0xf bank_mask:0xf\n"
"v_add_f32_dpp v180, v180, v180 row_ror:8 row_mask:0xf bank_mask:0xf\n"
"v_add_f32_dpp v181, v181, v181 row_ror:8 row_mask:0xf bank_mask:0xf\n"
"v_add_f32_dpp v182, v182, v182 row_ror:8 row_mask:0xf bank_mask:0xf\n"
"v_add_f32_dpp v183, v183, v183 row_ror:8 row_mask:0xf bank_mask:0xf\n"
"s_mov_b32 exec_lo, 0x00030003\n"
"s_mov_b32 exec_hi, 0x00030003\n"
"ds_write_b128 v238, v[176:179]\n"
"ds_write_b128 v238, v[180:183] offset:16\n"
"s_mov_b64 exec, -1\n"
"s_add_u32 s85, s85, 1\n"
"v_mov_b32 v189, s85\n"
"s_waitcnt vmcnt(0) lgkmcnt(0)\n"
"s_barrier\n"
"buffer_store_dword v189, v212, s[28:31], 0 offen sc1\n"
"ds_read_b32 v184, v239 offset:0\n"
"ds_read_b32 v185, v239 offset:64\n"
"ds_read_b32 v186, v239 offset:128\n"
"ds_read_b32 v187, v239 offset:192\n"
"s_lshl_b32 s56, s55, 7\n"
"s_add_u32 s56, s56, 32768\n"
"s_lshl_b32 s58, s41, 2\n"
"s_lshl_b32 s58, 15, s58\n"
"v_lshlrev_b32 v190, 3, v248\n"
"s_waitcnt lgkmcnt(2)\n"
"v_add_f32 v184, v184, v185\n"
"s_waitcnt lgkmcnt(0)\n"
"v_add_f32 v186, v186, v187\n"
"v_add_f32 v184, v184, v186\n"
"v_mov_b32 v185, v184\n"
"s_nop 1\n"
"v_permlane32_swap_b32 v184, v185\n"
"v_add_f32 v184, v184, v185\n"
"v_mov_b32 v185, v184\n"
"s_nop 1\n"
"v_permlane16_swap_b32 v184, v185\n"
"v_add_f32 v184, v184, v185\n"
"v_cvt_pk_bf16_f32 v188, v184, v184\n"
"v_and_b32 v188, v237, v188\n"
"s_mov_b32 exec_lo, s58\n"
"s_mov_b32 exec_hi, 0\n"
"buffer_store_dwordx2 v[188:189], v190, s[28:31], s56 offen sc1\n"
"s_mov_b64 exec, -1\n"
"s_getpc_b64 s[36:37]\n"
"AT_pc0:\n"
"s_sub_u32 s36, s36, AT_pc0-AT_start\n"
"s_subb_u32 s37, s37, 0\n"
"s_and_b32 s37, s37, 0xffff\n"
"s_mov_b32 s38, AT_kend-AT_start\n"
"s_mov_b32 s39, 0x20000\n"
"s_cmp_ge_u32 s42, 224\n"
"s_cselect_b32 s38, s38, 0\n"
"s_lshr_b32 s52, s42, 3\n"
"s_lshl_b32 s52, s52, 2\n"
"s_add_u32 s52, s52, s41\n"
"s_and_b32 s52, s52, 15\n"
"s_lshl_b32 s52, s52, 12\n"
"v_lshlrev_b32 v214, 6, v248\n"
"buffer_load_dword v250, v214, s[36:39], s52 offen\n"
"s_add_u32 s52, s52, 65536\n"
"buffer_load_dword v250, v214, s[36:39], s52 offen\n"
"s_add_u32 m0, s51, 114744\n"
"s_nop 0\n"
"buffer_load_dwordx4 v217, s[20:23], s50 offen offset:0 lds\n"
"s_add_u32 m0, s51, 122680\n"
"s_nop 0\n"
"buffer_load_dwordx4 v217, s[20:23], s50 offen offset:256 lds\n"
"s_add_u32 s50, s50, 32768\n"
"s_add_u32 m0, s51, 115768\n"
"s_nop 0\n"
"buffer_load_dwordx4 v217, s[20:23], s50 offen offset:0 lds\n"
"s_add_u32 m0, s51, 123704\n"
"s_nop 0\n"
"buffer_load_dwordx4 v217, s[20:23], s50 offen offset:256 lds\n"
"s_add_u32 s50, s50, 425984\n"
"s_mov_b32 m0, s51\n"
"s_nop 0\n"
"buffer_load_dwordx4 v217, s[20:23], s50 offen offset:0 lds\n"
"s_add_u32 m0, s51, 7936\n"
"s_nop 0\n"
"buffer_load_dwordx4 v217, s[20:23], s50 offen offset:256 lds\n"
"s_add_u32 s50, s50, 32768\n"
"s_add_u32 m0, s51, 1024\n"
"s_nop 0\n"
"buffer_load_dwordx4 v217, s[20:23], s50 offen offset:0 lds\n"
"s_add_u32 m0, s51, 8960\n"
"s_nop 0\n"
"buffer_load_dwordx4 v217, s[20:23], s50 offen offset:256 lds\n"
"s_add_u32 s50, s50, 32768\n"
"s_add_u32 m0, s51, 16392\n"
"s_nop 0\n"
"buffer_load_dwordx4 v217, s[20:23], s50 offen offset:0 lds\n"
"s_add_u32 m0, s51, 24328\n"
"s_nop 0\n"
"buffer_load_dwordx4 v217, s[20:23], s50 offen offset:256 lds\n"
"s_add_u32 s50, s50, 32768\n"
"s_add_u32 m0, s51, 17416\n"
"s_nop 0\n"
"buffer_load_dwordx4 v217, s[20:23], s50 offen offset:0 lds\n"
"s_add_u32 m0, s51, 25352\n"
"s_nop 0\n"
"buffer_load_dwordx4 v217, s[20:23], s50 offen offset:256 lds\n"
"s_add_u32 s50, s50, 425984\n"
"s_mov_b64 exec, 0\n"
"s_cmp_eq_u32 s41, 0\n"
"s_cbranch_scc1 AT_hw0_0\n"
"s_cmp_eq_u32 s41, 1\n"
"s_cbranch_scc1 AT_hw0_1\n"
"s_cmp_eq_u32 s41, 2\n"
"s_cbranch_scc1 AT_hw0_2\n"
"s_call_b64 %[kp], AT_pg4\n"
"s_call_b64 %[kp], AT_pg8\n"
"s_call_b64 %[kp], AT_pg12\n"
"s_branch AT_hx0\n"
"AT_pg19:\n"
"AT_pg20:\n"
"AT_hr:\n"
"s_setpc_b64 %[kp]\n"
"AT_hw0_0:\n"
"s_call_b64 %[kp], AT_pg1\n"
"s_call_b64 %[kp], AT_pg5\n"
"s_call_b64 %[kp], AT_pg9\n"
"s_branch AT_hx0\n"
"AT_hw0_1:\n"
"s_call_b64 %[kp], AT_pg2\n"
"s_call_b64 %[kp], AT_pg6\n"
"s_call_b64 %[kp], AT_pg10\n"
"s_branch AT_hx0\n"
"AT_hw0_2:\n"
"s_call_b64 %[kp], AT_pg3\n"
"s_call_b64 %[kp], AT_pg7\n"
"s_call_b64 %[kp], AT_pg11\n"
"AT_hx0:\n"
"s_mov_b64 exec, -1\n"
"buffer_load_dword v214, v213, s[28:31], 0 offen sc1\n"
"buffer_load_dwordx4 v[144:147], v225, s[28:31], s61 offen offset:0 sc1\n"
"buffer_load_dwordx4 v[148:151], v225, s[28:31], s61 offen offset:16 sc1\n"
"buffer_load_dwordx4 v[152:155], v225, s[28:31], s62 offen offset:0 sc1\n"
"buffer_load_dwordx4 v[156:159], v225, s[28:31], s62 offen offset:16 sc1\n"
"s_mov_b32 s87, 0x80000\n"
"s_waitcnt vmcnt(4)\n"
"AT_xc:\n"
"v_cmp_ne_u32 vcc, s85, v214\n"
"s_sub_u32 s87, s87, 1\n"
"s_cbranch_vccz AT_xk\n"
"s_cmp_eq_u32 s87, 0\n"
"s_cbranch_scc1 AT_xk\n"
"s_sleep 1\n"
"buffer_load_dword v214, v213, s[28:31], 0 offen sc1\n"
"s_waitcnt vmcnt(0)\n"
"s_branch AT_xc\n"
"AT_xk:\n"
"buffer_load_dwordx4 v[0:3], v249, s[28:31], s2 offen offset:0 sc1\n"
"buffer_load_dwordx4 v[4:7], v255, s[28:31], s2 offen offset:0 sc1\n"
"buffer_load_dwordx4 v[8:11], v249, s[28:31], s2 offen offset:1024 sc1\n"
"buffer_load_dwordx4 v[12:15], v255, s[28:31], s2 offen offset:1024 sc1\n"
"buffer_load_dwordx4 v[16:19], v249, s[28:31], s2 offen offset:2048 sc1\n"
"buffer_load_dwordx4 v[20:23], v255, s[28:31], s2 offen offset:2048 sc1\n"
"buffer_load_dwordx4 v[24:27], v249, s[28:31], s2 offen offset:3072 sc1\n"
"buffer_load_dwordx4 v[28:31], v255, s[28:31], s2 offen offset:3072 sc1\n"
"s_add_u32 s2, s2, 4096\n"
"buffer_load_dwordx4 v[32:35], v249, s[28:31], s2 offen offset:0 sc1\n"
"buffer_load_dwordx4 v[36:39], v255, s[28:31], s2 offen offset:0 sc1\n"
"s_mov_b32 s87, 0x80000\n"
"s_waitcnt vmcnt(10)\n"
"AT_gchk:\n"
"v_xor_b32 v192, s85, v145\n"
"v_xor_b32 v193, s85, v147\n"
"v_bitop3_b32 v192, v149, s85, v192 bitop3:0xbe\n"
"v_bitop3_b32 v193, v151, s85, v193 bitop3:0xbe\n"
"v_bitop3_b32 v192, v153, s85, v192 bitop3:0xbe\n"
"v_bitop3_b32 v193, v155, s85, v193 bitop3:0xbe\n"
"v_bitop3_b32 v192, v157, s85, v192 bitop3:0xbe\n"
"v_bitop3_b32 v193, v159, s85, v193 bitop3:0xbe\n"
"v_or_b32 v192, v192, v193\n"
"v_cmp_ne_u32 s[56:57], 0, v192\n"
"v_cmp_gt_u32 vcc, 144, v234\n"
"s_nop 1\n"
"s_and_b64 vcc, vcc, s[56:57]\n"
"s_cbranch_vccz AT_gok\n"
"s_sub_u32 s87, s87, 1\n"
"s_cmp_eq_u32 s87, 0\n"
"s_cbranch_scc1 AT_gok\n"
"s_sleep 2\n"
"buffer_load_dwordx4 v[144:147], v225, s[28:31], s61 offen offset:0 sc1\n"
"buffer_load_dwordx4 v[148:151], v225, s[28:31], s61 offen offset:16 sc1\n"
"buffer_load_dwordx4 v[152:155], v225, s[28:31], s62 offen offset:0 sc1\n"
"buffer_load_dwordx4 v[156:159], v225, s[28:31], s62 offen offset:16 sc1\n"
"s_waitcnt vmcnt(0)\n"
"s_branch AT_gchk\n"
"AT_gok:\n"
"v_mov_b32 v236, 0x3e0293ee\n"
"v_mul_f32 v192, v236, v144\n"
"v_fma_f32 v196, v152, v236, v236\n"
"v_mul_f32 v193, v236, v146\n"
"v_fma_f32 v197, v154, v236, v236\n"
"v_mul_f32 v194, v236, v148\n"
"v_fma_f32 v198, v156, v236, v236\n"
"v_mul_f32 v195, v236, v150\n"
"v_fma_f32 v199, v158, v236, v236\n"
"ds_write_b128 v216, v[196:199]\n"
"ds_write_b128 v216, v[192:195] offset:128\n"
"s_add_u32 m0, s51, 32784\n"
"s_nop 0\n"
"buffer_load_dwordx4 v217, s[20:23], s50 offen offset:0 lds\n"
"s_add_u32 m0, s51, 40720\n"
"s_nop 0\n"
"buffer_load_dwordx4 v217, s[20:23], s50 offen offset:256 lds\n"
"s_add_u32 s50, s50, 32768\n"
"s_add_u32 m0, s51, 33808\n"
"s_nop 0\n"
"buffer_load_dwordx4 v217, s[20:23], s50 offen offset:0 lds\n"
"s_add_u32 m0, s51, 41744\n"
"s_nop 0\n"
"buffer_load_dwordx4 v217, s[20:23], s50 offen offset:256 lds\n"
"s_add_u32 s50, s50, 32768\n"
"s_add_u32 m0, s51, 49176\n"
"s_nop 0\n"
"buffer_load_dwordx4 v217, s[20:23], s50 offen offset:0 lds\n"
"s_add_u32 m0, s51, 57112\n"
"s_nop 0\n"
"buffer_load_dwordx4 v217, s[20:23], s50 offen offset:256 lds\n"
"s_add_u32 s50, s50, 32768\n"
"s_add_u32 m0, s51, 50200\n"
"s_nop 0\n"
"buffer_load_dwordx4 v217, s[20:23], s50 offen offset:0 lds\n"
"s_add_u32 m0, s51, 58136\n"
"s_nop 0\n"
"buffer_load_dwordx4 v217, s[20:23], s50 offen offset:256 lds\n"
"s_add_u32 s50, s50, 425984\n"
"s_waitcnt vmcnt(31) lgkmcnt(0)\n"
"s_barrier\n"
"ds_read_b128 v[144:147], v224 offset:0\n"
"ds_read_b128 v[148:151], v224 offset:64\n"
"ds_read_b128 v[152:155], v224 offset:128\n"
"ds_read_b128 v[156:159], v224 offset:192\n"
"ds_read_b128 v[160:163], v224 offset:256\n"
"ds_read_b128 v[164:167], v224 offset:320\n"
"ds_read_b128 v[168:171], v224 offset:384\n"
"AT_pg1:\n"
"s_cbranch_execz AT_hr\n"
"ds_read_b128 v[172:175], v224 offset:448\n"
"s_waitcnt vmcnt(14)\n"
"ds_read_b64_tr_b16 a[160:161], v219 offset:32784\n"
"s_waitcnt lgkmcnt(6)\n"
"v_fma_mix_f32 v176, v0, v144, v152 op_sel:[0,0,0] op_sel_hi:[1,0,0]\n"
"v_fma_mix_f32 v177, v0, v145, v153 op_sel:[1,0,0] op_sel_hi:[1,0,0]\n"
"v_fma_mix_f32 v178, v1, v146, v154 op_sel:[0,0,0] op_sel_hi:[1,0,0]\n"
"ds_read_b64_tr_b16 a[162:163], v219 offset:33808\n"
"v_fma_mix_f32 v179, v1, v147, v155 op_sel:[1,0,0] op_sel_hi:[1,0,0]\n"
"s_waitcnt lgkmcnt(6)\n"
"v_fma_mix_f32 v180, v2, v148, v156 op_sel:[0,0,0] op_sel_hi:[1,0,0]\n"
"v_fma_mix_f32 v181, v2, v149, v157 op_sel:[1,0,0] op_sel_hi:[1,0,0]\n"
"ds_read_b64_tr_b16 a[164:165], v219 offset:36880\n"
"v_fma_mix_f32 v182, v3, v150, v158 op_sel:[0,0,0] op_sel_hi:[1,0,0]\n"
"v_fma_mix_f32 v183, v3, v151, v159 op_sel:[1,0,0] op_sel_hi:[1,0,0]\n"
"v_cvt_pk_bf16_f32 v0, v176, v177\n"
"ds_read_b64_tr_b16 a[166:167], v219 offset:37904\n"
"v_cvt_pk_bf16_f32 v1, v178, v179\n"
"v_cvt_pk_bf16_f32 v2, v180, v181\n"
"v_cvt_pk_bf16_f32 v3, v182, v183\n"
"ds_read_b64_tr_b16 a[168:169], v219 offset:32792\n"
"v_fma_mix_f32 v184, v4, v144, v152 op_sel:[0,0,0] op_sel_hi:[1,0,0]\n"
"v_fma_mix_f32 v185, v4, v145, v153 op_sel:[1,0,0] op_sel_hi:[1,0,0]\n"
"v_fma_mix_f32 v186, v5, v146, v154 op_sel:[0,0,0] op_sel_hi:[1,0,0]\n"
"ds_read_b64_tr_b16 a[170:171], v219 offset:33816\n"
"v_fma_mix_f32 v187, v5, v147, v155 op_sel:[1,0,0] op_sel_hi:[1,0,0]\n"
"v_fma_mix_f32 v188, v6, v148, v156 op_sel:[0,0,0] op_sel_hi:[1,0,0]\n"
"v_fma_mix_f32 v189, v6, v149, v157 op_sel:[1,0,0] op_sel_hi:[1,0,0]\n"
"ds_read_b64_tr_b16 a[172:173], v219 offset:36888\n"
"v_fma_mix_f32 v190, v7, v150, v158 op_sel:[0,0,0] op_sel_hi:[1,0,0]\n"
"v_fma_mix_f32 v191, v7, v151, v159 op_sel:[1,0,0] op_sel_hi:[1,0,0]\n"
"v_cvt_pk_bf16_f32 v4, v184, v185\n"
"ds_read_b64_tr_b16 a[174:175], v219 offset:37912\n"
"ds_read_b128 v[144:147], v224 offset:512\n"
"v_cvt_pk_bf16_f32 v5, v186, v187\n"
"v_cvt_pk_bf16_f32 v6, v188, v189\n"
"ds_read_b64_tr_b16 a[176:177], v219 offset:33040\n"
"v_cvt_pk_bf16_f32 v7, v190, v191\n"
"s_waitcnt lgkmcnt(11)\n"
"v_fma_mix_f32 v176, v8, v160, v168 op_sel:[0,0,0] op_sel_hi:[1,0,0]\n"
"v_fma_mix_f32 v177, v8, v161, v169 op_sel:[1,0,0] op_sel_hi:[1,0,0]\n"
"ds_read_b64_tr_b16 a[178:179], v219 offset:34064\n"
"v_fma_mix_f32 v178, v9, v162, v170 op_sel:[0,0,0] op_sel_hi:[1,0,0]\n"
"v_fma_mix_f32 v179, v9, v163, v171 op_sel:[1,0,0] op_sel_hi:[1,0,0]\n"
"s_waitcnt lgkmcnt(11)\n"
"v_fma_mix_f32 v180, v10, v164, v172 op_sel:[0,0,0] op_sel_hi:[1,0,0]\n"
"ds_read_b64_tr_b16 a[180:181], v219 offset:37136\n"
"ds_read_b128 v[148:151], v224 offset:576\n"
"v_fma_mix_f32 v181, v10, v165, v173 op_sel:[1,0,0] op_sel_hi:[1,0,0]\n"
"v_fma_mix_f32 v182, v11, v166, v174 op_sel:[0,0,0] op_sel_hi:[1,0,0]\n"
"ds_read_b64_tr_b16 a[182:183], v219 offset:38160\n"
"v_fma_mix_f32 v183, v11, v167, v175 op_sel:[1,0,0] op_sel_hi:[1,0,0]\n"
"v_cvt_pk_bf16_f32 v8, v176, v177\n"
"v_cvt_pk_bf16_f32 v9, v178, v179\n"
"ds_read_b64_tr_b16 a[184:185], v219 offset:33048\n"
"v_cvt_pk_bf16_f32 v10, v180, v181\n"
"v_cvt_pk_bf16_f32 v11, v182, v183\n"
"v_fma_mix_f32 v184, v12, v160, v168 op_sel:[0,0,0] op_sel_hi:[1,0,0]\n"
"ds_read_b64_tr_b16 a[186:187], v219 offset:34072\n"
"ds_read_b128 v[152:155], v224 offset:640\n"
"v_fma_mix_f32 v185, v12, v161, v169 op_sel:[1,0,0] op_sel_hi:[1,0,0]\n"
"v_fma_mix_f32 v186, v13, v162, v170 op_sel:[0,0,0] op_sel_hi:[1,0,0]\n"
"ds_read_b64_tr_b16 a[188:189], v219 offset:37144\n"
"v_fma_mix_f32 v187, v13, v163, v171 op_sel:[1,0,0] op_sel_hi:[1,0,0]\n"
"v_fma_mix_f32 v188, v14, v164, v172 op_sel:[0,0,0] op_sel_hi:[1,0,0]\n"
"v_fma_mix_f32 v189, v14, v165, v173 op_sel:[1,0,0] op_sel_hi:[1,0,0]\n"
"ds_read_b64_tr_b16 a[190:191], v219 offset:38168\n"
"v_fma_mix_f32 v190, v15, v166, v174 op_sel:[0,0,0] op_sel_hi:[1,0,0]\n"
"v_fma_mix_f32 v191, v15, v167, v175 op_sel:[1,0,0] op_sel_hi:[1,0,0]\n"
"v_cvt_pk_bf16_f32 v12, v184, v185\n"
"ds_read_b64_tr_b16 a[224:225], v219 offset:33296\n"
"ds_read_b128 v[156:159], v224 offset:704\n"
"ds_read_b128 v[160:163], v224 offset:768\n"
"v_cvt_pk_bf16_f32 v13, v186, v187\n"
"ds_read_b64_tr_b16 a[226:227], v219 offset:34320\n"
"v_cvt_pk_bf16_f32 v14, v188, v189\n"
"v_cvt_pk_bf16_f32 v15, v190, v191\n"
"s_waitcnt vmcnt(10)\n"
"ds_read_b64_tr_b16 a[228:229], v219 offset:37392\n"
"s_waitcnt lgkmcnt(7)\n"
"v_fma_mix_f32 v176, v16, v144, v152 op_sel:[0,0,0] op_sel_hi:[1,0,0]\n"
"v_fma_mix_f32 v177, v16, v145, v153 op_sel:[1,0,0] op_sel_hi:[1,0,0]\n"
"v_fma_mix_f32 v178, v17, v146, v154 op_sel:[0,0,0] op_sel_hi:[1,0,0]\n"
"ds_read_b64_tr_b16 a[230:231], v219 offset:38416\n"
"ds_read_b128 v[164:167], v224 offset:832\n"
"v_fma_mix_f32 v179, v17, v147, v155 op_sel:[1,0,0] op_sel_hi:[1,0,0]\n"
"s_waitcnt lgkmcnt(5)\n"
"v_fma_mix_f32 v180, v18, v148, v156 op_sel:[0,0,0] op_sel_hi:[1,0,0]\n"
"v_mfma_f32_16x16x32_bf16 a[0:3], a[160:163], v[0:3], 0\n"
"ds_read_b64_tr_b16 a[232:233], v219 offset:33304\n"
"v_fma_mix_f32 v181, v18, v149, v157 op_sel:[1,0,0] op_sel_hi:[1,0,0]\n"
"v_mfma_f32_16x16x32_bf16 a[0:3], a[164:167], v[8:11], a[0:3]\n"
"s_waitcnt vmcnt(23)\n"
"ds_read_b64_tr_b16 a[234:235], v219 offset:34328\n"
"v_fma_mix_f32 v182, v19, v150, v158 op_sel:[0,0,0] op_sel_hi:[1,0,0]\n"
"v_mfma_f32_16x16x32_bf16 a[4:7], a[160:163], v[4:7], 0\n"
"s_barrier\n"
"ds_read_b64_tr_b16 a[236:237], v219 offset:37400\n"
"buffer_load_dwordx4 v[40:43], v249, s[28:31], s2 offen offset:1024 sc1\n"
"v_mfma_f32_16x16x32_bf16 a[4:7], a[164:167], v[12:15], a[4:7]\n"
"ds_read_b64_tr_b16 a[238:239], v219 offset:38424\n"
"ds_read_b128 v[168:171], v224 offset:896\n"
"v_mfma_f32_16x16x32_bf16 a[8:11], a[168:171], v[0:3], 0\n"
"s_add_u32 m0, s51, 65568\n"
"ds_read_b64_tr_b16 a[240:241], v219 offset:33552\n"
"v_fma_mix_f32 v183, v19, v151, v159 op_sel:[1,0,0] op_sel_hi:[1,0,0]\n"
"v_mfma_f32_16x16x32_bf16 a[8:11], a[172:175], v[8:11], a[8:11]\n"
"ds_read_b64_tr_b16 a[242:243], v219 offset:34576\n"
"buffer_load_dwordx4 v217, s[20:23], s50 offen offset:0 lds\n"
"v_mfma_f32_16x16x32_bf16 a[12:15], a[168:171], v[4:7], 0\n"
"ds_read_b64_tr_b16 a[244:245], v219 offset:37648\n"
"ds_read_b128 v[172:175], v224 offset:960\n"
"v_cvt_pk_bf16_f32 v16, v176, v177\n"
"v_mfma_f32_16x16x32_bf16 a[12:15], a[172:175], v[12:15], a[12:15]\n"
"ds_read_b64_tr_b16 a[246:247], v219 offset:38672\n"
"v_cvt_pk_bf16_f32 v17, v178, v179\n"
"v_mfma_f32_16x16x32_bf16 a[16:19], a[176:179], v[0:3], 0\n"
"ds_read_b64_tr_b16 a[248:249], v219 offset:33560\n"
"v_cvt_pk_bf16_f32 v18, v180, v181\n"
"v_mfma_f32_16x16x32_bf16 a[16:19], a[180:183], v[8:11], a[16:19]\n"
"ds_read_b64_tr_b16 a[250:251], v219 offset:34584\n"
"v_cvt_pk_bf16_f32 v19, v182, v183\n"
"v_mfma_f32_16x16x32_bf16 a[20:23], a[176:179], v[4:7], 0\n"
"s_add_u32 m0, s51, 73504\n"
"ds_read_b64_tr_b16 a[252:253], v219 offset:37656\n"
"v_fma_mix_f32 v184, v20, v144, v152 op_sel:[0,0,0] op_sel_hi:[1,0,0]\n"
"v_mfma_f32_16x16x32_bf16 a[20:23], a[180:183], v[12:15], a[20:23]\n"
"ds_read_b64_tr_b16 a[254:255], v219 offset:38680\n"
"buffer_load_dwordx4 v217, s[20:23], s50 offen offset:256 lds\n"
"v_mfma_f32_16x16x32_bf16 a[24:27], a[184:187], v[0:3], 0\n"
"s_add_u32 s50, s50, 32768\n"
"ds_read_b64_tr_b16 a[160:161], v219 offset:40976\n"
"v_fma_mix_f32 v185, v20, v145, v153 op_sel:[1,0,0] op_sel_hi:[1,0,0]\n"
"v_mfma_f32_16x16x32_bf16 a[24:27], a[188:191], v[8:11], a[24:27]\n"
"ds_read_b64_tr_b16 a[162:163], v219 offset:42000\n"
"v_fma_mix_f32 v186, v21, v146, v154 op_sel:[0,0,0] op_sel_hi:[1,0,0]\n"
"v_mfma_f32_16x16x32_bf16 a[28:31], a[184:187], v[4:7], 0\n"
"ds_read_b64_tr_b16 a[164:165], v219 offset:45072\n"
"v_fma_mix_f32 v187, v21, v147, v155 op_sel:[1,0,0] op_sel_hi:[1,0,0]\n"
"v_mfma_f32_16x16x32_bf16 a[28:31], a[188:191], v[12:15], a[28:31]\n"
"s_waitcnt lgkmcnt(12)\n"
"ds_read_b64_tr_b16 a[166:167], v219 offset:46096\n"
"v_fma_mix_f32 v188, v22, v148, v156 op_sel:[0,0,0] op_sel_hi:[1,0,0]\n"
"v_mfma_f32_16x16x32_bf16 a[32:35], a[224:227], v[0:3], 0\n"
"s_add_u32 m0, s51, 66592\n"
"ds_read_b64_tr_b16 a[168:169], v219 offset:40984\n"
"v_fma_mix_f32 v189, v22, v149, v157 op_sel:[1,0,0] op_sel_hi:[1,0,0]\n"
"v_mfma_f32_16x16x32_bf16 a[32:35], a[228:231], v[8:11], a[32:35]\n"
"ds_read_b64_tr_b16 a[170:171], v219 offset:42008\n"
"buffer_load_dwordx4 v217, s[20:23], s50 offen offset:0 lds\n"
"v_mfma_f32_16x16x32_bf16 a[36:39], a[224:227], v[4:7], 0\n"
"ds_read_b64_tr_b16 a[172:173], v219 offset:45080\n"
"v_fma_mix_f32 v190, v23, v150, v158 op_sel:[0,0,0] op_sel_hi:[1,0,0]\n"
"v_mfma_f32_16x16x32_bf16 a[36:39], a[228:231], v[12:15], a[36:39]\n"
"ds_read_b64_tr_b16 a[174:175], v219 offset:46104\n"
"buffer_load_dwordx4 v[44:47], v255, s[28:31], s2 offen offset:1024 sc1\n"
"v_fma_mix_f32 v191, v23, v151, v159 op_sel:[1,0,0] op_sel_hi:[1,0,0]\n"
"v_mfma_f32_16x16x32_bf16 a[40:43], a[232:235], v[0:3], 0\n"
"ds_read_b64_tr_b16 a[176:177], v219 offset:41232\n"
"ds_read_b128 v[144:147], v224 offset:1024\n"
"v_mfma_f32_16x16x32_bf16 a[40:43], a[236:239], v[8:11], a[40:43]\n"
"ds_read_b64_tr_b16 a[178:179], v219 offset:42256\n"
"v_cvt_pk_bf16_f32 v20, v184, v185\n"
"v_mfma_f32_16x16x32_bf16 a[44:47], a[232:235], v[4:7], 0\n"
"s_add_u32 m0, s51, 74528\n"
"ds_read_b64_tr_b16 a[180:181], v219 offset:45328\n"
"v_cvt_pk_bf16_f32 v21, v186, v187\n"
"v_mfma_f32_16x16x32_bf16 a[44:47], a[236:239], v[12:15], a[44:47]\n"
"s_waitcnt lgkmcnt(12)\n"
"ds_read_b64_tr_b16 a[182:183], v219 offset:46352\n"
"buffer_load_dwordx4 v217, s[20:23], s50 offen offset:256 lds\n"
"v_mfma_f32_16x16x32_bf16 a[48:51], a[240:243], v[0:3], 0\n"
"s_add_u32 s50, s50, 32768\n"
"ds_read_b64_tr_b16 a[184:185], v219 offset:41240\n"
"ds_read_b128 v[148:151], v224 offset:1088\n"
"v_mfma_f32_16x16x32_bf16 a[48:51], a[244:247], v[8:11], a[48:51]\n"
"ds_read_b64_tr_b16 a[186:187], v219 offset:42264\n"
"v_cvt_pk_bf16_f32 v22, v188, v189\n"
"v_mfma_f32_16x16x32_bf16 a[52:55], a[240:243], v[4:7], 0\n"
"ds_read_b64_tr_b16 a[188:189], v219 offset:45336\n"
"v_cvt_pk_bf16_f32 v23, v190, v191\n"
"v_mfma_f32_16x16x32_bf16 a[52:55], a[244:247], v[12:15], a[52:55]\n"
"ds_read_b64_tr_b16 a[190:191], v219 offset:46360\n"
"ds_read_b128 v[152:155], v224 offset:1152\n"
"v_mfma_f32_16x16x32_bf16 a[56:59], a[248:251], v[0:3], 0\n"
"s_add_u32 m0, s51, 81960\n"
"ds_read_b64_tr_b16 a[224:225], v219 offset:41488\n"
"v_fma_mix_f32 v176, v24, v160, v168 op_sel:[0,0,0] op_sel_hi:[1,0,0]\n"
"v_mfma_f32_16x16x32_bf16 a[56:59], a[252:255], v[8:11], a[56:59]\n"
"ds_read_b64_tr_b16 a[226:227], v219 offset:42512\n"
"buffer_load_dwordx4 v217, s[20:23], s50 offen offset:0 lds\n"
"v_mfma_f32_16x16x32_bf16 a[60:63], a[248:251], v[4:7], 0\n"
"ds_read_b64_tr_b16 a[228:229], v219 offset:45584\n"
"ds_read_b128 v[156:159], v224 offset:1216\n"
"v_mfma_f32_16x16x32_bf16 a[60:63], a[252:255], v[12:15], a[60:63]\n"
"s_waitcnt lgkmcnt(14)\n"
"ds_read_b64_tr_b16 a[230:231], v219 offset:46608\n"
"v_fma_mix_f32 v177, v24, v161, v169 op_sel:[1,0,0] op_sel_hi:[1,0,0]\n"
"v_mfma_f32_16x16x32_bf16 a[64:67], a[160:163], v[0:3], 0\n"
"ds_read_b64_tr_b16 a[232:233], v219 offset:41496\n"
"buffer_load_dwordx4 v[48:51], v249, s[28:31], s2 offen offset:2048 sc1\n"
"v_fma_mix_f32 v178, v25, v162, v170 op_sel:[0,0,0] op_sel_hi:[1,0,0]\n"
"v_mfma_f32_16x16x32_bf16 a[64:67], a[164:167], v[8:11], a[64:67]\n"
"ds_read_b64_tr_b16 a[234:235], v219 offset:42520\n"
"v_fma_mix_f32 v179, v25, v163, v171 op_sel:[1,0,0] op_sel_hi:[1,0,0]\n"
"v_mfma_f32_16x16x32_bf16 a[68:71], a[160:163], v[4:7], 0\n"
"s_add_u32 m0, s51, 89896\n"
"ds_read_b64_tr_b16 a[236:237], v219 offset:45592\n"
"v_fma_mix_f32 v180, v26, v164, v172 op_sel:[0,0,0] op_sel_hi:[1,0,0]\n"
"v_mfma_f32_16x16x32_bf16 a[68:71], a[164:167], v[12:15], a[68:71]\n"
"ds_read_b64_tr_b16 a[238:239], v219 offset:46616\n"
"buffer_load_dwordx4 v217, s[20:23], s50 offen offset:256 lds\n"
"v_mfma_f32_16x16x32_bf16 a[72:75], a[168:171], v[0:3], 0\n"
"s_add_u32 s50, s50, 32768\n"
"ds_read_b64_tr_b16 a[240:241], v219 offset:41744\n"
"v_fma_mix_f32 v181, v26, v165, v173 op_sel:[1,0,0] op_sel_hi:[1,0,0]\n"
"v_mfma_f32_16x16x32_bf16 a[72:75], a[172:175], v[8:11], a[72:75]\n"
"ds_read_b64_tr_b16 a[242:243], v219 offset:42768\n"
"v_fma_mix_f32 v182, v27, v166, v174 op_sel:[0,0,0] op_sel_hi:[1,0,0]\n"
"v_mfma_f32_16x16x32_bf16 a[76:79], a[168:171], v[4:7], 0\n"
"ds_read_b64_tr_b16 a[244:245], v219 offset:45840\n"
"v_fma_mix_f32 v183, v27, v167, v175 op_sel:[1,0,0] op_sel_hi:[1,0,0]\n"
"v_mfma_f32_16x16x32_bf16 a[76:79], a[172:175], v[12:15], a[76:79]\n"
"s_waitcnt lgkmcnt(12)\n"
"ds_read_b64_tr_b16 a[246:247], v219 offset:46864\n"
"v_cvt_pk_bf16_f32 v24, v176, v177\n"
"v_mfma_f32_16x16x32_bf16 a[80:83], a[176:179], v[0:3], 0\n"
"s_add_u32 m0, s51, 82984\n"
"ds_read_b64_tr_b16 a[248:249], v219 offset:41752\n"
"v_cvt_pk_bf16_f32 v25, v178, v179\n"
"v_mfma_f32_16x16x32_bf16 a[80:83], a[180:183], v[8:11], a[80:83]\n"
"ds_read_b64_tr_b16 a[250:251], v219 offset:42776\n"
"buffer_load_dwordx4 v217, s[20:23], s50 offen offset:0 lds\n"
"v_mfma_f32_16x16x32_bf16 a[84:87], a[176:179], v[4:7], 0\n"
"ds_read_b64_tr_b16 a[252:253], v219 offset:45848\n"
"v_cvt_pk_bf16_f32 v26, v180, v181\n"
"v_mfma_f32_16x16x32_bf16 a[84:87], a[180:183], v[12:15], a[84:87]\n"
"ds_read_b64_tr_b16 a[254:255], v219 offset:46872\n"
"v_cvt_pk_bf16_f32 v27, v182, v183\n"
"v_mfma_f32_16x16x32_bf16 v[236:239], a[184:187], v[0:3], 0\n"
"ds_read_b64_tr_b16 a[160:161], v218 offset:0\n"
"v_fma_mix_f32 v184, v28, v160, v168 op_sel:[0,0,0] op_sel_hi:[1,0,0]\n"
"v_mfma_f32_16x16x32_bf16 v[236:239], a[188:191], v[8:11], v[236:239]\n"
"ds_read_b64_tr_b16 a[162:163], v218 offset:1024\n"
"v_fma_mix_f32 v185, v28, v161, v169 op_sel:[1,0,0] op_sel_hi:[1,0,0]\n"
"v_fma_mix_f32 v186, v29, v162, v170 op_sel:[0,0,0] op_sel_hi:[1,0,0]\n"
"v_mfma_f32_16x16x32_bf16 v[232:235], a[184:187], v[4:7], 0\n"
"s_add_u32 m0, s51, 90920\n"
"ds_read_b64_tr_b16 a[164:165], v218 offset:4096\n"
"v_fma_mix_f32 v187, v29, v163, v171 op_sel:[1,0,0] op_sel_hi:[1,0,0]\n"
"v_mfma_f32_16x16x32_bf16 v[232:235], a[188:191], v[12:15], v[232:235]\n"
"s_waitcnt lgkmcnt(11)\n"
"buffer_load_dwordx4 v217, s[20:23], s50 offen offset:256 lds\n"
"ds_read_b64_tr_b16 a[166:167], v218 offset:5120\n"
"v_mfma_f32_16x16x32_bf16 v[228:231], a[224:227], v[0:3], 0\n"
"s_add_u32 s50, s50, 425984\n"
"ds_read_b64_tr_b16 a[168:169], v218 offset:8\n"
"v_fma_mix_f32 v188, v30, v164, v172 op_sel:[0,0,0] op_sel_hi:[1,0,0]\n"
"v_mfma_f32_16x16x32_bf16 v[228:231], a[228:231], v[8:11], v[228:231]\n"
"ds_read_b64_tr_b16 a[170:171], v218 offset:1032\n"
"buffer_load_dwordx4 v[52:55], v255, s[28:31], s2 offen offset:2048 sc1\n"
"v_mfma_f32_16x16x32_bf16 v[220:223], a[224:227], v[4:7], 0\n"
"ds_read_b64_tr_b16 a[172:173], v218 offset:4104\n"
"v_fma_mix_f32 v189, v30, v165, v173 op_sel:[1,0,0] op_sel_hi:[1,0,0]\n"
"v_mfma_f32_16x16x32_bf16 v[220:223], a[228:231], v[12:15], v[220:223]\n"
"ds_read_b64_tr_b16 a[174:175], v218 offset:5128\n"
"v_fma_mix_f32 v190, v31, v166, v174 op_sel:[0,0,0] op_sel_hi:[1,0,0]\n"
"v_mfma_f32_16x16x32_bf16 v[212:215], a[232:235], v[0:3], 0\n"
"ds_read_b64_tr_b16 a[176:177], v218 offset:256\n"
"v_fma_mix_f32 v191, v31, v167, v175 op_sel:[1,0,0] op_sel_hi:[1,0,0]\n"
"v_mfma_f32_16x16x32_bf16 v[212:215], a[236:239], v[8:11], v[212:215]\n"
"ds_read_b64_tr_b16 a[178:179], v218 offset:1280\n"
"ds_read_b128 v[160:163], v224 offset:1280\n"
"v_mfma_f32_16x16x32_bf16 v[208:211], a[232:235], v[4:7], 0\n"
"ds_read_b64_tr_b16 a[180:181], v218 offset:4352\n"
"v_cvt_pk_bf16_f32 v28, v184, v185\n"
"v_mfma_f32_16x16x32_bf16 v[208:211], a[236:239], v[12:15], v[208:211]\n"
"s_waitcnt lgkmcnt(12)\n"
"ds_read_b64_tr_b16 a[182:183], v218 offset:5376\n"
"v_cvt_pk_bf16_f32 v29, v186, v187\n"
"v_mfma_f32_16x16x32_bf16 v[204:207], a[240:243], v[0:3], 0\n"
"ds_read_b64_tr_b16 a[184:185], v218 offset:264\n"
"ds_read_b128 v[164:167], v224 offset:1344\n"
"v_mfma_f32_16x16x32_bf16 v[204:207], a[244:247], v[8:11], v[204:207]\n"
"ds_read_b64_tr_b16 a[186:187], v218 offset:1288\n"
"v_cvt_pk_bf16_f32 v30, v188, v189\n"
"v_mfma_f32_16x16x32_bf16 v[200:203], a[240:243], v[4:7], 0\n"
"ds_read_b64_tr_b16 a[188:189], v218 offset:4360\n"
"v_cvt_pk_bf16_f32 v31, v190, v191\n"
"v_mfma_f32_16x16x32_bf16 v[200:203], a[244:247], v[12:15], v[200:203]\n"
"ds_read_b64_tr_b16 a[190:191], v218 offset:5384\n"
"ds_read_b128 v[168:171], v224 offset:1408\n"
"v_mfma_f32_16x16x32_bf16 v[196:199], a[248:251], v[0:3], 0\n"
"s_waitcnt vmcnt(7)\n"
"ds_read_b64_tr_b16 a[224:225], v218 offset:512\n"
"v_fma_mix_f32 v176, v32, v144, v152 op_sel:[0,0,0] op_sel_hi:[1,0,0]\n"
"v_mfma_f32_16x16x32_bf16 v[196:199], a[252:255], v[8:11], v[196:199]\n"
"ds_read_b64_tr_b16 a[226:227], v218 offset:1536\n"
"v_fma_mix_f32 v177, v32, v145, v153 op_sel:[1,0,0] op_sel_hi:[1,0,0]\n"
"v_mfma_f32_16x16x32_bf16 v[192:195], a[248:251], v[4:7], 0\n"
"ds_read_b64_tr_b16 a[228:229], v218 offset:4608\n"
"buffer_load_dwordx4 v[56:59], v249, s[28:31], s2 offen offset:3072 sc1\n"
"v_mfma_f32_16x16x32_bf16 v[192:195], a[252:255], v[12:15], v[192:195]\n"
"s_waitcnt lgkmcnt(14)\n"
"ds_read_b64_tr_b16 a[230:231], v218 offset:5632\n"
"ds_read_b128 v[172:175], v224 offset:1472\n"
"v_mfma_f32_16x16x32_bf16 a[0:3], a[160:163], v[16:19], a[0:3]\n"
"ds_read_b64_tr_b16 a[232:233], v218 offset:520\n"
"v_fma_mix_f32 v178, v33, v146, v154 op_sel:[0,0,0] op_sel_hi:[1,0,0]\n"
"v_mfma_f32_16x16x32_bf16 a[0:3], a[164:167], v[24:27], a[0:3]\n"
"s_waitcnt vmcnt(13)\n"
"ds_read_b64_tr_b16 a[234:235], v218 offset:1544\n"
"v_fma_mix_f32 v179, v33, v147, v155 op_sel:[1,0,0] op_sel_hi:[1,0,0]\n"
"v_mfma_f32_16x16x32_bf16 a[4:7], a[160:163], v[20:23], a[4:7]\n"
"s_barrier\n"
"ds_read_b64_tr_b16 a[236:237], v218 offset:4616\n"
"v_fma_mix_f32 v180, v34, v148, v156 op_sel:[0,0,0] op_sel_hi:[1,0,0]\n"
"v_mfma_f32_16x16x32_bf16 a[4:7], a[164:167], v[28:31], a[4:7]\n"
"ds_read_b64_tr_b16 a[238:239], v218 offset:5640\n"
"v_fma_mix_f32 v181, v34, v149, v157 op_sel:[1,0,0] op_sel_hi:[1,0,0]\n"
"v_mfma_f32_16x16x32_bf16 a[8:11], a[168:171], v[16:19], a[8:11]\n"
"s_add_u32 m0, s51, 98352\n"
"ds_read_b64_tr_b16 a[240:241], v218 offset:768\n"
"v_fma_mix_f32 v182, v35, v150, v158 op_sel:[0,0,0] op_sel_hi:[1,0,0]\n"
"v_mfma_f32_16x16x32_bf16 a[8:11], a[172:175], v[24:27], a[8:11]\n"
"ds_read_b64_tr_b16 a[242:243], v218 offset:1792\n"
"buffer_load_dwordx4 v217, s[20:23], s50 offen offset:0 lds\n"
"v_mfma_f32_16x16x32_bf16 a[12:15], a[168:171], v[20:23], a[12:15]\n"
"ds_read_b64_tr_b16 a[244:245], v218 offset:4864\n"
"v_fma_mix_f32 v183, v35, v151, v159 op_sel:[1,0,0] op_sel_hi:[1,0,0]\n"
"v_mfma_f32_16x16x32_bf16 a[12:15], a[172:175], v[28:31], a[12:15]\n"
"s_waitcnt lgkmcnt(12)\n"
"ds_read_b64_tr_b16 a[246:247], v218 offset:5888\n"
"v_cvt_pk_bf16_f32 v32, v176, v177\n"
"v_cvt_pk_bf16_f32 v33, v178, v179\n"
"v_mfma_f32_16x16x32_bf16 a[16:19], a[176:179], v[16:19], a[16:19]\n"
"ds_read_b64_tr_b16 a[248:249], v218 offset:776\n"
"v_cvt_pk_bf16_f32 v34, v180, v181\n"
"v_mfma_f32_16x16x32_bf16 a[16:19], a[180:183], v[24:27], a[16:19]\n"
"ds_read_b64_tr_b16 a[250:251], v218 offset:1800\n"
"v_cvt_pk_bf16_f32 v35, v182, v183\n"
"v_mfma_f32_16x16x32_bf16 a[20:23], a[176:179], v[20:23], a[20:23]\n"
"s_add_u32 m0, s51, 106288\n"
"ds_read_b64_tr_b16 a[252:253], v218 offset:4872\n"
"v_fma_mix_f32 v184, v36, v144, v152 op_sel:[0,0,0] op_sel_hi:[1,0,0]\n"
"v_mfma_f32_16x16x32_bf16 a[20:23], a[180:183], v[28:31], a[20:23]\n"
"ds_read_b64_tr_b16 a[254:255], v218 offset:5896\n"
"buffer_load_dwordx4 v217, s[20:23], s50 offen offset:256 lds\n"
"v_mfma_f32_16x16x32_bf16 a[24:27], a[184:187], v[16:19], a[24:27]\n"
"s_add_u32 s50, s50, 32768\n"
"ds_read_b64_tr_b16 a[160:161], v218 offset:8192\n"
"v_fma_mix_f32 v185, v36, v145, v153 op_sel:[1,0,0] op_sel_hi:[1,0,0]\n"
"v_mfma_f32_16x16x32_bf16 a[24:27], a[188:191], v[24:27], a[24:27]\n"
"ds_read_b64_tr_b16 a[162:163], v218 offset:9216\n"
"buffer_load_dwordx4 v[60:63], v255, s[28:31], s2 offen offset:3072 sc1\n"
"v_mfma_f32_16x16x32_bf16 a[28:31], a[184:187], v[20:23], a[28:31]\n"
"s_add_u32 s2, s2, 4096\n"
"ds_read_b64_tr_b16 a[164:165], v218 offset:12288\n"
"v_fma_mix_f32 v186, v37, v146, v154 op_sel:[0,0,0] op_sel_hi:[1,0,0]\n"
"v_mfma_f32_16x16x32_bf16 a[28:31], a[188:191], v[28:31], a[28:31]\n"
"s_waitcnt lgkmcnt(11)\n"
"ds_read_b64_tr_b16 a[166:167], v218 offset:13312\n"
"v_fma_mix_f32 v187, v37, v147, v155 op_sel:[1,0,0] op_sel_hi:[1,0,0]\n"
"v_mfma_f32_16x16x32_bf16 a[32:35], a[224:227], v[16:19], a[32:35]\n"
"s_add_u32 m0, s51, 99376\n"
"ds_read_b64_tr_b16 a[168:169], v218 offset:8200\n"
"v_fma_mix_f32 v188, v38, v148, v156 op_sel:[0,0,0] op_sel_hi:[1,0,0]\n"
"v_mfma_f32_16x16x32_bf16 a[32:35], a[228:231], v[24:27], a[32:35]\n"
"s_and_b32 s62, s42, 7\n"
"ds_read_b64_tr_b16 a[170:171], v218 offset:9224\n"
"buffer_load_dwordx4 v217, s[20:23], s50 offen offset:0 lds\n"
"v_mfma_f32_16x16x32_bf16 a[36:39], a[224:227], v[20:23], a[36:39]\n"
"ds_read_b64_tr_b16 a[172:173], v218 offset:12296\n"
"v_fma_mix_f32 v189, v38, v149, v157 op_sel:[1,0,0] op_sel_hi:[1,0,0]\n"
"v_mfma_f32_16x16x32_bf16 a[36:39], a[228:231], v[28:31], a[36:39]\n"
"s_mul_i32 s46, s62, 663552\n"
"ds_read_b64_tr_b16 a[174:175], v218 offset:13320\n"
"v_fma_mix_f32 v190, v39, v150, v158 op_sel:[0,0,0] op_sel_hi:[1,0,0]\n"
"v_mfma_f32_16x16x32_bf16 a[40:43], a[232:235], v[16:19], a[40:43]\n"
"ds_read_b64_tr_b16 a[176:177], v218 offset:8448\n"
"v_fma_mix_f32 v191, v39, v151, v159 op_sel:[1,0,0] op_sel_hi:[1,0,0]\n"
"v_cvt_pk_bf16_f32 v36, v184, v185\n"
"v_mfma_f32_16x16x32_bf16 a[40:43], a[236:239], v[24:27], a[40:43]\n"
"s_lshr_b32 s47, s43, 2\n"
"ds_read_b64_tr_b16 a[178:179], v218 offset:9472\n"
"ds_read_b128 v[144:147], v224 offset:1536\n"
"v_mfma_f32_16x16x32_bf16 a[44:47], a[232:235], v[20:23], a[44:47]\n"
"s_add_u32 m0, s51, 107312\n"
"ds_read_b64_tr_b16 a[180:181], v218 offset:12544\n"
"v_cvt_pk_bf16_f32 v37, v186, v187\n"
"v_mfma_f32_16x16x32_bf16 a[44:47], a[236:239], v[28:31], a[44:47]\n"
"s_waitcnt lgkmcnt(12)\n"
"s_mul_i32 s47, s47, 1179648\n"
"ds_read_b64_tr_b16 a[182:183], v218 offset:13568\n"
"buffer_load_dwordx4 v217, s[20:23], s50 offen offset:256 lds\n"
"v_mfma_f32_16x16x32_bf16 a[48:51], a[240:243], v[16:19], a[48:51]\n"
"s_add_u32 s50, s50, 32768\n"
"ds_read_b64_tr_b16 a[184:185], v218 offset:8456\n"
"ds_read_b128 v[148:151], v224 offset:1600\n"
"v_mfma_f32_16x16x32_bf16 a[48:51], a[244:247], v[24:27], a[48:51]\n"
"s_getpc_b64 s[32:33]\n"
"AT_pc1:\n"
"s_sub_u32 s32, s32, AT_pc1-AT_start\n"
"s_subb_u32 s33, s33, 0\n"
"ds_read_b64_tr_b16 a[186:187], v218 offset:9480\n"
"v_cvt_pk_bf16_f32 v38, v188, v189\n"
"v_mfma_f32_16x16x32_bf16 a[52:55], a[240:243], v[20:23], a[52:55]\n"
"ds_read_b64_tr_b16 a[188:189], v218 offset:12552\n"
"buffer_load_dwordx4 v[64:67], v249, s[28:31], s2 offen offset:0 sc1\n"
"v_mfma_f32_16x16x32_bf16 a[52:55], a[244:247], v[28:31], a[52:55]\n"
"s_and_b32 s33, s33, 0xffff\n"
"ds_read_b64_tr_b16 a[190:191], v218 offset:13576\n"
"ds_read_b128 v[152:155], v224 offset:1664\n"
"v_mfma_f32_16x16x32_bf16 a[56:59], a[248:251], v[16:19], a[56:59]\n"
"s_add_u32 m0, s51, 114744\n"
"ds_read_b64_tr_b16 a[224:225], v218 offset:8704\n"
"v_cvt_pk_bf16_f32 v39, v190, v191\n"
"v_mfma_f32_16x16x32_bf16 a[56:59], a[252:255], v[24:27], a[56:59]\n"
"s_mov_b32 s34, AT_kend-AT_start\n"
"ds_read_b64_tr_b16 a[226:227], v218 offset:9728\n"
"buffer_load_dwordx4 v217, s[20:23], s50 offen offset:0 lds\n"
"v_mfma_f32_16x16x32_bf16 a[60:63], a[248:251], v[20:23], a[60:63]\n"
"ds_read_b64_tr_b16 a[228:229], v218 offset:12800\n"
"ds_read_b128 v[156:159], v224 offset:1728\n"
"v_mfma_f32_16x16x32_bf16 a[60:63], a[252:255], v[28:31], a[60:63]\n"
"s_waitcnt lgkmcnt(14)\n"
"s_mov_b32 s35, 0x20000\n"
"ds_read_b64_tr_b16 a[230:231], v218 offset:13824\n"
"v_fma_mix_f32 v176, v40, v160, v168 op_sel:[0,0,0] op_sel_hi:[1,0,0]\n"
"v_mfma_f32_16x16x32_bf16 a[64:67], a[160:163], v[16:19], a[64:67]\n"
"ds_read_b64_tr_b16 a[232:233], v218 offset:8712\n"
"v_fma_mix_f32 v177, v40, v161, v169 op_sel:[1,0,0] op_sel_hi:[1,0,0]\n"
"v_mfma_f32_16x16x32_bf16 a[64:67], a[164:167], v[24:27], a[64:67]\n"
"s_lshr_b32 s62, s42, 3\n"
"ds_read_b64_tr_b16 a[234:235], v218 offset:9736\n"
"v_fma_mix_f32 v178, v41, v162, v170 op_sel:[0,0,0] op_sel_hi:[1,0,0]\n"
"v_fma_mix_f32 v179, v41, v163, v171 op_sel:[1,0,0] op_sel_hi:[1,0,0]\n"
"v_mfma_f32_16x16x32_bf16 a[68:71], a[160:163], v[20:23], a[68:71]\n"
"s_add_u32 m0, s51, 122680\n"
"ds_read_b64_tr_b16 a[236:237], v218 offset:12808\n"
"v_fma_mix_f32 v180, v42, v164, v172 op_sel:[0,0,0] op_sel_hi:[1,0,0]\n"
"v_mfma_f32_16x16x32_bf16 a[68:71], a[164:167], v[28:31], a[68:71]\n"
"s_lshl_b32 s62, s62, 2\n"
"ds_read_b64_tr_b16 a[238:239], v218 offset:13832\n"
"buffer_load_dwordx4 v217, s[20:23], s50 offen offset:256 lds\n"
"v_mfma_f32_16x16x32_bf16 a[72:75], a[168:171], v[16:19], a[72:75]\n"
"s_add_u32 s50, s50, 32768\n"
"ds_read_b64_tr_b16 a[240:241], v218 offset:8960\n"
"v_fma_mix_f32 v181, v42, v165, v173 op_sel:[1,0,0] op_sel_hi:[1,0,0]\n"
"v_mfma_f32_16x16x32_bf16 a[72:75], a[172:175], v[24:27], a[72:75]\n"
"s_add_u32 s62, s62, s41\n"
"ds_read_b64_tr_b16 a[242:243], v218 offset:9984\n"
"v_fma_mix_f32 v182, v43, v166, v174 op_sel:[0,0,0] op_sel_hi:[1,0,0]\n"
"v_mfma_f32_16x16x32_bf16 a[76:79], a[168:171], v[20:23], a[76:79]\n"
"ds_read_b64_tr_b16 a[244:245], v218 offset:13056\n"
"v_fma_mix_f32 v183, v43, v167, v175 op_sel:[1,0,0] op_sel_hi:[1,0,0]\n"
"v_mfma_f32_16x16x32_bf16 a[76:79], a[172:175], v[28:31], a[76:79]\n"
"s_waitcnt lgkmcnt(12)\n"
"s_and_b32 s62, s62, 31\n"
"ds_read_b64_tr_b16 a[246:247], v218 offset:14080\n"
"v_cvt_pk_bf16_f32 v40, v176, v177\n"
"v_mfma_f32_16x16x32_bf16 a[80:83], a[176:179], v[16:19], a[80:83]\n"
"s_add_u32 m0, s51, 115768\n"
"ds_read_b64_tr_b16 a[248:249], v218 offset:8968\n"
"v_cvt_pk_bf16_f32 v41, v178, v179\n"
"v_mfma_f32_16x16x32_bf16 a[80:83], a[180:183], v[24:27], a[80:83]\n"
"s_lshl_b32 s48, s62, 12\n"
"ds_read_b64_tr_b16 a[250:251], v218 offset:9992\n"
"buffer_load_dwordx4 v217, s[20:23], s50 offen offset:0 lds\n"
"v_mfma_f32_16x16x32_bf16 a[84:87], a[176:179], v[20:23], a[84:87]\n"
"ds_read_b64_tr_b16 a[252:253], v218 offset:13064\n"
"v_cvt_pk_bf16_f32 v42, v180, v181\n"
"v_mfma_f32_16x16x32_bf16 a[84:87], a[180:183], v[28:31], a[84:87]\n"
"s_lshl_b32 s54, s44, 2\n"
"ds_read_b64_tr_b16 a[254:255], v218 offset:14088\n"
"buffer_load_dwordx4 v[68:71], v255, s[28:31], s2 offen offset:0 sc1\n"
"v_mfma_f32_16x16x32_bf16 v[236:239], a[184:187], v[16:19], v[236:239]\n"
"ds_read_b64_tr_b16 a[160:161], v218 offset:32784\n"
"v_cvt_pk_bf16_f32 v43, v182, v183\n"
"v_mfma_f32_16x16x32_bf16 v[236:239], a[188:191], v[24:27], v[236:239]\n"
"s_add_u32 s54, s54, s41\n"
"ds_read_b64_tr_b16 a[162:163], v218 offset:33808\n"
"v_fma_mix_f32 v184, v44, v160, v168 op_sel:[0,0,0] op_sel_hi:[1,0,0]\n"
"v_mfma_f32_16x16x32_bf16 v[232:235], a[184:187], v[20:23], v[232:235]\n"
"s_add_u32 m0, s51, 123704\n"
"ds_read_b64_tr_b16 a[164:165], v218 offset:36880\n"
"v_fma_mix_f32 v185, v44, v161, v169 op_sel:[1,0,0] op_sel_hi:[1,0,0]\n"
"v_fma_mix_f32 v186, v45, v162, v170 op_sel:[0,0,0] op_sel_hi:[1,0,0]\n"
"v_mfma_f32_16x16x32_bf16 v[232:235], a[188:191], v[28:31], v[232:235]\n"
"s_waitcnt lgkmcnt(11)\n"
"s_mul_i32 s54, s54, 36864\n"
"buffer_load_dwordx4 v217, s[20:23], s50 offen offset:256 lds\n"
"ds_read_b64_tr_b16 a[166:167], v218 offset:37904\n"
"v_mfma_f32_16x16x32_bf16 v[228:231], a[224:227], v[16:19], v[228:231]\n"
"s_add_u32 s50, s50, 425984\n"
"ds_read_b64_tr_b16 a[168:169], v218 offset:32792\n"
"v_fma_mix_f32 v187, v45, v163, v171 op_sel:[1,0,0] op_sel_hi:[1,0,0]\n"
"v_mfma_f32_16x16x32_bf16 v[228:231], a[228:231], v[24:27], v[228:231]\n"
"s_lshr_b32 s62, s43, 2\n"
"ds_read_b64_tr_b16 a[170:171], v218 offset:33816\n"
"v_fma_mix_f32 v188, v46, v164, v172 op_sel:[0,0,0] op_sel_hi:[1,0,0]\n"
"v_mfma_f32_16x16x32_bf16 v[220:223], a[224:227], v[20:23], v[220:223]\n"
"ds_read_b64_tr_b16 a[172:173], v218 offset:36888\n"
"v_fma_mix_f32 v189, v46, v165, v173 op_sel:[1,0,0] op_sel_hi:[1,0,0]\n"
"v_mfma_f32_16x16x32_bf16 v[220:223], a[228:231], v[28:31], v[220:223]\n"
"s_mul_i32 s62, s62, 2359296\n"
"ds_read_b64_tr_b16 a[174:175], v218 offset:37912\n"
"v_fma_mix_f32 v190, v47, v166, v174 op_sel:[0,0,0] op_sel_hi:[1,0,0]\n"
"v_mfma_f32_16x16x32_bf16 v[212:215], a[232:235], v[16:19], v[212:215]\n"
"ds_read_b64_tr_b16 a[176:177], v218 offset:33040\n"
"v_fma_mix_f32 v191, v47, v167, v175 op_sel:[1,0,0] op_sel_hi:[1,0,0]\n"
"v_mfma_f32_16x16x32_bf16 v[212:215], a[236:239], v[24:27], v[212:215]\n"
"s_add_u32 s54, s54, s62\n"
"ds_read_b64_tr_b16 a[178:179], v218 offset:34064\n"
"ds_read_b128 v[160:163], v224 offset:1792\n"
"v_mfma_f32_16x16x32_bf16 v[208:211], a[232:235], v[20:23], v[208:211]\n"
"ds_read_b64_tr_b16 a[180:181], v218 offset:37136\n"
"v_cvt_pk_bf16_f32 v44, v184, v185\n"
"v_mfma_f32_16x16x32_bf16 v[208:211], a[236:239], v[28:31], v[208:211]\n"
"s_waitcnt lgkmcnt(12)\n"
"s_add_u32 s54, s54, 139264\n"
"ds_read_b64_tr_b16 a[182:183], v218 offset:38160\n"
"v_cvt_pk_bf16_f32 v45, v186, v187\n"
"v_mfma_f32_16x16x32_bf16 v[204:207], a[240:243], v[16:19], v[204:207]\n"
"ds_read_b64_tr_b16 a[184:185], v218 offset:33048\n"
"buffer_load_dwordx4 v[72:75], v249, s[28:31], s2 offen offset:1024 sc1\n"
"v_mfma_f32_16x16x32_bf16 v[204:207], a[244:247], v[24:27], v[204:207]\n"
"s_bfe_u32 s62, s43, 0x10001\n"
"ds_read_b64_tr_b16 a[186:187], v218 offset:34072\n"
"ds_read_b128 v[164:167], v224 offset:1856\n"
"v_mfma_f32_16x16x32_bf16 v[200:203], a[240:243], v[20:23], v[200:203]\n"
"ds_read_b64_tr_b16 a[188:189], v218 offset:37144\n"
"v_cvt_pk_bf16_f32 v46, v188, v189\n"
"v_mfma_f32_16x16x32_bf16 v[200:203], a[244:247], v[28:31], v[200:203]\n"
"s_mul_i32 s62, s62, 9216\n"
"ds_read_b64_tr_b16 a[190:191], v218 offset:38168\n"
"v_cvt_pk_bf16_f32 v47, v190, v191\n"
"v_mfma_f32_16x16x32_bf16 v[196:199], a[248:251], v[16:19], v[196:199]\n"
"AT_pg2:\n"
"s_cbranch_execz AT_hr\n"
"ds_read_b64_tr_b16 a[224:225], v218 offset:33296\n"
"ds_read_b128 v[168:171], v224 offset:1920\n"
"v_mfma_f32_16x16x32_bf16 v[196:199], a[252:255], v[24:27], v[196:199]\n"
"s_waitcnt vmcnt(9)\n"
"ds_read_b64_tr_b16 a[226:227], v218 offset:34320\n"
"v_fma_mix_f32 v176, v48, v144, v152 op_sel:[0,0,0] op_sel_hi:[1,0,0]\n"
"v_mfma_f32_16x16x32_bf16 v[192:195], a[248:251], v[20:23], v[192:195]\n"
"s_add_u32 s54, s54, s62\n"
"ds_read_b64_tr_b16 a[228:229], v218 offset:37392\n"
"v_fma_mix_f32 v177, v48, v145, v153 op_sel:[1,0,0] op_sel_hi:[1,0,0]\n"
"v_mfma_f32_16x16x32_bf16 v[192:195], a[252:255], v[28:31], v[192:195]\n"
"s_waitcnt lgkmcnt(14)\n"
"ds_read_b64_tr_b16 a[230:231], v218 offset:38416\n"
"ds_read_b128 v[172:175], v224 offset:1984\n"
"v_mfma_f32_16x16x32_bf16 a[0:3], a[160:163], v[32:35], a[0:3]\n"
"s_and_b32 s62, s43, 1\n"
"ds_read_b64_tr_b16 a[232:233], v218 offset:33304\n"
"v_fma_mix_f32 v178, v49, v146, v154 op_sel:[0,0,0] op_sel_hi:[1,0,0]\n"
"v_mfma_f32_16x16x32_bf16 a[0:3], a[164:167], v[40:43], a[0:3]\n"
"s_waitcnt vmcnt(14)\n"
"ds_read_b64_tr_b16 a[234:235], v218 offset:34328\n"
"v_fma_mix_f32 v179, v49, v147, v155 op_sel:[1,0,0] op_sel_hi:[1,0,0]\n"
"v_mfma_f32_16x16x32_bf16 a[4:7], a[160:163], v[36:39], a[4:7]\n"
"s_barrier\n"
"ds_read_b64_tr_b16 a[236:237], v218 offset:37400\n"
"v_fma_mix_f32 v180, v50, v148, v156 op_sel:[0,0,0] op_sel_hi:[1,0,0]\n"
"v_mfma_f32_16x16x32_bf16 a[4:7], a[164:167], v[44:47], a[4:7]\n"
"s_lshl_b32 s49, s62, 3\n"
"ds_read_b64_tr_b16 a[238:239], v218 offset:38424\n"
"v_fma_mix_f32 v181, v50, v149, v157 op_sel:[1,0,0] op_sel_hi:[1,0,0]\n"
"v_mfma_f32_16x16x32_bf16 a[8:11], a[168:171], v[32:35], a[8:11]\n"
"s_mov_b32 m0, s51\n"
"ds_read_b64_tr_b16 a[240:241], v218 offset:33552\n"
"v_fma_mix_f32 v182, v51, v150, v158 op_sel:[0,0,0] op_sel_hi:[1,0,0]\n"
"v_mfma_f32_16x16x32_bf16 a[8:11], a[172:175], v[40:43], a[8:11]\n"
"s_add_u32 s49, s49, s54\n"
"ds_read_b64_tr_b16 a[242:243], v218 offset:34576\n"
"buffer_load_dwordx4 v217, s[20:23], s50 offen offset:0 lds\n"
"v_mfma_f32_16x16x32_bf16 a[12:15], a[168:171], v[36:39], a[12:15]\n"
"ds_read_b64_tr_b16 a[244:245], v218 offset:37648\n"
"v_fma_mix_f32 v183, v51, v151, v159 op_sel:[1,0,0] op_sel_hi:[1,0,0]\n"
"v_mfma_f32_16x16x32_bf16 a[12:15], a[172:175], v[44:47], a[12:15]\n"
"s_waitcnt lgkmcnt(13)\n"
"s_add_u32 s49, s49, 4096\n"
"ds_read_b64_tr_b16 a[246:247], v218 offset:38672\n"
"buffer_load_dwordx4 v[76:79], v255, s[28:31], s2 offen offset:1024 sc1\n"
"v_mfma_f32_16x16x32_bf16 a[16:19], a[176:179], v[32:35], a[16:19]\n"
"ds_read_b64_tr_b16 a[248:249], v218 offset:33560\n"
"v_cvt_pk_bf16_f32 v48, v176, v177\n"
"v_cvt_pk_bf16_f32 v49, v178, v179\n"
"v_mfma_f32_16x16x32_bf16 a[16:19], a[180:183], v[40:43], a[16:19]\n"
"s_mul_i32 s62, s62, 5120\n"
"ds_read_b64_tr_b16 a[250:251], v218 offset:34584\n"
"v_cvt_pk_bf16_f32 v50, v180, v181\n"
"v_mfma_f32_16x16x32_bf16 a[20:23], a[176:179], v[36:39], a[20:23]\n"
"s_add_u32 m0, s51, 7936\n"
"ds_read_b64_tr_b16 a[252:253], v218 offset:37656\n"
"v_cvt_pk_bf16_f32 v51, v182, v183\n"
"v_mfma_f32_16x16x32_bf16 a[20:23], a[180:183], v[44:47], a[20:23]\n"
"s_add_u32 s54, s54, s62\n"
"ds_read_b64_tr_b16 a[254:255], v218 offset:38680\n"
"buffer_load_dwordx4 v217, s[20:23], s50 offen offset:256 lds\n"
"v_mfma_f32_16x16x32_bf16 a[24:27], a[184:187], v[32:35], a[24:27]\n"
"s_add_u32 s50, s50, 32768\n"
"ds_read_b64_tr_b16 a[160:161], v218 offset:40976\n"
"v_fma_mix_f32 v184, v52, v144, v152 op_sel:[0,0,0] op_sel_hi:[1,0,0]\n"
"v_mfma_f32_16x16x32_bf16 a[24:27], a[188:191], v[40:43], a[24:27]\n"
"s_and_b32 s62, s43, 1\n"
"ds_read_b64_tr_b16 a[162:163], v218 offset:42000\n"
"v_fma_mix_f32 v185, v52, v145, v153 op_sel:[1,0,0] op_sel_hi:[1,0,0]\n"
"v_mfma_f32_16x16x32_bf16 a[28:31], a[184:187], v[36:39], a[28:31]\n"
"ds_read_b64_tr_b16 a[164:165], v218 offset:45072\n"
"v_fma_mix_f32 v186, v53, v146, v154 op_sel:[0,0,0] op_sel_hi:[1,0,0]\n"
"v_mfma_f32_16x16x32_bf16 a[28:31], a[188:191], v[44:47], a[28:31]\n"
"s_waitcnt lgkmcnt(11)\n"
"s_lshl_b32 s62, s62, 10\n"
"ds_read_b64_tr_b16 a[166:167], v218 offset:46096\n"
"v_fma_mix_f32 v187, v53, v147, v155 op_sel:[1,0,0] op_sel_hi:[1,0,0]\n"
"v_mfma_f32_16x16x32_bf16 a[32:35], a[224:227], v[32:35], a[32:35]\n"
"s_add_u32 m0, s51, 1024\n"
"ds_read_b64_tr_b16 a[168:169], v218 offset:40984\n"
"v_fma_mix_f32 v188, v54, v148, v156 op_sel:[0,0,0] op_sel_hi:[1,0,0]\n"
"v_mfma_f32_16x16x32_bf16 a[32:35], a[228:231], v[40:43], a[32:35]\n"
"s_sub_u32 s54, s54, s62\n"
"ds_read_b64_tr_b16 a[170:171], v218 offset:42008\n"
"buffer_load_dwordx4 v217, s[20:23], s50 offen offset:0 lds\n"
"v_mfma_f32_16x16x32_bf16 a[36:39], a[224:227], v[36:39], a[36:39]\n"
"ds_read_b64_tr_b16 a[172:173], v218 offset:45080\n"
"v_fma_mix_f32 v189, v54, v149, v157 op_sel:[1,0,0] op_sel_hi:[1,0,0]\n"
"v_mfma_f32_16x16x32_bf16 a[36:39], a[228:231], v[44:47], a[36:39]\n"
"s_add_u32 s49, s54, 4096\n"
"ds_read_b64_tr_b16 a[174:175], v218 offset:46104\n"
"v_fma_mix_f32 v190, v55, v150, v158 op_sel:[0,0,0] op_sel_hi:[1,0,0]\n"
"v_mfma_f32_16x16x32_bf16 a[40:43], a[232:235], v[32:35], a[40:43]\n"
"ds_read_b64_tr_b16 a[176:177], v218 offset:41232\n"
"buffer_load_dwordx4 v[80:83], v249, s[28:31], s2 offen offset:2048 sc1\n"
"v_mfma_f32_16x16x32_bf16 a[40:43], a[236:239], v[40:43], a[40:43]\n"
"s_lshl_b32 s52, s44, 4\n"
"ds_read_b64_tr_b16 a[178:179], v218 offset:42256\n"
"v_fma_mix_f32 v191, v55, v151, v159 op_sel:[1,0,0] op_sel_hi:[1,0,0]\n"
"v_cvt_pk_bf16_f32 v52, v184, v185\n"
"v_mfma_f32_16x16x32_bf16 a[44:47], a[232:235], v[36:39], a[44:47]\n"
"s_add_u32 m0, s51, 8960\n"
"ds_read_b64_tr_b16 a[180:181], v218 offset:45328\n"
"ds_read_b128 v[144:147], v224 offset:2048\n"
"v_mfma_f32_16x16x32_bf16 a[44:47], a[236:239], v[44:47], a[44:47]\n"
"s_waitcnt lgkmcnt(12)\n"
"s_add_u32 s52, s52, s43\n"
"ds_read_b64_tr_b16 a[182:183], v218 offset:46352\n"
"buffer_load_dwordx4 v217, s[20:23], s50 offen offset:256 lds\n"
"v_mfma_f32_16x16x32_bf16 a[48:51], a[240:243], v[32:35], a[48:51]\n"
"s_add_u32 s50, s50, 32768\n"
"ds_read_b64_tr_b16 a[184:185], v218 offset:41240\n"
"v_cvt_pk_bf16_f32 v53, v186, v187\n"
"v_mfma_f32_16x16x32_bf16 a[48:51], a[244:247], v[40:43], a[48:51]\n"
"s_mul_i32 s52, s52, 18432\n"
"ds_read_b64_tr_b16 a[186:187], v218 offset:42264\n"
"ds_read_b128 v[148:151], v224 offset:2112\n"
"v_mfma_f32_16x16x32_bf16 a[52:55], a[240:243], v[36:39], a[52:55]\n"
"ds_read_b64_tr_b16 a[188:189], v218 offset:45336\n"
"v_cvt_pk_bf16_f32 v54, v188, v189\n"
"v_mfma_f32_16x16x32_bf16 a[52:55], a[244:247], v[44:47], a[52:55]\n"
"s_mul_i32 s62, s41, 5\n"
"ds_read_b64_tr_b16 a[190:191], v218 offset:46360\n"
"v_cvt_pk_bf16_f32 v55, v190, v191\n"
"v_mfma_f32_16x16x32_bf16 a[56:59], a[248:251], v[32:35], a[56:59]\n"
"s_add_u32 m0, s51, 16392\n"
"ds_read_b64_tr_b16 a[224:225], v218 offset:41488\n"
"ds_read_b128 v[152:155], v224 offset:2176\n"
"v_mfma_f32_16x16x32_bf16 a[56:59], a[252:255], v[40:43], a[56:59]\n"
"s_min_u32 s62, s62, 13\n"
"ds_read_b64_tr_b16 a[226:227], v218 offset:42512\n"
"buffer_load_dwordx4 v217, s[20:23], s50 offen offset:0 lds\n"
"v_mfma_f32_16x16x32_bf16 a[60:63], a[248:251], v[36:39], a[60:63]\n"
"ds_read_b64_tr_b16 a[228:229], v218 offset:45584\n"
"v_fma_mix_f32 v176, v56, v160, v168 op_sel:[0,0,0] op_sel_hi:[1,0,0]\n"
"v_mfma_f32_16x16x32_bf16 a[60:63], a[252:255], v[44:47], a[60:63]\n"
"s_waitcnt lgkmcnt(14)\n"
"s_lshl_b32 s62, s62, 10\n"
"ds_read_b64_tr_b16 a[230:231], v218 offset:46608\n"
"ds_read_b128 v[156:159], v224 offset:2240\n"
"v_mfma_f32_16x16x32_bf16 a[64:67], a[160:163], v[32:35], a[64:67]\n"
"ds_read_b64_tr_b16 a[232:233], v218 offset:41496\n"
"v_fma_mix_f32 v177, v56, v161, v169 op_sel:[1,0,0] op_sel_hi:[1,0,0]\n"
"v_mfma_f32_16x16x32_bf16 a[64:67], a[164:167], v[40:43], a[64:67]\n"
"s_add_u32 s52, s52, s62\n"
"ds_read_b64_tr_b16 a[234:235], v218 offset:42520\n"
"v_fma_mix_f32 v178, v57, v162, v170 op_sel:[0,0,0] op_sel_hi:[1,0,0]\n"
"v_mfma_f32_16x16x32_bf16 a[68:71], a[160:163], v[36:39], a[68:71]\n"
"s_add_u32 m0, s51, 24328\n"
"ds_read_b64_tr_b16 a[236:237], v218 offset:45592\n"
"v_fma_mix_f32 v179, v57, v163, v171 op_sel:[1,0,0] op_sel_hi:[1,0,0]\n"
"v_fma_mix_f32 v180, v58, v164, v172 op_sel:[0,0,0] op_sel_hi:[1,0,0]\n"
"v_mfma_f32_16x16x32_bf16 a[68:71], a[164:167], v[44:47], a[68:71]\n"
"s_add_u32 s52, s52, 26402816\n"
"ds_read_b64_tr_b16 a[238:239], v218 offset:46616\n"
"buffer_load_dwordx4 v217, s[20:23], s50 offen offset:256 lds\n"
"v_mfma_f32_16x16x32_bf16 a[72:75], a[168:171], v[32:35], a[72:75]\n"
"s_add_u32 s50, s50, 32768\n"
"ds_read_b64_tr_b16 a[240:241], v218 offset:41744\n"
"v_fma_mix_f32 v181, v58, v165, v173 op_sel:[1,0,0] op_sel_hi:[1,0,0]\n"
"v_mfma_f32_16x16x32_bf16 a[72:75], a[172:175], v[40:43], a[72:75]\n"
"s_add_u32 s53, s52, 4096\n"
"ds_read_b64_tr_b16 a[242:243], v218 offset:42768\n"
"buffer_load_dwordx4 v[84:87], v255, s[28:31], s2 offen offset:2048 sc1\n"
"v_mfma_f32_16x16x32_bf16 a[76:79], a[168:171], v[36:39], a[76:79]\n"
"ds_read_b64_tr_b16 a[244:245], v218 offset:45840\n"
"v_fma_mix_f32 v182, v59, v166, v174 op_sel:[0,0,0] op_sel_hi:[1,0,0]\n"
"v_mfma_f32_16x16x32_bf16 a[76:79], a[172:175], v[44:47], a[76:79]\n"
"s_waitcnt lgkmcnt(13)\n"
"ds_read_b64_tr_b16 a[246:247], v218 offset:46864\n"
"v_fma_mix_f32 v183, v59, v167, v175 op_sel:[1,0,0] op_sel_hi:[1,0,0]\n"
"v_mfma_f32_16x16x32_bf16 a[80:83], a[176:179], v[32:35], a[80:83]\n"
"s_add_u32 m0, s51, 17416\n"
"ds_read_b64_tr_b16 a[248:249], v218 offset:41752\n"
"v_cvt_pk_bf16_f32 v56, v176, v177\n"
"v_mfma_f32_16x16x32_bf16 a[80:83], a[180:183], v[40:43], a[80:83]\n"
"ds_read_b64_tr_b16 a[250:251], v218 offset:42776\n"
"buffer_load_dwordx4 v217, s[20:23], s50 offen offset:0 lds\n"
"v_mfma_f32_16x16x32_bf16 a[84:87], a[176:179], v[36:39], a[84:87]\n"
"ds_read_b64_tr_b16 a[252:253], v218 offset:45848\n"
"v_cvt_pk_bf16_f32 v57, v178, v179\n"
"v_mfma_f32_16x16x32_bf16 a[84:87], a[180:183], v[44:47], a[84:87]\n"
"ds_read_b64_tr_b16 a[254:255], v218 offset:46872\n"
"v_cvt_pk_bf16_f32 v58, v180, v181\n"
"v_mfma_f32_16x16x32_bf16 v[236:239], a[184:187], v[32:35], v[236:239]\n"
"ds_read_b64_tr_b16 a[160:161], v219 offset:0\n"
"v_cvt_pk_bf16_f32 v59, v182, v183\n"
"v_mfma_f32_16x16x32_bf16 v[236:239], a[188:191], v[40:43], v[236:239]\n"
"ds_read_b64_tr_b16 a[162:163], v219 offset:1024\n"
"v_fma_mix_f32 v184, v60, v160, v168 op_sel:[0,0,0] op_sel_hi:[1,0,0]\n"
"v_mfma_f32_16x16x32_bf16 v[232:235], a[184:187], v[36:39], v[232:235]\n"
"s_add_u32 m0, s51, 25352\n"
"ds_read_b64_tr_b16 a[164:165], v219 offset:4096\n"
"v_fma_mix_f32 v185, v60, v161, v169 op_sel:[1,0,0] op_sel_hi:[1,0,0]\n"
"v_mfma_f32_16x16x32_bf16 v[232:235], a[188:191], v[44:47], v[232:235]\n"
"s_waitcnt lgkmcnt(11)\n"
"buffer_load_dwordx4 v217, s[20:23], s50 offen offset:256 lds\n"
"ds_read_b64_tr_b16 a[166:167], v219 offset:5120\n"
"v_fma_mix_f32 v186, v61, v162, v170 op_sel:[0,0,0] op_sel_hi:[1,0,0]\n"
"v_mfma_f32_16x16x32_bf16 v[228:231], a[224:227], v[32:35], v[228:231]\n"
"s_add_u32 s50, s50, 425984\n"
"ds_read_b64_tr_b16 a[168:169], v219 offset:8\n"
"v_fma_mix_f32 v187, v61, v163, v171 op_sel:[1,0,0] op_sel_hi:[1,0,0]\n"
"v_mfma_f32_16x16x32_bf16 v[228:231], a[228:231], v[40:43], v[228:231]\n"
"ds_read_b64_tr_b16 a[170:171], v219 offset:1032\n"
"v_fma_mix_f32 v188, v62, v164, v172 op_sel:[0,0,0] op_sel_hi:[1,0,0]\n"
"v_mfma_f32_16x16x32_bf16 v[220:223], a[224:227], v[36:39], v[220:223]\n"
"ds_read_b64_tr_b16 a[172:173], v219 offset:4104\n"
"buffer_load_dwordx4 v[88:91], v249, s[28:31], s2 offen offset:3072 sc1\n"
"v_mfma_f32_16x16x32_bf16 v[220:223], a[228:231], v[44:47], v[220:223]\n"
"ds_read_b64_tr_b16 a[174:175], v219 offset:5128\n"
"v_fma_mix_f32 v189, v62, v165, v173 op_sel:[1,0,0] op_sel_hi:[1,0,0]\n"
"v_mfma_f32_16x16x32_bf16 v[212:215], a[232:235], v[32:35], v[212:215]\n"
"ds_read_b64_tr_b16 a[176:177], v219 offset:256\n"
"v_fma_mix_f32 v190, v63, v166, v174 op_sel:[0,0,0] op_sel_hi:[1,0,0]\n"
"v_mfma_f32_16x16x32_bf16 v[212:215], a[236:239], v[40:43], v[212:215]\n"
"ds_read_b64_tr_b16 a[178:179], v219 offset:1280\n"
"v_fma_mix_f32 v191, v63, v167, v175 op_sel:[1,0,0] op_sel_hi:[1,0,0]\n"
"v_mfma_f32_16x16x32_bf16 v[208:211], a[232:235], v[36:39], v[208:211]\n"
"ds_read_b64_tr_b16 a[180:181], v219 offset:4352\n"
"ds_read_b128 v[160:163], v224 offset:2304\n"
"v_mfma_f32_16x16x32_bf16 v[208:211], a[236:239], v[44:47], v[208:211]\n"
"s_waitcnt lgkmcnt(12)\n"
"ds_read_b64_tr_b16 a[182:183], v219 offset:5376\n"
"v_cvt_pk_bf16_f32 v60, v184, v185\n"
"v_mfma_f32_16x16x32_bf16 v[204:207], a[240:243], v[32:35], v[204:207]\n"
"ds_read_b64_tr_b16 a[184:185], v219 offset:264\n"
"v_cvt_pk_bf16_f32 v61, v186, v187\n"
"v_mfma_f32_16x16x32_bf16 v[204:207], a[244:247], v[40:43], v[204:207]\n"
"ds_read_b64_tr_b16 a[186:187], v219 offset:1288\n"
"ds_read_b128 v[164:167], v224 offset:2368\n"
"v_mfma_f32_16x16x32_bf16 v[200:203], a[240:243], v[36:39], v[200:203]\n"
"ds_read_b64_tr_b16 a[188:189], v219 offset:4360\n"
"v_cvt_pk_bf16_f32 v62, v188, v189\n"
"v_mfma_f32_16x16x32_bf16 v[200:203], a[244:247], v[44:47], v[200:203]\n"
"ds_read_b64_tr_b16 a[190:191], v219 offset:5384\n"
"v_cvt_pk_bf16_f32 v63, v190, v191\n"
"v_mfma_f32_16x16x32_bf16 v[196:199], a[248:251], v[32:35], v[196:199]\n"
"s_waitcnt vmcnt(10)\n"
"ds_read_b64_tr_b16 a[224:225], v219 offset:512\n"
"ds_read_b128 v[168:171], v224 offset:2432\n"
"v_fma_mix_f32 v176, v64, v144, v152 op_sel:[0,0,0] op_sel_hi:[1,0,0]\n"
"v_mfma_f32_16x16x32_bf16 v[196:199], a[252:255], v[40:43], v[196:199]\n"
"ds_read_b64_tr_b16 a[226:227], v219 offset:1536\n"
"v_fma_mix_f32 v177, v64, v145, v153 op_sel:[1,0,0] op_sel_hi:[1,0,0]\n"
"v_mfma_f32_16x16x32_bf16 v[192:195], a[248:251], v[36:39], v[192:195]\n"
"ds_read_b64_tr_b16 a[228:229], v219 offset:4608\n"
"v_fma_mix_f32 v178, v65, v146, v154 op_sel:[0,0,0] op_sel_hi:[1,0,0]\n"
"v_mfma_f32_16x16x32_bf16 v[192:195], a[252:255], v[44:47], v[192:195]\n"
"s_waitcnt lgkmcnt(14)\n"
"ds_read_b64_tr_b16 a[230:231], v219 offset:5632\n"
"buffer_load_dwordx4 v[92:95], v255, s[28:31], s2 offen offset:3072 sc1\n"
"v_mfma_f32_16x16x32_bf16 a[0:3], a[160:163], v[48:51], a[0:3]\n"
"s_add_u32 s2, s2, 4096\n"
"ds_read_b64_tr_b16 a[232:233], v219 offset:520\n"
"ds_read_b128 v[172:175], v224 offset:2496\n"
"v_mfma_f32_16x16x32_bf16 a[0:3], a[164:167], v[56:59], a[0:3]\n"
"s_waitcnt vmcnt(14)\n"
"ds_read_b64_tr_b16 a[234:235], v219 offset:1544\n"
"v_fma_mix_f32 v179, v65, v147, v155 op_sel:[1,0,0] op_sel_hi:[1,0,0]\n"
"v_mfma_f32_16x16x32_bf16 a[4:7], a[160:163], v[52:55], a[4:7]\n"
"s_barrier\n"
"ds_read_b64_tr_b16 a[236:237], v219 offset:4616\n"
"v_fma_mix_f32 v180, v66, v148, v156 op_sel:[0,0,0] op_sel_hi:[1,0,0]\n"
"v_mfma_f32_16x16x32_bf16 a[4:7], a[164:167], v[60:63], a[4:7]\n"
"ds_read_b64_tr_b16 a[238:239], v219 offset:5640\n"
"v_fma_mix_f32 v181, v66, v149, v157 op_sel:[1,0,0] op_sel_hi:[1,0,0]\n"
"v_mfma_f32_16x16x32_bf16 a[8:11], a[168:171], v[48:51], a[8:11]\n"
"s_add_u32 m0, s51, 32784\n"
"ds_read_b64_tr_b16 a[240:241], v219 offset:768\n"
"v_fma_mix_f32 v182, v67, v150, v158 op_sel:[0,0,0] op_sel_hi:[1,0,0]\n"
"v_mfma_f32_16x16x32_bf16 a[8:11], a[172:175], v[56:59], a[8:11]\n"
"ds_read_b64_tr_b16 a[242:243], v219 offset:1792\n"
"buffer_load_dwordx4 v217, s[20:23], s50 offen offset:0 lds\n"
"v_mfma_f32_16x16x32_bf16 a[12:15], a[168:171], v[52:55], a[12:15]\n"
"ds_read_b64_tr_b16 a[244:245], v219 offset:4864\n"
"v_fma_mix_f32 v183, v67, v151, v159 op_sel:[1,0,0] op_sel_hi:[1,0,0]\n"
"v_mfma_f32_16x16x32_bf16 a[12:15], a[172:175], v[60:63], a[12:15]\n"
"s_waitcnt lgkmcnt(13)\n"
"ds_read_b64_tr_b16 a[246:247], v219 offset:5888\n"
"v_cvt_pk_bf16_f32 v64, v176, v177\n"
"v_mfma_f32_16x16x32_bf16 a[16:19], a[176:179], v[48:51], a[16:19]\n"
"ds_read_b64_tr_b16 a[248:249], v219 offset:776\n"
"v_cvt_pk_bf16_f32 v65, v178, v179\n"
"v_mfma_f32_16x16x32_bf16 a[16:19], a[180:183], v[56:59], a[16:19]\n"
"ds_read_b64_tr_b16 a[250:251], v219 offset:1800\n"
"v_cvt_pk_bf16_f32 v66, v180, v181\n"
"v_cvt_pk_bf16_f32 v67, v182, v183\n"
"v_mfma_f32_16x16x32_bf16 a[20:23], a[176:179], v[52:55], a[20:23]\n"
"s_add_u32 m0, s51, 40720\n"
"ds_read_b64_tr_b16 a[252:253], v219 offset:4872\n"
"v_fma_mix_f32 v184, v68, v144, v152 op_sel:[0,0,0] op_sel_hi:[1,0,0]\n"
"v_mfma_f32_16x16x32_bf16 a[20:23], a[180:183], v[60:63], a[20:23]\n"
"ds_read_b64_tr_b16 a[254:255], v219 offset:5896\n"
"buffer_load_dwordx4 v217, s[20:23], s50 offen offset:256 lds\n"
"v_mfma_f32_16x16x32_bf16 a[24:27], a[184:187], v[48:51], a[24:27]\n"
"s_add_u32 s50, s50, 32768\n"
"ds_read_b64_tr_b16 a[160:161], v219 offset:8192\n"
"v_fma_mix_f32 v185, v68, v145, v153 op_sel:[1,0,0] op_sel_hi:[1,0,0]\n"
"v_mfma_f32_16x16x32_bf16 a[24:27], a[188:191], v[56:59], a[24:27]\n"
"ds_read_b64_tr_b16 a[162:163], v219 offset:9216\n"
"buffer_load_dwordx4 v[96:99], v249, s[28:31], s2 offen offset:0 sc1\n"
"v_mfma_f32_16x16x32_bf16 a[28:31], a[184:187], v[52:55], a[28:31]\n"
"ds_read_b64_tr_b16 a[164:165], v219 offset:12288\n"
"v_fma_mix_f32 v186, v69, v146, v154 op_sel:[0,0,0] op_sel_hi:[1,0,0]\n"
"v_mfma_f32_16x16x32_bf16 a[28:31], a[188:191], v[60:63], a[28:31]\n"
"s_waitcnt lgkmcnt(11)\n"
"ds_read_b64_tr_b16 a[166:167], v219 offset:13312\n"
"v_fma_mix_f32 v187, v69, v147, v155 op_sel:[1,0,0] op_sel_hi:[1,0,0]\n"
"v_mfma_f32_16x16x32_bf16 a[32:35], a[224:227], v[48:51], a[32:35]\n"
"s_add_u32 m0, s51, 33808\n"
"ds_read_b64_tr_b16 a[168:169], v219 offset:8200\n"
"v_fma_mix_f32 v188, v70, v148, v156 op_sel:[0,0,0] op_sel_hi:[1,0,0]\n"
"v_mfma_f32_16x16x32_bf16 a[32:35], a[228:231], v[56:59], a[32:35]\n"
"ds_read_b64_tr_b16 a[170:171], v219 offset:9224\n"
"buffer_load_dwordx4 v217, s[20:23], s50 offen offset:0 lds\n"
"v_mfma_f32_16x16x32_bf16 a[36:39], a[224:227], v[52:55], a[36:39]\n"
"ds_read_b64_tr_b16 a[172:173], v219 offset:12296\n"
"v_fma_mix_f32 v189, v70, v149, v157 op_sel:[1,0,0] op_sel_hi:[1,0,0]\n"
"v_mfma_f32_16x16x32_bf16 a[36:39], a[228:231], v[60:63], a[36:39]\n"
"ds_read_b64_tr_b16 a[174:175], v219 offset:13320\n"
"v_fma_mix_f32 v190, v71, v150, v158 op_sel:[0,0,0] op_sel_hi:[1,0,0]\n"
"v_mfma_f32_16x16x32_bf16 a[40:43], a[232:235], v[48:51], a[40:43]\n"
"ds_read_b64_tr_b16 a[176:177], v219 offset:8448\n"
"v_fma_mix_f32 v191, v71, v151, v159 op_sel:[1,0,0] op_sel_hi:[1,0,0]\n"
"v_mfma_f32_16x16x32_bf16 a[40:43], a[236:239], v[56:59], a[40:43]\n"
"ds_read_b64_tr_b16 a[178:179], v219 offset:9472\n"
"ds_read_b128 v[144:147], v224 offset:2560\n"
"v_mfma_f32_16x16x32_bf16 a[44:47], a[232:235], v[52:55], a[44:47]\n"
"s_add_u32 m0, s51, 41744\n"
"ds_read_b64_tr_b16 a[180:181], v219 offset:12544\n"
"v_cvt_pk_bf16_f32 v68, v184, v185\n"
"v_cvt_pk_bf16_f32 v69, v186, v187\n"
"v_mfma_f32_16x16x32_bf16 a[44:47], a[236:239], v[60:63], a[44:47]\n"
"s_waitcnt lgkmcnt(12)\n"
"ds_read_b64_tr_b16 a[182:183], v219 offset:13568\n"
"buffer_load_dwordx4 v217, s[20:23], s50 offen offset:256 lds\n"
"v_mfma_f32_16x16x32_bf16 a[48:51], a[240:243], v[48:51], a[48:51]\n"
"s_add_u32 s50, s50, 32768\n"
"ds_read_b64_tr_b16 a[184:185], v219 offset:8456\n"
"ds_read_b128 v[148:151], v224 offset:2624\n"
"v_mfma_f32_16x16x32_bf16 a[48:51], a[244:247], v[56:59], a[48:51]\n"
"ds_read_b64_tr_b16 a[186:187], v219 offset:9480\n"
"v_cvt_pk_bf16_f32 v70, v188, v189\n"
"v_mfma_f32_16x16x32_bf16 a[52:55], a[240:243], v[52:55], a[52:55]\n"
"ds_read_b64_tr_b16 a[188:189], v219 offset:12552\n"
"buffer_load_dwordx4 v[100:103], v255, s[28:31], s2 offen offset:0 sc1\n"
"v_mfma_f32_16x16x32_bf16 a[52:55], a[244:247], v[60:63], a[52:55]\n"
"ds_read_b64_tr_b16 a[190:191], v219 offset:13576\n"
"ds_read_b128 v[152:155], v224 offset:2688\n"
"v_mfma_f32_16x16x32_bf16 a[56:59], a[248:251], v[48:51], a[56:59]\n"
"s_add_u32 m0, s51, 49176\n"
"ds_read_b64_tr_b16 a[224:225], v219 offset:8704\n"
"v_cvt_pk_bf16_f32 v71, v190, v191\n"
"v_mfma_f32_16x16x32_bf16 a[56:59], a[252:255], v[56:59], a[56:59]\n"
"ds_read_b64_tr_b16 a[226:227], v219 offset:9728\n"
"buffer_load_dwordx4 v217, s[20:23], s50 offen offset:0 lds\n"
"v_mfma_f32_16x16x32_bf16 a[60:63], a[248:251], v[52:55], a[60:63]\n"
"ds_read_b64_tr_b16 a[228:229], v219 offset:12800\n"
"ds_read_b128 v[156:159], v224 offset:2752\n"
"v_mfma_f32_16x16x32_bf16 a[60:63], a[252:255], v[60:63], a[60:63]\n"
"s_waitcnt lgkmcnt(14)\n"
"ds_read_b64_tr_b16 a[230:231], v219 offset:13824\n"
"v_fma_mix_f32 v176, v72, v160, v168 op_sel:[0,0,0] op_sel_hi:[1,0,0]\n"
"v_mfma_f32_16x16x32_bf16 a[64:67], a[160:163], v[48:51], a[64:67]\n"
"ds_read_b64_tr_b16 a[232:233], v219 offset:8712\n"
"v_fma_mix_f32 v177, v72, v161, v169 op_sel:[1,0,0] op_sel_hi:[1,0,0]\n"
"v_mfma_f32_16x16x32_bf16 a[64:67], a[164:167], v[56:59], a[64:67]\n"
"ds_read_b64_tr_b16 a[234:235], v219 offset:9736\n"
"v_fma_mix_f32 v178, v73, v162, v170 op_sel:[0,0,0] op_sel_hi:[1,0,0]\n"
"v_mfma_f32_16x16x32_bf16 a[68:71], a[160:163], v[52:55], a[68:71]\n"
"s_add_u32 m0, s51, 57112\n"
"ds_read_b64_tr_b16 a[236:237], v219 offset:12808\n"
"v_fma_mix_f32 v179, v73, v163, v171 op_sel:[1,0,0] op_sel_hi:[1,0,0]\n"
"v_mfma_f32_16x16x32_bf16 a[68:71], a[164:167], v[60:63], a[68:71]\n"
"ds_read_b64_tr_b16 a[238:239], v219 offset:13832\n"
"buffer_load_dwordx4 v217, s[20:23], s50 offen offset:256 lds\n"
"v_fma_mix_f32 v180, v74, v164, v172 op_sel:[0,0,0] op_sel_hi:[1,0,0]\n"
"v_mfma_f32_16x16x32_bf16 a[72:75], a[168:171], v[48:51], a[72:75]\n"
"s_add_u32 s50, s50, 32768\n"
"ds_read_b64_tr_b16 a[240:241], v219 offset:8960\n"
"v_fma_mix_f32 v181, v74, v165, v173 op_sel:[1,0,0] op_sel_hi:[1,0,0]\n"
"v_mfma_f32_16x16x32_bf16 a[72:75], a[172:175], v[56:59], a[72:75]\n"
"ds_read_b64_tr_b16 a[242:243], v219 offset:9984\n"
"v_fma_mix_f32 v182, v75, v166, v174 op_sel:[0,0,0] op_sel_hi:[1,0,0]\n"
"v_mfma_f32_16x16x32_bf16 a[76:79], a[168:171], v[52:55], a[76:79]\n"
"ds_read_b64_tr_b16 a[244:245], v219 offset:13056\n"
"v_fma_mix_f32 v183, v75, v167, v175 op_sel:[1,0,0] op_sel_hi:[1,0,0]\n"
"v_mfma_f32_16x16x32_bf16 a[76:79], a[172:175], v[60:63], a[76:79]\n"
"s_waitcnt lgkmcnt(12)\n"
"ds_read_b64_tr_b16 a[246:247], v219 offset:14080\n"
"v_cvt_pk_bf16_f32 v72, v176, v177\n"
"v_mfma_f32_16x16x32_bf16 a[80:83], a[176:179], v[48:51], a[80:83]\n"
"s_add_u32 m0, s51, 50200\n"
"ds_read_b64_tr_b16 a[248:249], v219 offset:8968\n"
"v_cvt_pk_bf16_f32 v73, v178, v179\n"
"v_mfma_f32_16x16x32_bf16 a[80:83], a[180:183], v[56:59], a[80:83]\n"
"ds_read_b64_tr_b16 a[250:251], v219 offset:9992\n"
"buffer_load_dwordx4 v217, s[20:23], s50 offen offset:0 lds\n"
"v_mfma_f32_16x16x32_bf16 a[84:87], a[176:179], v[52:55], a[84:87]\n"
"ds_read_b64_tr_b16 a[252:253], v219 offset:13064\n"
"v_cvt_pk_bf16_f32 v74, v180, v181\n"
"v_mfma_f32_16x16x32_bf16 a[84:87], a[180:183], v[60:63], a[84:87]\n"
"ds_read_b64_tr_b16 a[254:255], v219 offset:14088\n"
"buffer_load_dwordx4 v[104:107], v249, s[28:31], s2 offen offset:1024 sc1\n"
"v_mfma_f32_16x16x32_bf16 v[236:239], a[184:187], v[48:51], v[236:239]\n"
"ds_read_b64_tr_b16 a[160:161], v219 offset:32784\n"
"v_cvt_pk_bf16_f32 v75, v182, v183\n"
"v_mfma_f32_16x16x32_bf16 v[236:239], a[188:191], v[56:59], v[236:239]\n"
"ds_read_b64_tr_b16 a[162:163], v219 offset:33808\n"
"v_fma_mix_f32 v184, v76, v160, v168 op_sel:[0,0,0] op_sel_hi:[1,0,0]\n"
"v_mfma_f32_16x16x32_bf16 v[232:235], a[184:187], v[52:55], v[232:235]\n"
"s_add_u32 m0, s51, 58136\n"
"ds_read_b64_tr_b16 a[164:165], v219 offset:36880\n"
"v_fma_mix_f32 v185, v76, v161, v169 op_sel:[1,0,0] op_sel_hi:[1,0,0]\n"
"v_mfma_f32_16x16x32_bf16 v[232:235], a[188:191], v[60:63], v[232:235]\n"
"s_waitcnt lgkmcnt(11)\n"
"buffer_load_dwordx4 v217, s[20:23], s50 offen offset:256 lds\n"
"ds_read_b64_tr_b16 a[166:167], v219 offset:37904\n"
"v_mfma_f32_16x16x32_bf16 v[228:231], a[224:227], v[48:51], v[228:231]\n"
"s_add_u32 s50, s50, 425984\n"
"ds_read_b64_tr_b16 a[168:169], v219 offset:32792\n"
"v_fma_mix_f32 v186, v77, v162, v170 op_sel:[0,0,0] op_sel_hi:[1,0,0]\n"
"v_fma_mix_f32 v187, v77, v163, v171 op_sel:[1,0,0] op_sel_hi:[1,0,0]\n"
"v_mfma_f32_16x16x32_bf16 v[228:231], a[228:231], v[56:59], v[228:231]\n"
"ds_read_b64_tr_b16 a[170:171], v219 offset:33816\n"
"v_fma_mix_f32 v188, v78, v164, v172 op_sel:[0,0,0] op_sel_hi:[1,0,0]\n"
"v_mfma_f32_16x16x32_bf16 v[220:223], a[224:227], v[52:55], v[220:223]\n"
"ds_read_b64_tr_b16 a[172:173], v219 offset:36888\n"
"v_fma_mix_f32 v189, v78, v165, v173 op_sel:[1,0,0] op_sel_hi:[1,0,0]\n"
"v_mfma_f32_16x16x32_bf16 v[220:223], a[228:231], v[60:63], v[220:223]\n"
"ds_read_b64_tr_b16 a[174:175], v219 offset:37912\n"
"v_fma_mix_f32 v190, v79, v166, v174 op_sel:[0,0,0] op_sel_hi:[1,0,0]\n"
"v_mfma_f32_16x16x32_bf16 v[212:215], a[232:235], v[48:51], v[212:215]\n"
"ds_read_b64_tr_b16 a[176:177], v219 offset:33040\n"
"v_fma_mix_f32 v191, v79, v167, v175 op_sel:[1,0,0] op_sel_hi:[1,0,0]\n"
"v_mfma_f32_16x16x32_bf16 v[212:215], a[236:239], v[56:59], v[212:215]\n"
"ds_read_b64_tr_b16 a[178:179], v219 offset:34064\n"
"ds_read_b128 v[160:163], v224 offset:2816\n"
"v_mfma_f32_16x16x32_bf16 v[208:211], a[232:235], v[52:55], v[208:211]\n"
"ds_read_b64_tr_b16 a[180:181], v219 offset:37136\n"
"v_cvt_pk_bf16_f32 v76, v184, v185\n"
"v_mfma_f32_16x16x32_bf16 v[208:211], a[236:239], v[60:63], v[208:211]\n"
"s_waitcnt lgkmcnt(12)\n"
"ds_read_b64_tr_b16 a[182:183], v219 offset:38160\n"
"v_cvt_pk_bf16_f32 v77, v186, v187\n"
"v_mfma_f32_16x16x32_bf16 v[204:207], a[240:243], v[48:51], v[204:207]\n"
"ds_read_b64_tr_b16 a[184:185], v219 offset:33048\n"
"buffer_load_dwordx4 v[108:111], v255, s[28:31], s2 offen offset:1024 sc1\n"
"v_mfma_f32_16x16x32_bf16 v[204:207], a[244:247], v[56:59], v[204:207]\n"
"ds_read_b64_tr_b16 a[186:187], v219 offset:34072\n"
"ds_read_b128 v[164:167], v224 offset:2880\n"
"v_mfma_f32_16x16x32_bf16 v[200:203], a[240:243], v[52:55], v[200:203]\n"
"ds_read_b64_tr_b16 a[188:189], v219 offset:37144\n"
"v_cvt_pk_bf16_f32 v78, v188, v189\n"
"v_mfma_f32_16x16x32_bf16 v[200:203], a[244:247], v[60:63], v[200:203]\n"
"ds_read_b64_tr_b16 a[190:191], v219 offset:38168\n"
"v_cvt_pk_bf16_f32 v79, v190, v191\n"
"v_mfma_f32_16x16x32_bf16 v[196:199], a[248:251], v[48:51], v[196:199]\n"
"ds_read_b64_tr_b16 a[224:225], v219 offset:33296\n"
"ds_read_b128 v[168:171], v224 offset:2944\n"
"v_mfma_f32_16x16x32_bf16 v[196:199], a[252:255], v[56:59], v[196:199]\n"
"s_waitcnt vmcnt(12)\n"
"ds_read_b64_tr_b16 a[226:227], v219 offset:34320\n"
"v_fma_mix_f32 v176, v80, v144, v152 op_sel:[0,0,0] op_sel_hi:[1,0,0]\n"
"v_fma_mix_f32 v177, v80, v145, v153 op_sel:[1,0,0] op_sel_hi:[1,0,0]\n"
"v_mfma_f32_16x16x32_bf16 v[192:195], a[248:251], v[52:55], v[192:195]\n"
"ds_read_b64_tr_b16 a[228:229], v219 offset:37392\n"
"v_fma_mix_f32 v178, v81, v146, v154 op_sel:[0,0,0] op_sel_hi:[1,0,0]\n"
"v_mfma_f32_16x16x32_bf16 v[192:195], a[252:255], v[60:63], v[192:195]\n"
"s_waitcnt lgkmcnt(14)\n"
"ds_read_b64_tr_b16 a[230:231], v219 offset:38416\n"
"ds_read_b128 v[172:175], v224 offset:3008\n"
"v_mfma_f32_16x16x32_bf16 a[0:3], a[160:163], v[64:67], a[0:3]\n"
"ds_read_b64_tr_b16 a[232:233], v219 offset:33304\n"
"v_fma_mix_f32 v179, v81, v147, v155 op_sel:[1,0,0] op_sel_hi:[1,0,0]\n"
"v_mfma_f32_16x16x32_bf16 a[0:3], a[164:167], v[72:75], a[0:3]\n"
"s_waitcnt vmcnt(14)\n"
"ds_read_b64_tr_b16 a[234:235], v219 offset:34328\n"
"v_fma_mix_f32 v180, v82, v148, v156 op_sel:[0,0,0] op_sel_hi:[1,0,0]\n"
"v_mfma_f32_16x16x32_bf16 a[4:7], a[160:163], v[68:71], a[4:7]\n"
"s_barrier\n"
"ds_read_b64_tr_b16 a[236:237], v219 offset:37400\n"
"v_fma_mix_f32 v181, v82, v149, v157 op_sel:[1,0,0] op_sel_hi:[1,0,0]\n"
"v_mfma_f32_16x16x32_bf16 a[4:7], a[164:167], v[76:79], a[4:7]\n"
"ds_read_b64_tr_b16 a[238:239], v219 offset:38424\n"
"v_fma_mix_f32 v182, v83, v150, v158 op_sel:[0,0,0] op_sel_hi:[1,0,0]\n"
"v_mfma_f32_16x16x32_bf16 a[8:11], a[168:171], v[64:67], a[8:11]\n"
"s_add_u32 m0, s51, 65568\n"
"ds_read_b64_tr_b16 a[240:241], v219 offset:33552\n"
"v_fma_mix_f32 v183, v83, v151, v159 op_sel:[1,0,0] op_sel_hi:[1,0,0]\n"
"v_mfma_f32_16x16x32_bf16 a[8:11], a[172:175], v[72:75], a[8:11]\n"
"ds_read_b64_tr_b16 a[242:243], v219 offset:34576\n"
"buffer_load_dwordx4 v217, s[20:23], s50 offen offset:0 lds\n"
"v_mfma_f32_16x16x32_bf16 a[12:15], a[168:171], v[68:71], a[12:15]\n"
"ds_read_b64_tr_b16 a[244:245], v219 offset:37648\n"
"v_cvt_pk_bf16_f32 v80, v176, v177\n"
"v_mfma_f32_16x16x32_bf16 a[12:15], a[172:175], v[76:79], a[12:15]\n"
"s_waitcnt lgkmcnt(13)\n"
"ds_read_b64_tr_b16 a[246:247], v219 offset:38672\n"
"buffer_load_dwordx4 v[112:115], v249, s[28:31], s2 offen offset:2048 sc1\n"
"v_mfma_f32_16x16x32_bf16 a[16:19], a[176:179], v[64:67], a[16:19]\n"
"ds_read_b64_tr_b16 a[248:249], v219 offset:33560\n"
"v_cvt_pk_bf16_f32 v81, v178, v179\n"
"v_mfma_f32_16x16x32_bf16 a[16:19], a[180:183], v[72:75], a[16:19]\n"
"ds_read_b64_tr_b16 a[250:251], v219 offset:34584\n"
"v_cvt_pk_bf16_f32 v82, v180, v181\n"
"v_mfma_f32_16x16x32_bf16 a[20:23], a[176:179], v[68:71], a[20:23]\n"
"s_add_u32 m0, s51, 73504\n"
"ds_read_b64_tr_b16 a[252:253], v219 offset:37656\n"
"v_cvt_pk_bf16_f32 v83, v182, v183\n"
"v_fma_mix_f32 v184, v84, v144, v152 op_sel:[0,0,0] op_sel_hi:[1,0,0]\n"
"v_mfma_f32_16x16x32_bf16 a[20:23], a[180:183], v[76:79], a[20:23]\n"
"ds_read_b64_tr_b16 a[254:255], v219 offset:38680\n"
"buffer_load_dwordx4 v217, s[20:23], s50 offen offset:256 lds\n"
"v_mfma_f32_16x16x32_bf16 a[24:27], a[184:187], v[64:67], a[24:27]\n"
"s_add_u32 s50, s50, 32768\n"
"ds_read_b64_tr_b16 a[160:161], v219 offset:40976\n"
"v_fma_mix_f32 v185, v84, v145, v153 op_sel:[1,0,0] op_sel_hi:[1,0,0]\n"
"v_mfma_f32_16x16x32_bf16 a[24:27], a[188:191], v[72:75], a[24:27]\n"
"ds_read_b64_tr_b16 a[162:163], v219 offset:42000\n"
"v_fma_mix_f32 v186, v85, v146, v154 op_sel:[0,0,0] op_sel_hi:[1,0,0]\n"
"v_mfma_f32_16x16x32_bf16 a[28:31], a[184:187], v[68:71], a[28:31]\n"
"ds_read_b64_tr_b16 a[164:165], v219 offset:45072\n"
"v_fma_mix_f32 v187, v85, v147, v155 op_sel:[1,0,0] op_sel_hi:[1,0,0]\n"
"v_mfma_f32_16x16x32_bf16 a[28:31], a[188:191], v[76:79], a[28:31]\n"
"s_waitcnt lgkmcnt(11)\n"
"ds_read_b64_tr_b16 a[166:167], v219 offset:46096\n"
"v_fma_mix_f32 v188, v86, v148, v156 op_sel:[0,0,0] op_sel_hi:[1,0,0]\n"
"v_mfma_f32_16x16x32_bf16 a[32:35], a[224:227], v[64:67], a[32:35]\n"
"s_add_u32 m0, s51, 66592\n"
"ds_read_b64_tr_b16 a[168:169], v219 offset:40984\n"
"AT_pg3:\n"
"s_cbranch_execz AT_hr\n"
"v_fma_mix_f32 v189, v86, v149, v157 op_sel:[1,0,0] op_sel_hi:[1,0,0]\n"
"v_mfma_f32_16x16x32_bf16 a[32:35], a[228:231], v[72:75], a[32:35]\n"
"ds_read_b64_tr_b16 a[170:171], v219 offset:42008\n"
"buffer_load_dwordx4 v217, s[20:23], s50 offen offset:0 lds\n"
"v_mfma_f32_16x16x32_bf16 a[36:39], a[224:227], v[68:71], a[36:39]\n"
"ds_read_b64_tr_b16 a[172:173], v219 offset:45080\n"
"v_fma_mix_f32 v190, v87, v150, v158 op_sel:[0,0,0] op_sel_hi:[1,0,0]\n"
"v_mfma_f32_16x16x32_bf16 a[36:39], a[228:231], v[76:79], a[36:39]\n"
"ds_read_b64_tr_b16 a[174:175], v219 offset:46104\n"
"v_fma_mix_f32 v191, v87, v151, v159 op_sel:[1,0,0] op_sel_hi:[1,0,0]\n"
"v_mfma_f32_16x16x32_bf16 a[40:43], a[232:235], v[64:67], a[40:43]\n"
"ds_read_b64_tr_b16 a[176:177], v219 offset:41232\n"
"buffer_load_dwordx4 v[116:119], v255, s[28:31], s2 offen offset:2048 sc1\n"
"v_mfma_f32_16x16x32_bf16 a[40:43], a[236:239], v[72:75], a[40:43]\n"
"ds_read_b64_tr_b16 a[178:179], v219 offset:42256\n"
"ds_read_b128 v[144:147], v224 offset:3072\n"
"v_mfma_f32_16x16x32_bf16 a[44:47], a[232:235], v[68:71], a[44:47]\n"
"s_add_u32 m0, s51, 74528\n"
"ds_read_b64_tr_b16 a[180:181], v219 offset:45328\n"
"v_cvt_pk_bf16_f32 v84, v184, v185\n"
"v_mfma_f32_16x16x32_bf16 a[44:47], a[236:239], v[76:79], a[44:47]\n"
"s_waitcnt lgkmcnt(12)\n"
"ds_read_b64_tr_b16 a[182:183], v219 offset:46352\n"
"buffer_load_dwordx4 v217, s[20:23], s50 offen offset:256 lds\n"
"v_cvt_pk_bf16_f32 v85, v186, v187\n"
"v_mfma_f32_16x16x32_bf16 a[48:51], a[240:243], v[64:67], a[48:51]\n"
"s_add_u32 s50, s50, 32768\n"
"ds_read_b64_tr_b16 a[184:185], v219 offset:41240\n"
"ds_read_b128 v[148:151], v224 offset:3136\n"
"v_mfma_f32_16x16x32_bf16 a[48:51], a[244:247], v[72:75], a[48:51]\n"
"ds_read_b64_tr_b16 a[186:187], v219 offset:42264\n"
"v_cvt_pk_bf16_f32 v86, v188, v189\n"
"v_mfma_f32_16x16x32_bf16 a[52:55], a[240:243], v[68:71], a[52:55]\n"
"ds_read_b64_tr_b16 a[188:189], v219 offset:45336\n"
"v_cvt_pk_bf16_f32 v87, v190, v191\n"
"v_mfma_f32_16x16x32_bf16 a[52:55], a[244:247], v[76:79], a[52:55]\n"
"ds_read_b64_tr_b16 a[190:191], v219 offset:46360\n"
"ds_read_b128 v[152:155], v224 offset:3200\n"
"v_mfma_f32_16x16x32_bf16 a[56:59], a[248:251], v[64:67], a[56:59]\n"
"s_add_u32 m0, s51, 81960\n"
"ds_read_b64_tr_b16 a[224:225], v219 offset:41488\n"
"v_fma_mix_f32 v176, v88, v160, v168 op_sel:[0,0,0] op_sel_hi:[1,0,0]\n"
"v_mfma_f32_16x16x32_bf16 a[56:59], a[252:255], v[72:75], a[56:59]\n"
"ds_read_b64_tr_b16 a[226:227], v219 offset:42512\n"
"buffer_load_dwordx4 v217, s[20:23], s50 offen offset:0 lds\n"
"v_mfma_f32_16x16x32_bf16 a[60:63], a[248:251], v[68:71], a[60:63]\n"
"ds_read_b64_tr_b16 a[228:229], v219 offset:45584\n"
"ds_read_b128 v[156:159], v224 offset:3264\n"
"v_mfma_f32_16x16x32_bf16 a[60:63], a[252:255], v[76:79], a[60:63]\n"
"s_waitcnt lgkmcnt(14)\n"
"ds_read_b64_tr_b16 a[230:231], v219 offset:46608\n"
"v_fma_mix_f32 v177, v88, v161, v169 op_sel:[1,0,0] op_sel_hi:[1,0,0]\n"
"v_mfma_f32_16x16x32_bf16 a[64:67], a[160:163], v[64:67], a[64:67]\n"
"ds_read_b64_tr_b16 a[232:233], v219 offset:41496\n"
"v_fma_mix_f32 v178, v89, v162, v170 op_sel:[0,0,0] op_sel_hi:[1,0,0]\n"
"v_mfma_f32_16x16x32_bf16 a[64:67], a[164:167], v[72:75], a[64:67]\n"
"ds_read_b64_tr_b16 a[234:235], v219 offset:42520\n"
"v_fma_mix_f32 v179, v89, v163, v171 op_sel:[1,0,0] op_sel_hi:[1,0,0]\n"
"v_mfma_f32_16x16x32_bf16 a[68:71], a[160:163], v[68:71], a[68:71]\n"
"s_add_u32 m0, s51, 89896\n"
"ds_read_b64_tr_b16 a[236:237], v219 offset:45592\n"
"v_fma_mix_f32 v180, v90, v164, v172 op_sel:[0,0,0] op_sel_hi:[1,0,0]\n"
"v_mfma_f32_16x16x32_bf16 a[68:71], a[164:167], v[76:79], a[68:71]\n"
"ds_read_b64_tr_b16 a[238:239], v219 offset:46616\n"
"buffer_load_dwordx4 v217, s[20:23], s50 offen offset:256 lds\n"
"v_mfma_f32_16x16x32_bf16 a[72:75], a[168:171], v[64:67], a[72:75]\n"
"s_add_u32 s50, s50, 32768\n"
"ds_read_b64_tr_b16 a[240:241], v219 offset:41744\n"
"v_fma_mix_f32 v181, v90, v165, v173 op_sel:[1,0,0] op_sel_hi:[1,0,0]\n"
"v_fma_mix_f32 v182, v91, v166, v174 op_sel:[0,0,0] op_sel_hi:[1,0,0]\n"
"v_mfma_f32_16x16x32_bf16 a[72:75], a[172:175], v[72:75], a[72:75]\n"
"ds_read_b64_tr_b16 a[242:243], v219 offset:42768\n"
"buffer_load_dwordx4 v[120:123], v249, s[28:31], s2 offen offset:3072 sc1\n"
"v_mfma_f32_16x16x32_bf16 a[76:79], a[168:171], v[68:71], a[76:79]\n"
"ds_read_b64_tr_b16 a[244:245], v219 offset:45840\n"
"v_fma_mix_f32 v183, v91, v167, v175 op_sel:[1,0,0] op_sel_hi:[1,0,0]\n"
"v_mfma_f32_16x16x32_bf16 a[76:79], a[172:175], v[76:79], a[76:79]\n"
"s_waitcnt lgkmcnt(12)\n"
"ds_read_b64_tr_b16 a[246:247], v219 offset:46864\n"
"v_cvt_pk_bf16_f32 v88, v176, v177\n"
"v_mfma_f32_16x16x32_bf16 a[80:83], a[176:179], v[64:67], a[80:83]\n"
"s_add_u32 m0, s51, 82984\n"
"ds_read_b64_tr_b16 a[248:249], v219 offset:41752\n"
"v_cvt_pk_bf16_f32 v89, v178, v179\n"
"v_mfma_f32_16x16x32_bf16 a[80:83], a[180:183], v[72:75], a[80:83]\n"
"ds_read_b64_tr_b16 a[250:251], v219 offset:42776\n"
"buffer_load_dwordx4 v217, s[20:23], s50 offen offset:0 lds\n"
"v_mfma_f32_16x16x32_bf16 a[84:87], a[176:179], v[68:71], a[84:87]\n"
"ds_read_b64_tr_b16 a[252:253], v219 offset:45848\n"
"v_cvt_pk_bf16_f32 v90, v180, v181\n"
"v_mfma_f32_16x16x32_bf16 a[84:87], a[180:183], v[76:79], a[84:87]\n"
"ds_read_b64_tr_b16 a[254:255], v219 offset:46872\n"
"v_cvt_pk_bf16_f32 v91, v182, v183\n"
"v_mfma_f32_16x16x32_bf16 v[236:239], a[184:187], v[64:67], v[236:239]\n"
"ds_read_b64_tr_b16 a[160:161], v218 offset:0\n"
"v_fma_mix_f32 v184, v92, v160, v168 op_sel:[0,0,0] op_sel_hi:[1,0,0]\n"
"v_mfma_f32_16x16x32_bf16 v[236:239], a[188:191], v[72:75], v[236:239]\n"
"ds_read_b64_tr_b16 a[162:163], v218 offset:1024\n"
"v_fma_mix_f32 v185, v92, v161, v169 op_sel:[1,0,0] op_sel_hi:[1,0,0]\n"
"v_mfma_f32_16x16x32_bf16 v[232:235], a[184:187], v[68:71], v[232:235]\n"
"s_add_u32 m0, s51, 90920\n"
"ds_read_b64_tr_b16 a[164:165], v218 offset:4096\n"
"v_fma_mix_f32 v186, v93, v162, v170 op_sel:[0,0,0] op_sel_hi:[1,0,0]\n"
"v_mfma_f32_16x16x32_bf16 v[232:235], a[188:191], v[76:79], v[232:235]\n"
"s_waitcnt lgkmcnt(11)\n"
"buffer_load_dwordx4 v217, s[20:23], s50 offen offset:256 lds\n"
"ds_read_b64_tr_b16 a[166:167], v218 offset:5120\n"
"v_mfma_f32_16x16x32_bf16 v[228:231], a[224:227], v[64:67], v[228:231]\n"
"s_add_u32 s50, s50, 425984\n"
"ds_read_b64_tr_b16 a[168:169], v218 offset:8\n"
"v_fma_mix_f32 v187, v93, v163, v171 op_sel:[1,0,0] op_sel_hi:[1,0,0]\n"
"v_mfma_f32_16x16x32_bf16 v[228:231], a[228:231], v[72:75], v[228:231]\n"
"ds_read_b64_tr_b16 a[170:171], v218 offset:1032\n"
"v_fma_mix_f32 v188, v94, v164, v172 op_sel:[0,0,0] op_sel_hi:[1,0,0]\n"
"v_fma_mix_f32 v189, v94, v165, v173 op_sel:[1,0,0] op_sel_hi:[1,0,0]\n"
"v_mfma_f32_16x16x32_bf16 v[220:223], a[224:227], v[68:71], v[220:223]\n"
"ds_read_b64_tr_b16 a[172:173], v218 offset:4104\n"
"buffer_load_dwordx4 v[124:127], v255, s[28:31], s2 offen offset:3072 sc1\n"
"v_mfma_f32_16x16x32_bf16 v[220:223], a[228:231], v[76:79], v[220:223]\n"
"s_add_u32 s2, s2, 4096\n"
"ds_read_b64_tr_b16 a[174:175], v218 offset:5128\n"
"v_fma_mix_f32 v190, v95, v166, v174 op_sel:[0,0,0] op_sel_hi:[1,0,0]\n"
"v_mfma_f32_16x16x32_bf16 v[212:215], a[232:235], v[64:67], v[212:215]\n"
"ds_read_b64_tr_b16 a[176:177], v218 offset:256\n"
"v_fma_mix_f32 v191, v95, v167, v175 op_sel:[1,0,0] op_sel_hi:[1,0,0]\n"
"v_mfma_f32_16x16x32_bf16 v[212:215], a[236:239], v[72:75], v[212:215]\n"
"ds_read_b64_tr_b16 a[178:179], v218 offset:1280\n"
"ds_read_b128 v[160:163], v224 offset:3328\n"
"v_mfma_f32_16x16x32_bf16 v[208:211], a[232:235], v[68:71], v[208:211]\n"
"ds_read_b64_tr_b16 a[180:181], v218 offset:4352\n"
"v_cvt_pk_bf16_f32 v92, v184, v185\n"
"v_mfma_f32_16x16x32_bf16 v[208:211], a[236:239], v[76:79], v[208:211]\n"
"s_waitcnt lgkmcnt(12)\n"
"ds_read_b64_tr_b16 a[182:183], v218 offset:5376\n"
"v_cvt_pk_bf16_f32 v93, v186, v187\n"
"v_mfma_f32_16x16x32_bf16 v[204:207], a[240:243], v[64:67], v[204:207]\n"
"ds_read_b64_tr_b16 a[184:185], v218 offset:264\n"
"ds_read_b128 v[164:167], v224 offset:3392\n"
"v_mfma_f32_16x16x32_bf16 v[204:207], a[244:247], v[72:75], v[204:207]\n"
"ds_read_b64_tr_b16 a[186:187], v218 offset:1288\n"
"v_cvt_pk_bf16_f32 v94, v188, v189\n"
"v_mfma_f32_16x16x32_bf16 v[200:203], a[240:243], v[68:71], v[200:203]\n"
"ds_read_b64_tr_b16 a[188:189], v218 offset:4360\n"
"v_cvt_pk_bf16_f32 v95, v190, v191\n"
"v_mfma_f32_16x16x32_bf16 v[200:203], a[244:247], v[76:79], v[200:203]\n"
"ds_read_b64_tr_b16 a[190:191], v218 offset:5384\n"
"ds_read_b128 v[168:171], v224 offset:3456\n"
"v_mfma_f32_16x16x32_bf16 v[196:199], a[248:251], v[64:67], v[196:199]\n"
"s_waitcnt vmcnt(12)\n"
"ds_read_b64_tr_b16 a[224:225], v218 offset:512\n"
"v_fma_mix_f32 v176, v96, v144, v152 op_sel:[0,0,0] op_sel_hi:[1,0,0]\n"
"v_mfma_f32_16x16x32_bf16 v[196:199], a[252:255], v[72:75], v[196:199]\n"
"ds_read_b64_tr_b16 a[226:227], v218 offset:1536\n"
"v_fma_mix_f32 v177, v96, v145, v153 op_sel:[1,0,0] op_sel_hi:[1,0,0]\n"
"v_mfma_f32_16x16x32_bf16 v[192:195], a[248:251], v[68:71], v[192:195]\n"
"ds_read_b64_tr_b16 a[228:229], v218 offset:4608\n"
"ds_read_b128 v[172:175], v224 offset:3520\n"
"v_fma_mix_f32 v178, v97, v146, v154 op_sel:[0,0,0] op_sel_hi:[1,0,0]\n"
"v_mfma_f32_16x16x32_bf16 v[192:195], a[252:255], v[76:79], v[192:195]\n"
"s_waitcnt lgkmcnt(14)\n"
"ds_read_b64_tr_b16 a[230:231], v218 offset:5632\n"
"buffer_load_dwordx4 v[128:131], v249, s[28:31], s2 offen offset:0 sc1\n"
"v_mfma_f32_16x16x32_bf16 a[0:3], a[160:163], v[80:83], a[0:3]\n"
"ds_read_b64_tr_b16 a[232:233], v218 offset:520\n"
"v_fma_mix_f32 v179, v97, v147, v155 op_sel:[1,0,0] op_sel_hi:[1,0,0]\n"
"v_mfma_f32_16x16x32_bf16 a[0:3], a[164:167], v[88:91], a[0:3]\n"
"s_waitcnt vmcnt(14)\n"
"ds_read_b64_tr_b16 a[234:235], v218 offset:1544\n"
"v_fma_mix_f32 v180, v98, v148, v156 op_sel:[0,0,0] op_sel_hi:[1,0,0]\n"
"v_mfma_f32_16x16x32_bf16 a[4:7], a[160:163], v[84:87], a[4:7]\n"
"s_barrier\n"
"ds_read_b64_tr_b16 a[236:237], v218 offset:4616\n"
"v_fma_mix_f32 v181, v98, v149, v157 op_sel:[1,0,0] op_sel_hi:[1,0,0]\n"
"v_mfma_f32_16x16x32_bf16 a[4:7], a[164:167], v[92:95], a[4:7]\n"
"ds_read_b64_tr_b16 a[238:239], v218 offset:5640\n"
"v_fma_mix_f32 v182, v99, v150, v158 op_sel:[0,0,0] op_sel_hi:[1,0,0]\n"
"v_mfma_f32_16x16x32_bf16 a[8:11], a[168:171], v[80:83], a[8:11]\n"
"s_add_u32 m0, s51, 98352\n"
"ds_read_b64_tr_b16 a[240:241], v218 offset:768\n"
"v_fma_mix_f32 v183, v99, v151, v159 op_sel:[1,0,0] op_sel_hi:[1,0,0]\n"
"v_mfma_f32_16x16x32_bf16 a[8:11], a[172:175], v[88:91], a[8:11]\n"
"ds_read_b64_tr_b16 a[242:243], v218 offset:1792\n"
"buffer_load_dwordx4 v217, s[20:23], s50 offen offset:0 lds\n"
"v_mfma_f32_16x16x32_bf16 a[12:15], a[168:171], v[84:87], a[12:15]\n"
"ds_read_b64_tr_b16 a[244:245], v218 offset:4864\n"
"v_cvt_pk_bf16_f32 v96, v176, v177\n"
"v_mfma_f32_16x16x32_bf16 a[12:15], a[172:175], v[92:95], a[12:15]\n"
"s_waitcnt lgkmcnt(12)\n"
"ds_read_b64_tr_b16 a[246:247], v218 offset:5888\n"
"v_cvt_pk_bf16_f32 v97, v178, v179\n"
"v_mfma_f32_16x16x32_bf16 a[16:19], a[176:179], v[80:83], a[16:19]\n"
"ds_read_b64_tr_b16 a[248:249], v218 offset:776\n"
"v_cvt_pk_bf16_f32 v98, v180, v181\n"
"v_mfma_f32_16x16x32_bf16 a[16:19], a[180:183], v[88:91], a[16:19]\n"
"ds_read_b64_tr_b16 a[250:251], v218 offset:1800\n"
"v_cvt_pk_bf16_f32 v99, v182, v183\n"
"v_mfma_f32_16x16x32_bf16 a[20:23], a[176:179], v[84:87], a[20:23]\n"
"s_add_u32 m0, s51, 106288\n"
"ds_read_b64_tr_b16 a[252:253], v218 offset:4872\n"
"v_fma_mix_f32 v184, v100, v144, v152 op_sel:[0,0,0] op_sel_hi:[1,0,0]\n"
"v_mfma_f32_16x16x32_bf16 a[20:23], a[180:183], v[92:95], a[20:23]\n"
"ds_read_b64_tr_b16 a[254:255], v218 offset:5896\n"
"buffer_load_dwordx4 v217, s[20:23], s50 offen offset:256 lds\n"
"v_fma_mix_f32 v185, v100, v145, v153 op_sel:[1,0,0] op_sel_hi:[1,0,0]\n"
"v_mfma_f32_16x16x32_bf16 a[24:27], a[184:187], v[80:83], a[24:27]\n"
"s_add_u32 s50, s50, 32768\n"
"ds_read_b64_tr_b16 a[160:161], v218 offset:8192\n"
"v_fma_mix_f32 v186, v101, v146, v154 op_sel:[0,0,0] op_sel_hi:[1,0,0]\n"
"v_mfma_f32_16x16x32_bf16 a[24:27], a[188:191], v[88:91], a[24:27]\n"
"ds_read_b64_tr_b16 a[162:163], v218 offset:9216\n"
"buffer_load_dwordx4 v[132:135], v255, s[28:31], s2 offen offset:0 sc1\n"
"v_mfma_f32_16x16x32_bf16 a[28:31], a[184:187], v[84:87], a[28:31]\n"
"ds_read_b64_tr_b16 a[164:165], v218 offset:12288\n"
"v_fma_mix_f32 v187, v101, v147, v155 op_sel:[1,0,0] op_sel_hi:[1,0,0]\n"
"v_mfma_f32_16x16x32_bf16 a[28:31], a[188:191], v[92:95], a[28:31]\n"
"s_waitcnt lgkmcnt(11)\n"
"ds_read_b64_tr_b16 a[166:167], v218 offset:13312\n"
"v_fma_mix_f32 v188, v102, v148, v156 op_sel:[0,0,0] op_sel_hi:[1,0,0]\n"
"v_mfma_f32_16x16x32_bf16 a[32:35], a[224:227], v[80:83], a[32:35]\n"
"s_add_u32 m0, s51, 99376\n"
"ds_read_b64_tr_b16 a[168:169], v218 offset:8200\n"
"v_fma_mix_f32 v189, v102, v149, v157 op_sel:[1,0,0] op_sel_hi:[1,0,0]\n"
"v_mfma_f32_16x16x32_bf16 a[32:35], a[228:231], v[88:91], a[32:35]\n"
"ds_read_b64_tr_b16 a[170:171], v218 offset:9224\n"
"buffer_load_dwordx4 v217, s[20:23], s50 offen offset:0 lds\n"
"v_mfma_f32_16x16x32_bf16 a[36:39], a[224:227], v[84:87], a[36:39]\n"
"ds_read_b64_tr_b16 a[172:173], v218 offset:12296\n"
"v_fma_mix_f32 v190, v103, v150, v158 op_sel:[0,0,0] op_sel_hi:[1,0,0]\n"
"v_mfma_f32_16x16x32_bf16 a[36:39], a[228:231], v[92:95], a[36:39]\n"
"ds_read_b64_tr_b16 a[174:175], v218 offset:13320\n"
"v_fma_mix_f32 v191, v103, v151, v159 op_sel:[1,0,0] op_sel_hi:[1,0,0]\n"
"v_mfma_f32_16x16x32_bf16 a[40:43], a[232:235], v[80:83], a[40:43]\n"
"ds_read_b64_tr_b16 a[176:177], v218 offset:8448\n"
"ds_read_b128 v[144:147], v224 offset:3584\n"
"v_mfma_f32_16x16x32_bf16 a[40:43], a[236:239], v[88:91], a[40:43]\n"
"ds_read_b64_tr_b16 a[178:179], v218 offset:9472\n"
"v_cvt_pk_bf16_f32 v100, v184, v185\n"
"v_mfma_f32_16x16x32_bf16 a[44:47], a[232:235], v[84:87], a[44:47]\n"
"s_add_u32 m0, s51, 107312\n"
"ds_read_b64_tr_b16 a[180:181], v218 offset:12544\n"
"v_cvt_pk_bf16_f32 v101, v186, v187\n"
"v_mfma_f32_16x16x32_bf16 a[44:47], a[236:239], v[92:95], a[44:47]\n"
"s_waitcnt lgkmcnt(12)\n"
"ds_read_b64_tr_b16 a[182:183], v218 offset:13568\n"
"buffer_load_dwordx4 v217, s[20:23], s50 offen offset:256 lds\n"
"v_mfma_f32_16x16x32_bf16 a[48:51], a[240:243], v[80:83], a[48:51]\n"
"s_add_u32 s50, s50, 32768\n"
"ds_read_b64_tr_b16 a[184:185], v218 offset:8456\n"
"ds_read_b128 v[148:151], v224 offset:3648\n"
"v_cvt_pk_bf16_f32 v102, v188, v189\n"
"v_mfma_f32_16x16x32_bf16 a[48:51], a[244:247], v[88:91], a[48:51]\n"
"ds_read_b64_tr_b16 a[186:187], v218 offset:9480\n"
"v_cvt_pk_bf16_f32 v103, v190, v191\n"
"v_mfma_f32_16x16x32_bf16 a[52:55], a[240:243], v[84:87], a[52:55]\n"
"ds_read_b64_tr_b16 a[188:189], v218 offset:12552\n"
"buffer_load_dwordx4 v[136:139], v249, s[28:31], s2 offen offset:1024 sc1\n"
"v_mfma_f32_16x16x32_bf16 a[52:55], a[244:247], v[92:95], a[52:55]\n"
"ds_read_b64_tr_b16 a[190:191], v218 offset:13576\n"
"ds_read_b128 v[152:155], v224 offset:3712\n"
"v_mfma_f32_16x16x32_bf16 a[56:59], a[248:251], v[80:83], a[56:59]\n"
"s_add_u32 m0, s51, 114744\n"
"ds_read_b64_tr_b16 a[224:225], v218 offset:8704\n"
"v_fma_mix_f32 v176, v104, v160, v168 op_sel:[0,0,0] op_sel_hi:[1,0,0]\n"
"v_mfma_f32_16x16x32_bf16 a[56:59], a[252:255], v[88:91], a[56:59]\n"
"ds_read_b64_tr_b16 a[226:227], v218 offset:9728\n"
"buffer_load_dwordx4 v217, s[20:23], s50 offen offset:0 lds\n"
"v_mfma_f32_16x16x32_bf16 a[60:63], a[248:251], v[84:87], a[60:63]\n"
"ds_read_b64_tr_b16 a[228:229], v218 offset:12800\n"
"ds_read_b128 v[156:159], v224 offset:3776\n"
"v_mfma_f32_16x16x32_bf16 a[60:63], a[252:255], v[92:95], a[60:63]\n"
"s_waitcnt lgkmcnt(14)\n"
"ds_read_b64_tr_b16 a[230:231], v218 offset:13824\n"
"v_fma_mix_f32 v177, v104, v161, v169 op_sel:[1,0,0] op_sel_hi:[1,0,0]\n"
"v_mfma_f32_16x16x32_bf16 a[64:67], a[160:163], v[80:83], a[64:67]\n"
"ds_read_b64_tr_b16 a[232:233], v218 offset:8712\n"
"v_fma_mix_f32 v178, v105, v162, v170 op_sel:[0,0,0] op_sel_hi:[1,0,0]\n"
"v_mfma_f32_16x16x32_bf16 a[64:67], a[164:167], v[88:91], a[64:67]\n"
"ds_read_b64_tr_b16 a[234:235], v218 offset:9736\n"
"v_fma_mix_f32 v179, v105, v163, v171 op_sel:[1,0,0] op_sel_hi:[1,0,0]\n"
"v_mfma_f32_16x16x32_bf16 a[68:71], a[160:163], v[84:87], a[68:71]\n"
"s_add_u32 m0, s51, 122680\n"
"ds_read_b64_tr_b16 a[236:237], v218 offset:12808\n"
"v_fma_mix_f32 v180, v106, v164, v172 op_sel:[0,0,0] op_sel_hi:[1,0,0]\n"
"v_mfma_f32_16x16x32_bf16 a[68:71], a[164:167], v[92:95], a[68:71]\n"
"ds_read_b64_tr_b16 a[238:239], v218 offset:13832\n"
"buffer_load_dwordx4 v217, s[20:23], s50 offen offset:256 lds\n"
"v_mfma_f32_16x16x32_bf16 a[72:75], a[168:171], v[80:83], a[72:75]\n"
"s_add_u32 s50, s50, 32768\n"
"ds_read_b64_tr_b16 a[240:241], v218 offset:8960\n"
"v_fma_mix_f32 v181, v106, v165, v173 op_sel:[1,0,0] op_sel_hi:[1,0,0]\n"
"v_mfma_f32_16x16x32_bf16 a[72:75], a[172:175], v[88:91], a[72:75]\n"
"ds_read_b64_tr_b16 a[242:243], v218 offset:9984\n"
"v_fma_mix_f32 v182, v107, v166, v174 op_sel:[0,0,0] op_sel_hi:[1,0,0]\n"
"v_fma_mix_f32 v183, v107, v167, v175 op_sel:[1,0,0] op_sel_hi:[1,0,0]\n"
"v_mfma_f32_16x16x32_bf16 a[76:79], a[168:171], v[84:87], a[76:79]\n"
"ds_read_b64_tr_b16 a[244:245], v218 offset:13056\n"
"v_cvt_pk_bf16_f32 v104, v176, v177\n"
"v_mfma_f32_16x16x32_bf16 a[76:79], a[172:175], v[92:95], a[76:79]\n"
"s_waitcnt lgkmcnt(12)\n"
"ds_read_b64_tr_b16 a[246:247], v218 offset:14080\n"
"v_cvt_pk_bf16_f32 v105, v178, v179\n"
"v_mfma_f32_16x16x32_bf16 a[80:83], a[176:179], v[80:83], a[80:83]\n"
"s_add_u32 m0, s51, 115768\n"
"ds_read_b64_tr_b16 a[248:249], v218 offset:8968\n"
"v_cvt_pk_bf16_f32 v106, v180, v181\n"
"v_mfma_f32_16x16x32_bf16 a[80:83], a[180:183], v[88:91], a[80:83]\n"
"ds_read_b64_tr_b16 a[250:251], v218 offset:9992\n"
"buffer_load_dwordx4 v217, s[20:23], s50 offen offset:0 lds\n"
"v_mfma_f32_16x16x32_bf16 a[84:87], a[176:179], v[84:87], a[84:87]\n"
"ds_read_b64_tr_b16 a[252:253], v218 offset:13064\n"
"v_cvt_pk_bf16_f32 v107, v182, v183\n"
"v_mfma_f32_16x16x32_bf16 a[84:87], a[180:183], v[92:95], a[84:87]\n"
"ds_read_b64_tr_b16 a[254:255], v218 offset:14088\n"
"buffer_load_dwordx4 v[140:143], v255, s[28:31], s2 offen offset:1024 sc1\n"
"v_mfma_f32_16x16x32_bf16 v[236:239], a[184:187], v[80:83], v[236:239]\n"
"ds_read_b64_tr_b16 a[160:161], v218 offset:32784\n"
"v_fma_mix_f32 v184, v108, v160, v168 op_sel:[0,0,0] op_sel_hi:[1,0,0]\n"
"v_mfma_f32_16x16x32_bf16 v[236:239], a[188:191], v[88:91], v[236:239]\n"
"ds_read_b64_tr_b16 a[162:163], v218 offset:33808\n"
"v_fma_mix_f32 v185, v108, v161, v169 op_sel:[1,0,0] op_sel_hi:[1,0,0]\n"
"v_mfma_f32_16x16x32_bf16 v[232:235], a[184:187], v[84:87], v[232:235]\n"
"s_add_u32 m0, s51, 123704\n"
"ds_read_b64_tr_b16 a[164:165], v218 offset:36880\n"
"v_fma_mix_f32 v186, v109, v162, v170 op_sel:[0,0,0] op_sel_hi:[1,0,0]\n"
"v_mfma_f32_16x16x32_bf16 v[232:235], a[188:191], v[92:95], v[232:235]\n"
"s_waitcnt lgkmcnt(11)\n"
"buffer_load_dwordx4 v217, s[20:23], s50 offen offset:256 lds\n"
"ds_read_b64_tr_b16 a[166:167], v218 offset:37904\n"
"v_mfma_f32_16x16x32_bf16 v[228:231], a[224:227], v[80:83], v[228:231]\n"
"s_add_u32 s50, s50, 425984\n"
"ds_read_b64_tr_b16 a[168:169], v218 offset:32792\n"
"v_fma_mix_f32 v187, v109, v163, v171 op_sel:[1,0,0] op_sel_hi:[1,0,0]\n"
"v_mfma_f32_16x16x32_bf16 v[228:231], a[228:231], v[88:91], v[228:231]\n"
"ds_read_b64_tr_b16 a[170:171], v218 offset:33816\n"
"v_fma_mix_f32 v188, v110, v164, v172 op_sel:[0,0,0] op_sel_hi:[1,0,0]\n"
"v_mfma_f32_16x16x32_bf16 v[220:223], a[224:227], v[84:87], v[220:223]\n"
"ds_read_b64_tr_b16 a[172:173], v218 offset:36888\n"
"v_fma_mix_f32 v189, v110, v165, v173 op_sel:[1,0,0] op_sel_hi:[1,0,0]\n"
"v_fma_mix_f32 v190, v111, v166, v174 op_sel:[0,0,0] op_sel_hi:[1,0,0]\n"
"v_mfma_f32_16x16x32_bf16 v[220:223], a[228:231], v[92:95], v[220:223]\n"
"ds_read_b64_tr_b16 a[174:175], v218 offset:37912\n"
"v_fma_mix_f32 v191, v111, v167, v175 op_sel:[1,0,0] op_sel_hi:[1,0,0]\n"
"v_mfma_f32_16x16x32_bf16 v[212:215], a[232:235], v[80:83], v[212:215]\n"
"ds_read_b64_tr_b16 a[176:177], v218 offset:33040\n"
"ds_read_b128 v[160:163], v224 offset:3840\n"
"v_mfma_f32_16x16x32_bf16 v[212:215], a[236:239], v[88:91], v[212:215]\n"
"ds_read_b64_tr_b16 a[178:179], v218 offset:34064\n"
"v_cvt_pk_bf16_f32 v108, v184, v185\n"
"v_mfma_f32_16x16x32_bf16 v[208:211], a[232:235], v[84:87], v[208:211]\n"
"ds_read_b64_tr_b16 a[180:181], v218 offset:37136\n"
"v_cvt_pk_bf16_f32 v109, v186, v187\n"
"v_mfma_f32_16x16x32_bf16 v[208:211], a[236:239], v[92:95], v[208:211]\n"
"s_waitcnt lgkmcnt(12)\n"
"ds_read_b64_tr_b16 a[182:183], v218 offset:38160\n"
"ds_read_b128 v[164:167], v224 offset:3904\n"
"v_mfma_f32_16x16x32_bf16 v[204:207], a[240:243], v[80:83], v[204:207]\n"
"ds_read_b64_tr_b16 a[184:185], v218 offset:33048\n"
"v_cvt_pk_bf16_f32 v110, v188, v189\n"
"v_mfma_f32_16x16x32_bf16 v[204:207], a[244:247], v[88:91], v[204:207]\n"
"ds_read_b64_tr_b16 a[186:187], v218 offset:34072\n"
"v_cvt_pk_bf16_f32 v111, v190, v191\n"
"v_mfma_f32_16x16x32_bf16 v[200:203], a[240:243], v[84:87], v[200:203]\n"
"ds_read_b64_tr_b16 a[188:189], v218 offset:37144\n"
"ds_read_b128 v[168:171], v224 offset:3968\n"
"v_mfma_f32_16x16x32_bf16 v[200:203], a[244:247], v[92:95], v[200:203]\n"
"s_waitcnt vmcnt(12)\n"
"ds_read_b64_tr_b16 a[190:191], v218 offset:38168\n"
"v_fma_mix_f32 v176, v112, v144, v152 op_sel:[0,0,0] op_sel_hi:[1,0,0]\n"
"v_mfma_f32_16x16x32_bf16 v[196:199], a[248:251], v[80:83], v[196:199]\n"
"ds_read_b64_tr_b16 a[224:225], v218 offset:33296\n"
"v_fma_mix_f32 v177, v112, v145, v153 op_sel:[1,0,0] op_sel_hi:[1,0,0]\n"
"v_mfma_f32_16x16x32_bf16 v[196:199], a[252:255], v[88:91], v[196:199]\n"
"ds_read_b64_tr_b16 a[226:227], v218 offset:34320\n"
"ds_read_b128 v[172:175], v224 offset:4032\n"
"v_mfma_f32_16x16x32_bf16 v[192:195], a[248:251], v[84:87], v[192:195]\n"
"ds_read_b64_tr_b16 a[228:229], v218 offset:37392\n"
"v_fma_mix_f32 v178, v113, v146, v154 op_sel:[0,0,0] op_sel_hi:[1,0,0]\n"
"v_mfma_f32_16x16x32_bf16 v[192:195], a[252:255], v[92:95], v[192:195]\n"
"s_waitcnt lgkmcnt(14)\n"
"ds_read_b64_tr_b16 a[230:231], v218 offset:38416\n"
"v_fma_mix_f32 v179, v113, v147, v155 op_sel:[1,0,0] op_sel_hi:[1,0,0]\n"
"v_fma_mix_f32 v180, v114, v148, v156 op_sel:[0,0,0] op_sel_hi:[1,0,0]\n"
"v_mfma_f32_16x16x32_bf16 a[0:3], a[160:163], v[96:99], a[0:3]\n"
"ds_read_b64_tr_b16 a[232:233], v218 offset:33304\n"
"v_fma_mix_f32 v181, v114, v149, v157 op_sel:[1,0,0] op_sel_hi:[1,0,0]\n"
"v_mfma_f32_16x16x32_bf16 a[0:3], a[164:167], v[104:107], a[0:3]\n"
"s_waitcnt vmcnt(13)\n"
"ds_read_b64_tr_b16 a[234:235], v218 offset:34328\n"
"v_fma_mix_f32 v182, v115, v150, v158 op_sel:[0,0,0] op_sel_hi:[1,0,0]\n"
"v_mfma_f32_16x16x32_bf16 a[4:7], a[160:163], v[100:103], a[4:7]\n"
"s_barrier\n"
"ds_read_b64_tr_b16 a[236:237], v218 offset:37400\n"
"v_fma_mix_f32 v183, v115, v151, v159 op_sel:[1,0,0] op_sel_hi:[1,0,0]\n"
"v_mfma_f32_16x16x32_bf16 a[4:7], a[164:167], v[108:111], a[4:7]\n"
"ds_read_b64_tr_b16 a[238:239], v218 offset:38424\n"
"v_cvt_pk_bf16_f32 v112, v176, v177\n"
"v_mfma_f32_16x16x32_bf16 a[8:11], a[168:171], v[96:99], a[8:11]\n"
"s_mov_b32 m0, s86\n"
"ds_read_b64_tr_b16 a[240:241], v218 offset:33552\n"
"v_cvt_pk_bf16_f32 v113, v178, v179\n"
"v_mfma_f32_16x16x32_bf16 a[8:11], a[172:175], v[104:107], a[8:11]\n"
"ds_read_b64_tr_b16 a[242:243], v218 offset:34576\n"
"buffer_load_dwordx4 v246, s[8:11], s84 offen offset:0 lds\n"
"v_mfma_f32_16x16x32_bf16 a[12:15], a[168:171], v[100:103], a[12:15]\n"
"ds_read_b64_tr_b16 a[244:245], v218 offset:37648\n"
"v_cvt_pk_bf16_f32 v114, v180, v181\n"
"v_mfma_f32_16x16x32_bf16 a[12:15], a[172:175], v[108:111], a[12:15]\n"
"s_waitcnt lgkmcnt(12)\n"
"ds_read_b64_tr_b16 a[246:247], v218 offset:38672\n"
"v_cvt_pk_bf16_f32 v115, v182, v183\n"
"v_mfma_f32_16x16x32_bf16 a[16:19], a[176:179], v[96:99], a[16:19]\n"
"ds_read_b64_tr_b16 a[248:249], v218 offset:33560\n"
"v_fma_mix_f32 v184, v116, v144, v152 op_sel:[0,0,0] op_sel_hi:[1,0,0]\n"
"v_mfma_f32_16x16x32_bf16 a[16:19], a[180:183], v[104:107], a[16:19]\n"
"ds_read_b64_tr_b16 a[250:251], v218 offset:34584\n"
"v_fma_mix_f32 v185, v116, v145, v153 op_sel:[1,0,0] op_sel_hi:[1,0,0]\n"
"v_mfma_f32_16x16x32_bf16 a[20:23], a[176:179], v[100:103], a[20:23]\n"
"ds_read_b64_tr_b16 a[252:253], v218 offset:37656\n"
"v_fma_mix_f32 v186, v117, v146, v154 op_sel:[0,0,0] op_sel_hi:[1,0,0]\n"
"v_mfma_f32_16x16x32_bf16 a[20:23], a[180:183], v[108:111], a[20:23]\n"
"ds_read_b64_tr_b16 a[254:255], v218 offset:38680\n"
"buffer_load_dwordx4 v246, s[8:11], s84 offen offset:1024 lds\n"
"v_mfma_f32_16x16x32_bf16 a[24:27], a[184:187], v[96:99], a[24:27]\n"
"ds_read_b64_tr_b16 a[160:161], v218 offset:40976\n"
"v_fma_mix_f32 v187, v117, v147, v155 op_sel:[1,0,0] op_sel_hi:[1,0,0]\n"
"v_fma_mix_f32 v188, v118, v148, v156 op_sel:[0,0,0] op_sel_hi:[1,0,0]\n"
"v_mfma_f32_16x16x32_bf16 a[24:27], a[188:191], v[104:107], a[24:27]\n"
"ds_read_b64_tr_b16 a[162:163], v218 offset:42000\n"
"v_fma_mix_f32 v189, v118, v149, v157 op_sel:[1,0,0] op_sel_hi:[1,0,0]\n"
"v_mfma_f32_16x16x32_bf16 a[28:31], a[184:187], v[100:103], a[28:31]\n"
"ds_read_b64_tr_b16 a[164:165], v218 offset:45072\n"
"v_fma_mix_f32 v190, v119, v150, v158 op_sel:[0,0,0] op_sel_hi:[1,0,0]\n"
"v_mfma_f32_16x16x32_bf16 a[28:31], a[188:191], v[108:111], a[28:31]\n"
"s_waitcnt lgkmcnt(11)\n"
"ds_read_b64_tr_b16 a[166:167], v218 offset:46096\n"
"v_fma_mix_f32 v191, v119, v151, v159 op_sel:[1,0,0] op_sel_hi:[1,0,0]\n"
"v_mfma_f32_16x16x32_bf16 a[32:35], a[224:227], v[96:99], a[32:35]\n"
"ds_read_b64_tr_b16 a[168:169], v218 offset:40984\n"
"ds_read_b128 v[144:147], v224 offset:4096\n"
"v_mfma_f32_16x16x32_bf16 a[32:35], a[228:231], v[104:107], a[32:35]\n"
"ds_read_b64_tr_b16 a[170:171], v218 offset:42008\n"
"buffer_load_dwordx4 v247, s[8:11], s84 offen offset:2048 lds\n"
"v_mfma_f32_16x16x32_bf16 a[36:39], a[224:227], v[100:103], a[36:39]\n"
"ds_read_b64_tr_b16 a[172:173], v218 offset:45080\n"
"v_cvt_pk_bf16_f32 v116, v184, v185\n"
"v_mfma_f32_16x16x32_bf16 a[36:39], a[228:231], v[108:111], a[36:39]\n"
"ds_read_b64_tr_b16 a[174:175], v218 offset:46104\n"
"ds_read_b128 v[148:151], v224 offset:4160\n"
"v_mfma_f32_16x16x32_bf16 a[40:43], a[232:235], v[96:99], a[40:43]\n"
"ds_read_b64_tr_b16 a[176:177], v218 offset:41232\n"
"v_cvt_pk_bf16_f32 v117, v186, v187\n"
"v_mfma_f32_16x16x32_bf16 a[40:43], a[236:239], v[104:107], a[40:43]\n"
"ds_read_b64_tr_b16 a[178:179], v218 offset:42256\n"
"v_cvt_pk_bf16_f32 v118, v188, v189\n"
"v_mfma_f32_16x16x32_bf16 a[44:47], a[232:235], v[100:103], a[44:47]\n"
"ds_read_b64_tr_b16 a[180:181], v218 offset:45328\n"
"ds_read_b128 v[152:155], v224 offset:4224\n"
"v_mfma_f32_16x16x32_bf16 a[44:47], a[236:239], v[108:111], a[44:47]\n"
"s_waitcnt lgkmcnt(14)\n"
"ds_read_b64_tr_b16 a[182:183], v218 offset:46352\n"
"buffer_load_dwordx4 v247, s[8:11], s84 offen offset:3072 lds\n"
"v_mfma_f32_16x16x32_bf16 a[48:51], a[240:243], v[96:99], a[48:51]\n"
"ds_read_b64_tr_b16 a[184:185], v218 offset:41240\n"
"v_cvt_pk_bf16_f32 v119, v190, v191\n"
"v_mfma_f32_16x16x32_bf16 a[48:51], a[244:247], v[104:107], a[48:51]\n"
"ds_read_b64_tr_b16 a[186:187], v218 offset:42264\n"
"ds_read_b128 v[156:159], v224 offset:4288\n"
"v_fma_mix_f32 v176, v120, v160, v168 op_sel:[0,0,0] op_sel_hi:[1,0,0]\n"
"v_mfma_f32_16x16x32_bf16 a[52:55], a[240:243], v[100:103], a[52:55]\n"
"ds_read_b64_tr_b16 a[188:189], v218 offset:45336\n"
"v_fma_mix_f32 v177, v120, v161, v169 op_sel:[1,0,0] op_sel_hi:[1,0,0]\n"
"v_mfma_f32_16x16x32_bf16 a[52:55], a[244:247], v[108:111], a[52:55]\n"
"ds_read_b64_tr_b16 a[190:191], v218 offset:46360\n"
"v_fma_mix_f32 v178, v121, v162, v170 op_sel:[0,0,0] op_sel_hi:[1,0,0]\n"
"v_mfma_f32_16x16x32_bf16 a[56:59], a[248:251], v[96:99], a[56:59]\n"
"s_add_u32 m0, s86, 16384\n"
"ds_read_b64_tr_b16 a[224:225], v218 offset:41488\n"
"v_fma_mix_f32 v179, v121, v163, v171 op_sel:[1,0,0] op_sel_hi:[1,0,0]\n"
"v_mfma_f32_16x16x32_bf16 a[56:59], a[252:255], v[104:107], a[56:59]\n"
"ds_read_b64_tr_b16 a[226:227], v218 offset:42512\n"
"buffer_load_dwordx4 v246, s[12:15], s84 offen offset:0 lds\n"
"v_mfma_f32_16x16x32_bf16 a[60:63], a[248:251], v[100:103], a[60:63]\n"
"ds_read_b64_tr_b16 a[228:229], v218 offset:45584\n"
"v_fma_mix_f32 v180, v122, v164, v172 op_sel:[0,0,0] op_sel_hi:[1,0,0]\n"
"v_mfma_f32_16x16x32_bf16 a[60:63], a[252:255], v[108:111], a[60:63]\n"
"s_waitcnt lgkmcnt(13)\n"
"ds_read_b64_tr_b16 a[230:231], v218 offset:46608\n"
"v_fma_mix_f32 v181, v122, v165, v173 op_sel:[1,0,0] op_sel_hi:[1,0,0]\n"
"v_mfma_f32_16x16x32_bf16 a[64:67], a[160:163], v[96:99], a[64:67]\n"
"ds_read_b64_tr_b16 a[232:233], v218 offset:41496\n"
"v_fma_mix_f32 v182, v123, v166, v174 op_sel:[0,0,0] op_sel_hi:[1,0,0]\n"
"v_mfma_f32_16x16x32_bf16 a[64:67], a[164:167], v[104:107], a[64:67]\n"
"ds_read_b64_tr_b16 a[234:235], v218 offset:42520\n"
"v_fma_mix_f32 v183, v123, v167, v175 op_sel:[1,0,0] op_sel_hi:[1,0,0]\n"
"v_mfma_f32_16x16x32_bf16 a[68:71], a[160:163], v[100:103], a[68:71]\n"
"ds_read_b64_tr_b16 a[236:237], v218 offset:45592\n"
"v_cvt_pk_bf16_f32 v120, v176, v177\n"
"v_mfma_f32_16x16x32_bf16 a[68:71], a[164:167], v[108:111], a[68:71]\n"
"ds_read_b64_tr_b16 a[238:239], v218 offset:46616\n"
"buffer_load_dwordx4 v247, s[12:15], s84 offen offset:1024 lds\n"
"v_mfma_f32_16x16x32_bf16 a[72:75], a[168:171], v[96:99], a[72:75]\n"
"ds_read_b64_tr_b16 a[240:241], v218 offset:41744\n"
"v_cvt_pk_bf16_f32 v121, v178, v179\n"
"v_mfma_f32_16x16x32_bf16 a[72:75], a[172:175], v[104:107], a[72:75]\n"
"ds_read_b64_tr_b16 a[242:243], v218 offset:42768\n"
"v_cvt_pk_bf16_f32 v122, v180, v181\n"
"v_mfma_f32_16x16x32_bf16 a[76:79], a[168:171], v[100:103], a[76:79]\n"
"ds_read_b64_tr_b16 a[244:245], v218 offset:45840\n"
"v_cvt_pk_bf16_f32 v123, v182, v183\n"
"v_fma_mix_f32 v184, v124, v160, v168 op_sel:[0,0,0] op_sel_hi:[1,0,0]\n"
"v_mfma_f32_16x16x32_bf16 a[76:79], a[172:175], v[108:111], a[76:79]\n"
"s_waitcnt lgkmcnt(11)\n"
"ds_read_b64_tr_b16 a[246:247], v218 offset:46864\n"
"v_fma_mix_f32 v185, v124, v161, v169 op_sel:[1,0,0] op_sel_hi:[1,0,0]\n"
"v_mfma_f32_16x16x32_bf16 a[80:83], a[176:179], v[96:99], a[80:83]\n"
"ds_read_b64_tr_b16 a[248:249], v218 offset:41752\n"
"v_fma_mix_f32 v186, v125, v162, v170 op_sel:[0,0,0] op_sel_hi:[1,0,0]\n"
"v_mfma_f32_16x16x32_bf16 a[80:83], a[180:183], v[104:107], a[80:83]\n"
"ds_read_b64_tr_b16 a[250:251], v218 offset:42776\n"
"buffer_load_dwordx4 v246, s[12:15], s84 offen offset:2048 lds\n"
"v_mfma_f32_16x16x32_bf16 a[84:87], a[176:179], v[100:103], a[84:87]\n"
"AT_pg4:\n"
"s_cbranch_execz AT_hr\n"
"ds_read_b64_tr_b16 a[252:253], v218 offset:45848\n"
"v_fma_mix_f32 v187, v125, v163, v171 op_sel:[1,0,0] op_sel_hi:[1,0,0]\n"
"v_mfma_f32_16x16x32_bf16 a[84:87], a[180:183], v[108:111], a[84:87]\n"
"ds_read_b64_tr_b16 a[254:255], v218 offset:46872\n"
"v_fma_mix_f32 v188, v126, v164, v172 op_sel:[0,0,0] op_sel_hi:[1,0,0]\n"
"v_mfma_f32_16x16x32_bf16 v[236:239], a[184:187], v[96:99], v[236:239]\n"
"ds_read_b64_tr_b16 a[160:161], v219 offset:0\n"
"v_fma_mix_f32 v189, v126, v165, v173 op_sel:[1,0,0] op_sel_hi:[1,0,0]\n"
"v_mfma_f32_16x16x32_bf16 v[236:239], a[188:191], v[104:107], v[236:239]\n"
"ds_read_b64_tr_b16 a[162:163], v219 offset:1024\n"
"v_fma_mix_f32 v190, v127, v166, v174 op_sel:[0,0,0] op_sel_hi:[1,0,0]\n"
"v_mfma_f32_16x16x32_bf16 v[232:235], a[184:187], v[100:103], v[232:235]\n"
"ds_read_b64_tr_b16 a[164:165], v219 offset:4096\n"
"v_fma_mix_f32 v191, v127, v167, v175 op_sel:[1,0,0] op_sel_hi:[1,0,0]\n"
"v_mfma_f32_16x16x32_bf16 v[232:235], a[188:191], v[108:111], v[232:235]\n"
"s_waitcnt lgkmcnt(11)\n"
"buffer_load_dwordx4 v247, s[12:15], s84 offen offset:3072 lds\n"
"ds_read_b64_tr_b16 a[166:167], v219 offset:5120\n"
"v_mfma_f32_16x16x32_bf16 v[228:231], a[224:227], v[96:99], v[228:231]\n"
"s_add_u32 s84, s84, 16384\n"
"ds_read_b64_tr_b16 a[168:169], v219 offset:8\n"
"ds_read_b128 v[160:163], v224 offset:4352\n"
"v_mfma_f32_16x16x32_bf16 v[228:231], a[228:231], v[104:107], v[228:231]\n"
"ds_read_b64_tr_b16 a[170:171], v219 offset:1032\n"
"v_cvt_pk_bf16_f32 v124, v184, v185\n"
"v_mfma_f32_16x16x32_bf16 v[220:223], a[224:227], v[100:103], v[220:223]\n"
"ds_read_b64_tr_b16 a[172:173], v219 offset:4104\n"
"v_cvt_pk_bf16_f32 v125, v186, v187\n"
"v_mfma_f32_16x16x32_bf16 v[220:223], a[228:231], v[108:111], v[220:223]\n"
"ds_read_b64_tr_b16 a[174:175], v219 offset:5128\n"
"ds_read_b128 v[164:167], v224 offset:4416\n"
"v_cvt_pk_bf16_f32 v126, v188, v189\n"
"v_mfma_f32_16x16x32_bf16 v[212:215], a[232:235], v[96:99], v[212:215]\n"
"ds_read_b64_tr_b16 a[176:177], v219 offset:256\n"
"v_cvt_pk_bf16_f32 v127, v190, v191\n"
"v_mfma_f32_16x16x32_bf16 v[212:215], a[236:239], v[104:107], v[212:215]\n"
"s_waitcnt vmcnt(9)\n"
"ds_read_b64_tr_b16 a[178:179], v219 offset:1280\n"
"v_fma_mix_f32 v176, v128, v144, v152 op_sel:[0,0,0] op_sel_hi:[1,0,0]\n"
"v_mfma_f32_16x16x32_bf16 v[208:211], a[232:235], v[100:103], v[208:211]\n"
"ds_read_b64_tr_b16 a[180:181], v219 offset:4352\n"
"ds_read_b128 v[168:171], v224 offset:4480\n"
"v_mfma_f32_16x16x32_bf16 v[208:211], a[236:239], v[108:111], v[208:211]\n"
"s_waitcnt lgkmcnt(14)\n"
"ds_read_b64_tr_b16 a[182:183], v219 offset:5376\n"
"v_fma_mix_f32 v177, v128, v145, v153 op_sel:[1,0,0] op_sel_hi:[1,0,0]\n"
"v_mfma_f32_16x16x32_bf16 v[204:207], a[240:243], v[96:99], v[204:207]\n"
"ds_read_b64_tr_b16 a[184:185], v219 offset:264\n"
"v_fma_mix_f32 v178, v129, v146, v154 op_sel:[0,0,0] op_sel_hi:[1,0,0]\n"
"v_mfma_f32_16x16x32_bf16 v[204:207], a[244:247], v[104:107], v[204:207]\n"
"ds_read_b64_tr_b16 a[186:187], v219 offset:1288\n"
"ds_read_b128 v[172:175], v224 offset:4544\n"
"v_mfma_f32_16x16x32_bf16 v[200:203], a[240:243], v[100:103], v[200:203]\n"
"ds_read_b64_tr_b16 a[188:189], v219 offset:4360\n"
"v_fma_mix_f32 v179, v129, v147, v155 op_sel:[1,0,0] op_sel_hi:[1,0,0]\n"
"v_mfma_f32_16x16x32_bf16 v[200:203], a[244:247], v[108:111], v[200:203]\n"
"ds_read_b64_tr_b16 a[190:191], v219 offset:5384\n"
"v_fma_mix_f32 v180, v130, v148, v156 op_sel:[0,0,0] op_sel_hi:[1,0,0]\n"
"v_mfma_f32_16x16x32_bf16 v[196:199], a[248:251], v[96:99], v[196:199]\n"
"ds_read_b64_tr_b16 a[224:225], v219 offset:512\n"
"v_fma_mix_f32 v181, v130, v149, v157 op_sel:[1,0,0] op_sel_hi:[1,0,0]\n"
"v_mfma_f32_16x16x32_bf16 v[196:199], a[252:255], v[104:107], v[196:199]\n"
"ds_read_b64_tr_b16 a[226:227], v219 offset:1536\n"
"v_fma_mix_f32 v182, v131, v150, v158 op_sel:[0,0,0] op_sel_hi:[1,0,0]\n"
"v_mfma_f32_16x16x32_bf16 v[192:195], a[248:251], v[100:103], v[192:195]\n"
"ds_read_b64_tr_b16 a[228:229], v219 offset:4608\n"
"v_fma_mix_f32 v183, v131, v151, v159 op_sel:[1,0,0] op_sel_hi:[1,0,0]\n"
"v_mfma_f32_16x16x32_bf16 v[192:195], a[252:255], v[108:111], v[192:195]\n"
"s_waitcnt lgkmcnt(13)\n"
"ds_read_b64_tr_b16 a[230:231], v219 offset:5632\n"
"v_cvt_pk_bf16_f32 v128, v176, v177\n"
"v_mfma_f32_16x16x32_bf16 a[0:3], a[160:163], v[112:115], a[0:3]\n"
"ds_read_b64_tr_b16 a[232:233], v219 offset:520\n"
"v_cvt_pk_bf16_f32 v129, v178, v179\n"
"v_cvt_pk_bf16_f32 v130, v180, v181\n"
"v_mfma_f32_16x16x32_bf16 a[0:3], a[164:167], v[120:123], a[0:3]\n"
"s_waitcnt vmcnt(8)\n"
"ds_read_b64_tr_b16 a[234:235], v219 offset:1544\n"
"v_cvt_pk_bf16_f32 v131, v182, v183\n"
"v_mfma_f32_16x16x32_bf16 a[4:7], a[160:163], v[116:119], a[4:7]\n"
"s_barrier\n"
"ds_read_b64_tr_b16 a[236:237], v219 offset:4616\n"
"v_fma_mix_f32 v184, v132, v144, v152 op_sel:[0,0,0] op_sel_hi:[1,0,0]\n"
"v_mfma_f32_16x16x32_bf16 a[4:7], a[164:167], v[124:127], a[4:7]\n"
"ds_read_b64_tr_b16 a[238:239], v219 offset:5640\n"
"v_fma_mix_f32 v185, v132, v145, v153 op_sel:[1,0,0] op_sel_hi:[1,0,0]\n"
"v_mfma_f32_16x16x32_bf16 a[8:11], a[168:171], v[112:115], a[8:11]\n"
"s_add_u32 m0, s86, 32768\n"
"ds_read_b64_tr_b16 a[240:241], v219 offset:768\n"
"v_fma_mix_f32 v186, v133, v146, v154 op_sel:[0,0,0] op_sel_hi:[1,0,0]\n"
"v_mfma_f32_16x16x32_bf16 a[8:11], a[172:175], v[120:123], a[8:11]\n"
"ds_read_b64_tr_b16 a[242:243], v219 offset:1792\n"
"buffer_load_dwordx4 v246, s[8:11], s84 offen offset:0 lds\n"
"v_mfma_f32_16x16x32_bf16 a[12:15], a[168:171], v[116:119], a[12:15]\n"
"ds_read_b64_tr_b16 a[244:245], v219 offset:4864\n"
"v_fma_mix_f32 v187, v133, v147, v155 op_sel:[1,0,0] op_sel_hi:[1,0,0]\n"
"v_mfma_f32_16x16x32_bf16 a[12:15], a[172:175], v[124:127], a[12:15]\n"
"s_waitcnt lgkmcnt(11)\n"
"ds_read_b64_tr_b16 a[246:247], v219 offset:5888\n"
"v_fma_mix_f32 v188, v134, v148, v156 op_sel:[0,0,0] op_sel_hi:[1,0,0]\n"
"v_mfma_f32_16x16x32_bf16 a[16:19], a[176:179], v[112:115], a[16:19]\n"
"ds_read_b64_tr_b16 a[248:249], v219 offset:776\n"
"v_fma_mix_f32 v189, v134, v149, v157 op_sel:[1,0,0] op_sel_hi:[1,0,0]\n"
"v_mfma_f32_16x16x32_bf16 a[16:19], a[180:183], v[120:123], a[16:19]\n"
"ds_read_b64_tr_b16 a[250:251], v219 offset:1800\n"
"v_fma_mix_f32 v190, v135, v150, v158 op_sel:[0,0,0] op_sel_hi:[1,0,0]\n"
"v_mfma_f32_16x16x32_bf16 a[20:23], a[176:179], v[116:119], a[20:23]\n"
"ds_read_b64_tr_b16 a[252:253], v219 offset:4872\n"
"v_fma_mix_f32 v191, v135, v151, v159 op_sel:[1,0,0] op_sel_hi:[1,0,0]\n"
"v_mfma_f32_16x16x32_bf16 a[20:23], a[180:183], v[124:127], a[20:23]\n"
"ds_read_b64_tr_b16 a[254:255], v219 offset:5896\n"
"buffer_load_dwordx4 v246, s[8:11], s84 offen offset:1024 lds\n"
"v_mfma_f32_16x16x32_bf16 a[24:27], a[184:187], v[112:115], a[24:27]\n"
"ds_read_b64_tr_b16 a[160:161], v219 offset:8192\n"
"v_cvt_pk_bf16_f32 v132, v184, v185\n"
"v_mfma_f32_16x16x32_bf16 a[24:27], a[188:191], v[120:123], a[24:27]\n"
"ds_read_b64_tr_b16 a[162:163], v219 offset:9216\n"
"v_cvt_pk_bf16_f32 v133, v186, v187\n"
"v_cvt_pk_bf16_f32 v134, v188, v189\n"
"v_mfma_f32_16x16x32_bf16 a[28:31], a[184:187], v[116:119], a[28:31]\n"
"ds_read_b64_tr_b16 a[164:165], v219 offset:12288\n"
"v_cvt_pk_bf16_f32 v135, v190, v191\n"
"v_mfma_f32_16x16x32_bf16 a[28:31], a[188:191], v[124:127], a[28:31]\n"
"s_waitcnt lgkmcnt(11)\n"
"ds_read_b64_tr_b16 a[166:167], v219 offset:13312\n"
"v_fma_mix_f32 v176, v136, v160, v168 op_sel:[0,0,0] op_sel_hi:[1,0,0]\n"
"v_mfma_f32_16x16x32_bf16 a[32:35], a[224:227], v[112:115], a[32:35]\n"
"ds_read_b64_tr_b16 a[168:169], v219 offset:8200\n"
"v_fma_mix_f32 v177, v136, v161, v169 op_sel:[1,0,0] op_sel_hi:[1,0,0]\n"
"v_mfma_f32_16x16x32_bf16 a[32:35], a[228:231], v[120:123], a[32:35]\n"
"ds_read_b64_tr_b16 a[170:171], v219 offset:9224\n"
"buffer_load_dwordx4 v247, s[8:11], s84 offen offset:2048 lds\n"
"v_mfma_f32_16x16x32_bf16 a[36:39], a[224:227], v[116:119], a[36:39]\n"
"ds_read_b64_tr_b16 a[172:173], v219 offset:12296\n"
"v_fma_mix_f32 v178, v137, v162, v170 op_sel:[0,0,0] op_sel_hi:[1,0,0]\n"
"v_mfma_f32_16x16x32_bf16 a[36:39], a[228:231], v[124:127], a[36:39]\n"
"ds_read_b64_tr_b16 a[174:175], v219 offset:13320\n"
"v_fma_mix_f32 v179, v137, v163, v171 op_sel:[1,0,0] op_sel_hi:[1,0,0]\n"
"v_mfma_f32_16x16x32_bf16 a[40:43], a[232:235], v[112:115], a[40:43]\n"
"ds_read_b64_tr_b16 a[176:177], v219 offset:8448\n"
"v_fma_mix_f32 v180, v138, v164, v172 op_sel:[0,0,0] op_sel_hi:[1,0,0]\n"
"v_mfma_f32_16x16x32_bf16 a[40:43], a[236:239], v[120:123], a[40:43]\n"
"ds_read_b64_tr_b16 a[178:179], v219 offset:9472\n"
"v_fma_mix_f32 v181, v138, v165, v173 op_sel:[1,0,0] op_sel_hi:[1,0,0]\n"
"v_mfma_f32_16x16x32_bf16 a[44:47], a[232:235], v[116:119], a[44:47]\n"
"ds_read_b64_tr_b16 a[180:181], v219 offset:12544\n"
"v_fma_mix_f32 v182, v139, v166, v174 op_sel:[0,0,0] op_sel_hi:[1,0,0]\n"
"v_mfma_f32_16x16x32_bf16 a[44:47], a[236:239], v[124:127], a[44:47]\n"
"s_waitcnt lgkmcnt(11)\n"
"ds_read_b64_tr_b16 a[182:183], v219 offset:13568\n"
"buffer_load_dwordx4 v247, s[8:11], s84 offen offset:3072 lds\n"
"v_mfma_f32_16x16x32_bf16 a[48:51], a[240:243], v[112:115], a[48:51]\n"
"ds_read_b64_tr_b16 a[184:185], v219 offset:8456\n"
"v_fma_mix_f32 v183, v139, v167, v175 op_sel:[1,0,0] op_sel_hi:[1,0,0]\n"
"v_mfma_f32_16x16x32_bf16 a[48:51], a[244:247], v[120:123], a[48:51]\n"
"ds_read_b64_tr_b16 a[186:187], v219 offset:9480\n"
"v_cvt_pk_bf16_f32 v136, v176, v177\n"
"v_mfma_f32_16x16x32_bf16 a[52:55], a[240:243], v[116:119], a[52:55]\n"
"ds_read_b64_tr_b16 a[188:189], v219 offset:12552\n"
"v_cvt_pk_bf16_f32 v137, v178, v179\n"
"v_cvt_pk_bf16_f32 v138, v180, v181\n"
"v_mfma_f32_16x16x32_bf16 a[52:55], a[244:247], v[124:127], a[52:55]\n"
"ds_read_b64_tr_b16 a[190:191], v219 offset:13576\n"
"v_cvt_pk_bf16_f32 v139, v182, v183\n"
"v_mfma_f32_16x16x32_bf16 a[56:59], a[248:251], v[112:115], a[56:59]\n"
"s_add_u32 m0, s86, 49152\n"
"ds_read_b64_tr_b16 a[224:225], v219 offset:8704\n"
"v_fma_mix_f32 v184, v140, v160, v168 op_sel:[0,0,0] op_sel_hi:[1,0,0]\n"
"v_mfma_f32_16x16x32_bf16 a[56:59], a[252:255], v[120:123], a[56:59]\n"
"ds_read_b64_tr_b16 a[226:227], v219 offset:9728\n"
"buffer_load_dwordx4 v246, s[12:15], s84 offen offset:0 lds\n"
"v_mfma_f32_16x16x32_bf16 a[60:63], a[248:251], v[116:119], a[60:63]\n"
"ds_read_b64_tr_b16 a[228:229], v219 offset:12800\n"
"v_fma_mix_f32 v185, v140, v161, v169 op_sel:[1,0,0] op_sel_hi:[1,0,0]\n"
"v_mfma_f32_16x16x32_bf16 a[60:63], a[252:255], v[124:127], a[60:63]\n"
"s_waitcnt lgkmcnt(11)\n"
"ds_read_b64_tr_b16 a[230:231], v219 offset:13824\n"
"v_fma_mix_f32 v186, v141, v162, v170 op_sel:[0,0,0] op_sel_hi:[1,0,0]\n"
"v_mfma_f32_16x16x32_bf16 a[64:67], a[160:163], v[112:115], a[64:67]\n"
"ds_read_b64_tr_b16 a[232:233], v219 offset:8712\n"
"v_fma_mix_f32 v187, v141, v163, v171 op_sel:[1,0,0] op_sel_hi:[1,0,0]\n"
"v_mfma_f32_16x16x32_bf16 a[64:67], a[164:167], v[120:123], a[64:67]\n"
"ds_read_b64_tr_b16 a[234:235], v219 offset:9736\n"
"v_fma_mix_f32 v188, v142, v164, v172 op_sel:[0,0,0] op_sel_hi:[1,0,0]\n"
"v_mfma_f32_16x16x32_bf16 a[68:71], a[160:163], v[116:119], a[68:71]\n"
"ds_read_b64_tr_b16 a[236:237], v219 offset:12808\n"
"v_fma_mix_f32 v189, v142, v165, v173 op_sel:[1,0,0] op_sel_hi:[1,0,0]\n"
"v_mfma_f32_16x16x32_bf16 a[68:71], a[164:167], v[124:127], a[68:71]\n"
"ds_read_b64_tr_b16 a[238:239], v219 offset:13832\n"
"buffer_load_dwordx4 v247, s[12:15], s84 offen offset:1024 lds\n"
"v_mfma_f32_16x16x32_bf16 a[72:75], a[168:171], v[112:115], a[72:75]\n"
"ds_read_b64_tr_b16 a[240:241], v219 offset:8960\n"
"v_fma_mix_f32 v190, v143, v166, v174 op_sel:[0,0,0] op_sel_hi:[1,0,0]\n"
"v_mfma_f32_16x16x32_bf16 a[72:75], a[172:175], v[120:123], a[72:75]\n"
"ds_read_b64_tr_b16 a[242:243], v219 offset:9984\n"
"v_fma_mix_f32 v191, v143, v167, v175 op_sel:[1,0,0] op_sel_hi:[1,0,0]\n"
"v_mfma_f32_16x16x32_bf16 a[76:79], a[168:171], v[116:119], a[76:79]\n"
"ds_read_b64_tr_b16 a[244:245], v219 offset:13056\n"
"v_cvt_pk_bf16_f32 v140, v184, v185\n"
"v_mfma_f32_16x16x32_bf16 a[76:79], a[172:175], v[124:127], a[76:79]\n"
"s_waitcnt lgkmcnt(11)\n"
"ds_read_b64_tr_b16 a[246:247], v219 offset:14080\n"
"v_cvt_pk_bf16_f32 v141, v186, v187\n"
"v_cvt_pk_bf16_f32 v142, v188, v189\n"
"v_mfma_f32_16x16x32_bf16 a[80:83], a[176:179], v[112:115], a[80:83]\n"
"ds_read_b64_tr_b16 a[248:249], v219 offset:8968\n"
"v_cvt_pk_bf16_f32 v143, v190, v191\n"
"v_mfma_f32_16x16x32_bf16 a[80:83], a[180:183], v[120:123], a[80:83]\n"
"ds_read_b64_tr_b16 a[250:251], v219 offset:9992\n"
"buffer_load_dwordx4 v246, s[12:15], s84 offen offset:2048 lds\n"
"v_mfma_f32_16x16x32_bf16 a[84:87], a[176:179], v[116:119], a[84:87]\n"
"ds_read_b64_tr_b16 a[252:253], v219 offset:13064\n"
"v_mfma_f32_16x16x32_bf16 a[84:87], a[180:183], v[124:127], a[84:87]\n"
"ds_read_b64_tr_b16 a[254:255], v219 offset:14088\n"
"v_mfma_f32_16x16x32_bf16 v[236:239], a[184:187], v[112:115], v[236:239]\n"
"ds_read_b64_tr_b16 a[160:161], v219 offset:32784\n"
"v_mfma_f32_16x16x32_bf16 v[236:239], a[188:191], v[120:123], v[236:239]\n"
"ds_read_b64_tr_b16 a[162:163], v219 offset:33808\n"
"v_mfma_f32_16x16x32_bf16 v[232:235], a[184:187], v[116:119], v[232:235]\n"
"ds_read_b64_tr_b16 a[164:165], v219 offset:36880\n"
"v_mfma_f32_16x16x32_bf16 v[232:235], a[188:191], v[124:127], v[232:235]\n"
"s_waitcnt lgkmcnt(11)\n"
"buffer_load_dwordx4 v247, s[12:15], s84 offen offset:3072 lds\n"
"ds_read_b64_tr_b16 a[166:167], v219 offset:37904\n"
"v_mfma_f32_16x16x32_bf16 v[228:231], a[224:227], v[112:115], v[228:231]\n"
"s_add_u32 s84, s84, 16384\n"
"ds_read_b64_tr_b16 a[168:169], v219 offset:32792\n"
"v_mfma_f32_16x16x32_bf16 v[228:231], a[228:231], v[120:123], v[228:231]\n"
"ds_read_b64_tr_b16 a[170:171], v219 offset:33816\n"
"v_mfma_f32_16x16x32_bf16 v[220:223], a[224:227], v[116:119], v[220:223]\n"
"ds_read_b64_tr_b16 a[172:173], v219 offset:36888\n"
"v_mfma_f32_16x16x32_bf16 v[220:223], a[228:231], v[124:127], v[220:223]\n"
"ds_read_b64_tr_b16 a[174:175], v219 offset:37912\n"
"v_mfma_f32_16x16x32_bf16 v[212:215], a[232:235], v[112:115], v[212:215]\n"
"ds_read_b64_tr_b16 a[176:177], v219 offset:33040\n"
"v_mfma_f32_16x16x32_bf16 v[212:215], a[236:239], v[120:123], v[212:215]\n"
"ds_read_b64_tr_b16 a[178:179], v219 offset:34064\n"
"v_mfma_f32_16x16x32_bf16 v[208:211], a[232:235], v[116:119], v[208:211]\n"
"ds_read_b64_tr_b16 a[180:181], v219 offset:37136\n"
"v_mfma_f32_16x16x32_bf16 v[208:211], a[236:239], v[124:127], v[208:211]\n"
"s_waitcnt lgkmcnt(11)\n"
"ds_read_b64_tr_b16 a[182:183], v219 offset:38160\n"
"v_mfma_f32_16x16x32_bf16 v[204:207], a[240:243], v[112:115], v[204:207]\n"
"ds_read_b64_tr_b16 a[184:185], v219 offset:33048\n"
"v_mfma_f32_16x16x32_bf16 v[204:207], a[244:247], v[120:123], v[204:207]\n"
"ds_read_b64_tr_b16 a[186:187], v219 offset:34072\n"
"v_mfma_f32_16x16x32_bf16 v[200:203], a[240:243], v[116:119], v[200:203]\n"
"ds_read_b64_tr_b16 a[188:189], v219 offset:37144\n"
"v_mfma_f32_16x16x32_bf16 v[200:203], a[244:247], v[124:127], v[200:203]\n"
"ds_read_b64_tr_b16 a[190:191], v219 offset:38168\n"
"v_mfma_f32_16x16x32_bf16 v[196:199], a[248:251], v[112:115], v[196:199]\n"
"ds_read_b64_tr_b16 a[224:225], v219 offset:33296\n"
"v_mfma_f32_16x16x32_bf16 v[196:199], a[252:255], v[120:123], v[196:199]\n"
"ds_read_b64_tr_b16 a[226:227], v219 offset:34320\n"
"v_mfma_f32_16x16x32_bf16 v[192:195], a[248:251], v[116:119], v[192:195]\n"
"ds_read_b64_tr_b16 a[228:229], v219 offset:37392\n"
"v_mfma_f32_16x16x32_bf16 v[192:195], a[252:255], v[124:127], v[192:195]\n"
"s_waitcnt lgkmcnt(11)\n"
"ds_read_b64_tr_b16 a[230:231], v219 offset:38416\n"
"v_mfma_f32_16x16x32_bf16 a[0:3], a[160:163], v[128:131], a[0:3]\n"
"ds_read_b64_tr_b16 a[232:233], v219 offset:33304\n"
"v_mfma_f32_16x16x32_bf16 a[0:3], a[164:167], v[136:139], a[0:3]\n"
"s_waitcnt vmcnt(8)\n"
"ds_read_b64_tr_b16 a[234:235], v219 offset:34328\n"
"v_mfma_f32_16x16x32_bf16 a[4:7], a[160:163], v[132:135], a[4:7]\n"
"s_barrier\n"
"ds_read_b64_tr_b16 a[236:237], v219 offset:37400\n"
"v_mfma_f32_16x16x32_bf16 a[4:7], a[164:167], v[140:143], a[4:7]\n"
"ds_read_b64_tr_b16 a[238:239], v219 offset:38424\n"
"v_mfma_f32_16x16x32_bf16 a[8:11], a[168:171], v[128:131], a[8:11]\n"
"s_add_u32 m0, s86, 65536\n"
"ds_read_b64_tr_b16 a[240:241], v219 offset:33552\n"
"v_mfma_f32_16x16x32_bf16 a[8:11], a[172:175], v[136:139], a[8:11]\n"
"ds_read_b64_tr_b16 a[242:243], v219 offset:34576\n"
"buffer_load_dwordx4 v246, s[8:11], s84 offen offset:0 lds\n"
"v_mfma_f32_16x16x32_bf16 a[12:15], a[168:171], v[132:135], a[12:15]\n"
"ds_read_b64_tr_b16 a[244:245], v219 offset:37648\n"
"v_accvgpr_read_b32 v176, a0\n"
"v_mfma_f32_16x16x32_bf16 a[12:15], a[172:175], v[140:143], a[12:15]\n"
"s_waitcnt lgkmcnt(11)\n"
"ds_read_b64_tr_b16 a[246:247], v219 offset:38672\n"
"v_accvgpr_read_b32 v177, a1\n"
"v_mfma_f32_16x16x32_bf16 a[16:19], a[176:179], v[128:131], a[16:19]\n"
"ds_read_b64_tr_b16 a[248:249], v219 offset:33560\n"
"v_accvgpr_read_b32 v178, a2\n"
"v_mfma_f32_16x16x32_bf16 a[16:19], a[180:183], v[136:139], a[16:19]\n"
"ds_read_b64_tr_b16 a[250:251], v219 offset:34584\n"
"v_accvgpr_read_b32 v179, a3\n"
"v_mfma_f32_16x16x32_bf16 a[20:23], a[176:179], v[132:135], a[20:23]\n"
"ds_read_b64_tr_b16 a[252:253], v219 offset:37656\n"
"v_cvt_pk_bf16_f32 v64, v176, v177\n"
"v_mfma_f32_16x16x32_bf16 a[20:23], a[180:183], v[140:143], a[20:23]\n"
"ds_read_b64_tr_b16 a[254:255], v219 offset:38680\n"
"buffer_load_dwordx4 v246, s[8:11], s84 offen offset:1024 lds\n"
"v_mfma_f32_16x16x32_bf16 a[24:27], a[184:187], v[128:131], a[24:27]\n"
"ds_read_b64_tr_b16 a[160:161], v219 offset:40976\n"
"v_cvt_pk_bf16_f32 v65, v178, v179\n"
"v_mfma_f32_16x16x32_bf16 a[24:27], a[188:191], v[136:139], a[24:27]\n"
"ds_read_b64_tr_b16 a[162:163], v219 offset:42000\n"
"v_accvgpr_read_b32 v180, a4\n"
"v_mfma_f32_16x16x32_bf16 a[28:31], a[184:187], v[132:135], a[28:31]\n"
"ds_read_b64_tr_b16 a[164:165], v219 offset:45072\n"
"v_accvgpr_read_b32 v181, a5\n"
"v_mfma_f32_16x16x32_bf16 a[28:31], a[188:191], v[140:143], a[28:31]\n"
"s_waitcnt lgkmcnt(11)\n"
"ds_read_b64_tr_b16 a[166:167], v219 offset:46096\n"
"v_accvgpr_read_b32 v182, a6\n"
"v_mfma_f32_16x16x32_bf16 a[32:35], a[224:227], v[128:131], a[32:35]\n"
"ds_read_b64_tr_b16 a[168:169], v219 offset:40984\n"
"v_accvgpr_read_b32 v183, a7\n"
"v_mfma_f32_16x16x32_bf16 a[32:35], a[228:231], v[136:139], a[32:35]\n"
"ds_read_b64_tr_b16 a[170:171], v219 offset:42008\n"
"buffer_load_dwordx4 v247, s[8:11], s84 offen offset:2048 lds\n"
"v_mfma_f32_16x16x32_bf16 a[36:39], a[224:227], v[132:135], a[36:39]\n"
"ds_read_b64_tr_b16 a[172:173], v219 offset:45080\n"
"v_cvt_pk_bf16_f32 v96, v180, v181\n"
"v_mfma_f32_16x16x32_bf16 a[36:39], a[228:231], v[140:143], a[36:39]\n"
"ds_read_b64_tr_b16 a[174:175], v219 offset:46104\n"
"v_cvt_pk_bf16_f32 v97, v182, v183\n"
"v_mfma_f32_16x16x32_bf16 a[40:43], a[232:235], v[128:131], a[40:43]\n"
"ds_read_b64_tr_b16 a[176:177], v219 offset:41232\n"
"v_accvgpr_read_b32 v184, a8\n"
"v_mfma_f32_16x16x32_bf16 a[40:43], a[236:239], v[136:139], a[40:43]\n"
"ds_read_b64_tr_b16 a[178:179], v219 offset:42256\n"
"v_accvgpr_read_b32 v185, a9\n"
"v_mfma_f32_16x16x32_bf16 a[44:47], a[232:235], v[132:135], a[44:47]\n"
"ds_read_b64_tr_b16 a[180:181], v219 offset:45328\n"
"v_accvgpr_read_b32 v186, a10\n"
"v_mfma_f32_16x16x32_bf16 a[44:47], a[236:239], v[140:143], a[44:47]\n"
"s_waitcnt lgkmcnt(11)\n"
"ds_read_b64_tr_b16 a[182:183], v219 offset:46352\n"
"buffer_load_dwordx4 v247, s[8:11], s84 offen offset:3072 lds\n"
"v_mfma_f32_16x16x32_bf16 a[48:51], a[240:243], v[128:131], a[48:51]\n"
"ds_read_b64_tr_b16 a[184:185], v219 offset:41240\n"
"v_accvgpr_read_b32 v187, a11\n"
"v_mfma_f32_16x16x32_bf16 a[48:51], a[244:247], v[136:139], a[48:51]\n"
"ds_read_b64_tr_b16 a[186:187], v219 offset:42264\n"
"v_cvt_pk_bf16_f32 v66, v184, v185\n"
"v_mfma_f32_16x16x32_bf16 a[52:55], a[240:243], v[132:135], a[52:55]\n"
"ds_read_b64_tr_b16 a[188:189], v219 offset:45336\n"
"v_cvt_pk_bf16_f32 v67, v186, v187\n"
"v_mfma_f32_16x16x32_bf16 a[52:55], a[244:247], v[140:143], a[52:55]\n"
"ds_read_b64_tr_b16 a[190:191], v219 offset:46360\n"
"v_accvgpr_read_b32 v188, a12\n"
"v_mfma_f32_16x16x32_bf16 a[56:59], a[248:251], v[128:131], a[56:59]\n"
"s_add_u32 m0, s86, 81920\n"
"ds_read_b64_tr_b16 a[224:225], v219 offset:41488\n"
"v_accvgpr_read_b32 v189, a13\n"
"v_mfma_f32_16x16x32_bf16 a[56:59], a[252:255], v[136:139], a[56:59]\n"
"ds_read_b64_tr_b16 a[226:227], v219 offset:42512\n"
"buffer_load_dwordx4 v246, s[12:15], s84 offen offset:0 lds\n"
"v_mfma_f32_16x16x32_bf16 a[60:63], a[248:251], v[132:135], a[60:63]\n"
"ds_read_b64_tr_b16 a[228:229], v219 offset:45584\n"
"v_accvgpr_read_b32 v190, a14\n"
"v_mfma_f32_16x16x32_bf16 a[60:63], a[252:255], v[140:143], a[60:63]\n"
"s_waitcnt lgkmcnt(11)\n"
"ds_read_b64_tr_b16 a[230:231], v219 offset:46608\n"
"v_accvgpr_read_b32 v191, a15\n"
"v_mfma_f32_16x16x32_bf16 a[64:67], a[160:163], v[128:131], a[64:67]\n"
"ds_read_b64_tr_b16 a[232:233], v219 offset:41496\n"
"v_cvt_pk_bf16_f32 v98, v188, v189\n"
"v_mfma_f32_16x16x32_bf16 a[64:67], a[164:167], v[136:139], a[64:67]\n"
"ds_read_b64_tr_b16 a[234:235], v219 offset:42520\n"
"v_cvt_pk_bf16_f32 v99, v190, v191\n"
"v_mfma_f32_16x16x32_bf16 a[68:71], a[160:163], v[132:135], a[68:71]\n"
"ds_read_b64_tr_b16 a[236:237], v219 offset:45592\n"
"v_accvgpr_read_b32 v176, a16\n"
"v_mfma_f32_16x16x32_bf16 a[68:71], a[164:167], v[140:143], a[68:71]\n"
"ds_read_b64_tr_b16 a[238:239], v219 offset:46616\n"
"buffer_load_dwordx4 v247, s[12:15], s84 offen offset:1024 lds\n"
"v_mfma_f32_16x16x32_bf16 a[72:75], a[168:171], v[128:131], a[72:75]\n"
"ds_read_b64_tr_b16 a[240:241], v219 offset:41744\n"
"v_accvgpr_read_b32 v177, a17\n"
"v_mfma_f32_16x16x32_bf16 a[72:75], a[172:175], v[136:139], a[72:75]\n"
"ds_read_b64_tr_b16 a[242:243], v219 offset:42768\n"
"v_accvgpr_read_b32 v178, a18\n"
"v_mfma_f32_16x16x32_bf16 a[76:79], a[168:171], v[132:135], a[76:79]\n"
"ds_read_b64_tr_b16 a[244:245], v219 offset:45840\n"
"v_accvgpr_read_b32 v179, a19\n"
"v_mfma_f32_16x16x32_bf16 a[76:79], a[172:175], v[140:143], a[76:79]\n"
"s_waitcnt lgkmcnt(11)\n"
"ds_read_b64_tr_b16 a[246:247], v219 offset:46864\n"
"v_cvt_pk_bf16_f32 v68, v176, v177\n"
"v_mfma_f32_16x16x32_bf16 a[80:83], a[176:179], v[128:131], a[80:83]\n"
"ds_read_b64_tr_b16 a[248:249], v219 offset:41752\n"
"v_cvt_pk_bf16_f32 v69, v178, v179\n"
"v_mfma_f32_16x16x32_bf16 a[80:83], a[180:183], v[136:139], a[80:83]\n"
"ds_read_b64_tr_b16 a[250:251], v219 offset:42776\n"
"buffer_load_dwordx4 v246, s[12:15], s84 offen offset:2048 lds\n"
"v_mfma_f32_16x16x32_bf16 a[84:87], a[176:179], v[132:135], a[84:87]\n"
"ds_read_b64_tr_b16 a[252:253], v219 offset:45848\n"
"v_accvgpr_read_b32 v180, a20\n"
"v_mfma_f32_16x16x32_bf16 a[84:87], a[180:183], v[140:143], a[84:87]\n"
"ds_read_b64_tr_b16 a[254:255], v219 offset:46872\n"
"v_accvgpr_read_b32 v181, a21\n"
"v_mfma_f32_16x16x32_bf16 v[236:239], a[184:187], v[128:131], v[236:239]\n"
"ds_read_b128 a[160:163], v254 offset:0\n"
"v_accvgpr_read_b32 v182, a22\n"
"v_mfma_f32_16x16x32_bf16 v[236:239], a[188:191], v[136:139], v[236:239]\n"
"ds_read_b128 a[164:167], v254 offset:256\n"
"v_accvgpr_read_b32 v183, a23\n"
"v_mfma_f32_16x16x32_bf16 v[232:235], a[184:187], v[132:135], v[232:235]\n"
"ds_read_b128 a[168:171], v254 offset:512\n"
"v_cvt_pk_bf16_f32 v100, v180, v181\n"
"v_mfma_f32_16x16x32_bf16 v[232:235], a[188:191], v[140:143], v[232:235]\n"
"s_waitcnt lgkmcnt(11)\n"
"buffer_load_dwordx4 v247, s[12:15], s84 offen offset:3072 lds\n"
"ds_read_b128 a[172:175], v254 offset:768\n"
"v_mfma_f32_16x16x32_bf16 v[228:231], a[224:227], v[128:131], v[228:231]\n"
"s_add_u32 s84, s84, 16384\n"
"ds_read_b128 a[128:131], v240 offset:0\n"
"v_cvt_pk_bf16_f32 v101, v182, v183\n"
"v_mfma_f32_16x16x32_bf16 v[228:231], a[228:231], v[136:139], v[228:231]\n"
"ds_read_b128 a[132:135], v240 offset:256\n"
"v_accvgpr_read_b32 v184, a24\n"
"v_mfma_f32_16x16x32_bf16 v[220:223], a[224:227], v[132:135], v[220:223]\n"
"ds_read_b128 a[136:139], v240 offset:512\n"
"v_accvgpr_read_b32 v185, a25\n"
"v_mfma_f32_16x16x32_bf16 v[220:223], a[228:231], v[140:143], v[220:223]\n"
"ds_read_b128 a[140:143], v240 offset:768\n"
"v_accvgpr_read_b32 v186, a26\n"
"v_mfma_f32_16x16x32_bf16 v[212:215], a[232:235], v[128:131], v[212:215]\n"
"ds_read_b128 a[144:147], v240 offset:4096\n"
"v_accvgpr_read_b32 v187, a27\n"
"v_mfma_f32_16x16x32_bf16 v[212:215], a[236:239], v[136:139], v[212:215]\n"
"ds_read_b128 a[148:151], v240 offset:4352\n"
"v_cvt_pk_bf16_f32 v70, v184, v185\n"
"v_mfma_f32_16x16x32_bf16 v[208:211], a[232:235], v[132:135], v[208:211]\n"
"ds_read_b128 a[152:155], v240 offset:4608\n"
"v_cvt_pk_bf16_f32 v71, v186, v187\n"
"v_mfma_f32_16x16x32_bf16 v[208:211], a[236:239], v[140:143], v[208:211]\n"
"s_waitcnt lgkmcnt(11)\n"
"ds_read_b128 a[156:159], v240 offset:4864\n"
"v_accvgpr_read_b32 v188, a28\n"
"v_mfma_f32_16x16x32_bf16 v[204:207], a[240:243], v[128:131], v[204:207]\n"
"ds_read_b64_tr_b16 a[192:193], v242 offset:16384\n"
"v_accvgpr_read_b32 v189, a29\n"
"v_mfma_f32_16x16x32_bf16 v[204:207], a[244:247], v[136:139], v[204:207]\n"
"ds_read_b64_tr_b16 a[194:195], v242 offset:20480\n"
"v_accvgpr_read_b32 v190, a30\n"
"v_mfma_f32_16x16x32_bf16 v[200:203], a[240:243], v[132:135], v[200:203]\n"
"ds_read_b64_tr_b16 a[196:197], v243 offset:16384\n"
"v_accvgpr_read_b32 v191, a31\n"
"v_mfma_f32_16x16x32_bf16 v[200:203], a[244:247], v[140:143], v[200:203]\n"
"ds_read_b64_tr_b16 a[198:199], v243 offset:20480\n"
"v_cvt_pk_bf16_f32 v102, v188, v189\n"
"v_mfma_f32_16x16x32_bf16 v[196:199], a[248:251], v[128:131], v[196:199]\n"
"ds_read_b64_tr_b16 a[200:201], v242 offset:16640\n"
"v_cvt_pk_bf16_f32 v103, v190, v191\n"
"v_mfma_f32_16x16x32_bf16 v[196:199], a[252:255], v[136:139], v[196:199]\n"
"ds_read_b64_tr_b16 a[202:203], v242 offset:20736\n"
"v_accvgpr_read_b32 v176, a32\n"
"v_mfma_f32_16x16x32_bf16 v[192:195], a[248:251], v[132:135], v[192:195]\n"
"ds_read_b64_tr_b16 a[204:205], v243 offset:16640\n"
"v_accvgpr_read_b32 v177, a33\n"
"v_mfma_f32_16x16x32_bf16 v[192:195], a[252:255], v[140:143], v[192:195]\n"
"ds_read_b64_tr_b16 a[206:207], v243 offset:20736\n"
"v_accvgpr_read_b32 v178, a34\n"
"s_barrier\n"
"ds_read_b64_tr_b16 a[208:209], v242 offset:16896\n"
"v_accvgpr_read_b32 v179, a35\n"
"v_cvt_pk_bf16_f32 v72, v176, v177\n"
"v_cvt_pk_bf16_f32 v73, v178, v179\n"
"ds_read_b64_tr_b16 a[210:211], v242 offset:20992\n"
"v_accvgpr_read_b32 v180, a36\n"
"v_accvgpr_read_b32 v181, a37\n"
"v_accvgpr_read_b32 v182, a38\n"
"ds_read_b64_tr_b16 a[212:213], v243 offset:16896\n"
"v_accvgpr_read_b32 v183, a39\n"
"v_cvt_pk_bf16_f32 v104, v180, v181\n"
"v_cvt_pk_bf16_f32 v105, v182, v183\n"
"ds_read_b64_tr_b16 a[214:215], v243 offset:20992\n"
"v_accvgpr_read_b32 v184, a40\n"
"v_accvgpr_read_b32 v185, a41\n"
"v_accvgpr_read_b32 v186, a42\n"
"ds_read_b64_tr_b16 a[216:217], v242 offset:17152\n"
"v_accvgpr_read_b32 v187, a43\n"
"v_cvt_pk_bf16_f32 v74, v184, v185\n"
"v_cvt_pk_bf16_f32 v75, v186, v187\n"
"ds_read_b64_tr_b16 a[218:219], v242 offset:21248\n"
"v_accvgpr_read_b32 v188, a44\n"
"v_accvgpr_read_b32 v189, a45\n"
"v_accvgpr_read_b32 v190, a46\n"
"ds_read_b64_tr_b16 a[220:221], v243 offset:17152\n"
"v_accvgpr_read_b32 v191, a47\n"
"v_cvt_pk_bf16_f32 v106, v188, v189\n"
"v_cvt_pk_bf16_f32 v107, v190, v191\n"
"ds_read_b64_tr_b16 a[222:223], v243 offset:21248\n"
"v_accvgpr_read_b32 v176, a48\n"
"v_accvgpr_read_b32 v177, a49\n"
"v_accvgpr_read_b32 v178, a50\n"
"v_accvgpr_read_b32 v179, a51\n"
"v_cvt_pk_bf16_f32 v76, v176, v177\n"
"v_cvt_pk_bf16_f32 v77, v178, v179\n"
"v_accvgpr_read_b32 v180, a52\n"
"v_accvgpr_read_b32 v181, a53\n"
"v_accvgpr_read_b32 v182, a54\n"
"v_accvgpr_read_b32 v183, a55\n"
"v_cvt_pk_bf16_f32 v108, v180, v181\n"
"v_cvt_pk_bf16_f32 v109, v182, v183\n"
"v_accvgpr_read_b32 v184, a56\n"
"v_accvgpr_read_b32 v185, a57\n"
"v_accvgpr_read_b32 v186, a58\n"
"v_accvgpr_read_b32 v187, a59\n"
"v_cvt_pk_bf16_f32 v78, v184, v185\n"
"v_cvt_pk_bf16_f32 v79, v186, v187\n"
"v_accvgpr_read_b32 v188, a60\n"
"v_accvgpr_read_b32 v189, a61\n"
"v_accvgpr_read_b32 v190, a62\n"
"v_accvgpr_read_b32 v191, a63\n"
"v_cvt_pk_bf16_f32 v110, v188, v189\n"
"v_cvt_pk_bf16_f32 v111, v190, v191\n"
"v_accvgpr_read_b32 v176, a64\n"
"v_accvgpr_read_b32 v177, a65\n"
"v_accvgpr_read_b32 v178, a66\n"
"s_nop 1\n"
"v_accvgpr_read_b32 v179, a67\n"
"v_cvt_pk_bf16_f32 v80, v176, v177\n"
"v_cvt_pk_bf16_f32 v81, v178, v179\n"
"v_accvgpr_read_b32 v180, a68\n"
"s_waitcnt lgkmcnt(14)\n"
"v_mfma_f32_16x16x32_bf16 v[144:147], a[160:163], v[64:67], 0\n"
"AT_pg5:\n"
"s_cbranch_execz AT_hr\n"
"v_accvgpr_read_b32 v181, a69\n"
"v_accvgpr_read_b32 v182, a70\n"
"v_mfma_f32_16x16x32_bf16 v[144:147], a[164:167], v[68:71], v[144:147]\n"
"v_accvgpr_read_b32 v183, a71\n"
"v_cvt_pk_bf16_f32 v112, v180, v181\n"
"v_mfma_f32_16x16x32_bf16 v[144:147], a[168:171], v[72:75], v[144:147]\n"
"v_cvt_pk_bf16_f32 v113, v182, v183\n"
"v_accvgpr_read_b32 v184, a72\n"
"v_mfma_f32_16x16x32_bf16 v[144:147], a[172:175], v[76:79], v[144:147]\n"
"v_accvgpr_read_b32 v185, a73\n"
"v_accvgpr_read_b32 v186, a74\n"
"v_mfma_f32_16x16x32_bf16 v[152:155], a[160:163], v[96:99], 0\n"
"v_accvgpr_read_b32 v187, a75\n"
"v_cvt_pk_bf16_f32 v82, v184, v185\n"
"v_mfma_f32_16x16x32_bf16 v[152:155], a[164:167], v[100:103], v[152:155]\n"
"v_cvt_pk_bf16_f32 v83, v186, v187\n"
"v_accvgpr_read_b32 v188, a76\n"
"v_mfma_f32_16x16x32_bf16 v[152:155], a[168:171], v[104:107], v[152:155]\n"
"v_accvgpr_read_b32 v189, a77\n"
"v_accvgpr_read_b32 v190, a78\n"
"v_mfma_f32_16x16x32_bf16 v[152:155], a[172:175], v[108:111], v[152:155]\n"
"v_accvgpr_read_b32 v191, a79\n"
"v_cvt_pk_bf16_f32 v114, v188, v189\n"
"v_cvt_pk_bf16_f32 v115, v190, v191\n"
"v_accvgpr_read_b32 v176, a80\n"
"v_accvgpr_read_b32 v177, a81\n"
"v_accvgpr_read_b32 v178, a82\n"
"v_accvgpr_read_b32 v179, a83\n"
"v_cvt_pk_bf16_f32 v84, v176, v177\n"
"v_cvt_pk_bf16_f32 v85, v178, v179\n"
"v_accvgpr_read_b32 v180, a84\n"
"v_accvgpr_read_b32 v181, a85\n"
"v_accvgpr_read_b32 v182, a86\n"
"v_accvgpr_read_b32 v183, a87\n"
"v_cvt_pk_bf16_f32 v116, v180, v181\n"
"v_cvt_pk_bf16_f32 v117, v182, v183\n"
"v_cvt_pk_bf16_f32 v86, v236, v237\n"
"v_cvt_pk_bf16_f32 v87, v238, v239\n"
"v_cvt_pk_bf16_f32 v118, v232, v233\n"
"v_cvt_pk_bf16_f32 v119, v234, v235\n"
"v_cvt_pk_bf16_f32 v88, v228, v229\n"
"v_cvt_pk_bf16_f32 v89, v230, v231\n"
"v_cvt_pk_bf16_f32 v120, v220, v221\n"
"v_cvt_pk_bf16_f32 v121, v222, v223\n"
"v_cvt_pk_bf16_f32 v90, v212, v213\n"
"v_cvt_pk_bf16_f32 v91, v214, v215\n"
"v_cvt_pk_bf16_f32 v122, v208, v209\n"
"v_cvt_pk_bf16_f32 v123, v210, v211\n"
"v_cvt_pk_bf16_f32 v92, v204, v205\n"
"v_cvt_pk_bf16_f32 v93, v206, v207\n"
"v_cvt_pk_bf16_f32 v124, v200, v201\n"
"v_cvt_pk_bf16_f32 v125, v202, v203\n"
"v_cvt_pk_bf16_f32 v94, v196, v197\n"
"v_cvt_pk_bf16_f32 v95, v198, v199\n"
"v_cvt_pk_bf16_f32 v126, v192, v193\n"
"v_cvt_pk_bf16_f32 v127, v194, v195\n"
"v_max3_f32 v144, v144, v145, v146\n"
"v_max_f32 v144, v144, v147\n"
"v_sub_f32 v24, 0xc2480000, v144\n"
"v_sub_f32 v25, 0xc2480000, v144\n"
"v_sub_f32 v26, 0xc2480000, v144\n"
"v_sub_f32 v27, 0xc2480000, v144\n"
"s_nop 1\n"
"v_mfma_f32_16x16x32_bf16 v[148:151], a[160:163], v[80:83], 0\n"
"v_max3_f32 v152, v152, v153, v154\n"
"v_max_f32 v152, v152, v155\n"
"v_mfma_f32_16x16x32_bf16 v[148:151], a[164:167], v[84:87], v[148:151]\n"
"v_sub_f32 v32, 0xc2480000, v152\n"
"v_sub_f32 v33, 0xc2480000, v152\n"
"v_mfma_f32_16x16x32_bf16 v[148:151], a[168:171], v[88:91], v[148:151]\n"
"v_sub_f32 v34, 0xc2480000, v152\n"
"v_sub_f32 v35, 0xc2480000, v152\n"
"v_mfma_f32_16x16x32_bf16 v[148:151], a[172:175], v[92:95], v[148:151]\n"
"v_mov_b32 v40, 0x3f803f80\n"
"v_mov_b32 v41, 0x3f803f80\n"
"v_mfma_f32_16x16x32_bf16 v[156:159], a[160:163], v[112:115], 0\n"
"v_mov_b32 v42, 0x3f803f80\n"
"v_mov_b32 v43, 0x3f803f80\n"
"v_mfma_f32_16x16x32_bf16 v[156:159], a[164:167], v[116:119], v[156:159]\n"
"v_mov_b32 v20, 0\n"
"v_mov_b32 v21, 0\n"
"v_mfma_f32_16x16x32_bf16 v[156:159], a[168:171], v[120:123], v[156:159]\n"
"v_mov_b32 v22, 0\n"
"v_mov_b32 v23, 0\n"
"v_mfma_f32_16x16x32_bf16 v[156:159], a[172:175], v[124:127], v[156:159]\n"
"v_mov_b32 v44, 0\n"
"v_mov_b32 v52, 0\n"
"v_mov_b32 v216, -1\n"
"v_mov_b32 v217, -1\n"
"v_mov_b32 v218, -1\n"
"v_mov_b32 v219, -1\n"
"v_lshl_add_u32 v255, v248, 6, s48\n"
"v_lshlrev_b32 v207, 4, v248\n"
"v_add_u32 v208, 18432, v207\n"
"s_waitcnt lgkmcnt(0)\n"
"AT_main:\n"
"s_or_b32 s56, s42, s41\n"
"s_cmp_lg_u32 s56, 0\n"
"s_cbranch_scc1 AT_h1\n"
"v_lshlrev_b32 v214, 7, v248\n"
"s_mov_b32 s57, 32768\n"
"s_mov_b32 s58, 40960\n"
"s_mov_b32 s59, 49152\n"
"s_mov_b32 s60, 57344\n"
"buffer_load_dword v210, v214, s[28:31], s57 offen offset:4 sc1\n"
"buffer_load_dword v211, v214, s[28:31], s58 offen offset:4 sc1\n"
"buffer_load_dword v212, v214, s[28:31], s59 offen offset:4 sc1\n"
"buffer_load_dword v213, v214, s[28:31], s60 offen offset:4 sc1\n"
"AT_h1:\n"
"v_mfma_f32_16x16x32_bf16 v[0:3], a[128:131], v[64:67], v[24:27]\n"
"s_nop 0\n"
"ds_read_b128 a[160:163], v240 offset:8192\n"
"v_mfma_f32_16x16x32_bf16 v[0:3], a[132:135], v[68:71], v[0:3]\n"
"s_add_u32 m0, s86, 98304\n"
"v_mfma_f32_16x16x32_bf16 v[0:3], a[136:139], v[72:75], v[0:3]\n"
"s_nop 0\n"
"buffer_load_dwordx4 v246, s[8:11], s84 offen offset:0 lds\n"
"ds_read_b128 a[164:167], v240 offset:8448\n"
"v_mfma_f32_16x16x32_bf16 v[0:3], a[140:143], v[76:79], v[0:3]\n"
"s_nop 0\n"
"v_mfma_f32_16x16x32_bf16 v[4:7], a[144:147], v[64:67], v[24:27]\n"
"ds_read_b128 a[168:171], v240 offset:8704\n"
"v_max3_f32 v148, v148, v149, v150\n"
"v_mfma_f32_16x16x32_bf16 v[4:7], a[148:151], v[68:71], v[4:7]\n"
"v_max_f32 v148, v148, v151\n"
"v_sub_f32 v28, 0xc2480000, v148\n"
"v_mfma_f32_16x16x32_bf16 v[4:7], a[152:155], v[72:75], v[4:7]\n"
"ds_read_b128 a[172:175], v240 offset:8960\n"
"v_sub_f32 v29, 0xc2480000, v148\n"
"v_mfma_f32_16x16x32_bf16 v[4:7], a[156:159], v[76:79], v[4:7]\n"
"v_exp_f32 v0, v0\n"
"v_sub_f32 v30, 0xc2480000, v148\n"
"v_mfma_f32_16x16x32_bf16 a[96:99], v[40:43], v[20:23], 0\n"
"buffer_load_dwordx4 v246, s[8:11], s84 offen offset:1024 lds\n"
"v_sub_f32 v31, 0xc2480000, v148\n"
"v_mfma_f32_16x16x32_bf16 a[100:103], v[40:43], v[20:23], 0\n"
"v_exp_f32 v1, v1\n"
"v_max3_f32 v156, v156, v157, v158\n"
"v_mfma_f32_16x16x32_bf16 a[104:107], v[40:43], v[20:23], 0\n"
"ds_read_b128 a[176:179], v240 offset:12288\n"
"v_cvt_pk_bf16_f32 v16, v0, v1\n"
"v_max_f32 v156, v156, v159\n"
"v_mfma_f32_16x16x32_bf16 a[108:111], v[40:43], v[20:23], 0\n"
"v_exp_f32 v2, v2\n"
"v_sub_f32 v36, 0xc2480000, v156\n"
"v_mfma_f32_16x16x32_bf16 a[112:115], v[40:43], v[20:23], 0\n"
"ds_read_b128 a[180:183], v240 offset:12544\n"
"v_add_f32 v44, v44, v0\n"
"v_sub_f32 v37, 0xc2480000, v156\n"
"v_mfma_f32_16x16x32_bf16 a[116:119], v[40:43], v[20:23], 0\n"
"v_exp_f32 v3, v3\n"
"v_sub_f32 v38, 0xc2480000, v156\n"
"v_mfma_f32_16x16x32_bf16 a[120:123], v[40:43], v[20:23], 0\n"
"ds_read_b128 a[184:187], v240 offset:12800\n"
"v_cvt_pk_bf16_f32 v17, v2, v3\n"
"v_sub_f32 v39, 0xc2480000, v156\n"
"v_mfma_f32_16x16x32_bf16 a[124:127], v[40:43], v[20:23], 0\n"
"s_nop 0\n"
"v_exp_f32 v4, v4\n"
"v_mfma_f32_16x16x32_bf16 v[56:59], v[40:43], v[20:23], 0\n"
"buffer_load_dwordx4 v247, s[8:11], s84 offen offset:2048 lds\n"
"ds_read_b128 a[188:191], v240 offset:13056\n"
"v_add_f32 v44, v44, v1\n"
"v_mfma_f32_16x16x32_bf16 v[8:11], a[128:131], v[80:83], v[28:31]\n"
"s_nop 0\n"
"v_exp_f32 v5, v5\n"
"v_mfma_f32_16x16x32_bf16 v[8:11], a[132:135], v[84:87], v[8:11]\n"
"ds_read_b64_tr_b16 a[224:225], v242 offset:24576\n"
"v_cvt_pk_bf16_f32 v18, v4, v5\n"
"v_mfma_f32_16x16x32_bf16 v[8:11], a[136:139], v[88:91], v[8:11]\n"
"s_nop 0\n"
"v_exp_f32 v6, v6\n"
"v_mfma_f32_16x16x32_bf16 v[8:11], a[140:143], v[92:95], v[8:11]\n"
"ds_read_b64_tr_b16 a[226:227], v242 offset:28672\n"
"v_add_f32 v44, v44, v2\n"
"v_mfma_f32_16x16x32_bf16 v[12:15], a[144:147], v[80:83], v[28:31]\n"
"s_nop 0\n"
"v_exp_f32 v7, v7\n"
"v_mfma_f32_16x16x32_bf16 v[12:15], a[148:151], v[84:87], v[12:15]\n"
"ds_read_b64_tr_b16 a[228:229], v243 offset:24576\n"
"v_cvt_pk_bf16_f32 v19, v6, v7\n"
"v_mfma_f32_16x16x32_bf16 v[12:15], a[152:155], v[88:91], v[12:15]\n"
"s_nop 0\n"
"buffer_load_dwordx4 v247, s[8:11], s84 offen offset:3072 lds\n"
"v_mfma_f32_16x16x32_bf16 v[12:15], a[156:159], v[92:95], v[12:15]\n"
"s_nop 0\n"
"v_exp_f32 v8, v8\n"
"v_mfma_f32_16x16x32_bf16 a[0:3], a[192:195], v[16:19], 0\n"
"ds_read_b64_tr_b16 a[230:231], v243 offset:28672\n"
"v_add_f32 v44, v44, v3\n"
"v_mfma_f32_16x16x32_bf16 a[4:7], a[196:199], v[16:19], 0\n"
"s_nop 0\n"
"v_exp_f32 v9, v9\n"
"v_mfma_f32_16x16x32_bf16 a[8:11], a[200:203], v[16:19], 0\n"
"ds_read_b64_tr_b16 a[232:233], v242 offset:24832\n"
"v_cvt_pk_bf16_f32 v20, v8, v9\n"
"v_mfma_f32_16x16x32_bf16 a[12:15], a[204:207], v[16:19], 0\n"
"s_nop 0\n"
"v_exp_f32 v10, v10\n"
"v_mfma_f32_16x16x32_bf16 a[16:19], a[208:211], v[16:19], 0\n"
"ds_read_b64_tr_b16 a[234:235], v242 offset:28928\n"
"v_add_f32 v44, v44, v4\n"
"v_mfma_f32_16x16x32_bf16 a[20:23], a[212:215], v[16:19], 0\n"
"s_add_u32 m0, s86, 114688\n"
"v_exp_f32 v11, v11\n"
"v_mfma_f32_16x16x32_bf16 a[24:27], a[216:219], v[16:19], 0\n"
"buffer_load_dwordx4 v246, s[12:15], s84 offen offset:0 lds\n"
"ds_read_b64_tr_b16 a[236:237], v243 offset:24832\n"
"v_cvt_pk_bf16_f32 v21, v10, v11\n"
"v_mfma_f32_16x16x32_bf16 a[28:31], a[220:223], v[16:19], 0\n"
"s_nop 0\n"
"v_exp_f32 v12, v12\n"
"v_mfma_f32_16x16x32_bf16 v[220:223], a[128:131], v[96:99], v[32:35]\n"
"ds_read_b64_tr_b16 a[238:239], v243 offset:28928\n"
"v_add_f32 v44, v44, v5\n"
"v_mfma_f32_16x16x32_bf16 v[220:223], a[132:135], v[100:103], v[220:223]\n"
"s_nop 0\n"
"v_exp_f32 v13, v13\n"
"v_mfma_f32_16x16x32_bf16 v[220:223], a[136:139], v[104:107], v[220:223]\n"
"ds_read_b64_tr_b16 a[240:241], v242 offset:25088\n"
"v_cvt_pk_bf16_f32 v22, v12, v13\n"
"v_mfma_f32_16x16x32_bf16 v[220:223], a[140:143], v[108:111], v[220:223]\n"
"s_nop 0\n"
"v_exp_f32 v14, v14\n"
"v_mfma_f32_16x16x32_bf16 v[224:227], a[144:147], v[96:99], v[32:35]\n"
"ds_read_b64_tr_b16 a[242:243], v242 offset:29184\n"
"v_add_f32 v44, v44, v6\n"
"v_mfma_f32_16x16x32_bf16 v[224:227], a[148:151], v[100:103], v[224:227]\n"
"s_nop 0\n"
"v_exp_f32 v15, v15\n"
"v_mfma_f32_16x16x32_bf16 v[224:227], a[152:155], v[104:107], v[224:227]\n"
"ds_read_b64_tr_b16 a[244:245], v243 offset:25088\n"
"v_cvt_pk_bf16_f32 v23, v14, v15\n"
"v_mfma_f32_16x16x32_bf16 v[224:227], a[156:159], v[108:111], v[224:227]\n"
"s_nop 0\n"
"v_exp_f32 v220, v220\n"
"v_mfma_f32_16x16x32_bf16 a[32:35], a[192:195], v[20:23], 0\n"
"s_nop 0\n"
"buffer_load_dwordx4 v247, s[12:15], s84 offen offset:1024 lds\n"
"v_mfma_f32_16x16x32_bf16 a[36:39], a[196:199], v[20:23], 0\n"
"s_nop 0\n"
"v_exp_f32 v221, v221\n"
"v_mfma_f32_16x16x32_bf16 a[40:43], a[200:203], v[20:23], 0\n"
"ds_read_b64_tr_b16 a[246:247], v243 offset:29184\n"
"v_cvt_pk_bf16_f32 v16, v220, v221\n"
"v_mfma_f32_16x16x32_bf16 a[44:47], a[204:207], v[20:23], 0\n"
"s_nop 0\n"
"v_exp_f32 v222, v222\n"
"v_mfma_f32_16x16x32_bf16 a[48:51], a[208:211], v[20:23], 0\n"
"ds_read_b64_tr_b16 a[248:249], v242 offset:25344\n"
"v_add_f32 v44, v44, v7\n"
"v_mfma_f32_16x16x32_bf16 a[52:55], a[212:215], v[20:23], 0\n"
"s_nop 0\n"
"v_exp_f32 v223, v223\n"
"v_mfma_f32_16x16x32_bf16 a[56:59], a[216:219], v[20:23], 0\n"
"ds_read_b64_tr_b16 a[250:251], v242 offset:29440\n"
"v_cvt_pk_bf16_f32 v17, v222, v223\n"
"v_mfma_f32_16x16x32_bf16 a[60:63], a[220:223], v[20:23], 0\n"
"s_nop 0\n"
"v_exp_f32 v224, v224\n"
"v_mfma_f32_16x16x32_bf16 v[48:51], v[40:43], v[20:23], 0\n"
"buffer_load_dwordx4 v246, s[12:15], s84 offen offset:2048 lds\n"
"ds_read_b64_tr_b16 a[252:253], v243 offset:25344\n"
"v_add_f32 v52, v52, v220\n"
"v_mfma_f32_16x16x32_bf16 v[228:231], a[128:131], v[112:115], v[36:39]\n"
"s_nop 0\n"
"v_exp_f32 v225, v225\n"
"v_mfma_f32_16x16x32_bf16 v[228:231], a[132:135], v[116:119], v[228:231]\n"
"ds_read_b64_tr_b16 a[254:255], v243 offset:29440\n"
"v_cvt_pk_bf16_f32 v18, v224, v225\n"
"v_mfma_f32_16x16x32_bf16 v[228:231], a[136:139], v[120:123], v[228:231]\n"
"s_nop 0\n"
"v_exp_f32 v226, v226\n"
"v_mfma_f32_16x16x32_bf16 v[228:231], a[140:143], v[124:127], v[228:231]\n"
"v_add_f32 v52, v52, v221\n"
"v_add_f32 v52, v52, v222\n"
"v_mfma_f32_16x16x32_bf16 v[232:235], a[144:147], v[112:115], v[36:39]\n"
"s_nop 0\n"
"v_exp_f32 v227, v227\n"
"v_mfma_f32_16x16x32_bf16 v[232:235], a[148:151], v[116:119], v[232:235]\n"
"v_cvt_pk_bf16_f32 v19, v226, v227\n"
"v_add_f32 v52, v52, v223\n"
"v_mfma_f32_16x16x32_bf16 v[232:235], a[152:155], v[120:123], v[232:235]\n"
"s_nop 0\n"
"buffer_load_dwordx4 v247, s[12:15], s84 offen offset:3072 lds\n"
"v_mfma_f32_16x16x32_bf16 v[232:235], a[156:159], v[124:127], v[232:235]\n"
"s_add_u32 s84, s84, 16384\n"
"v_exp_f32 v228, v228\n"
"v_mfma_f32_16x16x32_bf16 a[64:67], a[192:195], v[16:19], 0\n"
"v_add_f32 v52, v52, v224\n"
"v_add_f32 v52, v52, v225\n"
"v_mfma_f32_16x16x32_bf16 a[68:71], a[196:199], v[16:19], 0\n"
"s_nop 0\n"
"v_exp_f32 v229, v229\n"
"v_mfma_f32_16x16x32_bf16 a[72:75], a[200:203], v[16:19], 0\n"
"v_cvt_pk_bf16_f32 v20, v228, v229\n"
"v_add_f32 v52, v52, v226\n"
"v_mfma_f32_16x16x32_bf16 a[76:79], a[204:207], v[16:19], 0\n"
"s_waitcnt vmcnt(16) lgkmcnt(0)\n"
"v_exp_f32 v230, v230\n"
"v_mfma_f32_16x16x32_bf16 a[80:83], a[208:211], v[16:19], 0\n"
"s_barrier\n"
"v_mfma_f32_16x16x32_bf16 a[84:87], a[212:215], v[16:19], 0\n"
"s_nop 0\n"
"v_exp_f32 v231, v231\n"
"v_mfma_f32_16x16x32_bf16 a[88:91], a[216:219], v[16:19], 0\n"
"v_cvt_pk_bf16_f32 v21, v230, v231\n"
"v_add_f32 v52, v52, v227\n"
"v_mfma_f32_16x16x32_bf16 a[92:95], a[220:223], v[16:19], 0\n"
"s_nop 0\n"
"v_exp_f32 v232, v232\n"
"s_mov_b32 s87, 6\n"
".p2align 6\n"
"AT_loop:\n"
"v_mfma_f32_16x16x32_bf16 v[0:3], a[160:163], v[64:67], v[24:27]\n"
"ds_read_b128 a[128:131], v240 offset:32768\n"
"buffer_load_dword v250, v251, s[64:67], 0 offen\n"
"v_mfma_f32_16x16x32_bf16 v[0:3], a[164:167], v[68:71], v[0:3]\n"
"v_exp_f32 v233, v233\n"
"v_mfma_f32_16x16x32_bf16 v[0:3], a[168:171], v[72:75], v[0:3]\n"
"ds_read_b128 a[132:135], v240 offset:33024\n"
"v_cvt_pk_bf16_f32 v22, v232, v233\n"
"v_mfma_f32_16x16x32_bf16 v[0:3], a[172:175], v[76:79], v[0:3]\n"
"v_exp_f32 v234, v234\n"
"v_mfma_f32_16x16x32_bf16 v[4:7], a[176:179], v[64:67], v[24:27]\n"
"ds_read_b128 a[136:139], v240 offset:33280\n"
"v_mfma_f32_16x16x32_bf16 v[4:7], a[180:183], v[68:71], v[4:7]\n"
"v_exp_f32 v235, v235\n"
"v_mfma_f32_16x16x32_bf16 v[4:7], a[184:187], v[72:75], v[4:7]\n"
"ds_read_b128 a[140:143], v240 offset:33536\n"
"v_cvt_pk_bf16_f32 v23, v234, v235\n"
"v_mfma_f32_16x16x32_bf16 v[4:7], a[188:191], v[76:79], v[4:7]\n"
"s_mov_b32 m0, s86\n"
"v_exp_f32 v0, v0\n"
"v_mfma_f32_16x16x32_bf16 a[96:99], a[192:195], v[20:23], a[96:99]\n"
"buffer_load_dwordx4 v246, s[8:11], s84 offen offset:0 lds\n"
"v_mfma_f32_16x16x32_bf16 a[100:103], a[196:199], v[20:23], a[100:103]\n"
"v_exp_f32 v1, v1\n"
"v_mfma_f32_16x16x32_bf16 a[104:107], a[200:203], v[20:23], a[104:107]\n"
"ds_read_b128 a[144:147], v240 offset:36864\n"
"v_cvt_pk_bf16_f32 v16, v0, v1\n"
"v_mfma_f32_16x16x32_bf16 a[108:111], a[204:207], v[20:23], a[108:111]\n"
"v_exp_f32 v2, v2\n"
"v_mfma_f32_16x16x32_bf16 a[112:115], a[208:211], v[20:23], a[112:115]\n"
"ds_read_b128 a[148:151], v240 offset:37120\n"
"v_add_f32 v44, v44, v0\n"
"v_mfma_f32_16x16x32_bf16 a[116:119], a[212:215], v[20:23], a[116:119]\n"
"v_exp_f32 v3, v3\n"
"v_mfma_f32_16x16x32_bf16 a[120:123], a[216:219], v[20:23], a[120:123]\n"
"ds_read_b128 a[152:155], v240 offset:37376\n"
"v_cvt_pk_bf16_f32 v17, v2, v3\n"
"v_mfma_f32_16x16x32_bf16 a[124:127], a[220:223], v[20:23], a[124:127]\n"
"v_exp_f32 v4, v4\n"
"v_mfma_f32_16x16x32_bf16 v[56:59], v[40:43], v[20:23], v[56:59]\n"
"ds_read_b128 a[156:159], v240 offset:37632\n"
"v_add_f32 v44, v44, v1\n"
"v_mfma_f32_16x16x32_bf16 v[8:11], a[160:163], v[80:83], v[28:31]\n"
"v_exp_f32 v5, v5\n"
"v_mfma_f32_16x16x32_bf16 v[8:11], a[164:167], v[84:87], v[8:11]\n"
"ds_read_b64_tr_b16 a[192:193], v242 offset:49152\n"
"v_cvt_pk_bf16_f32 v18, v4, v5\n"
"v_mfma_f32_16x16x32_bf16 v[8:11], a[168:171], v[88:91], v[8:11]\n"
"v_exp_f32 v6, v6\n"
"v_mfma_f32_16x16x32_bf16 v[8:11], a[172:175], v[92:95], v[8:11]\n"
"ds_read_b64_tr_b16 a[194:195], v242 offset:53248\n"
"v_add_f32 v44, v44, v2\n"
"v_mfma_f32_16x16x32_bf16 v[12:15], a[176:179], v[80:83], v[28:31]\n"
"v_exp_f32 v7, v7\n"
"v_mfma_f32_16x16x32_bf16 v[12:15], a[180:183], v[84:87], v[12:15]\n"
"ds_read_b64_tr_b16 a[196:197], v243 offset:49152\n"
"v_cvt_pk_bf16_f32 v19, v6, v7\n"
"v_mfma_f32_16x16x32_bf16 v[12:15], a[184:187], v[88:91], v[12:15]\n"
"buffer_load_dwordx4 v246, s[8:11], s84 offen offset:1024 lds\n"
"v_mfma_f32_16x16x32_bf16 v[12:15], a[188:191], v[92:95], v[12:15]\n"
"s_waitcnt lgkmcnt(11)\n"
"v_exp_f32 v8, v8\n"
"v_mfma_f32_16x16x32_bf16 a[0:3], a[224:227], v[16:19], a[0:3]\n"
"ds_read_b64_tr_b16 a[198:199], v243 offset:53248\n"
"v_add_f32 v44, v44, v3\n"
"v_mfma_f32_16x16x32_bf16 a[4:7], a[228:231], v[16:19], a[4:7]\n"
"v_exp_f32 v9, v9\n"
"v_mfma_f32_16x16x32_bf16 a[8:11], a[232:235], v[16:19], a[8:11]\n"
"ds_read_b64_tr_b16 a[200:201], v242 offset:49408\n"
"v_cvt_pk_bf16_f32 v20, v8, v9\n"
"v_mfma_f32_16x16x32_bf16 a[12:15], a[236:239], v[16:19], a[12:15]\n"
"v_exp_f32 v10, v10\n"
"v_mfma_f32_16x16x32_bf16 a[16:19], a[240:243], v[16:19], a[16:19]\n"
"ds_read_b64_tr_b16 a[202:203], v242 offset:53504\n"
"v_add_f32 v44, v44, v4\n"
"v_mfma_f32_16x16x32_bf16 a[20:23], a[244:247], v[16:19], a[20:23]\n"
"v_exp_f32 v11, v11\n"
"v_mfma_f32_16x16x32_bf16 a[24:27], a[248:251], v[16:19], a[24:27]\n"
"ds_read_b64_tr_b16 a[204:205], v243 offset:49408\n"
"v_cvt_pk_bf16_f32 v21, v10, v11\n"
"v_mfma_f32_16x16x32_bf16 a[28:31], a[252:255], v[16:19], a[28:31]\n"
"v_exp_f32 v12, v12\n"
"v_mfma_f32_16x16x32_bf16 v[220:223], a[160:163], v[96:99], v[32:35]\n"
"ds_read_b64_tr_b16 a[206:207], v243 offset:53504\n"
"v_add_f32 v44, v44, v5\n"
"v_mfma_f32_16x16x32_bf16 v[220:223], a[164:167], v[100:103], v[220:223]\n"
"v_exp_f32 v13, v13\n"
"v_mfma_f32_16x16x32_bf16 v[220:223], a[168:171], v[104:107], v[220:223]\n"
"ds_read_b64_tr_b16 a[208:209], v242 offset:49664\n"
"v_cvt_pk_bf16_f32 v22, v12, v13\n"
"v_mfma_f32_16x16x32_bf16 v[220:223], a[172:175], v[108:111], v[220:223]\n"
"v_exp_f32 v14, v14\n"
"v_mfma_f32_16x16x32_bf16 v[224:227], a[176:179], v[96:99], v[32:35]\n"
"ds_read_b64_tr_b16 a[210:211], v242 offset:53760\n"
"v_add_f32 v44, v44, v6\n"
"v_mfma_f32_16x16x32_bf16 v[224:227], a[180:183], v[100:103], v[224:227]\n"
"v_exp_f32 v15, v15\n"
"v_mfma_f32_16x16x32_bf16 v[224:227], a[184:187], v[104:107], v[224:227]\n"
"ds_read_b64_tr_b16 a[212:213], v243 offset:49664\n"
"v_cvt_pk_bf16_f32 v23, v14, v15\n"
"v_mfma_f32_16x16x32_bf16 v[224:227], a[188:191], v[108:111], v[224:227]\n"
"v_exp_f32 v220, v220\n"
"v_mfma_f32_16x16x32_bf16 a[32:35], a[224:227], v[20:23], a[32:35]\n"
"buffer_load_dwordx4 v247, s[8:11], s84 offen offset:2048 lds\n"
"v_mfma_f32_16x16x32_bf16 a[36:39], a[228:231], v[20:23], a[36:39]\n"
"v_exp_f32 v221, v221\n"
"v_mfma_f32_16x16x32_bf16 a[40:43], a[232:235], v[20:23], a[40:43]\n"
"ds_read_b64_tr_b16 a[214:215], v243 offset:53760\n"
"v_cvt_pk_bf16_f32 v16, v220, v221\n"
"v_mfma_f32_16x16x32_bf16 a[44:47], a[236:239], v[20:23], a[44:47]\n"
"v_exp_f32 v222, v222\n"
"v_mfma_f32_16x16x32_bf16 a[48:51], a[240:243], v[20:23], a[48:51]\n"
"ds_read_b64_tr_b16 a[216:217], v242 offset:49920\n"
"v_add_f32 v44, v44, v7\n"
"v_mfma_f32_16x16x32_bf16 a[52:55], a[244:247], v[20:23], a[52:55]\n"
"v_exp_f32 v223, v223\n"
"v_mfma_f32_16x16x32_bf16 a[56:59], a[248:251], v[20:23], a[56:59]\n"
"ds_read_b64_tr_b16 a[218:219], v242 offset:54016\n"
"v_cvt_pk_bf16_f32 v17, v222, v223\n"
"v_mfma_f32_16x16x32_bf16 a[60:63], a[252:255], v[20:23], a[60:63]\n"
"v_exp_f32 v224, v224\n"
"v_mfma_f32_16x16x32_bf16 v[48:51], v[40:43], v[20:23], v[48:51]\n"
"ds_read_b64_tr_b16 a[220:221], v243 offset:49920\n"
"v_add_f32 v52, v52, v220\n"
"v_mfma_f32_16x16x32_bf16 v[228:231], a[160:163], v[112:115], v[36:39]\n"
"v_exp_f32 v225, v225\n"
"v_mfma_f32_16x16x32_bf16 v[228:231], a[164:167], v[116:119], v[228:231]\n"
"ds_read_b64_tr_b16 a[222:223], v243 offset:54016\n"
"v_cvt_pk_bf16_f32 v18, v224, v225\n"
"v_mfma_f32_16x16x32_bf16 v[228:231], a[168:171], v[120:123], v[228:231]\n"
"v_exp_f32 v226, v226\n"
"v_mfma_f32_16x16x32_bf16 v[228:231], a[172:175], v[124:127], v[228:231]\n"
"v_add_f32 v52, v52, v221\n"
"v_add_f32 v52, v52, v222\n"
"v_mfma_f32_16x16x32_bf16 v[232:235], a[176:179], v[112:115], v[36:39]\n"
"v_exp_f32 v227, v227\n"
"v_mfma_f32_16x16x32_bf16 v[232:235], a[180:183], v[116:119], v[232:235]\n"
"v_cvt_pk_bf16_f32 v19, v226, v227\n"
"v_add_f32 v52, v52, v223\n"
"v_mfma_f32_16x16x32_bf16 v[232:235], a[184:187], v[120:123], v[232:235]\n"
"buffer_load_dwordx4 v247, s[8:11], s84 offen offset:3072 lds\n"
"v_mfma_f32_16x16x32_bf16 v[232:235], a[188:191], v[124:127], v[232:235]\n"
"v_exp_f32 v228, v228\n"
"v_mfma_f32_16x16x32_bf16 a[64:67], a[224:227], v[16:19], a[64:67]\n"
"v_add_f32 v52, v52, v224\n"
"v_add_f32 v52, v52, v225\n"
"v_mfma_f32_16x16x32_bf16 a[68:71], a[228:231], v[16:19], a[68:71]\n"
"v_exp_f32 v229, v229\n"
"v_mfma_f32_16x16x32_bf16 a[72:75], a[232:235], v[16:19], a[72:75]\n"
"v_cvt_pk_bf16_f32 v20, v228, v229\n"
"v_add_f32 v52, v52, v226\n"
"v_mfma_f32_16x16x32_bf16 a[76:79], a[236:239], v[16:19], a[76:79]\n"
"v_exp_f32 v230, v230\n"
"v_mfma_f32_16x16x32_bf16 a[80:83], a[240:243], v[16:19], a[80:83]\n"
"s_waitcnt lgkmcnt(8)\n"
"buffer_load_dword v250, v255, s[32:35], 0 offen\n"
"v_mfma_f32_16x16x32_bf16 a[84:87], a[244:247], v[16:19], a[84:87]\n"
"v_exp_f32 v231, v231\n"
"v_mfma_f32_16x16x32_bf16 a[88:91], a[248:251], v[16:19], a[88:91]\n"
"v_cvt_pk_bf16_f32 v21, v230, v231\n"
"v_add_f32 v52, v52, v227\n"
"v_mfma_f32_16x16x32_bf16 a[92:95], a[252:255], v[16:19], a[92:95]\n"
"v_exp_f32 v232, v232\n"
"v_mfma_f32_16x16x32_bf16 v[0:3], a[128:131], v[64:67], v[24:27]\n"
"ds_read_b128 a[160:163], v240 offset:40960\n"
"v_mfma_f32_16x16x32_bf16 v[0:3], a[132:135], v[68:71], v[0:3]\n"
"v_exp_f32 v233, v233\n"
"v_mfma_f32_16x16x32_bf16 v[0:3], a[136:139], v[72:75], v[0:3]\n"
"ds_read_b128 a[164:167], v240 offset:41216\n"
"v_cvt_pk_bf16_f32 v22, v232, v233\n"
"v_mfma_f32_16x16x32_bf16 v[0:3], a[140:143], v[76:79], v[0:3]\n"
"v_exp_f32 v234, v234\n"
"v_mfma_f32_16x16x32_bf16 v[4:7], a[144:147], v[64:67], v[24:27]\n"
"ds_read_b128 a[168:171], v240 offset:41472\n"
"v_mfma_f32_16x16x32_bf16 v[4:7], a[148:151], v[68:71], v[4:7]\n"
"v_exp_f32 v235, v235\n"
"v_mfma_f32_16x16x32_bf16 v[4:7], a[152:155], v[72:75], v[4:7]\n"
"ds_read_b128 a[172:175], v240 offset:41728\n"
"v_cvt_pk_bf16_f32 v23, v234, v235\n"
"v_mfma_f32_16x16x32_bf16 v[4:7], a[156:159], v[76:79], v[4:7]\n"
"s_add_u32 m0, s86, 16384\n"
"v_exp_f32 v0, v0\n"
"v_mfma_f32_16x16x32_bf16 a[96:99], a[224:227], v[20:23], a[96:99]\n"
"buffer_load_dwordx4 v246, s[12:15], s84 offen offset:0 lds\n"
"v_mfma_f32_16x16x32_bf16 a[100:103], a[228:231], v[20:23], a[100:103]\n"
"v_exp_f32 v1, v1\n"
"v_mfma_f32_16x16x32_bf16 a[104:107], a[232:235], v[20:23], a[104:107]\n"
"ds_read_b128 a[176:179], v240 offset:45056\n"
"v_cvt_pk_bf16_f32 v16, v0, v1\n"
"v_mfma_f32_16x16x32_bf16 a[108:111], a[236:239], v[20:23], a[108:111]\n"
"v_exp_f32 v2, v2\n"
"v_mfma_f32_16x16x32_bf16 a[112:115], a[240:243], v[20:23], a[112:115]\n"
"ds_read_b128 a[180:183], v240 offset:45312\n"
"v_add_f32 v44, v44, v0\n"
"v_mfma_f32_16x16x32_bf16 a[116:119], a[244:247], v[20:23], a[116:119]\n"
"v_exp_f32 v3, v3\n"
"v_mfma_f32_16x16x32_bf16 a[120:123], a[248:251], v[20:23], a[120:123]\n"
"ds_read_b128 a[184:187], v240 offset:45568\n"
"v_cvt_pk_bf16_f32 v17, v2, v3\n"
"v_mfma_f32_16x16x32_bf16 a[124:127], a[252:255], v[20:23], a[124:127]\n"
"v_exp_f32 v4, v4\n"
"v_mfma_f32_16x16x32_bf16 v[56:59], v[40:43], v[20:23], v[56:59]\n"
"ds_read_b128 a[188:191], v240 offset:45824\n"
"v_add_f32 v44, v44, v1\n"
"v_mfma_f32_16x16x32_bf16 v[8:11], a[128:131], v[80:83], v[28:31]\n"
"v_exp_f32 v5, v5\n"
"v_mfma_f32_16x16x32_bf16 v[8:11], a[132:135], v[84:87], v[8:11]\n"
"ds_read_b64_tr_b16 a[224:225], v242 offset:57344\n"
"v_cvt_pk_bf16_f32 v18, v4, v5\n"
"v_mfma_f32_16x16x32_bf16 v[8:11], a[136:139], v[88:91], v[8:11]\n"
"v_exp_f32 v6, v6\n"
"v_mfma_f32_16x16x32_bf16 v[8:11], a[140:143], v[92:95], v[8:11]\n"
"ds_read_b64_tr_b16 a[226:227], v242 offset:61440\n"
"v_add_f32 v44, v44, v2\n"
"v_mfma_f32_16x16x32_bf16 v[12:15], a[144:147], v[80:83], v[28:31]\n"
"v_exp_f32 v7, v7\n"
"v_mfma_f32_16x16x32_bf16 v[12:15], a[148:151], v[84:87], v[12:15]\n"
"ds_read_b64_tr_b16 a[228:229], v243 offset:57344\n"
"v_cvt_pk_bf16_f32 v19, v6, v7\n"
"v_mfma_f32_16x16x32_bf16 v[12:15], a[152:155], v[88:91], v[12:15]\n"
"buffer_load_dwordx4 v247, s[12:15], s84 offen offset:1024 lds\n"
"v_mfma_f32_16x16x32_bf16 v[12:15], a[156:159], v[92:95], v[12:15]\n"
"s_waitcnt lgkmcnt(11)\n"
"v_exp_f32 v8, v8\n"
"v_mfma_f32_16x16x32_bf16 a[0:3], a[192:195], v[16:19], a[0:3]\n"
"ds_read_b64_tr_b16 a[230:231], v243 offset:61440\n"
"v_add_f32 v44, v44, v3\n"
"v_mfma_f32_16x16x32_bf16 a[4:7], a[196:199], v[16:19], a[4:7]\n"
"v_exp_f32 v9, v9\n"
"v_mfma_f32_16x16x32_bf16 a[8:11], a[200:203], v[16:19], a[8:11]\n"
"ds_read_b64_tr_b16 a[232:233], v242 offset:57600\n"
"v_cvt_pk_bf16_f32 v20, v8, v9\n"
"v_mfma_f32_16x16x32_bf16 a[12:15], a[204:207], v[16:19], a[12:15]\n"
"v_exp_f32 v10, v10\n"
"v_mfma_f32_16x16x32_bf16 a[16:19], a[208:211], v[16:19], a[16:19]\n"
"ds_read_b64_tr_b16 a[234:235], v242 offset:61696\n"
"v_add_f32 v44, v44, v4\n"
"v_mfma_f32_16x16x32_bf16 a[20:23], a[212:215], v[16:19], a[20:23]\n"
"v_exp_f32 v11, v11\n"
"v_mfma_f32_16x16x32_bf16 a[24:27], a[216:219], v[16:19], a[24:27]\n"
"ds_read_b64_tr_b16 a[236:237], v243 offset:57600\n"
"v_cvt_pk_bf16_f32 v21, v10, v11\n"
"v_mfma_f32_16x16x32_bf16 a[28:31], a[220:223], v[16:19], a[28:31]\n"
"v_exp_f32 v12, v12\n"
"v_mfma_f32_16x16x32_bf16 v[220:223], a[128:131], v[96:99], v[32:35]\n"
"ds_read_b64_tr_b16 a[238:239], v243 offset:61696\n"
"v_add_f32 v44, v44, v5\n"
"v_mfma_f32_16x16x32_bf16 v[220:223], a[132:135], v[100:103], v[220:223]\n"
"v_exp_f32 v13, v13\n"
"v_mfma_f32_16x16x32_bf16 v[220:223], a[136:139], v[104:107], v[220:223]\n"
"ds_read_b64_tr_b16 a[240:241], v242 offset:57856\n"
"v_cvt_pk_bf16_f32 v22, v12, v13\n"
"v_mfma_f32_16x16x32_bf16 v[220:223], a[140:143], v[108:111], v[220:223]\n"
"v_exp_f32 v14, v14\n"
"v_mfma_f32_16x16x32_bf16 v[224:227], a[144:147], v[96:99], v[32:35]\n"
"ds_read_b64_tr_b16 a[242:243], v242 offset:61952\n"
"v_add_f32 v44, v44, v6\n"
"v_mfma_f32_16x16x32_bf16 v[224:227], a[148:151], v[100:103], v[224:227]\n"
"v_exp_f32 v15, v15\n"
"v_mfma_f32_16x16x32_bf16 v[224:227], a[152:155], v[104:107], v[224:227]\n"
"ds_read_b64_tr_b16 a[244:245], v243 offset:57856\n"
"v_cvt_pk_bf16_f32 v23, v14, v15\n"
"v_mfma_f32_16x16x32_bf16 v[224:227], a[156:159], v[108:111], v[224:227]\n"
"v_exp_f32 v220, v220\n"
"v_mfma_f32_16x16x32_bf16 a[32:35], a[192:195], v[20:23], a[32:35]\n"
"buffer_load_dwordx4 v246, s[12:15], s84 offen offset:2048 lds\n"
"v_mfma_f32_16x16x32_bf16 a[36:39], a[196:199], v[20:23], a[36:39]\n"
"v_exp_f32 v221, v221\n"
"v_mfma_f32_16x16x32_bf16 a[40:43], a[200:203], v[20:23], a[40:43]\n"
"ds_read_b64_tr_b16 a[246:247], v243 offset:61952\n"
"v_cvt_pk_bf16_f32 v16, v220, v221\n"
"v_mfma_f32_16x16x32_bf16 a[44:47], a[204:207], v[20:23], a[44:47]\n"
"v_exp_f32 v222, v222\n"
"v_mfma_f32_16x16x32_bf16 a[48:51], a[208:211], v[20:23], a[48:51]\n"
"ds_read_b64_tr_b16 a[248:249], v242 offset:58112\n"
"v_add_f32 v44, v44, v7\n"
"v_mfma_f32_16x16x32_bf16 a[52:55], a[212:215], v[20:23], a[52:55]\n"
"v_exp_f32 v223, v223\n"
"v_mfma_f32_16x16x32_bf16 a[56:59], a[216:219], v[20:23], a[56:59]\n"
"ds_read_b64_tr_b16 a[250:251], v242 offset:62208\n"
"v_cvt_pk_bf16_f32 v17, v222, v223\n"
"v_mfma_f32_16x16x32_bf16 a[60:63], a[220:223], v[20:23], a[60:63]\n"
"v_exp_f32 v224, v224\n"
"v_mfma_f32_16x16x32_bf16 v[48:51], v[40:43], v[20:23], v[48:51]\n"
"ds_read_b64_tr_b16 a[252:253], v243 offset:58112\n"
"v_add_f32 v52, v52, v220\n"
"v_mfma_f32_16x16x32_bf16 v[228:231], a[128:131], v[112:115], v[36:39]\n"
"v_exp_f32 v225, v225\n"
"v_mfma_f32_16x16x32_bf16 v[228:231], a[132:135], v[116:119], v[228:231]\n"
"ds_read_b64_tr_b16 a[254:255], v243 offset:62208\n"
"v_cvt_pk_bf16_f32 v18, v224, v225\n"
"v_mfma_f32_16x16x32_bf16 v[228:231], a[136:139], v[120:123], v[228:231]\n"
"AT_pg6:\n"
"s_cbranch_execz AT_hr\n"
"v_exp_f32 v226, v226\n"
"v_mfma_f32_16x16x32_bf16 v[228:231], a[140:143], v[124:127], v[228:231]\n"
"v_add_f32 v52, v52, v221\n"
"v_add_f32 v52, v52, v222\n"
"v_mfma_f32_16x16x32_bf16 v[232:235], a[144:147], v[112:115], v[36:39]\n"
"v_exp_f32 v227, v227\n"
"v_mfma_f32_16x16x32_bf16 v[232:235], a[148:151], v[116:119], v[232:235]\n"
"v_cvt_pk_bf16_f32 v19, v226, v227\n"
"v_add_f32 v52, v52, v223\n"
"v_mfma_f32_16x16x32_bf16 v[232:235], a[152:155], v[120:123], v[232:235]\n"
"buffer_load_dwordx4 v247, s[12:15], s84 offen offset:3072 lds\n"
"v_mfma_f32_16x16x32_bf16 v[232:235], a[156:159], v[124:127], v[232:235]\n"
"s_add_u32 s84, s84, 16384\n"
"v_exp_f32 v228, v228\n"
"v_mfma_f32_16x16x32_bf16 a[64:67], a[192:195], v[16:19], a[64:67]\n"
"v_add_f32 v52, v52, v224\n"
"v_add_f32 v52, v52, v225\n"
"v_mfma_f32_16x16x32_bf16 a[68:71], a[196:199], v[16:19], a[68:71]\n"
"v_exp_f32 v229, v229\n"
"v_mfma_f32_16x16x32_bf16 a[72:75], a[200:203], v[16:19], a[72:75]\n"
"v_cvt_pk_bf16_f32 v20, v228, v229\n"
"v_add_f32 v52, v52, v226\n"
"v_mfma_f32_16x16x32_bf16 a[76:79], a[204:207], v[16:19], a[76:79]\n"
"s_waitcnt vmcnt(18) lgkmcnt(0)\n"
"v_exp_f32 v230, v230\n"
"v_mfma_f32_16x16x32_bf16 a[80:83], a[208:211], v[16:19], a[80:83]\n"
"s_barrier\n"
"v_mfma_f32_16x16x32_bf16 a[84:87], a[212:215], v[16:19], a[84:87]\n"
"v_exp_f32 v231, v231\n"
"v_mfma_f32_16x16x32_bf16 a[88:91], a[216:219], v[16:19], a[88:91]\n"
"v_cvt_pk_bf16_f32 v21, v230, v231\n"
"v_add_f32 v52, v52, v227\n"
"v_mfma_f32_16x16x32_bf16 a[92:95], a[220:223], v[16:19], a[92:95]\n"
"v_exp_f32 v232, v232\n"
"v_mfma_f32_16x16x32_bf16 v[0:3], a[160:163], v[64:67], v[24:27]\n"
"ds_read_b128 a[128:131], v241 offset:0\n"
"buffer_store_dwordx4 v[216:219], v207, s[28:31], s54 offen sc1\n"
"v_mfma_f32_16x16x32_bf16 v[0:3], a[164:167], v[68:71], v[0:3]\n"
"v_exp_f32 v233, v233\n"
"v_mfma_f32_16x16x32_bf16 v[0:3], a[168:171], v[72:75], v[0:3]\n"
"ds_read_b128 a[132:135], v241 offset:256\n"
"v_cvt_pk_bf16_f32 v22, v232, v233\n"
"v_mfma_f32_16x16x32_bf16 v[0:3], a[172:175], v[76:79], v[0:3]\n"
"v_exp_f32 v234, v234\n"
"v_mfma_f32_16x16x32_bf16 v[4:7], a[176:179], v[64:67], v[24:27]\n"
"ds_read_b128 a[136:139], v241 offset:512\n"
"v_mfma_f32_16x16x32_bf16 v[4:7], a[180:183], v[68:71], v[4:7]\n"
"v_exp_f32 v235, v235\n"
"v_mfma_f32_16x16x32_bf16 v[4:7], a[184:187], v[72:75], v[4:7]\n"
"ds_read_b128 a[140:143], v241 offset:768\n"
"v_cvt_pk_bf16_f32 v23, v234, v235\n"
"v_mfma_f32_16x16x32_bf16 v[4:7], a[188:191], v[76:79], v[4:7]\n"
"s_add_u32 m0, s86, 32768\n"
"v_exp_f32 v0, v0\n"
"v_mfma_f32_16x16x32_bf16 a[96:99], a[192:195], v[20:23], a[96:99]\n"
"buffer_load_dwordx4 v246, s[8:11], s84 offen offset:0 lds\n"
"v_mfma_f32_16x16x32_bf16 a[100:103], a[196:199], v[20:23], a[100:103]\n"
"v_exp_f32 v1, v1\n"
"v_mfma_f32_16x16x32_bf16 a[104:107], a[200:203], v[20:23], a[104:107]\n"
"ds_read_b128 a[144:147], v241 offset:4096\n"
"v_cvt_pk_bf16_f32 v16, v0, v1\n"
"v_mfma_f32_16x16x32_bf16 a[108:111], a[204:207], v[20:23], a[108:111]\n"
"v_exp_f32 v2, v2\n"
"v_mfma_f32_16x16x32_bf16 a[112:115], a[208:211], v[20:23], a[112:115]\n"
"ds_read_b128 a[148:151], v241 offset:4352\n"
"v_add_f32 v44, v44, v0\n"
"v_mfma_f32_16x16x32_bf16 a[116:119], a[212:215], v[20:23], a[116:119]\n"
"v_exp_f32 v3, v3\n"
"v_mfma_f32_16x16x32_bf16 a[120:123], a[216:219], v[20:23], a[120:123]\n"
"ds_read_b128 a[152:155], v241 offset:4608\n"
"v_cvt_pk_bf16_f32 v17, v2, v3\n"
"v_mfma_f32_16x16x32_bf16 a[124:127], a[220:223], v[20:23], a[124:127]\n"
"v_exp_f32 v4, v4\n"
"v_mfma_f32_16x16x32_bf16 v[56:59], v[40:43], v[20:23], v[56:59]\n"
"ds_read_b128 a[156:159], v241 offset:4864\n"
"v_add_f32 v44, v44, v1\n"
"v_mfma_f32_16x16x32_bf16 v[8:11], a[160:163], v[80:83], v[28:31]\n"
"v_exp_f32 v5, v5\n"
"v_mfma_f32_16x16x32_bf16 v[8:11], a[164:167], v[84:87], v[8:11]\n"
"ds_read_b64_tr_b16 a[192:193], v244 offset:16384\n"
"v_cvt_pk_bf16_f32 v18, v4, v5\n"
"v_mfma_f32_16x16x32_bf16 v[8:11], a[168:171], v[88:91], v[8:11]\n"
"v_exp_f32 v6, v6\n"
"v_mfma_f32_16x16x32_bf16 v[8:11], a[172:175], v[92:95], v[8:11]\n"
"ds_read_b64_tr_b16 a[194:195], v244 offset:20480\n"
"v_add_f32 v44, v44, v2\n"
"v_mfma_f32_16x16x32_bf16 v[12:15], a[176:179], v[80:83], v[28:31]\n"
"v_exp_f32 v7, v7\n"
"v_mfma_f32_16x16x32_bf16 v[12:15], a[180:183], v[84:87], v[12:15]\n"
"ds_read_b64_tr_b16 a[196:197], v245 offset:16384\n"
"v_cvt_pk_bf16_f32 v19, v6, v7\n"
"v_mfma_f32_16x16x32_bf16 v[12:15], a[184:187], v[88:91], v[12:15]\n"
"buffer_load_dwordx4 v246, s[8:11], s84 offen offset:1024 lds\n"
"v_mfma_f32_16x16x32_bf16 v[12:15], a[188:191], v[92:95], v[12:15]\n"
"s_waitcnt lgkmcnt(11)\n"
"v_exp_f32 v8, v8\n"
"v_mfma_f32_16x16x32_bf16 a[0:3], a[224:227], v[16:19], a[0:3]\n"
"ds_read_b64_tr_b16 a[198:199], v245 offset:20480\n"
"v_add_f32 v44, v44, v3\n"
"v_mfma_f32_16x16x32_bf16 a[4:7], a[228:231], v[16:19], a[4:7]\n"
"v_exp_f32 v9, v9\n"
"v_mfma_f32_16x16x32_bf16 a[8:11], a[232:235], v[16:19], a[8:11]\n"
"ds_read_b64_tr_b16 a[200:201], v244 offset:16640\n"
"v_cvt_pk_bf16_f32 v20, v8, v9\n"
"v_mfma_f32_16x16x32_bf16 a[12:15], a[236:239], v[16:19], a[12:15]\n"
"v_exp_f32 v10, v10\n"
"v_mfma_f32_16x16x32_bf16 a[16:19], a[240:243], v[16:19], a[16:19]\n"
"ds_read_b64_tr_b16 a[202:203], v244 offset:20736\n"
"v_add_f32 v44, v44, v4\n"
"v_mfma_f32_16x16x32_bf16 a[20:23], a[244:247], v[16:19], a[20:23]\n"
"v_exp_f32 v11, v11\n"
"v_mfma_f32_16x16x32_bf16 a[24:27], a[248:251], v[16:19], a[24:27]\n"
"ds_read_b64_tr_b16 a[204:205], v245 offset:16640\n"
"v_cvt_pk_bf16_f32 v21, v10, v11\n"
"v_mfma_f32_16x16x32_bf16 a[28:31], a[252:255], v[16:19], a[28:31]\n"
"v_exp_f32 v12, v12\n"
"v_mfma_f32_16x16x32_bf16 v[220:223], a[160:163], v[96:99], v[32:35]\n"
"ds_read_b64_tr_b16 a[206:207], v245 offset:20736\n"
"v_add_f32 v44, v44, v5\n"
"v_mfma_f32_16x16x32_bf16 v[220:223], a[164:167], v[100:103], v[220:223]\n"
"v_exp_f32 v13, v13\n"
"v_mfma_f32_16x16x32_bf16 v[220:223], a[168:171], v[104:107], v[220:223]\n"
"ds_read_b64_tr_b16 a[208:209], v244 offset:16896\n"
"v_cvt_pk_bf16_f32 v22, v12, v13\n"
"v_mfma_f32_16x16x32_bf16 v[220:223], a[172:175], v[108:111], v[220:223]\n"
"v_exp_f32 v14, v14\n"
"v_mfma_f32_16x16x32_bf16 v[224:227], a[176:179], v[96:99], v[32:35]\n"
"ds_read_b64_tr_b16 a[210:211], v244 offset:20992\n"
"v_add_f32 v44, v44, v6\n"
"v_mfma_f32_16x16x32_bf16 v[224:227], a[180:183], v[100:103], v[224:227]\n"
"v_exp_f32 v15, v15\n"
"v_mfma_f32_16x16x32_bf16 v[224:227], a[184:187], v[104:107], v[224:227]\n"
"ds_read_b64_tr_b16 a[212:213], v245 offset:16896\n"
"v_cvt_pk_bf16_f32 v23, v14, v15\n"
"v_mfma_f32_16x16x32_bf16 v[224:227], a[188:191], v[108:111], v[224:227]\n"
"v_exp_f32 v220, v220\n"
"v_mfma_f32_16x16x32_bf16 a[32:35], a[224:227], v[20:23], a[32:35]\n"
"buffer_load_dwordx4 v247, s[8:11], s84 offen offset:2048 lds\n"
"v_mfma_f32_16x16x32_bf16 a[36:39], a[228:231], v[20:23], a[36:39]\n"
"v_exp_f32 v221, v221\n"
"v_mfma_f32_16x16x32_bf16 a[40:43], a[232:235], v[20:23], a[40:43]\n"
"ds_read_b64_tr_b16 a[214:215], v245 offset:20992\n"
"v_cvt_pk_bf16_f32 v16, v220, v221\n"
"v_mfma_f32_16x16x32_bf16 a[44:47], a[236:239], v[20:23], a[44:47]\n"
"v_exp_f32 v222, v222\n"
"v_mfma_f32_16x16x32_bf16 a[48:51], a[240:243], v[20:23], a[48:51]\n"
"ds_read_b64_tr_b16 a[216:217], v244 offset:17152\n"
"v_add_f32 v44, v44, v7\n"
"v_mfma_f32_16x16x32_bf16 a[52:55], a[244:247], v[20:23], a[52:55]\n"
"v_exp_f32 v223, v223\n"
"v_mfma_f32_16x16x32_bf16 a[56:59], a[248:251], v[20:23], a[56:59]\n"
"ds_read_b64_tr_b16 a[218:219], v244 offset:21248\n"
"v_cvt_pk_bf16_f32 v17, v222, v223\n"
"v_mfma_f32_16x16x32_bf16 a[60:63], a[252:255], v[20:23], a[60:63]\n"
"v_exp_f32 v224, v224\n"
"v_mfma_f32_16x16x32_bf16 v[48:51], v[40:43], v[20:23], v[48:51]\n"
"ds_read_b64_tr_b16 a[220:221], v245 offset:17152\n"
"v_add_f32 v52, v52, v220\n"
"v_mfma_f32_16x16x32_bf16 v[228:231], a[160:163], v[112:115], v[36:39]\n"
"v_exp_f32 v225, v225\n"
"v_mfma_f32_16x16x32_bf16 v[228:231], a[164:167], v[116:119], v[228:231]\n"
"ds_read_b64_tr_b16 a[222:223], v245 offset:21248\n"
"v_cvt_pk_bf16_f32 v18, v224, v225\n"
"v_mfma_f32_16x16x32_bf16 v[228:231], a[168:171], v[120:123], v[228:231]\n"
"v_exp_f32 v226, v226\n"
"v_mfma_f32_16x16x32_bf16 v[228:231], a[172:175], v[124:127], v[228:231]\n"
"v_add_f32 v52, v52, v221\n"
"v_add_f32 v52, v52, v222\n"
"v_mfma_f32_16x16x32_bf16 v[232:235], a[176:179], v[112:115], v[36:39]\n"
"v_exp_f32 v227, v227\n"
"v_mfma_f32_16x16x32_bf16 v[232:235], a[180:183], v[116:119], v[232:235]\n"
"v_cvt_pk_bf16_f32 v19, v226, v227\n"
"v_add_f32 v52, v52, v223\n"
"v_mfma_f32_16x16x32_bf16 v[232:235], a[184:187], v[120:123], v[232:235]\n"
"buffer_load_dwordx4 v247, s[8:11], s84 offen offset:3072 lds\n"
"v_mfma_f32_16x16x32_bf16 v[232:235], a[188:191], v[124:127], v[232:235]\n"
"v_exp_f32 v228, v228\n"
"v_mfma_f32_16x16x32_bf16 a[64:67], a[224:227], v[16:19], a[64:67]\n"
"v_add_f32 v52, v52, v224\n"
"v_add_f32 v52, v52, v225\n"
"v_mfma_f32_16x16x32_bf16 a[68:71], a[228:231], v[16:19], a[68:71]\n"
"v_exp_f32 v229, v229\n"
"v_mfma_f32_16x16x32_bf16 a[72:75], a[232:235], v[16:19], a[72:75]\n"
"v_cvt_pk_bf16_f32 v20, v228, v229\n"
"v_add_f32 v52, v52, v226\n"
"v_mfma_f32_16x16x32_bf16 a[76:79], a[236:239], v[16:19], a[76:79]\n"
"v_exp_f32 v230, v230\n"
"v_mfma_f32_16x16x32_bf16 a[80:83], a[240:243], v[16:19], a[80:83]\n"
"s_waitcnt lgkmcnt(8)\n"
"buffer_load_dword v250, v251, s[68:71], 0 offen\n"
"v_mfma_f32_16x16x32_bf16 a[84:87], a[244:247], v[16:19], a[84:87]\n"
"v_exp_f32 v231, v231\n"
"v_mfma_f32_16x16x32_bf16 a[88:91], a[248:251], v[16:19], a[88:91]\n"
"v_cvt_pk_bf16_f32 v21, v230, v231\n"
"v_add_f32 v52, v52, v227\n"
"v_mfma_f32_16x16x32_bf16 a[92:95], a[252:255], v[16:19], a[92:95]\n"
"v_exp_f32 v232, v232\n"
"v_mfma_f32_16x16x32_bf16 v[0:3], a[128:131], v[64:67], v[24:27]\n"
"ds_read_b128 a[160:163], v241 offset:8192\n"
"v_add_u32 v251, 1179648, v251\n"
"v_mfma_f32_16x16x32_bf16 v[0:3], a[132:135], v[68:71], v[0:3]\n"
"v_exp_f32 v233, v233\n"
"v_mfma_f32_16x16x32_bf16 v[0:3], a[136:139], v[72:75], v[0:3]\n"
"ds_read_b128 a[164:167], v241 offset:8448\n"
"v_cvt_pk_bf16_f32 v22, v232, v233\n"
"v_mfma_f32_16x16x32_bf16 v[0:3], a[140:143], v[76:79], v[0:3]\n"
"v_exp_f32 v234, v234\n"
"v_mfma_f32_16x16x32_bf16 v[4:7], a[144:147], v[64:67], v[24:27]\n"
"ds_read_b128 a[168:171], v241 offset:8704\n"
"v_mfma_f32_16x16x32_bf16 v[4:7], a[148:151], v[68:71], v[4:7]\n"
"v_exp_f32 v235, v235\n"
"v_mfma_f32_16x16x32_bf16 v[4:7], a[152:155], v[72:75], v[4:7]\n"
"ds_read_b128 a[172:175], v241 offset:8960\n"
"v_cvt_pk_bf16_f32 v23, v234, v235\n"
"v_mfma_f32_16x16x32_bf16 v[4:7], a[156:159], v[76:79], v[4:7]\n"
"s_add_u32 m0, s86, 49152\n"
"v_exp_f32 v0, v0\n"
"v_mfma_f32_16x16x32_bf16 a[96:99], a[224:227], v[20:23], a[96:99]\n"
"buffer_load_dwordx4 v246, s[12:15], s84 offen offset:0 lds\n"
"v_mfma_f32_16x16x32_bf16 a[100:103], a[228:231], v[20:23], a[100:103]\n"
"v_exp_f32 v1, v1\n"
"v_mfma_f32_16x16x32_bf16 a[104:107], a[232:235], v[20:23], a[104:107]\n"
"ds_read_b128 a[176:179], v241 offset:12288\n"
"v_cvt_pk_bf16_f32 v16, v0, v1\n"
"v_mfma_f32_16x16x32_bf16 a[108:111], a[236:239], v[20:23], a[108:111]\n"
"v_exp_f32 v2, v2\n"
"v_mfma_f32_16x16x32_bf16 a[112:115], a[240:243], v[20:23], a[112:115]\n"
"ds_read_b128 a[180:183], v241 offset:12544\n"
"v_add_f32 v44, v44, v0\n"
"v_mfma_f32_16x16x32_bf16 a[116:119], a[244:247], v[20:23], a[116:119]\n"
"v_exp_f32 v3, v3\n"
"v_mfma_f32_16x16x32_bf16 a[120:123], a[248:251], v[20:23], a[120:123]\n"
"ds_read_b128 a[184:187], v241 offset:12800\n"
"v_cvt_pk_bf16_f32 v17, v2, v3\n"
"v_mfma_f32_16x16x32_bf16 a[124:127], a[252:255], v[20:23], a[124:127]\n"
"v_exp_f32 v4, v4\n"
"v_mfma_f32_16x16x32_bf16 v[56:59], v[40:43], v[20:23], v[56:59]\n"
"ds_read_b128 a[188:191], v241 offset:13056\n"
"v_add_f32 v44, v44, v1\n"
"v_mfma_f32_16x16x32_bf16 v[8:11], a[128:131], v[80:83], v[28:31]\n"
"v_exp_f32 v5, v5\n"
"v_mfma_f32_16x16x32_bf16 v[8:11], a[132:135], v[84:87], v[8:11]\n"
"ds_read_b64_tr_b16 a[224:225], v244 offset:24576\n"
"v_cvt_pk_bf16_f32 v18, v4, v5\n"
"v_mfma_f32_16x16x32_bf16 v[8:11], a[136:139], v[88:91], v[8:11]\n"
"v_exp_f32 v6, v6\n"
"v_mfma_f32_16x16x32_bf16 v[8:11], a[140:143], v[92:95], v[8:11]\n"
"ds_read_b64_tr_b16 a[226:227], v244 offset:28672\n"
"v_add_f32 v44, v44, v2\n"
"v_mfma_f32_16x16x32_bf16 v[12:15], a[144:147], v[80:83], v[28:31]\n"
"v_exp_f32 v7, v7\n"
"v_mfma_f32_16x16x32_bf16 v[12:15], a[148:151], v[84:87], v[12:15]\n"
"ds_read_b64_tr_b16 a[228:229], v245 offset:24576\n"
"v_cvt_pk_bf16_f32 v19, v6, v7\n"
"v_mfma_f32_16x16x32_bf16 v[12:15], a[152:155], v[88:91], v[12:15]\n"
"buffer_load_dwordx4 v247, s[12:15], s84 offen offset:1024 lds\n"
"v_mfma_f32_16x16x32_bf16 v[12:15], a[156:159], v[92:95], v[12:15]\n"
"s_waitcnt lgkmcnt(11)\n"
"v_exp_f32 v8, v8\n"
"v_mfma_f32_16x16x32_bf16 a[0:3], a[192:195], v[16:19], a[0:3]\n"
"ds_read_b64_tr_b16 a[230:231], v245 offset:28672\n"
"v_add_f32 v44, v44, v3\n"
"v_mfma_f32_16x16x32_bf16 a[4:7], a[196:199], v[16:19], a[4:7]\n"
"v_exp_f32 v9, v9\n"
"v_mfma_f32_16x16x32_bf16 a[8:11], a[200:203], v[16:19], a[8:11]\n"
"ds_read_b64_tr_b16 a[232:233], v244 offset:24832\n"
"v_cvt_pk_bf16_f32 v20, v8, v9\n"
"v_mfma_f32_16x16x32_bf16 a[12:15], a[204:207], v[16:19], a[12:15]\n"
"v_exp_f32 v10, v10\n"
"v_mfma_f32_16x16x32_bf16 a[16:19], a[208:211], v[16:19], a[16:19]\n"
"ds_read_b64_tr_b16 a[234:235], v244 offset:28928\n"
"v_add_f32 v44, v44, v4\n"
"v_mfma_f32_16x16x32_bf16 a[20:23], a[212:215], v[16:19], a[20:23]\n"
"v_exp_f32 v11, v11\n"
"v_mfma_f32_16x16x32_bf16 a[24:27], a[216:219], v[16:19], a[24:27]\n"
"ds_read_b64_tr_b16 a[236:237], v245 offset:24832\n"
"v_cvt_pk_bf16_f32 v21, v10, v11\n"
"v_mfma_f32_16x16x32_bf16 a[28:31], a[220:223], v[16:19], a[28:31]\n"
"v_exp_f32 v12, v12\n"
"v_mfma_f32_16x16x32_bf16 v[220:223], a[128:131], v[96:99], v[32:35]\n"
"ds_read_b64_tr_b16 a[238:239], v245 offset:28928\n"
"v_add_f32 v44, v44, v5\n"
"v_mfma_f32_16x16x32_bf16 v[220:223], a[132:135], v[100:103], v[220:223]\n"
"v_exp_f32 v13, v13\n"
"v_mfma_f32_16x16x32_bf16 v[220:223], a[136:139], v[104:107], v[220:223]\n"
"ds_read_b64_tr_b16 a[240:241], v244 offset:25088\n"
"v_cvt_pk_bf16_f32 v22, v12, v13\n"
"v_mfma_f32_16x16x32_bf16 v[220:223], a[140:143], v[108:111], v[220:223]\n"
"v_exp_f32 v14, v14\n"
"v_mfma_f32_16x16x32_bf16 v[224:227], a[144:147], v[96:99], v[32:35]\n"
"ds_read_b64_tr_b16 a[242:243], v244 offset:29184\n"
"v_add_f32 v44, v44, v6\n"
"v_mfma_f32_16x16x32_bf16 v[224:227], a[148:151], v[100:103], v[224:227]\n"
"v_exp_f32 v15, v15\n"
"v_mfma_f32_16x16x32_bf16 v[224:227], a[152:155], v[104:107], v[224:227]\n"
"ds_read_b64_tr_b16 a[244:245], v245 offset:25088\n"
"v_cvt_pk_bf16_f32 v23, v14, v15\n"
"v_mfma_f32_16x16x32_bf16 v[224:227], a[156:159], v[108:111], v[224:227]\n"
"v_exp_f32 v220, v220\n"
"v_mfma_f32_16x16x32_bf16 a[32:35], a[192:195], v[20:23], a[32:35]\n"
"buffer_load_dwordx4 v246, s[12:15], s84 offen offset:2048 lds\n"
"v_mfma_f32_16x16x32_bf16 a[36:39], a[196:199], v[20:23], a[36:39]\n"
"v_exp_f32 v221, v221\n"
"v_mfma_f32_16x16x32_bf16 a[40:43], a[200:203], v[20:23], a[40:43]\n"
"ds_read_b64_tr_b16 a[246:247], v245 offset:29184\n"
"v_cvt_pk_bf16_f32 v16, v220, v221\n"
"v_mfma_f32_16x16x32_bf16 a[44:47], a[204:207], v[20:23], a[44:47]\n"
"v_exp_f32 v222, v222\n"
"v_mfma_f32_16x16x32_bf16 a[48:51], a[208:211], v[20:23], a[48:51]\n"
"ds_read_b64_tr_b16 a[248:249], v244 offset:25344\n"
"v_add_f32 v44, v44, v7\n"
"v_mfma_f32_16x16x32_bf16 a[52:55], a[212:215], v[20:23], a[52:55]\n"
"v_exp_f32 v223, v223\n"
"v_mfma_f32_16x16x32_bf16 a[56:59], a[216:219], v[20:23], a[56:59]\n"
"ds_read_b64_tr_b16 a[250:251], v244 offset:29440\n"
"v_cvt_pk_bf16_f32 v17, v222, v223\n"
"v_mfma_f32_16x16x32_bf16 a[60:63], a[220:223], v[20:23], a[60:63]\n"
"v_exp_f32 v224, v224\n"
"v_mfma_f32_16x16x32_bf16 v[48:51], v[40:43], v[20:23], v[48:51]\n"
"ds_read_b64_tr_b16 a[252:253], v245 offset:25344\n"
"v_add_f32 v52, v52, v220\n"
"v_mfma_f32_16x16x32_bf16 v[228:231], a[128:131], v[112:115], v[36:39]\n"
"v_exp_f32 v225, v225\n"
"v_mfma_f32_16x16x32_bf16 v[228:231], a[132:135], v[116:119], v[228:231]\n"
"ds_read_b64_tr_b16 a[254:255], v245 offset:29440\n"
"v_cvt_pk_bf16_f32 v18, v224, v225\n"
"v_mfma_f32_16x16x32_bf16 v[228:231], a[136:139], v[120:123], v[228:231]\n"
"v_exp_f32 v226, v226\n"
"v_mfma_f32_16x16x32_bf16 v[228:231], a[140:143], v[124:127], v[228:231]\n"
"v_add_f32 v52, v52, v221\n"
"v_add_f32 v52, v52, v222\n"
"v_mfma_f32_16x16x32_bf16 v[232:235], a[144:147], v[112:115], v[36:39]\n"
"v_exp_f32 v227, v227\n"
"v_mfma_f32_16x16x32_bf16 v[232:235], a[148:151], v[116:119], v[232:235]\n"
"v_cvt_pk_bf16_f32 v19, v226, v227\n"
"v_add_f32 v52, v52, v223\n"
"v_mfma_f32_16x16x32_bf16 v[232:235], a[152:155], v[120:123], v[232:235]\n"
"buffer_load_dwordx4 v247, s[12:15], s84 offen offset:3072 lds\n"
"v_mfma_f32_16x16x32_bf16 v[232:235], a[156:159], v[124:127], v[232:235]\n"
"s_add_u32 s84, s84, 16384\n"
"v_exp_f32 v228, v228\n"
"v_mfma_f32_16x16x32_bf16 a[64:67], a[192:195], v[16:19], a[64:67]\n"
"v_add_f32 v52, v52, v224\n"
"v_add_f32 v52, v52, v225\n"
"v_mfma_f32_16x16x32_bf16 a[68:71], a[196:199], v[16:19], a[68:71]\n"
"v_exp_f32 v229, v229\n"
"v_mfma_f32_16x16x32_bf16 a[72:75], a[200:203], v[16:19], a[72:75]\n"
"v_cvt_pk_bf16_f32 v20, v228, v229\n"
"v_add_f32 v52, v52, v226\n"
"v_mfma_f32_16x16x32_bf16 a[76:79], a[204:207], v[16:19], a[76:79]\n"
"s_waitcnt vmcnt(20) lgkmcnt(0)\n"
"v_exp_f32 v230, v230\n"
"v_mfma_f32_16x16x32_bf16 a[80:83], a[208:211], v[16:19], a[80:83]\n"
"s_barrier\n"
"v_mfma_f32_16x16x32_bf16 a[84:87], a[212:215], v[16:19], a[84:87]\n"
"v_exp_f32 v231, v231\n"
"v_mfma_f32_16x16x32_bf16 a[88:91], a[216:219], v[16:19], a[88:91]\n"
"v_cvt_pk_bf16_f32 v21, v230, v231\n"
"v_add_f32 v52, v52, v227\n"
"v_mfma_f32_16x16x32_bf16 a[92:95], a[220:223], v[16:19], a[92:95]\n"
"v_exp_f32 v232, v232\n"
"v_mfma_f32_16x16x32_bf16 v[0:3], a[160:163], v[64:67], v[24:27]\n"
"ds_read_b128 a[128:131], v241 offset:32768\n"
"buffer_store_dwordx4 v[216:219], v207, s[28:31], s52 offen sc1\n"
"v_mfma_f32_16x16x32_bf16 v[0:3], a[164:167], v[68:71], v[0:3]\n"
"s_add_u32 s52, s52, 1024\n"
"v_exp_f32 v233, v233\n"
"v_mfma_f32_16x16x32_bf16 v[0:3], a[168:171], v[72:75], v[0:3]\n"
"s_min_u32 s52, s52, s53\n"
"ds_read_b128 a[132:135], v241 offset:33024\n"
"v_cvt_pk_bf16_f32 v22, v232, v233\n"
"v_mfma_f32_16x16x32_bf16 v[0:3], a[172:175], v[76:79], v[0:3]\n"
"v_exp_f32 v234, v234\n"
"v_mfma_f32_16x16x32_bf16 v[4:7], a[176:179], v[64:67], v[24:27]\n"
"ds_read_b128 a[136:139], v241 offset:33280\n"
"v_mfma_f32_16x16x32_bf16 v[4:7], a[180:183], v[68:71], v[4:7]\n"
"v_exp_f32 v235, v235\n"
"v_mfma_f32_16x16x32_bf16 v[4:7], a[184:187], v[72:75], v[4:7]\n"
"ds_read_b128 a[140:143], v241 offset:33536\n"
"v_cvt_pk_bf16_f32 v23, v234, v235\n"
"v_mfma_f32_16x16x32_bf16 v[4:7], a[188:191], v[76:79], v[4:7]\n"
"s_add_u32 m0, s86, 65536\n"
"v_exp_f32 v0, v0\n"
"v_mfma_f32_16x16x32_bf16 a[96:99], a[192:195], v[20:23], a[96:99]\n"
"buffer_load_dwordx4 v246, s[8:11], s84 offen offset:0 lds\n"
"v_mfma_f32_16x16x32_bf16 a[100:103], a[196:199], v[20:23], a[100:103]\n"
"v_exp_f32 v1, v1\n"
"v_mfma_f32_16x16x32_bf16 a[104:107], a[200:203], v[20:23], a[104:107]\n"
"ds_read_b128 a[144:147], v241 offset:36864\n"
"v_cvt_pk_bf16_f32 v16, v0, v1\n"
"v_mfma_f32_16x16x32_bf16 a[108:111], a[204:207], v[20:23], a[108:111]\n"
"v_exp_f32 v2, v2\n"
"v_mfma_f32_16x16x32_bf16 a[112:115], a[208:211], v[20:23], a[112:115]\n"
"ds_read_b128 a[148:151], v241 offset:37120\n"
"v_add_f32 v44, v44, v0\n"
"v_mfma_f32_16x16x32_bf16 a[116:119], a[212:215], v[20:23], a[116:119]\n"
"v_exp_f32 v3, v3\n"
"v_mfma_f32_16x16x32_bf16 a[120:123], a[216:219], v[20:23], a[120:123]\n"
"ds_read_b128 a[152:155], v241 offset:37376\n"
"v_cvt_pk_bf16_f32 v17, v2, v3\n"
"v_mfma_f32_16x16x32_bf16 a[124:127], a[220:223], v[20:23], a[124:127]\n"
"v_exp_f32 v4, v4\n"
"v_mfma_f32_16x16x32_bf16 v[56:59], v[40:43], v[20:23], v[56:59]\n"
"ds_read_b128 a[156:159], v241 offset:37632\n"
"v_add_f32 v44, v44, v1\n"
"v_mfma_f32_16x16x32_bf16 v[8:11], a[160:163], v[80:83], v[28:31]\n"
"v_exp_f32 v5, v5\n"
"v_mfma_f32_16x16x32_bf16 v[8:11], a[164:167], v[84:87], v[8:11]\n"
"ds_read_b64_tr_b16 a[192:193], v244 offset:49152\n"
"v_cvt_pk_bf16_f32 v18, v4, v5\n"
"v_mfma_f32_16x16x32_bf16 v[8:11], a[168:171], v[88:91], v[8:11]\n"
"v_exp_f32 v6, v6\n"
"v_mfma_f32_16x16x32_bf16 v[8:11], a[172:175], v[92:95], v[8:11]\n"
"ds_read_b64_tr_b16 a[194:195], v244 offset:53248\n"
"v_add_f32 v44, v44, v2\n"
"v_mfma_f32_16x16x32_bf16 v[12:15], a[176:179], v[80:83], v[28:31]\n"
"v_exp_f32 v7, v7\n"
"v_mfma_f32_16x16x32_bf16 v[12:15], a[180:183], v[84:87], v[12:15]\n"
"ds_read_b64_tr_b16 a[196:197], v245 offset:49152\n"
"v_cvt_pk_bf16_f32 v19, v6, v7\n"
"v_mfma_f32_16x16x32_bf16 v[12:15], a[184:187], v[88:91], v[12:15]\n"
"buffer_load_dwordx4 v246, s[8:11], s84 offen offset:1024 lds\n"
"v_mfma_f32_16x16x32_bf16 v[12:15], a[188:191], v[92:95], v[12:15]\n"
"s_waitcnt lgkmcnt(11)\n"
"v_exp_f32 v8, v8\n"
"v_mfma_f32_16x16x32_bf16 a[0:3], a[224:227], v[16:19], a[0:3]\n"
"ds_read_b64_tr_b16 a[198:199], v245 offset:53248\n"
"v_add_f32 v44, v44, v3\n"
"v_mfma_f32_16x16x32_bf16 a[4:7], a[228:231], v[16:19], a[4:7]\n"
"v_exp_f32 v9, v9\n"
"v_mfma_f32_16x16x32_bf16 a[8:11], a[232:235], v[16:19], a[8:11]\n"
"ds_read_b64_tr_b16 a[200:201], v244 offset:49408\n"
"v_cvt_pk_bf16_f32 v20, v8, v9\n"
"v_mfma_f32_16x16x32_bf16 a[12:15], a[236:239], v[16:19], a[12:15]\n"
"v_exp_f32 v10, v10\n"
"v_mfma_f32_16x16x32_bf16 a[16:19], a[240:243], v[16:19], a[16:19]\n"
"ds_read_b64_tr_b16 a[202:203], v244 offset:53504\n"
"v_add_f32 v44, v44, v4\n"
"v_mfma_f32_16x16x32_bf16 a[20:23], a[244:247], v[16:19], a[20:23]\n"
"v_exp_f32 v11, v11\n"
"v_mfma_f32_16x16x32_bf16 a[24:27], a[248:251], v[16:19], a[24:27]\n"
"ds_read_b64_tr_b16 a[204:205], v245 offset:49408\n"
"v_cvt_pk_bf16_f32 v21, v10, v11\n"
"v_mfma_f32_16x16x32_bf16 a[28:31], a[252:255], v[16:19], a[28:31]\n"
"v_exp_f32 v12, v12\n"
"v_mfma_f32_16x16x32_bf16 v[220:223], a[160:163], v[96:99], v[32:35]\n"
"ds_read_b64_tr_b16 a[206:207], v245 offset:53504\n"
"v_add_f32 v44, v44, v5\n"
"v_mfma_f32_16x16x32_bf16 v[220:223], a[164:167], v[100:103], v[220:223]\n"
"v_exp_f32 v13, v13\n"
"v_mfma_f32_16x16x32_bf16 v[220:223], a[168:171], v[104:107], v[220:223]\n"
"ds_read_b64_tr_b16 a[208:209], v244 offset:49664\n"
"v_cvt_pk_bf16_f32 v22, v12, v13\n"
"v_mfma_f32_16x16x32_bf16 v[220:223], a[172:175], v[108:111], v[220:223]\n"
"v_exp_f32 v14, v14\n"
"v_mfma_f32_16x16x32_bf16 v[224:227], a[176:179], v[96:99], v[32:35]\n"
"ds_read_b64_tr_b16 a[210:211], v244 offset:53760\n"
"v_add_f32 v44, v44, v6\n"
"v_mfma_f32_16x16x32_bf16 v[224:227], a[180:183], v[100:103], v[224:227]\n"
"v_exp_f32 v15, v15\n"
"v_mfma_f32_16x16x32_bf16 v[224:227], a[184:187], v[104:107], v[224:227]\n"
"ds_read_b64_tr_b16 a[212:213], v245 offset:49664\n"
"v_cvt_pk_bf16_f32 v23, v14, v15\n"
"v_mfma_f32_16x16x32_bf16 v[224:227], a[188:191], v[108:111], v[224:227]\n"
"v_exp_f32 v220, v220\n"
"v_mfma_f32_16x16x32_bf16 a[32:35], a[224:227], v[20:23], a[32:35]\n"
"buffer_load_dwordx4 v247, s[8:11], s84 offen offset:2048 lds\n"
"v_mfma_f32_16x16x32_bf16 a[36:39], a[228:231], v[20:23], a[36:39]\n"
"v_exp_f32 v221, v221\n"
"v_mfma_f32_16x16x32_bf16 a[40:43], a[232:235], v[20:23], a[40:43]\n"
"ds_read_b64_tr_b16 a[214:215], v245 offset:53760\n"
"v_cvt_pk_bf16_f32 v16, v220, v221\n"
"v_mfma_f32_16x16x32_bf16 a[44:47], a[236:239], v[20:23], a[44:47]\n"
"v_exp_f32 v222, v222\n"
"v_mfma_f32_16x16x32_bf16 a[48:51], a[240:243], v[20:23], a[48:51]\n"
"ds_read_b64_tr_b16 a[216:217], v244 offset:49920\n"
"v_add_f32 v44, v44, v7\n"
"v_mfma_f32_16x16x32_bf16 a[52:55], a[244:247], v[20:23], a[52:55]\n"
"v_exp_f32 v223, v223\n"
"v_mfma_f32_16x16x32_bf16 a[56:59], a[248:251], v[20:23], a[56:59]\n"
"ds_read_b64_tr_b16 a[218:219], v244 offset:54016\n"
"v_cvt_pk_bf16_f32 v17, v222, v223\n"
"v_mfma_f32_16x16x32_bf16 a[60:63], a[252:255], v[20:23], a[60:63]\n"
"v_exp_f32 v224, v224\n"
"v_mfma_f32_16x16x32_bf16 v[48:51], v[40:43], v[20:23], v[48:51]\n"
"ds_read_b64_tr_b16 a[220:221], v245 offset:49920\n"
"v_add_f32 v52, v52, v220\n"
"v_mfma_f32_16x16x32_bf16 v[228:231], a[160:163], v[112:115], v[36:39]\n"
"v_exp_f32 v225, v225\n"
"v_mfma_f32_16x16x32_bf16 v[228:231], a[164:167], v[116:119], v[228:231]\n"
"ds_read_b64_tr_b16 a[222:223], v245 offset:54016\n"
"v_cvt_pk_bf16_f32 v18, v224, v225\n"
"v_mfma_f32_16x16x32_bf16 v[228:231], a[168:171], v[120:123], v[228:231]\n"
"v_exp_f32 v226, v226\n"
"v_mfma_f32_16x16x32_bf16 v[228:231], a[172:175], v[124:127], v[228:231]\n"
"v_add_f32 v52, v52, v221\n"
"v_add_f32 v52, v52, v222\n"
"v_mfma_f32_16x16x32_bf16 v[232:235], a[176:179], v[112:115], v[36:39]\n"
"v_exp_f32 v227, v227\n"
"v_mfma_f32_16x16x32_bf16 v[232:235], a[180:183], v[116:119], v[232:235]\n"
"v_cvt_pk_bf16_f32 v19, v226, v227\n"
"v_add_f32 v52, v52, v223\n"
"v_mfma_f32_16x16x32_bf16 v[232:235], a[184:187], v[120:123], v[232:235]\n"
"buffer_load_dwordx4 v247, s[8:11], s84 offen offset:3072 lds\n"
"v_mfma_f32_16x16x32_bf16 v[232:235], a[188:191], v[124:127], v[232:235]\n"
"v_exp_f32 v228, v228\n"
"v_mfma_f32_16x16x32_bf16 a[64:67], a[224:227], v[16:19], a[64:67]\n"
"v_add_f32 v52, v52, v224\n"
"v_add_f32 v52, v52, v225\n"
"v_mfma_f32_16x16x32_bf16 a[68:71], a[228:231], v[16:19], a[68:71]\n"
"v_exp_f32 v229, v229\n"
"v_mfma_f32_16x16x32_bf16 a[72:75], a[232:235], v[16:19], a[72:75]\n"
"v_cvt_pk_bf16_f32 v20, v228, v229\n"
"v_add_f32 v52, v52, v226\n"
"v_mfma_f32_16x16x32_bf16 a[76:79], a[236:239], v[16:19], a[76:79]\n"
"v_exp_f32 v230, v230\n"
"v_mfma_f32_16x16x32_bf16 a[80:83], a[240:243], v[16:19], a[80:83]\n"
"s_waitcnt lgkmcnt(8)\n"
"buffer_load_dword v250, v252, s[72:75], s46 offen\n"
"v_mfma_f32_16x16x32_bf16 a[84:87], a[244:247], v[16:19], a[84:87]\n"
"v_exp_f32 v231, v231\n"
"v_mfma_f32_16x16x32_bf16 a[88:91], a[248:251], v[16:19], a[88:91]\n"
"v_cvt_pk_bf16_f32 v21, v230, v231\n"
"v_add_f32 v52, v52, v227\n"
"v_mfma_f32_16x16x32_bf16 a[92:95], a[252:255], v[16:19], a[92:95]\n"
"v_exp_f32 v232, v232\n"
"v_mfma_f32_16x16x32_bf16 v[0:3], a[128:131], v[64:67], v[24:27]\n"
"ds_read_b128 a[160:163], v241 offset:40960\n"
"v_add_u32 v252, 147456, v252\n"
"v_mfma_f32_16x16x32_bf16 v[0:3], a[132:135], v[68:71], v[0:3]\n"
"v_exp_f32 v233, v233\n"
"v_mfma_f32_16x16x32_bf16 v[0:3], a[136:139], v[72:75], v[0:3]\n"
"ds_read_b128 a[164:167], v241 offset:41216\n"
"v_cvt_pk_bf16_f32 v22, v232, v233\n"
"v_mfma_f32_16x16x32_bf16 v[0:3], a[140:143], v[76:79], v[0:3]\n"
"v_exp_f32 v234, v234\n"
"v_mfma_f32_16x16x32_bf16 v[4:7], a[144:147], v[64:67], v[24:27]\n"
"ds_read_b128 a[168:171], v241 offset:41472\n"
"v_mfma_f32_16x16x32_bf16 v[4:7], a[148:151], v[68:71], v[4:7]\n"
"v_exp_f32 v235, v235\n"
"v_mfma_f32_16x16x32_bf16 v[4:7], a[152:155], v[72:75], v[4:7]\n"
"ds_read_b128 a[172:175], v241 offset:41728\n"
"v_cvt_pk_bf16_f32 v23, v234, v235\n"
"v_mfma_f32_16x16x32_bf16 v[4:7], a[156:159], v[76:79], v[4:7]\n"
"s_add_u32 m0, s86, 81920\n"
"v_exp_f32 v0, v0\n"
"v_mfma_f32_16x16x32_bf16 a[96:99], a[224:227], v[20:23], a[96:99]\n"
"buffer_load_dwordx4 v246, s[12:15], s84 offen offset:0 lds\n"
"v_mfma_f32_16x16x32_bf16 a[100:103], a[228:231], v[20:23], a[100:103]\n"
"v_exp_f32 v1, v1\n"
"v_mfma_f32_16x16x32_bf16 a[104:107], a[232:235], v[20:23], a[104:107]\n"
"ds_read_b128 a[176:179], v241 offset:45056\n"
"v_cvt_pk_bf16_f32 v16, v0, v1\n"
"v_mfma_f32_16x16x32_bf16 a[108:111], a[236:239], v[20:23], a[108:111]\n"
"v_exp_f32 v2, v2\n"
"v_mfma_f32_16x16x32_bf16 a[112:115], a[240:243], v[20:23], a[112:115]\n"
"ds_read_b128 a[180:183], v241 offset:45312\n"
"v_add_f32 v44, v44, v0\n"
"v_mfma_f32_16x16x32_bf16 a[116:119], a[244:247], v[20:23], a[116:119]\n"
"v_exp_f32 v3, v3\n"
"v_mfma_f32_16x16x32_bf16 a[120:123], a[248:251], v[20:23], a[120:123]\n"
"ds_read_b128 a[184:187], v241 offset:45568\n"
"v_cvt_pk_bf16_f32 v17, v2, v3\n"
"v_mfma_f32_16x16x32_bf16 a[124:127], a[252:255], v[20:23], a[124:127]\n"
"v_exp_f32 v4, v4\n"
"v_mfma_f32_16x16x32_bf16 v[56:59], v[40:43], v[20:23], v[56:59]\n"
"ds_read_b128 a[188:191], v241 offset:45824\n"
"v_add_f32 v44, v44, v1\n"
"v_mfma_f32_16x16x32_bf16 v[8:11], a[128:131], v[80:83], v[28:31]\n"
"v_exp_f32 v5, v5\n"
"v_mfma_f32_16x16x32_bf16 v[8:11], a[132:135], v[84:87], v[8:11]\n"
"ds_read_b64_tr_b16 a[224:225], v244 offset:57344\n"
"v_cvt_pk_bf16_f32 v18, v4, v5\n"
"v_mfma_f32_16x16x32_bf16 v[8:11], a[136:139], v[88:91], v[8:11]\n"
"v_exp_f32 v6, v6\n"
"v_mfma_f32_16x16x32_bf16 v[8:11], a[140:143], v[92:95], v[8:11]\n"
"ds_read_b64_tr_b16 a[226:227], v244 offset:61440\n"
"v_add_f32 v44, v44, v2\n"
"v_mfma_f32_16x16x32_bf16 v[12:15], a[144:147], v[80:83], v[28:31]\n"
"v_exp_f32 v7, v7\n"
"v_mfma_f32_16x16x32_bf16 v[12:15], a[148:151], v[84:87], v[12:15]\n"
"ds_read_b64_tr_b16 a[228:229], v245 offset:57344\n"
"v_cvt_pk_bf16_f32 v19, v6, v7\n"
"v_mfma_f32_16x16x32_bf16 v[12:15], a[152:155], v[88:91], v[12:15]\n"
"buffer_load_dwordx4 v247, s[12:15], s84 offen offset:1024 lds\n"
"v_mfma_f32_16x16x32_bf16 v[12:15], a[156:159], v[92:95], v[12:15]\n"
"s_waitcnt lgkmcnt(11)\n"
"v_exp_f32 v8, v8\n"
"v_mfma_f32_16x16x32_bf16 a[0:3], a[192:195], v[16:19], a[0:3]\n"
"ds_read_b64_tr_b16 a[230:231], v245 offset:61440\n"
"v_add_f32 v44, v44, v3\n"
"v_mfma_f32_16x16x32_bf16 a[4:7], a[196:199], v[16:19], a[4:7]\n"
"v_exp_f32 v9, v9\n"
"v_mfma_f32_16x16x32_bf16 a[8:11], a[200:203], v[16:19], a[8:11]\n"
"ds_read_b64_tr_b16 a[232:233], v244 offset:57600\n"
"v_cvt_pk_bf16_f32 v20, v8, v9\n"
"v_mfma_f32_16x16x32_bf16 a[12:15], a[204:207], v[16:19], a[12:15]\n"
"v_exp_f32 v10, v10\n"
"v_mfma_f32_16x16x32_bf16 a[16:19], a[208:211], v[16:19], a[16:19]\n"
"ds_read_b64_tr_b16 a[234:235], v244 offset:61696\n"
"v_add_f32 v44, v44, v4\n"
"v_mfma_f32_16x16x32_bf16 a[20:23], a[212:215], v[16:19], a[20:23]\n"
"v_exp_f32 v11, v11\n"
"v_mfma_f32_16x16x32_bf16 a[24:27], a[216:219], v[16:19], a[24:27]\n"
"ds_read_b64_tr_b16 a[236:237], v245 offset:57600\n"
"v_cvt_pk_bf16_f32 v21, v10, v11\n"
"v_mfma_f32_16x16x32_bf16 a[28:31], a[220:223], v[16:19], a[28:31]\n"
"AT_pg7:\n"
"s_cbranch_execz AT_hr\n"
"v_exp_f32 v12, v12\n"
"v_mfma_f32_16x16x32_bf16 v[220:223], a[128:131], v[96:99], v[32:35]\n"
"ds_read_b64_tr_b16 a[238:239], v245 offset:61696\n"
"v_add_f32 v44, v44, v5\n"
"v_mfma_f32_16x16x32_bf16 v[220:223], a[132:135], v[100:103], v[220:223]\n"
"v_exp_f32 v13, v13\n"
"v_mfma_f32_16x16x32_bf16 v[220:223], a[136:139], v[104:107], v[220:223]\n"
"ds_read_b64_tr_b16 a[240:241], v244 offset:57856\n"
"v_cvt_pk_bf16_f32 v22, v12, v13\n"
"v_mfma_f32_16x16x32_bf16 v[220:223], a[140:143], v[108:111], v[220:223]\n"
"v_exp_f32 v14, v14\n"
"v_mfma_f32_16x16x32_bf16 v[224:227], a[144:147], v[96:99], v[32:35]\n"
"ds_read_b64_tr_b16 a[242:243], v244 offset:61952\n"
"v_add_f32 v44, v44, v6\n"
"v_mfma_f32_16x16x32_bf16 v[224:227], a[148:151], v[100:103], v[224:227]\n"
"v_exp_f32 v15, v15\n"
"v_mfma_f32_16x16x32_bf16 v[224:227], a[152:155], v[104:107], v[224:227]\n"
"ds_read_b64_tr_b16 a[244:245], v245 offset:57856\n"
"v_cvt_pk_bf16_f32 v23, v14, v15\n"
"v_mfma_f32_16x16x32_bf16 v[224:227], a[156:159], v[108:111], v[224:227]\n"
"v_exp_f32 v220, v220\n"
"v_mfma_f32_16x16x32_bf16 a[32:35], a[192:195], v[20:23], a[32:35]\n"
"buffer_load_dwordx4 v246, s[12:15], s84 offen offset:2048 lds\n"
"v_mfma_f32_16x16x32_bf16 a[36:39], a[196:199], v[20:23], a[36:39]\n"
"v_exp_f32 v221, v221\n"
"v_mfma_f32_16x16x32_bf16 a[40:43], a[200:203], v[20:23], a[40:43]\n"
"ds_read_b64_tr_b16 a[246:247], v245 offset:61952\n"
"v_cvt_pk_bf16_f32 v16, v220, v221\n"
"v_mfma_f32_16x16x32_bf16 a[44:47], a[204:207], v[20:23], a[44:47]\n"
"v_exp_f32 v222, v222\n"
"v_mfma_f32_16x16x32_bf16 a[48:51], a[208:211], v[20:23], a[48:51]\n"
"ds_read_b64_tr_b16 a[248:249], v244 offset:58112\n"
"v_add_f32 v44, v44, v7\n"
"v_mfma_f32_16x16x32_bf16 a[52:55], a[212:215], v[20:23], a[52:55]\n"
"v_exp_f32 v223, v223\n"
"v_mfma_f32_16x16x32_bf16 a[56:59], a[216:219], v[20:23], a[56:59]\n"
"ds_read_b64_tr_b16 a[250:251], v244 offset:62208\n"
"v_cvt_pk_bf16_f32 v17, v222, v223\n"
"v_mfma_f32_16x16x32_bf16 a[60:63], a[220:223], v[20:23], a[60:63]\n"
"v_exp_f32 v224, v224\n"
"v_mfma_f32_16x16x32_bf16 v[48:51], v[40:43], v[20:23], v[48:51]\n"
"ds_read_b64_tr_b16 a[252:253], v245 offset:58112\n"
"v_add_f32 v52, v52, v220\n"
"v_mfma_f32_16x16x32_bf16 v[228:231], a[128:131], v[112:115], v[36:39]\n"
"v_exp_f32 v225, v225\n"
"v_mfma_f32_16x16x32_bf16 v[228:231], a[132:135], v[116:119], v[228:231]\n"
"ds_read_b64_tr_b16 a[254:255], v245 offset:62208\n"
"v_cvt_pk_bf16_f32 v18, v224, v225\n"
"v_mfma_f32_16x16x32_bf16 v[228:231], a[136:139], v[120:123], v[228:231]\n"
"v_exp_f32 v226, v226\n"
"v_mfma_f32_16x16x32_bf16 v[228:231], a[140:143], v[124:127], v[228:231]\n"
"v_add_f32 v52, v52, v221\n"
"v_add_f32 v52, v52, v222\n"
"v_mfma_f32_16x16x32_bf16 v[232:235], a[144:147], v[112:115], v[36:39]\n"
"v_exp_f32 v227, v227\n"
"v_mfma_f32_16x16x32_bf16 v[232:235], a[148:151], v[116:119], v[232:235]\n"
"v_cvt_pk_bf16_f32 v19, v226, v227\n"
"v_add_f32 v52, v52, v223\n"
"v_mfma_f32_16x16x32_bf16 v[232:235], a[152:155], v[120:123], v[232:235]\n"
"buffer_load_dwordx4 v247, s[12:15], s84 offen offset:3072 lds\n"
"v_mfma_f32_16x16x32_bf16 v[232:235], a[156:159], v[124:127], v[232:235]\n"
"s_add_u32 s84, s84, 16384\n"
"v_exp_f32 v228, v228\n"
"v_mfma_f32_16x16x32_bf16 a[64:67], a[192:195], v[16:19], a[64:67]\n"
"v_add_f32 v52, v52, v224\n"
"v_add_f32 v52, v52, v225\n"
"v_mfma_f32_16x16x32_bf16 a[68:71], a[196:199], v[16:19], a[68:71]\n"
"v_exp_f32 v229, v229\n"
"v_mfma_f32_16x16x32_bf16 a[72:75], a[200:203], v[16:19], a[72:75]\n"
"v_cvt_pk_bf16_f32 v20, v228, v229\n"
"v_add_f32 v52, v52, v226\n"
"v_mfma_f32_16x16x32_bf16 a[76:79], a[204:207], v[16:19], a[76:79]\n"
"s_waitcnt vmcnt(20) lgkmcnt(0)\n"
"v_exp_f32 v230, v230\n"
"v_mfma_f32_16x16x32_bf16 a[80:83], a[208:211], v[16:19], a[80:83]\n"
"s_barrier\n"
"v_mfma_f32_16x16x32_bf16 a[84:87], a[212:215], v[16:19], a[84:87]\n"
"v_exp_f32 v231, v231\n"
"v_mfma_f32_16x16x32_bf16 a[88:91], a[216:219], v[16:19], a[88:91]\n"
"v_cvt_pk_bf16_f32 v21, v230, v231\n"
"v_add_f32 v52, v52, v227\n"
"v_mfma_f32_16x16x32_bf16 a[92:95], a[220:223], v[16:19], a[92:95]\n"
"v_exp_f32 v232, v232\n"
"v_mfma_f32_16x16x32_bf16 v[0:3], a[160:163], v[64:67], v[24:27]\n"
"ds_read_b128 a[128:131], v240 offset:0\n"
"buffer_store_dwordx4 v[216:219], v208, s[28:31], s54 offen sc1\n"
"v_mfma_f32_16x16x32_bf16 v[0:3], a[164:167], v[68:71], v[0:3]\n"
"s_add_u32 s54, s54, 1024\n"
"v_exp_f32 v233, v233\n"
"v_mfma_f32_16x16x32_bf16 v[0:3], a[168:171], v[72:75], v[0:3]\n"
"s_min_u32 s54, s54, s49\n"
"ds_read_b128 a[132:135], v240 offset:256\n"
"v_cvt_pk_bf16_f32 v22, v232, v233\n"
"v_mfma_f32_16x16x32_bf16 v[0:3], a[172:175], v[76:79], v[0:3]\n"
"v_exp_f32 v234, v234\n"
"v_mfma_f32_16x16x32_bf16 v[4:7], a[176:179], v[64:67], v[24:27]\n"
"ds_read_b128 a[136:139], v240 offset:512\n"
"v_mfma_f32_16x16x32_bf16 v[4:7], a[180:183], v[68:71], v[4:7]\n"
"v_exp_f32 v235, v235\n"
"v_mfma_f32_16x16x32_bf16 v[4:7], a[184:187], v[72:75], v[4:7]\n"
"ds_read_b128 a[140:143], v240 offset:768\n"
"v_cvt_pk_bf16_f32 v23, v234, v235\n"
"v_mfma_f32_16x16x32_bf16 v[4:7], a[188:191], v[76:79], v[4:7]\n"
"s_add_u32 m0, s86, 98304\n"
"v_exp_f32 v0, v0\n"
"v_mfma_f32_16x16x32_bf16 a[96:99], a[192:195], v[20:23], a[96:99]\n"
"buffer_load_dwordx4 v246, s[8:11], s84 offen offset:0 lds\n"
"v_mfma_f32_16x16x32_bf16 a[100:103], a[196:199], v[20:23], a[100:103]\n"
"v_exp_f32 v1, v1\n"
"v_mfma_f32_16x16x32_bf16 a[104:107], a[200:203], v[20:23], a[104:107]\n"
"ds_read_b128 a[144:147], v240 offset:4096\n"
"v_cvt_pk_bf16_f32 v16, v0, v1\n"
"v_mfma_f32_16x16x32_bf16 a[108:111], a[204:207], v[20:23], a[108:111]\n"
"v_exp_f32 v2, v2\n"
"v_mfma_f32_16x16x32_bf16 a[112:115], a[208:211], v[20:23], a[112:115]\n"
"ds_read_b128 a[148:151], v240 offset:4352\n"
"v_add_f32 v44, v44, v0\n"
"v_mfma_f32_16x16x32_bf16 a[116:119], a[212:215], v[20:23], a[116:119]\n"
"v_exp_f32 v3, v3\n"
"v_mfma_f32_16x16x32_bf16 a[120:123], a[216:219], v[20:23], a[120:123]\n"
"ds_read_b128 a[152:155], v240 offset:4608\n"
"v_cvt_pk_bf16_f32 v17, v2, v3\n"
"v_mfma_f32_16x16x32_bf16 a[124:127], a[220:223], v[20:23], a[124:127]\n"
"v_exp_f32 v4, v4\n"
"v_mfma_f32_16x16x32_bf16 v[56:59], v[40:43], v[20:23], v[56:59]\n"
"ds_read_b128 a[156:159], v240 offset:4864\n"
"v_add_f32 v44, v44, v1\n"
"v_mfma_f32_16x16x32_bf16 v[8:11], a[160:163], v[80:83], v[28:31]\n"
"v_exp_f32 v5, v5\n"
"v_mfma_f32_16x16x32_bf16 v[8:11], a[164:167], v[84:87], v[8:11]\n"
"ds_read_b64_tr_b16 a[192:193], v242 offset:16384\n"
"v_cvt_pk_bf16_f32 v18, v4, v5\n"
"v_mfma_f32_16x16x32_bf16 v[8:11], a[168:171], v[88:91], v[8:11]\n"
"v_exp_f32 v6, v6\n"
"v_mfma_f32_16x16x32_bf16 v[8:11], a[172:175], v[92:95], v[8:11]\n"
"ds_read_b64_tr_b16 a[194:195], v242 offset:20480\n"
"v_add_f32 v44, v44, v2\n"
"v_mfma_f32_16x16x32_bf16 v[12:15], a[176:179], v[80:83], v[28:31]\n"
"v_exp_f32 v7, v7\n"
"v_mfma_f32_16x16x32_bf16 v[12:15], a[180:183], v[84:87], v[12:15]\n"
"ds_read_b64_tr_b16 a[196:197], v243 offset:16384\n"
"v_cvt_pk_bf16_f32 v19, v6, v7\n"
"v_mfma_f32_16x16x32_bf16 v[12:15], a[184:187], v[88:91], v[12:15]\n"
"buffer_load_dwordx4 v246, s[8:11], s84 offen offset:1024 lds\n"
"v_mfma_f32_16x16x32_bf16 v[12:15], a[188:191], v[92:95], v[12:15]\n"
"s_waitcnt lgkmcnt(11)\n"
"v_exp_f32 v8, v8\n"
"v_mfma_f32_16x16x32_bf16 a[0:3], a[224:227], v[16:19], a[0:3]\n"
"ds_read_b64_tr_b16 a[198:199], v243 offset:20480\n"
"v_add_f32 v44, v44, v3\n"
"v_mfma_f32_16x16x32_bf16 a[4:7], a[228:231], v[16:19], a[4:7]\n"
"v_exp_f32 v9, v9\n"
"v_mfma_f32_16x16x32_bf16 a[8:11], a[232:235], v[16:19], a[8:11]\n"
"ds_read_b64_tr_b16 a[200:201], v242 offset:16640\n"
"v_cvt_pk_bf16_f32 v20, v8, v9\n"
"v_mfma_f32_16x16x32_bf16 a[12:15], a[236:239], v[16:19], a[12:15]\n"
"v_exp_f32 v10, v10\n"
"v_mfma_f32_16x16x32_bf16 a[16:19], a[240:243], v[16:19], a[16:19]\n"
"ds_read_b64_tr_b16 a[202:203], v242 offset:20736\n"
"v_add_f32 v44, v44, v4\n"
"v_mfma_f32_16x16x32_bf16 a[20:23], a[244:247], v[16:19], a[20:23]\n"
"v_exp_f32 v11, v11\n"
"v_mfma_f32_16x16x32_bf16 a[24:27], a[248:251], v[16:19], a[24:27]\n"
"ds_read_b64_tr_b16 a[204:205], v243 offset:16640\n"
"v_cvt_pk_bf16_f32 v21, v10, v11\n"
"v_mfma_f32_16x16x32_bf16 a[28:31], a[252:255], v[16:19], a[28:31]\n"
"v_exp_f32 v12, v12\n"
"v_mfma_f32_16x16x32_bf16 v[220:223], a[160:163], v[96:99], v[32:35]\n"
"ds_read_b64_tr_b16 a[206:207], v243 offset:20736\n"
"v_add_f32 v44, v44, v5\n"
"v_mfma_f32_16x16x32_bf16 v[220:223], a[164:167], v[100:103], v[220:223]\n"
"v_exp_f32 v13, v13\n"
"v_mfma_f32_16x16x32_bf16 v[220:223], a[168:171], v[104:107], v[220:223]\n"
"ds_read_b64_tr_b16 a[208:209], v242 offset:16896\n"
"v_cvt_pk_bf16_f32 v22, v12, v13\n"
"v_mfma_f32_16x16x32_bf16 v[220:223], a[172:175], v[108:111], v[220:223]\n"
"v_exp_f32 v14, v14\n"
"v_mfma_f32_16x16x32_bf16 v[224:227], a[176:179], v[96:99], v[32:35]\n"
"ds_read_b64_tr_b16 a[210:211], v242 offset:20992\n"
"v_add_f32 v44, v44, v6\n"
"v_mfma_f32_16x16x32_bf16 v[224:227], a[180:183], v[100:103], v[224:227]\n"
"v_exp_f32 v15, v15\n"
"v_mfma_f32_16x16x32_bf16 v[224:227], a[184:187], v[104:107], v[224:227]\n"
"ds_read_b64_tr_b16 a[212:213], v243 offset:16896\n"
"v_cvt_pk_bf16_f32 v23, v14, v15\n"
"v_mfma_f32_16x16x32_bf16 v[224:227], a[188:191], v[108:111], v[224:227]\n"
"v_exp_f32 v220, v220\n"
"v_mfma_f32_16x16x32_bf16 a[32:35], a[224:227], v[20:23], a[32:35]\n"
"buffer_load_dwordx4 v247, s[8:11], s84 offen offset:2048 lds\n"
"v_mfma_f32_16x16x32_bf16 a[36:39], a[228:231], v[20:23], a[36:39]\n"
"v_exp_f32 v221, v221\n"
"v_mfma_f32_16x16x32_bf16 a[40:43], a[232:235], v[20:23], a[40:43]\n"
"ds_read_b64_tr_b16 a[214:215], v243 offset:20992\n"
"v_cvt_pk_bf16_f32 v16, v220, v221\n"
"v_mfma_f32_16x16x32_bf16 a[44:47], a[236:239], v[20:23], a[44:47]\n"
"v_exp_f32 v222, v222\n"
"v_mfma_f32_16x16x32_bf16 a[48:51], a[240:243], v[20:23], a[48:51]\n"
"ds_read_b64_tr_b16 a[216:217], v242 offset:17152\n"
"v_add_f32 v44, v44, v7\n"
"v_mfma_f32_16x16x32_bf16 a[52:55], a[244:247], v[20:23], a[52:55]\n"
"v_exp_f32 v223, v223\n"
"v_mfma_f32_16x16x32_bf16 a[56:59], a[248:251], v[20:23], a[56:59]\n"
"ds_read_b64_tr_b16 a[218:219], v242 offset:21248\n"
"v_cvt_pk_bf16_f32 v17, v222, v223\n"
"v_mfma_f32_16x16x32_bf16 a[60:63], a[252:255], v[20:23], a[60:63]\n"
"v_exp_f32 v224, v224\n"
"v_mfma_f32_16x16x32_bf16 v[48:51], v[40:43], v[20:23], v[48:51]\n"
"ds_read_b64_tr_b16 a[220:221], v243 offset:17152\n"
"v_add_f32 v52, v52, v220\n"
"v_mfma_f32_16x16x32_bf16 v[228:231], a[160:163], v[112:115], v[36:39]\n"
"v_exp_f32 v225, v225\n"
"v_mfma_f32_16x16x32_bf16 v[228:231], a[164:167], v[116:119], v[228:231]\n"
"ds_read_b64_tr_b16 a[222:223], v243 offset:21248\n"
"v_cvt_pk_bf16_f32 v18, v224, v225\n"
"v_mfma_f32_16x16x32_bf16 v[228:231], a[168:171], v[120:123], v[228:231]\n"
"v_exp_f32 v226, v226\n"
"v_mfma_f32_16x16x32_bf16 v[228:231], a[172:175], v[124:127], v[228:231]\n"
"v_add_f32 v52, v52, v221\n"
"v_add_f32 v52, v52, v222\n"
"v_mfma_f32_16x16x32_bf16 v[232:235], a[176:179], v[112:115], v[36:39]\n"
"v_exp_f32 v227, v227\n"
"v_mfma_f32_16x16x32_bf16 v[232:235], a[180:183], v[116:119], v[232:235]\n"
"v_cvt_pk_bf16_f32 v19, v226, v227\n"
"v_add_f32 v52, v52, v223\n"
"v_mfma_f32_16x16x32_bf16 v[232:235], a[184:187], v[120:123], v[232:235]\n"
"buffer_load_dwordx4 v247, s[8:11], s84 offen offset:3072 lds\n"
"v_mfma_f32_16x16x32_bf16 v[232:235], a[188:191], v[124:127], v[232:235]\n"
"v_exp_f32 v228, v228\n"
"v_mfma_f32_16x16x32_bf16 a[64:67], a[224:227], v[16:19], a[64:67]\n"
"v_add_f32 v52, v52, v224\n"
"v_add_f32 v52, v52, v225\n"
"v_mfma_f32_16x16x32_bf16 a[68:71], a[228:231], v[16:19], a[68:71]\n"
"v_exp_f32 v229, v229\n"
"v_mfma_f32_16x16x32_bf16 a[72:75], a[232:235], v[16:19], a[72:75]\n"
"v_cvt_pk_bf16_f32 v20, v228, v229\n"
"v_add_f32 v52, v52, v226\n"
"v_mfma_f32_16x16x32_bf16 a[76:79], a[236:239], v[16:19], a[76:79]\n"
"v_exp_f32 v230, v230\n"
"v_mfma_f32_16x16x32_bf16 a[80:83], a[240:243], v[16:19], a[80:83]\n"
"s_waitcnt lgkmcnt(8)\n"
"buffer_load_dword v250, v253, s[24:27], s47 offen\n"
"v_mfma_f32_16x16x32_bf16 a[84:87], a[244:247], v[16:19], a[84:87]\n"
"v_exp_f32 v231, v231\n"
"v_mfma_f32_16x16x32_bf16 a[88:91], a[248:251], v[16:19], a[88:91]\n"
"v_cvt_pk_bf16_f32 v21, v230, v231\n"
"v_add_f32 v52, v52, v227\n"
"v_mfma_f32_16x16x32_bf16 a[92:95], a[252:255], v[16:19], a[92:95]\n"
"v_exp_f32 v232, v232\n"
"v_mfma_f32_16x16x32_bf16 v[0:3], a[128:131], v[64:67], v[24:27]\n"
"ds_read_b128 a[160:163], v240 offset:8192\n"
"v_add_u32 v253, 294912, v253\n"
"v_mfma_f32_16x16x32_bf16 v[0:3], a[132:135], v[68:71], v[0:3]\n"
"v_exp_f32 v233, v233\n"
"v_mfma_f32_16x16x32_bf16 v[0:3], a[136:139], v[72:75], v[0:3]\n"
"ds_read_b128 a[164:167], v240 offset:8448\n"
"v_cvt_pk_bf16_f32 v22, v232, v233\n"
"v_mfma_f32_16x16x32_bf16 v[0:3], a[140:143], v[76:79], v[0:3]\n"
"v_exp_f32 v234, v234\n"
"v_mfma_f32_16x16x32_bf16 v[4:7], a[144:147], v[64:67], v[24:27]\n"
"ds_read_b128 a[168:171], v240 offset:8704\n"
"v_mfma_f32_16x16x32_bf16 v[4:7], a[148:151], v[68:71], v[4:7]\n"
"v_exp_f32 v235, v235\n"
"v_mfma_f32_16x16x32_bf16 v[4:7], a[152:155], v[72:75], v[4:7]\n"
"ds_read_b128 a[172:175], v240 offset:8960\n"
"v_cvt_pk_bf16_f32 v23, v234, v235\n"
"v_mfma_f32_16x16x32_bf16 v[4:7], a[156:159], v[76:79], v[4:7]\n"
"s_add_u32 m0, s86, 114688\n"
"v_exp_f32 v0, v0\n"
"v_mfma_f32_16x16x32_bf16 a[96:99], a[224:227], v[20:23], a[96:99]\n"
"buffer_load_dwordx4 v246, s[12:15], s84 offen offset:0 lds\n"
"v_mfma_f32_16x16x32_bf16 a[100:103], a[228:231], v[20:23], a[100:103]\n"
"v_exp_f32 v1, v1\n"
"v_mfma_f32_16x16x32_bf16 a[104:107], a[232:235], v[20:23], a[104:107]\n"
"ds_read_b128 a[176:179], v240 offset:12288\n"
"v_cvt_pk_bf16_f32 v16, v0, v1\n"
"v_mfma_f32_16x16x32_bf16 a[108:111], a[236:239], v[20:23], a[108:111]\n"
"v_exp_f32 v2, v2\n"
"v_mfma_f32_16x16x32_bf16 a[112:115], a[240:243], v[20:23], a[112:115]\n"
"ds_read_b128 a[180:183], v240 offset:12544\n"
"v_add_f32 v44, v44, v0\n"
"v_mfma_f32_16x16x32_bf16 a[116:119], a[244:247], v[20:23], a[116:119]\n"
"v_exp_f32 v3, v3\n"
"v_mfma_f32_16x16x32_bf16 a[120:123], a[248:251], v[20:23], a[120:123]\n"
"ds_read_b128 a[184:187], v240 offset:12800\n"
"v_cvt_pk_bf16_f32 v17, v2, v3\n"
"v_mfma_f32_16x16x32_bf16 a[124:127], a[252:255], v[20:23], a[124:127]\n"
"v_exp_f32 v4, v4\n"
"v_mfma_f32_16x16x32_bf16 v[56:59], v[40:43], v[20:23], v[56:59]\n"
"ds_read_b128 a[188:191], v240 offset:13056\n"
"v_add_f32 v44, v44, v1\n"
"v_mfma_f32_16x16x32_bf16 v[8:11], a[128:131], v[80:83], v[28:31]\n"
"v_exp_f32 v5, v5\n"
"v_mfma_f32_16x16x32_bf16 v[8:11], a[132:135], v[84:87], v[8:11]\n"
"ds_read_b64_tr_b16 a[224:225], v242 offset:24576\n"
"v_cvt_pk_bf16_f32 v18, v4, v5\n"
"v_mfma_f32_16x16x32_bf16 v[8:11], a[136:139], v[88:91], v[8:11]\n"
"v_exp_f32 v6, v6\n"
"v_mfma_f32_16x16x32_bf16 v[8:11], a[140:143], v[92:95], v[8:11]\n"
"ds_read_b64_tr_b16 a[226:227], v242 offset:28672\n"
"v_add_f32 v44, v44, v2\n"
"v_mfma_f32_16x16x32_bf16 v[12:15], a[144:147], v[80:83], v[28:31]\n"
"v_exp_f32 v7, v7\n"
"v_mfma_f32_16x16x32_bf16 v[12:15], a[148:151], v[84:87], v[12:15]\n"
"ds_read_b64_tr_b16 a[228:229], v243 offset:24576\n"
"v_cvt_pk_bf16_f32 v19, v6, v7\n"
"v_mfma_f32_16x16x32_bf16 v[12:15], a[152:155], v[88:91], v[12:15]\n"
"buffer_load_dwordx4 v247, s[12:15], s84 offen offset:1024 lds\n"
"v_mfma_f32_16x16x32_bf16 v[12:15], a[156:159], v[92:95], v[12:15]\n"
"s_waitcnt lgkmcnt(11)\n"
"v_exp_f32 v8, v8\n"
"v_mfma_f32_16x16x32_bf16 a[0:3], a[192:195], v[16:19], a[0:3]\n"
"ds_read_b64_tr_b16 a[230:231], v243 offset:28672\n"
"v_add_f32 v44, v44, v3\n"
"v_mfma_f32_16x16x32_bf16 a[4:7], a[196:199], v[16:19], a[4:7]\n"
"v_exp_f32 v9, v9\n"
"v_mfma_f32_16x16x32_bf16 a[8:11], a[200:203], v[16:19], a[8:11]\n"
"ds_read_b64_tr_b16 a[232:233], v242 offset:24832\n"
"v_cvt_pk_bf16_f32 v20, v8, v9\n"
"v_mfma_f32_16x16x32_bf16 a[12:15], a[204:207], v[16:19], a[12:15]\n"
"v_exp_f32 v10, v10\n"
"v_mfma_f32_16x16x32_bf16 a[16:19], a[208:211], v[16:19], a[16:19]\n"
"ds_read_b64_tr_b16 a[234:235], v242 offset:28928\n"
"v_add_f32 v44, v44, v4\n"
"v_mfma_f32_16x16x32_bf16 a[20:23], a[212:215], v[16:19], a[20:23]\n"
"v_exp_f32 v11, v11\n"
"v_mfma_f32_16x16x32_bf16 a[24:27], a[216:219], v[16:19], a[24:27]\n"
"ds_read_b64_tr_b16 a[236:237], v243 offset:24832\n"
"v_cvt_pk_bf16_f32 v21, v10, v11\n"
"v_mfma_f32_16x16x32_bf16 a[28:31], a[220:223], v[16:19], a[28:31]\n"
"v_exp_f32 v12, v12\n"
"v_mfma_f32_16x16x32_bf16 v[220:223], a[128:131], v[96:99], v[32:35]\n"
"ds_read_b64_tr_b16 a[238:239], v243 offset:28928\n"
"v_add_f32 v44, v44, v5\n"
"v_mfma_f32_16x16x32_bf16 v[220:223], a[132:135], v[100:103], v[220:223]\n"
"v_exp_f32 v13, v13\n"
"v_mfma_f32_16x16x32_bf16 v[220:223], a[136:139], v[104:107], v[220:223]\n"
"ds_read_b64_tr_b16 a[240:241], v242 offset:25088\n"
"v_cvt_pk_bf16_f32 v22, v12, v13\n"
"v_mfma_f32_16x16x32_bf16 v[220:223], a[140:143], v[108:111], v[220:223]\n"
"v_exp_f32 v14, v14\n"
"v_mfma_f32_16x16x32_bf16 v[224:227], a[144:147], v[96:99], v[32:35]\n"
"ds_read_b64_tr_b16 a[242:243], v242 offset:29184\n"
"v_add_f32 v44, v44, v6\n"
"v_mfma_f32_16x16x32_bf16 v[224:227], a[148:151], v[100:103], v[224:227]\n"
"v_exp_f32 v15, v15\n"
"v_mfma_f32_16x16x32_bf16 v[224:227], a[152:155], v[104:107], v[224:227]\n"
"ds_read_b64_tr_b16 a[244:245], v243 offset:25088\n"
"v_cvt_pk_bf16_f32 v23, v14, v15\n"
"v_mfma_f32_16x16x32_bf16 v[224:227], a[156:159], v[108:111], v[224:227]\n"
"v_exp_f32 v220, v220\n"
"v_mfma_f32_16x16x32_bf16 a[32:35], a[192:195], v[20:23], a[32:35]\n"
"buffer_load_dwordx4 v246, s[12:15], s84 offen offset:2048 lds\n"
"v_mfma_f32_16x16x32_bf16 a[36:39], a[196:199], v[20:23], a[36:39]\n"
"v_exp_f32 v221, v221\n"
"v_mfma_f32_16x16x32_bf16 a[40:43], a[200:203], v[20:23], a[40:43]\n"
"ds_read_b64_tr_b16 a[246:247], v243 offset:29184\n"
"v_cvt_pk_bf16_f32 v16, v220, v221\n"
"v_mfma_f32_16x16x32_bf16 a[44:47], a[204:207], v[20:23], a[44:47]\n"
"v_exp_f32 v222, v222\n"
"v_mfma_f32_16x16x32_bf16 a[48:51], a[208:211], v[20:23], a[48:51]\n"
"ds_read_b64_tr_b16 a[248:249], v242 offset:25344\n"
"v_add_f32 v44, v44, v7\n"
"v_mfma_f32_16x16x32_bf16 a[52:55], a[212:215], v[20:23], a[52:55]\n"
"v_exp_f32 v223, v223\n"
"v_mfma_f32_16x16x32_bf16 a[56:59], a[216:219], v[20:23], a[56:59]\n"
"ds_read_b64_tr_b16 a[250:251], v242 offset:29440\n"
"v_cvt_pk_bf16_f32 v17, v222, v223\n"
"v_mfma_f32_16x16x32_bf16 a[60:63], a[220:223], v[20:23], a[60:63]\n"
"v_exp_f32 v224, v224\n"
"v_mfma_f32_16x16x32_bf16 v[48:51], v[40:43], v[20:23], v[48:51]\n"
"ds_read_b64_tr_b16 a[252:253], v243 offset:25344\n"
"v_add_f32 v52, v52, v220\n"
"v_mfma_f32_16x16x32_bf16 v[228:231], a[128:131], v[112:115], v[36:39]\n"
"v_exp_f32 v225, v225\n"
"v_mfma_f32_16x16x32_bf16 v[228:231], a[132:135], v[116:119], v[228:231]\n"
"ds_read_b64_tr_b16 a[254:255], v243 offset:29440\n"
"v_cvt_pk_bf16_f32 v18, v224, v225\n"
"v_mfma_f32_16x16x32_bf16 v[228:231], a[136:139], v[120:123], v[228:231]\n"
"v_exp_f32 v226, v226\n"
"v_mfma_f32_16x16x32_bf16 v[228:231], a[140:143], v[124:127], v[228:231]\n"
"v_add_f32 v52, v52, v221\n"
"v_add_f32 v52, v52, v222\n"
"v_mfma_f32_16x16x32_bf16 v[232:235], a[144:147], v[112:115], v[36:39]\n"
"v_exp_f32 v227, v227\n"
"v_mfma_f32_16x16x32_bf16 v[232:235], a[148:151], v[116:119], v[232:235]\n"
"v_cvt_pk_bf16_f32 v19, v226, v227\n"
"v_add_f32 v52, v52, v223\n"
"v_mfma_f32_16x16x32_bf16 v[232:235], a[152:155], v[120:123], v[232:235]\n"
"buffer_load_dwordx4 v247, s[12:15], s84 offen offset:3072 lds\n"
"v_mfma_f32_16x16x32_bf16 v[232:235], a[156:159], v[124:127], v[232:235]\n"
"s_add_u32 s84, s84, 16384\n"
"v_exp_f32 v228, v228\n"
"v_mfma_f32_16x16x32_bf16 a[64:67], a[192:195], v[16:19], a[64:67]\n"
"s_sub_u32 s87, s87, 1\n"
"v_add_f32 v52, v52, v224\n"
"v_add_f32 v52, v52, v225\n"
"v_mfma_f32_16x16x32_bf16 a[68:71], a[196:199], v[16:19], a[68:71]\n"
"s_cmp_lg_u32 s87, 0\n"
"v_exp_f32 v229, v229\n"
"v_mfma_f32_16x16x32_bf16 a[72:75], a[200:203], v[16:19], a[72:75]\n"
"v_cvt_pk_bf16_f32 v20, v228, v229\n"
"v_add_f32 v52, v52, v226\n"
"v_mfma_f32_16x16x32_bf16 a[76:79], a[204:207], v[16:19], a[76:79]\n"
"s_waitcnt vmcnt(20) lgkmcnt(0)\n"
"v_exp_f32 v230, v230\n"
"v_mfma_f32_16x16x32_bf16 a[80:83], a[208:211], v[16:19], a[80:83]\n"
"s_barrier\n"
"v_mfma_f32_16x16x32_bf16 a[84:87], a[212:215], v[16:19], a[84:87]\n"
"v_exp_f32 v231, v231\n"
"v_mfma_f32_16x16x32_bf16 a[88:91], a[216:219], v[16:19], a[88:91]\n"
"v_cvt_pk_bf16_f32 v21, v230, v231\n"
"v_add_f32 v52, v52, v227\n"
"v_mfma_f32_16x16x32_bf16 a[92:95], a[220:223], v[16:19], a[92:95]\n"
"v_exp_f32 v232, v232\n"
"s_cbranch_scc1 AT_loop\n"
"s_cmp_lg_u32 s56, 0\n"
"s_cbranch_scc1 AT_h3\n"
"s_mov_b32 s61, 0x80000\n"
"AT_h2:\n"
"v_xor_b32 v210, s85, v210\n"
"v_xor_b32 v211, s85, v211\n"
"v_xor_b32 v212, s85, v212\n"
"v_xor_b32 v213, s85, v213\n"
"v_or3_b32 v210, v210, v211, v212\n"
"v_or_b32 v210, v210, v213\n"
"v_cmp_ne_u32 vcc, 0, v210\n"
"s_sub_u32 s61, s61, 1\n"
"s_cbranch_vccz AT_h4\n"
"s_cmp_eq_u32 s61, 0\n"
"s_cbranch_scc1 AT_h4\n"
"s_sleep 2\n"
"buffer_load_dword v210, v214, s[28:31], s57 offen offset:4 sc1\n"
"buffer_load_dword v211, v214, s[28:31], s58 offen offset:4 sc1\n"
"buffer_load_dword v212, v214, s[28:31], s59 offen offset:4 sc1\n"
"buffer_load_dword v213, v214, s[28:31], s60 offen offset:4 sc1\n"
"s_waitcnt vmcnt(0)\n"
"s_branch AT_h2\n"
"AT_h4:\n"
"v_mov_b32 v210, s85\n"
"v_mov_b32 v211, 65536\n"
"s_mov_b64 exec, 1\n"
"buffer_store_dword v210, v211, s[28:31], 0 offen sc1\n"
"s_mov_b64 exec, -1\n"
"AT_h3:\n"
"v_mfma_f32_16x16x32_bf16 v[0:3], a[160:163], v[64:67], v[24:27]\n"
"ds_read_b128 a[128:131], v240 offset:32768\n"
"buffer_load_dword v250, v255, s[32:35], 0 offen\n"
"v_mfma_f32_16x16x32_bf16 v[0:3], a[164:167], v[68:71], v[0:3]\n"
"v_exp_f32 v233, v233\n"
"v_mfma_f32_16x16x32_bf16 v[0:3], a[168:171], v[72:75], v[0:3]\n"
"ds_read_b128 a[132:135], v240 offset:33024\n"
"v_cvt_pk_bf16_f32 v22, v232, v233\n"
"v_mfma_f32_16x16x32_bf16 v[0:3], a[172:175], v[76:79], v[0:3]\n"
"v_exp_f32 v234, v234\n"
"v_mfma_f32_16x16x32_bf16 v[4:7], a[176:179], v[64:67], v[24:27]\n"
"ds_read_b128 a[136:139], v240 offset:33280\n"
"v_lshlrev_b32 v216, 6, v248\n"
"v_mfma_f32_16x16x32_bf16 v[4:7], a[180:183], v[68:71], v[4:7]\n"
"s_and_b32 s48, s42, 7\n"
"v_exp_f32 v235, v235\n"
"v_mfma_f32_16x16x32_bf16 v[4:7], a[184:187], v[72:75], v[4:7]\n"
"s_mul_i32 s48, s48, 576\n"
"ds_read_b128 a[140:143], v240 offset:33536\n"
"v_cvt_pk_bf16_f32 v23, v234, v235\n"
"v_mfma_f32_16x16x32_bf16 v[4:7], a[188:191], v[76:79], v[4:7]\n"
"s_mov_b32 m0, s86\n"
"v_exp_f32 v0, v0\n"
"v_mfma_f32_16x16x32_bf16 a[96:99], a[192:195], v[20:23], a[96:99]\n"
"s_lshr_b32 s62, s42, 3\n"
"buffer_load_dwordx4 v246, s[8:11], s84 offen offset:0 lds\n"
"v_mfma_f32_16x16x32_bf16 a[100:103], a[196:199], v[20:23], a[100:103]\n"
"s_mul_i32 s62, s62, 24\n"
"v_exp_f32 v1, v1\n"
"v_mfma_f32_16x16x32_bf16 a[104:107], a[200:203], v[20:23], a[104:107]\n"
"s_add_u32 s48, s48, s62\n"
"ds_read_b128 a[144:147], v240 offset:36864\n"
"v_cvt_pk_bf16_f32 v16, v0, v1\n"
"v_mfma_f32_16x16x32_bf16 a[108:111], a[204:207], v[20:23], a[108:111]\n"
"s_cmp_lt_u32 s42, 192\n"
"s_cselect_b32 s74, s74, 0\n"
"s_cselect_b32 s66, s66, 0\n"
"s_cselect_b32 s70, s70, 0\n"
"v_exp_f32 v2, v2\n"
"v_mfma_f32_16x16x32_bf16 a[112:115], a[208:211], v[20:23], a[112:115]\n"
"s_mul_i32 s50, s48, 1152\n"
"ds_read_b128 a[148:151], v240 offset:37120\n"
"v_add_f32 v44, v44, v0\n"
"v_mfma_f32_16x16x32_bf16 a[116:119], a[212:215], v[20:23], a[116:119]\n"
"s_lshl_b32 s62, s41, 13\n"
"v_exp_f32 v3, v3\n"
"v_mfma_f32_16x16x32_bf16 a[120:123], a[216:219], v[20:23], a[120:123]\n"
"s_add_u32 s46, s50, s62\n"
"ds_read_b128 a[152:155], v240 offset:37376\n"
"v_cvt_pk_bf16_f32 v17, v2, v3\n"
"v_mfma_f32_16x16x32_bf16 a[124:127], a[220:223], v[20:23], a[124:127]\n"
"s_add_u32 s47, s46, 4096\n"
"v_exp_f32 v4, v4\n"
"v_mfma_f32_16x16x32_bf16 v[56:59], v[40:43], v[20:23], v[56:59]\n"
"ds_read_b128 a[156:159], v240 offset:37632\n"
"v_add_f32 v44, v44, v1\n"
"v_mfma_f32_16x16x32_bf16 v[8:11], a[160:163], v[80:83], v[28:31]\n"
"v_exp_f32 v5, v5\n"
"v_mfma_f32_16x16x32_bf16 v[8:11], a[164:167], v[84:87], v[8:11]\n"
"ds_read_b64_tr_b16 a[192:193], v242 offset:49152\n"
"v_cvt_pk_bf16_f32 v18, v4, v5\n"
"v_mfma_f32_16x16x32_bf16 v[8:11], a[168:171], v[88:91], v[8:11]\n"
"v_exp_f32 v6, v6\n"
"v_mfma_f32_16x16x32_bf16 v[8:11], a[172:175], v[92:95], v[8:11]\n"
"ds_read_b64_tr_b16 a[194:195], v242 offset:53248\n"
"v_add_f32 v44, v44, v2\n"
"v_mfma_f32_16x16x32_bf16 v[12:15], a[176:179], v[80:83], v[28:31]\n"
"v_exp_f32 v7, v7\n"
"v_mfma_f32_16x16x32_bf16 v[12:15], a[180:183], v[84:87], v[12:15]\n"
"ds_read_b64_tr_b16 a[196:197], v243 offset:49152\n"
"v_cvt_pk_bf16_f32 v19, v6, v7\n"
"v_mfma_f32_16x16x32_bf16 v[12:15], a[184:187], v[88:91], v[12:15]\n"
"buffer_load_dwordx4 v246, s[8:11], s84 offen offset:1024 lds\n"
"v_mfma_f32_16x16x32_bf16 v[12:15], a[188:191], v[92:95], v[12:15]\n"
"s_waitcnt lgkmcnt(11)\n"
"v_exp_f32 v8, v8\n"
"v_mfma_f32_16x16x32_bf16 a[0:3], a[224:227], v[16:19], a[0:3]\n"
"ds_read_b64_tr_b16 a[198:199], v243 offset:53248\n"
"v_add_f32 v44, v44, v3\n"
"v_mfma_f32_16x16x32_bf16 a[4:7], a[228:231], v[16:19], a[4:7]\n"
"v_exp_f32 v9, v9\n"
"v_mfma_f32_16x16x32_bf16 a[8:11], a[232:235], v[16:19], a[8:11]\n"
"ds_read_b64_tr_b16 a[200:201], v242 offset:49408\n"
"v_cvt_pk_bf16_f32 v20, v8, v9\n"
"v_mfma_f32_16x16x32_bf16 a[12:15], a[236:239], v[16:19], a[12:15]\n"
"v_exp_f32 v10, v10\n"
"v_mfma_f32_16x16x32_bf16 a[16:19], a[240:243], v[16:19], a[16:19]\n"
"ds_read_b64_tr_b16 a[202:203], v242 offset:53504\n"
"buffer_load_dword v250, v216, s[72:75], s46 offen\n"
"v_add_f32 v44, v44, v4\n"
"v_mfma_f32_16x16x32_bf16 a[20:23], a[244:247], v[16:19], a[20:23]\n"
"v_exp_f32 v11, v11\n"
"v_mfma_f32_16x16x32_bf16 a[24:27], a[248:251], v[16:19], a[24:27]\n"
"ds_read_b64_tr_b16 a[204:205], v243 offset:49408\n"
"v_cvt_pk_bf16_f32 v21, v10, v11\n"
"v_mfma_f32_16x16x32_bf16 a[28:31], a[252:255], v[16:19], a[28:31]\n"
"v_exp_f32 v12, v12\n"
"v_mfma_f32_16x16x32_bf16 v[220:223], a[160:163], v[96:99], v[32:35]\n"
"ds_read_b64_tr_b16 a[206:207], v243 offset:53504\n"
"v_add_f32 v44, v44, v5\n"
"v_mfma_f32_16x16x32_bf16 v[220:223], a[164:167], v[100:103], v[220:223]\n"
"v_exp_f32 v13, v13\n"
"v_mfma_f32_16x16x32_bf16 v[220:223], a[168:171], v[104:107], v[220:223]\n"
"ds_read_b64_tr_b16 a[208:209], v242 offset:49664\n"
"v_cvt_pk_bf16_f32 v22, v12, v13\n"
"v_mfma_f32_16x16x32_bf16 v[220:223], a[172:175], v[108:111], v[220:223]\n"
"v_exp_f32 v14, v14\n"
"v_mfma_f32_16x16x32_bf16 v[224:227], a[176:179], v[96:99], v[32:35]\n"
"ds_read_b64_tr_b16 a[210:211], v242 offset:53760\n"
"v_add_f32 v44, v44, v6\n"
"v_mfma_f32_16x16x32_bf16 v[224:227], a[180:183], v[100:103], v[224:227]\n"
"v_exp_f32 v15, v15\n"
"v_mfma_f32_16x16x32_bf16 v[224:227], a[184:187], v[104:107], v[224:227]\n"
"ds_read_b64_tr_b16 a[212:213], v243 offset:49664\n"
"v_cvt_pk_bf16_f32 v23, v14, v15\n"
"v_mfma_f32_16x16x32_bf16 v[224:227], a[188:191], v[108:111], v[224:227]\n"
"v_exp_f32 v220, v220\n"
"v_mfma_f32_16x16x32_bf16 a[32:35], a[224:227], v[20:23], a[32:35]\n"
"buffer_load_dwordx4 v247, s[8:11], s84 offen offset:2048 lds\n"
"v_mfma_f32_16x16x32_bf16 a[36:39], a[228:231], v[20:23], a[36:39]\n"
"v_exp_f32 v221, v221\n"
"v_mfma_f32_16x16x32_bf16 a[40:43], a[232:235], v[20:23], a[40:43]\n"
"ds_read_b64_tr_b16 a[214:215], v243 offset:53760\n"
"v_cvt_pk_bf16_f32 v16, v220, v221\n"
"v_mfma_f32_16x16x32_bf16 a[44:47], a[236:239], v[20:23], a[44:47]\n"
"v_exp_f32 v222, v222\n"
"v_mfma_f32_16x16x32_bf16 a[48:51], a[240:243], v[20:23], a[48:51]\n"
"ds_read_b64_tr_b16 a[216:217], v242 offset:49920\n"
"v_add_f32 v44, v44, v7\n"
"v_mfma_f32_16x16x32_bf16 a[52:55], a[244:247], v[20:23], a[52:55]\n"
"v_exp_f32 v223, v223\n"
"v_mfma_f32_16x16x32_bf16 a[56:59], a[248:251], v[20:23], a[56:59]\n"
"ds_read_b64_tr_b16 a[218:219], v242 offset:54016\n"
"v_cvt_pk_bf16_f32 v17, v222, v223\n"
"v_mfma_f32_16x16x32_bf16 a[60:63], a[252:255], v[20:23], a[60:63]\n"
"v_exp_f32 v224, v224\n"
"v_mfma_f32_16x16x32_bf16 v[48:51], v[40:43], v[20:23], v[48:51]\n"
"ds_read_b64_tr_b16 a[220:221], v243 offset:49920\n"
"v_add_f32 v52, v52, v220\n"
"v_mfma_f32_16x16x32_bf16 v[228:231], a[160:163], v[112:115], v[36:39]\n"
"v_exp_f32 v225, v225\n"
"v_mfma_f32_16x16x32_bf16 v[228:231], a[164:167], v[116:119], v[228:231]\n"
"ds_read_b64_tr_b16 a[222:223], v243 offset:54016\n"
"v_cvt_pk_bf16_f32 v18, v224, v225\n"
"v_mfma_f32_16x16x32_bf16 v[228:231], a[168:171], v[120:123], v[228:231]\n"
"v_exp_f32 v226, v226\n"
"v_mfma_f32_16x16x32_bf16 v[228:231], a[172:175], v[124:127], v[228:231]\n"
"v_add_f32 v52, v52, v221\n"
"v_add_f32 v52, v52, v222\n"
"v_mfma_f32_16x16x32_bf16 v[232:235], a[176:179], v[112:115], v[36:39]\n"
"v_exp_f32 v227, v227\n"
"v_mfma_f32_16x16x32_bf16 v[232:235], a[180:183], v[116:119], v[232:235]\n"
"v_cvt_pk_bf16_f32 v19, v226, v227\n"
"v_add_f32 v52, v52, v223\n"
"v_mfma_f32_16x16x32_bf16 v[232:235], a[184:187], v[120:123], v[232:235]\n"
"buffer_load_dwordx4 v247, s[8:11], s84 offen offset:3072 lds\n"
"v_mfma_f32_16x16x32_bf16 v[232:235], a[188:191], v[124:127], v[232:235]\n"
"v_exp_f32 v228, v228\n"
"v_mfma_f32_16x16x32_bf16 a[64:67], a[224:227], v[16:19], a[64:67]\n"
"buffer_load_dword v250, v216, s[72:75], s47 offen\n"
"v_add_f32 v52, v52, v224\n"
"v_add_f32 v52, v52, v225\n"
"v_mfma_f32_16x16x32_bf16 a[68:71], a[228:231], v[16:19], a[68:71]\n"
"v_exp_f32 v229, v229\n"
"v_mfma_f32_16x16x32_bf16 a[72:75], a[232:235], v[16:19], a[72:75]\n"
"v_cvt_pk_bf16_f32 v20, v228, v229\n"
"v_add_f32 v52, v52, v226\n"
"v_mfma_f32_16x16x32_bf16 a[76:79], a[236:239], v[16:19], a[76:79]\n"
"v_exp_f32 v230, v230\n"
"v_mfma_f32_16x16x32_bf16 a[80:83], a[240:243], v[16:19], a[80:83]\n"
"s_waitcnt lgkmcnt(8)\n"
"v_lshrrev_b32 v251, 2, v248\n"
"v_mfma_f32_16x16x32_bf16 a[84:87], a[244:247], v[16:19], a[84:87]\n"
"v_exp_f32 v231, v231\n"
"v_mfma_f32_16x16x32_bf16 a[88:91], a[248:251], v[16:19], a[88:91]\n"
"AT_pg8:\n"
"s_cbranch_execz AT_hr\n"
"v_cvt_pk_bf16_f32 v21, v230, v231\n"
"v_add_f32 v52, v52, v227\n"
"v_mfma_f32_16x16x32_bf16 a[92:95], a[252:255], v[16:19], a[92:95]\n"
"v_exp_f32 v232, v232\n"
"v_mfma_f32_16x16x32_bf16 v[0:3], a[128:131], v[64:67], v[24:27]\n"
"ds_read_b128 a[160:163], v240 offset:40960\n"
"v_mul_u32_u24 v251, 9216, v251\n"
"v_mfma_f32_16x16x32_bf16 v[0:3], a[132:135], v[68:71], v[0:3]\n"
"v_exp_f32 v233, v233\n"
"v_mfma_f32_16x16x32_bf16 v[0:3], a[136:139], v[72:75], v[0:3]\n"
"ds_read_b128 a[164:167], v240 offset:41216\n"
"v_cvt_pk_bf16_f32 v22, v232, v233\n"
"v_mfma_f32_16x16x32_bf16 v[0:3], a[140:143], v[76:79], v[0:3]\n"
"v_exp_f32 v234, v234\n"
"v_mfma_f32_16x16x32_bf16 v[4:7], a[144:147], v[64:67], v[24:27]\n"
"ds_read_b128 a[168:171], v240 offset:41472\n"
"v_and_b32 v217, 3, v248\n"
"v_mfma_f32_16x16x32_bf16 v[4:7], a[148:151], v[68:71], v[4:7]\n"
"v_exp_f32 v235, v235\n"
"v_mfma_f32_16x16x32_bf16 v[4:7], a[152:155], v[72:75], v[4:7]\n"
"ds_read_b128 a[172:175], v240 offset:41728\n"
"v_cvt_pk_bf16_f32 v23, v234, v235\n"
"v_mfma_f32_16x16x32_bf16 v[4:7], a[156:159], v[76:79], v[4:7]\n"
"s_add_u32 m0, s86, 16384\n"
"v_exp_f32 v0, v0\n"
"v_mfma_f32_16x16x32_bf16 a[96:99], a[224:227], v[20:23], a[96:99]\n"
"buffer_load_dwordx4 v246, s[12:15], s84 offen offset:0 lds\n"
"v_add_u32 v255, 1, v217\n"
"v_mfma_f32_16x16x32_bf16 a[100:103], a[228:231], v[20:23], a[100:103]\n"
"v_exp_f32 v1, v1\n"
"v_mfma_f32_16x16x32_bf16 a[104:107], a[232:235], v[20:23], a[104:107]\n"
"ds_read_b128 a[176:179], v240 offset:45056\n"
"v_cvt_pk_bf16_f32 v16, v0, v1\n"
"v_mfma_f32_16x16x32_bf16 a[108:111], a[236:239], v[20:23], a[108:111]\n"
"v_exp_f32 v2, v2\n"
"v_mfma_f32_16x16x32_bf16 a[112:115], a[240:243], v[20:23], a[112:115]\n"
"ds_read_b128 a[180:183], v240 offset:45312\n"
"v_add_f32 v44, v44, v0\n"
"v_mfma_f32_16x16x32_bf16 a[116:119], a[244:247], v[20:23], a[116:119]\n"
"v_exp_f32 v3, v3\n"
"v_mfma_f32_16x16x32_bf16 a[120:123], a[248:251], v[20:23], a[120:123]\n"
"ds_read_b128 a[184:187], v240 offset:45568\n"
"v_cvt_pk_bf16_f32 v17, v2, v3\n"
"v_mfma_f32_16x16x32_bf16 a[124:127], a[252:255], v[20:23], a[124:127]\n"
"v_exp_f32 v4, v4\n"
"v_and_b32 v255, 4, v255\n"
"v_mfma_f32_16x16x32_bf16 v[56:59], v[40:43], v[20:23], v[56:59]\n"
"ds_read_b128 a[188:191], v240 offset:45824\n"
"v_add_f32 v44, v44, v1\n"
"v_mfma_f32_16x16x32_bf16 v[8:11], a[128:131], v[80:83], v[28:31]\n"
"v_exp_f32 v5, v5\n"
"v_mfma_f32_16x16x32_bf16 v[8:11], a[132:135], v[84:87], v[8:11]\n"
"ds_read_b64_tr_b16 a[224:225], v242 offset:57344\n"
"v_cvt_pk_bf16_f32 v18, v4, v5\n"
"v_mfma_f32_16x16x32_bf16 v[8:11], a[136:139], v[88:91], v[8:11]\n"
"v_exp_f32 v6, v6\n"
"v_mfma_f32_16x16x32_bf16 v[8:11], a[140:143], v[92:95], v[8:11]\n"
"ds_read_b64_tr_b16 a[226:227], v242 offset:61440\n"
"v_add_f32 v44, v44, v2\n"
"v_mfma_f32_16x16x32_bf16 v[12:15], a[144:147], v[80:83], v[28:31]\n"
"v_exp_f32 v7, v7\n"
"v_mfma_f32_16x16x32_bf16 v[12:15], a[148:151], v[84:87], v[12:15]\n"
"ds_read_b64_tr_b16 a[228:229], v243 offset:57344\n"
"v_cvt_pk_bf16_f32 v19, v6, v7\n"
"v_lshlrev_b32 v255, 26, v255\n"
"v_mfma_f32_16x16x32_bf16 v[12:15], a[152:155], v[88:91], v[12:15]\n"
"buffer_load_dwordx4 v247, s[12:15], s84 offen offset:1024 lds\n"
"v_lshl_add_u32 v217, v217, 4, v255\n"
"v_mfma_f32_16x16x32_bf16 v[12:15], a[156:159], v[92:95], v[12:15]\n"
"s_waitcnt lgkmcnt(11)\n"
"v_exp_f32 v8, v8\n"
"v_mfma_f32_16x16x32_bf16 a[0:3], a[192:195], v[16:19], a[0:3]\n"
"ds_read_b64_tr_b16 a[230:231], v243 offset:61440\n"
"v_add_f32 v44, v44, v3\n"
"v_mfma_f32_16x16x32_bf16 a[4:7], a[196:199], v[16:19], a[4:7]\n"
"v_exp_f32 v9, v9\n"
"v_mfma_f32_16x16x32_bf16 a[8:11], a[200:203], v[16:19], a[8:11]\n"
"ds_read_b64_tr_b16 a[232:233], v242 offset:57600\n"
"v_cvt_pk_bf16_f32 v20, v8, v9\n"
"v_mfma_f32_16x16x32_bf16 a[12:15], a[204:207], v[16:19], a[12:15]\n"
"v_exp_f32 v10, v10\n"
"v_mfma_f32_16x16x32_bf16 a[16:19], a[208:211], v[16:19], a[16:19]\n"
"ds_read_b64_tr_b16 a[234:235], v242 offset:61696\n"
"v_add_f32 v44, v44, v4\n"
"v_mfma_f32_16x16x32_bf16 a[20:23], a[212:215], v[16:19], a[20:23]\n"
"v_exp_f32 v11, v11\n"
"v_lshl_add_u32 v252, v251, 1, v217\n"
"v_mfma_f32_16x16x32_bf16 a[24:27], a[216:219], v[16:19], a[24:27]\n"
"ds_read_b64_tr_b16 a[236:237], v243 offset:57600\n"
"v_cvt_pk_bf16_f32 v21, v10, v11\n"
"v_mfma_f32_16x16x32_bf16 a[28:31], a[220:223], v[16:19], a[28:31]\n"
"v_exp_f32 v12, v12\n"
"v_mfma_f32_16x16x32_bf16 v[220:223], a[128:131], v[96:99], v[32:35]\n"
"ds_read_b64_tr_b16 a[238:239], v243 offset:61696\n"
"v_add_f32 v44, v44, v5\n"
"v_mfma_f32_16x16x32_bf16 v[220:223], a[132:135], v[100:103], v[220:223]\n"
"v_exp_f32 v13, v13\n"
"v_mfma_f32_16x16x32_bf16 v[220:223], a[136:139], v[104:107], v[220:223]\n"
"ds_read_b64_tr_b16 a[240:241], v242 offset:57856\n"
"v_cvt_pk_bf16_f32 v22, v12, v13\n"
"v_mfma_f32_16x16x32_bf16 v[220:223], a[140:143], v[108:111], v[220:223]\n"
"v_exp_f32 v14, v14\n"
"v_mfma_f32_16x16x32_bf16 v[224:227], a[144:147], v[96:99], v[32:35]\n"
"ds_read_b64_tr_b16 a[242:243], v242 offset:61952\n"
"v_add_f32 v44, v44, v6\n"
"v_add_u32 v251, v251, v217\n"
"v_mfma_f32_16x16x32_bf16 v[224:227], a[148:151], v[100:103], v[224:227]\n"
"s_mul_i32 s62, s41, 1327104\n"
"v_exp_f32 v15, v15\n"
"v_mfma_f32_16x16x32_bf16 v[224:227], a[152:155], v[104:107], v[224:227]\n"
"s_lshl_b32 s49, s48, 1\n"
"ds_read_b64_tr_b16 a[244:245], v243 offset:57856\n"
"v_cvt_pk_bf16_f32 v23, v14, v15\n"
"v_mfma_f32_16x16x32_bf16 v[224:227], a[156:159], v[108:111], v[224:227]\n"
"s_add_u32 s49, s49, s62\n"
"v_exp_f32 v220, v220\n"
"v_mfma_f32_16x16x32_bf16 a[32:35], a[192:195], v[20:23], a[32:35]\n"
"buffer_load_dwordx4 v246, s[12:15], s84 offen offset:2048 lds\n"
"v_mfma_f32_16x16x32_bf16 a[36:39], a[196:199], v[20:23], a[36:39]\n"
"v_exp_f32 v221, v221\n"
"v_mfma_f32_16x16x32_bf16 a[40:43], a[200:203], v[20:23], a[40:43]\n"
"ds_read_b64_tr_b16 a[246:247], v243 offset:61952\n"
"buffer_load_dwordx4 v[128:131], v252, s[64:67], s49 offen\n"
"v_cvt_pk_bf16_f32 v16, v220, v221\n"
"v_mfma_f32_16x16x32_bf16 a[44:47], a[204:207], v[20:23], a[44:47]\n"
"v_exp_f32 v222, v222\n"
"v_mfma_f32_16x16x32_bf16 a[48:51], a[208:211], v[20:23], a[48:51]\n"
"ds_read_b64_tr_b16 a[248:249], v242 offset:58112\n"
"v_add_f32 v44, v44, v7\n"
"v_mfma_f32_16x16x32_bf16 a[52:55], a[212:215], v[20:23], a[52:55]\n"
"v_exp_f32 v223, v223\n"
"v_mfma_f32_16x16x32_bf16 a[56:59], a[216:219], v[20:23], a[56:59]\n"
"ds_read_b64_tr_b16 a[250:251], v242 offset:62208\n"
"v_cvt_pk_bf16_f32 v17, v222, v223\n"
"v_mfma_f32_16x16x32_bf16 a[60:63], a[220:223], v[20:23], a[60:63]\n"
"v_exp_f32 v224, v224\n"
"v_mfma_f32_16x16x32_bf16 v[48:51], v[40:43], v[20:23], v[48:51]\n"
"ds_read_b64_tr_b16 a[252:253], v243 offset:58112\n"
"v_add_f32 v52, v52, v220\n"
"v_mfma_f32_16x16x32_bf16 v[228:231], a[128:131], v[112:115], v[36:39]\n"
"v_exp_f32 v225, v225\n"
"v_mul_u32_u24 v249, 3641, v248\n"
"v_mfma_f32_16x16x32_bf16 v[228:231], a[132:135], v[116:119], v[228:231]\n"
"s_movk_i32 s60, 864\n"
"ds_read_b64_tr_b16 a[254:255], v243 offset:62208\n"
"v_cvt_pk_bf16_f32 v18, v224, v225\n"
"v_mfma_f32_16x16x32_bf16 v[228:231], a[136:139], v[120:123], v[228:231]\n"
"v_exp_f32 v226, v226\n"
"v_mfma_f32_16x16x32_bf16 v[228:231], a[140:143], v[124:127], v[228:231]\n"
"v_add_f32 v52, v52, v221\n"
"v_add_f32 v52, v52, v222\n"
"v_mfma_f32_16x16x32_bf16 v[232:235], a[144:147], v[112:115], v[36:39]\n"
"v_exp_f32 v227, v227\n"
"v_mfma_f32_16x16x32_bf16 v[232:235], a[148:151], v[116:119], v[232:235]\n"
"v_cvt_pk_bf16_f32 v19, v226, v227\n"
"v_add_f32 v52, v52, v223\n"
"v_mfma_f32_16x16x32_bf16 v[232:235], a[152:155], v[120:123], v[232:235]\n"
"buffer_load_dwordx4 v247, s[12:15], s84 offen offset:3072 lds\n"
"v_add_u32 v206, 4096, v207\n"
"v_mfma_f32_16x16x32_bf16 v[232:235], a[156:159], v[124:127], v[232:235]\n"
"s_add_u32 s84, s84, 16384\n"
"v_exp_f32 v228, v228\n"
"v_mfma_f32_16x16x32_bf16 a[64:67], a[192:195], v[16:19], a[64:67]\n"
"v_add_f32 v52, v52, v224\n"
"v_add_f32 v52, v52, v225\n"
"v_mfma_f32_16x16x32_bf16 a[68:71], a[196:199], v[16:19], a[68:71]\n"
"v_exp_f32 v229, v229\n"
"v_mfma_f32_16x16x32_bf16 a[72:75], a[200:203], v[16:19], a[72:75]\n"
"v_cvt_pk_bf16_f32 v20, v228, v229\n"
"v_add_f32 v52, v52, v226\n"
"v_mfma_f32_16x16x32_bf16 a[76:79], a[204:207], v[16:19], a[76:79]\n"
"s_waitcnt vmcnt(20) lgkmcnt(0)\n"
"v_exp_f32 v230, v230\n"
"v_mfma_f32_16x16x32_bf16 a[80:83], a[208:211], v[16:19], a[80:83]\n"
"s_barrier\n"
"v_mfma_f32_16x16x32_bf16 a[84:87], a[212:215], v[16:19], a[84:87]\n"
"v_exp_f32 v231, v231\n"
"v_mfma_f32_16x16x32_bf16 a[88:91], a[216:219], v[16:19], a[88:91]\n"
"v_cvt_pk_bf16_f32 v21, v230, v231\n"
"v_add_f32 v52, v52, v227\n"
"v_lshrrev_b32 v217, 16, v249\n"
"v_mfma_f32_16x16x32_bf16 a[92:95], a[220:223], v[16:19], a[92:95]\n"
"v_exp_f32 v232, v232\n"
"v_mfma_f32_16x16x32_bf16 v[0:3], a[160:163], v[64:67], v[24:27]\n"
"ds_read_b128 a[128:131], v241 offset:0\n"
"v_mad_u32_u24 v210, v217, s60, v207\n"
"v_mfma_f32_16x16x32_bf16 v[0:3], a[164:167], v[68:71], v[0:3]\n"
"v_exp_f32 v233, v233\n"
"v_mfma_f32_16x16x32_bf16 v[0:3], a[168:171], v[72:75], v[0:3]\n"
"ds_read_b128 a[132:135], v241 offset:256\n"
"v_cvt_pk_bf16_f32 v22, v232, v233\n"
"v_mfma_f32_16x16x32_bf16 v[0:3], a[172:175], v[76:79], v[0:3]\n"
"v_exp_f32 v234, v234\n"
"v_mfma_f32_16x16x32_bf16 v[4:7], a[176:179], v[64:67], v[24:27]\n"
"ds_read_b128 a[136:139], v241 offset:512\n"
"v_add_u32 v249, 233024, v249\n"
"v_mfma_f32_16x16x32_bf16 v[4:7], a[180:183], v[68:71], v[4:7]\n"
"v_exp_f32 v235, v235\n"
"v_mfma_f32_16x16x32_bf16 v[4:7], a[184:187], v[72:75], v[4:7]\n"
"ds_read_b128 a[140:143], v241 offset:768\n"
"v_cvt_pk_bf16_f32 v23, v234, v235\n"
"v_mfma_f32_16x16x32_bf16 v[4:7], a[188:191], v[76:79], v[4:7]\n"
"s_add_u32 m0, s86, 32768\n"
"v_exp_f32 v0, v0\n"
"v_mfma_f32_16x16x32_bf16 a[96:99], a[192:195], v[20:23], a[96:99]\n"
"buffer_load_dwordx4 v246, s[8:11], s84 offen offset:0 lds\n"
"v_lshrrev_b32 v217, 16, v249\n"
"v_mfma_f32_16x16x32_bf16 a[100:103], a[196:199], v[20:23], a[100:103]\n"
"v_exp_f32 v1, v1\n"
"v_mfma_f32_16x16x32_bf16 a[104:107], a[200:203], v[20:23], a[104:107]\n"
"ds_read_b128 a[144:147], v241 offset:4096\n"
"v_cvt_pk_bf16_f32 v16, v0, v1\n"
"v_mfma_f32_16x16x32_bf16 a[108:111], a[204:207], v[20:23], a[108:111]\n"
"v_exp_f32 v2, v2\n"
"v_mfma_f32_16x16x32_bf16 a[112:115], a[208:211], v[20:23], a[112:115]\n"
"ds_read_b128 a[148:151], v241 offset:4352\n"
"v_add_f32 v44, v44, v0\n"
"v_mfma_f32_16x16x32_bf16 a[116:119], a[212:215], v[20:23], a[116:119]\n"
"v_exp_f32 v3, v3\n"
"v_mfma_f32_16x16x32_bf16 a[120:123], a[216:219], v[20:23], a[120:123]\n"
"ds_read_b128 a[152:155], v241 offset:4608\n"
"v_cvt_pk_bf16_f32 v17, v2, v3\n"
"v_mfma_f32_16x16x32_bf16 a[124:127], a[220:223], v[20:23], a[124:127]\n"
"v_exp_f32 v4, v4\n"
"v_mad_u32_u24 v211, v217, s60, v207\n"
"v_mfma_f32_16x16x32_bf16 v[56:59], v[40:43], v[20:23], v[56:59]\n"
"ds_read_b128 a[156:159], v241 offset:4864\n"
"v_add_f32 v44, v44, v1\n"
"v_mfma_f32_16x16x32_bf16 v[8:11], a[160:163], v[80:83], v[28:31]\n"
"v_exp_f32 v5, v5\n"
"v_mfma_f32_16x16x32_bf16 v[8:11], a[164:167], v[84:87], v[8:11]\n"
"ds_read_b64_tr_b16 a[192:193], v244 offset:16384\n"
"v_cvt_pk_bf16_f32 v18, v4, v5\n"
"v_mfma_f32_16x16x32_bf16 v[8:11], a[168:171], v[88:91], v[8:11]\n"
"v_exp_f32 v6, v6\n"
"v_mfma_f32_16x16x32_bf16 v[8:11], a[172:175], v[92:95], v[8:11]\n"
"ds_read_b64_tr_b16 a[194:195], v244 offset:20480\n"
"v_add_f32 v44, v44, v2\n"
"v_mfma_f32_16x16x32_bf16 v[12:15], a[176:179], v[80:83], v[28:31]\n"
"v_exp_f32 v7, v7\n"
"v_mfma_f32_16x16x32_bf16 v[12:15], a[180:183], v[84:87], v[12:15]\n"
"ds_read_b64_tr_b16 a[196:197], v245 offset:16384\n"
"v_cvt_pk_bf16_f32 v19, v6, v7\n"
"v_add_u32 v249, 233024, v249\n"
"v_mfma_f32_16x16x32_bf16 v[12:15], a[184:187], v[88:91], v[12:15]\n"
"buffer_load_dwordx4 v246, s[8:11], s84 offen offset:1024 lds\n"
"v_lshrrev_b32 v217, 16, v249\n"
"v_mfma_f32_16x16x32_bf16 v[12:15], a[188:191], v[92:95], v[12:15]\n"
"s_waitcnt lgkmcnt(11)\n"
"v_exp_f32 v8, v8\n"
"v_mfma_f32_16x16x32_bf16 a[0:3], a[224:227], v[16:19], a[0:3]\n"
"ds_read_b64_tr_b16 a[198:199], v245 offset:20480\n"
"v_add_f32 v44, v44, v3\n"
"v_mfma_f32_16x16x32_bf16 a[4:7], a[228:231], v[16:19], a[4:7]\n"
"v_exp_f32 v9, v9\n"
"v_mfma_f32_16x16x32_bf16 a[8:11], a[232:235], v[16:19], a[8:11]\n"
"ds_read_b64_tr_b16 a[200:201], v244 offset:16640\n"
"v_cvt_pk_bf16_f32 v20, v8, v9\n"
"v_mfma_f32_16x16x32_bf16 a[12:15], a[236:239], v[16:19], a[12:15]\n"
"v_exp_f32 v10, v10\n"
"v_mfma_f32_16x16x32_bf16 a[16:19], a[240:243], v[16:19], a[16:19]\n"
"ds_read_b64_tr_b16 a[202:203], v244 offset:20736\n"
"v_add_f32 v44, v44, v4\n"
"v_mfma_f32_16x16x32_bf16 a[20:23], a[244:247], v[16:19], a[20:23]\n"
"v_exp_f32 v11, v11\n"
"v_mad_u32_u24 v212, v217, s60, v207\n"
"v_mfma_f32_16x16x32_bf16 a[24:27], a[248:251], v[16:19], a[24:27]\n"
"ds_read_b64_tr_b16 a[204:205], v245 offset:16640\n"
"v_cvt_pk_bf16_f32 v21, v10, v11\n"
"v_mfma_f32_16x16x32_bf16 a[28:31], a[252:255], v[16:19], a[28:31]\n"
"v_exp_f32 v12, v12\n"
"v_mfma_f32_16x16x32_bf16 v[220:223], a[160:163], v[96:99], v[32:35]\n"
"ds_read_b64_tr_b16 a[206:207], v245 offset:20736\n"
"v_add_f32 v44, v44, v5\n"
"v_mfma_f32_16x16x32_bf16 v[220:223], a[164:167], v[100:103], v[220:223]\n"
"v_exp_f32 v13, v13\n"
"v_mfma_f32_16x16x32_bf16 v[220:223], a[168:171], v[104:107], v[220:223]\n"
"ds_read_b64_tr_b16 a[208:209], v244 offset:16896\n"
"v_cvt_pk_bf16_f32 v22, v12, v13\n"
"v_mfma_f32_16x16x32_bf16 v[220:223], a[172:175], v[108:111], v[220:223]\n"
"v_exp_f32 v14, v14\n"
"v_mfma_f32_16x16x32_bf16 v[224:227], a[176:179], v[96:99], v[32:35]\n"
"ds_read_b64_tr_b16 a[210:211], v244 offset:20992\n"
"v_add_f32 v44, v44, v6\n"
"v_add_u32 v249, 233024, v249\n"
"v_mfma_f32_16x16x32_bf16 v[224:227], a[180:183], v[100:103], v[224:227]\n"
"v_exp_f32 v15, v15\n"
"v_mfma_f32_16x16x32_bf16 v[224:227], a[184:187], v[104:107], v[224:227]\n"
"ds_read_b64_tr_b16 a[212:213], v245 offset:16896\n"
"v_cvt_pk_bf16_f32 v23, v14, v15\n"
"v_mfma_f32_16x16x32_bf16 v[224:227], a[188:191], v[108:111], v[224:227]\n"
"v_exp_f32 v220, v220\n"
"v_mfma_f32_16x16x32_bf16 a[32:35], a[224:227], v[20:23], a[32:35]\n"
"buffer_load_dwordx4 v247, s[8:11], s84 offen offset:2048 lds\n"
"v_lshrrev_b32 v217, 16, v249\n"
"v_mfma_f32_16x16x32_bf16 a[36:39], a[228:231], v[20:23], a[36:39]\n"
"v_exp_f32 v221, v221\n"
"v_mfma_f32_16x16x32_bf16 a[40:43], a[232:235], v[20:23], a[40:43]\n"
"ds_read_b64_tr_b16 a[214:215], v245 offset:20992\n"
"v_cvt_pk_bf16_f32 v16, v220, v221\n"
"v_mfma_f32_16x16x32_bf16 a[44:47], a[236:239], v[20:23], a[44:47]\n"
"v_exp_f32 v222, v222\n"
"v_mfma_f32_16x16x32_bf16 a[48:51], a[240:243], v[20:23], a[48:51]\n"
"ds_read_b64_tr_b16 a[216:217], v244 offset:17152\n"
"v_add_f32 v44, v44, v7\n"
"v_mfma_f32_16x16x32_bf16 a[52:55], a[244:247], v[20:23], a[52:55]\n"
"v_exp_f32 v223, v223\n"
"v_mfma_f32_16x16x32_bf16 a[56:59], a[248:251], v[20:23], a[56:59]\n"
"ds_read_b64_tr_b16 a[218:219], v244 offset:21248\n"
"v_cvt_pk_bf16_f32 v17, v222, v223\n"
"v_mfma_f32_16x16x32_bf16 a[60:63], a[252:255], v[20:23], a[60:63]\n"
"v_exp_f32 v224, v224\n"
"v_mad_u32_u24 v213, v217, s60, v207\n"
"v_mfma_f32_16x16x32_bf16 v[48:51], v[40:43], v[20:23], v[48:51]\n"
"ds_read_b64_tr_b16 a[220:221], v245 offset:17152\n"
"v_add_f32 v52, v52, v220\n"
"v_mfma_f32_16x16x32_bf16 v[228:231], a[160:163], v[112:115], v[36:39]\n"
"v_exp_f32 v225, v225\n"
"v_mfma_f32_16x16x32_bf16 v[228:231], a[164:167], v[116:119], v[228:231]\n"
"ds_read_b64_tr_b16 a[222:223], v245 offset:21248\n"
"v_cvt_pk_bf16_f32 v18, v224, v225\n"
"v_mfma_f32_16x16x32_bf16 v[228:231], a[168:171], v[120:123], v[228:231]\n"
"v_exp_f32 v226, v226\n"
"v_mfma_f32_16x16x32_bf16 v[228:231], a[172:175], v[124:127], v[228:231]\n"
"v_add_f32 v52, v52, v221\n"
"v_add_f32 v52, v52, v222\n"
"v_mfma_f32_16x16x32_bf16 v[232:235], a[176:179], v[112:115], v[36:39]\n"
"v_exp_f32 v227, v227\n"
"v_mfma_f32_16x16x32_bf16 v[232:235], a[180:183], v[116:119], v[232:235]\n"
"v_cvt_pk_bf16_f32 v19, v226, v227\n"
"v_add_f32 v52, v52, v223\n"
"v_add_u32 v249, 233024, v249\n"
"v_mfma_f32_16x16x32_bf16 v[232:235], a[184:187], v[120:123], v[232:235]\n"
"buffer_load_dwordx4 v247, s[8:11], s84 offen offset:3072 lds\n"
"v_lshrrev_b32 v217, 16, v249\n"
"v_mfma_f32_16x16x32_bf16 v[232:235], a[188:191], v[124:127], v[232:235]\n"
"v_exp_f32 v228, v228\n"
"v_mfma_f32_16x16x32_bf16 a[64:67], a[224:227], v[16:19], a[64:67]\n"
"buffer_load_dwordx4 v[164:167], v252, s[68:71], s49 offen\n"
"v_add_f32 v52, v52, v224\n"
"v_add_f32 v52, v52, v225\n"
"v_mfma_f32_16x16x32_bf16 a[68:71], a[228:231], v[16:19], a[68:71]\n"
"v_exp_f32 v229, v229\n"
"v_mfma_f32_16x16x32_bf16 a[72:75], a[232:235], v[16:19], a[72:75]\n"
"v_cvt_pk_bf16_f32 v20, v228, v229\n"
"v_add_f32 v52, v52, v226\n"
"v_mfma_f32_16x16x32_bf16 a[76:79], a[236:239], v[16:19], a[76:79]\n"
"v_exp_f32 v230, v230\n"
"v_mfma_f32_16x16x32_bf16 a[80:83], a[240:243], v[16:19], a[80:83]\n"
"s_waitcnt lgkmcnt(8)\n"
"v_mad_u32_u24 v214, v217, s60, v206\n"
"v_mfma_f32_16x16x32_bf16 a[84:87], a[244:247], v[16:19], a[84:87]\n"
"v_exp_f32 v231, v231\n"
"v_mfma_f32_16x16x32_bf16 a[88:91], a[248:251], v[16:19], a[88:91]\n"
"v_cvt_pk_bf16_f32 v21, v230, v231\n"
"v_add_f32 v52, v52, v227\n"
"v_mfma_f32_16x16x32_bf16 a[92:95], a[252:255], v[16:19], a[92:95]\n"
"v_exp_f32 v232, v232\n"
"v_mfma_f32_16x16x32_bf16 v[0:3], a[128:131], v[64:67], v[24:27]\n"
"ds_read_b128 a[160:163], v241 offset:8192\n"
"v_add_u32 v249, 233024, v249\n"
"v_mfma_f32_16x16x32_bf16 v[0:3], a[132:135], v[68:71], v[0:3]\n"
"v_exp_f32 v233, v233\n"
"v_mfma_f32_16x16x32_bf16 v[0:3], a[136:139], v[72:75], v[0:3]\n"
"ds_read_b128 a[164:167], v241 offset:8448\n"
"v_cvt_pk_bf16_f32 v22, v232, v233\n"
"v_mfma_f32_16x16x32_bf16 v[0:3], a[140:143], v[76:79], v[0:3]\n"
"v_exp_f32 v234, v234\n"
"v_mfma_f32_16x16x32_bf16 v[4:7], a[144:147], v[64:67], v[24:27]\n"
"ds_read_b128 a[168:171], v241 offset:8704\n"
"v_lshrrev_b32 v217, 16, v249\n"
"v_mfma_f32_16x16x32_bf16 v[4:7], a[148:151], v[68:71], v[4:7]\n"
"v_exp_f32 v235, v235\n"
"v_mfma_f32_16x16x32_bf16 v[4:7], a[152:155], v[72:75], v[4:7]\n"
"ds_read_b128 a[172:175], v241 offset:8960\n"
"v_cvt_pk_bf16_f32 v23, v234, v235\n"
"v_mfma_f32_16x16x32_bf16 v[4:7], a[156:159], v[76:79], v[4:7]\n"
"s_add_u32 m0, s86, 49152\n"
"v_exp_f32 v0, v0\n"
"v_mfma_f32_16x16x32_bf16 a[96:99], a[224:227], v[20:23], a[96:99]\n"
"buffer_load_dwordx4 v246, s[12:15], s84 offen offset:0 lds\n"
"v_mad_u32_u24 v236, v217, s60, v206\n"
"v_mfma_f32_16x16x32_bf16 a[100:103], a[228:231], v[20:23], a[100:103]\n"
"v_exp_f32 v1, v1\n"
"v_mfma_f32_16x16x32_bf16 a[104:107], a[232:235], v[20:23], a[104:107]\n"
"ds_read_b128 a[176:179], v241 offset:12288\n"
"v_cvt_pk_bf16_f32 v16, v0, v1\n"
"v_mfma_f32_16x16x32_bf16 a[108:111], a[236:239], v[20:23], a[108:111]\n"
"v_exp_f32 v2, v2\n"
"v_mfma_f32_16x16x32_bf16 a[112:115], a[240:243], v[20:23], a[112:115]\n"
"ds_read_b128 a[180:183], v241 offset:12544\n"
"v_add_f32 v44, v44, v0\n"
"v_mfma_f32_16x16x32_bf16 a[116:119], a[244:247], v[20:23], a[116:119]\n"
"v_exp_f32 v3, v3\n"
"v_mfma_f32_16x16x32_bf16 a[120:123], a[248:251], v[20:23], a[120:123]\n"
"ds_read_b128 a[184:187], v241 offset:12800\n"
"v_cvt_pk_bf16_f32 v17, v2, v3\n"
"v_mfma_f32_16x16x32_bf16 a[124:127], a[252:255], v[20:23], a[124:127]\n"
"v_exp_f32 v4, v4\n"
"v_add_u32 v249, 233024, v249\n"
"v_mfma_f32_16x16x32_bf16 v[56:59], v[40:43], v[20:23], v[56:59]\n"
"ds_read_b128 a[188:191], v241 offset:13056\n"
"v_add_f32 v44, v44, v1\n"
"v_mfma_f32_16x16x32_bf16 v[8:11], a[128:131], v[80:83], v[28:31]\n"
"v_exp_f32 v5, v5\n"
"v_mfma_f32_16x16x32_bf16 v[8:11], a[132:135], v[84:87], v[8:11]\n"
"ds_read_b64_tr_b16 a[224:225], v244 offset:24576\n"
"v_cvt_pk_bf16_f32 v18, v4, v5\n"
"v_mfma_f32_16x16x32_bf16 v[8:11], a[136:139], v[88:91], v[8:11]\n"
"v_exp_f32 v6, v6\n"
"v_mfma_f32_16x16x32_bf16 v[8:11], a[140:143], v[92:95], v[8:11]\n"
"ds_read_b64_tr_b16 a[226:227], v244 offset:28672\n"
"v_add_f32 v44, v44, v2\n"
"v_mfma_f32_16x16x32_bf16 v[12:15], a[144:147], v[80:83], v[28:31]\n"
"v_exp_f32 v7, v7\n"
"v_mfma_f32_16x16x32_bf16 v[12:15], a[148:151], v[84:87], v[12:15]\n"
"ds_read_b64_tr_b16 a[228:229], v245 offset:24576\n"
"v_cvt_pk_bf16_f32 v19, v6, v7\n"
"v_lshrrev_b32 v217, 16, v249\n"
"v_mfma_f32_16x16x32_bf16 v[12:15], a[152:155], v[88:91], v[12:15]\n"
"buffer_load_dwordx4 v247, s[12:15], s84 offen offset:1024 lds\n"
"v_mad_u32_u24 v237, v217, s60, v206\n"
"v_mfma_f32_16x16x32_bf16 v[12:15], a[156:159], v[92:95], v[12:15]\n"
"s_waitcnt lgkmcnt(11)\n"
"v_exp_f32 v8, v8\n"
"v_mfma_f32_16x16x32_bf16 a[0:3], a[192:195], v[16:19], a[0:3]\n"
"ds_read_b64_tr_b16 a[230:231], v245 offset:28672\n"
"v_add_f32 v44, v44, v3\n"
"v_mfma_f32_16x16x32_bf16 a[4:7], a[196:199], v[16:19], a[4:7]\n"
"v_exp_f32 v9, v9\n"
"v_mfma_f32_16x16x32_bf16 a[8:11], a[200:203], v[16:19], a[8:11]\n"
"ds_read_b64_tr_b16 a[232:233], v244 offset:24832\n"
"v_cvt_pk_bf16_f32 v20, v8, v9\n"
"v_mfma_f32_16x16x32_bf16 a[12:15], a[204:207], v[16:19], a[12:15]\n"
"v_exp_f32 v10, v10\n"
"v_mfma_f32_16x16x32_bf16 a[16:19], a[208:211], v[16:19], a[16:19]\n"
"ds_read_b64_tr_b16 a[234:235], v244 offset:28928\n"
"v_add_f32 v44, v44, v4\n"
"v_mfma_f32_16x16x32_bf16 a[20:23], a[212:215], v[16:19], a[20:23]\n"
"v_exp_f32 v11, v11\n"
"v_add_u32 v249, 233024, v249\n"
"v_mfma_f32_16x16x32_bf16 a[24:27], a[216:219], v[16:19], a[24:27]\n"
"ds_read_b64_tr_b16 a[236:237], v245 offset:24832\n"
"v_cvt_pk_bf16_f32 v21, v10, v11\n"
"v_mfma_f32_16x16x32_bf16 a[28:31], a[220:223], v[16:19], a[28:31]\n"
"v_exp_f32 v12, v12\n"
"v_mfma_f32_16x16x32_bf16 v[220:223], a[128:131], v[96:99], v[32:35]\n"
"ds_read_b64_tr_b16 a[238:239], v245 offset:28928\n"
"v_add_f32 v44, v44, v5\n"
"v_mfma_f32_16x16x32_bf16 v[220:223], a[132:135], v[100:103], v[220:223]\n"
"v_exp_f32 v13, v13\n"
"v_mfma_f32_16x16x32_bf16 v[220:223], a[136:139], v[104:107], v[220:223]\n"
"ds_read_b64_tr_b16 a[240:241], v244 offset:25088\n"
"v_cvt_pk_bf16_f32 v22, v12, v13\n"
"v_mfma_f32_16x16x32_bf16 v[220:223], a[140:143], v[108:111], v[220:223]\n"
"v_exp_f32 v14, v14\n"
"v_mfma_f32_16x16x32_bf16 v[224:227], a[144:147], v[96:99], v[32:35]\n"
"ds_read_b64_tr_b16 a[242:243], v244 offset:29184\n"
"v_add_f32 v44, v44, v6\n"
"v_lshrrev_b32 v217, 16, v249\n"
"v_mfma_f32_16x16x32_bf16 v[224:227], a[148:151], v[100:103], v[224:227]\n"
"v_exp_f32 v15, v15\n"
"v_mfma_f32_16x16x32_bf16 v[224:227], a[152:155], v[104:107], v[224:227]\n"
"ds_read_b64_tr_b16 a[244:245], v245 offset:25088\n"
"v_cvt_pk_bf16_f32 v23, v14, v15\n"
"v_mfma_f32_16x16x32_bf16 v[224:227], a[156:159], v[108:111], v[224:227]\n"
"v_exp_f32 v220, v220\n"
"v_mfma_f32_16x16x32_bf16 a[32:35], a[192:195], v[20:23], a[32:35]\n"
"buffer_load_dwordx4 v246, s[12:15], s84 offen offset:2048 lds\n"
"v_mad_u32_u24 v238, v217, s60, v206\n"
"v_mfma_f32_16x16x32_bf16 a[36:39], a[196:199], v[20:23], a[36:39]\n"
"v_exp_f32 v221, v221\n"
"v_mfma_f32_16x16x32_bf16 a[40:43], a[200:203], v[20:23], a[40:43]\n"
"ds_read_b64_tr_b16 a[246:247], v245 offset:29184\n"
"v_cvt_pk_bf16_f32 v16, v220, v221\n"
"v_mfma_f32_16x16x32_bf16 a[44:47], a[204:207], v[20:23], a[44:47]\n"
"v_exp_f32 v222, v222\n"
"v_mfma_f32_16x16x32_bf16 a[48:51], a[208:211], v[20:23], a[48:51]\n"
"ds_read_b64_tr_b16 a[248:249], v244 offset:25344\n"
"v_add_f32 v44, v44, v7\n"
"v_mfma_f32_16x16x32_bf16 a[52:55], a[212:215], v[20:23], a[52:55]\n"
"v_exp_f32 v223, v223\n"
"v_mfma_f32_16x16x32_bf16 a[56:59], a[216:219], v[20:23], a[56:59]\n"
"ds_read_b64_tr_b16 a[250:251], v244 offset:29440\n"
"v_cvt_pk_bf16_f32 v17, v222, v223\n"
"v_mfma_f32_16x16x32_bf16 a[60:63], a[220:223], v[20:23], a[60:63]\n"
"v_exp_f32 v224, v224\n"
"v_add_u32 v206, 4096, v206\n"
"v_mfma_f32_16x16x32_bf16 v[48:51], v[40:43], v[20:23], v[48:51]\n"
"ds_read_b64_tr_b16 a[252:253], v245 offset:25344\n"
"v_add_f32 v52, v52, v220\n"
"v_mfma_f32_16x16x32_bf16 v[228:231], a[128:131], v[112:115], v[36:39]\n"
"v_exp_f32 v225, v225\n"
"v_mfma_f32_16x16x32_bf16 v[228:231], a[132:135], v[116:119], v[228:231]\n"
"ds_read_b64_tr_b16 a[254:255], v245 offset:29440\n"
"v_cvt_pk_bf16_f32 v18, v224, v225\n"
"v_mfma_f32_16x16x32_bf16 v[228:231], a[136:139], v[120:123], v[228:231]\n"
"v_exp_f32 v226, v226\n"
"v_mfma_f32_16x16x32_bf16 v[228:231], a[140:143], v[124:127], v[228:231]\n"
"v_add_f32 v52, v52, v221\n"
"v_add_f32 v52, v52, v222\n"
"v_mfma_f32_16x16x32_bf16 v[232:235], a[144:147], v[112:115], v[36:39]\n"
"v_exp_f32 v227, v227\n"
"v_mfma_f32_16x16x32_bf16 v[232:235], a[148:151], v[116:119], v[232:235]\n"
"v_cvt_pk_bf16_f32 v19, v226, v227\n"
"v_add_f32 v52, v52, v223\n"
"v_add_u32 v249, 233024, v249\n"
"v_mfma_f32_16x16x32_bf16 v[232:235], a[152:155], v[120:123], v[232:235]\n"
"buffer_load_dwordx4 v247, s[12:15], s84 offen offset:3072 lds\n"
"v_lshrrev_b32 v217, 16, v249\n"
"v_mfma_f32_16x16x32_bf16 v[232:235], a[156:159], v[124:127], v[232:235]\n"
"s_add_u32 s84, s84, 16384\n"
"v_exp_f32 v228, v228\n"
"v_mfma_f32_16x16x32_bf16 a[64:67], a[192:195], v[16:19], a[64:67]\n"
"v_add_f32 v52, v52, v224\n"
"v_add_f32 v52, v52, v225\n"
"v_mfma_f32_16x16x32_bf16 a[68:71], a[196:199], v[16:19], a[68:71]\n"
"v_exp_f32 v229, v229\n"
"v_mfma_f32_16x16x32_bf16 a[72:75], a[200:203], v[16:19], a[72:75]\n"
"v_cvt_pk_bf16_f32 v20, v228, v229\n"
"v_add_f32 v52, v52, v226\n"
"v_mfma_f32_16x16x32_bf16 a[76:79], a[204:207], v[16:19], a[76:79]\n"
"s_waitcnt vmcnt(21) lgkmcnt(0)\n"
"v_exp_f32 v230, v230\n"
"v_mfma_f32_16x16x32_bf16 a[80:83], a[208:211], v[16:19], a[80:83]\n"
"s_barrier\n"
"v_mfma_f32_16x16x32_bf16 a[84:87], a[212:215], v[16:19], a[84:87]\n"
"v_exp_f32 v231, v231\n"
"v_mfma_f32_16x16x32_bf16 a[88:91], a[216:219], v[16:19], a[88:91]\n"
"v_cvt_pk_bf16_f32 v21, v230, v231\n"
"v_add_f32 v52, v52, v227\n"
"v_mad_u32_u24 v239, v217, s60, v206\n"
"v_mfma_f32_16x16x32_bf16 a[92:95], a[220:223], v[16:19], a[92:95]\n"
"s_and_b32 s56, s43, 3\n"
"v_exp_f32 v232, v232\n"
"v_mfma_f32_16x16x32_bf16 v[0:3], a[160:163], v[64:67], v[24:27]\n"
"s_lshr_b32 s57, s43, 2\n"
"ds_read_b128 a[128:131], v241 offset:32768\n"
"v_mfma_f32_16x16x32_bf16 v[0:3], a[164:167], v[68:71], v[0:3]\n"
"s_add_u32 s49, s49, 9216\n"
"v_exp_f32 v233, v233\n"
"v_mfma_f32_16x16x32_bf16 v[0:3], a[168:171], v[72:75], v[0:3]\n"
"ds_read_b128 a[132:135], v241 offset:33024\n"
"buffer_load_dwordx4 v[132:135], v252, s[64:67], s49 offen\n"
"v_cvt_pk_bf16_f32 v22, v232, v233\n"
"v_mfma_f32_16x16x32_bf16 v[0:3], a[172:175], v[76:79], v[0:3]\n"
"s_mul_i32 s57, s57, 1179648\n"
"v_exp_f32 v234, v234\n"
"v_mfma_f32_16x16x32_bf16 v[4:7], a[176:179], v[64:67], v[24:27]\n"
"s_mul_i32 s62, s56, 288\n"
"ds_read_b128 a[136:139], v241 offset:33280\n"
"v_mfma_f32_16x16x32_bf16 v[4:7], a[180:183], v[68:71], v[4:7]\n"
"s_add_u32 s57, s57, s62\n"
"v_exp_f32 v235, v235\n"
"v_mfma_f32_16x16x32_bf16 v[4:7], a[184:187], v[72:75], v[4:7]\n"
"s_lshl_b32 s58, s56, 3\n"
"ds_read_b128 a[140:143], v241 offset:33536\n"
"v_cvt_pk_bf16_f32 v23, v234, v235\n"
"v_mfma_f32_16x16x32_bf16 v[4:7], a[188:191], v[76:79], v[4:7]\n"
"s_add_u32 m0, s86, 65536\n"
"v_exp_f32 v0, v0\n"
"v_mfma_f32_16x16x32_bf16 a[96:99], a[192:195], v[20:23], a[96:99]\n"
"s_add_u32 s58, s58, s41\n"
"buffer_load_dwordx4 v246, s[8:11], s84 offen offset:0 lds\n"
"v_mfma_f32_16x16x32_bf16 a[100:103], a[196:199], v[20:23], a[100:103]\n"
"s_add_u32 s58, s58, 0\n"
"v_exp_f32 v1, v1\n"
"v_mfma_f32_16x16x32_bf16 a[104:107], a[200:203], v[20:23], a[104:107]\n"
"s_mul_i32 s58, s58, 36864\n"
"ds_read_b128 a[144:147], v241 offset:36864\n"
"v_cvt_pk_bf16_f32 v16, v0, v1\n"
"v_mfma_f32_16x16x32_bf16 a[108:111], a[204:207], v[20:23], a[108:111]\n"
"s_add_u32 s20, s58, s57\n"
"v_exp_f32 v2, v2\n"
"v_mfma_f32_16x16x32_bf16 a[112:115], a[208:211], v[20:23], a[112:115]\n"
"s_lshl_b32 s58, s56, 3\n"
"ds_read_b128 a[148:151], v241 offset:37120\n"
"v_add_f32 v44, v44, v0\n"
"v_mfma_f32_16x16x32_bf16 a[116:119], a[212:215], v[20:23], a[116:119]\n"
"s_add_u32 s58, s58, s41\n"
"v_exp_f32 v3, v3\n"
"v_mfma_f32_16x16x32_bf16 a[120:123], a[216:219], v[20:23], a[120:123]\n"
"s_add_u32 s58, s58, 4\n"
"ds_read_b128 a[152:155], v241 offset:37376\n"
"v_cvt_pk_bf16_f32 v17, v2, v3\n"
"v_mfma_f32_16x16x32_bf16 a[124:127], a[220:223], v[20:23], a[124:127]\n"
"s_mul_i32 s58, s58, 36864\n"
"v_exp_f32 v4, v4\n"
"v_mfma_f32_16x16x32_bf16 v[56:59], v[40:43], v[20:23], v[56:59]\n"
"s_add_u32 s21, s58, s57\n"
"ds_read_b128 a[156:159], v241 offset:37632\n"
"v_add_f32 v44, v44, v1\n"
"v_mfma_f32_16x16x32_bf16 v[8:11], a[160:163], v[80:83], v[28:31]\n"
"s_add_u32 s58, s41, 0\n"
"v_exp_f32 v5, v5\n"
"v_mfma_f32_16x16x32_bf16 v[8:11], a[164:167], v[84:87], v[8:11]\n"
"s_mul_i32 s59, s58, 43\n"
"ds_read_b64_tr_b16 a[192:193], v244 offset:49152\n"
"v_cvt_pk_bf16_f32 v18, v4, v5\n"
"v_mfma_f32_16x16x32_bf16 v[8:11], a[168:171], v[88:91], v[8:11]\n"
"s_lshr_b32 s59, s59, 7\n"
"v_exp_f32 v6, v6\n"
"v_mfma_f32_16x16x32_bf16 v[8:11], a[172:175], v[92:95], v[8:11]\n"
"ds_read_b64_tr_b16 a[194:195], v244 offset:53248\n"
"v_add_f32 v44, v44, v2\n"
"v_mfma_f32_16x16x32_bf16 v[12:15], a[176:179], v[80:83], v[28:31]\n"
"v_exp_f32 v7, v7\n"
"v_mfma_f32_16x16x32_bf16 v[12:15], a[180:183], v[84:87], v[12:15]\n"
"ds_read_b64_tr_b16 a[196:197], v245 offset:49152\n"
"v_cvt_pk_bf16_f32 v19, v6, v7\n"
"v_mfma_f32_16x16x32_bf16 v[12:15], a[184:187], v[88:91], v[12:15]\n"
"AT_pg9:\n"
"s_cbranch_execz AT_hr\n"
"buffer_load_dwordx4 v246, s[8:11], s84 offen offset:1024 lds\n"
"v_mfma_f32_16x16x32_bf16 v[12:15], a[188:191], v[92:95], v[12:15]\n"
"s_waitcnt lgkmcnt(11)\n"
"v_exp_f32 v8, v8\n"
"v_mfma_f32_16x16x32_bf16 a[0:3], a[224:227], v[16:19], a[0:3]\n"
"ds_read_b64_tr_b16 a[198:199], v245 offset:53248\n"
"v_add_f32 v44, v44, v3\n"
"v_mfma_f32_16x16x32_bf16 a[4:7], a[228:231], v[16:19], a[4:7]\n"
"v_exp_f32 v9, v9\n"
"v_mfma_f32_16x16x32_bf16 a[8:11], a[232:235], v[16:19], a[8:11]\n"
"ds_read_b64_tr_b16 a[200:201], v244 offset:49408\n"
"v_cvt_pk_bf16_f32 v20, v8, v9\n"
"v_mfma_f32_16x16x32_bf16 a[12:15], a[236:239], v[16:19], a[12:15]\n"
"v_exp_f32 v10, v10\n"
"v_mfma_f32_16x16x32_bf16 a[16:19], a[240:243], v[16:19], a[16:19]\n"
"ds_read_b64_tr_b16 a[202:203], v244 offset:53504\n"
"v_add_f32 v44, v44, v4\n"
"v_mfma_f32_16x16x32_bf16 a[20:23], a[244:247], v[16:19], a[20:23]\n"
"v_exp_f32 v11, v11\n"
"v_mfma_f32_16x16x32_bf16 a[24:27], a[248:251], v[16:19], a[24:27]\n"
"ds_read_b64_tr_b16 a[204:205], v245 offset:49408\n"
"buffer_load_dwordx4 v[168:171], v252, s[68:71], s49 offen\n"
"v_cvt_pk_bf16_f32 v21, v10, v11\n"
"v_mfma_f32_16x16x32_bf16 a[28:31], a[252:255], v[16:19], a[28:31]\n"
"s_mul_i32 s60, s59, 3\n"
"v_exp_f32 v12, v12\n"
"v_mfma_f32_16x16x32_bf16 v[220:223], a[160:163], v[96:99], v[32:35]\n"
"s_sub_u32 s58, s58, s60\n"
"ds_read_b64_tr_b16 a[206:207], v245 offset:53504\n"
"v_add_f32 v44, v44, v5\n"
"v_mfma_f32_16x16x32_bf16 v[220:223], a[164:167], v[100:103], v[220:223]\n"
"s_add_u32 s58, s58, s56\n"
"v_exp_f32 v13, v13\n"
"v_mfma_f32_16x16x32_bf16 v[220:223], a[168:171], v[104:107], v[220:223]\n"
"s_add_u32 s58, s58, 1\n"
"ds_read_b64_tr_b16 a[208:209], v244 offset:49664\n"
"v_cvt_pk_bf16_f32 v22, v12, v13\n"
"v_mfma_f32_16x16x32_bf16 v[220:223], a[172:175], v[108:111], v[220:223]\n"
"s_and_b32 s58, s58, 3\n"
"v_exp_f32 v14, v14\n"
"v_mfma_f32_16x16x32_bf16 v[224:227], a[176:179], v[96:99], v[32:35]\n"
"s_lshl_b32 s58, s58, 3\n"
"ds_read_b64_tr_b16 a[210:211], v244 offset:53760\n"
"v_add_f32 v44, v44, v6\n"
"v_mfma_f32_16x16x32_bf16 v[224:227], a[180:183], v[100:103], v[224:227]\n"
"s_add_u32 s58, s58, s59\n"
"v_exp_f32 v15, v15\n"
"v_mfma_f32_16x16x32_bf16 v[224:227], a[184:187], v[104:107], v[224:227]\n"
"s_mul_i32 s58, s58, 36864\n"
"ds_read_b64_tr_b16 a[212:213], v245 offset:49664\n"
"v_cvt_pk_bf16_f32 v23, v14, v15\n"
"v_mfma_f32_16x16x32_bf16 v[224:227], a[188:191], v[108:111], v[224:227]\n"
"s_add_u32 s22, s58, s57\n"
"v_exp_f32 v220, v220\n"
"v_mfma_f32_16x16x32_bf16 a[32:35], a[224:227], v[20:23], a[32:35]\n"
"s_add_u32 s58, s41, 4\n"
"buffer_load_dwordx4 v247, s[8:11], s84 offen offset:2048 lds\n"
"v_mfma_f32_16x16x32_bf16 a[36:39], a[228:231], v[20:23], a[36:39]\n"
"s_mul_i32 s59, s58, 43\n"
"v_exp_f32 v221, v221\n"
"v_mfma_f32_16x16x32_bf16 a[40:43], a[232:235], v[20:23], a[40:43]\n"
"s_lshr_b32 s59, s59, 7\n"
"ds_read_b64_tr_b16 a[214:215], v245 offset:53760\n"
"v_cvt_pk_bf16_f32 v16, v220, v221\n"
"v_mfma_f32_16x16x32_bf16 a[44:47], a[236:239], v[20:23], a[44:47]\n"
"s_mul_i32 s60, s59, 3\n"
"v_exp_f32 v222, v222\n"
"v_mfma_f32_16x16x32_bf16 a[48:51], a[240:243], v[20:23], a[48:51]\n"
"s_sub_u32 s58, s58, s60\n"
"ds_read_b64_tr_b16 a[216:217], v244 offset:49920\n"
"v_add_f32 v44, v44, v7\n"
"v_mfma_f32_16x16x32_bf16 a[52:55], a[244:247], v[20:23], a[52:55]\n"
"s_add_u32 s58, s58, s56\n"
"v_exp_f32 v223, v223\n"
"v_mfma_f32_16x16x32_bf16 a[56:59], a[248:251], v[20:23], a[56:59]\n"
"s_add_u32 s58, s58, 1\n"
"ds_read_b64_tr_b16 a[218:219], v244 offset:54016\n"
"v_cvt_pk_bf16_f32 v17, v222, v223\n"
"v_mfma_f32_16x16x32_bf16 a[60:63], a[252:255], v[20:23], a[60:63]\n"
"s_add_u32 s49, s49, 285696\n"
"v_exp_f32 v224, v224\n"
"v_mfma_f32_16x16x32_bf16 v[48:51], v[40:43], v[20:23], v[48:51]\n"
"ds_read_b64_tr_b16 a[220:221], v245 offset:49920\n"
"v_add_f32 v52, v52, v220\n"
"v_mfma_f32_16x16x32_bf16 v[228:231], a[160:163], v[112:115], v[36:39]\n"
"v_exp_f32 v225, v225\n"
"v_mfma_f32_16x16x32_bf16 v[228:231], a[164:167], v[116:119], v[228:231]\n"
"ds_read_b64_tr_b16 a[222:223], v245 offset:54016\n"
"v_cvt_pk_bf16_f32 v18, v224, v225\n"
"v_mfma_f32_16x16x32_bf16 v[228:231], a[168:171], v[120:123], v[228:231]\n"
"v_exp_f32 v226, v226\n"
"v_mfma_f32_16x16x32_bf16 v[228:231], a[172:175], v[124:127], v[228:231]\n"
"v_add_f32 v52, v52, v221\n"
"v_add_f32 v52, v52, v222\n"
"v_mfma_f32_16x16x32_bf16 v[232:235], a[176:179], v[112:115], v[36:39]\n"
"v_exp_f32 v227, v227\n"
"v_mfma_f32_16x16x32_bf16 v[232:235], a[180:183], v[116:119], v[232:235]\n"
"v_cvt_pk_bf16_f32 v19, v226, v227\n"
"v_add_f32 v52, v52, v223\n"
"v_mfma_f32_16x16x32_bf16 v[232:235], a[184:187], v[120:123], v[232:235]\n"
"buffer_load_dwordx4 v247, s[8:11], s84 offen offset:3072 lds\n"
"v_mfma_f32_16x16x32_bf16 v[232:235], a[188:191], v[124:127], v[232:235]\n"
"v_exp_f32 v228, v228\n"
"v_mfma_f32_16x16x32_bf16 a[64:67], a[224:227], v[16:19], a[64:67]\n"
"v_add_f32 v52, v52, v224\n"
"v_add_f32 v52, v52, v225\n"
"v_mfma_f32_16x16x32_bf16 a[68:71], a[228:231], v[16:19], a[68:71]\n"
"v_exp_f32 v229, v229\n"
"v_mfma_f32_16x16x32_bf16 a[72:75], a[232:235], v[16:19], a[72:75]\n"
"buffer_load_dwordx4 v[136:139], v252, s[64:67], s49 offen\n"
"v_cvt_pk_bf16_f32 v20, v228, v229\n"
"v_add_f32 v52, v52, v226\n"
"v_mfma_f32_16x16x32_bf16 a[76:79], a[236:239], v[16:19], a[76:79]\n"
"s_and_b32 s58, s58, 3\n"
"v_exp_f32 v230, v230\n"
"v_mfma_f32_16x16x32_bf16 a[80:83], a[240:243], v[16:19], a[80:83]\n"
"s_waitcnt lgkmcnt(8)\n"
"v_mfma_f32_16x16x32_bf16 a[84:87], a[244:247], v[16:19], a[84:87]\n"
"s_lshl_b32 s58, s58, 3\n"
"v_exp_f32 v231, v231\n"
"v_mfma_f32_16x16x32_bf16 a[88:91], a[248:251], v[16:19], a[88:91]\n"
"s_add_u32 s58, s58, s59\n"
"v_cvt_pk_bf16_f32 v21, v230, v231\n"
"v_add_f32 v52, v52, v227\n"
"v_mfma_f32_16x16x32_bf16 a[92:95], a[252:255], v[16:19], a[92:95]\n"
"s_mul_i32 s58, s58, 36864\n"
"v_exp_f32 v232, v232\n"
"v_mfma_f32_16x16x32_bf16 v[0:3], a[128:131], v[64:67], v[24:27]\n"
"s_add_u32 s23, s58, s57\n"
"ds_read_b128 a[160:163], v241 offset:40960\n"
"v_mfma_f32_16x16x32_bf16 v[0:3], a[132:135], v[68:71], v[0:3]\n"
"s_add_u32 s58, s41, 8\n"
"v_exp_f32 v233, v233\n"
"v_mfma_f32_16x16x32_bf16 v[0:3], a[136:139], v[72:75], v[0:3]\n"
"s_mul_i32 s59, s58, 43\n"
"ds_read_b128 a[164:167], v241 offset:41216\n"
"v_cvt_pk_bf16_f32 v22, v232, v233\n"
"v_mfma_f32_16x16x32_bf16 v[0:3], a[140:143], v[76:79], v[0:3]\n"
"s_lshr_b32 s59, s59, 7\n"
"v_exp_f32 v234, v234\n"
"v_mfma_f32_16x16x32_bf16 v[4:7], a[144:147], v[64:67], v[24:27]\n"
"s_mul_i32 s60, s59, 3\n"
"ds_read_b128 a[168:171], v241 offset:41472\n"
"v_mfma_f32_16x16x32_bf16 v[4:7], a[148:151], v[68:71], v[4:7]\n"
"s_sub_u32 s58, s58, s60\n"
"v_exp_f32 v235, v235\n"
"v_mfma_f32_16x16x32_bf16 v[4:7], a[152:155], v[72:75], v[4:7]\n"
"s_add_u32 s58, s58, s56\n"
"ds_read_b128 a[172:175], v241 offset:41728\n"
"v_cvt_pk_bf16_f32 v23, v234, v235\n"
"v_mfma_f32_16x16x32_bf16 v[4:7], a[156:159], v[76:79], v[4:7]\n"
"s_add_u32 m0, s86, 81920\n"
"v_exp_f32 v0, v0\n"
"v_mfma_f32_16x16x32_bf16 a[96:99], a[224:227], v[20:23], a[96:99]\n"
"s_add_u32 s58, s58, 1\n"
"buffer_load_dwordx4 v246, s[12:15], s84 offen offset:0 lds\n"
"v_mfma_f32_16x16x32_bf16 a[100:103], a[228:231], v[20:23], a[100:103]\n"
"s_and_b32 s58, s58, 3\n"
"v_exp_f32 v1, v1\n"
"v_mfma_f32_16x16x32_bf16 a[104:107], a[232:235], v[20:23], a[104:107]\n"
"s_lshl_b32 s58, s58, 3\n"
"ds_read_b128 a[176:179], v241 offset:45056\n"
"v_cvt_pk_bf16_f32 v16, v0, v1\n"
"v_mfma_f32_16x16x32_bf16 a[108:111], a[236:239], v[20:23], a[108:111]\n"
"s_add_u32 s58, s58, s59\n"
"v_exp_f32 v2, v2\n"
"v_mfma_f32_16x16x32_bf16 a[112:115], a[240:243], v[20:23], a[112:115]\n"
"s_mul_i32 s58, s58, 36864\n"
"ds_read_b128 a[180:183], v241 offset:45312\n"
"v_add_f32 v44, v44, v0\n"
"v_mfma_f32_16x16x32_bf16 a[116:119], a[244:247], v[20:23], a[116:119]\n"
"v_exp_f32 v3, v3\n"
"v_mfma_f32_16x16x32_bf16 a[120:123], a[248:251], v[20:23], a[120:123]\n"
"ds_read_b128 a[184:187], v241 offset:45568\n"
"v_cvt_pk_bf16_f32 v17, v2, v3\n"
"v_mfma_f32_16x16x32_bf16 a[124:127], a[252:255], v[20:23], a[124:127]\n"
"v_exp_f32 v4, v4\n"
"v_mfma_f32_16x16x32_bf16 v[56:59], v[40:43], v[20:23], v[56:59]\n"
"ds_read_b128 a[188:191], v241 offset:45824\n"
"v_add_f32 v44, v44, v1\n"
"v_mfma_f32_16x16x32_bf16 v[8:11], a[128:131], v[80:83], v[28:31]\n"
"v_exp_f32 v5, v5\n"
"v_mfma_f32_16x16x32_bf16 v[8:11], a[132:135], v[84:87], v[8:11]\n"
"ds_read_b64_tr_b16 a[224:225], v244 offset:57344\n"
"v_cvt_pk_bf16_f32 v18, v4, v5\n"
"v_mfma_f32_16x16x32_bf16 v[8:11], a[136:139], v[88:91], v[8:11]\n"
"v_exp_f32 v6, v6\n"
"v_mfma_f32_16x16x32_bf16 v[8:11], a[140:143], v[92:95], v[8:11]\n"
"ds_read_b64_tr_b16 a[226:227], v244 offset:61440\n"
"v_add_f32 v44, v44, v2\n"
"v_mfma_f32_16x16x32_bf16 v[12:15], a[144:147], v[80:83], v[28:31]\n"
"v_exp_f32 v7, v7\n"
"v_mfma_f32_16x16x32_bf16 v[12:15], a[148:151], v[84:87], v[12:15]\n"
"ds_read_b64_tr_b16 a[228:229], v245 offset:57344\n"
"buffer_load_dwordx4 v[172:175], v252, s[68:71], s49 offen\n"
"v_cvt_pk_bf16_f32 v19, v6, v7\n"
"v_mfma_f32_16x16x32_bf16 v[12:15], a[152:155], v[88:91], v[12:15]\n"
"s_add_u32 s36, s58, s57\n"
"buffer_load_dwordx4 v247, s[12:15], s84 offen offset:1024 lds\n"
"v_mfma_f32_16x16x32_bf16 v[12:15], a[156:159], v[92:95], v[12:15]\n"
"s_waitcnt lgkmcnt(11)\n"
"v_exp_f32 v8, v8\n"
"v_mfma_f32_16x16x32_bf16 a[0:3], a[192:195], v[16:19], a[0:3]\n"
"s_add_u32 s58, s41, 12\n"
"ds_read_b64_tr_b16 a[230:231], v245 offset:61440\n"
"v_add_f32 v44, v44, v3\n"
"v_mfma_f32_16x16x32_bf16 a[4:7], a[196:199], v[16:19], a[4:7]\n"
"s_mul_i32 s59, s58, 43\n"
"v_exp_f32 v9, v9\n"
"v_mfma_f32_16x16x32_bf16 a[8:11], a[200:203], v[16:19], a[8:11]\n"
"s_lshr_b32 s59, s59, 7\n"
"ds_read_b64_tr_b16 a[232:233], v244 offset:57600\n"
"v_cvt_pk_bf16_f32 v20, v8, v9\n"
"v_mfma_f32_16x16x32_bf16 a[12:15], a[204:207], v[16:19], a[12:15]\n"
"s_mul_i32 s60, s59, 3\n"
"v_exp_f32 v10, v10\n"
"v_mfma_f32_16x16x32_bf16 a[16:19], a[208:211], v[16:19], a[16:19]\n"
"s_sub_u32 s58, s58, s60\n"
"ds_read_b64_tr_b16 a[234:235], v244 offset:61696\n"
"v_add_f32 v44, v44, v4\n"
"v_mfma_f32_16x16x32_bf16 a[20:23], a[212:215], v[16:19], a[20:23]\n"
"s_add_u32 s58, s58, s56\n"
"v_exp_f32 v11, v11\n"
"v_mfma_f32_16x16x32_bf16 a[24:27], a[216:219], v[16:19], a[24:27]\n"
"s_add_u32 s58, s58, 1\n"
"ds_read_b64_tr_b16 a[236:237], v245 offset:57600\n"
"v_cvt_pk_bf16_f32 v21, v10, v11\n"
"v_mfma_f32_16x16x32_bf16 a[28:31], a[220:223], v[16:19], a[28:31]\n"
"s_and_b32 s58, s58, 3\n"
"v_exp_f32 v12, v12\n"
"v_mfma_f32_16x16x32_bf16 v[220:223], a[128:131], v[96:99], v[32:35]\n"
"s_lshl_b32 s58, s58, 3\n"
"ds_read_b64_tr_b16 a[238:239], v245 offset:61696\n"
"v_add_f32 v44, v44, v5\n"
"v_mfma_f32_16x16x32_bf16 v[220:223], a[132:135], v[100:103], v[220:223]\n"
"s_add_u32 s58, s58, s59\n"
"v_exp_f32 v13, v13\n"
"v_mfma_f32_16x16x32_bf16 v[220:223], a[136:139], v[104:107], v[220:223]\n"
"s_mul_i32 s58, s58, 36864\n"
"ds_read_b64_tr_b16 a[240:241], v244 offset:57856\n"
"v_cvt_pk_bf16_f32 v22, v12, v13\n"
"v_mfma_f32_16x16x32_bf16 v[220:223], a[140:143], v[108:111], v[220:223]\n"
"s_add_u32 s37, s58, s57\n"
"v_exp_f32 v14, v14\n"
"v_mfma_f32_16x16x32_bf16 v[224:227], a[144:147], v[96:99], v[32:35]\n"
"s_add_u32 s58, s41, 16\n"
"ds_read_b64_tr_b16 a[242:243], v244 offset:61952\n"
"v_add_f32 v44, v44, v6\n"
"v_mfma_f32_16x16x32_bf16 v[224:227], a[148:151], v[100:103], v[224:227]\n"
"s_mul_i32 s59, s58, 43\n"
"v_exp_f32 v15, v15\n"
"v_mfma_f32_16x16x32_bf16 v[224:227], a[152:155], v[104:107], v[224:227]\n"
"s_lshr_b32 s59, s59, 7\n"
"ds_read_b64_tr_b16 a[244:245], v245 offset:57856\n"
"v_cvt_pk_bf16_f32 v23, v14, v15\n"
"v_mfma_f32_16x16x32_bf16 v[224:227], a[156:159], v[108:111], v[224:227]\n"
"s_add_u32 s49, s49, 9216\n"
"v_exp_f32 v220, v220\n"
"v_mfma_f32_16x16x32_bf16 a[32:35], a[192:195], v[20:23], a[32:35]\n"
"buffer_load_dwordx4 v246, s[12:15], s84 offen offset:2048 lds\n"
"v_mfma_f32_16x16x32_bf16 a[36:39], a[196:199], v[20:23], a[36:39]\n"
"v_exp_f32 v221, v221\n"
"v_mfma_f32_16x16x32_bf16 a[40:43], a[200:203], v[20:23], a[40:43]\n"
"ds_read_b64_tr_b16 a[246:247], v245 offset:61952\n"
"v_cvt_pk_bf16_f32 v16, v220, v221\n"
"v_mfma_f32_16x16x32_bf16 a[44:47], a[204:207], v[20:23], a[44:47]\n"
"v_exp_f32 v222, v222\n"
"v_mfma_f32_16x16x32_bf16 a[48:51], a[208:211], v[20:23], a[48:51]\n"
"ds_read_b64_tr_b16 a[248:249], v244 offset:58112\n"
"v_add_f32 v44, v44, v7\n"
"v_mfma_f32_16x16x32_bf16 a[52:55], a[212:215], v[20:23], a[52:55]\n"
"v_exp_f32 v223, v223\n"
"v_mfma_f32_16x16x32_bf16 a[56:59], a[216:219], v[20:23], a[56:59]\n"
"ds_read_b64_tr_b16 a[250:251], v244 offset:62208\n"
"v_cvt_pk_bf16_f32 v17, v222, v223\n"
"v_mfma_f32_16x16x32_bf16 a[60:63], a[220:223], v[20:23], a[60:63]\n"
"v_exp_f32 v224, v224\n"
"v_mfma_f32_16x16x32_bf16 v[48:51], v[40:43], v[20:23], v[48:51]\n"
"ds_read_b64_tr_b16 a[252:253], v245 offset:58112\n"
"v_add_f32 v52, v52, v220\n"
"v_mfma_f32_16x16x32_bf16 v[228:231], a[128:131], v[112:115], v[36:39]\n"
"v_exp_f32 v225, v225\n"
"v_mfma_f32_16x16x32_bf16 v[228:231], a[132:135], v[116:119], v[228:231]\n"
"ds_read_b64_tr_b16 a[254:255], v245 offset:62208\n"
"buffer_load_dwordx4 v[140:143], v252, s[64:67], s49 offen\n"
"v_cvt_pk_bf16_f32 v18, v224, v225\n"
"v_mfma_f32_16x16x32_bf16 v[228:231], a[136:139], v[120:123], v[228:231]\n"
"s_mul_i32 s60, s59, 3\n"
"v_exp_f32 v226, v226\n"
"v_mfma_f32_16x16x32_bf16 v[228:231], a[140:143], v[124:127], v[228:231]\n"
"s_sub_u32 s58, s58, s60\n"
"v_add_f32 v52, v52, v221\n"
"v_add_f32 v52, v52, v222\n"
"v_mfma_f32_16x16x32_bf16 v[232:235], a[144:147], v[112:115], v[36:39]\n"
"s_add_u32 s58, s58, s56\n"
"v_exp_f32 v227, v227\n"
"v_mfma_f32_16x16x32_bf16 v[232:235], a[148:151], v[116:119], v[232:235]\n"
"s_add_u32 s58, s58, 1\n"
"v_cvt_pk_bf16_f32 v19, v226, v227\n"
"v_add_f32 v52, v52, v223\n"
"v_mfma_f32_16x16x32_bf16 v[232:235], a[152:155], v[120:123], v[232:235]\n"
"s_and_b32 s58, s58, 3\n"
"buffer_load_dwordx4 v247, s[12:15], s84 offen offset:3072 lds\n"
"v_mfma_f32_16x16x32_bf16 v[232:235], a[156:159], v[124:127], v[232:235]\n"
"s_add_u32 s84, s84, 16384\n"
"v_exp_f32 v228, v228\n"
"v_mfma_f32_16x16x32_bf16 a[64:67], a[192:195], v[16:19], a[64:67]\n"
"s_lshl_b32 s58, s58, 3\n"
"v_add_f32 v52, v52, v224\n"
"v_add_f32 v52, v52, v225\n"
"v_mfma_f32_16x16x32_bf16 a[68:71], a[196:199], v[16:19], a[68:71]\n"
"s_add_u32 s58, s58, s59\n"
"v_exp_f32 v229, v229\n"
"v_mfma_f32_16x16x32_bf16 a[72:75], a[200:203], v[16:19], a[72:75]\n"
"s_mul_i32 s58, s58, 36864\n"
"v_cvt_pk_bf16_f32 v20, v228, v229\n"
"v_add_f32 v52, v52, v226\n"
"v_mfma_f32_16x16x32_bf16 a[76:79], a[204:207], v[16:19], a[76:79]\n"
"s_waitcnt vmcnt(22) lgkmcnt(0)\n"
"v_exp_f32 v230, v230\n"
"v_mfma_f32_16x16x32_bf16 a[80:83], a[208:211], v[16:19], a[80:83]\n"
"s_barrier\n"
"v_mfma_f32_16x16x32_bf16 a[84:87], a[212:215], v[16:19], a[84:87]\n"
"s_add_u32 s38, s58, s57\n"
"v_exp_f32 v231, v231\n"
"v_mfma_f32_16x16x32_bf16 a[88:91], a[216:219], v[16:19], a[88:91]\n"
"s_add_u32 s58, s41, 20\n"
"v_cvt_pk_bf16_f32 v21, v230, v231\n"
"v_add_f32 v52, v52, v227\n"
"v_mfma_f32_16x16x32_bf16 a[92:95], a[220:223], v[16:19], a[92:95]\n"
"s_mul_i32 s59, s58, 43\n"
"v_exp_f32 v232, v232\n"
"v_mfma_f32_16x16x32_bf16 v[0:3], a[160:163], v[64:67], v[24:27]\n"
"s_lshr_b32 s59, s59, 7\n"
"ds_read_b128 a[128:131], v240 offset:0\n"
"v_mfma_f32_16x16x32_bf16 v[0:3], a[164:167], v[68:71], v[0:3]\n"
"s_mul_i32 s60, s59, 3\n"
"v_exp_f32 v233, v233\n"
"v_mfma_f32_16x16x32_bf16 v[0:3], a[168:171], v[72:75], v[0:3]\n"
"s_sub_u32 s58, s58, s60\n"
"ds_read_b128 a[132:135], v240 offset:256\n"
"v_cvt_pk_bf16_f32 v22, v232, v233\n"
"v_mfma_f32_16x16x32_bf16 v[0:3], a[172:175], v[76:79], v[0:3]\n"
"s_add_u32 s58, s58, s56\n"
"v_exp_f32 v234, v234\n"
"v_mfma_f32_16x16x32_bf16 v[4:7], a[176:179], v[64:67], v[24:27]\n"
"s_add_u32 s58, s58, 1\n"
"ds_read_b128 a[136:139], v240 offset:512\n"
"v_mfma_f32_16x16x32_bf16 v[4:7], a[180:183], v[68:71], v[4:7]\n"
"v_exp_f32 v235, v235\n"
"v_mfma_f32_16x16x32_bf16 v[4:7], a[184:187], v[72:75], v[4:7]\n"
"ds_read_b128 a[140:143], v240 offset:768\n"
"v_cvt_pk_bf16_f32 v23, v234, v235\n"
"v_mfma_f32_16x16x32_bf16 v[4:7], a[188:191], v[76:79], v[4:7]\n"
"s_add_u32 m0, s86, 98304\n"
"v_exp_f32 v0, v0\n"
"v_mfma_f32_16x16x32_bf16 a[96:99], a[192:195], v[20:23], a[96:99]\n"
"buffer_load_dwordx4 v246, s[8:11], s84 offen offset:0 lds\n"
"v_mfma_f32_16x16x32_bf16 a[100:103], a[196:199], v[20:23], a[100:103]\n"
"v_exp_f32 v1, v1\n"
"v_mfma_f32_16x16x32_bf16 a[104:107], a[200:203], v[20:23], a[104:107]\n"
"ds_read_b128 a[144:147], v240 offset:4096\n"
"v_cvt_pk_bf16_f32 v16, v0, v1\n"
"v_mfma_f32_16x16x32_bf16 a[108:111], a[204:207], v[20:23], a[108:111]\n"
"v_exp_f32 v2, v2\n"
"v_mfma_f32_16x16x32_bf16 a[112:115], a[208:211], v[20:23], a[112:115]\n"
"ds_read_b128 a[148:151], v240 offset:4352\n"
"v_add_f32 v44, v44, v0\n"
"v_mfma_f32_16x16x32_bf16 a[116:119], a[212:215], v[20:23], a[116:119]\n"
"v_exp_f32 v3, v3\n"
"v_mfma_f32_16x16x32_bf16 a[120:123], a[216:219], v[20:23], a[120:123]\n"
"ds_read_b128 a[152:155], v240 offset:4608\n"
"buffer_load_dwordx4 v[176:179], v252, s[68:71], s49 offen\n"
"v_cvt_pk_bf16_f32 v17, v2, v3\n"
"v_mfma_f32_16x16x32_bf16 a[124:127], a[220:223], v[20:23], a[124:127]\n"
"s_and_b32 s58, s58, 3\n"
"v_exp_f32 v4, v4\n"
"v_mfma_f32_16x16x32_bf16 v[56:59], v[40:43], v[20:23], v[56:59]\n"
"s_lshl_b32 s58, s58, 3\n"
"ds_read_b128 a[156:159], v240 offset:4864\n"
"v_add_f32 v44, v44, v1\n"
"v_mfma_f32_16x16x32_bf16 v[8:11], a[160:163], v[80:83], v[28:31]\n"
"s_add_u32 s58, s58, s59\n"
"v_exp_f32 v5, v5\n"
"v_mfma_f32_16x16x32_bf16 v[8:11], a[164:167], v[84:87], v[8:11]\n"
"s_mul_i32 s58, s58, 36864\n"
"ds_read_b64_tr_b16 a[192:193], v242 offset:16384\n"
"v_cvt_pk_bf16_f32 v18, v4, v5\n"
"v_mfma_f32_16x16x32_bf16 v[8:11], a[168:171], v[88:91], v[8:11]\n"
"s_add_u32 s39, s58, s57\n"
"v_exp_f32 v6, v6\n"
"v_mfma_f32_16x16x32_bf16 v[8:11], a[172:175], v[92:95], v[8:11]\n"
"s_mul_i32 s58, s41, 9216\n"
"ds_read_b64_tr_b16 a[194:195], v242 offset:20480\n"
"v_add_f32 v44, v44, v2\n"
"v_mfma_f32_16x16x32_bf16 v[12:15], a[176:179], v[80:83], v[28:31]\n"
"s_add_u32 s58, s58, s40\n"
"v_exp_f32 v7, v7\n"
"v_mfma_f32_16x16x32_bf16 v[12:15], a[180:183], v[84:87], v[12:15]\n"
"s_sub_u32 s53, s58, 27648\n"
"ds_read_b64_tr_b16 a[196:197], v243 offset:16384\n"
"v_cvt_pk_bf16_f32 v19, v6, v7\n"
"v_mfma_f32_16x16x32_bf16 v[12:15], a[184:187], v[88:91], v[12:15]\n"
"s_add_u32 s58, s58, 131072\n"
"buffer_load_dwordx4 v246, s[8:11], s84 offen offset:1024 lds\n"
"v_mfma_f32_16x16x32_bf16 v[12:15], a[188:191], v[92:95], v[12:15]\n"
"s_waitcnt lgkmcnt(11)\n"
"v_exp_f32 v8, v8\n"
"v_mfma_f32_16x16x32_bf16 a[0:3], a[224:227], v[16:19], a[0:3]\n"
"s_cmp_lt_u32 s41, 3\n"
"s_cselect_b32 s51, s58, s40\n"
"ds_read_b64_tr_b16 a[198:199], v243 offset:20480\n"
"v_add_f32 v44, v44, v3\n"
"v_mfma_f32_16x16x32_bf16 a[4:7], a[228:231], v[16:19], a[4:7]\n"
"s_nop 0\n"
"v_exp_f32 v9, v9\n"
"v_mfma_f32_16x16x32_bf16 a[8:11], a[232:235], v[16:19], a[8:11]\n"
"s_and_b32 s58, s56, 1\n"
"ds_read_b64_tr_b16 a[200:201], v242 offset:16640\n"
"v_cvt_pk_bf16_f32 v20, v8, v9\n"
"v_mfma_f32_16x16x32_bf16 a[12:15], a[236:239], v[16:19], a[12:15]\n"
"s_lshl_b32 s59, s58, 5\n"
"v_exp_f32 v10, v10\n"
"v_mfma_f32_16x16x32_bf16 a[16:19], a[240:243], v[16:19], a[16:19]\n"
"s_add_u32 s59, s59, s40\n"
"ds_read_b64_tr_b16 a[202:203], v242 offset:20736\n"
"v_add_f32 v44, v44, v4\n"
"v_mfma_f32_16x16x32_bf16 a[20:23], a[244:247], v[16:19], a[20:23]\n"
"s_xor_b32 s60, s58, 1\n"
"v_exp_f32 v11, v11\n"
"v_mfma_f32_16x16x32_bf16 a[24:27], a[248:251], v[16:19], a[24:27]\n"
"s_lshl_b32 s60, s60, 8\n"
"ds_read_b64_tr_b16 a[204:205], v243 offset:16640\n"
"v_cvt_pk_bf16_f32 v21, v10, v11\n"
"v_mfma_f32_16x16x32_bf16 a[28:31], a[252:255], v[16:19], a[28:31]\n"
"s_add_u32 s49, s49, 285696\n"
"v_exp_f32 v12, v12\n"
"v_mfma_f32_16x16x32_bf16 v[220:223], a[160:163], v[96:99], v[32:35]\n"
"ds_read_b64_tr_b16 a[206:207], v243 offset:20736\n"
"v_add_f32 v44, v44, v5\n"
"v_mfma_f32_16x16x32_bf16 v[220:223], a[164:167], v[100:103], v[220:223]\n"
"v_exp_f32 v13, v13\n"
"v_mfma_f32_16x16x32_bf16 v[220:223], a[168:171], v[104:107], v[220:223]\n"
"ds_read_b64_tr_b16 a[208:209], v242 offset:16896\n"
"v_cvt_pk_bf16_f32 v22, v12, v13\n"
"v_mfma_f32_16x16x32_bf16 v[220:223], a[172:175], v[108:111], v[220:223]\n"
"v_exp_f32 v14, v14\n"
"v_mfma_f32_16x16x32_bf16 v[224:227], a[176:179], v[96:99], v[32:35]\n"
"ds_read_b64_tr_b16 a[210:211], v242 offset:20992\n"
"v_add_f32 v44, v44, v6\n"
"v_mfma_f32_16x16x32_bf16 v[224:227], a[180:183], v[100:103], v[224:227]\n"
"v_exp_f32 v15, v15\n"
"v_mfma_f32_16x16x32_bf16 v[224:227], a[184:187], v[104:107], v[224:227]\n"
"ds_read_b64_tr_b16 a[212:213], v243 offset:16896\n"
"v_cvt_pk_bf16_f32 v23, v14, v15\n"
"v_mfma_f32_16x16x32_bf16 v[224:227], a[188:191], v[108:111], v[224:227]\n"
"v_exp_f32 v220, v220\n"
"v_mfma_f32_16x16x32_bf16 a[32:35], a[224:227], v[20:23], a[32:35]\n"
"buffer_load_dwordx4 v247, s[8:11], s84 offen offset:2048 lds\n"
"v_mfma_f32_16x16x32_bf16 a[36:39], a[228:231], v[20:23], a[36:39]\n"
"v_exp_f32 v221, v221\n"
"v_mfma_f32_16x16x32_bf16 a[40:43], a[232:235], v[20:23], a[40:43]\n"
"ds_read_b64_tr_b16 a[214:215], v243 offset:20992\n"
"buffer_load_dwordx4 v[144:147], v252, s[64:67], s49 offen\n"
"v_cvt_pk_bf16_f32 v16, v220, v221\n"
"v_mfma_f32_16x16x32_bf16 a[44:47], a[236:239], v[20:23], a[44:47]\n"
"s_add_u32 s60, s60, s40\n"
"v_exp_f32 v222, v222\n"
"v_mfma_f32_16x16x32_bf16 a[48:51], a[240:243], v[20:23], a[48:51]\n"
"ds_read_b64_tr_b16 a[216:217], v242 offset:17152\n"
"v_add_f32 v44, v44, v7\n"
"v_mfma_f32_16x16x32_bf16 a[52:55], a[244:247], v[20:23], a[52:55]\n"
"v_exp_f32 v223, v223\n"
"v_mfma_f32_16x16x32_bf16 a[56:59], a[248:251], v[20:23], a[56:59]\n"
"ds_read_b64_tr_b16 a[218:219], v242 offset:21248\n"
"v_cvt_pk_bf16_f32 v17, v222, v223\n"
"v_mfma_f32_16x16x32_bf16 a[60:63], a[252:255], v[20:23], a[60:63]\n"
"v_exp_f32 v224, v224\n"
"v_mfma_f32_16x16x32_bf16 v[48:51], v[40:43], v[20:23], v[48:51]\n"
"ds_read_b64_tr_b16 a[220:221], v243 offset:17152\n"
"v_add_f32 v52, v52, v220\n"
"v_mfma_f32_16x16x32_bf16 v[228:231], a[160:163], v[112:115], v[36:39]\n"
"v_exp_f32 v225, v225\n"
"v_mfma_f32_16x16x32_bf16 v[228:231], a[164:167], v[116:119], v[228:231]\n"
"ds_read_b64_tr_b16 a[222:223], v243 offset:21248\n"
"v_cvt_pk_bf16_f32 v18, v224, v225\n"
"v_lshrrev_b32 v217, 2, v248\n"
"v_mfma_f32_16x16x32_bf16 v[228:231], a[168:171], v[120:123], v[228:231]\n"
"v_exp_f32 v226, v226\n"
"v_mfma_f32_16x16x32_bf16 v[228:231], a[172:175], v[124:127], v[228:231]\n"
"v_add_f32 v52, v52, v221\n"
"v_add_f32 v52, v52, v222\n"
"v_mfma_f32_16x16x32_bf16 v[232:235], a[176:179], v[112:115], v[36:39]\n"
"v_exp_f32 v227, v227\n"
"v_mfma_f32_16x16x32_bf16 v[232:235], a[180:183], v[116:119], v[232:235]\n"
"v_cvt_pk_bf16_f32 v19, v226, v227\n"
"v_add_f32 v52, v52, v223\n"
"v_mfma_f32_16x16x32_bf16 v[232:235], a[184:187], v[120:123], v[232:235]\n"
"buffer_load_dwordx4 v247, s[8:11], s84 offen offset:3072 lds\n"
"v_mul_u32_u24 v203, 288, v217\n"
"v_mfma_f32_16x16x32_bf16 v[232:235], a[188:191], v[124:127], v[232:235]\n"
"v_exp_f32 v228, v228\n"
"v_mfma_f32_16x16x32_bf16 a[64:67], a[224:227], v[16:19], a[64:67]\n"
"v_add_f32 v52, v52, v224\n"
"v_add_f32 v52, v52, v225\n"
"v_mfma_f32_16x16x32_bf16 a[68:71], a[228:231], v[16:19], a[68:71]\n"
"v_exp_f32 v229, v229\n"
"v_mfma_f32_16x16x32_bf16 a[72:75], a[232:235], v[16:19], a[72:75]\n"
"v_cvt_pk_bf16_f32 v20, v228, v229\n"
"v_add_f32 v52, v52, v226\n"
"v_mfma_f32_16x16x32_bf16 a[76:79], a[236:239], v[16:19], a[76:79]\n"
"v_exp_f32 v230, v230\n"
"v_mfma_f32_16x16x32_bf16 a[80:83], a[240:243], v[16:19], a[80:83]\n"
"s_waitcnt lgkmcnt(8)\n"
"v_and_b32 v217, 3, v248\n"
"v_mfma_f32_16x16x32_bf16 a[84:87], a[244:247], v[16:19], a[84:87]\n"
"v_exp_f32 v231, v231\n"
"v_mfma_f32_16x16x32_bf16 a[88:91], a[248:251], v[16:19], a[88:91]\n"
"v_cvt_pk_bf16_f32 v21, v230, v231\n"
"v_add_f32 v52, v52, v227\n"
"v_mfma_f32_16x16x32_bf16 a[92:95], a[252:255], v[16:19], a[92:95]\n"
"v_exp_f32 v232, v232\n"
"v_mfma_f32_16x16x32_bf16 v[0:3], a[128:131], v[64:67], v[24:27]\n"
"ds_read_b128 a[160:163], v240 offset:8192\n"
"v_lshl_add_u32 v203, v217, 3, v203\n"
"v_mfma_f32_16x16x32_bf16 v[0:3], a[132:135], v[68:71], v[0:3]\n"
"v_exp_f32 v233, v233\n"
"v_mfma_f32_16x16x32_bf16 v[0:3], a[136:139], v[72:75], v[0:3]\n"
"ds_read_b128 a[164:167], v240 offset:8448\n"
"v_cvt_pk_bf16_f32 v22, v232, v233\n"
"v_mfma_f32_16x16x32_bf16 v[0:3], a[140:143], v[76:79], v[0:3]\n"
"v_exp_f32 v234, v234\n"
"v_mfma_f32_16x16x32_bf16 v[4:7], a[144:147], v[64:67], v[24:27]\n"
"ds_read_b128 a[168:171], v240 offset:8704\n"
"v_add_u32 v200, s59, v203\n"
"v_mfma_f32_16x16x32_bf16 v[4:7], a[148:151], v[68:71], v[4:7]\n"
"v_exp_f32 v235, v235\n"
"v_mfma_f32_16x16x32_bf16 v[4:7], a[152:155], v[72:75], v[4:7]\n"
"ds_read_b128 a[172:175], v240 offset:8960\n"
"v_cvt_pk_bf16_f32 v23, v234, v235\n"
"v_mfma_f32_16x16x32_bf16 v[4:7], a[156:159], v[76:79], v[4:7]\n"
"s_add_u32 m0, s86, 114688\n"
"v_exp_f32 v0, v0\n"
"v_mfma_f32_16x16x32_bf16 a[96:99], a[224:227], v[20:23], a[96:99]\n"
"buffer_load_dwordx4 v246, s[12:15], s84 offen offset:0 lds\n"
"v_add_u32 v203, s60, v203\n"
"v_mfma_f32_16x16x32_bf16 a[100:103], a[228:231], v[20:23], a[100:103]\n"
"v_exp_f32 v1, v1\n"
"v_mfma_f32_16x16x32_bf16 a[104:107], a[232:235], v[20:23], a[104:107]\n"
"ds_read_b128 a[176:179], v240 offset:12288\n"
"v_cvt_pk_bf16_f32 v16, v0, v1\n"
"v_mfma_f32_16x16x32_bf16 a[108:111], a[236:239], v[20:23], a[108:111]\n"
"v_exp_f32 v2, v2\n"
"v_mfma_f32_16x16x32_bf16 a[112:115], a[240:243], v[20:23], a[112:115]\n"
"ds_read_b128 a[180:183], v240 offset:12544\n"
"v_add_f32 v44, v44, v0\n"
"v_mfma_f32_16x16x32_bf16 a[116:119], a[244:247], v[20:23], a[116:119]\n"
"v_exp_f32 v3, v3\n"
"v_mfma_f32_16x16x32_bf16 a[120:123], a[248:251], v[20:23], a[120:123]\n"
"ds_read_b128 a[184:187], v240 offset:12800\n"
"v_cvt_pk_bf16_f32 v17, v2, v3\n"
"v_mfma_f32_16x16x32_bf16 a[124:127], a[252:255], v[20:23], a[124:127]\n"
"v_exp_f32 v4, v4\n"
"v_add_u32 v201, 65536, v200\n"
"v_mfma_f32_16x16x32_bf16 v[56:59], v[40:43], v[20:23], v[56:59]\n"
"ds_read_b128 a[188:191], v240 offset:13056\n"
"v_add_f32 v44, v44, v1\n"
"v_mfma_f32_16x16x32_bf16 v[8:11], a[128:131], v[80:83], v[28:31]\n"
"v_exp_f32 v5, v5\n"
"v_mfma_f32_16x16x32_bf16 v[8:11], a[132:135], v[84:87], v[8:11]\n"
"ds_read_b64_tr_b16 a[224:225], v242 offset:24576\n"
"v_cvt_pk_bf16_f32 v18, v4, v5\n"
"v_mfma_f32_16x16x32_bf16 v[8:11], a[136:139], v[88:91], v[8:11]\n"
"v_exp_f32 v6, v6\n"
"v_mfma_f32_16x16x32_bf16 v[8:11], a[140:143], v[92:95], v[8:11]\n"
"ds_read_b64_tr_b16 a[226:227], v242 offset:28672\n"
"v_add_f32 v44, v44, v2\n"
"v_mfma_f32_16x16x32_bf16 v[12:15], a[144:147], v[80:83], v[28:31]\n"
"v_exp_f32 v7, v7\n"
"v_mfma_f32_16x16x32_bf16 v[12:15], a[148:151], v[84:87], v[12:15]\n"
"ds_read_b64_tr_b16 a[228:229], v243 offset:24576\n"
"v_cvt_pk_bf16_f32 v19, v6, v7\n"
"v_add_u32 v204, 65536, v203\n"
"v_mfma_f32_16x16x32_bf16 v[12:15], a[152:155], v[88:91], v[12:15]\n"
"buffer_load_dwordx4 v247, s[12:15], s84 offen offset:1024 lds\n"
"v_add_u32 v202, 131072, v200\n"
"v_mfma_f32_16x16x32_bf16 v[12:15], a[156:159], v[92:95], v[12:15]\n"
"s_waitcnt lgkmcnt(11)\n"
"v_exp_f32 v8, v8\n"
"v_mfma_f32_16x16x32_bf16 a[0:3], a[192:195], v[16:19], a[0:3]\n"
"ds_read_b64_tr_b16 a[230:231], v243 offset:28672\n"
"v_add_f32 v44, v44, v3\n"
"v_mfma_f32_16x16x32_bf16 a[4:7], a[196:199], v[16:19], a[4:7]\n"
"v_exp_f32 v9, v9\n"
"v_mfma_f32_16x16x32_bf16 a[8:11], a[200:203], v[16:19], a[8:11]\n"
"ds_read_b64_tr_b16 a[232:233], v242 offset:24832\n"
"v_cvt_pk_bf16_f32 v20, v8, v9\n"
"v_mfma_f32_16x16x32_bf16 a[12:15], a[204:207], v[16:19], a[12:15]\n"
"v_exp_f32 v10, v10\n"
"v_mfma_f32_16x16x32_bf16 a[16:19], a[208:211], v[16:19], a[16:19]\n"
"ds_read_b64_tr_b16 a[234:235], v242 offset:28928\n"
"v_add_f32 v44, v44, v4\n"
"v_mfma_f32_16x16x32_bf16 a[20:23], a[212:215], v[16:19], a[20:23]\n"
"v_exp_f32 v11, v11\n"
"v_add_u32 v205, 131072, v203\n"
"v_mfma_f32_16x16x32_bf16 a[24:27], a[216:219], v[16:19], a[24:27]\n"
"s_add_u32 s62, s40, 163776\n"
"ds_read_b64_tr_b16 a[236:237], v243 offset:24832\n"
"v_cvt_pk_bf16_f32 v21, v10, v11\n"
"v_mfma_f32_16x16x32_bf16 a[28:31], a[220:223], v[16:19], a[28:31]\n"
"v_exp_f32 v12, v12\n"
"v_mfma_f32_16x16x32_bf16 v[220:223], a[128:131], v[96:99], v[32:35]\n"
"ds_read_b64_tr_b16 a[238:239], v243 offset:28928\n"
"v_add_f32 v44, v44, v5\n"
"v_mfma_f32_16x16x32_bf16 v[220:223], a[132:135], v[100:103], v[220:223]\n"
"v_exp_f32 v13, v13\n"
"v_mfma_f32_16x16x32_bf16 v[220:223], a[136:139], v[104:107], v[220:223]\n"
"ds_read_b64_tr_b16 a[240:241], v242 offset:25088\n"
"v_cvt_pk_bf16_f32 v22, v12, v13\n"
"v_mfma_f32_16x16x32_bf16 v[220:223], a[140:143], v[108:111], v[220:223]\n"
"v_exp_f32 v14, v14\n"
"v_mfma_f32_16x16x32_bf16 v[224:227], a[144:147], v[96:99], v[32:35]\n"
"ds_read_b64_tr_b16 a[242:243], v242 offset:29184\n"
"v_add_f32 v44, v44, v6\n"
"v_mfma_f32_16x16x32_bf16 v[224:227], a[148:151], v[100:103], v[224:227]\n"
"v_exp_f32 v15, v15\n"
"v_lshl_add_u32 v209, v217, 2, s62\n"
"v_mfma_f32_16x16x32_bf16 v[224:227], a[152:155], v[104:107], v[224:227]\n"
"s_lshl_b32 s55, s44, 4\n"
"ds_read_b64_tr_b16 a[244:245], v243 offset:25088\n"
"v_cvt_pk_bf16_f32 v23, v14, v15\n"
"v_mfma_f32_16x16x32_bf16 v[224:227], a[156:159], v[108:111], v[224:227]\n"
"s_add_u32 s55, s55, s43\n"
"v_exp_f32 v220, v220\n"
"v_mfma_f32_16x16x32_bf16 a[32:35], a[192:195], v[20:23], a[32:35]\n"
"s_lshl_b32 s52, s55, 2\n"
"buffer_load_dwordx4 v246, s[12:15], s84 offen offset:2048 lds\n"
"v_mfma_f32_16x16x32_bf16 a[36:39], a[196:199], v[20:23], a[36:39]\n"
"AT_pg10:\n"
"s_cbranch_execz AT_hr\n"
"v_exp_f32 v221, v221\n"
"v_mfma_f32_16x16x32_bf16 a[40:43], a[200:203], v[20:23], a[40:43]\n"
"ds_read_b64_tr_b16 a[246:247], v243 offset:29184\n"
"buffer_load_dwordx4 v[180:183], v252, s[68:71], s49 offen\n"
"v_cvt_pk_bf16_f32 v16, v220, v221\n"
"v_mfma_f32_16x16x32_bf16 a[44:47], a[204:207], v[20:23], a[44:47]\n"
"s_add_u32 s52, s52, s41\n"
"v_exp_f32 v222, v222\n"
"v_mfma_f32_16x16x32_bf16 a[48:51], a[208:211], v[20:23], a[48:51]\n"
"s_lshl_b32 s52, s52, 14\n"
"ds_read_b64_tr_b16 a[248:249], v242 offset:25344\n"
"v_add_f32 v44, v44, v7\n"
"v_mfma_f32_16x16x32_bf16 a[52:55], a[212:215], v[20:23], a[52:55]\n"
"s_add_u32 s52, s52, 9592832\n"
"v_exp_f32 v223, v223\n"
"v_mfma_f32_16x16x32_bf16 a[56:59], a[216:219], v[20:23], a[56:59]\n"
"s_add_u32 s49, s49, 9216\n"
"ds_read_b64_tr_b16 a[250:251], v242 offset:29440\n"
"v_cvt_pk_bf16_f32 v17, v222, v223\n"
"v_mfma_f32_16x16x32_bf16 a[60:63], a[220:223], v[20:23], a[60:63]\n"
"v_exp_f32 v224, v224\n"
"v_mfma_f32_16x16x32_bf16 v[48:51], v[40:43], v[20:23], v[48:51]\n"
"ds_read_b64_tr_b16 a[252:253], v243 offset:25344\n"
"v_add_f32 v52, v52, v220\n"
"v_mfma_f32_16x16x32_bf16 v[228:231], a[128:131], v[112:115], v[36:39]\n"
"v_exp_f32 v225, v225\n"
"v_mfma_f32_16x16x32_bf16 v[228:231], a[132:135], v[116:119], v[228:231]\n"
"ds_read_b64_tr_b16 a[254:255], v243 offset:29440\n"
"v_cvt_pk_bf16_f32 v18, v224, v225\n"
"v_mfma_f32_16x16x32_bf16 v[228:231], a[136:139], v[120:123], v[228:231]\n"
"v_exp_f32 v226, v226\n"
"v_mfma_f32_16x16x32_bf16 v[228:231], a[140:143], v[124:127], v[228:231]\n"
"v_add_f32 v52, v52, v221\n"
"v_add_f32 v52, v52, v222\n"
"v_mfma_f32_16x16x32_bf16 v[232:235], a[144:147], v[112:115], v[36:39]\n"
"v_exp_f32 v227, v227\n"
"v_mfma_f32_16x16x32_bf16 v[232:235], a[148:151], v[116:119], v[232:235]\n"
"v_cvt_pk_bf16_f32 v19, v226, v227\n"
"v_add_f32 v52, v52, v223\n"
"v_mfma_f32_16x16x32_bf16 v[232:235], a[152:155], v[120:123], v[232:235]\n"
"buffer_load_dwordx4 v247, s[12:15], s84 offen offset:3072 lds\n"
"v_mfma_f32_16x16x32_bf16 v[232:235], a[156:159], v[124:127], v[232:235]\n"
"s_add_u32 s84, s84, 16384\n"
"v_exp_f32 v228, v228\n"
"v_mfma_f32_16x16x32_bf16 a[64:67], a[192:195], v[16:19], a[64:67]\n"
"v_add_f32 v52, v52, v224\n"
"v_add_f32 v52, v52, v225\n"
"v_mfma_f32_16x16x32_bf16 a[68:71], a[196:199], v[16:19], a[68:71]\n"
"v_exp_f32 v229, v229\n"
"v_mfma_f32_16x16x32_bf16 a[72:75], a[200:203], v[16:19], a[72:75]\n"
"v_cvt_pk_bf16_f32 v20, v228, v229\n"
"v_add_f32 v52, v52, v226\n"
"v_mfma_f32_16x16x32_bf16 a[76:79], a[204:207], v[16:19], a[76:79]\n"
"s_waitcnt vmcnt(24) lgkmcnt(0)\n"
"v_exp_f32 v230, v230\n"
"v_mfma_f32_16x16x32_bf16 a[80:83], a[208:211], v[16:19], a[80:83]\n"
"s_barrier\n"
"v_mfma_f32_16x16x32_bf16 a[84:87], a[212:215], v[16:19], a[84:87]\n"
"v_exp_f32 v231, v231\n"
"v_mfma_f32_16x16x32_bf16 a[88:91], a[216:219], v[16:19], a[88:91]\n"
"v_cvt_pk_bf16_f32 v21, v230, v231\n"
"v_add_f32 v52, v52, v227\n"
"v_mfma_f32_16x16x32_bf16 a[92:95], a[220:223], v[16:19], a[92:95]\n"
"v_exp_f32 v232, v232\n"
"v_mfma_f32_16x16x32_bf16 v[0:3], a[160:163], v[64:67], v[24:27]\n"
"ds_read_b128 a[128:131], v240 offset:32768\n"
"v_mfma_f32_16x16x32_bf16 v[0:3], a[164:167], v[68:71], v[0:3]\n"
"v_exp_f32 v233, v233\n"
"v_mfma_f32_16x16x32_bf16 v[0:3], a[168:171], v[72:75], v[0:3]\n"
"ds_read_b128 a[132:135], v240 offset:33024\n"
"v_cvt_pk_bf16_f32 v22, v232, v233\n"
"v_mfma_f32_16x16x32_bf16 v[0:3], a[172:175], v[76:79], v[0:3]\n"
"v_exp_f32 v234, v234\n"
"v_mfma_f32_16x16x32_bf16 v[4:7], a[176:179], v[64:67], v[24:27]\n"
"ds_read_b128 a[136:139], v240 offset:33280\n"
"v_mfma_f32_16x16x32_bf16 v[4:7], a[180:183], v[68:71], v[4:7]\n"
"v_exp_f32 v235, v235\n"
"v_mfma_f32_16x16x32_bf16 v[4:7], a[184:187], v[72:75], v[4:7]\n"
"ds_read_b128 a[140:143], v240 offset:33536\n"
"buffer_load_dwordx4 v[148:151], v252, s[64:67], s49 offen\n"
"v_cvt_pk_bf16_f32 v23, v234, v235\n"
"v_mfma_f32_16x16x32_bf16 v[4:7], a[188:191], v[76:79], v[4:7]\n"
"s_mov_b32 m0, s51\n"
"v_exp_f32 v0, v0\n"
"v_mfma_f32_16x16x32_bf16 a[96:99], a[192:195], v[20:23], a[96:99]\n"
"buffer_load_dwordx4 v210, s[24:27], s20 offen offset:0 lds\n"
"v_mfma_f32_16x16x32_bf16 a[100:103], a[196:199], v[20:23], a[100:103]\n"
"v_exp_f32 v1, v1\n"
"v_mfma_f32_16x16x32_bf16 a[104:107], a[200:203], v[20:23], a[104:107]\n"
"ds_read_b128 a[144:147], v240 offset:36864\n"
"v_cvt_pk_bf16_f32 v16, v0, v1\n"
"v_mfma_f32_16x16x32_bf16 a[108:111], a[204:207], v[20:23], a[108:111]\n"
"v_exp_f32 v2, v2\n"
"v_mfma_f32_16x16x32_bf16 a[112:115], a[208:211], v[20:23], a[112:115]\n"
"ds_read_b128 a[148:151], v240 offset:37120\n"
"v_add_f32 v44, v44, v0\n"
"v_mfma_f32_16x16x32_bf16 a[116:119], a[212:215], v[20:23], a[116:119]\n"
"v_exp_f32 v3, v3\n"
"v_mfma_f32_16x16x32_bf16 a[120:123], a[216:219], v[20:23], a[120:123]\n"
"ds_read_b128 a[152:155], v240 offset:37376\n"
"v_cvt_pk_bf16_f32 v17, v2, v3\n"
"v_mfma_f32_16x16x32_bf16 a[124:127], a[220:223], v[20:23], a[124:127]\n"
"v_exp_f32 v4, v4\n"
"v_mfma_f32_16x16x32_bf16 v[56:59], v[40:43], v[20:23], v[56:59]\n"
"ds_read_b128 a[156:159], v240 offset:37632\n"
"v_add_f32 v44, v44, v1\n"
"v_mfma_f32_16x16x32_bf16 v[8:11], a[160:163], v[80:83], v[28:31]\n"
"v_exp_f32 v5, v5\n"
"v_mfma_f32_16x16x32_bf16 v[8:11], a[164:167], v[84:87], v[8:11]\n"
"ds_read_b64_tr_b16 a[192:193], v242 offset:49152\n"
"v_cvt_pk_bf16_f32 v18, v4, v5\n"
"v_mfma_f32_16x16x32_bf16 v[8:11], a[168:171], v[88:91], v[8:11]\n"
"v_exp_f32 v6, v6\n"
"v_mfma_f32_16x16x32_bf16 v[8:11], a[172:175], v[92:95], v[8:11]\n"
"ds_read_b64_tr_b16 a[194:195], v242 offset:53248\n"
"v_add_f32 v44, v44, v2\n"
"v_mfma_f32_16x16x32_bf16 v[12:15], a[176:179], v[80:83], v[28:31]\n"
"v_exp_f32 v7, v7\n"
"v_mfma_f32_16x16x32_bf16 v[12:15], a[180:183], v[84:87], v[12:15]\n"
"s_mov_b32 m0, s51\n"
"ds_read_b64_tr_b16 a[196:197], v243 offset:49152\n"
"v_cvt_pk_bf16_f32 v19, v6, v7\n"
"v_mfma_f32_16x16x32_bf16 v[12:15], a[184:187], v[88:91], v[12:15]\n"
"buffer_load_dwordx4 v211, s[24:27], s20 offen offset:1024 lds\n"
"v_mfma_f32_16x16x32_bf16 v[12:15], a[188:191], v[92:95], v[12:15]\n"
"s_waitcnt lgkmcnt(11)\n"
"v_exp_f32 v8, v8\n"
"v_mfma_f32_16x16x32_bf16 a[0:3], a[224:227], v[16:19], a[0:3]\n"
"ds_read_b64_tr_b16 a[198:199], v243 offset:53248\n"
"v_add_f32 v44, v44, v3\n"
"v_mfma_f32_16x16x32_bf16 a[4:7], a[228:231], v[16:19], a[4:7]\n"
"v_exp_f32 v9, v9\n"
"v_mfma_f32_16x16x32_bf16 a[8:11], a[232:235], v[16:19], a[8:11]\n"
"ds_read_b64_tr_b16 a[200:201], v242 offset:49408\n"
"v_cvt_pk_bf16_f32 v20, v8, v9\n"
"v_mfma_f32_16x16x32_bf16 a[12:15], a[236:239], v[16:19], a[12:15]\n"
"v_exp_f32 v10, v10\n"
"v_mfma_f32_16x16x32_bf16 a[16:19], a[240:243], v[16:19], a[16:19]\n"
"ds_read_b64_tr_b16 a[202:203], v242 offset:53504\n"
"v_add_f32 v44, v44, v4\n"
"v_mfma_f32_16x16x32_bf16 a[20:23], a[244:247], v[16:19], a[20:23]\n"
"v_exp_f32 v11, v11\n"
"v_mfma_f32_16x16x32_bf16 a[24:27], a[248:251], v[16:19], a[24:27]\n"
"ds_read_b64_tr_b16 a[204:205], v243 offset:49408\n"
"v_cvt_pk_bf16_f32 v21, v10, v11\n"
"v_mfma_f32_16x16x32_bf16 a[28:31], a[252:255], v[16:19], a[28:31]\n"
"v_exp_f32 v12, v12\n"
"v_mfma_f32_16x16x32_bf16 v[220:223], a[160:163], v[96:99], v[32:35]\n"
"ds_read_b64_tr_b16 a[206:207], v243 offset:53504\n"
"v_add_f32 v44, v44, v5\n"
"v_mfma_f32_16x16x32_bf16 v[220:223], a[164:167], v[100:103], v[220:223]\n"
"v_exp_f32 v13, v13\n"
"v_mfma_f32_16x16x32_bf16 v[220:223], a[168:171], v[104:107], v[220:223]\n"
"ds_read_b64_tr_b16 a[208:209], v242 offset:49664\n"
"buffer_load_dwordx4 v[184:187], v252, s[68:71], s49 offen\n"
"v_cvt_pk_bf16_f32 v22, v12, v13\n"
"v_mfma_f32_16x16x32_bf16 v[220:223], a[172:175], v[108:111], v[220:223]\n"
"s_add_u32 s49, s49, 285696\n"
"v_exp_f32 v14, v14\n"
"v_mfma_f32_16x16x32_bf16 v[224:227], a[176:179], v[96:99], v[32:35]\n"
"ds_read_b64_tr_b16 a[210:211], v242 offset:53760\n"
"v_add_f32 v44, v44, v6\n"
"v_mfma_f32_16x16x32_bf16 v[224:227], a[180:183], v[100:103], v[224:227]\n"
"v_exp_f32 v15, v15\n"
"v_mfma_f32_16x16x32_bf16 v[224:227], a[184:187], v[104:107], v[224:227]\n"
"ds_read_b64_tr_b16 a[212:213], v243 offset:49664\n"
"v_cvt_pk_bf16_f32 v23, v14, v15\n"
"v_mfma_f32_16x16x32_bf16 v[224:227], a[188:191], v[108:111], v[224:227]\n"
"s_mov_b32 m0, s51\n"
"v_exp_f32 v220, v220\n"
"v_mfma_f32_16x16x32_bf16 a[32:35], a[224:227], v[20:23], a[32:35]\n"
"buffer_load_dwordx4 v212, s[24:27], s20 offen offset:2048 lds\n"
"v_mfma_f32_16x16x32_bf16 a[36:39], a[228:231], v[20:23], a[36:39]\n"
"v_exp_f32 v221, v221\n"
"v_mfma_f32_16x16x32_bf16 a[40:43], a[232:235], v[20:23], a[40:43]\n"
"ds_read_b64_tr_b16 a[214:215], v243 offset:53760\n"
"v_cvt_pk_bf16_f32 v16, v220, v221\n"
"v_mfma_f32_16x16x32_bf16 a[44:47], a[236:239], v[20:23], a[44:47]\n"
"v_exp_f32 v222, v222\n"
"v_mfma_f32_16x16x32_bf16 a[48:51], a[240:243], v[20:23], a[48:51]\n"
"ds_read_b64_tr_b16 a[216:217], v242 offset:49920\n"
"v_add_f32 v44, v44, v7\n"
"v_mfma_f32_16x16x32_bf16 a[52:55], a[244:247], v[20:23], a[52:55]\n"
"v_exp_f32 v223, v223\n"
"v_mfma_f32_16x16x32_bf16 a[56:59], a[248:251], v[20:23], a[56:59]\n"
"ds_read_b64_tr_b16 a[218:219], v242 offset:54016\n"
"v_cvt_pk_bf16_f32 v17, v222, v223\n"
"v_mfma_f32_16x16x32_bf16 a[60:63], a[252:255], v[20:23], a[60:63]\n"
"v_exp_f32 v224, v224\n"
"v_mfma_f32_16x16x32_bf16 v[48:51], v[40:43], v[20:23], v[48:51]\n"
"ds_read_b64_tr_b16 a[220:221], v243 offset:49920\n"
"v_add_f32 v52, v52, v220\n"
"v_mfma_f32_16x16x32_bf16 v[228:231], a[160:163], v[112:115], v[36:39]\n"
"v_exp_f32 v225, v225\n"
"v_mfma_f32_16x16x32_bf16 v[228:231], a[164:167], v[116:119], v[228:231]\n"
"ds_read_b64_tr_b16 a[222:223], v243 offset:54016\n"
"v_cvt_pk_bf16_f32 v18, v224, v225\n"
"v_mfma_f32_16x16x32_bf16 v[228:231], a[168:171], v[120:123], v[228:231]\n"
"v_exp_f32 v226, v226\n"
"v_mfma_f32_16x16x32_bf16 v[228:231], a[172:175], v[124:127], v[228:231]\n"
"v_add_f32 v52, v52, v221\n"
"v_add_f32 v52, v52, v222\n"
"v_mfma_f32_16x16x32_bf16 v[232:235], a[176:179], v[112:115], v[36:39]\n"
"v_exp_f32 v227, v227\n"
"v_mfma_f32_16x16x32_bf16 v[232:235], a[180:183], v[116:119], v[232:235]\n"
"s_mov_b32 m0, s51\n"
"v_cvt_pk_bf16_f32 v19, v226, v227\n"
"v_add_f32 v52, v52, v223\n"
"v_mfma_f32_16x16x32_bf16 v[232:235], a[184:187], v[120:123], v[232:235]\n"
"buffer_load_dwordx4 v213, s[24:27], s20 offen offset:3072 lds\n"
"v_mfma_f32_16x16x32_bf16 v[232:235], a[188:191], v[124:127], v[232:235]\n"
"v_exp_f32 v228, v228\n"
"v_mfma_f32_16x16x32_bf16 a[64:67], a[224:227], v[16:19], a[64:67]\n"
"v_add_f32 v52, v52, v224\n"
"v_add_f32 v52, v52, v225\n"
"v_mfma_f32_16x16x32_bf16 a[68:71], a[228:231], v[16:19], a[68:71]\n"
"v_exp_f32 v229, v229\n"
"v_mfma_f32_16x16x32_bf16 a[72:75], a[232:235], v[16:19], a[72:75]\n"
"v_cvt_pk_bf16_f32 v20, v228, v229\n"
"v_add_f32 v52, v52, v226\n"
"v_mfma_f32_16x16x32_bf16 a[76:79], a[236:239], v[16:19], a[76:79]\n"
"v_exp_f32 v230, v230\n"
"v_mfma_f32_16x16x32_bf16 a[80:83], a[240:243], v[16:19], a[80:83]\n"
"s_waitcnt lgkmcnt(8)\n"
"v_mfma_f32_16x16x32_bf16 a[84:87], a[244:247], v[16:19], a[84:87]\n"
"v_exp_f32 v231, v231\n"
"v_mfma_f32_16x16x32_bf16 a[88:91], a[248:251], v[16:19], a[88:91]\n"
"buffer_load_dwordx4 v[152:155], v252, s[64:67], s49 offen\n"
"v_cvt_pk_bf16_f32 v21, v230, v231\n"
"v_add_f32 v52, v52, v227\n"
"v_mfma_f32_16x16x32_bf16 a[92:95], a[252:255], v[16:19], a[92:95]\n"
"v_exp_f32 v232, v232\n"
"v_mfma_f32_16x16x32_bf16 v[0:3], a[128:131], v[64:67], v[24:27]\n"
"ds_read_b128 a[160:163], v240 offset:40960\n"
"v_mfma_f32_16x16x32_bf16 v[0:3], a[132:135], v[68:71], v[0:3]\n"
"v_exp_f32 v233, v233\n"
"v_mfma_f32_16x16x32_bf16 v[0:3], a[136:139], v[72:75], v[0:3]\n"
"ds_read_b128 a[164:167], v240 offset:41216\n"
"v_cvt_pk_bf16_f32 v22, v232, v233\n"
"v_mfma_f32_16x16x32_bf16 v[0:3], a[140:143], v[76:79], v[0:3]\n"
"v_exp_f32 v234, v234\n"
"v_mfma_f32_16x16x32_bf16 v[4:7], a[144:147], v[64:67], v[24:27]\n"
"ds_read_b128 a[168:171], v240 offset:41472\n"
"v_mfma_f32_16x16x32_bf16 v[4:7], a[148:151], v[68:71], v[4:7]\n"
"v_exp_f32 v235, v235\n"
"v_mfma_f32_16x16x32_bf16 v[4:7], a[152:155], v[72:75], v[4:7]\n"
"ds_read_b128 a[172:175], v240 offset:41728\n"
"v_cvt_pk_bf16_f32 v23, v234, v235\n"
"v_mfma_f32_16x16x32_bf16 v[4:7], a[156:159], v[76:79], v[4:7]\n"
"s_add_u32 m0, s51, 4096\n"
"v_exp_f32 v0, v0\n"
"v_mfma_f32_16x16x32_bf16 a[96:99], a[224:227], v[20:23], a[96:99]\n"
"buffer_load_dwordx4 v214, s[24:27], s20 offen offset:0 lds\n"
"v_mfma_f32_16x16x32_bf16 a[100:103], a[228:231], v[20:23], a[100:103]\n"
"v_exp_f32 v1, v1\n"
"v_mfma_f32_16x16x32_bf16 a[104:107], a[232:235], v[20:23], a[104:107]\n"
"ds_read_b128 a[176:179], v240 offset:45056\n"
"v_cvt_pk_bf16_f32 v16, v0, v1\n"
"v_mfma_f32_16x16x32_bf16 a[108:111], a[236:239], v[20:23], a[108:111]\n"
"v_exp_f32 v2, v2\n"
"v_mfma_f32_16x16x32_bf16 a[112:115], a[240:243], v[20:23], a[112:115]\n"
"ds_read_b128 a[180:183], v240 offset:45312\n"
"v_add_f32 v44, v44, v0\n"
"v_mfma_f32_16x16x32_bf16 a[116:119], a[244:247], v[20:23], a[116:119]\n"
"v_exp_f32 v3, v3\n"
"v_mfma_f32_16x16x32_bf16 a[120:123], a[248:251], v[20:23], a[120:123]\n"
"ds_read_b128 a[184:187], v240 offset:45568\n"
"v_cvt_pk_bf16_f32 v17, v2, v3\n"
"v_mfma_f32_16x16x32_bf16 a[124:127], a[252:255], v[20:23], a[124:127]\n"
"v_exp_f32 v4, v4\n"
"v_mfma_f32_16x16x32_bf16 v[56:59], v[40:43], v[20:23], v[56:59]\n"
"ds_read_b128 a[188:191], v240 offset:45824\n"
"v_add_f32 v44, v44, v1\n"
"v_mfma_f32_16x16x32_bf16 v[8:11], a[128:131], v[80:83], v[28:31]\n"
"v_exp_f32 v5, v5\n"
"v_mfma_f32_16x16x32_bf16 v[8:11], a[132:135], v[84:87], v[8:11]\n"
"ds_read_b64_tr_b16 a[224:225], v242 offset:57344\n"
"v_cvt_pk_bf16_f32 v18, v4, v5\n"
"v_mfma_f32_16x16x32_bf16 v[8:11], a[136:139], v[88:91], v[8:11]\n"
"v_exp_f32 v6, v6\n"
"v_mfma_f32_16x16x32_bf16 v[8:11], a[140:143], v[92:95], v[8:11]\n"
"ds_read_b64_tr_b16 a[226:227], v242 offset:61440\n"
"v_add_f32 v44, v44, v2\n"
"v_mfma_f32_16x16x32_bf16 v[12:15], a[144:147], v[80:83], v[28:31]\n"
"v_exp_f32 v7, v7\n"
"v_mfma_f32_16x16x32_bf16 v[12:15], a[148:151], v[84:87], v[12:15]\n"
"s_add_u32 m0, s51, 4096\n"
"ds_read_b64_tr_b16 a[228:229], v243 offset:57344\n"
"v_cvt_pk_bf16_f32 v19, v6, v7\n"
"v_mfma_f32_16x16x32_bf16 v[12:15], a[152:155], v[88:91], v[12:15]\n"
"buffer_load_dwordx4 v236, s[24:27], s20 offen offset:1024 lds\n"
"v_mfma_f32_16x16x32_bf16 v[12:15], a[156:159], v[92:95], v[12:15]\n"
"s_waitcnt lgkmcnt(11)\n"
"v_exp_f32 v8, v8\n"
"v_mfma_f32_16x16x32_bf16 a[0:3], a[192:195], v[16:19], a[0:3]\n"
"ds_read_b64_tr_b16 a[230:231], v243 offset:61440\n"
"v_add_f32 v44, v44, v3\n"
"v_mfma_f32_16x16x32_bf16 a[4:7], a[196:199], v[16:19], a[4:7]\n"
"v_exp_f32 v9, v9\n"
"v_mfma_f32_16x16x32_bf16 a[8:11], a[200:203], v[16:19], a[8:11]\n"
"ds_read_b64_tr_b16 a[232:233], v242 offset:57600\n"
"buffer_load_dwordx4 v[188:191], v252, s[68:71], s49 offen\n"
"v_cvt_pk_bf16_f32 v20, v8, v9\n"
"v_mfma_f32_16x16x32_bf16 a[12:15], a[204:207], v[16:19], a[12:15]\n"
"s_add_u32 s49, s49, 9216\n"
"v_exp_f32 v10, v10\n"
"v_mfma_f32_16x16x32_bf16 a[16:19], a[208:211], v[16:19], a[16:19]\n"
"ds_read_b64_tr_b16 a[234:235], v242 offset:61696\n"
"v_add_f32 v44, v44, v4\n"
"v_mfma_f32_16x16x32_bf16 a[20:23], a[212:215], v[16:19], a[20:23]\n"
"s_add_u32 m0, s51, 4096\n"
"v_exp_f32 v11, v11\n"
"v_mfma_f32_16x16x32_bf16 a[24:27], a[216:219], v[16:19], a[24:27]\n"
"buffer_load_dwordx4 v237, s[24:27], s20 offen offset:2048 lds\n"
"ds_read_b64_tr_b16 a[236:237], v243 offset:57600\n"
"v_cvt_pk_bf16_f32 v21, v10, v11\n"
"v_mfma_f32_16x16x32_bf16 a[28:31], a[220:223], v[16:19], a[28:31]\n"
"v_exp_f32 v12, v12\n"
"v_mfma_f32_16x16x32_bf16 v[220:223], a[128:131], v[96:99], v[32:35]\n"
"ds_read_b64_tr_b16 a[238:239], v243 offset:61696\n"
"v_add_f32 v44, v44, v5\n"
"v_mfma_f32_16x16x32_bf16 v[220:223], a[132:135], v[100:103], v[220:223]\n"
"v_exp_f32 v13, v13\n"
"v_mfma_f32_16x16x32_bf16 v[220:223], a[136:139], v[104:107], v[220:223]\n"
"ds_read_b64_tr_b16 a[240:241], v242 offset:57856\n"
"v_cvt_pk_bf16_f32 v22, v12, v13\n"
"v_mfma_f32_16x16x32_bf16 v[220:223], a[140:143], v[108:111], v[220:223]\n"
"v_exp_f32 v14, v14\n"
"v_mfma_f32_16x16x32_bf16 v[224:227], a[144:147], v[96:99], v[32:35]\n"
"ds_read_b64_tr_b16 a[242:243], v242 offset:61952\n"
"v_add_f32 v44, v44, v6\n"
"v_mfma_f32_16x16x32_bf16 v[224:227], a[148:151], v[100:103], v[224:227]\n"
"v_exp_f32 v15, v15\n"
"v_mfma_f32_16x16x32_bf16 v[224:227], a[152:155], v[104:107], v[224:227]\n"
"ds_read_b64_tr_b16 a[244:245], v243 offset:57856\n"
"v_cvt_pk_bf16_f32 v23, v14, v15\n"
"v_mfma_f32_16x16x32_bf16 v[224:227], a[156:159], v[108:111], v[224:227]\n"
"s_add_u32 m0, s51, 4096\n"
"v_exp_f32 v220, v220\n"
"v_mfma_f32_16x16x32_bf16 a[32:35], a[192:195], v[20:23], a[32:35]\n"
"buffer_load_dwordx4 v238, s[24:27], s20 offen offset:3072 lds\n"
"v_mfma_f32_16x16x32_bf16 a[36:39], a[196:199], v[20:23], a[36:39]\n"
"v_exp_f32 v221, v221\n"
"v_mfma_f32_16x16x32_bf16 a[40:43], a[200:203], v[20:23], a[40:43]\n"
"ds_read_b64_tr_b16 a[246:247], v243 offset:61952\n"
"v_cvt_pk_bf16_f32 v16, v220, v221\n"
"v_mfma_f32_16x16x32_bf16 a[44:47], a[204:207], v[20:23], a[44:47]\n"
"v_exp_f32 v222, v222\n"
"v_mfma_f32_16x16x32_bf16 a[48:51], a[208:211], v[20:23], a[48:51]\n"
"ds_read_b64_tr_b16 a[248:249], v242 offset:58112\n"
"v_add_f32 v44, v44, v7\n"
"v_mfma_f32_16x16x32_bf16 a[52:55], a[212:215], v[20:23], a[52:55]\n"
"v_exp_f32 v223, v223\n"
"v_mfma_f32_16x16x32_bf16 a[56:59], a[216:219], v[20:23], a[56:59]\n"
"ds_read_b64_tr_b16 a[250:251], v242 offset:62208\n"
"v_cvt_pk_bf16_f32 v17, v222, v223\n"
"v_mfma_f32_16x16x32_bf16 a[60:63], a[220:223], v[20:23], a[60:63]\n"
"v_exp_f32 v224, v224\n"
"v_mfma_f32_16x16x32_bf16 v[48:51], v[40:43], v[20:23], v[48:51]\n"
"ds_read_b64_tr_b16 a[252:253], v243 offset:58112\n"
"v_add_f32 v52, v52, v220\n"
"v_mfma_f32_16x16x32_bf16 v[228:231], a[128:131], v[112:115], v[36:39]\n"
"v_exp_f32 v225, v225\n"
"v_mfma_f32_16x16x32_bf16 v[228:231], a[132:135], v[116:119], v[228:231]\n"
"ds_read_b64_tr_b16 a[254:255], v243 offset:62208\n"
"v_cvt_pk_bf16_f32 v18, v224, v225\n"
"v_mfma_f32_16x16x32_bf16 v[228:231], a[136:139], v[120:123], v[228:231]\n"
"v_exp_f32 v226, v226\n"
"v_mfma_f32_16x16x32_bf16 v[228:231], a[140:143], v[124:127], v[228:231]\n"
"v_add_f32 v52, v52, v221\n"
"v_add_f32 v52, v52, v222\n"
"v_mfma_f32_16x16x32_bf16 v[232:235], a[144:147], v[112:115], v[36:39]\n"
"v_exp_f32 v227, v227\n"
"v_mfma_f32_16x16x32_bf16 v[232:235], a[148:151], v[116:119], v[232:235]\n"
"s_add_u32 m0, s51, 8192\n"
"buffer_load_dwordx4 v[156:159], v252, s[64:67], s49 offen\n"
"v_cvt_pk_bf16_f32 v19, v226, v227\n"
"v_add_f32 v52, v52, v223\n"
"v_mfma_f32_16x16x32_bf16 v[232:235], a[152:155], v[120:123], v[232:235]\n"
"buffer_load_dwordx4 v239, s[24:27], s20 offen offset:0 lds\n"
"v_mfma_f32_16x16x32_bf16 v[232:235], a[156:159], v[124:127], v[232:235]\n"
"v_exp_f32 v228, v228\n"
"v_mfma_f32_16x16x32_bf16 a[64:67], a[192:195], v[16:19], a[64:67]\n"
"v_add_f32 v52, v52, v224\n"
"v_add_f32 v52, v52, v225\n"
"v_mfma_f32_16x16x32_bf16 a[68:71], a[196:199], v[16:19], a[68:71]\n"
"v_exp_f32 v229, v229\n"
"v_mfma_f32_16x16x32_bf16 a[72:75], a[200:203], v[16:19], a[72:75]\n"
"v_cvt_pk_bf16_f32 v20, v228, v229\n"
"v_add_f32 v52, v52, v226\n"
"v_mfma_f32_16x16x32_bf16 a[76:79], a[204:207], v[16:19], a[76:79]\n"
"s_waitcnt vmcnt(25) lgkmcnt(0)\n"
"v_exp_f32 v230, v230\n"
"v_mfma_f32_16x16x32_bf16 a[80:83], a[208:211], v[16:19], a[80:83]\n"
"s_barrier\n"
"v_mfma_f32_16x16x32_bf16 a[84:87], a[212:215], v[16:19], a[84:87]\n"
"v_exp_f32 v231, v231\n"
"v_mfma_f32_16x16x32_bf16 a[88:91], a[216:219], v[16:19], a[88:91]\n"
"v_cvt_pk_bf16_f32 v21, v230, v231\n"
"v_add_f32 v52, v52, v227\n"
"v_mfma_f32_16x16x32_bf16 a[92:95], a[220:223], v[16:19], a[92:95]\n"
"v_exp_f32 v232, v232\n"
"v_mfma_f32_16x16x32_bf16 v[0:3], a[160:163], v[64:67], v[24:27]\n"
"ds_read_b128 a[128:131], v241 offset:0\n"
"v_mfma_f32_16x16x32_bf16 v[0:3], a[164:167], v[68:71], v[0:3]\n"
"v_exp_f32 v233, v233\n"
"v_mfma_f32_16x16x32_bf16 v[0:3], a[168:171], v[72:75], v[0:3]\n"
"ds_read_b128 a[132:135], v241 offset:256\n"
"v_cvt_pk_bf16_f32 v22, v232, v233\n"
"v_mfma_f32_16x16x32_bf16 v[0:3], a[172:175], v[76:79], v[0:3]\n"
"v_exp_f32 v234, v234\n"
"v_mfma_f32_16x16x32_bf16 v[4:7], a[176:179], v[64:67], v[24:27]\n"
"ds_read_b128 a[136:139], v241 offset:512\n"
"v_mfma_f32_16x16x32_bf16 v[4:7], a[180:183], v[68:71], v[4:7]\n"
"v_exp_f32 v235, v235\n"
"v_mfma_f32_16x16x32_bf16 v[4:7], a[184:187], v[72:75], v[4:7]\n"
"ds_read_b128 a[140:143], v241 offset:768\n"
"v_cvt_pk_bf16_f32 v23, v234, v235\n"
"v_mfma_f32_16x16x32_bf16 v[4:7], a[188:191], v[76:79], v[4:7]\n"
"s_add_u32 m0, s53, 36864\n"
"v_exp_f32 v0, v0\n"
"v_mfma_f32_16x16x32_bf16 a[96:99], a[192:195], v[20:23], a[96:99]\n"
"buffer_load_dwordx4 v210, s[24:27], s21 offen offset:0 lds\n"
"v_mfma_f32_16x16x32_bf16 a[100:103], a[196:199], v[20:23], a[100:103]\n"
"v_exp_f32 v1, v1\n"
"v_mfma_f32_16x16x32_bf16 a[104:107], a[200:203], v[20:23], a[104:107]\n"
"ds_read_b128 a[144:147], v241 offset:4096\n"
"v_cvt_pk_bf16_f32 v16, v0, v1\n"
"v_mfma_f32_16x16x32_bf16 a[108:111], a[204:207], v[20:23], a[108:111]\n"
"v_exp_f32 v2, v2\n"
"v_mfma_f32_16x16x32_bf16 a[112:115], a[208:211], v[20:23], a[112:115]\n"
"ds_read_b128 a[148:151], v241 offset:4352\n"
"v_add_f32 v44, v44, v0\n"
"v_mfma_f32_16x16x32_bf16 a[116:119], a[212:215], v[20:23], a[116:119]\n"
"v_exp_f32 v3, v3\n"
"v_mfma_f32_16x16x32_bf16 a[120:123], a[216:219], v[20:23], a[120:123]\n"
"ds_read_b128 a[152:155], v241 offset:4608\n"
"v_cvt_pk_bf16_f32 v17, v2, v3\n"
"v_mfma_f32_16x16x32_bf16 a[124:127], a[220:223], v[20:23], a[124:127]\n"
"v_exp_f32 v4, v4\n"
"v_mfma_f32_16x16x32_bf16 v[56:59], v[40:43], v[20:23], v[56:59]\n"
"ds_read_b128 a[156:159], v241 offset:4864\n"
"v_add_f32 v44, v44, v1\n"
"v_mfma_f32_16x16x32_bf16 v[8:11], a[160:163], v[80:83], v[28:31]\n"
"v_exp_f32 v5, v5\n"
"v_mfma_f32_16x16x32_bf16 v[8:11], a[164:167], v[84:87], v[8:11]\n"
"ds_read_b64_tr_b16 a[192:193], v244 offset:16384\n"
"buffer_load_dwordx4 v[192:195], v252, s[68:71], s49 offen\n"
"v_cvt_pk_bf16_f32 v18, v4, v5\n"
"v_mfma_f32_16x16x32_bf16 v[8:11], a[168:171], v[88:91], v[8:11]\n"
"s_add_u32 s49, s49, 285696\n"
"v_exp_f32 v6, v6\n"
"v_mfma_f32_16x16x32_bf16 v[8:11], a[172:175], v[92:95], v[8:11]\n"
"ds_read_b64_tr_b16 a[194:195], v244 offset:20480\n"
"v_add_f32 v44, v44, v2\n"
"v_mfma_f32_16x16x32_bf16 v[12:15], a[176:179], v[80:83], v[28:31]\n"
"v_exp_f32 v7, v7\n"
"v_mfma_f32_16x16x32_bf16 v[12:15], a[180:183], v[84:87], v[12:15]\n"
"s_add_u32 m0, s53, 36864\n"
"ds_read_b64_tr_b16 a[196:197], v245 offset:16384\n"
"v_cvt_pk_bf16_f32 v19, v6, v7\n"
"v_mfma_f32_16x16x32_bf16 v[12:15], a[184:187], v[88:91], v[12:15]\n"
"buffer_load_dwordx4 v211, s[24:27], s21 offen offset:1024 lds\n"
"v_mfma_f32_16x16x32_bf16 v[12:15], a[188:191], v[92:95], v[12:15]\n"
"s_waitcnt lgkmcnt(11)\n"
"v_exp_f32 v8, v8\n"
"v_mfma_f32_16x16x32_bf16 a[0:3], a[224:227], v[16:19], a[0:3]\n"
"ds_read_b64_tr_b16 a[198:199], v245 offset:20480\n"
"v_add_f32 v44, v44, v3\n"
"v_mfma_f32_16x16x32_bf16 a[4:7], a[228:231], v[16:19], a[4:7]\n"
"v_exp_f32 v9, v9\n"
"v_mfma_f32_16x16x32_bf16 a[8:11], a[232:235], v[16:19], a[8:11]\n"
"ds_read_b64_tr_b16 a[200:201], v244 offset:16640\n"
"v_cvt_pk_bf16_f32 v20, v8, v9\n"
"v_mfma_f32_16x16x32_bf16 a[12:15], a[236:239], v[16:19], a[12:15]\n"
"v_exp_f32 v10, v10\n"
"v_mfma_f32_16x16x32_bf16 a[16:19], a[240:243], v[16:19], a[16:19]\n"
"ds_read_b64_tr_b16 a[202:203], v244 offset:20736\n"
"v_add_f32 v44, v44, v4\n"
"v_mfma_f32_16x16x32_bf16 a[20:23], a[244:247], v[16:19], a[20:23]\n"
"v_exp_f32 v11, v11\n"
"v_mfma_f32_16x16x32_bf16 a[24:27], a[248:251], v[16:19], a[24:27]\n"
"ds_read_b64_tr_b16 a[204:205], v245 offset:16640\n"
"v_cvt_pk_bf16_f32 v21, v10, v11\n"
"v_mfma_f32_16x16x32_bf16 a[28:31], a[252:255], v[16:19], a[28:31]\n"
"v_exp_f32 v12, v12\n"
"v_mfma_f32_16x16x32_bf16 v[220:223], a[160:163], v[96:99], v[32:35]\n"
"ds_read_b64_tr_b16 a[206:207], v245 offset:20736\n"
"v_add_f32 v44, v44, v5\n"
"v_mfma_f32_16x16x32_bf16 v[220:223], a[164:167], v[100:103], v[220:223]\n"
"v_exp_f32 v13, v13\n"
"v_mfma_f32_16x16x32_bf16 v[220:223], a[168:171], v[104:107], v[220:223]\n"
"ds_read_b64_tr_b16 a[208:209], v244 offset:16896\n"
"v_cvt_pk_bf16_f32 v22, v12, v13\n"
"v_mfma_f32_16x16x32_bf16 v[220:223], a[172:175], v[108:111], v[220:223]\n"
"v_exp_f32 v14, v14\n"
"v_mfma_f32_16x16x32_bf16 v[224:227], a[176:179], v[96:99], v[32:35]\n"
"ds_read_b64_tr_b16 a[210:211], v244 offset:20992\n"
"v_add_f32 v44, v44, v6\n"
"v_mfma_f32_16x16x32_bf16 v[224:227], a[180:183], v[100:103], v[224:227]\n"
"v_exp_f32 v15, v15\n"
"v_mfma_f32_16x16x32_bf16 v[224:227], a[184:187], v[104:107], v[224:227]\n"
"ds_read_b64_tr_b16 a[212:213], v245 offset:16896\n"
"v_cvt_pk_bf16_f32 v23, v14, v15\n"
"v_mfma_f32_16x16x32_bf16 v[224:227], a[188:191], v[108:111], v[224:227]\n"
"s_add_u32 m0, s53, 36864\n"
"v_exp_f32 v220, v220\n"
"v_mfma_f32_16x16x32_bf16 a[32:35], a[224:227], v[20:23], a[32:35]\n"
"buffer_load_dwordx4 v212, s[24:27], s21 offen offset:2048 lds\n"
"v_mfma_f32_16x16x32_bf16 a[36:39], a[228:231], v[20:23], a[36:39]\n"
"v_exp_f32 v221, v221\n"
"v_mfma_f32_16x16x32_bf16 a[40:43], a[232:235], v[20:23], a[40:43]\n"
"ds_read_b64_tr_b16 a[214:215], v245 offset:20992\n"
"v_cvt_pk_bf16_f32 v16, v220, v221\n"
"v_mfma_f32_16x16x32_bf16 a[44:47], a[236:239], v[20:23], a[44:47]\n"
"v_exp_f32 v222, v222\n"
"v_mfma_f32_16x16x32_bf16 a[48:51], a[240:243], v[20:23], a[48:51]\n"
"ds_read_b64_tr_b16 a[216:217], v244 offset:17152\n"
"v_add_f32 v44, v44, v7\n"
"v_mfma_f32_16x16x32_bf16 a[52:55], a[244:247], v[20:23], a[52:55]\n"
"v_exp_f32 v223, v223\n"
"v_mfma_f32_16x16x32_bf16 a[56:59], a[248:251], v[20:23], a[56:59]\n"
"ds_read_b64_tr_b16 a[218:219], v244 offset:21248\n"
"buffer_load_dwordx4 v[160:163], v251, s[64:67], s49 offen\n"
"v_cvt_pk_bf16_f32 v17, v222, v223\n"
"v_mfma_f32_16x16x32_bf16 a[60:63], a[252:255], v[20:23], a[60:63]\n"
"v_exp_f32 v224, v224\n"
"v_mfma_f32_16x16x32_bf16 v[48:51], v[40:43], v[20:23], v[48:51]\n"
"ds_read_b64_tr_b16 a[220:221], v245 offset:17152\n"
"v_add_f32 v52, v52, v220\n"
"v_mfma_f32_16x16x32_bf16 v[228:231], a[160:163], v[112:115], v[36:39]\n"
"v_exp_f32 v225, v225\n"
"v_mfma_f32_16x16x32_bf16 v[228:231], a[164:167], v[116:119], v[228:231]\n"
"ds_read_b64_tr_b16 a[222:223], v245 offset:21248\n"
"v_cvt_pk_bf16_f32 v18, v224, v225\n"
"v_mfma_f32_16x16x32_bf16 v[228:231], a[168:171], v[120:123], v[228:231]\n"
"v_exp_f32 v226, v226\n"
"v_mfma_f32_16x16x32_bf16 v[228:231], a[172:175], v[124:127], v[228:231]\n"
"v_add_f32 v52, v52, v221\n"
"v_add_f32 v52, v52, v222\n"
"v_mfma_f32_16x16x32_bf16 v[232:235], a[176:179], v[112:115], v[36:39]\n"
"v_exp_f32 v227, v227\n"
"v_mfma_f32_16x16x32_bf16 v[232:235], a[180:183], v[116:119], v[232:235]\n"
"s_add_u32 m0, s53, 36864\n"
"v_cvt_pk_bf16_f32 v19, v226, v227\n"
"v_add_f32 v52, v52, v223\n"
"v_mfma_f32_16x16x32_bf16 v[232:235], a[184:187], v[120:123], v[232:235]\n"
"buffer_load_dwordx4 v213, s[24:27], s21 offen offset:3072 lds\n"
"v_mfma_f32_16x16x32_bf16 v[232:235], a[188:191], v[124:127], v[232:235]\n"
"v_exp_f32 v228, v228\n"
"v_mfma_f32_16x16x32_bf16 a[64:67], a[224:227], v[16:19], a[64:67]\n"
"v_add_f32 v52, v52, v224\n"
"v_add_f32 v52, v52, v225\n"
"v_mfma_f32_16x16x32_bf16 a[68:71], a[228:231], v[16:19], a[68:71]\n"
"v_exp_f32 v229, v229\n"
"v_mfma_f32_16x16x32_bf16 a[72:75], a[232:235], v[16:19], a[72:75]\n"
"v_cvt_pk_bf16_f32 v20, v228, v229\n"
"v_add_f32 v52, v52, v226\n"
"v_mfma_f32_16x16x32_bf16 a[76:79], a[236:239], v[16:19], a[76:79]\n"
"v_exp_f32 v230, v230\n"
"v_mfma_f32_16x16x32_bf16 a[80:83], a[240:243], v[16:19], a[80:83]\n"
"s_waitcnt lgkmcnt(8)\n"
"v_mfma_f32_16x16x32_bf16 a[84:87], a[244:247], v[16:19], a[84:87]\n"
"v_exp_f32 v231, v231\n"
"v_mfma_f32_16x16x32_bf16 a[88:91], a[248:251], v[16:19], a[88:91]\n"
"v_cvt_pk_bf16_f32 v21, v230, v231\n"
"v_add_f32 v52, v52, v227\n"
"v_mfma_f32_16x16x32_bf16 a[92:95], a[252:255], v[16:19], a[92:95]\n"
"v_exp_f32 v232, v232\n"
"v_mfma_f32_16x16x32_bf16 v[0:3], a[128:131], v[64:67], v[24:27]\n"
"ds_read_b128 a[160:163], v241 offset:8192\n"
"v_mfma_f32_16x16x32_bf16 v[0:3], a[132:135], v[68:71], v[0:3]\n"
"v_exp_f32 v233, v233\n"
"v_mfma_f32_16x16x32_bf16 v[0:3], a[136:139], v[72:75], v[0:3]\n"
"ds_read_b128 a[164:167], v241 offset:8448\n"
"v_cvt_pk_bf16_f32 v22, v232, v233\n"
"v_mfma_f32_16x16x32_bf16 v[0:3], a[140:143], v[76:79], v[0:3]\n"
"v_exp_f32 v234, v234\n"
"v_mfma_f32_16x16x32_bf16 v[4:7], a[144:147], v[64:67], v[24:27]\n"
"ds_read_b128 a[168:171], v241 offset:8704\n"
"v_mfma_f32_16x16x32_bf16 v[4:7], a[148:151], v[68:71], v[4:7]\n"
"v_exp_f32 v235, v235\n"
"v_mfma_f32_16x16x32_bf16 v[4:7], a[152:155], v[72:75], v[4:7]\n"
"ds_read_b128 a[172:175], v241 offset:8960\n"
"v_cvt_pk_bf16_f32 v23, v234, v235\n"
"v_mfma_f32_16x16x32_bf16 v[4:7], a[156:159], v[76:79], v[4:7]\n"
"s_add_u32 m0, s53, 40960\n"
"v_exp_f32 v0, v0\n"
"v_mfma_f32_16x16x32_bf16 a[96:99], a[224:227], v[20:23], a[96:99]\n"
"buffer_load_dwordx4 v214, s[24:27], s21 offen offset:0 lds\n"
"v_mfma_f32_16x16x32_bf16 a[100:103], a[228:231], v[20:23], a[100:103]\n"
"v_exp_f32 v1, v1\n"
"v_mfma_f32_16x16x32_bf16 a[104:107], a[232:235], v[20:23], a[104:107]\n"
"ds_read_b128 a[176:179], v241 offset:12288\n"
"buffer_load_dwordx4 v[196:199], v251, s[68:71], s49 offen\n"
"v_cvt_pk_bf16_f32 v16, v0, v1\n"
"v_mfma_f32_16x16x32_bf16 a[108:111], a[236:239], v[20:23], a[108:111]\n"
"s_lshl_b32 s62, s41, 6\n"
"v_exp_f32 v2, v2\n"
"v_mfma_f32_16x16x32_bf16 a[112:115], a[240:243], v[20:23], a[112:115]\n"
"ds_read_b128 a[180:183], v241 offset:12544\n"
"v_add_f32 v44, v44, v0\n"
"v_mfma_f32_16x16x32_bf16 a[116:119], a[244:247], v[20:23], a[116:119]\n"
"AT_pg11:\n"
"s_cbranch_execz AT_hr\n"
"v_exp_f32 v3, v3\n"
"v_mfma_f32_16x16x32_bf16 a[120:123], a[248:251], v[20:23], a[120:123]\n"
"ds_read_b128 a[184:187], v241 offset:12800\n"
"v_cvt_pk_bf16_f32 v17, v2, v3\n"
"v_mfma_f32_16x16x32_bf16 a[124:127], a[252:255], v[20:23], a[124:127]\n"
"v_exp_f32 v4, v4\n"
"v_mfma_f32_16x16x32_bf16 v[56:59], v[40:43], v[20:23], v[56:59]\n"
"ds_read_b128 a[188:191], v241 offset:13056\n"
"v_add_f32 v44, v44, v1\n"
"v_mfma_f32_16x16x32_bf16 v[8:11], a[128:131], v[80:83], v[28:31]\n"
"v_exp_f32 v5, v5\n"
"v_mfma_f32_16x16x32_bf16 v[8:11], a[132:135], v[84:87], v[8:11]\n"
"ds_read_b64_tr_b16 a[224:225], v244 offset:24576\n"
"v_cvt_pk_bf16_f32 v18, v4, v5\n"
"v_add_u32 v253, s62, v248\n"
"v_mfma_f32_16x16x32_bf16 v[8:11], a[136:139], v[88:91], v[8:11]\n"
"v_exp_f32 v6, v6\n"
"v_mfma_f32_16x16x32_bf16 v[8:11], a[140:143], v[92:95], v[8:11]\n"
"ds_read_b64_tr_b16 a[226:227], v244 offset:28672\n"
"v_add_f32 v44, v44, v2\n"
"v_mfma_f32_16x16x32_bf16 v[12:15], a[144:147], v[80:83], v[28:31]\n"
"v_exp_f32 v7, v7\n"
"v_mfma_f32_16x16x32_bf16 v[12:15], a[148:151], v[84:87], v[12:15]\n"
"s_add_u32 m0, s53, 40960\n"
"ds_read_b64_tr_b16 a[228:229], v245 offset:24576\n"
"v_cvt_pk_bf16_f32 v19, v6, v7\n"
"v_mfma_f32_16x16x32_bf16 v[12:15], a[152:155], v[88:91], v[12:15]\n"
"buffer_load_dwordx4 v236, s[24:27], s21 offen offset:1024 lds\n"
"v_mov_b32 v255, 0x10000000\n"
"v_mfma_f32_16x16x32_bf16 v[12:15], a[156:159], v[92:95], v[12:15]\n"
"s_waitcnt lgkmcnt(11)\n"
"v_exp_f32 v8, v8\n"
"v_mfma_f32_16x16x32_bf16 a[0:3], a[192:195], v[16:19], a[0:3]\n"
"ds_read_b64_tr_b16 a[230:231], v245 offset:28672\n"
"v_add_f32 v44, v44, v3\n"
"v_mfma_f32_16x16x32_bf16 a[4:7], a[196:199], v[16:19], a[4:7]\n"
"v_exp_f32 v9, v9\n"
"v_mfma_f32_16x16x32_bf16 a[8:11], a[200:203], v[16:19], a[8:11]\n"
"ds_read_b64_tr_b16 a[232:233], v244 offset:24832\n"
"v_cvt_pk_bf16_f32 v20, v8, v9\n"
"v_mfma_f32_16x16x32_bf16 a[12:15], a[204:207], v[16:19], a[12:15]\n"
"v_exp_f32 v10, v10\n"
"v_mfma_f32_16x16x32_bf16 a[16:19], a[208:211], v[16:19], a[16:19]\n"
"ds_read_b64_tr_b16 a[234:235], v244 offset:28928\n"
"v_add_f32 v44, v44, v4\n"
"v_mfma_f32_16x16x32_bf16 a[20:23], a[212:215], v[16:19], a[20:23]\n"
"s_add_u32 m0, s53, 40960\n"
"v_exp_f32 v11, v11\n"
"v_cmp_gt_u32 vcc, 144, v253\n"
"v_mfma_f32_16x16x32_bf16 a[24:27], a[216:219], v[16:19], a[24:27]\n"
"buffer_load_dwordx4 v237, s[24:27], s21 offen offset:2048 lds\n"
"ds_read_b64_tr_b16 a[236:237], v245 offset:24832\n"
"v_cvt_pk_bf16_f32 v21, v10, v11\n"
"v_mfma_f32_16x16x32_bf16 a[28:31], a[220:223], v[16:19], a[28:31]\n"
"v_exp_f32 v12, v12\n"
"v_mfma_f32_16x16x32_bf16 v[220:223], a[128:131], v[96:99], v[32:35]\n"
"ds_read_b64_tr_b16 a[238:239], v245 offset:28928\n"
"v_add_f32 v44, v44, v5\n"
"v_mfma_f32_16x16x32_bf16 v[220:223], a[132:135], v[100:103], v[220:223]\n"
"v_exp_f32 v13, v13\n"
"v_mfma_f32_16x16x32_bf16 v[220:223], a[136:139], v[104:107], v[220:223]\n"
"ds_read_b64_tr_b16 a[240:241], v244 offset:25088\n"
"v_cvt_pk_bf16_f32 v22, v12, v13\n"
"v_mfma_f32_16x16x32_bf16 v[220:223], a[140:143], v[108:111], v[220:223]\n"
"v_exp_f32 v14, v14\n"
"v_mfma_f32_16x16x32_bf16 v[224:227], a[144:147], v[96:99], v[32:35]\n"
"ds_read_b64_tr_b16 a[242:243], v244 offset:29184\n"
"v_add_f32 v44, v44, v6\n"
"v_lshlrev_b32 v254, 3, v253\n"
"v_mfma_f32_16x16x32_bf16 v[224:227], a[148:151], v[100:103], v[224:227]\n"
"s_nop 0\n"
"v_exp_f32 v15, v15\n"
"v_mfma_f32_16x16x32_bf16 v[224:227], a[152:155], v[104:107], v[224:227]\n"
"ds_read_b64_tr_b16 a[244:245], v245 offset:25088\n"
"v_cvt_pk_bf16_f32 v23, v14, v15\n"
"v_mfma_f32_16x16x32_bf16 v[224:227], a[156:159], v[108:111], v[224:227]\n"
"s_add_u32 m0, s53, 40960\n"
"v_exp_f32 v220, v220\n"
"v_mfma_f32_16x16x32_bf16 a[32:35], a[192:195], v[20:23], a[32:35]\n"
"buffer_load_dwordx4 v238, s[24:27], s21 offen offset:3072 lds\n"
"v_cndmask_b32 v254, v255, v254, vcc\n"
"v_mfma_f32_16x16x32_bf16 a[36:39], a[196:199], v[20:23], a[36:39]\n"
"v_exp_f32 v221, v221\n"
"v_mfma_f32_16x16x32_bf16 a[40:43], a[200:203], v[20:23], a[40:43]\n"
"ds_read_b64_tr_b16 a[246:247], v245 offset:29184\n"
"v_cvt_pk_bf16_f32 v16, v220, v221\n"
"v_mfma_f32_16x16x32_bf16 a[44:47], a[204:207], v[20:23], a[44:47]\n"
"v_exp_f32 v222, v222\n"
"v_mfma_f32_16x16x32_bf16 a[48:51], a[208:211], v[20:23], a[48:51]\n"
"ds_read_b64_tr_b16 a[248:249], v244 offset:25344\n"
"v_add_f32 v44, v44, v7\n"
"v_mfma_f32_16x16x32_bf16 a[52:55], a[212:215], v[20:23], a[52:55]\n"
"v_exp_f32 v223, v223\n"
"v_mfma_f32_16x16x32_bf16 a[56:59], a[216:219], v[20:23], a[56:59]\n"
"ds_read_b64_tr_b16 a[250:251], v244 offset:29440\n"
"v_cvt_pk_bf16_f32 v17, v222, v223\n"
"v_mfma_f32_16x16x32_bf16 a[60:63], a[220:223], v[20:23], a[60:63]\n"
"v_exp_f32 v224, v224\n"
"v_lshlrev_b32 v216, 6, v248\n"
"v_mfma_f32_16x16x32_bf16 v[48:51], v[40:43], v[20:23], v[48:51]\n"
"ds_read_b64_tr_b16 a[252:253], v245 offset:25344\n"
"v_add_f32 v52, v52, v220\n"
"v_mfma_f32_16x16x32_bf16 v[228:231], a[128:131], v[112:115], v[36:39]\n"
"v_exp_f32 v225, v225\n"
"v_mfma_f32_16x16x32_bf16 v[228:231], a[132:135], v[116:119], v[228:231]\n"
"ds_read_b64_tr_b16 a[254:255], v245 offset:29440\n"
"v_cvt_pk_bf16_f32 v18, v224, v225\n"
"v_mfma_f32_16x16x32_bf16 v[228:231], a[136:139], v[120:123], v[228:231]\n"
"v_exp_f32 v226, v226\n"
"v_mfma_f32_16x16x32_bf16 v[228:231], a[140:143], v[124:127], v[228:231]\n"
"v_add_f32 v52, v52, v221\n"
"v_add_f32 v52, v52, v222\n"
"v_mfma_f32_16x16x32_bf16 v[232:235], a[144:147], v[112:115], v[36:39]\n"
"v_exp_f32 v227, v227\n"
"v_mfma_f32_16x16x32_bf16 v[232:235], a[148:151], v[116:119], v[232:235]\n"
"s_add_u32 m0, s53, 45056\n"
"v_cvt_pk_bf16_f32 v19, v226, v227\n"
"v_add_f32 v52, v52, v223\n"
"v_lshrrev_b32 v215, 3, v248\n"
"v_mfma_f32_16x16x32_bf16 v[232:235], a[152:155], v[120:123], v[232:235]\n"
"buffer_load_dwordx4 v239, s[24:27], s21 offen offset:0 lds\n"
"v_mul_u32_u24 v215, 1152, v215\n"
"v_mfma_f32_16x16x32_bf16 v[232:235], a[156:159], v[124:127], v[232:235]\n"
"v_exp_f32 v228, v228\n"
"v_mfma_f32_16x16x32_bf16 a[64:67], a[192:195], v[16:19], a[64:67]\n"
"v_add_f32 v52, v52, v224\n"
"v_add_f32 v52, v52, v225\n"
"v_mfma_f32_16x16x32_bf16 a[68:71], a[196:199], v[16:19], a[68:71]\n"
"v_exp_f32 v229, v229\n"
"v_mfma_f32_16x16x32_bf16 a[72:75], a[200:203], v[16:19], a[72:75]\n"
"v_cvt_pk_bf16_f32 v20, v228, v229\n"
"v_add_f32 v52, v52, v226\n"
"v_mfma_f32_16x16x32_bf16 a[76:79], a[204:207], v[16:19], a[76:79]\n"
"s_waitcnt vmcnt(26) lgkmcnt(0)\n"
"v_exp_f32 v230, v230\n"
"v_mfma_f32_16x16x32_bf16 a[80:83], a[208:211], v[16:19], a[80:83]\n"
"s_barrier\n"
"v_mfma_f32_16x16x32_bf16 a[84:87], a[212:215], v[16:19], a[84:87]\n"
"v_exp_f32 v231, v231\n"
"v_mfma_f32_16x16x32_bf16 a[88:91], a[216:219], v[16:19], a[88:91]\n"
"v_cvt_pk_bf16_f32 v21, v230, v231\n"
"v_add_f32 v52, v52, v227\n"
"v_and_b32 v217, 7, v248\n"
"v_mfma_f32_16x16x32_bf16 a[92:95], a[220:223], v[16:19], a[92:95]\n"
"v_exp_f32 v232, v232\n"
"v_mfma_f32_16x16x32_bf16 v[0:3], a[160:163], v[64:67], v[24:27]\n"
"ds_read_b128 a[128:131], v241 offset:32768\n"
"v_lshl_add_u32 v215, v217, 7, v215\n"
"v_mfma_f32_16x16x32_bf16 v[0:3], a[164:167], v[68:71], v[0:3]\n"
"s_and_b32 s46, s44, 7\n"
"v_exp_f32 v233, v233\n"
"v_mfma_f32_16x16x32_bf16 v[0:3], a[168:171], v[72:75], v[0:3]\n"
"s_lshl_b32 s46, s46, 2\n"
"ds_read_b128 a[132:135], v241 offset:33024\n"
"v_cvt_pk_bf16_f32 v22, v232, v233\n"
"v_mfma_f32_16x16x32_bf16 v[0:3], a[172:175], v[76:79], v[0:3]\n"
"s_add_u32 s46, s46, s41\n"
"v_exp_f32 v234, v234\n"
"v_mfma_f32_16x16x32_bf16 v[4:7], a[176:179], v[64:67], v[24:27]\n"
"s_mul_i32 s46, s46, 9216\n"
"ds_read_b128 a[136:139], v241 offset:33280\n"
"v_mfma_f32_16x16x32_bf16 v[4:7], a[180:183], v[68:71], v[4:7]\n"
"s_mul_i32 s62, s43, 294912\n"
"v_exp_f32 v235, v235\n"
"v_mfma_f32_16x16x32_bf16 v[4:7], a[184:187], v[72:75], v[4:7]\n"
"s_add_u32 s46, s46, s62\n"
"ds_read_b128 a[140:143], v241 offset:33536\n"
"v_cvt_pk_bf16_f32 v23, v234, v235\n"
"v_mfma_f32_16x16x32_bf16 v[4:7], a[188:191], v[76:79], v[4:7]\n"
"s_add_u32 m0, s53, 73728\n"
"v_exp_f32 v0, v0\n"
"v_mfma_f32_16x16x32_bf16 a[96:99], a[192:195], v[20:23], a[96:99]\n"
"buffer_load_dwordx4 v210, s[24:27], s22 offen offset:0 lds\n"
"v_mfma_f32_16x16x32_bf16 a[100:103], a[196:199], v[20:23], a[100:103]\n"
"v_exp_f32 v1, v1\n"
"v_mfma_f32_16x16x32_bf16 a[104:107], a[200:203], v[20:23], a[104:107]\n"
"ds_read_b128 a[144:147], v241 offset:36864\n"
"buffer_load_dword v250, v215, s[24:27], s46 offen offset:128\n"
"v_cvt_pk_bf16_f32 v16, v0, v1\n"
"v_mfma_f32_16x16x32_bf16 a[108:111], a[204:207], v[20:23], a[108:111]\n"
"v_exp_f32 v2, v2\n"
"v_mfma_f32_16x16x32_bf16 a[112:115], a[208:211], v[20:23], a[112:115]\n"
"ds_read_b128 a[148:151], v241 offset:37120\n"
"v_add_f32 v44, v44, v0\n"
"v_mfma_f32_16x16x32_bf16 a[116:119], a[212:215], v[20:23], a[116:119]\n"
"v_exp_f32 v3, v3\n"
"v_mfma_f32_16x16x32_bf16 a[120:123], a[216:219], v[20:23], a[120:123]\n"
"ds_read_b128 a[152:155], v241 offset:37376\n"
"v_cvt_pk_bf16_f32 v17, v2, v3\n"
"v_mfma_f32_16x16x32_bf16 a[124:127], a[220:223], v[20:23], a[124:127]\n"
"v_exp_f32 v4, v4\n"
"v_mfma_f32_16x16x32_bf16 v[56:59], v[40:43], v[20:23], v[56:59]\n"
"ds_read_b128 a[156:159], v241 offset:37632\n"
"v_add_f32 v44, v44, v1\n"
"v_mfma_f32_16x16x32_bf16 v[8:11], a[160:163], v[80:83], v[28:31]\n"
"v_exp_f32 v5, v5\n"
"v_mfma_f32_16x16x32_bf16 v[8:11], a[164:167], v[84:87], v[8:11]\n"
"ds_read_b64_tr_b16 a[192:193], v244 offset:49152\n"
"v_cvt_pk_bf16_f32 v18, v4, v5\n"
"v_mfma_f32_16x16x32_bf16 v[8:11], a[168:171], v[88:91], v[8:11]\n"
"v_exp_f32 v6, v6\n"
"v_mfma_f32_16x16x32_bf16 v[8:11], a[172:175], v[92:95], v[8:11]\n"
"ds_read_b64_tr_b16 a[194:195], v244 offset:53248\n"
"v_add_f32 v44, v44, v2\n"
"v_mfma_f32_16x16x32_bf16 v[12:15], a[176:179], v[80:83], v[28:31]\n"
"v_exp_f32 v7, v7\n"
"v_mfma_f32_16x16x32_bf16 v[12:15], a[180:183], v[84:87], v[12:15]\n"
"s_add_u32 m0, s53, 73728\n"
"ds_read_b64_tr_b16 a[196:197], v245 offset:49152\n"
"v_cvt_pk_bf16_f32 v19, v6, v7\n"
"v_mfma_f32_16x16x32_bf16 v[12:15], a[184:187], v[88:91], v[12:15]\n"
"buffer_load_dwordx4 v211, s[24:27], s22 offen offset:1024 lds\n"
"v_mfma_f32_16x16x32_bf16 v[12:15], a[188:191], v[92:95], v[12:15]\n"
"s_waitcnt lgkmcnt(11)\n"
"v_exp_f32 v8, v8\n"
"v_mfma_f32_16x16x32_bf16 a[0:3], a[224:227], v[16:19], a[0:3]\n"
"ds_read_b64_tr_b16 a[198:199], v245 offset:53248\n"
"v_add_f32 v44, v44, v3\n"
"v_mfma_f32_16x16x32_bf16 a[4:7], a[228:231], v[16:19], a[4:7]\n"
"v_exp_f32 v9, v9\n"
"v_mfma_f32_16x16x32_bf16 a[8:11], a[232:235], v[16:19], a[8:11]\n"
"ds_read_b64_tr_b16 a[200:201], v244 offset:49408\n"
"v_cvt_pk_bf16_f32 v20, v8, v9\n"
"v_mfma_f32_16x16x32_bf16 a[12:15], a[236:239], v[16:19], a[12:15]\n"
"v_exp_f32 v10, v10\n"
"v_mfma_f32_16x16x32_bf16 a[16:19], a[240:243], v[16:19], a[16:19]\n"
"ds_read_b64_tr_b16 a[202:203], v244 offset:53504\n"
"v_add_f32 v44, v44, v4\n"
"v_mfma_f32_16x16x32_bf16 a[20:23], a[244:247], v[16:19], a[20:23]\n"
"v_exp_f32 v11, v11\n"
"v_mfma_f32_16x16x32_bf16 a[24:27], a[248:251], v[16:19], a[24:27]\n"
"ds_read_b64_tr_b16 a[204:205], v245 offset:49408\n"
"v_cvt_pk_bf16_f32 v21, v10, v11\n"
"v_mfma_f32_16x16x32_bf16 a[28:31], a[252:255], v[16:19], a[28:31]\n"
"v_exp_f32 v12, v12\n"
"v_mfma_f32_16x16x32_bf16 v[220:223], a[160:163], v[96:99], v[32:35]\n"
"ds_read_b64_tr_b16 a[206:207], v245 offset:53504\n"
"v_add_f32 v44, v44, v5\n"
"v_mfma_f32_16x16x32_bf16 v[220:223], a[164:167], v[100:103], v[220:223]\n"
"v_exp_f32 v13, v13\n"
"v_mfma_f32_16x16x32_bf16 v[220:223], a[168:171], v[104:107], v[220:223]\n"
"ds_read_b64_tr_b16 a[208:209], v244 offset:49664\n"
"v_cvt_pk_bf16_f32 v22, v12, v13\n"
"v_mfma_f32_16x16x32_bf16 v[220:223], a[172:175], v[108:111], v[220:223]\n"
"v_exp_f32 v14, v14\n"
"v_mfma_f32_16x16x32_bf16 v[224:227], a[176:179], v[96:99], v[32:35]\n"
"ds_read_b64_tr_b16 a[210:211], v244 offset:53760\n"
"v_add_f32 v44, v44, v6\n"
"v_mfma_f32_16x16x32_bf16 v[224:227], a[180:183], v[100:103], v[224:227]\n"
"v_exp_f32 v15, v15\n"
"v_mfma_f32_16x16x32_bf16 v[224:227], a[184:187], v[104:107], v[224:227]\n"
"ds_read_b64_tr_b16 a[212:213], v245 offset:49664\n"
"buffer_load_dword v250, v215, s[24:27], s46 offen offset:192\n"
"v_cvt_pk_bf16_f32 v23, v14, v15\n"
"v_mfma_f32_16x16x32_bf16 v[224:227], a[188:191], v[108:111], v[224:227]\n"
"s_add_u32 m0, s53, 73728\n"
"v_exp_f32 v220, v220\n"
"v_mfma_f32_16x16x32_bf16 a[32:35], a[224:227], v[20:23], a[32:35]\n"
"buffer_load_dwordx4 v212, s[24:27], s22 offen offset:2048 lds\n"
"v_mfma_f32_16x16x32_bf16 a[36:39], a[228:231], v[20:23], a[36:39]\n"
"v_exp_f32 v221, v221\n"
"v_mfma_f32_16x16x32_bf16 a[40:43], a[232:235], v[20:23], a[40:43]\n"
"ds_read_b64_tr_b16 a[214:215], v245 offset:53760\n"
"v_cvt_pk_bf16_f32 v16, v220, v221\n"
"v_mfma_f32_16x16x32_bf16 a[44:47], a[236:239], v[20:23], a[44:47]\n"
"v_exp_f32 v222, v222\n"
"v_mfma_f32_16x16x32_bf16 a[48:51], a[240:243], v[20:23], a[48:51]\n"
"ds_read_b64_tr_b16 a[216:217], v244 offset:49920\n"
"v_add_f32 v44, v44, v7\n"
"v_mfma_f32_16x16x32_bf16 a[52:55], a[244:247], v[20:23], a[52:55]\n"
"v_exp_f32 v223, v223\n"
"v_mfma_f32_16x16x32_bf16 a[56:59], a[248:251], v[20:23], a[56:59]\n"
"ds_read_b64_tr_b16 a[218:219], v244 offset:54016\n"
"v_cvt_pk_bf16_f32 v17, v222, v223\n"
"v_mfma_f32_16x16x32_bf16 a[60:63], a[252:255], v[20:23], a[60:63]\n"
"v_exp_f32 v224, v224\n"
"v_mfma_f32_16x16x32_bf16 v[48:51], v[40:43], v[20:23], v[48:51]\n"
"ds_read_b64_tr_b16 a[220:221], v245 offset:49920\n"
"v_add_f32 v52, v52, v220\n"
"v_mfma_f32_16x16x32_bf16 v[228:231], a[160:163], v[112:115], v[36:39]\n"
"v_exp_f32 v225, v225\n"
"v_mfma_f32_16x16x32_bf16 v[228:231], a[164:167], v[116:119], v[228:231]\n"
"ds_read_b64_tr_b16 a[222:223], v245 offset:54016\n"
"v_cvt_pk_bf16_f32 v18, v224, v225\n"
"v_mfma_f32_16x16x32_bf16 v[228:231], a[168:171], v[120:123], v[228:231]\n"
"v_exp_f32 v226, v226\n"
"v_mfma_f32_16x16x32_bf16 v[228:231], a[172:175], v[124:127], v[228:231]\n"
"v_add_f32 v52, v52, v221\n"
"v_add_f32 v52, v52, v222\n"
"v_mfma_f32_16x16x32_bf16 v[232:235], a[176:179], v[112:115], v[36:39]\n"
"v_exp_f32 v227, v227\n"
"v_mfma_f32_16x16x32_bf16 v[232:235], a[180:183], v[116:119], v[232:235]\n"
"s_add_u32 m0, s53, 73728\n"
"v_cvt_pk_bf16_f32 v19, v226, v227\n"
"v_add_f32 v52, v52, v223\n"
"v_mfma_f32_16x16x32_bf16 v[232:235], a[184:187], v[120:123], v[232:235]\n"
"buffer_load_dwordx4 v213, s[24:27], s22 offen offset:3072 lds\n"
"v_mfma_f32_16x16x32_bf16 v[232:235], a[188:191], v[124:127], v[232:235]\n"
"v_exp_f32 v228, v228\n"
"v_mfma_f32_16x16x32_bf16 a[64:67], a[224:227], v[16:19], a[64:67]\n"
"v_add_f32 v52, v52, v224\n"
"v_add_f32 v52, v52, v225\n"
"v_mfma_f32_16x16x32_bf16 a[68:71], a[228:231], v[16:19], a[68:71]\n"
"v_exp_f32 v229, v229\n"
"v_mfma_f32_16x16x32_bf16 a[72:75], a[232:235], v[16:19], a[72:75]\n"
"v_cvt_pk_bf16_f32 v20, v228, v229\n"
"v_add_f32 v52, v52, v226\n"
"v_mfma_f32_16x16x32_bf16 a[76:79], a[236:239], v[16:19], a[76:79]\n"
"v_exp_f32 v230, v230\n"
"v_mfma_f32_16x16x32_bf16 a[80:83], a[240:243], v[16:19], a[80:83]\n"
"s_waitcnt lgkmcnt(8)\n"
"v_mfma_f32_16x16x32_bf16 a[84:87], a[244:247], v[16:19], a[84:87]\n"
"v_exp_f32 v231, v231\n"
"v_mfma_f32_16x16x32_bf16 a[88:91], a[248:251], v[16:19], a[88:91]\n"
"v_cvt_pk_bf16_f32 v21, v230, v231\n"
"v_add_f32 v52, v52, v227\n"
"v_mfma_f32_16x16x32_bf16 a[92:95], a[252:255], v[16:19], a[92:95]\n"
"v_exp_f32 v232, v232\n"
"v_mfma_f32_16x16x32_bf16 v[0:3], a[128:131], v[64:67], v[24:27]\n"
"ds_read_b128 a[160:163], v241 offset:40960\n"
"v_mfma_f32_16x16x32_bf16 v[0:3], a[132:135], v[68:71], v[0:3]\n"
"v_exp_f32 v233, v233\n"
"v_mfma_f32_16x16x32_bf16 v[0:3], a[136:139], v[72:75], v[0:3]\n"
"ds_read_b128 a[164:167], v241 offset:41216\n"
"v_cvt_pk_bf16_f32 v22, v232, v233\n"
"v_mfma_f32_16x16x32_bf16 v[0:3], a[140:143], v[76:79], v[0:3]\n"
"v_exp_f32 v234, v234\n"
"v_mfma_f32_16x16x32_bf16 v[4:7], a[144:147], v[64:67], v[24:27]\n"
"ds_read_b128 a[168:171], v241 offset:41472\n"
"v_mfma_f32_16x16x32_bf16 v[4:7], a[148:151], v[68:71], v[4:7]\n"
"v_exp_f32 v235, v235\n"
"v_mfma_f32_16x16x32_bf16 v[4:7], a[152:155], v[72:75], v[4:7]\n"
"ds_read_b128 a[172:175], v241 offset:41728\n"
"v_cvt_pk_bf16_f32 v23, v234, v235\n"
"v_mfma_f32_16x16x32_bf16 v[4:7], a[156:159], v[76:79], v[4:7]\n"
"s_add_u32 m0, s53, 77824\n"
"v_exp_f32 v0, v0\n"
"v_mfma_f32_16x16x32_bf16 a[96:99], a[224:227], v[20:23], a[96:99]\n"
"buffer_load_dwordx4 v214, s[24:27], s22 offen offset:0 lds\n"
"v_mfma_f32_16x16x32_bf16 a[100:103], a[228:231], v[20:23], a[100:103]\n"
"v_exp_f32 v1, v1\n"
"v_mfma_f32_16x16x32_bf16 a[104:107], a[232:235], v[20:23], a[104:107]\n"
"ds_read_b128 a[176:179], v241 offset:45056\n"
"v_cvt_pk_bf16_f32 v16, v0, v1\n"
"v_mfma_f32_16x16x32_bf16 a[108:111], a[236:239], v[20:23], a[108:111]\n"
"v_exp_f32 v2, v2\n"
"v_mfma_f32_16x16x32_bf16 a[112:115], a[240:243], v[20:23], a[112:115]\n"
"ds_read_b128 a[180:183], v241 offset:45312\n"
"v_add_f32 v44, v44, v0\n"
"v_mfma_f32_16x16x32_bf16 a[116:119], a[244:247], v[20:23], a[116:119]\n"
"v_exp_f32 v3, v3\n"
"v_mfma_f32_16x16x32_bf16 a[120:123], a[248:251], v[20:23], a[120:123]\n"
"ds_read_b128 a[184:187], v241 offset:45568\n"
"v_cvt_pk_bf16_f32 v17, v2, v3\n"
"v_mfma_f32_16x16x32_bf16 a[124:127], a[252:255], v[20:23], a[124:127]\n"
"v_exp_f32 v4, v4\n"
"v_mfma_f32_16x16x32_bf16 v[56:59], v[40:43], v[20:23], v[56:59]\n"
"ds_read_b128 a[188:191], v241 offset:45824\n"
"v_add_f32 v44, v44, v1\n"
"v_mfma_f32_16x16x32_bf16 v[8:11], a[128:131], v[80:83], v[28:31]\n"
"v_exp_f32 v5, v5\n"
"v_mfma_f32_16x16x32_bf16 v[8:11], a[132:135], v[84:87], v[8:11]\n"
"ds_read_b64_tr_b16 a[224:225], v244 offset:57344\n"
"v_cvt_pk_bf16_f32 v18, v4, v5\n"
"v_mfma_f32_16x16x32_bf16 v[8:11], a[136:139], v[88:91], v[8:11]\n"
"v_exp_f32 v6, v6\n"
"v_mfma_f32_16x16x32_bf16 v[8:11], a[140:143], v[92:95], v[8:11]\n"
"ds_read_b64_tr_b16 a[226:227], v244 offset:61440\n"
"v_add_f32 v44, v44, v2\n"
"v_mfma_f32_16x16x32_bf16 v[12:15], a[144:147], v[80:83], v[28:31]\n"
"v_exp_f32 v7, v7\n"
"v_mfma_f32_16x16x32_bf16 v[12:15], a[148:151], v[84:87], v[12:15]\n"
"s_add_u32 m0, s53, 77824\n"
"ds_read_b64_tr_b16 a[228:229], v245 offset:57344\n"
"v_cvt_pk_bf16_f32 v19, v6, v7\n"
"v_mfma_f32_16x16x32_bf16 v[12:15], a[152:155], v[88:91], v[12:15]\n"
"buffer_load_dwordx4 v236, s[24:27], s22 offen offset:1024 lds\n"
"v_mfma_f32_16x16x32_bf16 v[12:15], a[156:159], v[92:95], v[12:15]\n"
"s_waitcnt lgkmcnt(11)\n"
"v_exp_f32 v8, v8\n"
"v_mfma_f32_16x16x32_bf16 a[0:3], a[192:195], v[16:19], a[0:3]\n"
"ds_read_b64_tr_b16 a[230:231], v245 offset:61440\n"
"v_add_f32 v44, v44, v3\n"
"v_mfma_f32_16x16x32_bf16 a[4:7], a[196:199], v[16:19], a[4:7]\n"
"v_exp_f32 v9, v9\n"
"v_mfma_f32_16x16x32_bf16 a[8:11], a[200:203], v[16:19], a[8:11]\n"
"ds_read_b64_tr_b16 a[232:233], v244 offset:57600\n"
"v_cvt_pk_bf16_f32 v20, v8, v9\n"
"v_mfma_f32_16x16x32_bf16 a[12:15], a[204:207], v[16:19], a[12:15]\n"
"v_exp_f32 v10, v10\n"
"v_mfma_f32_16x16x32_bf16 a[16:19], a[208:211], v[16:19], a[16:19]\n"
"ds_read_b64_tr_b16 a[234:235], v244 offset:61696\n"
"v_add_f32 v44, v44, v4\n"
"v_mfma_f32_16x16x32_bf16 a[20:23], a[212:215], v[16:19], a[20:23]\n"
"s_add_u32 m0, s53, 77824\n"
"v_exp_f32 v11, v11\n"
"v_mfma_f32_16x16x32_bf16 a[24:27], a[216:219], v[16:19], a[24:27]\n"
"buffer_load_dwordx4 v237, s[24:27], s22 offen offset:2048 lds\n"
"ds_read_b64_tr_b16 a[236:237], v245 offset:57600\n"
"v_cvt_pk_bf16_f32 v21, v10, v11\n"
"v_mfma_f32_16x16x32_bf16 a[28:31], a[220:223], v[16:19], a[28:31]\n"
"v_exp_f32 v12, v12\n"
"v_mfma_f32_16x16x32_bf16 v[220:223], a[128:131], v[96:99], v[32:35]\n"
"ds_read_b64_tr_b16 a[238:239], v245 offset:61696\n"
"v_add_f32 v44, v44, v5\n"
"v_mfma_f32_16x16x32_bf16 v[220:223], a[132:135], v[100:103], v[220:223]\n"
"v_exp_f32 v13, v13\n"
"v_mfma_f32_16x16x32_bf16 v[220:223], a[136:139], v[104:107], v[220:223]\n"
"ds_read_b64_tr_b16 a[240:241], v244 offset:57856\n"
"v_cvt_pk_bf16_f32 v22, v12, v13\n"
"v_mfma_f32_16x16x32_bf16 v[220:223], a[140:143], v[108:111], v[220:223]\n"
"v_exp_f32 v14, v14\n"
"v_mfma_f32_16x16x32_bf16 v[224:227], a[144:147], v[96:99], v[32:35]\n"
"ds_read_b64_tr_b16 a[242:243], v244 offset:61952\n"
"v_add_f32 v44, v44, v6\n"
"v_mfma_f32_16x16x32_bf16 v[224:227], a[148:151], v[100:103], v[224:227]\n"
"v_exp_f32 v15, v15\n"
"v_mfma_f32_16x16x32_bf16 v[224:227], a[152:155], v[104:107], v[224:227]\n"
"ds_read_b64_tr_b16 a[244:245], v245 offset:57856\n"
"v_cvt_pk_bf16_f32 v23, v14, v15\n"
"v_mfma_f32_16x16x32_bf16 v[224:227], a[156:159], v[108:111], v[224:227]\n"
"s_add_u32 m0, s53, 77824\n"
"v_exp_f32 v220, v220\n"
"v_mfma_f32_16x16x32_bf16 a[32:35], a[192:195], v[20:23], a[32:35]\n"
"buffer_load_dwordx4 v238, s[24:27], s22 offen offset:3072 lds\n"
"v_mfma_f32_16x16x32_bf16 a[36:39], a[196:199], v[20:23], a[36:39]\n"
"v_exp_f32 v221, v221\n"
"v_mfma_f32_16x16x32_bf16 a[40:43], a[200:203], v[20:23], a[40:43]\n"
"ds_read_b64_tr_b16 a[246:247], v245 offset:61952\n"
"v_cvt_pk_bf16_f32 v16, v220, v221\n"
"v_mfma_f32_16x16x32_bf16 a[44:47], a[204:207], v[20:23], a[44:47]\n"
"v_exp_f32 v222, v222\n"
"v_mfma_f32_16x16x32_bf16 a[48:51], a[208:211], v[20:23], a[48:51]\n"
"ds_read_b64_tr_b16 a[248:249], v244 offset:58112\n"
"v_add_f32 v44, v44, v7\n"
"v_mfma_f32_16x16x32_bf16 a[52:55], a[212:215], v[20:23], a[52:55]\n"
"v_exp_f32 v223, v223\n"
"v_mfma_f32_16x16x32_bf16 a[56:59], a[216:219], v[20:23], a[56:59]\n"
"ds_read_b64_tr_b16 a[250:251], v244 offset:62208\n"
"v_cvt_pk_bf16_f32 v17, v222, v223\n"
"v_mfma_f32_16x16x32_bf16 a[60:63], a[220:223], v[20:23], a[60:63]\n"
"v_exp_f32 v224, v224\n"
"v_mfma_f32_16x16x32_bf16 v[48:51], v[40:43], v[20:23], v[48:51]\n"
"ds_read_b64_tr_b16 a[252:253], v245 offset:58112\n"
"v_add_f32 v52, v52, v220\n"
"v_mfma_f32_16x16x32_bf16 v[228:231], a[128:131], v[112:115], v[36:39]\n"
"v_exp_f32 v225, v225\n"
"v_mfma_f32_16x16x32_bf16 v[228:231], a[132:135], v[116:119], v[228:231]\n"
"ds_read_b64_tr_b16 a[254:255], v245 offset:62208\n"
"v_cvt_pk_bf16_f32 v18, v224, v225\n"
"v_mfma_f32_16x16x32_bf16 v[228:231], a[136:139], v[120:123], v[228:231]\n"
"v_exp_f32 v226, v226\n"
"v_mfma_f32_16x16x32_bf16 v[228:231], a[140:143], v[124:127], v[228:231]\n"
"v_add_f32 v52, v52, v221\n"
"v_add_f32 v52, v52, v222\n"
"v_mfma_f32_16x16x32_bf16 v[232:235], a[144:147], v[112:115], v[36:39]\n"
"v_exp_f32 v227, v227\n"
"v_mfma_f32_16x16x32_bf16 v[232:235], a[148:151], v[116:119], v[232:235]\n"
"s_add_u32 m0, s53, 81920\n"
"v_cvt_pk_bf16_f32 v19, v226, v227\n"
"v_add_f32 v52, v52, v223\n"
"v_mfma_f32_16x16x32_bf16 v[232:235], a[152:155], v[120:123], v[232:235]\n"
"buffer_load_dwordx4 v239, s[24:27], s22 offen offset:0 lds\n"
"v_mfma_f32_16x16x32_bf16 v[232:235], a[156:159], v[124:127], v[232:235]\n"
"v_exp_f32 v228, v228\n"
"v_mfma_f32_16x16x32_bf16 a[64:67], a[192:195], v[16:19], a[64:67]\n"
"v_add_f32 v52, v52, v224\n"
"v_add_f32 v52, v52, v225\n"
"v_mfma_f32_16x16x32_bf16 a[68:71], a[196:199], v[16:19], a[68:71]\n"
"v_exp_f32 v229, v229\n"
"v_mfma_f32_16x16x32_bf16 a[72:75], a[200:203], v[16:19], a[72:75]\n"
"v_cvt_pk_bf16_f32 v20, v228, v229\n"
"v_add_f32 v52, v52, v226\n"
"v_mfma_f32_16x16x32_bf16 a[76:79], a[204:207], v[16:19], a[76:79]\n"
"s_waitcnt vmcnt(23) lgkmcnt(0)\n"
"v_exp_f32 v230, v230\n"
"v_mfma_f32_16x16x32_bf16 a[80:83], a[208:211], v[16:19], a[80:83]\n"
"s_barrier\n"
"v_mfma_f32_16x16x32_bf16 a[84:87], a[212:215], v[16:19], a[84:87]\n"
"v_exp_f32 v231, v231\n"
"v_mfma_f32_16x16x32_bf16 a[88:91], a[216:219], v[16:19], a[88:91]\n"
"v_cvt_pk_bf16_f32 v21, v230, v231\n"
"v_add_f32 v52, v52, v227\n"
"v_mfma_f32_16x16x32_bf16 a[92:95], a[220:223], v[16:19], a[92:95]\n"
"v_exp_f32 v232, v232\n"
"v_mfma_f32_16x16x32_bf16 v[0:3], a[160:163], v[64:67], v[24:27]\n"
"ds_read_b64_tr_b16 a[128:129], v202 offset:0\n"
"v_mfma_f32_16x16x32_bf16 v[0:3], a[164:167], v[68:71], v[0:3]\n"
"s_add_u32 m0, s53, 110592\n"
"v_exp_f32 v233, v233\n"
"v_mfma_f32_16x16x32_bf16 v[0:3], a[168:171], v[72:75], v[0:3]\n"
"buffer_load_dwordx4 v210, s[24:27], s23 offen offset:0 lds\n"
"ds_read_b64_tr_b16 a[130:131], v202 offset:4608\n"
"v_cvt_pk_bf16_f32 v22, v232, v233\n"
"v_mfma_f32_16x16x32_bf16 v[0:3], a[172:175], v[76:79], v[0:3]\n"
"v_exp_f32 v234, v234\n"
"v_mfma_f32_16x16x32_bf16 v[4:7], a[176:179], v[64:67], v[24:27]\n"
"ds_read_b64_tr_b16 a[132:133], v202 offset:32\n"
"v_mfma_f32_16x16x32_bf16 v[4:7], a[180:183], v[68:71], v[4:7]\n"
"s_add_u32 m0, s53, 110592\n"
"v_exp_f32 v235, v235\n"
"v_mfma_f32_16x16x32_bf16 v[4:7], a[184:187], v[72:75], v[4:7]\n"
"buffer_load_dwordx4 v211, s[24:27], s23 offen offset:1024 lds\n"
"ds_read_b64_tr_b16 a[134:135], v202 offset:4640\n"
"v_cvt_pk_bf16_f32 v23, v234, v235\n"
"v_mfma_f32_16x16x32_bf16 v[4:7], a[188:191], v[76:79], v[4:7]\n"
"v_exp_f32 v0, v0\n"
"v_mfma_f32_16x16x32_bf16 a[96:99], a[192:195], v[20:23], a[96:99]\n"
"s_nop 0\n"
"v_mfma_f32_16x16x32_bf16 a[100:103], a[196:199], v[20:23], a[100:103]\n"
"s_add_u32 m0, s53, 110592\n"
"v_exp_f32 v1, v1\n"
"v_mfma_f32_16x16x32_bf16 a[104:107], a[200:203], v[20:23], a[104:107]\n"
"buffer_load_dwordx4 v212, s[24:27], s23 offen offset:2048 lds\n"
"ds_read_b64_tr_b16 a[136:137], v202 offset:64\n"
"v_cvt_pk_bf16_f32 v16, v0, v1\n"
"v_mfma_f32_16x16x32_bf16 a[108:111], a[204:207], v[20:23], a[108:111]\n"
"v_exp_f32 v2, v2\n"
"v_mfma_f32_16x16x32_bf16 a[112:115], a[208:211], v[20:23], a[112:115]\n"
"ds_read_b64_tr_b16 a[138:139], v202 offset:4672\n"
"v_add_f32 v44, v44, v0\n"
"v_mfma_f32_16x16x32_bf16 a[116:119], a[212:215], v[20:23], a[116:119]\n"
"s_add_u32 m0, s53, 110592\n"
"v_exp_f32 v3, v3\n"
"v_mfma_f32_16x16x32_bf16 a[120:123], a[216:219], v[20:23], a[120:123]\n"
"buffer_load_dwordx4 v213, s[24:27], s23 offen offset:3072 lds\n"
"ds_read_b64_tr_b16 a[140:141], v202 offset:96\n"
"v_cvt_pk_bf16_f32 v17, v2, v3\n"
"v_mfma_f32_16x16x32_bf16 a[124:127], a[220:223], v[20:23], a[124:127]\n"
"v_exp_f32 v4, v4\n"
"v_mfma_f32_16x16x32_bf16 v[56:59], v[40:43], v[20:23], v[56:59]\n"
"ds_read_b64_tr_b16 a[142:143], v202 offset:4704\n"
"v_add_f32 v44, v44, v1\n"
"v_mfma_f32_16x16x32_bf16 v[8:11], a[160:163], v[80:83], v[28:31]\n"
"s_add_u32 m0, s53, 114688\n"
"v_exp_f32 v5, v5\n"
"v_mfma_f32_16x16x32_bf16 v[8:11], a[164:167], v[84:87], v[8:11]\n"
"buffer_load_dwordx4 v214, s[24:27], s23 offen offset:0 lds\n"
"ds_read_b64_tr_b16 a[144:145], v202 offset:128\n"
"v_cvt_pk_bf16_f32 v18, v4, v5\n"
"v_mfma_f32_16x16x32_bf16 v[8:11], a[168:171], v[88:91], v[8:11]\n"
"v_exp_f32 v6, v6\n"
"v_mfma_f32_16x16x32_bf16 v[8:11], a[172:175], v[92:95], v[8:11]\n"
"ds_read_b64_tr_b16 a[146:147], v202 offset:4736\n"
"v_add_f32 v44, v44, v2\n"
"v_mfma_f32_16x16x32_bf16 v[12:15], a[176:179], v[80:83], v[28:31]\n"
"s_add_u32 m0, s53, 114688\n"
"v_exp_f32 v7, v7\n"
"v_mfma_f32_16x16x32_bf16 v[12:15], a[180:183], v[84:87], v[12:15]\n"
"buffer_load_dwordx4 v236, s[24:27], s23 offen offset:1024 lds\n"
"ds_read_b64_tr_b16 a[148:149], v202 offset:160\n"
"v_cvt_pk_bf16_f32 v19, v6, v7\n"
"v_mfma_f32_16x16x32_bf16 v[12:15], a[184:187], v[88:91], v[12:15]\n"
"s_nop 0\n"
"v_mfma_f32_16x16x32_bf16 v[12:15], a[188:191], v[92:95], v[12:15]\n"
"s_add_u32 m0, s53, 114688\n"
"v_exp_f32 v8, v8\n"
"v_mfma_f32_16x16x32_bf16 a[0:3], a[224:227], v[16:19], a[0:3]\n"
"buffer_load_dwordx4 v237, s[24:27], s23 offen offset:2048 lds\n"
"ds_read_b64_tr_b16 a[150:151], v202 offset:4768\n"
"v_add_f32 v44, v44, v3\n"
"v_mfma_f32_16x16x32_bf16 a[4:7], a[228:231], v[16:19], a[4:7]\n"
"v_exp_f32 v9, v9\n"
"v_mfma_f32_16x16x32_bf16 a[8:11], a[232:235], v[16:19], a[8:11]\n"
"ds_read_b64_tr_b16 a[152:153], v202 offset:192\n"
"v_cvt_pk_bf16_f32 v20, v8, v9\n"
"v_mfma_f32_16x16x32_bf16 a[12:15], a[236:239], v[16:19], a[12:15]\n"
"s_add_u32 m0, s53, 114688\n"
"v_exp_f32 v10, v10\n"
"v_mfma_f32_16x16x32_bf16 a[16:19], a[240:243], v[16:19], a[16:19]\n"
"buffer_load_dwordx4 v238, s[24:27], s23 offen offset:3072 lds\n"
"ds_read_b64_tr_b16 a[154:155], v202 offset:4800\n"
"v_add_f32 v44, v44, v4\n"
"v_mfma_f32_16x16x32_bf16 a[20:23], a[244:247], v[16:19], a[20:23]\n"
"v_exp_f32 v11, v11\n"
"v_mfma_f32_16x16x32_bf16 a[24:27], a[248:251], v[16:19], a[24:27]\n"
"ds_read_b64_tr_b16 a[156:157], v202 offset:224\n"
"v_cvt_pk_bf16_f32 v21, v10, v11\n"
"v_mfma_f32_16x16x32_bf16 a[28:31], a[252:255], v[16:19], a[28:31]\n"
"s_add_u32 m0, s53, 118784\n"
"v_exp_f32 v12, v12\n"
"v_mfma_f32_16x16x32_bf16 v[220:223], a[160:163], v[96:99], v[32:35]\n"
"buffer_load_dwordx4 v239, s[24:27], s23 offen offset:0 lds\n"
"ds_read_b64_tr_b16 a[158:159], v202 offset:4832\n"
"v_add_f32 v44, v44, v5\n"
"v_mfma_f32_16x16x32_bf16 v[220:223], a[164:167], v[100:103], v[220:223]\n"
"v_exp_f32 v13, v13\n"
"v_mfma_f32_16x16x32_bf16 v[220:223], a[168:171], v[104:107], v[220:223]\n"
"ds_read_b64_tr_b16 a[192:193], v205 offset:0\n"
"v_cvt_pk_bf16_f32 v22, v12, v13\n"
"v_mfma_f32_16x16x32_bf16 v[220:223], a[172:175], v[108:111], v[220:223]\n"
"v_exp_f32 v14, v14\n"
"v_mfma_f32_16x16x32_bf16 v[224:227], a[176:179], v[96:99], v[32:35]\n"
"ds_read_b64_tr_b16 a[194:195], v205 offset:4608\n"
"v_add_f32 v44, v44, v6\n"
"v_mfma_f32_16x16x32_bf16 v[224:227], a[180:183], v[100:103], v[224:227]\n"
"v_exp_f32 v15, v15\n"
"v_mfma_f32_16x16x32_bf16 v[224:227], a[184:187], v[104:107], v[224:227]\n"
"ds_read_b64_tr_b16 a[196:197], v202 offset:9216\n"
"v_cvt_pk_bf16_f32 v23, v14, v15\n"
"v_mfma_f32_16x16x32_bf16 v[224:227], a[188:191], v[108:111], v[224:227]\n"
"v_exp_f32 v220, v220\n"
"v_mfma_f32_16x16x32_bf16 a[32:35], a[224:227], v[20:23], a[32:35]\n"
"AT_pg12:\n"
"s_cbranch_execz AT_hr\n"
"v_mfma_f32_16x16x32_bf16 a[36:39], a[228:231], v[20:23], a[36:39]\n"
"v_exp_f32 v221, v221\n"
"v_mfma_f32_16x16x32_bf16 a[40:43], a[232:235], v[20:23], a[40:43]\n"
"ds_read_b64_tr_b16 a[198:199], v202 offset:13824\n"
"v_cvt_pk_bf16_f32 v16, v220, v221\n"
"v_mfma_f32_16x16x32_bf16 a[44:47], a[236:239], v[20:23], a[44:47]\n"
"v_exp_f32 v222, v222\n"
"v_mfma_f32_16x16x32_bf16 a[48:51], a[240:243], v[20:23], a[48:51]\n"
"v_add_f32 v44, v44, v7\n"
"v_mfma_f32_16x16x32_bf16 a[52:55], a[244:247], v[20:23], a[52:55]\n"
"v_exp_f32 v223, v223\n"
"s_add_u32 s46, s52, 4096\n"
"s_add_u32 s47, s52, 8192\n"
"s_add_u32 s48, s52, 12288\n"
"v_mov_b32 v45, v44\n"
"v_mfma_f32_16x16x32_bf16 a[56:59], a[248:251], v[20:23], a[56:59]\n"
"v_cvt_pk_bf16_f32 v17, v222, v223\n"
"s_nop 0\n"
"s_nop 0\n"
"v_permlane16_swap_b32 v44, v45\n"
"v_add_f32 v44, v44, v45\n"
"v_mfma_f32_16x16x32_bf16 a[60:63], a[252:255], v[20:23], a[60:63]\n"
"v_exp_f32 v224, v224\n"
"v_mov_b32 v45, v44\n"
"s_nop 0\n"
"s_nop 0\n"
"v_permlane32_swap_b32 v44, v45\n"
"v_mfma_f32_16x16x32_bf16 v[48:51], v[40:43], v[20:23], v[48:51]\n"
"v_add_f32 v52, v52, v220\n"
"v_add_f32 v44, v44, v45\n"
"v_rcp_f32 v46, v44\n"
"v_accvgpr_read_b32 v216, a0\n"
"v_accvgpr_read_b32 v217, a1\n"
"v_mfma_f32_16x16x32_bf16 v[228:231], a[160:163], v[112:115], v[36:39]\n"
"v_exp_f32 v225, v225\n"
"v_accvgpr_read_b32 v218, a2\n"
"v_accvgpr_read_b32 v219, a3\n"
"v_accvgpr_read_b32 v60, a4\n"
"v_accvgpr_read_b32 v61, a5\n"
"v_mfma_f32_16x16x32_bf16 v[228:231], a[164:167], v[116:119], v[228:231]\n"
"v_cvt_pk_bf16_f32 v18, v224, v225\n"
"v_accvgpr_read_b32 v62, a6\n"
"v_accvgpr_read_b32 v63, a7\n"
"v_mul_f32 v216, v216, v46\n"
"v_mul_f32 v217, v217, v46\n"
"v_mfma_f32_16x16x32_bf16 v[228:231], a[168:171], v[120:123], v[228:231]\n"
"v_exp_f32 v226, v226\n"
"v_mul_f32 v218, v218, v46\n"
"v_mul_f32 v219, v219, v46\n"
"v_mul_f32 v60, v60, v46\n"
"v_mul_f32 v61, v61, v46\n"
"v_mfma_f32_16x16x32_bf16 v[228:231], a[172:175], v[124:127], v[228:231]\n"
"v_add_f32 v52, v52, v221\n"
"v_add_f32 v52, v52, v222\n"
"v_mul_f32 v62, v62, v46\n"
"v_mul_f32 v63, v63, v46\n"
"v_cvt_pk_bf16_f32 v0, v216, v217\n"
"v_cvt_pk_bf16_f32 v1, v218, v219\n"
"v_mfma_f32_16x16x32_bf16 v[232:235], a[176:179], v[112:115], v[36:39]\n"
"v_exp_f32 v227, v227\n"
"v_cvt_pk_bf16_f32 v2, v60, v61\n"
"v_cvt_pk_bf16_f32 v3, v62, v63\n"
"buffer_store_dwordx4 v[0:3], v207, s[28:31], s52 offen offset:0 sc1\n"
"v_accvgpr_read_b32 v216, a8\n"
"v_mfma_f32_16x16x32_bf16 v[232:235], a[180:183], v[116:119], v[232:235]\n"
"v_cvt_pk_bf16_f32 v19, v226, v227\n"
"v_add_f32 v52, v52, v223\n"
"v_accvgpr_read_b32 v217, a9\n"
"v_accvgpr_read_b32 v218, a10\n"
"v_accvgpr_read_b32 v219, a11\n"
"v_accvgpr_read_b32 v60, a12\n"
"v_mfma_f32_16x16x32_bf16 v[232:235], a[184:187], v[120:123], v[232:235]\n"
"v_accvgpr_read_b32 v61, a13\n"
"v_accvgpr_read_b32 v62, a14\n"
"v_accvgpr_read_b32 v63, a15\n"
"v_mul_f32 v216, v216, v46\n"
"v_mfma_f32_16x16x32_bf16 v[232:235], a[188:191], v[124:127], v[232:235]\n"
"v_exp_f32 v228, v228\n"
"v_mul_f32 v217, v217, v46\n"
"v_mul_f32 v218, v218, v46\n"
"v_mul_f32 v219, v219, v46\n"
"v_mul_f32 v60, v60, v46\n"
"v_mfma_f32_16x16x32_bf16 a[64:67], a[224:227], v[16:19], a[64:67]\n"
"v_add_f32 v52, v52, v224\n"
"v_add_f32 v52, v52, v225\n"
"v_mul_f32 v61, v61, v46\n"
"v_mul_f32 v62, v62, v46\n"
"v_mul_f32 v63, v63, v46\n"
"v_cvt_pk_bf16_f32 v4, v216, v217\n"
"v_mfma_f32_16x16x32_bf16 a[68:71], a[228:231], v[16:19], a[68:71]\n"
"v_exp_f32 v229, v229\n"
"v_cvt_pk_bf16_f32 v5, v218, v219\n"
"v_cvt_pk_bf16_f32 v6, v60, v61\n"
"v_cvt_pk_bf16_f32 v7, v62, v63\n"
"buffer_store_dwordx4 v[4:7], v207, s[28:31], s52 offen offset:2048 sc1\n"
"v_mfma_f32_16x16x32_bf16 a[72:75], a[232:235], v[16:19], a[72:75]\n"
"v_cvt_pk_bf16_f32 v20, v228, v229\n"
"v_add_f32 v52, v52, v226\n"
"v_accvgpr_read_b32 v216, a16\n"
"v_accvgpr_read_b32 v217, a17\n"
"v_accvgpr_read_b32 v218, a18\n"
"v_accvgpr_read_b32 v219, a19\n"
"v_mfma_f32_16x16x32_bf16 a[76:79], a[236:239], v[16:19], a[76:79]\n"
"v_exp_f32 v230, v230\n"
"v_accvgpr_read_b32 v60, a20\n"
"v_accvgpr_read_b32 v61, a21\n"
"v_accvgpr_read_b32 v62, a22\n"
"v_accvgpr_read_b32 v63, a23\n"
"v_mfma_f32_16x16x32_bf16 a[80:83], a[240:243], v[16:19], a[80:83]\n"
"v_mul_f32 v216, v216, v46\n"
"v_mul_f32 v217, v217, v46\n"
"v_mul_f32 v218, v218, v46\n"
"v_mul_f32 v219, v219, v46\n"
"v_mfma_f32_16x16x32_bf16 a[84:87], a[244:247], v[16:19], a[84:87]\n"
"v_exp_f32 v231, v231\n"
"v_mul_f32 v60, v60, v46\n"
"v_mul_f32 v61, v61, v46\n"
"v_mul_f32 v62, v62, v46\n"
"v_mul_f32 v63, v63, v46\n"
"v_mfma_f32_16x16x32_bf16 a[88:91], a[248:251], v[16:19], a[88:91]\n"
"v_cvt_pk_bf16_f32 v21, v230, v231\n"
"v_add_f32 v52, v52, v227\n"
"v_cvt_pk_bf16_f32 v8, v216, v217\n"
"v_cvt_pk_bf16_f32 v9, v218, v219\n"
"v_cvt_pk_bf16_f32 v10, v60, v61\n"
"v_cvt_pk_bf16_f32 v11, v62, v63\n"
"v_mfma_f32_16x16x32_bf16 a[92:95], a[252:255], v[16:19], a[92:95]\n"
"v_exp_f32 v232, v232\n"
"buffer_store_dwordx4 v[8:11], v207, s[28:31], s46 offen offset:0 sc1\n"
"v_accvgpr_read_b32 v216, a24\n"
"v_accvgpr_read_b32 v217, a25\n"
"v_accvgpr_read_b32 v218, a26\n"
"v_exp_f32 v233, v233\n"
"v_accvgpr_read_b32 v219, a27\n"
"v_cvt_pk_bf16_f32 v22, v232, v233\n"
"v_accvgpr_read_b32 v60, a28\n"
"v_exp_f32 v234, v234\n"
"v_accvgpr_read_b32 v61, a29\n"
"v_exp_f32 v235, v235\n"
"v_accvgpr_read_b32 v62, a30\n"
"v_cvt_pk_bf16_f32 v23, v234, v235\n"
"v_accvgpr_read_b32 v63, a31\n"
"v_mul_f32 v216, v216, v46\n"
"v_mul_f32 v217, v217, v46\n"
"v_mfma_f32_16x16x32_bf16 a[96:99], a[224:227], v[20:23], a[96:99]\n"
"v_mul_f32 v218, v218, v46\n"
"v_mul_f32 v219, v219, v46\n"
"v_mfma_f32_16x16x32_bf16 a[100:103], a[228:231], v[20:23], a[100:103]\n"
"v_mul_f32 v60, v60, v46\n"
"v_mul_f32 v61, v61, v46\n"
"v_mfma_f32_16x16x32_bf16 a[104:107], a[232:235], v[20:23], a[104:107]\n"
"v_mul_f32 v62, v62, v46\n"
"v_mul_f32 v63, v63, v46\n"
"v_mfma_f32_16x16x32_bf16 a[108:111], a[236:239], v[20:23], a[108:111]\n"
"v_cvt_pk_bf16_f32 v12, v216, v217\n"
"v_cvt_pk_bf16_f32 v13, v218, v219\n"
"v_mfma_f32_16x16x32_bf16 a[112:115], a[240:243], v[20:23], a[112:115]\n"
"v_cvt_pk_bf16_f32 v14, v60, v61\n"
"v_cvt_pk_bf16_f32 v15, v62, v63\n"
"v_mfma_f32_16x16x32_bf16 a[116:119], a[244:247], v[20:23], a[116:119]\n"
"buffer_store_dwordx4 v[12:15], v207, s[28:31], s46 offen offset:2048 sc1\n"
"v_rcp_f32 v48, v49\n"
"v_mfma_f32_16x16x32_bf16 a[120:123], a[248:251], v[20:23], a[120:123]\n"
"v_accvgpr_read_b32 v216, a48\n"
"v_accvgpr_read_b32 v217, a49\n"
"v_mfma_f32_16x16x32_bf16 a[124:127], a[252:255], v[20:23], a[124:127]\n"
"v_accvgpr_read_b32 v218, a50\n"
"v_accvgpr_read_b32 v219, a51\n"
"v_mfma_f32_16x16x32_bf16 v[56:59], v[40:43], v[20:23], v[56:59]\n"
"v_accvgpr_read_b32 v60, a52\n"
"v_accvgpr_read_b32 v61, a53\n"
"v_accvgpr_read_b32 v62, a54\n"
"v_accvgpr_read_b32 v63, a55\n"
"v_mul_f32 v216, v216, v48\n"
"v_mul_f32 v217, v217, v48\n"
"v_mul_f32 v218, v218, v48\n"
"v_mul_f32 v219, v219, v48\n"
"v_mul_f32 v60, v60, v48\n"
"v_mul_f32 v61, v61, v48\n"
"v_mul_f32 v62, v62, v48\n"
"v_mul_f32 v63, v63, v48\n"
"v_cvt_pk_bf16_f32 v24, v216, v217\n"
"v_cvt_pk_bf16_f32 v25, v218, v219\n"
"v_cvt_pk_bf16_f32 v26, v60, v61\n"
"v_cvt_pk_bf16_f32 v27, v62, v63\n"
"buffer_store_dwordx4 v[24:27], v207, s[28:31], s48 offen offset:0 sc1\n"
"v_accvgpr_read_b32 v216, a56\n"
"v_accvgpr_read_b32 v217, a57\n"
"v_accvgpr_read_b32 v218, a58\n"
"v_accvgpr_read_b32 v219, a59\n"
"v_accvgpr_read_b32 v60, a60\n"
"v_accvgpr_read_b32 v61, a61\n"
"v_accvgpr_read_b32 v62, a62\n"
"v_accvgpr_read_b32 v63, a63\n"
"v_mul_f32 v216, v216, v48\n"
"v_mul_f32 v217, v217, v48\n"
"v_mul_f32 v218, v218, v48\n"
"v_mul_f32 v219, v219, v48\n"
"v_mul_f32 v60, v60, v48\n"
"v_mul_f32 v61, v61, v48\n"
"v_mul_f32 v62, v62, v48\n"
"v_mul_f32 v63, v63, v48\n"
"v_cvt_pk_bf16_f32 v28, v216, v217\n"
"v_cvt_pk_bf16_f32 v29, v218, v219\n"
"v_cvt_pk_bf16_f32 v30, v60, v61\n"
"v_cvt_pk_bf16_f32 v31, v62, v63\n"
"buffer_store_dwordx4 v[28:31], v207, s[28:31], s48 offen offset:2048 sc1\n"
"v_mov_b32 v226, v52\n"
"s_nop 0\n"
"s_nop 0\n"
"v_permlane16_swap_b32 v52, v226\n"
"v_add_f32 v52, v52, v226\n"
"v_mov_b32 v226, v52\n"
"s_nop 0\n"
"s_nop 0\n"
"v_permlane32_swap_b32 v52, v226\n"
"v_add_f32 v52, v52, v226\n"
"v_mov_b32 v219, 0x5f800000\n"
"s_nop 7\n"
"v_cmp_nle_f32 s[56:57], v44, v219\n"
"v_cmp_nle_f32 s[58:59], v49, v219\n"
"v_cmp_nle_f32 s[60:61], v52, v219\n"
"v_cmp_nle_f32 s[62:63], v56, v219\n"
"v_rcp_f32 v218, v44\n"
"v_rcp_f32 v220, v49\n"
"v_rcp_f32 v222, v52\n"
"v_rcp_f32 v224, v56\n"
"v_accvgpr_read_b32 v226, a32\n"
"v_accvgpr_read_b32 v227, a33\n"
"v_accvgpr_read_b32 v228, a34\n"
"v_accvgpr_read_b32 v229, a35\n"
"v_accvgpr_read_b32 v230, a36\n"
"v_accvgpr_read_b32 v231, a37\n"
"v_accvgpr_read_b32 v232, a38\n"
"v_accvgpr_read_b32 v233, a39\n"
"v_pk_mul_f32 v[226:227], v[226:227], v[220:221] op_sel_hi:[1,0]\n"
"v_pk_mul_f32 v[228:229], v[228:229], v[220:221] op_sel_hi:[1,0]\n"
"v_pk_mul_f32 v[230:231], v[230:231], v[220:221] op_sel_hi:[1,0]\n"
"v_pk_mul_f32 v[232:233], v[232:233], v[220:221] op_sel_hi:[1,0]\n"
"v_cvt_pk_bf16_f32 v16, v226, v227\n"
"v_cvt_pk_bf16_f32 v17, v228, v229\n"
"v_cvt_pk_bf16_f32 v18, v230, v231\n"
"v_cvt_pk_bf16_f32 v19, v232, v233\n"
"buffer_store_dwordx4 v[16:19], v207, s[28:31], s47 offen offset:0 sc1\n"
"s_or_b64 s[56:57], s[56:57], s[58:59]\n"
"s_or_b64 s[60:61], s[60:61], s[62:63]\n"
"s_or_b64 s[56:57], s[56:57], s[60:61]\n"
"s_cselect_b32 s56, 1, 0\n"
"s_cmp_eq_u32 s45, 0\n"
"s_cselect_b32 s56, s56, 0\n"
"s_lshl_b32 s57, s41, 2\n"
"s_add_u32 s57, s57, s40\n"
"s_add_u32 s57, s57, 163776\n"
"v_mov_b32 v234, s56\n"
"v_mov_b32 v235, s57\n"
"ds_write_b32 v235, v234\n"
"v_accvgpr_read_b32 v226, a40\n"
"v_accvgpr_read_b32 v227, a41\n"
"v_accvgpr_read_b32 v228, a42\n"
"v_accvgpr_read_b32 v229, a43\n"
"v_accvgpr_read_b32 v230, a44\n"
"v_accvgpr_read_b32 v231, a45\n"
"v_accvgpr_read_b32 v232, a46\n"
"v_accvgpr_read_b32 v233, a47\n"
"v_pk_mul_f32 v[226:227], v[226:227], v[220:221] op_sel_hi:[1,0]\n"
"v_pk_mul_f32 v[228:229], v[228:229], v[220:221] op_sel_hi:[1,0]\n"
"v_pk_mul_f32 v[230:231], v[230:231], v[220:221] op_sel_hi:[1,0]\n"
"v_pk_mul_f32 v[232:233], v[232:233], v[220:221] op_sel_hi:[1,0]\n"
"v_cvt_pk_bf16_f32 v20, v226, v227\n"
"v_cvt_pk_bf16_f32 v21, v228, v229\n"
"v_cvt_pk_bf16_f32 v22, v230, v231\n"
"v_cvt_pk_bf16_f32 v23, v232, v233\n"
"buffer_store_dwordx4 v[20:23], v207, s[28:31], s47 offen offset:2048 sc1\n"
"s_and_b32 s60, s55, 4294967292\n"
"s_mov_b32 s23, 9576448\n"
"s_lshl_b32 s62, s60, 6\n"
"v_and_b32 v215, 3, v248\n"
"v_lshlrev_b32 v215, 6, v215\n"
"v_add_u32 v215, s62, v215\n"
"v_min_u32 v251, 1, v253\n"
"v_lshlrev_b32 v251, 28, v251\n"
"s_lshl_b32 s62, s55, 6\n"
"v_add_u32 v251, s62, v251\n"
"v_mov_b32 v252, s85\n"
"v_accvgpr_read_b32 v226, a64\n"
"v_accvgpr_read_b32 v227, a65\n"
"v_accvgpr_read_b32 v228, a66\n"
"v_accvgpr_read_b32 v229, a67\n"
"v_accvgpr_read_b32 v230, a68\n"
"v_accvgpr_read_b32 v231, a69\n"
"v_accvgpr_read_b32 v232, a70\n"
"v_accvgpr_read_b32 v233, a71\n"
"v_pk_mul_f32 v[226:227], v[226:227], v[222:223] op_sel_hi:[1,0]\n"
"v_pk_mul_f32 v[228:229], v[228:229], v[222:223] op_sel_hi:[1,0]\n"
"v_pk_mul_f32 v[230:231], v[230:231], v[222:223] op_sel_hi:[1,0]\n"
"v_pk_mul_f32 v[232:233], v[232:233], v[222:223] op_sel_hi:[1,0]\n"
"v_cvt_pk_bf16_f32 v32, v226, v227\n"
"v_cvt_pk_bf16_f32 v33, v228, v229\n"
"v_cvt_pk_bf16_f32 v34, v230, v231\n"
"v_cvt_pk_bf16_f32 v35, v232, v233\n"
"buffer_store_dwordx4 v[32:35], v207, s[28:31], s52 offen offset:1024 sc1\n"
"s_add_u32 s61, s43, 1\n"
"s_and_b32 s61, s61, 3\n"
"s_add_u32 s61, s61, s60\n"
"s_lshl_b32 s61, s61, 2\n"
"s_add_u32 s61, s61, s41\n"
"s_lshl_b32 s61, s61, 14\n"
"s_add_u32 s20, s61, 9592832\n"
"v_accvgpr_read_b32 v226, a72\n"
"v_accvgpr_read_b32 v227, a73\n"
"v_accvgpr_read_b32 v228, a74\n"
"v_accvgpr_read_b32 v229, a75\n"
"v_accvgpr_read_b32 v230, a76\n"
"v_accvgpr_read_b32 v231, a77\n"
"v_accvgpr_read_b32 v232, a78\n"
"v_accvgpr_read_b32 v233, a79\n"
"v_pk_mul_f32 v[226:227], v[226:227], v[222:223] op_sel_hi:[1,0]\n"
"v_pk_mul_f32 v[228:229], v[228:229], v[222:223] op_sel_hi:[1,0]\n"
"v_pk_mul_f32 v[230:231], v[230:231], v[222:223] op_sel_hi:[1,0]\n"
"v_pk_mul_f32 v[232:233], v[232:233], v[222:223] op_sel_hi:[1,0]\n"
"v_cvt_pk_bf16_f32 v36, v226, v227\n"
"v_cvt_pk_bf16_f32 v37, v228, v229\n"
"v_cvt_pk_bf16_f32 v38, v230, v231\n"
"v_cvt_pk_bf16_f32 v39, v232, v233\n"
"buffer_store_dwordx4 v[36:39], v207, s[28:31], s52 offen offset:3072 sc1\n"
"s_add_u32 s61, s43, 2\n"
"s_and_b32 s61, s61, 3\n"
"s_add_u32 s61, s61, s60\n"
"s_lshl_b32 s61, s61, 2\n"
"s_add_u32 s61, s61, s41\n"
"s_lshl_b32 s61, s61, 14\n"
"s_add_u32 s21, s61, 9592832\n"
"v_accvgpr_read_b32 v226, a80\n"
"v_accvgpr_read_b32 v227, a81\n"
"v_accvgpr_read_b32 v228, a82\n"
"v_accvgpr_read_b32 v229, a83\n"
"v_accvgpr_read_b32 v230, a84\n"
"v_accvgpr_read_b32 v231, a85\n"
"v_accvgpr_read_b32 v232, a86\n"
"v_accvgpr_read_b32 v233, a87\n"
"v_pk_mul_f32 v[226:227], v[226:227], v[222:223] op_sel_hi:[1,0]\n"
"v_pk_mul_f32 v[228:229], v[228:229], v[222:223] op_sel_hi:[1,0]\n"
"v_pk_mul_f32 v[230:231], v[230:231], v[222:223] op_sel_hi:[1,0]\n"
"v_pk_mul_f32 v[232:233], v[232:233], v[222:223] op_sel_hi:[1,0]\n"
"v_cvt_pk_bf16_f32 v40, v226, v227\n"
"v_cvt_pk_bf16_f32 v41, v228, v229\n"
"v_cvt_pk_bf16_f32 v42, v230, v231\n"
"v_cvt_pk_bf16_f32 v43, v232, v233\n"
"buffer_store_dwordx4 v[40:43], v207, s[28:31], s46 offen offset:1024 sc1\n"
"s_add_u32 s61, s43, 3\n"
"s_and_b32 s61, s61, 3\n"
"s_add_u32 s61, s61, s60\n"
"s_lshl_b32 s61, s61, 2\n"
"s_add_u32 s61, s61, s41\n"
"s_lshl_b32 s61, s61, 14\n"
"s_add_u32 s22, s61, 9592832\n"
"v_accvgpr_read_b32 v226, a88\n"
"v_accvgpr_read_b32 v227, a89\n"
"v_accvgpr_read_b32 v228, a90\n"
"v_accvgpr_read_b32 v229, a91\n"
"v_accvgpr_read_b32 v230, a92\n"
"v_accvgpr_read_b32 v231, a93\n"
"v_accvgpr_read_b32 v232, a94\n"
"v_accvgpr_read_b32 v233, a95\n"
"v_pk_mul_f32 v[226:227], v[226:227], v[222:223] op_sel_hi:[1,0]\n"
"v_pk_mul_f32 v[228:229], v[228:229], v[222:223] op_sel_hi:[1,0]\n"
"v_pk_mul_f32 v[230:231], v[230:231], v[222:223] op_sel_hi:[1,0]\n"
"v_pk_mul_f32 v[232:233], v[232:233], v[222:223] op_sel_hi:[1,0]\n"
"v_cvt_pk_bf16_f32 v44, v226, v227\n"
"v_cvt_pk_bf16_f32 v45, v228, v229\n"
"v_cvt_pk_bf16_f32 v46, v230, v231\n"
"v_cvt_pk_bf16_f32 v47, v232, v233\n"
"buffer_store_dwordx4 v[44:47], v207, s[28:31], s46 offen offset:3072 sc1\n"
"s_waitcnt vmcnt(32) lgkmcnt(0)\n"
"s_barrier\n"
"ds_read_b32 v234, v209\n"
"v_accvgpr_read_b32 v226, a96\n"
"v_accvgpr_read_b32 v227, a97\n"
"v_accvgpr_read_b32 v228, a98\n"
"v_accvgpr_read_b32 v229, a99\n"
"v_accvgpr_read_b32 v230, a100\n"
"v_accvgpr_read_b32 v231, a101\n"
"v_accvgpr_read_b32 v232, a102\n"
"v_accvgpr_read_b32 v233, a103\n"
"v_pk_mul_f32 v[226:227], v[226:227], v[224:225] op_sel_hi:[1,0]\n"
"v_pk_mul_f32 v[228:229], v[228:229], v[224:225] op_sel_hi:[1,0]\n"
"v_pk_mul_f32 v[230:231], v[230:231], v[224:225] op_sel_hi:[1,0]\n"
"v_pk_mul_f32 v[232:233], v[232:233], v[224:225] op_sel_hi:[1,0]\n"
"v_cvt_pk_bf16_f32 v48, v226, v227\n"
"v_cvt_pk_bf16_f32 v49, v228, v229\n"
"v_cvt_pk_bf16_f32 v50, v230, v231\n"
"v_cvt_pk_bf16_f32 v51, v232, v233\n"
"buffer_store_dwordx4 v[48:51], v207, s[28:31], s47 offen offset:1024 sc1\n"
"v_accvgpr_read_b32 v226, a104\n"
"v_accvgpr_read_b32 v227, a105\n"
"v_accvgpr_read_b32 v228, a106\n"
"v_accvgpr_read_b32 v229, a107\n"
"v_accvgpr_read_b32 v230, a108\n"
"v_accvgpr_read_b32 v231, a109\n"
"v_accvgpr_read_b32 v232, a110\n"
"v_accvgpr_read_b32 v233, a111\n"
"v_pk_mul_f32 v[226:227], v[226:227], v[224:225] op_sel_hi:[1,0]\n"
"v_pk_mul_f32 v[228:229], v[228:229], v[224:225] op_sel_hi:[1,0]\n"
"v_pk_mul_f32 v[230:231], v[230:231], v[224:225] op_sel_hi:[1,0]\n"
"v_pk_mul_f32 v[232:233], v[232:233], v[224:225] op_sel_hi:[1,0]\n"
"v_cvt_pk_bf16_f32 v52, v226, v227\n"
"v_cvt_pk_bf16_f32 v53, v228, v229\n"
"v_cvt_pk_bf16_f32 v54, v230, v231\n"
"v_cvt_pk_bf16_f32 v55, v232, v233\n"
"buffer_store_dwordx4 v[52:55], v207, s[28:31], s47 offen offset:3072 sc1\n"
"v_accvgpr_read_b32 v226, a112\n"
"v_accvgpr_read_b32 v227, a113\n"
"v_accvgpr_read_b32 v228, a114\n"
"v_accvgpr_read_b32 v229, a115\n"
"v_accvgpr_read_b32 v230, a116\n"
"v_accvgpr_read_b32 v231, a117\n"
"v_accvgpr_read_b32 v232, a118\n"
"v_accvgpr_read_b32 v233, a119\n"
"v_pk_mul_f32 v[226:227], v[226:227], v[224:225] op_sel_hi:[1,0]\n"
"v_pk_mul_f32 v[228:229], v[228:229], v[224:225] op_sel_hi:[1,0]\n"
"v_pk_mul_f32 v[230:231], v[230:231], v[224:225] op_sel_hi:[1,0]\n"
"v_pk_mul_f32 v[232:233], v[232:233], v[224:225] op_sel_hi:[1,0]\n"
"v_cvt_pk_bf16_f32 v56, v226, v227\n"
"v_cvt_pk_bf16_f32 v57, v228, v229\n"
"v_cvt_pk_bf16_f32 v58, v230, v231\n"
"v_cvt_pk_bf16_f32 v59, v232, v233\n"
"buffer_store_dwordx4 v[56:59], v207, s[28:31], s48 offen offset:1024 sc1\n"
"v_accvgpr_read_b32 v226, a120\n"
"v_accvgpr_read_b32 v227, a121\n"
"v_accvgpr_read_b32 v228, a122\n"
"v_accvgpr_read_b32 v229, a123\n"
"v_accvgpr_read_b32 v230, a124\n"
"v_accvgpr_read_b32 v231, a125\n"
"v_accvgpr_read_b32 v232, a126\n"
"v_accvgpr_read_b32 v233, a127\n"
"v_pk_mul_f32 v[226:227], v[226:227], v[224:225] op_sel_hi:[1,0]\n"
"v_pk_mul_f32 v[228:229], v[228:229], v[224:225] op_sel_hi:[1,0]\n"
"v_pk_mul_f32 v[230:231], v[230:231], v[224:225] op_sel_hi:[1,0]\n"
"v_pk_mul_f32 v[232:233], v[232:233], v[224:225] op_sel_hi:[1,0]\n"
"v_cvt_pk_bf16_f32 v60, v226, v227\n"
"v_cvt_pk_bf16_f32 v61, v228, v229\n"
"v_cvt_pk_bf16_f32 v62, v230, v231\n"
"v_cvt_pk_bf16_f32 v63, v232, v233\n"
"buffer_store_dwordx4 v[60:63], v207, s[28:31], s48 offen offset:3072 sc1\n"
"s_waitcnt lgkmcnt(0)\n"
"v_cmp_ne_u32 vcc, 0, v234\n"
"s_nop 0\n"
"s_cbranch_vccnz AT_fb\n"
"v_mfma_f32_16x16x32_bf16 v[64:67], a[128:131], v[0:3], 0\n"
"ds_read_b64_tr_b16 a[200:201], v202 offset:9248\n"
"v_mfma_f32_16x16x32_bf16 v[68:71], a[128:131], v[32:35], 0\n"
"ds_read_b64_tr_b16 a[202:203], v202 offset:13856\n"
"v_mfma_f32_16x16x32_bf16 v[72:75], a[132:135], v[0:3], 0\n"
"s_mov_b32 s56, 41984\n"
"ds_read_b64_tr_b16 a[204:205], v202 offset:9280\n"
"v_mfma_f32_16x16x32_bf16 v[76:79], a[132:135], v[32:35], 0\n"
"s_mov_b32 s57, 46592\n"
"ds_read_b64_tr_b16 a[206:207], v202 offset:13888\n"
"v_mfma_f32_16x16x32_bf16 v[80:83], a[136:139], v[0:3], 0\n"
"s_mov_b32 s58, 51200\n"
"ds_read_b64_tr_b16 a[208:209], v202 offset:9312\n"
"v_mfma_f32_16x16x32_bf16 v[84:87], a[136:139], v[32:35], 0\n"
"s_mov_b32 s59, 55808\n"
"ds_read_b64_tr_b16 a[210:211], v202 offset:13920\n"
"v_mfma_f32_16x16x32_bf16 v[88:91], a[140:143], v[0:3], 0\n"
"ds_read_b64_tr_b16 a[212:213], v202 offset:9344\n"
"v_lshlrev_b32 v246, 2, v254\n"
"v_mfma_f32_16x16x32_bf16 v[92:95], a[140:143], v[32:35], 0\n"
"s_lshl_b32 s54, s44, 2\n"
"ds_read_b64_tr_b16 a[214:215], v202 offset:13952\n"
"v_mfma_f32_16x16x32_bf16 v[96:99], a[144:147], v[0:3], 0\n"
"s_add_u32 s54, s54, s41\n"
"ds_read_b64_tr_b16 a[216:217], v202 offset:9376\n"
"v_mfma_f32_16x16x32_bf16 v[100:103], a[144:147], v[32:35], 0\n"
"s_mul_i32 s54, s54, 36864\n"
"ds_read_b64_tr_b16 a[218:219], v202 offset:13984\n"
"v_mfma_f32_16x16x32_bf16 v[104:107], a[148:151], v[0:3], 0\n"
"s_lshr_b32 s62, s43, 2\n"
"ds_read_b64_tr_b16 a[220:221], v202 offset:9408\n"
"v_mfma_f32_16x16x32_bf16 v[108:111], a[148:151], v[32:35], 0\n"
"s_mul_i32 s62, s62, 2359296\n"
"ds_read_b64_tr_b16 a[222:223], v202 offset:14016\n"
"v_mfma_f32_16x16x32_bf16 v[112:115], a[152:155], v[0:3], 0\n"
"s_add_u32 s54, s54, s62\n"
"ds_read_b64_tr_b16 a[128:129], v202 offset:9440\n"
"v_mfma_f32_16x16x32_bf16 v[116:119], a[152:155], v[32:35], 0\n"
"s_add_u32 s54, s54, 139264\n"
"ds_read_b64_tr_b16 a[130:131], v202 offset:14048\n"
"v_mfma_f32_16x16x32_bf16 v[120:123], a[156:159], v[0:3], 0\n"
"s_bfe_u32 s62, s43, 0x10001\n"
"ds_read_b64_tr_b16 a[132:133], v205 offset:9216\n"
"v_mfma_f32_16x16x32_bf16 v[124:127], a[156:159], v[32:35], 0\n"
"s_mul_i32 s62, s62, 9216\n"
"ds_read_b64_tr_b16 a[134:135], v205 offset:13824\n"
"v_mfma_f32_16x16x32_bf16 v[226:229], a[192:195], v[0:3], 0\n"
"s_add_u32 s54, s54, s62\n"
"ds_read_b64_tr_b16 a[136:137], v202 offset:18432\n"
"v_mfma_f32_16x16x32_bf16 v[230:233], a[192:195], v[32:35], 0\n"
"s_and_b32 s62, s43, 1\n"
"ds_read_b64_tr_b16 a[138:139], v202 offset:23040\n"
"v_mfma_f32_16x16x32_bf16 v[64:67], a[196:199], v[4:7], v[64:67]\n"
"s_lshl_b32 s49, s62, 3\n"
"ds_read_b64_tr_b16 a[140:141], v202 offset:18464\n"
"v_mfma_f32_16x16x32_bf16 v[68:71], a[196:199], v[36:39], v[68:71]\n"
"s_waitcnt lgkmcnt(11)\n"
"s_add_u32 s49, s49, s54\n"
"ds_read_b64_tr_b16 a[142:143], v202 offset:23072\n"
"v_mfma_f32_16x16x32_bf16 v[72:75], a[200:203], v[4:7], v[72:75]\n"
"s_add_u32 s49, s49, 4096\n"
"ds_read_b64_tr_b16 a[144:145], v202 offset:18496\n"
"v_mfma_f32_16x16x32_bf16 v[76:79], a[200:203], v[36:39], v[76:79]\n"
"s_mul_i32 s62, s62, 5120\n"
"ds_read_b64_tr_b16 a[146:147], v202 offset:23104\n"
"v_mfma_f32_16x16x32_bf16 v[80:83], a[204:207], v[4:7], v[80:83]\n"
"s_add_u32 s54, s54, s62\n"
"ds_read_b64_tr_b16 a[148:149], v202 offset:18528\n"
"v_mfma_f32_16x16x32_bf16 v[84:87], a[204:207], v[36:39], v[84:87]\n"
"s_mul_i32 s46, s55, 9216\n"
"ds_read_b64_tr_b16 a[150:151], v202 offset:23136\n"
"v_mfma_f32_16x16x32_bf16 v[88:91], a[208:211], v[4:7], v[88:91]\n"
"s_min_u32 s62, s41, 2\n"
"ds_read_b64_tr_b16 a[152:153], v202 offset:18560\n"
"v_mfma_f32_16x16x32_bf16 v[92:95], a[208:211], v[36:39], v[92:95]\n"
"s_mul_i32 s62, s62, 3072\n"
"ds_read_b64_tr_b16 a[154:155], v202 offset:23168\n"
"v_mfma_f32_16x16x32_bf16 v[96:99], a[212:215], v[4:7], v[96:99]\n"
"s_add_u32 s46, s46, s62\n"
"ds_read_b64_tr_b16 a[156:157], v202 offset:18592\n"
"v_mfma_f32_16x16x32_bf16 v[100:103], a[212:215], v[36:39], v[100:103]\n"
"s_waitcnt lgkmcnt(11)\n"
"s_add_u32 s45, s40, s62\n"
"ds_read_b64_tr_b16 a[158:159], v202 offset:23200\n"
"v_mfma_f32_16x16x32_bf16 v[104:107], a[216:219], v[4:7], v[104:107]\n"
"s_lshr_b32 s47, s55, 1\n"
"ds_read_b64_tr_b16 a[192:193], v202 offset:18624\n"
"v_mfma_f32_16x16x32_bf16 v[108:111], a[216:219], v[36:39], v[108:111]\n"
"s_mul_i32 s47, s47, 18432\n"
"ds_read_b64_tr_b16 a[194:195], v202 offset:23232\n"
"v_mfma_f32_16x16x32_bf16 v[112:115], a[220:223], v[4:7], v[112:115]\n"
"s_and_b32 s62, s55, 1\n"
"ds_read_b64_tr_b16 a[196:197], v202 offset:18656\n"
"v_mfma_f32_16x16x32_bf16 v[116:119], a[220:223], v[36:39], v[116:119]\n"
"s_lshl_b32 s62, s62, 7\n"
"ds_read_b64_tr_b16 a[198:199], v202 offset:23264\n"
"v_mfma_f32_16x16x32_bf16 v[120:123], a[128:131], v[4:7], v[120:123]\n"
"s_add_u32 s47, s47, s62\n"
"ds_read_b64_tr_b16 a[200:201], v205 offset:18432\n"
"v_mfma_f32_16x16x32_bf16 v[124:127], a[128:131], v[36:39], v[124:127]\n"
"s_add_u32 s47, s47, 139264\n"
"ds_read_b64_tr_b16 a[202:203], v205 offset:23040\n"
"v_mfma_f32_16x16x32_bf16 v[226:229], a[132:135], v[4:7], v[226:229]\n"
"s_mul_i32 s48, s55, 73728\n"
"ds_read_b64_tr_b16 a[204:205], v200 offset:0\n"
"v_mfma_f32_16x16x32_bf16 v[230:233], a[132:135], v[36:39], v[230:233]\n"
"s_waitcnt lgkmcnt(11)\n"
"s_add_u32 s48, s48, 37888000\n"
"ds_read_b64_tr_b16 a[206:207], v200 offset:4608\n"
"v_mfma_f32_16x16x32_bf16 v[64:67], a[136:139], v[8:11], v[64:67]\n"
"s_add_u32 s62, s40, 119808\n"
"ds_read_b64_tr_b16 a[208:209], v200 offset:32\n"
"v_mfma_f32_16x16x32_bf16 v[68:71], a[136:139], v[40:43], v[68:71]\n"
"ds_read_b64_tr_b16 a[210:211], v200 offset:4640\n"
"v_and_b32 v206, 7, v248\n"
"v_mfma_f32_16x16x32_bf16 v[72:75], a[140:143], v[8:11], v[72:75]\n"
"ds_read_b64_tr_b16 a[212:213], v200 offset:64\n"
"v_lshrrev_b32 v255, 5, v253\n"
"v_mfma_f32_16x16x32_bf16 v[76:79], a[140:143], v[40:43], v[76:79]\n"
"ds_read_b64_tr_b16 a[214:215], v200 offset:4672\n"
"v_bfe_u32 v216, v253, 3, 2\n"
"v_mfma_f32_16x16x32_bf16 v[80:83], a[144:147], v[8:11], v[80:83]\n"
"ds_read_b64_tr_b16 a[216:217], v200 offset:96\n"
"v_lshlrev_b32 v241, 10, v255\n"
"v_mfma_f32_16x16x32_bf16 v[84:87], a[144:147], v[40:43], v[84:87]\n"
"ds_read_b64_tr_b16 a[218:219], v200 offset:4704\n"
"v_lshl_add_u32 v241, v216, 8, v241\n"
"v_mfma_f32_16x16x32_bf16 v[88:91], a[148:151], v[8:11], v[88:91]\n"
"ds_read_b64_tr_b16 a[220:221], v200 offset:128\n"
"v_lshl_add_u32 v241, v206, 4, v241\n"
"v_mfma_f32_16x16x32_bf16 v[92:95], a[148:151], v[40:43], v[92:95]\n"
"s_waitcnt lgkmcnt(11)\n"
"ds_read_b64_tr_b16 a[222:223], v200 offset:4736\n"
"v_add_u32 v242, 6144, v241\n"
"v_mfma_f32_16x16x32_bf16 v[96:99], a[152:155], v[8:11], v[96:99]\n"
"ds_read_b64_tr_b16 a[128:129], v200 offset:160\n"
"v_add_u32 v243, 12288, v241\n"
"v_mfma_f32_16x16x32_bf16 v[100:103], a[152:155], v[40:43], v[100:103]\n"
"ds_read_b64_tr_b16 a[130:131], v200 offset:4768\n"
"v_lshlrev_b32 v255, 5, v255\n"
"v_mfma_f32_16x16x32_bf16 v[104:107], a[156:159], v[8:11], v[104:107]\n"
"ds_read_b64_tr_b16 a[132:133], v200 offset:192\n"
"v_lshl_add_u32 v255, v216, 2, v255\n"
"v_mfma_f32_16x16x32_bf16 v[108:111], a[156:159], v[40:43], v[108:111]\n"
"ds_read_b64_tr_b16 a[134:135], v200 offset:4800\n"
"v_lshl_add_u32 v244, v255, 2, s62\n"
"v_mfma_f32_16x16x32_bf16 v[112:115], a[192:195], v[8:11], v[112:115]\n"
"AT_pg13:\n"
"s_cbranch_execz AT_hr\n"
"ds_read_b64_tr_b16 a[136:137], v200 offset:224\n"
"v_mul_u32_u24 v206, 1152, v206\n"
"v_mfma_f32_16x16x32_bf16 v[116:119], a[192:195], v[40:43], v[116:119]\n"
"ds_read_b64_tr_b16 a[138:139], v200 offset:4832\n"
"v_lshl_add_u32 v240, v255, 1, v206\n"
"v_mfma_f32_16x16x32_bf16 v[120:123], a[196:199], v[8:11], v[120:123]\n"
"ds_read_b64_tr_b16 a[140:141], v203 offset:0\n"
"v_add_u32 v240, s40, v240\n"
"v_mfma_f32_16x16x32_bf16 v[124:127], a[196:199], v[40:43], v[124:127]\n"
"s_waitcnt lgkmcnt(11)\n"
"ds_read_b64_tr_b16 a[142:143], v203 offset:4608\n"
"v_add_u32 v245, v206, v255\n"
"v_mfma_f32_16x16x32_bf16 v[226:229], a[200:203], v[8:11], v[226:229]\n"
"ds_read_b64_tr_b16 a[144:145], v200 offset:9216\n"
"v_lshlrev_b32 v245, 3, v245\n"
"v_mfma_f32_16x16x32_bf16 v[230:233], a[200:203], v[40:43], v[230:233]\n"
"ds_read_b64_tr_b16 a[146:147], v200 offset:13824\n"
"v_lshl_add_u32 v247, v253, 4, s62\n"
"v_mfma_f32_16x16x32_bf16 v[64:67], a[204:207], v[12:15], v[64:67]\n"
"s_mul_i32 s62, s41, 7\n"
"ds_read_b64_tr_b16 a[148:149], v200 offset:9248\n"
"v_mfma_f32_16x16x32_bf16 v[68:71], a[204:207], v[44:47], v[68:71]\n"
"s_min_u32 s62, s62, 20\n"
"ds_read_b64_tr_b16 a[150:151], v200 offset:13856\n"
"v_mfma_f32_16x16x32_bf16 v[72:75], a[208:211], v[12:15], v[72:75]\n"
"s_lshl_b32 s62, s62, 10\n"
"ds_read_b64_tr_b16 a[152:153], v200 offset:9280\n"
"v_mfma_f32_16x16x32_bf16 v[76:79], a[208:211], v[44:47], v[76:79]\n"
"s_add_u32 s50, s50, s62\n"
"ds_read_b64_tr_b16 a[154:155], v200 offset:13888\n"
"v_mfma_f32_16x16x32_bf16 v[80:83], a[212:215], v[12:15], v[80:83]\n"
"s_add_u32 s84, s40, s62\n"
"ds_read_b64_tr_b16 a[156:157], v200 offset:9312\n"
"v_mfma_f32_16x16x32_bf16 v[84:87], a[212:215], v[44:47], v[84:87]\n"
"s_waitcnt lgkmcnt(11)\n"
"ds_read_b64_tr_b16 a[158:159], v200 offset:13920\n"
"v_mfma_f32_16x16x32_bf16 v[88:91], a[216:219], v[12:15], v[88:91]\n"
"ds_read_b64_tr_b16 a[192:193], v200 offset:9344\n"
"v_mfma_f32_16x16x32_bf16 v[92:95], a[216:219], v[44:47], v[92:95]\n"
"ds_read_b64_tr_b16 a[194:195], v200 offset:13952\n"
"v_mfma_f32_16x16x32_bf16 v[96:99], a[220:223], v[12:15], v[96:99]\n"
"ds_read_b64_tr_b16 a[196:197], v200 offset:9376\n"
"v_mfma_f32_16x16x32_bf16 v[100:103], a[220:223], v[44:47], v[100:103]\n"
"ds_read_b64_tr_b16 a[198:199], v200 offset:13984\n"
"v_mfma_f32_16x16x32_bf16 v[104:107], a[128:131], v[12:15], v[104:107]\n"
"ds_read_b64_tr_b16 a[200:201], v200 offset:9408\n"
"v_mfma_f32_16x16x32_bf16 v[108:111], a[128:131], v[44:47], v[108:111]\n"
"ds_read_b64_tr_b16 a[202:203], v200 offset:14016\n"
"v_mfma_f32_16x16x32_bf16 v[112:115], a[132:135], v[12:15], v[112:115]\n"
"ds_read_b64_tr_b16 a[204:205], v200 offset:9440\n"
"v_mfma_f32_16x16x32_bf16 v[116:119], a[132:135], v[44:47], v[116:119]\n"
"s_waitcnt lgkmcnt(11)\n"
"ds_read_b64_tr_b16 a[206:207], v200 offset:14048\n"
"v_mfma_f32_16x16x32_bf16 v[120:123], a[136:139], v[12:15], v[120:123]\n"
"ds_read_b64_tr_b16 a[208:209], v203 offset:9216\n"
"v_mfma_f32_16x16x32_bf16 v[124:127], a[136:139], v[44:47], v[124:127]\n"
"ds_read_b64_tr_b16 a[210:211], v203 offset:13824\n"
"v_mfma_f32_16x16x32_bf16 v[226:229], a[140:143], v[12:15], v[226:229]\n"
"ds_read_b64_tr_b16 a[212:213], v200 offset:18432\n"
"v_mfma_f32_16x16x32_bf16 v[230:233], a[140:143], v[44:47], v[230:233]\n"
"ds_read_b64_tr_b16 a[214:215], v200 offset:23040\n"
"v_mfma_f32_16x16x32_bf16 v[64:67], a[144:147], v[16:19], v[64:67]\n"
"ds_read_b64_tr_b16 a[216:217], v200 offset:18464\n"
"v_mfma_f32_16x16x32_bf16 v[68:71], a[144:147], v[48:51], v[68:71]\n"
"s_waitcnt vmcnt(0)\n"
"ds_read_b64_tr_b16 a[218:219], v200 offset:23072\n"
"v_mfma_f32_16x16x32_bf16 v[72:75], a[148:151], v[16:19], v[72:75]\n"
"s_barrier\n"
"ds_read_b64_tr_b16 a[220:221], v200 offset:18496\n"
"buffer_store_dword v252, v251, s[28:31], s23 offen sc1\n"
"v_mfma_f32_16x16x32_bf16 v[76:79], a[148:151], v[48:51], v[76:79]\n"
"s_waitcnt lgkmcnt(11)\n"
"ds_read_b64_tr_b16 a[222:223], v200 offset:23104\n"
"buffer_load_dwordx4 a[160:163], v246, s[28:31], s56 offen offset:0 sc1\n"
"v_mfma_f32_16x16x32_bf16 v[80:83], a[152:155], v[16:19], v[80:83]\n"
"s_mov_b32 m0, s51\n"
"ds_read_b64_tr_b16 a[128:129], v200 offset:18528\n"
"v_mfma_f32_16x16x32_bf16 v[84:87], a[152:155], v[48:51], v[84:87]\n"
"ds_read_b64_tr_b16 a[130:131], v200 offset:23136\n"
"buffer_load_dwordx4 v210, s[24:27], s36 offen offset:0 lds\n"
"v_mfma_f32_16x16x32_bf16 v[88:91], a[156:159], v[16:19], v[88:91]\n"
"ds_read_b64_tr_b16 a[132:133], v200 offset:18560\n"
"v_mfma_f32_16x16x32_bf16 v[92:95], a[156:159], v[48:51], v[92:95]\n"
"ds_read_b64_tr_b16 a[134:135], v200 offset:23168\n"
"v_mfma_f32_16x16x32_bf16 v[96:99], a[192:195], v[16:19], v[96:99]\n"
"ds_read_b64_tr_b16 a[136:137], v200 offset:18592\n"
"v_mfma_f32_16x16x32_bf16 v[100:103], a[192:195], v[48:51], v[100:103]\n"
"ds_read_b64_tr_b16 a[138:139], v200 offset:23200\n"
"buffer_load_dwordx4 a[164:167], v246, s[28:31], s56 offen offset:16 sc1\n"
"v_mfma_f32_16x16x32_bf16 v[104:107], a[196:199], v[16:19], v[104:107]\n"
"ds_read_b64_tr_b16 a[140:141], v200 offset:18624\n"
"v_mfma_f32_16x16x32_bf16 v[108:111], a[196:199], v[48:51], v[108:111]\n"
"s_waitcnt lgkmcnt(11)\n"
"s_mov_b32 m0, s51\n"
"ds_read_b64_tr_b16 a[142:143], v200 offset:23232\n"
"v_mfma_f32_16x16x32_bf16 v[112:115], a[200:203], v[16:19], v[112:115]\n"
"ds_read_b64_tr_b16 a[144:145], v200 offset:18656\n"
"buffer_load_dwordx4 v211, s[24:27], s36 offen offset:1024 lds\n"
"v_mfma_f32_16x16x32_bf16 v[116:119], a[200:203], v[48:51], v[116:119]\n"
"ds_read_b64_tr_b16 a[146:147], v200 offset:23264\n"
"v_mfma_f32_16x16x32_bf16 v[120:123], a[204:207], v[16:19], v[120:123]\n"
"ds_read_b64_tr_b16 a[148:149], v203 offset:18432\n"
"v_mfma_f32_16x16x32_bf16 v[124:127], a[204:207], v[48:51], v[124:127]\n"
"ds_read_b64_tr_b16 a[150:151], v203 offset:23040\n"
"buffer_load_dwordx4 a[168:171], v246, s[28:31], s57 offen offset:0 sc1\n"
"v_mfma_f32_16x16x32_bf16 v[226:229], a[208:211], v[16:19], v[226:229]\n"
"ds_read_b64_tr_b16 a[152:153], v200 offset:27648\n"
"v_mfma_f32_16x16x32_bf16 v[230:233], a[208:211], v[48:51], v[230:233]\n"
"ds_read_b64_tr_b16 a[154:155], v200 offset:32256\n"
"v_mfma_f32_16x16x32_bf16 v[64:67], a[212:215], v[20:23], v[64:67]\n"
"s_mov_b32 m0, s51\n"
"ds_read_b64_tr_b16 a[156:157], v200 offset:27680\n"
"v_mfma_f32_16x16x32_bf16 v[68:71], a[212:215], v[52:55], v[68:71]\n"
"s_waitcnt lgkmcnt(11)\n"
"ds_read_b64_tr_b16 a[158:159], v200 offset:32288\n"
"buffer_load_dwordx4 v212, s[24:27], s36 offen offset:2048 lds\n"
"v_mfma_f32_16x16x32_bf16 v[72:75], a[216:219], v[20:23], v[72:75]\n"
"ds_read_b64_tr_b16 a[192:193], v200 offset:27712\n"
"v_mfma_f32_16x16x32_bf16 v[76:79], a[216:219], v[52:55], v[76:79]\n"
"ds_read_b64_tr_b16 a[194:195], v200 offset:32320\n"
"buffer_load_dwordx4 a[172:175], v246, s[28:31], s57 offen offset:16 sc1\n"
"v_mfma_f32_16x16x32_bf16 v[80:83], a[220:223], v[20:23], v[80:83]\n"
"ds_read_b64_tr_b16 a[196:197], v200 offset:27744\n"
"v_mfma_f32_16x16x32_bf16 v[84:87], a[220:223], v[52:55], v[84:87]\n"
"ds_read_b64_tr_b16 a[198:199], v200 offset:32352\n"
"v_mfma_f32_16x16x32_bf16 v[88:91], a[128:131], v[20:23], v[88:91]\n"
"ds_read_b64_tr_b16 a[200:201], v200 offset:27776\n"
"v_mfma_f32_16x16x32_bf16 v[92:95], a[128:131], v[52:55], v[92:95]\n"
"s_mov_b32 m0, s51\n"
"ds_read_b64_tr_b16 a[202:203], v200 offset:32384\n"
"v_mfma_f32_16x16x32_bf16 v[96:99], a[132:135], v[20:23], v[96:99]\n"
"ds_read_b64_tr_b16 a[204:205], v200 offset:27808\n"
"buffer_load_dwordx4 v213, s[24:27], s36 offen offset:3072 lds\n"
"v_mfma_f32_16x16x32_bf16 v[100:103], a[132:135], v[52:55], v[100:103]\n"
"s_waitcnt lgkmcnt(11)\n"
"ds_read_b64_tr_b16 a[206:207], v200 offset:32416\n"
"buffer_load_dwordx4 a[176:179], v246, s[28:31], s58 offen offset:0 sc1\n"
"v_mfma_f32_16x16x32_bf16 v[104:107], a[136:139], v[20:23], v[104:107]\n"
"ds_read_b64_tr_b16 a[208:209], v200 offset:27840\n"
"v_mfma_f32_16x16x32_bf16 v[108:111], a[136:139], v[52:55], v[108:111]\n"
"ds_read_b64_tr_b16 a[210:211], v200 offset:32448\n"
"v_mfma_f32_16x16x32_bf16 v[112:115], a[140:143], v[20:23], v[112:115]\n"
"ds_read_b64_tr_b16 a[212:213], v200 offset:27872\n"
"v_mfma_f32_16x16x32_bf16 v[116:119], a[140:143], v[52:55], v[116:119]\n"
"ds_read_b64_tr_b16 a[214:215], v200 offset:32480\n"
"v_mfma_f32_16x16x32_bf16 v[120:123], a[144:147], v[20:23], v[120:123]\n"
"s_add_u32 m0, s51, 4096\n"
"ds_read_b64_tr_b16 a[216:217], v203 offset:27648\n"
"v_mfma_f32_16x16x32_bf16 v[124:127], a[144:147], v[52:55], v[124:127]\n"
"ds_read_b64_tr_b16 a[218:219], v203 offset:32256\n"
"buffer_load_dwordx4 v214, s[24:27], s36 offen offset:0 lds\n"
"v_mfma_f32_16x16x32_bf16 v[226:229], a[148:151], v[20:23], v[226:229]\n"
"ds_read_b64_tr_b16 a[220:221], v200 offset:36864\n"
"buffer_load_dwordx4 a[180:183], v246, s[28:31], s58 offen offset:16 sc1\n"
"v_mfma_f32_16x16x32_bf16 v[230:233], a[148:151], v[52:55], v[230:233]\n"
"s_waitcnt lgkmcnt(11)\n"
"ds_read_b64_tr_b16 a[222:223], v200 offset:41472\n"
"v_mfma_f32_16x16x32_bf16 v[64:67], a[152:155], v[24:27], v[64:67]\n"
"ds_read_b64_tr_b16 a[128:129], v200 offset:36896\n"
"v_mfma_f32_16x16x32_bf16 v[68:71], a[152:155], v[56:59], v[68:71]\n"
"ds_read_b64_tr_b16 a[130:131], v200 offset:41504\n"
"v_mfma_f32_16x16x32_bf16 v[72:75], a[156:159], v[24:27], v[72:75]\n"
"ds_read_b64_tr_b16 a[132:133], v200 offset:36928\n"
"v_mfma_f32_16x16x32_bf16 v[76:79], a[156:159], v[56:59], v[76:79]\n"
"s_add_u32 m0, s51, 4096\n"
"ds_read_b64_tr_b16 a[134:135], v200 offset:41536\n"
"v_mfma_f32_16x16x32_bf16 v[80:83], a[192:195], v[24:27], v[80:83]\n"
"ds_read_b64_tr_b16 a[136:137], v200 offset:36960\n"
"buffer_load_dwordx4 v236, s[24:27], s36 offen offset:1024 lds\n"
"v_mfma_f32_16x16x32_bf16 v[84:87], a[192:195], v[56:59], v[84:87]\n"
"ds_read_b64_tr_b16 a[138:139], v200 offset:41568\n"
"buffer_load_dwordx4 a[184:187], v246, s[28:31], s59 offen offset:0 sc1\n"
"v_mfma_f32_16x16x32_bf16 v[88:91], a[196:199], v[24:27], v[88:91]\n"
"ds_read_b64_tr_b16 a[140:141], v200 offset:36992\n"
"v_mfma_f32_16x16x32_bf16 v[92:95], a[196:199], v[56:59], v[92:95]\n"
"s_waitcnt lgkmcnt(11)\n"
"ds_read_b64_tr_b16 a[142:143], v200 offset:41600\n"
"v_mfma_f32_16x16x32_bf16 v[96:99], a[200:203], v[24:27], v[96:99]\n"
"ds_read_b64_tr_b16 a[144:145], v200 offset:37024\n"
"v_mfma_f32_16x16x32_bf16 v[100:103], a[200:203], v[56:59], v[100:103]\n"
"ds_read_b64_tr_b16 a[146:147], v200 offset:41632\n"
"v_mfma_f32_16x16x32_bf16 v[104:107], a[204:207], v[24:27], v[104:107]\n"
"s_add_u32 m0, s51, 4096\n"
"ds_read_b64_tr_b16 a[148:149], v200 offset:37056\n"
"v_mfma_f32_16x16x32_bf16 v[108:111], a[204:207], v[56:59], v[108:111]\n"
"ds_read_b64_tr_b16 a[150:151], v200 offset:41664\n"
"buffer_load_dwordx4 v237, s[24:27], s36 offen offset:2048 lds\n"
"v_mfma_f32_16x16x32_bf16 v[112:115], a[208:211], v[24:27], v[112:115]\n"
"ds_read_b64_tr_b16 a[152:153], v200 offset:37088\n"
"buffer_load_dwordx4 a[188:191], v246, s[28:31], s59 offen offset:16 sc1\n"
"v_mfma_f32_16x16x32_bf16 v[116:119], a[208:211], v[56:59], v[116:119]\n"
"ds_read_b64_tr_b16 a[154:155], v200 offset:41696\n"
"v_mfma_f32_16x16x32_bf16 v[120:123], a[212:215], v[24:27], v[120:123]\n"
"ds_read_b64_tr_b16 a[156:157], v203 offset:36864\n"
"v_mfma_f32_16x16x32_bf16 v[124:127], a[212:215], v[56:59], v[124:127]\n"
"s_waitcnt lgkmcnt(11)\n"
"ds_read_b64_tr_b16 a[158:159], v203 offset:41472\n"
"v_mfma_f32_16x16x32_bf16 v[226:229], a[216:219], v[24:27], v[226:229]\n"
"ds_read_b64_tr_b16 a[192:193], v200 offset:46080\n"
"v_mfma_f32_16x16x32_bf16 v[230:233], a[216:219], v[56:59], v[230:233]\n"
"s_add_u32 m0, s51, 4096\n"
"ds_read_b64_tr_b16 a[194:195], v200 offset:50688\n"
"v_mfma_f32_16x16x32_bf16 v[64:67], a[220:223], v[28:31], v[64:67]\n"
"ds_read_b64_tr_b16 a[196:197], v200 offset:46112\n"
"buffer_load_dwordx4 v238, s[24:27], s36 offen offset:3072 lds\n"
"v_mfma_f32_16x16x32_bf16 v[68:71], a[220:223], v[60:63], v[68:71]\n"
"ds_read_b64_tr_b16 a[198:199], v200 offset:50720\n"
"buffer_load_dwordx2 a[224:225], v254, s[80:83], 0 offen\n"
"v_mfma_f32_16x16x32_bf16 v[72:75], a[128:131], v[28:31], v[72:75]\n"
"ds_read_b64_tr_b16 a[200:201], v200 offset:46144\n"
"v_mfma_f32_16x16x32_bf16 v[76:79], a[128:131], v[60:63], v[76:79]\n"
"ds_read_b64_tr_b16 a[202:203], v200 offset:50752\n"
"v_mfma_f32_16x16x32_bf16 v[80:83], a[132:135], v[28:31], v[80:83]\n"
"ds_read_b64_tr_b16 a[204:205], v200 offset:46176\n"
"v_mfma_f32_16x16x32_bf16 v[84:87], a[132:135], v[60:63], v[84:87]\n"
"s_waitcnt lgkmcnt(11)\n"
"ds_read_b64_tr_b16 a[206:207], v200 offset:50784\n"
"v_mfma_f32_16x16x32_bf16 v[88:91], a[136:139], v[28:31], v[88:91]\n"
"s_add_u32 m0, s51, 8192\n"
"ds_read_b64_tr_b16 a[208:209], v200 offset:46208\n"
"v_mfma_f32_16x16x32_bf16 v[92:95], a[136:139], v[60:63], v[92:95]\n"
"ds_read_b64_tr_b16 a[210:211], v200 offset:50816\n"
"buffer_load_dwordx4 v239, s[24:27], s36 offen offset:0 lds\n"
"v_mfma_f32_16x16x32_bf16 v[96:99], a[140:143], v[28:31], v[96:99]\n"
"ds_read_b64_tr_b16 a[212:213], v200 offset:46240\n"
"v_mfma_f32_16x16x32_bf16 v[100:103], a[140:143], v[60:63], v[100:103]\n"
"ds_read_b64_tr_b16 a[214:215], v200 offset:50848\n"
"v_mfma_f32_16x16x32_bf16 v[104:107], a[144:147], v[28:31], v[104:107]\n"
"ds_read_b64_tr_b16 a[216:217], v200 offset:46272\n"
"v_mfma_f32_16x16x32_bf16 v[108:111], a[144:147], v[60:63], v[108:111]\n"
"ds_read_b64_tr_b16 a[218:219], v200 offset:50880\n"
"v_mfma_f32_16x16x32_bf16 v[112:115], a[148:151], v[28:31], v[112:115]\n"
"ds_read_b64_tr_b16 a[220:221], v200 offset:46304\n"
"v_mfma_f32_16x16x32_bf16 v[116:119], a[148:151], v[60:63], v[116:119]\n"
"s_waitcnt lgkmcnt(11)\n"
"ds_read_b64_tr_b16 a[222:223], v200 offset:50912\n"
"v_mfma_f32_16x16x32_bf16 v[120:123], a[152:155], v[28:31], v[120:123]\n"
"ds_read_b64_tr_b16 a[128:129], v203 offset:46080\n"
"buffer_load_dword v249, v215, s[28:31], s23 offen sc1\n"
"v_mfma_f32_16x16x32_bf16 v[124:127], a[152:155], v[60:63], v[124:127]\n"
"ds_read_b64_tr_b16 a[130:131], v203 offset:50688\n"
"v_mfma_f32_16x16x32_bf16 v[226:229], a[156:159], v[28:31], v[226:229]\n"
"ds_read_b64_tr_b16 a[132:133], v200 offset:55296\n"
"v_mfma_f32_16x16x32_bf16 v[230:233], a[156:159], v[60:63], v[230:233]\n"
"ds_read_b64_tr_b16 a[134:135], v200 offset:59904\n"
"s_barrier\n"
"s_waitcnt vmcnt(2)\n"
"s_mov_b32 s87, 0x80000\n"
"v_accvgpr_read_b32 v26, a160\n"
"v_accvgpr_read_b32 v27, a161\n"
"v_accvgpr_read_b32 v28, a162\n"
"v_accvgpr_read_b32 v29, a163\n"
"v_accvgpr_read_b32 v30, a164\n"
"v_accvgpr_read_b32 v31, a165\n"
"v_accvgpr_read_b32 v32, a166\n"
"v_accvgpr_read_b32 v33, a167\n"
"v_accvgpr_read_b32 v34, a168\n"
"v_accvgpr_read_b32 v35, a169\n"
"v_accvgpr_read_b32 v36, a170\n"
"v_accvgpr_read_b32 v37, a171\n"
"v_accvgpr_read_b32 v38, a172\n"
"v_accvgpr_read_b32 v39, a173\n"
"v_accvgpr_read_b32 v40, a174\n"
"v_accvgpr_read_b32 v41, a175\n"
"v_accvgpr_read_b32 v42, a176\n"
"v_accvgpr_read_b32 v43, a177\n"
"v_accvgpr_read_b32 v44, a178\n"
"v_accvgpr_read_b32 v45, a179\n"
"v_accvgpr_read_b32 v46, a180\n"
"v_accvgpr_read_b32 v47, a181\n"
"v_accvgpr_read_b32 v48, a182\n"
"v_accvgpr_read_b32 v49, a183\n"
"v_accvgpr_read_b32 v50, a184\n"
"v_accvgpr_read_b32 v51, a185\n"
"v_accvgpr_read_b32 v52, a186\n"
"v_accvgpr_read_b32 v53, a187\n"
"v_accvgpr_read_b32 v54, a188\n"
"v_accvgpr_read_b32 v55, a189\n"
"v_accvgpr_read_b32 v56, a190\n"
"v_accvgpr_read_b32 v57, a191\n"
"v_accvgpr_read_b32 v58, a224\n"
"v_accvgpr_read_b32 v59, a225\n"
"AT_tchk:\n"
"v_xor_b32 v24, s85, v27\n"
"v_xor_b32 v25, s85, v29\n"
"v_bitop3_b32 v24, v31, s85, v24 bitop3:0xbe\n"
"v_bitop3_b32 v25, v33, s85, v25 bitop3:0xbe\n"
"v_bitop3_b32 v24, v35, s85, v24 bitop3:0xbe\n"
"v_bitop3_b32 v25, v37, s85, v25 bitop3:0xbe\n"
"v_bitop3_b32 v24, v39, s85, v24 bitop3:0xbe\n"
"v_bitop3_b32 v25, v41, s85, v25 bitop3:0xbe\n"
"v_bitop3_b32 v24, v43, s85, v24 bitop3:0xbe\n"
"v_bitop3_b32 v25, v45, s85, v25 bitop3:0xbe\n"
"v_bitop3_b32 v24, v47, s85, v24 bitop3:0xbe\n"
"v_bitop3_b32 v25, v49, s85, v25 bitop3:0xbe\n"
"v_bitop3_b32 v24, v51, s85, v24 bitop3:0xbe\n"
"v_bitop3_b32 v25, v53, s85, v25 bitop3:0xbe\n"
"v_bitop3_b32 v24, v55, s85, v24 bitop3:0xbe\n"
"v_bitop3_b32 v25, v57, s85, v25 bitop3:0xbe\n"
"v_or_b32 v24, v24, v25\n"
"v_cmp_ne_u32 s[62:63], 0, v24\n"
"v_cmp_gt_u32 vcc, 144, v253\n"
"s_nop 1\n"
"s_and_b64 s[62:63], vcc, s[62:63]\n"
"s_cmp_eq_u64 s[62:63], 0\n"
"s_cbranch_scc1 AT_tok\n"
"s_sub_u32 s87, s87, 1\n"
"s_cmp_eq_u32 s87, 0\n"
"s_cbranch_scc1 AT_tok\n"
"s_sleep 2\n"
"buffer_load_dwordx4 v[26:29], v246, s[28:31], s56 offen offset:0 sc1\n"
"buffer_load_dwordx4 v[30:33], v246, s[28:31], s56 offen offset:16 sc1\n"
"buffer_load_dwordx4 v[34:37], v246, s[28:31], s57 offen offset:0 sc1\n"
"buffer_load_dwordx4 v[38:41], v246, s[28:31], s57 offen offset:16 sc1\n"
"buffer_load_dwordx4 v[42:45], v246, s[28:31], s58 offen offset:0 sc1\n"
"buffer_load_dwordx4 v[46:49], v246, s[28:31], s58 offen offset:16 sc1\n"
"buffer_load_dwordx4 v[50:53], v246, s[28:31], s59 offen offset:0 sc1\n"
"buffer_load_dwordx4 v[54:57], v246, s[28:31], s59 offen offset:16 sc1\n"
"s_waitcnt vmcnt(0)\n"
"s_branch AT_tchk\n"
"AT_tok:\n"
"s_waitcnt vmcnt(0)\n"
"v_cmp_ne_u32 vcc, s85, v249\n"
"s_nop 0\n"
"s_cbranch_vccnz AT_x1\n"
"AT_x2:\n"
"buffer_load_dwordx4 a[0:3], v207, s[28:31], s20 offen offset:0 sc1\n"
"buffer_load_dwordx4 a[4:7], v207, s[28:31], s20 offen offset:1024 sc1\n"
"buffer_load_dwordx4 a[8:11], v207, s[28:31], s21 offen offset:0 sc1\n"
"buffer_load_dwordx4 a[12:15], v207, s[28:31], s21 offen offset:1024 sc1\n"
"buffer_load_dwordx4 a[16:19], v207, s[28:31], s22 offen offset:0 sc1\n"
"buffer_load_dwordx4 a[20:23], v207, s[28:31], s22 offen offset:1024 sc1\n"
"buffer_load_dwordx4 a[24:27], v207, s[28:31], s20 offen offset:2048 sc1\n"
"buffer_load_dwordx4 a[28:31], v207, s[28:31], s20 offen offset:3072 sc1\n"
"s_add_u32 s20, s20, 4096\n"
"v_cmp_gt_u32 vcc, 144, v253\n"
"v_lshlrev_b32 v0, 16, v58\n"
"v_and_b32 v1, 0xffff0000, v58\n"
"v_lshlrev_b32 v2, 16, v59\n"
"v_and_b32 v3, 0xffff0000, v59\n"
"v_mov_b32 v4, v26\n"
"v_add_f32 v12, 1.0, v42\n"
"v_mov_b32 v16, v34\n"
"v_mov_b32 v20, v50\n"
"v_mul_f32 v8, v12, v0\n"
"v_mov_b32 v5, v28\n"
"v_add_f32 v13, 1.0, v44\n"
"v_mov_b32 v17, v36\n"
"v_mov_b32 v21, v52\n"
"v_mul_f32 v9, v13, v1\n"
"v_mov_b32 v6, v30\n"
"v_add_f32 v14, 1.0, v46\n"
"v_mov_b32 v18, v38\n"
"v_mov_b32 v22, v54\n"
"v_mul_f32 v10, v14, v2\n"
"v_mov_b32 v7, v32\n"
"v_add_f32 v15, 1.0, v48\n"
"v_mov_b32 v19, v40\n"
"v_mov_b32 v23, v56\n"
"v_mul_f32 v11, v15, v3\n"
"s_mov_b64 exec, vcc\n"
"ds_write_b128 v247, v[4:7] offset:0\n"
"ds_write_b128 v247, v[8:11] offset:2304\n"
"ds_write_b128 v247, v[12:15] offset:4608\n"
"ds_write_b128 v247, v[16:19] offset:6912\n"
"ds_write_b128 v247, v[20:23] offset:38912\n"
"s_mov_b64 exec, -1\n"
"v_add_f32 v24, v8, v9\n"
"v_mul_f32 v25, v16, v0\n"
"v_add_f32 v24, v24, v10\n"
"v_fmac_f32 v25, v17, v1\n"
"v_add_f32 v24, v24, v11\n"
"v_fmac_f32 v25, v18, v2\n"
"s_nop 0\n"
"v_fmac_f32 v25, v19, v3\n"
"s_nop 1\n"
"v_add_f32_dpp v24, v24, v24 row_ror:1 row_mask:0xf bank_mask:0xf\n"
"v_add_f32_dpp v25, v25, v25 row_ror:1 row_mask:0xf bank_mask:0xf\n"
"s_nop 0\n"
"v_add_f32_dpp v24, v24, v24 row_ror:2 row_mask:0xf bank_mask:0xf\n"
"v_add_f32_dpp v25, v25, v25 row_ror:2 row_mask:0xf bank_mask:0xf\n"
"s_nop 0\n"
"v_add_f32_dpp v24, v24, v24 row_ror:4 row_mask:0xf bank_mask:0xf\n"
"v_add_f32_dpp v25, v25, v25 row_ror:4 row_mask:0xf bank_mask:0xf\n"
"s_nop 0\n"
"v_add_f32_dpp v24, v24, v24 row_ror:8 row_mask:0xf bank_mask:0xf\n"
"v_add_f32_dpp v25, v25, v25 row_ror:8 row_mask:0xf bank_mask:0xf\n"
"s_nop 0\n"
"v_mov_b32 v26, v24\n"
"v_mov_b32 v27, v25\n"
"s_nop 0\n"
"v_permlane16_swap_b32 v24, v26\n"
"v_permlane16_swap_b32 v25, v27\n"
"v_add_f32 v24, v24, v26\n"
"v_add_f32 v25, v25, v27\n"
"v_mov_b32 v26, v24\n"
"v_mov_b32 v27, v25\n"
"s_nop 0\n"
"v_permlane32_swap_b32 v24, v26\n"
"v_permlane32_swap_b32 v25, v27\n"
"v_add_f32 v24, v24, v26\n"
"v_add_f32 v25, v25, v27\n"
"s_lshl_b32 s62, s41, 3\n"
"s_add_u32 s62, s62, s40\n"
"v_mov_b32 v26, s62\n"
"v_add_u32 v26, 161536, v26\n"
"s_mov_b64 exec, 1\n"
"ds_write_b64 v26, v[24:25]\n"
"s_mov_b64 exec, -1\n"
"s_mov_b64 exec, 0\n"
"s_cmp_eq_u32 s41, 0\n"
"s_cbranch_scc1 AT_hw1_0\n"
"s_cmp_eq_u32 s41, 1\n"
"s_cbranch_scc1 AT_hw1_1\n"
"s_cmp_eq_u32 s41, 2\n"
"s_cbranch_scc1 AT_hw1_2\n"
"s_call_b64 %[kp], AT_pgA0\n"
"s_branch AT_hx1\n"
"AT_hw1_0:\n"
"s_call_b64 %[kp], AT_pg14\n"
"s_branch AT_hx1\n"
"AT_hw1_1:\n"
"s_call_b64 %[kp], AT_pg15\n"
"s_branch AT_hx1\n"
"AT_hw1_2:\n"
"s_call_b64 %[kp], AT_pg16\n"
"AT_hx1:\n"
"s_mov_b64 exec, -1\n"
"s_waitcnt vmcnt(6)\n"
"v_mfma_f32_16x16x32_bf16 v[64:67], a[192:195], a[0:3], v[64:67]\n"
"ds_read_b64_tr_b16 a[136:137], v200 offset:55328\n"
"buffer_load_dwordx4 a[32:35], v207, s[28:31], s21 offen offset:2048 sc1\n"
"v_mfma_f32_16x16x32_bf16 v[68:71], a[192:195], a[4:7], v[68:71]\n"
"s_add_u32 m0, s53, 36864\n"
"ds_read_b64_tr_b16 a[138:139], v200 offset:59936\n"
"v_mfma_f32_16x16x32_bf16 v[72:75], a[196:199], a[0:3], v[72:75]\n"
"ds_read_b64_tr_b16 a[140:141], v200 offset:55360\n"
"buffer_load_dwordx4 v210, s[24:27], s37 offen offset:0 lds\n"
"v_mfma_f32_16x16x32_bf16 v[76:79], a[196:199], a[4:7], v[76:79]\n"
"s_waitcnt lgkmcnt(14)\n"
"ds_read_b64_tr_b16 a[142:143], v200 offset:59968\n"
"v_mfma_f32_16x16x32_bf16 v[80:83], a[200:203], a[0:3], v[80:83]\n"
"ds_read_b64_tr_b16 a[144:145], v200 offset:55392\n"
"v_mfma_f32_16x16x32_bf16 v[84:87], a[200:203], a[4:7], v[84:87]\n"
"ds_read_b64_tr_b16 a[146:147], v200 offset:60000\n"
"buffer_load_dwordx4 a[36:39], v207, s[28:31], s21 offen offset:3072 sc1\n"
"v_mfma_f32_16x16x32_bf16 v[88:91], a[204:207], a[0:3], v[88:91]\n"
"s_add_u32 s21, s21, 4096\n"
"ds_read_b64_tr_b16 a[148:149], v200 offset:55424\n"
"v_mfma_f32_16x16x32_bf16 v[92:95], a[204:207], a[4:7], v[92:95]\n"
"ds_read_b64_tr_b16 a[150:151], v200 offset:60032\n"
"v_mfma_f32_16x16x32_bf16 v[96:99], a[208:211], a[0:3], v[96:99]\n"
"s_add_u32 m0, s53, 36864\n"
"ds_read_b64_tr_b16 a[152:153], v200 offset:55456\n"
"v_mfma_f32_16x16x32_bf16 v[100:103], a[208:211], a[4:7], v[100:103]\n"
"ds_read_b64_tr_b16 a[154:155], v200 offset:60064\n"
"buffer_load_dwordx4 v211, s[24:27], s37 offen offset:1024 lds\n"
"v_mfma_f32_16x16x32_bf16 v[104:107], a[212:215], a[0:3], v[104:107]\n"
"ds_read_b64_tr_b16 a[156:157], v200 offset:55488\n"
"buffer_load_dwordx4 a[40:43], v207, s[28:31], s22 offen offset:2048 sc1\n"
"v_mfma_f32_16x16x32_bf16 v[108:111], a[212:215], a[4:7], v[108:111]\n"
"ds_read_b64_tr_b16 a[158:159], v200 offset:60096\n"
"v_mfma_f32_16x16x32_bf16 v[112:115], a[216:219], a[0:3], v[112:115]\n"
"ds_read_b64_tr_b16 a[192:193], v200 offset:55520\n"
"v_mfma_f32_16x16x32_bf16 v[116:119], a[216:219], a[4:7], v[116:119]\n"
"s_waitcnt lgkmcnt(11)\n"
"ds_read_b64_tr_b16 a[194:195], v200 offset:60128\n"
"v_mfma_f32_16x16x32_bf16 v[120:123], a[220:223], a[0:3], v[120:123]\n"
"ds_read_b64_tr_b16 a[196:197], v203 offset:55296\n"
"v_mfma_f32_16x16x32_bf16 v[124:127], a[220:223], a[4:7], v[124:127]\n"
"s_add_u32 m0, s53, 36864\n"
"ds_read_b64_tr_b16 a[198:199], v203 offset:59904\n"
"buffer_load_dwordx4 a[44:47], v207, s[28:31], s22 offen offset:3072 sc1\n"
"v_mfma_f32_16x16x32_bf16 v[226:229], a[128:131], a[0:3], v[226:229]\n"
"s_add_u32 s22, s22, 4096\n"
"ds_read_b64_tr_b16 a[200:201], v200 offset:64512\n"
"buffer_load_dwordx4 v212, s[24:27], s37 offen offset:2048 lds\n"
"v_mfma_f32_16x16x32_bf16 v[230:233], a[128:131], a[4:7], v[230:233]\n"
"s_waitcnt vmcnt(11)\n"
"ds_read_b64_tr_b16 a[202:203], v201 offset:3584\n"
"v_mfma_f32_16x16x32_bf16 v[64:67], a[132:135], a[8:11], v[64:67]\n"
"ds_read_b64_tr_b16 a[204:205], v200 offset:64544\n"
"v_mfma_f32_16x16x32_bf16 v[68:71], a[132:135], a[12:15], v[68:71]\n"
"ds_read_b64_tr_b16 a[206:207], v201 offset:3616\n"
"v_mfma_f32_16x16x32_bf16 v[72:75], a[136:139], a[8:11], v[72:75]\n"
"ds_read_b64_tr_b16 a[208:209], v200 offset:64576\n"
"buffer_load_dwordx4 a[48:51], v207, s[28:31], s20 offen offset:0 sc1\n"
"v_mfma_f32_16x16x32_bf16 v[76:79], a[136:139], a[12:15], v[76:79]\n"
"s_waitcnt lgkmcnt(11)\n"
"ds_read_b64_tr_b16 a[210:211], v201 offset:3648\n"
"v_mfma_f32_16x16x32_bf16 v[80:83], a[140:143], a[8:11], v[80:83]\n"
"s_add_u32 m0, s53, 36864\n"
"ds_read_b64_tr_b16 a[212:213], v200 offset:64608\n"
"v_mfma_f32_16x16x32_bf16 v[84:87], a[140:143], a[12:15], v[84:87]\n"
"ds_read_b64_tr_b16 a[214:215], v201 offset:3680\n"
"buffer_load_dwordx4 v213, s[24:27], s37 offen offset:3072 lds\n"
"v_mfma_f32_16x16x32_bf16 v[88:91], a[144:147], a[8:11], v[88:91]\n"
"ds_read_b64_tr_b16 a[216:217], v200 offset:64640\n"
"v_mfma_f32_16x16x32_bf16 v[92:95], a[144:147], a[12:15], v[92:95]\n"
"ds_read_b64_tr_b16 a[218:219], v201 offset:3712\n"
"buffer_load_dwordx4 a[52:55], v207, s[28:31], s20 offen offset:1024 sc1\n"
"v_mfma_f32_16x16x32_bf16 v[96:99], a[148:151], a[8:11], v[96:99]\n"
"ds_read_b64_tr_b16 a[220:221], v200 offset:64672\n"
"v_mfma_f32_16x16x32_bf16 v[100:103], a[148:151], a[12:15], v[100:103]\n"
"ds_read_b64_tr_b16 a[222:223], v201 offset:3744\n"
"v_mfma_f32_16x16x32_bf16 v[104:107], a[152:155], a[8:11], v[104:107]\n"
"ds_read_b64_tr_b16 a[128:129], v200 offset:64704\n"
"v_mfma_f32_16x16x32_bf16 v[108:111], a[152:155], a[12:15], v[108:111]\n"
"s_waitcnt lgkmcnt(11)\n"
"ds_read_b64_tr_b16 a[130:131], v201 offset:3776\n"
"v_mfma_f32_16x16x32_bf16 v[112:115], a[156:159], a[8:11], v[112:115]\n"
"s_add_u32 m0, s53, 40960\n"
"ds_read_b64_tr_b16 a[132:133], v200 offset:64736\n"
"buffer_load_dwordx4 a[56:59], v207, s[28:31], s21 offen offset:0 sc1\n"
"v_mfma_f32_16x16x32_bf16 v[116:119], a[156:159], a[12:15], v[116:119]\n"
"ds_read_b64_tr_b16 a[134:135], v201 offset:3808\n"
"buffer_load_dwordx4 v214, s[24:27], s37 offen offset:0 lds\n"
"v_mfma_f32_16x16x32_bf16 v[120:123], a[192:195], a[8:11], v[120:123]\n"
"ds_read_b64_tr_b16 a[136:137], v203 offset:64512\n"
"v_mfma_f32_16x16x32_bf16 v[124:127], a[192:195], a[12:15], v[124:127]\n"
"ds_read_b64_tr_b16 a[138:139], v204 offset:3584\n"
"v_mfma_f32_16x16x32_bf16 v[226:229], a[196:199], a[8:11], v[226:229]\n"
"ds_read_b64_tr_b16 a[140:141], v201 offset:8192\n"
"v_mfma_f32_16x16x32_bf16 v[230:233], a[196:199], a[12:15], v[230:233]\n"
"s_waitcnt vmcnt(14)\n"
"ds_read_b64_tr_b16 a[142:143], v201 offset:12800\n"
"v_mfma_f32_16x16x32_bf16 v[64:67], a[200:203], a[16:19], v[64:67]\n"
"ds_read_b64_tr_b16 a[144:145], v201 offset:8224\n"
"v_mfma_f32_16x16x32_bf16 v[68:71], a[200:203], a[20:23], v[68:71]\n"
"s_waitcnt lgkmcnt(11)\n"
"s_add_u32 m0, s53, 40960\n"
"ds_read_b64_tr_b16 a[146:147], v201 offset:12832\n"
"buffer_load_dwordx4 a[60:63], v207, s[28:31], s21 offen offset:1024 sc1\n"
"v_mfma_f32_16x16x32_bf16 v[72:75], a[204:207], a[16:19], v[72:75]\n"
"ds_read_b64_tr_b16 a[148:149], v201 offset:8256\n"
"buffer_load_dwordx4 v236, s[24:27], s37 offen offset:1024 lds\n"
"v_mfma_f32_16x16x32_bf16 v[76:79], a[204:207], a[20:23], v[76:79]\n"
"ds_read_b64_tr_b16 a[150:151], v201 offset:12864\n"
"v_mfma_f32_16x16x32_bf16 v[80:83], a[208:211], a[16:19], v[80:83]\n"
"ds_read_b64_tr_b16 a[152:153], v201 offset:8288\n"
"v_mfma_f32_16x16x32_bf16 v[84:87], a[208:211], a[20:23], v[84:87]\n"
"ds_read_b64_tr_b16 a[154:155], v201 offset:12896\n"
"v_mfma_f32_16x16x32_bf16 v[88:91], a[212:215], a[16:19], v[88:91]\n"
"ds_read_b64_tr_b16 a[156:157], v201 offset:8320\n"
"v_mfma_f32_16x16x32_bf16 v[92:95], a[212:215], a[20:23], v[92:95]\n"
"ds_read_b64_tr_b16 a[158:159], v201 offset:12928\n"
"v_mfma_f32_16x16x32_bf16 v[96:99], a[216:219], a[16:19], v[96:99]\n"
"s_add_u32 m0, s53, 40960\n"
"ds_read_b64_tr_b16 a[192:193], v201 offset:8352\n"
"buffer_load_dwordx4 a[64:67], v207, s[28:31], s22 offen offset:0 sc1\n"
"v_mfma_f32_16x16x32_bf16 v[100:103], a[216:219], a[20:23], v[100:103]\n"
"s_waitcnt lgkmcnt(11)\n"
"ds_read_b64_tr_b16 a[194:195], v201 offset:12960\n"
"buffer_load_dwordx4 v237, s[24:27], s37 offen offset:2048 lds\n"
"v_mfma_f32_16x16x32_bf16 v[104:107], a[220:223], a[16:19], v[104:107]\n"
"ds_read_b64_tr_b16 a[196:197], v201 offset:8384\n"
"v_mfma_f32_16x16x32_bf16 v[108:111], a[220:223], a[20:23], v[108:111]\n"
"ds_read_b64_tr_b16 a[198:199], v201 offset:12992\n"
"v_mfma_f32_16x16x32_bf16 v[112:115], a[128:131], a[16:19], v[112:115]\n"
"ds_read_b64_tr_b16 a[200:201], v201 offset:8416\n"
"v_mfma_f32_16x16x32_bf16 v[116:119], a[128:131], a[20:23], v[116:119]\n"
"ds_read_b64_tr_b16 a[202:203], v201 offset:13024\n"
"v_mfma_f32_16x16x32_bf16 v[120:123], a[132:135], a[16:19], v[120:123]\n"
"ds_read_b64_tr_b16 a[204:205], v204 offset:8192\n"
"v_mfma_f32_16x16x32_bf16 v[124:127], a[132:135], a[20:23], v[124:127]\n"
"s_add_u32 m0, s53, 40960\n"
"ds_read_b64_tr_b16 a[206:207], v204 offset:12800\n"
"buffer_load_dwordx4 a[68:71], v207, s[28:31], s22 offen offset:1024 sc1\n"
"v_mfma_f32_16x16x32_bf16 v[226:229], a[136:139], a[16:19], v[226:229]\n"
"ds_read_b64_tr_b16 a[208:209], v201 offset:17408\n"
"buffer_load_dwordx4 v238, s[24:27], s37 offen offset:3072 lds\n"
"v_mfma_f32_16x16x32_bf16 v[230:233], a[136:139], a[20:23], v[230:233]\n"
"s_waitcnt lgkmcnt(11)\n"
"s_waitcnt vmcnt(18)\n"
"ds_read_b64_tr_b16 a[210:211], v201 offset:22016\n"
"v_mfma_f32_16x16x32_bf16 v[64:67], a[140:143], a[24:27], v[64:67]\n"
"ds_read_b64_tr_b16 a[212:213], v201 offset:17440\n"
"v_mfma_f32_16x16x32_bf16 v[68:71], a[140:143], a[28:31], v[68:71]\n"
"ds_read_b64_tr_b16 a[214:215], v201 offset:22048\n"
"v_mfma_f32_16x16x32_bf16 v[72:75], a[144:147], a[24:27], v[72:75]\n"
"ds_read_b64_tr_b16 a[216:217], v201 offset:17472\n"
"v_mfma_f32_16x16x32_bf16 v[76:79], a[144:147], a[28:31], v[76:79]\n"
"AT_pg14:\n"
"s_cbranch_execz AT_hr\n"
"ds_read_b64_tr_b16 a[218:219], v201 offset:22080\n"
"v_mfma_f32_16x16x32_bf16 v[80:83], a[148:151], a[24:27], v[80:83]\n"
"s_add_u32 m0, s53, 45056\n"
"ds_read_b64_tr_b16 a[220:221], v201 offset:17504\n"
"buffer_load_dwordx4 a[72:75], v207, s[28:31], s20 offen offset:2048 sc1\n"
"v_mfma_f32_16x16x32_bf16 v[84:87], a[148:151], a[28:31], v[84:87]\n"
"ds_read_b64_tr_b16 a[222:223], v201 offset:22112\n"
"buffer_load_dwordx4 v239, s[24:27], s37 offen offset:0 lds\n"
"v_mfma_f32_16x16x32_bf16 v[88:91], a[152:155], a[24:27], v[88:91]\n"
"ds_read_b64_tr_b16 a[128:129], v201 offset:17536\n"
"v_mfma_f32_16x16x32_bf16 v[92:95], a[152:155], a[28:31], v[92:95]\n"
"s_waitcnt lgkmcnt(11)\n"
"ds_read_b64_tr_b16 a[130:131], v201 offset:22144\n"
"v_mfma_f32_16x16x32_bf16 v[96:99], a[156:159], a[24:27], v[96:99]\n"
"ds_read_b64_tr_b16 a[132:133], v201 offset:17568\n"
"v_mfma_f32_16x16x32_bf16 v[100:103], a[156:159], a[28:31], v[100:103]\n"
"ds_read_b64_tr_b16 a[134:135], v201 offset:22176\n"
"v_mfma_f32_16x16x32_bf16 v[104:107], a[192:195], a[24:27], v[104:107]\n"
"ds_read_b64_tr_b16 a[136:137], v201 offset:17600\n"
"v_mfma_f32_16x16x32_bf16 v[108:111], a[192:195], a[28:31], v[108:111]\n"
"ds_read_b64_tr_b16 a[138:139], v201 offset:22208\n"
"buffer_load_dwordx4 a[76:79], v207, s[28:31], s20 offen offset:3072 sc1\n"
"v_mfma_f32_16x16x32_bf16 v[112:115], a[196:199], a[24:27], v[112:115]\n"
"s_add_u32 s20, s20, 4096\n"
"ds_read_b64_tr_b16 a[140:141], v201 offset:17632\n"
"v_mfma_f32_16x16x32_bf16 v[116:119], a[196:199], a[28:31], v[116:119]\n"
"ds_read_b64_tr_b16 a[142:143], v201 offset:22240\n"
"v_mfma_f32_16x16x32_bf16 v[120:123], a[200:203], a[24:27], v[120:123]\n"
"ds_read_b64_tr_b16 a[144:145], v204 offset:17408\n"
"v_mfma_f32_16x16x32_bf16 v[124:127], a[200:203], a[28:31], v[124:127]\n"
"s_waitcnt lgkmcnt(11)\n"
"ds_read_b64_tr_b16 a[146:147], v204 offset:22016\n"
"v_mfma_f32_16x16x32_bf16 v[226:229], a[204:207], a[24:27], v[226:229]\n"
"ds_read_b64_tr_b16 a[148:149], v201 offset:26624\n"
"v_mfma_f32_16x16x32_bf16 v[230:233], a[204:207], a[28:31], v[230:233]\n"
"s_waitcnt vmcnt(18)\n"
"ds_read_b64_tr_b16 a[150:151], v201 offset:31232\n"
"v_mfma_f32_16x16x32_bf16 v[64:67], a[208:211], a[32:35], v[64:67]\n"
"ds_read_b64_tr_b16 a[152:153], v201 offset:26656\n"
"buffer_load_dwordx4 a[80:83], v207, s[28:31], s21 offen offset:2048 sc1\n"
"v_mfma_f32_16x16x32_bf16 v[68:71], a[208:211], a[36:39], v[68:71]\n"
"s_waitcnt vmcnt(31)\n"
"ds_read_b64_tr_b16 a[154:155], v201 offset:31264\n"
"v_mfma_f32_16x16x32_bf16 v[72:75], a[212:215], a[32:35], v[72:75]\n"
"s_barrier\n"
"ds_read_b64_tr_b16 a[156:157], v201 offset:26688\n"
"v_mfma_f32_16x16x32_bf16 v[76:79], a[212:215], a[36:39], v[76:79]\n"
"ds_read_b64_tr_b16 a[158:159], v201 offset:31296\n"
"v_mfma_f32_16x16x32_bf16 v[80:83], a[216:219], a[32:35], v[80:83]\n"
"s_add_u32 m0, s53, 73728\n"
"ds_read_b64_tr_b16 a[192:193], v201 offset:26720\n"
"v_mfma_f32_16x16x32_bf16 v[84:87], a[216:219], a[36:39], v[84:87]\n"
"s_waitcnt lgkmcnt(11)\n"
"ds_read_b64_tr_b16 a[194:195], v201 offset:31328\n"
"buffer_load_dwordx4 v210, s[24:27], s38 offen offset:0 lds\n"
"v_mfma_f32_16x16x32_bf16 v[88:91], a[220:223], a[32:35], v[88:91]\n"
"ds_read_b64_tr_b16 a[196:197], v201 offset:26752\n"
"v_mfma_f32_16x16x32_bf16 v[92:95], a[220:223], a[36:39], v[92:95]\n"
"ds_read_b64_tr_b16 a[198:199], v201 offset:31360\n"
"buffer_load_dwordx4 a[84:87], v207, s[28:31], s21 offen offset:3072 sc1\n"
"v_mfma_f32_16x16x32_bf16 v[96:99], a[128:131], a[32:35], v[96:99]\n"
"s_add_u32 s21, s21, 4096\n"
"ds_read_b64_tr_b16 a[200:201], v201 offset:26784\n"
"v_mfma_f32_16x16x32_bf16 v[100:103], a[128:131], a[36:39], v[100:103]\n"
"ds_read_b64_tr_b16 a[202:203], v201 offset:31392\n"
"v_mfma_f32_16x16x32_bf16 v[104:107], a[132:135], a[32:35], v[104:107]\n"
"ds_read_b64_tr_b16 a[204:205], v201 offset:26816\n"
"v_mfma_f32_16x16x32_bf16 v[108:111], a[132:135], a[36:39], v[108:111]\n"
"s_add_u32 m0, s53, 73728\n"
"ds_read_b64_tr_b16 a[206:207], v201 offset:31424\n"
"v_mfma_f32_16x16x32_bf16 v[112:115], a[136:139], a[32:35], v[112:115]\n"
"ds_read_b64_tr_b16 a[208:209], v201 offset:26848\n"
"buffer_load_dwordx4 v211, s[24:27], s38 offen offset:1024 lds\n"
"v_mfma_f32_16x16x32_bf16 v[116:119], a[136:139], a[36:39], v[116:119]\n"
"s_waitcnt lgkmcnt(11)\n"
"ds_read_b64_tr_b16 a[210:211], v201 offset:31456\n"
"v_mfma_f32_16x16x32_bf16 v[120:123], a[140:143], a[32:35], v[120:123]\n"
"ds_read_b64_tr_b16 a[212:213], v204 offset:26624\n"
"buffer_load_dwordx4 a[88:91], v207, s[28:31], s22 offen offset:2048 sc1\n"
"v_mfma_f32_16x16x32_bf16 v[124:127], a[140:143], a[36:39], v[124:127]\n"
"ds_read_b64_tr_b16 a[214:215], v204 offset:31232\n"
"v_mfma_f32_16x16x32_bf16 v[226:229], a[144:147], a[32:35], v[226:229]\n"
"ds_read_b64_tr_b16 a[216:217], v201 offset:35840\n"
"v_mfma_f32_16x16x32_bf16 v[230:233], a[144:147], a[36:39], v[230:233]\n"
"s_waitcnt vmcnt(20)\n"
"ds_read_b64_tr_b16 a[218:219], v201 offset:40448\n"
"v_mfma_f32_16x16x32_bf16 v[64:67], a[148:151], a[40:43], v[64:67]\n"
"s_add_u32 m0, s53, 73728\n"
"ds_read_b64_tr_b16 a[220:221], v201 offset:35872\n"
"v_mfma_f32_16x16x32_bf16 v[68:71], a[148:151], a[44:47], v[68:71]\n"
"ds_read_b64_tr_b16 a[222:223], v201 offset:40480\n"
"buffer_load_dwordx4 v212, s[24:27], s38 offen offset:2048 lds\n"
"v_mfma_f32_16x16x32_bf16 v[72:75], a[152:155], a[40:43], v[72:75]\n"
"ds_read_b64_tr_b16 a[128:129], v201 offset:35904\n"
"v_mfma_f32_16x16x32_bf16 v[76:79], a[152:155], a[44:47], v[76:79]\n"
"s_waitcnt lgkmcnt(11)\n"
"ds_read_b64_tr_b16 a[130:131], v201 offset:40512\n"
"buffer_load_dwordx4 a[92:95], v207, s[28:31], s22 offen offset:3072 sc1\n"
"v_mfma_f32_16x16x32_bf16 v[80:83], a[156:159], a[40:43], v[80:83]\n"
"s_add_u32 s22, s22, 4096\n"
"ds_read_b64_tr_b16 a[132:133], v201 offset:35936\n"
"v_mfma_f32_16x16x32_bf16 v[84:87], a[156:159], a[44:47], v[84:87]\n"
"ds_read_b64_tr_b16 a[134:135], v201 offset:40544\n"
"v_mfma_f32_16x16x32_bf16 v[88:91], a[192:195], a[40:43], v[88:91]\n"
"ds_read_b64_tr_b16 a[136:137], v201 offset:35968\n"
"v_mfma_f32_16x16x32_bf16 v[92:95], a[192:195], a[44:47], v[92:95]\n"
"s_add_u32 m0, s53, 73728\n"
"ds_read_b64_tr_b16 a[138:139], v201 offset:40576\n"
"v_mfma_f32_16x16x32_bf16 v[96:99], a[196:199], a[40:43], v[96:99]\n"
"ds_read_b64_tr_b16 a[140:141], v201 offset:36000\n"
"buffer_load_dwordx4 v213, s[24:27], s38 offen offset:3072 lds\n"
"v_mfma_f32_16x16x32_bf16 v[100:103], a[196:199], a[44:47], v[100:103]\n"
"ds_read_b64_tr_b16 a[142:143], v201 offset:40608\n"
"v_mfma_f32_16x16x32_bf16 v[104:107], a[200:203], a[40:43], v[104:107]\n"
"ds_read_b64_tr_b16 a[144:145], v201 offset:36032\n"
"buffer_load_dwordx4 a[96:99], v207, s[28:31], s20 offen offset:0 sc1\n"
"v_mfma_f32_16x16x32_bf16 v[108:111], a[200:203], a[44:47], v[108:111]\n"
"s_waitcnt lgkmcnt(11)\n"
"ds_read_b64_tr_b16 a[146:147], v201 offset:40640\n"
"v_mfma_f32_16x16x32_bf16 v[112:115], a[204:207], a[40:43], v[112:115]\n"
"ds_read_b64_tr_b16 a[148:149], v201 offset:36064\n"
"v_mfma_f32_16x16x32_bf16 v[116:119], a[204:207], a[44:47], v[116:119]\n"
"ds_read_b64_tr_b16 a[150:151], v201 offset:40672\n"
"v_mfma_f32_16x16x32_bf16 v[120:123], a[208:211], a[40:43], v[120:123]\n"
"s_add_u32 m0, s53, 77824\n"
"ds_read_b64_tr_b16 a[152:153], v204 offset:35840\n"
"v_mfma_f32_16x16x32_bf16 v[124:127], a[208:211], a[44:47], v[124:127]\n"
"ds_read_b64_tr_b16 a[154:155], v204 offset:40448\n"
"buffer_load_dwordx4 v214, s[24:27], s38 offen offset:0 lds\n"
"v_mfma_f32_16x16x32_bf16 v[226:229], a[212:215], a[40:43], v[226:229]\n"
"ds_read_b64_tr_b16 a[156:157], v201 offset:45056\n"
"v_mfma_f32_16x16x32_bf16 v[230:233], a[212:215], a[44:47], v[230:233]\n"
"s_waitcnt vmcnt(21)\n"
"ds_read_b64_tr_b16 a[158:159], v201 offset:49664\n"
"buffer_load_dwordx4 a[100:103], v207, s[28:31], s20 offen offset:1024 sc1\n"
"v_mfma_f32_16x16x32_bf16 v[64:67], a[216:219], a[48:51], v[64:67]\n"
"ds_read_b64_tr_b16 a[192:193], v201 offset:45088\n"
"v_mfma_f32_16x16x32_bf16 v[68:71], a[216:219], a[52:55], v[68:71]\n"
"s_waitcnt lgkmcnt(11)\n"
"ds_read_b64_tr_b16 a[194:195], v201 offset:49696\n"
"v_mfma_f32_16x16x32_bf16 v[72:75], a[220:223], a[48:51], v[72:75]\n"
"ds_read_b64_tr_b16 a[196:197], v201 offset:45120\n"
"v_mfma_f32_16x16x32_bf16 v[76:79], a[220:223], a[52:55], v[76:79]\n"
"s_add_u32 m0, s53, 77824\n"
"ds_read_b64_tr_b16 a[198:199], v201 offset:49728\n"
"v_mfma_f32_16x16x32_bf16 v[80:83], a[128:131], a[48:51], v[80:83]\n"
"ds_read_b64_tr_b16 a[200:201], v201 offset:45152\n"
"buffer_load_dwordx4 v236, s[24:27], s38 offen offset:1024 lds\n"
"v_mfma_f32_16x16x32_bf16 v[84:87], a[128:131], a[52:55], v[84:87]\n"
"ds_read_b64_tr_b16 a[202:203], v201 offset:49760\n"
"v_mfma_f32_16x16x32_bf16 v[88:91], a[132:135], a[48:51], v[88:91]\n"
"ds_read_b64_tr_b16 a[204:205], v201 offset:45184\n"
"buffer_load_dwordx4 a[104:107], v207, s[28:31], s21 offen offset:0 sc1\n"
"v_mfma_f32_16x16x32_bf16 v[92:95], a[132:135], a[52:55], v[92:95]\n"
"ds_read_b64_tr_b16 a[206:207], v201 offset:49792\n"
"v_mfma_f32_16x16x32_bf16 v[96:99], a[136:139], a[48:51], v[96:99]\n"
"ds_read_b64_tr_b16 a[208:209], v201 offset:45216\n"
"v_mfma_f32_16x16x32_bf16 v[100:103], a[136:139], a[52:55], v[100:103]\n"
"s_waitcnt lgkmcnt(11)\n"
"ds_read_b64_tr_b16 a[210:211], v201 offset:49824\n"
"v_mfma_f32_16x16x32_bf16 v[104:107], a[140:143], a[48:51], v[104:107]\n"
"s_add_u32 m0, s53, 77824\n"
"ds_read_b64_tr_b16 a[212:213], v201 offset:45248\n"
"v_mfma_f32_16x16x32_bf16 v[108:111], a[140:143], a[52:55], v[108:111]\n"
"ds_read_b64_tr_b16 a[214:215], v201 offset:49856\n"
"buffer_load_dwordx4 v237, s[24:27], s38 offen offset:2048 lds\n"
"v_mfma_f32_16x16x32_bf16 v[112:115], a[144:147], a[48:51], v[112:115]\n"
"ds_read_b64_tr_b16 a[216:217], v201 offset:45280\n"
"v_mfma_f32_16x16x32_bf16 v[116:119], a[144:147], a[52:55], v[116:119]\n"
"ds_read_b64_tr_b16 a[218:219], v201 offset:49888\n"
"buffer_load_dwordx4 a[108:111], v207, s[28:31], s21 offen offset:1024 sc1\n"
"v_mfma_f32_16x16x32_bf16 v[120:123], a[148:151], a[48:51], v[120:123]\n"
"ds_read_b64_tr_b16 a[220:221], v204 offset:45056\n"
"v_mfma_f32_16x16x32_bf16 v[124:127], a[148:151], a[52:55], v[124:127]\n"
"ds_read_b64_tr_b16 a[222:223], v204 offset:49664\n"
"v_mfma_f32_16x16x32_bf16 v[226:229], a[152:155], a[48:51], v[226:229]\n"
"ds_read_b64_tr_b16 a[128:129], v202 offset:0\n"
"v_mfma_f32_16x16x32_bf16 v[230:233], a[152:155], a[52:55], v[230:233]\n"
"s_waitcnt lgkmcnt(11)\n"
"s_waitcnt vmcnt(23)\n"
"ds_read_b64_tr_b16 a[130:131], v202 offset:4608\n"
"v_mfma_f32_16x16x32_bf16 v[64:67], a[156:159], a[56:59], v[64:67]\n"
"s_add_u32 m0, s53, 77824\n"
"ds_read_b64_tr_b16 a[132:133], v202 offset:32\n"
"v_mfma_f32_16x16x32_bf16 v[68:71], a[156:159], a[60:63], v[68:71]\n"
"ds_read_b64_tr_b16 a[134:135], v202 offset:4640\n"
"buffer_load_dwordx4 v238, s[24:27], s38 offen offset:3072 lds\n"
"v_mfma_f32_16x16x32_bf16 v[72:75], a[192:195], a[56:59], v[72:75]\n"
"ds_read_b64_tr_b16 a[136:137], v202 offset:64\n"
"buffer_load_dwordx4 a[112:115], v207, s[28:31], s22 offen offset:0 sc1\n"
"v_mfma_f32_16x16x32_bf16 v[76:79], a[192:195], a[60:63], v[76:79]\n"
"ds_read_b64_tr_b16 a[138:139], v202 offset:4672\n"
"v_accvgpr_write_b32 a48, v128\n"
"v_mfma_f32_16x16x32_bf16 v[80:83], a[196:199], a[56:59], v[80:83]\n"
"ds_read_b64_tr_b16 a[140:141], v202 offset:96\n"
"v_accvgpr_write_b32 a49, v129\n"
"v_mfma_f32_16x16x32_bf16 v[84:87], a[196:199], a[60:63], v[84:87]\n"
"ds_read_b64_tr_b16 a[142:143], v202 offset:4704\n"
"v_accvgpr_write_b32 a50, v130\n"
"v_mfma_f32_16x16x32_bf16 v[88:91], a[200:203], a[56:59], v[88:91]\n"
"ds_read_b64_tr_b16 a[144:145], v202 offset:128\n"
"v_accvgpr_write_b32 a51, v131\n"
"v_mfma_f32_16x16x32_bf16 v[92:95], a[200:203], a[60:63], v[92:95]\n"
"s_waitcnt lgkmcnt(11)\n"
"s_add_u32 m0, s53, 81920\n"
"ds_read_b64_tr_b16 a[146:147], v202 offset:4736\n"
"v_accvgpr_write_b32 a52, v132\n"
"v_mfma_f32_16x16x32_bf16 v[96:99], a[204:207], a[56:59], v[96:99]\n"
"ds_read_b64_tr_b16 a[148:149], v202 offset:160\n"
"buffer_load_dwordx4 v239, s[24:27], s38 offen offset:0 lds\n"
"v_mfma_f32_16x16x32_bf16 v[100:103], a[204:207], a[60:63], v[100:103]\n"
"ds_read_b64_tr_b16 a[150:151], v202 offset:4768\n"
"buffer_load_dwordx4 a[116:119], v207, s[28:31], s22 offen offset:1024 sc1\n"
"v_mfma_f32_16x16x32_bf16 v[104:107], a[208:211], a[56:59], v[104:107]\n"
"ds_read_b64_tr_b16 a[152:153], v202 offset:192\n"
"v_accvgpr_write_b32 a53, v133\n"
"v_mfma_f32_16x16x32_bf16 v[108:111], a[208:211], a[60:63], v[108:111]\n"
"ds_read_b64_tr_b16 a[154:155], v202 offset:4800\n"
"v_accvgpr_write_b32 a54, v134\n"
"v_mfma_f32_16x16x32_bf16 v[112:115], a[212:215], a[56:59], v[112:115]\n"
"ds_read_b64_tr_b16 a[156:157], v202 offset:224\n"
"v_accvgpr_write_b32 a55, v135\n"
"v_mfma_f32_16x16x32_bf16 v[116:119], a[212:215], a[60:63], v[116:119]\n"
"ds_read_b64_tr_b16 a[158:159], v202 offset:4832\n"
"v_mfma_f32_16x16x32_bf16 v[120:123], a[216:219], a[56:59], v[120:123]\n"
"ds_read_b64_tr_b16 a[192:193], v205 offset:0\n"
"v_mfma_f32_16x16x32_bf16 v[124:127], a[216:219], a[60:63], v[124:127]\n"
"s_waitcnt lgkmcnt(11)\n"
"ds_read_b64_tr_b16 a[194:195], v205 offset:4608\n"
"v_mfma_f32_16x16x32_bf16 v[226:229], a[220:223], a[56:59], v[226:229]\n"
"ds_read_b64_tr_b16 a[196:197], v202 offset:9216\n"
"buffer_load_dwordx4 a[120:123], v207, s[28:31], s20 offen offset:2048 sc1\n"
"v_mfma_f32_16x16x32_bf16 v[230:233], a[220:223], a[60:63], v[230:233]\n"
"s_waitcnt vmcnt(24)\n"
"ds_read_b64_tr_b16 a[198:199], v202 offset:13824\n"
"v_mfma_f32_16x16x32_bf16 v[64:67], a[128:131], a[64:67], v[64:67]\n"
"ds_read_b64_tr_b16 a[200:201], v202 offset:9248\n"
"v_mfma_f32_16x16x32_bf16 v[68:71], a[128:131], a[68:71], v[68:71]\n"
"s_waitcnt vmcnt(21)\n"
"ds_read_b64_tr_b16 a[202:203], v202 offset:13856\n"
"v_accvgpr_write_b32 a56, v136\n"
"v_mfma_f32_16x16x32_bf16 v[72:75], a[132:135], a[64:67], v[72:75]\n"
"s_barrier\n"
"ds_read_b64_tr_b16 a[204:205], v202 offset:9280\n"
"v_accvgpr_write_b32 a57, v137\n"
"v_mfma_f32_16x16x32_bf16 v[76:79], a[132:135], a[68:71], v[76:79]\n"
"ds_read_b64_tr_b16 a[206:207], v202 offset:13888\n"
"v_accvgpr_write_b32 a58, v138\n"
"v_mfma_f32_16x16x32_bf16 v[80:83], a[136:139], a[64:67], v[80:83]\n"
"ds_read_b64_tr_b16 a[208:209], v202 offset:9312\n"
"v_accvgpr_write_b32 a59, v139\n"
"v_mfma_f32_16x16x32_bf16 v[84:87], a[136:139], a[68:71], v[84:87]\n"
"s_waitcnt lgkmcnt(11)\n"
"s_add_u32 m0, s53, 110592\n"
"ds_read_b64_tr_b16 a[210:211], v202 offset:13920\n"
"buffer_load_dwordx4 a[124:127], v207, s[28:31], s20 offen offset:3072 sc1\n"
"v_mfma_f32_16x16x32_bf16 v[88:91], a[140:143], a[64:67], v[88:91]\n"
"s_add_u32 s20, s20, 4096\n"
"ds_read_b64_tr_b16 a[212:213], v202 offset:9344\n"
"buffer_load_dwordx4 v210, s[24:27], s39 offen offset:0 lds\n"
"v_mfma_f32_16x16x32_bf16 v[92:95], a[140:143], a[68:71], v[92:95]\n"
"ds_read_b64_tr_b16 a[214:215], v202 offset:13952\n"
"v_accvgpr_write_b32 a60, v140\n"
"v_mfma_f32_16x16x32_bf16 v[96:99], a[144:147], a[64:67], v[96:99]\n"
"ds_read_b64_tr_b16 a[216:217], v202 offset:9376\n"
"v_accvgpr_write_b32 a61, v141\n"
"v_mfma_f32_16x16x32_bf16 v[100:103], a[144:147], a[68:71], v[100:103]\n"
"ds_read_b64_tr_b16 a[218:219], v202 offset:13984\n"
"v_accvgpr_write_b32 a62, v142\n"
"v_mfma_f32_16x16x32_bf16 v[104:107], a[148:151], a[64:67], v[104:107]\n"
"ds_read_b64_tr_b16 a[220:221], v202 offset:9408\n"
"v_accvgpr_write_b32 a63, v143\n"
"v_mfma_f32_16x16x32_bf16 v[108:111], a[148:151], a[68:71], v[108:111]\n"
"ds_read_b64_tr_b16 a[222:223], v202 offset:14016\n"
"v_mfma_f32_16x16x32_bf16 v[112:115], a[152:155], a[64:67], v[112:115]\n"
"s_add_u32 m0, s53, 110592\n"
"ds_read_b64_tr_b16 a[128:129], v202 offset:9440\n"
"buffer_load_dwordx4 v[0:3], v207, s[28:31], s21 offen offset:2048 sc1\n"
"v_mfma_f32_16x16x32_bf16 v[116:119], a[152:155], a[68:71], v[116:119]\n"
"s_waitcnt lgkmcnt(11)\n"
"ds_read_b64_tr_b16 a[130:131], v202 offset:14048\n"
"buffer_load_dwordx4 v211, s[24:27], s39 offen offset:1024 lds\n"
"v_mfma_f32_16x16x32_bf16 v[120:123], a[156:159], a[64:67], v[120:123]\n"
"ds_read_b64_tr_b16 a[132:133], v205 offset:9216\n"
"v_mfma_f32_16x16x32_bf16 v[124:127], a[156:159], a[68:71], v[124:127]\n"
"ds_read_b64_tr_b16 a[134:135], v205 offset:13824\n"
"v_mfma_f32_16x16x32_bf16 v[226:229], a[192:195], a[64:67], v[226:229]\n"
"ds_read_b64_tr_b16 a[136:137], v202 offset:18432\n"
"v_mfma_f32_16x16x32_bf16 v[230:233], a[192:195], a[68:71], v[230:233]\n"
"s_waitcnt vmcnt(24)\n"
"ds_read_b64_tr_b16 a[138:139], v202 offset:23040\n"
"v_mfma_f32_16x16x32_bf16 v[64:67], a[196:199], a[72:75], v[64:67]\n"
"ds_read_b64_tr_b16 a[140:141], v202 offset:18464\n"
"v_mfma_f32_16x16x32_bf16 v[68:71], a[196:199], a[76:79], v[68:71]\n"
"s_add_u32 m0, s53, 110592\n"
"ds_read_b64_tr_b16 a[142:143], v202 offset:23072\n"
"buffer_load_dwordx4 v[4:7], v207, s[28:31], s21 offen offset:3072 sc1\n"
"v_mfma_f32_16x16x32_bf16 v[72:75], a[200:203], a[72:75], v[72:75]\n"
"s_add_u32 s21, s21, 4096\n"
"ds_read_b64_tr_b16 a[144:145], v202 offset:18496\n"
"buffer_load_dwordx4 v212, s[24:27], s39 offen offset:2048 lds\n"
"v_mfma_f32_16x16x32_bf16 v[76:79], a[200:203], a[76:79], v[76:79]\n"
"s_waitcnt lgkmcnt(11)\n"
"ds_read_b64_tr_b16 a[146:147], v202 offset:23104\n"
"v_accvgpr_write_b32 a64, v144\n"
"v_mfma_f32_16x16x32_bf16 v[80:83], a[204:207], a[72:75], v[80:83]\n"
"ds_read_b64_tr_b16 a[148:149], v202 offset:18528\n"
"v_accvgpr_write_b32 a65, v145\n"
"v_mfma_f32_16x16x32_bf16 v[84:87], a[204:207], a[76:79], v[84:87]\n"
"ds_read_b64_tr_b16 a[150:151], v202 offset:23136\n"
"v_accvgpr_write_b32 a66, v146\n"
"v_mfma_f32_16x16x32_bf16 v[88:91], a[208:211], a[72:75], v[88:91]\n"
"ds_read_b64_tr_b16 a[152:153], v202 offset:18560\n"
"v_accvgpr_write_b32 a67, v147\n"
"v_mfma_f32_16x16x32_bf16 v[92:95], a[208:211], a[76:79], v[92:95]\n"
"ds_read_b64_tr_b16 a[154:155], v202 offset:23168\n"
"v_accvgpr_write_b32 a68, v148\n"
"v_mfma_f32_16x16x32_bf16 v[96:99], a[212:215], a[72:75], v[96:99]\n"
"s_add_u32 m0, s53, 110592\n"
"ds_read_b64_tr_b16 a[156:157], v202 offset:18592\n"
"buffer_load_dwordx4 v[8:11], v207, s[28:31], s22 offen offset:2048 sc1\n"
"v_mfma_f32_16x16x32_bf16 v[100:103], a[212:215], a[76:79], v[100:103]\n"
"ds_read_b64_tr_b16 a[158:159], v202 offset:23200\n"
"buffer_load_dwordx4 v213, s[24:27], s39 offen offset:3072 lds\n"
"v_mfma_f32_16x16x32_bf16 v[104:107], a[216:219], a[72:75], v[104:107]\n"
"ds_read_b64_tr_b16 a[192:193], v202 offset:18624\n"
"v_accvgpr_write_b32 a69, v149\n"
"v_mfma_f32_16x16x32_bf16 v[108:111], a[216:219], a[76:79], v[108:111]\n"
"s_waitcnt lgkmcnt(11)\n"
"ds_read_b64_tr_b16 a[194:195], v202 offset:23232\n"
"v_accvgpr_write_b32 a70, v150\n"
"v_mfma_f32_16x16x32_bf16 v[112:115], a[220:223], a[72:75], v[112:115]\n"
"ds_read_b64_tr_b16 a[196:197], v202 offset:18656\n"
"v_accvgpr_write_b32 a71, v151\n"
"v_mfma_f32_16x16x32_bf16 v[116:119], a[220:223], a[76:79], v[116:119]\n"
"ds_read_b64_tr_b16 a[198:199], v202 offset:23264\n"
"v_mfma_f32_16x16x32_bf16 v[120:123], a[128:131], a[72:75], v[120:123]\n"
"ds_read_b64_tr_b16 a[200:201], v205 offset:18432\n"
"v_mfma_f32_16x16x32_bf16 v[124:127], a[128:131], a[76:79], v[124:127]\n"
"s_add_u32 m0, s53, 114688\n"
"ds_read_b64_tr_b16 a[202:203], v205 offset:23040\n"
"buffer_load_dwordx4 v[12:15], v207, s[28:31], s22 offen offset:3072 sc1\n"
"v_mfma_f32_16x16x32_bf16 v[226:229], a[132:135], a[72:75], v[226:229]\n"
"s_add_u32 s22, s22, 4096\n"
"ds_read_b64_tr_b16 a[204:205], v200 offset:0\n"
"buffer_load_dwordx4 v214, s[24:27], s39 offen offset:0 lds\n"
"v_mfma_f32_16x16x32_bf16 v[230:233], a[132:135], a[76:79], v[230:233]\n"
"s_waitcnt vmcnt(27)\n"
"ds_read_b64_tr_b16 a[206:207], v200 offset:4608\n"
"v_mfma_f32_16x16x32_bf16 v[64:67], a[136:139], a[80:83], v[64:67]\n"
"ds_read_b64_tr_b16 a[208:209], v200 offset:32\n"
"v_mfma_f32_16x16x32_bf16 v[68:71], a[136:139], a[84:87], v[68:71]\n"
"s_waitcnt lgkmcnt(11)\n"
"ds_read_b64_tr_b16 a[210:211], v200 offset:4640\n"
"v_accvgpr_write_b32 a72, v152\n"
"v_mfma_f32_16x16x32_bf16 v[72:75], a[140:143], a[80:83], v[72:75]\n"
"ds_read_b64_tr_b16 a[212:213], v200 offset:64\n"
"v_accvgpr_write_b32 a73, v153\n"
"v_mfma_f32_16x16x32_bf16 v[76:79], a[140:143], a[84:87], v[76:79]\n"
"ds_read_b64_tr_b16 a[214:215], v200 offset:4672\n"
"v_accvgpr_write_b32 a74, v154\n"
"v_mfma_f32_16x16x32_bf16 v[80:83], a[144:147], a[80:83], v[80:83]\n"
"s_add_u32 m0, s53, 114688\n"
"ds_read_b64_tr_b16 a[216:217], v200 offset:96\n"
"buffer_load_dwordx4 v[16:19], v207, s[28:31], s20 offen offset:0 sc1\n"
"v_mfma_f32_16x16x32_bf16 v[84:87], a[144:147], a[84:87], v[84:87]\n"
"ds_read_b64_tr_b16 a[218:219], v200 offset:4704\n"
"buffer_load_dwordx4 v236, s[24:27], s39 offen offset:1024 lds\n"
"v_mfma_f32_16x16x32_bf16 v[88:91], a[148:151], a[80:83], v[88:91]\n"
"ds_read_b64_tr_b16 a[220:221], v200 offset:128\n"
"v_accvgpr_write_b32 a75, v155\n"
"v_mfma_f32_16x16x32_bf16 v[92:95], a[148:151], a[84:87], v[92:95]\n"
"ds_read_b64_tr_b16 a[222:223], v200 offset:4736\n"
"v_accvgpr_write_b32 a76, v156\n"
"v_mfma_f32_16x16x32_bf16 v[96:99], a[152:155], a[80:83], v[96:99]\n"
"ds_read_b64_tr_b16 a[128:129], v200 offset:160\n"
"v_accvgpr_write_b32 a77, v157\n"
"v_mfma_f32_16x16x32_bf16 v[100:103], a[152:155], a[84:87], v[100:103]\n"
"s_waitcnt lgkmcnt(11)\n"
"ds_read_b64_tr_b16 a[130:131], v200 offset:4768\n"
"v_accvgpr_write_b32 a78, v158\n"
"v_mfma_f32_16x16x32_bf16 v[104:107], a[156:159], a[80:83], v[104:107]\n"
"ds_read_b64_tr_b16 a[132:133], v200 offset:192\n"
"v_accvgpr_write_b32 a79, v159\n"
"v_mfma_f32_16x16x32_bf16 v[108:111], a[156:159], a[84:87], v[108:111]\n"
"s_add_u32 m0, s53, 114688\n"
"ds_read_b64_tr_b16 a[134:135], v200 offset:4800\n"
"buffer_load_dwordx4 v[20:23], v207, s[28:31], s20 offen offset:1024 sc1\n"
"v_mfma_f32_16x16x32_bf16 v[112:115], a[192:195], a[80:83], v[112:115]\n"
"ds_read_b64_tr_b16 a[136:137], v200 offset:224\n"
"buffer_load_dwordx4 v237, s[24:27], s39 offen offset:2048 lds\n"
"v_mfma_f32_16x16x32_bf16 v[116:119], a[192:195], a[84:87], v[116:119]\n"
"ds_read_b64_tr_b16 a[138:139], v200 offset:4832\n"
"v_mfma_f32_16x16x32_bf16 v[120:123], a[196:199], a[80:83], v[120:123]\n"
"ds_read_b64_tr_b16 a[140:141], v203 offset:0\n"
"v_mfma_f32_16x16x32_bf16 v[124:127], a[196:199], a[84:87], v[124:127]\n"
"ds_read_b64_tr_b16 a[142:143], v203 offset:4608\n"
"v_mfma_f32_16x16x32_bf16 v[226:229], a[200:203], a[80:83], v[226:229]\n"
"ds_read_b64_tr_b16 a[144:145], v200 offset:9216\n"
"v_mfma_f32_16x16x32_bf16 v[230:233], a[200:203], a[84:87], v[230:233]\n"
"s_waitcnt lgkmcnt(11)\n"
"s_waitcnt vmcnt(27)\n"
"ds_read_b64_tr_b16 a[146:147], v200 offset:13824\n"
"v_mfma_f32_16x16x32_bf16 v[64:67], a[204:207], a[88:91], v[64:67]\n"
"s_add_u32 m0, s53, 114688\n"
"ds_read_b64_tr_b16 a[148:149], v200 offset:9248\n"
"buffer_load_dwordx4 v[24:27], v207, s[28:31], s21 offen offset:0 sc1\n"
"v_mfma_f32_16x16x32_bf16 v[68:71], a[204:207], a[92:95], v[68:71]\n"
"ds_read_b64_tr_b16 a[150:151], v200 offset:13856\n"
"buffer_load_dwordx4 v238, s[24:27], s39 offen offset:3072 lds\n"
"v_mfma_f32_16x16x32_bf16 v[72:75], a[208:211], a[88:91], v[72:75]\n"
"ds_read_b64_tr_b16 a[152:153], v200 offset:9280\n"
"v_accvgpr_write_b32 a80, v160\n"
"v_mfma_f32_16x16x32_bf16 v[76:79], a[208:211], a[92:95], v[76:79]\n"
"ds_read_b64_tr_b16 a[154:155], v200 offset:13888\n"
"v_accvgpr_write_b32 a81, v161\n"
"v_mfma_f32_16x16x32_bf16 v[80:83], a[212:215], a[88:91], v[80:83]\n"
"ds_read_b64_tr_b16 a[156:157], v200 offset:9312\n"
"v_accvgpr_write_b32 a82, v162\n"
"v_mfma_f32_16x16x32_bf16 v[84:87], a[212:215], a[92:95], v[84:87]\n"
"ds_read_b64_tr_b16 a[158:159], v200 offset:13920\n"
"v_accvgpr_write_b32 a83, v163\n"
"v_mfma_f32_16x16x32_bf16 v[88:91], a[216:219], a[88:91], v[88:91]\n"
"ds_read_b64_tr_b16 a[192:193], v200 offset:9344\n"
"v_accvgpr_write_b32 a84, v164\n"
"v_mfma_f32_16x16x32_bf16 v[92:95], a[216:219], a[92:95], v[92:95]\n"
"s_waitcnt lgkmcnt(11)\n"
"s_add_u32 m0, s53, 118784\n"
"ds_read_b64_tr_b16 a[194:195], v200 offset:13952\n"
"buffer_load_dwordx4 v[28:31], v207, s[28:31], s21 offen offset:1024 sc1\n"
"v_mfma_f32_16x16x32_bf16 v[96:99], a[220:223], a[88:91], v[96:99]\n"
"ds_read_b64_tr_b16 a[196:197], v200 offset:9376\n"
"buffer_load_dwordx4 v239, s[24:27], s39 offen offset:0 lds\n"
"v_mfma_f32_16x16x32_bf16 v[100:103], a[220:223], a[92:95], v[100:103]\n"
"ds_read_b64_tr_b16 a[198:199], v200 offset:13984\n"
"v_accvgpr_write_b32 a85, v165\n"
"v_mfma_f32_16x16x32_bf16 v[104:107], a[128:131], a[88:91], v[104:107]\n"
"ds_read_b64_tr_b16 a[200:201], v200 offset:9408\n"
"v_accvgpr_write_b32 a86, v166\n"
"v_mfma_f32_16x16x32_bf16 v[108:111], a[128:131], a[92:95], v[108:111]\n"
"ds_read_b64_tr_b16 a[202:203], v200 offset:14016\n"
"v_accvgpr_write_b32 a87, v167\n"
"v_mfma_f32_16x16x32_bf16 v[112:115], a[132:135], a[88:91], v[112:115]\n"
"ds_read_b64_tr_b16 a[204:205], v200 offset:9440\n"
"v_mfma_f32_16x16x32_bf16 v[116:119], a[132:135], a[92:95], v[116:119]\n"
"ds_read_b64_tr_b16 a[206:207], v200 offset:14048\n"
"v_mfma_f32_16x16x32_bf16 v[120:123], a[136:139], a[88:91], v[120:123]\n"
"ds_read_b64_tr_b16 a[208:209], v203 offset:9216\n"
"buffer_load_dwordx4 v[32:35], v207, s[28:31], s22 offen offset:0 sc1\n"
"v_mfma_f32_16x16x32_bf16 v[124:127], a[136:139], a[92:95], v[124:127]\n"
"s_waitcnt lgkmcnt(11)\n"
"ds_read_b64_tr_b16 a[210:211], v203 offset:13824\n"
"v_mfma_f32_16x16x32_bf16 v[226:229], a[140:143], a[88:91], v[226:229]\n"
"ds_read_b64_tr_b16 a[212:213], v200 offset:18432\n"
"v_mfma_f32_16x16x32_bf16 v[230:233], a[140:143], a[92:95], v[230:233]\n"
"s_waitcnt vmcnt(28)\n"
"ds_read_b64_tr_b16 a[214:215], v200 offset:23040\n"
"v_mfma_f32_16x16x32_bf16 v[64:67], a[144:147], a[96:99], v[64:67]\n"
"ds_read_b64_tr_b16 a[216:217], v200 offset:18464\n"
"v_mfma_f32_16x16x32_bf16 v[68:71], a[144:147], a[100:103], v[68:71]\n"
"ds_read_b64_tr_b16 a[218:219], v200 offset:23072\n"
"v_accvgpr_write_b32 a88, v168\n"
"v_mfma_f32_16x16x32_bf16 v[72:75], a[148:151], a[96:99], v[72:75]\n"
"ds_read_b64_tr_b16 a[220:221], v200 offset:18496\n"
"v_accvgpr_write_b32 a89, v169\n"
"v_mfma_f32_16x16x32_bf16 v[76:79], a[148:151], a[100:103], v[76:79]\n"
"ds_read_b64_tr_b16 a[222:223], v200 offset:23104\n"
"buffer_load_dwordx4 v[36:39], v207, s[28:31], s22 offen offset:1024 sc1\n"
"v_mfma_f32_16x16x32_bf16 v[80:83], a[152:155], a[96:99], v[80:83]\n"
"ds_read_b64_tr_b16 a[128:129], v200 offset:18528\n"
"v_accvgpr_write_b32 a90, v170\n"
"v_mfma_f32_16x16x32_bf16 v[84:87], a[152:155], a[100:103], v[84:87]\n"
"s_waitcnt lgkmcnt(11)\n"
"ds_read_b64_tr_b16 a[130:131], v200 offset:23136\n"
"v_accvgpr_write_b32 a91, v171\n"
"v_mfma_f32_16x16x32_bf16 v[88:91], a[156:159], a[96:99], v[88:91]\n"
"ds_read_b64_tr_b16 a[132:133], v200 offset:18560\n"
"v_accvgpr_write_b32 a92, v172\n"
"v_mfma_f32_16x16x32_bf16 v[92:95], a[156:159], a[100:103], v[92:95]\n"
"ds_read_b64_tr_b16 a[134:135], v200 offset:23168\n"
"v_accvgpr_write_b32 a93, v173\n"
"v_mfma_f32_16x16x32_bf16 v[96:99], a[192:195], a[96:99], v[96:99]\n"
"ds_read_b64_tr_b16 a[136:137], v200 offset:18592\n"
"v_accvgpr_write_b32 a94, v174\n"
"v_mfma_f32_16x16x32_bf16 v[100:103], a[192:195], a[100:103], v[100:103]\n"
"ds_read_b64_tr_b16 a[138:139], v200 offset:23200\n"
"v_accvgpr_write_b32 a95, v175\n"
"v_mfma_f32_16x16x32_bf16 v[104:107], a[196:199], a[96:99], v[104:107]\n"
"ds_read_b64_tr_b16 a[140:141], v200 offset:18624\n"
"buffer_load_dwordx4 v[40:43], v207, s[28:31], s20 offen offset:2048 sc1\n"
"v_mfma_f32_16x16x32_bf16 v[108:111], a[196:199], a[100:103], v[108:111]\n"
"ds_read_b64_tr_b16 a[142:143], v200 offset:23232\n"
"v_mfma_f32_16x16x32_bf16 v[112:115], a[200:203], a[96:99], v[112:115]\n"
"ds_read_b64_tr_b16 a[144:145], v200 offset:18656\n"
"v_mfma_f32_16x16x32_bf16 v[116:119], a[200:203], a[100:103], v[116:119]\n"
"s_waitcnt lgkmcnt(11)\n"
"ds_read_b64_tr_b16 a[146:147], v200 offset:23264\n"
"v_mfma_f32_16x16x32_bf16 v[120:123], a[204:207], a[96:99], v[120:123]\n"
"ds_read_b64_tr_b16 a[148:149], v203 offset:18432\n"
"v_mfma_f32_16x16x32_bf16 v[124:127], a[204:207], a[100:103], v[124:127]\n"
"ds_read_b64_tr_b16 a[150:151], v203 offset:23040\n"
"v_mfma_f32_16x16x32_bf16 v[226:229], a[208:211], a[96:99], v[226:229]\n"
"ds_read_b64_tr_b16 a[152:153], v200 offset:27648\n"
"v_mfma_f32_16x16x32_bf16 v[230:233], a[208:211], a[100:103], v[230:233]\n"
"s_waitcnt vmcnt(26)\n"
"ds_read_b64_tr_b16 a[154:155], v200 offset:32256\n"
"buffer_load_dwordx4 v[44:47], v207, s[28:31], s20 offen offset:3072 sc1\n"
"v_mfma_f32_16x16x32_bf16 v[64:67], a[212:215], a[104:107], v[64:67]\n"
"s_add_u32 s20, s20, 4096\n"
"ds_read_b64_tr_b16 a[156:157], v200 offset:27680\n"
"v_mfma_f32_16x16x32_bf16 v[68:71], a[212:215], a[108:111], v[68:71]\n"
"ds_read_b64_tr_b16 a[158:159], v200 offset:32288\n"
"v_accvgpr_write_b32 a96, v176\n"
"v_mfma_f32_16x16x32_bf16 v[72:75], a[216:219], a[104:107], v[72:75]\n"
"ds_read_b64_tr_b16 a[192:193], v200 offset:27712\n"
"v_accvgpr_write_b32 a97, v177\n"
"v_mfma_f32_16x16x32_bf16 v[76:79], a[216:219], a[108:111], v[76:79]\n"
"s_waitcnt lgkmcnt(11)\n"
"ds_read_b64_tr_b16 a[194:195], v200 offset:32320\n"
"v_accvgpr_write_b32 a98, v178\n"
"v_mfma_f32_16x16x32_bf16 v[80:83], a[220:223], a[104:107], v[80:83]\n"
"ds_read_b64_tr_b16 a[196:197], v200 offset:27744\n"
"v_accvgpr_write_b32 a99, v179\n"
"v_mfma_f32_16x16x32_bf16 v[84:87], a[220:223], a[108:111], v[84:87]\n"
"ds_read_b64_tr_b16 a[198:199], v200 offset:32352\n"
"v_accvgpr_write_b32 a100, v180\n"
"v_mfma_f32_16x16x32_bf16 v[88:91], a[128:131], a[104:107], v[88:91]\n"
"ds_read_b64_tr_b16 a[200:201], v200 offset:27776\n"
"buffer_load_dwordx4 v[48:51], v207, s[28:31], s21 offen offset:2048 sc1\n"
"v_mfma_f32_16x16x32_bf16 v[92:95], a[128:131], a[108:111], v[92:95]\n"
"AT_pg15:\n"
"s_cbranch_execz AT_hr\n"
"ds_read_b64_tr_b16 a[202:203], v200 offset:32384\n"
"v_accvgpr_write_b32 a101, v181\n"
"v_mfma_f32_16x16x32_bf16 v[96:99], a[132:135], a[104:107], v[96:99]\n"
"ds_read_b64_tr_b16 a[204:205], v200 offset:27808\n"
"v_accvgpr_write_b32 a102, v182\n"
"v_mfma_f32_16x16x32_bf16 v[100:103], a[132:135], a[108:111], v[100:103]\n"
"ds_read_b64_tr_b16 a[206:207], v200 offset:32416\n"
"v_accvgpr_write_b32 a103, v183\n"
"v_mfma_f32_16x16x32_bf16 v[104:107], a[136:139], a[104:107], v[104:107]\n"
"ds_read_b64_tr_b16 a[208:209], v200 offset:27840\n"
"v_mfma_f32_16x16x32_bf16 v[108:111], a[136:139], a[108:111], v[108:111]\n"
"s_waitcnt lgkmcnt(11)\n"
"ds_read_b64_tr_b16 a[210:211], v200 offset:32448\n"
"v_mfma_f32_16x16x32_bf16 v[112:115], a[140:143], a[104:107], v[112:115]\n"
"ds_read_b64_tr_b16 a[212:213], v200 offset:27872\n"
"v_mfma_f32_16x16x32_bf16 v[116:119], a[140:143], a[108:111], v[116:119]\n"
"ds_read_b64_tr_b16 a[214:215], v200 offset:32480\n"
"buffer_load_dwordx4 v[52:55], v207, s[28:31], s21 offen offset:3072 sc1\n"
"v_mfma_f32_16x16x32_bf16 v[120:123], a[144:147], a[104:107], v[120:123]\n"
"s_add_u32 s21, s21, 4096\n"
"ds_read_b64_tr_b16 a[216:217], v203 offset:27648\n"
"v_mfma_f32_16x16x32_bf16 v[124:127], a[144:147], a[108:111], v[124:127]\n"
"ds_read_b64_tr_b16 a[218:219], v203 offset:32256\n"
"v_mfma_f32_16x16x32_bf16 v[226:229], a[148:151], a[104:107], v[226:229]\n"
"ds_read_b64_tr_b16 a[220:221], v200 offset:36864\n"
"v_mfma_f32_16x16x32_bf16 v[230:233], a[148:151], a[108:111], v[230:233]\n"
"s_waitcnt vmcnt(25)\n"
"ds_read_b64_tr_b16 a[222:223], v200 offset:41472\n"
"v_mfma_f32_16x16x32_bf16 v[64:67], a[152:155], a[112:115], v[64:67]\n"
"ds_read_b64_tr_b16 a[128:129], v200 offset:36896\n"
"v_mfma_f32_16x16x32_bf16 v[68:71], a[152:155], a[116:119], v[68:71]\n"
"s_waitcnt lgkmcnt(11)\n"
"ds_read_b64_tr_b16 a[130:131], v200 offset:41504\n"
"v_accvgpr_write_b32 a104, v184\n"
"v_mfma_f32_16x16x32_bf16 v[72:75], a[156:159], a[112:115], v[72:75]\n"
"ds_read_b64_tr_b16 a[132:133], v200 offset:36928\n"
"buffer_load_dwordx4 v[56:59], v207, s[28:31], s22 offen offset:2048 sc1\n"
"v_mfma_f32_16x16x32_bf16 v[76:79], a[156:159], a[116:119], v[76:79]\n"
"ds_read_b64_tr_b16 a[134:135], v200 offset:41536\n"
"v_accvgpr_write_b32 a105, v185\n"
"v_mfma_f32_16x16x32_bf16 v[80:83], a[192:195], a[112:115], v[80:83]\n"
"ds_read_b64_tr_b16 a[136:137], v200 offset:36960\n"
"v_accvgpr_write_b32 a106, v186\n"
"v_mfma_f32_16x16x32_bf16 v[84:87], a[192:195], a[116:119], v[84:87]\n"
"ds_read_b64_tr_b16 a[138:139], v200 offset:41568\n"
"v_accvgpr_write_b32 a107, v187\n"
"v_mfma_f32_16x16x32_bf16 v[88:91], a[196:199], a[112:115], v[88:91]\n"
"ds_read_b64_tr_b16 a[140:141], v200 offset:36992\n"
"v_accvgpr_write_b32 a108, v188\n"
"v_mfma_f32_16x16x32_bf16 v[92:95], a[196:199], a[116:119], v[92:95]\n"
"ds_read_b64_tr_b16 a[142:143], v200 offset:41600\n"
"v_accvgpr_write_b32 a109, v189\n"
"v_mfma_f32_16x16x32_bf16 v[96:99], a[200:203], a[112:115], v[96:99]\n"
"ds_read_b64_tr_b16 a[144:145], v200 offset:37024\n"
"v_accvgpr_write_b32 a110, v190\n"
"v_mfma_f32_16x16x32_bf16 v[100:103], a[200:203], a[116:119], v[100:103]\n"
"s_waitcnt lgkmcnt(11)\n"
"ds_read_b64_tr_b16 a[146:147], v200 offset:41632\n"
"buffer_load_dwordx4 v[60:63], v207, s[28:31], s22 offen offset:3072 sc1\n"
"v_mfma_f32_16x16x32_bf16 v[104:107], a[204:207], a[112:115], v[104:107]\n"
"s_add_u32 s22, s22, 4096\n"
"ds_read_b64_tr_b16 a[148:149], v200 offset:37056\n"
"v_accvgpr_write_b32 a111, v191\n"
"v_mfma_f32_16x16x32_bf16 v[108:111], a[204:207], a[116:119], v[108:111]\n"
"s_waitcnt vmcnt(8)\n"
"ds_read_b64_tr_b16 a[150:151], v200 offset:41664\n"
"v_mfma_f32_16x16x32_bf16 v[112:115], a[208:211], a[112:115], v[112:115]\n"
"s_barrier\n"
"ds_read_b64_tr_b16 a[152:153], v200 offset:37088\n"
"v_mfma_f32_16x16x32_bf16 v[116:119], a[208:211], a[116:119], v[116:119]\n"
"ds_read_b64_tr_b16 a[154:155], v200 offset:41696\n"
"v_mfma_f32_16x16x32_bf16 v[120:123], a[212:215], a[112:115], v[120:123]\n"
"ds_read_b64_tr_b16 a[156:157], v203 offset:36864\n"
"v_mfma_f32_16x16x32_bf16 v[124:127], a[212:215], a[116:119], v[124:127]\n"
"ds_read_b64_tr_b16 a[158:159], v203 offset:41472\n"
"v_mfma_f32_16x16x32_bf16 v[226:229], a[216:219], a[112:115], v[226:229]\n"
"ds_read_b64_tr_b16 a[192:193], v203 offset:46080\n"
"v_mfma_f32_16x16x32_bf16 v[230:233], a[216:219], a[116:119], v[230:233]\n"
"s_waitcnt lgkmcnt(11)\n"
"s_waitcnt vmcnt(25)\n"
"ds_read_b64_tr_b16 a[194:195], v203 offset:50688\n"
"v_mfma_f32_16x16x32_bf16 v[64:67], a[220:223], a[120:123], v[64:67]\n"
"ds_read_b64_tr_b16 a[196:197], v203 offset:55296\n"
"v_mfma_f32_16x16x32_bf16 v[68:71], a[220:223], a[124:127], v[68:71]\n"
"ds_read_b64_tr_b16 a[198:199], v203 offset:59904\n"
"v_accvgpr_write_b32 a112, v192\n"
"v_mfma_f32_16x16x32_bf16 v[72:75], a[128:131], a[120:123], v[72:75]\n"
"ds_read_b64_tr_b16 a[200:201], v203 offset:64512\n"
"v_accvgpr_write_b32 a113, v193\n"
"v_mfma_f32_16x16x32_bf16 v[76:79], a[128:131], a[124:127], v[76:79]\n"
"ds_read_b64_tr_b16 a[202:203], v204 offset:3584\n"
"v_accvgpr_write_b32 a114, v194\n"
"v_mfma_f32_16x16x32_bf16 v[80:83], a[132:135], a[120:123], v[80:83]\n"
"ds_read_b64_tr_b16 a[204:205], v204 offset:8192\n"
"v_accvgpr_write_b32 a115, v195\n"
"v_mfma_f32_16x16x32_bf16 v[84:87], a[132:135], a[124:127], v[84:87]\n"
"ds_read_b64_tr_b16 a[206:207], v204 offset:12800\n"
"v_accvgpr_write_b32 a116, v196\n"
"v_mfma_f32_16x16x32_bf16 v[88:91], a[136:139], a[120:123], v[88:91]\n"
"ds_read_b64_tr_b16 a[208:209], v204 offset:17408\n"
"v_accvgpr_write_b32 a117, v197\n"
"v_mfma_f32_16x16x32_bf16 v[92:95], a[136:139], a[124:127], v[92:95]\n"
"s_waitcnt lgkmcnt(11)\n"
"ds_read_b64_tr_b16 a[210:211], v204 offset:22016\n"
"v_accvgpr_write_b32 a118, v198\n"
"v_mfma_f32_16x16x32_bf16 v[96:99], a[140:143], a[120:123], v[96:99]\n"
"ds_read_b64_tr_b16 a[212:213], v204 offset:26624\n"
"v_accvgpr_write_b32 a119, v199\n"
"v_mfma_f32_16x16x32_bf16 v[100:103], a[140:143], a[124:127], v[100:103]\n"
"ds_read_b64_tr_b16 a[214:215], v204 offset:31232\n"
"v_mfma_f32_16x16x32_bf16 v[104:107], a[144:147], a[120:123], v[104:107]\n"
"ds_read_b64_tr_b16 a[216:217], v204 offset:35840\n"
"v_mfma_f32_16x16x32_bf16 v[108:111], a[144:147], a[124:127], v[108:111]\n"
"ds_read_b64_tr_b16 a[218:219], v204 offset:40448\n"
"v_mfma_f32_16x16x32_bf16 v[112:115], a[148:151], a[120:123], v[112:115]\n"
"ds_read_b64_tr_b16 a[220:221], v204 offset:45056\n"
"v_mfma_f32_16x16x32_bf16 v[116:119], a[148:151], a[124:127], v[116:119]\n"
"ds_read_b64_tr_b16 a[222:223], v204 offset:49664\n"
"v_mfma_f32_16x16x32_bf16 v[120:123], a[152:155], a[120:123], v[120:123]\n"
"ds_read_b64_tr_b16 a[128:129], v200 offset:46080\n"
"v_mfma_f32_16x16x32_bf16 v[124:127], a[152:155], a[124:127], v[124:127]\n"
"s_waitcnt lgkmcnt(11)\n"
"s_mov_b32 m0, s45\n"
"ds_read_b64_tr_b16 a[130:131], v200 offset:50688\n"
"v_mfma_f32_16x16x32_bf16 v[226:229], a[156:159], a[120:123], v[226:229]\n"
"ds_read_b64_tr_b16 a[132:133], v200 offset:55296\n"
"buffer_load_dwordx4 v207, s[16:19], s46 offen offset:0 lds\n"
"v_mfma_f32_16x16x32_bf16 v[230:233], a[156:159], a[124:127], v[230:233]\n"
"s_waitcnt vmcnt(22)\n"
"ds_read_b64_tr_b16 a[134:135], v200 offset:59904\n"
"v_mfma_f32_16x16x32_bf16 v[226:229], a[192:195], v[0:3], v[226:229]\n"
"ds_read_b64_tr_b16 a[136:137], v200 offset:64512\n"
"v_mfma_f32_16x16x32_bf16 v[230:233], a[192:195], v[4:7], v[230:233]\n"
"s_waitcnt vmcnt(18)\n"
"ds_read_b64_tr_b16 a[138:139], v201 offset:3584\n"
"v_mfma_f32_16x16x32_bf16 v[226:229], a[196:199], v[8:11], v[226:229]\n"
"s_mov_b32 m0, s45\n"
"ds_read_b64_tr_b16 a[140:141], v201 offset:8192\n"
"v_mfma_f32_16x16x32_bf16 v[230:233], a[196:199], v[12:15], v[230:233]\n"
"s_waitcnt vmcnt(14)\n"
"ds_read_b64_tr_b16 a[142:143], v201 offset:12800\n"
"buffer_load_dwordx4 v207, s[16:19], s46 offen offset:1024 lds\n"
"v_mfma_f32_16x16x32_bf16 v[226:229], a[200:203], v[16:19], v[226:229]\n"
"ds_read_b64_tr_b16 a[144:145], v201 offset:17408\n"
"v_mfma_f32_16x16x32_bf16 v[230:233], a[200:203], v[20:23], v[230:233]\n"
"s_waitcnt lgkmcnt(11)\n"
"s_waitcnt vmcnt(11)\n"
"ds_read_b64_tr_b16 a[146:147], v201 offset:22016\n"
"v_mfma_f32_16x16x32_bf16 v[226:229], a[204:207], v[24:27], v[226:229]\n"
"ds_read_b64_tr_b16 a[148:149], v201 offset:26624\n"
"v_mfma_f32_16x16x32_bf16 v[230:233], a[204:207], v[28:31], v[230:233]\n"
"s_waitcnt vmcnt(8)\n"
"ds_read_b64_tr_b16 a[150:151], v201 offset:31232\n"
"v_mfma_f32_16x16x32_bf16 v[226:229], a[208:211], v[32:35], v[226:229]\n"
"s_mov_b32 m0, s45\n"
"ds_read_b64_tr_b16 a[152:153], v201 offset:35840\n"
"v_mfma_f32_16x16x32_bf16 v[230:233], a[208:211], v[36:39], v[230:233]\n"
"s_waitcnt vmcnt(6)\n"
"ds_read_b64_tr_b16 a[154:155], v201 offset:40448\n"
"buffer_load_dwordx4 v207, s[16:19], s46 offen offset:2048 lds\n"
"v_mfma_f32_16x16x32_bf16 v[226:229], a[212:215], v[40:43], v[226:229]\n"
"ds_read_b64_tr_b16 a[156:157], v201 offset:45056\n"
"v_mfma_f32_16x16x32_bf16 v[230:233], a[212:215], v[44:47], v[230:233]\n"
"s_waitcnt vmcnt(5)\n"
"ds_read_b64_tr_b16 a[158:159], v201 offset:49664\n"
"v_mfma_f32_16x16x32_bf16 v[226:229], a[216:219], v[48:51], v[226:229]\n"
"ds_read_b64_tr_b16 a[192:193], v200 offset:46112\n"
"v_mfma_f32_16x16x32_bf16 v[230:233], a[216:219], v[52:55], v[230:233]\n"
"s_waitcnt lgkmcnt(11)\n"
"s_waitcnt vmcnt(3)\n"
"ds_read_b64_tr_b16 a[194:195], v200 offset:50720\n"
"v_mfma_f32_16x16x32_bf16 v[226:229], a[220:223], v[56:59], v[226:229]\n"
"ds_read_b64_tr_b16 a[196:197], v200 offset:55328\n"
"v_mfma_f32_16x16x32_bf16 v[230:233], a[220:223], v[60:63], v[230:233]\n"
"ds_read_b64_tr_b16 a[198:199], v200 offset:59936\n"
"v_mfma_f32_16x16x32_bf16 v[64:67], a[128:131], v[0:3], v[64:67]\n"
"ds_read_b64_tr_b16 a[200:201], v200 offset:64544\n"
"v_mfma_f32_16x16x32_bf16 v[68:71], a[128:131], v[4:7], v[68:71]\n"
"s_add_u32 m0, s84, 131072\n"
"ds_read_b64_tr_b16 a[202:203], v201 offset:3616\n"
"v_mfma_f32_16x16x32_bf16 v[64:67], a[132:135], v[8:11], v[64:67]\n"
"ds_read_b64_tr_b16 a[204:205], v201 offset:8224\n"
"buffer_load_dwordx4 v207, s[72:75], s50 offen offset:0 lds\n"
"v_mfma_f32_16x16x32_bf16 v[68:71], a[132:135], v[12:15], v[68:71]\n"
"ds_read_b64_tr_b16 a[206:207], v201 offset:12832\n"
"v_cvt_pk_bf16_f32 v218, v226, v227\n"
"v_mfma_f32_16x16x32_bf16 v[64:67], a[136:139], v[16:19], v[64:67]\n"
"ds_read_b64_tr_b16 a[208:209], v201 offset:17440\n"
"v_cvt_pk_bf16_f32 v219, v228, v229\n"
"v_mfma_f32_16x16x32_bf16 v[68:71], a[136:139], v[20:23], v[68:71]\n"
"s_waitcnt lgkmcnt(11)\n"
"ds_read_b64_tr_b16 a[210:211], v201 offset:22048\n"
"v_cvt_pk_bf16_f32 v222, v230, v231\n"
"v_mfma_f32_16x16x32_bf16 v[64:67], a[140:143], v[24:27], v[64:67]\n"
"ds_read_b64_tr_b16 a[212:213], v201 offset:26656\n"
"v_cvt_pk_bf16_f32 v223, v232, v233\n"
"v_mfma_f32_16x16x32_bf16 v[68:71], a[140:143], v[28:31], v[68:71]\n"
"ds_read_b64_tr_b16 a[214:215], v201 offset:31264\n"
"v_mfma_f32_16x16x32_bf16 v[64:67], a[144:147], v[32:35], v[64:67]\n"
"s_add_u32 m0, s84, 131072\n"
"ds_read_b64_tr_b16 a[216:217], v201 offset:35872\n"
"buffer_store_dwordx2 v[218:219], v207, s[28:31], s49 offen sc1\n"
"v_mfma_f32_16x16x32_bf16 v[68:71], a[144:147], v[36:39], v[68:71]\n"
"ds_read_b64_tr_b16 a[218:219], v201 offset:40480\n"
"buffer_load_dwordx4 v207, s[72:75], s50 offen offset:1024 lds\n"
"v_mfma_f32_16x16x32_bf16 v[64:67], a[148:151], v[40:43], v[64:67]\n"
"ds_read_b64_tr_b16 a[220:221], v201 offset:45088\n"
"buffer_store_dwordx2 v[222:223], v208, s[28:31], s49 offen sc1\n"
"v_mfma_f32_16x16x32_bf16 v[68:71], a[148:151], v[44:47], v[68:71]\n"
"ds_read_b64_tr_b16 a[222:223], v201 offset:49696\n"
"v_mfma_f32_16x16x32_bf16 v[64:67], a[152:155], v[48:51], v[64:67]\n"
"ds_read_b64_tr_b16 a[128:129], v200 offset:46144\n"
"v_mfma_f32_16x16x32_bf16 v[68:71], a[152:155], v[52:55], v[68:71]\n"
"s_waitcnt lgkmcnt(11)\n"
"ds_read_b64_tr_b16 a[130:131], v200 offset:50752\n"
"v_mfma_f32_16x16x32_bf16 v[64:67], a[156:159], v[56:59], v[64:67]\n"
"s_add_u32 m0, s84, 131072\n"
"ds_read_b64_tr_b16 a[132:133], v200 offset:55360\n"
"v_mfma_f32_16x16x32_bf16 v[68:71], a[156:159], v[60:63], v[68:71]\n"
"ds_read_b64_tr_b16 a[134:135], v200 offset:59968\n"
"buffer_load_dwordx4 v207, s[72:75], s50 offen offset:2048 lds\n"
"v_mfma_f32_16x16x32_bf16 v[72:75], a[192:195], v[0:3], v[72:75]\n"
"ds_read_b64_tr_b16 a[136:137], v200 offset:64576\n"
"v_mfma_f32_16x16x32_bf16 v[76:79], a[192:195], v[4:7], v[76:79]\n"
"ds_read_b64_tr_b16 a[138:139], v201 offset:3648\n"
"v_mfma_f32_16x16x32_bf16 v[72:75], a[196:199], v[8:11], v[72:75]\n"
"ds_read_b64_tr_b16 a[140:141], v201 offset:8256\n"
"v_mfma_f32_16x16x32_bf16 v[76:79], a[196:199], v[12:15], v[76:79]\n"
"ds_read_b64_tr_b16 a[142:143], v201 offset:12864\n"
"v_cvt_pk_bf16_f32 v218, v64, v65\n"
"v_mfma_f32_16x16x32_bf16 v[72:75], a[200:203], v[16:19], v[72:75]\n"
"ds_read_b64_tr_b16 a[144:145], v201 offset:17472\n"
"v_cvt_pk_bf16_f32 v219, v66, v67\n"
"v_mfma_f32_16x16x32_bf16 v[76:79], a[200:203], v[20:23], v[76:79]\n"
"s_waitcnt lgkmcnt(11)\n"
"ds_read_b64_tr_b16 a[146:147], v201 offset:22080\n"
"v_cvt_pk_bf16_f32 v222, v68, v69\n"
"v_mfma_f32_16x16x32_bf16 v[72:75], a[204:207], v[24:27], v[72:75]\n"
"s_add_u32 m0, s84, 131072\n"
"ds_read_b64_tr_b16 a[148:149], v201 offset:26688\n"
"v_cvt_pk_bf16_f32 v223, v70, v71\n"
"v_mfma_f32_16x16x32_bf16 v[76:79], a[204:207], v[28:31], v[76:79]\n"
"ds_read_b64_tr_b16 a[150:151], v201 offset:31296\n"
"buffer_load_dwordx4 v207, s[72:75], s50 offen offset:3072 lds\n"
"v_mfma_f32_16x16x32_bf16 v[72:75], a[208:211], v[32:35], v[72:75]\n"
"ds_read_b64_tr_b16 a[152:153], v201 offset:35904\n"
"v_mfma_f32_16x16x32_bf16 v[76:79], a[208:211], v[36:39], v[76:79]\n"
"ds_read_b64_tr_b16 a[154:155], v201 offset:40512\n"
"v_mfma_f32_16x16x32_bf16 v[72:75], a[212:215], v[40:43], v[72:75]\n"
"ds_read_b64_tr_b16 a[156:157], v201 offset:45120\n"
"v_mfma_f32_16x16x32_bf16 v[76:79], a[212:215], v[44:47], v[76:79]\n"
"ds_read_b64_tr_b16 a[158:159], v201 offset:49728\n"
"v_mfma_f32_16x16x32_bf16 v[72:75], a[216:219], v[48:51], v[72:75]\n"
"s_add_u32 m0, s84, 135168\n"
"s_add_u32 s50, s50, 4096\n"
"ds_read_b64_tr_b16 a[192:193], v200 offset:46176\n"
"v_mfma_f32_16x16x32_bf16 v[76:79], a[216:219], v[52:55], v[76:79]\n"
"s_waitcnt lgkmcnt(11)\n"
"ds_read_b64_tr_b16 a[194:195], v200 offset:50784\n"
"buffer_load_dwordx4 v207, s[72:75], s50 offen offset:0 lds\n"
"v_mfma_f32_16x16x32_bf16 v[72:75], a[220:223], v[56:59], v[72:75]\n"
"ds_read_b64_tr_b16 a[196:197], v200 offset:55392\n"
"v_mfma_f32_16x16x32_bf16 v[76:79], a[220:223], v[60:63], v[76:79]\n"
"ds_read_b64_tr_b16 a[198:199], v200 offset:60000\n"
"v_mfma_f32_16x16x32_bf16 v[80:83], a[128:131], v[0:3], v[80:83]\n"
"ds_read_b64_tr_b16 a[200:201], v200 offset:64608\n"
"v_mfma_f32_16x16x32_bf16 v[84:87], a[128:131], v[4:7], v[84:87]\n"
"ds_read_b64_tr_b16 a[202:203], v201 offset:3680\n"
"v_mfma_f32_16x16x32_bf16 v[80:83], a[132:135], v[8:11], v[80:83]\n"
"ds_read_b64_tr_b16 a[204:205], v201 offset:8288\n"
"v_mfma_f32_16x16x32_bf16 v[84:87], a[132:135], v[12:15], v[84:87]\n"
"ds_read_b64_tr_b16 a[206:207], v201 offset:12896\n"
"v_cvt_pk_bf16_f32 v220, v72, v73\n"
"v_mfma_f32_16x16x32_bf16 v[80:83], a[136:139], v[16:19], v[80:83]\n"
"ds_read_b64_tr_b16 a[208:209], v201 offset:17504\n"
"v_cvt_pk_bf16_f32 v221, v74, v75\n"
"v_mfma_f32_16x16x32_bf16 v[84:87], a[136:139], v[20:23], v[84:87]\n"
"s_waitcnt lgkmcnt(11)\n"
"ds_read_b64_tr_b16 a[210:211], v201 offset:22112\n"
"v_cvt_pk_bf16_f32 v224, v76, v77\n"
"v_mfma_f32_16x16x32_bf16 v[80:83], a[140:143], v[24:27], v[80:83]\n"
"s_add_u32 m0, s84, 135168\n"
"ds_read_b64_tr_b16 a[212:213], v201 offset:26720\n"
"v_cvt_pk_bf16_f32 v225, v78, v79\n"
"v_mfma_f32_16x16x32_bf16 v[84:87], a[140:143], v[28:31], v[84:87]\n"
"ds_read_b64_tr_b16 a[214:215], v201 offset:31328\n"
"buffer_load_dwordx4 v207, s[72:75], s50 offen offset:1024 lds\n"
"v_mfma_f32_16x16x32_bf16 v[80:83], a[144:147], v[32:35], v[80:83]\n"
"ds_read_b64_tr_b16 a[216:217], v201 offset:35936\n"
"buffer_store_dwordx4 v[218:221], v207, s[28:31], s54 offen offset:0 sc1\n"
"v_mfma_f32_16x16x32_bf16 v[84:87], a[144:147], v[36:39], v[84:87]\n"
"ds_read_b64_tr_b16 a[218:219], v201 offset:40544\n"
"v_mfma_f32_16x16x32_bf16 v[80:83], a[148:151], v[40:43], v[80:83]\n"
"ds_read_b64_tr_b16 a[220:221], v201 offset:45152\n"
"buffer_store_dwordx4 v[222:225], v208, s[28:31], s54 offen offset:0 sc1\n"
"v_mfma_f32_16x16x32_bf16 v[84:87], a[148:151], v[44:47], v[84:87]\n"
"ds_read_b64_tr_b16 a[222:223], v201 offset:49760\n"
"v_mfma_f32_16x16x32_bf16 v[80:83], a[152:155], v[48:51], v[80:83]\n"
"s_add_u32 m0, s84, 135168\n"
"ds_read_b64_tr_b16 a[128:129], v200 offset:46208\n"
"v_mfma_f32_16x16x32_bf16 v[84:87], a[152:155], v[52:55], v[84:87]\n"
"s_waitcnt lgkmcnt(11)\n"
"ds_read_b64_tr_b16 a[130:131], v200 offset:50816\n"
"buffer_load_dwordx4 v207, s[72:75], s50 offen offset:2048 lds\n"
"v_mfma_f32_16x16x32_bf16 v[80:83], a[156:159], v[56:59], v[80:83]\n"
"ds_read_b64_tr_b16 a[132:133], v200 offset:55424\n"
"v_mfma_f32_16x16x32_bf16 v[84:87], a[156:159], v[60:63], v[84:87]\n"
"ds_read_b64_tr_b16 a[134:135], v200 offset:60032\n"
"v_mfma_f32_16x16x32_bf16 v[88:91], a[192:195], v[0:3], v[88:91]\n"
"ds_read_b64_tr_b16 a[136:137], v200 offset:64640\n"
"v_mfma_f32_16x16x32_bf16 v[92:95], a[192:195], v[4:7], v[92:95]\n"
"ds_read_b64_tr_b16 a[138:139], v201 offset:3712\n"
"v_mfma_f32_16x16x32_bf16 v[88:91], a[196:199], v[8:11], v[88:91]\n"
"ds_read_b64_tr_b16 a[140:141], v201 offset:8320\n"
"v_mfma_f32_16x16x32_bf16 v[92:95], a[196:199], v[12:15], v[92:95]\n"
"ds_read_b64_tr_b16 a[142:143], v201 offset:12928\n"
"v_cvt_pk_bf16_f32 v218, v80, v81\n"
"v_mfma_f32_16x16x32_bf16 v[88:91], a[200:203], v[16:19], v[88:91]\n"
"ds_read_b64_tr_b16 a[144:145], v201 offset:17536\n"
"v_cvt_pk_bf16_f32 v219, v82, v83\n"
"v_mfma_f32_16x16x32_bf16 v[92:95], a[200:203], v[20:23], v[92:95]\n"
"s_waitcnt lgkmcnt(11)\n"
"ds_read_b64_tr_b16 a[146:147], v201 offset:22144\n"
"v_cvt_pk_bf16_f32 v222, v84, v85\n"
"v_mfma_f32_16x16x32_bf16 v[88:91], a[204:207], v[24:27], v[88:91]\n"
"ds_read_b64_tr_b16 a[148:149], v201 offset:26752\n"
"v_cvt_pk_bf16_f32 v223, v86, v87\n"
"v_mfma_f32_16x16x32_bf16 v[92:95], a[204:207], v[28:31], v[92:95]\n"
"ds_read_b64_tr_b16 a[150:151], v201 offset:31360\n"
"v_mfma_f32_16x16x32_bf16 v[88:91], a[208:211], v[32:35], v[88:91]\n"
"ds_read_b64_tr_b16 a[152:153], v201 offset:35968\n"
"v_mfma_f32_16x16x32_bf16 v[92:95], a[208:211], v[36:39], v[92:95]\n"
"ds_read_b64_tr_b16 a[154:155], v201 offset:40576\n"
"v_mfma_f32_16x16x32_bf16 v[88:91], a[212:215], v[40:43], v[88:91]\n"
"ds_read_b64_tr_b16 a[156:157], v201 offset:45184\n"
"v_mfma_f32_16x16x32_bf16 v[92:95], a[212:215], v[44:47], v[92:95]\n"
"ds_read_b64_tr_b16 a[158:159], v201 offset:49792\n"
"v_mfma_f32_16x16x32_bf16 v[88:91], a[216:219], v[48:51], v[88:91]\n"
"ds_read_b64_tr_b16 a[192:193], v200 offset:46240\n"
"v_mfma_f32_16x16x32_bf16 v[92:95], a[216:219], v[52:55], v[92:95]\n"
"s_waitcnt lgkmcnt(11)\n"
"ds_read_b64_tr_b16 a[194:195], v200 offset:50848\n"
"v_mfma_f32_16x16x32_bf16 v[88:91], a[220:223], v[56:59], v[88:91]\n"
"ds_read_b64_tr_b16 a[196:197], v200 offset:55456\n"
"v_mfma_f32_16x16x32_bf16 v[92:95], a[220:223], v[60:63], v[92:95]\n"
"ds_read_b64_tr_b16 a[198:199], v200 offset:60064\n"
"v_mfma_f32_16x16x32_bf16 v[96:99], a[128:131], v[0:3], v[96:99]\n"
"ds_read_b64_tr_b16 a[200:201], v200 offset:64672\n"
"v_mfma_f32_16x16x32_bf16 v[100:103], a[128:131], v[4:7], v[100:103]\n"
"ds_read_b64_tr_b16 a[202:203], v201 offset:3744\n"
"v_mfma_f32_16x16x32_bf16 v[96:99], a[132:135], v[8:11], v[96:99]\n"
"ds_read_b64_tr_b16 a[204:205], v201 offset:8352\n"
"v_mfma_f32_16x16x32_bf16 v[100:103], a[132:135], v[12:15], v[100:103]\n"
"ds_read_b64_tr_b16 a[206:207], v201 offset:12960\n"
"v_cvt_pk_bf16_f32 v220, v88, v89\n"
"v_mfma_f32_16x16x32_bf16 v[96:99], a[136:139], v[16:19], v[96:99]\n"
"ds_read_b64_tr_b16 a[208:209], v201 offset:17568\n"
"v_cvt_pk_bf16_f32 v221, v90, v91\n"
"v_mfma_f32_16x16x32_bf16 v[100:103], a[136:139], v[20:23], v[100:103]\n"
"s_waitcnt lgkmcnt(11)\n"
"ds_read_b64_tr_b16 a[210:211], v201 offset:22176\n"
"v_cvt_pk_bf16_f32 v224, v92, v93\n"
"v_mfma_f32_16x16x32_bf16 v[96:99], a[140:143], v[24:27], v[96:99]\n"
"ds_read_b64_tr_b16 a[212:213], v201 offset:26784\n"
"v_cvt_pk_bf16_f32 v225, v94, v95\n"
"v_mfma_f32_16x16x32_bf16 v[100:103], a[140:143], v[28:31], v[100:103]\n"
"ds_read_b64_tr_b16 a[214:215], v201 offset:31392\n"
"v_mfma_f32_16x16x32_bf16 v[96:99], a[144:147], v[32:35], v[96:99]\n"
"ds_read_b64_tr_b16 a[216:217], v201 offset:36000\n"
"buffer_store_dwordx4 v[218:221], v207, s[28:31], s54 offen offset:1024 sc1\n"
"v_mfma_f32_16x16x32_bf16 v[100:103], a[144:147], v[36:39], v[100:103]\n"
"ds_read_b64_tr_b16 a[218:219], v201 offset:40608\n"
"buffer_store_dword v252, v251, s[28:31], 0 offen sc1\n"
"v_mfma_f32_16x16x32_bf16 v[96:99], a[148:151], v[40:43], v[96:99]\n"
"ds_read_b64_tr_b16 a[220:221], v201 offset:45216\n"
"buffer_store_dwordx4 v[222:225], v208, s[28:31], s54 offen offset:1024 sc1\n"
"v_mfma_f32_16x16x32_bf16 v[100:103], a[148:151], v[44:47], v[100:103]\n"
"ds_read_b64_tr_b16 a[222:223], v201 offset:49824\n"
"v_mfma_f32_16x16x32_bf16 v[96:99], a[152:155], v[48:51], v[96:99]\n"
"ds_read_b64_tr_b16 a[128:129], v200 offset:46272\n"
"v_mfma_f32_16x16x32_bf16 v[100:103], a[152:155], v[52:55], v[100:103]\n"
"s_waitcnt lgkmcnt(11)\n"
"ds_read_b64_tr_b16 a[130:131], v200 offset:50880\n"
"v_mfma_f32_16x16x32_bf16 v[96:99], a[156:159], v[56:59], v[96:99]\n"
"ds_read_b64_tr_b16 a[132:133], v200 offset:55488\n"
"v_mfma_f32_16x16x32_bf16 v[100:103], a[156:159], v[60:63], v[100:103]\n"
"ds_read_b64_tr_b16 a[134:135], v200 offset:60096\n"
"v_mfma_f32_16x16x32_bf16 v[104:107], a[192:195], v[0:3], v[104:107]\n"
"ds_read_b64_tr_b16 a[136:137], v200 offset:64704\n"
"v_mfma_f32_16x16x32_bf16 v[108:111], a[192:195], v[4:7], v[108:111]\n"
"ds_read_b64_tr_b16 a[138:139], v201 offset:3776\n"
"v_mfma_f32_16x16x32_bf16 v[104:107], a[196:199], v[8:11], v[104:107]\n"
"ds_read_b64_tr_b16 a[140:141], v201 offset:8384\n"
"v_mfma_f32_16x16x32_bf16 v[108:111], a[196:199], v[12:15], v[108:111]\n"
"ds_read_b64_tr_b16 a[142:143], v201 offset:12992\n"
"v_cvt_pk_bf16_f32 v218, v96, v97\n"
"v_mfma_f32_16x16x32_bf16 v[104:107], a[200:203], v[16:19], v[104:107]\n"
"ds_read_b64_tr_b16 a[144:145], v201 offset:17600\n"
"v_cvt_pk_bf16_f32 v219, v98, v99\n"
"v_mfma_f32_16x16x32_bf16 v[108:111], a[200:203], v[20:23], v[108:111]\n"
"s_waitcnt lgkmcnt(11)\n"
"ds_read_b64_tr_b16 a[146:147], v201 offset:22208\n"
"v_cvt_pk_bf16_f32 v222, v100, v101\n"
"v_mfma_f32_16x16x32_bf16 v[104:107], a[204:207], v[24:27], v[104:107]\n"
"ds_read_b64_tr_b16 a[148:149], v201 offset:26816\n"
"v_cvt_pk_bf16_f32 v223, v102, v103\n"
"v_mfma_f32_16x16x32_bf16 v[108:111], a[204:207], v[28:31], v[108:111]\n"
"s_waitcnt vmcnt(14)\n"
"ds_read_b64_tr_b16 a[150:151], v201 offset:31424\n"
"v_mfma_f32_16x16x32_bf16 v[104:107], a[208:211], v[32:35], v[104:107]\n"
"s_barrier\n"
"ds_read_b64_tr_b16 a[152:153], v201 offset:36032\n"
"v_mfma_f32_16x16x32_bf16 v[108:111], a[208:211], v[36:39], v[108:111]\n"
"ds_read_b64_tr_b16 a[154:155], v201 offset:40640\n"
"v_mfma_f32_16x16x32_bf16 v[104:107], a[212:215], v[40:43], v[104:107]\n"
"ds_read_b64_tr_b16 a[156:157], v201 offset:45248\n"
"v_mfma_f32_16x16x32_bf16 v[108:111], a[212:215], v[44:47], v[108:111]\n"
"ds_read_b64_tr_b16 a[158:159], v201 offset:49856\n"
"v_mfma_f32_16x16x32_bf16 v[104:107], a[216:219], v[48:51], v[104:107]\n"
"ds_read_b64_tr_b16 a[192:193], v200 offset:46304\n"
"v_mfma_f32_16x16x32_bf16 v[108:111], a[216:219], v[52:55], v[108:111]\n"
"s_waitcnt lgkmcnt(11)\n"
"ds_read_b64_tr_b16 a[194:195], v200 offset:50912\n"
"v_mfma_f32_16x16x32_bf16 v[104:107], a[220:223], v[56:59], v[104:107]\n"
"ds_read_b64_tr_b16 a[196:197], v200 offset:55520\n"
"v_mfma_f32_16x16x32_bf16 v[108:111], a[220:223], v[60:63], v[108:111]\n"
"ds_read_b64_tr_b16 a[198:199], v200 offset:60128\n"
"v_mfma_f32_16x16x32_bf16 v[112:115], a[128:131], v[0:3], v[112:115]\n"
"ds_read_b64_tr_b16 a[200:201], v200 offset:64736\n"
"v_mfma_f32_16x16x32_bf16 v[116:119], a[128:131], v[4:7], v[116:119]\n"
"ds_read_b64_tr_b16 a[202:203], v201 offset:3808\n"
"v_mfma_f32_16x16x32_bf16 v[112:115], a[132:135], v[8:11], v[112:115]\n"
"ds_read_b64_tr_b16 a[204:205], v201 offset:8416\n"
"v_mfma_f32_16x16x32_bf16 v[116:119], a[132:135], v[12:15], v[116:119]\n"
"ds_read_b64_tr_b16 a[206:207], v201 offset:13024\n"
"v_cvt_pk_bf16_f32 v220, v104, v105\n"
"v_mfma_f32_16x16x32_bf16 v[112:115], a[136:139], v[16:19], v[112:115]\n"
"ds_read_b64_tr_b16 a[208:209], v201 offset:17632\n"
"v_cvt_pk_bf16_f32 v221, v106, v107\n"
"v_mfma_f32_16x16x32_bf16 v[116:119], a[136:139], v[20:23], v[116:119]\n"
"s_waitcnt lgkmcnt(11)\n"
"ds_read_b64_tr_b16 a[210:211], v201 offset:22240\n"
"v_cvt_pk_bf16_f32 v224, v108, v109\n"
"v_mfma_f32_16x16x32_bf16 v[112:115], a[140:143], v[24:27], v[112:115]\n"
"ds_read_b64_tr_b16 a[212:213], v201 offset:26848\n"
"v_cvt_pk_bf16_f32 v225, v110, v111\n"
"v_mfma_f32_16x16x32_bf16 v[116:119], a[140:143], v[28:31], v[116:119]\n"
"ds_read_b64_tr_b16 a[214:215], v201 offset:31456\n"
"v_mfma_f32_16x16x32_bf16 v[112:115], a[144:147], v[32:35], v[112:115]\n"
"ds_read_b64_tr_b16 a[216:217], v201 offset:36064\n"
"buffer_store_dwordx4 v[218:221], v207, s[28:31], s54 offen offset:2048 sc1\n"
"v_mfma_f32_16x16x32_bf16 v[116:119], a[144:147], v[36:39], v[116:119]\n"
"ds_read_b64_tr_b16 a[218:219], v201 offset:40672\n"
"v_mfma_f32_16x16x32_bf16 v[112:115], a[148:151], v[40:43], v[112:115]\n"
"ds_read_b64_tr_b16 a[220:221], v201 offset:45280\n"
"buffer_store_dwordx4 v[222:225], v208, s[28:31], s54 offen offset:2048 sc1\n"
"v_mfma_f32_16x16x32_bf16 v[116:119], a[148:151], v[44:47], v[116:119]\n"
"ds_read_b64_tr_b16 a[222:223], v201 offset:49888\n"
"v_mfma_f32_16x16x32_bf16 v[112:115], a[152:155], v[48:51], v[112:115]\n"
"s_nop 0\n"
"v_mfma_f32_16x16x32_bf16 v[116:119], a[152:155], v[52:55], v[116:119]\n"
"s_waitcnt lgkmcnt(10)\n"
"v_mfma_f32_16x16x32_bf16 v[112:115], a[156:159], v[56:59], v[112:115]\n"
"s_nop 0\n"
"v_mfma_f32_16x16x32_bf16 v[116:119], a[156:159], v[60:63], v[116:119]\n"
"s_nop 0\n"
"v_mfma_f32_16x16x32_bf16 v[120:123], a[192:195], v[0:3], v[120:123]\n"
"s_nop 0\n"
"v_mfma_f32_16x16x32_bf16 v[124:127], a[192:195], v[4:7], v[124:127]\n"
"s_nop 0\n"
"v_mfma_f32_16x16x32_bf16 v[120:123], a[196:199], v[8:11], v[120:123]\n"
"s_nop 0\n"
"v_mfma_f32_16x16x32_bf16 v[124:127], a[196:199], v[12:15], v[124:127]\n"
"v_cvt_pk_bf16_f32 v218, v112, v113\n"
"v_mfma_f32_16x16x32_bf16 v[120:123], a[200:203], v[16:19], v[120:123]\n"
"v_cvt_pk_bf16_f32 v219, v114, v115\n"
"v_mfma_f32_16x16x32_bf16 v[124:127], a[200:203], v[20:23], v[124:127]\n"
"s_waitcnt lgkmcnt(2)\n"
"v_cvt_pk_bf16_f32 v222, v116, v117\n"
"v_mfma_f32_16x16x32_bf16 v[120:123], a[204:207], v[24:27], v[120:123]\n"
"v_cvt_pk_bf16_f32 v223, v118, v119\n"
"v_mfma_f32_16x16x32_bf16 v[124:127], a[204:207], v[28:31], v[124:127]\n"
"s_nop 0\n"
"v_mfma_f32_16x16x32_bf16 v[120:123], a[208:211], v[32:35], v[120:123]\n"
"s_nop 0\n"
"v_mfma_f32_16x16x32_bf16 v[124:127], a[208:211], v[36:39], v[124:127]\n"
"s_nop 0\n"
"v_mfma_f32_16x16x32_bf16 v[120:123], a[212:215], v[40:43], v[120:123]\n"
"s_nop 0\n"
"v_mfma_f32_16x16x32_bf16 v[124:127], a[212:215], v[44:47], v[124:127]\n"
"s_nop 0\n"
"v_mfma_f32_16x16x32_bf16 v[120:123], a[216:219], v[48:51], v[120:123]\n"
"s_nop 0\n"
"v_mfma_f32_16x16x32_bf16 v[124:127], a[216:219], v[52:55], v[124:127]\n"
"s_waitcnt lgkmcnt(0)\n"
"v_mfma_f32_16x16x32_bf16 v[120:123], a[220:223], v[56:59], v[120:123]\n"
"s_nop 0\n"
"v_mfma_f32_16x16x32_bf16 v[124:127], a[220:223], v[60:63], v[124:127]\n"
"s_nop 0\n"
"s_nop 1\n"
"s_nop 1\n"
"s_nop 1\n"
"s_nop 1\n"
"v_cvt_pk_bf16_f32 v220, v120, v121\n"
"s_nop 1\n"
"v_cvt_pk_bf16_f32 v221, v122, v123\n"
"s_nop 1\n"
"v_cvt_pk_bf16_f32 v224, v124, v125\n"
"s_nop 1\n"
"v_cvt_pk_bf16_f32 v225, v126, v127\n"
"s_nop 1\n"
"s_nop 1\n"
"buffer_store_dwordx4 v[218:221], v207, s[28:31], s54 offen offset:3072 sc1\n"
"s_nop 1\n"
"s_nop 1\n"
"buffer_store_dwordx4 v[222:225], v208, s[28:31], s54 offen offset:3072 sc1\n"
"s_nop 1\n"
"s_add_u32 s56, s40, 161024\n"
"s_nop 1\n"
"v_mov_b32 v227, s56\n"
"v_mov_b32 v224, 0x3f80\n"
"v_mov_b32 v225, 0x3f800000\n"
"v_and_b32 v226, 7, v248\n"
"v_lshl_add_u32 v226, v226, 4, v227\n"
"s_mov_b32 s14, 0xfffff800\n"
"s_cmp_lg_u32 s41, 3\n"
"s_cbranch_scc1 AT_A0\n"
"s_mov_b64 s[8:9], 0\n"
"s_mov_b64 exec, 0\n"
"s_call_b64 %[kp], AT_pg17\n"
"s_mov_b64 exec, -1\n"
"s_mul_i32 s63, s41, 9216\n"
"s_add_u32 s63, s63, s40\n"
"s_add_u32 s63, s63, 9216\n"
"v_lshrrev_b32 v185, 2, v248\n"
"v_and_b32 v186, 3, v248\n"
"v_mul_u32_u24 v187, 96, v185\n"
"v_lshl_add_u32 v188, v186, 4, s63\n"
"v_lshl_add_u32 v189, v187, 1, v188\n"
"v_add_u32 v188, v187, v188\n"
"v_lshrrev_b32 v190, 4, v248\n"
"v_mul_u32_u24 v191, 384, v190\n"
"v_add_u32 v187, v187, v191\n"
"v_lshl_add_u32 v187, v186, 3, v187\n"
"v_add_u32 v187, s63, v187\n"
"s_mov_b32 exec_lo, 0x77777777\n"
"s_mov_b32 exec_hi, 0x77777777\n"
"ds_write_b128 v189, a[48:51] offset:0\n"
"ds_write_b128 v189, a[84:87] offset:48\n"
"ds_write_b128 v189, a[52:55] offset:96\n"
"ds_write_b128 v189, a[88:91] offset:144\n"
"ds_write_b128 v189, a[56:59] offset:3072\n"
"ds_write_b128 v189, a[92:95] offset:3120\n"
"ds_write_b128 v189, a[60:63] offset:3168\n"
"ds_write_b128 v189, a[96:99] offset:3216\n"
"ds_write_b128 v189, a[64:67] offset:6144\n"
"ds_write_b128 v189, a[100:103] offset:6192\n"
"ds_write_b128 v189, a[68:71] offset:6240\n"
"ds_write_b128 v189, a[104:107] offset:6288\n"
"s_mov_b64 exec, -1\n"
"AT_pg16:\n"
"s_cbranch_execz AT_hr\n"
"ds_read_b64_tr_b16 a[128:129], v187 offset:0\n"
"ds_read_b64_tr_b16 a[130:131], v187 offset:384\n"
"ds_read_b64_tr_b16 a[132:133], v187 offset:32\n"
"ds_read_b64_tr_b16 a[134:135], v187 offset:416\n"
"ds_read_b64_tr_b16 a[136:137], v187 offset:64\n"
"ds_read_b64_tr_b16 a[138:139], v187 offset:448\n"
"ds_read_b64_tr_b16 a[140:141], v187 offset:3072\n"
"ds_read_b64_tr_b16 a[142:143], v187 offset:3456\n"
"ds_read_b64_tr_b16 a[144:145], v187 offset:3104\n"
"ds_read_b64_tr_b16 a[146:147], v187 offset:3488\n"
"ds_read_b64_tr_b16 a[148:149], v187 offset:3136\n"
"ds_read_b64_tr_b16 a[150:151], v187 offset:3520\n"
"ds_read_b64_tr_b16 a[152:153], v187 offset:6144\n"
"ds_read_b64_tr_b16 a[154:155], v187 offset:6528\n"
"ds_read_b64_tr_b16 a[156:157], v187 offset:6176\n"
"ds_read_b64_tr_b16 a[158:159], v187 offset:6560\n"
"ds_read_b64_tr_b16 a[160:161], v187 offset:6208\n"
"ds_read_b64_tr_b16 a[162:163], v187 offset:6592\n"
"s_mov_b32 exec_lo, 0x77777777\n"
"s_mov_b32 exec_hi, 0x77777777\n"
"ds_write_b128 v189, a[72:75] offset:0\n"
"ds_write_b128 v189, a[108:111] offset:48\n"
"ds_write_b128 v189, a[76:79] offset:96\n"
"ds_write_b128 v189, a[112:115] offset:144\n"
"ds_write_b128 v188, a[80:83] offset:3072\n"
"ds_write_b128 v188, a[116:119] offset:3120\n"
"s_mov_b64 exec, -1\n"
"ds_read_b64_tr_b16 a[164:165], v187 offset:0\n"
"ds_read_b64_tr_b16 a[166:167], v187 offset:384\n"
"ds_read_b64_tr_b16 a[168:169], v187 offset:32\n"
"ds_read_b64_tr_b16 a[170:171], v187 offset:416\n"
"ds_read_b64_tr_b16 a[172:173], v187 offset:64\n"
"ds_read_b64_tr_b16 a[174:175], v187 offset:448\n"
"ds_read_b64_tr_b16 a[176:177], v187 offset:3072\n"
"ds_read_b64_tr_b16 a[178:179], v187 offset:3456\n"
"ds_read_b64_tr_b16 a[180:181], v187 offset:3104\n"
"ds_read_b64_tr_b16 a[182:183], v187 offset:3488\n"
"ds_read_b64_tr_b16 a[184:185], v187 offset:3136\n"
"ds_read_b64_tr_b16 a[186:187], v187 offset:3520\n"
"v_and_b32 v241, 1, v248\n"
"v_mul_u32_u24 v241, 1152, v241\n"
"v_lshl_add_u32 v241, v190, 4, v241\n"
"s_mul_i32 s63, s41, 288\n"
"v_add_u32 v241, s63, v241\n"
"v_and_b32 v242, 10, v248\n"
"v_lshl_add_u32 v241, v242, 26, v241\n"
"v_and_b32 v242, 32, v248\n"
"v_lshlrev_b32 v242, 25, v242\n"
"s_add_u32 s63, s63, s40\n"
"s_add_u32 s63, s63, 131072\n"
"v_cmp_eq_u32 vcc, 3, v190\n"
"v_and_b32 v185, 3, v185\n"
"v_lshlrev_b32 v191, 3, v190\n"
"v_cndmask_b32_e64 v191, v191, 0, vcc\n"
"v_add_u32 v191, v191, v185\n"
"v_mul_u32_u24 v191, 1152, v191\n"
"v_lshl_add_u32 v191, v186, 3, v191\n"
"v_add_u32 v255, s63, v191\n"
"v_lshrrev_b32 v243, 3, v248\n"
"v_mul_u32_u24 v243, 24576, v243\n"
"v_and_b32 v247, 7, v248\n"
"v_lshl_add_u32 v243, v247, 4, v243\n"
"v_mul_u32_u24 v224, 24576, v190\n"
"v_and_b32 v246, 15, v248\n"
"v_lshl_add_u32 v224, v246, 3, v224\n"
"v_or_b32 v224, v242, v224\n"
"v_subrev_u32 v252, 65536, v227\n"
"s_mul_i32 s63, s41, 384\n"
"v_mul_u32_u24 v244, 192, v190\n"
"v_lshl_add_u32 v244, v246, 2, v244\n"
"v_add3_u32 v244, v244, v252, s63\n"
"v_cmp_lt_u32 vcc, 23, v248\n"
"v_lshl_add_u32 v249, v248, 2, v252\n"
"v_add_u32 v185, 96, v249\n"
"v_cndmask_b32 v249, v249, v185, vcc\n"
"s_lshl_b32 s63, s41, 9\n"
"v_add_u32 v252, s63, v252\n"
"v_lshl_add_u32 v251, v248, 1, v252\n"
"v_add_u32 v185, 208, v251\n"
"v_cndmask_b32 v251, v251, v185, vcc\n"
"v_lshl_add_u32 v185, v248, 3, v252\n"
"v_lshl_add_u32 v252, v247, 6, v252\n"
"v_lshl_add_u32 v252, v190, 4, v252\n"
"s_min_u32 s63, s41, 2\n"
"s_mul_i32 s63, s63, 192\n"
"v_add_u32 v183, s63, v248\n"
"v_mul_u32_u24 v183, 911, v183\n"
"s_lshl_b32 s63, s55, 3\n"
"v_lshrrev_b32 v180, 16, v183\n"
"v_add_u32 v180, s63, v180\n"
"v_add_u32 v183, 58304, v183\n"
"v_lshrrev_b32 v181, 16, v183\n"
"v_add_u32 v181, s63, v181\n"
"v_add_u32 v183, 58304, v183\n"
"v_lshrrev_b32 v182, 16, v183\n"
"v_add_u32 v182, s63, v182\n"
"v_lshlrev_b32 v250, 4, v248\n"
"v_add_u32 v184, s45, v250\n"
"s_cmp_eq_u32 s41, 3\n"
"s_cselect_b32 s63, 0x10000000, 0\n"
"v_or_b32 v250, s63, v250\n"
"ds_read_b128 v[212:215], v227 offset:512\n"
"ds_read_b128 v[216:219], v227 offset:528\n"
"s_lshl_b32 s56, s55, 3\n"
"s_sub_u32 s56, 2047, s56\n"
"v_and_b32 v220, 7, v248\n"
"v_sub_u32 v220, s56, v220\n"
"s_waitcnt lgkmcnt(0)\n"
"v_add_f32 v212, v212, v214\n"
"v_add_f32 v213, v213, v215\n"
"v_add_f32 v212, v212, v216\n"
"v_add_f32 v213, v213, v217\n"
"v_mov_b32 v145, s85\n"
"v_mov_b32 v147, s85\n"
"s_mul_i32 s56, s55, 64\n"
"s_add_u32 s56, s56, 26370048\n"
"v_mov_b32 v148, 0\n"
"v_mul_u32_u24 v149, 64, v248\n"
"s_mov_b32 s57, 26370048\n"
"s_mov_b32 s58, 26374144\n"
"s_mov_b32 s59, 26378240\n"
"s_mov_b32 s60, 26382336\n"
"s_mov_b32 s87, 0x80000\n"
"s_waitcnt vmcnt(0)\n"
"s_barrier\n"
"ds_read_b128 v[200:203], v226 offset:0\n"
"ds_read_b128 v[204:207], v226 offset:128\n"
"ds_read_b128 v[208:211], v226 offset:256\n"
"s_waitcnt lgkmcnt(1)\n"
"v_add_f32 v200, v200, v204\n"
"v_add_f32 v201, v201, v205\n"
"v_add_f32 v202, v202, v206\n"
"s_waitcnt lgkmcnt(0)\n"
"v_add_f32 v200, v200, v208\n"
"v_add_f32 v201, v201, v209\n"
"v_add_f32 v202, v202, v210\n"
"v_mul_f32 v200, 0x3ae38e39, v200\n"
"v_mul_f32 v201, 0x3ae38e39, v201\n"
"v_fma_f32 v201, -v200, v200, v201\n"
"v_max_f32 v201, 0, v201\n"
"v_add_f32 v201, 0x3727c5ac, v201\n"
"v_rsq_f32 v192, v201\n"
"v_fma_f32 v202, -v200, v212, v202\n"
"v_fma_f32 v196, v192, v202, v213\n"
"v_ashrrev_i32 v203, 31, v196\n"
"v_or_b32 v203, 0x80000000, v203\n"
"v_xor_b32 v196, v196, v203\n"
"v_and_or_b32 v196, v196, s14, v220\n"
"v_mul_f32 v194, v200, v192\n"
"v_sub_f32 v194, 0, v194\n"
"v_mov_b32_dpp v203, v196 row_ror:1 row_mask:0xf bank_mask:0xf\n"
"v_min_u32 v198, v196, v203\n"
"v_max_u32 v197, v196, v203\n"
"s_nop 0\n"
"v_mov_b32_dpp v204, v198 row_ror:2 row_mask:0xf bank_mask:0xf\n"
"v_mov_b32_dpp v203, v197 row_ror:2 row_mask:0xf bank_mask:0xf\n"
"v_min_u32 v205, v197, v203\n"
"v_max_u32 v197, v197, v203\n"
"v_max3_u32 v198, v198, v204, v205\n"
"s_nop 0\n"
"v_mov_b32_dpp v203, v197 row_ror:4 row_mask:0xf bank_mask:0xf\n"
"v_mov_b32_dpp v204, v198 row_ror:4 row_mask:0xf bank_mask:0xf\n"
"v_min_u32 v205, v197, v203\n"
"v_max_u32 v197, v197, v203\n"
"v_max3_u32 v198, v198, v204, v205\n"
"v_mov_b32 v144, v197\n"
"v_mov_b32 v146, v198\n"
"s_mov_b64 exec, 1\n"
"buffer_store_dwordx4 v[144:147], v148, s[28:31], s56 offen sc1\n"
"s_mov_b64 exec, -1\n"
"AT_S2:\n"
"buffer_load_dwordx4 v[152:155], v149, s[28:31], s57 offen sc1\n"
"buffer_load_dwordx4 v[156:159], v149, s[28:31], s58 offen sc1\n"
"buffer_load_dwordx4 v[160:163], v149, s[28:31], s59 offen sc1\n"
"buffer_load_dwordx4 v[164:167], v149, s[28:31], s60 offen sc1\n"
"s_waitcnt vmcnt(0)\n"
"v_xor_b32 v150, s85, v153\n"
"v_xor_b32 v151, s85, v155\n"
"v_bitop3_b32 v150, v157, s85, v150 bitop3:0xbe\n"
"v_bitop3_b32 v151, v159, s85, v151 bitop3:0xbe\n"
"v_bitop3_b32 v150, v161, s85, v150 bitop3:0xbe\n"
"v_bitop3_b32 v151, v163, s85, v151 bitop3:0xbe\n"
"v_bitop3_b32 v150, v165, s85, v150 bitop3:0xbe\n"
"v_bitop3_b32 v151, v167, s85, v151 bitop3:0xbe\n"
"v_or_b32 v150, v150, v151\n"
"v_cmp_ne_u32 vcc, 0, v150\n"
"s_nop 0\n"
"s_cbranch_vccz AT_S3\n"
"s_sub_u32 s87, s87, 1\n"
"s_cmp_eq_u32 s87, 0\n"
"s_cbranch_scc1 AT_S3\n"
"s_sleep 1\n"
"s_branch AT_S2\n"
"AT_S3:\n"
"v_min_u32 v205, v152, v156\n"
"v_max_u32 v152, v152, v156\n"
"v_max3_u32 v154, v154, v158, v205\n"
"v_min_u32 v205, v160, v164\n"
"v_max_u32 v160, v160, v164\n"
"v_max3_u32 v162, v162, v166, v205\n"
"v_min_u32 v205, v152, v160\n"
"v_max_u32 v152, v152, v160\n"
"v_max3_u32 v154, v154, v162, v205\n"
"s_nop 0\n"
"v_mov_b32_dpp v156, v152 row_ror:1 row_mask:0xf bank_mask:0xf\n"
"v_mov_b32_dpp v158, v154 row_ror:1 row_mask:0xf bank_mask:0xf\n"
"v_min_u32 v205, v152, v156\n"
"v_max_u32 v152, v152, v156\n"
"v_max3_u32 v154, v154, v158, v205\n"
"s_nop 0\n"
"v_mov_b32_dpp v156, v152 row_ror:2 row_mask:0xf bank_mask:0xf\n"
"v_mov_b32_dpp v158, v154 row_ror:2 row_mask:0xf bank_mask:0xf\n"
"v_min_u32 v205, v152, v156\n"
"v_max_u32 v152, v152, v156\n"
"v_max3_u32 v154, v154, v158, v205\n"
"s_nop 0\n"
"v_mov_b32_dpp v156, v152 row_ror:4 row_mask:0xf bank_mask:0xf\n"
"v_mov_b32_dpp v158, v154 row_ror:4 row_mask:0xf bank_mask:0xf\n"
"v_min_u32 v205, v152, v156\n"
"v_max_u32 v152, v152, v156\n"
"v_max3_u32 v154, v154, v158, v205\n"
"s_nop 0\n"
"v_mov_b32_dpp v156, v152 row_ror:8 row_mask:0xf bank_mask:0xf\n"
"v_mov_b32_dpp v158, v154 row_ror:8 row_mask:0xf bank_mask:0xf\n"
"v_min_u32 v205, v152, v156\n"
"v_max_u32 v152, v152, v156\n"
"v_max3_u32 v154, v154, v158, v205\n"
"v_mov_b32 v156, v152\n"
"v_mov_b32 v158, v154\n"
"s_nop 0\n"
"v_permlane16_swap_b32 v152, v156\n"
"v_permlane16_swap_b32 v154, v158\n"
"v_min_u32 v205, v152, v156\n"
"v_max_u32 v152, v152, v156\n"
"v_max3_u32 v154, v154, v158, v205\n"
"v_mov_b32 v156, v152\n"
"v_mov_b32 v158, v154\n"
"s_nop 0\n"
"v_permlane32_swap_b32 v152, v156\n"
"v_permlane32_swap_b32 v154, v158\n"
"v_min_u32 v205, v152, v156\n"
"v_max_u32 v152, v152, v156\n"
"v_max3_u32 v154, v154, v158, v205\n"
"v_mov_b32 v153, v154\n"
"s_mov_b64 exec, 1\n"
"ds_write_b64 v227, v[152:153] offset:656\n"
"s_mov_b64 exec, -1\n"
"s_branch AT_B3\n"
"AT_A0:\n"
"AT_pgA0:\n"
"s_cbranch_execz AT_hr\n"
"v_and_b32 v192, 15, v248\n"
"v_lshlrev_b32 v192, 6, v192\n"
"s_lshl_b32 s57, s44, 10\n"
"buffer_load_dword v193, v192, s[28:31], s57 offen sc1\n"
"ds_read_b64 v[228:229], v240 offset:0\n"
"ds_read_b64 v[230:231], v240 offset:32\n"
"ds_read_b64 v[232:233], v240 offset:384\n"
"ds_read_b64 v[234:235], v240 offset:416\n"
"ds_read_b64 v[236:237], v240 offset:768\n"
"ds_read_b64 v[238:239], v240 offset:800\n"
"ds_read_b128 v[96:99], v244 offset:0\n"
"ds_read_b128 v[100:103], v244 offset:64\n"
"ds_read_b128 v[104:107], v244 offset:768\n"
"ds_read_b128 v[108:111], v244 offset:832\n"
"ds_read_b128 v[112:115], v244 offset:1536\n"
"ds_read_b128 v[116:119], v244 offset:1600\n"
"ds_read_b128 v[0:3], v244 offset:2304\n"
"ds_read_b128 v[4:7], v244 offset:2368\n"
"ds_read_b128 v[8:11], v244 offset:3072\n"
"ds_read_b128 v[12:15], v244 offset:3136\n"
"ds_read_b128 v[16:19], v244 offset:3840\n"
"ds_read_b128 v[20:23], v244 offset:3904\n"
"v_mov_b32 v48, 0\n"
"v_mov_b32 v49, 0\n"
"v_mov_b32 v50, 0\n"
"v_mov_b32 v51, 0\n"
"v_mov_b32 v52, 0\n"
"v_mov_b32 v53, 0\n"
"v_mov_b32 v54, 0\n"
"v_mov_b32 v55, 0\n"
"v_mov_b32 v56, 0\n"
"v_mov_b32 v57, 0\n"
"v_mov_b32 v58, 0\n"
"v_mov_b32 v59, 0\n"
"v_mov_b32 v60, 0\n"
"v_mov_b32 v61, 0\n"
"v_mov_b32 v62, 0\n"
"v_mov_b32 v63, 0\n"
"v_mov_b32 v64, 0\n"
"v_mov_b32 v65, 0\n"
"v_mov_b32 v66, 0\n"
"v_mov_b32 v67, 0\n"
"v_mov_b32 v68, 0\n"
"v_mov_b32 v69, 0\n"
"v_mov_b32 v70, 0\n"
"v_mov_b32 v71, 0\n"
"v_mov_b32 v72, 0\n"
"v_mov_b32 v73, 0\n"
"v_mov_b32 v74, 0\n"
"v_mov_b32 v75, 0\n"
"v_mov_b32 v76, 0\n"
"v_mov_b32 v77, 0\n"
"v_mov_b32 v78, 0\n"
"v_mov_b32 v79, 0\n"
"v_mov_b32 v80, 0\n"
"v_mov_b32 v81, 0\n"
"v_mov_b32 v82, 0\n"
"v_mov_b32 v83, 0\n"
"v_mov_b32 v84, 0\n"
"v_mov_b32 v85, 0\n"
"v_mov_b32 v86, 0\n"
"v_mov_b32 v87, 0\n"
"v_mov_b32 v88, 0\n"
"v_mov_b32 v89, 0\n"
"v_mov_b32 v90, 0\n"
"v_mov_b32 v91, 0\n"
"v_mov_b32 v92, 0\n"
"v_mov_b32 v93, 0\n"
"v_mov_b32 v94, 0\n"
"v_mov_b32 v95, 0\n"
"ds_read_b128 v[212:215], v227 offset:512\n"
"ds_read_b128 v[216:219], v227 offset:528\n"
"s_lshl_b32 s56, s55, 3\n"
"s_sub_u32 s56, 2047, s56\n"
"v_and_b32 v220, 7, v248\n"
"v_sub_u32 v220, s56, v220\n"
"s_waitcnt lgkmcnt(0)\n"
"v_add_f32 v212, v212, v214\n"
"v_add_f32 v213, v213, v215\n"
"v_add_f32 v212, v212, v216\n"
"v_add_f32 v213, v213, v217\n"
"s_mul_i32 s56, s41, 1536\n"
"s_add_u32 s56, s56, s40\n"
"s_add_u32 s56, s56, 49152\n"
"v_and_b32 v176, 32, v248\n"
"v_lshl_add_u32 v176, v176, 2, s56\n"
"v_bfe_u32 v177, v248, 3, 2\n"
"v_lshl_add_u32 v176, v177, 4, v176\n"
"v_lshl_add_u32 v177, v248, 5, s56\n"
"v_lshrrev_b32 v178, 3, v248\n"
"v_mul_u32_u24 v179, 43, v178\n"
"v_lshrrev_b32 v179, 7, v179\n"
"v_mul_u32_u24 v195, 3, v179\n"
"v_sub_u32 v178, v178, v195\n"
"v_mul_u32_u24 v178, 384, v178\n"
"v_and_b32 v195, 7, v248\n"
"v_lshl_add_u32 v178, v195, 4, v178\n"
"v_and_b32 v179, 2, v179\n"
"v_lshl_add_u32 v178, v179, 27, v178\n"
"s_lshl_b32 s56, s41, 7\n"
"v_add_u32 v178, s56, v178\n"
"s_mov_b32 s52, 0\n"
"s_mov_b32 s87, 0x80000\n"
"v_lshlrev_b32 v24, 16, v228\n"
"v_and_b32 v25, 0xffff0000, v228\n"
"v_lshlrev_b32 v26, 16, v229\n"
"v_and_b32 v27, 0xffff0000, v229\n"
"v_lshlrev_b32 v28, 16, v230\n"
"v_and_b32 v29, 0xffff0000, v230\n"
"v_lshlrev_b32 v30, 16, v231\n"
"v_and_b32 v31, 0xffff0000, v231\n"
"v_lshlrev_b32 v32, 16, v232\n"
"v_and_b32 v33, 0xffff0000, v232\n"
"v_lshlrev_b32 v34, 16, v233\n"
"v_and_b32 v35, 0xffff0000, v233\n"
"v_lshlrev_b32 v36, 16, v234\n"
"v_and_b32 v37, 0xffff0000, v234\n"
"v_lshlrev_b32 v38, 16, v235\n"
"v_and_b32 v39, 0xffff0000, v235\n"
"v_lshlrev_b32 v40, 16, v236\n"
"v_and_b32 v41, 0xffff0000, v236\n"
"v_lshlrev_b32 v42, 16, v237\n"
"v_and_b32 v43, 0xffff0000, v237\n"
"v_lshlrev_b32 v44, 16, v238\n"
"v_and_b32 v45, 0xffff0000, v238\n"
"v_lshlrev_b32 v46, 16, v239\n"
"v_and_b32 v47, 0xffff0000, v239\n"
"s_mul_i32 s63, s41, 9216\n"
"s_add_u32 s63, s63, s40\n"
"s_add_u32 s63, s63, 9216\n"
"v_lshrrev_b32 v185, 2, v248\n"
"v_and_b32 v186, 3, v248\n"
"v_mul_u32_u24 v187, 96, v185\n"
"v_lshl_add_u32 v188, v186, 4, s63\n"
"v_lshl_add_u32 v189, v187, 1, v188\n"
"v_add_u32 v188, v187, v188\n"
"v_lshrrev_b32 v190, 4, v248\n"
"v_mul_u32_u24 v191, 384, v190\n"
"v_add_u32 v187, v187, v191\n"
"v_lshl_add_u32 v187, v186, 3, v187\n"
"v_add_u32 v187, s63, v187\n"
"s_mov_b32 exec_lo, 0x77777777\n"
"s_mov_b32 exec_hi, 0x77777777\n"
"ds_write_b128 v189, a[48:51] offset:0\n"
"ds_write_b128 v189, a[84:87] offset:48\n"
"ds_write_b128 v189, a[52:55] offset:96\n"
"ds_write_b128 v189, a[88:91] offset:144\n"
"ds_write_b128 v189, a[56:59] offset:3072\n"
"ds_write_b128 v189, a[92:95] offset:3120\n"
"ds_write_b128 v189, a[60:63] offset:3168\n"
"ds_write_b128 v189, a[96:99] offset:3216\n"
"ds_write_b128 v189, a[64:67] offset:6144\n"
"ds_write_b128 v189, a[100:103] offset:6192\n"
"ds_write_b128 v189, a[68:71] offset:6240\n"
"ds_write_b128 v189, a[104:107] offset:6288\n"
"s_mov_b64 exec, -1\n"
"ds_read_b64_tr_b16 a[128:129], v187 offset:0\n"
"ds_read_b64_tr_b16 a[130:131], v187 offset:384\n"
"ds_read_b64_tr_b16 a[132:133], v187 offset:32\n"
"ds_read_b64_tr_b16 a[134:135], v187 offset:416\n"
"ds_read_b64_tr_b16 a[136:137], v187 offset:64\n"
"ds_read_b64_tr_b16 a[138:139], v187 offset:448\n"
"ds_read_b64_tr_b16 a[140:141], v187 offset:3072\n"
"ds_read_b64_tr_b16 a[142:143], v187 offset:3456\n"
"ds_read_b64_tr_b16 a[144:145], v187 offset:3104\n"
"ds_read_b64_tr_b16 a[146:147], v187 offset:3488\n"
"ds_read_b64_tr_b16 a[148:149], v187 offset:3136\n"
"ds_read_b64_tr_b16 a[150:151], v187 offset:3520\n"
"ds_read_b64_tr_b16 a[152:153], v187 offset:6144\n"
"ds_read_b64_tr_b16 a[154:155], v187 offset:6528\n"
"ds_read_b64_tr_b16 a[156:157], v187 offset:6176\n"
"ds_read_b64_tr_b16 a[158:159], v187 offset:6560\n"
"ds_read_b64_tr_b16 a[160:161], v187 offset:6208\n"
"ds_read_b64_tr_b16 a[162:163], v187 offset:6592\n"
"s_mov_b32 s10, 0\n"
"s_waitcnt vmcnt(0)\n"
"AT_A1:\n"
"v_cmp_eq_u32 vcc, s85, v193\n"
"s_sub_u32 s87, s87, 1\n"
"s_lshr_b32 s53, vcc_lo, 1\n"
"s_and_b32 s53, s53, vcc_lo\n"
"s_lshr_b32 s59, s53, 2\n"
"s_and_b32 s53, s53, s59\n"
"s_and_b32 s53, s53, 0x1111\n"
"s_cmp_lt_i32 s87, 0\n"
"s_cselect_b32 s53, 0x1111, s53\n"
"s_andn2_b32 s53, s53, s52\n"
"s_cmp_lg_u32 s53, 0\n"
"s_cbranch_scc1 AT_A2\n"
"s_sleep 2\n"
"AT_A1b:\n"
"buffer_load_dword v193, v192, s[28:31], s57 offen sc1\n"
"s_waitcnt vmcnt(0)\n"
"s_branch AT_A1\n"
"AT_A2:\n"
"s_bcnt1_i32_b32 s54, s53\n"
"s_bitcmp1_b32 s53, 0\n"
"s_cbranch_scc0 AT_Ai0\n"
"s_add_u32 s56, s47, 0\n"
"buffer_load_dwordx4 v[128:131], v241, s[28:31], s56 offen sc1\n"
"buffer_load_dwordx4 v[132:135], v242, s[28:31], s56 offen sc1\n"
"buffer_load_dwordx4 v[136:139], v243, s[28:31], s56 offen sc1\n"
"AT_Ai0:\n"
"s_bitcmp1_b32 s53, 4\n"
"s_cbranch_scc0 AT_Ai1\n"
"s_add_u32 s56, s47, 2359296\n"
"buffer_load_dwordx4 v[140:143], v241, s[28:31], s56 offen sc1\n"
"buffer_load_dwordx4 v[144:147], v242, s[28:31], s56 offen sc1\n"
"buffer_load_dwordx4 v[148:151], v243, s[28:31], s56 offen sc1\n"
"AT_Ai1:\n"
"s_bitcmp1_b32 s53, 8\n"
"s_cbranch_scc0 AT_Ai2\n"
"s_add_u32 s56, s47, 4718592\n"
"buffer_load_dwordx4 v[152:155], v241, s[28:31], s56 offen sc1\n"
"buffer_load_dwordx4 v[156:159], v242, s[28:31], s56 offen sc1\n"
"buffer_load_dwordx4 v[160:163], v243, s[28:31], s56 offen sc1\n"
"AT_Ai2:\n"
"s_bitcmp1_b32 s53, 12\n"
"s_cbranch_scc0 AT_Ai3\n"
"s_add_u32 s56, s47, 7077888\n"
"buffer_load_dwordx4 v[164:167], v241, s[28:31], s56 offen sc1\n"
"buffer_load_dwordx4 v[168:171], v242, s[28:31], s56 offen sc1\n"
"buffer_load_dwordx4 v[172:175], v243, s[28:31], s56 offen sc1\n"
"AT_Ai3:\n"
"s_cmp_lg_u32 s10, 0\n"
"s_cbranch_scc1 AT_Au\n"
"s_mov_b32 s10, 1\n"
"s_mov_b32 exec_lo, 0x77777777\n"
"s_mov_b32 exec_hi, 0x77777777\n"
"ds_write_b128 v189, a[72:75] offset:0\n"
"ds_write_b128 v189, a[108:111] offset:48\n"
"ds_write_b128 v189, a[76:79] offset:96\n"
"ds_write_b128 v189, a[112:115] offset:144\n"
"ds_write_b128 v188, a[80:83] offset:3072\n"
"ds_write_b128 v188, a[116:119] offset:3120\n"
"s_mov_b64 exec, -1\n"
"ds_read_b64_tr_b16 a[164:165], v187 offset:0\n"
"ds_read_b64_tr_b16 a[166:167], v187 offset:384\n"
"ds_read_b64_tr_b16 a[168:169], v187 offset:32\n"
"ds_read_b64_tr_b16 a[170:171], v187 offset:416\n"
"ds_read_b64_tr_b16 a[172:173], v187 offset:64\n"
"ds_read_b64_tr_b16 a[174:175], v187 offset:448\n"
"ds_read_b64_tr_b16 a[176:177], v187 offset:3072\n"
"ds_read_b64_tr_b16 a[178:179], v187 offset:3456\n"
"ds_read_b64_tr_b16 a[180:181], v187 offset:3104\n"
"ds_read_b64_tr_b16 a[182:183], v187 offset:3488\n"
"ds_read_b64_tr_b16 a[184:185], v187 offset:3136\n"
"ds_read_b64_tr_b16 a[186:187], v187 offset:3520\n"
"AT_Au:\n"
"s_bitcmp1_b32 s53, 0\n"
"s_cbranch_scc0 AT_Ar0\n"
"s_sub_u32 s54, s54, 1\n"
"s_cmp_eq_u32 s54, 0\n"
"s_cbranch_scc1 AT_Aw0_0\n"
"s_cmp_eq_u32 s54, 1\n"
"s_cbranch_scc1 AT_Aw0_1\n"
"s_cmp_eq_u32 s54, 2\n"
"s_cbranch_scc1 AT_Aw0_2\n"
"s_waitcnt vmcnt(9)\n"
"s_branch AT_Ax0\n"
"AT_Aw0_2:\n"
"s_waitcnt vmcnt(6)\n"
"s_branch AT_Ax0\n"
"AT_Aw0_1:\n"
"s_waitcnt vmcnt(3)\n"
"s_branch AT_Ax0\n"
"AT_Aw0_0:\n"
"s_waitcnt vmcnt(0)\n"
"AT_Ax0:\n"
"v_max3_u32 v194, v128, v129, v130\n"
"v_max3_u32 v195, v131, v132, v133\n"
"v_max3_u32 v194, v194, v134, v135\n"
"v_max3_u32 v195, v195, v136, v137\n"
"v_max3_u32 v194, v194, v138, v139\n"
"v_max_u32 v194, v194, v195\n"
"v_cmp_eq_u32 vcc, -1, v194\n"
"s_cmp_lt_i32 s87, 0\n"
"s_cselect_b64 vcc, 0, vcc\n"
"s_cbranch_vccnz AT_Ar0\n"
"s_bitset1_b32 s52, 0\n"
"v_dot2c_f32_bf16 v48, v128, v224\n"
"v_dot2c_f32_bf16 v49, v128, v225\n"
"v_dot2c_f32_bf16 v50, v129, v224\n"
"v_dot2c_f32_bf16 v51, v129, v225\n"
"v_dot2c_f32_bf16 v52, v130, v224\n"
"v_dot2c_f32_bf16 v53, v130, v225\n"
"v_dot2c_f32_bf16 v54, v131, v224\n"
"v_dot2c_f32_bf16 v55, v131, v225\n"
"v_dot2c_f32_bf16 v56, v132, v224\n"
"v_dot2c_f32_bf16 v57, v132, v225\n"
"v_dot2c_f32_bf16 v58, v133, v224\n"
"v_dot2c_f32_bf16 v59, v133, v225\n"
"v_dot2c_f32_bf16 v60, v134, v224\n"
"v_dot2c_f32_bf16 v61, v134, v225\n"
"v_dot2c_f32_bf16 v62, v135, v224\n"
"v_dot2c_f32_bf16 v63, v135, v225\n"
"v_dot2c_f32_bf16 v64, v136, v224\n"
"v_dot2c_f32_bf16 v65, v136, v225\n"
"v_dot2c_f32_bf16 v66, v137, v224\n"
"v_dot2c_f32_bf16 v67, v137, v225\n"
"v_dot2c_f32_bf16 v68, v138, v224\n"
"v_dot2c_f32_bf16 v69, v138, v225\n"
"v_dot2c_f32_bf16 v70, v139, v224\n"
"v_dot2c_f32_bf16 v71, v139, v225\n"
"AT_Ar0:\n"
"s_bitcmp1_b32 s53, 4\n"
"s_cbranch_scc0 AT_Ar1\n"
"s_sub_u32 s54, s54, 1\n"
"s_cmp_eq_u32 s54, 0\n"
"s_cbranch_scc1 AT_Aw1_0\n"
"s_cmp_eq_u32 s54, 1\n"
"s_cbranch_scc1 AT_Aw1_1\n"
"s_waitcnt vmcnt(6)\n"
"s_branch AT_Ax1\n"
"AT_Aw1_1:\n"
"s_waitcnt vmcnt(3)\n"
"s_branch AT_Ax1\n"
"AT_Aw1_0:\n"
"s_waitcnt vmcnt(0)\n"
"AT_Ax1:\n"
"v_max3_u32 v194, v140, v141, v142\n"
"v_max3_u32 v195, v143, v144, v145\n"
"v_max3_u32 v194, v194, v146, v147\n"
"v_max3_u32 v195, v195, v148, v149\n"
"v_max3_u32 v194, v194, v150, v151\n"
"v_max_u32 v194, v194, v195\n"
"v_cmp_eq_u32 vcc, -1, v194\n"
"s_cmp_lt_i32 s87, 0\n"
"s_cselect_b64 vcc, 0, vcc\n"
"s_cbranch_vccnz AT_Ar1\n"
"s_bitset1_b32 s52, 4\n"
"v_dot2c_f32_bf16 v48, v140, v224\n"
"v_dot2c_f32_bf16 v49, v140, v225\n"
"v_dot2c_f32_bf16 v50, v141, v224\n"
"v_dot2c_f32_bf16 v51, v141, v225\n"
"v_dot2c_f32_bf16 v52, v142, v224\n"
"v_dot2c_f32_bf16 v53, v142, v225\n"
"v_dot2c_f32_bf16 v54, v143, v224\n"
"v_dot2c_f32_bf16 v55, v143, v225\n"
"v_dot2c_f32_bf16 v56, v144, v224\n"
"v_dot2c_f32_bf16 v57, v144, v225\n"
"v_dot2c_f32_bf16 v58, v145, v224\n"
"v_dot2c_f32_bf16 v59, v145, v225\n"
"v_dot2c_f32_bf16 v60, v146, v224\n"
"v_dot2c_f32_bf16 v61, v146, v225\n"
"v_dot2c_f32_bf16 v62, v147, v224\n"
"v_dot2c_f32_bf16 v63, v147, v225\n"
"v_dot2c_f32_bf16 v64, v148, v224\n"
"v_dot2c_f32_bf16 v65, v148, v225\n"
"v_dot2c_f32_bf16 v66, v149, v224\n"
"v_dot2c_f32_bf16 v67, v149, v225\n"
"v_dot2c_f32_bf16 v68, v150, v224\n"
"v_dot2c_f32_bf16 v69, v150, v225\n"
"v_dot2c_f32_bf16 v70, v151, v224\n"
"v_dot2c_f32_bf16 v71, v151, v225\n"
"AT_Ar1:\n"
"s_bitcmp1_b32 s53, 8\n"
"s_cbranch_scc0 AT_Ar2\n"
"s_sub_u32 s54, s54, 1\n"
"s_cmp_eq_u32 s54, 0\n"
"s_cbranch_scc1 AT_Aw2_0\n"
"s_waitcnt vmcnt(3)\n"
"s_branch AT_Ax2\n"
"AT_Aw2_0:\n"
"s_waitcnt vmcnt(0)\n"
"AT_Ax2:\n"
"v_max3_u32 v194, v152, v153, v154\n"
"v_max3_u32 v195, v155, v156, v157\n"
"v_max3_u32 v194, v194, v158, v159\n"
"v_max3_u32 v195, v195, v160, v161\n"
"v_max3_u32 v194, v194, v162, v163\n"
"v_max_u32 v194, v194, v195\n"
"v_cmp_eq_u32 vcc, -1, v194\n"
"s_cmp_lt_i32 s87, 0\n"
"s_cselect_b64 vcc, 0, vcc\n"
"s_cbranch_vccnz AT_Ar2\n"
"s_bitset1_b32 s52, 8\n"
"v_dot2c_f32_bf16 v72, v152, v224\n"
"v_dot2c_f32_bf16 v73, v152, v225\n"
"v_dot2c_f32_bf16 v74, v153, v224\n"
"v_dot2c_f32_bf16 v75, v153, v225\n"
"v_dot2c_f32_bf16 v76, v154, v224\n"
"v_dot2c_f32_bf16 v77, v154, v225\n"
"v_dot2c_f32_bf16 v78, v155, v224\n"
"v_dot2c_f32_bf16 v79, v155, v225\n"
"v_dot2c_f32_bf16 v80, v156, v224\n"
"v_dot2c_f32_bf16 v81, v156, v225\n"
"v_dot2c_f32_bf16 v82, v157, v224\n"
"v_dot2c_f32_bf16 v83, v157, v225\n"
"v_dot2c_f32_bf16 v84, v158, v224\n"
"v_dot2c_f32_bf16 v85, v158, v225\n"
"v_dot2c_f32_bf16 v86, v159, v224\n"
"v_dot2c_f32_bf16 v87, v159, v225\n"
"v_dot2c_f32_bf16 v88, v160, v224\n"
"v_dot2c_f32_bf16 v89, v160, v225\n"
"v_dot2c_f32_bf16 v90, v161, v224\n"
"v_dot2c_f32_bf16 v91, v161, v225\n"
"v_dot2c_f32_bf16 v92, v162, v224\n"
"v_dot2c_f32_bf16 v93, v162, v225\n"
"v_dot2c_f32_bf16 v94, v163, v224\n"
"v_dot2c_f32_bf16 v95, v163, v225\n"
"AT_Ar2:\n"
"s_bitcmp1_b32 s53, 12\n"
"s_cbranch_scc0 AT_Ar3\n"
"s_sub_u32 s54, s54, 1\n"
"s_waitcnt vmcnt(0)\n"
"AT_Ax3:\n"
"v_max3_u32 v194, v164, v165, v166\n"
"v_max3_u32 v195, v167, v168, v169\n"
"v_max3_u32 v194, v194, v170, v171\n"
"v_max3_u32 v195, v195, v172, v173\n"
"v_max3_u32 v194, v194, v174, v175\n"
"v_max_u32 v194, v194, v195\n"
"v_cmp_eq_u32 vcc, -1, v194\n"
"s_cmp_lt_i32 s87, 0\n"
"s_cselect_b64 vcc, 0, vcc\n"
"s_cbranch_vccnz AT_Ar3\n"
"s_bitset1_b32 s52, 12\n"
"v_dot2c_f32_bf16 v72, v164, v224\n"
"v_dot2c_f32_bf16 v73, v164, v225\n"
"v_dot2c_f32_bf16 v74, v165, v224\n"
"v_dot2c_f32_bf16 v75, v165, v225\n"
"v_dot2c_f32_bf16 v76, v166, v224\n"
"v_dot2c_f32_bf16 v77, v166, v225\n"
"v_dot2c_f32_bf16 v78, v167, v224\n"
"v_dot2c_f32_bf16 v79, v167, v225\n"
"v_dot2c_f32_bf16 v80, v168, v224\n"
"v_dot2c_f32_bf16 v81, v168, v225\n"
"v_dot2c_f32_bf16 v82, v169, v224\n"
"v_dot2c_f32_bf16 v83, v169, v225\n"
"v_dot2c_f32_bf16 v84, v170, v224\n"
"v_dot2c_f32_bf16 v85, v170, v225\n"
"v_dot2c_f32_bf16 v86, v171, v224\n"
"v_dot2c_f32_bf16 v87, v171, v225\n"
"v_dot2c_f32_bf16 v88, v172, v224\n"
"v_dot2c_f32_bf16 v89, v172, v225\n"
"v_dot2c_f32_bf16 v90, v173, v224\n"
"v_dot2c_f32_bf16 v91, v173, v225\n"
"v_dot2c_f32_bf16 v92, v174, v224\n"
"v_dot2c_f32_bf16 v93, v174, v225\n"
"v_dot2c_f32_bf16 v94, v175, v224\n"
"v_dot2c_f32_bf16 v95, v175, v225\n"
"AT_Ar3:\n"
"s_cmp_lg_u32 s52, 0x1111\n"
"s_cbranch_scc1 AT_A1b\n"
"v_pk_add_f32 v[48:49], v[48:49], v[72:73]\n"
"v_pk_add_f32 v[50:51], v[50:51], v[74:75]\n"
"v_pk_add_f32 v[52:53], v[52:53], v[76:77]\n"
"v_pk_add_f32 v[54:55], v[54:55], v[78:79]\n"
"v_pk_add_f32 v[56:57], v[56:57], v[80:81]\n"
"v_pk_add_f32 v[58:59], v[58:59], v[82:83]\n"
"v_pk_add_f32 v[60:61], v[60:61], v[84:85]\n"
"v_pk_add_f32 v[62:63], v[62:63], v[86:87]\n"
"v_pk_add_f32 v[64:65], v[64:65], v[88:89]\n"
"v_pk_add_f32 v[66:67], v[66:67], v[90:91]\n"
"v_pk_add_f32 v[68:69], v[68:69], v[92:93]\n"
"v_pk_add_f32 v[70:71], v[70:71], v[94:95]\n"
"v_pk_fma_f32 v[48:49], v[48:49], v[96:97], v[24:25]\n"
"v_pk_fma_f32 v[50:51], v[50:51], v[98:99], v[26:27]\n"
"v_pk_fma_f32 v[52:53], v[52:53], v[100:101], v[28:29]\n"
"v_pk_fma_f32 v[54:55], v[54:55], v[102:103], v[30:31]\n"
"v_pk_fma_f32 v[56:57], v[56:57], v[104:105], v[32:33]\n"
"v_pk_fma_f32 v[58:59], v[58:59], v[106:107], v[34:35]\n"
"v_pk_fma_f32 v[60:61], v[60:61], v[108:109], v[36:37]\n"
"v_pk_fma_f32 v[62:63], v[62:63], v[110:111], v[38:39]\n"
"v_pk_fma_f32 v[64:65], v[64:65], v[112:113], v[40:41]\n"
"v_pk_fma_f32 v[66:67], v[66:67], v[114:115], v[42:43]\n"
"v_pk_fma_f32 v[68:69], v[68:69], v[116:117], v[44:45]\n"
"v_pk_fma_f32 v[70:71], v[70:71], v[118:119], v[46:47]\n"
"v_pk_mul_f32 v[194:195], v[48:49], v[48:49]\n"
"v_pk_mul_f32 v[196:197], v[48:49], v[0:1]\n"
"v_pk_add_f32 v[192:193], v[48:49], v[50:51]\n"
"v_pk_fma_f32 v[194:195], v[50:51], v[50:51], v[194:195]\n"
"v_pk_fma_f32 v[196:197], v[50:51], v[2:3], v[196:197]\n"
"v_pk_add_f32 v[192:193], v[192:193], v[52:53]\n"
"v_pk_fma_f32 v[194:195], v[52:53], v[52:53], v[194:195]\n"
"v_pk_fma_f32 v[196:197], v[52:53], v[4:5], v[196:197]\n"
"v_pk_add_f32 v[192:193], v[192:193], v[54:55]\n"
"v_pk_fma_f32 v[194:195], v[54:55], v[54:55], v[194:195]\n"
"v_pk_fma_f32 v[196:197], v[54:55], v[6:7], v[196:197]\n"
"v_pk_add_f32 v[192:193], v[192:193], v[56:57]\n"
"v_pk_fma_f32 v[194:195], v[56:57], v[56:57], v[194:195]\n"
"v_pk_fma_f32 v[196:197], v[56:57], v[8:9], v[196:197]\n"
"AT_pg17:\n"
"s_cbranch_execz AT_hr\n"
"v_pk_add_f32 v[192:193], v[192:193], v[58:59]\n"
"v_pk_fma_f32 v[194:195], v[58:59], v[58:59], v[194:195]\n"
"v_pk_fma_f32 v[196:197], v[58:59], v[10:11], v[196:197]\n"
"v_pk_add_f32 v[192:193], v[192:193], v[60:61]\n"
"v_pk_fma_f32 v[194:195], v[60:61], v[60:61], v[194:195]\n"
"v_pk_fma_f32 v[196:197], v[60:61], v[12:13], v[196:197]\n"
"v_pk_add_f32 v[192:193], v[192:193], v[62:63]\n"
"v_pk_fma_f32 v[194:195], v[62:63], v[62:63], v[194:195]\n"
"v_pk_fma_f32 v[196:197], v[62:63], v[14:15], v[196:197]\n"
"v_pk_add_f32 v[192:193], v[192:193], v[64:65]\n"
"v_pk_fma_f32 v[194:195], v[64:65], v[64:65], v[194:195]\n"
"v_pk_fma_f32 v[196:197], v[64:65], v[16:17], v[196:197]\n"
"v_pk_add_f32 v[192:193], v[192:193], v[66:67]\n"
"v_pk_fma_f32 v[194:195], v[66:67], v[66:67], v[194:195]\n"
"v_pk_fma_f32 v[196:197], v[66:67], v[18:19], v[196:197]\n"
"v_pk_add_f32 v[192:193], v[192:193], v[68:69]\n"
"v_pk_fma_f32 v[194:195], v[68:69], v[68:69], v[194:195]\n"
"v_pk_fma_f32 v[196:197], v[68:69], v[20:21], v[196:197]\n"
"v_pk_add_f32 v[192:193], v[192:193], v[70:71]\n"
"v_pk_fma_f32 v[194:195], v[70:71], v[70:71], v[194:195]\n"
"v_pk_fma_f32 v[196:197], v[70:71], v[22:23], v[196:197]\n"
"v_add_f32 v192, v192, v193\n"
"v_add_f32 v193, v194, v195\n"
"v_add_f32 v194, v196, v197\n"
"s_lshl_b32 s56, s41, 7\n"
"v_add_u32 v199, s56, v226\n"
"v_add_f32_dpp v192, v192, v192 row_ror:8 row_mask:0xf bank_mask:0xf\n"
"v_add_f32_dpp v193, v193, v193 row_ror:8 row_mask:0xf bank_mask:0xf\n"
"v_add_f32_dpp v194, v194, v194 row_ror:8 row_mask:0xf bank_mask:0xf\n"
"v_mov_b32 v196, v192\n"
"v_mov_b32 v197, v193\n"
"v_mov_b32 v198, v194\n"
"v_permlane16_swap_b32 v192, v196\n"
"v_permlane16_swap_b32 v193, v197\n"
"v_permlane16_swap_b32 v194, v198\n"
"v_add_f32 v192, v192, v196\n"
"v_add_f32 v193, v193, v197\n"
"v_add_f32 v194, v194, v198\n"
"v_mov_b32 v196, v192\n"
"v_mov_b32 v197, v193\n"
"v_mov_b32 v198, v194\n"
"v_permlane32_swap_b32 v192, v196\n"
"v_permlane32_swap_b32 v193, v197\n"
"v_permlane32_swap_b32 v194, v198\n"
"v_add_f32 v192, v192, v196\n"
"v_add_f32 v193, v193, v197\n"
"v_add_f32 v194, v194, v198\n"
"s_mov_b64 exec, 0xff\n"
"ds_write_b128 v199, v[192:195]\n"
"s_mov_b64 exec, -1\n"
"s_waitcnt lgkmcnt(0)\n"
"s_barrier\n"
"ds_read_b128 v[200:203], v226 offset:0\n"
"ds_read_b128 v[204:207], v226 offset:128\n"
"ds_read_b128 v[208:211], v226 offset:256\n"
"ds_read_b128 v[0:3], v244 offset:4608\n"
"ds_read_b128 v[4:7], v244 offset:4672\n"
"ds_read_b128 v[8:11], v244 offset:5376\n"
"ds_read_b128 v[12:15], v244 offset:5440\n"
"ds_read_b128 v[16:19], v244 offset:6144\n"
"ds_read_b128 v[20:23], v244 offset:6208\n"
"ds_read_b128 v[24:27], v244 offset:6912\n"
"ds_read_b128 v[28:31], v244 offset:6976\n"
"ds_read_b128 v[32:35], v244 offset:7680\n"
"ds_read_b128 v[36:39], v244 offset:7744\n"
"ds_read_b128 v[40:43], v244 offset:8448\n"
"ds_read_b128 v[44:47], v244 offset:8512\n"
"s_waitcnt lgkmcnt(13)\n"
"v_add_f32 v200, v200, v204\n"
"v_add_f32 v201, v201, v205\n"
"v_add_f32 v202, v202, v206\n"
"s_waitcnt lgkmcnt(12)\n"
"v_add_f32 v200, v200, v208\n"
"v_add_f32 v201, v201, v209\n"
"v_add_f32 v202, v202, v210\n"
"v_mul_f32 v200, 0x3ae38e39, v200\n"
"v_mul_f32 v201, 0x3ae38e39, v201\n"
"v_fma_f32 v201, -v200, v200, v201\n"
"v_max_f32 v201, 0, v201\n"
"v_add_f32 v201, 0x3727c5ac, v201\n"
"v_rsq_f32 v192, v201\n"
"v_fma_f32 v202, -v200, v212, v202\n"
"v_fma_f32 v196, v192, v202, v213\n"
"v_ashrrev_i32 v203, 31, v196\n"
"v_or_b32 v203, 0x80000000, v203\n"
"v_xor_b32 v196, v196, v203\n"
"v_and_or_b32 v196, v196, s14, v220\n"
"v_mul_f32 v194, v200, v192\n"
"v_sub_f32 v194, 0, v194\n"
"v_mov_b32_dpp v203, v196 row_ror:1 row_mask:0xf bank_mask:0xf\n"
"v_min_u32 v198, v196, v203\n"
"v_max_u32 v197, v196, v203\n"
"s_nop 0\n"
"v_mov_b32_dpp v204, v198 row_ror:2 row_mask:0xf bank_mask:0xf\n"
"v_mov_b32_dpp v203, v197 row_ror:2 row_mask:0xf bank_mask:0xf\n"
"v_min_u32 v205, v197, v203\n"
"v_max_u32 v197, v197, v203\n"
"v_max3_u32 v198, v198, v204, v205\n"
"s_nop 0\n"
"v_mov_b32_dpp v203, v197 row_ror:4 row_mask:0xf bank_mask:0xf\n"
"v_mov_b32_dpp v204, v198 row_ror:4 row_mask:0xf bank_mask:0xf\n"
"v_min_u32 v205, v197, v203\n"
"v_max_u32 v197, v197, v203\n"
"v_max3_u32 v198, v198, v204, v205\n"
"v_cmp_ge_u32 s[8:9], v196, v198\n"
"ds_read_b128 v[72:75], v244 offset:38912\n"
"ds_read_b128 v[76:79], v244 offset:38976\n"
"ds_read_b128 v[80:83], v244 offset:39680\n"
"ds_read_b128 v[84:87], v244 offset:39744\n"
"ds_read_b128 v[88:91], v244 offset:40448\n"
"ds_read_b128 v[92:95], v244 offset:40512\n"
"v_pk_fma_f32 v[144:145], v[48:49], v[192:193], v[194:195] op_sel_hi:[1,0,0]\n"
"v_pk_fma_f32 v[146:147], v[50:51], v[192:193], v[194:195] op_sel_hi:[1,0,0]\n"
"v_pk_fma_f32 v[148:149], v[52:53], v[192:193], v[194:195] op_sel_hi:[1,0,0]\n"
"v_pk_fma_f32 v[150:151], v[54:55], v[192:193], v[194:195] op_sel_hi:[1,0,0]\n"
"v_pk_fma_f32 v[152:153], v[56:57], v[192:193], v[194:195] op_sel_hi:[1,0,0]\n"
"v_pk_fma_f32 v[154:155], v[58:59], v[192:193], v[194:195] op_sel_hi:[1,0,0]\n"
"v_pk_fma_f32 v[156:157], v[60:61], v[192:193], v[194:195] op_sel_hi:[1,0,0]\n"
"v_pk_fma_f32 v[158:159], v[62:63], v[192:193], v[194:195] op_sel_hi:[1,0,0]\n"
"v_pk_fma_f32 v[160:161], v[64:65], v[192:193], v[194:195] op_sel_hi:[1,0,0]\n"
"v_pk_fma_f32 v[162:163], v[66:67], v[192:193], v[194:195] op_sel_hi:[1,0,0]\n"
"v_pk_fma_f32 v[164:165], v[68:69], v[192:193], v[194:195] op_sel_hi:[1,0,0]\n"
"v_pk_fma_f32 v[166:167], v[70:71], v[192:193], v[194:195] op_sel_hi:[1,0,0]\n"
"s_waitcnt lgkmcnt(6)\n"
"v_fma_f32 v96, v144, v0, v24\n"
"v_fma_f32 v97, v145, v1, v25\n"
"v_fma_f32 v98, v146, v2, v26\n"
"v_fma_f32 v99, v147, v3, v27\n"
"v_fma_f32 v100, v148, v4, v28\n"
"v_fma_f32 v101, v149, v5, v29\n"
"v_fma_f32 v102, v150, v6, v30\n"
"v_fma_f32 v103, v151, v7, v31\n"
"v_fma_f32 v104, v152, v8, v32\n"
"v_fma_f32 v105, v153, v9, v33\n"
"v_fma_f32 v106, v154, v10, v34\n"
"v_fma_f32 v107, v155, v11, v35\n"
"v_fma_f32 v108, v156, v12, v36\n"
"v_fma_f32 v109, v157, v13, v37\n"
"v_fma_f32 v110, v158, v14, v38\n"
"v_fma_f32 v111, v159, v15, v39\n"
"v_fma_f32 v112, v160, v16, v40\n"
"v_fma_f32 v113, v161, v17, v41\n"
"v_fma_f32 v114, v162, v18, v42\n"
"v_fma_f32 v115, v163, v19, v43\n"
"v_fma_f32 v116, v164, v20, v44\n"
"v_fma_f32 v117, v165, v21, v45\n"
"v_fma_f32 v118, v166, v22, v46\n"
"v_fma_f32 v119, v167, v23, v47\n"
"v_cmp_ne_u32 vcc, v196, v197\n"
"v_add_u32 v135, 768, v176\n"
"v_cmp_gt_u32 s[58:59], 24, v248\n"
"v_cndmask_b32 v134, v176, v135, vcc\n"
"s_mov_b64 exec, s[8:9]\n"
"ds_write_b128 v134, v[96:99] offset:0\n"
"ds_write_b128 v134, v[100:103] offset:64\n"
"ds_write_b128 v134, v[104:107] offset:256\n"
"ds_write_b128 v134, v[108:111] offset:320\n"
"ds_write_b128 v134, v[112:115] offset:512\n"
"ds_write_b128 v134, v[116:119] offset:576\n"
"s_mov_b64 exec, -1\n"
"ds_read_b128 v[136:139], v177 offset:0\n"
"ds_read_b128 v[140:143], v177 offset:16\n"
"v_cndmask_b32 v135, v198, v197, s[58:59]\n"
"v_not_b32 v135, v135\n"
"v_and_b32 v135, 7, v135\n"
"v_mul_u32_u24 v135, 2304, v135\n"
"v_add_u32 v135, v135, v178\n"
"s_mul_i32 s60, s55, 18432\n"
"s_add_u32 s60, s60, 26402816\n"
"s_waitcnt lgkmcnt(0)\n"
"v_cvt_pk_bf16_f32 v120, v136, v137\n"
"v_cvt_pk_bf16_f32 v121, v138, v139\n"
"v_cvt_pk_bf16_f32 v122, v140, v141\n"
"v_cvt_pk_bf16_f32 v123, v142, v143\n"
"buffer_store_dwordx4 v[120:123], v135, s[28:31], s60 offen sc1\n"
"v_lshlrev_b32 v128, 16, v120\n"
"v_and_b32 v129, 0xffff0000, v120\n"
"v_sub_f32 v136, v136, v128\n"
"v_sub_f32 v137, v137, v129\n"
"v_lshlrev_b32 v128, 16, v121\n"
"v_and_b32 v129, 0xffff0000, v121\n"
"v_sub_f32 v138, v138, v128\n"
"v_sub_f32 v139, v139, v129\n"
"v_lshlrev_b32 v128, 16, v122\n"
"v_and_b32 v129, 0xffff0000, v122\n"
"v_sub_f32 v140, v140, v128\n"
"v_sub_f32 v141, v141, v129\n"
"v_lshlrev_b32 v128, 16, v123\n"
"v_and_b32 v129, 0xffff0000, v123\n"
"v_sub_f32 v142, v142, v128\n"
"v_sub_f32 v143, v143, v129\n"
"v_cvt_pk_bf16_f32 v124, v136, v137\n"
"v_cvt_pk_bf16_f32 v125, v138, v139\n"
"v_cvt_pk_bf16_f32 v126, v140, v141\n"
"v_cvt_pk_bf16_f32 v127, v142, v143\n"
"buffer_store_dwordx4 v[124:127], v135, s[28:31], s60 offen offset:1152 sc1\n"
"v_fma_f32 v200, v72, v96, v48\n"
"v_fma_f32 v201, v73, v97, v49\n"
"v_fma_f32 v202, v74, v98, v50\n"
"v_fma_f32 v203, v75, v99, v51\n"
"v_fma_f32 v204, v76, v100, v52\n"
"v_fma_f32 v205, v77, v101, v53\n"
"v_fma_f32 v206, v78, v102, v54\n"
"v_fma_f32 v207, v79, v103, v55\n"
"v_fma_f32 v208, v80, v104, v56\n"
"v_fma_f32 v209, v81, v105, v57\n"
"v_fma_f32 v210, v82, v106, v58\n"
"v_fma_f32 v211, v83, v107, v59\n"
"v_fma_f32 v212, v84, v108, v60\n"
"v_fma_f32 v213, v85, v109, v61\n"
"v_fma_f32 v214, v86, v110, v62\n"
"v_fma_f32 v215, v87, v111, v63\n"
"v_fma_f32 v216, v88, v112, v64\n"
"v_fma_f32 v217, v89, v113, v65\n"
"v_fma_f32 v218, v90, v114, v66\n"
"v_fma_f32 v219, v91, v115, v67\n"
"v_fma_f32 v220, v92, v116, v68\n"
"v_fma_f32 v221, v93, v117, v69\n"
"v_fma_f32 v222, v94, v118, v70\n"
"v_fma_f32 v223, v95, v119, v71\n"
"v_cvt_pk_bf16_f32 v228, v200, v201\n"
"v_cvt_pk_bf16_f32 v229, v202, v203\n"
"v_cvt_pk_bf16_f32 v230, v204, v205\n"
"v_cvt_pk_bf16_f32 v231, v206, v207\n"
"v_cvt_pk_bf16_f32 v232, v208, v209\n"
"v_cvt_pk_bf16_f32 v233, v210, v211\n"
"v_cvt_pk_bf16_f32 v234, v212, v213\n"
"v_cvt_pk_bf16_f32 v235, v214, v215\n"
"v_cvt_pk_bf16_f32 v236, v216, v217\n"
"v_cvt_pk_bf16_f32 v237, v218, v219\n"
"v_cvt_pk_bf16_f32 v238, v220, v221\n"
"v_cvt_pk_bf16_f32 v239, v222, v223\n"
"ds_write_b64 v240, v[228:229] offset:0\n"
"ds_write_b64 v240, v[230:231] offset:32\n"
"ds_write_b64 v240, v[232:233] offset:384\n"
"ds_write_b64 v240, v[234:235] offset:416\n"
"ds_write_b64 v240, v[236:237] offset:768\n"
"ds_write_b64 v240, v[238:239] offset:800\n"
"s_mov_b64 s[8:9], -1\n"
"v_mov_b32 v225, v196\n"
"v_and_b32 v241, 1, v248\n"
"v_mul_u32_u24 v241, 1152, v241\n"
"v_lshl_add_u32 v241, v190, 4, v241\n"
"s_mul_i32 s63, s41, 288\n"
"v_add_u32 v241, s63, v241\n"
"v_and_b32 v242, 10, v248\n"
"v_lshl_add_u32 v241, v242, 26, v241\n"
"v_and_b32 v242, 32, v248\n"
"v_lshlrev_b32 v242, 25, v242\n"
"s_add_u32 s63, s63, s40\n"
"s_add_u32 s63, s63, 131072\n"
"v_cmp_eq_u32 vcc, 3, v190\n"
"v_and_b32 v185, 3, v185\n"
"v_lshlrev_b32 v191, 3, v190\n"
"v_cndmask_b32_e64 v191, v191, 0, vcc\n"
"v_add_u32 v191, v191, v185\n"
"v_mul_u32_u24 v191, 1152, v191\n"
"v_lshl_add_u32 v191, v186, 3, v191\n"
"v_add_u32 v255, s63, v191\n"
"v_lshrrev_b32 v243, 3, v248\n"
"v_mul_u32_u24 v243, 24576, v243\n"
"v_and_b32 v247, 7, v248\n"
"v_lshl_add_u32 v243, v247, 4, v243\n"
"v_mul_u32_u24 v224, 24576, v190\n"
"v_and_b32 v246, 15, v248\n"
"v_lshl_add_u32 v224, v246, 3, v224\n"
"v_or_b32 v224, v242, v224\n"
"v_subrev_u32 v252, 65536, v227\n"
"s_mul_i32 s63, s41, 384\n"
"v_mul_u32_u24 v244, 192, v190\n"
"v_lshl_add_u32 v244, v246, 2, v244\n"
"v_add3_u32 v244, v244, v252, s63\n"
"v_cmp_lt_u32 vcc, 23, v248\n"
"v_lshl_add_u32 v249, v248, 2, v252\n"
"v_add_u32 v185, 96, v249\n"
"v_cndmask_b32 v249, v249, v185, vcc\n"
"s_lshl_b32 s63, s41, 9\n"
"v_add_u32 v252, s63, v252\n"
"v_lshl_add_u32 v251, v248, 1, v252\n"
"v_add_u32 v185, 208, v251\n"
"v_cndmask_b32 v251, v251, v185, vcc\n"
"v_lshl_add_u32 v185, v248, 3, v252\n"
"v_lshl_add_u32 v252, v247, 6, v252\n"
"v_lshl_add_u32 v252, v190, 4, v252\n"
"s_min_u32 s63, s41, 2\n"
"s_mul_i32 s63, s63, 192\n"
"v_add_u32 v183, s63, v248\n"
"v_mul_u32_u24 v183, 911, v183\n"
"s_lshl_b32 s63, s55, 3\n"
"v_lshrrev_b32 v180, 16, v183\n"
"v_add_u32 v180, s63, v180\n"
"v_add_u32 v183, 58304, v183\n"
"v_lshrrev_b32 v181, 16, v183\n"
"v_add_u32 v181, s63, v181\n"
"v_add_u32 v183, 58304, v183\n"
"v_lshrrev_b32 v182, 16, v183\n"
"v_add_u32 v182, s63, v182\n"
"v_lshlrev_b32 v250, 4, v248\n"
"v_add_u32 v184, s45, v250\n"
"s_cmp_eq_u32 s41, 3\n"
"s_cselect_b32 s63, 0x10000000, 0\n"
"v_or_b32 v250, s63, v250\n"
"AT_B3:\n"
"s_waitcnt lgkmcnt(0)\n"
"s_barrier\n"
"ds_read_b64 v[246:247], v227 offset:656\n"
"v_and_b32 v186, 4, v248\n"
"v_cmp_eq_u32 vcc, 0, v186\n"
"s_mov_b32 s56, 26402816\n"
"s_waitcnt lgkmcnt(0)\n"
"v_cndmask_b32 v186, v247, v246, vcc\n"
"v_readfirstlane_b32 s10, v246\n"
"v_not_b32 v186, v186\n"
"v_readfirstlane_b32 s11, v247\n"
"v_and_b32 v186, 0x7ff, v186\n"
"v_mul_u32_u24 v186, 2304, v186\n"
"v_add_u32 v241, v241, v186\n"
"v_or_b32 v242, v242, v241\n"
"s_andn2_b32 s12, 0x7ff, s10\n"
"s_andn2_b32 s13, 0x7ff, s11\n"
"s_cmp_ge_u32 s42, 192\n"
"s_cbranch_scc1 AT_F0\n"
"buffer_load_dwordx4 v[0:3], v241, s[28:31], s56 offen offset:0 sc1\n"
"buffer_load_dwordx4 v[4:7], v241, s[28:31], s56 offen offset:64 sc1\n"
"buffer_load_dwordx4 v[8:11], v241, s[28:31], s56 offen offset:128 sc1\n"
"buffer_load_dwordx4 v[12:15], v241, s[28:31], s56 offen offset:192 sc1\n"
"buffer_load_dwordx4 v[16:19], v242, s[28:31], s56 offen offset:256 sc1\n"
"v_mov_b32 v20, 0\n"
"v_mov_b32 v21, 0\n"
"ds_write_b64 v185, v[20:21] offset:4096\n"
"ds_read_b64_tr_b16 a[192:193], v255 offset:0\n"
"ds_read_b64_tr_b16 a[194:195], v255 offset:4608\n"
"ds_read_b64_tr_b16 a[196:197], v255 offset:32\n"
"ds_read_b64_tr_b16 a[198:199], v255 offset:4640\n"
"ds_read_b64_tr_b16 a[200:201], v255 offset:64\n"
"ds_read_b64_tr_b16 a[202:203], v255 offset:4672\n"
"ds_read_b64_tr_b16 a[204:205], v255 offset:96\n"
"ds_read_b64_tr_b16 a[206:207], v255 offset:4704\n"
"ds_read_b64_tr_b16 a[208:209], v255 offset:128\n"
"ds_read_b64_tr_b16 a[210:211], v255 offset:4736\n"
"ds_read_b64_tr_b16 a[212:213], v255 offset:160\n"
"ds_read_b64_tr_b16 a[214:215], v255 offset:4768\n"
"ds_read_b64_tr_b16 a[216:217], v255 offset:192\n"
"ds_read_b64_tr_b16 a[218:219], v255 offset:4800\n"
"ds_read_b64_tr_b16 a[220:221], v255 offset:224\n"
"ds_read_b64_tr_b16 a[222:223], v255 offset:4832\n"
"ds_read_b64_tr_b16 a[224:225], v255 offset:256\n"
"ds_read_b64_tr_b16 a[226:227], v255 offset:4864\n"
"s_mul_i32 s57, s41, 442368\n"
"s_lshl_b32 s58, s42, 7\n"
"s_add_u32 s57, s57, s58\n"
"s_add_u32 s57, s57, 56762368\n"
"s_add_u32 s62, s57, 196608\n"
"s_add_u32 s63, s57, 393216\n"
"v_mov_b32 v85, s85\n"
"v_mov_b32 v87, s85\n"
"v_mov_b32 v89, s85\n"
"v_mov_b32 v91, s85\n"
"v_mov_b32 v93, s85\n"
"s_mov_b32 s87, 0x80000\n"
"s_waitcnt vmcnt(0)\n"
"AT_A6:\n"
"v_max3_u32 v22, v0, v1, v2\n"
"v_max3_u32 v23, v3, v4, v5\n"
"v_max3_u32 v22, v22, v6, v7\n"
"v_max3_u32 v23, v23, v8, v9\n"
"v_max3_u32 v22, v22, v10, v11\n"
"v_max3_u32 v23, v23, v12, v13\n"
"v_max3_u32 v22, v22, v14, v15\n"
"v_max3_u32 v23, v23, v16, v17\n"
"v_max3_u32 v22, v22, v18, v19\n"
"v_max_u32 v22, v22, v23\n"
"v_cmp_eq_u32 vcc, -1, v22\n"
"s_nop 0\n"
"s_cbranch_vccz AT_A7\n"
"s_sub_u32 s87, s87, 1\n"
"s_cmp_eq_u32 s87, 0\n"
"s_cbranch_scc1 AT_A7\n"
"s_sleep 1\n"
"buffer_load_dwordx4 v[0:3], v241, s[28:31], s56 offen offset:0 sc1\n"
"buffer_load_dwordx4 v[4:7], v241, s[28:31], s56 offen offset:64 sc1\n"
"buffer_load_dwordx4 v[8:11], v241, s[28:31], s56 offen offset:128 sc1\n"
"buffer_load_dwordx4 v[12:15], v241, s[28:31], s56 offen offset:192 sc1\n"
"buffer_load_dwordx4 v[16:19], v242, s[28:31], s56 offen offset:256 sc1\n"
"s_waitcnt vmcnt(0)\n"
"s_branch AT_A6\n"
"AT_A7:\n"
"v_mfma_f32_16x16x32_bf16 v[24:27], v[0:3], a[128:131], 0\n"
"v_mfma_f32_16x16x32_bf16 v[28:31], v[0:3], a[132:135], 0\n"
"v_mfma_f32_16x16x32_bf16 v[32:35], v[0:3], a[136:139], 0\n"
"v_mfma_f32_16x16x32_bf16 v[24:27], v[4:7], a[140:143], v[24:27]\n"
"v_mfma_f32_16x16x32_bf16 v[28:31], v[4:7], a[144:147], v[28:31]\n"
"v_mfma_f32_16x16x32_bf16 v[32:35], v[4:7], a[148:151], v[32:35]\n"
"v_mfma_f32_16x16x32_bf16 v[24:27], v[8:11], a[152:155], v[24:27]\n"
"v_mfma_f32_16x16x32_bf16 v[28:31], v[8:11], a[156:159], v[28:31]\n"
"v_mfma_f32_16x16x32_bf16 v[32:35], v[8:11], a[160:163], v[32:35]\n"
"v_mfma_f32_16x16x32_bf16 v[24:27], v[12:15], a[164:167], v[24:27]\n"
"v_mfma_f32_16x16x32_bf16 v[28:31], v[12:15], a[168:171], v[28:31]\n"
"v_mfma_f32_16x16x32_bf16 v[32:35], v[12:15], a[172:175], v[32:35]\n"
"v_mfma_f32_16x16x32_bf16 v[24:27], v[16:19], a[176:179], v[24:27]\n"
"v_mfma_f32_16x16x32_bf16 v[28:31], v[16:19], a[180:183], v[28:31]\n"
"v_mfma_f32_16x16x32_bf16 v[32:35], v[16:19], a[184:187], v[32:35]\n"
"s_nop 5\n"
"v_add_f32 v24, v24, v25\n"
"v_add_f32 v28, v28, v29\n"
"v_add_f32 v32, v32, v33\n"
"s_mov_b32 exec_hi, 0\n"
"ds_write_b32 v244, v24 offset:2048\n"
"ds_write_b32 v244, v28 offset:2112\n"
"ds_write_b32 v244, v32 offset:2176\n"
"s_mov_b64 exec, -1\n"
"v_cmp_eq_u32 vcc, s10, v225\n"
"v_cmp_eq_u32 s[58:59], s11, v225\n"
"s_nop 0\n"
"s_or_b64 s[58:59], s[58:59], vcc\n"
"s_and_b64 exec, s[8:9], s[58:59]\n"
"s_cbranch_execz AT_A5\n"
"s_add_u32 s60, s48, 4608\n"
"v_mov_b32 v101, s85\n"
"v_mov_b32 v103, s85\n"
"v_mov_b32 v105, s85\n"
"v_mov_b32 v107, s85\n"
"v_mov_b32 v109, s85\n"
"v_mov_b32 v111, s85\n"
"v_mov_b32 v113, s85\n"
"v_mov_b32 v115, s85\n"
"v_mov_b32 v100, v200\n"
"v_mov_b32 v102, v201\n"
"buffer_store_dwordx4 v[100:103], v245, s[28:31], s60 offen offset:0 sc1\n"
"v_mov_b32 v104, v202\n"
"v_mov_b32 v106, v203\n"
"buffer_store_dwordx4 v[104:107], v245, s[28:31], s60 offen offset:16 sc1\n"
"v_mov_b32 v108, v204\n"
"v_mov_b32 v110, v205\n"
"buffer_store_dwordx4 v[108:111], v245, s[28:31], s60 offen offset:128 sc1\n"
"v_mov_b32 v112, v206\n"
"v_mov_b32 v114, v207\n"
"buffer_store_dwordx4 v[112:115], v245, s[28:31], s60 offen offset:144 sc1\n"
"v_mov_b32 v100, v208\n"
"v_mov_b32 v102, v209\n"
"buffer_store_dwordx4 v[100:103], v245, s[28:31], s60 offen offset:1536 sc1\n"
"v_mov_b32 v104, v210\n"
"v_mov_b32 v106, v211\n"
"buffer_store_dwordx4 v[104:107], v245, s[28:31], s60 offen offset:1552 sc1\n"
"v_mov_b32 v108, v212\n"
"v_mov_b32 v110, v213\n"
"buffer_store_dwordx4 v[108:111], v245, s[28:31], s60 offen offset:1664 sc1\n"
"v_mov_b32 v112, v214\n"
"v_mov_b32 v114, v215\n"
"buffer_store_dwordx4 v[112:115], v245, s[28:31], s60 offen offset:1680 sc1\n"
"v_mov_b32 v100, v216\n"
"v_mov_b32 v102, v217\n"
"buffer_store_dwordx4 v[100:103], v245, s[28:31], s60 offen offset:3072 sc1\n"
"v_mov_b32 v104, v218\n"
"v_mov_b32 v106, v219\n"
"buffer_store_dwordx4 v[104:107], v245, s[28:31], s60 offen offset:3088 sc1\n"
"v_mov_b32 v108, v220\n"
"v_mov_b32 v110, v221\n"
"buffer_store_dwordx4 v[108:111], v245, s[28:31], s60 offen offset:3200 sc1\n"
"v_mov_b32 v112, v222\n"
"v_mov_b32 v114, v223\n"
"buffer_store_dwordx4 v[112:115], v245, s[28:31], s60 offen offset:3216 sc1\n"
"AT_A5:\n"
"s_mov_b64 exec, -1\n"
"s_waitcnt lgkmcnt(0)\n"
"s_barrier\n"
"ds_read_b32 v0, v249 offset:2048\n"
"ds_read_b32 v1, v249 offset:2144\n"
"ds_read_b32 v2, v249 offset:2432\n"
"ds_read_b32 v3, v249 offset:2528\n"
"ds_read_b32 v4, v249 offset:2816\n"
"ds_read_b32 v5, v249 offset:2912\n"
"ds_read_b32 v6, v249 offset:3200\n"
"ds_read_b32 v7, v249 offset:3296\n"
"v_cmp_gt_u32 s[58:59], 32, v248\n"
"s_waitcnt lgkmcnt(5)\n"
"v_add_f32 v0, v0, v2\n"
"s_waitcnt lgkmcnt(1)\n"
"v_add_f32 v4, v4, v6\n"
"v_add_f32 v1, v1, v3\n"
"v_add_f32 v0, v0, v4\n"
"s_waitcnt lgkmcnt(0)\n"
"v_add_f32 v5, v5, v7\n"
"v_mul_f32 v2, 0xbfb8aa3b, v0\n"
"v_add_f32 v1, v1, v5\n"
"v_exp_f32 v2, v2\n"
"v_mul_f32 v0, v0, v1\n"
"v_add_f32 v2, 1.0, v2\n"
"v_rcp_f32 v2, v2\n"
"s_mov_b32 exec_hi, 0xffff\n"
"v_mul_f32 v0, v0, v2\n"
"v_cvt_pk_bf16_f32 v1, v0, v0\n"
"ds_write_b16 v251, v1 offset:4096\n"
"v_and_b32 v2, 0xffff0000, v1\n"
"v_sub_f32 v0, v0, v2\n"
"v_cvt_pk_bf16_f32 v0, v0, v0\n"
"ds_write_b16 v251, v0 offset:4160\n"
"s_mov_b64 exec, -1\n"
"ds_read_b128 v[8:11], v252 offset:4096\n"
"s_waitcnt lgkmcnt(0)\n"
"v_mfma_f32_16x16x32_bf16 v[40:43], v[8:11], a[192:195], 0\n"
"v_mfma_f32_16x16x32_bf16 v[44:47], v[8:11], a[196:199], 0\n"
"v_mfma_f32_16x16x32_bf16 v[48:51], v[8:11], a[200:203], 0\n"
"v_mfma_f32_16x16x32_bf16 v[52:55], v[8:11], a[204:207], 0\n"
"v_mfma_f32_16x16x32_bf16 v[56:59], v[8:11], a[208:211], 0\n"
"v_mfma_f32_16x16x32_bf16 v[60:63], v[8:11], a[212:215], 0\n"
"v_mfma_f32_16x16x32_bf16 v[64:67], v[8:11], a[216:219], 0\n"
"v_mfma_f32_16x16x32_bf16 v[68:71], v[8:11], a[220:223], 0\n"
"v_mfma_f32_16x16x32_bf16 v[72:75], v[8:11], a[224:227], 0\n"
"v_add_f32 v40, v40, v41\n"
"v_add_f32 v44, v44, v45\n"
"v_add_f32 v48, v48, v49\n"
"v_add_f32 v52, v52, v53\n"
"v_cndmask_b32 v84, v48, v40, s[58:59]\n"
"v_cndmask_b32 v86, v52, v44, s[58:59]\n"
"buffer_store_dwordx4 v[84:87], v243, s[28:31], s57 offen sc1\n"
"v_add_f32 v56, v56, v57\n"
"v_add_f32 v60, v60, v61\n"
"v_add_f32 v64, v64, v65\n"
"v_add_f32 v68, v68, v69\n"
"v_cndmask_b32 v88, v64, v56, s[58:59]\n"
"v_cndmask_b32 v90, v68, v60, s[58:59]\n"
"buffer_store_dwordx4 v[88:91], v243, s[28:31], s62 offen sc1\n"
"v_add_f32 v92, v72, v73\n"
"buffer_store_dwordx2 v[92:93], v224, s[28:31], s63 offen sc1\n"
"ds_read_b128 v[228:231], v184 offset:0\n"
"ds_read_b128 v[232:235], v184 offset:1024\n"
"ds_read_b128 v[236:239], v184 offset:2048\n"
"v_cmp_ne_u32 vcc, s12, v180\n"
"v_cmp_ne_u32 s[60:61], s13, v180\n"
"s_waitcnt lgkmcnt(2)\n"
"s_and_b64 exec, vcc, s[60:61]\n"
"buffer_store_dwordx4 v[228:231], v250, s[76:79], s46 offen offset:0 sc1\n"
"s_mov_b64 exec, -1\n"
"v_cmp_ne_u32 vcc, s12, v181\n"
"v_cmp_ne_u32 s[60:61], s13, v181\n"
"s_waitcnt lgkmcnt(1)\n"
"s_and_b64 exec, vcc, s[60:61]\n"
"buffer_store_dwordx4 v[232:235], v250, s[76:79], s46 offen offset:1024 sc1\n"
"s_mov_b64 exec, -1\n"
"v_cmp_ne_u32 vcc, s12, v182\n"
"v_cmp_ne_u32 s[60:61], s13, v182\n"
"s_waitcnt lgkmcnt(0)\n"
"s_and_b64 exec, vcc, s[60:61]\n"
"buffer_store_dwordx4 v[236:239], v250, s[76:79], s46 offen offset:2048 sc1\n"
"s_mov_b64 exec, -1\n"
"s_branch AT_F1\n"
"AT_F0:\n"
"v_cmp_eq_u32 vcc, s10, v225\n"
"v_cmp_eq_u32 s[58:59], s11, v225\n"
"s_nop 0\n"
"s_or_b64 s[58:59], s[58:59], vcc\n"
"s_and_b64 exec, s[8:9], s[58:59]\n"
"s_cbranch_execz AT_F0b\n"
"s_add_u32 s60, s48, 4608\n"
"v_mov_b32 v101, s85\n"
"v_mov_b32 v103, s85\n"
"v_mov_b32 v105, s85\n"
"v_mov_b32 v107, s85\n"
"v_mov_b32 v109, s85\n"
"v_mov_b32 v111, s85\n"
"v_mov_b32 v113, s85\n"
"v_mov_b32 v115, s85\n"
"v_mov_b32 v100, v200\n"
"v_mov_b32 v102, v201\n"
"buffer_store_dwordx4 v[100:103], v245, s[28:31], s60 offen offset:0 sc1\n"
"v_mov_b32 v104, v202\n"
"v_mov_b32 v106, v203\n"
"buffer_store_dwordx4 v[104:107], v245, s[28:31], s60 offen offset:16 sc1\n"
"v_mov_b32 v108, v204\n"
"v_mov_b32 v110, v205\n"
"buffer_store_dwordx4 v[108:111], v245, s[28:31], s60 offen offset:128 sc1\n"
"v_mov_b32 v112, v206\n"
"v_mov_b32 v114, v207\n"
"buffer_store_dwordx4 v[112:115], v245, s[28:31], s60 offen offset:144 sc1\n"
"v_mov_b32 v100, v208\n"
"v_mov_b32 v102, v209\n"
"buffer_store_dwordx4 v[100:103], v245, s[28:31], s60 offen offset:1536 sc1\n"
"v_mov_b32 v104, v210\n"
"v_mov_b32 v106, v211\n"
"buffer_store_dwordx4 v[104:107], v245, s[28:31], s60 offen offset:1552 sc1\n"
"v_mov_b32 v108, v212\n"
"v_mov_b32 v110, v213\n"
"buffer_store_dwordx4 v[108:111], v245, s[28:31], s60 offen offset:1664 sc1\n"
"v_mov_b32 v112, v214\n"
"v_mov_b32 v114, v215\n"
"buffer_store_dwordx4 v[112:115], v245, s[28:31], s60 offen offset:1680 sc1\n"
"v_mov_b32 v100, v216\n"
"v_mov_b32 v102, v217\n"
"buffer_store_dwordx4 v[100:103], v245, s[28:31], s60 offen offset:3072 sc1\n"
"v_mov_b32 v104, v218\n"
"v_mov_b32 v106, v219\n"
"buffer_store_dwordx4 v[104:107], v245, s[28:31], s60 offen offset:3088 sc1\n"
"v_mov_b32 v108, v220\n"
"v_mov_b32 v110, v221\n"
"buffer_store_dwordx4 v[108:111], v245, s[28:31], s60 offen offset:3200 sc1\n"
"v_mov_b32 v112, v222\n"
"v_mov_b32 v114, v223\n"
"buffer_store_dwordx4 v[112:115], v245, s[28:31], s60 offen offset:3216 sc1\n"
"AT_F0b:\n"
"s_mov_b64 exec, -1\n"
"ds_read_b128 v[228:231], v184 offset:0\n"
"ds_read_b128 v[232:235], v184 offset:1024\n"
"ds_read_b128 v[236:239], v184 offset:2048\n"
"v_cmp_ne_u32 vcc, s12, v180\n"
"v_cmp_ne_u32 s[60:61], s13, v180\n"
"s_waitcnt lgkmcnt(2)\n"
"s_and_b64 exec, vcc, s[60:61]\n"
"buffer_store_dwordx4 v[228:231], v250, s[76:79], s46 offen offset:0 sc1\n"
"s_mov_b64 exec, -1\n"
"v_cmp_ne_u32 vcc, s12, v181\n"
"v_cmp_ne_u32 s[60:61], s13, v181\n"
"s_waitcnt lgkmcnt(1)\n"
"s_and_b64 exec, vcc, s[60:61]\n"
"buffer_store_dwordx4 v[232:235], v250, s[76:79], s46 offen offset:1024 sc1\n"
"s_mov_b64 exec, -1\n"
"v_cmp_ne_u32 vcc, s12, v182\n"
"v_cmp_ne_u32 s[60:61], s13, v182\n"
"s_waitcnt lgkmcnt(0)\n"
"s_and_b64 exec, vcc, s[60:61]\n"
"buffer_store_dwordx4 v[236:239], v250, s[76:79], s46 offen offset:2048 sc1\n"
"s_mov_b64 exec, -1\n"
"s_sleep 28\n"
"AT_F1:\n"
"s_cmp_lt_u32 s42, 184\n"
"s_cbranch_scc1 AT_end\n"
"s_sub_u32 s56, s42, 184\n"
"s_mul_i32 s57, s56, 57\n"
"s_lshr_b32 s57, s57, 10\n"
"s_mul_i32 s58, s57, 144\n"
"s_mul_i32 s57, s57, 18\n"
"s_sub_u32 s57, s56, s57\n"
"s_lshr_b32 s61, s57, 2\n"
"s_lshl_b32 s61, s61, 5\n"
"s_add_u32 s58, s58, s61\n"
"s_and_b32 s61, s57, 1\n"
"s_lshl_b32 s62, s61, 3\n"
"s_bfe_u32 s63, s57, 0x10001\n"
"s_cmp_ge_u32 s57, 16\n"
"s_cselect_b32 s62, 0, s62\n"
"s_cselect_b32 s63, s61, s63\n"
"s_cselect_b64 vcc, -1, 0\n"
"s_add_u32 s58, s58, s62\n"
"s_cmp_eq_u32 s63, 1\n"
"s_cselect_b32 s59, s13, s12\n"
"s_cselect_b32 s60, s11, s10\n"
"s_mul_i32 s56, s56, 24576\n"
"s_add_u32 s56, s56, 56762368\n"
"s_mul_i32 s61, s59, 1152\n"
"s_add_u32 s61, s61, s58\n"
"s_lshl_b32 s61, s61, 3\n"
"s_add_u32 s61, s61, 37892608\n"
"v_lshlrev_b32 v30, 4, v253\n"
"v_add_u32 v31, 4096, v30\n"
"v_add_u32 v32, 8192, v30\n"
"v_add_u32 v33, 12288, v30\n"
"v_add_u32 v34, 16384, v30\n"
"v_add_u32 v35, 20480, v30\n"
"v_and_b32 v36, 15, v248\n"
"v_lshlrev_b32 v37, 3, v36\n"
"v_and_b32 v41, 1, v36\n"
"v_lshrrev_b32 v42, 1, v36\n"
"v_lshl_add_u32 v41, v41, 4, v42\n"
"v_cndmask_b32 v41, v41, v36, vcc\n"
"v_lshlrev_b32 v42, 3, v41\n"
"s_lshl_b32 s62, s58, 2\n"
"s_add_u32 s62, s62, s40\n"
"s_add_u32 s62, s62, 158720\n"
"v_lshl_add_u32 v38, v41, 2, s62\n"
"AT_pg18:\n"
"s_cbranch_execz AT_hr\n"
"ds_read_b32 v39, v38\n"
"s_mov_b32 s87, 0x80000\n"
"AT_F2:\n"
"buffer_load_dwordx4 v[0:3], v30, s[28:31], s56 offen sc1\n"
"buffer_load_dwordx4 v[4:7], v31, s[28:31], s56 offen sc1\n"
"buffer_load_dwordx4 v[8:11], v32, s[28:31], s56 offen sc1\n"
"buffer_load_dwordx4 v[12:15], v33, s[28:31], s56 offen sc1\n"
"buffer_load_dwordx4 v[16:19], v34, s[28:31], s56 offen sc1\n"
"buffer_load_dwordx4 v[20:23], v35, s[28:31], s56 offen sc1\n"
"buffer_load_dwordx2 v[24:25], v42, s[28:31], s61 offen sc1\n"
"s_and_b32 s62, s60, s14\n"
"s_ashr_i32 s63, s62, 31\n"
"s_not_b32 s63, s63\n"
"s_and_b32 s63, s63, s14\n"
"s_or_b32 s63, s63, 0x80000000\n"
"s_xor_b32 s62, s62, s63\n"
"v_mov_b32 v40, s62\n"
"v_mul_f32 v40, 0xbfb8aa3b, v40\n"
"v_exp_f32 v40, v40\n"
"s_nop 0\n"
"v_add_f32 v40, 1.0, v40\n"
"v_rcp_f32 v40, v40\n"
"s_waitcnt vmcnt(0) lgkmcnt(0)\n"
"v_xor_b32 v26, s85, v1\n"
"v_xor_b32 v27, s85, v3\n"
"v_bitop3_b32 v26, v5, s85, v26 bitop3:0xbe\n"
"v_bitop3_b32 v27, v7, s85, v27 bitop3:0xbe\n"
"v_bitop3_b32 v26, v9, s85, v26 bitop3:0xbe\n"
"v_bitop3_b32 v27, v11, s85, v27 bitop3:0xbe\n"
"v_bitop3_b32 v26, v13, s85, v26 bitop3:0xbe\n"
"v_bitop3_b32 v27, v15, s85, v27 bitop3:0xbe\n"
"v_bitop3_b32 v26, v17, s85, v26 bitop3:0xbe\n"
"v_bitop3_b32 v27, v19, s85, v27 bitop3:0xbe\n"
"v_bitop3_b32 v26, v21, s85, v26 bitop3:0xbe\n"
"v_bitop3_b32 v27, v23, s85, v27 bitop3:0xbe\n"
"v_bitop3_b32 v26, v25, s85, v26 bitop3:0xbe\n"
"v_or_b32 v26, v26, v27\n"
"v_cmp_ne_u32 vcc, 0, v26\n"
"s_nop 0\n"
"s_cbranch_vccz AT_F3\n"
"s_sub_u32 s87, s87, 1\n"
"s_cmp_eq_u32 s87, 0\n"
"s_cbranch_scc1 AT_F3\n"
"s_sleep 2\n"
"s_branch AT_F2\n"
"AT_F3:\n"
"v_add_f32 v0, v0, v4\n"
"v_add_f32 v2, v2, v6\n"
"v_add_f32 v8, v8, v12\n"
"v_add_f32 v10, v10, v14\n"
"v_add_f32 v16, v16, v20\n"
"v_add_f32 v18, v18, v22\n"
"v_add_f32 v0, v0, v8\n"
"v_add_f32 v2, v2, v10\n"
"v_add_f32 v0, v0, v16\n"
"v_add_f32 v1, v2, v18\n"
"v_mul_f32 v40, v40, v39\n"
"s_lshl_b32 s62, s41, 6\n"
"v_add3_u32 v28, v37, v227, s62\n"
"v_add_f32_dpp v0, v0, v0 row_ror:8 row_mask:0xf bank_mask:0xf\n"
"v_add_f32_dpp v1, v1, v1 row_ror:8 row_mask:0xf bank_mask:0xf\n"
"s_nop 0\n"
"v_mov_b32 v2, v0\n"
"v_mov_b32 v3, v1\n"
"s_nop 0\n"
"v_permlane16_swap_b32 v0, v2\n"
"v_permlane16_swap_b32 v1, v3\n"
"v_add_f32 v0, v0, v2\n"
"v_add_f32 v1, v1, v3\n"
"v_mov_b32 v2, v0\n"
"v_mov_b32 v3, v1\n"
"s_nop 0\n"
"v_permlane32_swap_b32 v0, v2\n"
"v_permlane32_swap_b32 v1, v3\n"
"v_add_f32 v0, v0, v2\n"
"v_add_f32 v1, v1, v3\n"
"s_mov_b64 exec, 0xff\n"
"ds_write_b64 v28, v[0:1] offset:1024\n"
"s_mov_b64 exec, -1\n"
"v_lshl_add_u32 v28, v36, 2, v227\n"
"s_waitcnt lgkmcnt(0)\n"
"s_barrier\n"
"s_cmp_lg_u32 s41, 0\n"
"s_cbranch_scc1 AT_end\n"
"ds_read_b32 v0, v28 offset:1024\n"
"ds_read_b32 v1, v28 offset:1088\n"
"ds_read_b32 v2, v28 offset:1152\n"
"ds_read_b32 v3, v28 offset:1216\n"
"s_mul_i32 s61, s59, 576\n"
"s_add_u32 s61, s61, s58\n"
"s_lshl_b32 s61, s61, 1\n"
"v_lshlrev_b32 v5, 1, v41\n"
"s_waitcnt lgkmcnt(0)\n"
"v_add_f32 v0, v0, v1\n"
"v_add_f32 v2, v2, v3\n"
"v_add_f32 v0, v0, v2\n"
"v_fma_f32 v0, v0, v40, v24\n"
"v_cvt_pk_bf16_f32 v0, v0, v0\n"
"s_mov_b64 exec, 0xffff\n"
"buffer_store_short v0, v5, s[76:79], s61 offen sc1\n"
"s_branch AT_end\n"
"AT_x1:\n"
"s_mov_b32 s87, 0x80000\n"
"AT_x3:\n"
"buffer_load_dword v249, v215, s[28:31], s23 offen sc1\n"
"s_waitcnt vmcnt(0)\n"
"v_cmp_ne_u32 vcc, s85, v249\n"
"s_sub_u32 s87, s87, 1\n"
"s_cbranch_vccz AT_x2\n"
"s_cmp_eq_u32 s87, 0\n"
"s_cbranch_scc1 AT_x2\n"
"s_sleep 1\n"
"s_branch AT_x3\n"
"AT_fb:\n"
"s_waitcnt vmcnt(0) lgkmcnt(0)\n"
"s_barrier\n"
"s_mov_b32 s45, 1\n"
"s_mov_b32 s87, 32\n"
"s_lshl_b32 s84, s41, 12\n"
"s_lshl_b32 s56, s43, 19\n"
"s_add_u32 s84, s84, s56\n"
"v_mov_b32 v160, 0xff800000\n"
"v_mov_b32 v161, 0xff800000\n"
"v_mov_b32 v162, 0xff800000\n"
"v_mov_b32 v163, 0xff800000\n"
"AT_fbl:\n"
"s_mov_b32 m0, s86\n"
"s_nop 0\n"
"buffer_load_dwordx4 v246, s[8:11], s84 offen offset:0 lds\n"
"buffer_load_dwordx4 v246, s[8:11], s84 offen offset:1024 lds\n"
"buffer_load_dwordx4 v247, s[8:11], s84 offen offset:2048 lds\n"
"buffer_load_dwordx4 v247, s[8:11], s84 offen offset:3072 lds\n"
"s_add_u32 s84, s84, 16384\n"
"s_waitcnt vmcnt(0)\n"
"s_barrier\n"
"ds_read_b128 a[128:131], v240 offset:0\n"
"ds_read_b128 a[132:135], v240 offset:256\n"
"ds_read_b128 a[136:139], v240 offset:512\n"
"ds_read_b128 a[140:143], v240 offset:768\n"
"ds_read_b128 a[144:147], v240 offset:4096\n"
"ds_read_b128 a[148:151], v240 offset:4352\n"
"ds_read_b128 a[152:155], v240 offset:4608\n"
"ds_read_b128 a[156:159], v240 offset:4864\n"
"s_waitcnt lgkmcnt(0)\n"
"v_mfma_f32_16x16x32_bf16 v[128:131], a[128:131], v[64:67], 0\n"
"v_mfma_f32_16x16x32_bf16 v[128:131], a[132:135], v[68:71], v[128:131]\n"
"v_mfma_f32_16x16x32_bf16 v[128:131], a[136:139], v[72:75], v[128:131]\n"
"v_mfma_f32_16x16x32_bf16 v[128:131], a[140:143], v[76:79], v[128:131]\n"
"v_mfma_f32_16x16x32_bf16 v[132:135], a[144:147], v[64:67], 0\n"
"v_mfma_f32_16x16x32_bf16 v[132:135], a[148:151], v[68:71], v[132:135]\n"
"v_mfma_f32_16x16x32_bf16 v[132:135], a[152:155], v[72:75], v[132:135]\n"
"v_mfma_f32_16x16x32_bf16 v[132:135], a[156:159], v[76:79], v[132:135]\n"
"v_mfma_f32_16x16x32_bf16 v[136:139], a[128:131], v[80:83], 0\n"
"v_mfma_f32_16x16x32_bf16 v[136:139], a[132:135], v[84:87], v[136:139]\n"
"v_mfma_f32_16x16x32_bf16 v[136:139], a[136:139], v[88:91], v[136:139]\n"
"v_mfma_f32_16x16x32_bf16 v[136:139], a[140:143], v[92:95], v[136:139]\n"
"v_mfma_f32_16x16x32_bf16 v[140:143], a[144:147], v[80:83], 0\n"
"v_mfma_f32_16x16x32_bf16 v[140:143], a[148:151], v[84:87], v[140:143]\n"
"v_mfma_f32_16x16x32_bf16 v[140:143], a[152:155], v[88:91], v[140:143]\n"
"v_mfma_f32_16x16x32_bf16 v[140:143], a[156:159], v[92:95], v[140:143]\n"
"v_mfma_f32_16x16x32_bf16 v[144:147], a[128:131], v[96:99], 0\n"
"v_mfma_f32_16x16x32_bf16 v[144:147], a[132:135], v[100:103], v[144:147]\n"
"v_mfma_f32_16x16x32_bf16 v[144:147], a[136:139], v[104:107], v[144:147]\n"
"v_mfma_f32_16x16x32_bf16 v[144:147], a[140:143], v[108:111], v[144:147]\n"
"v_mfma_f32_16x16x32_bf16 v[148:151], a[144:147], v[96:99], 0\n"
"v_mfma_f32_16x16x32_bf16 v[148:151], a[148:151], v[100:103], v[148:151]\n"
"v_mfma_f32_16x16x32_bf16 v[148:151], a[152:155], v[104:107], v[148:151]\n"
"v_mfma_f32_16x16x32_bf16 v[148:151], a[156:159], v[108:111], v[148:151]\n"
"v_mfma_f32_16x16x32_bf16 v[152:155], a[128:131], v[112:115], 0\n"
"v_mfma_f32_16x16x32_bf16 v[152:155], a[132:135], v[116:119], v[152:155]\n"
"v_mfma_f32_16x16x32_bf16 v[152:155], a[136:139], v[120:123], v[152:155]\n"
"v_mfma_f32_16x16x32_bf16 v[152:155], a[140:143], v[124:127], v[152:155]\n"
"v_mfma_f32_16x16x32_bf16 v[156:159], a[144:147], v[112:115], 0\n"
"v_mfma_f32_16x16x32_bf16 v[156:159], a[148:151], v[116:119], v[156:159]\n"
"v_mfma_f32_16x16x32_bf16 v[156:159], a[152:155], v[120:123], v[156:159]\n"
"v_mfma_f32_16x16x32_bf16 v[156:159], a[156:159], v[124:127], v[156:159]\n"
"s_nop 7\n"
"v_max3_f32 v160, v160, v128, v129\n"
"v_max3_f32 v160, v160, v130, v131\n"
"v_max3_f32 v160, v160, v132, v133\n"
"v_max3_f32 v160, v160, v134, v135\n"
"v_max3_f32 v161, v161, v136, v137\n"
"v_max3_f32 v161, v161, v138, v139\n"
"v_max3_f32 v161, v161, v140, v141\n"
"v_max3_f32 v161, v161, v142, v143\n"
"v_max3_f32 v162, v162, v144, v145\n"
"v_max3_f32 v162, v162, v146, v147\n"
"v_max3_f32 v162, v162, v148, v149\n"
"v_max3_f32 v162, v162, v150, v151\n"
"v_max3_f32 v163, v163, v152, v153\n"
"v_max3_f32 v163, v163, v154, v155\n"
"v_max3_f32 v163, v163, v156, v157\n"
"v_max3_f32 v163, v163, v158, v159\n"
"ds_read_b128 a[128:131], v240 offset:8192\n"
"ds_read_b128 a[132:135], v240 offset:8448\n"
"ds_read_b128 a[136:139], v240 offset:8704\n"
"ds_read_b128 a[140:143], v240 offset:8960\n"
"ds_read_b128 a[144:147], v240 offset:12288\n"
"ds_read_b128 a[148:151], v240 offset:12544\n"
"ds_read_b128 a[152:155], v240 offset:12800\n"
"ds_read_b128 a[156:159], v240 offset:13056\n"
"s_waitcnt lgkmcnt(0)\n"
"v_mfma_f32_16x16x32_bf16 v[128:131], a[128:131], v[64:67], 0\n"
"v_mfma_f32_16x16x32_bf16 v[128:131], a[132:135], v[68:71], v[128:131]\n"
"v_mfma_f32_16x16x32_bf16 v[128:131], a[136:139], v[72:75], v[128:131]\n"
"v_mfma_f32_16x16x32_bf16 v[128:131], a[140:143], v[76:79], v[128:131]\n"
"v_mfma_f32_16x16x32_bf16 v[132:135], a[144:147], v[64:67], 0\n"
"v_mfma_f32_16x16x32_bf16 v[132:135], a[148:151], v[68:71], v[132:135]\n"
"v_mfma_f32_16x16x32_bf16 v[132:135], a[152:155], v[72:75], v[132:135]\n"
"v_mfma_f32_16x16x32_bf16 v[132:135], a[156:159], v[76:79], v[132:135]\n"
"v_mfma_f32_16x16x32_bf16 v[136:139], a[128:131], v[80:83], 0\n"
"v_mfma_f32_16x16x32_bf16 v[136:139], a[132:135], v[84:87], v[136:139]\n"
"v_mfma_f32_16x16x32_bf16 v[136:139], a[136:139], v[88:91], v[136:139]\n"
"v_mfma_f32_16x16x32_bf16 v[136:139], a[140:143], v[92:95], v[136:139]\n"
"v_mfma_f32_16x16x32_bf16 v[140:143], a[144:147], v[80:83], 0\n"
"v_mfma_f32_16x16x32_bf16 v[140:143], a[148:151], v[84:87], v[140:143]\n"
"v_mfma_f32_16x16x32_bf16 v[140:143], a[152:155], v[88:91], v[140:143]\n"
"v_mfma_f32_16x16x32_bf16 v[140:143], a[156:159], v[92:95], v[140:143]\n"
"v_mfma_f32_16x16x32_bf16 v[144:147], a[128:131], v[96:99], 0\n"
"v_mfma_f32_16x16x32_bf16 v[144:147], a[132:135], v[100:103], v[144:147]\n"
"v_mfma_f32_16x16x32_bf16 v[144:147], a[136:139], v[104:107], v[144:147]\n"
"v_mfma_f32_16x16x32_bf16 v[144:147], a[140:143], v[108:111], v[144:147]\n"
"v_mfma_f32_16x16x32_bf16 v[148:151], a[144:147], v[96:99], 0\n"
"v_mfma_f32_16x16x32_bf16 v[148:151], a[148:151], v[100:103], v[148:151]\n"
"v_mfma_f32_16x16x32_bf16 v[148:151], a[152:155], v[104:107], v[148:151]\n"
"v_mfma_f32_16x16x32_bf16 v[148:151], a[156:159], v[108:111], v[148:151]\n"
"v_mfma_f32_16x16x32_bf16 v[152:155], a[128:131], v[112:115], 0\n"
"v_mfma_f32_16x16x32_bf16 v[152:155], a[132:135], v[116:119], v[152:155]\n"
"v_mfma_f32_16x16x32_bf16 v[152:155], a[136:139], v[120:123], v[152:155]\n"
"v_mfma_f32_16x16x32_bf16 v[152:155], a[140:143], v[124:127], v[152:155]\n"
"v_mfma_f32_16x16x32_bf16 v[156:159], a[144:147], v[112:115], 0\n"
"v_mfma_f32_16x16x32_bf16 v[156:159], a[148:151], v[116:119], v[156:159]\n"
"v_mfma_f32_16x16x32_bf16 v[156:159], a[152:155], v[120:123], v[156:159]\n"
"v_mfma_f32_16x16x32_bf16 v[156:159], a[156:159], v[124:127], v[156:159]\n"
"s_nop 7\n"
"v_max3_f32 v160, v160, v128, v129\n"
"v_max3_f32 v160, v160, v130, v131\n"
"v_max3_f32 v160, v160, v132, v133\n"
"v_max3_f32 v160, v160, v134, v135\n"
"v_max3_f32 v161, v161, v136, v137\n"
"v_max3_f32 v161, v161, v138, v139\n"
"v_max3_f32 v161, v161, v140, v141\n"
"v_max3_f32 v161, v161, v142, v143\n"
"v_max3_f32 v162, v162, v144, v145\n"
"v_max3_f32 v162, v162, v146, v147\n"
"v_max3_f32 v162, v162, v148, v149\n"
"v_max3_f32 v162, v162, v150, v151\n"
"v_max3_f32 v163, v163, v152, v153\n"
"v_max3_f32 v163, v163, v154, v155\n"
"v_max3_f32 v163, v163, v156, v157\n"
"v_max3_f32 v163, v163, v158, v159\n"
"s_barrier\n"
"s_sub_u32 s87, s87, 1\n"
"s_cmp_lg_u32 s87, 0\n"
"s_cbranch_scc1 AT_fbl\n"
"v_mov_b32 v164, v160\n"
"s_nop 1\n"
"v_permlane32_swap_b32 v160, v164\n"
"v_max_f32 v160, v160, v164\n"
"v_mov_b32 v164, v160\n"
"s_nop 1\n"
"v_permlane16_swap_b32 v160, v164\n"
"v_max_f32 v160, v160, v164\n"
"v_sub_f32 v24, 0, v160\n"
"v_sub_f32 v25, 0, v160\n"
"v_sub_f32 v26, 0, v160\n"
"v_sub_f32 v27, 0, v160\n"
"v_mov_b32 v164, v161\n"
"s_nop 1\n"
"v_permlane32_swap_b32 v161, v164\n"
"v_max_f32 v161, v161, v164\n"
"v_mov_b32 v164, v161\n"
"s_nop 1\n"
"v_permlane16_swap_b32 v161, v164\n"
"v_max_f32 v161, v161, v164\n"
"v_sub_f32 v28, 0, v161\n"
"v_sub_f32 v29, 0, v161\n"
"v_sub_f32 v30, 0, v161\n"
"v_sub_f32 v31, 0, v161\n"
"v_mov_b32 v164, v162\n"
"s_nop 1\n"
"v_permlane32_swap_b32 v162, v164\n"
"v_max_f32 v162, v162, v164\n"
"v_mov_b32 v164, v162\n"
"s_nop 1\n"
"v_permlane16_swap_b32 v162, v164\n"
"v_max_f32 v162, v162, v164\n"
"v_sub_f32 v32, 0, v162\n"
"v_sub_f32 v33, 0, v162\n"
"v_sub_f32 v34, 0, v162\n"
"v_sub_f32 v35, 0, v162\n"
"v_mov_b32 v164, v163\n"
"s_nop 1\n"
"v_permlane32_swap_b32 v163, v164\n"
"v_max_f32 v163, v163, v164\n"
"v_mov_b32 v164, v163\n"
"s_nop 1\n"
"v_permlane16_swap_b32 v163, v164\n"
"v_max_f32 v163, v163, v164\n"
"v_sub_f32 v36, 0, v163\n"
"v_sub_f32 v37, 0, v163\n"
"v_sub_f32 v38, 0, v163\n"
"v_sub_f32 v39, 0, v163\n"
"s_and_b32 s62, s42, 7\n"
"s_mul_i32 s46, s62, 663552\n"
"s_lshr_b32 s47, s43, 2\n"
"s_mul_i32 s47, s47, 1179648\n"
"s_getpc_b64 s[32:33]\n"
"AT_pc2:\n"
"s_sub_u32 s32, s32, AT_pc2-AT_start\n"
"s_subb_u32 s33, s33, 0\n"
"s_and_b32 s33, s33, 0xffff\n"
"s_mov_b32 s34, AT_kend-AT_start\n"
"s_mov_b32 s35, 0x20000\n"
"s_lshr_b32 s62, s42, 3\n"
"s_lshl_b32 s62, s62, 2\n"
"s_add_u32 s62, s62, s41\n"
"s_and_b32 s62, s62, 31\n"
"s_lshl_b32 s48, s62, 12\n"
"s_lshl_b32 s54, s44, 2\n"
"s_add_u32 s54, s54, s41\n"
"s_mul_i32 s54, s54, 36864\n"
"s_lshr_b32 s62, s43, 2\n"
"s_mul_i32 s62, s62, 2359296\n"
"s_add_u32 s54, s54, s62\n"
"s_add_u32 s54, s54, 139264\n"
"s_bfe_u32 s62, s43, 0x10001\n"
"s_mul_i32 s62, s62, 9216\n"
"s_add_u32 s54, s54, s62\n"
"s_and_b32 s62, s43, 1\n"
"s_lshl_b32 s49, s62, 3\n"
"s_add_u32 s49, s49, s54\n"
"s_add_u32 s49, s49, 4096\n"
"s_mul_i32 s62, s62, 5120\n"
"s_add_u32 s54, s54, s62\n"
"s_and_b32 s62, s43, 1\n"
"s_lshl_b32 s62, s62, 10\n"
"s_sub_u32 s54, s54, s62\n"
"s_add_u32 s49, s54, 4096\n"
"s_lshl_b32 s52, s44, 4\n"
"s_add_u32 s52, s52, s43\n"
"s_mul_i32 s52, s52, 18432\n"
"s_mul_i32 s62, s41, 5\n"
"s_min_u32 s62, s62, 13\n"
"s_lshl_b32 s62, s62, 10\n"
"s_add_u32 s52, s52, s62\n"
"s_add_u32 s52, s52, 26402816\n"
"s_add_u32 s53, s52, 4096\n"
"v_mov_b32 v40, 0x3f803f80\n"
"v_mov_b32 v41, 0x3f803f80\n"
"v_mov_b32 v42, 0x3f803f80\n"
"v_mov_b32 v43, 0x3f803f80\n"
"v_mov_b32 v20, 0\n"
"v_mov_b32 v21, 0\n"
"v_mov_b32 v22, 0\n"
"v_mov_b32 v23, 0\n"
"v_mov_b32 v44, 0\n"
"v_mov_b32 v52, 0\n"
"v_mov_b32 v216, -1\n"
"v_mov_b32 v217, -1\n"
"v_mov_b32 v218, -1\n"
"v_mov_b32 v219, -1\n"
"v_lshl_add_u32 v255, v248, 6, s48\n"
"v_lshlrev_b32 v207, 4, v248\n"
"v_add_u32 v208, 18432, v207\n"
"s_lshl_b32 s84, s41, 12\n"
"s_lshl_b32 s56, s43, 19\n"
"s_add_u32 s84, s84, s56\n"
"v_mov_b32 v251, 0x40000000\n"
"v_mov_b32 v252, 0x40000000\n"
"v_mov_b32 v253, 0x40000000\n"
"v_add_f32 v148, 0xc2480000, v161\n"
"v_add_f32 v149, 0xc2480000, v161\n"
"v_add_f32 v150, 0xc2480000, v161\n"
"v_add_f32 v151, 0xc2480000, v161\n"
"v_add_f32 v156, 0xc2480000, v163\n"
"v_add_f32 v157, 0xc2480000, v163\n"
"v_add_f32 v158, 0xc2480000, v163\n"
"v_add_f32 v159, 0xc2480000, v163\n"
"s_mov_b32 m0, s86\n"
"s_nop 0\n"
"buffer_load_dwordx4 v246, s[8:11], s84 offen offset:0 lds\n"
"buffer_load_dwordx4 v246, s[8:11], s84 offen offset:1024 lds\n"
"buffer_load_dwordx4 v247, s[8:11], s84 offen offset:2048 lds\n"
"buffer_load_dwordx4 v247, s[8:11], s84 offen offset:3072 lds\n"
"s_add_u32 m0, s86, 16384\n"
"s_nop 0\n"
"buffer_load_dwordx4 v246, s[12:15], s84 offen offset:0 lds\n"
"buffer_load_dwordx4 v247, s[12:15], s84 offen offset:1024 lds\n"
"buffer_load_dwordx4 v246, s[12:15], s84 offen offset:2048 lds\n"
"buffer_load_dwordx4 v247, s[12:15], s84 offen offset:3072 lds\n"
"s_add_u32 s84, s84, 16384\n"
"s_add_u32 m0, s86, 32768\n"
"s_nop 0\n"
"buffer_load_dwordx4 v246, s[8:11], s84 offen offset:0 lds\n"
"buffer_load_dwordx4 v246, s[8:11], s84 offen offset:1024 lds\n"
"buffer_load_dwordx4 v247, s[8:11], s84 offen offset:2048 lds\n"
"buffer_load_dwordx4 v247, s[8:11], s84 offen offset:3072 lds\n"
"s_add_u32 m0, s86, 49152\n"
"s_nop 0\n"
"buffer_load_dwordx4 v246, s[12:15], s84 offen offset:0 lds\n"
"buffer_load_dwordx4 v247, s[12:15], s84 offen offset:1024 lds\n"
"buffer_load_dwordx4 v246, s[12:15], s84 offen offset:2048 lds\n"
"buffer_load_dwordx4 v247, s[12:15], s84 offen offset:3072 lds\n"
"s_add_u32 s84, s84, 16384\n"
"s_add_u32 m0, s86, 65536\n"
"s_nop 0\n"
"buffer_load_dwordx4 v246, s[8:11], s84 offen offset:0 lds\n"
"buffer_load_dwordx4 v246, s[8:11], s84 offen offset:1024 lds\n"
"buffer_load_dwordx4 v247, s[8:11], s84 offen offset:2048 lds\n"
"buffer_load_dwordx4 v247, s[8:11], s84 offen offset:3072 lds\n"
"s_add_u32 m0, s86, 81920\n"
"s_nop 0\n"
"buffer_load_dwordx4 v246, s[12:15], s84 offen offset:0 lds\n"
"buffer_load_dwordx4 v247, s[12:15], s84 offen offset:1024 lds\n"
"buffer_load_dwordx4 v246, s[12:15], s84 offen offset:2048 lds\n"
"buffer_load_dwordx4 v247, s[12:15], s84 offen offset:3072 lds\n"
"s_add_u32 s84, s84, 16384\n"
"s_waitcnt vmcnt(16)\n"
"s_barrier\n"
"ds_read_b128 a[128:131], v240 offset:0\n"
"ds_read_b128 a[132:135], v240 offset:256\n"
"ds_read_b128 a[136:139], v240 offset:512\n"
"ds_read_b128 a[140:143], v240 offset:768\n"
"ds_read_b128 a[144:147], v240 offset:4096\n"
"ds_read_b128 a[148:151], v240 offset:4352\n"
"ds_read_b128 a[152:155], v240 offset:4608\n"
"ds_read_b128 a[156:159], v240 offset:4864\n"
"ds_read_b64_tr_b16 a[192:193], v242 offset:16384\n"
"ds_read_b64_tr_b16 a[194:195], v242 offset:20480\n"
"ds_read_b64_tr_b16 a[196:197], v243 offset:16384\n"
"ds_read_b64_tr_b16 a[198:199], v243 offset:20480\n"
"ds_read_b64_tr_b16 a[200:201], v242 offset:16640\n"
"ds_read_b64_tr_b16 a[202:203], v242 offset:20736\n"
"ds_read_b64_tr_b16 a[204:205], v243 offset:16640\n"
"ds_read_b64_tr_b16 a[206:207], v243 offset:20736\n"
"ds_read_b64_tr_b16 a[208:209], v242 offset:16896\n"
"ds_read_b64_tr_b16 a[210:211], v242 offset:20992\n"
"ds_read_b64_tr_b16 a[212:213], v243 offset:16896\n"
"ds_read_b64_tr_b16 a[214:215], v243 offset:20992\n"
"ds_read_b64_tr_b16 a[216:217], v242 offset:17152\n"
"ds_read_b64_tr_b16 a[218:219], v242 offset:21248\n"
"ds_read_b64_tr_b16 a[220:221], v243 offset:17152\n"
"ds_read_b64_tr_b16 a[222:223], v243 offset:21248\n"
"s_waitcnt lgkmcnt(0)\n"
"s_branch AT_main\n"
"AT_end:\n"
"AT_kend:\n"
  :: [kp] "s"(__builtin_amdgcn_kernarg_segment_ptr()), [blk] "s"(blockIdx.x), [wid] "s"(wid), [lds] "s"(ldsb) : "v0", "v1", "v2", "v3", "v4", "v5", "v6", "v7", "v8", "v9", "v10", "v11", "v12", "v13", "v14", "v15", "v16", "v17", "v18", "v19", "v20", "v21", "v22", "v23", "v24", "v25", "v26", "v27", "v28", "v29", "v30", "v31", "v32", "v33", "v34", "v35", "v36", "v37", "v38", "v39", "v40", "v41", "v42", "v43", "v44", "v45", "v46", "v47", "v48", "v49", "v50", "v51", "v52", "v53", "v54", "v55", "v56", "v57", "v58", "v59", "v60", "v61", "v62", "v63", "v64", "v65", "v66", "v67", "v68", "v69", "v70", "v71", "v72", "v73", "v74", "v75", "v76", "v77", "v78", "v79", "v80", "v81", "v82", "v83", "v84", "v85", "v86", "v87", "v88", "v89", "v90", "v91", "v92", "v93", "v94", "v95", "v96", "v97", "v98", "v99", "v100", "v101", "v102", "v103", "v104", "v105", "v106", "v107", "v108", "v109", "v110", "v111", "v112", "v113", "v114", "v115", "v116", "v117", "v118", "v119", "v120", "v121", "v122", "v123", "v124", "v125", "v126", "v127", "v128", "v129", "v130", "v131", "v132", "v133", "v134", "v135", "v136", "v137", "v138", "v139", "v140", "v141", "v142", "v143", "v144", "v145", "v146", "v147", "v148", "v149", "v150", "v151", "v152", "v153", "v154", "v155", "v156", "v157", "v158", "v159", "v160", "v161", "v162", "v163", "v164", "v165", "v166", "v167", "v168", "v169", "v170", "v171", "v172", "v173", "v174", "v175", "v176", "v177", "v178", "v179", "v180", "v181", "v182", "v183", "v184", "v185", "v186", "v187", "v188", "v189", "v190", "v191", "v192", "v193", "v194", "v195", "v196", "v197", "v198", "v199", "v200", "v201", "v202", "v203", "v204", "v205", "v206", "v207", "v208", "v209", "v210", "v211", "v212", "v213", "v214", "v215", "v216", "v217", "v218", "v219", "v220", "v221", "v222", "v223", "v224", "v225", "v226", "v227", "v228", "v229", "v230", "v231", "v232", "v233", "v234", "v235", "v236", "v237", "v238", "v239", "v240", "v241", "v242", "v243", "v244", "v245", "v246", "v247", "v248", "v249", "v250", "v251", "v252", "v253", "v254", "v255", "a0", "a1", "a2", "a3", "a4", "a5", "a6", "a7", "a8", "a9", "a10", "a11", "a12", "a13", "a14", "a15", "a16", "a17", "a18", "a19", "a20", "a21", "a22", "a23", "a24", "a25", "a26", "a27", "a28", "a29", "a30", "a31", "a32", "a33", "a34", "a35", "a36", "a37", "a38", "a39", "a40", "a41", "a42", "a43", "a44", "a45", "a46", "a47", "a48", "a49", "a50", "a51", "a52", "a53", "a54", "a55", "a56", "a57", "a58", "a59", "a60", "a61", "a62", "a63", "a64", "a65", "a66", "a67", "a68", "a69", "a70", "a71", "a72", "a73", "a74", "a75", "a76", "a77", "a78", "a79", "a80", "a81", "a82", "a83", "a84", "a85", "a86", "a87", "a88", "a89", "a90", "a91", "a92", "a93", "a94", "a95", "a96", "a97", "a98", "a99", "a100", "a101", "a102", "a103", "a104", "a105", "a106", "a107", "a108", "a109", "a110", "a111", "a112", "a113", "a114", "a115", "a116", "a117", "a118", "a119", "a120", "a121", "a122", "a123", "a124", "a125", "a126", "a127", "a128", "a129", "a130", "a131", "a132", "a133", "a134", "a135", "a136", "a137", "a138", "a139", "a140", "a141", "a142", "a143", "a144", "a145", "a146", "a147", "a148", "a149", "a150", "a151", "a152", "a153", "a154", "a155", "a156", "a157", "a158", "a159", "a160", "a161", "a162", "a163", "a164", "a165", "a166", "a167", "a168", "a169", "a170", "a171", "a172", "a173", "a174", "a175", "a176", "a177", "a178", "a179", "a180", "a181", "a182", "a183", "a184", "a185", "a186", "a187", "a188", "a189", "a190", "a191", "a192", "a193", "a194", "a195", "a196", "a197", "a198", "a199", "a200", "a201", "a202", "a203", "a204", "a205", "a206", "a207", "a208", "a209", "a210", "a211", "a212", "a213", "a214", "a215", "a216", "a217", "a218", "a219", "a220", "a221", "a222", "a223", "a224", "a225", "a226", "a227", "a228", "a229", "a230", "a231", "a232", "a233", "a234", "a235", "a236", "a237", "a238", "a239", "a240", "a241", "a242", "a243", "a244", "a245", "a246", "a247", "a248", "a249", "a250", "a251", "a252", "a253", "a254", "a255", "s8", "s9", "s10", "s11", "s12", "s13", "s14", "s15", "s16", "s17", "s18", "s19", "s20", "s21", "s22", "s23", "s24", "s25", "s26", "s27", "s28", "s29", "s30", "s31", "s32", "s33", "s34", "s35", "s36", "s37", "s38", "s39", "s40", "s41", "s42", "s43", "s44", "s45", "s46", "s47", "s48", "s49", "s50", "s51", "s52", "s53", "s54", "s55", "s56", "s57", "s58", "s59", "s60", "s61", "s62", "s63", "s64", "s65", "s66", "s67", "s68", "s69", "s70", "s71", "s72", "s73", "s74", "s75", "s76", "s77", "s78", "s79", "s80", "s81", "s82", "s83", "s84", "s85", "s86", "s87", "s88", "s89", "s90", "s91", "vcc", "scc", "memory");
}

extern "C" void kernel_launch(void* const* d_in, const int* in_sizes, int n_in, void* d_out, int out_size, void* d_ws, size_t ws_size,
                              hipStream_t stream) {
  const unsigned F = 0x20000u;
  KArgs a;
  a.k = {d_in[1], HKV * SQ * 256u, F}; a.v = {d_in[2], HKV * SQ * 256u, F}; a.x = {d_in[0], SQ * DM * 2u, F}; a.wq = {d_in[3], DM * 4096u * 2u, F};
  a.wo = {d_in[4], DM * 4096u * 2u, F}; a.ws = {d_ws, (unsigned)WS_END, F}; a.c = {d_in[10], DM * 2u, F}; a.wmod = {d_in[11], DM * 6u * DM * 2u, F};
  a.wg = {d_in[7], DM * FF * 2u, F}; a.wu = {d_in[8], DM * FF * 2u, F}; a.wd = {d_in[9], DM * FF * 2u, F}; a.out = {d_out, SQ * DM * 2u, F}; a.wr = {d_in[6], DM * 2u, F};
  (void)hipFuncSetAttribute((const void*)k_main, hipFuncAttributeMaxDynamicSharedMemorySize, 163840);
  k_main<<<256, 256, 163840, stream>>>(a);
}
```

```cpp
#include <hip/hip_runtime.h>
#include <stdint.h>


constexpr int SQ = 2048, DM = 576, HKV = 16, FF = 4608;

constexpr size_t WS_ST  = 69632;
constexpr size_t WS_END = 35282944;

struct Buf { const void* p; unsigned bytes; unsigned fmt; };
struct KArgs { Buf k, v, x, wq, wo, ws, c, wmod, wg, wu, wd, out, wr; };

__global__ __launch_bounds__(256, 1) __attribute__((aligned(4096))) void k_main(KArgs args) {
  extern __shared__ __attribute__((aligned(16))) char lds[];
  const unsigned wid = __builtin_amdgcn_readfirstlane(threadIdx.x >> 6);
  const unsigned ldsb = (unsigned)(size_t)lds;
  asm volatile(
"AT_start:\n"
"s_load_dwordx16 s[24:39], %[kp], 64\n"
"s_load_dwordx16 s[8:23], %[kp], 0\n"
"s_load_dwordx16 s[64:79], %[kp], 128\n"
"s_load_dwordx4 s[80:83], %[kp], 192\n"
"s_mov_b32 s42, %[blk]\n"
"s_mov_b32 s41, %[wid]\n"
"s_mov_b32 s40, %[lds]\n"
"s_mov_b32 s45, 0\n"
"v_mbcnt_lo_u32_b32 v248, -1, 0\n"
"v_mbcnt_hi_u32_b32 v248, -1, v248\n"
"s_and_b32 s56, s42, 3\n"
"s_lshr_b32 s57, s42, 3\n"
"s_and_b32 s58, s57, 3\n"
"s_lshl_b32 s56, s56, 2\n"
"s_add_u32 s43, s56, s58\n"
"s_lshr_b32 s44, s57, 2\n"
"s_and_b32 s56, s42, 7\n"
"s_lshr_b32 s56, s56, 2\n"
"s_lshl_b32 s56, s56, 3\n"
"s_add_u32 s44, s44, s56\n"
"s_lshr_b32 s56, s42, 5\n"
"s_lshl_b32 s56, s56, 3\n"
"s_and_b32 s58, s42, 7\n"
"s_add_u32 s56, s56, s58\n"
"s_lshl_b32 s56, s56, 2\n"
"s_and_b32 s57, s57, 3\n"
"s_add_u32 s55, s56, s57\n"
"v_lshrrev_b32 v226, 4, v248\n"
"v_bfe_u32 v227, v248, 2, 2\n"
"v_and_b32 v228, 3, v248\n"
"v_and_b32 v229, 15, v248\n"
"v_lshrrev_b32 v230, 3, v248\n"
"v_and_b32 v231, 7, v248\n"
"s_lshl_b32 s56, s41, 6\n"
"v_add_u32 v234, s56, v248\n"
"v_lshrrev_b32 v232, 1, v234\n"
"v_and_b32 v233, 1, v234\n"
"v_lshlrev_b32 v236, 1, v232\n"
"v_mul_u32_u24 v232, 6912, v232\n"
"v_lshl_add_u32 v235, v233, 4, v232\n"
"s_lshl_b32 s7, s55, 5\n"
"s_add_u32 s84, s55, 0\n"
"s_and_b32 s84, s84, 3\n"
"s_mul_i32 s3, s84, 884736\n"
"s_add_u32 s3, s3, s7\n"
"s_lshl_b32 s84, s84, 8\n"
"s_add_u32 s86, s55, 1\n"
"s_and_b32 s86, s86, 3\n"
"s_mul_i32 s4, s86, 884736\n"
"s_add_u32 s4, s4, s7\n"
"s_lshl_b32 s86, s86, 8\n"
"s_add_u32 s87, s55, 2\n"
"s_and_b32 s87, s87, 3\n"
"s_mul_i32 s5, s87, 884736\n"
"s_add_u32 s5, s5, s7\n"
"s_lshl_b32 s87, s87, 8\n"
"s_add_u32 s54, s55, 3\n"
"s_and_b32 s54, s54, 3\n"
"s_mul_i32 s6, s54, 884736\n"
"s_add_u32 s6, s6, s7\n"
"s_lshl_b32 s54, s54, 8\n"
"s_add_u32 s7, s7, 3538944\n"
"s_lshl_b32 s63, s44, 4\n"
"s_add_u32 s63, s63, s43\n"
"s_mul_i32 s46, s63, 9216\n"
"s_cmp_eq_u32 s41, 3\n"
"s_cselect_b32 s61, 0x10000000, 0\n"
"v_lshrrev_b32 v220, 3, v234\n"
"v_mul_u32_u24 v233, 1152, v231\n"
"v_lshl_add_u32 v233, v220, 4, v233\n"
"v_or_b32 v222, s61, v233\n"
"s_waitcnt lgkmcnt(0)\n"
"s_load_dword s85, s[28:29], 65536\n"
"s_cmp_lt_u32 s55, 216\n"
"s_cselect_b32 s38, s38, 0\n"
"buffer_load_dwordx4 v[192:195], v222, s[16:19], s46 offen offset:0\n"
"buffer_load_dwordx4 v[196:199], v222, s[16:19], s46 offen offset:384\n"
"buffer_load_dwordx4 v[200:203], v222, s[16:19], s46 offen offset:768\n"
"s_mul_i32 s56, s63, 9216\n"
"s_add_u32 s56, s56, 26386432\n"
"v_mov_b32 v214, 0\n"
"buffer_load_dword v250, v214, s[28:31], s56 offen\n"
"buffer_load_dword v250, v214, s[28:31], 0 offen offset:32\n"
"buffer_load_dwordx4 v[144:147], v235, s[36:39], s3 offen\n"
"buffer_load_ushort v164, v236, s[32:35], s84 offen\n"
"buffer_load_ushort v165, v236, s[32:35], s86 offen\n"
"buffer_load_ushort v166, v236, s[32:35], s87 offen\n"
"buffer_load_ushort v167, v236, s[32:35], s54 offen\n"
"buffer_load_ushort v168, v236, s[32:35], 0 offen offset:1024\n"
"s_lshl_b32 s50, s41, 17\n"
"s_lshl_b32 s56, s43, 9\n"
"s_add_u32 s50, s50, s56\n"
"s_lshl_b32 s56, s41, 11\n"
"s_add_u32 s51, s40, s56\n"
"v_lshlrev_b32 v232, 13, v227\n"
"v_lshl_add_u32 v232, v226, 6, v232\n"
"v_lshl_add_u32 v217, v228, 4, v232\n"
"v_lshlrev_b32 v232, 8, v220\n"
"v_lshl_add_u32 v232, v231, 4, v232\n"
"v_or_b32 v223, s61, v232\n"
"s_lshr_b32 s47, s63, 1\n"
"s_mul_i32 s47, s47, 18432\n"
"s_and_b32 s56, s63, 1\n"
"s_lshl_b32 s56, s56, 7\n"
"s_add_u32 s47, s47, s56\n"
"s_add_u32 s47, s47, 26386432\n"
"s_add_u32 s48, s47, 6144\n"
"s_add_u32 s49, s47, 12288\n"
"s_add_u32 s56, s40, 146944\n"
"v_lshl_add_u32 v220, v231, 3, s56\n"
"s_lshl_b32 s56, s41, 6\n"
"v_add_u32 v221, s56, v220\n"
"v_min_u32 v212, 1, v234\n"
"v_lshlrev_b32 v212, 28, v212\n"
"s_lshl_b32 s56, s63, 6\n"
"s_add_u32 s56, s56, 26370048\n"
"v_add_u32 v212, s56, v212\n"
"s_lshl_b32 s56, s44, 2\n"
"s_add_u32 s56, s56, s41\n"
"s_mul_i32 s2, s56, 36864\n"
"s_add_u32 s2, s2, 26386432\n"
"s_lshl_b32 s56, s56, 8\n"
"s_add_u32 s56, s56, 26370048\n"
"v_lshl_add_u32 v213, v228, 6, s56\n"
"v_lshlrev_b32 v249, 4, v248\n"
"v_add_u32 v255, 18432, v249\n"
"v_mov_b32 v215, 0x3f803f80\n"
"s_lshl_b32 s56, s41, 12\n"
"s_add_u32 s86, s40, s56\n"
"s_lshl_b32 s84, s41, 12\n"
"s_lshl_b32 s56, s43, 19\n"
"s_add_u32 s84, s84, s56\n"
"s_mov_b32 s61, 32768\n"
"s_mov_b32 s62, 37376\n"
"v_and_b32 v232, 2, v227\n"
"v_xor_b32 v232, v232, v226\n"
"v_lshlrev_b32 v233, 10, v227\n"
"v_lshl_add_u32 v233, v228, 6, v233\n"
"v_lshl_add_u32 v233, v232, 4, v233\n"
"v_add_u32 v240, s40, v233\n"
"v_add_u32 v241, 0x10000, v240\n"
"v_lshlrev_b32 v233, 10, v226\n"
"v_lshl_add_u32 v233, v227, 6, v233\n"
"v_lshl_add_u32 v233, v228, 3, v233\n"
"v_add_u32 v233, s40, v233\n"
"v_and_b32 v232, 1, v226\n"
"v_lshl_add_u32 v242, v232, 5, v233\n"
"v_xor_b32 v243, 32, v242\n"
"v_add_u32 v244, 0x10000, v242\n"
"v_add_u32 v245, 0x10000, v243\n"
"v_lshlrev_b32 v233, 8, v227\n"
"v_lshl_add_u32 v233, v226, 6, v233\n"
"v_lshl_add_u32 v246, v228, 4, v233\n"
"v_xor_b32 v247, 32, v246\n"
"v_mul_u32_u24 v233, 16392, v232\n"
"v_lshrrev_b32 v232, 1, v226\n"
"v_lshl_add_u32 v233, v232, 11, v233\n"
"v_lshl_add_u32 v233, v227, 6, v233\n"
"v_lshl_add_u32 v233, v228, 4, v233\n"
"v_add_u32 v218, s40, v233\n"
"v_add_u32 v219, 65568, v218\n"
"s_add_u32 s56, s40, 148480\n"
"v_lshl_add_u32 v224, v226, 4, s56\n"
"v_lshrrev_b32 v232, 3, v234\n"
"v_lshlrev_b32 v233, 8, v232\n"
"v_and_b32 v232, 1, v234\n"
"v_lshl_add_u32 v233, v232, 6, v233\n"
"v_bfe_u32 v232, v234, 1, 2\n"
"v_lshl_add_u32 v233, v232, 4, v233\n"
"v_add_u32 v216, s56, v233\n"
"v_lshlrev_b32 v225, 5, v234\n"
"v_mov_b32 v232, 0x40000000\n"
"v_cmp_gt_u32 vcc, 144, v234\n"
"s_nop 1\n"
"v_cndmask_b32 v225, v232, v225, vcc\n"
"s_lshl_b32 s56, s41, 8\n"
"s_add_u32 s57, s40, 147456\n"
"s_add_u32 s56, s56, s57\n"
"v_and_b32 v232, 1, v248\n"
"v_lshlrev_b32 v232, 5, v232\n"
"v_lshl_add_u32 v232, v226, 6, v232\n"
"v_add_u32 v238, s56, v232\n"
"v_lshlrev_b32 v232, 8, v226\n"
"v_lshl_add_u32 v232, v229, 2, v232\n"
"v_add_u32 v239, s57, v232\n"
"v_lshlrev_b32 v232, 6, v228\n"
"v_lshl_add_u32 v232, v226, 4, v232\n"
"v_add_u32 v254, s40, v232\n"
"s_lshr_b32 s57, s42, 3\n"
"s_lshl_b32 s57, s57, 2\n"
"s_add_u32 s57, s57, s41\n"
"s_sub_u32 s57, s57, 128\n"
"s_and_b32 s58, s42, 7\n"
"s_mul_i32 s59, s57, 9216\n"
"s_mul_i32 s60, s58, 1152\n"
"s_add_u32 s59, s59, s60\n"
"v_lshlrev_b32 v252, 6, v248\n"
"v_add_u32 v253, 0x40000000, v252\n"
"v_cmp_gt_u32 vcc, 18, v248\n"
"s_nop 1\n"
"v_cndmask_b32 v251, v253, v252, vcc\n"
"v_cmp_gt_u32 vcc, 36, v248\n"
"s_nop 1\n"
"v_cndmask_b32 v253, v253, v252, vcc\n"
"s_mul_i32 s60, s57, 1152\n"
"s_lshl_b32 s57, s60, 1\n"
"v_add_u32 v252, s60, v251\n"
"v_add_u32 v251, s59, v251\n"
"v_add_u32 v253, s57, v253\n"
"v_mov_b32 v237, 0xffff0000\n"
"buffer_load_dwordx4 v[148:151], v235, s[36:39], s4 offen\n"
"buffer_load_dwordx4 v[152:155], v235, s[36:39], s5 offen\n"
"buffer_load_dwordx4 v[156:159], v235, s[36:39], s6 offen\n"
"buffer_load_dwordx4 v[160:163], v235, s[36:39], s7 offen\n"
"s_waitcnt vmcnt(12)\n"
"v_dot2_f32_bf16 v204, v192, v215, 0\n"
"v_dot2_f32_bf16 v205, v192, v192, 0\n"
"v_dot2_f32_bf16 v206, v193, v215, 0\n"
"v_dot2_f32_bf16 v207, v193, v193, 0\n"
"v_dot2_f32_bf16 v204, v194, v215, v204\n"
"v_dot2_f32_bf16 v205, v194, v194, v205\n"
"v_dot2_f32_bf16 v206, v195, v215, v206\n"
"v_dot2_f32_bf16 v207, v195, v195, v207\n"
"v_dot2_f32_bf16 v204, v196, v215, v204\n"
"v_dot2_f32_bf16 v205, v196, v196, v205\n"
"v_dot2_f32_bf16 v206, v197, v215, v206\n"
"v_dot2_f32_bf16 v207, v197, v197, v207\n"
"v_dot2_f32_bf16 v204, v198, v215, v204\n"
"v_dot2_f32_bf16 v205, v198, v198, v205\n"
"v_dot2_f32_bf16 v206, v199, v215, v206\n"
"v_dot2_f32_bf16 v207, v199, v199, v207\n"
"v_dot2_f32_bf16 v204, v200, v215, v204\n"
"v_dot2_f32_bf16 v205, v200, v200, v205\n"
"v_dot2_f32_bf16 v206, v201, v215, v206\n"
"v_dot2_f32_bf16 v207, v201, v201, v207\n"
"v_dot2_f32_bf16 v204, v202, v215, v204\n"
"v_dot2_f32_bf16 v205, v202, v202, v205\n"
"v_dot2_f32_bf16 v206, v203, v215, v206\n"
"v_dot2_f32_bf16 v207, v203, v203, v207\n"
"s_nop 2\n"
"v_add_f32 v204, v204, v206\n"
"v_add_f32 v205, v205, v207\n"
"s_nop 0\n"
"v_add_f32_dpp v204, v204, v204 row_ror:8 row_mask:0xf bank_mask:0xf\n"
"v_add_f32_dpp v205, v205, v205 row_ror:8 row_mask:0xf bank_mask:0xf\n"
"s_nop 0\n"
"v_mov_b32 v206, v204\n"
"v_mov_b32 v207, v205\n"
"s_nop 0\n"
"v_permlane16_swap_b32 v204, v206\n"
"v_permlane16_swap_b32 v205, v207\n"
"v_add_f32 v204, v204, v206\n"
"v_add_f32 v205, v205, v207\n"
"v_mov_b32 v206, v204\n"
"v_mov_b32 v207, v205\n"
"s_nop 0\n"
"v_permlane32_swap_b32 v204, v206\n"
"v_permlane32_swap_b32 v205, v207\n"
"v_add_f32 v204, v204, v206\n"
"v_add_f32 v205, v205, v207\n"
"s_mov_b64 exec, 0xff\n"
"ds_write_b64 v221, v[204:205]\n"
"s_mov_b64 exec, -1\n"
"v_lshlrev_b32 v0, 16, v192\n"
"v_and_b32 v1, v237, v192\n"
"v_lshlrev_b32 v2, 16, v193\n"
"v_and_b32 v3, v237, v193\n"
"v_lshlrev_b32 v4, 16, v194\n"
"v_and_b32 v5, v237, v194\n"
"v_lshlrev_b32 v6, 16, v195\n"
"v_and_b32 v7, v237, v195\n"
"v_lshlrev_b32 v8, 16, v196\n"
"v_and_b32 v9, v237, v196\n"
"v_lshlrev_b32 v10, 16, v197\n"
"v_and_b32 v11, v237, v197\n"
"v_lshlrev_b32 v12, 16, v198\n"
"v_and_b32 v13, v237, v198\n"
"v_lshlrev_b32 v14, 16, v199\n"
"v_and_b32 v15, v237, v199\n"
"v_lshlrev_b32 v16, 16, v200\n"
"v_and_b32 v17, v237, v200\n"
"v_lshlrev_b32 v18, 16, v201\n"
"v_and_b32 v19, v237, v201\n"
"v_lshlrev_b32 v20, 16, v202\n"
"v_and_b32 v21, v237, v202\n"
"v_lshlrev_b32 v22, 16, v203\n"
"v_and_b32 v23, v237, v203\n"
"s_waitcnt lgkmcnt(0)\n"
"s_barrier\n"
"ds_read_b64 v[204:205], v220 offset:0\n"
"ds_read_b64 v[206:207], v220 offset:64\n"
"ds_read_b64 v[208:209], v220 offset:128\n"
"s_waitcnt lgkmcnt(1)\n"
"v_add_f32 v204, v204, v206\n"
"v_add_f32 v205, v205, v207\n"
"s_waitcnt lgkmcnt(0)\n"
"v_add_f32 v204, v204, v208\n"
"v_add_f32 v205, v205, v209\n"
"v_mul_f32 v204, 0x3ae38e39, v204\n"
"v_mul_f32 v205, 0x3ae38e39, v205\n"
"v_fma_f32 v205, -v204, v204, v205\n"
"v_max_f32 v205, 0, v205\n"
"v_add_f32 v205, 0x3727c5ac, v205\n"
"v_rsq_f32 v206, v205\n"
"s_nop 0\n"
"v_mul_f32_e64 v208, -v204, v206\n"
"v_pk_fma_f32 v[0:1], v[0:1], v[206:207], v[208:209] op_sel_hi:[1,0,0]\n"
"v_pk_fma_f32 v[2:3], v[2:3], v[206:207], v[208:209] op_sel_hi:[1,0,0]\n"
"v_pk_fma_f32 v[4:5], v[4:5], v[206:207], v[208:209] op_sel_hi:[1,0,0]\n"
"v_pk_fma_f32 v[6:7], v[6:7], v[206:207], v[208:209] op_sel_hi:[1,0,0]\n"
"v_cvt_pk_f16_f32 v192, v0, v1\n"
"v_cvt_pk_f16_f32 v193, v2, v3\n"
"v_cvt_pk_f16_f32 v194, v4, v5\n"
"v_cvt_pk_f16_f32 v195, v6, v7\n"
"buffer_store_dwordx4 v[192:195], v223, s[28:31], s47 offen sc1\n"
"v_pk_fma_f32 v[8:9], v[8:9], v[206:207], v[208:209] op_sel_hi:[1,0,0]\n"
"v_pk_fma_f32 v[10:11], v[10:11], v[206:207], v[208:209] op_sel_hi:[1,0,0]\n"
"v_pk_fma_f32 v[12:13], v[12:13], v[206:207], v[208:209] op_sel_hi:[1,0,0]\n"
"v_pk_fma_f32 v[14:15], v[14:15], v[206:207], v[208:209] op_sel_hi:[1,0,0]\n"
"v_cvt_pk_f16_f32 v196, v8, v9\n"
"v_cvt_pk_f16_f32 v197, v10, v11\n"
"v_cvt_pk_f16_f32 v198, v12, v13\n"
"v_cvt_pk_f16_f32 v199, v14, v15\n"
"buffer_store_dwordx4 v[196:199], v223, s[28:31], s48 offen sc1\n"
"v_pk_fma_f32 v[16:17], v[16:17], v[206:207], v[208:209] op_sel_hi:[1,0,0]\n"
"v_pk_fma_f32 v[18:19], v[18:19], v[206:207], v[208:209] op_sel_hi:[1,0,0]\n"
"v_pk_fma_f32 v[20:21], v[20:21], v[206:207], v[208:209] op_sel_hi:[1,0,0]\n"
"v_pk_fma_f32 v[22:23], v[22:23], v[206:207], v[208:209] op_sel_hi:[1,0,0]\n"
"v_cvt_pk_f16_f32 v200, v16, v17\n"
"v_cvt_pk_f16_f32 v201, v18, v19\n"
"v_cvt_pk_f16_f32 v202, v20, v21\n"
"v_cvt_pk_f16_f32 v203, v22, v23\n"
"buffer_store_dwordx4 v[200:203], v223, s[28:31], s49 offen sc1\n"
"s_waitcnt vmcnt(7)\n"
"v_lshlrev_b32 v164, 16, v164\n"
"v_mul_f32 v184, 0xbfb8aa3b, v164\n"
"v_lshlrev_b32 v165, 16, v165\n"
"v_mul_f32 v185, 0xbfb8aa3b, v165\n"
"s_add_u32 m0, s51, 98352\n"
"v_lshlrev_b32 v166, 16, v166\n"
"buffer_load_dwordx4 v217, s[20:23], s50 offen offset:0 lds\n"
"v_mul_f32 v186, 0xbfb8aa3b, v166\n"
"v_lshlrev_b32 v167, 16, v167\n"
"v_mul_f32 v187, 0xbfb8aa3b, v167\n"
"v_lshlrev_b32 v168, 16, v168\n"
"v_mul_f32 v188, 0xbfb8aa3b, v168\n"
"s_add_u32 m0, s51, 106288\n"
"v_exp_f32 v184, v184\n"
"buffer_load_dwordx4 v217, s[20:23], s50 offen offset:256 lds\n"
"s_add_u32 s50, s50, 32768\n"
"v_exp_f32 v185, v185\n"
"v_exp_f32 v186, v186\n"
"v_exp_f32 v187, v187\n"
"v_exp_f32 v188, v188\n"
"v_add_f32 v184, 1.0, v184\n"
"s_add_u32 m0, s51, 99376\n"
"v_add_f32 v185, 1.0, v185\n"
"buffer_load_dwordx4 v217, s[20:23], s50 offen offset:0 lds\n"
"v_add_f32 v186, 1.0, v186\n"
"v_add_f32 v187, 1.0, v187\n"
"v_add_f32 v188, 1.0, v188\n"
"v_rcp_f32 v184, v184\n"
"v_rcp_f32 v185, v185\n"
"s_add_u32 m0, s51, 107312\n"
"v_rcp_f32 v186, v186\n"
"buffer_load_dwordx4 v217, s[20:23], s50 offen offset:256 lds\n"
"s_add_u32 s50, s50, 32768\n"
"v_rcp_f32 v187, v187\n"
"v_rcp_f32 v188, v188\n"
"v_mul_f32 v164, v164, v184\n"
"v_mul_f32 v165, v165, v185\n"
"v_mul_f32 v166, v166, v186\n"
"v_mul_f32 v167, v167, v187\n"
"v_mul_f32 v168, v168, v188\n"
"v_cvt_pk_bf16_f32 v164, v164, v164\n"
"v_and_b32 v164, v237, v164\n"
"v_cvt_pk_bf16_f32 v165, v165, v165\n"
"v_and_b32 v165, v237, v165\n"
"v_cvt_pk_bf16_f32 v166, v166, v166\n"
"v_and_b32 v166, v237, v166\n"
"v_cvt_pk_bf16_f32 v167, v167, v167\n"
"v_and_b32 v167, v237, v167\n"
"v_cvt_pk_bf16_f32 v168, v168, v168\n"
"v_and_b32 v168, v237, v168\n"
"s_waitcnt vmcnt(16)\n"
"v_lshlrev_b32 v184, 16, v144\n"
"v_and_b32 v185, v237, v144\n"
"v_lshlrev_b32 v186, 16, v145\n"
"v_and_b32 v187, v237, v145\n"
"v_lshlrev_b32 v188, 16, v146\n"
"v_and_b32 v189, v237, v146\n"
"v_lshlrev_b32 v190, 16, v147\n"
"v_and_b32 v191, v237, v147\n"
"v_mul_f32 v176, v164, v184\n"
"v_mul_f32 v177, v164, v185\n"
"v_mul_f32 v178, v164, v186\n"
"v_mul_f32 v179, v164, v187\n"
"v_mul_f32 v180, v164, v188\n"
"v_mul_f32 v181, v164, v189\n"
"v_mul_f32 v182, v164, v190\n"
"v_mul_f32 v183, v164, v191\n"
"s_waitcnt vmcnt(10)\n"
"v_lshlrev_b32 v184, 16, v148\n"
"v_and_b32 v185, v237, v148\n"
"v_lshlrev_b32 v186, 16, v149\n"
"v_and_b32 v187, v237, v149\n"
"v_lshlrev_b32 v188, 16, v150\n"
"v_and_b32 v189, v237, v150\n"
"v_lshlrev_b32 v190, 16, v151\n"
"v_and_b32 v191, v237, v151\n"
"v_fmac_f32 v176, v165, v184\n"
"v_fmac_f32 v177, v165, v185\n"
"v_fmac_f32 v178, v165, v186\n"
"v_fmac_f32 v179, v165, v187\n"
"v_fmac_f32 v180, v165, v188\n"
"v_fmac_f32 v181, v165, v189\n"
"v_fmac_f32 v182, v165, v190\n"
"v_fmac_f32 v183, v165, v191\n"
"s_waitcnt vmcnt(9)\n"
"v_lshlrev_b32 v184, 16, v152\n"
"v_and_b32 v185, v237, v152\n"
"v_lshlrev_b32 v186, 16, v153\n"
"v_and_b32 v187, v237, v153\n"
"v_lshlrev_b32 v188, 16, v154\n"
"v_and_b32 v189, v237, v154\n"
"v_lshlrev_b32 v190, 16, v155\n"
"v_and_b32 v191, v237, v155\n"
"v_fmac_f32 v176, v166, v184\n"
"v_fmac_f32 v177, v166, v185\n"
"v_fmac_f32 v178, v166, v186\n"
"v_fmac_f32 v179, v166, v187\n"
"v_fmac_f32 v180, v166, v188\n"
"v_fmac_f32 v181, v166, v189\n"
"v_fmac_f32 v182, v166, v190\n"
"v_fmac_f32 v183, v166, v191\n"
"s_waitcnt vmcnt(8)\n"
"v_lshlrev_b32 v184, 16, v156\n"
"v_and_b32 v185, v237, v156\n"
"v_lshlrev_b32 v186, 16, v157\n"
"v_and_b32 v187, v237, v157\n"
"v_lshlrev_b32 v188, 16, v158\n"
"v_and_b32 v189, v237, v158\n"
"v_lshlrev_b32 v190, 16, v159\n"
"v_and_b32 v191, v237, v159\n"
"v_fmac_f32 v176, v167, v184\n"
"v_fmac_f32 v177, v167, v185\n"
"v_fmac_f32 v178, v167, v186\n"
"v_fmac_f32 v179, v167, v187\n"
"v_fmac_f32 v180, v167, v188\n"
"v_fmac_f32 v181, v167, v189\n"
"v_fmac_f32 v182, v167, v190\n"
"v_fmac_f32 v183, v167, v191\n"
"s_waitcnt vmcnt(7)\n"
"v_lshlrev_b32 v184, 16, v160\n"
"v_and_b32 v185, v237, v160\n"
"v_lshlrev_b32 v186, 16, v161\n"
"v_and_b32 v187, v237, v161\n"
"v_lshlrev_b32 v188, 16, v162\n"
"v_and_b32 v189, v237, v162\n"
"v_lshlrev_b32 v190, 16, v163\n"
"v_and_b32 v191, v237, v163\n"
"v_fmac_f32 v176, v168, v184\n"
"v_fmac_f32 v177, v168, v185\n"
"v_fmac_f32 v178, v168, v186\n"
"v_fmac_f32 v179, v168, v187\n"
"v_fmac_f32 v180, v168, v188\n"
"v_fmac_f32 v181, v168, v189\n"
"v_fmac_f32 v182, v168, v190\n"
"v_fmac_f32 v183, v168, v191\n"
"v_add_f32_dpp v176, v176, v176 row_ror:2 row_mask:0xf bank_mask:0xf\n"
"v_add_f32_dpp v177, v177, v177 row_ror:2 row_mask:0xf bank_mask:0xf\n"
"v_add_f32_dpp v178, v178, v178 row_ror:2 row_mask:0xf bank_mask:0xf\n"
"v_add_f32_dpp v179, v179, v179 row_ror:2 row_mask:0xf bank_mask:0xf\n"
"v_add_f32_dpp v180, v180, v180 row_ror:2 row_mask:0xf bank_mask:0xf\n"
"v_add_f32_dpp v181, v181, v181 row_ror:2 row_mask:0xf bank_mask:0xf\n"
"v_add_f32_dpp v182, v182, v182 row_ror:2 row_mask:0xf bank_mask:0xf\n"
"v_add_f32_dpp v183, v183, v183 row_ror:2 row_mask:0xf bank_mask:0xf\n"
"v_add_f32_dpp v176, v176, v176 row_ror:4 row_mask:0xf bank_mask:0xf\n"
"v_add_f32_dpp v177, v177, v177 row_ror:4 row_mask:0xf bank_mask:0xf\n"
"v_add_f32_dpp v178, v178, v178 row_ror:4 row_mask:0xf bank_mask:0xf\n"
"v_add_f32_dpp v179, v179, v179 row_ror:4 row_mask:0xf bank_mask:0xf\n"
"v_add_f32_dpp v180, v180, v180 row_ror:4 row_mask:0xf bank_mask:0xf\n"
"v_add_f32_dpp v181, v181, v181 row_ror:4 row_mask:0xf bank_mask:0xf\n"
"v_add_f32_dpp v182, v182, v182 row_ror:4 row_mask:0xf bank_mask:0xf\n"
"v_add_f32_dpp v183, v183, v183 row_ror:4 row_mask:0xf bank_mask:0xf\n"
"v_add_f32_dpp v176, v176, v176 row_ror:8 row_mask:0xf bank_mask:0xf\n"
"v_add_f32_dpp v177, v177, v177 row_ror:8 row_mask:0xf bank_mask:0xf\n"
"v_add_f32_dpp v178, v178, v178 row_ror:8 row_mask:0xf bank_mask:0xf\n"
"v_add_f32_dpp v179, v179, v179 row_ror:8 row_mask:0xf bank_mask:0xf\n"
"v_add_f32_dpp v180, v180, v180 row_ror:8 row_mask:0xf bank_mask:0xf\n"
"v_add_f32_dpp v181, v181, v181 row_ror:8 row_mask:0xf bank_mask:0xf\n"
"v_add_f32_dpp v182, v182, v182 row_ror:8 row_mask:0xf bank_mask:0xf\n"
"v_add_f32_dpp v183, v183, v183 row_ror:8 row_mask:0xf bank_mask:0xf\n"
"s_mov_b32 exec_lo, 0x00030003\n"
"s_mov_b32 exec_hi, 0x00030003\n"
"ds_write_b128 v238, v[176:179]\n"
"ds_write_b128 v238, v[180:183] offset:16\n"
"s_mov_b64 exec, -1\n"
"s_add_u32 s85, s85, 1\n"
"v_mov_b32 v189, s85\n"
"s_waitcnt vmcnt(0) lgkmcnt(0)\n"
"s_barrier\n"
"buffer_store_dword v189, v212, s[28:31], 0 offen sc1\n"
"ds_read_b32 v184, v239 offset:0\n"
"ds_read_b32 v185, v239 offset:64\n"
"ds_read_b32 v186, v239 offset:128\n"
"ds_read_b32 v187, v239 offset:192\n"
"s_lshl_b32 s56, s55, 7\n"
"s_add_u32 s56, s56, 32768\n"
"s_lshl_b32 s58, s41, 2\n"
"s_lshl_b32 s58, 15, s58\n"
"v_lshlrev_b32 v190, 3, v248\n"
"s_waitcnt lgkmcnt(2)\n"
"v_add_f32 v184, v184, v185\n"
"s_waitcnt lgkmcnt(0)\n"
"v_add_f32 v186, v186, v187\n"
"v_add_f32 v184, v184, v186\n"
"v_mov_b32 v185, v184\n"
"s_nop 1\n"
"v_permlane32_swap_b32 v184, v185\n"
"v_add_f32 v184, v184, v185\n"
"v_mov_b32 v185, v184\n"
"s_nop 1\n"
"v_permlane16_swap_b32 v184, v185\n"
"v_add_f32 v184, v184, v185\n"
"v_cvt_pk_bf16_f32 v188, v184, v184\n"
"v_and_b32 v188, v237, v188\n"
"s_mov_b32 exec_lo, s58\n"
"s_mov_b32 exec_hi, 0\n"
"buffer_store_dwordx2 v[188:189], v190, s[28:31], s56 offen sc1\n"
"s_mov_b64 exec, -1\n"
"s_getpc_b64 s[36:37]\n"
"AT_pc0:\n"
"s_sub_u32 s36, s36, AT_pc0-AT_start\n"
"s_subb_u32 s37, s37, 0\n"
"s_and_b32 s37, s37, 0xffff\n"
"s_mov_b32 s38, AT_kend-AT_start\n"
"s_mov_b32 s39, 0x20000\n"
"s_cmp_ge_u32 s42, 224\n"
"s_cselect_b32 s38, s38, 0\n"
"s_lshr_b32 s52, s42, 3\n"
"s_lshl_b32 s52, s52, 2\n"
"s_add_u32 s52, s52, s41\n"
"s_and_b32 s52, s52, 15\n"
"s_lshl_b32 s52, s52, 12\n"
"v_lshlrev_b32 v214, 6, v248\n"
"buffer_load_dword v250, v214, s[36:39], s52 offen\n"
"s_add_u32 s52, s52, 65536\n"
"buffer_load_dword v250, v214, s[36:39], s52 offen\n"
"s_add_u32 m0, s51, 114744\n"
"s_nop 0\n"
"buffer_load_dwordx4 v217, s[20:23], s50 offen offset:0 lds\n"
"s_add_u32 m0, s51, 122680\n"
"s_nop 0\n"
"buffer_load_dwordx4 v217, s[20:23], s50 offen offset:256 lds\n"
"s_add_u32 s50, s50, 32768\n"
"s_add_u32 m0, s51, 115768\n"
"s_nop 0\n"
"buffer_load_dwordx4 v217, s[20:23], s50 offen offset:0 lds\n"
"s_add_u32 m0, s51, 123704\n"
"s_nop 0\n"
"buffer_load_dwordx4 v217, s[20:23], s50 offen offset:256 lds\n"
"s_add_u32 s50, s50, 425984\n"
"s_mov_b32 m0, s51\n"
"s_nop 0\n"
"buffer_load_dwordx4 v217, s[20:23], s50 offen offset:0 lds\n"
"s_add_u32 m0, s51, 7936\n"
"s_nop 0\n"
"buffer_load_dwordx4 v217, s[20:23], s50 offen offset:256 lds\n"
"s_add_u32 s50, s50, 32768\n"
"s_add_u32 m0, s51, 1024\n"
"s_nop 0\n"
"buffer_load_dwordx4 v217, s[20:23], s50 offen offset:0 lds\n"
"s_add_u32 m0, s51, 8960\n"
"s_nop 0\n"
"buffer_load_dwordx4 v217, s[20:23], s50 offen offset:256 lds\n"
"s_add_u32 s50, s50, 32768\n"
"s_add_u32 m0, s51, 16392\n"
"s_nop 0\n"
"buffer_load_dwordx4 v217, s[20:23], s50 offen offset:0 lds\n"
"s_add_u32 m0, s51, 24328\n"
"s_nop 0\n"
"buffer_load_dwordx4 v217, s[20:23], s50 offen offset:256 lds\n"
"s_add_u32 s50, s50, 32768\n"
"s_add_u32 m0, s51, 17416\n"
"s_nop 0\n"
"buffer_load_dwordx4 v217, s[20:23], s50 offen offset:0 lds\n"
"s_add_u32 m0, s51, 25352\n"
"s_nop 0\n"
"buffer_load_dwordx4 v217, s[20:23], s50 offen offset:256 lds\n"
"s_add_u32 s50, s50, 425984\n"
"s_mov_b64 exec, 0\n"
"s_cmp_eq_u32 s41, 0\n"
"s_cbranch_scc1 AT_hw0_0\n"
"s_cmp_eq_u32 s41, 1\n"
"s_cbranch_scc1 AT_hw0_1\n"
"s_cmp_eq_u32 s41, 2\n"
"s_cbranch_scc1 AT_hw0_2\n"
"s_call_b64 %[kp], AT_pg4\n"
"s_call_b64 %[kp], AT_pg8\n"
"s_call_b64 %[kp], AT_pg12\n"
"s_branch AT_hx0\n"
"AT_pg19:\n"
"AT_pg20:\n"
"AT_hr:\n"
"s_setpc_b64 %[kp]\n"
"AT_hw0_0:\n"
"s_call_b64 %[kp], AT_pg1\n"
"s_call_b64 %[kp], AT_pg5\n"
"s_call_b64 %[kp], AT_pg9\n"
"s_branch AT_hx0\n"
"AT_hw0_1:\n"
"s_call_b64 %[kp], AT_pg2\n"
"s_call_b64 %[kp], AT_pg6\n"
"s_call_b64 %[kp], AT_pg10\n"
"s_branch AT_hx0\n"
"AT_hw0_2:\n"
"s_call_b64 %[kp], AT_pg3\n"
"s_call_b64 %[kp], AT_pg7\n"
"s_call_b64 %[kp], AT_pg11\n"
"AT_hx0:\n"
"s_mov_b64 exec, -1\n"
"buffer_load_dword v214, v213, s[28:31], 0 offen sc1\n"
"buffer_load_dwordx4 v[144:147], v225, s[28:31], s61 offen offset:0 sc1\n"
"buffer_load_dwordx4 v[148:151], v225, s[28:31], s61 offen offset:16 sc1\n"
"buffer_load_dwordx4 v[152:155], v225, s[28:31], s62 offen offset:0 sc1\n"
"buffer_load_dwordx4 v[156:159], v225, s[28:31], s62 offen offset:16 sc1\n"
"s_mov_b32 s87, 0x80000\n"
"s_waitcnt vmcnt(4)\n"
"AT_xc:\n"
"v_cmp_ne_u32 vcc, s85, v214\n"
"s_sub_u32 s87, s87, 1\n"
"s_cbranch_vccz AT_xk\n"
"s_cmp_eq_u32 s87, 0\n"
"s_cbranch_scc1 AT_xk\n"
"s_sleep 1\n"
"buffer_load_dword v214, v213, s[28:31], 0 offen sc1\n"
"s_waitcnt vmcnt(0)\n"
"s_branch AT_xc\n"
"AT_xk:\n"
"buffer_load_dwordx4 v[0:3], v249, s[28:31], s2 offen offset:0 sc1\n"
"buffer_load_dwordx4 v[4:7], v255, s[28:31], s2 offen offset:0 sc1\n"
"buffer_load_dwordx4 v[8:11], v249, s[28:31], s2 offen offset:1024 sc1\n"
"buffer_load_dwordx4 v[12:15], v255, s[28:31], s2 offen offset:1024 sc1\n"
"buffer_load_dwordx4 v[16:19], v249, s[28:31], s2 offen offset:2048 sc1\n"
"buffer_load_dwordx4 v[20:23], v255, s[28:31], s2 offen offset:2048 sc1\n"
"buffer_load_dwordx4 v[24:27], v249, s[28:31], s2 offen offset:3072 sc1\n"
"buffer_load_dwordx4 v[28:31], v255, s[28:31], s2 offen offset:3072 sc1\n"
"s_add_u32 s2, s2, 4096\n"
"buffer_load_dwordx4 v[32:35], v249, s[28:31], s2 offen offset:0 sc1\n"
"buffer_load_dwordx4 v[36:39], v255, s[28:31], s2 offen offset:0 sc1\n"
"s_mov_b32 s87, 0x80000\n"
"s_waitcnt vmcnt(10)\n"
"AT_gchk:\n"
"v_xor_b32 v192, s85, v145\n"
"v_xor_b32 v193, s85, v147\n"
"v_bitop3_b32 v192, v149, s85, v192 bitop3:0xbe\n"
"v_bitop3_b32 v193, v151, s85, v193 bitop3:0xbe\n"
"v_bitop3_b32 v192, v153, s85, v192 bitop3:0xbe\n"
"v_bitop3_b32 v193, v155, s85, v193 bitop3:0xbe\n"
"v_bitop3_b32 v192, v157, s85, v192 bitop3:0xbe\n"
"v_bitop3_b32 v193, v159, s85, v193 bitop3:0xbe\n"
"v_or_b32 v192, v192, v193\n"
"v_cmp_ne_u32 s[56:57], 0, v192\n"
"v_cmp_gt_u32 vcc, 144, v234\n"
"s_nop 1\n"
"s_and_b64 vcc, vcc, s[56:57]\n"
"s_cbranch_vccz AT_gok\n"
"s_sub_u32 s87, s87, 1\n"
"s_cmp_eq_u32 s87, 0\n"
"s_cbranch_scc1 AT_gok\n"
"s_sleep 2\n"
"buffer_load_dwordx4 v[144:147], v225, s[28:31], s61 offen offset:0 sc1\n"
"buffer_load_dwordx4 v[148:151], v225, s[28:31], s61 offen offset:16 sc1\n"
"buffer_load_dwordx4 v[152:155], v225, s[28:31], s62 offen offset:0 sc1\n"
"buffer_load_dwordx4 v[156:159], v225, s[28:31], s62 offen offset:16 sc1\n"
"s_waitcnt vmcnt(0)\n"
"s_branch AT_gchk\n"
"AT_gok:\n"
"v_mov_b32 v236, 0x3e0293ee\n"
"v_mul_f32 v192, v236, v144\n"
"v_fma_f32 v196, v152, v236, v236\n"
"v_mul_f32 v193, v236, v146\n"
"v_fma_f32 v197, v154, v236, v236\n"
"v_mul_f32 v194, v236, v148\n"
"v_fma_f32 v198, v156, v236, v236\n"
"v_mul_f32 v195, v236, v150\n"
"v_fma_f32 v199, v158, v236, v236\n"
"ds_write_b128 v216, v[196:199]\n"
"ds_write_b128 v216, v[192:195] offset:128\n"
"s_add_u32 m0, s51, 32784\n"
"s_nop 0\n"
"buffer_load_dwordx4 v217, s[20:23], s50 offen offset:0 lds\n"
"s_add_u32 m0, s51, 40720\n"
"s_nop 0\n"
"buffer_load_dwordx4 v217, s[20:23], s50 offen offset:256 lds\n"
"s_add_u32 s50, s50, 32768\n"
"s_add_u32 m0, s51, 33808\n"
"s_nop 0\n"
"buffer_load_dwordx4 v217, s[20:23], s50 offen offset:0 lds\n"
"s_add_u32 m0, s51, 41744\n"
"s_nop 0\n"
"buffer_load_dwordx4 v217, s[20:23], s50 offen offset:256 lds\n"
"s_add_u32 s50, s50, 32768\n"
"s_add_u32 m0, s51, 49176\n"
"s_nop 0\n"
"buffer_load_dwordx4 v217, s[20:23], s50 offen offset:0 lds\n"
"s_add_u32 m0, s51, 57112\n"
"s_nop 0\n"
"buffer_load_dwordx4 v217, s[20:23], s50 offen offset:256 lds\n"
"s_add_u32 s50, s50, 32768\n"
"s_add_u32 m0, s51, 50200\n"
"s_nop 0\n"
"buffer_load_dwordx4 v217, s[20:23], s50 offen offset:0 lds\n"
"s_add_u32 m0, s51, 58136\n"
"s_nop 0\n"
"buffer_load_dwordx4 v217, s[20:23], s50 offen offset:256 lds\n"
"s_add_u32 s50, s50, 425984\n"
"s_waitcnt vmcnt(31) lgkmcnt(0)\n"
"s_barrier\n"
"ds_read_b128 v[144:147], v224 offset:0\n"
"ds_read_b128 v[148:151], v224 offset:64\n"
"ds_read_b128 v[152:155], v224 offset:128\n"
"ds_read_b128 v[156:159], v224 offset:192\n"
"ds_read_b128 v[160:163], v224 offset:256\n"
"ds_read_b128 v[164:167], v224 offset:320\n"
"ds_read_b128 v[168:171], v224 offset:384\n"
"AT_pg1:\n"
"s_cbranch_execz AT_hr\n"
"ds_read_b128 v[172:175], v224 offset:448\n"
"s_waitcnt vmcnt(14)\n"
"ds_read_b64_tr_b16 a[160:161], v219 offset:32784\n"
"s_waitcnt lgkmcnt(6)\n"
"v_fma_mix_f32 v176, v0, v144, v152 op_sel:[0,0,0] op_sel_hi:[1,0,0]\n"
"v_fma_mix_f32 v177, v0, v145, v153 op_sel:[1,0,0] op_sel_hi:[1,0,0]\n"
"v_fma_mix_f32 v178, v1, v146, v154 op_sel:[0,0,0] op_sel_hi:[1,0,0]\n"
"ds_read_b64_tr_b16 a[162:163], v219 offset:33808\n"
"v_fma_mix_f32 v179, v1, v147, v155 op_sel:[1,0,0] op_sel_hi:[1,0,0]\n"
"s_waitcnt lgkmcnt(6)\n"
"v_fma_mix_f32 v180, v2, v148, v156 op_sel:[0,0,0] op_sel_hi:[1,0,0]\n"
"v_fma_mix_f32 v181, v2, v149, v157 op_sel:[1,0,0] op_sel_hi:[1,0,0]\n"
"ds_read_b64_tr_b16 a[164:165], v219 offset:36880\n"
"v_fma_mix_f32 v182, v3, v150, v158 op_sel:[0,0,0] op_sel_hi:[1,0,0]\n"
"v_fma_mix_f32 v183, v3, v151, v159 op_sel:[1,0,0] op_sel_hi:[1,0,0]\n"
"v_cvt_pk_bf16_f32 v0, v176, v177\n"
"ds_read_b64_tr_b16 a[166:167], v219 offset:37904\n"
"v_cvt_pk_bf16_f32 v1, v178, v179\n"
"v_cvt_pk_bf16_f32 v2, v180, v181\n"
"v_cvt_pk_bf16_f32 v3, v182, v183\n"
"ds_read_b64_tr_b16 a[168:169], v219 offset:32792\n"
"v_fma_mix_f32 v184, v4, v144, v152 op_sel:[0,0,0] op_sel_hi:[1,0,0]\n"
"v_fma_mix_f32 v185, v4, v145, v153 op_sel:[1,0,0] op_sel_hi:[1,0,0]\n"
"v_fma_mix_f32 v186, v5, v146, v154 op_sel:[0,0,0] op_sel_hi:[1,0,0]\n"
"ds_read_b64_tr_b16 a[170:171], v219 offset:33816\n"
"v_fma_mix_f32 v187, v5, v147, v155 op_sel:[1,0,0] op_sel_hi:[1,0,0]\n"
"v_fma_mix_f32 v188, v6, v148, v156 op_sel:[0,0,0] op_sel_hi:[1,0,0]\n"
"v_fma_mix_f32 v189, v6, v149, v157 op_sel:[1,0,0] op_sel_hi:[1,0,0]\n"
"ds_read_b64_tr_b16 a[172:173], v219 offset:36888\n"
"v_fma_mix_f32 v190, v7, v150, v158 op_sel:[0,0,0] op_sel_hi:[1,0,0]\n"
"v_fma_mix_f32 v191, v7, v151, v159 op_sel:[1,0,0] op_sel_hi:[1,0,0]\n"
"v_cvt_pk_bf16_f32 v4, v184, v185\n"
"ds_read_b64_tr_b16 a[174:175], v219 offset:37912\n"
"ds_read_b128 v[144:147], v224 offset:512\n"
"v_cvt_pk_bf16_f32 v5, v186, v187\n"
"v_cvt_pk_bf16_f32 v6, v188, v189\n"
"ds_read_b64_tr_b16 a[176:177], v219 offset:33040\n"
"v_cvt_pk_bf16_f32 v7, v190, v191\n"
"s_waitcnt lgkmcnt(11)\n"
"v_fma_mix_f32 v176, v8, v160, v168 op_sel:[0,0,0] op_sel_hi:[1,0,0]\n"
"v_fma_mix_f32 v177, v8, v161, v169 op_sel:[1,0,0] op_sel_hi:[1,0,0]\n"
"ds_read_b64_tr_b16 a[178:179], v219 offset:34064\n"
"v_fma_mix_f32 v178, v9, v162, v170 op_sel:[0,0,0] op_sel_hi:[1,0,0]\n"
"v_fma_mix_f32 v179, v9, v163, v171 op_sel:[1,0,0] op_sel_hi:[1,0,0]\n"
"s_waitcnt lgkmcnt(11)\n"
"v_fma_mix_f32 v180, v10, v164, v172 op_sel:[0,0,0] op_sel_hi:[1,0,0]\n"
"ds_read_b64_tr_b16 a[180:181], v219 offset:37136\n"
"ds_read_b128 v[148:151], v224 offset:576\n"
"v_fma_mix_f32 v181, v10, v165, v173 op_sel:[1,0,0] op_sel_hi:[1,0,0]\n"
"v_fma_mix_f32 v182, v11, v166, v174 op_sel:[0,0,0] op_sel_hi:[1,0,0]\n"
"ds_read_b64_tr_b16 a[182:183], v219 offset:38160\n"
"v_fma_mix_f32 v183, v11, v167, v175 op_sel:[1,0,0] op_sel_hi:[1,0,0]\n"
"v_cvt_pk_bf16_f32 v8, v176, v177\n"
"v_cvt_pk_bf16_f32 v9, v178, v179\n"
"ds_read_b64_tr_b16 a[184:185], v219 offset:33048\n"
"v_cvt_pk_bf16_f32 v10, v180, v181\n"
"v_cvt_pk_bf16_f32 v11, v182, v183\n"
"v_fma_mix_f32 v184, v12, v160, v168 op_sel:[0,0,0] op_sel_hi:[1,0,0]\n"
"ds_read_b64_tr_b16 a[186:187], v219 offset:34072\n"
"ds_read_b128 v[152:155], v224 offset:640\n"
"v_fma_mix_f32 v185, v12, v161, v169 op_sel:[1,0,0] op_sel_hi:[1,0,0]\n"
"v_fma_mix_f32 v186, v13, v162, v170 op_sel:[0,0,0] op_sel_hi:[1,0,0]\n"
"ds_read_b64_tr_b16 a[188:189], v219 offset:37144\n"
"v_fma_mix_f32 v187, v13, v163, v171 op_sel:[1,0,0] op_sel_hi:[1,0,0]\n"
"v_fma_mix_f32 v188, v14, v164, v172 op_sel:[0,0,0] op_sel_hi:[1,0,0]\n"
"v_fma_mix_f32 v189, v14, v165, v173 op_sel:[1,0,0] op_sel_hi:[1,0,0]\n"
"ds_read_b64_tr_b16 a[190:191], v219 offset:38168\n"
"v_fma_mix_f32 v190, v15, v166, v174 op_sel:[0,0,0] op_sel_hi:[1,0,0]\n"
"v_fma_mix_f32 v191, v15, v167, v175 op_sel:[1,0,0] op_sel_hi:[1,0,0]\n"
"v_cvt_pk_bf16_f32 v12, v184, v185\n"
"ds_read_b64_tr_b16 a[224:225], v219 offset:33296\n"
"ds_read_b128 v[156:159], v224 offset:704\n"
"ds_read_b128 v[160:163], v224 offset:768\n"
"v_cvt_pk_bf16_f32 v13, v186, v187\n"
"ds_read_b64_tr_b16 a[226:227], v219 offset:34320\n"
"v_cvt_pk_bf16_f32 v14, v188, v189\n"
"v_cvt_pk_bf16_f32 v15, v190, v191\n"
"s_waitcnt vmcnt(10)\n"
"ds_read_b64_tr_b16 a[228:229], v219 offset:37392\n"
"s_waitcnt lgkmcnt(7)\n"
"v_fma_mix_f32 v176, v16, v144, v152 op_sel:[0,0,0] op_sel_hi:[1,0,0]\n"
"v_fma_mix_f32 v177, v16, v145, v153 op_sel:[1,0,0] op_sel_hi:[1,0,0]\n"
"v_fma_mix_f32 v178, v17, v146, v154 op_sel:[0,0,0] op_sel_hi:[1,0,0]\n"
"ds_read_b64_tr_b16 a[230:231], v219 offset:38416\n"
"ds_read_b128 v[164:167], v224 offset:832\n"
"v_fma_mix_f32 v179, v17, v147, v155 op_sel:[1,0,0] op_sel_hi:[1,0,0]\n"
"s_waitcnt lgkmcnt(5)\n"
"v_fma_mix_f32 v180, v18, v148, v156 op_sel:[0,0,0] op_sel_hi:[1,0,0]\n"
"v_mfma_f32_16x16x32_bf16 a[0:3], a[160:163], v[0:3], 0\n"
"ds_read_b64_tr_b16 a[232:233], v219 offset:33304\n"
"v_fma_mix_f32 v181, v18, v149, v157 op_sel:[1,0,0] op_sel_hi:[1,0,0]\n"
"v_mfma_f32_16x16x32_bf16 a[0:3], a[164:167], v[8:11], a[0:3]\n"
"s_waitcnt vmcnt(23)\n"
"ds_read_b64_tr_b16 a[234:235], v219 offset:34328\n"
"v_fma_mix_f32 v182, v19, v150, v158 op_sel:[0,0,0] op_sel_hi:[1,0,0]\n"
"v_mfma_f32_16x16x32_bf16 a[4:7], a[160:163], v[4:7], 0\n"
"s_barrier\n"
"ds_read_b64_tr_b16 a[236:237], v219 offset:37400\n"
"buffer_load_dwordx4 v[40:43], v249, s[28:31], s2 offen offset:1024 sc1\n"
"v_mfma_f32_16x16x32_bf16 a[4:7], a[164:167], v[12:15], a[4:7]\n"
"ds_read_b64_tr_b16 a[238:239], v219 offset:38424\n"
"ds_read_b128 v[168:171], v224 offset:896\n"
"v_mfma_f32_16x16x32_bf16 a[8:11], a[168:171], v[0:3], 0\n"
"s_add_u32 m0, s51, 65568\n"
"ds_read_b64_tr_b16 a[240:241], v219 offset:33552\n"
"v_fma_mix_f32 v183, v19, v151, v159 op_sel:[1,0,0] op_sel_hi:[1,0,0]\n"
"v_mfma_f32_16x16x32_bf16 a[8:11], a[172:175], v[8:11], a[8:11]\n"
"ds_read_b64_tr_b16 a[242:243], v219 offset:34576\n"
"buffer_load_dwordx4 v217, s[20:23], s50 offen offset:0 lds\n"
"v_mfma_f32_16x16x32_bf16 a[12:15], a[168:171], v[4:7], 0\n"
"ds_read_b64_tr_b16 a[244:245], v219 offset:37648\n"
"ds_read_b128 v[172:175], v224 offset:960\n"
"v_cvt_pk_bf16_f32 v16, v176, v177\n"
"v_mfma_f32_16x16x32_bf16 a[12:15], a[172:175], v[12:15], a[12:15]\n"
"ds_read_b64_tr_b16 a[246:247], v219 offset:38672\n"
"v_cvt_pk_bf16_f32 v17, v178, v179\n"
"v_mfma_f32_16x16x32_bf16 a[16:19], a[176:179], v[0:3], 0\n"
"ds_read_b64_tr_b16 a[248:249], v219 offset:33560\n"
"v_cvt_pk_bf16_f32 v18, v180, v181\n"
"v_mfma_f32_16x16x32_bf16 a[16:19], a[180:183], v[8:11], a[16:19]\n"
"ds_read_b64_tr_b16 a[250:251], v219 offset:34584\n"
"v_cvt_pk_bf16_f32 v19, v182, v183\n"
"v_mfma_f32_16x16x32_bf16 a[20:23], a[176:179], v[4:7], 0\n"
"s_add_u32 m0, s51, 73504\n"
"ds_read_b64_tr_b16 a[252:253], v219 offset:37656\n"
"v_fma_mix_f32 v184, v20, v144, v152 op_sel:[0,0,0] op_sel_hi:[1,0,0]\n"
"v_mfma_f32_16x16x32_bf16 a[20:23], a[180:183], v[12:15], a[20:23]\n"
"ds_read_b64_tr_b16 a[254:255], v219 offset:38680\n"
"buffer_load_dwordx4 v217, s[20:23], s50 offen offset:256 lds\n"
"v_mfma_f32_16x16x32_bf16 a[24:27], a[184:187], v[0:3], 0\n"
"s_add_u32 s50, s50, 32768\n"
"ds_read_b64_tr_b16 a[160:161], v219 offset:40976\n"
"v_fma_mix_f32 v185, v20, v145, v153 op_sel:[1,0,0] op_sel_hi:[1,0,0]\n"
"v_mfma_f32_16x16x32_bf16 a[24:27], a[188:191], v[8:11], a[24:27]\n"
"ds_read_b64_tr_b16 a[162:163], v219 offset:42000\n"
"v_fma_mix_f32 v186, v21, v146, v154 op_sel:[0,0,0] op_sel_hi:[1,0,0]\n"
"v_mfma_f32_16x16x32_bf16 a[28:31], a[184:187], v[4:7], 0\n"
"ds_read_b64_tr_b16 a[164:165], v219 offset:45072\n"
"v_fma_mix_f32 v187, v21, v147, v155 op_sel:[1,0,0] op_sel_hi:[1,0,0]\n"
"v_mfma_f32_16x16x32_bf16 a[28:31], a[188:191], v[12:15], a[28:31]\n"
"s_waitcnt lgkmcnt(12)\n"
"ds_read_b64_tr_b16 a[166:167], v219 offset:46096\n"
"v_fma_mix_f32 v188, v22, v148, v156 op_sel:[0,0,0] op_sel_hi:[1,0,0]\n"
"v_mfma_f32_16x16x32_bf16 a[32:35], a[224:227], v[0:3], 0\n"
"s_add_u32 m0, s51, 66592\n"
"ds_read_b64_tr_b16 a[168:169], v219 offset:40984\n"
"v_fma_mix_f32 v189, v22, v149, v157 op_sel:[1,0,0] op_sel_hi:[1,0,0]\n"
"v_mfma_f32_16x16x32_bf16 a[32:35], a[228:231], v[8:11], a[32:35]\n"
"ds_read_b64_tr_b16 a[170:171], v219 offset:42008\n"
"buffer_load_dwordx4 v217, s[20:23], s50 offen offset:0 lds\n"
"v_mfma_f32_16x16x32_bf16 a[36:39], a[224:227], v[4:7], 0\n"
"ds_read_b64_tr_b16 a[172:173], v219 offset:45080\n"
"v_fma_mix_f32 v190, v23, v150, v158 op_sel:[0,0,0] op_sel_hi:[1,0,0]\n"
"v_mfma_f32_16x16x32_bf16 a[36:39], a[228:231], v[12:15], a[36:39]\n"
"ds_read_b64_tr_b16 a[174:175], v219 offset:46104\n"
"buffer_load_dwordx4 v[44:47], v255, s[28:31], s2 offen offset:1024 sc1\n"
"v_fma_mix_f32 v191, v23, v151, v159 op_sel:[1,0,0] op_sel_hi:[1,0,0]\n"
"v_mfma_f32_16x16x32_bf16 a[40:43], a[232:235], v[0:3], 0\n"
"ds_read_b64_tr_b16 a[176:177], v219 offset:41232\n"
"ds_read_b128 v[144:147], v224 offset:1024\n"
"v_mfma_f32_16x16x32_bf16 a[40:43], a[236:239], v[8:11], a[40:43]\n"
"ds_read_b64_tr_b16 a[178:179], v219 offset:42256\n"
"v_cvt_pk_bf16_f32 v20, v184, v185\n"
"v_mfma_f32_16x16x32_bf16 a[44:47], a[232:235], v[4:7], 0\n"
"s_add_u32 m0, s51, 74528\n"
"ds_read_b64_tr_b16 a[180:181], v219 offset:45328\n"
"v_cvt_pk_bf16_f32 v21, v186, v187\n"
"v_mfma_f32_16x16x32_bf16 a[44:47], a[236:239], v[12:15], a[44:47]\n"
"s_waitcnt lgkmcnt(12)\n"
"ds_read_b64_tr_b16 a[182:183], v219 offset:46352\n"
"buffer_load_dwordx4 v217, s[20:23], s50 offen offset:256 lds\n"
"v_mfma_f32_16x16x32_bf16 a[48:51], a[240:243], v[0:3], 0\n"
"s_add_u32 s50, s50, 32768\n"
"ds_read_b64_tr_b16 a[184:185], v219 offset:41240\n"
"ds_read_b128 v[148:151], v224 offset:1088\n"
"v_mfma_f32_16x16x32_bf16 a[48:51], a[244:247], v[8:11], a[48:51]\n"
"ds_read_b64_tr_b16 a[186:187], v219 offset:42264\n"
"v_cvt_pk_bf16_f32 v22, v188, v189\n"
"v_mfma_f32_16x16x32_bf16 a[52:55], a[240:243], v[4:7], 0\n"
"ds_read_b64_tr_b16 a[188:189], v219 offset:45336\n"
"v_cvt_pk_bf16_f32 v23, v190, v191\n"
"v_mfma_f32_16x16x32_bf16 a[52:55], a[244:247], v[12:15], a[52:55]\n"
"ds_read_b64_tr_b16 a[190:191], v219 offset:46360\n"
"ds_read_b128 v[152:155], v224 offset:1152\n"
"v_mfma_f32_16x16x32_bf16 a[56:59], a[248:251], v[0:3], 0\n"
"s_add_u32 m0, s51, 81960\n"
"ds_read_b64_tr_b16 a[224:225], v219 offset:41488\n"
"v_fma_mix_f32 v176, v24, v160, v168 op_sel:[0,0,0] op_sel_hi:[1,0,0]\n"
"v_mfma_f32_16x16x32_bf16 a[56:59], a[252:255], v[8:11], a[56:59]\n"
"ds_read_b64_tr_b16 a[226:227], v219 offset:42512\n"
"buffer_load_dwordx4 v217, s[20:23], s50 offen offset:0 lds\n"
"v_mfma_f32_16x16x32_bf16 a[60:63], a[248:251], v[4:7], 0\n"
"ds_read_b64_tr_b16 a[228:229], v219 offset:45584\n"
"ds_read_b128 v[156:159], v224 offset:1216\n"
"v_mfma_f32_16x16x32_bf16 a[60:63], a[252:255], v[12:15], a[60:63]\n"
"s_waitcnt lgkmcnt(14)\n"
"ds_read_b64_tr_b16 a[230:231], v219 offset:46608\n"
"v_fma_mix_f32 v177, v24, v161, v169 op_sel:[1,0,0] op_sel_hi:[1,0,0]\n"
"v_mfma_f32_16x16x32_bf16 a[64:67], a[160:163], v[0:3], 0\n"
"ds_read_b64_tr_b16 a[232:233], v219 offset:41496\n"
"buffer_load_dwordx4 v[48:51], v249, s[28:31], s2 offen offset:2048 sc1\n"
"v_fma_mix_f32 v178, v25, v162, v170 op_sel:[0,0,0] op_sel_hi:[1,0,0]\n"
"v_mfma_f32_16x16x32_bf16 a[64:67], a[164:167], v[8:11], a[64:67]\n"
"ds_read_b64_tr_b16 a[234:235], v219 offset:42520\n"
"v_fma_mix_f32 v179, v25, v163, v171 op_sel:[1,0,0] op_sel_hi:[1,0,0]\n"
"v_mfma_f32_16x16x32_bf16 a[68:71], a[160:163], v[4:7], 0\n"
"s_add_u32 m0, s51, 89896\n"
"ds_read_b64_tr_b16 a[236:237], v219 offset:45592\n"
"v_fma_mix_f32 v180, v26, v164, v172 op_sel:[0,0,0] op_sel_hi:[1,0,0]\n"
"v_mfma_f32_16x16x32_bf16 a[68:71], a[164:167], v[12:15], a[68:71]\n"
"ds_read_b64_tr_b16 a[238:239], v219 offset:46616\n"
"buffer_load_dwordx4 v217, s[20:23], s50 offen offset:256 lds\n"
"v_mfma_f32_16x16x32_bf16 a[72:75], a[168:171], v[0:3], 0\n"
"s_add_u32 s50, s50, 32768\n"
"ds_read_b64_tr_b16 a[240:241], v219 offset:41744\n"
"v_fma_mix_f32 v181, v26, v165, v173 op_sel:[1,0,0] op_sel_hi:[1,0,0]\n"
"v_mfma_f32_16x16x32_bf16 a[72:75], a[172:175], v[8:11], a[72:75]\n"
"ds_read_b64_tr_b16 a[242:243], v219 offset:42768\n"
"v_fma_mix_f32 v182, v27, v166, v174 op_sel:[0,0,0] op_sel_hi:[1,0,0]\n"
"v_mfma_f32_16x16x32_bf16 a[76:79], a[168:171], v[4:7], 0\n"
"ds_read_b64_tr_b16 a[244:245], v219 offset:45840\n"
"v_fma_mix_f32 v183, v27, v167, v175 op_sel:[1,0,0] op_sel_hi:[1,0,0]\n"
"v_mfma_f32_16x16x32_bf16 a[76:79], a[172:175], v[12:15], a[76:79]\n"
"s_waitcnt lgkmcnt(12)\n"
"ds_read_b64_tr_b16 a[246:247], v219 offset:46864\n"
"v_cvt_pk_bf16_f32 v24, v176, v177\n"
"v_mfma_f32_16x16x32_bf16 a[80:83], a[176:179], v[0:3], 0\n"
"s_add_u32 m0, s51, 82984\n"
"ds_read_b64_tr_b16 a[248:249], v219 offset:41752\n"
"v_cvt_pk_bf16_f32 v25, v178, v179\n"
"v_mfma_f32_16x16x32_bf16 a[80:83], a[180:183], v[8:11], a[80:83]\n"
"ds_read_b64_tr_b16 a[250:251], v219 offset:42776\n"
"buffer_load_dwordx4 v217, s[20:23], s50 offen offset:0 lds\n"
"v_mfma_f32_16x16x32_bf16 a[84:87], a[176:179], v[4:7], 0\n"
"ds_read_b64_tr_b16 a[252:253], v219 offset:45848\n"
"v_cvt_pk_bf16_f32 v26, v180, v181\n"
"v_mfma_f32_16x16x32_bf16 a[84:87], a[180:183], v[12:15], a[84:87]\n"
"ds_read_b64_tr_b16 a[254:255], v219 offset:46872\n"
"v_cvt_pk_bf16_f32 v27, v182, v183\n"
"v_mfma_f32_16x16x32_bf16 v[236:239], a[184:187], v[0:3], 0\n"
"ds_read_b64_tr_b16 a[160:161], v218 offset:0\n"
"v_fma_mix_f32 v184, v28, v160, v168 op_sel:[0,0,0] op_sel_hi:[1,0,0]\n"
"v_mfma_f32_16x16x32_bf16 v[236:239], a[188:191], v[8:11], v[236:239]\n"
"ds_read_b64_tr_b16 a[162:163], v218 offset:1024\n"
"v_fma_mix_f32 v185, v28, v161, v169 op_sel:[1,0,0] op_sel_hi:[1,0,0]\n"
"v_fma_mix_f32 v186, v29, v162, v170 op_sel:[0,0,0] op_sel_hi:[1,0,0]\n"
"v_mfma_f32_16x16x32_bf16 v[232:235], a[184:187], v[4:7], 0\n"
"s_add_u32 m0, s51, 90920\n"
"ds_read_b64_tr_b16 a[164:165], v218 offset:4096\n"
"v_fma_mix_f32 v187, v29, v163, v171 op_sel:[1,0,0] op_sel_hi:[1,0,0]\n"
"v_mfma_f32_16x16x32_bf16 v[232:235], a[188:191], v[12:15], v[232:235]\n"
"s_waitcnt lgkmcnt(11)\n"
"buffer_load_dwordx4 v217, s[20:23], s50 offen offset:256 lds\n"
"ds_read_b64_tr_b16 a[166:167], v218 offset:5120\n"
"v_mfma_f32_16x16x32_bf16 v[228:231], a[224:227], v[0:3], 0\n"
"s_add_u32 s50, s50, 425984\n"
"ds_read_b64_tr_b16 a[168:169], v218 offset:8\n"
"v_fma_mix_f32 v188, v30, v164, v172 op_sel:[0,0,0] op_sel_hi:[1,0,0]\n"
"v_mfma_f32_16x16x32_bf16 v[228:231], a[228:231], v[8:11], v[228:231]\n"
"ds_read_b64_tr_b16 a[170:171], v218 offset:1032\n"
"buffer_load_dwordx4 v[52:55], v255, s[28:31], s2 offen offset:2048 sc1\n"
"v_mfma_f32_16x16x32_bf16 v[220:223], a[224:227], v[4:7], 0\n"
"ds_read_b64_tr_b16 a[172:173], v218 offset:4104\n"
"v_fma_mix_f32 v189, v30, v165, v173 op_sel:[1,0,0] op_sel_hi:[1,0,0]\n"
"v_mfma_f32_16x16x32_bf16 v[220:223], a[228:231], v[12:15], v[220:223]\n"
"ds_read_b64_tr_b16 a[174:175], v218 offset:5128\n"
"v_fma_mix_f32 v190, v31, v166, v174 op_sel:[0,0,0] op_sel_hi:[1,0,0]\n"
"v_mfma_f32_16x16x32_bf16 v[212:215], a[232:235], v[0:3], 0\n"
"ds_read_b64_tr_b16 a[176:177], v218 offset:256\n"
"v_fma_mix_f32 v191, v31, v167, v175 op_sel:[1,0,0] op_sel_hi:[1,0,0]\n"
"v_mfma_f32_16x16x32_bf16 v[212:215], a[236:239], v[8:11], v[212:215]\n"
"ds_read_b64_tr_b16 a[178:179], v218 offset:1280\n"
"ds_read_b128 v[160:163], v224 offset:1280\n"
"v_mfma_f32_16x16x32_bf16 v[208:211], a[232:235], v[4:7], 0\n"
"ds_read_b64_tr_b16 a[180:181], v218 offset:4352\n"
"v_cvt_pk_bf16_f32 v28, v184, v185\n"
"v_mfma_f32_16x16x32_bf16 v[208:211], a[236:239], v[12:15], v[208:211]\n"
"s_waitcnt lgkmcnt(12)\n"
"ds_read_b64_tr_b16 a[182:183], v218 offset:5376\n"
"v_cvt_pk_bf16_f32 v29, v186, v187\n"
"v_mfma_f32_16x16x32_bf16 v[204:207], a[240:243], v[0:3], 0\n"
"ds_read_b64_tr_b16 a[184:185], v218 offset:264\n"
"ds_read_b128 v[164:167], v224 offset:1344\n"
"v_mfma_f32_16x16x32_bf16 v[204:207], a[244:247], v[8:11], v[204:207]\n"
"ds_read_b64_tr_b16 a[186:187], v218 offset:1288\n"
"v_cvt_pk_bf16_f32 v30, v188, v189\n"
"v_mfma_f32_16x16x32_bf16 v[200:203], a[240:243], v[4:7], 0\n"
"ds_read_b64_tr_b16 a[188:189], v218 offset:4360\n"
"v_cvt_pk_bf16_f32 v31, v190, v191\n"
"v_mfma_f32_16x16x32_bf16 v[200:203], a[244:247], v[12:15], v[200:203]\n"
"ds_read_b64_tr_b16 a[190:191], v218 offset:5384\n"
"ds_read_b128 v[168:171], v224 offset:1408\n"
"v_mfma_f32_16x16x32_bf16 v[196:199], a[248:251], v[0:3], 0\n"
"s_waitcnt vmcnt(7)\n"
"ds_read_b64_tr_b16 a[224:225], v218 offset:512\n"
"v_fma_mix_f32 v176, v32, v144, v152 op_sel:[0,0,0] op_sel_hi:[1,0,0]\n"
"v_mfma_f32_16x16x32_bf16 v[196:199], a[252:255], v[8:11], v[196:199]\n"
"ds_read_b64_tr_b16 a[226:227], v218 offset:1536\n"
"v_fma_mix_f32 v177, v32, v145, v153 op_sel:[1,0,0] op_sel_hi:[1,0,0]\n"
"v_mfma_f32_16x16x32_bf16 v[192:195], a[248:251], v[4:7], 0\n"
"ds_read_b64_tr_b16 a[228:229], v218 offset:4608\n"
"buffer_load_dwordx4 v[56:59], v249, s[28:31], s2 offen offset:3072 sc1\n"
"v_mfma_f32_16x16x32_bf16 v[192:195], a[252:255], v[12:15], v[192:195]\n"
"s_waitcnt lgkmcnt(14)\n"
"ds_read_b64_tr_b16 a[230:231], v218 offset:5632\n"
"ds_read_b128 v[172:175], v224 offset:1472\n"
"v_mfma_f32_16x16x32_bf16 a[0:3], a[160:163], v[16:19], a[0:3]\n"
"ds_read_b64_tr_b16 a[232:233], v218 offset:520\n"
"v_fma_mix_f32 v178, v33, v146, v154 op_sel:[0,0,0] op_sel_hi:[1,0,0]\n"
"v_mfma_f32_16x16x32_bf16 a[0:3], a[164:167], v[24:27], a[0:3]\n"
"s_waitcnt vmcnt(13)\n"
"ds_read_b64_tr_b16 a[234:235], v218 offset:1544\n"
"v_fma_mix_f32 v179, v33, v147, v155 op_sel:[1,0,0] op_sel_hi:[1,0,0]\n"
"v_mfma_f32_16x16x32_bf16 a[4:7], a[160:163], v[20:23], a[4:7]\n"
"s_barrier\n"
"ds_read_b64_tr_b16 a[236:237], v218 offset:4616\n"
"v_fma_mix_f32 v180, v34, v148, v156 op_sel:[0,0,0] op_sel_hi:[1,0,0]\n"
"v_mfma_f32_16x16x32_bf16 a[4:7], a[164:167], v[28:31], a[4:7]\n"
"ds_read_b64_tr_b16 a[238:239], v218 offset:5640\n"
"v_fma_mix_f32 v181, v34, v149, v157 op_sel:[1,0,0] op_sel_hi:[1,0,0]\n"
"v_mfma_f32_16x16x32_bf16 a[8:11], a[168:171], v[16:19], a[8:11]\n"
"s_add_u32 m0, s51, 98352\n"
"ds_read_b64_tr_b16 a[240:241], v218 offset:768\n"
"v_fma_mix_f32 v182, v35, v150, v158 op_sel:[0,0,0] op_sel_hi:[1,0,0]\n"
"v_mfma_f32_16x16x32_bf16 a[8:11], a[172:175], v[24:27], a[8:11]\n"
"ds_read_b64_tr_b16 a[242:243], v218 offset:1792\n"
"buffer_load_dwordx4 v217, s[20:23], s50 offen offset:0 lds\n"
"v_mfma_f32_16x16x32_bf16 a[12:15], a[168:171], v[20:23], a[12:15]\n"
"ds_read_b64_tr_b16 a[244:245], v218 offset:4864\n"
"v_fma_mix_f32 v183, v35, v151, v159 op_sel:[1,0,0] op_sel_hi:[1,0,0]\n"
"v_mfma_f32_16x16x32_bf16 a[12:15], a[172:175], v[28:31], a[12:15]\n"
"s_waitcnt lgkmcnt(12)\n"
"ds_read_b64_tr_b16 a[246:247], v218 offset:5888\n"
"v_cvt_pk_bf16_f32 v32, v176, v177\n"
"v_cvt_pk_bf16_f32 v33, v178, v179\n"
"v_mfma_f32_16x16x32_bf16 a[16:19], a[176:179], v[16:19], a[16:19]\n"
"ds_read_b64_tr_b16 a[248:249], v218 offset:776\n"
"v_cvt_pk_bf16_f32 v34, v180, v181\n"
"v_mfma_f32_16x16x32_bf16 a[16:19], a[180:183], v[24:27], a[16:19]\n"
"ds_read_b64_tr_b16 a[250:251], v218 offset:1800\n"
"v_cvt_pk_bf16_f32 v35, v182, v183\n"
"v_mfma_f32_16x16x32_bf16 a[20:23], a[176:179], v[20:23], a[20:23]\n"
"s_add_u32 m0, s51, 106288\n"
"ds_read_b64_tr_b16 a[252:253], v218 offset:4872\n"
"v_fma_mix_f32 v184, v36, v144, v152 op_sel:[0,0,0] op_sel_hi:[1,0,0]\n"
"v_mfma_f32_16x16x32_bf16 a[20:23], a[180:183], v[28:31], a[20:23]\n"
"ds_read_b64_tr_b16 a[254:255], v218 offset:5896\n"
"buffer_load_dwordx4 v217, s[20:23], s50 offen offset:256 lds\n"
"v_mfma_f32_16x16x32_bf16 a[24:27], a[184:187], v[16:19], a[24:27]\n"
"s_add_u32 s50, s50, 32768\n"
"ds_read_b64_tr_b16 a[160:161], v218 offset:8192\n"
"v_fma_mix_f32 v185, v36, v145, v153 op_sel:[1,0,0] op_sel_hi:[1,0,0]\n"
"v_mfma_f32_16x16x32_bf16 a[24:27], a[188:191], v[24:27], a[24:27]\n"
"ds_read_b64_tr_b16 a[162:163], v218 offset:9216\n"
"buffer_load_dwordx4 v[60:63], v255, s[28:31], s2 offen offset:3072 sc1\n"
"v_mfma_f32_16x16x32_bf16 a[28:31], a[184:187], v[20:23], a[28:31]\n"
"s_add_u32 s2, s2, 4096\n"
"ds_read_b64_tr_b16 a[164:165], v218 offset:12288\n"
"v_fma_mix_f32 v186, v37, v146, v154 op_sel:[0,0,0] op_sel_hi:[1,0,0]\n"
"v_mfma_f32_16x16x32_bf16 a[28:31], a[188:191], v[28:31], a[28:31]\n"
"s_waitcnt lgkmcnt(11)\n"
"ds_read_b64_tr_b16 a[166:167], v218 offset:13312\n"
"v_fma_mix_f32 v187, v37, v147, v155 op_sel:[1,0,0] op_sel_hi:[1,0,0]\n"
"v_mfma_f32_16x16x32_bf16 a[32:35], a[224:227], v[16:19], a[32:35]\n"
"s_add_u32 m0, s51, 99376\n"
"ds_read_b64_tr_b16 a[168:169], v218 offset:8200\n"
"v_fma_mix_f32 v188, v38, v148, v156 op_sel:[0,0,0] op_sel_hi:[1,0,0]\n"
"v_mfma_f32_16x16x32_bf16 a[32:35], a[228:231], v[24:27], a[32:35]\n"
"s_and_b32 s62, s42, 7\n"
"ds_read_b64_tr_b16 a[170:171], v218 offset:9224\n"
"buffer_load_dwordx4 v217, s[20:23], s50 offen offset:0 lds\n"
"v_mfma_f32_16x16x32_bf16 a[36:39], a[224:227], v[20:23], a[36:39]\n"
"ds_read_b64_tr_b16 a[172:173], v218 offset:12296\n"
"v_fma_mix_f32 v189, v38, v149, v157 op_sel:[1,0,0] op_sel_hi:[1,0,0]\n"
"v_mfma_f32_16x16x32_bf16 a[36:39], a[228:231], v[28:31], a[36:39]\n"
"s_mul_i32 s46, s62, 663552\n"
"ds_read_b64_tr_b16 a[174:175], v218 offset:13320\n"
"v_fma_mix_f32 v190, v39, v150, v158 op_sel:[0,0,0] op_sel_hi:[1,0,0]\n"
"v_mfma_f32_16x16x32_bf16 a[40:43], a[232:235], v[16:19], a[40:43]\n"
"ds_read_b64_tr_b16 a[176:177], v218 offset:8448\n"
"v_fma_mix_f32 v191, v39, v151, v159 op_sel:[1,0,0] op_sel_hi:[1,0,0]\n"
"v_cvt_pk_bf16_f32 v36, v184, v185\n"
"v_mfma_f32_16x16x32_bf16 a[40:43], a[236:239], v[24:27], a[40:43]\n"
"s_lshr_b32 s47, s43, 2\n"
"ds_read_b64_tr_b16 a[178:179], v218 offset:9472\n"
"ds_read_b128 v[144:147], v224 offset:1536\n"
"v_mfma_f32_16x16x32_bf16 a[44:47], a[232:235], v[20:23], a[44:47]\n"
"s_add_u32 m0, s51, 107312\n"
"ds_read_b64_tr_b16 a[180:181], v218 offset:12544\n"
"v_cvt_pk_bf16_f32 v37, v186, v187\n"
"v_mfma_f32_16x16x32_bf16 a[44:47], a[236:239], v[28:31], a[44:47]\n"
"s_waitcnt lgkmcnt(12)\n"
"s_mul_i32 s47, s47, 1179648\n"
"ds_read_b64_tr_b16 a[182:183], v218 offset:13568\n"
"buffer_load_dwordx4 v217, s[20:23], s50 offen offset:256 lds\n"
"v_mfma_f32_16x16x32_bf16 a[48:51], a[240:243], v[16:19], a[48:51]\n"
"s_add_u32 s50, s50, 32768\n"
"ds_read_b64_tr_b16 a[184:185], v218 offset:8456\n"
"ds_read_b128 v[148:151], v224 offset:1600\n"
"v_mfma_f32_16x16x32_bf16 a[48:51], a[244:247], v[24:27], a[48:51]\n"
"s_getpc_b64 s[32:33]\n"
"AT_pc1:\n"
"s_sub_u32 s32, s32, AT_pc1-AT_start\n"
"s_subb_u32 s33, s33, 0\n"
"ds_read_b64_tr_b16 a[186:187], v218 offset:9480\n"
"v_cvt_pk_bf16_f32 v38, v188, v189\n"
"v_mfma_f32_16x16x32_bf16 a[52:55], a[240:243], v[20:23], a[52:55]\n"
"ds_read_b64_tr_b16 a[188:189], v218 offset:12552\n"
"buffer_load_dwordx4 v[64:67], v249, s[28:31], s2 offen offset:0 sc1\n"
"v_mfma_f32_16x16x32_bf16 a[52:55], a[244:247], v[28:31], a[52:55]\n"
"s_and_b32 s33, s33, 0xffff\n"
"ds_read_b64_tr_b16 a[190:191], v218 offset:13576\n"
"ds_read_b128 v[152:155], v224 offset:1664\n"
"v_mfma_f32_16x16x32_bf16 a[56:59], a[248:251], v[16:19], a[56:59]\n"
"s_add_u32 m0, s51, 114744\n"
"ds_read_b64_tr_b16 a[224:225], v218 offset:8704\n"
"v_cvt_pk_bf16_f32 v39, v190, v191\n"
"v_mfma_f32_16x16x32_bf16 a[56:59], a[252:255], v[24:27], a[56:59]\n"
"s_mov_b32 s34, AT_kend-AT_start\n"
"ds_read_b64_tr_b16 a[226:227], v218 offset:9728\n"
"buffer_load_dwordx4 v217, s[20:23], s50 offen offset:0 lds\n"
"v_mfma_f32_16x16x32_bf16 a[60:63], a[248:251], v[20:23], a[60:63]\n"
"ds_read_b64_tr_b16 a[228:229], v218 offset:12800\n"
"ds_read_b128 v[156:159], v224 offset:1728\n"
"v_mfma_f32_16x16x32_bf16 a[60:63], a[252:255], v[28:31], a[60:63]\n"
"s_waitcnt lgkmcnt(14)\n"
"s_mov_b32 s35, 0x20000\n"
"ds_read_b64_tr_b16 a[230:231], v218 offset:13824\n"
"v_fma_mix_f32 v176, v40, v160, v168 op_sel:[0,0,0] op_sel_hi:[1,0,0]\n"
"v_mfma_f32_16x16x32_bf16 a[64:67], a[160:163], v[16:19], a[64:67]\n"
"ds_read_b64_tr_b16 a[232:233], v218 offset:8712\n"
"v_fma_mix_f32 v177, v40, v161, v169 op_sel:[1,0,0] op_sel_hi:[1,0,0]\n"
"v_mfma_f32_16x16x32_bf16 a[64:67], a[164:167], v[24:27], a[64:67]\n"
"s_lshr_b32 s62, s42, 3\n"
"ds_read_b64_tr_b16 a[234:235], v218 offset:9736\n"
"v_fma_mix_f32 v178, v41, v162, v170 op_sel:[0,0,0] op_sel_hi:[1,0,0]\n"
"v_fma_mix_f32 v179, v41, v163, v171 op_sel:[1,0,0] op_sel_hi:[1,0,0]\n"
"v_mfma_f32_16x16x32_bf16 a[68:71], a[160:163], v[20:23], a[68:71]\n"
"s_add_u32 m0, s51, 122680\n"
"ds_read_b64_tr_b16 a[236:237], v218 offset:12808\n"
"v_fma_mix_f32 v180, v42, v164, v172 op_sel:[0,0,0] op_sel_hi:[1,0,0]\n"
"v_mfma_f32_16x16x32_bf16 a[68:71], a[164:167], v[28:31], a[68:71]\n"
"s_lshl_b32 s62, s62, 2\n"
"ds_read_b64_tr_b16 a[238:239], v218 offset:13832\n"
"buffer_load_dwordx4 v217, s[20:23], s50 offen offset:256 lds\n"
"v_mfma_f32_16x16x32_bf16 a[72:75], a[168:171], v[16:19], a[72:75]\n"
"s_add_u32 s50, s50, 32768\n"
"ds_read_b64_tr_b16 a[240:241], v218 offset:8960\n"
"v_fma_mix_f32 v181, v42, v165, v173 op_sel:[1,0,0] op_sel_hi:[1,0,0]\n"
"v_mfma_f32_16x16x32_bf16 a[72:75], a[172:175], v[24:27], a[72:75]\n"
"s_add_u32 s62, s62, s41\n"
"ds_read_b64_tr_b16 a[242:243], v218 offset:9984\n"
"v_fma_mix_f32 v182, v43, v166, v174 op_sel:[0,0,0] op_sel_hi:[1,0,0]\n"
"v_mfma_f32_16x16x32_bf16 a[76:79], a[168:171], v[20:23], a[76:79]\n"
"ds_read_b64_tr_b16 a[244:245], v218 offset:13056\n"
"v_fma_mix_f32 v183, v43, v167, v175 op_sel:[1,0,0] op_sel_hi:[1,0,0]\n"
"v_mfma_f32_16x16x32_bf16 a[76:79], a[172:175], v[28:31], a[76:79]\n"
"s_waitcnt lgkmcnt(12)\n"
"s_and_b32 s62, s62, 31\n"
"ds_read_b64_tr_b16 a[246:247], v218 offset:14080\n"
"v_cvt_pk_bf16_f32 v40, v176, v177\n"
"v_mfma_f32_16x16x32_bf16 a[80:83], a[176:179], v[16:19], a[80:83]\n"
"s_add_u32 m0, s51, 115768\n"
"ds_read_b64_tr_b16 a[248:249], v218 offset:8968\n"
"v_cvt_pk_bf16_f32 v41, v178, v179\n"
"v_mfma_f32_16x16x32_bf16 a[80:83], a[180:183], v[24:27], a[80:83]\n"
"s_lshl_b32 s48, s62, 12\n"
"ds_read_b64_tr_b16 a[250:251], v218 offset:9992\n"
"buffer_load_dwordx4 v217, s[20:23], s50 offen offset:0 lds\n"
"v_mfma_f32_16x16x32_bf16 a[84:87], a[176:179], v[20:23], a[84:87]\n"
"ds_read_b64_tr_b16 a[252:253], v218 offset:13064\n"
"v_cvt_pk_bf16_f32 v42, v180, v181\n"
"v_mfma_f32_16x16x32_bf16 a[84:87], a[180:183], v[28:31], a[84:87]\n"
"s_lshl_b32 s54, s44, 2\n"
"ds_read_b64_tr_b16 a[254:255], v218 offset:14088\n"
"buffer_load_dwordx4 v[68:71], v255, s[28:31], s2 offen offset:0 sc1\n"
"v_mfma_f32_16x16x32_bf16 v[236:239], a[184:187], v[16:19], v[236:239]\n"
"ds_read_b64_tr_b16 a[160:161], v218 offset:32784\n"
"v_cvt_pk_bf16_f32 v43, v182, v183\n"
"v_mfma_f32_16x16x32_bf16 v[236:239], a[188:191], v[24:27], v[236:239]\n"
"s_add_u32 s54, s54, s41\n"
"ds_read_b64_tr_b16 a[162:163], v218 offset:33808\n"
"v_fma_mix_f32 v184, v44, v160, v168 op_sel:[0,0,0] op_sel_hi:[1,0,0]\n"
"v_mfma_f32_16x16x32_bf16 v[232:235], a[184:187], v[20:23], v[232:235]\n"
"s_add_u32 m0, s51, 123704\n"
"ds_read_b64_tr_b16 a[164:165], v218 offset:36880\n"
"v_fma_mix_f32 v185, v44, v161, v169 op_sel:[1,0,0] op_sel_hi:[1,0,0]\n"
"v_fma_mix_f32 v186, v45, v162, v170 op_sel:[0,0,0] op_sel_hi:[1,0,0]\n"
"v_mfma_f32_16x16x32_bf16 v[232:235], a[188:191], v[28:31], v[232:235]\n"
"s_waitcnt lgkmcnt(11)\n"
"s_mul_i32 s54, s54, 36864\n"
"buffer_load_dwordx4 v217, s[20:23], s50 offen offset:256 lds\n"
"ds_read_b64_tr_b16 a[166:167], v218 offset:37904\n"
"v_mfma_f32_16x16x32_bf16 v[228:231], a[224:227], v[16:19], v[228:231]\n"
"s_add_u32 s50, s50, 425984\n"
"ds_read_b64_tr_b16 a[168:169], v218 offset:32792\n"
"v_fma_mix_f32 v187, v45, v163, v171 op_sel:[1,0,0] op_sel_hi:[1,0,0]\n"
"v_mfma_f32_16x16x32_bf16 v[228:231], a[228:231], v[24:27], v[228:231]\n"
"s_lshr_b32 s62, s43, 2\n"
"ds_read_b64_tr_b16 a[170:171], v218 offset:33816\n"
"v_fma_mix_f32 v188, v46, v164, v172 op_sel:[0,0,0] op_sel_hi:[1,0,0]\n"
"v_mfma_f32_16x16x32_bf16 v[220:223], a[224:227], v[20:23], v[220:223]\n"
"ds_read_b64_tr_b16 a[172:173], v218 offset:36888\n"
"v_fma_mix_f32 v189, v46, v165, v173 op_sel:[1,0,0] op_sel_hi:[1,0,0]\n"
"v_mfma_f32_16x16x32_bf16 v[220:223], a[228:231], v[28:31], v[220:223]\n"
"s_mul_i32 s62, s62, 2359296\n"
"ds_read_b64_tr_b16 a[174:175], v218 offset:37912\n"
"v_fma_mix_f32 v190, v47, v166, v174 op_sel:[0,0,0] op_sel_hi:[1,0,0]\n"
"v_mfma_f32_16x16x32_bf16 v[212:215], a[232:235], v[16:19], v[212:215]\n"
"ds_read_b64_tr_b16 a[176:177], v218 offset:33040\n"
"v_fma_mix_f32 v191, v47, v167, v175 op_sel:[1,0,0] op_sel_hi:[1,0,0]\n"
"v_mfma_f32_16x16x32_bf16 v[212:215], a[236:239], v[24:27], v[212:215]\n"
"s_add_u32 s54, s54, s62\n"
"ds_read_b64_tr_b16 a[178:179], v218 offset:34064\n"
"ds_read_b128 v[160:163], v224 offset:1792\n"
"v_mfma_f32_16x16x32_bf16 v[208:211], a[232:235], v[20:23], v[208:211]\n"
"ds_read_b64_tr_b16 a[180:181], v218 offset:37136\n"
"v_cvt_pk_bf16_f32 v44, v184, v185\n"
"v_mfma_f32_16x16x32_bf16 v[208:211], a[236:239], v[28:31], v[208:211]\n"
"s_waitcnt lgkmcnt(12)\n"
"s_add_u32 s54, s54, 139264\n"
"ds_read_b64_tr_b16 a[182:183], v218 offset:38160\n"
"v_cvt_pk_bf16_f32 v45, v186, v187\n"
"v_mfma_f32_16x16x32_bf16 v[204:207], a[240:243], v[16:19], v[204:207]\n"
"ds_read_b64_tr_b16 a[184:185], v218 offset:33048\n"
"buffer_load_dwordx4 v[72:75], v249, s[28:31], s2 offen offset:1024 sc1\n"
"v_mfma_f32_16x16x32_bf16 v[204:207], a[244:247], v[24:27], v[204:207]\n"
"s_bfe_u32 s62, s43, 0x10001\n"
"ds_read_b64_tr_b16 a[186:187], v218 offset:34072\n"
"ds_read_b128 v[164:167], v224 offset:1856\n"
"v_mfma_f32_16x16x32_bf16 v[200:203], a[240:243], v[20:23], v[200:203]\n"
"ds_read_b64_tr_b16 a[188:189], v218 offset:37144\n"
"v_cvt_pk_bf16_f32 v46, v188, v189\n"
"v_mfma_f32_16x16x32_bf16 v[200:203], a[244:247], v[28:31], v[200:203]\n"
"s_mul_i32 s62, s62, 9216\n"
"ds_read_b64_tr_b16 a[190:191], v218 offset:38168\n"
"v_cvt_pk_bf16_f32 v47, v190, v191\n"
"v_mfma_f32_16x16x32_bf16 v[196:199], a[248:251], v[16:19], v[196:199]\n"
"AT_pg2:\n"
"s_cbranch_execz AT_hr\n"
"ds_read_b64_tr_b16 a[224:225], v218 offset:33296\n"
"ds_read_b128 v[168:171], v224 offset:1920\n"
"v_mfma_f32_16x16x32_bf16 v[196:199], a[252:255], v[24:27], v[196:199]\n"
"s_waitcnt vmcnt(9)\n"
"ds_read_b64_tr_b16 a[226:227], v218 offset:34320\n"
"v_fma_mix_f32 v176, v48, v144, v152 op_sel:[0,0,0] op_sel_hi:[1,0,0]\n"
"v_mfma_f32_16x16x32_bf16 v[192:195], a[248:251], v[20:23], v[192:195]\n"
"s_add_u32 s54, s54, s62\n"
"ds_read_b64_tr_b16 a[228:229], v218 offset:37392\n"
"v_fma_mix_f32 v177, v48, v145, v153 op_sel:[1,0,0] op_sel_hi:[1,0,0]\n"
"v_mfma_f32_16x16x32_bf16 v[192:195], a[252:255], v[28:31], v[192:195]\n"
"s_waitcnt lgkmcnt(14)\n"
"ds_read_b64_tr_b16 a[230:231], v218 offset:38416\n"
"ds_read_b128 v[172:175], v224 offset:1984\n"
"v_mfma_f32_16x16x32_bf16 a[0:3], a[160:163], v[32:35], a[0:3]\n"
"s_and_b32 s62, s43, 1\n"
"ds_read_b64_tr_b16 a[232:233], v218 offset:33304\n"
"v_fma_mix_f32 v178, v49, v146, v154 op_sel:[0,0,0] op_sel_hi:[1,0,0]\n"
"v_mfma_f32_16x16x32_bf16 a[0:3], a[164:167], v[40:43], a[0:3]\n"
"s_waitcnt vmcnt(14)\n"
"ds_read_b64_tr_b16 a[234:235], v218 offset:34328\n"
"v_fma_mix_f32 v179, v49, v147, v155 op_sel:[1,0,0] op_sel_hi:[1,0,0]\n"
"v_mfma_f32_16x16x32_bf16 a[4:7], a[160:163], v[36:39], a[4:7]\n"
"s_barrier\n"
"ds_read_b64_tr_b16 a[236:237], v218 offset:37400\n"
"v_fma_mix_f32 v180, v50, v148, v156 op_sel:[0,0,0] op_sel_hi:[1,0,0]\n"
"v_mfma_f32_16x16x32_bf16 a[4:7], a[164:167], v[44:47], a[4:7]\n"
"s_lshl_b32 s49, s62, 3\n"
"ds_read_b64_tr_b16 a[238:239], v218 offset:38424\n"
"v_fma_mix_f32 v181, v50, v149, v157 op_sel:[1,0,0] op_sel_hi:[1,0,0]\n"
"v_mfma_f32_16x16x32_bf16 a[8:11], a[168:171], v[32:35], a[8:11]\n"
"s_mov_b32 m0, s51\n"
"ds_read_b64_tr_b16 a[240:241], v218 offset:33552\n"
"v_fma_mix_f32 v182, v51, v150, v158 op_sel:[0,0,0] op_sel_hi:[1,0,0]\n"
"v_mfma_f32_16x16x32_bf16 a[8:11], a[172:175], v[40:43], a[8:11]\n"
"s_add_u32 s49, s49, s54\n"
"ds_read_b64_tr_b16 a[242:243], v218 offset:34576\n"
"buffer_load_dwordx4 v217, s[20:23], s50 offen offset:0 lds\n"
"v_mfma_f32_16x16x32_bf16 a[12:15], a[168:171], v[36:39], a[12:15]\n"
"ds_read_b64_tr_b16 a[244:245], v218 offset:37648\n"
"v_fma_mix_f32 v183, v51, v151, v159 op_sel:[1,0,0] op_sel_hi:[1,0,0]\n"
"v_mfma_f32_16x16x32_bf16 a[12:15], a[172:175], v[44:47], a[12:15]\n"
"s_waitcnt lgkmcnt(13)\n"
"s_add_u32 s49, s49, 4096\n"
"ds_read_b64_tr_b16 a[246:247], v218 offset:38672\n"
"buffer_load_dwordx4 v[76:79], v255, s[28:31], s2 offen offset:1024 sc1\n"
"v_mfma_f32_16x16x32_bf16 a[16:19], a[176:179], v[32:35], a[16:19]\n"
"ds_read_b64_tr_b16 a[248:249], v218 offset:33560\n"
"v_cvt_pk_bf16_f32 v48, v176, v177\n"
"v_cvt_pk_bf16_f32 v49, v178, v179\n"
"v_mfma_f32_16x16x32_bf16 a[16:19], a[180:183], v[40:43], a[16:19]\n"
"s_mul_i32 s62, s62, 5120\n"
"ds_read_b64_tr_b16 a[250:251], v218 offset:34584\n"
"v_cvt_pk_bf16_f32 v50, v180, v181\n"
"v_mfma_f32_16x16x32_bf16 a[20:23], a[176:179], v[36:39], a[20:23]\n"
"s_add_u32 m0, s51, 7936\n"
"ds_read_b64_tr_b16 a[252:253], v218 offset:37656\n"
"v_cvt_pk_bf16_f32 v51, v182, v183\n"
"v_mfma_f32_16x16x32_bf16 a[20:23], a[180:183], v[44:47], a[20:23]\n"
"s_add_u32 s54, s54, s62\n"
"ds_read_b64_tr_b16 a[254:255], v218 offset:38680\n"
"buffer_load_dwordx4 v217, s[20:23], s50 offen offset:256 lds\n"
"v_mfma_f32_16x16x32_bf16 a[24:27], a[184:187], v[32:35], a[24:27]\n"
"s_add_u32 s50, s50, 32768\n"
"ds_read_b64_tr_b16 a[160:161], v218 offset:40976\n"
"v_fma_mix_f32 v184, v52, v144, v152 op_sel:[0,0,0] op_sel_hi:[1,0,0]\n"
"v_mfma_f32_16x16x32_bf16 a[24:27], a[188:191], v[40:43], a[24:27]\n"
"s_and_b32 s62, s43, 1\n"
"ds_read_b64_tr_b16 a[162:163], v218 offset:42000\n"
"v_fma_mix_f32 v185, v52, v145, v153 op_sel:[1,0,0] op_sel_hi:[1,0,0]\n"
"v_mfma_f32_16x16x32_bf16 a[28:31], a[184:187], v[36:39], a[28:31]\n"
"ds_read_b64_tr_b16 a[164:165], v218 offset:45072\n"
"v_fma_mix_f32 v186, v53, v146, v154 op_sel:[0,0,0] op_sel_hi:[1,0,0]\n"
"v_mfma_f32_16x16x32_bf16 a[28:31], a[188:191], v[44:47], a[28:31]\n"
"s_waitcnt lgkmcnt(11)\n"
"s_lshl_b32 s62, s62, 10\n"
"ds_read_b64_tr_b16 a[166:167], v218 offset:46096\n"
"v_fma_mix_f32 v187, v53, v147, v155 op_sel:[1,0,0] op_sel_hi:[1,0,0]\n"
"v_mfma_f32_16x16x32_bf16 a[32:35], a[224:227], v[32:35], a[32:35]\n"
"s_add_u32 m0, s51, 1024\n"
"ds_read_b64_tr_b16 a[168:169], v218 offset:40984\n"
"v_fma_mix_f32 v188, v54, v148, v156 op_sel:[0,0,0] op_sel_hi:[1,0,0]\n"
"v_mfma_f32_16x16x32_bf16 a[32:35], a[228:231], v[40:43], a[32:35]\n"
"s_sub_u32 s54, s54, s62\n"
"ds_read_b64_tr_b16 a[170:171], v218 offset:42008\n"
"buffer_load_dwordx4 v217, s[20:23], s50 offen offset:0 lds\n"
"v_mfma_f32_16x16x32_bf16 a[36:39], a[224:227], v[36:39], a[36:39]\n"
"ds_read_b64_tr_b16 a[172:173], v218 offset:45080\n"
"v_fma_mix_f32 v189, v54, v149, v157 op_sel:[1,0,0] op_sel_hi:[1,0,0]\n"
"v_mfma_f32_16x16x32_bf16 a[36:39], a[228:231], v[44:47], a[36:39]\n"
"s_add_u32 s49, s54, 4096\n"
"ds_read_b64_tr_b16 a[174:175], v218 offset:46104\n"
"v_fma_mix_f32 v190, v55, v150, v158 op_sel:[0,0,0] op_sel_hi:[1,0,0]\n"
"v_mfma_f32_16x16x32_bf16 a[40:43], a[232:235], v[32:35], a[40:43]\n"
"ds_read_b64_tr_b16 a[176:177], v218 offset:41232\n"
"buffer_load_dwordx4 v[80:83], v249, s[28:31], s2 offen offset:2048 sc1\n"
"v_mfma_f32_16x16x32_bf16 a[40:43], a[236:239], v[40:43], a[40:43]\n"
"s_lshl_b32 s52, s44, 4\n"
"ds_read_b64_tr_b16 a[178:179], v218 offset:42256\n"
"v_fma_mix_f32 v191, v55, v151, v159 op_sel:[1,0,0] op_sel_hi:[1,0,0]\n"
"v_cvt_pk_bf16_f32 v52, v184, v185\n"
"v_mfma_f32_16x16x32_bf16 a[44:47], a[232:235], v[36:39], a[44:47]\n"
"s_add_u32 m0, s51, 8960\n"
"ds_read_b64_tr_b16 a[180:181], v218 offset:45328\n"
"ds_read_b128 v[144:147], v224 offset:2048\n"
"v_mfma_f32_16x16x32_bf16 a[44:47], a[236:239], v[44:47], a[44:47]\n"
"s_waitcnt lgkmcnt(12)\n"
"s_add_u32 s52, s52, s43\n"
"ds_read_b64_tr_b16 a[182:183], v218 offset:46352\n"
"buffer_load_dwordx4 v217, s[20:23], s50 offen offset:256 lds\n"
"v_mfma_f32_16x16x32_bf16 a[48:51], a[240:243], v[32:35], a[48:51]\n"
"s_add_u32 s50, s50, 32768\n"
"ds_read_b64_tr_b16 a[184:185], v218 offset:41240\n"
"v_cvt_pk_bf16_f32 v53, v186, v187\n"
"v_mfma_f32_16x16x32_bf16 a[48:51], a[244:247], v[40:43], a[48:51]\n"
"s_mul_i32 s52, s52, 18432\n"
"ds_read_b64_tr_b16 a[186:187], v218 offset:42264\n"
"ds_read_b128 v[148:151], v224 offset:2112\n"
"v_mfma_f32_16x16x32_bf16 a[52:55], a[240:243], v[36:39], a[52:55]\n"
"ds_read_b64_tr_b16 a[188:189], v218 offset:45336\n"
"v_cvt_pk_bf16_f32 v54, v188, v189\n"
"v_mfma_f32_16x16x32_bf16 a[52:55], a[244:247], v[44:47], a[52:55]\n"
"s_mul_i32 s62, s41, 5\n"
"ds_read_b64_tr_b16 a[190:191], v218 offset:46360\n"
"v_cvt_pk_bf16_f32 v55, v190, v191\n"
"v_mfma_f32_16x16x32_bf16 a[56:59], a[248:251], v[32:35], a[56:59]\n"
"s_add_u32 m0, s51, 16392\n"
"ds_read_b64_tr_b16 a[224:225], v218 offset:41488\n"
"ds_read_b128 v[152:155], v224 offset:2176\n"
"v_mfma_f32_16x16x32_bf16 a[56:59], a[252:255], v[40:43], a[56:59]\n"
"s_min_u32 s62, s62, 13\n"
"ds_read_b64_tr_b16 a[226:227], v218 offset:42512\n"
"buffer_load_dwordx4 v217, s[20:23], s50 offen offset:0 lds\n"
"v_mfma_f32_16x16x32_bf16 a[60:63], a[248:251], v[36:39], a[60:63]\n"
"ds_read_b64_tr_b16 a[228:229], v218 offset:45584\n"
"v_fma_mix_f32 v176, v56, v160, v168 op_sel:[0,0,0] op_sel_hi:[1,0,0]\n"
"v_mfma_f32_16x16x32_bf16 a[60:63], a[252:255], v[44:47], a[60:63]\n"
"s_waitcnt lgkmcnt(14)\n"
"s_lshl_b32 s62, s62, 10\n"
"ds_read_b64_tr_b16 a[230:231], v218 offset:46608\n"
"ds_read_b128 v[156:159], v224 offset:2240\n"
"v_mfma_f32_16x16x32_bf16 a[64:67], a[160:163], v[32:35], a[64:67]\n"
"ds_read_b64_tr_b16 a[232:233], v218 offset:41496\n"
"v_fma_mix_f32 v177, v56, v161, v169 op_sel:[1,0,0] op_sel_hi:[1,0,0]\n"
"v_mfma_f32_16x16x32_bf16 a[64:67], a[164:167], v[40:43], a[64:67]\n"
"s_add_u32 s52, s52, s62\n"
"ds_read_b64_tr_b16 a[234:235], v218 offset:42520\n"
"v_fma_mix_f32 v178, v57, v162, v170 op_sel:[0,0,0] op_sel_hi:[1,0,0]\n"
"v_mfma_f32_16x16x32_bf16 a[68:71], a[160:163], v[36:39], a[68:71]\n"
"s_add_u32 m0, s51, 24328\n"
"ds_read_b64_tr_b16 a[236:237], v218 offset:45592\n"
"v_fma_mix_f32 v179, v57, v163, v171 op_sel:[1,0,0] op_sel_hi:[1,0,0]\n"
"v_fma_mix_f32 v180, v58, v164, v172 op_sel:[0,0,0] op_sel_hi:[1,0,0]\n"
"v_mfma_f32_16x16x32_bf16 a[68:71], a[164:167], v[44:47], a[68:71]\n"
"s_add_u32 s52, s52, 30564352\n"
"ds_read_b64_tr_b16 a[238:239], v218 offset:46616\n"
"buffer_load_dwordx4 v217, s[20:23], s50 offen offset:256 lds\n"
"v_mfma_f32_16x16x32_bf16 a[72:75], a[168:171], v[32:35], a[72:75]\n"
"s_add_u32 s50, s50, 32768\n"
"ds_read_b64_tr_b16 a[240:241], v218 offset:41744\n"
"v_fma_mix_f32 v181, v58, v165, v173 op_sel:[1,0,0] op_sel_hi:[1,0,0]\n"
"v_mfma_f32_16x16x32_bf16 a[72:75], a[172:175], v[40:43], a[72:75]\n"
"s_add_u32 s53, s52, 4096\n"
"ds_read_b64_tr_b16 a[242:243], v218 offset:42768\n"
"buffer_load_dwordx4 v[84:87], v255, s[28:31], s2 offen offset:2048 sc1\n"
"v_mfma_f32_16x16x32_bf16 a[76:79], a[168:171], v[36:39], a[76:79]\n"
"ds_read_b64_tr_b16 a[244:245], v218 offset:45840\n"
"v_fma_mix_f32 v182, v59, v166, v174 op_sel:[0,0,0] op_sel_hi:[1,0,0]\n"
"v_mfma_f32_16x16x32_bf16 a[76:79], a[172:175], v[44:47], a[76:79]\n"
"s_waitcnt lgkmcnt(13)\n"
"ds_read_b64_tr_b16 a[246:247], v218 offset:46864\n"
"v_fma_mix_f32 v183, v59, v167, v175 op_sel:[1,0,0] op_sel_hi:[1,0,0]\n"
"v_mfma_f32_16x16x32_bf16 a[80:83], a[176:179], v[32:35], a[80:83]\n"
"s_add_u32 m0, s51, 17416\n"
"ds_read_b64_tr_b16 a[248:249], v218 offset:41752\n"
"v_cvt_pk_bf16_f32 v56, v176, v177\n"
"v_mfma_f32_16x16x32_bf16 a[80:83], a[180:183], v[40:43], a[80:83]\n"
"ds_read_b64_tr_b16 a[250:251], v218 offset:42776\n"
"buffer_load_dwordx4 v217, s[20:23], s50 offen offset:0 lds\n"
"v_mfma_f32_16x16x32_bf16 a[84:87], a[176:179], v[36:39], a[84:87]\n"
"ds_read_b64_tr_b16 a[252:253], v218 offset:45848\n"
"v_cvt_pk_bf16_f32 v57, v178, v179\n"
"v_mfma_f32_16x16x32_bf16 a[84:87], a[180:183], v[44:47], a[84:87]\n"
"ds_read_b64_tr_b16 a[254:255], v218 offset:46872\n"
"v_cvt_pk_bf16_f32 v58, v180, v181\n"
"v_mfma_f32_16x16x32_bf16 v[236:239], a[184:187], v[32:35], v[236:239]\n"
"ds_read_b64_tr_b16 a[160:161], v219 offset:0\n"
"v_cvt_pk_bf16_f32 v59, v182, v183\n"
"v_mfma_f32_16x16x32_bf16 v[236:239], a[188:191], v[40:43], v[236:239]\n"
"ds_read_b64_tr_b16 a[162:163], v219 offset:1024\n"
"v_fma_mix_f32 v184, v60, v160, v168 op_sel:[0,0,0] op_sel_hi:[1,0,0]\n"
"v_mfma_f32_16x16x32_bf16 v[232:235], a[184:187], v[36:39], v[232:235]\n"
"s_add_u32 m0, s51, 25352\n"
"ds_read_b64_tr_b16 a[164:165], v219 offset:4096\n"
"v_fma_mix_f32 v185, v60, v161, v169 op_sel:[1,0,0] op_sel_hi:[1,0,0]\n"
"v_mfma_f32_16x16x32_bf16 v[232:235], a[188:191], v[44:47], v[232:235]\n"
"s_waitcnt lgkmcnt(11)\n"
"buffer_load_dwordx4 v217, s[20:23], s50 offen offset:256 lds\n"
"ds_read_b64_tr_b16 a[166:167], v219 offset:5120\n"
"v_fma_mix_f32 v186, v61, v162, v170 op_sel:[0,0,0] op_sel_hi:[1,0,0]\n"
"v_mfma_f32_16x16x32_bf16 v[228:231], a[224:227], v[32:35], v[228:231]\n"
"s_add_u32 s50, s50, 425984\n"
"ds_read_b64_tr_b16 a[168:169], v219 offset:8\n"
"v_fma_mix_f32 v187, v61, v163, v171 op_sel:[1,0,0] op_sel_hi:[1,0,0]\n"
"v_mfma_f32_16x16x32_bf16 v[228:231], a[228:231], v[40:43], v[228:231]\n"
"ds_read_b64_tr_b16 a[170:171], v219 offset:1032\n"
"v_fma_mix_f32 v188, v62, v164, v172 op_sel:[0,0,0] op_sel_hi:[1,0,0]\n"
"v_mfma_f32_16x16x32_bf16 v[220:223], a[224:227], v[36:39], v[220:223]\n"
"ds_read_b64_tr_b16 a[172:173], v219 offset:4104\n"
"buffer_load_dwordx4 v[88:91], v249, s[28:31], s2 offen offset:3072 sc1\n"
"v_mfma_f32_16x16x32_bf16 v[220:223], a[228:231], v[44:47], v[220:223]\n"
"ds_read_b64_tr_b16 a[174:175], v219 offset:5128\n"
"v_fma_mix_f32 v189, v62, v165, v173 op_sel:[1,0,0] op_sel_hi:[1,0,0]\n"
"v_mfma_f32_16x16x32_bf16 v[212:215], a[232:235], v[32:35], v[212:215]\n"
"ds_read_b64_tr_b16 a[176:177], v219 offset:256\n"
"v_fma_mix_f32 v190, v63, v166, v174 op_sel:[0,0,0] op_sel_hi:[1,0,0]\n"
"v_mfma_f32_16x16x32_bf16 v[212:215], a[236:239], v[40:43], v[212:215]\n"
"ds_read_b64_tr_b16 a[178:179], v219 offset:1280\n"
"v_fma_mix_f32 v191, v63, v167, v175 op_sel:[1,0,0] op_sel_hi:[1,0,0]\n"
"v_mfma_f32_16x16x32_bf16 v[208:211], a[232:235], v[36:39], v[208:211]\n"
"ds_read_b64_tr_b16 a[180:181], v219 offset:4352\n"
"ds_read_b128 v[160:163], v224 offset:2304\n"
"v_mfma_f32_16x16x32_bf16 v[208:211], a[236:239], v[44:47], v[208:211]\n"
"s_waitcnt lgkmcnt(12)\n"
"ds_read_b64_tr_b16 a[182:183], v219 offset:5376\n"
"v_cvt_pk_bf16_f32 v60, v184, v185\n"
"v_mfma_f32_16x16x32_bf16 v[204:207], a[240:243], v[32:35], v[204:207]\n"
"ds_read_b64_tr_b16 a[184:185], v219 offset:264\n"
"v_cvt_pk_bf16_f32 v61, v186, v187\n"
"v_mfma_f32_16x16x32_bf16 v[204:207], a[244:247], v[40:43], v[204:207]\n"
"ds_read_b64_tr_b16 a[186:187], v219 offset:1288\n"
"ds_read_b128 v[164:167], v224 offset:2368\n"
"v_mfma_f32_16x16x32_bf16 v[200:203], a[240:243], v[36:39], v[200:203]\n"
"ds_read_b64_tr_b16 a[188:189], v219 offset:4360\n"
"v_cvt_pk_bf16_f32 v62, v188, v189\n"
"v_mfma_f32_16x16x32_bf16 v[200:203], a[244:247], v[44:47], v[200:203]\n"
"ds_read_b64_tr_b16 a[190:191], v219 offset:5384\n"
"v_cvt_pk_bf16_f32 v63, v190, v191\n"
"v_mfma_f32_16x16x32_bf16 v[196:199], a[248:251], v[32:35], v[196:199]\n"
"s_waitcnt vmcnt(10)\n"
"ds_read_b64_tr_b16 a[224:225], v219 offset:512\n"
"ds_read_b128 v[168:171], v224 offset:2432\n"
"v_fma_mix_f32 v176, v64, v144, v152 op_sel:[0,0,0] op_sel_hi:[1,0,0]\n"
"v_mfma_f32_16x16x32_bf16 v[196:199], a[252:255], v[40:43], v[196:199]\n"
"ds_read_b64_tr_b16 a[226:227], v219 offset:1536\n"
"v_fma_mix_f32 v177, v64, v145, v153 op_sel:[1,0,0] op_sel_hi:[1,0,0]\n"
"v_mfma_f32_16x16x32_bf16 v[192:195], a[248:251], v[36:39], v[192:195]\n"
"ds_read_b64_tr_b16 a[228:229], v219 offset:4608\n"
"v_fma_mix_f32 v178, v65, v146, v154 op_sel:[0,0,0] op_sel_hi:[1,0,0]\n"
"v_mfma_f32_16x16x32_bf16 v[192:195], a[252:255], v[44:47], v[192:195]\n"
"s_waitcnt lgkmcnt(14)\n"
"ds_read_b64_tr_b16 a[230:231], v219 offset:5632\n"
"buffer_load_dwordx4 v[92:95], v255, s[28:31], s2 offen offset:3072 sc1\n"
"v_mfma_f32_16x16x32_bf16 a[0:3], a[160:163], v[48:51], a[0:3]\n"
"s_add_u32 s2, s2, 4096\n"
"ds_read_b64_tr_b16 a[232:233], v219 offset:520\n"
"ds_read_b128 v[172:175], v224 offset:2496\n"
"v_mfma_f32_16x16x32_bf16 a[0:3], a[164:167], v[56:59], a[0:3]\n"
"s_waitcnt vmcnt(14)\n"
"ds_read_b64_tr_b16 a[234:235], v219 offset:1544\n"
"v_fma_mix_f32 v179, v65, v147, v155 op_sel:[1,0,0] op_sel_hi:[1,0,0]\n"
"v_mfma_f32_16x16x32_bf16 a[4:7], a[160:163], v[52:55], a[4:7]\n"
"s_barrier\n"
"ds_read_b64_tr_b16 a[236:237], v219 offset:4616\n"
"v_fma_mix_f32 v180, v66, v148, v156 op_sel:[0,0,0] op_sel_hi:[1,0,0]\n"
"v_mfma_f32_16x16x32_bf16 a[4:7], a[164:167], v[60:63], a[4:7]\n"
"ds_read_b64_tr_b16 a[238:239], v219 offset:5640\n"
"v_fma_mix_f32 v181, v66, v149, v157 op_sel:[1,0,0] op_sel_hi:[1,0,0]\n"
"v_mfma_f32_16x16x32_bf16 a[8:11], a[168:171], v[48:51], a[8:11]\n"
"s_add_u32 m0, s51, 32784\n"
"ds_read_b64_tr_b16 a[240:241], v219 offset:768\n"
"v_fma_mix_f32 v182, v67, v150, v158 op_sel:[0,0,0] op_sel_hi:[1,0,0]\n"
"v_mfma_f32_16x16x32_bf16 a[8:11], a[172:175], v[56:59], a[8:11]\n"
"ds_read_b64_tr_b16 a[242:243], v219 offset:1792\n"
"buffer_load_dwordx4 v217, s[20:23], s50 offen offset:0 lds\n"
"v_mfma_f32_16x16x32_bf16 a[12:15], a[168:171], v[52:55], a[12:15]\n"
"ds_read_b64_tr_b16 a[244:245], v219 offset:4864\n"
"v_fma_mix_f32 v183, v67, v151, v159 op_sel:[1,0,0] op_sel_hi:[1,0,0]\n"
"v_mfma_f32_16x16x32_bf16 a[12:15], a[172:175], v[60:63], a[12:15]\n"
"s_waitcnt lgkmcnt(13)\n"
"ds_read_b64_tr_b16 a[246:247], v219 offset:5888\n"
"v_cvt_pk_bf16_f32 v64, v176, v177\n"
"v_mfma_f32_16x16x32_bf16 a[16:19], a[176:179], v[48:51], a[16:19]\n"
"ds_read_b64_tr_b16 a[248:249], v219 offset:776\n"
"v_cvt_pk_bf16_f32 v65, v178, v179\n"
"v_mfma_f32_16x16x32_bf16 a[16:19], a[180:183], v[56:59], a[16:19]\n"
"ds_read_b64_tr_b16 a[250:251], v219 offset:1800\n"
"v_cvt_pk_bf16_f32 v66, v180, v181\n"
"v_cvt_pk_bf16_f32 v67, v182, v183\n"
"v_mfma_f32_16x16x32_bf16 a[20:23], a[176:179], v[52:55], a[20:23]\n"
"s_add_u32 m0, s51, 40720\n"
"ds_read_b64_tr_b16 a[252:253], v219 offset:4872\n"
"v_fma_mix_f32 v184, v68, v144, v152 op_sel:[0,0,0] op_sel_hi:[1,0,0]\n"
"v_mfma_f32_16x16x32_bf16 a[20:23], a[180:183], v[60:63], a[20:23]\n"
"ds_read_b64_tr_b16 a[254:255], v219 offset:5896\n"
"buffer_load_dwordx4 v217, s[20:23], s50 offen offset:256 lds\n"
"v_mfma_f32_16x16x32_bf16 a[24:27], a[184:187], v[48:51], a[24:27]\n"
"s_add_u32 s50, s50, 32768\n"
"ds_read_b64_tr_b16 a[160:161], v219 offset:8192\n"
"v_fma_mix_f32 v185, v68, v145, v153 op_sel:[1,0,0] op_sel_hi:[1,0,0]\n"
"v_mfma_f32_16x16x32_bf16 a[24:27], a[188:191], v[56:59], a[24:27]\n"
"ds_read_b64_tr_b16 a[162:163], v219 offset:9216\n"
"buffer_load_dwordx4 v[96:99], v249, s[28:31], s2 offen offset:0 sc1\n"
"v_mfma_f32_16x16x32_bf16 a[28:31], a[184:187], v[52:55], a[28:31]\n"
"ds_read_b64_tr_b16 a[164:165], v219 offset:12288\n"
"v_fma_mix_f32 v186, v69, v146, v154 op_sel:[0,0,0] op_sel_hi:[1,0,0]\n"
"v_mfma_f32_16x16x32_bf16 a[28:31], a[188:191], v[60:63], a[28:31]\n"
"s_waitcnt lgkmcnt(11)\n"
"ds_read_b64_tr_b16 a[166:167], v219 offset:13312\n"
"v_fma_mix_f32 v187, v69, v147, v155 op_sel:[1,0,0] op_sel_hi:[1,0,0]\n"
"v_mfma_f32_16x16x32_bf16 a[32:35], a[224:227], v[48:51], a[32:35]\n"
"s_add_u32 m0, s51, 33808\n"
"ds_read_b64_tr_b16 a[168:169], v219 offset:8200\n"
"v_fma_mix_f32 v188, v70, v148, v156 op_sel:[0,0,0] op_sel_hi:[1,0,0]\n"
"v_mfma_f32_16x16x32_bf16 a[32:35], a[228:231], v[56:59], a[32:35]\n"
"ds_read_b64_tr_b16 a[170:171], v219 offset:9224\n"
"buffer_load_dwordx4 v217, s[20:23], s50 offen offset:0 lds\n"
"v_mfma_f32_16x16x32_bf16 a[36:39], a[224:227], v[52:55], a[36:39]\n"
"ds_read_b64_tr_b16 a[172:173], v219 offset:12296\n"
"v_fma_mix_f32 v189, v70, v149, v157 op_sel:[1,0,0] op_sel_hi:[1,0,0]\n"
"v_mfma_f32_16x16x32_bf16 a[36:39], a[228:231], v[60:63], a[36:39]\n"
"ds_read_b64_tr_b16 a[174:175], v219 offset:13320\n"
"v_fma_mix_f32 v190, v71, v150, v158 op_sel:[0,0,0] op_sel_hi:[1,0,0]\n"
"v_mfma_f32_16x16x32_bf16 a[40:43], a[232:235], v[48:51], a[40:43]\n"
"ds_read_b64_tr_b16 a[176:177], v219 offset:8448\n"
"v_fma_mix_f32 v191, v71, v151, v159 op_sel:[1,0,0] op_sel_hi:[1,0,0]\n"
"v_mfma_f32_16x16x32_bf16 a[40:43], a[236:239], v[56:59], a[40:43]\n"
"ds_read_b64_tr_b16 a[178:179], v219 offset:9472\n"
"ds_read_b128 v[144:147], v224 offset:2560\n"
"v_mfma_f32_16x16x32_bf16 a[44:47], a[232:235], v[52:55], a[44:47]\n"
"s_add_u32 m0, s51, 41744\n"
"ds_read_b64_tr_b16 a[180:181], v219 offset:12544\n"
"v_cvt_pk_bf16_f32 v68, v184, v185\n"
"v_cvt_pk_bf16_f32 v69, v186, v187\n"
"v_mfma_f32_16x16x32_bf16 a[44:47], a[236:239], v[60:63], a[44:47]\n"
"s_waitcnt lgkmcnt(12)\n"
"ds_read_b64_tr_b16 a[182:183], v219 offset:13568\n"
"buffer_load_dwordx4 v217, s[20:23], s50 offen offset:256 lds\n"
"v_mfma_f32_16x16x32_bf16 a[48:51], a[240:243], v[48:51], a[48:51]\n"
"s_add_u32 s50, s50, 32768\n"
"ds_read_b64_tr_b16 a[184:185], v219 offset:8456\n"
"ds_read_b128 v[148:151], v224 offset:2624\n"
"v_mfma_f32_16x16x32_bf16 a[48:51], a[244:247], v[56:59], a[48:51]\n"
"ds_read_b64_tr_b16 a[186:187], v219 offset:9480\n"
"v_cvt_pk_bf16_f32 v70, v188, v189\n"
"v_mfma_f32_16x16x32_bf16 a[52:55], a[240:243], v[52:55], a[52:55]\n"
"ds_read_b64_tr_b16 a[188:189], v219 offset:12552\n"
"buffer_load_dwordx4 v[100:103], v255, s[28:31], s2 offen offset:0 sc1\n"
"v_mfma_f32_16x16x32_bf16 a[52:55], a[244:247], v[60:63], a[52:55]\n"
"ds_read_b64_tr_b16 a[190:191], v219 offset:13576\n"
"ds_read_b128 v[152:155], v224 offset:2688\n"
"v_mfma_f32_16x16x32_bf16 a[56:59], a[248:251], v[48:51], a[56:59]\n"
"s_add_u32 m0, s51, 49176\n"
"ds_read_b64_tr_b16 a[224:225], v219 offset:8704\n"
"v_cvt_pk_bf16_f32 v71, v190, v191\n"
"v_mfma_f32_16x16x32_bf16 a[56:59], a[252:255], v[56:59], a[56:59]\n"
"ds_read_b64_tr_b16 a[226:227], v219 offset:9728\n"
"buffer_load_dwordx4 v217, s[20:23], s50 offen offset:0 lds\n"
"v_mfma_f32_16x16x32_bf16 a[60:63], a[248:251], v[52:55], a[60:63]\n"
"ds_read_b64_tr_b16 a[228:229], v219 offset:12800\n"
"ds_read_b128 v[156:159], v224 offset:2752\n"
"v_mfma_f32_16x16x32_bf16 a[60:63], a[252:255], v[60:63], a[60:63]\n"
"s_waitcnt lgkmcnt(14)\n"
"ds_read_b64_tr_b16 a[230:231], v219 offset:13824\n"
"v_fma_mix_f32 v176, v72, v160, v168 op_sel:[0,0,0] op_sel_hi:[1,0,0]\n"
"v_mfma_f32_16x16x32_bf16 a[64:67], a[160:163], v[48:51], a[64:67]\n"
"ds_read_b64_tr_b16 a[232:233], v219 offset:8712\n"
"v_fma_mix_f32 v177, v72, v161, v169 op_sel:[1,0,0] op_sel_hi:[1,0,0]\n"
"v_mfma_f32_16x16x32_bf16 a[64:67], a[164:167], v[56:59], a[64:67]\n"
"ds_read_b64_tr_b16 a[234:235], v219 offset:9736\n"
"v_fma_mix_f32 v178, v73, v162, v170 op_sel:[0,0,0] op_sel_hi:[1,0,0]\n"
"v_mfma_f32_16x16x32_bf16 a[68:71], a[160:163], v[52:55], a[68:71]\n"
"s_add_u32 m0, s51, 57112\n"
"ds_read_b64_tr_b16 a[236:237], v219 offset:12808\n"
"v_fma_mix_f32 v179, v73, v163, v171 op_sel:[1,0,0] op_sel_hi:[1,0,0]\n"
"v_mfma_f32_16x16x32_bf16 a[68:71], a[164:167], v[60:63], a[68:71]\n"
"ds_read_b64_tr_b16 a[238:239], v219 offset:13832\n"
"buffer_load_dwordx4 v217, s[20:23], s50 offen offset:256 lds\n"
"v_fma_mix_f32 v180, v74, v164, v172 op_sel:[0,0,0] op_sel_hi:[1,0,0]\n"
"v_mfma_f32_16x16x32_bf16 a[72:75], a[168:171], v[48:51], a[72:75]\n"
"s_add_u32 s50, s50, 32768\n"
"ds_read_b64_tr_b16 a[240:241], v219 offset:8960\n"
"v_fma_mix_f32 v181, v74, v165, v173 op_sel:[1,0,0] op_sel_hi:[1,0,0]\n"
"v_mfma_f32_16x16x32_bf16 a[72:75], a[172:175], v[56:59], a[72:75]\n"
"ds_read_b64_tr_b16 a[242:243], v219 offset:9984\n"
"v_fma_mix_f32 v182, v75, v166, v174 op_sel:[0,0,0] op_sel_hi:[1,0,0]\n"
"v_mfma_f32_16x16x32_bf16 a[76:79], a[168:171], v[52:55], a[76:79]\n"
"ds_read_b64_tr_b16 a[244:245], v219 offset:13056\n"
"v_fma_mix_f32 v183, v75, v167, v175 op_sel:[1,0,0] op_sel_hi:[1,0,0]\n"
"v_mfma_f32_16x16x32_bf16 a[76:79], a[172:175], v[60:63], a[76:79]\n"
"s_waitcnt lgkmcnt(12)\n"
"ds_read_b64_tr_b16 a[246:247], v219 offset:14080\n"
"v_cvt_pk_bf16_f32 v72, v176, v177\n"
"v_mfma_f32_16x16x32_bf16 a[80:83], a[176:179], v[48:51], a[80:83]\n"
"s_add_u32 m0, s51, 50200\n"
"ds_read_b64_tr_b16 a[248:249], v219 offset:8968\n"
"v_cvt_pk_bf16_f32 v73, v178, v179\n"
"v_mfma_f32_16x16x32_bf16 a[80:83], a[180:183], v[56:59], a[80:83]\n"
"ds_read_b64_tr_b16 a[250:251], v219 offset:9992\n"
"buffer_load_dwordx4 v217, s[20:23], s50 offen offset:0 lds\n"
"v_mfma_f32_16x16x32_bf16 a[84:87], a[176:179], v[52:55], a[84:87]\n"
"ds_read_b64_tr_b16 a[252:253], v219 offset:13064\n"
"v_cvt_pk_bf16_f32 v74, v180, v181\n"
"v_mfma_f32_16x16x32_bf16 a[84:87], a[180:183], v[60:63], a[84:87]\n"
"ds_read_b64_tr_b16 a[254:255], v219 offset:14088\n"
"buffer_load_dwordx4 v[104:107], v249, s[28:31], s2 offen offset:1024 sc1\n"
"v_mfma_f32_16x16x32_bf16 v[236:239], a[184:187], v[48:51], v[236:239]\n"
"ds_read_b64_tr_b16 a[160:161], v219 offset:32784\n"
"v_cvt_pk_bf16_f32 v75, v182, v183\n"
"v_mfma_f32_16x16x32_bf16 v[236:239], a[188:191], v[56:59], v[236:239]\n"
"ds_read_b64_tr_b16 a[162:163], v219 offset:33808\n"
"v_fma_mix_f32 v184, v76, v160, v168 op_sel:[0,0,0] op_sel_hi:[1,0,0]\n"
"v_mfma_f32_16x16x32_bf16 v[232:235], a[184:187], v[52:55], v[232:235]\n"
"s_add_u32 m0, s51, 58136\n"
"ds_read_b64_tr_b16 a[164:165], v219 offset:36880\n"
"v_fma_mix_f32 v185, v76, v161, v169 op_sel:[1,0,0] op_sel_hi:[1,0,0]\n"
"v_mfma_f32_16x16x32_bf16 v[232:235], a[188:191], v[60:63], v[232:235]\n"
"s_waitcnt lgkmcnt(11)\n"
"buffer_load_dwordx4 v217, s[20:23], s50 offen offset:256 lds\n"
"ds_read_b64_tr_b16 a[166:167], v219 offset:37904\n"
"v_mfma_f32_16x16x32_bf16 v[228:231], a[224:227], v[48:51], v[228:231]\n"
"s_add_u32 s50, s50, 425984\n"
"ds_read_b64_tr_b16 a[168:169], v219 offset:32792\n"
"v_fma_mix_f32 v186, v77, v162, v170 op_sel:[0,0,0] op_sel_hi:[1,0,0]\n"
"v_fma_mix_f32 v187, v77, v163, v171 op_sel:[1,0,0] op_sel_hi:[1,0,0]\n"
"v_mfma_f32_16x16x32_bf16 v[228:231], a[228:231], v[56:59], v[228:231]\n"
"ds_read_b64_tr_b16 a[170:171], v219 offset:33816\n"
"v_fma_mix_f32 v188, v78, v164, v172 op_sel:[0,0,0] op_sel_hi:[1,0,0]\n"
"v_mfma_f32_16x16x32_bf16 v[220:223], a[224:227], v[52:55], v[220:223]\n"
"ds_read_b64_tr_b16 a[172:173], v219 offset:36888\n"
"v_fma_mix_f32 v189, v78, v165, v173 op_sel:[1,0,0] op_sel_hi:[1,0,0]\n"
"v_mfma_f32_16x16x32_bf16 v[220:223], a[228:231], v[60:63], v[220:223]\n"
"ds_read_b64_tr_b16 a[174:175], v219 offset:37912\n"
"v_fma_mix_f32 v190, v79, v166, v174 op_sel:[0,0,0] op_sel_hi:[1,0,0]\n"
"v_mfma_f32_16x16x32_bf16 v[212:215], a[232:235], v[48:51], v[212:215]\n"
"ds_read_b64_tr_b16 a[176:177], v219 offset:33040\n"
"v_fma_mix_f32 v191, v79, v167, v175 op_sel:[1,0,0] op_sel_hi:[1,0,0]\n"
"v_mfma_f32_16x16x32_bf16 v[212:215], a[236:239], v[56:59], v[212:215]\n"
"ds_read_b64_tr_b16 a[178:179], v219 offset:34064\n"
"ds_read_b128 v[160:163], v224 offset:2816\n"
"v_mfma_f32_16x16x32_bf16 v[208:211], a[232:235], v[52:55], v[208:211]\n"
"ds_read_b64_tr_b16 a[180:181], v219 offset:37136\n"
"v_cvt_pk_bf16_f32 v76, v184, v185\n"
"v_mfma_f32_16x16x32_bf16 v[208:211], a[236:239], v[60:63], v[208:211]\n"
"s_waitcnt lgkmcnt(12)\n"
"ds_read_b64_tr_b16 a[182:183], v219 offset:38160\n"
"v_cvt_pk_bf16_f32 v77, v186, v187\n"
"v_mfma_f32_16x16x32_bf16 v[204:207], a[240:243], v[48:51], v[204:207]\n"
"ds_read_b64_tr_b16 a[184:185], v219 offset:33048\n"
"buffer_load_dwordx4 v[108:111], v255, s[28:31], s2 offen offset:1024 sc1\n"
"v_mfma_f32_16x16x32_bf16 v[204:207], a[244:247], v[56:59], v[204:207]\n"
"ds_read_b64_tr_b16 a[186:187], v219 offset:34072\n"
"ds_read_b128 v[164:167], v224 offset:2880\n"
"v_mfma_f32_16x16x32_bf16 v[200:203], a[240:243], v[52:55], v[200:203]\n"
"ds_read_b64_tr_b16 a[188:189], v219 offset:37144\n"
"v_cvt_pk_bf16_f32 v78, v188, v189\n"
"v_mfma_f32_16x16x32_bf16 v[200:203], a[244:247], v[60:63], v[200:203]\n"
"ds_read_b64_tr_b16 a[190:191], v219 offset:38168\n"
"v_cvt_pk_bf16_f32 v79, v190, v191\n"
"v_mfma_f32_16x16x32_bf16 v[196:199], a[248:251], v[48:51], v[196:199]\n"
"ds_read_b64_tr_b16 a[224:225], v219 offset:33296\n"
"ds_read_b128 v[168:171], v224 offset:2944\n"
"v_mfma_f32_16x16x32_bf16 v[196:199], a[252:255], v[56:59], v[196:199]\n"
"s_waitcnt vmcnt(12)\n"
"ds_read_b64_tr_b16 a[226:227], v219 offset:34320\n"
"v_fma_mix_f32 v176, v80, v144, v152 op_sel:[0,0,0] op_sel_hi:[1,0,0]\n"
"v_fma_mix_f32 v177, v80, v145, v153 op_sel:[1,0,0] op_sel_hi:[1,0,0]\n"
"v_mfma_f32_16x16x32_bf16 v[192:195], a[248:251], v[52:55], v[192:195]\n"
"ds_read_b64_tr_b16 a[228:229], v219 offset:37392\n"
"v_fma_mix_f32 v178, v81, v146, v154 op_sel:[0,0,0] op_sel_hi:[1,0,0]\n"
"v_mfma_f32_16x16x32_bf16 v[192:195], a[252:255], v[60:63], v[192:195]\n"
"s_waitcnt lgkmcnt(14)\n"
"ds_read_b64_tr_b16 a[230:231], v219 offset:38416\n"
"ds_read_b128 v[172:175], v224 offset:3008\n"
"v_mfma_f32_16x16x32_bf16 a[0:3], a[160:163], v[64:67], a[0:3]\n"
"ds_read_b64_tr_b16 a[232:233], v219 offset:33304\n"
"v_fma_mix_f32 v179, v81, v147, v155 op_sel:[1,0,0] op_sel_hi:[1,0,0]\n"
"v_mfma_f32_16x16x32_bf16 a[0:3], a[164:167], v[72:75], a[0:3]\n"
"s_waitcnt vmcnt(14)\n"
"ds_read_b64_tr_b16 a[234:235], v219 offset:34328\n"
"v_fma_mix_f32 v180, v82, v148, v156 op_sel:[0,0,0] op_sel_hi:[1,0,0]\n"
"v_mfma_f32_16x16x32_bf16 a[4:7], a[160:163], v[68:71], a[4:7]\n"
"s_barrier\n"
"ds_read_b64_tr_b16 a[236:237], v219 offset:37400\n"
"v_fma_mix_f32 v181, v82, v149, v157 op_sel:[1,0,0] op_sel_hi:[1,0,0]\n"
"v_mfma_f32_16x16x32_bf16 a[4:7], a[164:167], v[76:79], a[4:7]\n"
"ds_read_b64_tr_b16 a[238:239], v219 offset:38424\n"
"v_fma_mix_f32 v182, v83, v150, v158 op_sel:[0,0,0] op_sel_hi:[1,0,0]\n"
"v_mfma_f32_16x16x32_bf16 a[8:11], a[168:171], v[64:67], a[8:11]\n"
"s_add_u32 m0, s51, 65568\n"
"ds_read_b64_tr_b16 a[240:241], v219 offset:33552\n"
"v_fma_mix_f32 v183, v83, v151, v159 op_sel:[1,0,0] op_sel_hi:[1,0,0]\n"
"v_mfma_f32_16x16x32_bf16 a[8:11], a[172:175], v[72:75], a[8:11]\n"
"ds_read_b64_tr_b16 a[242:243], v219 offset:34576\n"
"buffer_load_dwordx4 v217, s[20:23], s50 offen offset:0 lds\n"
"v_mfma_f32_16x16x32_bf16 a[12:15], a[168:171], v[68:71], a[12:15]\n"
"ds_read_b64_tr_b16 a[244:245], v219 offset:37648\n"
"v_cvt_pk_bf16_f32 v80, v176, v177\n"
"v_mfma_f32_16x16x32_bf16 a[12:15], a[172:175], v[76:79], a[12:15]\n"
"s_waitcnt lgkmcnt(13)\n"
"ds_read_b64_tr_b16 a[246:247], v219 offset:38672\n"
"buffer_load_dwordx4 v[112:115], v249, s[28:31], s2 offen offset:2048 sc1\n"
"v_mfma_f32_16x16x32_bf16 a[16:19], a[176:179], v[64:67], a[16:19]\n"
"ds_read_b64_tr_b16 a[248:249], v219 offset:33560\n"
"v_cvt_pk_bf16_f32 v81, v178, v179\n"
"v_mfma_f32_16x16x32_bf16 a[16:19], a[180:183], v[72:75], a[16:19]\n"
"ds_read_b64_tr_b16 a[250:251], v219 offset:34584\n"
"v_cvt_pk_bf16_f32 v82, v180, v181\n"
"v_mfma_f32_16x16x32_bf16 a[20:23], a[176:179], v[68:71], a[20:23]\n"
"s_add_u32 m0, s51, 73504\n"
"ds_read_b64_tr_b16 a[252:253], v219 offset:37656\n"
"v_cvt_pk_bf16_f32 v83, v182, v183\n"
"v_fma_mix_f32 v184, v84, v144, v152 op_sel:[0,0,0] op_sel_hi:[1,0,0]\n"
"v_mfma_f32_16x16x32_bf16 a[20:23], a[180:183], v[76:79], a[20:23]\n"
"ds_read_b64_tr_b16 a[254:255], v219 offset:38680\n"
"buffer_load_dwordx4 v217, s[20:23], s50 offen offset:256 lds\n"
"v_mfma_f32_16x16x32_bf16 a[24:27], a[184:187], v[64:67], a[24:27]\n"
"s_add_u32 s50, s50, 32768\n"
"ds_read_b64_tr_b16 a[160:161], v219 offset:40976\n"
"v_fma_mix_f32 v185, v84, v145, v153 op_sel:[1,0,0] op_sel_hi:[1,0,0]\n"
"v_mfma_f32_16x16x32_bf16 a[24:27], a[188:191], v[72:75], a[24:27]\n"
"ds_read_b64_tr_b16 a[162:163], v219 offset:42000\n"
"v_fma_mix_f32 v186, v85, v146, v154 op_sel:[0,0,0] op_sel_hi:[1,0,0]\n"
"v_mfma_f32_16x16x32_bf16 a[28:31], a[184:187], v[68:71], a[28:31]\n"
"ds_read_b64_tr_b16 a[164:165], v219 offset:45072\n"
"v_fma_mix_f32 v187, v85, v147, v155 op_sel:[1,0,0] op_sel_hi:[1,0,0]\n"
"v_mfma_f32_16x16x32_bf16 a[28:31], a[188:191], v[76:79], a[28:31]\n"
"s_waitcnt lgkmcnt(11)\n"
"ds_read_b64_tr_b16 a[166:167], v219 offset:46096\n"
"v_fma_mix_f32 v188, v86, v148, v156 op_sel:[0,0,0] op_sel_hi:[1,0,0]\n"
"v_mfma_f32_16x16x32_bf16 a[32:35], a[224:227], v[64:67], a[32:35]\n"
"s_add_u32 m0, s51, 66592\n"
"ds_read_b64_tr_b16 a[168:169], v219 offset:40984\n"
"AT_pg3:\n"
"s_cbranch_execz AT_hr\n"
"v_fma_mix_f32 v189, v86, v149, v157 op_sel:[1,0,0] op_sel_hi:[1,0,0]\n"
"v_mfma_f32_16x16x32_bf16 a[32:35], a[228:231], v[72:75], a[32:35]\n"
"ds_read_b64_tr_b16 a[170:171], v219 offset:42008\n"
"buffer_load_dwordx4 v217, s[20:23], s50 offen offset:0 lds\n"
"v_mfma_f32_16x16x32_bf16 a[36:39], a[224:227], v[68:71], a[36:39]\n"
"ds_read_b64_tr_b16 a[172:173], v219 offset:45080\n"
"v_fma_mix_f32 v190, v87, v150, v158 op_sel:[0,0,0] op_sel_hi:[1,0,0]\n"
"v_mfma_f32_16x16x32_bf16 a[36:39], a[228:231], v[76:79], a[36:39]\n"
"ds_read_b64_tr_b16 a[174:175], v219 offset:46104\n"
"v_fma_mix_f32 v191, v87, v151, v159 op_sel:[1,0,0] op_sel_hi:[1,0,0]\n"
"v_mfma_f32_16x16x32_bf16 a[40:43], a[232:235], v[64:67], a[40:43]\n"
"ds_read_b64_tr_b16 a[176:177], v219 offset:41232\n"
"buffer_load_dwordx4 v[116:119], v255, s[28:31], s2 offen offset:2048 sc1\n"
"v_mfma_f32_16x16x32_bf16 a[40:43], a[236:239], v[72:75], a[40:43]\n"
"ds_read_b64_tr_b16 a[178:179], v219 offset:42256\n"
"ds_read_b128 v[144:147], v224 offset:3072\n"
"v_mfma_f32_16x16x32_bf16 a[44:47], a[232:235], v[68:71], a[44:47]\n"
"s_add_u32 m0, s51, 74528\n"
"ds_read_b64_tr_b16 a[180:181], v219 offset:45328\n"
"v_cvt_pk_bf16_f32 v84, v184, v185\n"
"v_mfma_f32_16x16x32_bf16 a[44:47], a[236:239], v[76:79], a[44:47]\n"
"s_waitcnt lgkmcnt(12)\n"
"ds_read_b64_tr_b16 a[182:183], v219 offset:46352\n"
"buffer_load_dwordx4 v217, s[20:23], s50 offen offset:256 lds\n"
"v_cvt_pk_bf16_f32 v85, v186, v187\n"
"v_mfma_f32_16x16x32_bf16 a[48:51], a[240:243], v[64:67], a[48:51]\n"
"s_add_u32 s50, s50, 32768\n"
"ds_read_b64_tr_b16 a[184:185], v219 offset:41240\n"
"ds_read_b128 v[148:151], v224 offset:3136\n"
"v_mfma_f32_16x16x32_bf16 a[48:51], a[244:247], v[72:75], a[48:51]\n"
"ds_read_b64_tr_b16 a[186:187], v219 offset:42264\n"
"v_cvt_pk_bf16_f32 v86, v188, v189\n"
"v_mfma_f32_16x16x32_bf16 a[52:55], a[240:243], v[68:71], a[52:55]\n"
"ds_read_b64_tr_b16 a[188:189], v219 offset:45336\n"
"v_cvt_pk_bf16_f32 v87, v190, v191\n"
"v_mfma_f32_16x16x32_bf16 a[52:55], a[244:247], v[76:79], a[52:55]\n"
"ds_read_b64_tr_b16 a[190:191], v219 offset:46360\n"
"ds_read_b128 v[152:155], v224 offset:3200\n"
"v_mfma_f32_16x16x32_bf16 a[56:59], a[248:251], v[64:67], a[56:59]\n"
"s_add_u32 m0, s51, 81960\n"
"ds_read_b64_tr_b16 a[224:225], v219 offset:41488\n"
"v_fma_mix_f32 v176, v88, v160, v168 op_sel:[0,0,0] op_sel_hi:[1,0,0]\n"
"v_mfma_f32_16x16x32_bf16 a[56:59], a[252:255], v[72:75], a[56:59]\n"
"ds_read_b64_tr_b16 a[226:227], v219 offset:42512\n"
"buffer_load_dwordx4 v217, s[20:23], s50 offen offset:0 lds\n"
"v_mfma_f32_16x16x32_bf16 a[60:63], a[248:251], v[68:71], a[60:63]\n"
"ds_read_b64_tr_b16 a[228:229], v219 offset:45584\n"
"ds_read_b128 v[156:159], v224 offset:3264\n"
"v_mfma_f32_16x16x32_bf16 a[60:63], a[252:255], v[76:79], a[60:63]\n"
"s_waitcnt lgkmcnt(14)\n"
"ds_read_b64_tr_b16 a[230:231], v219 offset:46608\n"
"v_fma_mix_f32 v177, v88, v161, v169 op_sel:[1,0,0] op_sel_hi:[1,0,0]\n"
"v_mfma_f32_16x16x32_bf16 a[64:67], a[160:163], v[64:67], a[64:67]\n"
"ds_read_b64_tr_b16 a[232:233], v219 offset:41496\n"
"v_fma_mix_f32 v178, v89, v162, v170 op_sel:[0,0,0] op_sel_hi:[1,0,0]\n"
"v_mfma_f32_16x16x32_bf16 a[64:67], a[164:167], v[72:75], a[64:67]\n"
"ds_read_b64_tr_b16 a[234:235], v219 offset:42520\n"
"v_fma_mix_f32 v179, v89, v163, v171 op_sel:[1,0,0] op_sel_hi:[1,0,0]\n"
"v_mfma_f32_16x16x32_bf16 a[68:71], a[160:163], v[68:71], a[68:71]\n"
"s_add_u32 m0, s51, 89896\n"
"ds_read_b64_tr_b16 a[236:237], v219 offset:45592\n"
"v_fma_mix_f32 v180, v90, v164, v172 op_sel:[0,0,0] op_sel_hi:[1,0,0]\n"
"v_mfma_f32_16x16x32_bf16 a[68:71], a[164:167], v[76:79], a[68:71]\n"
"ds_read_b64_tr_b16 a[238:239], v219 offset:46616\n"
"buffer_load_dwordx4 v217, s[20:23], s50 offen offset:256 lds\n"
"v_mfma_f32_16x16x32_bf16 a[72:75], a[168:171], v[64:67], a[72:75]\n"
"s_add_u32 s50, s50, 32768\n"
"ds_read_b64_tr_b16 a[240:241], v219 offset:41744\n"
"v_fma_mix_f32 v181, v90, v165, v173 op_sel:[1,0,0] op_sel_hi:[1,0,0]\n"
"v_fma_mix_f32 v182, v91, v166, v174 op_sel:[0,0,0] op_sel_hi:[1,0,0]\n"
"v_mfma_f32_16x16x32_bf16 a[72:75], a[172:175], v[72:75], a[72:75]\n"
"ds_read_b64_tr_b16 a[242:243], v219 offset:42768\n"
"buffer_load_dwordx4 v[120:123], v249, s[28:31], s2 offen offset:3072 sc1\n"
"v_mfma_f32_16x16x32_bf16 a[76:79], a[168:171], v[68:71], a[76:79]\n"
"ds_read_b64_tr_b16 a[244:245], v219 offset:45840\n"
"v_fma_mix_f32 v183, v91, v167, v175 op_sel:[1,0,0] op_sel_hi:[1,0,0]\n"
"v_mfma_f32_16x16x32_bf16 a[76:79], a[172:175], v[76:79], a[76:79]\n"
"s_waitcnt lgkmcnt(12)\n"
"ds_read_b64_tr_b16 a[246:247], v219 offset:46864\n"
"v_cvt_pk_bf16_f32 v88, v176, v177\n"
"v_mfma_f32_16x16x32_bf16 a[80:83], a[176:179], v[64:67], a[80:83]\n"
"s_add_u32 m0, s51, 82984\n"
"ds_read_b64_tr_b16 a[248:249], v219 offset:41752\n"
"v_cvt_pk_bf16_f32 v89, v178, v179\n"
"v_mfma_f32_16x16x32_bf16 a[80:83], a[180:183], v[72:75], a[80:83]\n"
"ds_read_b64_tr_b16 a[250:251], v219 offset:42776\n"
"buffer_load_dwordx4 v217, s[20:23], s50 offen offset:0 lds\n"
"v_mfma_f32_16x16x32_bf16 a[84:87], a[176:179], v[68:71], a[84:87]\n"
"ds_read_b64_tr_b16 a[252:253], v219 offset:45848\n"
"v_cvt_pk_bf16_f32 v90, v180, v181\n"
"v_mfma_f32_16x16x32_bf16 a[84:87], a[180:183], v[76:79], a[84:87]\n"
"ds_read_b64_tr_b16 a[254:255], v219 offset:46872\n"
"v_cvt_pk_bf16_f32 v91, v182, v183\n"
"v_mfma_f32_16x16x32_bf16 v[236:239], a[184:187], v[64:67], v[236:239]\n"
"ds_read_b64_tr_b16 a[160:161], v218 offset:0\n"
"v_fma_mix_f32 v184, v92, v160, v168 op_sel:[0,0,0] op_sel_hi:[1,0,0]\n"
"v_mfma_f32_16x16x32_bf16 v[236:239], a[188:191], v[72:75], v[236:239]\n"
"ds_read_b64_tr_b16 a[162:163], v218 offset:1024\n"
"v_fma_mix_f32 v185, v92, v161, v169 op_sel:[1,0,0] op_sel_hi:[1,0,0]\n"
"v_mfma_f32_16x16x32_bf16 v[232:235], a[184:187], v[68:71], v[232:235]\n"
"s_add_u32 m0, s51, 90920\n"
"ds_read_b64_tr_b16 a[164:165], v218 offset:4096\n"
"v_fma_mix_f32 v186, v93, v162, v170 op_sel:[0,0,0] op_sel_hi:[1,0,0]\n"
"v_mfma_f32_16x16x32_bf16 v[232:235], a[188:191], v[76:79], v[232:235]\n"
"s_waitcnt lgkmcnt(11)\n"
"buffer_load_dwordx4 v217, s[20:23], s50 offen offset:256 lds\n"
"ds_read_b64_tr_b16 a[166:167], v218 offset:5120\n"
"v_mfma_f32_16x16x32_bf16 v[228:231], a[224:227], v[64:67], v[228:231]\n"
"s_add_u32 s50, s50, 425984\n"
"ds_read_b64_tr_b16 a[168:169], v218 offset:8\n"
"v_fma_mix_f32 v187, v93, v163, v171 op_sel:[1,0,0] op_sel_hi:[1,0,0]\n"
"v_mfma_f32_16x16x32_bf16 v[228:231], a[228:231], v[72:75], v[228:231]\n"
"ds_read_b64_tr_b16 a[170:171], v218 offset:1032\n"
"v_fma_mix_f32 v188, v94, v164, v172 op_sel:[0,0,0] op_sel_hi:[1,0,0]\n"
"v_fma_mix_f32 v189, v94, v165, v173 op_sel:[1,0,0] op_sel_hi:[1,0,0]\n"
"v_mfma_f32_16x16x32_bf16 v[220:223], a[224:227], v[68:71], v[220:223]\n"
"ds_read_b64_tr_b16 a[172:173], v218 offset:4104\n"
"buffer_load_dwordx4 v[124:127], v255, s[28:31], s2 offen offset:3072 sc1\n"
"v_mfma_f32_16x16x32_bf16 v[220:223], a[228:231], v[76:79], v[220:223]\n"
"s_add_u32 s2, s2, 4096\n"
"ds_read_b64_tr_b16 a[174:175], v218 offset:5128\n"
"v_fma_mix_f32 v190, v95, v166, v174 op_sel:[0,0,0] op_sel_hi:[1,0,0]\n"
"v_mfma_f32_16x16x32_bf16 v[212:215], a[232:235], v[64:67], v[212:215]\n"
"ds_read_b64_tr_b16 a[176:177], v218 offset:256\n"
"v_fma_mix_f32 v191, v95, v167, v175 op_sel:[1,0,0] op_sel_hi:[1,0,0]\n"
"v_mfma_f32_16x16x32_bf16 v[212:215], a[236:239], v[72:75], v[212:215]\n"
"ds_read_b64_tr_b16 a[178:179], v218 offset:1280\n"
"ds_read_b128 v[160:163], v224 offset:3328\n"
"v_mfma_f32_16x16x32_bf16 v[208:211], a[232:235], v[68:71], v[208:211]\n"
"ds_read_b64_tr_b16 a[180:181], v218 offset:4352\n"
"v_cvt_pk_bf16_f32 v92, v184, v185\n"
"v_mfma_f32_16x16x32_bf16 v[208:211], a[236:239], v[76:79], v[208:211]\n"
"s_waitcnt lgkmcnt(12)\n"
"ds_read_b64_tr_b16 a[182:183], v218 offset:5376\n"
"v_cvt_pk_bf16_f32 v93, v186, v187\n"
"v_mfma_f32_16x16x32_bf16 v[204:207], a[240:243], v[64:67], v[204:207]\n"
"ds_read_b64_tr_b16 a[184:185], v218 offset:264\n"
"ds_read_b128 v[164:167], v224 offset:3392\n"
"v_mfma_f32_16x16x32_bf16 v[204:207], a[244:247], v[72:75], v[204:207]\n"
"ds_read_b64_tr_b16 a[186:187], v218 offset:1288\n"
"v_cvt_pk_bf16_f32 v94, v188, v189\n"
"v_mfma_f32_16x16x32_bf16 v[200:203], a[240:243], v[68:71], v[200:203]\n"
"ds_read_b64_tr_b16 a[188:189], v218 offset:4360\n"
"v_cvt_pk_bf16_f32 v95, v190, v191\n"
"v_mfma_f32_16x16x32_bf16 v[200:203], a[244:247], v[76:79], v[200:203]\n"
"ds_read_b64_tr_b16 a[190:191], v218 offset:5384\n"
"ds_read_b128 v[168:171], v224 offset:3456\n"
"v_mfma_f32_16x16x32_bf16 v[196:199], a[248:251], v[64:67], v[196:199]\n"
"s_waitcnt vmcnt(12)\n"
"ds_read_b64_tr_b16 a[224:225], v218 offset:512\n"
"v_fma_mix_f32 v176, v96, v144, v152 op_sel:[0,0,0] op_sel_hi:[1,0,0]\n"
"v_mfma_f32_16x16x32_bf16 v[196:199], a[252:255], v[72:75], v[196:199]\n"
"ds_read_b64_tr_b16 a[226:227], v218 offset:1536\n"
"v_fma_mix_f32 v177, v96, v145, v153 op_sel:[1,0,0] op_sel_hi:[1,0,0]\n"
"v_mfma_f32_16x16x32_bf16 v[192:195], a[248:251], v[68:71], v[192:195]\n"
"ds_read_b64_tr_b16 a[228:229], v218 offset:4608\n"
"ds_read_b128 v[172:175], v224 offset:3520\n"
"v_fma_mix_f32 v178, v97, v146, v154 op_sel:[0,0,0] op_sel_hi:[1,0,0]\n"
"v_mfma_f32_16x16x32_bf16 v[192:195], a[252:255], v[76:79], v[192:195]\n"
"s_waitcnt lgkmcnt(14)\n"
"ds_read_b64_tr_b16 a[230:231], v218 offset:5632\n"
"buffer_load_dwordx4 v[128:131], v249, s[28:31], s2 offen offset:0 sc1\n"
"v_mfma_f32_16x16x32_bf16 a[0:3], a[160:163], v[80:83], a[0:3]\n"
"ds_read_b64_tr_b16 a[232:233], v218 offset:520\n"
"v_fma_mix_f32 v179, v97, v147, v155 op_sel:[1,0,0] op_sel_hi:[1,0,0]\n"
"v_mfma_f32_16x16x32_bf16 a[0:3], a[164:167], v[88:91], a[0:3]\n"
"s_waitcnt vmcnt(14)\n"
"ds_read_b64_tr_b16 a[234:235], v218 offset:1544\n"
"v_fma_mix_f32 v180, v98, v148, v156 op_sel:[0,0,0] op_sel_hi:[1,0,0]\n"
"v_mfma_f32_16x16x32_bf16 a[4:7], a[160:163], v[84:87], a[4:7]\n"
"s_barrier\n"
"ds_read_b64_tr_b16 a[236:237], v218 offset:4616\n"
"v_fma_mix_f32 v181, v98, v149, v157 op_sel:[1,0,0] op_sel_hi:[1,0,0]\n"
"v_mfma_f32_16x16x32_bf16 a[4:7], a[164:167], v[92:95], a[4:7]\n"
"ds_read_b64_tr_b16 a[238:239], v218 offset:5640\n"
"v_fma_mix_f32 v182, v99, v150, v158 op_sel:[0,0,0] op_sel_hi:[1,0,0]\n"
"v_mfma_f32_16x16x32_bf16 a[8:11], a[168:171], v[80:83], a[8:11]\n"
"s_add_u32 m0, s51, 98352\n"
"ds_read_b64_tr_b16 a[240:241], v218 offset:768\n"
"v_fma_mix_f32 v183, v99, v151, v159 op_sel:[1,0,0] op_sel_hi:[1,0,0]\n"
"v_mfma_f32_16x16x32_bf16 a[8:11], a[172:175], v[88:91], a[8:11]\n"
"ds_read_b64_tr_b16 a[242:243], v218 offset:1792\n"
"buffer_load_dwordx4 v217, s[20:23], s50 offen offset:0 lds\n"
"v_mfma_f32_16x16x32_bf16 a[12:15], a[168:171], v[84:87], a[12:15]\n"
"ds_read_b64_tr_b16 a[244:245], v218 offset:4864\n"
"v_cvt_pk_bf16_f32 v96, v176, v177\n"
"v_mfma_f32_16x16x32_bf16 a[12:15], a[172:175], v[92:95], a[12:15]\n"
"s_waitcnt lgkmcnt(12)\n"
"ds_read_b64_tr_b16 a[246:247], v218 offset:5888\n"
"v_cvt_pk_bf16_f32 v97, v178, v179\n"
"v_mfma_f32_16x16x32_bf16 a[16:19], a[176:179], v[80:83], a[16:19]\n"
"ds_read_b64_tr_b16 a[248:249], v218 offset:776\n"
"v_cvt_pk_bf16_f32 v98, v180, v181\n"
"v_mfma_f32_16x16x32_bf16 a[16:19], a[180:183], v[88:91], a[16:19]\n"
"ds_read_b64_tr_b16 a[250:251], v218 offset:1800\n"
"v_cvt_pk_bf16_f32 v99, v182, v183\n"
"v_mfma_f32_16x16x32_bf16 a[20:23], a[176:179], v[84:87], a[20:23]\n"
"s_add_u32 m0, s51, 106288\n"
"ds_read_b64_tr_b16 a[252:253], v218 offset:4872\n"
"v_fma_mix_f32 v184, v100, v144, v152 op_sel:[0,0,0] op_sel_hi:[1,0,0]\n"
"v_mfma_f32_16x16x32_bf16 a[20:23], a[180:183], v[92:95], a[20:23]\n"
"ds_read_b64_tr_b16 a[254:255], v218 offset:5896\n"
"buffer_load_dwordx4 v217, s[20:23], s50 offen offset:256 lds\n"
"v_fma_mix_f32 v185, v100, v145, v153 op_sel:[1,0,0] op_sel_hi:[1,0,0]\n"
"v_mfma_f32_16x16x32_bf16 a[24:27], a[184:187], v[80:83], a[24:27]\n"
"s_add_u32 s50, s50, 32768\n"
"ds_read_b64_tr_b16 a[160:161], v218 offset:8192\n"
"v_fma_mix_f32 v186, v101, v146, v154 op_sel:[0,0,0] op_sel_hi:[1,0,0]\n"
"v_mfma_f32_16x16x32_bf16 a[24:27], a[188:191], v[88:91], a[24:27]\n"
"ds_read_b64_tr_b16 a[162:163], v218 offset:9216\n"
"buffer_load_dwordx4 v[132:135], v255, s[28:31], s2 offen offset:0 sc1\n"
"v_mfma_f32_16x16x32_bf16 a[28:31], a[184:187], v[84:87], a[28:31]\n"
"ds_read_b64_tr_b16 a[164:165], v218 offset:12288\n"
"v_fma_mix_f32 v187, v101, v147, v155 op_sel:[1,0,0] op_sel_hi:[1,0,0]\n"
"v_mfma_f32_16x16x32_bf16 a[28:31], a[188:191], v[92:95], a[28:31]\n"
"s_waitcnt lgkmcnt(11)\n"
"ds_read_b64_tr_b16 a[166:167], v218 offset:13312\n"
"v_fma_mix_f32 v188, v102, v148, v156 op_sel:[0,0,0] op_sel_hi:[1,0,0]\n"
"v_mfma_f32_16x16x32_bf16 a[32:35], a[224:227], v[80:83], a[32:35]\n"
"s_add_u32 m0, s51, 99376\n"
"ds_read_b64_tr_b16 a[168:169], v218 offset:8200\n"
"v_fma_mix_f32 v189, v102, v149, v157 op_sel:[1,0,0] op_sel_hi:[1,0,0]\n"
"v_mfma_f32_16x16x32_bf16 a[32:35], a[228:231], v[88:91], a[32:35]\n"
"ds_read_b64_tr_b16 a[170:171], v218 offset:9224\n"
"buffer_load_dwordx4 v217, s[20:23], s50 offen offset:0 lds\n"
"v_mfma_f32_16x16x32_bf16 a[36:39], a[224:227], v[84:87], a[36:39]\n"
"ds_read_b64_tr_b16 a[172:173], v218 offset:12296\n"
"v_fma_mix_f32 v190, v103, v150, v158 op_sel:[0,0,0] op_sel_hi:[1,0,0]\n"
"v_mfma_f32_16x16x32_bf16 a[36:39], a[228:231], v[92:95], a[36:39]\n"
"ds_read_b64_tr_b16 a[174:175], v218 offset:13320\n"
"v_fma_mix_f32 v191, v103, v151, v159 op_sel:[1,0,0] op_sel_hi:[1,0,0]\n"
"v_mfma_f32_16x16x32_bf16 a[40:43], a[232:235], v[80:83], a[40:43]\n"
"ds_read_b64_tr_b16 a[176:177], v218 offset:8448\n"
"ds_read_b128 v[144:147], v224 offset:3584\n"
"v_mfma_f32_16x16x32_bf16 a[40:43], a[236:239], v[88:91], a[40:43]\n"
"ds_read_b64_tr_b16 a[178:179], v218 offset:9472\n"
"v_cvt_pk_bf16_f32 v100, v184, v185\n"
"v_mfma_f32_16x16x32_bf16 a[44:47], a[232:235], v[84:87], a[44:47]\n"
"s_add_u32 m0, s51, 107312\n"
"ds_read_b64_tr_b16 a[180:181], v218 offset:12544\n"
"v_cvt_pk_bf16_f32 v101, v186, v187\n"
"v_mfma_f32_16x16x32_bf16 a[44:47], a[236:239], v[92:95], a[44:47]\n"
"s_waitcnt lgkmcnt(12)\n"
"ds_read_b64_tr_b16 a[182:183], v218 offset:13568\n"
"buffer_load_dwordx4 v217, s[20:23], s50 offen offset:256 lds\n"
"v_mfma_f32_16x16x32_bf16 a[48:51], a[240:243], v[80:83], a[48:51]\n"
"s_add_u32 s50, s50, 32768\n"
"ds_read_b64_tr_b16 a[184:185], v218 offset:8456\n"
"ds_read_b128 v[148:151], v224 offset:3648\n"
"v_cvt_pk_bf16_f32 v102, v188, v189\n"
"v_mfma_f32_16x16x32_bf16 a[48:51], a[244:247], v[88:91], a[48:51]\n"
"ds_read_b64_tr_b16 a[186:187], v218 offset:9480\n"
"v_cvt_pk_bf16_f32 v103, v190, v191\n"
"v_mfma_f32_16x16x32_bf16 a[52:55], a[240:243], v[84:87], a[52:55]\n"
"ds_read_b64_tr_b16 a[188:189], v218 offset:12552\n"
"buffer_load_dwordx4 v[136:139], v249, s[28:31], s2 offen offset:1024 sc1\n"
"v_mfma_f32_16x16x32_bf16 a[52:55], a[244:247], v[92:95], a[52:55]\n"
"ds_read_b64_tr_b16 a[190:191], v218 offset:13576\n"
"ds_read_b128 v[152:155], v224 offset:3712\n"
"v_mfma_f32_16x16x32_bf16 a[56:59], a[248:251], v[80:83], a[56:59]\n"
"s_add_u32 m0, s51, 114744\n"
"ds_read_b64_tr_b16 a[224:225], v218 offset:8704\n"
"v_fma_mix_f32 v176, v104, v160, v168 op_sel:[0,0,0] op_sel_hi:[1,0,0]\n"
"v_mfma_f32_16x16x32_bf16 a[56:59], a[252:255], v[88:91], a[56:59]\n"
"ds_read_b64_tr_b16 a[226:227], v218 offset:9728\n"
"buffer_load_dwordx4 v217, s[20:23], s50 offen offset:0 lds\n"
"v_mfma_f32_16x16x32_bf16 a[60:63], a[248:251], v[84:87], a[60:63]\n"
"ds_read_b64_tr_b16 a[228:229], v218 offset:12800\n"
"ds_read_b128 v[156:159], v224 offset:3776\n"
"v_mfma_f32_16x16x32_bf16 a[60:63], a[252:255], v[92:95], a[60:63]\n"
"s_waitcnt lgkmcnt(14)\n"
"ds_read_b64_tr_b16 a[230:231], v218 offset:13824\n"
"v_fma_mix_f32 v177, v104, v161, v169 op_sel:[1,0,0] op_sel_hi:[1,0,0]\n"
"v_mfma_f32_16x16x32_bf16 a[64:67], a[160:163], v[80:83], a[64:67]\n"
"ds_read_b64_tr_b16 a[232:233], v218 offset:8712\n"
"v_fma_mix_f32 v178, v105, v162, v170 op_sel:[0,0,0] op_sel_hi:[1,0,0]\n"
"v_mfma_f32_16x16x32_bf16 a[64:67], a[164:167], v[88:91], a[64:67]\n"
"ds_read_b64_tr_b16 a[234:235], v218 offset:9736\n"
"v_fma_mix_f32 v179, v105, v163, v171 op_sel:[1,0,0] op_sel_hi:[1,0,0]\n"
"v_mfma_f32_16x16x32_bf16 a[68:71], a[160:163], v[84:87], a[68:71]\n"
"s_add_u32 m0, s51, 122680\n"
"ds_read_b64_tr_b16 a[236:237], v218 offset:12808\n"
"v_fma_mix_f32 v180, v106, v164, v172 op_sel:[0,0,0] op_sel_hi:[1,0,0]\n"
"v_mfma_f32_16x16x32_bf16 a[68:71], a[164:167], v[92:95], a[68:71]\n"
"ds_read_b64_tr_b16 a[238:239], v218 offset:13832\n"
"buffer_load_dwordx4 v217, s[20:23], s50 offen offset:256 lds\n"
"v_mfma_f32_16x16x32_bf16 a[72:75], a[168:171], v[80:83], a[72:75]\n"
"s_add_u32 s50, s50, 32768\n"
"ds_read_b64_tr_b16 a[240:241], v218 offset:8960\n"
"v_fma_mix_f32 v181, v106, v165, v173 op_sel:[1,0,0] op_sel_hi:[1,0,0]\n"
"v_mfma_f32_16x16x32_bf16 a[72:75], a[172:175], v[88:91], a[72:75]\n"
"ds_read_b64_tr_b16 a[242:243], v218 offset:9984\n"
"v_fma_mix_f32 v182, v107, v166, v174 op_sel:[0,0,0] op_sel_hi:[1,0,0]\n"
"v_fma_mix_f32 v183, v107, v167, v175 op_sel:[1,0,0] op_sel_hi:[1,0,0]\n"
"v_mfma_f32_16x16x32_bf16 a[76:79], a[168:171], v[84:87], a[76:79]\n"
"ds_read_b64_tr_b16 a[244:245], v218 offset:13056\n"
"v_cvt_pk_bf16_f32 v104, v176, v177\n"
"v_mfma_f32_16x16x32_bf16 a[76:79], a[172:175], v[92:95], a[76:79]\n"
"s_waitcnt lgkmcnt(12)\n"
"ds_read_b64_tr_b16 a[246:247], v218 offset:14080\n"
"v_cvt_pk_bf16_f32 v105, v178, v179\n"
"v_mfma_f32_16x16x32_bf16 a[80:83], a[176:179], v[80:83], a[80:83]\n"
"s_add_u32 m0, s51, 115768\n"
"ds_read_b64_tr_b16 a[248:249], v218 offset:8968\n"
"v_cvt_pk_bf16_f32 v106, v180, v181\n"
"v_mfma_f32_16x16x32_bf16 a[80:83], a[180:183], v[88:91], a[80:83]\n"
"ds_read_b64_tr_b16 a[250:251], v218 offset:9992\n"
"buffer_load_dwordx4 v217, s[20:23], s50 offen offset:0 lds\n"
"v_mfma_f32_16x16x32_bf16 a[84:87], a[176:179], v[84:87], a[84:87]\n"
"ds_read_b64_tr_b16 a[252:253], v218 offset:13064\n"
"v_cvt_pk_bf16_f32 v107, v182, v183\n"
"v_mfma_f32_16x16x32_bf16 a[84:87], a[180:183], v[92:95], a[84:87]\n"
"ds_read_b64_tr_b16 a[254:255], v218 offset:14088\n"
"buffer_load_dwordx4 v[140:143], v255, s[28:31], s2 offen offset:1024 sc1\n"
"v_mfma_f32_16x16x32_bf16 v[236:239], a[184:187], v[80:83], v[236:239]\n"
"ds_read_b64_tr_b16 a[160:161], v218 offset:32784\n"
"v_fma_mix_f32 v184, v108, v160, v168 op_sel:[0,0,0] op_sel_hi:[1,0,0]\n"
"v_mfma_f32_16x16x32_bf16 v[236:239], a[188:191], v[88:91], v[236:239]\n"
"ds_read_b64_tr_b16 a[162:163], v218 offset:33808\n"
"v_fma_mix_f32 v185, v108, v161, v169 op_sel:[1,0,0] op_sel_hi:[1,0,0]\n"
"v_mfma_f32_16x16x32_bf16 v[232:235], a[184:187], v[84:87], v[232:235]\n"
"s_add_u32 m0, s51, 123704\n"
"ds_read_b64_tr_b16 a[164:165], v218 offset:36880\n"
"v_fma_mix_f32 v186, v109, v162, v170 op_sel:[0,0,0] op_sel_hi:[1,0,0]\n"
"v_mfma_f32_16x16x32_bf16 v[232:235], a[188:191], v[92:95], v[232:235]\n"
"s_waitcnt lgkmcnt(11)\n"
"buffer_load_dwordx4 v217, s[20:23], s50 offen offset:256 lds\n"
"ds_read_b64_tr_b16 a[166:167], v218 offset:37904\n"
"v_mfma_f32_16x16x32_bf16 v[228:231], a[224:227], v[80:83], v[228:231]\n"
"s_add_u32 s50, s50, 425984\n"
"ds_read_b64_tr_b16 a[168:169], v218 offset:32792\n"
"v_fma_mix_f32 v187, v109, v163, v171 op_sel:[1,0,0] op_sel_hi:[1,0,0]\n"
"v_mfma_f32_16x16x32_bf16 v[228:231], a[228:231], v[88:91], v[228:231]\n"
"ds_read_b64_tr_b16 a[170:171], v218 offset:33816\n"
"v_fma_mix_f32 v188, v110, v164, v172 op_sel:[0,0,0] op_sel_hi:[1,0,0]\n"
"v_mfma_f32_16x16x32_bf16 v[220:223], a[224:227], v[84:87], v[220:223]\n"
"ds_read_b64_tr_b16 a[172:173], v218 offset:36888\n"
"v_fma_mix_f32 v189, v110, v165, v173 op_sel:[1,0,0] op_sel_hi:[1,0,0]\n"
"v_fma_mix_f32 v190, v111, v166, v174 op_sel:[0,0,0] op_sel_hi:[1,0,0]\n"
"v_mfma_f32_16x16x32_bf16 v[220:223], a[228:231], v[92:95], v[220:223]\n"
"ds_read_b64_tr_b16 a[174:175], v218 offset:37912\n"
"v_fma_mix_f32 v191, v111, v167, v175 op_sel:[1,0,0] op_sel_hi:[1,0,0]\n"
"v_mfma_f32_16x16x32_bf16 v[212:215], a[232:235], v[80:83], v[212:215]\n"
"ds_read_b64_tr_b16 a[176:177], v218 offset:33040\n"
"ds_read_b128 v[160:163], v224 offset:3840\n"
"v_mfma_f32_16x16x32_bf16 v[212:215], a[236:239], v[88:91], v[212:215]\n"
"ds_read_b64_tr_b16 a[178:179], v218 offset:34064\n"
"v_cvt_pk_bf16_f32 v108, v184, v185\n"
"v_mfma_f32_16x16x32_bf16 v[208:211], a[232:235], v[84:87], v[208:211]\n"
"ds_read_b64_tr_b16 a[180:181], v218 offset:37136\n"
"v_cvt_pk_bf16_f32 v109, v186, v187\n"
"v_mfma_f32_16x16x32_bf16 v[208:211], a[236:239], v[92:95], v[208:211]\n"
"s_waitcnt lgkmcnt(12)\n"
"ds_read_b64_tr_b16 a[182:183], v218 offset:38160\n"
"ds_read_b128 v[164:167], v224 offset:3904\n"
"v_mfma_f32_16x16x32_bf16 v[204:207], a[240:243], v[80:83], v[204:207]\n"
"ds_read_b64_tr_b16 a[184:185], v218 offset:33048\n"
"v_cvt_pk_bf16_f32 v110, v188, v189\n"
"v_mfma_f32_16x16x32_bf16 v[204:207], a[244:247], v[88:91], v[204:207]\n"
"ds_read_b64_tr_b16 a[186:187], v218 offset:34072\n"
"v_cvt_pk_bf16_f32 v111, v190, v191\n"
"v_mfma_f32_16x16x32_bf16 v[200:203], a[240:243], v[84:87], v[200:203]\n"
"ds_read_b64_tr_b16 a[188:189], v218 offset:37144\n"
"ds_read_b128 v[168:171], v224 offset:3968\n"
"v_mfma_f32_16x16x32_bf16 v[200:203], a[244:247], v[92:95], v[200:203]\n"
"s_waitcnt vmcnt(12)\n"
"ds_read_b64_tr_b16 a[190:191], v218 offset:38168\n"
"v_fma_mix_f32 v176, v112, v144, v152 op_sel:[0,0,0] op_sel_hi:[1,0,0]\n"
"v_mfma_f32_16x16x32_bf16 v[196:199], a[248:251], v[80:83], v[196:199]\n"
"ds_read_b64_tr_b16 a[224:225], v218 offset:33296\n"
"v_fma_mix_f32 v177, v112, v145, v153 op_sel:[1,0,0] op_sel_hi:[1,0,0]\n"
"v_mfma_f32_16x16x32_bf16 v[196:199], a[252:255], v[88:91], v[196:199]\n"
"ds_read_b64_tr_b16 a[226:227], v218 offset:34320\n"
"ds_read_b128 v[172:175], v224 offset:4032\n"
"v_mfma_f32_16x16x32_bf16 v[192:195], a[248:251], v[84:87], v[192:195]\n"
"ds_read_b64_tr_b16 a[228:229], v218 offset:37392\n"
"v_fma_mix_f32 v178, v113, v146, v154 op_sel:[0,0,0] op_sel_hi:[1,0,0]\n"
"v_mfma_f32_16x16x32_bf16 v[192:195], a[252:255], v[92:95], v[192:195]\n"
"s_waitcnt lgkmcnt(14)\n"
"ds_read_b64_tr_b16 a[230:231], v218 offset:38416\n"
"v_fma_mix_f32 v179, v113, v147, v155 op_sel:[1,0,0] op_sel_hi:[1,0,0]\n"
"v_fma_mix_f32 v180, v114, v148, v156 op_sel:[0,0,0] op_sel_hi:[1,0,0]\n"
"v_mfma_f32_16x16x32_bf16 a[0:3], a[160:163], v[96:99], a[0:3]\n"
"ds_read_b64_tr_b16 a[232:233], v218 offset:33304\n"
"v_fma_mix_f32 v181, v114, v149, v157 op_sel:[1,0,0] op_sel_hi:[1,0,0]\n"
"v_mfma_f32_16x16x32_bf16 a[0:3], a[164:167], v[104:107], a[0:3]\n"
"s_waitcnt vmcnt(13)\n"
"ds_read_b64_tr_b16 a[234:235], v218 offset:34328\n"
"v_fma_mix_f32 v182, v115, v150, v158 op_sel:[0,0,0] op_sel_hi:[1,0,0]\n"
"v_mfma_f32_16x16x32_bf16 a[4:7], a[160:163], v[100:103], a[4:7]\n"
"s_barrier\n"
"ds_read_b64_tr_b16 a[236:237], v218 offset:37400\n"
"v_fma_mix_f32 v183, v115, v151, v159 op_sel:[1,0,0] op_sel_hi:[1,0,0]\n"
"v_mfma_f32_16x16x32_bf16 a[4:7], a[164:167], v[108:111], a[4:7]\n"
"ds_read_b64_tr_b16 a[238:239], v218 offset:38424\n"
"v_cvt_pk_bf16_f32 v112, v176, v177\n"
"v_mfma_f32_16x16x32_bf16 a[8:11], a[168:171], v[96:99], a[8:11]\n"
"s_mov_b32 m0, s86\n"
"ds_read_b64_tr_b16 a[240:241], v218 offset:33552\n"
"v_cvt_pk_bf16_f32 v113, v178, v179\n"
"v_mfma_f32_16x16x32_bf16 a[8:11], a[172:175], v[104:107], a[8:11]\n"
"ds_read_b64_tr_b16 a[242:243], v218 offset:34576\n"
"buffer_load_dwordx4 v246, s[8:11], s84 offen offset:0 lds\n"
"v_mfma_f32_16x16x32_bf16 a[12:15], a[168:171], v[100:103], a[12:15]\n"
"ds_read_b64_tr_b16 a[244:245], v218 offset:37648\n"
"v_cvt_pk_bf16_f32 v114, v180, v181\n"
"v_mfma_f32_16x16x32_bf16 a[12:15], a[172:175], v[108:111], a[12:15]\n"
"s_waitcnt lgkmcnt(12)\n"
"ds_read_b64_tr_b16 a[246:247], v218 offset:38672\n"
"v_cvt_pk_bf16_f32 v115, v182, v183\n"
"v_mfma_f32_16x16x32_bf16 a[16:19], a[176:179], v[96:99], a[16:19]\n"
"ds_read_b64_tr_b16 a[248:249], v218 offset:33560\n"
"v_fma_mix_f32 v184, v116, v144, v152 op_sel:[0,0,0] op_sel_hi:[1,0,0]\n"
"v_mfma_f32_16x16x32_bf16 a[16:19], a[180:183], v[104:107], a[16:19]\n"
"ds_read_b64_tr_b16 a[250:251], v218 offset:34584\n"
"v_fma_mix_f32 v185, v116, v145, v153 op_sel:[1,0,0] op_sel_hi:[1,0,0]\n"
"v_mfma_f32_16x16x32_bf16 a[20:23], a[176:179], v[100:103], a[20:23]\n"
"ds_read_b64_tr_b16 a[252:253], v218 offset:37656\n"
"v_fma_mix_f32 v186, v117, v146, v154 op_sel:[0,0,0] op_sel_hi:[1,0,0]\n"
"v_mfma_f32_16x16x32_bf16 a[20:23], a[180:183], v[108:111], a[20:23]\n"
"ds_read_b64_tr_b16 a[254:255], v218 offset:38680\n"
"buffer_load_dwordx4 v246, s[8:11], s84 offen offset:1024 lds\n"
"v_mfma_f32_16x16x32_bf16 a[24:27], a[184:187], v[96:99], a[24:27]\n"
"ds_read_b64_tr_b16 a[160:161], v218 offset:40976\n"
"v_fma_mix_f32 v187, v117, v147, v155 op_sel:[1,0,0] op_sel_hi:[1,0,0]\n"
"v_fma_mix_f32 v188, v118, v148, v156 op_sel:[0,0,0] op_sel_hi:[1,0,0]\n"
"v_mfma_f32_16x16x32_bf16 a[24:27], a[188:191], v[104:107], a[24:27]\n"
"ds_read_b64_tr_b16 a[162:163], v218 offset:42000\n"
"v_fma_mix_f32 v189, v118, v149, v157 op_sel:[1,0,0] op_sel_hi:[1,0,0]\n"
"v_mfma_f32_16x16x32_bf16 a[28:31], a[184:187], v[100:103], a[28:31]\n"
"ds_read_b64_tr_b16 a[164:165], v218 offset:45072\n"
"v_fma_mix_f32 v190, v119, v150, v158 op_sel:[0,0,0] op_sel_hi:[1,0,0]\n"
"v_mfma_f32_16x16x32_bf16 a[28:31], a[188:191], v[108:111], a[28:31]\n"
"s_waitcnt lgkmcnt(11)\n"
"ds_read_b64_tr_b16 a[166:167], v218 offset:46096\n"
"v_fma_mix_f32 v191, v119, v151, v159 op_sel:[1,0,0] op_sel_hi:[1,0,0]\n"
"v_mfma_f32_16x16x32_bf16 a[32:35], a[224:227], v[96:99], a[32:35]\n"
"ds_read_b64_tr_b16 a[168:169], v218 offset:40984\n"
"ds_read_b128 v[144:147], v224 offset:4096\n"
"v_mfma_f32_16x16x32_bf16 a[32:35], a[228:231], v[104:107], a[32:35]\n"
"ds_read_b64_tr_b16 a[170:171], v218 offset:42008\n"
"buffer_load_dwordx4 v247, s[8:11], s84 offen offset:2048 lds\n"
"v_mfma_f32_16x16x32_bf16 a[36:39], a[224:227], v[100:103], a[36:39]\n"
"ds_read_b64_tr_b16 a[172:173], v218 offset:45080\n"
"v_cvt_pk_bf16_f32 v116, v184, v185\n"
"v_mfma_f32_16x16x32_bf16 a[36:39], a[228:231], v[108:111], a[36:39]\n"
"ds_read_b64_tr_b16 a[174:175], v218 offset:46104\n"
"ds_read_b128 v[148:151], v224 offset:4160\n"
"v_mfma_f32_16x16x32_bf16 a[40:43], a[232:235], v[96:99], a[40:43]\n"
"ds_read_b64_tr_b16 a[176:177], v218 offset:41232\n"
"v_cvt_pk_bf16_f32 v117, v186, v187\n"
"v_mfma_f32_16x16x32_bf16 a[40:43], a[236:239], v[104:107], a[40:43]\n"
"ds_read_b64_tr_b16 a[178:179], v218 offset:42256\n"
"v_cvt_pk_bf16_f32 v118, v188, v189\n"
"v_mfma_f32_16x16x32_bf16 a[44:47], a[232:235], v[100:103], a[44:47]\n"
"ds_read_b64_tr_b16 a[180:181], v218 offset:45328\n"
"ds_read_b128 v[152:155], v224 offset:4224\n"
"v_mfma_f32_16x16x32_bf16 a[44:47], a[236:239], v[108:111], a[44:47]\n"
"s_waitcnt lgkmcnt(14)\n"
"ds_read_b64_tr_b16 a[182:183], v218 offset:46352\n"
"buffer_load_dwordx4 v247, s[8:11], s84 offen offset:3072 lds\n"
"v_mfma_f32_16x16x32_bf16 a[48:51], a[240:243], v[96:99], a[48:51]\n"
"ds_read_b64_tr_b16 a[184:185], v218 offset:41240\n"
"v_cvt_pk_bf16_f32 v119, v190, v191\n"
"v_mfma_f32_16x16x32_bf16 a[48:51], a[244:247], v[104:107], a[48:51]\n"
"ds_read_b64_tr_b16 a[186:187], v218 offset:42264\n"
"ds_read_b128 v[156:159], v224 offset:4288\n"
"v_fma_mix_f32 v176, v120, v160, v168 op_sel:[0,0,0] op_sel_hi:[1,0,0]\n"
"v_mfma_f32_16x16x32_bf16 a[52:55], a[240:243], v[100:103], a[52:55]\n"
"ds_read_b64_tr_b16 a[188:189], v218 offset:45336\n"
"v_fma_mix_f32 v177, v120, v161, v169 op_sel:[1,0,0] op_sel_hi:[1,0,0]\n"
"v_mfma_f32_16x16x32_bf16 a[52:55], a[244:247], v[108:111], a[52:55]\n"
"ds_read_b64_tr_b16 a[190:191], v218 offset:46360\n"
"v_fma_mix_f32 v178, v121, v162, v170 op_sel:[0,0,0] op_sel_hi:[1,0,0]\n"
"v_mfma_f32_16x16x32_bf16 a[56:59], a[248:251], v[96:99], a[56:59]\n"
"s_add_u32 m0, s86, 16384\n"
"ds_read_b64_tr_b16 a[224:225], v218 offset:41488\n"
"v_fma_mix_f32 v179, v121, v163, v171 op_sel:[1,0,0] op_sel_hi:[1,0,0]\n"
"v_mfma_f32_16x16x32_bf16 a[56:59], a[252:255], v[104:107], a[56:59]\n"
"ds_read_b64_tr_b16 a[226:227], v218 offset:42512\n"
"buffer_load_dwordx4 v246, s[12:15], s84 offen offset:0 lds\n"
"v_mfma_f32_16x16x32_bf16 a[60:63], a[248:251], v[100:103], a[60:63]\n"
"ds_read_b64_tr_b16 a[228:229], v218 offset:45584\n"
"v_fma_mix_f32 v180, v122, v164, v172 op_sel:[0,0,0] op_sel_hi:[1,0,0]\n"
"v_mfma_f32_16x16x32_bf16 a[60:63], a[252:255], v[108:111], a[60:63]\n"
"s_waitcnt lgkmcnt(13)\n"
"ds_read_b64_tr_b16 a[230:231], v218 offset:46608\n"
"v_fma_mix_f32 v181, v122, v165, v173 op_sel:[1,0,0] op_sel_hi:[1,0,0]\n"
"v_mfma_f32_16x16x32_bf16 a[64:67], a[160:163], v[96:99], a[64:67]\n"
"ds_read_b64_tr_b16 a[232:233], v218 offset:41496\n"
"v_fma_mix_f32 v182, v123, v166, v174 op_sel:[0,0,0] op_sel_hi:[1,0,0]\n"
"v_mfma_f32_16x16x32_bf16 a[64:67], a[164:167], v[104:107], a[64:67]\n"
"ds_read_b64_tr_b16 a[234:235], v218 offset:42520\n"
"v_fma_mix_f32 v183, v123, v167, v175 op_sel:[1,0,0] op_sel_hi:[1,0,0]\n"
"v_mfma_f32_16x16x32_bf16 a[68:71], a[160:163], v[100:103], a[68:71]\n"
"ds_read_b64_tr_b16 a[236:237], v218 offset:45592\n"
"v_cvt_pk_bf16_f32 v120, v176, v177\n"
"v_mfma_f32_16x16x32_bf16 a[68:71], a[164:167], v[108:111], a[68:71]\n"
"ds_read_b64_tr_b16 a[238:239], v218 offset:46616\n"
"buffer_load_dwordx4 v247, s[12:15], s84 offen offset:1024 lds\n"
"v_mfma_f32_16x16x32_bf16 a[72:75], a[168:171], v[96:99], a[72:75]\n"
"ds_read_b64_tr_b16 a[240:241], v218 offset:41744\n"
"v_cvt_pk_bf16_f32 v121, v178, v179\n"
"v_mfma_f32_16x16x32_bf16 a[72:75], a[172:175], v[104:107], a[72:75]\n"
"ds_read_b64_tr_b16 a[242:243], v218 offset:42768\n"
"v_cvt_pk_bf16_f32 v122, v180, v181\n"
"v_mfma_f32_16x16x32_bf16 a[76:79], a[168:171], v[100:103], a[76:79]\n"
"ds_read_b64_tr_b16 a[244:245], v218 offset:45840\n"
"v_cvt_pk_bf16_f32 v123, v182, v183\n"
"v_fma_mix_f32 v184, v124, v160, v168 op_sel:[0,0,0] op_sel_hi:[1,0,0]\n"
"v_mfma_f32_16x16x32_bf16 a[76:79], a[172:175], v[108:111], a[76:79]\n"
"s_waitcnt lgkmcnt(11)\n"
"ds_read_b64_tr_b16 a[246:247], v218 offset:46864\n"
"v_fma_mix_f32 v185, v124, v161, v169 op_sel:[1,0,0] op_sel_hi:[1,0,0]\n"
"v_mfma_f32_16x16x32_bf16 a[80:83], a[176:179], v[96:99], a[80:83]\n"
"ds_read_b64_tr_b16 a[248:249], v218 offset:41752\n"
"v_fma_mix_f32 v186, v125, v162, v170 op_sel:[0,0,0] op_sel_hi:[1,0,0]\n"
"v_mfma_f32_16x16x32_bf16 a[80:83], a[180:183], v[104:107], a[80:83]\n"
"ds_read_b64_tr_b16 a[250:251], v218 offset:42776\n"
"buffer_load_dwordx4 v246, s[12:15], s84 offen offset:2048 lds\n"
"v_mfma_f32_16x16x32_bf16 a[84:87], a[176:179], v[100:103], a[84:87]\n"
"AT_pg4:\n"
"s_cbranch_execz AT_hr\n"
"ds_read_b64_tr_b16 a[252:253], v218 offset:45848\n"
"v_fma_mix_f32 v187, v125, v163, v171 op_sel:[1,0,0] op_sel_hi:[1,0,0]\n"
"v_mfma_f32_16x16x32_bf16 a[84:87], a[180:183], v[108:111], a[84:87]\n"
"ds_read_b64_tr_b16 a[254:255], v218 offset:46872\n"
"v_fma_mix_f32 v188, v126, v164, v172 op_sel:[0,0,0] op_sel_hi:[1,0,0]\n"
"v_mfma_f32_16x16x32_bf16 v[236:239], a[184:187], v[96:99], v[236:239]\n"
"ds_read_b64_tr_b16 a[160:161], v219 offset:0\n"
"v_fma_mix_f32 v189, v126, v165, v173 op_sel:[1,0,0] op_sel_hi:[1,0,0]\n"
"v_mfma_f32_16x16x32_bf16 v[236:239], a[188:191], v[104:107], v[236:239]\n"
"ds_read_b64_tr_b16 a[162:163], v219 offset:1024\n"
"v_fma_mix_f32 v190, v127, v166, v174 op_sel:[0,0,0] op_sel_hi:[1,0,0]\n"
"v_mfma_f32_16x16x32_bf16 v[232:235], a[184:187], v[100:103], v[232:235]\n"
"ds_read_b64_tr_b16 a[164:165], v219 offset:4096\n"
"v_fma_mix_f32 v191, v127, v167, v175 op_sel:[1,0,0] op_sel_hi:[1,0,0]\n"
"v_mfma_f32_16x16x32_bf16 v[232:235], a[188:191], v[108:111], v[232:235]\n"
"s_waitcnt lgkmcnt(11)\n"
"buffer_load_dwordx4 v247, s[12:15], s84 offen offset:3072 lds\n"
"ds_read_b64_tr_b16 a[166:167], v219 offset:5120\n"
"v_mfma_f32_16x16x32_bf16 v[228:231], a[224:227], v[96:99], v[228:231]\n"
"s_add_u32 s84, s84, 16384\n"
"ds_read_b64_tr_b16 a[168:169], v219 offset:8\n"
"ds_read_b128 v[160:163], v224 offset:4352\n"
"v_mfma_f32_16x16x32_bf16 v[228:231], a[228:231], v[104:107], v[228:231]\n"
"ds_read_b64_tr_b16 a[170:171], v219 offset:1032\n"
"v_cvt_pk_bf16_f32 v124, v184, v185\n"
"v_mfma_f32_16x16x32_bf16 v[220:223], a[224:227], v[100:103], v[220:223]\n"
"ds_read_b64_tr_b16 a[172:173], v219 offset:4104\n"
"v_cvt_pk_bf16_f32 v125, v186, v187\n"
"v_mfma_f32_16x16x32_bf16 v[220:223], a[228:231], v[108:111], v[220:223]\n"
"ds_read_b64_tr_b16 a[174:175], v219 offset:5128\n"
"ds_read_b128 v[164:167], v224 offset:4416\n"
"v_cvt_pk_bf16_f32 v126, v188, v189\n"
"v_mfma_f32_16x16x32_bf16 v[212:215], a[232:235], v[96:99], v[212:215]\n"
"ds_read_b64_tr_b16 a[176:177], v219 offset:256\n"
"v_cvt_pk_bf16_f32 v127, v190, v191\n"
"v_mfma_f32_16x16x32_bf16 v[212:215], a[236:239], v[104:107], v[212:215]\n"
"s_waitcnt vmcnt(9)\n"
"ds_read_b64_tr_b16 a[178:179], v219 offset:1280\n"
"v_fma_mix_f32 v176, v128, v144, v152 op_sel:[0,0,0] op_sel_hi:[1,0,0]\n"
"v_mfma_f32_16x16x32_bf16 v[208:211], a[232:235], v[100:103], v[208:211]\n"
"ds_read_b64_tr_b16 a[180:181], v219 offset:4352\n"
"ds_read_b128 v[168:171], v224 offset:4480\n"
"v_mfma_f32_16x16x32_bf16 v[208:211], a[236:239], v[108:111], v[208:211]\n"
"s_waitcnt lgkmcnt(14)\n"
"ds_read_b64_tr_b16 a[182:183], v219 offset:5376\n"
"v_fma_mix_f32 v177, v128, v145, v153 op_sel:[1,0,0] op_sel_hi:[1,0,0]\n"
"v_mfma_f32_16x16x32_bf16 v[204:207], a[240:243], v[96:99], v[204:207]\n"
"ds_read_b64_tr_b16 a[184:185], v219 offset:264\n"
"v_fma_mix_f32 v178, v129, v146, v154 op_sel:[0,0,0] op_sel_hi:[1,0,0]\n"
"v_mfma_f32_16x16x32_bf16 v[204:207], a[244:247], v[104:107], v[204:207]\n"
"ds_read_b64_tr_b16 a[186:187], v219 offset:1288\n"
"ds_read_b128 v[172:175], v224 offset:4544\n"
"v_mfma_f32_16x16x32_bf16 v[200:203], a[240:243], v[100:103], v[200:203]\n"
"ds_read_b64_tr_b16 a[188:189], v219 offset:4360\n"
"v_fma_mix_f32 v179, v129, v147, v155 op_sel:[1,0,0] op_sel_hi:[1,0,0]\n"
"v_mfma_f32_16x16x32_bf16 v[200:203], a[244:247], v[108:111], v[200:203]\n"
"ds_read_b64_tr_b16 a[190:191], v219 offset:5384\n"
"v_fma_mix_f32 v180, v130, v148, v156 op_sel:[0,0,0] op_sel_hi:[1,0,0]\n"
"v_mfma_f32_16x16x32_bf16 v[196:199], a[248:251], v[96:99], v[196:199]\n"
"ds_read_b64_tr_b16 a[224:225], v219 offset:512\n"
"v_fma_mix_f32 v181, v130, v149, v157 op_sel:[1,0,0] op_sel_hi:[1,0,0]\n"
"v_mfma_f32_16x16x32_bf16 v[196:199], a[252:255], v[104:107], v[196:199]\n"
"ds_read_b64_tr_b16 a[226:227], v219 offset:1536\n"
"v_fma_mix_f32 v182, v131, v150, v158 op_sel:[0,0,0] op_sel_hi:[1,0,0]\n"
"v_mfma_f32_16x16x32_bf16 v[192:195], a[248:251], v[100:103], v[192:195]\n"
"ds_read_b64_tr_b16 a[228:229], v219 offset:4608\n"
"v_fma_mix_f32 v183, v131, v151, v159 op_sel:[1,0,0] op_sel_hi:[1,0,0]\n"
"v_mfma_f32_16x16x32_bf16 v[192:195], a[252:255], v[108:111], v[192:195]\n"
"s_waitcnt lgkmcnt(13)\n"
"ds_read_b64_tr_b16 a[230:231], v219 offset:5632\n"
"v_cvt_pk_bf16_f32 v128, v176, v177\n"
"v_mfma_f32_16x16x32_bf16 a[0:3], a[160:163], v[112:115], a[0:3]\n"
"ds_read_b64_tr_b16 a[232:233], v219 offset:520\n"
"v_cvt_pk_bf16_f32 v129, v178, v179\n"
"v_cvt_pk_bf16_f32 v130, v180, v181\n"
"v_mfma_f32_16x16x32_bf16 a[0:3], a[164:167], v[120:123], a[0:3]\n"
"s_waitcnt vmcnt(8)\n"
"ds_read_b64_tr_b16 a[234:235], v219 offset:1544\n"
"v_cvt_pk_bf16_f32 v131, v182, v183\n"
"v_mfma_f32_16x16x32_bf16 a[4:7], a[160:163], v[116:119], a[4:7]\n"
"s_barrier\n"
"ds_read_b64_tr_b16 a[236:237], v219 offset:4616\n"
"v_fma_mix_f32 v184, v132, v144, v152 op_sel:[0,0,0] op_sel_hi:[1,0,0]\n"
"v_mfma_f32_16x16x32_bf16 a[4:7], a[164:167], v[124:127], a[4:7]\n"
"ds_read_b64_tr_b16 a[238:239], v219 offset:5640\n"
"v_fma_mix_f32 v185, v132, v145, v153 op_sel:[1,0,0] op_sel_hi:[1,0,0]\n"
"v_mfma_f32_16x16x32_bf16 a[8:11], a[168:171], v[112:115], a[8:11]\n"
"s_add_u32 m0, s86, 32768\n"
"ds_read_b64_tr_b16 a[240:241], v219 offset:768\n"
"v_fma_mix_f32 v186, v133, v146, v154 op_sel:[0,0,0] op_sel_hi:[1,0,0]\n"
"v_mfma_f32_16x16x32_bf16 a[8:11], a[172:175], v[120:123], a[8:11]\n"
"ds_read_b64_tr_b16 a[242:243], v219 offset:1792\n"
"buffer_load_dwordx4 v246, s[8:11], s84 offen offset:0 lds\n"
"v_mfma_f32_16x16x32_bf16 a[12:15], a[168:171], v[116:119], a[12:15]\n"
"ds_read_b64_tr_b16 a[244:245], v219 offset:4864\n"
"v_fma_mix_f32 v187, v133, v147, v155 op_sel:[1,0,0] op_sel_hi:[1,0,0]\n"
"v_mfma_f32_16x16x32_bf16 a[12:15], a[172:175], v[124:127], a[12:15]\n"
"s_waitcnt lgkmcnt(11)\n"
"ds_read_b64_tr_b16 a[246:247], v219 offset:5888\n"
"v_fma_mix_f32 v188, v134, v148, v156 op_sel:[0,0,0] op_sel_hi:[1,0,0]\n"
"v_mfma_f32_16x16x32_bf16 a[16:19], a[176:179], v[112:115], a[16:19]\n"
"ds_read_b64_tr_b16 a[248:249], v219 offset:776\n"
"v_fma_mix_f32 v189, v134, v149, v157 op_sel:[1,0,0] op_sel_hi:[1,0,0]\n"
"v_mfma_f32_16x16x32_bf16 a[16:19], a[180:183], v[120:123], a[16:19]\n"
"ds_read_b64_tr_b16 a[250:251], v219 offset:1800\n"
"v_fma_mix_f32 v190, v135, v150, v158 op_sel:[0,0,0] op_sel_hi:[1,0,0]\n"
"v_mfma_f32_16x16x32_bf16 a[20:23], a[176:179], v[116:119], a[20:23]\n"
"ds_read_b64_tr_b16 a[252:253], v219 offset:4872\n"
"v_fma_mix_f32 v191, v135, v151, v159 op_sel:[1,0,0] op_sel_hi:[1,0,0]\n"
"v_mfma_f32_16x16x32_bf16 a[20:23], a[180:183], v[124:127], a[20:23]\n"
"ds_read_b64_tr_b16 a[254:255], v219 offset:5896\n"
"buffer_load_dwordx4 v246, s[8:11], s84 offen offset:1024 lds\n"
"v_mfma_f32_16x16x32_bf16 a[24:27], a[184:187], v[112:115], a[24:27]\n"
"ds_read_b64_tr_b16 a[160:161], v219 offset:8192\n"
"v_cvt_pk_bf16_f32 v132, v184, v185\n"
"v_mfma_f32_16x16x32_bf16 a[24:27], a[188:191], v[120:123], a[24:27]\n"
"ds_read_b64_tr_b16 a[162:163], v219 offset:9216\n"
"v_cvt_pk_bf16_f32 v133, v186, v187\n"
"v_cvt_pk_bf16_f32 v134, v188, v189\n"
"v_mfma_f32_16x16x32_bf16 a[28:31], a[184:187], v[116:119], a[28:31]\n"
"ds_read_b64_tr_b16 a[164:165], v219 offset:12288\n"
"v_cvt_pk_bf16_f32 v135, v190, v191\n"
"v_mfma_f32_16x16x32_bf16 a[28:31], a[188:191], v[124:127], a[28:31]\n"
"s_waitcnt lgkmcnt(11)\n"
"ds_read_b64_tr_b16 a[166:167], v219 offset:13312\n"
"v_fma_mix_f32 v176, v136, v160, v168 op_sel:[0,0,0] op_sel_hi:[1,0,0]\n"
"v_mfma_f32_16x16x32_bf16 a[32:35], a[224:227], v[112:115], a[32:35]\n"
"ds_read_b64_tr_b16 a[168:169], v219 offset:8200\n"
"v_fma_mix_f32 v177, v136, v161, v169 op_sel:[1,0,0] op_sel_hi:[1,0,0]\n"
"v_mfma_f32_16x16x32_bf16 a[32:35], a[228:231], v[120:123], a[32:35]\n"
"ds_read_b64_tr_b16 a[170:171], v219 offset:9224\n"
"buffer_load_dwordx4 v247, s[8:11], s84 offen offset:2048 lds\n"
"v_mfma_f32_16x16x32_bf16 a[36:39], a[224:227], v[116:119], a[36:39]\n"
"ds_read_b64_tr_b16 a[172:173], v219 offset:12296\n"
"v_fma_mix_f32 v178, v137, v162, v170 op_sel:[0,0,0] op_sel_hi:[1,0,0]\n"
"v_mfma_f32_16x16x32_bf16 a[36:39], a[228:231], v[124:127], a[36:39]\n"
"ds_read_b64_tr_b16 a[174:175], v219 offset:13320\n"
"v_fma_mix_f32 v179, v137, v163, v171 op_sel:[1,0,0] op_sel_hi:[1,0,0]\n"
"v_mfma_f32_16x16x32_bf16 a[40:43], a[232:235], v[112:115], a[40:43]\n"
"ds_read_b64_tr_b16 a[176:177], v219 offset:8448\n"
"v_fma_mix_f32 v180, v138, v164, v172 op_sel:[0,0,0] op_sel_hi:[1,0,0]\n"
"v_mfma_f32_16x16x32_bf16 a[40:43], a[236:239], v[120:123], a[40:43]\n"
"ds_read_b64_tr_b16 a[178:179], v219 offset:9472\n"
"v_fma_mix_f32 v181, v138, v165, v173 op_sel:[1,0,0] op_sel_hi:[1,0,0]\n"
"v_mfma_f32_16x16x32_bf16 a[44:47], a[232:235], v[116:119], a[44:47]\n"
"ds_read_b64_tr_b16 a[180:181], v219 offset:12544\n"
"v_fma_mix_f32 v182, v139, v166, v174 op_sel:[0,0,0] op_sel_hi:[1,0,0]\n"
"v_mfma_f32_16x16x32_bf16 a[44:47], a[236:239], v[124:127], a[44:47]\n"
"s_waitcnt lgkmcnt(11)\n"
"ds_read_b64_tr_b16 a[182:183], v219 offset:13568\n"
"buffer_load_dwordx4 v247, s[8:11], s84 offen offset:3072 lds\n"
"v_mfma_f32_16x16x32_bf16 a[48:51], a[240:243], v[112:115], a[48:51]\n"
"ds_read_b64_tr_b16 a[184:185], v219 offset:8456\n"
"v_fma_mix_f32 v183, v139, v167, v175 op_sel:[1,0,0] op_sel_hi:[1,0,0]\n"
"v_mfma_f32_16x16x32_bf16 a[48:51], a[244:247], v[120:123], a[48:51]\n"
"ds_read_b64_tr_b16 a[186:187], v219 offset:9480\n"
"v_cvt_pk_bf16_f32 v136, v176, v177\n"
"v_mfma_f32_16x16x32_bf16 a[52:55], a[240:243], v[116:119], a[52:55]\n"
"ds_read_b64_tr_b16 a[188:189], v219 offset:12552\n"
"v_cvt_pk_bf16_f32 v137, v178, v179\n"
"v_cvt_pk_bf16_f32 v138, v180, v181\n"
"v_mfma_f32_16x16x32_bf16 a[52:55], a[244:247], v[124:127], a[52:55]\n"
"ds_read_b64_tr_b16 a[190:191], v219 offset:13576\n"
"v_cvt_pk_bf16_f32 v139, v182, v183\n"
"v_mfma_f32_16x16x32_bf16 a[56:59], a[248:251], v[112:115], a[56:59]\n"
"s_add_u32 m0, s86, 49152\n"
"ds_read_b64_tr_b16 a[224:225], v219 offset:8704\n"
"v_fma_mix_f32 v184, v140, v160, v168 op_sel:[0,0,0] op_sel_hi:[1,0,0]\n"
"v_mfma_f32_16x16x32_bf16 a[56:59], a[252:255], v[120:123], a[56:59]\n"
"ds_read_b64_tr_b16 a[226:227], v219 offset:9728\n"
"buffer_load_dwordx4 v246, s[12:15], s84 offen offset:0 lds\n"
"v_mfma_f32_16x16x32_bf16 a[60:63], a[248:251], v[116:119], a[60:63]\n"
"ds_read_b64_tr_b16 a[228:229], v219 offset:12800\n"
"v_fma_mix_f32 v185, v140, v161, v169 op_sel:[1,0,0] op_sel_hi:[1,0,0]\n"
"v_mfma_f32_16x16x32_bf16 a[60:63], a[252:255], v[124:127], a[60:63]\n"
"s_waitcnt lgkmcnt(11)\n"
"ds_read_b64_tr_b16 a[230:231], v219 offset:13824\n"
"v_fma_mix_f32 v186, v141, v162, v170 op_sel:[0,0,0] op_sel_hi:[1,0,0]\n"
"v_mfma_f32_16x16x32_bf16 a[64:67], a[160:163], v[112:115], a[64:67]\n"
"ds_read_b64_tr_b16 a[232:233], v219 offset:8712\n"
"v_fma_mix_f32 v187, v141, v163, v171 op_sel:[1,0,0] op_sel_hi:[1,0,0]\n"
"v_mfma_f32_16x16x32_bf16 a[64:67], a[164:167], v[120:123], a[64:67]\n"
"ds_read_b64_tr_b16 a[234:235], v219 offset:9736\n"
"v_fma_mix_f32 v188, v142, v164, v172 op_sel:[0,0,0] op_sel_hi:[1,0,0]\n"
"v_mfma_f32_16x16x32_bf16 a[68:71], a[160:163], v[116:119], a[68:71]\n"
"ds_read_b64_tr_b16 a[236:237], v219 offset:12808\n"
"v_fma_mix_f32 v189, v142, v165, v173 op_sel:[1,0,0] op_sel_hi:[1,0,0]\n"
"v_mfma_f32_16x16x32_bf16 a[68:71], a[164:167], v[124:127], a[68:71]\n"
"ds_read_b64_tr_b16 a[238:239], v219 offset:13832\n"
"buffer_load_dwordx4 v247, s[12:15], s84 offen offset:1024 lds\n"
"v_mfma_f32_16x16x32_bf16 a[72:75], a[168:171], v[112:115], a[72:75]\n"
"ds_read_b64_tr_b16 a[240:241], v219 offset:8960\n"
"v_fma_mix_f32 v190, v143, v166, v174 op_sel:[0,0,0] op_sel_hi:[1,0,0]\n"
"v_mfma_f32_16x16x32_bf16 a[72:75], a[172:175], v[120:123], a[72:75]\n"
"ds_read_b64_tr_b16 a[242:243], v219 offset:9984\n"
"v_fma_mix_f32 v191, v143, v167, v175 op_sel:[1,0,0] op_sel_hi:[1,0,0]\n"
"v_mfma_f32_16x16x32_bf16 a[76:79], a[168:171], v[116:119], a[76:79]\n"
"ds_read_b64_tr_b16 a[244:245], v219 offset:13056\n"
"v_cvt_pk_bf16_f32 v140, v184, v185\n"
"v_mfma_f32_16x16x32_bf16 a[76:79], a[172:175], v[124:127], a[76:79]\n"
"s_waitcnt lgkmcnt(11)\n"
"ds_read_b64_tr_b16 a[246:247], v219 offset:14080\n"
"v_cvt_pk_bf16_f32 v141, v186, v187\n"
"v_cvt_pk_bf16_f32 v142, v188, v189\n"
"v_mfma_f32_16x16x32_bf16 a[80:83], a[176:179], v[112:115], a[80:83]\n"
"ds_read_b64_tr_b16 a[248:249], v219 offset:8968\n"
"v_cvt_pk_bf16_f32 v143, v190, v191\n"
"v_mfma_f32_16x16x32_bf16 a[80:83], a[180:183], v[120:123], a[80:83]\n"
"ds_read_b64_tr_b16 a[250:251], v219 offset:9992\n"
"buffer_load_dwordx4 v246, s[12:15], s84 offen offset:2048 lds\n"
"v_mfma_f32_16x16x32_bf16 a[84:87], a[176:179], v[116:119], a[84:87]\n"
"ds_read_b64_tr_b16 a[252:253], v219 offset:13064\n"
"v_mfma_f32_16x16x32_bf16 a[84:87], a[180:183], v[124:127], a[84:87]\n"
"ds_read_b64_tr_b16 a[254:255], v219 offset:14088\n"
"v_mfma_f32_16x16x32_bf16 v[236:239], a[184:187], v[112:115], v[236:239]\n"
"ds_read_b64_tr_b16 a[160:161], v219 offset:32784\n"
"v_mfma_f32_16x16x32_bf16 v[236:239], a[188:191], v[120:123], v[236:239]\n"
"ds_read_b64_tr_b16 a[162:163], v219 offset:33808\n"
"v_mfma_f32_16x16x32_bf16 v[232:235], a[184:187], v[116:119], v[232:235]\n"
"ds_read_b64_tr_b16 a[164:165], v219 offset:36880\n"
"v_mfma_f32_16x16x32_bf16 v[232:235], a[188:191], v[124:127], v[232:235]\n"
"s_waitcnt lgkmcnt(11)\n"
"buffer_load_dwordx4 v247, s[12:15], s84 offen offset:3072 lds\n"
"ds_read_b64_tr_b16 a[166:167], v219 offset:37904\n"
"v_mfma_f32_16x16x32_bf16 v[228:231], a[224:227], v[112:115], v[228:231]\n"
"s_add_u32 s84, s84, 16384\n"
"ds_read_b64_tr_b16 a[168:169], v219 offset:32792\n"
"v_mfma_f32_16x16x32_bf16 v[228:231], a[228:231], v[120:123], v[228:231]\n"
"ds_read_b64_tr_b16 a[170:171], v219 offset:33816\n"
"v_mfma_f32_16x16x32_bf16 v[220:223], a[224:227], v[116:119], v[220:223]\n"
"ds_read_b64_tr_b16 a[172:173], v219 offset:36888\n"
"v_mfma_f32_16x16x32_bf16 v[220:223], a[228:231], v[124:127], v[220:223]\n"
"ds_read_b64_tr_b16 a[174:175], v219 offset:37912\n"
"v_mfma_f32_16x16x32_bf16 v[212:215], a[232:235], v[112:115], v[212:215]\n"
"ds_read_b64_tr_b16 a[176:177], v219 offset:33040\n"
"v_mfma_f32_16x16x32_bf16 v[212:215], a[236:239], v[120:123], v[212:215]\n"
"ds_read_b64_tr_b16 a[178:179], v219 offset:34064\n"
"v_mfma_f32_16x16x32_bf16 v[208:211], a[232:235], v[116:119], v[208:211]\n"
"ds_read_b64_tr_b16 a[180:181], v219 offset:37136\n"
"v_mfma_f32_16x16x32_bf16 v[208:211], a[236:239], v[124:127], v[208:211]\n"
"s_waitcnt lgkmcnt(11)\n"
"ds_read_b64_tr_b16 a[182:183], v219 offset:38160\n"
"v_mfma_f32_16x16x32_bf16 v[204:207], a[240:243], v[112:115], v[204:207]\n"
"ds_read_b64_tr_b16 a[184:185], v219 offset:33048\n"
"v_mfma_f32_16x16x32_bf16 v[204:207], a[244:247], v[120:123], v[204:207]\n"
"ds_read_b64_tr_b16 a[186:187], v219 offset:34072\n"
"v_mfma_f32_16x16x32_bf16 v[200:203], a[240:243], v[116:119], v[200:203]\n"
"ds_read_b64_tr_b16 a[188:189], v219 offset:37144\n"
"v_mfma_f32_16x16x32_bf16 v[200:203], a[244:247], v[124:127], v[200:203]\n"
"ds_read_b64_tr_b16 a[190:191], v219 offset:38168\n"
"v_mfma_f32_16x16x32_bf16 v[196:199], a[248:251], v[112:115], v[196:199]\n"
"ds_read_b64_tr_b16 a[224:225], v219 offset:33296\n"
"v_mfma_f32_16x16x32_bf16 v[196:199], a[252:255], v[120:123], v[196:199]\n"
"ds_read_b64_tr_b16 a[226:227], v219 offset:34320\n"
"v_mfma_f32_16x16x32_bf16 v[192:195], a[248:251], v[116:119], v[192:195]\n"
"ds_read_b64_tr_b16 a[228:229], v219 offset:37392\n"
"v_mfma_f32_16x16x32_bf16 v[192:195], a[252:255], v[124:127], v[192:195]\n"
"s_waitcnt lgkmcnt(11)\n"
"ds_read_b64_tr_b16 a[230:231], v219 offset:38416\n"
"v_mfma_f32_16x16x32_bf16 a[0:3], a[160:163], v[128:131], a[0:3]\n"
"ds_read_b64_tr_b16 a[232:233], v219 offset:33304\n"
"v_mfma_f32_16x16x32_bf16 a[0:3], a[164:167], v[136:139], a[0:3]\n"
"s_waitcnt vmcnt(8)\n"
"ds_read_b64_tr_b16 a[234:235], v219 offset:34328\n"
"v_mfma_f32_16x16x32_bf16 a[4:7], a[160:163], v[132:135], a[4:7]\n"
"s_barrier\n"
"ds_read_b64_tr_b16 a[236:237], v219 offset:37400\n"
"v_mfma_f32_16x16x32_bf16 a[4:7], a[164:167], v[140:143], a[4:7]\n"
"ds_read_b64_tr_b16 a[238:239], v219 offset:38424\n"
"v_mfma_f32_16x16x32_bf16 a[8:11], a[168:171], v[128:131], a[8:11]\n"
"s_add_u32 m0, s86, 65536\n"
"ds_read_b64_tr_b16 a[240:241], v219 offset:33552\n"
"v_mfma_f32_16x16x32_bf16 a[8:11], a[172:175], v[136:139], a[8:11]\n"
"ds_read_b64_tr_b16 a[242:243], v219 offset:34576\n"
"buffer_load_dwordx4 v246, s[8:11], s84 offen offset:0 lds\n"
"v_mfma_f32_16x16x32_bf16 a[12:15], a[168:171], v[132:135], a[12:15]\n"
"ds_read_b64_tr_b16 a[244:245], v219 offset:37648\n"
"v_accvgpr_read_b32 v176, a0\n"
"v_mfma_f32_16x16x32_bf16 a[12:15], a[172:175], v[140:143], a[12:15]\n"
"s_waitcnt lgkmcnt(11)\n"
"ds_read_b64_tr_b16 a[246:247], v219 offset:38672\n"
"v_accvgpr_read_b32 v177, a1\n"
"v_mfma_f32_16x16x32_bf16 a[16:19], a[176:179], v[128:131], a[16:19]\n"
"ds_read_b64_tr_b16 a[248:249], v219 offset:33560\n"
"v_accvgpr_read_b32 v178, a2\n"
"v_mfma_f32_16x16x32_bf16 a[16:19], a[180:183], v[136:139], a[16:19]\n"
"ds_read_b64_tr_b16 a[250:251], v219 offset:34584\n"
"v_accvgpr_read_b32 v179, a3\n"
"v_mfma_f32_16x16x32_bf16 a[20:23], a[176:179], v[132:135], a[20:23]\n"
"ds_read_b64_tr_b16 a[252:253], v219 offset:37656\n"
"v_cvt_pk_bf16_f32 v64, v176, v177\n"
"v_mfma_f32_16x16x32_bf16 a[20:23], a[180:183], v[140:143], a[20:23]\n"
"ds_read_b64_tr_b16 a[254:255], v219 offset:38680\n"
"buffer_load_dwordx4 v246, s[8:11], s84 offen offset:1024 lds\n"
"v_mfma_f32_16x16x32_bf16 a[24:27], a[184:187], v[128:131], a[24:27]\n"
"ds_read_b64_tr_b16 a[160:161], v219 offset:40976\n"
"v_cvt_pk_bf16_f32 v65, v178, v179\n"
"v_mfma_f32_16x16x32_bf16 a[24:27], a[188:191], v[136:139], a[24:27]\n"
"ds_read_b64_tr_b16 a[162:163], v219 offset:42000\n"
"v_accvgpr_read_b32 v180, a4\n"
"v_mfma_f32_16x16x32_bf16 a[28:31], a[184:187], v[132:135], a[28:31]\n"
"ds_read_b64_tr_b16 a[164:165], v219 offset:45072\n"
"v_accvgpr_read_b32 v181, a5\n"
"v_mfma_f32_16x16x32_bf16 a[28:31], a[188:191], v[140:143], a[28:31]\n"
"s_waitcnt lgkmcnt(11)\n"
"ds_read_b64_tr_b16 a[166:167], v219 offset:46096\n"
"v_accvgpr_read_b32 v182, a6\n"
"v_mfma_f32_16x16x32_bf16 a[32:35], a[224:227], v[128:131], a[32:35]\n"
"ds_read_b64_tr_b16 a[168:169], v219 offset:40984\n"
"v_accvgpr_read_b32 v183, a7\n"
"v_mfma_f32_16x16x32_bf16 a[32:35], a[228:231], v[136:139], a[32:35]\n"
"ds_read_b64_tr_b16 a[170:171], v219 offset:42008\n"
"buffer_load_dwordx4 v247, s[8:11], s84 offen offset:2048 lds\n"
"v_mfma_f32_16x16x32_bf16 a[36:39], a[224:227], v[132:135], a[36:39]\n"
"ds_read_b64_tr_b16 a[172:173], v219 offset:45080\n"
"v_cvt_pk_bf16_f32 v96, v180, v181\n"
"v_mfma_f32_16x16x32_bf16 a[36:39], a[228:231], v[140:143], a[36:39]\n"
"ds_read_b64_tr_b16 a[174:175], v219 offset:46104\n"
"v_cvt_pk_bf16_f32 v97, v182, v183\n"
"v_mfma_f32_16x16x32_bf16 a[40:43], a[232:235], v[128:131], a[40:43]\n"
"ds_read_b64_tr_b16 a[176:177], v219 offset:41232\n"
"v_accvgpr_read_b32 v184, a8\n"
"v_mfma_f32_16x16x32_bf16 a[40:43], a[236:239], v[136:139], a[40:43]\n"
"ds_read_b64_tr_b16 a[178:179], v219 offset:42256\n"
"v_accvgpr_read_b32 v185, a9\n"
"v_mfma_f32_16x16x32_bf16 a[44:47], a[232:235], v[132:135], a[44:47]\n"
"ds_read_b64_tr_b16 a[180:181], v219 offset:45328\n"
"v_accvgpr_read_b32 v186, a10\n"
"v_mfma_f32_16x16x32_bf16 a[44:47], a[236:239], v[140:143], a[44:47]\n"
"s_waitcnt lgkmcnt(11)\n"
"ds_read_b64_tr_b16 a[182:183], v219 offset:46352\n"
"buffer_load_dwordx4 v247, s[8:11], s84 offen offset:3072 lds\n"
"v_mfma_f32_16x16x32_bf16 a[48:51], a[240:243], v[128:131], a[48:51]\n"
"ds_read_b64_tr_b16 a[184:185], v219 offset:41240\n"
"v_accvgpr_read_b32 v187, a11\n"
"v_mfma_f32_16x16x32_bf16 a[48:51], a[244:247], v[136:139], a[48:51]\n"
"ds_read_b64_tr_b16 a[186:187], v219 offset:42264\n"
"v_cvt_pk_bf16_f32 v66, v184, v185\n"
"v_mfma_f32_16x16x32_bf16 a[52:55], a[240:243], v[132:135], a[52:55]\n"
"ds_read_b64_tr_b16 a[188:189], v219 offset:45336\n"
"v_cvt_pk_bf16_f32 v67, v186, v187\n"
"v_mfma_f32_16x16x32_bf16 a[52:55], a[244:247], v[140:143], a[52:55]\n"
"ds_read_b64_tr_b16 a[190:191], v219 offset:46360\n"
"v_accvgpr_read_b32 v188, a12\n"
"v_mfma_f32_16x16x32_bf16 a[56:59], a[248:251], v[128:131], a[56:59]\n"
"s_add_u32 m0, s86, 81920\n"
"ds_read_b64_tr_b16 a[224:225], v219 offset:41488\n"
"v_accvgpr_read_b32 v189, a13\n"
"v_mfma_f32_16x16x32_bf16 a[56:59], a[252:255], v[136:139], a[56:59]\n"
"ds_read_b64_tr_b16 a[226:227], v219 offset:42512\n"
"buffer_load_dwordx4 v246, s[12:15], s84 offen offset:0 lds\n"
"v_mfma_f32_16x16x32_bf16 a[60:63], a[248:251], v[132:135], a[60:63]\n"
"ds_read_b64_tr_b16 a[228:229], v219 offset:45584\n"
"v_accvgpr_read_b32 v190, a14\n"
"v_mfma_f32_16x16x32_bf16 a[60:63], a[252:255], v[140:143], a[60:63]\n"
"s_waitcnt lgkmcnt(11)\n"
"ds_read_b64_tr_b16 a[230:231], v219 offset:46608\n"
"v_accvgpr_read_b32 v191, a15\n"
"v_mfma_f32_16x16x32_bf16 a[64:67], a[160:163], v[128:131], a[64:67]\n"
"ds_read_b64_tr_b16 a[232:233], v219 offset:41496\n"
"v_cvt_pk_bf16_f32 v98, v188, v189\n"
"v_mfma_f32_16x16x32_bf16 a[64:67], a[164:167], v[136:139], a[64:67]\n"
"ds_read_b64_tr_b16 a[234:235], v219 offset:42520\n"
"v_cvt_pk_bf16_f32 v99, v190, v191\n"
"v_mfma_f32_16x16x32_bf16 a[68:71], a[160:163], v[132:135], a[68:71]\n"
"ds_read_b64_tr_b16 a[236:237], v219 offset:45592\n"
"v_accvgpr_read_b32 v176, a16\n"
"v_mfma_f32_16x16x32_bf16 a[68:71], a[164:167], v[140:143], a[68:71]\n"
"ds_read_b64_tr_b16 a[238:239], v219 offset:46616\n"
"buffer_load_dwordx4 v247, s[12:15], s84 offen offset:1024 lds\n"
"v_mfma_f32_16x16x32_bf16 a[72:75], a[168:171], v[128:131], a[72:75]\n"
"ds_read_b64_tr_b16 a[240:241], v219 offset:41744\n"
"v_accvgpr_read_b32 v177, a17\n"
"v_mfma_f32_16x16x32_bf16 a[72:75], a[172:175], v[136:139], a[72:75]\n"
"ds_read_b64_tr_b16 a[242:243], v219 offset:42768\n"
"v_accvgpr_read_b32 v178, a18\n"
"v_mfma_f32_16x16x32_bf16 a[76:79], a[168:171], v[132:135], a[76:79]\n"
"ds_read_b64_tr_b16 a[244:245], v219 offset:45840\n"
"v_accvgpr_read_b32 v179, a19\n"
"v_mfma_f32_16x16x32_bf16 a[76:79], a[172:175], v[140:143], a[76:79]\n"
"s_waitcnt lgkmcnt(11)\n"
"ds_read_b64_tr_b16 a[246:247], v219 offset:46864\n"
"v_cvt_pk_bf16_f32 v68, v176, v177\n"
"v_mfma_f32_16x16x32_bf16 a[80:83], a[176:179], v[128:131], a[80:83]\n"
"ds_read_b64_tr_b16 a[248:249], v219 offset:41752\n"
"v_cvt_pk_bf16_f32 v69, v178, v179\n"
"v_mfma_f32_16x16x32_bf16 a[80:83], a[180:183], v[136:139], a[80:83]\n"
"ds_read_b64_tr_b16 a[250:251], v219 offset:42776\n"
"buffer_load_dwordx4 v246, s[12:15], s84 offen offset:2048 lds\n"
"v_mfma_f32_16x16x32_bf16 a[84:87], a[176:179], v[132:135], a[84:87]\n"
"ds_read_b64_tr_b16 a[252:253], v219 offset:45848\n"
"v_accvgpr_read_b32 v180, a20\n"
"v_mfma_f32_16x16x32_bf16 a[84:87], a[180:183], v[140:143], a[84:87]\n"
"ds_read_b64_tr_b16 a[254:255], v219 offset:46872\n"
"v_accvgpr_read_b32 v181, a21\n"
"v_mfma_f32_16x16x32_bf16 v[236:239], a[184:187], v[128:131], v[236:239]\n"
"ds_read_b128 a[160:163], v254 offset:0\n"
"v_accvgpr_read_b32 v182, a22\n"
"v_mfma_f32_16x16x32_bf16 v[236:239], a[188:191], v[136:139], v[236:239]\n"
"ds_read_b128 a[164:167], v254 offset:256\n"
"v_accvgpr_read_b32 v183, a23\n"
"v_mfma_f32_16x16x32_bf16 v[232:235], a[184:187], v[132:135], v[232:235]\n"
"ds_read_b128 a[168:171], v254 offset:512\n"
"v_cvt_pk_bf16_f32 v100, v180, v181\n"
"v_mfma_f32_16x16x32_bf16 v[232:235], a[188:191], v[140:143], v[232:235]\n"
"s_waitcnt lgkmcnt(11)\n"
"buffer_load_dwordx4 v247, s[12:15], s84 offen offset:3072 lds\n"
"ds_read_b128 a[172:175], v254 offset:768\n"
"v_mfma_f32_16x16x32_bf16 v[228:231], a[224:227], v[128:131], v[228:231]\n"
"s_add_u32 s84, s84, 16384\n"
"ds_read_b128 a[128:131], v240 offset:0\n"
"v_cvt_pk_bf16_f32 v101, v182, v183\n"
"v_mfma_f32_16x16x32_bf16 v[228:231], a[228:231], v[136:139], v[228:231]\n"
"ds_read_b128 a[132:135], v240 offset:256\n"
"v_accvgpr_read_b32 v184, a24\n"
"v_mfma_f32_16x16x32_bf16 v[220:223], a[224:227], v[132:135], v[220:223]\n"
"ds_read_b128 a[136:139], v240 offset:512\n"
"v_accvgpr_read_b32 v185, a25\n"
"v_mfma_f32_16x16x32_bf16 v[220:223], a[228:231], v[140:143], v[220:223]\n"
"ds_read_b128 a[140:143], v240 offset:768\n"
"v_accvgpr_read_b32 v186, a26\n"
"v_mfma_f32_16x16x32_bf16 v[212:215], a[232:235], v[128:131], v[212:215]\n"
"ds_read_b128 a[144:147], v240 offset:4096\n"
"v_accvgpr_read_b32 v187, a27\n"
"v_mfma_f32_16x16x32_bf16 v[212:215], a[236:239], v[136:139], v[212:215]\n"
"ds_read_b128 a[148:151], v240 offset:4352\n"
"v_cvt_pk_bf16_f32 v70, v184, v185\n"
"v_mfma_f32_16x16x32_bf16 v[208:211], a[232:235], v[132:135], v[208:211]\n"
"ds_read_b128 a[152:155], v240 offset:4608\n"
"v_cvt_pk_bf16_f32 v71, v186, v187\n"
"v_mfma_f32_16x16x32_bf16 v[208:211], a[236:239], v[140:143], v[208:211]\n"
"s_waitcnt lgkmcnt(11)\n"
"ds_read_b128 a[156:159], v240 offset:4864\n"
"v_accvgpr_read_b32 v188, a28\n"
"v_mfma_f32_16x16x32_bf16 v[204:207], a[240:243], v[128:131], v[204:207]\n"
"ds_read_b64_tr_b16 a[192:193], v242 offset:16384\n"
"v_accvgpr_read_b32 v189, a29\n"
"v_mfma_f32_16x16x32_bf16 v[204:207], a[244:247], v[136:139], v[204:207]\n"
"ds_read_b64_tr_b16 a[194:195], v242 offset:20480\n"
"v_accvgpr_read_b32 v190, a30\n"
"v_mfma_f32_16x16x32_bf16 v[200:203], a[240:243], v[132:135], v[200:203]\n"
"ds_read_b64_tr_b16 a[196:197], v243 offset:16384\n"
"v_accvgpr_read_b32 v191, a31\n"
"v_mfma_f32_16x16x32_bf16 v[200:203], a[244:247], v[140:143], v[200:203]\n"
"ds_read_b64_tr_b16 a[198:199], v243 offset:20480\n"
"v_cvt_pk_bf16_f32 v102, v188, v189\n"
"v_mfma_f32_16x16x32_bf16 v[196:199], a[248:251], v[128:131], v[196:199]\n"
"ds_read_b64_tr_b16 a[200:201], v242 offset:16640\n"
"v_cvt_pk_bf16_f32 v103, v190, v191\n"
"v_mfma_f32_16x16x32_bf16 v[196:199], a[252:255], v[136:139], v[196:199]\n"
"ds_read_b64_tr_b16 a[202:203], v242 offset:20736\n"
"v_accvgpr_read_b32 v176, a32\n"
"v_mfma_f32_16x16x32_bf16 v[192:195], a[248:251], v[132:135], v[192:195]\n"
"ds_read_b64_tr_b16 a[204:205], v243 offset:16640\n"
"v_accvgpr_read_b32 v177, a33\n"
"v_mfma_f32_16x16x32_bf16 v[192:195], a[252:255], v[140:143], v[192:195]\n"
"ds_read_b64_tr_b16 a[206:207], v243 offset:20736\n"
"v_accvgpr_read_b32 v178, a34\n"
"s_barrier\n"
"ds_read_b64_tr_b16 a[208:209], v242 offset:16896\n"
"v_accvgpr_read_b32 v179, a35\n"
"v_cvt_pk_bf16_f32 v72, v176, v177\n"
"v_cvt_pk_bf16_f32 v73, v178, v179\n"
"ds_read_b64_tr_b16 a[210:211], v242 offset:20992\n"
"v_accvgpr_read_b32 v180, a36\n"
"v_accvgpr_read_b32 v181, a37\n"
"v_accvgpr_read_b32 v182, a38\n"
"ds_read_b64_tr_b16 a[212:213], v243 offset:16896\n"
"v_accvgpr_read_b32 v183, a39\n"
"v_cvt_pk_bf16_f32 v104, v180, v181\n"
"v_cvt_pk_bf16_f32 v105, v182, v183\n"
"ds_read_b64_tr_b16 a[214:215], v243 offset:20992\n"
"v_accvgpr_read_b32 v184, a40\n"
"v_accvgpr_read_b32 v185, a41\n"
"v_accvgpr_read_b32 v186, a42\n"
"ds_read_b64_tr_b16 a[216:217], v242 offset:17152\n"
"v_accvgpr_read_b32 v187, a43\n"
"v_cvt_pk_bf16_f32 v74, v184, v185\n"
"v_cvt_pk_bf16_f32 v75, v186, v187\n"
"ds_read_b64_tr_b16 a[218:219], v242 offset:21248\n"
"v_accvgpr_read_b32 v188, a44\n"
"v_accvgpr_read_b32 v189, a45\n"
"v_accvgpr_read_b32 v190, a46\n"
"ds_read_b64_tr_b16 a[220:221], v243 offset:17152\n"
"v_accvgpr_read_b32 v191, a47\n"
"v_cvt_pk_bf16_f32 v106, v188, v189\n"
"v_cvt_pk_bf16_f32 v107, v190, v191\n"
"ds_read_b64_tr_b16 a[222:223], v243 offset:21248\n"
"v_accvgpr_read_b32 v176, a48\n"
"v_accvgpr_read_b32 v177, a49\n"
"v_accvgpr_read_b32 v178, a50\n"
"v_accvgpr_read_b32 v179, a51\n"
"v_cvt_pk_bf16_f32 v76, v176, v177\n"
"v_cvt_pk_bf16_f32 v77, v178, v179\n"
"v_accvgpr_read_b32 v180, a52\n"
"v_accvgpr_read_b32 v181, a53\n"
"v_accvgpr_read_b32 v182, a54\n"
"v_accvgpr_read_b32 v183, a55\n"
"v_cvt_pk_bf16_f32 v108, v180, v181\n"
"v_cvt_pk_bf16_f32 v109, v182, v183\n"
"v_accvgpr_read_b32 v184, a56\n"
"v_accvgpr_read_b32 v185, a57\n"
"v_accvgpr_read_b32 v186, a58\n"
"v_accvgpr_read_b32 v187, a59\n"
"v_cvt_pk_bf16_f32 v78, v184, v185\n"
"v_cvt_pk_bf16_f32 v79, v186, v187\n"
"v_accvgpr_read_b32 v188, a60\n"
"v_accvgpr_read_b32 v189, a61\n"
"v_accvgpr_read_b32 v190, a62\n"
"v_accvgpr_read_b32 v191, a63\n"
"v_cvt_pk_bf16_f32 v110, v188, v189\n"
"v_cvt_pk_bf16_f32 v111, v190, v191\n"
"v_accvgpr_read_b32 v176, a64\n"
"v_accvgpr_read_b32 v177, a65\n"
"v_accvgpr_read_b32 v178, a66\n"
"s_nop 1\n"
"v_accvgpr_read_b32 v179, a67\n"
"v_cvt_pk_bf16_f32 v80, v176, v177\n"
"v_cvt_pk_bf16_f32 v81, v178, v179\n"
"v_accvgpr_read_b32 v180, a68\n"
"s_waitcnt lgkmcnt(14)\n"
"v_mfma_f32_16x16x32_bf16 v[144:147], a[160:163], v[64:67], 0\n"
"AT_pg5:\n"
"s_cbranch_execz AT_hr\n"
"v_accvgpr_read_b32 v181, a69\n"
"v_accvgpr_read_b32 v182, a70\n"
"v_mfma_f32_16x16x32_bf16 v[144:147], a[164:167], v[68:71], v[144:147]\n"
"v_accvgpr_read_b32 v183, a71\n"
"v_cvt_pk_bf16_f32 v112, v180, v181\n"
"v_mfma_f32_16x16x32_bf16 v[144:147], a[168:171], v[72:75], v[144:147]\n"
"v_cvt_pk_bf16_f32 v113, v182, v183\n"
"v_accvgpr_read_b32 v184, a72\n"
"v_mfma_f32_16x16x32_bf16 v[144:147], a[172:175], v[76:79], v[144:147]\n"
"v_accvgpr_read_b32 v185, a73\n"
"v_accvgpr_read_b32 v186, a74\n"
"v_mfma_f32_16x16x32_bf16 v[152:155], a[160:163], v[96:99], 0\n"
"v_accvgpr_read_b32 v187, a75\n"
"v_cvt_pk_bf16_f32 v82, v184, v185\n"
"v_mfma_f32_16x16x32_bf16 v[152:155], a[164:167], v[100:103], v[152:155]\n"
"v_cvt_pk_bf16_f32 v83, v186, v187\n"
"v_accvgpr_read_b32 v188, a76\n"
"v_mfma_f32_16x16x32_bf16 v[152:155], a[168:171], v[104:107], v[152:155]\n"
"v_accvgpr_read_b32 v189, a77\n"
"v_accvgpr_read_b32 v190, a78\n"
"v_mfma_f32_16x16x32_bf16 v[152:155], a[172:175], v[108:111], v[152:155]\n"
"v_accvgpr_read_b32 v191, a79\n"
"v_cvt_pk_bf16_f32 v114, v188, v189\n"
"v_cvt_pk_bf16_f32 v115, v190, v191\n"
"v_accvgpr_read_b32 v176, a80\n"
"v_accvgpr_read_b32 v177, a81\n"
"v_accvgpr_read_b32 v178, a82\n"
"v_accvgpr_read_b32 v179, a83\n"
"v_cvt_pk_bf16_f32 v84, v176, v177\n"
"v_cvt_pk_bf16_f32 v85, v178, v179\n"
"v_accvgpr_read_b32 v180, a84\n"
"v_accvgpr_read_b32 v181, a85\n"
"v_accvgpr_read_b32 v182, a86\n"
"v_accvgpr_read_b32 v183, a87\n"
"v_cvt_pk_bf16_f32 v116, v180, v181\n"
"v_cvt_pk_bf16_f32 v117, v182, v183\n"
"v_cvt_pk_bf16_f32 v86, v236, v237\n"
"v_cvt_pk_bf16_f32 v87, v238, v239\n"
"v_cvt_pk_bf16_f32 v118, v232, v233\n"
"v_cvt_pk_bf16_f32 v119, v234, v235\n"
"v_cvt_pk_bf16_f32 v88, v228, v229\n"
"v_cvt_pk_bf16_f32 v89, v230, v231\n"
"v_cvt_pk_bf16_f32 v120, v220, v221\n"
"v_cvt_pk_bf16_f32 v121, v222, v223\n"
"v_cvt_pk_bf16_f32 v90, v212, v213\n"
"v_cvt_pk_bf16_f32 v91, v214, v215\n"
"v_cvt_pk_bf16_f32 v122, v208, v209\n"
"v_cvt_pk_bf16_f32 v123, v210, v211\n"
"v_cvt_pk_bf16_f32 v92, v204, v205\n"
"v_cvt_pk_bf16_f32 v93, v206, v207\n"
"v_cvt_pk_bf16_f32 v124, v200, v201\n"
"v_cvt_pk_bf16_f32 v125, v202, v203\n"
"v_cvt_pk_bf16_f32 v94, v196, v197\n"
"v_cvt_pk_bf16_f32 v95, v198, v199\n"
"v_cvt_pk_bf16_f32 v126, v192, v193\n"
"v_cvt_pk_bf16_f32 v127, v194, v195\n"
"v_max3_f32 v144, v144, v145, v146\n"
"v_max_f32 v144, v144, v147\n"
"v_sub_f32 v24, 0xc2480000, v144\n"
"v_sub_f32 v25, 0xc2480000, v144\n"
"v_sub_f32 v26, 0xc2480000, v144\n"
"v_sub_f32 v27, 0xc2480000, v144\n"
"s_nop 1\n"
"v_mfma_f32_16x16x32_bf16 v[148:151], a[160:163], v[80:83], 0\n"
"v_max3_f32 v152, v152, v153, v154\n"
"v_max_f32 v152, v152, v155\n"
"v_mfma_f32_16x16x32_bf16 v[148:151], a[164:167], v[84:87], v[148:151]\n"
"v_sub_f32 v32, 0xc2480000, v152\n"
"v_sub_f32 v33, 0xc2480000, v152\n"
"v_mfma_f32_16x16x32_bf16 v[148:151], a[168:171], v[88:91], v[148:151]\n"
"v_sub_f32 v34, 0xc2480000, v152\n"
"v_sub_f32 v35, 0xc2480000, v152\n"
"v_mfma_f32_16x16x32_bf16 v[148:151], a[172:175], v[92:95], v[148:151]\n"
"v_mov_b32 v40, 0x3f803f80\n"
"v_mov_b32 v41, 0x3f803f80\n"
"v_mfma_f32_16x16x32_bf16 v[156:159], a[160:163], v[112:115], 0\n"
"v_mov_b32 v42, 0x3f803f80\n"
"v_mov_b32 v43, 0x3f803f80\n"
"v_mfma_f32_16x16x32_bf16 v[156:159], a[164:167], v[116:119], v[156:159]\n"
"v_mov_b32 v20, 0\n"
"v_mov_b32 v21, 0\n"
"v_mfma_f32_16x16x32_bf16 v[156:159], a[168:171], v[120:123], v[156:159]\n"
"v_mov_b32 v22, 0\n"
"v_mov_b32 v23, 0\n"
"v_mfma_f32_16x16x32_bf16 v[156:159], a[172:175], v[124:127], v[156:159]\n"
"v_mov_b32 v44, 0\n"
"v_mov_b32 v52, 0\n"
"v_mov_b32 v216, -1\n"
"v_mov_b32 v217, -1\n"
"v_mov_b32 v218, -1\n"
"v_mov_b32 v219, -1\n"
"v_lshl_add_u32 v255, v248, 6, s48\n"
"v_lshlrev_b32 v207, 4, v248\n"
"v_add_u32 v208, 18432, v207\n"
"s_waitcnt lgkmcnt(0)\n"
"AT_main:\n"
"s_or_b32 s56, s42, s41\n"
"s_cmp_lg_u32 s56, 0\n"
"s_cbranch_scc1 AT_h1\n"
"v_lshlrev_b32 v214, 7, v248\n"
"s_mov_b32 s57, 32768\n"
"s_mov_b32 s58, 40960\n"
"s_mov_b32 s59, 49152\n"
"s_mov_b32 s60, 57344\n"
"buffer_load_dword v210, v214, s[28:31], s57 offen offset:4 sc1\n"
"buffer_load_dword v211, v214, s[28:31], s58 offen offset:4 sc1\n"
"buffer_load_dword v212, v214, s[28:31], s59 offen offset:4 sc1\n"
"buffer_load_dword v213, v214, s[28:31], s60 offen offset:4 sc1\n"
"AT_h1:\n"
"v_mfma_f32_16x16x32_bf16 v[0:3], a[128:131], v[64:67], v[24:27]\n"
"s_nop 0\n"
"ds_read_b128 a[160:163], v240 offset:8192\n"
"v_mfma_f32_16x16x32_bf16 v[0:3], a[132:135], v[68:71], v[0:3]\n"
"s_add_u32 m0, s86, 98304\n"
"v_mfma_f32_16x16x32_bf16 v[0:3], a[136:139], v[72:75], v[0:3]\n"
"s_nop 0\n"
"buffer_load_dwordx4 v246, s[8:11], s84 offen offset:0 lds\n"
"ds_read_b128 a[164:167], v240 offset:8448\n"
"v_mfma_f32_16x16x32_bf16 v[0:3], a[140:143], v[76:79], v[0:3]\n"
"s_nop 0\n"
"v_mfma_f32_16x16x32_bf16 v[4:7], a[144:147], v[64:67], v[24:27]\n"
"ds_read_b128 a[168:171], v240 offset:8704\n"
"v_max3_f32 v148, v148, v149, v150\n"
"v_mfma_f32_16x16x32_bf16 v[4:7], a[148:151], v[68:71], v[4:7]\n"
"v_max_f32 v148, v148, v151\n"
"v_sub_f32 v28, 0xc2480000, v148\n"
"v_mfma_f32_16x16x32_bf16 v[4:7], a[152:155], v[72:75], v[4:7]\n"
"ds_read_b128 a[172:175], v240 offset:8960\n"
"v_sub_f32 v29, 0xc2480000, v148\n"
"v_mfma_f32_16x16x32_bf16 v[4:7], a[156:159], v[76:79], v[4:7]\n"
"v_exp_f32 v0, v0\n"
"v_sub_f32 v30, 0xc2480000, v148\n"
"v_mfma_f32_16x16x32_bf16 a[96:99], v[40:43], v[20:23], 0\n"
"buffer_load_dwordx4 v246, s[8:11], s84 offen offset:1024 lds\n"
"v_sub_f32 v31, 0xc2480000, v148\n"
"v_mfma_f32_16x16x32_bf16 a[100:103], v[40:43], v[20:23], 0\n"
"v_exp_f32 v1, v1\n"
"v_max3_f32 v156, v156, v157, v158\n"
"v_mfma_f32_16x16x32_bf16 a[104:107], v[40:43], v[20:23], 0\n"
"ds_read_b128 a[176:179], v240 offset:12288\n"
"v_cvt_pk_bf16_f32 v16, v0, v1\n"
"v_max_f32 v156, v156, v159\n"
"v_mfma_f32_16x16x32_bf16 a[108:111], v[40:43], v[20:23], 0\n"
"v_exp_f32 v2, v2\n"
"v_sub_f32 v36, 0xc2480000, v156\n"
"v_mfma_f32_16x16x32_bf16 a[112:115], v[40:43], v[20:23], 0\n"
"ds_read_b128 a[180:183], v240 offset:12544\n"
"v_add_f32 v44, v44, v0\n"
"v_sub_f32 v37, 0xc2480000, v156\n"
"v_mfma_f32_16x16x32_bf16 a[116:119], v[40:43], v[20:23], 0\n"
"v_exp_f32 v3, v3\n"
"v_sub_f32 v38, 0xc2480000, v156\n"
"v_mfma_f32_16x16x32_bf16 a[120:123], v[40:43], v[20:23], 0\n"
"ds_read_b128 a[184:187], v240 offset:12800\n"
"v_cvt_pk_bf16_f32 v17, v2, v3\n"
"v_sub_f32 v39, 0xc2480000, v156\n"
"v_mfma_f32_16x16x32_bf16 a[124:127], v[40:43], v[20:23], 0\n"
"s_nop 0\n"
"v_exp_f32 v4, v4\n"
"v_mfma_f32_16x16x32_bf16 v[56:59], v[40:43], v[20:23], 0\n"
"buffer_load_dwordx4 v247, s[8:11], s84 offen offset:2048 lds\n"
"ds_read_b128 a[188:191], v240 offset:13056\n"
"v_add_f32 v44, v44, v1\n"
"v_mfma_f32_16x16x32_bf16 v[8:11], a[128:131], v[80:83], v[28:31]\n"
"s_nop 0\n"
"v_exp_f32 v5, v5\n"
"v_mfma_f32_16x16x32_bf16 v[8:11], a[132:135], v[84:87], v[8:11]\n"
"ds_read_b64_tr_b16 a[224:225], v242 offset:24576\n"
"v_cvt_pk_bf16_f32 v18, v4, v5\n"
"v_mfma_f32_16x16x32_bf16 v[8:11], a[136:139], v[88:91], v[8:11]\n"
"s_nop 0\n"
"v_exp_f32 v6, v6\n"
"v_mfma_f32_16x16x32_bf16 v[8:11], a[140:143], v[92:95], v[8:11]\n"
"ds_read_b64_tr_b16 a[226:227], v242 offset:28672\n"
"v_add_f32 v44, v44, v2\n"
"v_mfma_f32_16x16x32_bf16 v[12:15], a[144:147], v[80:83], v[28:31]\n"
"s_nop 0\n"
"v_exp_f32 v7, v7\n"
"v_mfma_f32_16x16x32_bf16 v[12:15], a[148:151], v[84:87], v[12:15]\n"
"ds_read_b64_tr_b16 a[228:229], v243 offset:24576\n"
"v_cvt_pk_bf16_f32 v19, v6, v7\n"
"v_mfma_f32_16x16x32_bf16 v[12:15], a[152:155], v[88:91], v[12:15]\n"
"s_nop 0\n"
"buffer_load_dwordx4 v247, s[8:11], s84 offen offset:3072 lds\n"
"v_mfma_f32_16x16x32_bf16 v[12:15], a[156:159], v[92:95], v[12:15]\n"
"s_nop 0\n"
"v_exp_f32 v8, v8\n"
"v_mfma_f32_16x16x32_bf16 a[0:3], a[192:195], v[16:19], 0\n"
"ds_read_b64_tr_b16 a[230:231], v243 offset:28672\n"
"v_add_f32 v44, v44, v3\n"
"v_mfma_f32_16x16x32_bf16 a[4:7], a[196:199], v[16:19], 0\n"
"s_nop 0\n"
"v_exp_f32 v9, v9\n"
"v_mfma_f32_16x16x32_bf16 a[8:11], a[200:203], v[16:19], 0\n"
"ds_read_b64_tr_b16 a[232:233], v242 offset:24832\n"
"v_cvt_pk_bf16_f32 v20, v8, v9\n"
"v_mfma_f32_16x16x32_bf16 a[12:15], a[204:207], v[16:19], 0\n"
"s_nop 0\n"
"v_exp_f32 v10, v10\n"
"v_mfma_f32_16x16x32_bf16 a[16:19], a[208:211], v[16:19], 0\n"
"ds_read_b64_tr_b16 a[234:235], v242 offset:28928\n"
"v_add_f32 v44, v44, v4\n"
"v_mfma_f32_16x16x32_bf16 a[20:23], a[212:215], v[16:19], 0\n"
"s_add_u32 m0, s86, 114688\n"
"v_exp_f32 v11, v11\n"
"v_mfma_f32_16x16x32_bf16 a[24:27], a[216:219], v[16:19], 0\n"
"buffer_load_dwordx4 v246, s[12:15], s84 offen offset:0 lds\n"
"ds_read_b64_tr_b16 a[236:237], v243 offset:24832\n"
"v_cvt_pk_bf16_f32 v21, v10, v11\n"
"v_mfma_f32_16x16x32_bf16 a[28:31], a[220:223], v[16:19], 0\n"
"s_nop 0\n"
"v_exp_f32 v12, v12\n"
"v_mfma_f32_16x16x32_bf16 v[220:223], a[128:131], v[96:99], v[32:35]\n"
"ds_read_b64_tr_b16 a[238:239], v243 offset:28928\n"
"v_add_f32 v44, v44, v5\n"
"v_mfma_f32_16x16x32_bf16 v[220:223], a[132:135], v[100:103], v[220:223]\n"
"s_nop 0\n"
"v_exp_f32 v13, v13\n"
"v_mfma_f32_16x16x32_bf16 v[220:223], a[136:139], v[104:107], v[220:223]\n"
"ds_read_b64_tr_b16 a[240:241], v242 offset:25088\n"
"v_cvt_pk_bf16_f32 v22, v12, v13\n"
"v_mfma_f32_16x16x32_bf16 v[220:223], a[140:143], v[108:111], v[220:223]\n"
"s_nop 0\n"
"v_exp_f32 v14, v14\n"
"v_mfma_f32_16x16x32_bf16 v[224:227], a[144:147], v[96:99], v[32:35]\n"
"ds_read_b64_tr_b16 a[242:243], v242 offset:29184\n"
"v_add_f32 v44, v44, v6\n"
"v_mfma_f32_16x16x32_bf16 v[224:227], a[148:151], v[100:103], v[224:227]\n"
"s_nop 0\n"
"v_exp_f32 v15, v15\n"
"v_mfma_f32_16x16x32_bf16 v[224:227], a[152:155], v[104:107], v[224:227]\n"
"ds_read_b64_tr_b16 a[244:245], v243 offset:25088\n"
"v_cvt_pk_bf16_f32 v23, v14, v15\n"
"v_mfma_f32_16x16x32_bf16 v[224:227], a[156:159], v[108:111], v[224:227]\n"
"s_nop 0\n"
"v_exp_f32 v220, v220\n"
"v_mfma_f32_16x16x32_bf16 a[32:35], a[192:195], v[20:23], 0\n"
"s_nop 0\n"
"buffer_load_dwordx4 v247, s[12:15], s84 offen offset:1024 lds\n"
"v_mfma_f32_16x16x32_bf16 a[36:39], a[196:199], v[20:23], 0\n"
"s_nop 0\n"
"v_exp_f32 v221, v221\n"
"v_mfma_f32_16x16x32_bf16 a[40:43], a[200:203], v[20:23], 0\n"
"ds_read_b64_tr_b16 a[246:247], v243 offset:29184\n"
"v_cvt_pk_bf16_f32 v16, v220, v221\n"
"v_mfma_f32_16x16x32_bf16 a[44:47], a[204:207], v[20:23], 0\n"
"s_nop 0\n"
"v_exp_f32 v222, v222\n"
"v_mfma_f32_16x16x32_bf16 a[48:51], a[208:211], v[20:23], 0\n"
"ds_read_b64_tr_b16 a[248:249], v242 offset:25344\n"
"v_add_f32 v44, v44, v7\n"
"v_mfma_f32_16x16x32_bf16 a[52:55], a[212:215], v[20:23], 0\n"
"s_nop 0\n"
"v_exp_f32 v223, v223\n"
"v_mfma_f32_16x16x32_bf16 a[56:59], a[216:219], v[20:23], 0\n"
"ds_read_b64_tr_b16 a[250:251], v242 offset:29440\n"
"v_cvt_pk_bf16_f32 v17, v222, v223\n"
"v_mfma_f32_16x16x32_bf16 a[60:63], a[220:223], v[20:23], 0\n"
"s_nop 0\n"
"v_exp_f32 v224, v224\n"
"v_mfma_f32_16x16x32_bf16 v[48:51], v[40:43], v[20:23], 0\n"
"buffer_load_dwordx4 v246, s[12:15], s84 offen offset:2048 lds\n"
"ds_read_b64_tr_b16 a[252:253], v243 offset:25344\n"
"v_add_f32 v52, v52, v220\n"
"v_mfma_f32_16x16x32_bf16 v[228:231], a[128:131], v[112:115], v[36:39]\n"
"s_nop 0\n"
"v_exp_f32 v225, v225\n"
"v_mfma_f32_16x16x32_bf16 v[228:231], a[132:135], v[116:119], v[228:231]\n"
"ds_read_b64_tr_b16 a[254:255], v243 offset:29440\n"
"v_cvt_pk_bf16_f32 v18, v224, v225\n"
"v_mfma_f32_16x16x32_bf16 v[228:231], a[136:139], v[120:123], v[228:231]\n"
"s_nop 0\n"
"v_exp_f32 v226, v226\n"
"v_mfma_f32_16x16x32_bf16 v[228:231], a[140:143], v[124:127], v[228:231]\n"
"v_add_f32 v52, v52, v221\n"
"v_add_f32 v52, v52, v222\n"
"v_mfma_f32_16x16x32_bf16 v[232:235], a[144:147], v[112:115], v[36:39]\n"
"s_nop 0\n"
"v_exp_f32 v227, v227\n"
"v_mfma_f32_16x16x32_bf16 v[232:235], a[148:151], v[116:119], v[232:235]\n"
"v_cvt_pk_bf16_f32 v19, v226, v227\n"
"v_add_f32 v52, v52, v223\n"
"v_mfma_f32_16x16x32_bf16 v[232:235], a[152:155], v[120:123], v[232:235]\n"
"s_nop 0\n"
"buffer_load_dwordx4 v247, s[12:15], s84 offen offset:3072 lds\n"
"v_mfma_f32_16x16x32_bf16 v[232:235], a[156:159], v[124:127], v[232:235]\n"
"s_add_u32 s84, s84, 16384\n"
"v_exp_f32 v228, v228\n"
"v_mfma_f32_16x16x32_bf16 a[64:67], a[192:195], v[16:19], 0\n"
"v_add_f32 v52, v52, v224\n"
"v_add_f32 v52, v52, v225\n"
"v_mfma_f32_16x16x32_bf16 a[68:71], a[196:199], v[16:19], 0\n"
"s_nop 0\n"
"v_exp_f32 v229, v229\n"
"v_mfma_f32_16x16x32_bf16 a[72:75], a[200:203], v[16:19], 0\n"
"v_cvt_pk_bf16_f32 v20, v228, v229\n"
"v_add_f32 v52, v52, v226\n"
"v_mfma_f32_16x16x32_bf16 a[76:79], a[204:207], v[16:19], 0\n"
"s_waitcnt vmcnt(16) lgkmcnt(0)\n"
"v_exp_f32 v230, v230\n"
"v_mfma_f32_16x16x32_bf16 a[80:83], a[208:211], v[16:19], 0\n"
"s_barrier\n"
"v_mfma_f32_16x16x32_bf16 a[84:87], a[212:215], v[16:19], 0\n"
"s_nop 0\n"
"v_exp_f32 v231, v231\n"
"v_mfma_f32_16x16x32_bf16 a[88:91], a[216:219], v[16:19], 0\n"
"v_cvt_pk_bf16_f32 v21, v230, v231\n"
"v_add_f32 v52, v52, v227\n"
"v_mfma_f32_16x16x32_bf16 a[92:95], a[220:223], v[16:19], 0\n"
"s_nop 0\n"
"v_exp_f32 v232, v232\n"
"s_mov_b32 s87, 6\n"
".p2align 6\n"
"AT_loop:\n"
"v_mfma_f32_16x16x32_bf16 v[0:3], a[160:163], v[64:67], v[24:27]\n"
"ds_read_b128 a[128:131], v240 offset:32768\n"
"buffer_load_dword v250, v251, s[64:67], 0 offen\n"
"v_mfma_f32_16x16x32_bf16 v[0:3], a[164:167], v[68:71], v[0:3]\n"
"v_exp_f32 v233, v233\n"
"v_mfma_f32_16x16x32_bf16 v[0:3], a[168:171], v[72:75], v[0:3]\n"
"ds_read_b128 a[132:135], v240 offset:33024\n"
"v_cvt_pk_bf16_f32 v22, v232, v233\n"
"v_mfma_f32_16x16x32_bf16 v[0:3], a[172:175], v[76:79], v[0:3]\n"
"v_exp_f32 v234, v234\n"
"v_mfma_f32_16x16x32_bf16 v[4:7], a[176:179], v[64:67], v[24:27]\n"
"ds_read_b128 a[136:139], v240 offset:33280\n"
"v_mfma_f32_16x16x32_bf16 v[4:7], a[180:183], v[68:71], v[4:7]\n"
"v_exp_f32 v235, v235\n"
"v_mfma_f32_16x16x32_bf16 v[4:7], a[184:187], v[72:75], v[4:7]\n"
"ds_read_b128 a[140:143], v240 offset:33536\n"
"v_cvt_pk_bf16_f32 v23, v234, v235\n"
"v_mfma_f32_16x16x32_bf16 v[4:7], a[188:191], v[76:79], v[4:7]\n"
"s_mov_b32 m0, s86\n"
"v_exp_f32 v0, v0\n"
"v_mfma_f32_16x16x32_bf16 a[96:99], a[192:195], v[20:23], a[96:99]\n"
"buffer_load_dwordx4 v246, s[8:11], s84 offen offset:0 lds\n"
"v_mfma_f32_16x16x32_bf16 a[100:103], a[196:199], v[20:23], a[100:103]\n"
"v_exp_f32 v1, v1\n"
"v_mfma_f32_16x16x32_bf16 a[104:107], a[200:203], v[20:23], a[104:107]\n"
"ds_read_b128 a[144:147], v240 offset:36864\n"
"v_cvt_pk_bf16_f32 v16, v0, v1\n"
"v_mfma_f32_16x16x32_bf16 a[108:111], a[204:207], v[20:23], a[108:111]\n"
"v_exp_f32 v2, v2\n"
"v_mfma_f32_16x16x32_bf16 a[112:115], a[208:211], v[20:23], a[112:115]\n"
"ds_read_b128 a[148:151], v240 offset:37120\n"
"v_add_f32 v44, v44, v0\n"
"v_mfma_f32_16x16x32_bf16 a[116:119], a[212:215], v[20:23], a[116:119]\n"
"v_exp_f32 v3, v3\n"
"v_mfma_f32_16x16x32_bf16 a[120:123], a[216:219], v[20:23], a[120:123]\n"
"ds_read_b128 a[152:155], v240 offset:37376\n"
"v_cvt_pk_bf16_f32 v17, v2, v3\n"
"v_mfma_f32_16x16x32_bf16 a[124:127], a[220:223], v[20:23], a[124:127]\n"
"v_exp_f32 v4, v4\n"
"v_mfma_f32_16x16x32_bf16 v[56:59], v[40:43], v[20:23], v[56:59]\n"
"ds_read_b128 a[156:159], v240 offset:37632\n"
"v_add_f32 v44, v44, v1\n"
"v_mfma_f32_16x16x32_bf16 v[8:11], a[160:163], v[80:83], v[28:31]\n"
"v_exp_f32 v5, v5\n"
"v_mfma_f32_16x16x32_bf16 v[8:11], a[164:167], v[84:87], v[8:11]\n"
"ds_read_b64_tr_b16 a[192:193], v242 offset:49152\n"
"v_cvt_pk_bf16_f32 v18, v4, v5\n"
"v_mfma_f32_16x16x32_bf16 v[8:11], a[168:171], v[88:91], v[8:11]\n"
"v_exp_f32 v6, v6\n"
"v_mfma_f32_16x16x32_bf16 v[8:11], a[172:175], v[92:95], v[8:11]\n"
"ds_read_b64_tr_b16 a[194:195], v242 offset:53248\n"
"v_add_f32 v44, v44, v2\n"
"v_mfma_f32_16x16x32_bf16 v[12:15], a[176:179], v[80:83], v[28:31]\n"
"v_exp_f32 v7, v7\n"
"v_mfma_f32_16x16x32_bf16 v[12:15], a[180:183], v[84:87], v[12:15]\n"
"ds_read_b64_tr_b16 a[196:197], v243 offset:49152\n"
"v_cvt_pk_bf16_f32 v19, v6, v7\n"
"v_mfma_f32_16x16x32_bf16 v[12:15], a[184:187], v[88:91], v[12:15]\n"
"buffer_load_dwordx4 v246, s[8:11], s84 offen offset:1024 lds\n"
"v_mfma_f32_16x16x32_bf16 v[12:15], a[188:191], v[92:95], v[12:15]\n"
"s_waitcnt lgkmcnt(11)\n"
"v_exp_f32 v8, v8\n"
"v_mfma_f32_16x16x32_bf16 a[0:3], a[224:227], v[16:19], a[0:3]\n"
"ds_read_b64_tr_b16 a[198:199], v243 offset:53248\n"
"v_add_f32 v44, v44, v3\n"
"v_mfma_f32_16x16x32_bf16 a[4:7], a[228:231], v[16:19], a[4:7]\n"
"v_exp_f32 v9, v9\n"
"v_mfma_f32_16x16x32_bf16 a[8:11], a[232:235], v[16:19], a[8:11]\n"
"ds_read_b64_tr_b16 a[200:201], v242 offset:49408\n"
"v_cvt_pk_bf16_f32 v20, v8, v9\n"
"v_mfma_f32_16x16x32_bf16 a[12:15], a[236:239], v[16:19], a[12:15]\n"
"v_exp_f32 v10, v10\n"
"v_mfma_f32_16x16x32_bf16 a[16:19], a[240:243], v[16:19], a[16:19]\n"
"ds_read_b64_tr_b16 a[202:203], v242 offset:53504\n"
"v_add_f32 v44, v44, v4\n"
"v_mfma_f32_16x16x32_bf16 a[20:23], a[244:247], v[16:19], a[20:23]\n"
"v_exp_f32 v11, v11\n"
"v_mfma_f32_16x16x32_bf16 a[24:27], a[248:251], v[16:19], a[24:27]\n"
"ds_read_b64_tr_b16 a[204:205], v243 offset:49408\n"
"v_cvt_pk_bf16_f32 v21, v10, v11\n"
"v_mfma_f32_16x16x32_bf16 a[28:31], a[252:255], v[16:19], a[28:31]\n"
"v_exp_f32 v12, v12\n"
"v_mfma_f32_16x16x32_bf16 v[220:223], a[160:163], v[96:99], v[32:35]\n"
"ds_read_b64_tr_b16 a[206:207], v243 offset:53504\n"
"v_add_f32 v44, v44, v5\n"
"v_mfma_f32_16x16x32_bf16 v[220:223], a[164:167], v[100:103], v[220:223]\n"
"v_exp_f32 v13, v13\n"
"v_mfma_f32_16x16x32_bf16 v[220:223], a[168:171], v[104:107], v[220:223]\n"
"ds_read_b64_tr_b16 a[208:209], v242 offset:49664\n"
"v_cvt_pk_bf16_f32 v22, v12, v13\n"
"v_mfma_f32_16x16x32_bf16 v[220:223], a[172:175], v[108:111], v[220:223]\n"
"v_exp_f32 v14, v14\n"
"v_mfma_f32_16x16x32_bf16 v[224:227], a[176:179], v[96:99], v[32:35]\n"
"ds_read_b64_tr_b16 a[210:211], v242 offset:53760\n"
"v_add_f32 v44, v44, v6\n"
"v_mfma_f32_16x16x32_bf16 v[224:227], a[180:183], v[100:103], v[224:227]\n"
"v_exp_f32 v15, v15\n"
"v_mfma_f32_16x16x32_bf16 v[224:227], a[184:187], v[104:107], v[224:227]\n"
"ds_read_b64_tr_b16 a[212:213], v243 offset:49664\n"
"v_cvt_pk_bf16_f32 v23, v14, v15\n"
"v_mfma_f32_16x16x32_bf16 v[224:227], a[188:191], v[108:111], v[224:227]\n"
"v_exp_f32 v220, v220\n"
"v_mfma_f32_16x16x32_bf16 a[32:35], a[224:227], v[20:23], a[32:35]\n"
"buffer_load_dwordx4 v247, s[8:11], s84 offen offset:2048 lds\n"
"v_mfma_f32_16x16x32_bf16 a[36:39], a[228:231], v[20:23], a[36:39]\n"
"v_exp_f32 v221, v221\n"
"v_mfma_f32_16x16x32_bf16 a[40:43], a[232:235], v[20:23], a[40:43]\n"
"ds_read_b64_tr_b16 a[214:215], v243 offset:53760\n"
"v_cvt_pk_bf16_f32 v16, v220, v221\n"
"v_mfma_f32_16x16x32_bf16 a[44:47], a[236:239], v[20:23], a[44:47]\n"
"v_exp_f32 v222, v222\n"
"v_mfma_f32_16x16x32_bf16 a[48:51], a[240:243], v[20:23], a[48:51]\n"
"ds_read_b64_tr_b16 a[216:217], v242 offset:49920\n"
"v_add_f32 v44, v44, v7\n"
"v_mfma_f32_16x16x32_bf16 a[52:55], a[244:247], v[20:23], a[52:55]\n"
"v_exp_f32 v223, v223\n"
"v_mfma_f32_16x16x32_bf16 a[56:59], a[248:251], v[20:23], a[56:59]\n"
"ds_read_b64_tr_b16 a[218:219], v242 offset:54016\n"
"v_cvt_pk_bf16_f32 v17, v222, v223\n"
"v_mfma_f32_16x16x32_bf16 a[60:63], a[252:255], v[20:23], a[60:63]\n"
"v_exp_f32 v224, v224\n"
"v_mfma_f32_16x16x32_bf16 v[48:51], v[40:43], v[20:23], v[48:51]\n"
"ds_read_b64_tr_b16 a[220:221], v243 offset:49920\n"
"v_add_f32 v52, v52, v220\n"
"v_mfma_f32_16x16x32_bf16 v[228:231], a[160:163], v[112:115], v[36:39]\n"
"v_exp_f32 v225, v225\n"
"v_mfma_f32_16x16x32_bf16 v[228:231], a[164:167], v[116:119], v[228:231]\n"
"ds_read_b64_tr_b16 a[222:223], v243 offset:54016\n"
"v_cvt_pk_bf16_f32 v18, v224, v225\n"
"v_mfma_f32_16x16x32_bf16 v[228:231], a[168:171], v[120:123], v[228:231]\n"
"v_exp_f32 v226, v226\n"
"v_mfma_f32_16x16x32_bf16 v[228:231], a[172:175], v[124:127], v[228:231]\n"
"v_add_f32 v52, v52, v221\n"
"v_add_f32 v52, v52, v222\n"
"v_mfma_f32_16x16x32_bf16 v[232:235], a[176:179], v[112:115], v[36:39]\n"
"v_exp_f32 v227, v227\n"
"v_mfma_f32_16x16x32_bf16 v[232:235], a[180:183], v[116:119], v[232:235]\n"
"v_cvt_pk_bf16_f32 v19, v226, v227\n"
"v_add_f32 v52, v52, v223\n"
"v_mfma_f32_16x16x32_bf16 v[232:235], a[184:187], v[120:123], v[232:235]\n"
"buffer_load_dwordx4 v247, s[8:11], s84 offen offset:3072 lds\n"
"v_mfma_f32_16x16x32_bf16 v[232:235], a[188:191], v[124:127], v[232:235]\n"
"v_exp_f32 v228, v228\n"
"v_mfma_f32_16x16x32_bf16 a[64:67], a[224:227], v[16:19], a[64:67]\n"
"v_add_f32 v52, v52, v224\n"
"v_add_f32 v52, v52, v225\n"
"v_mfma_f32_16x16x32_bf16 a[68:71], a[228:231], v[16:19], a[68:71]\n"
"v_exp_f32 v229, v229\n"
"v_mfma_f32_16x16x32_bf16 a[72:75], a[232:235], v[16:19], a[72:75]\n"
"v_cvt_pk_bf16_f32 v20, v228, v229\n"
"v_add_f32 v52, v52, v226\n"
"v_mfma_f32_16x16x32_bf16 a[76:79], a[236:239], v[16:19], a[76:79]\n"
"v_exp_f32 v230, v230\n"
"v_mfma_f32_16x16x32_bf16 a[80:83], a[240:243], v[16:19], a[80:83]\n"
"s_waitcnt lgkmcnt(8)\n"
"buffer_load_dword v250, v255, s[32:35], 0 offen\n"
"v_mfma_f32_16x16x32_bf16 a[84:87], a[244:247], v[16:19], a[84:87]\n"
"v_exp_f32 v231, v231\n"
"v_mfma_f32_16x16x32_bf16 a[88:91], a[248:251], v[16:19], a[88:91]\n"
"v_cvt_pk_bf16_f32 v21, v230, v231\n"
"v_add_f32 v52, v52, v227\n"
"v_mfma_f32_16x16x32_bf16 a[92:95], a[252:255], v[16:19], a[92:95]\n"
"v_exp_f32 v232, v232\n"
"v_mfma_f32_16x16x32_bf16 v[0:3], a[128:131], v[64:67], v[24:27]\n"
"ds_read_b128 a[160:163], v240 offset:40960\n"
"v_mfma_f32_16x16x32_bf16 v[0:3], a[132:135], v[68:71], v[0:3]\n"
"v_exp_f32 v233, v233\n"
"v_mfma_f32_16x16x32_bf16 v[0:3], a[136:139], v[72:75], v[0:3]\n"
"ds_read_b128 a[164:167], v240 offset:41216\n"
"v_cvt_pk_bf16_f32 v22, v232, v233\n"
"v_mfma_f32_16x16x32_bf16 v[0:3], a[140:143], v[76:79], v[0:3]\n"
"v_exp_f32 v234, v234\n"
"v_mfma_f32_16x16x32_bf16 v[4:7], a[144:147], v[64:67], v[24:27]\n"
"ds_read_b128 a[168:171], v240 offset:41472\n"
"v_mfma_f32_16x16x32_bf16 v[4:7], a[148:151], v[68:71], v[4:7]\n"
"v_exp_f32 v235, v235\n"
"v_mfma_f32_16x16x32_bf16 v[4:7], a[152:155], v[72:75], v[4:7]\n"
"ds_read_b128 a[172:175], v240 offset:41728\n"
"v_cvt_pk_bf16_f32 v23, v234, v235\n"
"v_mfma_f32_16x16x32_bf16 v[4:7], a[156:159], v[76:79], v[4:7]\n"
"s_add_u32 m0, s86, 16384\n"
"v_exp_f32 v0, v0\n"
"v_mfma_f32_16x16x32_bf16 a[96:99], a[224:227], v[20:23], a[96:99]\n"
"buffer_load_dwordx4 v246, s[12:15], s84 offen offset:0 lds\n"
"v_mfma_f32_16x16x32_bf16 a[100:103], a[228:231], v[20:23], a[100:103]\n"
"v_exp_f32 v1, v1\n"
"v_mfma_f32_16x16x32_bf16 a[104:107], a[232:235], v[20:23], a[104:107]\n"
"ds_read_b128 a[176:179], v240 offset:45056\n"
"v_cvt_pk_bf16_f32 v16, v0, v1\n"
"v_mfma_f32_16x16x32_bf16 a[108:111], a[236:239], v[20:23], a[108:111]\n"
"v_exp_f32 v2, v2\n"
"v_mfma_f32_16x16x32_bf16 a[112:115], a[240:243], v[20:23], a[112:115]\n"
"ds_read_b128 a[180:183], v240 offset:45312\n"
"v_add_f32 v44, v44, v0\n"
"v_mfma_f32_16x16x32_bf16 a[116:119], a[244:247], v[20:23], a[116:119]\n"
"v_exp_f32 v3, v3\n"
"v_mfma_f32_16x16x32_bf16 a[120:123], a[248:251], v[20:23], a[120:123]\n"
"ds_read_b128 a[184:187], v240 offset:45568\n"
"v_cvt_pk_bf16_f32 v17, v2, v3\n"
"v_mfma_f32_16x16x32_bf16 a[124:127], a[252:255], v[20:23], a[124:127]\n"
"v_exp_f32 v4, v4\n"
"v_mfma_f32_16x16x32_bf16 v[56:59], v[40:43], v[20:23], v[56:59]\n"
"ds_read_b128 a[188:191], v240 offset:45824\n"
"v_add_f32 v44, v44, v1\n"
"v_mfma_f32_16x16x32_bf16 v[8:11], a[128:131], v[80:83], v[28:31]\n"
"v_exp_f32 v5, v5\n"
"v_mfma_f32_16x16x32_bf16 v[8:11], a[132:135], v[84:87], v[8:11]\n"
"ds_read_b64_tr_b16 a[224:225], v242 offset:57344\n"
"v_cvt_pk_bf16_f32 v18, v4, v5\n"
"v_mfma_f32_16x16x32_bf16 v[8:11], a[136:139], v[88:91], v[8:11]\n"
"v_exp_f32 v6, v6\n"
"v_mfma_f32_16x16x32_bf16 v[8:11], a[140:143], v[92:95], v[8:11]\n"
"ds_read_b64_tr_b16 a[226:227], v242 offset:61440\n"
"v_add_f32 v44, v44, v2\n"
"v_mfma_f32_16x16x32_bf16 v[12:15], a[144:147], v[80:83], v[28:31]\n"
"v_exp_f32 v7, v7\n"
"v_mfma_f32_16x16x32_bf16 v[12:15], a[148:151], v[84:87], v[12:15]\n"
"ds_read_b64_tr_b16 a[228:229], v243 offset:57344\n"
"v_cvt_pk_bf16_f32 v19, v6, v7\n"
"v_mfma_f32_16x16x32_bf16 v[12:15], a[152:155], v[88:91], v[12:15]\n"
"buffer_load_dwordx4 v247, s[12:15], s84 offen offset:1024 lds\n"
"v_mfma_f32_16x16x32_bf16 v[12:15], a[156:159], v[92:95], v[12:15]\n"
"s_waitcnt lgkmcnt(11)\n"
"v_exp_f32 v8, v8\n"
"v_mfma_f32_16x16x32_bf16 a[0:3], a[192:195], v[16:19], a[0:3]\n"
"ds_read_b64_tr_b16 a[230:231], v243 offset:61440\n"
"v_add_f32 v44, v44, v3\n"
"v_mfma_f32_16x16x32_bf16 a[4:7], a[196:199], v[16:19], a[4:7]\n"
"v_exp_f32 v9, v9\n"
"v_mfma_f32_16x16x32_bf16 a[8:11], a[200:203], v[16:19], a[8:11]\n"
"ds_read_b64_tr_b16 a[232:233], v242 offset:57600\n"
"v_cvt_pk_bf16_f32 v20, v8, v9\n"
"v_mfma_f32_16x16x32_bf16 a[12:15], a[204:207], v[16:19], a[12:15]\n"
"v_exp_f32 v10, v10\n"
"v_mfma_f32_16x16x32_bf16 a[16:19], a[208:211], v[16:19], a[16:19]\n"
"ds_read_b64_tr_b16 a[234:235], v242 offset:61696\n"
"v_add_f32 v44, v44, v4\n"
"v_mfma_f32_16x16x32_bf16 a[20:23], a[212:215], v[16:19], a[20:23]\n"
"v_exp_f32 v11, v11\n"
"v_mfma_f32_16x16x32_bf16 a[24:27], a[216:219], v[16:19], a[24:27]\n"
"ds_read_b64_tr_b16 a[236:237], v243 offset:57600\n"
"v_cvt_pk_bf16_f32 v21, v10, v11\n"
"v_mfma_f32_16x16x32_bf16 a[28:31], a[220:223], v[16:19], a[28:31]\n"
"v_exp_f32 v12, v12\n"
"v_mfma_f32_16x16x32_bf16 v[220:223], a[128:131], v[96:99], v[32:35]\n"
"ds_read_b64_tr_b16 a[238:239], v243 offset:61696\n"
"v_add_f32 v44, v44, v5\n"
"v_mfma_f32_16x16x32_bf16 v[220:223], a[132:135], v[100:103], v[220:223]\n"
"v_exp_f32 v13, v13\n"
"v_mfma_f32_16x16x32_bf16 v[220:223], a[136:139], v[104:107], v[220:223]\n"
"ds_read_b64_tr_b16 a[240:241], v242 offset:57856\n"
"v_cvt_pk_bf16_f32 v22, v12, v13\n"
"v_mfma_f32_16x16x32_bf16 v[220:223], a[140:143], v[108:111], v[220:223]\n"
"v_exp_f32 v14, v14\n"
"v_mfma_f32_16x16x32_bf16 v[224:227], a[144:147], v[96:99], v[32:35]\n"
"ds_read_b64_tr_b16 a[242:243], v242 offset:61952\n"
"v_add_f32 v44, v44, v6\n"
"v_mfma_f32_16x16x32_bf16 v[224:227], a[148:151], v[100:103], v[224:227]\n"
"v_exp_f32 v15, v15\n"
"v_mfma_f32_16x16x32_bf16 v[224:227], a[152:155], v[104:107], v[224:227]\n"
"ds_read_b64_tr_b16 a[244:245], v243 offset:57856\n"
"v_cvt_pk_bf16_f32 v23, v14, v15\n"
"v_mfma_f32_16x16x32_bf16 v[224:227], a[156:159], v[108:111], v[224:227]\n"
"v_exp_f32 v220, v220\n"
"v_mfma_f32_16x16x32_bf16 a[32:35], a[192:195], v[20:23], a[32:35]\n"
"buffer_load_dwordx4 v246, s[12:15], s84 offen offset:2048 lds\n"
"v_mfma_f32_16x16x32_bf16 a[36:39], a[196:199], v[20:23], a[36:39]\n"
"v_exp_f32 v221, v221\n"
"v_mfma_f32_16x16x32_bf16 a[40:43], a[200:203], v[20:23], a[40:43]\n"
"ds_read_b64_tr_b16 a[246:247], v243 offset:61952\n"
"v_cvt_pk_bf16_f32 v16, v220, v221\n"
"v_mfma_f32_16x16x32_bf16 a[44:47], a[204:207], v[20:23], a[44:47]\n"
"v_exp_f32 v222, v222\n"
"v_mfma_f32_16x16x32_bf16 a[48:51], a[208:211], v[20:23], a[48:51]\n"
"ds_read_b64_tr_b16 a[248:249], v242 offset:58112\n"
"v_add_f32 v44, v44, v7\n"
"v_mfma_f32_16x16x32_bf16 a[52:55], a[212:215], v[20:23], a[52:55]\n"
"v_exp_f32 v223, v223\n"
"v_mfma_f32_16x16x32_bf16 a[56:59], a[216:219], v[20:23], a[56:59]\n"
"ds_read_b64_tr_b16 a[250:251], v242 offset:62208\n"
"v_cvt_pk_bf16_f32 v17, v222, v223\n"
"v_mfma_f32_16x16x32_bf16 a[60:63], a[220:223], v[20:23], a[60:63]\n"
"v_exp_f32 v224, v224\n"
"v_mfma_f32_16x16x32_bf16 v[48:51], v[40:43], v[20:23], v[48:51]\n"
"ds_read_b64_tr_b16 a[252:253], v243 offset:58112\n"
"v_add_f32 v52, v52, v220\n"
"v_mfma_f32_16x16x32_bf16 v[228:231], a[128:131], v[112:115], v[36:39]\n"
"v_exp_f32 v225, v225\n"
"v_mfma_f32_16x16x32_bf16 v[228:231], a[132:135], v[116:119], v[228:231]\n"
"ds_read_b64_tr_b16 a[254:255], v243 offset:62208\n"
"v_cvt_pk_bf16_f32 v18, v224, v225\n"
"v_mfma_f32_16x16x32_bf16 v[228:231], a[136:139], v[120:123], v[228:231]\n"
"AT_pg6:\n"
"s_cbranch_execz AT_hr\n"
"v_exp_f32 v226, v226\n"
"v_mfma_f32_16x16x32_bf16 v[228:231], a[140:143], v[124:127], v[228:231]\n"
"v_add_f32 v52, v52, v221\n"
"v_add_f32 v52, v52, v222\n"
"v_mfma_f32_16x16x32_bf16 v[232:235], a[144:147], v[112:115], v[36:39]\n"
"v_exp_f32 v227, v227\n"
"v_mfma_f32_16x16x32_bf16 v[232:235], a[148:151], v[116:119], v[232:235]\n"
"v_cvt_pk_bf16_f32 v19, v226, v227\n"
"v_add_f32 v52, v52, v223\n"
"v_mfma_f32_16x16x32_bf16 v[232:235], a[152:155], v[120:123], v[232:235]\n"
"buffer_load_dwordx4 v247, s[12:15], s84 offen offset:3072 lds\n"
"v_mfma_f32_16x16x32_bf16 v[232:235], a[156:159], v[124:127], v[232:235]\n"
"s_add_u32 s84, s84, 16384\n"
"v_exp_f32 v228, v228\n"
"v_mfma_f32_16x16x32_bf16 a[64:67], a[192:195], v[16:19], a[64:67]\n"
"v_add_f32 v52, v52, v224\n"
"v_add_f32 v52, v52, v225\n"
"v_mfma_f32_16x16x32_bf16 a[68:71], a[196:199], v[16:19], a[68:71]\n"
"v_exp_f32 v229, v229\n"
"v_mfma_f32_16x16x32_bf16 a[72:75], a[200:203], v[16:19], a[72:75]\n"
"v_cvt_pk_bf16_f32 v20, v228, v229\n"
"v_add_f32 v52, v52, v226\n"
"v_mfma_f32_16x16x32_bf16 a[76:79], a[204:207], v[16:19], a[76:79]\n"
"s_waitcnt vmcnt(18) lgkmcnt(0)\n"
"v_exp_f32 v230, v230\n"
"v_mfma_f32_16x16x32_bf16 a[80:83], a[208:211], v[16:19], a[80:83]\n"
"s_barrier\n"
"v_mfma_f32_16x16x32_bf16 a[84:87], a[212:215], v[16:19], a[84:87]\n"
"v_exp_f32 v231, v231\n"
"v_mfma_f32_16x16x32_bf16 a[88:91], a[216:219], v[16:19], a[88:91]\n"
"v_cvt_pk_bf16_f32 v21, v230, v231\n"
"v_add_f32 v52, v52, v227\n"
"v_mfma_f32_16x16x32_bf16 a[92:95], a[220:223], v[16:19], a[92:95]\n"
"v_exp_f32 v232, v232\n"
"v_mfma_f32_16x16x32_bf16 v[0:3], a[160:163], v[64:67], v[24:27]\n"
"ds_read_b128 a[128:131], v241 offset:0\n"
"buffer_store_dwordx4 v[216:219], v207, s[28:31], s54 offen sc1\n"
"v_mfma_f32_16x16x32_bf16 v[0:3], a[164:167], v[68:71], v[0:3]\n"
"v_exp_f32 v233, v233\n"
"v_mfma_f32_16x16x32_bf16 v[0:3], a[168:171], v[72:75], v[0:3]\n"
"ds_read_b128 a[132:135], v241 offset:256\n"
"v_cvt_pk_bf16_f32 v22, v232, v233\n"
"v_mfma_f32_16x16x32_bf16 v[0:3], a[172:175], v[76:79], v[0:3]\n"
"v_exp_f32 v234, v234\n"
"v_mfma_f32_16x16x32_bf16 v[4:7], a[176:179], v[64:67], v[24:27]\n"
"ds_read_b128 a[136:139], v241 offset:512\n"
"v_mfma_f32_16x16x32_bf16 v[4:7], a[180:183], v[68:71], v[4:7]\n"
"v_exp_f32 v235, v235\n"
"v_mfma_f32_16x16x32_bf16 v[4:7], a[184:187], v[72:75], v[4:7]\n"
"ds_read_b128 a[140:143], v241 offset:768\n"
"v_cvt_pk_bf16_f32 v23, v234, v235\n"
"v_mfma_f32_16x16x32_bf16 v[4:7], a[188:191], v[76:79], v[4:7]\n"
"s_add_u32 m0, s86, 32768\n"
"v_exp_f32 v0, v0\n"
"v_mfma_f32_16x16x32_bf16 a[96:99], a[192:195], v[20:23], a[96:99]\n"
"buffer_load_dwordx4 v246, s[8:11], s84 offen offset:0 lds\n"
"v_mfma_f32_16x16x32_bf16 a[100:103], a[196:199], v[20:23], a[100:103]\n"
"v_exp_f32 v1, v1\n"
"v_mfma_f32_16x16x32_bf16 a[104:107], a[200:203], v[20:23], a[104:107]\n"
"ds_read_b128 a[144:147], v241 offset:4096\n"
"v_cvt_pk_bf16_f32 v16, v0, v1\n"
"v_mfma_f32_16x16x32_bf16 a[108:111], a[204:207], v[20:23], a[108:111]\n"
"v_exp_f32 v2, v2\n"
"v_mfma_f32_16x16x32_bf16 a[112:115], a[208:211], v[20:23], a[112:115]\n"
"ds_read_b128 a[148:151], v241 offset:4352\n"
"v_add_f32 v44, v44, v0\n"
"v_mfma_f32_16x16x32_bf16 a[116:119], a[212:215], v[20:23], a[116:119]\n"
"v_exp_f32 v3, v3\n"
"v_mfma_f32_16x16x32_bf16 a[120:123], a[216:219], v[20:23], a[120:123]\n"
"ds_read_b128 a[152:155], v241 offset:4608\n"
"v_cvt_pk_bf16_f32 v17, v2, v3\n"
"v_mfma_f32_16x16x32_bf16 a[124:127], a[220:223], v[20:23], a[124:127]\n"
"v_exp_f32 v4, v4\n"
"v_mfma_f32_16x16x32_bf16 v[56:59], v[40:43], v[20:23], v[56:59]\n"
"ds_read_b128 a[156:159], v241 offset:4864\n"
"v_add_f32 v44, v44, v1\n"
"v_mfma_f32_16x16x32_bf16 v[8:11], a[160:163], v[80:83], v[28:31]\n"
"v_exp_f32 v5, v5\n"
"v_mfma_f32_16x16x32_bf16 v[8:11], a[164:167], v[84:87], v[8:11]\n"
"ds_read_b64_tr_b16 a[192:193], v244 offset:16384\n"
"v_cvt_pk_bf16_f32 v18, v4, v5\n"
"v_mfma_f32_16x16x32_bf16 v[8:11], a[168:171], v[88:91], v[8:11]\n"
"v_exp_f32 v6, v6\n"
"v_mfma_f32_16x16x32_bf16 v[8:11], a[172:175], v[92:95], v[8:11]\n"
"ds_read_b64_tr_b16 a[194:195], v244 offset:20480\n"
"v_add_f32 v44, v44, v2\n"
"v_mfma_f32_16x16x32_bf16 v[12:15], a[176:179], v[80:83], v[28:31]\n"
"v_exp_f32 v7, v7\n"
"v_mfma_f32_16x16x32_bf16 v[12:15], a[180:183], v[84:87], v[12:15]\n"
"ds_read_b64_tr_b16 a[196:197], v245 offset:16384\n"
"v_cvt_pk_bf16_f32 v19, v6, v7\n"
"v_mfma_f32_16x16x32_bf16 v[12:15], a[184:187], v[88:91], v[12:15]\n"
"buffer_load_dwordx4 v246, s[8:11], s84 offen offset:1024 lds\n"
"v_mfma_f32_16x16x32_bf16 v[12:15], a[188:191], v[92:95], v[12:15]\n"
"s_waitcnt lgkmcnt(11)\n"
"v_exp_f32 v8, v8\n"
"v_mfma_f32_16x16x32_bf16 a[0:3], a[224:227], v[16:19], a[0:3]\n"
"ds_read_b64_tr_b16 a[198:199], v245 offset:20480\n"
"v_add_f32 v44, v44, v3\n"
"v_mfma_f32_16x16x32_bf16 a[4:7], a[228:231], v[16:19], a[4:7]\n"
"v_exp_f32 v9, v9\n"
"v_mfma_f32_16x16x32_bf16 a[8:11], a[232:235], v[16:19], a[8:11]\n"
"ds_read_b64_tr_b16 a[200:201], v244 offset:16640\n"
"v_cvt_pk_bf16_f32 v20, v8, v9\n"
"v_mfma_f32_16x16x32_bf16 a[12:15], a[236:239], v[16:19], a[12:15]\n"
"v_exp_f32 v10, v10\n"
"v_mfma_f32_16x16x32_bf16 a[16:19], a[240:243], v[16:19], a[16:19]\n"
"ds_read_b64_tr_b16 a[202:203], v244 offset:20736\n"
"v_add_f32 v44, v44, v4\n"
"v_mfma_f32_16x16x32_bf16 a[20:23], a[244:247], v[16:19], a[20:23]\n"
"v_exp_f32 v11, v11\n"
"v_mfma_f32_16x16x32_bf16 a[24:27], a[248:251], v[16:19], a[24:27]\n"
"ds_read_b64_tr_b16 a[204:205], v245 offset:16640\n"
"v_cvt_pk_bf16_f32 v21, v10, v11\n"
"v_mfma_f32_16x16x32_bf16 a[28:31], a[252:255], v[16:19], a[28:31]\n"
"v_exp_f32 v12, v12\n"
"v_mfma_f32_16x16x32_bf16 v[220:223], a[160:163], v[96:99], v[32:35]\n"
"ds_read_b64_tr_b16 a[206:207], v245 offset:20736\n"
"v_add_f32 v44, v44, v5\n"
"v_mfma_f32_16x16x32_bf16 v[220:223], a[164:167], v[100:103], v[220:223]\n"
"v_exp_f32 v13, v13\n"
"v_mfma_f32_16x16x32_bf16 v[220:223], a[168:171], v[104:107], v[220:223]\n"
"ds_read_b64_tr_b16 a[208:209], v244 offset:16896\n"
"v_cvt_pk_bf16_f32 v22, v12, v13\n"
"v_mfma_f32_16x16x32_bf16 v[220:223], a[172:175], v[108:111], v[220:223]\n"
"v_exp_f32 v14, v14\n"
"v_mfma_f32_16x16x32_bf16 v[224:227], a[176:179], v[96:99], v[32:35]\n"
"ds_read_b64_tr_b16 a[210:211], v244 offset:20992\n"
"v_add_f32 v44, v44, v6\n"
"v_mfma_f32_16x16x32_bf16 v[224:227], a[180:183], v[100:103], v[224:227]\n"
"v_exp_f32 v15, v15\n"
"v_mfma_f32_16x16x32_bf16 v[224:227], a[184:187], v[104:107], v[224:227]\n"
"ds_read_b64_tr_b16 a[212:213], v245 offset:16896\n"
"v_cvt_pk_bf16_f32 v23, v14, v15\n"
"v_mfma_f32_16x16x32_bf16 v[224:227], a[188:191], v[108:111], v[224:227]\n"
"v_exp_f32 v220, v220\n"
"v_mfma_f32_16x16x32_bf16 a[32:35], a[224:227], v[20:23], a[32:35]\n"
"buffer_load_dwordx4 v247, s[8:11], s84 offen offset:2048 lds\n"
"v_mfma_f32_16x16x32_bf16 a[36:39], a[228:231], v[20:23], a[36:39]\n"
"v_exp_f32 v221, v221\n"
"v_mfma_f32_16x16x32_bf16 a[40:43], a[232:235], v[20:23], a[40:43]\n"
"ds_read_b64_tr_b16 a[214:215], v245 offset:20992\n"
"v_cvt_pk_bf16_f32 v16, v220, v221\n"
"v_mfma_f32_16x16x32_bf16 a[44:47], a[236:239], v[20:23], a[44:47]\n"
"v_exp_f32 v222, v222\n"
"v_mfma_f32_16x16x32_bf16 a[48:51], a[240:243], v[20:23], a[48:51]\n"
"ds_read_b64_tr_b16 a[216:217], v244 offset:17152\n"
"v_add_f32 v44, v44, v7\n"
"v_mfma_f32_16x16x32_bf16 a[52:55], a[244:247], v[20:23], a[52:55]\n"
"v_exp_f32 v223, v223\n"
"v_mfma_f32_16x16x32_bf16 a[56:59], a[248:251], v[20:23], a[56:59]\n"
"ds_read_b64_tr_b16 a[218:219], v244 offset:21248\n"
"v_cvt_pk_bf16_f32 v17, v222, v223\n"
"v_mfma_f32_16x16x32_bf16 a[60:63], a[252:255], v[20:23], a[60:63]\n"
"v_exp_f32 v224, v224\n"
"v_mfma_f32_16x16x32_bf16 v[48:51], v[40:43], v[20:23], v[48:51]\n"
"ds_read_b64_tr_b16 a[220:221], v245 offset:17152\n"
"v_add_f32 v52, v52, v220\n"
"v_mfma_f32_16x16x32_bf16 v[228:231], a[160:163], v[112:115], v[36:39]\n"
"v_exp_f32 v225, v225\n"
"v_mfma_f32_16x16x32_bf16 v[228:231], a[164:167], v[116:119], v[228:231]\n"
"ds_read_b64_tr_b16 a[222:223], v245 offset:21248\n"
"v_cvt_pk_bf16_f32 v18, v224, v225\n"
"v_mfma_f32_16x16x32_bf16 v[228:231], a[168:171], v[120:123], v[228:231]\n"
"v_exp_f32 v226, v226\n"
"v_mfma_f32_16x16x32_bf16 v[228:231], a[172:175], v[124:127], v[228:231]\n"
"v_add_f32 v52, v52, v221\n"
"v_add_f32 v52, v52, v222\n"
"v_mfma_f32_16x16x32_bf16 v[232:235], a[176:179], v[112:115], v[36:39]\n"
"v_exp_f32 v227, v227\n"
"v_mfma_f32_16x16x32_bf16 v[232:235], a[180:183], v[116:119], v[232:235]\n"
"v_cvt_pk_bf16_f32 v19, v226, v227\n"
"v_add_f32 v52, v52, v223\n"
"v_mfma_f32_16x16x32_bf16 v[232:235], a[184:187], v[120:123], v[232:235]\n"
"buffer_load_dwordx4 v247, s[8:11], s84 offen offset:3072 lds\n"
"v_mfma_f32_16x16x32_bf16 v[232:235], a[188:191], v[124:127], v[232:235]\n"
"v_exp_f32 v228, v228\n"
"v_mfma_f32_16x16x32_bf16 a[64:67], a[224:227], v[16:19], a[64:67]\n"
"v_add_f32 v52, v52, v224\n"
"v_add_f32 v52, v52, v225\n"
"v_mfma_f32_16x16x32_bf16 a[68:71], a[228:231], v[16:19], a[68:71]\n"
"v_exp_f32 v229, v229\n"
"v_mfma_f32_16x16x32_bf16 a[72:75], a[232:235], v[16:19], a[72:75]\n"
"v_cvt_pk_bf16_f32 v20, v228, v229\n"
"v_add_f32 v52, v52, v226\n"
"v_mfma_f32_16x16x32_bf16 a[76:79], a[236:239], v[16:19], a[76:79]\n"
"v_exp_f32 v230, v230\n"
"v_mfma_f32_16x16x32_bf16 a[80:83], a[240:243], v[16:19], a[80:83]\n"
"s_waitcnt lgkmcnt(8)\n"
"buffer_load_dword v250, v251, s[68:71], 0 offen\n"
"v_mfma_f32_16x16x32_bf16 a[84:87], a[244:247], v[16:19], a[84:87]\n"
"v_exp_f32 v231, v231\n"
"v_mfma_f32_16x16x32_bf16 a[88:91], a[248:251], v[16:19], a[88:91]\n"
"v_cvt_pk_bf16_f32 v21, v230, v231\n"
"v_add_f32 v52, v52, v227\n"
"v_mfma_f32_16x16x32_bf16 a[92:95], a[252:255], v[16:19], a[92:95]\n"
"v_exp_f32 v232, v232\n"
"v_mfma_f32_16x16x32_bf16 v[0:3], a[128:131], v[64:67], v[24:27]\n"
"ds_read_b128 a[160:163], v241 offset:8192\n"
"v_add_u32 v251, 1179648, v251\n"
"v_mfma_f32_16x16x32_bf16 v[0:3], a[132:135], v[68:71], v[0:3]\n"
"v_exp_f32 v233, v233\n"
"v_mfma_f32_16x16x32_bf16 v[0:3], a[136:139], v[72:75], v[0:3]\n"
"ds_read_b128 a[164:167], v241 offset:8448\n"
"v_cvt_pk_bf16_f32 v22, v232, v233\n"
"v_mfma_f32_16x16x32_bf16 v[0:3], a[140:143], v[76:79], v[0:3]\n"
"v_exp_f32 v234, v234\n"
"v_mfma_f32_16x16x32_bf16 v[4:7], a[144:147], v[64:67], v[24:27]\n"
"ds_read_b128 a[168:171], v241 offset:8704\n"
"v_mfma_f32_16x16x32_bf16 v[4:7], a[148:151], v[68:71], v[4:7]\n"
"v_exp_f32 v235, v235\n"
"v_mfma_f32_16x16x32_bf16 v[4:7], a[152:155], v[72:75], v[4:7]\n"
"ds_read_b128 a[172:175], v241 offset:8960\n"
"v_cvt_pk_bf16_f32 v23, v234, v235\n"
"v_mfma_f32_16x16x32_bf16 v[4:7], a[156:159], v[76:79], v[4:7]\n"
"s_add_u32 m0, s86, 49152\n"
"v_exp_f32 v0, v0\n"
"v_mfma_f32_16x16x32_bf16 a[96:99], a[224:227], v[20:23], a[96:99]\n"
"buffer_load_dwordx4 v246, s[12:15], s84 offen offset:0 lds\n"
"v_mfma_f32_16x16x32_bf16 a[100:103], a[228:231], v[20:23], a[100:103]\n"
"v_exp_f32 v1, v1\n"
"v_mfma_f32_16x16x32_bf16 a[104:107], a[232:235], v[20:23], a[104:107]\n"
"ds_read_b128 a[176:179], v241 offset:12288\n"
"v_cvt_pk_bf16_f32 v16, v0, v1\n"
"v_mfma_f32_16x16x32_bf16 a[108:111], a[236:239], v[20:23], a[108:111]\n"
"v_exp_f32 v2, v2\n"
"v_mfma_f32_16x16x32_bf16 a[112:115], a[240:243], v[20:23], a[112:115]\n"
"ds_read_b128 a[180:183], v241 offset:12544\n"
"v_add_f32 v44, v44, v0\n"
"v_mfma_f32_16x16x32_bf16 a[116:119], a[244:247], v[20:23], a[116:119]\n"
"v_exp_f32 v3, v3\n"
"v_mfma_f32_16x16x32_bf16 a[120:123], a[248:251], v[20:23], a[120:123]\n"
"ds_read_b128 a[184:187], v241 offset:12800\n"
"v_cvt_pk_bf16_f32 v17, v2, v3\n"
"v_mfma_f32_16x16x32_bf16 a[124:127], a[252:255], v[20:23], a[124:127]\n"
"v_exp_f32 v4, v4\n"
"v_mfma_f32_16x16x32_bf16 v[56:59], v[40:43], v[20:23], v[56:59]\n"
"ds_read_b128 a[188:191], v241 offset:13056\n"
"v_add_f32 v44, v44, v1\n"
"v_mfma_f32_16x16x32_bf16 v[8:11], a[128:131], v[80:83], v[28:31]\n"
"v_exp_f32 v5, v5\n"
"v_mfma_f32_16x16x32_bf16 v[8:11], a[132:135], v[84:87], v[8:11]\n"
"ds_read_b64_tr_b16 a[224:225], v244 offset:24576\n"
"v_cvt_pk_bf16_f32 v18, v4, v5\n"
"v_mfma_f32_16x16x32_bf16 v[8:11], a[136:139], v[88:91], v[8:11]\n"
"v_exp_f32 v6, v6\n"
"v_mfma_f32_16x16x32_bf16 v[8:11], a[140:143], v[92:95], v[8:11]\n"
"ds_read_b64_tr_b16 a[226:227], v244 offset:28672\n"
"v_add_f32 v44, v44, v2\n"
"v_mfma_f32_16x16x32_bf16 v[12:15], a[144:147], v[80:83], v[28:31]\n"
"v_exp_f32 v7, v7\n"
"v_mfma_f32_16x16x32_bf16 v[12:15], a[148:151], v[84:87], v[12:15]\n"
"ds_read_b64_tr_b16 a[228:229], v245 offset:24576\n"
"v_cvt_pk_bf16_f32 v19, v6, v7\n"
"v_mfma_f32_16x16x32_bf16 v[12:15], a[152:155], v[88:91], v[12:15]\n"
"buffer_load_dwordx4 v247, s[12:15], s84 offen offset:1024 lds\n"
"v_mfma_f32_16x16x32_bf16 v[12:15], a[156:159], v[92:95], v[12:15]\n"
"s_waitcnt lgkmcnt(11)\n"
"v_exp_f32 v8, v8\n"
"v_mfma_f32_16x16x32_bf16 a[0:3], a[192:195], v[16:19], a[0:3]\n"
"ds_read_b64_tr_b16 a[230:231], v245 offset:28672\n"
"v_add_f32 v44, v44, v3\n"
"v_mfma_f32_16x16x32_bf16 a[4:7], a[196:199], v[16:19], a[4:7]\n"
"v_exp_f32 v9, v9\n"
"v_mfma_f32_16x16x32_bf16 a[8:11], a[200:203], v[16:19], a[8:11]\n"
"ds_read_b64_tr_b16 a[232:233], v244 offset:24832\n"
"v_cvt_pk_bf16_f32 v20, v8, v9\n"
"v_mfma_f32_16x16x32_bf16 a[12:15], a[204:207], v[16:19], a[12:15]\n"
"v_exp_f32 v10, v10\n"
"v_mfma_f32_16x16x32_bf16 a[16:19], a[208:211], v[16:19], a[16:19]\n"
"ds_read_b64_tr_b16 a[234:235], v244 offset:28928\n"
"v_add_f32 v44, v44, v4\n"
"v_mfma_f32_16x16x32_bf16 a[20:23], a[212:215], v[16:19], a[20:23]\n"
"v_exp_f32 v11, v11\n"
"v_mfma_f32_16x16x32_bf16 a[24:27], a[216:219], v[16:19], a[24:27]\n"
"ds_read_b64_tr_b16 a[236:237], v245 offset:24832\n"
"v_cvt_pk_bf16_f32 v21, v10, v11\n"
"v_mfma_f32_16x16x32_bf16 a[28:31], a[220:223], v[16:19], a[28:31]\n"
"v_exp_f32 v12, v12\n"
"v_mfma_f32_16x16x32_bf16 v[220:223], a[128:131], v[96:99], v[32:35]\n"
"ds_read_b64_tr_b16 a[238:239], v245 offset:28928\n"
"v_add_f32 v44, v44, v5\n"
"v_mfma_f32_16x16x32_bf16 v[220:223], a[132:135], v[100:103], v[220:223]\n"
"v_exp_f32 v13, v13\n"
"v_mfma_f32_16x16x32_bf16 v[220:223], a[136:139], v[104:107], v[220:223]\n"
"ds_read_b64_tr_b16 a[240:241], v244 offset:25088\n"
"v_cvt_pk_bf16_f32 v22, v12, v13\n"
"v_mfma_f32_16x16x32_bf16 v[220:223], a[140:143], v[108:111], v[220:223]\n"
"v_exp_f32 v14, v14\n"
"v_mfma_f32_16x16x32_bf16 v[224:227], a[144:147], v[96:99], v[32:35]\n"
"ds_read_b64_tr_b16 a[242:243], v244 offset:29184\n"
"v_add_f32 v44, v44, v6\n"
"v_mfma_f32_16x16x32_bf16 v[224:227], a[148:151], v[100:103], v[224:227]\n"
"v_exp_f32 v15, v15\n"
"v_mfma_f32_16x16x32_bf16 v[224:227], a[152:155], v[104:107], v[224:227]\n"
"ds_read_b64_tr_b16 a[244:245], v245 offset:25088\n"
"v_cvt_pk_bf16_f32 v23, v14, v15\n"
"v_mfma_f32_16x16x32_bf16 v[224:227], a[156:159], v[108:111], v[224:227]\n"
"v_exp_f32 v220, v220\n"
"v_mfma_f32_16x16x32_bf16 a[32:35], a[192:195], v[20:23], a[32:35]\n"
"buffer_load_dwordx4 v246, s[12:15], s84 offen offset:2048 lds\n"
"v_mfma_f32_16x16x32_bf16 a[36:39], a[196:199], v[20:23], a[36:39]\n"
"v_exp_f32 v221, v221\n"
"v_mfma_f32_16x16x32_bf16 a[40:43], a[200:203], v[20:23], a[40:43]\n"
"ds_read_b64_tr_b16 a[246:247], v245 offset:29184\n"
"v_cvt_pk_bf16_f32 v16, v220, v221\n"
"v_mfma_f32_16x16x32_bf16 a[44:47], a[204:207], v[20:23], a[44:47]\n"
"v_exp_f32 v222, v222\n"
"v_mfma_f32_16x16x32_bf16 a[48:51], a[208:211], v[20:23], a[48:51]\n"
"ds_read_b64_tr_b16 a[248:249], v244 offset:25344\n"
"v_add_f32 v44, v44, v7\n"
"v_mfma_f32_16x16x32_bf16 a[52:55], a[212:215], v[20:23], a[52:55]\n"
"v_exp_f32 v223, v223\n"
"v_mfma_f32_16x16x32_bf16 a[56:59], a[216:219], v[20:23], a[56:59]\n"
"ds_read_b64_tr_b16 a[250:251], v244 offset:29440\n"
"v_cvt_pk_bf16_f32 v17, v222, v223\n"
"v_mfma_f32_16x16x32_bf16 a[60:63], a[220:223], v[20:23], a[60:63]\n"
"v_exp_f32 v224, v224\n"
"v_mfma_f32_16x16x32_bf16 v[48:51], v[40:43], v[20:23], v[48:51]\n"
"ds_read_b64_tr_b16 a[252:253], v245 offset:25344\n"
"v_add_f32 v52, v52, v220\n"
"v_mfma_f32_16x16x32_bf16 v[228:231], a[128:131], v[112:115], v[36:39]\n"
"v_exp_f32 v225, v225\n"
"v_mfma_f32_16x16x32_bf16 v[228:231], a[132:135], v[116:119], v[228:231]\n"
"ds_read_b64_tr_b16 a[254:255], v245 offset:29440\n"
"v_cvt_pk_bf16_f32 v18, v224, v225\n"
"v_mfma_f32_16x16x32_bf16 v[228:231], a[136:139], v[120:123], v[228:231]\n"
"v_exp_f32 v226, v226\n"
"v_mfma_f32_16x16x32_bf16 v[228:231], a[140:143], v[124:127], v[228:231]\n"
"v_add_f32 v52, v52, v221\n"
"v_add_f32 v52, v52, v222\n"
"v_mfma_f32_16x16x32_bf16 v[232:235], a[144:147], v[112:115], v[36:39]\n"
"v_exp_f32 v227, v227\n"
"v_mfma_f32_16x16x32_bf16 v[232:235], a[148:151], v[116:119], v[232:235]\n"
"v_cvt_pk_bf16_f32 v19, v226, v227\n"
"v_add_f32 v52, v52, v223\n"
"v_mfma_f32_16x16x32_bf16 v[232:235], a[152:155], v[120:123], v[232:235]\n"
"buffer_load_dwordx4 v247, s[12:15], s84 offen offset:3072 lds\n"
"v_mfma_f32_16x16x32_bf16 v[232:235], a[156:159], v[124:127], v[232:235]\n"
"s_add_u32 s84, s84, 16384\n"
"v_exp_f32 v228, v228\n"
"v_mfma_f32_16x16x32_bf16 a[64:67], a[192:195], v[16:19], a[64:67]\n"
"v_add_f32 v52, v52, v224\n"
"v_add_f32 v52, v52, v225\n"
"v_mfma_f32_16x16x32_bf16 a[68:71], a[196:199], v[16:19], a[68:71]\n"
"v_exp_f32 v229, v229\n"
"v_mfma_f32_16x16x32_bf16 a[72:75], a[200:203], v[16:19], a[72:75]\n"
"v_cvt_pk_bf16_f32 v20, v228, v229\n"
"v_add_f32 v52, v52, v226\n"
"v_mfma_f32_16x16x32_bf16 a[76:79], a[204:207], v[16:19], a[76:79]\n"
"s_waitcnt vmcnt(20) lgkmcnt(0)\n"
"v_exp_f32 v230, v230\n"
"v_mfma_f32_16x16x32_bf16 a[80:83], a[208:211], v[16:19], a[80:83]\n"
"s_barrier\n"
"v_mfma_f32_16x16x32_bf16 a[84:87], a[212:215], v[16:19], a[84:87]\n"
"v_exp_f32 v231, v231\n"
"v_mfma_f32_16x16x32_bf16 a[88:91], a[216:219], v[16:19], a[88:91]\n"
"v_cvt_pk_bf16_f32 v21, v230, v231\n"
"v_add_f32 v52, v52, v227\n"
"v_mfma_f32_16x16x32_bf16 a[92:95], a[220:223], v[16:19], a[92:95]\n"
"v_exp_f32 v232, v232\n"
"v_mfma_f32_16x16x32_bf16 v[0:3], a[160:163], v[64:67], v[24:27]\n"
"ds_read_b128 a[128:131], v241 offset:32768\n"
"buffer_store_dwordx4 v[216:219], v207, s[28:31], s52 offen sc1\n"
"v_mfma_f32_16x16x32_bf16 v[0:3], a[164:167], v[68:71], v[0:3]\n"
"s_add_u32 s52, s52, 1024\n"
"v_exp_f32 v233, v233\n"
"v_mfma_f32_16x16x32_bf16 v[0:3], a[168:171], v[72:75], v[0:3]\n"
"s_min_u32 s52, s52, s53\n"
"ds_read_b128 a[132:135], v241 offset:33024\n"
"v_cvt_pk_bf16_f32 v22, v232, v233\n"
"v_mfma_f32_16x16x32_bf16 v[0:3], a[172:175], v[76:79], v[0:3]\n"
"v_exp_f32 v234, v234\n"
"v_mfma_f32_16x16x32_bf16 v[4:7], a[176:179], v[64:67], v[24:27]\n"
"ds_read_b128 a[136:139], v241 offset:33280\n"
"v_mfma_f32_16x16x32_bf16 v[4:7], a[180:183], v[68:71], v[4:7]\n"
"v_exp_f32 v235, v235\n"
"v_mfma_f32_16x16x32_bf16 v[4:7], a[184:187], v[72:75], v[4:7]\n"
"ds_read_b128 a[140:143], v241 offset:33536\n"
"v_cvt_pk_bf16_f32 v23, v234, v235\n"
"v_mfma_f32_16x16x32_bf16 v[4:7], a[188:191], v[76:79], v[4:7]\n"
"s_add_u32 m0, s86, 65536\n"
"v_exp_f32 v0, v0\n"
"v_mfma_f32_16x16x32_bf16 a[96:99], a[192:195], v[20:23], a[96:99]\n"
"buffer_load_dwordx4 v246, s[8:11], s84 offen offset:0 lds\n"
"v_mfma_f32_16x16x32_bf16 a[100:103], a[196:199], v[20:23], a[100:103]\n"
"v_exp_f32 v1, v1\n"
"v_mfma_f32_16x16x32_bf16 a[104:107], a[200:203], v[20:23], a[104:107]\n"
"ds_read_b128 a[144:147], v241 offset:36864\n"
"v_cvt_pk_bf16_f32 v16, v0, v1\n"
"v_mfma_f32_16x16x32_bf16 a[108:111], a[204:207], v[20:23], a[108:111]\n"
"v_exp_f32 v2, v2\n"
"v_mfma_f32_16x16x32_bf16 a[112:115], a[208:211], v[20:23], a[112:115]\n"
"ds_read_b128 a[148:151], v241 offset:37120\n"
"v_add_f32 v44, v44, v0\n"
"v_mfma_f32_16x16x32_bf16 a[116:119], a[212:215], v[20:23], a[116:119]\n"
"v_exp_f32 v3, v3\n"
"v_mfma_f32_16x16x32_bf16 a[120:123], a[216:219], v[20:23], a[120:123]\n"
"ds_read_b128 a[152:155], v241 offset:37376\n"
"v_cvt_pk_bf16_f32 v17, v2, v3\n"
"v_mfma_f32_16x16x32_bf16 a[124:127], a[220:223], v[20:23], a[124:127]\n"
"v_exp_f32 v4, v4\n"
"v_mfma_f32_16x16x32_bf16 v[56:59], v[40:43], v[20:23], v[56:59]\n"
"ds_read_b128 a[156:159], v241 offset:37632\n"
"v_add_f32 v44, v44, v1\n"
"v_mfma_f32_16x16x32_bf16 v[8:11], a[160:163], v[80:83], v[28:31]\n"
"v_exp_f32 v5, v5\n"
"v_mfma_f32_16x16x32_bf16 v[8:11], a[164:167], v[84:87], v[8:11]\n"
"ds_read_b64_tr_b16 a[192:193], v244 offset:49152\n"
"v_cvt_pk_bf16_f32 v18, v4, v5\n"
"v_mfma_f32_16x16x32_bf16 v[8:11], a[168:171], v[88:91], v[8:11]\n"
"v_exp_f32 v6, v6\n"
"v_mfma_f32_16x16x32_bf16 v[8:11], a[172:175], v[92:95], v[8:11]\n"
"ds_read_b64_tr_b16 a[194:195], v244 offset:53248\n"
"v_add_f32 v44, v44, v2\n"
"v_mfma_f32_16x16x32_bf16 v[12:15], a[176:179], v[80:83], v[28:31]\n"
"v_exp_f32 v7, v7\n"
"v_mfma_f32_16x16x32_bf16 v[12:15], a[180:183], v[84:87], v[12:15]\n"
"ds_read_b64_tr_b16 a[196:197], v245 offset:49152\n"
"v_cvt_pk_bf16_f32 v19, v6, v7\n"
"v_mfma_f32_16x16x32_bf16 v[12:15], a[184:187], v[88:91], v[12:15]\n"
"buffer_load_dwordx4 v246, s[8:11], s84 offen offset:1024 lds\n"
"v_mfma_f32_16x16x32_bf16 v[12:15], a[188:191], v[92:95], v[12:15]\n"
"s_waitcnt lgkmcnt(11)\n"
"v_exp_f32 v8, v8\n"
"v_mfma_f32_16x16x32_bf16 a[0:3], a[224:227], v[16:19], a[0:3]\n"
"ds_read_b64_tr_b16 a[198:199], v245 offset:53248\n"
"v_add_f32 v44, v44, v3\n"
"v_mfma_f32_16x16x32_bf16 a[4:7], a[228:231], v[16:19], a[4:7]\n"
"v_exp_f32 v9, v9\n"
"v_mfma_f32_16x16x32_bf16 a[8:11], a[232:235], v[16:19], a[8:11]\n"
"ds_read_b64_tr_b16 a[200:201], v244 offset:49408\n"
"v_cvt_pk_bf16_f32 v20, v8, v9\n"
"v_mfma_f32_16x16x32_bf16 a[12:15], a[236:239], v[16:19], a[12:15]\n"
"v_exp_f32 v10, v10\n"
"v_mfma_f32_16x16x32_bf16 a[16:19], a[240:243], v[16:19], a[16:19]\n"
"ds_read_b64_tr_b16 a[202:203], v244 offset:53504\n"
"v_add_f32 v44, v44, v4\n"
"v_mfma_f32_16x16x32_bf16 a[20:23], a[244:247], v[16:19], a[20:23]\n"
"v_exp_f32 v11, v11\n"
"v_mfma_f32_16x16x32_bf16 a[24:27], a[248:251], v[16:19], a[24:27]\n"
"ds_read_b64_tr_b16 a[204:205], v245 offset:49408\n"
"v_cvt_pk_bf16_f32 v21, v10, v11\n"
"v_mfma_f32_16x16x32_bf16 a[28:31], a[252:255], v[16:19], a[28:31]\n"
"v_exp_f32 v12, v12\n"
"v_mfma_f32_16x16x32_bf16 v[220:223], a[160:163], v[96:99], v[32:35]\n"
"ds_read_b64_tr_b16 a[206:207], v245 offset:53504\n"
"v_add_f32 v44, v44, v5\n"
"v_mfma_f32_16x16x32_bf16 v[220:223], a[164:167], v[100:103], v[220:223]\n"
"v_exp_f32 v13, v13\n"
"v_mfma_f32_16x16x32_bf16 v[220:223], a[168:171], v[104:107], v[220:223]\n"
"ds_read_b64_tr_b16 a[208:209], v244 offset:49664\n"
"v_cvt_pk_bf16_f32 v22, v12, v13\n"
"v_mfma_f32_16x16x32_bf16 v[220:223], a[172:175], v[108:111], v[220:223]\n"
"v_exp_f32 v14, v14\n"
"v_mfma_f32_16x16x32_bf16 v[224:227], a[176:179], v[96:99], v[32:35]\n"
"ds_read_b64_tr_b16 a[210:211], v244 offset:53760\n"
"v_add_f32 v44, v44, v6\n"
"v_mfma_f32_16x16x32_bf16 v[224:227], a[180:183], v[100:103], v[224:227]\n"
"v_exp_f32 v15, v15\n"
"v_mfma_f32_16x16x32_bf16 v[224:227], a[184:187], v[104:107], v[224:227]\n"
"ds_read_b64_tr_b16 a[212:213], v245 offset:49664\n"
"v_cvt_pk_bf16_f32 v23, v14, v15\n"
"v_mfma_f32_16x16x32_bf16 v[224:227], a[188:191], v[108:111], v[224:227]\n"
"v_exp_f32 v220, v220\n"
"v_mfma_f32_16x16x32_bf16 a[32:35], a[224:227], v[20:23], a[32:35]\n"
"buffer_load_dwordx4 v247, s[8:11], s84 offen offset:2048 lds\n"
"v_mfma_f32_16x16x32_bf16 a[36:39], a[228:231], v[20:23], a[36:39]\n"
"v_exp_f32 v221, v221\n"
"v_mfma_f32_16x16x32_bf16 a[40:43], a[232:235], v[20:23], a[40:43]\n"
"ds_read_b64_tr_b16 a[214:215], v245 offset:53760\n"
"v_cvt_pk_bf16_f32 v16, v220, v221\n"
"v_mfma_f32_16x16x32_bf16 a[44:47], a[236:239], v[20:23], a[44:47]\n"
"v_exp_f32 v222, v222\n"
"v_mfma_f32_16x16x32_bf16 a[48:51], a[240:243], v[20:23], a[48:51]\n"
"ds_read_b64_tr_b16 a[216:217], v244 offset:49920\n"
"v_add_f32 v44, v44, v7\n"
"v_mfma_f32_16x16x32_bf16 a[52:55], a[244:247], v[20:23], a[52:55]\n"
"v_exp_f32 v223, v223\n"
"v_mfma_f32_16x16x32_bf16 a[56:59], a[248:251], v[20:23], a[56:59]\n"
"ds_read_b64_tr_b16 a[218:219], v244 offset:54016\n"
"v_cvt_pk_bf16_f32 v17, v222, v223\n"
"v_mfma_f32_16x16x32_bf16 a[60:63], a[252:255], v[20:23], a[60:63]\n"
"v_exp_f32 v224, v224\n"
"v_mfma_f32_16x16x32_bf16 v[48:51], v[40:43], v[20:23], v[48:51]\n"
"ds_read_b64_tr_b16 a[220:221], v245 offset:49920\n"
"v_add_f32 v52, v52, v220\n"
"v_mfma_f32_16x16x32_bf16 v[228:231], a[160:163], v[112:115], v[36:39]\n"
"v_exp_f32 v225, v225\n"
"v_mfma_f32_16x16x32_bf16 v[228:231], a[164:167], v[116:119], v[228:231]\n"
"ds_read_b64_tr_b16 a[222:223], v245 offset:54016\n"
"v_cvt_pk_bf16_f32 v18, v224, v225\n"
"v_mfma_f32_16x16x32_bf16 v[228:231], a[168:171], v[120:123], v[228:231]\n"
"v_exp_f32 v226, v226\n"
"v_mfma_f32_16x16x32_bf16 v[228:231], a[172:175], v[124:127], v[228:231]\n"
"v_add_f32 v52, v52, v221\n"
"v_add_f32 v52, v52, v222\n"
"v_mfma_f32_16x16x32_bf16 v[232:235], a[176:179], v[112:115], v[36:39]\n"
"v_exp_f32 v227, v227\n"
"v_mfma_f32_16x16x32_bf16 v[232:235], a[180:183], v[116:119], v[232:235]\n"
"v_cvt_pk_bf16_f32 v19, v226, v227\n"
"v_add_f32 v52, v52, v223\n"
"v_mfma_f32_16x16x32_bf16 v[232:235], a[184:187], v[120:123], v[232:235]\n"
"buffer_load_dwordx4 v247, s[8:11], s84 offen offset:3072 lds\n"
"v_mfma_f32_16x16x32_bf16 v[232:235], a[188:191], v[124:127], v[232:235]\n"
"v_exp_f32 v228, v228\n"
"v_mfma_f32_16x16x32_bf16 a[64:67], a[224:227], v[16:19], a[64:67]\n"
"v_add_f32 v52, v52, v224\n"
"v_add_f32 v52, v52, v225\n"
"v_mfma_f32_16x16x32_bf16 a[68:71], a[228:231], v[16:19], a[68:71]\n"
"v_exp_f32 v229, v229\n"
"v_mfma_f32_16x16x32_bf16 a[72:75], a[232:235], v[16:19], a[72:75]\n"
"v_cvt_pk_bf16_f32 v20, v228, v229\n"
"v_add_f32 v52, v52, v226\n"
"v_mfma_f32_16x16x32_bf16 a[76:79], a[236:239], v[16:19], a[76:79]\n"
"v_exp_f32 v230, v230\n"
"v_mfma_f32_16x16x32_bf16 a[80:83], a[240:243], v[16:19], a[80:83]\n"
"s_waitcnt lgkmcnt(8)\n"
"buffer_load_dword v250, v252, s[72:75], s46 offen\n"
"v_mfma_f32_16x16x32_bf16 a[84:87], a[244:247], v[16:19], a[84:87]\n"
"v_exp_f32 v231, v231\n"
"v_mfma_f32_16x16x32_bf16 a[88:91], a[248:251], v[16:19], a[88:91]\n"
"v_cvt_pk_bf16_f32 v21, v230, v231\n"
"v_add_f32 v52, v52, v227\n"
"v_mfma_f32_16x16x32_bf16 a[92:95], a[252:255], v[16:19], a[92:95]\n"
"v_exp_f32 v232, v232\n"
"v_mfma_f32_16x16x32_bf16 v[0:3], a[128:131], v[64:67], v[24:27]\n"
"ds_read_b128 a[160:163], v241 offset:40960\n"
"v_add_u32 v252, 147456, v252\n"
"v_mfma_f32_16x16x32_bf16 v[0:3], a[132:135], v[68:71], v[0:3]\n"
"v_exp_f32 v233, v233\n"
"v_mfma_f32_16x16x32_bf16 v[0:3], a[136:139], v[72:75], v[0:3]\n"
"ds_read_b128 a[164:167], v241 offset:41216\n"
"v_cvt_pk_bf16_f32 v22, v232, v233\n"
"v_mfma_f32_16x16x32_bf16 v[0:3], a[140:143], v[76:79], v[0:3]\n"
"v_exp_f32 v234, v234\n"
"v_mfma_f32_16x16x32_bf16 v[4:7], a[144:147], v[64:67], v[24:27]\n"
"ds_read_b128 a[168:171], v241 offset:41472\n"
"v_mfma_f32_16x16x32_bf16 v[4:7], a[148:151], v[68:71], v[4:7]\n"
"v_exp_f32 v235, v235\n"
"v_mfma_f32_16x16x32_bf16 v[4:7], a[152:155], v[72:75], v[4:7]\n"
"ds_read_b128 a[172:175], v241 offset:41728\n"
"v_cvt_pk_bf16_f32 v23, v234, v235\n"
"v_mfma_f32_16x16x32_bf16 v[4:7], a[156:159], v[76:79], v[4:7]\n"
"s_add_u32 m0, s86, 81920\n"
"v_exp_f32 v0, v0\n"
"v_mfma_f32_16x16x32_bf16 a[96:99], a[224:227], v[20:23], a[96:99]\n"
"buffer_load_dwordx4 v246, s[12:15], s84 offen offset:0 lds\n"
"v_mfma_f32_16x16x32_bf16 a[100:103], a[228:231], v[20:23], a[100:103]\n"
"v_exp_f32 v1, v1\n"
"v_mfma_f32_16x16x32_bf16 a[104:107], a[232:235], v[20:23], a[104:107]\n"
"ds_read_b128 a[176:179], v241 offset:45056\n"
"v_cvt_pk_bf16_f32 v16, v0, v1\n"
"v_mfma_f32_16x16x32_bf16 a[108:111], a[236:239], v[20:23], a[108:111]\n"
"v_exp_f32 v2, v2\n"
"v_mfma_f32_16x16x32_bf16 a[112:115], a[240:243], v[20:23], a[112:115]\n"
"ds_read_b128 a[180:183], v241 offset:45312\n"
"v_add_f32 v44, v44, v0\n"
"v_mfma_f32_16x16x32_bf16 a[116:119], a[244:247], v[20:23], a[116:119]\n"
"v_exp_f32 v3, v3\n"
"v_mfma_f32_16x16x32_bf16 a[120:123], a[248:251], v[20:23], a[120:123]\n"
"ds_read_b128 a[184:187], v241 offset:45568\n"
"v_cvt_pk_bf16_f32 v17, v2, v3\n"
"v_mfma_f32_16x16x32_bf16 a[124:127], a[252:255], v[20:23], a[124:127]\n"
"v_exp_f32 v4, v4\n"
"v_mfma_f32_16x16x32_bf16 v[56:59], v[40:43], v[20:23], v[56:59]\n"
"ds_read_b128 a[188:191], v241 offset:45824\n"
"v_add_f32 v44, v44, v1\n"
"v_mfma_f32_16x16x32_bf16 v[8:11], a[128:131], v[80:83], v[28:31]\n"
"v_exp_f32 v5, v5\n"
"v_mfma_f32_16x16x32_bf16 v[8:11], a[132:135], v[84:87], v[8:11]\n"
"ds_read_b64_tr_b16 a[224:225], v244 offset:57344\n"
"v_cvt_pk_bf16_f32 v18, v4, v5\n"
"v_mfma_f32_16x16x32_bf16 v[8:11], a[136:139], v[88:91], v[8:11]\n"
"v_exp_f32 v6, v6\n"
"v_mfma_f32_16x16x32_bf16 v[8:11], a[140:143], v[92:95], v[8:11]\n"
"ds_read_b64_tr_b16 a[226:227], v244 offset:61440\n"
"v_add_f32 v44, v44, v2\n"
"v_mfma_f32_16x16x32_bf16 v[12:15], a[144:147], v[80:83], v[28:31]\n"
"v_exp_f32 v7, v7\n"
"v_mfma_f32_16x16x32_bf16 v[12:15], a[148:151], v[84:87], v[12:15]\n"
"ds_read_b64_tr_b16 a[228:229], v245 offset:57344\n"
"v_cvt_pk_bf16_f32 v19, v6, v7\n"
"v_mfma_f32_16x16x32_bf16 v[12:15], a[152:155], v[88:91], v[12:15]\n"
"buffer_load_dwordx4 v247, s[12:15], s84 offen offset:1024 lds\n"
"v_mfma_f32_16x16x32_bf16 v[12:15], a[156:159], v[92:95], v[12:15]\n"
"s_waitcnt lgkmcnt(11)\n"
"v_exp_f32 v8, v8\n"
"v_mfma_f32_16x16x32_bf16 a[0:3], a[192:195], v[16:19], a[0:3]\n"
"ds_read_b64_tr_b16 a[230:231], v245 offset:61440\n"
"v_add_f32 v44, v44, v3\n"
"v_mfma_f32_16x16x32_bf16 a[4:7], a[196:199], v[16:19], a[4:7]\n"
"v_exp_f32 v9, v9\n"
"v_mfma_f32_16x16x32_bf16 a[8:11], a[200:203], v[16:19], a[8:11]\n"
"ds_read_b64_tr_b16 a[232:233], v244 offset:57600\n"
"v_cvt_pk_bf16_f32 v20, v8, v9\n"
"v_mfma_f32_16x16x32_bf16 a[12:15], a[204:207], v[16:19], a[12:15]\n"
"v_exp_f32 v10, v10\n"
"v_mfma_f32_16x16x32_bf16 a[16:19], a[208:211], v[16:19], a[16:19]\n"
"ds_read_b64_tr_b16 a[234:235], v244 offset:61696\n"
"v_add_f32 v44, v44, v4\n"
"v_mfma_f32_16x16x32_bf16 a[20:23], a[212:215], v[16:19], a[20:23]\n"
"v_exp_f32 v11, v11\n"
"v_mfma_f32_16x16x32_bf16 a[24:27], a[216:219], v[16:19], a[24:27]\n"
"ds_read_b64_tr_b16 a[236:237], v245 offset:57600\n"
"v_cvt_pk_bf16_f32 v21, v10, v11\n"
"v_mfma_f32_16x16x32_bf16 a[28:31], a[220:223], v[16:19], a[28:31]\n"
"AT_pg7:\n"
"s_cbranch_execz AT_hr\n"
"v_exp_f32 v12, v12\n"
"v_mfma_f32_16x16x32_bf16 v[220:223], a[128:131], v[96:99], v[32:35]\n"
"ds_read_b64_tr_b16 a[238:239], v245 offset:61696\n"
"v_add_f32 v44, v44, v5\n"
"v_mfma_f32_16x16x32_bf16 v[220:223], a[132:135], v[100:103], v[220:223]\n"
"v_exp_f32 v13, v13\n"
"v_mfma_f32_16x16x32_bf16 v[220:223], a[136:139], v[104:107], v[220:223]\n"
"ds_read_b64_tr_b16 a[240:241], v244 offset:57856\n"
"v_cvt_pk_bf16_f32 v22, v12, v13\n"
"v_mfma_f32_16x16x32_bf16 v[220:223], a[140:143], v[108:111], v[220:223]\n"
"v_exp_f32 v14, v14\n"
"v_mfma_f32_16x16x32_bf16 v[224:227], a[144:147], v[96:99], v[32:35]\n"
"ds_read_b64_tr_b16 a[242:243], v244 offset:61952\n"
"v_add_f32 v44, v44, v6\n"
"v_mfma_f32_16x16x32_bf16 v[224:227], a[148:151], v[100:103], v[224:227]\n"
"v_exp_f32 v15, v15\n"
"v_mfma_f32_16x16x32_bf16 v[224:227], a[152:155], v[104:107], v[224:227]\n"
"ds_read_b64_tr_b16 a[244:245], v245 offset:57856\n"
"v_cvt_pk_bf16_f32 v23, v14, v15\n"
"v_mfma_f32_16x16x32_bf16 v[224:227], a[156:159], v[108:111], v[224:227]\n"
"v_exp_f32 v220, v220\n"
"v_mfma_f32_16x16x32_bf16 a[32:35], a[192:195], v[20:23], a[32:35]\n"
"buffer_load_dwordx4 v246, s[12:15], s84 offen offset:2048 lds\n"
"v_mfma_f32_16x16x32_bf16 a[36:39], a[196:199], v[20:23], a[36:39]\n"
"v_exp_f32 v221, v221\n"
"v_mfma_f32_16x16x32_bf16 a[40:43], a[200:203], v[20:23], a[40:43]\n"
"ds_read_b64_tr_b16 a[246:247], v245 offset:61952\n"
"v_cvt_pk_bf16_f32 v16, v220, v221\n"
"v_mfma_f32_16x16x32_bf16 a[44:47], a[204:207], v[20:23], a[44:47]\n"
"v_exp_f32 v222, v222\n"
"v_mfma_f32_16x16x32_bf16 a[48:51], a[208:211], v[20:23], a[48:51]\n"
"ds_read_b64_tr_b16 a[248:249], v244 offset:58112\n"
"v_add_f32 v44, v44, v7\n"
"v_mfma_f32_16x16x32_bf16 a[52:55], a[212:215], v[20:23], a[52:55]\n"
"v_exp_f32 v223, v223\n"
"v_mfma_f32_16x16x32_bf16 a[56:59], a[216:219], v[20:23], a[56:59]\n"
"ds_read_b64_tr_b16 a[250:251], v244 offset:62208\n"
"v_cvt_pk_bf16_f32 v17, v222, v223\n"
"v_mfma_f32_16x16x32_bf16 a[60:63], a[220:223], v[20:23], a[60:63]\n"
"v_exp_f32 v224, v224\n"
"v_mfma_f32_16x16x32_bf16 v[48:51], v[40:43], v[20:23], v[48:51]\n"
"ds_read_b64_tr_b16 a[252:253], v245 offset:58112\n"
"v_add_f32 v52, v52, v220\n"
"v_mfma_f32_16x16x32_bf16 v[228:231], a[128:131], v[112:115], v[36:39]\n"
"v_exp_f32 v225, v225\n"
"v_mfma_f32_16x16x32_bf16 v[228:231], a[132:135], v[116:119], v[228:231]\n"
"ds_read_b64_tr_b16 a[254:255], v245 offset:62208\n"
"v_cvt_pk_bf16_f32 v18, v224, v225\n"
"v_mfma_f32_16x16x32_bf16 v[228:231], a[136:139], v[120:123], v[228:231]\n"
"v_exp_f32 v226, v226\n"
"v_mfma_f32_16x16x32_bf16 v[228:231], a[140:143], v[124:127], v[228:231]\n"
"v_add_f32 v52, v52, v221\n"
"v_add_f32 v52, v52, v222\n"
"v_mfma_f32_16x16x32_bf16 v[232:235], a[144:147], v[112:115], v[36:39]\n"
"v_exp_f32 v227, v227\n"
"v_mfma_f32_16x16x32_bf16 v[232:235], a[148:151], v[116:119], v[232:235]\n"
"v_cvt_pk_bf16_f32 v19, v226, v227\n"
"v_add_f32 v52, v52, v223\n"
"v_mfma_f32_16x16x32_bf16 v[232:235], a[152:155], v[120:123], v[232:235]\n"
"buffer_load_dwordx4 v247, s[12:15], s84 offen offset:3072 lds\n"
"v_mfma_f32_16x16x32_bf16 v[232:235], a[156:159], v[124:127], v[232:235]\n"
"s_add_u32 s84, s84, 16384\n"
"v_exp_f32 v228, v228\n"
"v_mfma_f32_16x16x32_bf16 a[64:67], a[192:195], v[16:19], a[64:67]\n"
"v_add_f32 v52, v52, v224\n"
"v_add_f32 v52, v52, v225\n"
"v_mfma_f32_16x16x32_bf16 a[68:71], a[196:199], v[16:19], a[68:71]\n"
"v_exp_f32 v229, v229\n"
"v_mfma_f32_16x16x32_bf16 a[72:75], a[200:203], v[16:19], a[72:75]\n"
"v_cvt_pk_bf16_f32 v20, v228, v229\n"
"v_add_f32 v52, v52, v226\n"
"v_mfma_f32_16x16x32_bf16 a[76:79], a[204:207], v[16:19], a[76:79]\n"
"s_waitcnt vmcnt(20) lgkmcnt(0)\n"
"v_exp_f32 v230, v230\n"
"v_mfma_f32_16x16x32_bf16 a[80:83], a[208:211], v[16:19], a[80:83]\n"
"s_barrier\n"
"v_mfma_f32_16x16x32_bf16 a[84:87], a[212:215], v[16:19], a[84:87]\n"
"v_exp_f32 v231, v231\n"
"v_mfma_f32_16x16x32_bf16 a[88:91], a[216:219], v[16:19], a[88:91]\n"
"v_cvt_pk_bf16_f32 v21, v230, v231\n"
"v_add_f32 v52, v52, v227\n"
"v_mfma_f32_16x16x32_bf16 a[92:95], a[220:223], v[16:19], a[92:95]\n"
"v_exp_f32 v232, v232\n"
"v_mfma_f32_16x16x32_bf16 v[0:3], a[160:163], v[64:67], v[24:27]\n"
"ds_read_b128 a[128:131], v240 offset:0\n"
"buffer_store_dwordx4 v[216:219], v208, s[28:31], s54 offen sc1\n"
"v_mfma_f32_16x16x32_bf16 v[0:3], a[164:167], v[68:71], v[0:3]\n"
"s_add_u32 s54, s54, 1024\n"
"v_exp_f32 v233, v233\n"
"v_mfma_f32_16x16x32_bf16 v[0:3], a[168:171], v[72:75], v[0:3]\n"
"s_min_u32 s54, s54, s49\n"
"ds_read_b128 a[132:135], v240 offset:256\n"
"v_cvt_pk_bf16_f32 v22, v232, v233\n"
"v_mfma_f32_16x16x32_bf16 v[0:3], a[172:175], v[76:79], v[0:3]\n"
"v_exp_f32 v234, v234\n"
"v_mfma_f32_16x16x32_bf16 v[4:7], a[176:179], v[64:67], v[24:27]\n"
"ds_read_b128 a[136:139], v240 offset:512\n"
"v_mfma_f32_16x16x32_bf16 v[4:7], a[180:183], v[68:71], v[4:7]\n"
"v_exp_f32 v235, v235\n"
"v_mfma_f32_16x16x32_bf16 v[4:7], a[184:187], v[72:75], v[4:7]\n"
"ds_read_b128 a[140:143], v240 offset:768\n"
"v_cvt_pk_bf16_f32 v23, v234, v235\n"
"v_mfma_f32_16x16x32_bf16 v[4:7], a[188:191], v[76:79], v[4:7]\n"
"s_add_u32 m0, s86, 98304\n"
"v_exp_f32 v0, v0\n"
"v_mfma_f32_16x16x32_bf16 a[96:99], a[192:195], v[20:23], a[96:99]\n"
"buffer_load_dwordx4 v246, s[8:11], s84 offen offset:0 lds\n"
"v_mfma_f32_16x16x32_bf16 a[100:103], a[196:199], v[20:23], a[100:103]\n"
"v_exp_f32 v1, v1\n"
"v_mfma_f32_16x16x32_bf16 a[104:107], a[200:203], v[20:23], a[104:107]\n"
"ds_read_b128 a[144:147], v240 offset:4096\n"
"v_cvt_pk_bf16_f32 v16, v0, v1\n"
"v_mfma_f32_16x16x32_bf16 a[108:111], a[204:207], v[20:23], a[108:111]\n"
"v_exp_f32 v2, v2\n"
"v_mfma_f32_16x16x32_bf16 a[112:115], a[208:211], v[20:23], a[112:115]\n"
"ds_read_b128 a[148:151], v240 offset:4352\n"
"v_add_f32 v44, v44, v0\n"
"v_mfma_f32_16x16x32_bf16 a[116:119], a[212:215], v[20:23], a[116:119]\n"
"v_exp_f32 v3, v3\n"
"v_mfma_f32_16x16x32_bf16 a[120:123], a[216:219], v[20:23], a[120:123]\n"
"ds_read_b128 a[152:155], v240 offset:4608\n"
"v_cvt_pk_bf16_f32 v17, v2, v3\n"
"v_mfma_f32_16x16x32_bf16 a[124:127], a[220:223], v[20:23], a[124:127]\n"
"v_exp_f32 v4, v4\n"
"v_mfma_f32_16x16x32_bf16 v[56:59], v[40:43], v[20:23], v[56:59]\n"
"ds_read_b128 a[156:159], v240 offset:4864\n"
"v_add_f32 v44, v44, v1\n"
"v_mfma_f32_16x16x32_bf16 v[8:11], a[160:163], v[80:83], v[28:31]\n"
"v_exp_f32 v5, v5\n"
"v_mfma_f32_16x16x32_bf16 v[8:11], a[164:167], v[84:87], v[8:11]\n"
"ds_read_b64_tr_b16 a[192:193], v242 offset:16384\n"
"v_cvt_pk_bf16_f32 v18, v4, v5\n"
"v_mfma_f32_16x16x32_bf16 v[8:11], a[168:171], v[88:91], v[8:11]\n"
"v_exp_f32 v6, v6\n"
"v_mfma_f32_16x16x32_bf16 v[8:11], a[172:175], v[92:95], v[8:11]\n"
"ds_read_b64_tr_b16 a[194:195], v242 offset:20480\n"
"v_add_f32 v44, v44, v2\n"
"v_mfma_f32_16x16x32_bf16 v[12:15], a[176:179], v[80:83], v[28:31]\n"
"v_exp_f32 v7, v7\n"
"v_mfma_f32_16x16x32_bf16 v[12:15], a[180:183], v[84:87], v[12:15]\n"
"ds_read_b64_tr_b16 a[196:197], v243 offset:16384\n"
"v_cvt_pk_bf16_f32 v19, v6, v7\n"
"v_mfma_f32_16x16x32_bf16 v[12:15], a[184:187], v[88:91], v[12:15]\n"
"buffer_load_dwordx4 v246, s[8:11], s84 offen offset:1024 lds\n"
"v_mfma_f32_16x16x32_bf16 v[12:15], a[188:191], v[92:95], v[12:15]\n"
"s_waitcnt lgkmcnt(11)\n"
"v_exp_f32 v8, v8\n"
"v_mfma_f32_16x16x32_bf16 a[0:3], a[224:227], v[16:19], a[0:3]\n"
"ds_read_b64_tr_b16 a[198:199], v243 offset:20480\n"
"v_add_f32 v44, v44, v3\n"
"v_mfma_f32_16x16x32_bf16 a[4:7], a[228:231], v[16:19], a[4:7]\n"
"v_exp_f32 v9, v9\n"
"v_mfma_f32_16x16x32_bf16 a[8:11], a[232:235], v[16:19], a[8:11]\n"
"ds_read_b64_tr_b16 a[200:201], v242 offset:16640\n"
"v_cvt_pk_bf16_f32 v20, v8, v9\n"
"v_mfma_f32_16x16x32_bf16 a[12:15], a[236:239], v[16:19], a[12:15]\n"
"v_exp_f32 v10, v10\n"
"v_mfma_f32_16x16x32_bf16 a[16:19], a[240:243], v[16:19], a[16:19]\n"
"ds_read_b64_tr_b16 a[202:203], v242 offset:20736\n"
"v_add_f32 v44, v44, v4\n"
"v_mfma_f32_16x16x32_bf16 a[20:23], a[244:247], v[16:19], a[20:23]\n"
"v_exp_f32 v11, v11\n"
"v_mfma_f32_16x16x32_bf16 a[24:27], a[248:251], v[16:19], a[24:27]\n"
"ds_read_b64_tr_b16 a[204:205], v243 offset:16640\n"
"v_cvt_pk_bf16_f32 v21, v10, v11\n"
"v_mfma_f32_16x16x32_bf16 a[28:31], a[252:255], v[16:19], a[28:31]\n"
"v_exp_f32 v12, v12\n"
"v_mfma_f32_16x16x32_bf16 v[220:223], a[160:163], v[96:99], v[32:35]\n"
"ds_read_b64_tr_b16 a[206:207], v243 offset:20736\n"
"v_add_f32 v44, v44, v5\n"
"v_mfma_f32_16x16x32_bf16 v[220:223], a[164:167], v[100:103], v[220:223]\n"
"v_exp_f32 v13, v13\n"
"v_mfma_f32_16x16x32_bf16 v[220:223], a[168:171], v[104:107], v[220:223]\n"
"ds_read_b64_tr_b16 a[208:209], v242 offset:16896\n"
"v_cvt_pk_bf16_f32 v22, v12, v13\n"
"v_mfma_f32_16x16x32_bf16 v[220:223], a[172:175], v[108:111], v[220:223]\n"
"v_exp_f32 v14, v14\n"
"v_mfma_f32_16x16x32_bf16 v[224:227], a[176:179], v[96:99], v[32:35]\n"
"ds_read_b64_tr_b16 a[210:211], v242 offset:20992\n"
"v_add_f32 v44, v44, v6\n"
"v_mfma_f32_16x16x32_bf16 v[224:227], a[180:183], v[100:103], v[224:227]\n"
"v_exp_f32 v15, v15\n"
"v_mfma_f32_16x16x32_bf16 v[224:227], a[184:187], v[104:107], v[224:227]\n"
"ds_read_b64_tr_b16 a[212:213], v243 offset:16896\n"
"v_cvt_pk_bf16_f32 v23, v14, v15\n"
"v_mfma_f32_16x16x32_bf16 v[224:227], a[188:191], v[108:111], v[224:227]\n"
"v_exp_f32 v220, v220\n"
"v_mfma_f32_16x16x32_bf16 a[32:35], a[224:227], v[20:23], a[32:35]\n"
"buffer_load_dwordx4 v247, s[8:11], s84 offen offset:2048 lds\n"
"v_mfma_f32_16x16x32_bf16 a[36:39], a[228:231], v[20:23], a[36:39]\n"
"v_exp_f32 v221, v221\n"
"v_mfma_f32_16x16x32_bf16 a[40:43], a[232:235], v[20:23], a[40:43]\n"
"ds_read_b64_tr_b16 a[214:215], v243 offset:20992\n"
"v_cvt_pk_bf16_f32 v16, v220, v221\n"
"v_mfma_f32_16x16x32_bf16 a[44:47], a[236:239], v[20:23], a[44:47]\n"
"v_exp_f32 v222, v222\n"
"v_mfma_f32_16x16x32_bf16 a[48:51], a[240:243], v[20:23], a[48:51]\n"
"ds_read_b64_tr_b16 a[216:217], v242 offset:17152\n"
"v_add_f32 v44, v44, v7\n"
"v_mfma_f32_16x16x32_bf16 a[52:55], a[244:247], v[20:23], a[52:55]\n"
"v_exp_f32 v223, v223\n"
"v_mfma_f32_16x16x32_bf16 a[56:59], a[248:251], v[20:23], a[56:59]\n"
"ds_read_b64_tr_b16 a[218:219], v242 offset:21248\n"
"v_cvt_pk_bf16_f32 v17, v222, v223\n"
"v_mfma_f32_16x16x32_bf16 a[60:63], a[252:255], v[20:23], a[60:63]\n"
"v_exp_f32 v224, v224\n"
"v_mfma_f32_16x16x32_bf16 v[48:51], v[40:43], v[20:23], v[48:51]\n"
"ds_read_b64_tr_b16 a[220:221], v243 offset:17152\n"
"v_add_f32 v52, v52, v220\n"
"v_mfma_f32_16x16x32_bf16 v[228:231], a[160:163], v[112:115], v[36:39]\n"
"v_exp_f32 v225, v225\n"
"v_mfma_f32_16x16x32_bf16 v[228:231], a[164:167], v[116:119], v[228:231]\n"
"ds_read_b64_tr_b16 a[222:223], v243 offset:21248\n"
"v_cvt_pk_bf16_f32 v18, v224, v225\n"
"v_mfma_f32_16x16x32_bf16 v[228:231], a[168:171], v[120:123], v[228:231]\n"
"v_exp_f32 v226, v226\n"
"v_mfma_f32_16x16x32_bf16 v[228:231], a[172:175], v[124:127], v[228:231]\n"
"v_add_f32 v52, v52, v221\n"
"v_add_f32 v52, v52, v222\n"
"v_mfma_f32_16x16x32_bf16 v[232:235], a[176:179], v[112:115], v[36:39]\n"
"v_exp_f32 v227, v227\n"
"v_mfma_f32_16x16x32_bf16 v[232:235], a[180:183], v[116:119], v[232:235]\n"
"v_cvt_pk_bf16_f32 v19, v226, v227\n"
"v_add_f32 v52, v52, v223\n"
"v_mfma_f32_16x16x32_bf16 v[232:235], a[184:187], v[120:123], v[232:235]\n"
"buffer_load_dwordx4 v247, s[8:11], s84 offen offset:3072 lds\n"
"v_mfma_f32_16x16x32_bf16 v[232:235], a[188:191], v[124:127], v[232:235]\n"
"v_exp_f32 v228, v228\n"
"v_mfma_f32_16x16x32_bf16 a[64:67], a[224:227], v[16:19], a[64:67]\n"
"v_add_f32 v52, v52, v224\n"
"v_add_f32 v52, v52, v225\n"
"v_mfma_f32_16x16x32_bf16 a[68:71], a[228:231], v[16:19], a[68:71]\n"
"v_exp_f32 v229, v229\n"
"v_mfma_f32_16x16x32_bf16 a[72:75], a[232:235], v[16:19], a[72:75]\n"
"v_cvt_pk_bf16_f32 v20, v228, v229\n"
"v_add_f32 v52, v52, v226\n"
"v_mfma_f32_16x16x32_bf16 a[76:79], a[236:239], v[16:19], a[76:79]\n"
"v_exp_f32 v230, v230\n"
"v_mfma_f32_16x16x32_bf16 a[80:83], a[240:243], v[16:19], a[80:83]\n"
"s_waitcnt lgkmcnt(8)\n"
"buffer_load_dword v250, v253, s[24:27], s47 offen\n"
"v_mfma_f32_16x16x32_bf16 a[84:87], a[244:247], v[16:19], a[84:87]\n"
"v_exp_f32 v231, v231\n"
"v_mfma_f32_16x16x32_bf16 a[88:91], a[248:251], v[16:19], a[88:91]\n"
"v_cvt_pk_bf16_f32 v21, v230, v231\n"
"v_add_f32 v52, v52, v227\n"
"v_mfma_f32_16x16x32_bf16 a[92:95], a[252:255], v[16:19], a[92:95]\n"
"v_exp_f32 v232, v232\n"
"v_mfma_f32_16x16x32_bf16 v[0:3], a[128:131], v[64:67], v[24:27]\n"
"ds_read_b128 a[160:163], v240 offset:8192\n"
"v_add_u32 v253, 294912, v253\n"
"v_mfma_f32_16x16x32_bf16 v[0:3], a[132:135], v[68:71], v[0:3]\n"
"v_exp_f32 v233, v233\n"
"v_mfma_f32_16x16x32_bf16 v[0:3], a[136:139], v[72:75], v[0:3]\n"
"ds_read_b128 a[164:167], v240 offset:8448\n"
"v_cvt_pk_bf16_f32 v22, v232, v233\n"
"v_mfma_f32_16x16x32_bf16 v[0:3], a[140:143], v[76:79], v[0:3]\n"
"v_exp_f32 v234, v234\n"
"v_mfma_f32_16x16x32_bf16 v[4:7], a[144:147], v[64:67], v[24:27]\n"
"ds_read_b128 a[168:171], v240 offset:8704\n"
"v_mfma_f32_16x16x32_bf16 v[4:7], a[148:151], v[68:71], v[4:7]\n"
"v_exp_f32 v235, v235\n"
"v_mfma_f32_16x16x32_bf16 v[4:7], a[152:155], v[72:75], v[4:7]\n"
"ds_read_b128 a[172:175], v240 offset:8960\n"
"v_cvt_pk_bf16_f32 v23, v234, v235\n"
"v_mfma_f32_16x16x32_bf16 v[4:7], a[156:159], v[76:79], v[4:7]\n"
"s_add_u32 m0, s86, 114688\n"
"v_exp_f32 v0, v0\n"
"v_mfma_f32_16x16x32_bf16 a[96:99], a[224:227], v[20:23], a[96:99]\n"
"buffer_load_dwordx4 v246, s[12:15], s84 offen offset:0 lds\n"
"v_mfma_f32_16x16x32_bf16 a[100:103], a[228:231], v[20:23], a[100:103]\n"
"v_exp_f32 v1, v1\n"
"v_mfma_f32_16x16x32_bf16 a[104:107], a[232:235], v[20:23], a[104:107]\n"
"ds_read_b128 a[176:179], v240 offset:12288\n"
"v_cvt_pk_bf16_f32 v16, v0, v1\n"
"v_mfma_f32_16x16x32_bf16 a[108:111], a[236:239], v[20:23], a[108:111]\n"
"v_exp_f32 v2, v2\n"
"v_mfma_f32_16x16x32_bf16 a[112:115], a[240:243], v[20:23], a[112:115]\n"
"ds_read_b128 a[180:183], v240 offset:12544\n"
"v_add_f32 v44, v44, v0\n"
"v_mfma_f32_16x16x32_bf16 a[116:119], a[244:247], v[20:23], a[116:119]\n"
"v_exp_f32 v3, v3\n"
"v_mfma_f32_16x16x32_bf16 a[120:123], a[248:251], v[20:23], a[120:123]\n"
"ds_read_b128 a[184:187], v240 offset:12800\n"
"v_cvt_pk_bf16_f32 v17, v2, v3\n"
"v_mfma_f32_16x16x32_bf16 a[124:127], a[252:255], v[20:23], a[124:127]\n"
"v_exp_f32 v4, v4\n"
"v_mfma_f32_16x16x32_bf16 v[56:59], v[40:43], v[20:23], v[56:59]\n"
"ds_read_b128 a[188:191], v240 offset:13056\n"
"v_add_f32 v44, v44, v1\n"
"v_mfma_f32_16x16x32_bf16 v[8:11], a[128:131], v[80:83], v[28:31]\n"
"v_exp_f32 v5, v5\n"
"v_mfma_f32_16x16x32_bf16 v[8:11], a[132:135], v[84:87], v[8:11]\n"
"ds_read_b64_tr_b16 a[224:225], v242 offset:24576\n"
"v_cvt_pk_bf16_f32 v18, v4, v5\n"
"v_mfma_f32_16x16x32_bf16 v[8:11], a[136:139], v[88:91], v[8:11]\n"
"v_exp_f32 v6, v6\n"
"v_mfma_f32_16x16x32_bf16 v[8:11], a[140:143], v[92:95], v[8:11]\n"
"ds_read_b64_tr_b16 a[226:227], v242 offset:28672\n"
"v_add_f32 v44, v44, v2\n"
"v_mfma_f32_16x16x32_bf16 v[12:15], a[144:147], v[80:83], v[28:31]\n"
"v_exp_f32 v7, v7\n"
"v_mfma_f32_16x16x32_bf16 v[12:15], a[148:151], v[84:87], v[12:15]\n"
"ds_read_b64_tr_b16 a[228:229], v243 offset:24576\n"
"v_cvt_pk_bf16_f32 v19, v6, v7\n"
"v_mfma_f32_16x16x32_bf16 v[12:15], a[152:155], v[88:91], v[12:15]\n"
"buffer_load_dwordx4 v247, s[12:15], s84 offen offset:1024 lds\n"
"v_mfma_f32_16x16x32_bf16 v[12:15], a[156:159], v[92:95], v[12:15]\n"
"s_waitcnt lgkmcnt(11)\n"
"v_exp_f32 v8, v8\n"
"v_mfma_f32_16x16x32_bf16 a[0:3], a[192:195], v[16:19], a[0:3]\n"
"ds_read_b64_tr_b16 a[230:231], v243 offset:28672\n"
"v_add_f32 v44, v44, v3\n"
"v_mfma_f32_16x16x32_bf16 a[4:7], a[196:199], v[16:19], a[4:7]\n"
"v_exp_f32 v9, v9\n"
"v_mfma_f32_16x16x32_bf16 a[8:11], a[200:203], v[16:19], a[8:11]\n"
"ds_read_b64_tr_b16 a[232:233], v242 offset:24832\n"
"v_cvt_pk_bf16_f32 v20, v8, v9\n"
"v_mfma_f32_16x16x32_bf16 a[12:15], a[204:207], v[16:19], a[12:15]\n"
"v_exp_f32 v10, v10\n"
"v_mfma_f32_16x16x32_bf16 a[16:19], a[208:211], v[16:19], a[16:19]\n"
"ds_read_b64_tr_b16 a[234:235], v242 offset:28928\n"
"v_add_f32 v44, v44, v4\n"
"v_mfma_f32_16x16x32_bf16 a[20:23], a[212:215], v[16:19], a[20:23]\n"
"v_exp_f32 v11, v11\n"
"v_mfma_f32_16x16x32_bf16 a[24:27], a[216:219], v[16:19], a[24:27]\n"
"ds_read_b64_tr_b16 a[236:237], v243 offset:24832\n"
"v_cvt_pk_bf16_f32 v21, v10, v11\n"
"v_mfma_f32_16x16x32_bf16 a[28:31], a[220:223], v[16:19], a[28:31]\n"
"v_exp_f32 v12, v12\n"
"v_mfma_f32_16x16x32_bf16 v[220:223], a[128:131], v[96:99], v[32:35]\n"
"ds_read_b64_tr_b16 a[238:239], v243 offset:28928\n"
"v_add_f32 v44, v44, v5\n"
"v_mfma_f32_16x16x32_bf16 v[220:223], a[132:135], v[100:103], v[220:223]\n"
"v_exp_f32 v13, v13\n"
"v_mfma_f32_16x16x32_bf16 v[220:223], a[136:139], v[104:107], v[220:223]\n"
"ds_read_b64_tr_b16 a[240:241], v242 offset:25088\n"
"v_cvt_pk_bf16_f32 v22, v12, v13\n"
"v_mfma_f32_16x16x32_bf16 v[220:223], a[140:143], v[108:111], v[220:223]\n"
"v_exp_f32 v14, v14\n"
"v_mfma_f32_16x16x32_bf16 v[224:227], a[144:147], v[96:99], v[32:35]\n"
"ds_read_b64_tr_b16 a[242:243], v242 offset:29184\n"
"v_add_f32 v44, v44, v6\n"
"v_mfma_f32_16x16x32_bf16 v[224:227], a[148:151], v[100:103], v[224:227]\n"
"v_exp_f32 v15, v15\n"
"v_mfma_f32_16x16x32_bf16 v[224:227], a[152:155], v[104:107], v[224:227]\n"
"ds_read_b64_tr_b16 a[244:245], v243 offset:25088\n"
"v_cvt_pk_bf16_f32 v23, v14, v15\n"
"v_mfma_f32_16x16x32_bf16 v[224:227], a[156:159], v[108:111], v[224:227]\n"
"v_exp_f32 v220, v220\n"
"v_mfma_f32_16x16x32_bf16 a[32:35], a[192:195], v[20:23], a[32:35]\n"
"buffer_load_dwordx4 v246, s[12:15], s84 offen offset:2048 lds\n"
"v_mfma_f32_16x16x32_bf16 a[36:39], a[196:199], v[20:23], a[36:39]\n"
"v_exp_f32 v221, v221\n"
"v_mfma_f32_16x16x32_bf16 a[40:43], a[200:203], v[20:23], a[40:43]\n"
"ds_read_b64_tr_b16 a[246:247], v243 offset:29184\n"
"v_cvt_pk_bf16_f32 v16, v220, v221\n"
"v_mfma_f32_16x16x32_bf16 a[44:47], a[204:207], v[20:23], a[44:47]\n"
"v_exp_f32 v222, v222\n"
"v_mfma_f32_16x16x32_bf16 a[48:51], a[208:211], v[20:23], a[48:51]\n"
"ds_read_b64_tr_b16 a[248:249], v242 offset:25344\n"
"v_add_f32 v44, v44, v7\n"
"v_mfma_f32_16x16x32_bf16 a[52:55], a[212:215], v[20:23], a[52:55]\n"
"v_exp_f32 v223, v223\n"
"v_mfma_f32_16x16x32_bf16 a[56:59], a[216:219], v[20:23], a[56:59]\n"
"ds_read_b64_tr_b16 a[250:251], v242 offset:29440\n"
"v_cvt_pk_bf16_f32 v17, v222, v223\n"
"v_mfma_f32_16x16x32_bf16 a[60:63], a[220:223], v[20:23], a[60:63]\n"
"v_exp_f32 v224, v224\n"
"v_mfma_f32_16x16x32_bf16 v[48:51], v[40:43], v[20:23], v[48:51]\n"
"ds_read_b64_tr_b16 a[252:253], v243 offset:25344\n"
"v_add_f32 v52, v52, v220\n"
"v_mfma_f32_16x16x32_bf16 v[228:231], a[128:131], v[112:115], v[36:39]\n"
"v_exp_f32 v225, v225\n"
"v_mfma_f32_16x16x32_bf16 v[228:231], a[132:135], v[116:119], v[228:231]\n"
"ds_read_b64_tr_b16 a[254:255], v243 offset:29440\n"
"v_cvt_pk_bf16_f32 v18, v224, v225\n"
"v_mfma_f32_16x16x32_bf16 v[228:231], a[136:139], v[120:123], v[228:231]\n"
"v_exp_f32 v226, v226\n"
"v_mfma_f32_16x16x32_bf16 v[228:231], a[140:143], v[124:127], v[228:231]\n"
"v_add_f32 v52, v52, v221\n"
"v_add_f32 v52, v52, v222\n"
"v_mfma_f32_16x16x32_bf16 v[232:235], a[144:147], v[112:115], v[36:39]\n"
"v_exp_f32 v227, v227\n"
"v_mfma_f32_16x16x32_bf16 v[232:235], a[148:151], v[116:119], v[232:235]\n"
"v_cvt_pk_bf16_f32 v19, v226, v227\n"
"v_add_f32 v52, v52, v223\n"
"v_mfma_f32_16x16x32_bf16 v[232:235], a[152:155], v[120:123], v[232:235]\n"
"buffer_load_dwordx4 v247, s[12:15], s84 offen offset:3072 lds\n"
"v_mfma_f32_16x16x32_bf16 v[232:235], a[156:159], v[124:127], v[232:235]\n"
"s_add_u32 s84, s84, 16384\n"
"v_exp_f32 v228, v228\n"
"v_mfma_f32_16x16x32_bf16 a[64:67], a[192:195], v[16:19], a[64:67]\n"
"s_sub_u32 s87, s87, 1\n"
"v_add_f32 v52, v52, v224\n"
"v_add_f32 v52, v52, v225\n"
"v_mfma_f32_16x16x32_bf16 a[68:71], a[196:199], v[16:19], a[68:71]\n"
"s_cmp_lg_u32 s87, 0\n"
"v_exp_f32 v229, v229\n"
"v_mfma_f32_16x16x32_bf16 a[72:75], a[200:203], v[16:19], a[72:75]\n"
"v_cvt_pk_bf16_f32 v20, v228, v229\n"
"v_add_f32 v52, v52, v226\n"
"v_mfma_f32_16x16x32_bf16 a[76:79], a[204:207], v[16:19], a[76:79]\n"
"s_waitcnt vmcnt(20) lgkmcnt(0)\n"
"v_exp_f32 v230, v230\n"
"v_mfma_f32_16x16x32_bf16 a[80:83], a[208:211], v[16:19], a[80:83]\n"
"s_barrier\n"
"v_mfma_f32_16x16x32_bf16 a[84:87], a[212:215], v[16:19], a[84:87]\n"
"v_exp_f32 v231, v231\n"
"v_mfma_f32_16x16x32_bf16 a[88:91], a[216:219], v[16:19], a[88:91]\n"
"v_cvt_pk_bf16_f32 v21, v230, v231\n"
"v_add_f32 v52, v52, v227\n"
"v_mfma_f32_16x16x32_bf16 a[92:95], a[220:223], v[16:19], a[92:95]\n"
"v_exp_f32 v232, v232\n"
"s_cbranch_scc1 AT_loop\n"
"s_cmp_lg_u32 s56, 0\n"
"s_cbranch_scc1 AT_h3\n"
"s_mov_b32 s61, 0x80000\n"
"AT_h2:\n"
"v_xor_b32 v210, s85, v210\n"
"v_xor_b32 v211, s85, v211\n"
"v_xor_b32 v212, s85, v212\n"
"v_xor_b32 v213, s85, v213\n"
"v_or3_b32 v210, v210, v211, v212\n"
"v_or_b32 v210, v210, v213\n"
"v_cmp_ne_u32 vcc, 0, v210\n"
"s_sub_u32 s61, s61, 1\n"
"s_cbranch_vccz AT_h4\n"
"s_cmp_eq_u32 s61, 0\n"
"s_cbranch_scc1 AT_h4\n"
"s_sleep 2\n"
"buffer_load_dword v210, v214, s[28:31], s57 offen offset:4 sc1\n"
"buffer_load_dword v211, v214, s[28:31], s58 offen offset:4 sc1\n"
"buffer_load_dword v212, v214, s[28:31], s59 offen offset:4 sc1\n"
"buffer_load_dword v213, v214, s[28:31], s60 offen offset:4 sc1\n"
"s_waitcnt vmcnt(0)\n"
"s_branch AT_h2\n"
"AT_h4:\n"
"v_mov_b32 v210, s85\n"
"v_mov_b32 v211, 65536\n"
"s_mov_b64 exec, 1\n"
"buffer_store_dword v210, v211, s[28:31], 0 offen sc1\n"
"s_mov_b64 exec, -1\n"
"AT_h3:\n"
"v_mfma_f32_16x16x32_bf16 v[0:3], a[160:163], v[64:67], v[24:27]\n"
"ds_read_b128 a[128:131], v240 offset:32768\n"
"buffer_load_dword v250, v255, s[32:35], 0 offen\n"
"v_mfma_f32_16x16x32_bf16 v[0:3], a[164:167], v[68:71], v[0:3]\n"
"v_exp_f32 v233, v233\n"
"v_mfma_f32_16x16x32_bf16 v[0:3], a[168:171], v[72:75], v[0:3]\n"
"ds_read_b128 a[132:135], v240 offset:33024\n"
"v_cvt_pk_bf16_f32 v22, v232, v233\n"
"v_mfma_f32_16x16x32_bf16 v[0:3], a[172:175], v[76:79], v[0:3]\n"
"v_exp_f32 v234, v234\n"
"v_mfma_f32_16x16x32_bf16 v[4:7], a[176:179], v[64:67], v[24:27]\n"
"ds_read_b128 a[136:139], v240 offset:33280\n"
"v_lshlrev_b32 v216, 6, v248\n"
"v_mfma_f32_16x16x32_bf16 v[4:7], a[180:183], v[68:71], v[4:7]\n"
"s_and_b32 s48, s42, 7\n"
"v_exp_f32 v235, v235\n"
"v_mfma_f32_16x16x32_bf16 v[4:7], a[184:187], v[72:75], v[4:7]\n"
"s_mul_i32 s48, s48, 576\n"
"ds_read_b128 a[140:143], v240 offset:33536\n"
"v_cvt_pk_bf16_f32 v23, v234, v235\n"
"v_mfma_f32_16x16x32_bf16 v[4:7], a[188:191], v[76:79], v[4:7]\n"
"s_mov_b32 m0, s86\n"
"v_exp_f32 v0, v0\n"
"v_mfma_f32_16x16x32_bf16 a[96:99], a[192:195], v[20:23], a[96:99]\n"
"s_lshr_b32 s62, s42, 3\n"
"buffer_load_dwordx4 v246, s[8:11], s84 offen offset:0 lds\n"
"v_mfma_f32_16x16x32_bf16 a[100:103], a[196:199], v[20:23], a[100:103]\n"
"s_mul_i32 s62, s62, 24\n"
"v_exp_f32 v1, v1\n"
"v_mfma_f32_16x16x32_bf16 a[104:107], a[200:203], v[20:23], a[104:107]\n"
"s_add_u32 s48, s48, s62\n"
"ds_read_b128 a[144:147], v240 offset:36864\n"
"v_cvt_pk_bf16_f32 v16, v0, v1\n"
"v_mfma_f32_16x16x32_bf16 a[108:111], a[204:207], v[20:23], a[108:111]\n"
"s_cmp_lt_u32 s42, 192\n"
"s_cselect_b32 s74, s74, 0\n"
"s_cselect_b32 s66, s66, 0\n"
"s_cselect_b32 s70, s70, 0\n"
"v_exp_f32 v2, v2\n"
"v_mfma_f32_16x16x32_bf16 a[112:115], a[208:211], v[20:23], a[112:115]\n"
"s_mul_i32 s50, s48, 1152\n"
"ds_read_b128 a[148:151], v240 offset:37120\n"
"v_add_f32 v44, v44, v0\n"
"v_mfma_f32_16x16x32_bf16 a[116:119], a[212:215], v[20:23], a[116:119]\n"
"s_lshl_b32 s62, s41, 13\n"
"v_exp_f32 v3, v3\n"
"v_mfma_f32_16x16x32_bf16 a[120:123], a[216:219], v[20:23], a[120:123]\n"
"s_add_u32 s46, s50, s62\n"
"ds_read_b128 a[152:155], v240 offset:37376\n"
"v_cvt_pk_bf16_f32 v17, v2, v3\n"
"v_mfma_f32_16x16x32_bf16 a[124:127], a[220:223], v[20:23], a[124:127]\n"
"s_add_u32 s47, s46, 4096\n"
"v_exp_f32 v4, v4\n"
"v_mfma_f32_16x16x32_bf16 v[56:59], v[40:43], v[20:23], v[56:59]\n"
"ds_read_b128 a[156:159], v240 offset:37632\n"
"v_add_f32 v44, v44, v1\n"
"v_mfma_f32_16x16x32_bf16 v[8:11], a[160:163], v[80:83], v[28:31]\n"
"v_exp_f32 v5, v5\n"
"v_mfma_f32_16x16x32_bf16 v[8:11], a[164:167], v[84:87], v[8:11]\n"
"ds_read_b64_tr_b16 a[192:193], v242 offset:49152\n"
"v_cvt_pk_bf16_f32 v18, v4, v5\n"
"v_mfma_f32_16x16x32_bf16 v[8:11], a[168:171], v[88:91], v[8:11]\n"
"v_exp_f32 v6, v6\n"
"v_mfma_f32_16x16x32_bf16 v[8:11], a[172:175], v[92:95], v[8:11]\n"
"ds_read_b64_tr_b16 a[194:195], v242 offset:53248\n"
"v_add_f32 v44, v44, v2\n"
"v_mfma_f32_16x16x32_bf16 v[12:15], a[176:179], v[80:83], v[28:31]\n"
"v_exp_f32 v7, v7\n"
"v_mfma_f32_16x16x32_bf16 v[12:15], a[180:183], v[84:87], v[12:15]\n"
"ds_read_b64_tr_b16 a[196:197], v243 offset:49152\n"
"v_cvt_pk_bf16_f32 v19, v6, v7\n"
"v_mfma_f32_16x16x32_bf16 v[12:15], a[184:187], v[88:91], v[12:15]\n"
"buffer_load_dwordx4 v246, s[8:11], s84 offen offset:1024 lds\n"
"v_mfma_f32_16x16x32_bf16 v[12:15], a[188:191], v[92:95], v[12:15]\n"
"s_waitcnt lgkmcnt(11)\n"
"v_exp_f32 v8, v8\n"
"v_mfma_f32_16x16x32_bf16 a[0:3], a[224:227], v[16:19], a[0:3]\n"
"ds_read_b64_tr_b16 a[198:199], v243 offset:53248\n"
"v_add_f32 v44, v44, v3\n"
"v_mfma_f32_16x16x32_bf16 a[4:7], a[228:231], v[16:19], a[4:7]\n"
"v_exp_f32 v9, v9\n"
"v_mfma_f32_16x16x32_bf16 a[8:11], a[232:235], v[16:19], a[8:11]\n"
"ds_read_b64_tr_b16 a[200:201], v242 offset:49408\n"
"v_cvt_pk_bf16_f32 v20, v8, v9\n"
"v_mfma_f32_16x16x32_bf16 a[12:15], a[236:239], v[16:19], a[12:15]\n"
"v_exp_f32 v10, v10\n"
"v_mfma_f32_16x16x32_bf16 a[16:19], a[240:243], v[16:19], a[16:19]\n"
"ds_read_b64_tr_b16 a[202:203], v242 offset:53504\n"
"buffer_load_dword v250, v216, s[72:75], s46 offen\n"
"v_add_f32 v44, v44, v4\n"
"v_mfma_f32_16x16x32_bf16 a[20:23], a[244:247], v[16:19], a[20:23]\n"
"v_exp_f32 v11, v11\n"
"v_mfma_f32_16x16x32_bf16 a[24:27], a[248:251], v[16:19], a[24:27]\n"
"ds_read_b64_tr_b16 a[204:205], v243 offset:49408\n"
"v_cvt_pk_bf16_f32 v21, v10, v11\n"
"v_mfma_f32_16x16x32_bf16 a[28:31], a[252:255], v[16:19], a[28:31]\n"
"v_exp_f32 v12, v12\n"
"v_mfma_f32_16x16x32_bf16 v[220:223], a[160:163], v[96:99], v[32:35]\n"
"ds_read_b64_tr_b16 a[206:207], v243 offset:53504\n"
"v_add_f32 v44, v44, v5\n"
"v_mfma_f32_16x16x32_bf16 v[220:223], a[164:167], v[100:103], v[220:223]\n"
"v_exp_f32 v13, v13\n"
"v_mfma_f32_16x16x32_bf16 v[220:223], a[168:171], v[104:107], v[220:223]\n"
"ds_read_b64_tr_b16 a[208:209], v242 offset:49664\n"
"v_cvt_pk_bf16_f32 v22, v12, v13\n"
"v_mfma_f32_16x16x32_bf16 v[220:223], a[172:175], v[108:111], v[220:223]\n"
"v_exp_f32 v14, v14\n"
"v_mfma_f32_16x16x32_bf16 v[224:227], a[176:179], v[96:99], v[32:35]\n"
"ds_read_b64_tr_b16 a[210:211], v242 offset:53760\n"
"v_add_f32 v44, v44, v6\n"
"v_mfma_f32_16x16x32_bf16 v[224:227], a[180:183], v[100:103], v[224:227]\n"
"v_exp_f32 v15, v15\n"
"v_mfma_f32_16x16x32_bf16 v[224:227], a[184:187], v[104:107], v[224:227]\n"
"ds_read_b64_tr_b16 a[212:213], v243 offset:49664\n"
"v_cvt_pk_bf16_f32 v23, v14, v15\n"
"v_mfma_f32_16x16x32_bf16 v[224:227], a[188:191], v[108:111], v[224:227]\n"
"v_exp_f32 v220, v220\n"
"v_mfma_f32_16x16x32_bf16 a[32:35], a[224:227], v[20:23], a[32:35]\n"
"buffer_load_dwordx4 v247, s[8:11], s84 offen offset:2048 lds\n"
"v_mfma_f32_16x16x32_bf16 a[36:39], a[228:231], v[20:23], a[36:39]\n"
"v_exp_f32 v221, v221\n"
"v_mfma_f32_16x16x32_bf16 a[40:43], a[232:235], v[20:23], a[40:43]\n"
"ds_read_b64_tr_b16 a[214:215], v243 offset:53760\n"
"v_cvt_pk_bf16_f32 v16, v220, v221\n"
"v_mfma_f32_16x16x32_bf16 a[44:47], a[236:239], v[20:23], a[44:47]\n"
"v_exp_f32 v222, v222\n"
"v_mfma_f32_16x16x32_bf16 a[48:51], a[240:243], v[20:23], a[48:51]\n"
"ds_read_b64_tr_b16 a[216:217], v242 offset:49920\n"
"v_add_f32 v44, v44, v7\n"
"v_mfma_f32_16x16x32_bf16 a[52:55], a[244:247], v[20:23], a[52:55]\n"
"v_exp_f32 v223, v223\n"
"v_mfma_f32_16x16x32_bf16 a[56:59], a[248:251], v[20:23], a[56:59]\n"
"ds_read_b64_tr_b16 a[218:219], v242 offset:54016\n"
"v_cvt_pk_bf16_f32 v17, v222, v223\n"
"v_mfma_f32_16x16x32_bf16 a[60:63], a[252:255], v[20:23], a[60:63]\n"
"v_exp_f32 v224, v224\n"
"v_mfma_f32_16x16x32_bf16 v[48:51], v[40:43], v[20:23], v[48:51]\n"
"ds_read_b64_tr_b16 a[220:221], v243 offset:49920\n"
"v_add_f32 v52, v52, v220\n"
"v_mfma_f32_16x16x32_bf16 v[228:231], a[160:163], v[112:115], v[36:39]\n"
"v_exp_f32 v225, v225\n"
"v_mfma_f32_16x16x32_bf16 v[228:231], a[164:167], v[116:119], v[228:231]\n"
"ds_read_b64_tr_b16 a[222:223], v243 offset:54016\n"
"v_cvt_pk_bf16_f32 v18, v224, v225\n"
"v_mfma_f32_16x16x32_bf16 v[228:231], a[168:171], v[120:123], v[228:231]\n"
"v_exp_f32 v226, v226\n"
"v_mfma_f32_16x16x32_bf16 v[228:231], a[172:175], v[124:127], v[228:231]\n"
"v_add_f32 v52, v52, v221\n"
"v_add_f32 v52, v52, v222\n"
"v_mfma_f32_16x16x32_bf16 v[232:235], a[176:179], v[112:115], v[36:39]\n"
"v_exp_f32 v227, v227\n"
"v_mfma_f32_16x16x32_bf16 v[232:235], a[180:183], v[116:119], v[232:235]\n"
"v_cvt_pk_bf16_f32 v19, v226, v227\n"
"v_add_f32 v52, v52, v223\n"
"v_mfma_f32_16x16x32_bf16 v[232:235], a[184:187], v[120:123], v[232:235]\n"
"buffer_load_dwordx4 v247, s[8:11], s84 offen offset:3072 lds\n"
"v_mfma_f32_16x16x32_bf16 v[232:235], a[188:191], v[124:127], v[232:235]\n"
"v_exp_f32 v228, v228\n"
"v_mfma_f32_16x16x32_bf16 a[64:67], a[224:227], v[16:19], a[64:67]\n"
"buffer_load_dword v250, v216, s[72:75], s47 offen\n"
"v_add_f32 v52, v52, v224\n"
"v_add_f32 v52, v52, v225\n"
"v_mfma_f32_16x16x32_bf16 a[68:71], a[228:231], v[16:19], a[68:71]\n"
"v_exp_f32 v229, v229\n"
"v_mfma_f32_16x16x32_bf16 a[72:75], a[232:235], v[16:19], a[72:75]\n"
"v_cvt_pk_bf16_f32 v20, v228, v229\n"
"v_add_f32 v52, v52, v226\n"
"v_mfma_f32_16x16x32_bf16 a[76:79], a[236:239], v[16:19], a[76:79]\n"
"v_exp_f32 v230, v230\n"
"v_mfma_f32_16x16x32_bf16 a[80:83], a[240:243], v[16:19], a[80:83]\n"
"s_waitcnt lgkmcnt(8)\n"
"v_lshrrev_b32 v251, 2, v248\n"
"v_mfma_f32_16x16x32_bf16 a[84:87], a[244:247], v[16:19], a[84:87]\n"
"v_exp_f32 v231, v231\n"
"v_mfma_f32_16x16x32_bf16 a[88:91], a[248:251], v[16:19], a[88:91]\n"
"AT_pg8:\n"
"s_cbranch_execz AT_hr\n"
"v_cvt_pk_bf16_f32 v21, v230, v231\n"
"v_add_f32 v52, v52, v227\n"
"v_mfma_f32_16x16x32_bf16 a[92:95], a[252:255], v[16:19], a[92:95]\n"
"v_exp_f32 v232, v232\n"
"v_mfma_f32_16x16x32_bf16 v[0:3], a[128:131], v[64:67], v[24:27]\n"
"ds_read_b128 a[160:163], v240 offset:40960\n"
"v_mul_u32_u24 v251, 9216, v251\n"
"v_mfma_f32_16x16x32_bf16 v[0:3], a[132:135], v[68:71], v[0:3]\n"
"v_exp_f32 v233, v233\n"
"v_mfma_f32_16x16x32_bf16 v[0:3], a[136:139], v[72:75], v[0:3]\n"
"ds_read_b128 a[164:167], v240 offset:41216\n"
"v_cvt_pk_bf16_f32 v22, v232, v233\n"
"v_mfma_f32_16x16x32_bf16 v[0:3], a[140:143], v[76:79], v[0:3]\n"
"v_exp_f32 v234, v234\n"
"v_mfma_f32_16x16x32_bf16 v[4:7], a[144:147], v[64:67], v[24:27]\n"
"ds_read_b128 a[168:171], v240 offset:41472\n"
"v_and_b32 v217, 3, v248\n"
"v_mfma_f32_16x16x32_bf16 v[4:7], a[148:151], v[68:71], v[4:7]\n"
"v_exp_f32 v235, v235\n"
"v_mfma_f32_16x16x32_bf16 v[4:7], a[152:155], v[72:75], v[4:7]\n"
"ds_read_b128 a[172:175], v240 offset:41728\n"
"v_cvt_pk_bf16_f32 v23, v234, v235\n"
"v_mfma_f32_16x16x32_bf16 v[4:7], a[156:159], v[76:79], v[4:7]\n"
"s_add_u32 m0, s86, 16384\n"
"v_exp_f32 v0, v0\n"
"v_mfma_f32_16x16x32_bf16 a[96:99], a[224:227], v[20:23], a[96:99]\n"
"buffer_load_dwordx4 v246, s[12:15], s84 offen offset:0 lds\n"
"v_add_u32 v255, 1, v217\n"
"v_mfma_f32_16x16x32_bf16 a[100:103], a[228:231], v[20:23], a[100:103]\n"
"v_exp_f32 v1, v1\n"
"v_mfma_f32_16x16x32_bf16 a[104:107], a[232:235], v[20:23], a[104:107]\n"
"ds_read_b128 a[176:179], v240 offset:45056\n"
"v_cvt_pk_bf16_f32 v16, v0, v1\n"
"v_mfma_f32_16x16x32_bf16 a[108:111], a[236:239], v[20:23], a[108:111]\n"
"v_exp_f32 v2, v2\n"
"v_mfma_f32_16x16x32_bf16 a[112:115], a[240:243], v[20:23], a[112:115]\n"
"ds_read_b128 a[180:183], v240 offset:45312\n"
"v_add_f32 v44, v44, v0\n"
"v_mfma_f32_16x16x32_bf16 a[116:119], a[244:247], v[20:23], a[116:119]\n"
"v_exp_f32 v3, v3\n"
"v_mfma_f32_16x16x32_bf16 a[120:123], a[248:251], v[20:23], a[120:123]\n"
"ds_read_b128 a[184:187], v240 offset:45568\n"
"v_cvt_pk_bf16_f32 v17, v2, v3\n"
"v_mfma_f32_16x16x32_bf16 a[124:127], a[252:255], v[20:23], a[124:127]\n"
"v_exp_f32 v4, v4\n"
"v_and_b32 v255, 4, v255\n"
"v_mfma_f32_16x16x32_bf16 v[56:59], v[40:43], v[20:23], v[56:59]\n"
"ds_read_b128 a[188:191], v240 offset:45824\n"
"v_add_f32 v44, v44, v1\n"
"v_mfma_f32_16x16x32_bf16 v[8:11], a[128:131], v[80:83], v[28:31]\n"
"v_exp_f32 v5, v5\n"
"v_mfma_f32_16x16x32_bf16 v[8:11], a[132:135], v[84:87], v[8:11]\n"
"ds_read_b64_tr_b16 a[224:225], v242 offset:57344\n"
"v_cvt_pk_bf16_f32 v18, v4, v5\n"
"v_mfma_f32_16x16x32_bf16 v[8:11], a[136:139], v[88:91], v[8:11]\n"
"v_exp_f32 v6, v6\n"
"v_mfma_f32_16x16x32_bf16 v[8:11], a[140:143], v[92:95], v[8:11]\n"
"ds_read_b64_tr_b16 a[226:227], v242 offset:61440\n"
"v_add_f32 v44, v44, v2\n"
"v_mfma_f32_16x16x32_bf16 v[12:15], a[144:147], v[80:83], v[28:31]\n"
"v_exp_f32 v7, v7\n"
"v_mfma_f32_16x16x32_bf16 v[12:15], a[148:151], v[84:87], v[12:15]\n"
"ds_read_b64_tr_b16 a[228:229], v243 offset:57344\n"
"v_cvt_pk_bf16_f32 v19, v6, v7\n"
"v_lshlrev_b32 v255, 26, v255\n"
"v_mfma_f32_16x16x32_bf16 v[12:15], a[152:155], v[88:91], v[12:15]\n"
"buffer_load_dwordx4 v247, s[12:15], s84 offen offset:1024 lds\n"
"v_lshl_add_u32 v217, v217, 4, v255\n"
"v_mfma_f32_16x16x32_bf16 v[12:15], a[156:159], v[92:95], v[12:15]\n"
"s_waitcnt lgkmcnt(11)\n"
"v_exp_f32 v8, v8\n"
"v_mfma_f32_16x16x32_bf16 a[0:3], a[192:195], v[16:19], a[0:3]\n"
"ds_read_b64_tr_b16 a[230:231], v243 offset:61440\n"
"v_add_f32 v44, v44, v3\n"
"v_mfma_f32_16x16x32_bf16 a[4:7], a[196:199], v[16:19], a[4:7]\n"
"v_exp_f32 v9, v9\n"
"v_mfma_f32_16x16x32_bf16 a[8:11], a[200:203], v[16:19], a[8:11]\n"
"ds_read_b64_tr_b16 a[232:233], v242 offset:57600\n"
"v_cvt_pk_bf16_f32 v20, v8, v9\n"
"v_mfma_f32_16x16x32_bf16 a[12:15], a[204:207], v[16:19], a[12:15]\n"
"v_exp_f32 v10, v10\n"
"v_mfma_f32_16x16x32_bf16 a[16:19], a[208:211], v[16:19], a[16:19]\n"
"ds_read_b64_tr_b16 a[234:235], v242 offset:61696\n"
"v_add_f32 v44, v44, v4\n"
"v_mfma_f32_16x16x32_bf16 a[20:23], a[212:215], v[16:19], a[20:23]\n"
"v_exp_f32 v11, v11\n"
"v_lshl_add_u32 v252, v251, 1, v217\n"
"v_mfma_f32_16x16x32_bf16 a[24:27], a[216:219], v[16:19], a[24:27]\n"
"ds_read_b64_tr_b16 a[236:237], v243 offset:57600\n"
"v_cvt_pk_bf16_f32 v21, v10, v11\n"
"v_mfma_f32_16x16x32_bf16 a[28:31], a[220:223], v[16:19], a[28:31]\n"
"v_exp_f32 v12, v12\n"
"v_mfma_f32_16x16x32_bf16 v[220:223], a[128:131], v[96:99], v[32:35]\n"
"ds_read_b64_tr_b16 a[238:239], v243 offset:61696\n"
"v_add_f32 v44, v44, v5\n"
"v_mfma_f32_16x16x32_bf16 v[220:223], a[132:135], v[100:103], v[220:223]\n"
"v_exp_f32 v13, v13\n"
"v_mfma_f32_16x16x32_bf16 v[220:223], a[136:139], v[104:107], v[220:223]\n"
"ds_read_b64_tr_b16 a[240:241], v242 offset:57856\n"
"v_cvt_pk_bf16_f32 v22, v12, v13\n"
"v_mfma_f32_16x16x32_bf16 v[220:223], a[140:143], v[108:111], v[220:223]\n"
"v_exp_f32 v14, v14\n"
"v_mfma_f32_16x16x32_bf16 v[224:227], a[144:147], v[96:99], v[32:35]\n"
"ds_read_b64_tr_b16 a[242:243], v242 offset:61952\n"
"v_add_f32 v44, v44, v6\n"
"v_add_u32 v251, v251, v217\n"
"v_mfma_f32_16x16x32_bf16 v[224:227], a[148:151], v[100:103], v[224:227]\n"
"s_mul_i32 s62, s41, 1327104\n"
"v_exp_f32 v15, v15\n"
"v_mfma_f32_16x16x32_bf16 v[224:227], a[152:155], v[104:107], v[224:227]\n"
"s_lshl_b32 s49, s48, 1\n"
"ds_read_b64_tr_b16 a[244:245], v243 offset:57856\n"
"v_cvt_pk_bf16_f32 v23, v14, v15\n"
"v_mfma_f32_16x16x32_bf16 v[224:227], a[156:159], v[108:111], v[224:227]\n"
"s_add_u32 s49, s49, s62\n"
"v_exp_f32 v220, v220\n"
"v_mfma_f32_16x16x32_bf16 a[32:35], a[192:195], v[20:23], a[32:35]\n"
"buffer_load_dwordx4 v246, s[12:15], s84 offen offset:2048 lds\n"
"v_mfma_f32_16x16x32_bf16 a[36:39], a[196:199], v[20:23], a[36:39]\n"
"v_exp_f32 v221, v221\n"
"v_mfma_f32_16x16x32_bf16 a[40:43], a[200:203], v[20:23], a[40:43]\n"
"ds_read_b64_tr_b16 a[246:247], v243 offset:61952\n"
"buffer_load_dwordx4 v[128:131], v252, s[64:67], s49 offen\n"
"v_cvt_pk_bf16_f32 v16, v220, v221\n"
"v_mfma_f32_16x16x32_bf16 a[44:47], a[204:207], v[20:23], a[44:47]\n"
"v_exp_f32 v222, v222\n"
"v_mfma_f32_16x16x32_bf16 a[48:51], a[208:211], v[20:23], a[48:51]\n"
"ds_read_b64_tr_b16 a[248:249], v242 offset:58112\n"
"v_add_f32 v44, v44, v7\n"
"v_mfma_f32_16x16x32_bf16 a[52:55], a[212:215], v[20:23], a[52:55]\n"
"v_exp_f32 v223, v223\n"
"v_mfma_f32_16x16x32_bf16 a[56:59], a[216:219], v[20:23], a[56:59]\n"
"ds_read_b64_tr_b16 a[250:251], v242 offset:62208\n"
"v_cvt_pk_bf16_f32 v17, v222, v223\n"
"v_mfma_f32_16x16x32_bf16 a[60:63], a[220:223], v[20:23], a[60:63]\n"
"v_exp_f32 v224, v224\n"
"v_mfma_f32_16x16x32_bf16 v[48:51], v[40:43], v[20:23], v[48:51]\n"
"ds_read_b64_tr_b16 a[252:253], v243 offset:58112\n"
"v_add_f32 v52, v52, v220\n"
"v_mfma_f32_16x16x32_bf16 v[228:231], a[128:131], v[112:115], v[36:39]\n"
"v_exp_f32 v225, v225\n"
"v_mul_u32_u24 v249, 3641, v248\n"
"v_mfma_f32_16x16x32_bf16 v[228:231], a[132:135], v[116:119], v[228:231]\n"
"s_movk_i32 s60, 864\n"
"ds_read_b64_tr_b16 a[254:255], v243 offset:62208\n"
"v_cvt_pk_bf16_f32 v18, v224, v225\n"
"v_mfma_f32_16x16x32_bf16 v[228:231], a[136:139], v[120:123], v[228:231]\n"
"v_exp_f32 v226, v226\n"
"v_mfma_f32_16x16x32_bf16 v[228:231], a[140:143], v[124:127], v[228:231]\n"
"v_add_f32 v52, v52, v221\n"
"v_add_f32 v52, v52, v222\n"
"v_mfma_f32_16x16x32_bf16 v[232:235], a[144:147], v[112:115], v[36:39]\n"
"v_exp_f32 v227, v227\n"
"v_mfma_f32_16x16x32_bf16 v[232:235], a[148:151], v[116:119], v[232:235]\n"
"v_cvt_pk_bf16_f32 v19, v226, v227\n"
"v_add_f32 v52, v52, v223\n"
"v_mfma_f32_16x16x32_bf16 v[232:235], a[152:155], v[120:123], v[232:235]\n"
"buffer_load_dwordx4 v247, s[12:15], s84 offen offset:3072 lds\n"
"v_add_u32 v206, 4096, v207\n"
"v_mfma_f32_16x16x32_bf16 v[232:235], a[156:159], v[124:127], v[232:235]\n"
"s_add_u32 s84, s84, 16384\n"
"v_exp_f32 v228, v228\n"
"v_mfma_f32_16x16x32_bf16 a[64:67], a[192:195], v[16:19], a[64:67]\n"
"v_add_f32 v52, v52, v224\n"
"v_add_f32 v52, v52, v225\n"
"v_mfma_f32_16x16x32_bf16 a[68:71], a[196:199], v[16:19], a[68:71]\n"
"v_exp_f32 v229, v229\n"
"v_mfma_f32_16x16x32_bf16 a[72:75], a[200:203], v[16:19], a[72:75]\n"
"v_cvt_pk_bf16_f32 v20, v228, v229\n"
"v_add_f32 v52, v52, v226\n"
"v_mfma_f32_16x16x32_bf16 a[76:79], a[204:207], v[16:19], a[76:79]\n"
"s_waitcnt vmcnt(20) lgkmcnt(0)\n"
"v_exp_f32 v230, v230\n"
"v_mfma_f32_16x16x32_bf16 a[80:83], a[208:211], v[16:19], a[80:83]\n"
"s_barrier\n"
"v_mfma_f32_16x16x32_bf16 a[84:87], a[212:215], v[16:19], a[84:87]\n"
"v_exp_f32 v231, v231\n"
"v_mfma_f32_16x16x32_bf16 a[88:91], a[216:219], v[16:19], a[88:91]\n"
"v_cvt_pk_bf16_f32 v21, v230, v231\n"
"v_add_f32 v52, v52, v227\n"
"v_lshrrev_b32 v217, 16, v249\n"
"v_mfma_f32_16x16x32_bf16 a[92:95], a[220:223], v[16:19], a[92:95]\n"
"v_exp_f32 v232, v232\n"
"v_mfma_f32_16x16x32_bf16 v[0:3], a[160:163], v[64:67], v[24:27]\n"
"ds_read_b128 a[128:131], v241 offset:0\n"
"v_mad_u32_u24 v210, v217, s60, v207\n"
"v_mfma_f32_16x16x32_bf16 v[0:3], a[164:167], v[68:71], v[0:3]\n"
"v_exp_f32 v233, v233\n"
"v_mfma_f32_16x16x32_bf16 v[0:3], a[168:171], v[72:75], v[0:3]\n"
"ds_read_b128 a[132:135], v241 offset:256\n"
"v_cvt_pk_bf16_f32 v22, v232, v233\n"
"v_mfma_f32_16x16x32_bf16 v[0:3], a[172:175], v[76:79], v[0:3]\n"
"v_exp_f32 v234, v234\n"
"v_mfma_f32_16x16x32_bf16 v[4:7], a[176:179], v[64:67], v[24:27]\n"
"ds_read_b128 a[136:139], v241 offset:512\n"
"v_add_u32 v249, 233024, v249\n"
"v_mfma_f32_16x16x32_bf16 v[4:7], a[180:183], v[68:71], v[4:7]\n"
"v_exp_f32 v235, v235\n"
"v_mfma_f32_16x16x32_bf16 v[4:7], a[184:187], v[72:75], v[4:7]\n"
"ds_read_b128 a[140:143], v241 offset:768\n"
"v_cvt_pk_bf16_f32 v23, v234, v235\n"
"v_mfma_f32_16x16x32_bf16 v[4:7], a[188:191], v[76:79], v[4:7]\n"
"s_add_u32 m0, s86, 32768\n"
"v_exp_f32 v0, v0\n"
"v_mfma_f32_16x16x32_bf16 a[96:99], a[192:195], v[20:23], a[96:99]\n"
"buffer_load_dwordx4 v246, s[8:11], s84 offen offset:0 lds\n"
"v_lshrrev_b32 v217, 16, v249\n"
"v_mfma_f32_16x16x32_bf16 a[100:103], a[196:199], v[20:23], a[100:103]\n"
"v_exp_f32 v1, v1\n"
"v_mfma_f32_16x16x32_bf16 a[104:107], a[200:203], v[20:23], a[104:107]\n"
"ds_read_b128 a[144:147], v241 offset:4096\n"
"v_cvt_pk_bf16_f32 v16, v0, v1\n"
"v_mfma_f32_16x16x32_bf16 a[108:111], a[204:207], v[20:23], a[108:111]\n"
"v_exp_f32 v2, v2\n"
"v_mfma_f32_16x16x32_bf16 a[112:115], a[208:211], v[20:23], a[112:115]\n"
"ds_read_b128 a[148:151], v241 offset:4352\n"
"v_add_f32 v44, v44, v0\n"
"v_mfma_f32_16x16x32_bf16 a[116:119], a[212:215], v[20:23], a[116:119]\n"
"v_exp_f32 v3, v3\n"
"v_mfma_f32_16x16x32_bf16 a[120:123], a[216:219], v[20:23], a[120:123]\n"
"ds_read_b128 a[152:155], v241 offset:4608\n"
"v_cvt_pk_bf16_f32 v17, v2, v3\n"
"v_mfma_f32_16x16x32_bf16 a[124:127], a[220:223], v[20:23], a[124:127]\n"
"v_exp_f32 v4, v4\n"
"v_mad_u32_u24 v211, v217, s60, v207\n"
"v_mfma_f32_16x16x32_bf16 v[56:59], v[40:43], v[20:23], v[56:59]\n"
"ds_read_b128 a[156:159], v241 offset:4864\n"
"v_add_f32 v44, v44, v1\n"
"v_mfma_f32_16x16x32_bf16 v[8:11], a[160:163], v[80:83], v[28:31]\n"
"v_exp_f32 v5, v5\n"
"v_mfma_f32_16x16x32_bf16 v[8:11], a[164:167], v[84:87], v[8:11]\n"
"ds_read_b64_tr_b16 a[192:193], v244 offset:16384\n"
"v_cvt_pk_bf16_f32 v18, v4, v5\n"
"v_mfma_f32_16x16x32_bf16 v[8:11], a[168:171], v[88:91], v[8:11]\n"
"v_exp_f32 v6, v6\n"
"v_mfma_f32_16x16x32_bf16 v[8:11], a[172:175], v[92:95], v[8:11]\n"
"ds_read_b64_tr_b16 a[194:195], v244 offset:20480\n"
"v_add_f32 v44, v44, v2\n"
"v_mfma_f32_16x16x32_bf16 v[12:15], a[176:179], v[80:83], v[28:31]\n"
"v_exp_f32 v7, v7\n"
"v_mfma_f32_16x16x32_bf16 v[12:15], a[180:183], v[84:87], v[12:15]\n"
"ds_read_b64_tr_b16 a[196:197], v245 offset:16384\n"
"v_cvt_pk_bf16_f32 v19, v6, v7\n"
"v_add_u32 v249, 233024, v249\n"
"v_mfma_f32_16x16x32_bf16 v[12:15], a[184:187], v[88:91], v[12:15]\n"
"buffer_load_dwordx4 v246, s[8:11], s84 offen offset:1024 lds\n"
"v_lshrrev_b32 v217, 16, v249\n"
"v_mfma_f32_16x16x32_bf16 v[12:15], a[188:191], v[92:95], v[12:15]\n"
"s_waitcnt lgkmcnt(11)\n"
"v_exp_f32 v8, v8\n"
"v_mfma_f32_16x16x32_bf16 a[0:3], a[224:227], v[16:19], a[0:3]\n"
"ds_read_b64_tr_b16 a[198:199], v245 offset:20480\n"
"v_add_f32 v44, v44, v3\n"
"v_mfma_f32_16x16x32_bf16 a[4:7], a[228:231], v[16:19], a[4:7]\n"
"v_exp_f32 v9, v9\n"
"v_mfma_f32_16x16x32_bf16 a[8:11], a[232:235], v[16:19], a[8:11]\n"
"ds_read_b64_tr_b16 a[200:201], v244 offset:16640\n"
"v_cvt_pk_bf16_f32 v20, v8, v9\n"
"v_mfma_f32_16x16x32_bf16 a[12:15], a[236:239], v[16:19], a[12:15]\n"
"v_exp_f32 v10, v10\n"
"v_mfma_f32_16x16x32_bf16 a[16:19], a[240:243], v[16:19], a[16:19]\n"
"ds_read_b64_tr_b16 a[202:203], v244 offset:20736\n"
"v_add_f32 v44, v44, v4\n"
"v_mfma_f32_16x16x32_bf16 a[20:23], a[244:247], v[16:19], a[20:23]\n"
"v_exp_f32 v11, v11\n"
"v_mad_u32_u24 v212, v217, s60, v207\n"
"v_mfma_f32_16x16x32_bf16 a[24:27], a[248:251], v[16:19], a[24:27]\n"
"ds_read_b64_tr_b16 a[204:205], v245 offset:16640\n"
"v_cvt_pk_bf16_f32 v21, v10, v11\n"
"v_mfma_f32_16x16x32_bf16 a[28:31], a[252:255], v[16:19], a[28:31]\n"
"v_exp_f32 v12, v12\n"
"v_mfma_f32_16x16x32_bf16 v[220:223], a[160:163], v[96:99], v[32:35]\n"
"ds_read_b64_tr_b16 a[206:207], v245 offset:20736\n"
"v_add_f32 v44, v44, v5\n"
"v_mfma_f32_16x16x32_bf16 v[220:223], a[164:167], v[100:103], v[220:223]\n"
"v_exp_f32 v13, v13\n"
"v_mfma_f32_16x16x32_bf16 v[220:223], a[168:171], v[104:107], v[220:223]\n"
"ds_read_b64_tr_b16 a[208:209], v244 offset:16896\n"
"v_cvt_pk_bf16_f32 v22, v12, v13\n"
"v_mfma_f32_16x16x32_bf16 v[220:223], a[172:175], v[108:111], v[220:223]\n"
"v_exp_f32 v14, v14\n"
"v_mfma_f32_16x16x32_bf16 v[224:227], a[176:179], v[96:99], v[32:35]\n"
"ds_read_b64_tr_b16 a[210:211], v244 offset:20992\n"
"v_add_f32 v44, v44, v6\n"
"v_add_u32 v249, 233024, v249\n"
"v_mfma_f32_16x16x32_bf16 v[224:227], a[180:183], v[100:103], v[224:227]\n"
"v_exp_f32 v15, v15\n"
"v_mfma_f32_16x16x32_bf16 v[224:227], a[184:187], v[104:107], v[224:227]\n"
"ds_read_b64_tr_b16 a[212:213], v245 offset:16896\n"
"v_cvt_pk_bf16_f32 v23, v14, v15\n"
"v_mfma_f32_16x16x32_bf16 v[224:227], a[188:191], v[108:111], v[224:227]\n"
"v_exp_f32 v220, v220\n"
"v_mfma_f32_16x16x32_bf16 a[32:35], a[224:227], v[20:23], a[32:35]\n"
"buffer_load_dwordx4 v247, s[8:11], s84 offen offset:2048 lds\n"
"v_lshrrev_b32 v217, 16, v249\n"
"v_mfma_f32_16x16x32_bf16 a[36:39], a[228:231], v[20:23], a[36:39]\n"
"v_exp_f32 v221, v221\n"
"v_mfma_f32_16x16x32_bf16 a[40:43], a[232:235], v[20:23], a[40:43]\n"
"ds_read_b64_tr_b16 a[214:215], v245 offset:20992\n"
"v_cvt_pk_bf16_f32 v16, v220, v221\n"
"v_mfma_f32_16x16x32_bf16 a[44:47], a[236:239], v[20:23], a[44:47]\n"
"v_exp_f32 v222, v222\n"
"v_mfma_f32_16x16x32_bf16 a[48:51], a[240:243], v[20:23], a[48:51]\n"
"ds_read_b64_tr_b16 a[216:217], v244 offset:17152\n"
"v_add_f32 v44, v44, v7\n"
"v_mfma_f32_16x16x32_bf16 a[52:55], a[244:247], v[20:23], a[52:55]\n"
"v_exp_f32 v223, v223\n"
"v_mfma_f32_16x16x32_bf16 a[56:59], a[248:251], v[20:23], a[56:59]\n"
"ds_read_b64_tr_b16 a[218:219], v244 offset:21248\n"
"v_cvt_pk_bf16_f32 v17, v222, v223\n"
"v_mfma_f32_16x16x32_bf16 a[60:63], a[252:255], v[20:23], a[60:63]\n"
"v_exp_f32 v224, v224\n"
"v_mad_u32_u24 v213, v217, s60, v207\n"
"v_mfma_f32_16x16x32_bf16 v[48:51], v[40:43], v[20:23], v[48:51]\n"
"ds_read_b64_tr_b16 a[220:221], v245 offset:17152\n"
"v_add_f32 v52, v52, v220\n"
"v_mfma_f32_16x16x32_bf16 v[228:231], a[160:163], v[112:115], v[36:39]\n"
"v_exp_f32 v225, v225\n"
"v_mfma_f32_16x16x32_bf16 v[228:231], a[164:167], v[116:119], v[228:231]\n"
"ds_read_b64_tr_b16 a[222:223], v245 offset:21248\n"
"v_cvt_pk_bf16_f32 v18, v224, v225\n"
"v_mfma_f32_16x16x32_bf16 v[228:231], a[168:171], v[120:123], v[228:231]\n"
"v_exp_f32 v226, v226\n"
"v_mfma_f32_16x16x32_bf16 v[228:231], a[172:175], v[124:127], v[228:231]\n"
"v_add_f32 v52, v52, v221\n"
"v_add_f32 v52, v52, v222\n"
"v_mfma_f32_16x16x32_bf16 v[232:235], a[176:179], v[112:115], v[36:39]\n"
"v_exp_f32 v227, v227\n"
"v_mfma_f32_16x16x32_bf16 v[232:235], a[180:183], v[116:119], v[232:235]\n"
"v_cvt_pk_bf16_f32 v19, v226, v227\n"
"v_add_f32 v52, v52, v223\n"
"v_add_u32 v249, 233024, v249\n"
"v_mfma_f32_16x16x32_bf16 v[232:235], a[184:187], v[120:123], v[232:235]\n"
"buffer_load_dwordx4 v247, s[8:11], s84 offen offset:3072 lds\n"
"v_lshrrev_b32 v217, 16, v249\n"
"v_mfma_f32_16x16x32_bf16 v[232:235], a[188:191], v[124:127], v[232:235]\n"
"v_exp_f32 v228, v228\n"
"v_mfma_f32_16x16x32_bf16 a[64:67], a[224:227], v[16:19], a[64:67]\n"
"buffer_load_dwordx4 v[164:167], v252, s[68:71], s49 offen\n"
"v_add_f32 v52, v52, v224\n"
"v_add_f32 v52, v52, v225\n"
"v_mfma_f32_16x16x32_bf16 a[68:71], a[228:231], v[16:19], a[68:71]\n"
"v_exp_f32 v229, v229\n"
"v_mfma_f32_16x16x32_bf16 a[72:75], a[232:235], v[16:19], a[72:75]\n"
"v_cvt_pk_bf16_f32 v20, v228, v229\n"
"v_add_f32 v52, v52, v226\n"
"v_mfma_f32_16x16x32_bf16 a[76:79], a[236:239], v[16:19], a[76:79]\n"
"v_exp_f32 v230, v230\n"
"v_mfma_f32_16x16x32_bf16 a[80:83], a[240:243], v[16:19], a[80:83]\n"
"s_waitcnt lgkmcnt(8)\n"
"v_mad_u32_u24 v214, v217, s60, v206\n"
"v_mfma_f32_16x16x32_bf16 a[84:87], a[244:247], v[16:19], a[84:87]\n"
"v_exp_f32 v231, v231\n"
"v_mfma_f32_16x16x32_bf16 a[88:91], a[248:251], v[16:19], a[88:91]\n"
"v_cvt_pk_bf16_f32 v21, v230, v231\n"
"v_add_f32 v52, v52, v227\n"
"v_mfma_f32_16x16x32_bf16 a[92:95], a[252:255], v[16:19], a[92:95]\n"
"v_exp_f32 v232, v232\n"
"v_mfma_f32_16x16x32_bf16 v[0:3], a[128:131], v[64:67], v[24:27]\n"
"ds_read_b128 a[160:163], v241 offset:8192\n"
"v_add_u32 v249, 233024, v249\n"
"v_mfma_f32_16x16x32_bf16 v[0:3], a[132:135], v[68:71], v[0:3]\n"
"v_exp_f32 v233, v233\n"
"v_mfma_f32_16x16x32_bf16 v[0:3], a[136:139], v[72:75], v[0:3]\n"
"ds_read_b128 a[164:167], v241 offset:8448\n"
"v_cvt_pk_bf16_f32 v22, v232, v233\n"
"v_mfma_f32_16x16x32_bf16 v[0:3], a[140:143], v[76:79], v[0:3]\n"
"v_exp_f32 v234, v234\n"
"v_mfma_f32_16x16x32_bf16 v[4:7], a[144:147], v[64:67], v[24:27]\n"
"ds_read_b128 a[168:171], v241 offset:8704\n"
"v_lshrrev_b32 v217, 16, v249\n"
"v_mfma_f32_16x16x32_bf16 v[4:7], a[148:151], v[68:71], v[4:7]\n"
"v_exp_f32 v235, v235\n"
"v_mfma_f32_16x16x32_bf16 v[4:7], a[152:155], v[72:75], v[4:7]\n"
"ds_read_b128 a[172:175], v241 offset:8960\n"
"v_cvt_pk_bf16_f32 v23, v234, v235\n"
"v_mfma_f32_16x16x32_bf16 v[4:7], a[156:159], v[76:79], v[4:7]\n"
"s_add_u32 m0, s86, 49152\n"
"v_exp_f32 v0, v0\n"
"v_mfma_f32_16x16x32_bf16 a[96:99], a[224:227], v[20:23], a[96:99]\n"
"buffer_load_dwordx4 v246, s[12:15], s84 offen offset:0 lds\n"
"v_mad_u32_u24 v236, v217, s60, v206\n"
"v_mfma_f32_16x16x32_bf16 a[100:103], a[228:231], v[20:23], a[100:103]\n"
"v_exp_f32 v1, v1\n"
"v_mfma_f32_16x16x32_bf16 a[104:107], a[232:235], v[20:23], a[104:107]\n"
"ds_read_b128 a[176:179], v241 offset:12288\n"
"v_cvt_pk_bf16_f32 v16, v0, v1\n"
"v_mfma_f32_16x16x32_bf16 a[108:111], a[236:239], v[20:23], a[108:111]\n"
"v_exp_f32 v2, v2\n"
"v_mfma_f32_16x16x32_bf16 a[112:115], a[240:243], v[20:23], a[112:115]\n"
"ds_read_b128 a[180:183], v241 offset:12544\n"
"v_add_f32 v44, v44, v0\n"
"v_mfma_f32_16x16x32_bf16 a[116:119], a[244:247], v[20:23], a[116:119]\n"
"v_exp_f32 v3, v3\n"
"v_mfma_f32_16x16x32_bf16 a[120:123], a[248:251], v[20:23], a[120:123]\n"
"ds_read_b128 a[184:187], v241 offset:12800\n"
"v_cvt_pk_bf16_f32 v17, v2, v3\n"
"v_mfma_f32_16x16x32_bf16 a[124:127], a[252:255], v[20:23], a[124:127]\n"
"v_exp_f32 v4, v4\n"
"v_add_u32 v249, 233024, v249\n"
"v_mfma_f32_16x16x32_bf16 v[56:59], v[40:43], v[20:23], v[56:59]\n"
"ds_read_b128 a[188:191], v241 offset:13056\n"
"v_add_f32 v44, v44, v1\n"
"v_mfma_f32_16x16x32_bf16 v[8:11], a[128:131], v[80:83], v[28:31]\n"
"v_exp_f32 v5, v5\n"
"v_mfma_f32_16x16x32_bf16 v[8:11], a[132:135], v[84:87], v[8:11]\n"
"ds_read_b64_tr_b16 a[224:225], v244 offset:24576\n"
"v_cvt_pk_bf16_f32 v18, v4, v5\n"
"v_mfma_f32_16x16x32_bf16 v[8:11], a[136:139], v[88:91], v[8:11]\n"
"v_exp_f32 v6, v6\n"
"v_mfma_f32_16x16x32_bf16 v[8:11], a[140:143], v[92:95], v[8:11]\n"
"ds_read_b64_tr_b16 a[226:227], v244 offset:28672\n"
"v_add_f32 v44, v44, v2\n"
"v_mfma_f32_16x16x32_bf16 v[12:15], a[144:147], v[80:83], v[28:31]\n"
"v_exp_f32 v7, v7\n"
"v_mfma_f32_16x16x32_bf16 v[12:15], a[148:151], v[84:87], v[12:15]\n"
"ds_read_b64_tr_b16 a[228:229], v245 offset:24576\n"
"v_cvt_pk_bf16_f32 v19, v6, v7\n"
"v_lshrrev_b32 v217, 16, v249\n"
"v_mfma_f32_16x16x32_bf16 v[12:15], a[152:155], v[88:91], v[12:15]\n"
"buffer_load_dwordx4 v247, s[12:15], s84 offen offset:1024 lds\n"
"v_mad_u32_u24 v237, v217, s60, v206\n"
"v_mfma_f32_16x16x32_bf16 v[12:15], a[156:159], v[92:95], v[12:15]\n"
"s_waitcnt lgkmcnt(11)\n"
"v_exp_f32 v8, v8\n"
"v_mfma_f32_16x16x32_bf16 a[0:3], a[192:195], v[16:19], a[0:3]\n"
"ds_read_b64_tr_b16 a[230:231], v245 offset:28672\n"
"v_add_f32 v44, v44, v3\n"
"v_mfma_f32_16x16x32_bf16 a[4:7], a[196:199], v[16:19], a[4:7]\n"
"v_exp_f32 v9, v9\n"
"v_mfma_f32_16x16x32_bf16 a[8:11], a[200:203], v[16:19], a[8:11]\n"
"ds_read_b64_tr_b16 a[232:233], v244 offset:24832\n"
"v_cvt_pk_bf16_f32 v20, v8, v9\n"
"v_mfma_f32_16x16x32_bf16 a[12:15], a[204:207], v[16:19], a[12:15]\n"
"v_exp_f32 v10, v10\n"
"v_mfma_f32_16x16x32_bf16 a[16:19], a[208:211], v[16:19], a[16:19]\n"
"ds_read_b64_tr_b16 a[234:235], v244 offset:28928\n"
"v_add_f32 v44, v44, v4\n"
"v_mfma_f32_16x16x32_bf16 a[20:23], a[212:215], v[16:19], a[20:23]\n"
"v_exp_f32 v11, v11\n"
"v_add_u32 v249, 233024, v249\n"
"v_mfma_f32_16x16x32_bf16 a[24:27], a[216:219], v[16:19], a[24:27]\n"
"ds_read_b64_tr_b16 a[236:237], v245 offset:24832\n"
"v_cvt_pk_bf16_f32 v21, v10, v11\n"
"v_mfma_f32_16x16x32_bf16 a[28:31], a[220:223], v[16:19], a[28:31]\n"
"v_exp_f32 v12, v12\n"
"v_mfma_f32_16x16x32_bf16 v[220:223], a[128:131], v[96:99], v[32:35]\n"
"ds_read_b64_tr_b16 a[238:239], v245 offset:28928\n"
"v_add_f32 v44, v44, v5\n"
"v_mfma_f32_16x16x32_bf16 v[220:223], a[132:135], v[100:103], v[220:223]\n"
"v_exp_f32 v13, v13\n"
"v_mfma_f32_16x16x32_bf16 v[220:223], a[136:139], v[104:107], v[220:223]\n"
"ds_read_b64_tr_b16 a[240:241], v244 offset:25088\n"
"v_cvt_pk_bf16_f32 v22, v12, v13\n"
"v_mfma_f32_16x16x32_bf16 v[220:223], a[140:143], v[108:111], v[220:223]\n"
"v_exp_f32 v14, v14\n"
"v_mfma_f32_16x16x32_bf16 v[224:227], a[144:147], v[96:99], v[32:35]\n"
"ds_read_b64_tr_b16 a[242:243], v244 offset:29184\n"
"v_add_f32 v44, v44, v6\n"
"v_lshrrev_b32 v217, 16, v249\n"
"v_mfma_f32_16x16x32_bf16 v[224:227], a[148:151], v[100:103], v[224:227]\n"
"v_exp_f32 v15, v15\n"
"v_mfma_f32_16x16x32_bf16 v[224:227], a[152:155], v[104:107], v[224:227]\n"
"ds_read_b64_tr_b16 a[244:245], v245 offset:25088\n"
"v_cvt_pk_bf16_f32 v23, v14, v15\n"
"v_mfma_f32_16x16x32_bf16 v[224:227], a[156:159], v[108:111], v[224:227]\n"
"v_exp_f32 v220, v220\n"
"v_mfma_f32_16x16x32_bf16 a[32:35], a[192:195], v[20:23], a[32:35]\n"
"buffer_load_dwordx4 v246, s[12:15], s84 offen offset:2048 lds\n"
"v_mad_u32_u24 v238, v217, s60, v206\n"
"v_mfma_f32_16x16x32_bf16 a[36:39], a[196:199], v[20:23], a[36:39]\n"
"v_exp_f32 v221, v221\n"
"v_mfma_f32_16x16x32_bf16 a[40:43], a[200:203], v[20:23], a[40:43]\n"
"ds_read_b64_tr_b16 a[246:247], v245 offset:29184\n"
"v_cvt_pk_bf16_f32 v16, v220, v221\n"
"v_mfma_f32_16x16x32_bf16 a[44:47], a[204:207], v[20:23], a[44:47]\n"
"v_exp_f32 v222, v222\n"
"v_mfma_f32_16x16x32_bf16 a[48:51], a[208:211], v[20:23], a[48:51]\n"
"ds_read_b64_tr_b16 a[248:249], v244 offset:25344\n"
"v_add_f32 v44, v44, v7\n"
"v_mfma_f32_16x16x32_bf16 a[52:55], a[212:215], v[20:23], a[52:55]\n"
"v_exp_f32 v223, v223\n"
"v_mfma_f32_16x16x32_bf16 a[56:59], a[216:219], v[20:23], a[56:59]\n"
"ds_read_b64_tr_b16 a[250:251], v244 offset:29440\n"
"v_cvt_pk_bf16_f32 v17, v222, v223\n"
"v_mfma_f32_16x16x32_bf16 a[60:63], a[220:223], v[20:23], a[60:63]\n"
"v_exp_f32 v224, v224\n"
"v_add_u32 v206, 4096, v206\n"
"v_mfma_f32_16x16x32_bf16 v[48:51], v[40:43], v[20:23], v[48:51]\n"
"ds_read_b64_tr_b16 a[252:253], v245 offset:25344\n"
"v_add_f32 v52, v52, v220\n"
"v_mfma_f32_16x16x32_bf16 v[228:231], a[128:131], v[112:115], v[36:39]\n"
"v_exp_f32 v225, v225\n"
"v_mfma_f32_16x16x32_bf16 v[228:231], a[132:135], v[116:119], v[228:231]\n"
"ds_read_b64_tr_b16 a[254:255], v245 offset:29440\n"
"v_cvt_pk_bf16_f32 v18, v224, v225\n"
"v_mfma_f32_16x16x32_bf16 v[228:231], a[136:139], v[120:123], v[228:231]\n"
"v_exp_f32 v226, v226\n"
"v_mfma_f32_16x16x32_bf16 v[228:231], a[140:143], v[124:127], v[228:231]\n"
"v_add_f32 v52, v52, v221\n"
"v_add_f32 v52, v52, v222\n"
"v_mfma_f32_16x16x32_bf16 v[232:235], a[144:147], v[112:115], v[36:39]\n"
"v_exp_f32 v227, v227\n"
"v_mfma_f32_16x16x32_bf16 v[232:235], a[148:151], v[116:119], v[232:235]\n"
"v_cvt_pk_bf16_f32 v19, v226, v227\n"
"v_add_f32 v52, v52, v223\n"
"v_add_u32 v249, 233024, v249\n"
"v_mfma_f32_16x16x32_bf16 v[232:235], a[152:155], v[120:123], v[232:235]\n"
"buffer_load_dwordx4 v247, s[12:15], s84 offen offset:3072 lds\n"
"v_lshrrev_b32 v217, 16, v249\n"
"v_mfma_f32_16x16x32_bf16 v[232:235], a[156:159], v[124:127], v[232:235]\n"
"s_add_u32 s84, s84, 16384\n"
"v_exp_f32 v228, v228\n"
"v_mfma_f32_16x16x32_bf16 a[64:67], a[192:195], v[16:19], a[64:67]\n"
"v_add_f32 v52, v52, v224\n"
"v_add_f32 v52, v52, v225\n"
"v_mfma_f32_16x16x32_bf16 a[68:71], a[196:199], v[16:19], a[68:71]\n"
"v_exp_f32 v229, v229\n"
"v_mfma_f32_16x16x32_bf16 a[72:75], a[200:203], v[16:19], a[72:75]\n"
"v_cvt_pk_bf16_f32 v20, v228, v229\n"
"v_add_f32 v52, v52, v226\n"
"v_mfma_f32_16x16x32_bf16 a[76:79], a[204:207], v[16:19], a[76:79]\n"
"s_waitcnt vmcnt(21) lgkmcnt(0)\n"
"v_exp_f32 v230, v230\n"
"v_mfma_f32_16x16x32_bf16 a[80:83], a[208:211], v[16:19], a[80:83]\n"
"s_barrier\n"
"v_mfma_f32_16x16x32_bf16 a[84:87], a[212:215], v[16:19], a[84:87]\n"
"v_exp_f32 v231, v231\n"
"v_mfma_f32_16x16x32_bf16 a[88:91], a[216:219], v[16:19], a[88:91]\n"
"v_cvt_pk_bf16_f32 v21, v230, v231\n"
"v_add_f32 v52, v52, v227\n"
"v_mad_u32_u24 v239, v217, s60, v206\n"
"v_mfma_f32_16x16x32_bf16 a[92:95], a[220:223], v[16:19], a[92:95]\n"
"s_and_b32 s56, s43, 3\n"
"v_exp_f32 v232, v232\n"
"v_mfma_f32_16x16x32_bf16 v[0:3], a[160:163], v[64:67], v[24:27]\n"
"s_lshr_b32 s57, s43, 2\n"
"ds_read_b128 a[128:131], v241 offset:32768\n"
"v_mfma_f32_16x16x32_bf16 v[0:3], a[164:167], v[68:71], v[0:3]\n"
"s_add_u32 s49, s49, 9216\n"
"v_exp_f32 v233, v233\n"
"v_mfma_f32_16x16x32_bf16 v[0:3], a[168:171], v[72:75], v[0:3]\n"
"ds_read_b128 a[132:135], v241 offset:33024\n"
"buffer_load_dwordx4 v[132:135], v252, s[64:67], s49 offen\n"
"v_cvt_pk_bf16_f32 v22, v232, v233\n"
"v_mfma_f32_16x16x32_bf16 v[0:3], a[172:175], v[76:79], v[0:3]\n"
"s_mul_i32 s57, s57, 1179648\n"
"v_exp_f32 v234, v234\n"
"v_mfma_f32_16x16x32_bf16 v[4:7], a[176:179], v[64:67], v[24:27]\n"
"s_mul_i32 s62, s56, 288\n"
"ds_read_b128 a[136:139], v241 offset:33280\n"
"v_mfma_f32_16x16x32_bf16 v[4:7], a[180:183], v[68:71], v[4:7]\n"
"s_add_u32 s57, s57, s62\n"
"v_exp_f32 v235, v235\n"
"v_mfma_f32_16x16x32_bf16 v[4:7], a[184:187], v[72:75], v[4:7]\n"
"s_lshl_b32 s58, s56, 3\n"
"ds_read_b128 a[140:143], v241 offset:33536\n"
"v_cvt_pk_bf16_f32 v23, v234, v235\n"
"v_mfma_f32_16x16x32_bf16 v[4:7], a[188:191], v[76:79], v[4:7]\n"
"s_add_u32 m0, s86, 65536\n"
"v_exp_f32 v0, v0\n"
"v_mfma_f32_16x16x32_bf16 a[96:99], a[192:195], v[20:23], a[96:99]\n"
"s_add_u32 s58, s58, s41\n"
"buffer_load_dwordx4 v246, s[8:11], s84 offen offset:0 lds\n"
"v_mfma_f32_16x16x32_bf16 a[100:103], a[196:199], v[20:23], a[100:103]\n"
"s_add_u32 s58, s58, 0\n"
"v_exp_f32 v1, v1\n"
"v_mfma_f32_16x16x32_bf16 a[104:107], a[200:203], v[20:23], a[104:107]\n"
"s_mul_i32 s58, s58, 36864\n"
"ds_read_b128 a[144:147], v241 offset:36864\n"
"v_cvt_pk_bf16_f32 v16, v0, v1\n"
"v_mfma_f32_16x16x32_bf16 a[108:111], a[204:207], v[20:23], a[108:111]\n"
"s_add_u32 s20, s58, s57\n"
"v_exp_f32 v2, v2\n"
"v_mfma_f32_16x16x32_bf16 a[112:115], a[208:211], v[20:23], a[112:115]\n"
"s_lshl_b32 s58, s56, 3\n"
"ds_read_b128 a[148:151], v241 offset:37120\n"
"v_add_f32 v44, v44, v0\n"
"v_mfma_f32_16x16x32_bf16 a[116:119], a[212:215], v[20:23], a[116:119]\n"
"s_add_u32 s58, s58, s41\n"
"v_exp_f32 v3, v3\n"
"v_mfma_f32_16x16x32_bf16 a[120:123], a[216:219], v[20:23], a[120:123]\n"
"s_add_u32 s58, s58, 4\n"
"ds_read_b128 a[152:155], v241 offset:37376\n"
"v_cvt_pk_bf16_f32 v17, v2, v3\n"
"v_mfma_f32_16x16x32_bf16 a[124:127], a[220:223], v[20:23], a[124:127]\n"
"s_mul_i32 s58, s58, 36864\n"
"v_exp_f32 v4, v4\n"
"v_mfma_f32_16x16x32_bf16 v[56:59], v[40:43], v[20:23], v[56:59]\n"
"s_add_u32 s21, s58, s57\n"
"ds_read_b128 a[156:159], v241 offset:37632\n"
"v_add_f32 v44, v44, v1\n"
"v_mfma_f32_16x16x32_bf16 v[8:11], a[160:163], v[80:83], v[28:31]\n"
"s_add_u32 s58, s41, 0\n"
"v_exp_f32 v5, v5\n"
"v_mfma_f32_16x16x32_bf16 v[8:11], a[164:167], v[84:87], v[8:11]\n"
"s_mul_i32 s59, s58, 43\n"
"ds_read_b64_tr_b16 a[192:193], v244 offset:49152\n"
"v_cvt_pk_bf16_f32 v18, v4, v5\n"
"v_mfma_f32_16x16x32_bf16 v[8:11], a[168:171], v[88:91], v[8:11]\n"
"s_lshr_b32 s59, s59, 7\n"
"v_exp_f32 v6, v6\n"
"v_mfma_f32_16x16x32_bf16 v[8:11], a[172:175], v[92:95], v[8:11]\n"
"ds_read_b64_tr_b16 a[194:195], v244 offset:53248\n"
"v_add_f32 v44, v44, v2\n"
"v_mfma_f32_16x16x32_bf16 v[12:15], a[176:179], v[80:83], v[28:31]\n"
"v_exp_f32 v7, v7\n"
"v_mfma_f32_16x16x32_bf16 v[12:15], a[180:183], v[84:87], v[12:15]\n"
"ds_read_b64_tr_b16 a[196:197], v245 offset:49152\n"
"v_cvt_pk_bf16_f32 v19, v6, v7\n"
"v_mfma_f32_16x16x32_bf16 v[12:15], a[184:187], v[88:91], v[12:15]\n"
"AT_pg9:\n"
"s_cbranch_execz AT_hr\n"
"buffer_load_dwordx4 v246, s[8:11], s84 offen offset:1024 lds\n"
"v_mfma_f32_16x16x32_bf16 v[12:15], a[188:191], v[92:95], v[12:15]\n"
"s_waitcnt lgkmcnt(11)\n"
"v_exp_f32 v8, v8\n"
"v_mfma_f32_16x16x32_bf16 a[0:3], a[224:227], v[16:19], a[0:3]\n"
"ds_read_b64_tr_b16 a[198:199], v245 offset:53248\n"
"v_add_f32 v44, v44, v3\n"
"v_mfma_f32_16x16x32_bf16 a[4:7], a[228:231], v[16:19], a[4:7]\n"
"v_exp_f32 v9, v9\n"
"v_mfma_f32_16x16x32_bf16 a[8:11], a[232:235], v[16:19], a[8:11]\n"
"ds_read_b64_tr_b16 a[200:201], v244 offset:49408\n"
"v_cvt_pk_bf16_f32 v20, v8, v9\n"
"v_mfma_f32_16x16x32_bf16 a[12:15], a[236:239], v[16:19], a[12:15]\n"
"v_exp_f32 v10, v10\n"
"v_mfma_f32_16x16x32_bf16 a[16:19], a[240:243], v[16:19], a[16:19]\n"
"ds_read_b64_tr_b16 a[202:203], v244 offset:53504\n"
"v_add_f32 v44, v44, v4\n"
"v_mfma_f32_16x16x32_bf16 a[20:23], a[244:247], v[16:19], a[20:23]\n"
"v_exp_f32 v11, v11\n"
"v_mfma_f32_16x16x32_bf16 a[24:27], a[248:251], v[16:19], a[24:27]\n"
"ds_read_b64_tr_b16 a[204:205], v245 offset:49408\n"
"buffer_load_dwordx4 v[168:171], v252, s[68:71], s49 offen\n"
"v_cvt_pk_bf16_f32 v21, v10, v11\n"
"v_mfma_f32_16x16x32_bf16 a[28:31], a[252:255], v[16:19], a[28:31]\n"
"s_mul_i32 s60, s59, 3\n"
"v_exp_f32 v12, v12\n"
"v_mfma_f32_16x16x32_bf16 v[220:223], a[160:163], v[96:99], v[32:35]\n"
"s_sub_u32 s58, s58, s60\n"
"ds_read_b64_tr_b16 a[206:207], v245 offset:53504\n"
"v_add_f32 v44, v44, v5\n"
"v_mfma_f32_16x16x32_bf16 v[220:223], a[164:167], v[100:103], v[220:223]\n"
"s_add_u32 s58, s58, s56\n"
"v_exp_f32 v13, v13\n"
"v_mfma_f32_16x16x32_bf16 v[220:223], a[168:171], v[104:107], v[220:223]\n"
"s_add_u32 s58, s58, 1\n"
"ds_read_b64_tr_b16 a[208:209], v244 offset:49664\n"
"v_cvt_pk_bf16_f32 v22, v12, v13\n"
"v_mfma_f32_16x16x32_bf16 v[220:223], a[172:175], v[108:111], v[220:223]\n"
"s_and_b32 s58, s58, 3\n"
"v_exp_f32 v14, v14\n"
"v_mfma_f32_16x16x32_bf16 v[224:227], a[176:179], v[96:99], v[32:35]\n"
"s_lshl_b32 s58, s58, 3\n"
"ds_read_b64_tr_b16 a[210:211], v244 offset:53760\n"
"v_add_f32 v44, v44, v6\n"
"v_mfma_f32_16x16x32_bf16 v[224:227], a[180:183], v[100:103], v[224:227]\n"
"s_add_u32 s58, s58, s59\n"
"v_exp_f32 v15, v15\n"
"v_mfma_f32_16x16x32_bf16 v[224:227], a[184:187], v[104:107], v[224:227]\n"
"s_mul_i32 s58, s58, 36864\n"
"ds_read_b64_tr_b16 a[212:213], v245 offset:49664\n"
"v_cvt_pk_bf16_f32 v23, v14, v15\n"
"v_mfma_f32_16x16x32_bf16 v[224:227], a[188:191], v[108:111], v[224:227]\n"
"s_add_u32 s22, s58, s57\n"
"v_exp_f32 v220, v220\n"
"v_mfma_f32_16x16x32_bf16 a[32:35], a[224:227], v[20:23], a[32:35]\n"
"s_add_u32 s58, s41, 4\n"
"buffer_load_dwordx4 v247, s[8:11], s84 offen offset:2048 lds\n"
"v_mfma_f32_16x16x32_bf16 a[36:39], a[228:231], v[20:23], a[36:39]\n"
"s_mul_i32 s59, s58, 43\n"
"v_exp_f32 v221, v221\n"
"v_mfma_f32_16x16x32_bf16 a[40:43], a[232:235], v[20:23], a[40:43]\n"
"s_lshr_b32 s59, s59, 7\n"
"ds_read_b64_tr_b16 a[214:215], v245 offset:53760\n"
"v_cvt_pk_bf16_f32 v16, v220, v221\n"
"v_mfma_f32_16x16x32_bf16 a[44:47], a[236:239], v[20:23], a[44:47]\n"
"s_mul_i32 s60, s59, 3\n"
"v_exp_f32 v222, v222\n"
"v_mfma_f32_16x16x32_bf16 a[48:51], a[240:243], v[20:23], a[48:51]\n"
"s_sub_u32 s58, s58, s60\n"
"ds_read_b64_tr_b16 a[216:217], v244 offset:49920\n"
"v_add_f32 v44, v44, v7\n"
"v_mfma_f32_16x16x32_bf16 a[52:55], a[244:247], v[20:23], a[52:55]\n"
"s_add_u32 s58, s58, s56\n"
"v_exp_f32 v223, v223\n"
"v_mfma_f32_16x16x32_bf16 a[56:59], a[248:251], v[20:23], a[56:59]\n"
"s_add_u32 s58, s58, 1\n"
"ds_read_b64_tr_b16 a[218:219], v244 offset:54016\n"
"v_cvt_pk_bf16_f32 v17, v222, v223\n"
"v_mfma_f32_16x16x32_bf16 a[60:63], a[252:255], v[20:23], a[60:63]\n"
"s_add_u32 s49, s49, 285696\n"
"v_exp_f32 v224, v224\n"
"v_mfma_f32_16x16x32_bf16 v[48:51], v[40:43], v[20:23], v[48:51]\n"
"ds_read_b64_tr_b16 a[220:221], v245 offset:49920\n"
"v_add_f32 v52, v52, v220\n"
"v_mfma_f32_16x16x32_bf16 v[228:231], a[160:163], v[112:115], v[36:39]\n"
"v_exp_f32 v225, v225\n"
"v_mfma_f32_16x16x32_bf16 v[228:231], a[164:167], v[116:119], v[228:231]\n"
"ds_read_b64_tr_b16 a[222:223], v245 offset:54016\n"
"v_cvt_pk_bf16_f32 v18, v224, v225\n"
"v_mfma_f32_16x16x32_bf16 v[228:231], a[168:171], v[120:123], v[228:231]\n"
"v_exp_f32 v226, v226\n"
"v_mfma_f32_16x16x32_bf16 v[228:231], a[172:175], v[124:127], v[228:231]\n"
"v_add_f32 v52, v52, v221\n"
"v_add_f32 v52, v52, v222\n"
"v_mfma_f32_16x16x32_bf16 v[232:235], a[176:179], v[112:115], v[36:39]\n"
"v_exp_f32 v227, v227\n"
"v_mfma_f32_16x16x32_bf16 v[232:235], a[180:183], v[116:119], v[232:235]\n"
"v_cvt_pk_bf16_f32 v19, v226, v227\n"
"v_add_f32 v52, v52, v223\n"
"v_mfma_f32_16x16x32_bf16 v[232:235], a[184:187], v[120:123], v[232:235]\n"
"buffer_load_dwordx4 v247, s[8:11], s84 offen offset:3072 lds\n"
"v_mfma_f32_16x16x32_bf16 v[232:235], a[188:191], v[124:127], v[232:235]\n"
"v_exp_f32 v228, v228\n"
"v_mfma_f32_16x16x32_bf16 a[64:67], a[224:227], v[16:19], a[64:67]\n"
"v_add_f32 v52, v52, v224\n"
"v_add_f32 v52, v52, v225\n"
"v_mfma_f32_16x16x32_bf16 a[68:71], a[228:231], v[16:19], a[68:71]\n"
"v_exp_f32 v229, v229\n"
"v_mfma_f32_16x16x32_bf16 a[72:75], a[232:235], v[16:19], a[72:75]\n"
"buffer_load_dwordx4 v[136:139], v252, s[64:67], s49 offen\n"
"v_cvt_pk_bf16_f32 v20, v228, v229\n"
"v_add_f32 v52, v52, v226\n"
"v_mfma_f32_16x16x32_bf16 a[76:79], a[236:239], v[16:19], a[76:79]\n"
"s_and_b32 s58, s58, 3\n"
"v_exp_f32 v230, v230\n"
"v_mfma_f32_16x16x32_bf16 a[80:83], a[240:243], v[16:19], a[80:83]\n"
"s_waitcnt lgkmcnt(8)\n"
"v_mfma_f32_16x16x32_bf16 a[84:87], a[244:247], v[16:19], a[84:87]\n"
"s_lshl_b32 s58, s58, 3\n"
"v_exp_f32 v231, v231\n"
"v_mfma_f32_16x16x32_bf16 a[88:91], a[248:251], v[16:19], a[88:91]\n"
"s_add_u32 s58, s58, s59\n"
"v_cvt_pk_bf16_f32 v21, v230, v231\n"
"v_add_f32 v52, v52, v227\n"
"v_mfma_f32_16x16x32_bf16 a[92:95], a[252:255], v[16:19], a[92:95]\n"
"s_mul_i32 s58, s58, 36864\n"
"v_exp_f32 v232, v232\n"
"v_mfma_f32_16x16x32_bf16 v[0:3], a[128:131], v[64:67], v[24:27]\n"
"s_add_u32 s23, s58, s57\n"
"ds_read_b128 a[160:163], v241 offset:40960\n"
"v_mfma_f32_16x16x32_bf16 v[0:3], a[132:135], v[68:71], v[0:3]\n"
"s_add_u32 s58, s41, 8\n"
"v_exp_f32 v233, v233\n"
"v_mfma_f32_16x16x32_bf16 v[0:3], a[136:139], v[72:75], v[0:3]\n"
"s_mul_i32 s59, s58, 43\n"
"ds_read_b128 a[164:167], v241 offset:41216\n"
"v_cvt_pk_bf16_f32 v22, v232, v233\n"
"v_mfma_f32_16x16x32_bf16 v[0:3], a[140:143], v[76:79], v[0:3]\n"
"s_lshr_b32 s59, s59, 7\n"
"v_exp_f32 v234, v234\n"
"v_mfma_f32_16x16x32_bf16 v[4:7], a[144:147], v[64:67], v[24:27]\n"
"s_mul_i32 s60, s59, 3\n"
"ds_read_b128 a[168:171], v241 offset:41472\n"
"v_mfma_f32_16x16x32_bf16 v[4:7], a[148:151], v[68:71], v[4:7]\n"
"s_sub_u32 s58, s58, s60\n"
"v_exp_f32 v235, v235\n"
"v_mfma_f32_16x16x32_bf16 v[4:7], a[152:155], v[72:75], v[4:7]\n"
"s_add_u32 s58, s58, s56\n"
"ds_read_b128 a[172:175], v241 offset:41728\n"
"v_cvt_pk_bf16_f32 v23, v234, v235\n"
"v_mfma_f32_16x16x32_bf16 v[4:7], a[156:159], v[76:79], v[4:7]\n"
"s_add_u32 m0, s86, 81920\n"
"v_exp_f32 v0, v0\n"
"v_mfma_f32_16x16x32_bf16 a[96:99], a[224:227], v[20:23], a[96:99]\n"
"s_add_u32 s58, s58, 1\n"
"buffer_load_dwordx4 v246, s[12:15], s84 offen offset:0 lds\n"
"v_mfma_f32_16x16x32_bf16 a[100:103], a[228:231], v[20:23], a[100:103]\n"
"s_and_b32 s58, s58, 3\n"
"v_exp_f32 v1, v1\n"
"v_mfma_f32_16x16x32_bf16 a[104:107], a[232:235], v[20:23], a[104:107]\n"
"s_lshl_b32 s58, s58, 3\n"
"ds_read_b128 a[176:179], v241 offset:45056\n"
"v_cvt_pk_bf16_f32 v16, v0, v1\n"
"v_mfma_f32_16x16x32_bf16 a[108:111], a[236:239], v[20:23], a[108:111]\n"
"s_add_u32 s58, s58, s59\n"
"v_exp_f32 v2, v2\n"
"v_mfma_f32_16x16x32_bf16 a[112:115], a[240:243], v[20:23], a[112:115]\n"
"s_mul_i32 s58, s58, 36864\n"
"ds_read_b128 a[180:183], v241 offset:45312\n"
"v_add_f32 v44, v44, v0\n"
"v_mfma_f32_16x16x32_bf16 a[116:119], a[244:247], v[20:23], a[116:119]\n"
"v_exp_f32 v3, v3\n"
"v_mfma_f32_16x16x32_bf16 a[120:123], a[248:251], v[20:23], a[120:123]\n"
"ds_read_b128 a[184:187], v241 offset:45568\n"
"v_cvt_pk_bf16_f32 v17, v2, v3\n"
"v_mfma_f32_16x16x32_bf16 a[124:127], a[252:255], v[20:23], a[124:127]\n"
"v_exp_f32 v4, v4\n"
"v_mfma_f32_16x16x32_bf16 v[56:59], v[40:43], v[20:23], v[56:59]\n"
"ds_read_b128 a[188:191], v241 offset:45824\n"
"v_add_f32 v44, v44, v1\n"
"v_mfma_f32_16x16x32_bf16 v[8:11], a[128:131], v[80:83], v[28:31]\n"
"v_exp_f32 v5, v5\n"
"v_mfma_f32_16x16x32_bf16 v[8:11], a[132:135], v[84:87], v[8:11]\n"
"ds_read_b64_tr_b16 a[224:225], v244 offset:57344\n"
"v_cvt_pk_bf16_f32 v18, v4, v5\n"
"v_mfma_f32_16x16x32_bf16 v[8:11], a[136:139], v[88:91], v[8:11]\n"
"v_exp_f32 v6, v6\n"
"v_mfma_f32_16x16x32_bf16 v[8:11], a[140:143], v[92:95], v[8:11]\n"
"ds_read_b64_tr_b16 a[226:227], v244 offset:61440\n"
"v_add_f32 v44, v44, v2\n"
"v_mfma_f32_16x16x32_bf16 v[12:15], a[144:147], v[80:83], v[28:31]\n"
"v_exp_f32 v7, v7\n"
"v_mfma_f32_16x16x32_bf16 v[12:15], a[148:151], v[84:87], v[12:15]\n"
"ds_read_b64_tr_b16 a[228:229], v245 offset:57344\n"
"buffer_load_dwordx4 v[172:175], v252, s[68:71], s49 offen\n"
"v_cvt_pk_bf16_f32 v19, v6, v7\n"
"v_mfma_f32_16x16x32_bf16 v[12:15], a[152:155], v[88:91], v[12:15]\n"
"s_add_u32 s36, s58, s57\n"
"buffer_load_dwordx4 v247, s[12:15], s84 offen offset:1024 lds\n"
"v_mfma_f32_16x16x32_bf16 v[12:15], a[156:159], v[92:95], v[12:15]\n"
"s_waitcnt lgkmcnt(11)\n"
"v_exp_f32 v8, v8\n"
"v_mfma_f32_16x16x32_bf16 a[0:3], a[192:195], v[16:19], a[0:3]\n"
"s_add_u32 s58, s41, 12\n"
"ds_read_b64_tr_b16 a[230:231], v245 offset:61440\n"
"v_add_f32 v44, v44, v3\n"
"v_mfma_f32_16x16x32_bf16 a[4:7], a[196:199], v[16:19], a[4:7]\n"
"s_mul_i32 s59, s58, 43\n"
"v_exp_f32 v9, v9\n"
"v_mfma_f32_16x16x32_bf16 a[8:11], a[200:203], v[16:19], a[8:11]\n"
"s_lshr_b32 s59, s59, 7\n"
"ds_read_b64_tr_b16 a[232:233], v244 offset:57600\n"
"v_cvt_pk_bf16_f32 v20, v8, v9\n"
"v_mfma_f32_16x16x32_bf16 a[12:15], a[204:207], v[16:19], a[12:15]\n"
"s_mul_i32 s60, s59, 3\n"
"v_exp_f32 v10, v10\n"
"v_mfma_f32_16x16x32_bf16 a[16:19], a[208:211], v[16:19], a[16:19]\n"
"s_sub_u32 s58, s58, s60\n"
"ds_read_b64_tr_b16 a[234:235], v244 offset:61696\n"
"v_add_f32 v44, v44, v4\n"
"v_mfma_f32_16x16x32_bf16 a[20:23], a[212:215], v[16:19], a[20:23]\n"
"s_add_u32 s58, s58, s56\n"
"v_exp_f32 v11, v11\n"
"v_mfma_f32_16x16x32_bf16 a[24:27], a[216:219], v[16:19], a[24:27]\n"
"s_add_u32 s58, s58, 1\n"
"ds_read_b64_tr_b16 a[236:237], v245 offset:57600\n"
"v_cvt_pk_bf16_f32 v21, v10, v11\n"
"v_mfma_f32_16x16x32_bf16 a[28:31], a[220:223], v[16:19], a[28:31]\n"
"s_and_b32 s58, s58, 3\n"
"v_exp_f32 v12, v12\n"
"v_mfma_f32_16x16x32_bf16 v[220:223], a[128:131], v[96:99], v[32:35]\n"
"s_lshl_b32 s58, s58, 3\n"
"ds_read_b64_tr_b16 a[238:239], v245 offset:61696\n"
"v_add_f32 v44, v44, v5\n"
"v_mfma_f32_16x16x32_bf16 v[220:223], a[132:135], v[100:103], v[220:223]\n"
"s_add_u32 s58, s58, s59\n"
"v_exp_f32 v13, v13\n"
"v_mfma_f32_16x16x32_bf16 v[220:223], a[136:139], v[104:107], v[220:223]\n"
"s_mul_i32 s58, s58, 36864\n"
"ds_read_b64_tr_b16 a[240:241], v244 offset:57856\n"
"v_cvt_pk_bf16_f32 v22, v12, v13\n"
"v_mfma_f32_16x16x32_bf16 v[220:223], a[140:143], v[108:111], v[220:223]\n"
"s_add_u32 s37, s58, s57\n"
"v_exp_f32 v14, v14\n"
"v_mfma_f32_16x16x32_bf16 v[224:227], a[144:147], v[96:99], v[32:35]\n"
"s_add_u32 s58, s41, 16\n"
"ds_read_b64_tr_b16 a[242:243], v244 offset:61952\n"
"v_add_f32 v44, v44, v6\n"
"v_mfma_f32_16x16x32_bf16 v[224:227], a[148:151], v[100:103], v[224:227]\n"
"s_mul_i32 s59, s58, 43\n"
"v_exp_f32 v15, v15\n"
"v_mfma_f32_16x16x32_bf16 v[224:227], a[152:155], v[104:107], v[224:227]\n"
"s_lshr_b32 s59, s59, 7\n"
"ds_read_b64_tr_b16 a[244:245], v245 offset:57856\n"
"v_cvt_pk_bf16_f32 v23, v14, v15\n"
"v_mfma_f32_16x16x32_bf16 v[224:227], a[156:159], v[108:111], v[224:227]\n"
"s_add_u32 s49, s49, 9216\n"
"v_exp_f32 v220, v220\n"
"v_mfma_f32_16x16x32_bf16 a[32:35], a[192:195], v[20:23], a[32:35]\n"
"buffer_load_dwordx4 v246, s[12:15], s84 offen offset:2048 lds\n"
"v_mfma_f32_16x16x32_bf16 a[36:39], a[196:199], v[20:23], a[36:39]\n"
"v_exp_f32 v221, v221\n"
"v_mfma_f32_16x16x32_bf16 a[40:43], a[200:203], v[20:23], a[40:43]\n"
"ds_read_b64_tr_b16 a[246:247], v245 offset:61952\n"
"v_cvt_pk_bf16_f32 v16, v220, v221\n"
"v_mfma_f32_16x16x32_bf16 a[44:47], a[204:207], v[20:23], a[44:47]\n"
"v_exp_f32 v222, v222\n"
"v_mfma_f32_16x16x32_bf16 a[48:51], a[208:211], v[20:23], a[48:51]\n"
"ds_read_b64_tr_b16 a[248:249], v244 offset:58112\n"
"v_add_f32 v44, v44, v7\n"
"v_mfma_f32_16x16x32_bf16 a[52:55], a[212:215], v[20:23], a[52:55]\n"
"v_exp_f32 v223, v223\n"
"v_mfma_f32_16x16x32_bf16 a[56:59], a[216:219], v[20:23], a[56:59]\n"
"ds_read_b64_tr_b16 a[250:251], v244 offset:62208\n"
"v_cvt_pk_bf16_f32 v17, v222, v223\n"
"v_mfma_f32_16x16x32_bf16 a[60:63], a[220:223], v[20:23], a[60:63]\n"
"v_exp_f32 v224, v224\n"
"v_mfma_f32_16x16x32_bf16 v[48:51], v[40:43], v[20:23], v[48:51]\n"
"ds_read_b64_tr_b16 a[252:253], v245 offset:58112\n"
"v_add_f32 v52, v52, v220\n"
"v_mfma_f32_16x16x32_bf16 v[228:231], a[128:131], v[112:115], v[36:39]\n"
"v_exp_f32 v225, v225\n"
"v_mfma_f32_16x16x32_bf16 v[228:231], a[132:135], v[116:119], v[228:231]\n"
"ds_read_b64_tr_b16 a[254:255], v245 offset:62208\n"
"buffer_load_dwordx4 v[140:143], v252, s[64:67], s49 offen\n"
"v_cvt_pk_bf16_f32 v18, v224, v225\n"
"v_mfma_f32_16x16x32_bf16 v[228:231], a[136:139], v[120:123], v[228:231]\n"
"s_mul_i32 s60, s59, 3\n"
"v_exp_f32 v226, v226\n"
"v_mfma_f32_16x16x32_bf16 v[228:231], a[140:143], v[124:127], v[228:231]\n"
"s_sub_u32 s58, s58, s60\n"
"v_add_f32 v52, v52, v221\n"
"v_add_f32 v52, v52, v222\n"
"v_mfma_f32_16x16x32_bf16 v[232:235], a[144:147], v[112:115], v[36:39]\n"
"s_add_u32 s58, s58, s56\n"
"v_exp_f32 v227, v227\n"
"v_mfma_f32_16x16x32_bf16 v[232:235], a[148:151], v[116:119], v[232:235]\n"
"s_add_u32 s58, s58, 1\n"
"v_cvt_pk_bf16_f32 v19, v226, v227\n"
"v_add_f32 v52, v52, v223\n"
"v_mfma_f32_16x16x32_bf16 v[232:235], a[152:155], v[120:123], v[232:235]\n"
"s_and_b32 s58, s58, 3\n"
"buffer_load_dwordx4 v247, s[12:15], s84 offen offset:3072 lds\n"
"v_mfma_f32_16x16x32_bf16 v[232:235], a[156:159], v[124:127], v[232:235]\n"
"s_add_u32 s84, s84, 16384\n"
"v_exp_f32 v228, v228\n"
"v_mfma_f32_16x16x32_bf16 a[64:67], a[192:195], v[16:19], a[64:67]\n"
"s_lshl_b32 s58, s58, 3\n"
"v_add_f32 v52, v52, v224\n"
"v_add_f32 v52, v52, v225\n"
"v_mfma_f32_16x16x32_bf16 a[68:71], a[196:199], v[16:19], a[68:71]\n"
"s_add_u32 s58, s58, s59\n"
"v_exp_f32 v229, v229\n"
"v_mfma_f32_16x16x32_bf16 a[72:75], a[200:203], v[16:19], a[72:75]\n"
"s_mul_i32 s58, s58, 36864\n"
"v_cvt_pk_bf16_f32 v20, v228, v229\n"
"v_add_f32 v52, v52, v226\n"
"v_mfma_f32_16x16x32_bf16 a[76:79], a[204:207], v[16:19], a[76:79]\n"
"s_waitcnt vmcnt(22) lgkmcnt(0)\n"
"v_exp_f32 v230, v230\n"
"v_mfma_f32_16x16x32_bf16 a[80:83], a[208:211], v[16:19], a[80:83]\n"
"s_barrier\n"
"v_mfma_f32_16x16x32_bf16 a[84:87], a[212:215], v[16:19], a[84:87]\n"
"s_add_u32 s38, s58, s57\n"
"v_exp_f32 v231, v231\n"
"v_mfma_f32_16x16x32_bf16 a[88:91], a[216:219], v[16:19], a[88:91]\n"
"s_add_u32 s58, s41, 20\n"
"v_cvt_pk_bf16_f32 v21, v230, v231\n"
"v_add_f32 v52, v52, v227\n"
"v_mfma_f32_16x16x32_bf16 a[92:95], a[220:223], v[16:19], a[92:95]\n"
"s_mul_i32 s59, s58, 43\n"
"v_exp_f32 v232, v232\n"
"v_mfma_f32_16x16x32_bf16 v[0:3], a[160:163], v[64:67], v[24:27]\n"
"s_lshr_b32 s59, s59, 7\n"
"ds_read_b128 a[128:131], v240 offset:0\n"
"v_mfma_f32_16x16x32_bf16 v[0:3], a[164:167], v[68:71], v[0:3]\n"
"s_mul_i32 s60, s59, 3\n"
"v_exp_f32 v233, v233\n"
"v_mfma_f32_16x16x32_bf16 v[0:3], a[168:171], v[72:75], v[0:3]\n"
"s_sub_u32 s58, s58, s60\n"
"ds_read_b128 a[132:135], v240 offset:256\n"
"v_cvt_pk_bf16_f32 v22, v232, v233\n"
"v_mfma_f32_16x16x32_bf16 v[0:3], a[172:175], v[76:79], v[0:3]\n"
"s_add_u32 s58, s58, s56\n"
"v_exp_f32 v234, v234\n"
"v_mfma_f32_16x16x32_bf16 v[4:7], a[176:179], v[64:67], v[24:27]\n"
"s_add_u32 s58, s58, 1\n"
"ds_read_b128 a[136:139], v240 offset:512\n"
"v_mfma_f32_16x16x32_bf16 v[4:7], a[180:183], v[68:71], v[4:7]\n"
"v_exp_f32 v235, v235\n"
"v_mfma_f32_16x16x32_bf16 v[4:7], a[184:187], v[72:75], v[4:7]\n"
"ds_read_b128 a[140:143], v240 offset:768\n"
"v_cvt_pk_bf16_f32 v23, v234, v235\n"
"v_mfma_f32_16x16x32_bf16 v[4:7], a[188:191], v[76:79], v[4:7]\n"
"s_add_u32 m0, s86, 98304\n"
"v_exp_f32 v0, v0\n"
"v_mfma_f32_16x16x32_bf16 a[96:99], a[192:195], v[20:23], a[96:99]\n"
"buffer_load_dwordx4 v246, s[8:11], s84 offen offset:0 lds\n"
"v_mfma_f32_16x16x32_bf16 a[100:103], a[196:199], v[20:23], a[100:103]\n"
"v_exp_f32 v1, v1\n"
"v_mfma_f32_16x16x32_bf16 a[104:107], a[200:203], v[20:23], a[104:107]\n"
"ds_read_b128 a[144:147], v240 offset:4096\n"
"v_cvt_pk_bf16_f32 v16, v0, v1\n"
"v_mfma_f32_16x16x32_bf16 a[108:111], a[204:207], v[20:23], a[108:111]\n"
"v_exp_f32 v2, v2\n"
"v_mfma_f32_16x16x32_bf16 a[112:115], a[208:211], v[20:23], a[112:115]\n"
"ds_read_b128 a[148:151], v240 offset:4352\n"
"v_add_f32 v44, v44, v0\n"
"v_mfma_f32_16x16x32_bf16 a[116:119], a[212:215], v[20:23], a[116:119]\n"
"v_exp_f32 v3, v3\n"
"v_mfma_f32_16x16x32_bf16 a[120:123], a[216:219], v[20:23], a[120:123]\n"
"ds_read_b128 a[152:155], v240 offset:4608\n"
"buffer_load_dwordx4 v[176:179], v252, s[68:71], s49 offen\n"
"v_cvt_pk_bf16_f32 v17, v2, v3\n"
"v_mfma_f32_16x16x32_bf16 a[124:127], a[220:223], v[20:23], a[124:127]\n"
"s_and_b32 s58, s58, 3\n"
"v_exp_f32 v4, v4\n"
"v_mfma_f32_16x16x32_bf16 v[56:59], v[40:43], v[20:23], v[56:59]\n"
"s_lshl_b32 s58, s58, 3\n"
"ds_read_b128 a[156:159], v240 offset:4864\n"
"v_add_f32 v44, v44, v1\n"
"v_mfma_f32_16x16x32_bf16 v[8:11], a[160:163], v[80:83], v[28:31]\n"
"s_add_u32 s58, s58, s59\n"
"v_exp_f32 v5, v5\n"
"v_mfma_f32_16x16x32_bf16 v[8:11], a[164:167], v[84:87], v[8:11]\n"
"s_mul_i32 s58, s58, 36864\n"
"ds_read_b64_tr_b16 a[192:193], v242 offset:16384\n"
"v_cvt_pk_bf16_f32 v18, v4, v5\n"
"v_mfma_f32_16x16x32_bf16 v[8:11], a[168:171], v[88:91], v[8:11]\n"
"s_add_u32 s39, s58, s57\n"
"v_exp_f32 v6, v6\n"
"v_mfma_f32_16x16x32_bf16 v[8:11], a[172:175], v[92:95], v[8:11]\n"
"s_mul_i32 s58, s41, 9216\n"
"ds_read_b64_tr_b16 a[194:195], v242 offset:20480\n"
"v_add_f32 v44, v44, v2\n"
"v_mfma_f32_16x16x32_bf16 v[12:15], a[176:179], v[80:83], v[28:31]\n"
"s_add_u32 s58, s58, s40\n"
"v_exp_f32 v7, v7\n"
"v_mfma_f32_16x16x32_bf16 v[12:15], a[180:183], v[84:87], v[12:15]\n"
"s_sub_u32 s53, s58, 27648\n"
"ds_read_b64_tr_b16 a[196:197], v243 offset:16384\n"
"v_cvt_pk_bf16_f32 v19, v6, v7\n"
"v_mfma_f32_16x16x32_bf16 v[12:15], a[184:187], v[88:91], v[12:15]\n"
"s_add_u32 s58, s58, 131072\n"
"buffer_load_dwordx4 v246, s[8:11], s84 offen offset:1024 lds\n"
"v_mfma_f32_16x16x32_bf16 v[12:15], a[188:191], v[92:95], v[12:15]\n"
"s_waitcnt lgkmcnt(11)\n"
"v_exp_f32 v8, v8\n"
"v_mfma_f32_16x16x32_bf16 a[0:3], a[224:227], v[16:19], a[0:3]\n"
"s_cmp_lt_u32 s41, 3\n"
"s_cselect_b32 s51, s58, s40\n"
"ds_read_b64_tr_b16 a[198:199], v243 offset:20480\n"
"v_add_f32 v44, v44, v3\n"
"v_mfma_f32_16x16x32_bf16 a[4:7], a[228:231], v[16:19], a[4:7]\n"
"s_nop 0\n"
"v_exp_f32 v9, v9\n"
"v_mfma_f32_16x16x32_bf16 a[8:11], a[232:235], v[16:19], a[8:11]\n"
"s_and_b32 s58, s56, 1\n"
"ds_read_b64_tr_b16 a[200:201], v242 offset:16640\n"
"v_cvt_pk_bf16_f32 v20, v8, v9\n"
"v_mfma_f32_16x16x32_bf16 a[12:15], a[236:239], v[16:19], a[12:15]\n"
"s_lshl_b32 s59, s58, 5\n"
"v_exp_f32 v10, v10\n"
"v_mfma_f32_16x16x32_bf16 a[16:19], a[240:243], v[16:19], a[16:19]\n"
"s_add_u32 s59, s59, s40\n"
"ds_read_b64_tr_b16 a[202:203], v242 offset:20736\n"
"v_add_f32 v44, v44, v4\n"
"v_mfma_f32_16x16x32_bf16 a[20:23], a[244:247], v[16:19], a[20:23]\n"
"s_xor_b32 s60, s58, 1\n"
"v_exp_f32 v11, v11\n"
"v_mfma_f32_16x16x32_bf16 a[24:27], a[248:251], v[16:19], a[24:27]\n"
"s_lshl_b32 s60, s60, 8\n"
"ds_read_b64_tr_b16 a[204:205], v243 offset:16640\n"
"v_cvt_pk_bf16_f32 v21, v10, v11\n"
"v_mfma_f32_16x16x32_bf16 a[28:31], a[252:255], v[16:19], a[28:31]\n"
"s_add_u32 s49, s49, 285696\n"
"v_exp_f32 v12, v12\n"
"v_mfma_f32_16x16x32_bf16 v[220:223], a[160:163], v[96:99], v[32:35]\n"
"ds_read_b64_tr_b16 a[206:207], v243 offset:20736\n"
"v_add_f32 v44, v44, v5\n"
"v_mfma_f32_16x16x32_bf16 v[220:223], a[164:167], v[100:103], v[220:223]\n"
"v_exp_f32 v13, v13\n"
"v_mfma_f32_16x16x32_bf16 v[220:223], a[168:171], v[104:107], v[220:223]\n"
"ds_read_b64_tr_b16 a[208:209], v242 offset:16896\n"
"v_cvt_pk_bf16_f32 v22, v12, v13\n"
"v_mfma_f32_16x16x32_bf16 v[220:223], a[172:175], v[108:111], v[220:223]\n"
"v_exp_f32 v14, v14\n"
"v_mfma_f32_16x16x32_bf16 v[224:227], a[176:179], v[96:99], v[32:35]\n"
"ds_read_b64_tr_b16 a[210:211], v242 offset:20992\n"
"v_add_f32 v44, v44, v6\n"
"v_mfma_f32_16x16x32_bf16 v[224:227], a[180:183], v[100:103], v[224:227]\n"
"v_exp_f32 v15, v15\n"
"v_mfma_f32_16x16x32_bf16 v[224:227], a[184:187], v[104:107], v[224:227]\n"
"ds_read_b64_tr_b16 a[212:213], v243 offset:16896\n"
"v_cvt_pk_bf16_f32 v23, v14, v15\n"
"v_mfma_f32_16x16x32_bf16 v[224:227], a[188:191], v[108:111], v[224:227]\n"
"v_exp_f32 v220, v220\n"
"v_mfma_f32_16x16x32_bf16 a[32:35], a[224:227], v[20:23], a[32:35]\n"
"buffer_load_dwordx4 v247, s[8:11], s84 offen offset:2048 lds\n"
"v_mfma_f32_16x16x32_bf16 a[36:39], a[228:231], v[20:23], a[36:39]\n"
"v_exp_f32 v221, v221\n"
"v_mfma_f32_16x16x32_bf16 a[40:43], a[232:235], v[20:23], a[40:43]\n"
"ds_read_b64_tr_b16 a[214:215], v243 offset:20992\n"
"buffer_load_dwordx4 v[144:147], v252, s[64:67], s49 offen\n"
"v_cvt_pk_bf16_f32 v16, v220, v221\n"
"v_mfma_f32_16x16x32_bf16 a[44:47], a[236:239], v[20:23], a[44:47]\n"
"s_add_u32 s60, s60, s40\n"
"v_exp_f32 v222, v222\n"
"v_mfma_f32_16x16x32_bf16 a[48:51], a[240:243], v[20:23], a[48:51]\n"
"ds_read_b64_tr_b16 a[216:217], v242 offset:17152\n"
"v_add_f32 v44, v44, v7\n"
"v_mfma_f32_16x16x32_bf16 a[52:55], a[244:247], v[20:23], a[52:55]\n"
"v_exp_f32 v223, v223\n"
"v_mfma_f32_16x16x32_bf16 a[56:59], a[248:251], v[20:23], a[56:59]\n"
"ds_read_b64_tr_b16 a[218:219], v242 offset:21248\n"
"v_cvt_pk_bf16_f32 v17, v222, v223\n"
"v_mfma_f32_16x16x32_bf16 a[60:63], a[252:255], v[20:23], a[60:63]\n"
"v_exp_f32 v224, v224\n"
"v_mfma_f32_16x16x32_bf16 v[48:51], v[40:43], v[20:23], v[48:51]\n"
"ds_read_b64_tr_b16 a[220:221], v243 offset:17152\n"
"v_add_f32 v52, v52, v220\n"
"v_mfma_f32_16x16x32_bf16 v[228:231], a[160:163], v[112:115], v[36:39]\n"
"v_exp_f32 v225, v225\n"
"v_mfma_f32_16x16x32_bf16 v[228:231], a[164:167], v[116:119], v[228:231]\n"
"ds_read_b64_tr_b16 a[222:223], v243 offset:21248\n"
"v_cvt_pk_bf16_f32 v18, v224, v225\n"
"v_lshrrev_b32 v217, 2, v248\n"
"v_mfma_f32_16x16x32_bf16 v[228:231], a[168:171], v[120:123], v[228:231]\n"
"v_exp_f32 v226, v226\n"
"v_mfma_f32_16x16x32_bf16 v[228:231], a[172:175], v[124:127], v[228:231]\n"
"v_add_f32 v52, v52, v221\n"
"v_add_f32 v52, v52, v222\n"
"v_mfma_f32_16x16x32_bf16 v[232:235], a[176:179], v[112:115], v[36:39]\n"
"v_exp_f32 v227, v227\n"
"v_mfma_f32_16x16x32_bf16 v[232:235], a[180:183], v[116:119], v[232:235]\n"
"v_cvt_pk_bf16_f32 v19, v226, v227\n"
"v_add_f32 v52, v52, v223\n"
"v_mfma_f32_16x16x32_bf16 v[232:235], a[184:187], v[120:123], v[232:235]\n"
"buffer_load_dwordx4 v247, s[8:11], s84 offen offset:3072 lds\n"
"v_mul_u32_u24 v203, 288, v217\n"
"v_mfma_f32_16x16x32_bf16 v[232:235], a[188:191], v[124:127], v[232:235]\n"
"v_exp_f32 v228, v228\n"
"v_mfma_f32_16x16x32_bf16 a[64:67], a[224:227], v[16:19], a[64:67]\n"
"v_add_f32 v52, v52, v224\n"
"v_add_f32 v52, v52, v225\n"
"v_mfma_f32_16x16x32_bf16 a[68:71], a[228:231], v[16:19], a[68:71]\n"
"v_exp_f32 v229, v229\n"
"v_mfma_f32_16x16x32_bf16 a[72:75], a[232:235], v[16:19], a[72:75]\n"
"v_cvt_pk_bf16_f32 v20, v228, v229\n"
"v_add_f32 v52, v52, v226\n"
"v_mfma_f32_16x16x32_bf16 a[76:79], a[236:239], v[16:19], a[76:79]\n"
"v_exp_f32 v230, v230\n"
"v_mfma_f32_16x16x32_bf16 a[80:83], a[240:243], v[16:19], a[80:83]\n"
"s_waitcnt lgkmcnt(8)\n"
"v_and_b32 v217, 3, v248\n"
"v_mfma_f32_16x16x32_bf16 a[84:87], a[244:247], v[16:19], a[84:87]\n"
"v_exp_f32 v231, v231\n"
"v_mfma_f32_16x16x32_bf16 a[88:91], a[248:251], v[16:19], a[88:91]\n"
"v_cvt_pk_bf16_f32 v21, v230, v231\n"
"v_add_f32 v52, v52, v227\n"
"v_mfma_f32_16x16x32_bf16 a[92:95], a[252:255], v[16:19], a[92:95]\n"
"v_exp_f32 v232, v232\n"
"v_mfma_f32_16x16x32_bf16 v[0:3], a[128:131], v[64:67], v[24:27]\n"
"ds_read_b128 a[160:163], v240 offset:8192\n"
"v_lshl_add_u32 v203, v217, 3, v203\n"
"v_mfma_f32_16x16x32_bf16 v[0:3], a[132:135], v[68:71], v[0:3]\n"
"v_exp_f32 v233, v233\n"
"v_mfma_f32_16x16x32_bf16 v[0:3], a[136:139], v[72:75], v[0:3]\n"
"ds_read_b128 a[164:167], v240 offset:8448\n"
"v_cvt_pk_bf16_f32 v22, v232, v233\n"
"v_mfma_f32_16x16x32_bf16 v[0:3], a[140:143], v[76:79], v[0:3]\n"
"v_exp_f32 v234, v234\n"
"v_mfma_f32_16x16x32_bf16 v[4:7], a[144:147], v[64:67], v[24:27]\n"
"ds_read_b128 a[168:171], v240 offset:8704\n"
"v_add_u32 v200, s59, v203\n"
"v_mfma_f32_16x16x32_bf16 v[4:7], a[148:151], v[68:71], v[4:7]\n"
"v_exp_f32 v235, v235\n"
"v_mfma_f32_16x16x32_bf16 v[4:7], a[152:155], v[72:75], v[4:7]\n"
"ds_read_b128 a[172:175], v240 offset:8960\n"
"v_cvt_pk_bf16_f32 v23, v234, v235\n"
"v_mfma_f32_16x16x32_bf16 v[4:7], a[156:159], v[76:79], v[4:7]\n"
"s_add_u32 m0, s86, 114688\n"
"v_exp_f32 v0, v0\n"
"v_mfma_f32_16x16x32_bf16 a[96:99], a[224:227], v[20:23], a[96:99]\n"
"buffer_load_dwordx4 v246, s[12:15], s84 offen offset:0 lds\n"
"v_add_u32 v203, s60, v203\n"
"v_mfma_f32_16x16x32_bf16 a[100:103], a[228:231], v[20:23], a[100:103]\n"
"v_exp_f32 v1, v1\n"
"v_mfma_f32_16x16x32_bf16 a[104:107], a[232:235], v[20:23], a[104:107]\n"
"ds_read_b128 a[176:179], v240 offset:12288\n"
"v_cvt_pk_bf16_f32 v16, v0, v1\n"
"v_mfma_f32_16x16x32_bf16 a[108:111], a[236:239], v[20:23], a[108:111]\n"
"v_exp_f32 v2, v2\n"
"v_mfma_f32_16x16x32_bf16 a[112:115], a[240:243], v[20:23], a[112:115]\n"
"ds_read_b128 a[180:183], v240 offset:12544\n"
"v_add_f32 v44, v44, v0\n"
"v_mfma_f32_16x16x32_bf16 a[116:119], a[244:247], v[20:23], a[116:119]\n"
"v_exp_f32 v3, v3\n"
"v_mfma_f32_16x16x32_bf16 a[120:123], a[248:251], v[20:23], a[120:123]\n"
"ds_read_b128 a[184:187], v240 offset:12800\n"
"v_cvt_pk_bf16_f32 v17, v2, v3\n"
"v_mfma_f32_16x16x32_bf16 a[124:127], a[252:255], v[20:23], a[124:127]\n"
"v_exp_f32 v4, v4\n"
"v_add_u32 v201, 65536, v200\n"
"v_mfma_f32_16x16x32_bf16 v[56:59], v[40:43], v[20:23], v[56:59]\n"
"ds_read_b128 a[188:191], v240 offset:13056\n"
"v_add_f32 v44, v44, v1\n"
"v_mfma_f32_16x16x32_bf16 v[8:11], a[128:131], v[80:83], v[28:31]\n"
"v_exp_f32 v5, v5\n"
"v_mfma_f32_16x16x32_bf16 v[8:11], a[132:135], v[84:87], v[8:11]\n"
"ds_read_b64_tr_b16 a[224:225], v242 offset:24576\n"
"v_cvt_pk_bf16_f32 v18, v4, v5\n"
"v_mfma_f32_16x16x32_bf16 v[8:11], a[136:139], v[88:91], v[8:11]\n"
"v_exp_f32 v6, v6\n"
"v_mfma_f32_16x16x32_bf16 v[8:11], a[140:143], v[92:95], v[8:11]\n"
"ds_read_b64_tr_b16 a[226:227], v242 offset:28672\n"
"v_add_f32 v44, v44, v2\n"
"v_mfma_f32_16x16x32_bf16 v[12:15], a[144:147], v[80:83], v[28:31]\n"
"v_exp_f32 v7, v7\n"
"v_mfma_f32_16x16x32_bf16 v[12:15], a[148:151], v[84:87], v[12:15]\n"
"ds_read_b64_tr_b16 a[228:229], v243 offset:24576\n"
"v_cvt_pk_bf16_f32 v19, v6, v7\n"
"v_add_u32 v204, 65536, v203\n"
"v_mfma_f32_16x16x32_bf16 v[12:15], a[152:155], v[88:91], v[12:15]\n"
"buffer_load_dwordx4 v247, s[12:15], s84 offen offset:1024 lds\n"
"v_add_u32 v202, 131072, v200\n"
"v_mfma_f32_16x16x32_bf16 v[12:15], a[156:159], v[92:95], v[12:15]\n"
"s_waitcnt lgkmcnt(11)\n"
"v_exp_f32 v8, v8\n"
"v_mfma_f32_16x16x32_bf16 a[0:3], a[192:195], v[16:19], a[0:3]\n"
"ds_read_b64_tr_b16 a[230:231], v243 offset:28672\n"
"v_add_f32 v44, v44, v3\n"
"v_mfma_f32_16x16x32_bf16 a[4:7], a[196:199], v[16:19], a[4:7]\n"
"v_exp_f32 v9, v9\n"
"v_mfma_f32_16x16x32_bf16 a[8:11], a[200:203], v[16:19], a[8:11]\n"
"ds_read_b64_tr_b16 a[232:233], v242 offset:24832\n"
"v_cvt_pk_bf16_f32 v20, v8, v9\n"
"v_mfma_f32_16x16x32_bf16 a[12:15], a[204:207], v[16:19], a[12:15]\n"
"v_exp_f32 v10, v10\n"
"v_mfma_f32_16x16x32_bf16 a[16:19], a[208:211], v[16:19], a[16:19]\n"
"ds_read_b64_tr_b16 a[234:235], v242 offset:28928\n"
"v_add_f32 v44, v44, v4\n"
"v_mfma_f32_16x16x32_bf16 a[20:23], a[212:215], v[16:19], a[20:23]\n"
"v_exp_f32 v11, v11\n"
"v_add_u32 v205, 131072, v203\n"
"v_mfma_f32_16x16x32_bf16 a[24:27], a[216:219], v[16:19], a[24:27]\n"
"s_add_u32 s62, s40, 163776\n"
"ds_read_b64_tr_b16 a[236:237], v243 offset:24832\n"
"v_cvt_pk_bf16_f32 v21, v10, v11\n"
"v_mfma_f32_16x16x32_bf16 a[28:31], a[220:223], v[16:19], a[28:31]\n"
"v_exp_f32 v12, v12\n"
"v_mfma_f32_16x16x32_bf16 v[220:223], a[128:131], v[96:99], v[32:35]\n"
"ds_read_b64_tr_b16 a[238:239], v243 offset:28928\n"
"v_add_f32 v44, v44, v5\n"
"v_mfma_f32_16x16x32_bf16 v[220:223], a[132:135], v[100:103], v[220:223]\n"
"v_exp_f32 v13, v13\n"
"v_mfma_f32_16x16x32_bf16 v[220:223], a[136:139], v[104:107], v[220:223]\n"
"ds_read_b64_tr_b16 a[240:241], v242 offset:25088\n"
"v_cvt_pk_bf16_f32 v22, v12, v13\n"
"v_mfma_f32_16x16x32_bf16 v[220:223], a[140:143], v[108:111], v[220:223]\n"
"v_exp_f32 v14, v14\n"
"v_mfma_f32_16x16x32_bf16 v[224:227], a[144:147], v[96:99], v[32:35]\n"
"ds_read_b64_tr_b16 a[242:243], v242 offset:29184\n"
"v_add_f32 v44, v44, v6\n"
"v_mfma_f32_16x16x32_bf16 v[224:227], a[148:151], v[100:103], v[224:227]\n"
"v_exp_f32 v15, v15\n"
"v_lshl_add_u32 v209, v217, 2, s62\n"
"v_mfma_f32_16x16x32_bf16 v[224:227], a[152:155], v[104:107], v[224:227]\n"
"s_lshl_b32 s55, s44, 4\n"
"ds_read_b64_tr_b16 a[244:245], v243 offset:25088\n"
"v_cvt_pk_bf16_f32 v23, v14, v15\n"
"v_mfma_f32_16x16x32_bf16 v[224:227], a[156:159], v[108:111], v[224:227]\n"
"s_add_u32 s55, s55, s43\n"
"v_exp_f32 v220, v220\n"
"v_mfma_f32_16x16x32_bf16 a[32:35], a[192:195], v[20:23], a[32:35]\n"
"s_lshl_b32 s52, s55, 2\n"
"buffer_load_dwordx4 v246, s[12:15], s84 offen offset:2048 lds\n"
"v_mfma_f32_16x16x32_bf16 a[36:39], a[196:199], v[20:23], a[36:39]\n"
"AT_pg10:\n"
"s_cbranch_execz AT_hr\n"
"v_exp_f32 v221, v221\n"
"v_mfma_f32_16x16x32_bf16 a[40:43], a[200:203], v[20:23], a[40:43]\n"
"ds_read_b64_tr_b16 a[246:247], v243 offset:29184\n"
"buffer_load_dwordx4 v[180:183], v252, s[68:71], s49 offen\n"
"v_cvt_pk_bf16_f32 v16, v220, v221\n"
"v_mfma_f32_16x16x32_bf16 a[44:47], a[204:207], v[20:23], a[44:47]\n"
"s_add_u32 s52, s52, s41\n"
"v_exp_f32 v222, v222\n"
"v_mfma_f32_16x16x32_bf16 a[48:51], a[208:211], v[20:23], a[48:51]\n"
"s_lshl_b32 s52, s52, 14\n"
"ds_read_b64_tr_b16 a[248:249], v242 offset:25344\n"
"v_add_f32 v44, v44, v7\n"
"v_mfma_f32_16x16x32_bf16 a[52:55], a[212:215], v[20:23], a[52:55]\n"
"s_add_u32 s52, s52, 9592832\n"
"v_exp_f32 v223, v223\n"
"v_mfma_f32_16x16x32_bf16 a[56:59], a[216:219], v[20:23], a[56:59]\n"
"s_add_u32 s49, s49, 9216\n"
"ds_read_b64_tr_b16 a[250:251], v242 offset:29440\n"
"v_cvt_pk_bf16_f32 v17, v222, v223\n"
"v_mfma_f32_16x16x32_bf16 a[60:63], a[220:223], v[20:23], a[60:63]\n"
"v_exp_f32 v224, v224\n"
"v_mfma_f32_16x16x32_bf16 v[48:51], v[40:43], v[20:23], v[48:51]\n"
"ds_read_b64_tr_b16 a[252:253], v243 offset:25344\n"
"v_add_f32 v52, v52, v220\n"
"v_mfma_f32_16x16x32_bf16 v[228:231], a[128:131], v[112:115], v[36:39]\n"
"v_exp_f32 v225, v225\n"
"v_mfma_f32_16x16x32_bf16 v[228:231], a[132:135], v[116:119], v[228:231]\n"
"ds_read_b64_tr_b16 a[254:255], v243 offset:29440\n"
"v_cvt_pk_bf16_f32 v18, v224, v225\n"
"v_mfma_f32_16x16x32_bf16 v[228:231], a[136:139], v[120:123], v[228:231]\n"
"v_exp_f32 v226, v226\n"
"v_mfma_f32_16x16x32_bf16 v[228:231], a[140:143], v[124:127], v[228:231]\n"
"v_add_f32 v52, v52, v221\n"
"v_add_f32 v52, v52, v222\n"
"v_mfma_f32_16x16x32_bf16 v[232:235], a[144:147], v[112:115], v[36:39]\n"
"v_exp_f32 v227, v227\n"
"v_mfma_f32_16x16x32_bf16 v[232:235], a[148:151], v[116:119], v[232:235]\n"
"v_cvt_pk_bf16_f32 v19, v226, v227\n"
"v_add_f32 v52, v52, v223\n"
"v_mfma_f32_16x16x32_bf16 v[232:235], a[152:155], v[120:123], v[232:235]\n"
"buffer_load_dwordx4 v247, s[12:15], s84 offen offset:3072 lds\n"
"v_mfma_f32_16x16x32_bf16 v[232:235], a[156:159], v[124:127], v[232:235]\n"
"s_add_u32 s84, s84, 16384\n"
"v_exp_f32 v228, v228\n"
"v_mfma_f32_16x16x32_bf16 a[64:67], a[192:195], v[16:19], a[64:67]\n"
"v_add_f32 v52, v52, v224\n"
"v_add_f32 v52, v52, v225\n"
"v_mfma_f32_16x16x32_bf16 a[68:71], a[196:199], v[16:19], a[68:71]\n"
"v_exp_f32 v229, v229\n"
"v_mfma_f32_16x16x32_bf16 a[72:75], a[200:203], v[16:19], a[72:75]\n"
"v_cvt_pk_bf16_f32 v20, v228, v229\n"
"v_add_f32 v52, v52, v226\n"
"v_mfma_f32_16x16x32_bf16 a[76:79], a[204:207], v[16:19], a[76:79]\n"
"s_waitcnt vmcnt(24) lgkmcnt(0)\n"
"v_exp_f32 v230, v230\n"
"v_mfma_f32_16x16x32_bf16 a[80:83], a[208:211], v[16:19], a[80:83]\n"
"s_barrier\n"
"v_mfma_f32_16x16x32_bf16 a[84:87], a[212:215], v[16:19], a[84:87]\n"
"v_exp_f32 v231, v231\n"
"v_mfma_f32_16x16x32_bf16 a[88:91], a[216:219], v[16:19], a[88:91]\n"
"v_cvt_pk_bf16_f32 v21, v230, v231\n"
"v_add_f32 v52, v52, v227\n"
"v_mfma_f32_16x16x32_bf16 a[92:95], a[220:223], v[16:19], a[92:95]\n"
"v_exp_f32 v232, v232\n"
"v_mfma_f32_16x16x32_bf16 v[0:3], a[160:163], v[64:67], v[24:27]\n"
"ds_read_b128 a[128:131], v240 offset:32768\n"
"v_mfma_f32_16x16x32_bf16 v[0:3], a[164:167], v[68:71], v[0:3]\n"
"v_exp_f32 v233, v233\n"
"v_mfma_f32_16x16x32_bf16 v[0:3], a[168:171], v[72:75], v[0:3]\n"
"ds_read_b128 a[132:135], v240 offset:33024\n"
"v_cvt_pk_bf16_f32 v22, v232, v233\n"
"v_mfma_f32_16x16x32_bf16 v[0:3], a[172:175], v[76:79], v[0:3]\n"
"v_exp_f32 v234, v234\n"
"v_mfma_f32_16x16x32_bf16 v[4:7], a[176:179], v[64:67], v[24:27]\n"
"ds_read_b128 a[136:139], v240 offset:33280\n"
"v_mfma_f32_16x16x32_bf16 v[4:7], a[180:183], v[68:71], v[4:7]\n"
"v_exp_f32 v235, v235\n"
"v_mfma_f32_16x16x32_bf16 v[4:7], a[184:187], v[72:75], v[4:7]\n"
"ds_read_b128 a[140:143], v240 offset:33536\n"
"buffer_load_dwordx4 v[148:151], v252, s[64:67], s49 offen\n"
"v_cvt_pk_bf16_f32 v23, v234, v235\n"
"v_mfma_f32_16x16x32_bf16 v[4:7], a[188:191], v[76:79], v[4:7]\n"
"s_mov_b32 m0, s51\n"
"v_exp_f32 v0, v0\n"
"v_mfma_f32_16x16x32_bf16 a[96:99], a[192:195], v[20:23], a[96:99]\n"
"buffer_load_dwordx4 v210, s[24:27], s20 offen offset:0 lds\n"
"v_mfma_f32_16x16x32_bf16 a[100:103], a[196:199], v[20:23], a[100:103]\n"
"v_exp_f32 v1, v1\n"
"v_mfma_f32_16x16x32_bf16 a[104:107], a[200:203], v[20:23], a[104:107]\n"
"ds_read_b128 a[144:147], v240 offset:36864\n"
"v_cvt_pk_bf16_f32 v16, v0, v1\n"
"v_mfma_f32_16x16x32_bf16 a[108:111], a[204:207], v[20:23], a[108:111]\n"
"v_exp_f32 v2, v2\n"
"v_mfma_f32_16x16x32_bf16 a[112:115], a[208:211], v[20:23], a[112:115]\n"
"ds_read_b128 a[148:151], v240 offset:37120\n"
"v_add_f32 v44, v44, v0\n"
"v_mfma_f32_16x16x32_bf16 a[116:119], a[212:215], v[20:23], a[116:119]\n"
"v_exp_f32 v3, v3\n"
"v_mfma_f32_16x16x32_bf16 a[120:123], a[216:219], v[20:23], a[120:123]\n"
"ds_read_b128 a[152:155], v240 offset:37376\n"
"v_cvt_pk_bf16_f32 v17, v2, v3\n"
"v_mfma_f32_16x16x32_bf16 a[124:127], a[220:223], v[20:23], a[124:127]\n"
"v_exp_f32 v4, v4\n"
"v_mfma_f32_16x16x32_bf16 v[56:59], v[40:43], v[20:23], v[56:59]\n"
"ds_read_b128 a[156:159], v240 offset:37632\n"
"v_add_f32 v44, v44, v1\n"
"v_mfma_f32_16x16x32_bf16 v[8:11], a[160:163], v[80:83], v[28:31]\n"
"v_exp_f32 v5, v5\n"
"v_mfma_f32_16x16x32_bf16 v[8:11], a[164:167], v[84:87], v[8:11]\n"
"ds_read_b64_tr_b16 a[192:193], v242 offset:49152\n"
"v_cvt_pk_bf16_f32 v18, v4, v5\n"
"v_mfma_f32_16x16x32_bf16 v[8:11], a[168:171], v[88:91], v[8:11]\n"
"v_exp_f32 v6, v6\n"
"v_mfma_f32_16x16x32_bf16 v[8:11], a[172:175], v[92:95], v[8:11]\n"
"ds_read_b64_tr_b16 a[194:195], v242 offset:53248\n"
"v_add_f32 v44, v44, v2\n"
"v_mfma_f32_16x16x32_bf16 v[12:15], a[176:179], v[80:83], v[28:31]\n"
"v_exp_f32 v7, v7\n"
"v_mfma_f32_16x16x32_bf16 v[12:15], a[180:183], v[84:87], v[12:15]\n"
"s_mov_b32 m0, s51\n"
"ds_read_b64_tr_b16 a[196:197], v243 offset:49152\n"
"v_cvt_pk_bf16_f32 v19, v6, v7\n"
"v_mfma_f32_16x16x32_bf16 v[12:15], a[184:187], v[88:91], v[12:15]\n"
"buffer_load_dwordx4 v211, s[24:27], s20 offen offset:1024 lds\n"
"v_mfma_f32_16x16x32_bf16 v[12:15], a[188:191], v[92:95], v[12:15]\n"
"s_waitcnt lgkmcnt(11)\n"
"v_exp_f32 v8, v8\n"
"v_mfma_f32_16x16x32_bf16 a[0:3], a[224:227], v[16:19], a[0:3]\n"
"ds_read_b64_tr_b16 a[198:199], v243 offset:53248\n"
"v_add_f32 v44, v44, v3\n"
"v_mfma_f32_16x16x32_bf16 a[4:7], a[228:231], v[16:19], a[4:7]\n"
"v_exp_f32 v9, v9\n"
"v_mfma_f32_16x16x32_bf16 a[8:11], a[232:235], v[16:19], a[8:11]\n"
"ds_read_b64_tr_b16 a[200:201], v242 offset:49408\n"
"v_cvt_pk_bf16_f32 v20, v8, v9\n"
"v_mfma_f32_16x16x32_bf16 a[12:15], a[236:239], v[16:19], a[12:15]\n"
"v_exp_f32 v10, v10\n"
"v_mfma_f32_16x16x32_bf16 a[16:19], a[240:243], v[16:19], a[16:19]\n"
"ds_read_b64_tr_b16 a[202:203], v242 offset:53504\n"
"v_add_f32 v44, v44, v4\n"
"v_mfma_f32_16x16x32_bf16 a[20:23], a[244:247], v[16:19], a[20:23]\n"
"v_exp_f32 v11, v11\n"
"v_mfma_f32_16x16x32_bf16 a[24:27], a[248:251], v[16:19], a[24:27]\n"
"ds_read_b64_tr_b16 a[204:205], v243 offset:49408\n"
"v_cvt_pk_bf16_f32 v21, v10, v11\n"
"v_mfma_f32_16x16x32_bf16 a[28:31], a[252:255], v[16:19], a[28:31]\n"
"v_exp_f32 v12, v12\n"
"v_mfma_f32_16x16x32_bf16 v[220:223], a[160:163], v[96:99], v[32:35]\n"
"ds_read_b64_tr_b16 a[206:207], v243 offset:53504\n"
"v_add_f32 v44, v44, v5\n"
"v_mfma_f32_16x16x32_bf16 v[220:223], a[164:167], v[100:103], v[220:223]\n"
"v_exp_f32 v13, v13\n"
"v_mfma_f32_16x16x32_bf16 v[220:223], a[168:171], v[104:107], v[220:223]\n"
"ds_read_b64_tr_b16 a[208:209], v242 offset:49664\n"
"buffer_load_dwordx4 v[184:187], v252, s[68:71], s49 offen\n"
"v_cvt_pk_bf16_f32 v22, v12, v13\n"
"v_mfma_f32_16x16x32_bf16 v[220:223], a[172:175], v[108:111], v[220:223]\n"
"s_add_u32 s49, s49, 285696\n"
"v_exp_f32 v14, v14\n"
"v_mfma_f32_16x16x32_bf16 v[224:227], a[176:179], v[96:99], v[32:35]\n"
"ds_read_b64_tr_b16 a[210:211], v242 offset:53760\n"
"v_add_f32 v44, v44, v6\n"
"v_mfma_f32_16x16x32_bf16 v[224:227], a[180:183], v[100:103], v[224:227]\n"
"v_exp_f32 v15, v15\n"
"v_mfma_f32_16x16x32_bf16 v[224:227], a[184:187], v[104:107], v[224:227]\n"
"ds_read_b64_tr_b16 a[212:213], v243 offset:49664\n"
"v_cvt_pk_bf16_f32 v23, v14, v15\n"
"v_mfma_f32_16x16x32_bf16 v[224:227], a[188:191], v[108:111], v[224:227]\n"
"s_mov_b32 m0, s51\n"
"v_exp_f32 v220, v220\n"
"v_mfma_f32_16x16x32_bf16 a[32:35], a[224:227], v[20:23], a[32:35]\n"
"buffer_load_dwordx4 v212, s[24:27], s20 offen offset:2048 lds\n"
"v_mfma_f32_16x16x32_bf16 a[36:39], a[228:231], v[20:23], a[36:39]\n"
"v_exp_f32 v221, v221\n"
"v_mfma_f32_16x16x32_bf16 a[40:43], a[232:235], v[20:23], a[40:43]\n"
"ds_read_b64_tr_b16 a[214:215], v243 offset:53760\n"
"v_cvt_pk_bf16_f32 v16, v220, v221\n"
"v_mfma_f32_16x16x32_bf16 a[44:47], a[236:239], v[20:23], a[44:47]\n"
"v_exp_f32 v222, v222\n"
"v_mfma_f32_16x16x32_bf16 a[48:51], a[240:243], v[20:23], a[48:51]\n"
"ds_read_b64_tr_b16 a[216:217], v242 offset:49920\n"
"v_add_f32 v44, v44, v7\n"
"v_mfma_f32_16x16x32_bf16 a[52:55], a[244:247], v[20:23], a[52:55]\n"
"v_exp_f32 v223, v223\n"
"v_mfma_f32_16x16x32_bf16 a[56:59], a[248:251], v[20:23], a[56:59]\n"
"ds_read_b64_tr_b16 a[218:219], v242 offset:54016\n"
"v_cvt_pk_bf16_f32 v17, v222, v223\n"
"v_mfma_f32_16x16x32_bf16 a[60:63], a[252:255], v[20:23], a[60:63]\n"
"v_exp_f32 v224, v224\n"
"v_mfma_f32_16x16x32_bf16 v[48:51], v[40:43], v[20:23], v[48:51]\n"
"ds_read_b64_tr_b16 a[220:221], v243 offset:49920\n"
"v_add_f32 v52, v52, v220\n"
"v_mfma_f32_16x16x32_bf16 v[228:231], a[160:163], v[112:115], v[36:39]\n"
"v_exp_f32 v225, v225\n"
"v_mfma_f32_16x16x32_bf16 v[228:231], a[164:167], v[116:119], v[228:231]\n"
"ds_read_b64_tr_b16 a[222:223], v243 offset:54016\n"
"v_cvt_pk_bf16_f32 v18, v224, v225\n"
"v_mfma_f32_16x16x32_bf16 v[228:231], a[168:171], v[120:123], v[228:231]\n"
"v_exp_f32 v226, v226\n"
"v_mfma_f32_16x16x32_bf16 v[228:231], a[172:175], v[124:127], v[228:231]\n"
"v_add_f32 v52, v52, v221\n"
"v_add_f32 v52, v52, v222\n"
"v_mfma_f32_16x16x32_bf16 v[232:235], a[176:179], v[112:115], v[36:39]\n"
"v_exp_f32 v227, v227\n"
"v_mfma_f32_16x16x32_bf16 v[232:235], a[180:183], v[116:119], v[232:235]\n"
"s_mov_b32 m0, s51\n"
"v_cvt_pk_bf16_f32 v19, v226, v227\n"
"v_add_f32 v52, v52, v223\n"
"v_mfma_f32_16x16x32_bf16 v[232:235], a[184:187], v[120:123], v[232:235]\n"
"buffer_load_dwordx4 v213, s[24:27], s20 offen offset:3072 lds\n"
"v_mfma_f32_16x16x32_bf16 v[232:235], a[188:191], v[124:127], v[232:235]\n"
"v_exp_f32 v228, v228\n"
"v_mfma_f32_16x16x32_bf16 a[64:67], a[224:227], v[16:19], a[64:67]\n"
"v_add_f32 v52, v52, v224\n"
"v_add_f32 v52, v52, v225\n"
"v_mfma_f32_16x16x32_bf16 a[68:71], a[228:231], v[16:19], a[68:71]\n"
"v_exp_f32 v229, v229\n"
"v_mfma_f32_16x16x32_bf16 a[72:75], a[232:235], v[16:19], a[72:75]\n"
"v_cvt_pk_bf16_f32 v20, v228, v229\n"
"v_add_f32 v52, v52, v226\n"
"v_mfma_f32_16x16x32_bf16 a[76:79], a[236:239], v[16:19], a[76:79]\n"
"v_exp_f32 v230, v230\n"
"v_mfma_f32_16x16x32_bf16 a[80:83], a[240:243], v[16:19], a[80:83]\n"
"s_waitcnt lgkmcnt(8)\n"
"v_mfma_f32_16x16x32_bf16 a[84:87], a[244:247], v[16:19], a[84:87]\n"
"v_exp_f32 v231, v231\n"
"v_mfma_f32_16x16x32_bf16 a[88:91], a[248:251], v[16:19], a[88:91]\n"
"buffer_load_dwordx4 v[152:155], v252, s[64:67], s49 offen\n"
"v_cvt_pk_bf16_f32 v21, v230, v231\n"
"v_add_f32 v52, v52, v227\n"
"v_mfma_f32_16x16x32_bf16 a[92:95], a[252:255], v[16:19], a[92:95]\n"
"v_exp_f32 v232, v232\n"
"v_mfma_f32_16x16x32_bf16 v[0:3], a[128:131], v[64:67], v[24:27]\n"
"ds_read_b128 a[160:163], v240 offset:40960\n"
"v_mfma_f32_16x16x32_bf16 v[0:3], a[132:135], v[68:71], v[0:3]\n"
"v_exp_f32 v233, v233\n"
"v_mfma_f32_16x16x32_bf16 v[0:3], a[136:139], v[72:75], v[0:3]\n"
"ds_read_b128 a[164:167], v240 offset:41216\n"
"v_cvt_pk_bf16_f32 v22, v232, v233\n"
"v_mfma_f32_16x16x32_bf16 v[0:3], a[140:143], v[76:79], v[0:3]\n"
"v_exp_f32 v234, v234\n"
"v_mfma_f32_16x16x32_bf16 v[4:7], a[144:147], v[64:67], v[24:27]\n"
"ds_read_b128 a[168:171], v240 offset:41472\n"
"v_mfma_f32_16x16x32_bf16 v[4:7], a[148:151], v[68:71], v[4:7]\n"
"v_exp_f32 v235, v235\n"
"v_mfma_f32_16x16x32_bf16 v[4:7], a[152:155], v[72:75], v[4:7]\n"
"ds_read_b128 a[172:175], v240 offset:41728\n"
"v_cvt_pk_bf16_f32 v23, v234, v235\n"
"v_mfma_f32_16x16x32_bf16 v[4:7], a[156:159], v[76:79], v[4:7]\n"
"s_add_u32 m0, s51, 4096\n"
"v_exp_f32 v0, v0\n"
"v_mfma_f32_16x16x32_bf16 a[96:99], a[224:227], v[20:23], a[96:99]\n"
"buffer_load_dwordx4 v214, s[24:27], s20 offen offset:0 lds\n"
"v_mfma_f32_16x16x32_bf16 a[100:103], a[228:231], v[20:23], a[100:103]\n"
"v_exp_f32 v1, v1\n"
"v_mfma_f32_16x16x32_bf16 a[104:107], a[232:235], v[20:23], a[104:107]\n"
"ds_read_b128 a[176:179], v240 offset:45056\n"
"v_cvt_pk_bf16_f32 v16, v0, v1\n"
"v_mfma_f32_16x16x32_bf16 a[108:111], a[236:239], v[20:23], a[108:111]\n"
"v_exp_f32 v2, v2\n"
"v_mfma_f32_16x16x32_bf16 a[112:115], a[240:243], v[20:23], a[112:115]\n"
"ds_read_b128 a[180:183], v240 offset:45312\n"
"v_add_f32 v44, v44, v0\n"
"v_mfma_f32_16x16x32_bf16 a[116:119], a[244:247], v[20:23], a[116:119]\n"
"v_exp_f32 v3, v3\n"
"v_mfma_f32_16x16x32_bf16 a[120:123], a[248:251], v[20:23], a[120:123]\n"
"ds_read_b128 a[184:187], v240 offset:45568\n"
"v_cvt_pk_bf16_f32 v17, v2, v3\n"
"v_mfma_f32_16x16x32_bf16 a[124:127], a[252:255], v[20:23], a[124:127]\n"
"v_exp_f32 v4, v4\n"
"v_mfma_f32_16x16x32_bf16 v[56:59], v[40:43], v[20:23], v[56:59]\n"
"ds_read_b128 a[188:191], v240 offset:45824\n"
"v_add_f32 v44, v44, v1\n"
"v_mfma_f32_16x16x32_bf16 v[8:11], a[128:131], v[80:83], v[28:31]\n"
"v_exp_f32 v5, v5\n"
"v_mfma_f32_16x16x32_bf16 v[8:11], a[132:135], v[84:87], v[8:11]\n"
"ds_read_b64_tr_b16 a[224:225], v242 offset:57344\n"
"v_cvt_pk_bf16_f32 v18, v4, v5\n"
"v_mfma_f32_16x16x32_bf16 v[8:11], a[136:139], v[88:91], v[8:11]\n"
"v_exp_f32 v6, v6\n"
"v_mfma_f32_16x16x32_bf16 v[8:11], a[140:143], v[92:95], v[8:11]\n"
"ds_read_b64_tr_b16 a[226:227], v242 offset:61440\n"
"v_add_f32 v44, v44, v2\n"
"v_mfma_f32_16x16x32_bf16 v[12:15], a[144:147], v[80:83], v[28:31]\n"
"v_exp_f32 v7, v7\n"
"v_mfma_f32_16x16x32_bf16 v[12:15], a[148:151], v[84:87], v[12:15]\n"
"s_add_u32 m0, s51, 4096\n"
"ds_read_b64_tr_b16 a[228:229], v243 offset:57344\n"
"v_cvt_pk_bf16_f32 v19, v6, v7\n"
"v_mfma_f32_16x16x32_bf16 v[12:15], a[152:155], v[88:91], v[12:15]\n"
"buffer_load_dwordx4 v236, s[24:27], s20 offen offset:1024 lds\n"
"v_mfma_f32_16x16x32_bf16 v[12:15], a[156:159], v[92:95], v[12:15]\n"
"s_waitcnt lgkmcnt(11)\n"
"v_exp_f32 v8, v8\n"
"v_mfma_f32_16x16x32_bf16 a[0:3], a[192:195], v[16:19], a[0:3]\n"
"ds_read_b64_tr_b16 a[230:231], v243 offset:61440\n"
"v_add_f32 v44, v44, v3\n"
"v_mfma_f32_16x16x32_bf16 a[4:7], a[196:199], v[16:19], a[4:7]\n"
"v_exp_f32 v9, v9\n"
"v_mfma_f32_16x16x32_bf16 a[8:11], a[200:203], v[16:19], a[8:11]\n"
"ds_read_b64_tr_b16 a[232:233], v242 offset:57600\n"
"buffer_load_dwordx4 v[188:191], v252, s[68:71], s49 offen\n"
"v_cvt_pk_bf16_f32 v20, v8, v9\n"
"v_mfma_f32_16x16x32_bf16 a[12:15], a[204:207], v[16:19], a[12:15]\n"
"s_add_u32 s49, s49, 9216\n"
"v_exp_f32 v10, v10\n"
"v_mfma_f32_16x16x32_bf16 a[16:19], a[208:211], v[16:19], a[16:19]\n"
"ds_read_b64_tr_b16 a[234:235], v242 offset:61696\n"
"v_add_f32 v44, v44, v4\n"
"v_mfma_f32_16x16x32_bf16 a[20:23], a[212:215], v[16:19], a[20:23]\n"
"s_add_u32 m0, s51, 4096\n"
"v_exp_f32 v11, v11\n"
"v_mfma_f32_16x16x32_bf16 a[24:27], a[216:219], v[16:19], a[24:27]\n"
"buffer_load_dwordx4 v237, s[24:27], s20 offen offset:2048 lds\n"
"ds_read_b64_tr_b16 a[236:237], v243 offset:57600\n"
"v_cvt_pk_bf16_f32 v21, v10, v11\n"
"v_mfma_f32_16x16x32_bf16 a[28:31], a[220:223], v[16:19], a[28:31]\n"
"v_exp_f32 v12, v12\n"
"v_mfma_f32_16x16x32_bf16 v[220:223], a[128:131], v[96:99], v[32:35]\n"
"ds_read_b64_tr_b16 a[238:239], v243 offset:61696\n"
"v_add_f32 v44, v44, v5\n"
"v_mfma_f32_16x16x32_bf16 v[220:223], a[132:135], v[100:103], v[220:223]\n"
"v_exp_f32 v13, v13\n"
"v_mfma_f32_16x16x32_bf16 v[220:223], a[136:139], v[104:107], v[220:223]\n"
"ds_read_b64_tr_b16 a[240:241], v242 offset:57856\n"
"v_cvt_pk_bf16_f32 v22, v12, v13\n"
"v_mfma_f32_16x16x32_bf16 v[220:223], a[140:143], v[108:111], v[220:223]\n"
"v_exp_f32 v14, v14\n"
"v_mfma_f32_16x16x32_bf16 v[224:227], a[144:147], v[96:99], v[32:35]\n"
"ds_read_b64_tr_b16 a[242:243], v242 offset:61952\n"
"v_add_f32 v44, v44, v6\n"
"v_mfma_f32_16x16x32_bf16 v[224:227], a[148:151], v[100:103], v[224:227]\n"
"v_exp_f32 v15, v15\n"
"v_mfma_f32_16x16x32_bf16 v[224:227], a[152:155], v[104:107], v[224:227]\n"
"ds_read_b64_tr_b16 a[244:245], v243 offset:57856\n"
"v_cvt_pk_bf16_f32 v23, v14, v15\n"
"v_mfma_f32_16x16x32_bf16 v[224:227], a[156:159], v[108:111], v[224:227]\n"
"s_add_u32 m0, s51, 4096\n"
"v_exp_f32 v220, v220\n"
"v_mfma_f32_16x16x32_bf16 a[32:35], a[192:195], v[20:23], a[32:35]\n"
"buffer_load_dwordx4 v238, s[24:27], s20 offen offset:3072 lds\n"
"v_mfma_f32_16x16x32_bf16 a[36:39], a[196:199], v[20:23], a[36:39]\n"
"v_exp_f32 v221, v221\n"
"v_mfma_f32_16x16x32_bf16 a[40:43], a[200:203], v[20:23], a[40:43]\n"
"ds_read_b64_tr_b16 a[246:247], v243 offset:61952\n"
"v_cvt_pk_bf16_f32 v16, v220, v221\n"
"v_mfma_f32_16x16x32_bf16 a[44:47], a[204:207], v[20:23], a[44:47]\n"
"v_exp_f32 v222, v222\n"
"v_mfma_f32_16x16x32_bf16 a[48:51], a[208:211], v[20:23], a[48:51]\n"
"ds_read_b64_tr_b16 a[248:249], v242 offset:58112\n"
"v_add_f32 v44, v44, v7\n"
"v_mfma_f32_16x16x32_bf16 a[52:55], a[212:215], v[20:23], a[52:55]\n"
"v_exp_f32 v223, v223\n"
"v_mfma_f32_16x16x32_bf16 a[56:59], a[216:219], v[20:23], a[56:59]\n"
"ds_read_b64_tr_b16 a[250:251], v242 offset:62208\n"
"v_cvt_pk_bf16_f32 v17, v222, v223\n"
"v_mfma_f32_16x16x32_bf16 a[60:63], a[220:223], v[20:23], a[60:63]\n"
"v_exp_f32 v224, v224\n"
"v_mfma_f32_16x16x32_bf16 v[48:51], v[40:43], v[20:23], v[48:51]\n"
"ds_read_b64_tr_b16 a[252:253], v243 offset:58112\n"
"v_add_f32 v52, v52, v220\n"
"v_mfma_f32_16x16x32_bf16 v[228:231], a[128:131], v[112:115], v[36:39]\n"
"v_exp_f32 v225, v225\n"
"v_mfma_f32_16x16x32_bf16 v[228:231], a[132:135], v[116:119], v[228:231]\n"
"ds_read_b64_tr_b16 a[254:255], v243 offset:62208\n"
"v_cvt_pk_bf16_f32 v18, v224, v225\n"
"v_mfma_f32_16x16x32_bf16 v[228:231], a[136:139], v[120:123], v[228:231]\n"
"v_exp_f32 v226, v226\n"
"v_mfma_f32_16x16x32_bf16 v[228:231], a[140:143], v[124:127], v[228:231]\n"
"v_add_f32 v52, v52, v221\n"
"v_add_f32 v52, v52, v222\n"
"v_mfma_f32_16x16x32_bf16 v[232:235], a[144:147], v[112:115], v[36:39]\n"
"v_exp_f32 v227, v227\n"
"v_mfma_f32_16x16x32_bf16 v[232:235], a[148:151], v[116:119], v[232:235]\n"
"s_add_u32 m0, s51, 8192\n"
"buffer_load_dwordx4 v[156:159], v252, s[64:67], s49 offen\n"
"v_cvt_pk_bf16_f32 v19, v226, v227\n"
"v_add_f32 v52, v52, v223\n"
"v_mfma_f32_16x16x32_bf16 v[232:235], a[152:155], v[120:123], v[232:235]\n"
"buffer_load_dwordx4 v239, s[24:27], s20 offen offset:0 lds\n"
"v_mfma_f32_16x16x32_bf16 v[232:235], a[156:159], v[124:127], v[232:235]\n"
"v_exp_f32 v228, v228\n"
"v_mfma_f32_16x16x32_bf16 a[64:67], a[192:195], v[16:19], a[64:67]\n"
"v_add_f32 v52, v52, v224\n"
"v_add_f32 v52, v52, v225\n"
"v_mfma_f32_16x16x32_bf16 a[68:71], a[196:199], v[16:19], a[68:71]\n"
"v_exp_f32 v229, v229\n"
"v_mfma_f32_16x16x32_bf16 a[72:75], a[200:203], v[16:19], a[72:75]\n"
"v_cvt_pk_bf16_f32 v20, v228, v229\n"
"v_add_f32 v52, v52, v226\n"
"v_mfma_f32_16x16x32_bf16 a[76:79], a[204:207], v[16:19], a[76:79]\n"
"s_waitcnt vmcnt(25) lgkmcnt(0)\n"
"v_exp_f32 v230, v230\n"
"v_mfma_f32_16x16x32_bf16 a[80:83], a[208:211], v[16:19], a[80:83]\n"
"s_barrier\n"
"v_mfma_f32_16x16x32_bf16 a[84:87], a[212:215], v[16:19], a[84:87]\n"
"v_exp_f32 v231, v231\n"
"v_mfma_f32_16x16x32_bf16 a[88:91], a[216:219], v[16:19], a[88:91]\n"
"v_cvt_pk_bf16_f32 v21, v230, v231\n"
"v_add_f32 v52, v52, v227\n"
"v_mfma_f32_16x16x32_bf16 a[92:95], a[220:223], v[16:19], a[92:95]\n"
"v_exp_f32 v232, v232\n"
"v_mfma_f32_16x16x32_bf16 v[0:3], a[160:163], v[64:67], v[24:27]\n"
"ds_read_b128 a[128:131], v241 offset:0\n"
"v_mfma_f32_16x16x32_bf16 v[0:3], a[164:167], v[68:71], v[0:3]\n"
"v_exp_f32 v233, v233\n"
"v_mfma_f32_16x16x32_bf16 v[0:3], a[168:171], v[72:75], v[0:3]\n"
"ds_read_b128 a[132:135], v241 offset:256\n"
"v_cvt_pk_bf16_f32 v22, v232, v233\n"
"v_mfma_f32_16x16x32_bf16 v[0:3], a[172:175], v[76:79], v[0:3]\n"
"v_exp_f32 v234, v234\n"
"v_mfma_f32_16x16x32_bf16 v[4:7], a[176:179], v[64:67], v[24:27]\n"
"ds_read_b128 a[136:139], v241 offset:512\n"
"v_mfma_f32_16x16x32_bf16 v[4:7], a[180:183], v[68:71], v[4:7]\n"
"v_exp_f32 v235, v235\n"
"v_mfma_f32_16x16x32_bf16 v[4:7], a[184:187], v[72:75], v[4:7]\n"
"ds_read_b128 a[140:143], v241 offset:768\n"
"v_cvt_pk_bf16_f32 v23, v234, v235\n"
"v_mfma_f32_16x16x32_bf16 v[4:7], a[188:191], v[76:79], v[4:7]\n"
"s_add_u32 m0, s53, 36864\n"
"v_exp_f32 v0, v0\n"
"v_mfma_f32_16x16x32_bf16 a[96:99], a[192:195], v[20:23], a[96:99]\n"
"buffer_load_dwordx4 v210, s[24:27], s21 offen offset:0 lds\n"
"v_mfma_f32_16x16x32_bf16 a[100:103], a[196:199], v[20:23], a[100:103]\n"
"v_exp_f32 v1, v1\n"
"v_mfma_f32_16x16x32_bf16 a[104:107], a[200:203], v[20:23], a[104:107]\n"
"ds_read_b128 a[144:147], v241 offset:4096\n"
"v_cvt_pk_bf16_f32 v16, v0, v1\n"
"v_mfma_f32_16x16x32_bf16 a[108:111], a[204:207], v[20:23], a[108:111]\n"
"v_exp_f32 v2, v2\n"
"v_mfma_f32_16x16x32_bf16 a[112:115], a[208:211], v[20:23], a[112:115]\n"
"ds_read_b128 a[148:151], v241 offset:4352\n"
"v_add_f32 v44, v44, v0\n"
"v_mfma_f32_16x16x32_bf16 a[116:119], a[212:215], v[20:23], a[116:119]\n"
"v_exp_f32 v3, v3\n"
"v_mfma_f32_16x16x32_bf16 a[120:123], a[216:219], v[20:23], a[120:123]\n"
"ds_read_b128 a[152:155], v241 offset:4608\n"
"v_cvt_pk_bf16_f32 v17, v2, v3\n"
"v_mfma_f32_16x16x32_bf16 a[124:127], a[220:223], v[20:23], a[124:127]\n"
"v_exp_f32 v4, v4\n"
"v_mfma_f32_16x16x32_bf16 v[56:59], v[40:43], v[20:23], v[56:59]\n"
"ds_read_b128 a[156:159], v241 offset:4864\n"
"v_add_f32 v44, v44, v1\n"
"v_mfma_f32_16x16x32_bf16 v[8:11], a[160:163], v[80:83], v[28:31]\n"
"v_exp_f32 v5, v5\n"
"v_mfma_f32_16x16x32_bf16 v[8:11], a[164:167], v[84:87], v[8:11]\n"
"ds_read_b64_tr_b16 a[192:193], v244 offset:16384\n"
"buffer_load_dwordx4 v[192:195], v252, s[68:71], s49 offen\n"
"v_cvt_pk_bf16_f32 v18, v4, v5\n"
"v_mfma_f32_16x16x32_bf16 v[8:11], a[168:171], v[88:91], v[8:11]\n"
"s_add_u32 s49, s49, 285696\n"
"v_exp_f32 v6, v6\n"
"v_mfma_f32_16x16x32_bf16 v[8:11], a[172:175], v[92:95], v[8:11]\n"
"ds_read_b64_tr_b16 a[194:195], v244 offset:20480\n"
"v_add_f32 v44, v44, v2\n"
"v_mfma_f32_16x16x32_bf16 v[12:15], a[176:179], v[80:83], v[28:31]\n"
"v_exp_f32 v7, v7\n"
"v_mfma_f32_16x16x32_bf16 v[12:15], a[180:183], v[84:87], v[12:15]\n"
"s_add_u32 m0, s53, 36864\n"
"ds_read_b64_tr_b16 a[196:197], v245 offset:16384\n"
"v_cvt_pk_bf16_f32 v19, v6, v7\n"
"v_mfma_f32_16x16x32_bf16 v[12:15], a[184:187], v[88:91], v[12:15]\n"
"buffer_load_dwordx4 v211, s[24:27], s21 offen offset:1024 lds\n"
"v_mfma_f32_16x16x32_bf16 v[12:15], a[188:191], v[92:95], v[12:15]\n"
"s_waitcnt lgkmcnt(11)\n"
"v_exp_f32 v8, v8\n"
"v_mfma_f32_16x16x32_bf16 a[0:3], a[224:227], v[16:19], a[0:3]\n"
"ds_read_b64_tr_b16 a[198:199], v245 offset:20480\n"
"v_add_f32 v44, v44, v3\n"
"v_mfma_f32_16x16x32_bf16 a[4:7], a[228:231], v[16:19], a[4:7]\n"
"v_exp_f32 v9, v9\n"
"v_mfma_f32_16x16x32_bf16 a[8:11], a[232:235], v[16:19], a[8:11]\n"
"ds_read_b64_tr_b16 a[200:201], v244 offset:16640\n"
"v_cvt_pk_bf16_f32 v20, v8, v9\n"
"v_mfma_f32_16x16x32_bf16 a[12:15], a[236:239], v[16:19], a[12:15]\n"
"v_exp_f32 v10, v10\n"
"v_mfma_f32_16x16x32_bf16 a[16:19], a[240:243], v[16:19], a[16:19]\n"
"ds_read_b64_tr_b16 a[202:203], v244 offset:20736\n"
"v_add_f32 v44, v44, v4\n"
"v_mfma_f32_16x16x32_bf16 a[20:23], a[244:247], v[16:19], a[20:23]\n"
"v_exp_f32 v11, v11\n"
"v_mfma_f32_16x16x32_bf16 a[24:27], a[248:251], v[16:19], a[24:27]\n"
"ds_read_b64_tr_b16 a[204:205], v245 offset:16640\n"
"v_cvt_pk_bf16_f32 v21, v10, v11\n"
"v_mfma_f32_16x16x32_bf16 a[28:31], a[252:255], v[16:19], a[28:31]\n"
"v_exp_f32 v12, v12\n"
"v_mfma_f32_16x16x32_bf16 v[220:223], a[160:163], v[96:99], v[32:35]\n"
"ds_read_b64_tr_b16 a[206:207], v245 offset:20736\n"
"v_add_f32 v44, v44, v5\n"
"v_mfma_f32_16x16x32_bf16 v[220:223], a[164:167], v[100:103], v[220:223]\n"
"v_exp_f32 v13, v13\n"
"v_mfma_f32_16x16x32_bf16 v[220:223], a[168:171], v[104:107], v[220:223]\n"
"ds_read_b64_tr_b16 a[208:209], v244 offset:16896\n"
"v_cvt_pk_bf16_f32 v22, v12, v13\n"
"v_mfma_f32_16x16x32_bf16 v[220:223], a[172:175], v[108:111], v[220:223]\n"
"v_exp_f32 v14, v14\n"
"v_mfma_f32_16x16x32_bf16 v[224:227], a[176:179], v[96:99], v[32:35]\n"
"ds_read_b64_tr_b16 a[210:211], v244 offset:20992\n"
"v_add_f32 v44, v44, v6\n"
"v_mfma_f32_16x16x32_bf16 v[224:227], a[180:183], v[100:103], v[224:227]\n"
"v_exp_f32 v15, v15\n"
"v_mfma_f32_16x16x32_bf16 v[224:227], a[184:187], v[104:107], v[224:227]\n"
"ds_read_b64_tr_b16 a[212:213], v245 offset:16896\n"
"v_cvt_pk_bf16_f32 v23, v14, v15\n"
"v_mfma_f32_16x16x32_bf16 v[224:227], a[188:191], v[108:111], v[224:227]\n"
"s_add_u32 m0, s53, 36864\n"
"v_exp_f32 v220, v220\n"
"v_mfma_f32_16x16x32_bf16 a[32:35], a[224:227], v[20:23], a[32:35]\n"
"buffer_load_dwordx4 v212, s[24:27], s21 offen offset:2048 lds\n"
"v_mfma_f32_16x16x32_bf16 a[36:39], a[228:231], v[20:23], a[36:39]\n"
"v_exp_f32 v221, v221\n"
"v_mfma_f32_16x16x32_bf16 a[40:43], a[232:235], v[20:23], a[40:43]\n"
"ds_read_b64_tr_b16 a[214:215], v245 offset:20992\n"
"v_cvt_pk_bf16_f32 v16, v220, v221\n"
"v_mfma_f32_16x16x32_bf16 a[44:47], a[236:239], v[20:23], a[44:47]\n"
"v_exp_f32 v222, v222\n"
"v_mfma_f32_16x16x32_bf16 a[48:51], a[240:243], v[20:23], a[48:51]\n"
"ds_read_b64_tr_b16 a[216:217], v244 offset:17152\n"
"v_add_f32 v44, v44, v7\n"
"v_mfma_f32_16x16x32_bf16 a[52:55], a[244:247], v[20:23], a[52:55]\n"
"v_exp_f32 v223, v223\n"
"v_mfma_f32_16x16x32_bf16 a[56:59], a[248:251], v[20:23], a[56:59]\n"
"ds_read_b64_tr_b16 a[218:219], v244 offset:21248\n"
"buffer_load_dwordx4 v[160:163], v251, s[64:67], s49 offen\n"
"v_cvt_pk_bf16_f32 v17, v222, v223\n"
"v_mfma_f32_16x16x32_bf16 a[60:63], a[252:255], v[20:23], a[60:63]\n"
"v_exp_f32 v224, v224\n"
"v_mfma_f32_16x16x32_bf16 v[48:51], v[40:43], v[20:23], v[48:51]\n"
"ds_read_b64_tr_b16 a[220:221], v245 offset:17152\n"
"v_add_f32 v52, v52, v220\n"
"v_mfma_f32_16x16x32_bf16 v[228:231], a[160:163], v[112:115], v[36:39]\n"
"v_exp_f32 v225, v225\n"
"v_mfma_f32_16x16x32_bf16 v[228:231], a[164:167], v[116:119], v[228:231]\n"
"ds_read_b64_tr_b16 a[222:223], v245 offset:21248\n"
"v_cvt_pk_bf16_f32 v18, v224, v225\n"
"v_mfma_f32_16x16x32_bf16 v[228:231], a[168:171], v[120:123], v[228:231]\n"
"v_exp_f32 v226, v226\n"
"v_mfma_f32_16x16x32_bf16 v[228:231], a[172:175], v[124:127], v[228:231]\n"
"v_add_f32 v52, v52, v221\n"
"v_add_f32 v52, v52, v222\n"
"v_mfma_f32_16x16x32_bf16 v[232:235], a[176:179], v[112:115], v[36:39]\n"
"v_exp_f32 v227, v227\n"
"v_mfma_f32_16x16x32_bf16 v[232:235], a[180:183], v[116:119], v[232:235]\n"
"s_add_u32 m0, s53, 36864\n"
"v_cvt_pk_bf16_f32 v19, v226, v227\n"
"v_add_f32 v52, v52, v223\n"
"v_mfma_f32_16x16x32_bf16 v[232:235], a[184:187], v[120:123], v[232:235]\n"
"buffer_load_dwordx4 v213, s[24:27], s21 offen offset:3072 lds\n"
"v_mfma_f32_16x16x32_bf16 v[232:235], a[188:191], v[124:127], v[232:235]\n"
"v_exp_f32 v228, v228\n"
"v_mfma_f32_16x16x32_bf16 a[64:67], a[224:227], v[16:19], a[64:67]\n"
"v_add_f32 v52, v52, v224\n"
"v_add_f32 v52, v52, v225\n"
"v_mfma_f32_16x16x32_bf16 a[68:71], a[228:231], v[16:19], a[68:71]\n"
"v_exp_f32 v229, v229\n"
"v_mfma_f32_16x16x32_bf16 a[72:75], a[232:235], v[16:19], a[72:75]\n"
"v_cvt_pk_bf16_f32 v20, v228, v229\n"
"v_add_f32 v52, v52, v226\n"
"v_mfma_f32_16x16x32_bf16 a[76:79], a[236:239], v[16:19], a[76:79]\n"
"v_exp_f32 v230, v230\n"
"v_mfma_f32_16x16x32_bf16 a[80:83], a[240:243], v[16:19], a[80:83]\n"
"s_waitcnt lgkmcnt(8)\n"
"v_mfma_f32_16x16x32_bf16 a[84:87], a[244:247], v[16:19], a[84:87]\n"
"v_exp_f32 v231, v231\n"
"v_mfma_f32_16x16x32_bf16 a[88:91], a[248:251], v[16:19], a[88:91]\n"
"v_cvt_pk_bf16_f32 v21, v230, v231\n"
"v_add_f32 v52, v52, v227\n"
"v_mfma_f32_16x16x32_bf16 a[92:95], a[252:255], v[16:19], a[92:95]\n"
"v_exp_f32 v232, v232\n"
"v_mfma_f32_16x16x32_bf16 v[0:3], a[128:131], v[64:67], v[24:27]\n"
"ds_read_b128 a[160:163], v241 offset:8192\n"
"v_mfma_f32_16x16x32_bf16 v[0:3], a[132:135], v[68:71], v[0:3]\n"
"v_exp_f32 v233, v233\n"
"v_mfma_f32_16x16x32_bf16 v[0:3], a[136:139], v[72:75], v[0:3]\n"
"ds_read_b128 a[164:167], v241 offset:8448\n"
"v_cvt_pk_bf16_f32 v22, v232, v233\n"
"v_mfma_f32_16x16x32_bf16 v[0:3], a[140:143], v[76:79], v[0:3]\n"
"v_exp_f32 v234, v234\n"
"v_mfma_f32_16x16x32_bf16 v[4:7], a[144:147], v[64:67], v[24:27]\n"
"ds_read_b128 a[168:171], v241 offset:8704\n"
"v_mfma_f32_16x16x32_bf16 v[4:7], a[148:151], v[68:71], v[4:7]\n"
"v_exp_f32 v235, v235\n"
"v_mfma_f32_16x16x32_bf16 v[4:7], a[152:155], v[72:75], v[4:7]\n"
"ds_read_b128 a[172:175], v241 offset:8960\n"
"v_cvt_pk_bf16_f32 v23, v234, v235\n"
"v_mfma_f32_16x16x32_bf16 v[4:7], a[156:159], v[76:79], v[4:7]\n"
"s_add_u32 m0, s53, 40960\n"
"v_exp_f32 v0, v0\n"
"v_mfma_f32_16x16x32_bf16 a[96:99], a[224:227], v[20:23], a[96:99]\n"
"buffer_load_dwordx4 v214, s[24:27], s21 offen offset:0 lds\n"
"v_mfma_f32_16x16x32_bf16 a[100:103], a[228:231], v[20:23], a[100:103]\n"
"v_exp_f32 v1, v1\n"
"v_mfma_f32_16x16x32_bf16 a[104:107], a[232:235], v[20:23], a[104:107]\n"
"ds_read_b128 a[176:179], v241 offset:12288\n"
"buffer_load_dwordx4 v[196:199], v251, s[68:71], s49 offen\n"
"v_cvt_pk_bf16_f32 v16, v0, v1\n"
"v_mfma_f32_16x16x32_bf16 a[108:111], a[236:239], v[20:23], a[108:111]\n"
"s_lshl_b32 s62, s41, 6\n"
"v_exp_f32 v2, v2\n"
"v_mfma_f32_16x16x32_bf16 a[112:115], a[240:243], v[20:23], a[112:115]\n"
"ds_read_b128 a[180:183], v241 offset:12544\n"
"v_add_f32 v44, v44, v0\n"
"v_mfma_f32_16x16x32_bf16 a[116:119], a[244:247], v[20:23], a[116:119]\n"
"AT_pg11:\n"
"s_cbranch_execz AT_hr\n"
"v_exp_f32 v3, v3\n"
"v_mfma_f32_16x16x32_bf16 a[120:123], a[248:251], v[20:23], a[120:123]\n"
"ds_read_b128 a[184:187], v241 offset:12800\n"
"v_cvt_pk_bf16_f32 v17, v2, v3\n"
"v_mfma_f32_16x16x32_bf16 a[124:127], a[252:255], v[20:23], a[124:127]\n"
"v_exp_f32 v4, v4\n"
"v_mfma_f32_16x16x32_bf16 v[56:59], v[40:43], v[20:23], v[56:59]\n"
"ds_read_b128 a[188:191], v241 offset:13056\n"
"v_add_f32 v44, v44, v1\n"
"v_mfma_f32_16x16x32_bf16 v[8:11], a[128:131], v[80:83], v[28:31]\n"
"v_exp_f32 v5, v5\n"
"v_mfma_f32_16x16x32_bf16 v[8:11], a[132:135], v[84:87], v[8:11]\n"
"ds_read_b64_tr_b16 a[224:225], v244 offset:24576\n"
"v_cvt_pk_bf16_f32 v18, v4, v5\n"
"v_add_u32 v253, s62, v248\n"
"v_mfma_f32_16x16x32_bf16 v[8:11], a[136:139], v[88:91], v[8:11]\n"
"v_exp_f32 v6, v6\n"
"v_mfma_f32_16x16x32_bf16 v[8:11], a[140:143], v[92:95], v[8:11]\n"
"ds_read_b64_tr_b16 a[226:227], v244 offset:28672\n"
"v_add_f32 v44, v44, v2\n"
"v_mfma_f32_16x16x32_bf16 v[12:15], a[144:147], v[80:83], v[28:31]\n"
"v_exp_f32 v7, v7\n"
"v_mfma_f32_16x16x32_bf16 v[12:15], a[148:151], v[84:87], v[12:15]\n"
"s_add_u32 m0, s53, 40960\n"
"ds_read_b64_tr_b16 a[228:229], v245 offset:24576\n"
"v_cvt_pk_bf16_f32 v19, v6, v7\n"
"v_mfma_f32_16x16x32_bf16 v[12:15], a[152:155], v[88:91], v[12:15]\n"
"buffer_load_dwordx4 v236, s[24:27], s21 offen offset:1024 lds\n"
"v_mov_b32 v255, 0x10000000\n"
"v_mfma_f32_16x16x32_bf16 v[12:15], a[156:159], v[92:95], v[12:15]\n"
"s_waitcnt lgkmcnt(11)\n"
"v_exp_f32 v8, v8\n"
"v_mfma_f32_16x16x32_bf16 a[0:3], a[192:195], v[16:19], a[0:3]\n"
"ds_read_b64_tr_b16 a[230:231], v245 offset:28672\n"
"v_add_f32 v44, v44, v3\n"
"v_mfma_f32_16x16x32_bf16 a[4:7], a[196:199], v[16:19], a[4:7]\n"
"v_exp_f32 v9, v9\n"
"v_mfma_f32_16x16x32_bf16 a[8:11], a[200:203], v[16:19], a[8:11]\n"
"ds_read_b64_tr_b16 a[232:233], v244 offset:24832\n"
"v_cvt_pk_bf16_f32 v20, v8, v9\n"
"v_mfma_f32_16x16x32_bf16 a[12:15], a[204:207], v[16:19], a[12:15]\n"
"v_exp_f32 v10, v10\n"
"v_mfma_f32_16x16x32_bf16 a[16:19], a[208:211], v[16:19], a[16:19]\n"
"ds_read_b64_tr_b16 a[234:235], v244 offset:28928\n"
"v_add_f32 v44, v44, v4\n"
"v_mfma_f32_16x16x32_bf16 a[20:23], a[212:215], v[16:19], a[20:23]\n"
"s_add_u32 m0, s53, 40960\n"
"v_exp_f32 v11, v11\n"
"v_cmp_gt_u32 vcc, 144, v253\n"
"v_mfma_f32_16x16x32_bf16 a[24:27], a[216:219], v[16:19], a[24:27]\n"
"buffer_load_dwordx4 v237, s[24:27], s21 offen offset:2048 lds\n"
"ds_read_b64_tr_b16 a[236:237], v245 offset:24832\n"
"v_cvt_pk_bf16_f32 v21, v10, v11\n"
"v_mfma_f32_16x16x32_bf16 a[28:31], a[220:223], v[16:19], a[28:31]\n"
"v_exp_f32 v12, v12\n"
"v_mfma_f32_16x16x32_bf16 v[220:223], a[128:131], v[96:99], v[32:35]\n"
"ds_read_b64_tr_b16 a[238:239], v245 offset:28928\n"
"v_add_f32 v44, v44, v5\n"
"v_mfma_f32_16x16x32_bf16 v[220:223], a[132:135], v[100:103], v[220:223]\n"
"v_exp_f32 v13, v13\n"
"v_mfma_f32_16x16x32_bf16 v[220:223], a[136:139], v[104:107], v[220:223]\n"
"ds_read_b64_tr_b16 a[240:241], v244 offset:25088\n"
"v_cvt_pk_bf16_f32 v22, v12, v13\n"
"v_mfma_f32_16x16x32_bf16 v[220:223], a[140:143], v[108:111], v[220:223]\n"
"v_exp_f32 v14, v14\n"
"v_mfma_f32_16x16x32_bf16 v[224:227], a[144:147], v[96:99], v[32:35]\n"
"ds_read_b64_tr_b16 a[242:243], v244 offset:29184\n"
"v_add_f32 v44, v44, v6\n"
"v_lshlrev_b32 v254, 3, v253\n"
"v_mfma_f32_16x16x32_bf16 v[224:227], a[148:151], v[100:103], v[224:227]\n"
"s_nop 0\n"
"v_exp_f32 v15, v15\n"
"v_mfma_f32_16x16x32_bf16 v[224:227], a[152:155], v[104:107], v[224:227]\n"
"ds_read_b64_tr_b16 a[244:245], v245 offset:25088\n"
"v_cvt_pk_bf16_f32 v23, v14, v15\n"
"v_mfma_f32_16x16x32_bf16 v[224:227], a[156:159], v[108:111], v[224:227]\n"
"s_add_u32 m0, s53, 40960\n"
"v_exp_f32 v220, v220\n"
"v_mfma_f32_16x16x32_bf16 a[32:35], a[192:195], v[20:23], a[32:35]\n"
"buffer_load_dwordx4 v238, s[24:27], s21 offen offset:3072 lds\n"
"v_cndmask_b32 v254, v255, v254, vcc\n"
"v_mfma_f32_16x16x32_bf16 a[36:39], a[196:199], v[20:23], a[36:39]\n"
"v_exp_f32 v221, v221\n"
"v_mfma_f32_16x16x32_bf16 a[40:43], a[200:203], v[20:23], a[40:43]\n"
"ds_read_b64_tr_b16 a[246:247], v245 offset:29184\n"
"v_cvt_pk_bf16_f32 v16, v220, v221\n"
"v_mfma_f32_16x16x32_bf16 a[44:47], a[204:207], v[20:23], a[44:47]\n"
"v_exp_f32 v222, v222\n"
"v_mfma_f32_16x16x32_bf16 a[48:51], a[208:211], v[20:23], a[48:51]\n"
"ds_read_b64_tr_b16 a[248:249], v244 offset:25344\n"
"v_add_f32 v44, v44, v7\n"
"v_mfma_f32_16x16x32_bf16 a[52:55], a[212:215], v[20:23], a[52:55]\n"
"v_exp_f32 v223, v223\n"
"v_mfma_f32_16x16x32_bf16 a[56:59], a[216:219], v[20:23], a[56:59]\n"
"ds_read_b64_tr_b16 a[250:251], v244 offset:29440\n"
"v_cvt_pk_bf16_f32 v17, v222, v223\n"
"v_mfma_f32_16x16x32_bf16 a[60:63], a[220:223], v[20:23], a[60:63]\n"
"v_exp_f32 v224, v224\n"
"v_lshlrev_b32 v216, 6, v248\n"
"v_mfma_f32_16x16x32_bf16 v[48:51], v[40:43], v[20:23], v[48:51]\n"
"ds_read_b64_tr_b16 a[252:253], v245 offset:25344\n"
"v_add_f32 v52, v52, v220\n"
"v_mfma_f32_16x16x32_bf16 v[228:231], a[128:131], v[112:115], v[36:39]\n"
"v_exp_f32 v225, v225\n"
"v_mfma_f32_16x16x32_bf16 v[228:231], a[132:135], v[116:119], v[228:231]\n"
"ds_read_b64_tr_b16 a[254:255], v245 offset:29440\n"
"v_cvt_pk_bf16_f32 v18, v224, v225\n"
"v_mfma_f32_16x16x32_bf16 v[228:231], a[136:139], v[120:123], v[228:231]\n"
"v_exp_f32 v226, v226\n"
"v_mfma_f32_16x16x32_bf16 v[228:231], a[140:143], v[124:127], v[228:231]\n"
"v_add_f32 v52, v52, v221\n"
"v_add_f32 v52, v52, v222\n"
"v_mfma_f32_16x16x32_bf16 v[232:235], a[144:147], v[112:115], v[36:39]\n"
"v_exp_f32 v227, v227\n"
"v_mfma_f32_16x16x32_bf16 v[232:235], a[148:151], v[116:119], v[232:235]\n"
"s_add_u32 m0, s53, 45056\n"
"v_cvt_pk_bf16_f32 v19, v226, v227\n"
"v_add_f32 v52, v52, v223\n"
"v_lshrrev_b32 v215, 3, v248\n"
"v_mfma_f32_16x16x32_bf16 v[232:235], a[152:155], v[120:123], v[232:235]\n"
"buffer_load_dwordx4 v239, s[24:27], s21 offen offset:0 lds\n"
"v_mul_u32_u24 v215, 1152, v215\n"
"v_mfma_f32_16x16x32_bf16 v[232:235], a[156:159], v[124:127], v[232:235]\n"
"v_exp_f32 v228, v228\n"
"v_mfma_f32_16x16x32_bf16 a[64:67], a[192:195], v[16:19], a[64:67]\n"
"v_add_f32 v52, v52, v224\n"
"v_add_f32 v52, v52, v225\n"
"v_mfma_f32_16x16x32_bf16 a[68:71], a[196:199], v[16:19], a[68:71]\n"
"v_exp_f32 v229, v229\n"
"v_mfma_f32_16x16x32_bf16 a[72:75], a[200:203], v[16:19], a[72:75]\n"
"v_cvt_pk_bf16_f32 v20, v228, v229\n"
"v_add_f32 v52, v52, v226\n"
"v_mfma_f32_16x16x32_bf16 a[76:79], a[204:207], v[16:19], a[76:79]\n"
"s_waitcnt vmcnt(26) lgkmcnt(0)\n"
"v_exp_f32 v230, v230\n"
"v_mfma_f32_16x16x32_bf16 a[80:83], a[208:211], v[16:19], a[80:83]\n"
"s_barrier\n"
"v_mfma_f32_16x16x32_bf16 a[84:87], a[212:215], v[16:19], a[84:87]\n"
"v_exp_f32 v231, v231\n"
"v_mfma_f32_16x16x32_bf16 a[88:91], a[216:219], v[16:19], a[88:91]\n"
"v_cvt_pk_bf16_f32 v21, v230, v231\n"
"v_add_f32 v52, v52, v227\n"
"v_and_b32 v217, 7, v248\n"
"v_mfma_f32_16x16x32_bf16 a[92:95], a[220:223], v[16:19], a[92:95]\n"
"v_exp_f32 v232, v232\n"
"v_mfma_f32_16x16x32_bf16 v[0:3], a[160:163], v[64:67], v[24:27]\n"
"ds_read_b128 a[128:131], v241 offset:32768\n"
"v_lshl_add_u32 v215, v217, 7, v215\n"
"v_mfma_f32_16x16x32_bf16 v[0:3], a[164:167], v[68:71], v[0:3]\n"
"s_and_b32 s46, s44, 7\n"
"v_exp_f32 v233, v233\n"
"v_mfma_f32_16x16x32_bf16 v[0:3], a[168:171], v[72:75], v[0:3]\n"
"s_lshl_b32 s46, s46, 2\n"
"ds_read_b128 a[132:135], v241 offset:33024\n"
"v_cvt_pk_bf16_f32 v22, v232, v233\n"
"v_mfma_f32_16x16x32_bf16 v[0:3], a[172:175], v[76:79], v[0:3]\n"
"s_add_u32 s46, s46, s41\n"
"v_exp_f32 v234, v234\n"
"v_mfma_f32_16x16x32_bf16 v[4:7], a[176:179], v[64:67], v[24:27]\n"
"s_mul_i32 s46, s46, 9216\n"
"ds_read_b128 a[136:139], v241 offset:33280\n"
"v_mfma_f32_16x16x32_bf16 v[4:7], a[180:183], v[68:71], v[4:7]\n"
"s_mul_i32 s62, s43, 294912\n"
"v_exp_f32 v235, v235\n"
"v_mfma_f32_16x16x32_bf16 v[4:7], a[184:187], v[72:75], v[4:7]\n"
"s_add_u32 s46, s46, s62\n"
"ds_read_b128 a[140:143], v241 offset:33536\n"
"v_cvt_pk_bf16_f32 v23, v234, v235\n"
"v_mfma_f32_16x16x32_bf16 v[4:7], a[188:191], v[76:79], v[4:7]\n"
"s_add_u32 m0, s53, 73728\n"
"v_exp_f32 v0, v0\n"
"v_mfma_f32_16x16x32_bf16 a[96:99], a[192:195], v[20:23], a[96:99]\n"
"buffer_load_dwordx4 v210, s[24:27], s22 offen offset:0 lds\n"
"v_mfma_f32_16x16x32_bf16 a[100:103], a[196:199], v[20:23], a[100:103]\n"
"v_exp_f32 v1, v1\n"
"v_mfma_f32_16x16x32_bf16 a[104:107], a[200:203], v[20:23], a[104:107]\n"
"ds_read_b128 a[144:147], v241 offset:36864\n"
"buffer_load_dword v250, v215, s[24:27], s46 offen offset:128\n"
"v_cvt_pk_bf16_f32 v16, v0, v1\n"
"v_mfma_f32_16x16x32_bf16 a[108:111], a[204:207], v[20:23], a[108:111]\n"
"v_exp_f32 v2, v2\n"
"v_mfma_f32_16x16x32_bf16 a[112:115], a[208:211], v[20:23], a[112:115]\n"
"ds_read_b128 a[148:151], v241 offset:37120\n"
"v_add_f32 v44, v44, v0\n"
"v_mfma_f32_16x16x32_bf16 a[116:119], a[212:215], v[20:23], a[116:119]\n"
"v_exp_f32 v3, v3\n"
"v_mfma_f32_16x16x32_bf16 a[120:123], a[216:219], v[20:23], a[120:123]\n"
"ds_read_b128 a[152:155], v241 offset:37376\n"
"v_cvt_pk_bf16_f32 v17, v2, v3\n"
"v_mfma_f32_16x16x32_bf16 a[124:127], a[220:223], v[20:23], a[124:127]\n"
"v_exp_f32 v4, v4\n"
"v_mfma_f32_16x16x32_bf16 v[56:59], v[40:43], v[20:23], v[56:59]\n"
"ds_read_b128 a[156:159], v241 offset:37632\n"
"v_add_f32 v44, v44, v1\n"
"v_mfma_f32_16x16x32_bf16 v[8:11], a[160:163], v[80:83], v[28:31]\n"
"v_exp_f32 v5, v5\n"
"v_mfma_f32_16x16x32_bf16 v[8:11], a[164:167], v[84:87], v[8:11]\n"
"ds_read_b64_tr_b16 a[192:193], v244 offset:49152\n"
"v_cvt_pk_bf16_f32 v18, v4, v5\n"
"v_mfma_f32_16x16x32_bf16 v[8:11], a[168:171], v[88:91], v[8:11]\n"
"v_exp_f32 v6, v6\n"
"v_mfma_f32_16x16x32_bf16 v[8:11], a[172:175], v[92:95], v[8:11]\n"
"ds_read_b64_tr_b16 a[194:195], v244 offset:53248\n"
"v_add_f32 v44, v44, v2\n"
"v_mfma_f32_16x16x32_bf16 v[12:15], a[176:179], v[80:83], v[28:31]\n"
"v_exp_f32 v7, v7\n"
"v_mfma_f32_16x16x32_bf16 v[12:15], a[180:183], v[84:87], v[12:15]\n"
"s_add_u32 m0, s53, 73728\n"
"ds_read_b64_tr_b16 a[196:197], v245 offset:49152\n"
"v_cvt_pk_bf16_f32 v19, v6, v7\n"
"v_mfma_f32_16x16x32_bf16 v[12:15], a[184:187], v[88:91], v[12:15]\n"
"buffer_load_dwordx4 v211, s[24:27], s22 offen offset:1024 lds\n"
"v_mfma_f32_16x16x32_bf16 v[12:15], a[188:191], v[92:95], v[12:15]\n"
"s_waitcnt lgkmcnt(11)\n"
"v_exp_f32 v8, v8\n"
"v_mfma_f32_16x16x32_bf16 a[0:3], a[224:227], v[16:19], a[0:3]\n"
"ds_read_b64_tr_b16 a[198:199], v245 offset:53248\n"
"v_add_f32 v44, v44, v3\n"
"v_mfma_f32_16x16x32_bf16 a[4:7], a[228:231], v[16:19], a[4:7]\n"
"v_exp_f32 v9, v9\n"
"v_mfma_f32_16x16x32_bf16 a[8:11], a[232:235], v[16:19], a[8:11]\n"
"ds_read_b64_tr_b16 a[200:201], v244 offset:49408\n"
"v_cvt_pk_bf16_f32 v20, v8, v9\n"
"v_mfma_f32_16x16x32_bf16 a[12:15], a[236:239], v[16:19], a[12:15]\n"
"v_exp_f32 v10, v10\n"
"v_mfma_f32_16x16x32_bf16 a[16:19], a[240:243], v[16:19], a[16:19]\n"
"ds_read_b64_tr_b16 a[202:203], v244 offset:53504\n"
"v_add_f32 v44, v44, v4\n"
"v_mfma_f32_16x16x32_bf16 a[20:23], a[244:247], v[16:19], a[20:23]\n"
"v_exp_f32 v11, v11\n"
"v_mfma_f32_16x16x32_bf16 a[24:27], a[248:251], v[16:19], a[24:27]\n"
"ds_read_b64_tr_b16 a[204:205], v245 offset:49408\n"
"v_cvt_pk_bf16_f32 v21, v10, v11\n"
"v_mfma_f32_16x16x32_bf16 a[28:31], a[252:255], v[16:19], a[28:31]\n"
"v_exp_f32 v12, v12\n"
"v_mfma_f32_16x16x32_bf16 v[220:223], a[160:163], v[96:99], v[32:35]\n"
"ds_read_b64_tr_b16 a[206:207], v245 offset:53504\n"
"v_add_f32 v44, v44, v5\n"
"v_mfma_f32_16x16x32_bf16 v[220:223], a[164:167], v[100:103], v[220:223]\n"
"v_exp_f32 v13, v13\n"
"v_mfma_f32_16x16x32_bf16 v[220:223], a[168:171], v[104:107], v[220:223]\n"
"ds_read_b64_tr_b16 a[208:209], v244 offset:49664\n"
"v_cvt_pk_bf16_f32 v22, v12, v13\n"
"v_mfma_f32_16x16x32_bf16 v[220:223], a[172:175], v[108:111], v[220:223]\n"
"v_exp_f32 v14, v14\n"
"v_mfma_f32_16x16x32_bf16 v[224:227], a[176:179], v[96:99], v[32:35]\n"
"ds_read_b64_tr_b16 a[210:211], v244 offset:53760\n"
"v_add_f32 v44, v44, v6\n"
"v_mfma_f32_16x16x32_bf16 v[224:227], a[180:183], v[100:103], v[224:227]\n"
"v_exp_f32 v15, v15\n"
"v_mfma_f32_16x16x32_bf16 v[224:227], a[184:187], v[104:107], v[224:227]\n"
"ds_read_b64_tr_b16 a[212:213], v245 offset:49664\n"
"buffer_load_dword v250, v215, s[24:27], s46 offen offset:192\n"
"v_cvt_pk_bf16_f32 v23, v14, v15\n"
"v_mfma_f32_16x16x32_bf16 v[224:227], a[188:191], v[108:111], v[224:227]\n"
"s_add_u32 m0, s53, 73728\n"
"v_exp_f32 v220, v220\n"
"v_mfma_f32_16x16x32_bf16 a[32:35], a[224:227], v[20:23], a[32:35]\n"
"buffer_load_dwordx4 v212, s[24:27], s22 offen offset:2048 lds\n"
"v_mfma_f32_16x16x32_bf16 a[36:39], a[228:231], v[20:23], a[36:39]\n"
"v_exp_f32 v221, v221\n"
"v_mfma_f32_16x16x32_bf16 a[40:43], a[232:235], v[20:23], a[40:43]\n"
"ds_read_b64_tr_b16 a[214:215], v245 offset:53760\n"
"v_cvt_pk_bf16_f32 v16, v220, v221\n"
"v_mfma_f32_16x16x32_bf16 a[44:47], a[236:239], v[20:23], a[44:47]\n"
"v_exp_f32 v222, v222\n"
"v_mfma_f32_16x16x32_bf16 a[48:51], a[240:243], v[20:23], a[48:51]\n"
"ds_read_b64_tr_b16 a[216:217], v244 offset:49920\n"
"v_add_f32 v44, v44, v7\n"
"v_mfma_f32_16x16x32_bf16 a[52:55], a[244:247], v[20:23], a[52:55]\n"
"v_exp_f32 v223, v223\n"
"v_mfma_f32_16x16x32_bf16 a[56:59], a[248:251], v[20:23], a[56:59]\n"
"ds_read_b64_tr_b16 a[218:219], v244 offset:54016\n"
"v_cvt_pk_bf16_f32 v17, v222, v223\n"
"v_mfma_f32_16x16x32_bf16 a[60:63], a[252:255], v[20:23], a[60:63]\n"
"v_exp_f32 v224, v224\n"
"v_mfma_f32_16x16x32_bf16 v[48:51], v[40:43], v[20:23], v[48:51]\n"
"ds_read_b64_tr_b16 a[220:221], v245 offset:49920\n"
"v_add_f32 v52, v52, v220\n"
"v_mfma_f32_16x16x32_bf16 v[228:231], a[160:163], v[112:115], v[36:39]\n"
"v_exp_f32 v225, v225\n"
"v_mfma_f32_16x16x32_bf16 v[228:231], a[164:167], v[116:119], v[228:231]\n"
"ds_read_b64_tr_b16 a[222:223], v245 offset:54016\n"
"v_cvt_pk_bf16_f32 v18, v224, v225\n"
"v_mfma_f32_16x16x32_bf16 v[228:231], a[168:171], v[120:123], v[228:231]\n"
"v_exp_f32 v226, v226\n"
"v_mfma_f32_16x16x32_bf16 v[228:231], a[172:175], v[124:127], v[228:231]\n"
"v_add_f32 v52, v52, v221\n"
"v_add_f32 v52, v52, v222\n"
"v_mfma_f32_16x16x32_bf16 v[232:235], a[176:179], v[112:115], v[36:39]\n"
"v_exp_f32 v227, v227\n"
"v_mfma_f32_16x16x32_bf16 v[232:235], a[180:183], v[116:119], v[232:235]\n"
"s_add_u32 m0, s53, 73728\n"
"v_cvt_pk_bf16_f32 v19, v226, v227\n"
"v_add_f32 v52, v52, v223\n"
"v_mfma_f32_16x16x32_bf16 v[232:235], a[184:187], v[120:123], v[232:235]\n"
"buffer_load_dwordx4 v213, s[24:27], s22 offen offset:3072 lds\n"
"v_mfma_f32_16x16x32_bf16 v[232:235], a[188:191], v[124:127], v[232:235]\n"
"v_exp_f32 v228, v228\n"
"v_mfma_f32_16x16x32_bf16 a[64:67], a[224:227], v[16:19], a[64:67]\n"
"v_add_f32 v52, v52, v224\n"
"v_add_f32 v52, v52, v225\n"
"v_mfma_f32_16x16x32_bf16 a[68:71], a[228:231], v[16:19], a[68:71]\n"
"v_exp_f32 v229, v229\n"
"v_mfma_f32_16x16x32_bf16 a[72:75], a[232:235], v[16:19], a[72:75]\n"
"v_cvt_pk_bf16_f32 v20, v228, v229\n"
"v_add_f32 v52, v52, v226\n"
"v_mfma_f32_16x16x32_bf16 a[76:79], a[236:239], v[16:19], a[76:79]\n"
"v_exp_f32 v230, v230\n"
"v_mfma_f32_16x16x32_bf16 a[80:83], a[240:243], v[16:19], a[80:83]\n"
"s_waitcnt lgkmcnt(8)\n"
"v_mfma_f32_16x16x32_bf16 a[84:87], a[244:247], v[16:19], a[84:87]\n"
"v_exp_f32 v231, v231\n"
"v_mfma_f32_16x16x32_bf16 a[88:91], a[248:251], v[16:19], a[88:91]\n"
"v_cvt_pk_bf16_f32 v21, v230, v231\n"
"v_add_f32 v52, v52, v227\n"
"v_mfma_f32_16x16x32_bf16 a[92:95], a[252:255], v[16:19], a[92:95]\n"
"v_exp_f32 v232, v232\n"
"v_mfma_f32_16x16x32_bf16 v[0:3], a[128:131], v[64:67], v[24:27]\n"
"ds_read_b128 a[160:163], v241 offset:40960\n"
"v_mfma_f32_16x16x32_bf16 v[0:3], a[132:135], v[68:71], v[0:3]\n"
"v_exp_f32 v233, v233\n"
"v_mfma_f32_16x16x32_bf16 v[0:3], a[136:139], v[72:75], v[0:3]\n"
"ds_read_b128 a[164:167], v241 offset:41216\n"
"v_cvt_pk_bf16_f32 v22, v232, v233\n"
"v_mfma_f32_16x16x32_bf16 v[0:3], a[140:143], v[76:79], v[0:3]\n"
"v_exp_f32 v234, v234\n"
"v_mfma_f32_16x16x32_bf16 v[4:7], a[144:147], v[64:67], v[24:27]\n"
"ds_read_b128 a[168:171], v241 offset:41472\n"
"v_mfma_f32_16x16x32_bf16 v[4:7], a[148:151], v[68:71], v[4:7]\n"
"v_exp_f32 v235, v235\n"
"v_mfma_f32_16x16x32_bf16 v[4:7], a[152:155], v[72:75], v[4:7]\n"
"ds_read_b128 a[172:175], v241 offset:41728\n"
"v_cvt_pk_bf16_f32 v23, v234, v235\n"
"v_mfma_f32_16x16x32_bf16 v[4:7], a[156:159], v[76:79], v[4:7]\n"
"s_add_u32 m0, s53, 77824\n"
"v_exp_f32 v0, v0\n"
"v_mfma_f32_16x16x32_bf16 a[96:99], a[224:227], v[20:23], a[96:99]\n"
"buffer_load_dwordx4 v214, s[24:27], s22 offen offset:0 lds\n"
"v_mfma_f32_16x16x32_bf16 a[100:103], a[228:231], v[20:23], a[100:103]\n"
"v_exp_f32 v1, v1\n"
"v_mfma_f32_16x16x32_bf16 a[104:107], a[232:235], v[20:23], a[104:107]\n"
"ds_read_b128 a[176:179], v241 offset:45056\n"
"v_cvt_pk_bf16_f32 v16, v0, v1\n"
"v_mfma_f32_16x16x32_bf16 a[108:111], a[236:239], v[20:23], a[108:111]\n"
"v_exp_f32 v2, v2\n"
"v_mfma_f32_16x16x32_bf16 a[112:115], a[240:243], v[20:23], a[112:115]\n"
"ds_read_b128 a[180:183], v241 offset:45312\n"
"v_add_f32 v44, v44, v0\n"
"v_mfma_f32_16x16x32_bf16 a[116:119], a[244:247], v[20:23], a[116:119]\n"
"v_exp_f32 v3, v3\n"
"v_mfma_f32_16x16x32_bf16 a[120:123], a[248:251], v[20:23], a[120:123]\n"
"ds_read_b128 a[184:187], v241 offset:45568\n"
"v_cvt_pk_bf16_f32 v17, v2, v3\n"
"v_mfma_f32_16x16x32_bf16 a[124:127], a[252:255], v[20:23], a[124:127]\n"
"v_exp_f32 v4, v4\n"
"v_mfma_f32_16x16x32_bf16 v[56:59], v[40:43], v[20:23], v[56:59]\n"
"ds_read_b128 a[188:191], v241 offset:45824\n"
"v_add_f32 v44, v44, v1\n"
"v_mfma_f32_16x16x32_bf16 v[8:11], a[128:131], v[80:83], v[28:31]\n"
"v_exp_f32 v5, v5\n"
"v_mfma_f32_16x16x32_bf16 v[8:11], a[132:135], v[84:87], v[8:11]\n"
"ds_read_b64_tr_b16 a[224:225], v244 offset:57344\n"
"v_cvt_pk_bf16_f32 v18, v4, v5\n"
"v_mfma_f32_16x16x32_bf16 v[8:11], a[136:139], v[88:91], v[8:11]\n"
"v_exp_f32 v6, v6\n"
"v_mfma_f32_16x16x32_bf16 v[8:11], a[140:143], v[92:95], v[8:11]\n"
"ds_read_b64_tr_b16 a[226:227], v244 offset:61440\n"
"v_add_f32 v44, v44, v2\n"
"v_mfma_f32_16x16x32_bf16 v[12:15], a[144:147], v[80:83], v[28:31]\n"
"v_exp_f32 v7, v7\n"
"v_mfma_f32_16x16x32_bf16 v[12:15], a[148:151], v[84:87], v[12:15]\n"
"s_add_u32 m0, s53, 77824\n"
"ds_read_b64_tr_b16 a[228:229], v245 offset:57344\n"
"v_cvt_pk_bf16_f32 v19, v6, v7\n"
"v_mfma_f32_16x16x32_bf16 v[12:15], a[152:155], v[88:91], v[12:15]\n"
"buffer_load_dwordx4 v236, s[24:27], s22 offen offset:1024 lds\n"
"v_mfma_f32_16x16x32_bf16 v[12:15], a[156:159], v[92:95], v[12:15]\n"
"s_waitcnt lgkmcnt(11)\n"
"v_exp_f32 v8, v8\n"
"v_mfma_f32_16x16x32_bf16 a[0:3], a[192:195], v[16:19], a[0:3]\n"
"ds_read_b64_tr_b16 a[230:231], v245 offset:61440\n"
"v_add_f32 v44, v44, v3\n"
"v_mfma_f32_16x16x32_bf16 a[4:7], a[196:199], v[16:19], a[4:7]\n"
"v_exp_f32 v9, v9\n"
"v_mfma_f32_16x16x32_bf16 a[8:11], a[200:203], v[16:19], a[8:11]\n"
"ds_read_b64_tr_b16 a[232:233], v244 offset:57600\n"
"v_cvt_pk_bf16_f32 v20, v8, v9\n"
"v_mfma_f32_16x16x32_bf16 a[12:15], a[204:207], v[16:19], a[12:15]\n"
"v_exp_f32 v10, v10\n"
"v_mfma_f32_16x16x32_bf16 a[16:19], a[208:211], v[16:19], a[16:19]\n"
"ds_read_b64_tr_b16 a[234:235], v244 offset:61696\n"
"v_add_f32 v44, v44, v4\n"
"v_mfma_f32_16x16x32_bf16 a[20:23], a[212:215], v[16:19], a[20:23]\n"
"s_add_u32 m0, s53, 77824\n"
"v_exp_f32 v11, v11\n"
"v_mfma_f32_16x16x32_bf16 a[24:27], a[216:219], v[16:19], a[24:27]\n"
"buffer_load_dwordx4 v237, s[24:27], s22 offen offset:2048 lds\n"
"ds_read_b64_tr_b16 a[236:237], v245 offset:57600\n"
"v_cvt_pk_bf16_f32 v21, v10, v11\n"
"v_mfma_f32_16x16x32_bf16 a[28:31], a[220:223], v[16:19], a[28:31]\n"
"v_exp_f32 v12, v12\n"
"v_mfma_f32_16x16x32_bf16 v[220:223], a[128:131], v[96:99], v[32:35]\n"
"ds_read_b64_tr_b16 a[238:239], v245 offset:61696\n"
"v_add_f32 v44, v44, v5\n"
"v_mfma_f32_16x16x32_bf16 v[220:223], a[132:135], v[100:103], v[220:223]\n"
"v_exp_f32 v13, v13\n"
"v_mfma_f32_16x16x32_bf16 v[220:223], a[136:139], v[104:107], v[220:223]\n"
"ds_read_b64_tr_b16 a[240:241], v244 offset:57856\n"
"v_cvt_pk_bf16_f32 v22, v12, v13\n"
"v_mfma_f32_16x16x32_bf16 v[220:223], a[140:143], v[108:111], v[220:223]\n"
"v_exp_f32 v14, v14\n"
"v_mfma_f32_16x16x32_bf16 v[224:227], a[144:147], v[96:99], v[32:35]\n"
"ds_read_b64_tr_b16 a[242:243], v244 offset:61952\n"
"v_add_f32 v44, v44, v6\n"
"v_mfma_f32_16x16x32_bf16 v[224:227], a[148:151], v[100:103], v[224:227]\n"
"v_exp_f32 v15, v15\n"
"v_mfma_f32_16x16x32_bf16 v[224:227], a[152:155], v[104:107], v[224:227]\n"
"ds_read_b64_tr_b16 a[244:245], v245 offset:57856\n"
"v_cvt_pk_bf16_f32 v23, v14, v15\n"
"v_mfma_f32_16x16x32_bf16 v[224:227], a[156:159], v[108:111], v[224:227]\n"
"s_add_u32 m0, s53, 77824\n"
"v_exp_f32 v220, v220\n"
"v_mfma_f32_16x16x32_bf16 a[32:35], a[192:195], v[20:23], a[32:35]\n"
"buffer_load_dwordx4 v238, s[24:27], s22 offen offset:3072 lds\n"
"v_mfma_f32_16x16x32_bf16 a[36:39], a[196:199], v[20:23], a[36:39]\n"
"v_exp_f32 v221, v221\n"
"v_mfma_f32_16x16x32_bf16 a[40:43], a[200:203], v[20:23], a[40:43]\n"
"ds_read_b64_tr_b16 a[246:247], v245 offset:61952\n"
"v_cvt_pk_bf16_f32 v16, v220, v221\n"
"v_mfma_f32_16x16x32_bf16 a[44:47], a[204:207], v[20:23], a[44:47]\n"
"v_exp_f32 v222, v222\n"
"v_mfma_f32_16x16x32_bf16 a[48:51], a[208:211], v[20:23], a[48:51]\n"
"ds_read_b64_tr_b16 a[248:249], v244 offset:58112\n"
"v_add_f32 v44, v44, v7\n"
"v_mfma_f32_16x16x32_bf16 a[52:55], a[212:215], v[20:23], a[52:55]\n"
"v_exp_f32 v223, v223\n"
"v_mfma_f32_16x16x32_bf16 a[56:59], a[216:219], v[20:23], a[56:59]\n"
"ds_read_b64_tr_b16 a[250:251], v244 offset:62208\n"
"v_cvt_pk_bf16_f32 v17, v222, v223\n"
"v_mfma_f32_16x16x32_bf16 a[60:63], a[220:223], v[20:23], a[60:63]\n"
"v_exp_f32 v224, v224\n"
"v_mfma_f32_16x16x32_bf16 v[48:51], v[40:43], v[20:23], v[48:51]\n"
"ds_read_b64_tr_b16 a[252:253], v245 offset:58112\n"
"v_add_f32 v52, v52, v220\n"
"v_mfma_f32_16x16x32_bf16 v[228:231], a[128:131], v[112:115], v[36:39]\n"
"v_exp_f32 v225, v225\n"
"v_mfma_f32_16x16x32_bf16 v[228:231], a[132:135], v[116:119], v[228:231]\n"
"ds_read_b64_tr_b16 a[254:255], v245 offset:62208\n"
"v_cvt_pk_bf16_f32 v18, v224, v225\n"
"v_mfma_f32_16x16x32_bf16 v[228:231], a[136:139], v[120:123], v[228:231]\n"
"v_exp_f32 v226, v226\n"
"v_mfma_f32_16x16x32_bf16 v[228:231], a[140:143], v[124:127], v[228:231]\n"
"v_add_f32 v52, v52, v221\n"
"v_add_f32 v52, v52, v222\n"
"v_mfma_f32_16x16x32_bf16 v[232:235], a[144:147], v[112:115], v[36:39]\n"
"v_exp_f32 v227, v227\n"
"v_mfma_f32_16x16x32_bf16 v[232:235], a[148:151], v[116:119], v[232:235]\n"
"s_add_u32 m0, s53, 81920\n"
"v_cvt_pk_bf16_f32 v19, v226, v227\n"
"v_add_f32 v52, v52, v223\n"
"v_mfma_f32_16x16x32_bf16 v[232:235], a[152:155], v[120:123], v[232:235]\n"
"buffer_load_dwordx4 v239, s[24:27], s22 offen offset:0 lds\n"
"v_mfma_f32_16x16x32_bf16 v[232:235], a[156:159], v[124:127], v[232:235]\n"
"v_exp_f32 v228, v228\n"
"v_mfma_f32_16x16x32_bf16 a[64:67], a[192:195], v[16:19], a[64:67]\n"
"v_add_f32 v52, v52, v224\n"
"v_add_f32 v52, v52, v225\n"
"v_mfma_f32_16x16x32_bf16 a[68:71], a[196:199], v[16:19], a[68:71]\n"
"v_exp_f32 v229, v229\n"
"v_mfma_f32_16x16x32_bf16 a[72:75], a[200:203], v[16:19], a[72:75]\n"
"v_cvt_pk_bf16_f32 v20, v228, v229\n"
"v_add_f32 v52, v52, v226\n"
"v_mfma_f32_16x16x32_bf16 a[76:79], a[204:207], v[16:19], a[76:79]\n"
"s_waitcnt vmcnt(23) lgkmcnt(0)\n"
"v_exp_f32 v230, v230\n"
"v_mfma_f32_16x16x32_bf16 a[80:83], a[208:211], v[16:19], a[80:83]\n"
"s_barrier\n"
"v_mfma_f32_16x16x32_bf16 a[84:87], a[212:215], v[16:19], a[84:87]\n"
"v_exp_f32 v231, v231\n"
"v_mfma_f32_16x16x32_bf16 a[88:91], a[216:219], v[16:19], a[88:91]\n"
"v_cvt_pk_bf16_f32 v21, v230, v231\n"
"v_add_f32 v52, v52, v227\n"
"v_mfma_f32_16x16x32_bf16 a[92:95], a[220:223], v[16:19], a[92:95]\n"
"v_exp_f32 v232, v232\n"
"v_mfma_f32_16x16x32_bf16 v[0:3], a[160:163], v[64:67], v[24:27]\n"
"ds_read_b64_tr_b16 a[128:129], v202 offset:0\n"
"v_mfma_f32_16x16x32_bf16 v[0:3], a[164:167], v[68:71], v[0:3]\n"
"s_add_u32 m0, s53, 110592\n"
"v_exp_f32 v233, v233\n"
"v_mfma_f32_16x16x32_bf16 v[0:3], a[168:171], v[72:75], v[0:3]\n"
"buffer_load_dwordx4 v210, s[24:27], s23 offen offset:0 lds\n"
"ds_read_b64_tr_b16 a[130:131], v202 offset:4608\n"
"v_cvt_pk_bf16_f32 v22, v232, v233\n"
"v_mfma_f32_16x16x32_bf16 v[0:3], a[172:175], v[76:79], v[0:3]\n"
"v_exp_f32 v234, v234\n"
"v_mfma_f32_16x16x32_bf16 v[4:7], a[176:179], v[64:67], v[24:27]\n"
"ds_read_b64_tr_b16 a[132:133], v202 offset:32\n"
"v_mfma_f32_16x16x32_bf16 v[4:7], a[180:183], v[68:71], v[4:7]\n"
"s_add_u32 m0, s53, 110592\n"
"v_exp_f32 v235, v235\n"
"v_mfma_f32_16x16x32_bf16 v[4:7], a[184:187], v[72:75], v[4:7]\n"
"buffer_load_dwordx4 v211, s[24:27], s23 offen offset:1024 lds\n"
"ds_read_b64_tr_b16 a[134:135], v202 offset:4640\n"
"v_cvt_pk_bf16_f32 v23, v234, v235\n"
"v_mfma_f32_16x16x32_bf16 v[4:7], a[188:191], v[76:79], v[4:7]\n"
"v_exp_f32 v0, v0\n"
"v_mfma_f32_16x16x32_bf16 a[96:99], a[192:195], v[20:23], a[96:99]\n"
"s_nop 0\n"
"v_mfma_f32_16x16x32_bf16 a[100:103], a[196:199], v[20:23], a[100:103]\n"
"s_add_u32 m0, s53, 110592\n"
"v_exp_f32 v1, v1\n"
"v_mfma_f32_16x16x32_bf16 a[104:107], a[200:203], v[20:23], a[104:107]\n"
"buffer_load_dwordx4 v212, s[24:27], s23 offen offset:2048 lds\n"
"ds_read_b64_tr_b16 a[136:137], v202 offset:64\n"
"v_cvt_pk_bf16_f32 v16, v0, v1\n"
"v_mfma_f32_16x16x32_bf16 a[108:111], a[204:207], v[20:23], a[108:111]\n"
"v_exp_f32 v2, v2\n"
"v_mfma_f32_16x16x32_bf16 a[112:115], a[208:211], v[20:23], a[112:115]\n"
"ds_read_b64_tr_b16 a[138:139], v202 offset:4672\n"
"v_add_f32 v44, v44, v0\n"
"v_mfma_f32_16x16x32_bf16 a[116:119], a[212:215], v[20:23], a[116:119]\n"
"s_add_u32 m0, s53, 110592\n"
"v_exp_f32 v3, v3\n"
"v_mfma_f32_16x16x32_bf16 a[120:123], a[216:219], v[20:23], a[120:123]\n"
"buffer_load_dwordx4 v213, s[24:27], s23 offen offset:3072 lds\n"
"ds_read_b64_tr_b16 a[140:141], v202 offset:96\n"
"v_cvt_pk_bf16_f32 v17, v2, v3\n"
"v_mfma_f32_16x16x32_bf16 a[124:127], a[220:223], v[20:23], a[124:127]\n"
"v_exp_f32 v4, v4\n"
"v_mfma_f32_16x16x32_bf16 v[56:59], v[40:43], v[20:23], v[56:59]\n"
"ds_read_b64_tr_b16 a[142:143], v202 offset:4704\n"
"v_add_f32 v44, v44, v1\n"
"v_mfma_f32_16x16x32_bf16 v[8:11], a[160:163], v[80:83], v[28:31]\n"
"s_add_u32 m0, s53, 114688\n"
"v_exp_f32 v5, v5\n"
"v_mfma_f32_16x16x32_bf16 v[8:11], a[164:167], v[84:87], v[8:11]\n"
"buffer_load_dwordx4 v214, s[24:27], s23 offen offset:0 lds\n"
"ds_read_b64_tr_b16 a[144:145], v202 offset:128\n"
"v_cvt_pk_bf16_f32 v18, v4, v5\n"
"v_mfma_f32_16x16x32_bf16 v[8:11], a[168:171], v[88:91], v[8:11]\n"
"v_exp_f32 v6, v6\n"
"v_mfma_f32_16x16x32_bf16 v[8:11], a[172:175], v[92:95], v[8:11]\n"
"ds_read_b64_tr_b16 a[146:147], v202 offset:4736\n"
"v_add_f32 v44, v44, v2\n"
"v_mfma_f32_16x16x32_bf16 v[12:15], a[176:179], v[80:83], v[28:31]\n"
"s_add_u32 m0, s53, 114688\n"
"v_exp_f32 v7, v7\n"
"v_mfma_f32_16x16x32_bf16 v[12:15], a[180:183], v[84:87], v[12:15]\n"
"buffer_load_dwordx4 v236, s[24:27], s23 offen offset:1024 lds\n"
"ds_read_b64_tr_b16 a[148:149], v202 offset:160\n"
"v_cvt_pk_bf16_f32 v19, v6, v7\n"
"v_mfma_f32_16x16x32_bf16 v[12:15], a[184:187], v[88:91], v[12:15]\n"
"s_nop 0\n"
"v_mfma_f32_16x16x32_bf16 v[12:15], a[188:191], v[92:95], v[12:15]\n"
"s_add_u32 m0, s53, 114688\n"
"v_exp_f32 v8, v8\n"
"v_mfma_f32_16x16x32_bf16 a[0:3], a[224:227], v[16:19], a[0:3]\n"
"buffer_load_dwordx4 v237, s[24:27], s23 offen offset:2048 lds\n"
"ds_read_b64_tr_b16 a[150:151], v202 offset:4768\n"
"v_add_f32 v44, v44, v3\n"
"v_mfma_f32_16x16x32_bf16 a[4:7], a[228:231], v[16:19], a[4:7]\n"
"v_exp_f32 v9, v9\n"
"v_mfma_f32_16x16x32_bf16 a[8:11], a[232:235], v[16:19], a[8:11]\n"
"ds_read_b64_tr_b16 a[152:153], v202 offset:192\n"
"v_cvt_pk_bf16_f32 v20, v8, v9\n"
"v_mfma_f32_16x16x32_bf16 a[12:15], a[236:239], v[16:19], a[12:15]\n"
"s_add_u32 m0, s53, 114688\n"
"v_exp_f32 v10, v10\n"
"v_mfma_f32_16x16x32_bf16 a[16:19], a[240:243], v[16:19], a[16:19]\n"
"buffer_load_dwordx4 v238, s[24:27], s23 offen offset:3072 lds\n"
"ds_read_b64_tr_b16 a[154:155], v202 offset:4800\n"
"v_add_f32 v44, v44, v4\n"
"v_mfma_f32_16x16x32_bf16 a[20:23], a[244:247], v[16:19], a[20:23]\n"
"v_exp_f32 v11, v11\n"
"v_mfma_f32_16x16x32_bf16 a[24:27], a[248:251], v[16:19], a[24:27]\n"
"ds_read_b64_tr_b16 a[156:157], v202 offset:224\n"
"v_cvt_pk_bf16_f32 v21, v10, v11\n"
"v_mfma_f32_16x16x32_bf16 a[28:31], a[252:255], v[16:19], a[28:31]\n"
"s_add_u32 m0, s53, 118784\n"
"v_exp_f32 v12, v12\n"
"v_mfma_f32_16x16x32_bf16 v[220:223], a[160:163], v[96:99], v[32:35]\n"
"buffer_load_dwordx4 v239, s[24:27], s23 offen offset:0 lds\n"
"ds_read_b64_tr_b16 a[158:159], v202 offset:4832\n"
"v_add_f32 v44, v44, v5\n"
"v_mfma_f32_16x16x32_bf16 v[220:223], a[164:167], v[100:103], v[220:223]\n"
"v_exp_f32 v13, v13\n"
"v_mfma_f32_16x16x32_bf16 v[220:223], a[168:171], v[104:107], v[220:223]\n"
"ds_read_b64_tr_b16 a[192:193], v205 offset:0\n"
"v_cvt_pk_bf16_f32 v22, v12, v13\n"
"v_mfma_f32_16x16x32_bf16 v[220:223], a[172:175], v[108:111], v[220:223]\n"
"v_exp_f32 v14, v14\n"
"v_mfma_f32_16x16x32_bf16 v[224:227], a[176:179], v[96:99], v[32:35]\n"
"ds_read_b64_tr_b16 a[194:195], v205 offset:4608\n"
"v_add_f32 v44, v44, v6\n"
"v_mfma_f32_16x16x32_bf16 v[224:227], a[180:183], v[100:103], v[224:227]\n"
"v_exp_f32 v15, v15\n"
"v_mfma_f32_16x16x32_bf16 v[224:227], a[184:187], v[104:107], v[224:227]\n"
"ds_read_b64_tr_b16 a[196:197], v202 offset:9216\n"
"v_cvt_pk_bf16_f32 v23, v14, v15\n"
"v_mfma_f32_16x16x32_bf16 v[224:227], a[188:191], v[108:111], v[224:227]\n"
"v_exp_f32 v220, v220\n"
"v_mfma_f32_16x16x32_bf16 a[32:35], a[224:227], v[20:23], a[32:35]\n"
"AT_pg12:\n"
"s_cbranch_execz AT_hr\n"
"v_mfma_f32_16x16x32_bf16 a[36:39], a[228:231], v[20:23], a[36:39]\n"
"v_exp_f32 v221, v221\n"
"v_mfma_f32_16x16x32_bf16 a[40:43], a[232:235], v[20:23], a[40:43]\n"
"ds_read_b64_tr_b16 a[198:199], v202 offset:13824\n"
"v_cvt_pk_bf16_f32 v16, v220, v221\n"
"v_mfma_f32_16x16x32_bf16 a[44:47], a[236:239], v[20:23], a[44:47]\n"
"v_exp_f32 v222, v222\n"
"v_mfma_f32_16x16x32_bf16 a[48:51], a[240:243], v[20:23], a[48:51]\n"
"v_add_f32 v44, v44, v7\n"
"v_mfma_f32_16x16x32_bf16 a[52:55], a[244:247], v[20:23], a[52:55]\n"
"v_exp_f32 v223, v223\n"
"s_add_u32 s46, s52, 4096\n"
"s_add_u32 s47, s52, 8192\n"
"s_add_u32 s48, s52, 12288\n"
"v_mov_b32 v45, v44\n"
"v_mfma_f32_16x16x32_bf16 a[56:59], a[248:251], v[20:23], a[56:59]\n"
"v_cvt_pk_bf16_f32 v17, v222, v223\n"
"s_nop 0\n"
"s_nop 0\n"
"v_permlane16_swap_b32 v44, v45\n"
"v_add_f32 v44, v44, v45\n"
"v_mfma_f32_16x16x32_bf16 a[60:63], a[252:255], v[20:23], a[60:63]\n"
"v_exp_f32 v224, v224\n"
"v_mov_b32 v45, v44\n"
"s_nop 0\n"
"s_nop 0\n"
"v_permlane32_swap_b32 v44, v45\n"
"v_mfma_f32_16x16x32_bf16 v[48:51], v[40:43], v[20:23], v[48:51]\n"
"v_add_f32 v52, v52, v220\n"
"v_add_f32 v44, v44, v45\n"
"v_rcp_f32 v46, v44\n"
"v_accvgpr_read_b32 v216, a0\n"
"v_accvgpr_read_b32 v217, a1\n"
"v_mfma_f32_16x16x32_bf16 v[228:231], a[160:163], v[112:115], v[36:39]\n"
"v_exp_f32 v225, v225\n"
"v_accvgpr_read_b32 v218, a2\n"
"v_accvgpr_read_b32 v219, a3\n"
"v_accvgpr_read_b32 v60, a4\n"
"v_accvgpr_read_b32 v61, a5\n"
"v_mfma_f32_16x16x32_bf16 v[228:231], a[164:167], v[116:119], v[228:231]\n"
"v_cvt_pk_bf16_f32 v18, v224, v225\n"
"v_accvgpr_read_b32 v62, a6\n"
"v_accvgpr_read_b32 v63, a7\n"
"v_mul_f32 v216, v216, v46\n"
"v_mul_f32 v217, v217, v46\n"
"v_mfma_f32_16x16x32_bf16 v[228:231], a[168:171], v[120:123], v[228:231]\n"
"v_exp_f32 v226, v226\n"
"v_mul_f32 v218, v218, v46\n"
"v_mul_f32 v219, v219, v46\n"
"v_mul_f32 v60, v60, v46\n"
"v_mul_f32 v61, v61, v46\n"
"v_mfma_f32_16x16x32_bf16 v[228:231], a[172:175], v[124:127], v[228:231]\n"
"v_add_f32 v52, v52, v221\n"
"v_add_f32 v52, v52, v222\n"
"v_mul_f32 v62, v62, v46\n"
"v_mul_f32 v63, v63, v46\n"
"v_cvt_pk_bf16_f32 v0, v216, v217\n"
"v_cvt_pk_bf16_f32 v1, v218, v219\n"
"v_mfma_f32_16x16x32_bf16 v[232:235], a[176:179], v[112:115], v[36:39]\n"
"v_exp_f32 v227, v227\n"
"v_cvt_pk_bf16_f32 v2, v60, v61\n"
"v_cvt_pk_bf16_f32 v3, v62, v63\n"
"buffer_store_dwordx4 v[0:3], v207, s[28:31], s52 offen offset:0 sc1\n"
"v_accvgpr_read_b32 v216, a8\n"
"v_mfma_f32_16x16x32_bf16 v[232:235], a[180:183], v[116:119], v[232:235]\n"
"v_cvt_pk_bf16_f32 v19, v226, v227\n"
"v_add_f32 v52, v52, v223\n"
"v_accvgpr_read_b32 v217, a9\n"
"v_accvgpr_read_b32 v218, a10\n"
"v_accvgpr_read_b32 v219, a11\n"
"v_accvgpr_read_b32 v60, a12\n"
"v_mfma_f32_16x16x32_bf16 v[232:235], a[184:187], v[120:123], v[232:235]\n"
"v_accvgpr_read_b32 v61, a13\n"
"v_accvgpr_read_b32 v62, a14\n"
"v_accvgpr_read_b32 v63, a15\n"
"v_mul_f32 v216, v216, v46\n"
"v_mfma_f32_16x16x32_bf16 v[232:235], a[188:191], v[124:127], v[232:235]\n"
"v_exp_f32 v228, v228\n"
"v_mul_f32 v217, v217, v46\n"
"v_mul_f32 v218, v218, v46\n"
"v_mul_f32 v219, v219, v46\n"
"v_mul_f32 v60, v60, v46\n"
"v_mfma_f32_16x16x32_bf16 a[64:67], a[224:227], v[16:19], a[64:67]\n"
"v_add_f32 v52, v52, v224\n"
"v_add_f32 v52, v52, v225\n"
"v_mul_f32 v61, v61, v46\n"
"v_mul_f32 v62, v62, v46\n"
"v_mul_f32 v63, v63, v46\n"
"v_cvt_pk_bf16_f32 v4, v216, v217\n"
"v_mfma_f32_16x16x32_bf16 a[68:71], a[228:231], v[16:19], a[68:71]\n"
"v_exp_f32 v229, v229\n"
"v_cvt_pk_bf16_f32 v5, v218, v219\n"
"v_cvt_pk_bf16_f32 v6, v60, v61\n"
"v_cvt_pk_bf16_f32 v7, v62, v63\n"
"buffer_store_dwordx4 v[4:7], v207, s[28:31], s52 offen offset:2048 sc1\n"
"v_mfma_f32_16x16x32_bf16 a[72:75], a[232:235], v[16:19], a[72:75]\n"
"v_cvt_pk_bf16_f32 v20, v228, v229\n"
"v_add_f32 v52, v52, v226\n"
"v_accvgpr_read_b32 v216, a16\n"
"v_accvgpr_read_b32 v217, a17\n"
"v_accvgpr_read_b32 v218, a18\n"
"v_accvgpr_read_b32 v219, a19\n"
"v_mfma_f32_16x16x32_bf16 a[76:79], a[236:239], v[16:19], a[76:79]\n"
"v_exp_f32 v230, v230\n"
"v_accvgpr_read_b32 v60, a20\n"
"v_accvgpr_read_b32 v61, a21\n"
"v_accvgpr_read_b32 v62, a22\n"
"v_accvgpr_read_b32 v63, a23\n"
"v_mfma_f32_16x16x32_bf16 a[80:83], a[240:243], v[16:19], a[80:83]\n"
"v_mul_f32 v216, v216, v46\n"
"v_mul_f32 v217, v217, v46\n"
"v_mul_f32 v218, v218, v46\n"
"v_mul_f32 v219, v219, v46\n"
"v_mfma_f32_16x16x32_bf16 a[84:87], a[244:247], v[16:19], a[84:87]\n"
"v_exp_f32 v231, v231\n"
"v_mul_f32 v60, v60, v46\n"
"v_mul_f32 v61, v61, v46\n"
"v_mul_f32 v62, v62, v46\n"
"v_mul_f32 v63, v63, v46\n"
"v_mfma_f32_16x16x32_bf16 a[88:91], a[248:251], v[16:19], a[88:91]\n"
"v_cvt_pk_bf16_f32 v21, v230, v231\n"
"v_add_f32 v52, v52, v227\n"
"v_cvt_pk_bf16_f32 v8, v216, v217\n"
"v_cvt_pk_bf16_f32 v9, v218, v219\n"
"v_cvt_pk_bf16_f32 v10, v60, v61\n"
"v_cvt_pk_bf16_f32 v11, v62, v63\n"
"v_mfma_f32_16x16x32_bf16 a[92:95], a[252:255], v[16:19], a[92:95]\n"
"v_exp_f32 v232, v232\n"
"buffer_store_dwordx4 v[8:11], v207, s[28:31], s46 offen offset:0 sc1\n"
"v_accvgpr_read_b32 v216, a24\n"
"v_accvgpr_read_b32 v217, a25\n"
"v_accvgpr_read_b32 v218, a26\n"
"v_exp_f32 v233, v233\n"
"v_accvgpr_read_b32 v219, a27\n"
"v_cvt_pk_bf16_f32 v22, v232, v233\n"
"v_accvgpr_read_b32 v60, a28\n"
"v_exp_f32 v234, v234\n"
"v_accvgpr_read_b32 v61, a29\n"
"v_exp_f32 v235, v235\n"
"v_accvgpr_read_b32 v62, a30\n"
"v_cvt_pk_bf16_f32 v23, v234, v235\n"
"v_accvgpr_read_b32 v63, a31\n"
"v_mul_f32 v216, v216, v46\n"
"v_mul_f32 v217, v217, v46\n"
"v_mfma_f32_16x16x32_bf16 a[96:99], a[224:227], v[20:23], a[96:99]\n"
"v_mul_f32 v218, v218, v46\n"
"v_mul_f32 v219, v219, v46\n"
"v_mfma_f32_16x16x32_bf16 a[100:103], a[228:231], v[20:23], a[100:103]\n"
"v_mul_f32 v60, v60, v46\n"
"v_mul_f32 v61, v61, v46\n"
"v_mfma_f32_16x16x32_bf16 a[104:107], a[232:235], v[20:23], a[104:107]\n"
"v_mul_f32 v62, v62, v46\n"
"v_mul_f32 v63, v63, v46\n"
"v_mfma_f32_16x16x32_bf16 a[108:111], a[236:239], v[20:23], a[108:111]\n"
"v_cvt_pk_bf16_f32 v12, v216, v217\n"
"v_cvt_pk_bf16_f32 v13, v218, v219\n"
"v_mfma_f32_16x16x32_bf16 a[112:115], a[240:243], v[20:23], a[112:115]\n"
"v_cvt_pk_bf16_f32 v14, v60, v61\n"
"v_cvt_pk_bf16_f32 v15, v62, v63\n"
"v_mfma_f32_16x16x32_bf16 a[116:119], a[244:247], v[20:23], a[116:119]\n"
"buffer_store_dwordx4 v[12:15], v207, s[28:31], s46 offen offset:2048 sc1\n"
"v_rcp_f32 v48, v49\n"
"v_mfma_f32_16x16x32_bf16 a[120:123], a[248:251], v[20:23], a[120:123]\n"
"v_accvgpr_read_b32 v216, a48\n"
"v_accvgpr_read_b32 v217, a49\n"
"v_mfma_f32_16x16x32_bf16 a[124:127], a[252:255], v[20:23], a[124:127]\n"
"v_accvgpr_read_b32 v218, a50\n"
"v_accvgpr_read_b32 v219, a51\n"
"v_mfma_f32_16x16x32_bf16 v[56:59], v[40:43], v[20:23], v[56:59]\n"
"v_accvgpr_read_b32 v60, a52\n"
"v_accvgpr_read_b32 v61, a53\n"
"v_accvgpr_read_b32 v62, a54\n"
"v_accvgpr_read_b32 v63, a55\n"
"v_mul_f32 v216, v216, v48\n"
"v_mul_f32 v217, v217, v48\n"
"v_mul_f32 v218, v218, v48\n"
"v_mul_f32 v219, v219, v48\n"
"v_mul_f32 v60, v60, v48\n"
"v_mul_f32 v61, v61, v48\n"
"v_mul_f32 v62, v62, v48\n"
"v_mul_f32 v63, v63, v48\n"
"v_cvt_pk_bf16_f32 v24, v216, v217\n"
"v_cvt_pk_bf16_f32 v25, v218, v219\n"
"v_cvt_pk_bf16_f32 v26, v60, v61\n"
"v_cvt_pk_bf16_f32 v27, v62, v63\n"
"buffer_store_dwordx4 v[24:27], v207, s[28:31], s48 offen offset:0 sc1\n"
"v_accvgpr_read_b32 v216, a56\n"
"v_accvgpr_read_b32 v217, a57\n"
"v_accvgpr_read_b32 v218, a58\n"
"v_accvgpr_read_b32 v219, a59\n"
"v_accvgpr_read_b32 v60, a60\n"
"v_accvgpr_read_b32 v61, a61\n"
"v_accvgpr_read_b32 v62, a62\n"
"v_accvgpr_read_b32 v63, a63\n"
"v_mul_f32 v216, v216, v48\n"
"v_mul_f32 v217, v217, v48\n"
"v_mul_f32 v218, v218, v48\n"
"v_mul_f32 v219, v219, v48\n"
"v_mul_f32 v60, v60, v48\n"
"v_mul_f32 v61, v61, v48\n"
"v_mul_f32 v62, v62, v48\n"
"v_mul_f32 v63, v63, v48\n"
"v_cvt_pk_bf16_f32 v28, v216, v217\n"
"v_cvt_pk_bf16_f32 v29, v218, v219\n"
"v_cvt_pk_bf16_f32 v30, v60, v61\n"
"v_cvt_pk_bf16_f32 v31, v62, v63\n"
"buffer_store_dwordx4 v[28:31], v207, s[28:31], s48 offen offset:2048 sc1\n"
"v_mov_b32 v226, v52\n"
"s_nop 0\n"
"s_nop 0\n"
"v_permlane16_swap_b32 v52, v226\n"
"v_add_f32 v52, v52, v226\n"
"v_mov_b32 v226, v52\n"
"s_nop 0\n"
"s_nop 0\n"
"v_permlane32_swap_b32 v52, v226\n"
"v_add_f32 v52, v52, v226\n"
"v_mov_b32 v219, 0x5f800000\n"
"s_nop 7\n"
"v_cmp_nle_f32 s[56:57], v44, v219\n"
"v_cmp_nle_f32 s[58:59], v49, v219\n"
"v_cmp_nle_f32 s[60:61], v52, v219\n"
"v_cmp_nle_f32 s[62:63], v56, v219\n"
"v_rcp_f32 v218, v44\n"
"v_rcp_f32 v220, v49\n"
"v_rcp_f32 v222, v52\n"
"v_rcp_f32 v224, v56\n"
"v_accvgpr_read_b32 v226, a32\n"
"v_accvgpr_read_b32 v227, a33\n"
"v_accvgpr_read_b32 v228, a34\n"
"v_accvgpr_read_b32 v229, a35\n"
"v_accvgpr_read_b32 v230, a36\n"
"v_accvgpr_read_b32 v231, a37\n"
"v_accvgpr_read_b32 v232, a38\n"
"v_accvgpr_read_b32 v233, a39\n"
"v_pk_mul_f32 v[226:227], v[226:227], v[220:221] op_sel_hi:[1,0]\n"
"v_pk_mul_f32 v[228:229], v[228:229], v[220:221] op_sel_hi:[1,0]\n"
"v_pk_mul_f32 v[230:231], v[230:231], v[220:221] op_sel_hi:[1,0]\n"
"v_pk_mul_f32 v[232:233], v[232:233], v[220:221] op_sel_hi:[1,0]\n"
"v_cvt_pk_bf16_f32 v16, v226, v227\n"
"v_cvt_pk_bf16_f32 v17, v228, v229\n"
"v_cvt_pk_bf16_f32 v18, v230, v231\n"
"v_cvt_pk_bf16_f32 v19, v232, v233\n"
"buffer_store_dwordx4 v[16:19], v207, s[28:31], s47 offen offset:0 sc1\n"
"s_or_b64 s[56:57], s[56:57], s[58:59]\n"
"s_or_b64 s[60:61], s[60:61], s[62:63]\n"
"s_or_b64 s[56:57], s[56:57], s[60:61]\n"
"s_cselect_b32 s56, 1, 0\n"
"s_cmp_eq_u32 s45, 0\n"
"s_cselect_b32 s56, s56, 0\n"
"s_lshl_b32 s57, s41, 2\n"
"s_add_u32 s57, s57, s40\n"
"s_add_u32 s57, s57, 163776\n"
"v_mov_b32 v234, s56\n"
"v_mov_b32 v235, s57\n"
"ds_write_b32 v235, v234\n"
"v_accvgpr_read_b32 v226, a40\n"
"v_accvgpr_read_b32 v227, a41\n"
"v_accvgpr_read_b32 v228, a42\n"
"v_accvgpr_read_b32 v229, a43\n"
"v_accvgpr_read_b32 v230, a44\n"
"v_accvgpr_read_b32 v231, a45\n"
"v_accvgpr_read_b32 v232, a46\n"
"v_accvgpr_read_b32 v233, a47\n"
"v_pk_mul_f32 v[226:227], v[226:227], v[220:221] op_sel_hi:[1,0]\n"
"v_pk_mul_f32 v[228:229], v[228:229], v[220:221] op_sel_hi:[1,0]\n"
"v_pk_mul_f32 v[230:231], v[230:231], v[220:221] op_sel_hi:[1,0]\n"
"v_pk_mul_f32 v[232:233], v[232:233], v[220:221] op_sel_hi:[1,0]\n"
"v_cvt_pk_bf16_f32 v20, v226, v227\n"
"v_cvt_pk_bf16_f32 v21, v228, v229\n"
"v_cvt_pk_bf16_f32 v22, v230, v231\n"
"v_cvt_pk_bf16_f32 v23, v232, v233\n"
"buffer_store_dwordx4 v[20:23], v207, s[28:31], s47 offen offset:2048 sc1\n"
"s_and_b32 s60, s55, 4294967292\n"
"s_mov_b32 s23, 9576448\n"
"s_lshl_b32 s62, s60, 6\n"
"v_and_b32 v215, 3, v248\n"
"v_lshlrev_b32 v215, 6, v215\n"
"v_add_u32 v215, s62, v215\n"
"v_min_u32 v251, 1, v253\n"
"v_lshlrev_b32 v251, 28, v251\n"
"s_lshl_b32 s62, s55, 6\n"
"v_add_u32 v251, s62, v251\n"
"v_mov_b32 v252, s85\n"
"v_accvgpr_read_b32 v226, a64\n"
"v_accvgpr_read_b32 v227, a65\n"
"v_accvgpr_read_b32 v228, a66\n"
"v_accvgpr_read_b32 v229, a67\n"
"v_accvgpr_read_b32 v230, a68\n"
"v_accvgpr_read_b32 v231, a69\n"
"v_accvgpr_read_b32 v232, a70\n"
"v_accvgpr_read_b32 v233, a71\n"
"v_pk_mul_f32 v[226:227], v[226:227], v[222:223] op_sel_hi:[1,0]\n"
"v_pk_mul_f32 v[228:229], v[228:229], v[222:223] op_sel_hi:[1,0]\n"
"v_pk_mul_f32 v[230:231], v[230:231], v[222:223] op_sel_hi:[1,0]\n"
"v_pk_mul_f32 v[232:233], v[232:233], v[222:223] op_sel_hi:[1,0]\n"
"v_cvt_pk_bf16_f32 v32, v226, v227\n"
"v_cvt_pk_bf16_f32 v33, v228, v229\n"
"v_cvt_pk_bf16_f32 v34, v230, v231\n"
"v_cvt_pk_bf16_f32 v35, v232, v233\n"
"buffer_store_dwordx4 v[32:35], v207, s[28:31], s52 offen offset:1024 sc1\n"
"s_add_u32 s61, s43, 1\n"
"s_and_b32 s61, s61, 3\n"
"s_add_u32 s61, s61, s60\n"
"s_lshl_b32 s61, s61, 2\n"
"s_add_u32 s61, s61, s41\n"
"s_lshl_b32 s61, s61, 14\n"
"s_add_u32 s20, s61, 9592832\n"
"v_accvgpr_read_b32 v226, a72\n"
"v_accvgpr_read_b32 v227, a73\n"
"v_accvgpr_read_b32 v228, a74\n"
"v_accvgpr_read_b32 v229, a75\n"
"v_accvgpr_read_b32 v230, a76\n"
"v_accvgpr_read_b32 v231, a77\n"
"v_accvgpr_read_b32 v232, a78\n"
"v_accvgpr_read_b32 v233, a79\n"
"v_pk_mul_f32 v[226:227], v[226:227], v[222:223] op_sel_hi:[1,0]\n"
"v_pk_mul_f32 v[228:229], v[228:229], v[222:223] op_sel_hi:[1,0]\n"
"v_pk_mul_f32 v[230:231], v[230:231], v[222:223] op_sel_hi:[1,0]\n"
"v_pk_mul_f32 v[232:233], v[232:233], v[222:223] op_sel_hi:[1,0]\n"
"v_cvt_pk_bf16_f32 v36, v226, v227\n"
"v_cvt_pk_bf16_f32 v37, v228, v229\n"
"v_cvt_pk_bf16_f32 v38, v230, v231\n"
"v_cvt_pk_bf16_f32 v39, v232, v233\n"
"buffer_store_dwordx4 v[36:39], v207, s[28:31], s52 offen offset:3072 sc1\n"
"s_add_u32 s61, s43, 2\n"
"s_and_b32 s61, s61, 3\n"
"s_add_u32 s61, s61, s60\n"
"s_lshl_b32 s61, s61, 2\n"
"s_add_u32 s61, s61, s41\n"
"s_lshl_b32 s61, s61, 14\n"
"s_add_u32 s21, s61, 9592832\n"
"v_accvgpr_read_b32 v226, a80\n"
"v_accvgpr_read_b32 v227, a81\n"
"v_accvgpr_read_b32 v228, a82\n"
"v_accvgpr_read_b32 v229, a83\n"
"v_accvgpr_read_b32 v230, a84\n"
"v_accvgpr_read_b32 v231, a85\n"
"v_accvgpr_read_b32 v232, a86\n"
"v_accvgpr_read_b32 v233, a87\n"
"v_pk_mul_f32 v[226:227], v[226:227], v[222:223] op_sel_hi:[1,0]\n"
"v_pk_mul_f32 v[228:229], v[228:229], v[222:223] op_sel_hi:[1,0]\n"
"v_pk_mul_f32 v[230:231], v[230:231], v[222:223] op_sel_hi:[1,0]\n"
"v_pk_mul_f32 v[232:233], v[232:233], v[222:223] op_sel_hi:[1,0]\n"
"v_cvt_pk_bf16_f32 v40, v226, v227\n"
"v_cvt_pk_bf16_f32 v41, v228, v229\n"
"v_cvt_pk_bf16_f32 v42, v230, v231\n"
"v_cvt_pk_bf16_f32 v43, v232, v233\n"
"buffer_store_dwordx4 v[40:43], v207, s[28:31], s46 offen offset:1024 sc1\n"
"s_add_u32 s61, s43, 3\n"
"s_and_b32 s61, s61, 3\n"
"s_add_u32 s61, s61, s60\n"
"s_lshl_b32 s61, s61, 2\n"
"s_add_u32 s61, s61, s41\n"
"s_lshl_b32 s61, s61, 14\n"
"s_add_u32 s22, s61, 9592832\n"
"v_accvgpr_read_b32 v226, a88\n"
"v_accvgpr_read_b32 v227, a89\n"
"v_accvgpr_read_b32 v228, a90\n"
"v_accvgpr_read_b32 v229, a91\n"
"v_accvgpr_read_b32 v230, a92\n"
"v_accvgpr_read_b32 v231, a93\n"
"v_accvgpr_read_b32 v232, a94\n"
"v_accvgpr_read_b32 v233, a95\n"
"v_pk_mul_f32 v[226:227], v[226:227], v[222:223] op_sel_hi:[1,0]\n"
"v_pk_mul_f32 v[228:229], v[228:229], v[222:223] op_sel_hi:[1,0]\n"
"v_pk_mul_f32 v[230:231], v[230:231], v[222:223] op_sel_hi:[1,0]\n"
"v_pk_mul_f32 v[232:233], v[232:233], v[222:223] op_sel_hi:[1,0]\n"
"v_cvt_pk_bf16_f32 v44, v226, v227\n"
"v_cvt_pk_bf16_f32 v45, v228, v229\n"
"v_cvt_pk_bf16_f32 v46, v230, v231\n"
"v_cvt_pk_bf16_f32 v47, v232, v233\n"
"buffer_store_dwordx4 v[44:47], v207, s[28:31], s46 offen offset:3072 sc1\n"
"s_waitcnt vmcnt(32) lgkmcnt(0)\n"
"s_barrier\n"
"ds_read_b32 v234, v209\n"
"v_accvgpr_read_b32 v226, a96\n"
"v_accvgpr_read_b32 v227, a97\n"
"v_accvgpr_read_b32 v228, a98\n"
"v_accvgpr_read_b32 v229, a99\n"
"v_accvgpr_read_b32 v230, a100\n"
"v_accvgpr_read_b32 v231, a101\n"
"v_accvgpr_read_b32 v232, a102\n"
"v_accvgpr_read_b32 v233, a103\n"
"v_pk_mul_f32 v[226:227], v[226:227], v[224:225] op_sel_hi:[1,0]\n"
"v_pk_mul_f32 v[228:229], v[228:229], v[224:225] op_sel_hi:[1,0]\n"
"v_pk_mul_f32 v[230:231], v[230:231], v[224:225] op_sel_hi:[1,0]\n"
"v_pk_mul_f32 v[232:233], v[232:233], v[224:225] op_sel_hi:[1,0]\n"
"v_cvt_pk_bf16_f32 v48, v226, v227\n"
"v_cvt_pk_bf16_f32 v49, v228, v229\n"
"v_cvt_pk_bf16_f32 v50, v230, v231\n"
"v_cvt_pk_bf16_f32 v51, v232, v233\n"
"buffer_store_dwordx4 v[48:51], v207, s[28:31], s47 offen offset:1024 sc1\n"
"v_accvgpr_read_b32 v226, a104\n"
"v_accvgpr_read_b32 v227, a105\n"
"v_accvgpr_read_b32 v228, a106\n"
"v_accvgpr_read_b32 v229, a107\n"
"v_accvgpr_read_b32 v230, a108\n"
"v_accvgpr_read_b32 v231, a109\n"
"v_accvgpr_read_b32 v232, a110\n"
"v_accvgpr_read_b32 v233, a111\n"
"v_pk_mul_f32 v[226:227], v[226:227], v[224:225] op_sel_hi:[1,0]\n"
"v_pk_mul_f32 v[228:229], v[228:229], v[224:225] op_sel_hi:[1,0]\n"
"v_pk_mul_f32 v[230:231], v[230:231], v[224:225] op_sel_hi:[1,0]\n"
"v_pk_mul_f32 v[232:233], v[232:233], v[224:225] op_sel_hi:[1,0]\n"
"v_cvt_pk_bf16_f32 v52, v226, v227\n"
"v_cvt_pk_bf16_f32 v53, v228, v229\n"
"v_cvt_pk_bf16_f32 v54, v230, v231\n"
"v_cvt_pk_bf16_f32 v55, v232, v233\n"
"buffer_store_dwordx4 v[52:55], v207, s[28:31], s47 offen offset:3072 sc1\n"
"v_accvgpr_read_b32 v226, a112\n"
"v_accvgpr_read_b32 v227, a113\n"
"v_accvgpr_read_b32 v228, a114\n"
"v_accvgpr_read_b32 v229, a115\n"
"v_accvgpr_read_b32 v230, a116\n"
"v_accvgpr_read_b32 v231, a117\n"
"v_accvgpr_read_b32 v232, a118\n"
"v_accvgpr_read_b32 v233, a119\n"
"v_pk_mul_f32 v[226:227], v[226:227], v[224:225] op_sel_hi:[1,0]\n"
"v_pk_mul_f32 v[228:229], v[228:229], v[224:225] op_sel_hi:[1,0]\n"
"v_pk_mul_f32 v[230:231], v[230:231], v[224:225] op_sel_hi:[1,0]\n"
"v_pk_mul_f32 v[232:233], v[232:233], v[224:225] op_sel_hi:[1,0]\n"
"v_cvt_pk_bf16_f32 v56, v226, v227\n"
"v_cvt_pk_bf16_f32 v57, v228, v229\n"
"v_cvt_pk_bf16_f32 v58, v230, v231\n"
"v_cvt_pk_bf16_f32 v59, v232, v233\n"
"buffer_store_dwordx4 v[56:59], v207, s[28:31], s48 offen offset:1024 sc1\n"
"v_accvgpr_read_b32 v226, a120\n"
"v_accvgpr_read_b32 v227, a121\n"
"v_accvgpr_read_b32 v228, a122\n"
"v_accvgpr_read_b32 v229, a123\n"
"v_accvgpr_read_b32 v230, a124\n"
"v_accvgpr_read_b32 v231, a125\n"
"v_accvgpr_read_b32 v232, a126\n"
"v_accvgpr_read_b32 v233, a127\n"
"v_pk_mul_f32 v[226:227], v[226:227], v[224:225] op_sel_hi:[1,0]\n"
"v_pk_mul_f32 v[228:229], v[228:229], v[224:225] op_sel_hi:[1,0]\n"
"v_pk_mul_f32 v[230:231], v[230:231], v[224:225] op_sel_hi:[1,0]\n"
"v_pk_mul_f32 v[232:233], v[232:233], v[224:225] op_sel_hi:[1,0]\n"
"v_cvt_pk_bf16_f32 v60, v226, v227\n"
"v_cvt_pk_bf16_f32 v61, v228, v229\n"
"v_cvt_pk_bf16_f32 v62, v230, v231\n"
"v_cvt_pk_bf16_f32 v63, v232, v233\n"
"buffer_store_dwordx4 v[60:63], v207, s[28:31], s48 offen offset:3072 sc1\n"
"s_waitcnt lgkmcnt(0)\n"
"v_cmp_ne_u32 vcc, 0, v234\n"
"s_nop 0\n"
"s_cbranch_vccnz AT_fb\n"
"v_mfma_f32_16x16x32_bf16 v[64:67], a[128:131], v[0:3], 0\n"
"ds_read_b64_tr_b16 a[200:201], v202 offset:9248\n"
"v_mfma_f32_16x16x32_bf16 v[68:71], a[128:131], v[32:35], 0\n"
"ds_read_b64_tr_b16 a[202:203], v202 offset:13856\n"
"v_mfma_f32_16x16x32_bf16 v[72:75], a[132:135], v[0:3], 0\n"
"s_mov_b32 s56, 41984\n"
"ds_read_b64_tr_b16 a[204:205], v202 offset:9280\n"
"v_mfma_f32_16x16x32_bf16 v[76:79], a[132:135], v[32:35], 0\n"
"s_mov_b32 s57, 46592\n"
"ds_read_b64_tr_b16 a[206:207], v202 offset:13888\n"
"v_mfma_f32_16x16x32_bf16 v[80:83], a[136:139], v[0:3], 0\n"
"s_mov_b32 s58, 51200\n"
"ds_read_b64_tr_b16 a[208:209], v202 offset:9312\n"
"v_mfma_f32_16x16x32_bf16 v[84:87], a[136:139], v[32:35], 0\n"
"s_mov_b32 s59, 55808\n"
"ds_read_b64_tr_b16 a[210:211], v202 offset:13920\n"
"v_mfma_f32_16x16x32_bf16 v[88:91], a[140:143], v[0:3], 0\n"
"ds_read_b64_tr_b16 a[212:213], v202 offset:9344\n"
"v_lshlrev_b32 v246, 2, v254\n"
"v_mfma_f32_16x16x32_bf16 v[92:95], a[140:143], v[32:35], 0\n"
"s_lshl_b32 s54, s44, 2\n"
"ds_read_b64_tr_b16 a[214:215], v202 offset:13952\n"
"v_mfma_f32_16x16x32_bf16 v[96:99], a[144:147], v[0:3], 0\n"
"s_add_u32 s54, s54, s41\n"
"ds_read_b64_tr_b16 a[216:217], v202 offset:9376\n"
"v_mfma_f32_16x16x32_bf16 v[100:103], a[144:147], v[32:35], 0\n"
"s_mul_i32 s54, s54, 36864\n"
"ds_read_b64_tr_b16 a[218:219], v202 offset:13984\n"
"v_mfma_f32_16x16x32_bf16 v[104:107], a[148:151], v[0:3], 0\n"
"s_lshr_b32 s62, s43, 2\n"
"ds_read_b64_tr_b16 a[220:221], v202 offset:9408\n"
"v_mfma_f32_16x16x32_bf16 v[108:111], a[148:151], v[32:35], 0\n"
"s_mul_i32 s62, s62, 2359296\n"
"ds_read_b64_tr_b16 a[222:223], v202 offset:14016\n"
"v_mfma_f32_16x16x32_bf16 v[112:115], a[152:155], v[0:3], 0\n"
"s_add_u32 s54, s54, s62\n"
"ds_read_b64_tr_b16 a[128:129], v202 offset:9440\n"
"v_mfma_f32_16x16x32_bf16 v[116:119], a[152:155], v[32:35], 0\n"
"s_add_u32 s54, s54, 139264\n"
"ds_read_b64_tr_b16 a[130:131], v202 offset:14048\n"
"v_mfma_f32_16x16x32_bf16 v[120:123], a[156:159], v[0:3], 0\n"
"s_bfe_u32 s62, s43, 0x10001\n"
"ds_read_b64_tr_b16 a[132:133], v205 offset:9216\n"
"v_mfma_f32_16x16x32_bf16 v[124:127], a[156:159], v[32:35], 0\n"
"s_mul_i32 s62, s62, 9216\n"
"ds_read_b64_tr_b16 a[134:135], v205 offset:13824\n"
"v_mfma_f32_16x16x32_bf16 v[226:229], a[192:195], v[0:3], 0\n"
"s_add_u32 s54, s54, s62\n"
"ds_read_b64_tr_b16 a[136:137], v202 offset:18432\n"
"v_mfma_f32_16x16x32_bf16 v[230:233], a[192:195], v[32:35], 0\n"
"s_and_b32 s62, s43, 1\n"
"ds_read_b64_tr_b16 a[138:139], v202 offset:23040\n"
"v_mfma_f32_16x16x32_bf16 v[64:67], a[196:199], v[4:7], v[64:67]\n"
"s_lshl_b32 s49, s62, 3\n"
"ds_read_b64_tr_b16 a[140:141], v202 offset:18464\n"
"v_mfma_f32_16x16x32_bf16 v[68:71], a[196:199], v[36:39], v[68:71]\n"
"s_waitcnt lgkmcnt(11)\n"
"s_add_u32 s49, s49, s54\n"
"ds_read_b64_tr_b16 a[142:143], v202 offset:23072\n"
"v_mfma_f32_16x16x32_bf16 v[72:75], a[200:203], v[4:7], v[72:75]\n"
"s_add_u32 s49, s49, 4096\n"
"ds_read_b64_tr_b16 a[144:145], v202 offset:18496\n"
"v_mfma_f32_16x16x32_bf16 v[76:79], a[200:203], v[36:39], v[76:79]\n"
"s_mul_i32 s62, s62, 5120\n"
"ds_read_b64_tr_b16 a[146:147], v202 offset:23104\n"
"v_mfma_f32_16x16x32_bf16 v[80:83], a[204:207], v[4:7], v[80:83]\n"
"s_add_u32 s54, s54, s62\n"
"ds_read_b64_tr_b16 a[148:149], v202 offset:18528\n"
"v_mfma_f32_16x16x32_bf16 v[84:87], a[204:207], v[36:39], v[84:87]\n"
"s_mul_i32 s46, s55, 9216\n"
"ds_read_b64_tr_b16 a[150:151], v202 offset:23136\n"
"v_mfma_f32_16x16x32_bf16 v[88:91], a[208:211], v[4:7], v[88:91]\n"
"s_min_u32 s62, s41, 2\n"
"ds_read_b64_tr_b16 a[152:153], v202 offset:18560\n"
"v_mfma_f32_16x16x32_bf16 v[92:95], a[208:211], v[36:39], v[92:95]\n"
"s_mul_i32 s62, s62, 3072\n"
"ds_read_b64_tr_b16 a[154:155], v202 offset:23168\n"
"v_mfma_f32_16x16x32_bf16 v[96:99], a[212:215], v[4:7], v[96:99]\n"
"s_add_u32 s46, s46, s62\n"
"ds_read_b64_tr_b16 a[156:157], v202 offset:18592\n"
"v_mfma_f32_16x16x32_bf16 v[100:103], a[212:215], v[36:39], v[100:103]\n"
"s_waitcnt lgkmcnt(11)\n"
"s_add_u32 s45, s40, s62\n"
"ds_read_b64_tr_b16 a[158:159], v202 offset:23200\n"
"v_mfma_f32_16x16x32_bf16 v[104:107], a[216:219], v[4:7], v[104:107]\n"
"s_lshr_b32 s47, s55, 1\n"
"ds_read_b64_tr_b16 a[192:193], v202 offset:18624\n"
"v_mfma_f32_16x16x32_bf16 v[108:111], a[216:219], v[36:39], v[108:111]\n"
"s_mul_i32 s47, s47, 18432\n"
"ds_read_b64_tr_b16 a[194:195], v202 offset:23232\n"
"v_mfma_f32_16x16x32_bf16 v[112:115], a[220:223], v[4:7], v[112:115]\n"
"s_and_b32 s62, s55, 1\n"
"ds_read_b64_tr_b16 a[196:197], v202 offset:18656\n"
"v_mfma_f32_16x16x32_bf16 v[116:119], a[220:223], v[36:39], v[116:119]\n"
"s_lshl_b32 s62, s62, 7\n"
"ds_read_b64_tr_b16 a[198:199], v202 offset:23264\n"
"v_mfma_f32_16x16x32_bf16 v[120:123], a[128:131], v[4:7], v[120:123]\n"
"s_add_u32 s47, s47, s62\n"
"ds_read_b64_tr_b16 a[200:201], v205 offset:18432\n"
"v_mfma_f32_16x16x32_bf16 v[124:127], a[128:131], v[36:39], v[124:127]\n"
"s_add_u32 s47, s47, 139264\n"
"ds_read_b64_tr_b16 a[202:203], v205 offset:23040\n"
"v_mfma_f32_16x16x32_bf16 v[226:229], a[132:135], v[4:7], v[226:229]\n"
"s_add_u32 s62, s40, 119808\n"
"ds_read_b64_tr_b16 a[204:205], v200 offset:0\n"
"v_mfma_f32_16x16x32_bf16 v[230:233], a[132:135], v[36:39], v[230:233]\n"
"s_waitcnt lgkmcnt(11)\n"
"ds_read_b64_tr_b16 a[206:207], v200 offset:4608\n"
"v_and_b32 v206, 7, v248\n"
"v_mfma_f32_16x16x32_bf16 v[64:67], a[136:139], v[8:11], v[64:67]\n"
"ds_read_b64_tr_b16 a[208:209], v200 offset:32\n"
"v_lshrrev_b32 v255, 5, v253\n"
"v_mfma_f32_16x16x32_bf16 v[68:71], a[136:139], v[40:43], v[68:71]\n"
"ds_read_b64_tr_b16 a[210:211], v200 offset:4640\n"
"v_bfe_u32 v216, v253, 3, 2\n"
"v_mfma_f32_16x16x32_bf16 v[72:75], a[140:143], v[8:11], v[72:75]\n"
"ds_read_b64_tr_b16 a[212:213], v200 offset:64\n"
"v_lshlrev_b32 v241, 10, v255\n"
"v_mfma_f32_16x16x32_bf16 v[76:79], a[140:143], v[40:43], v[76:79]\n"
"ds_read_b64_tr_b16 a[214:215], v200 offset:4672\n"
"v_lshl_add_u32 v241, v216, 8, v241\n"
"v_mfma_f32_16x16x32_bf16 v[80:83], a[144:147], v[8:11], v[80:83]\n"
"ds_read_b64_tr_b16 a[216:217], v200 offset:96\n"
"v_lshl_add_u32 v241, v206, 4, v241\n"
"v_mfma_f32_16x16x32_bf16 v[84:87], a[144:147], v[40:43], v[84:87]\n"
"ds_read_b64_tr_b16 a[218:219], v200 offset:4704\n"
"v_add_u32 v242, 6144, v241\n"
"v_mfma_f32_16x16x32_bf16 v[88:91], a[148:151], v[8:11], v[88:91]\n"
"ds_read_b64_tr_b16 a[220:221], v200 offset:128\n"
"v_add_u32 v243, 12288, v241\n"
"v_mfma_f32_16x16x32_bf16 v[92:95], a[148:151], v[40:43], v[92:95]\n"
"s_waitcnt lgkmcnt(11)\n"
"ds_read_b64_tr_b16 a[222:223], v200 offset:4736\n"
"v_lshlrev_b32 v255, 5, v255\n"
"v_mfma_f32_16x16x32_bf16 v[96:99], a[152:155], v[8:11], v[96:99]\n"
"ds_read_b64_tr_b16 a[128:129], v200 offset:160\n"
"v_lshl_add_u32 v255, v216, 2, v255\n"
"v_mfma_f32_16x16x32_bf16 v[100:103], a[152:155], v[40:43], v[100:103]\n"
"ds_read_b64_tr_b16 a[130:131], v200 offset:4768\n"
"v_lshl_add_u32 v244, v255, 2, s62\n"
"v_mfma_f32_16x16x32_bf16 v[104:107], a[156:159], v[8:11], v[104:107]\n"
"ds_read_b64_tr_b16 a[132:133], v200 offset:192\n"
"v_mul_u32_u24 v206, 1152, v206\n"
"v_mfma_f32_16x16x32_bf16 v[108:111], a[156:159], v[40:43], v[108:111]\n"
"ds_read_b64_tr_b16 a[134:135], v200 offset:4800\n"
"v_lshl_add_u32 v240, v255, 1, v206\n"
"v_mfma_f32_16x16x32_bf16 v[112:115], a[192:195], v[8:11], v[112:115]\n"
"AT_pg13:\n"
"s_cbranch_execz AT_hr\n"
"ds_read_b64_tr_b16 a[136:137], v200 offset:224\n"
"v_add_u32 v240, s40, v240\n"
"v_mfma_f32_16x16x32_bf16 v[116:119], a[192:195], v[40:43], v[116:119]\n"
"ds_read_b64_tr_b16 a[138:139], v200 offset:4832\n"
"v_lshlrev_b32 v245, 3, v255\n"
"v_mfma_f32_16x16x32_bf16 v[120:123], a[196:199], v[8:11], v[120:123]\n"
"ds_read_b64_tr_b16 a[140:141], v203 offset:0\n"
"v_lshl_add_u32 v247, v253, 4, s62\n"
"v_mfma_f32_16x16x32_bf16 v[124:127], a[196:199], v[40:43], v[124:127]\n"
"s_waitcnt lgkmcnt(11)\n"
"s_mul_i32 s62, s41, 7\n"
"ds_read_b64_tr_b16 a[142:143], v203 offset:4608\n"
"v_mfma_f32_16x16x32_bf16 v[226:229], a[200:203], v[8:11], v[226:229]\n"
"s_min_u32 s62, s62, 20\n"
"ds_read_b64_tr_b16 a[144:145], v200 offset:9216\n"
"v_mfma_f32_16x16x32_bf16 v[230:233], a[200:203], v[40:43], v[230:233]\n"
"s_lshl_b32 s62, s62, 10\n"
"ds_read_b64_tr_b16 a[146:147], v200 offset:13824\n"
"v_mfma_f32_16x16x32_bf16 v[64:67], a[204:207], v[12:15], v[64:67]\n"
"s_add_u32 s50, s50, s62\n"
"ds_read_b64_tr_b16 a[148:149], v200 offset:9248\n"
"v_mfma_f32_16x16x32_bf16 v[68:71], a[204:207], v[44:47], v[68:71]\n"
"s_add_u32 s84, s40, s62\n"
"ds_read_b64_tr_b16 a[150:151], v200 offset:13856\n"
"v_mfma_f32_16x16x32_bf16 v[72:75], a[208:211], v[12:15], v[72:75]\n"
"ds_read_b64_tr_b16 a[152:153], v200 offset:9280\n"
"v_mfma_f32_16x16x32_bf16 v[76:79], a[208:211], v[44:47], v[76:79]\n"
"ds_read_b64_tr_b16 a[154:155], v200 offset:13888\n"
"v_mfma_f32_16x16x32_bf16 v[80:83], a[212:215], v[12:15], v[80:83]\n"
"ds_read_b64_tr_b16 a[156:157], v200 offset:9312\n"
"v_mfma_f32_16x16x32_bf16 v[84:87], a[212:215], v[44:47], v[84:87]\n"
"s_waitcnt lgkmcnt(11)\n"
"ds_read_b64_tr_b16 a[158:159], v200 offset:13920\n"
"v_mfma_f32_16x16x32_bf16 v[88:91], a[216:219], v[12:15], v[88:91]\n"
"ds_read_b64_tr_b16 a[192:193], v200 offset:9344\n"
"v_mfma_f32_16x16x32_bf16 v[92:95], a[216:219], v[44:47], v[92:95]\n"
"ds_read_b64_tr_b16 a[194:195], v200 offset:13952\n"
"v_mfma_f32_16x16x32_bf16 v[96:99], a[220:223], v[12:15], v[96:99]\n"
"ds_read_b64_tr_b16 a[196:197], v200 offset:9376\n"
"v_mfma_f32_16x16x32_bf16 v[100:103], a[220:223], v[44:47], v[100:103]\n"
"ds_read_b64_tr_b16 a[198:199], v200 offset:13984\n"
"v_mfma_f32_16x16x32_bf16 v[104:107], a[128:131], v[12:15], v[104:107]\n"
"ds_read_b64_tr_b16 a[200:201], v200 offset:9408\n"
"v_mfma_f32_16x16x32_bf16 v[108:111], a[128:131], v[44:47], v[108:111]\n"
"ds_read_b64_tr_b16 a[202:203], v200 offset:14016\n"
"v_mfma_f32_16x16x32_bf16 v[112:115], a[132:135], v[12:15], v[112:115]\n"
"ds_read_b64_tr_b16 a[204:205], v200 offset:9440\n"
"v_mfma_f32_16x16x32_bf16 v[116:119], a[132:135], v[44:47], v[116:119]\n"
"s_waitcnt lgkmcnt(11)\n"
"ds_read_b64_tr_b16 a[206:207], v200 offset:14048\n"
"v_mfma_f32_16x16x32_bf16 v[120:123], a[136:139], v[12:15], v[120:123]\n"
"ds_read_b64_tr_b16 a[208:209], v203 offset:9216\n"
"v_mfma_f32_16x16x32_bf16 v[124:127], a[136:139], v[44:47], v[124:127]\n"
"ds_read_b64_tr_b16 a[210:211], v203 offset:13824\n"
"v_mfma_f32_16x16x32_bf16 v[226:229], a[140:143], v[12:15], v[226:229]\n"
"ds_read_b64_tr_b16 a[212:213], v200 offset:18432\n"
"v_mfma_f32_16x16x32_bf16 v[230:233], a[140:143], v[44:47], v[230:233]\n"
"ds_read_b64_tr_b16 a[214:215], v200 offset:23040\n"
"v_mfma_f32_16x16x32_bf16 v[64:67], a[144:147], v[16:19], v[64:67]\n"
"ds_read_b64_tr_b16 a[216:217], v200 offset:18464\n"
"v_mfma_f32_16x16x32_bf16 v[68:71], a[144:147], v[48:51], v[68:71]\n"
"s_waitcnt vmcnt(0)\n"
"ds_read_b64_tr_b16 a[218:219], v200 offset:23072\n"
"v_mfma_f32_16x16x32_bf16 v[72:75], a[148:151], v[16:19], v[72:75]\n"
"s_barrier\n"
"ds_read_b64_tr_b16 a[220:221], v200 offset:18496\n"
"buffer_store_dword v252, v251, s[28:31], s23 offen sc1\n"
"v_mfma_f32_16x16x32_bf16 v[76:79], a[148:151], v[48:51], v[76:79]\n"
"s_waitcnt lgkmcnt(11)\n"
"ds_read_b64_tr_b16 a[222:223], v200 offset:23104\n"
"buffer_load_dwordx4 a[160:163], v246, s[28:31], s56 offen offset:0 sc1\n"
"v_mfma_f32_16x16x32_bf16 v[80:83], a[152:155], v[16:19], v[80:83]\n"
"s_mov_b32 m0, s51\n"
"ds_read_b64_tr_b16 a[128:129], v200 offset:18528\n"
"v_mfma_f32_16x16x32_bf16 v[84:87], a[152:155], v[48:51], v[84:87]\n"
"ds_read_b64_tr_b16 a[130:131], v200 offset:23136\n"
"buffer_load_dwordx4 v210, s[24:27], s36 offen offset:0 lds\n"
"v_mfma_f32_16x16x32_bf16 v[88:91], a[156:159], v[16:19], v[88:91]\n"
"ds_read_b64_tr_b16 a[132:133], v200 offset:18560\n"
"v_mfma_f32_16x16x32_bf16 v[92:95], a[156:159], v[48:51], v[92:95]\n"
"ds_read_b64_tr_b16 a[134:135], v200 offset:23168\n"
"v_mfma_f32_16x16x32_bf16 v[96:99], a[192:195], v[16:19], v[96:99]\n"
"ds_read_b64_tr_b16 a[136:137], v200 offset:18592\n"
"v_mfma_f32_16x16x32_bf16 v[100:103], a[192:195], v[48:51], v[100:103]\n"
"ds_read_b64_tr_b16 a[138:139], v200 offset:23200\n"
"buffer_load_dwordx4 a[164:167], v246, s[28:31], s56 offen offset:16 sc1\n"
"v_mfma_f32_16x16x32_bf16 v[104:107], a[196:199], v[16:19], v[104:107]\n"
"ds_read_b64_tr_b16 a[140:141], v200 offset:18624\n"
"v_mfma_f32_16x16x32_bf16 v[108:111], a[196:199], v[48:51], v[108:111]\n"
"s_waitcnt lgkmcnt(11)\n"
"s_mov_b32 m0, s51\n"
"ds_read_b64_tr_b16 a[142:143], v200 offset:23232\n"
"v_mfma_f32_16x16x32_bf16 v[112:115], a[200:203], v[16:19], v[112:115]\n"
"ds_read_b64_tr_b16 a[144:145], v200 offset:18656\n"
"buffer_load_dwordx4 v211, s[24:27], s36 offen offset:1024 lds\n"
"v_mfma_f32_16x16x32_bf16 v[116:119], a[200:203], v[48:51], v[116:119]\n"
"ds_read_b64_tr_b16 a[146:147], v200 offset:23264\n"
"v_mfma_f32_16x16x32_bf16 v[120:123], a[204:207], v[16:19], v[120:123]\n"
"ds_read_b64_tr_b16 a[148:149], v203 offset:18432\n"
"v_mfma_f32_16x16x32_bf16 v[124:127], a[204:207], v[48:51], v[124:127]\n"
"ds_read_b64_tr_b16 a[150:151], v203 offset:23040\n"
"buffer_load_dwordx4 a[168:171], v246, s[28:31], s57 offen offset:0 sc1\n"
"v_mfma_f32_16x16x32_bf16 v[226:229], a[208:211], v[16:19], v[226:229]\n"
"ds_read_b64_tr_b16 a[152:153], v200 offset:27648\n"
"v_mfma_f32_16x16x32_bf16 v[230:233], a[208:211], v[48:51], v[230:233]\n"
"ds_read_b64_tr_b16 a[154:155], v200 offset:32256\n"
"v_mfma_f32_16x16x32_bf16 v[64:67], a[212:215], v[20:23], v[64:67]\n"
"s_mov_b32 m0, s51\n"
"ds_read_b64_tr_b16 a[156:157], v200 offset:27680\n"
"v_mfma_f32_16x16x32_bf16 v[68:71], a[212:215], v[52:55], v[68:71]\n"
"s_waitcnt lgkmcnt(11)\n"
"ds_read_b64_tr_b16 a[158:159], v200 offset:32288\n"
"buffer_load_dwordx4 v212, s[24:27], s36 offen offset:2048 lds\n"
"v_mfma_f32_16x16x32_bf16 v[72:75], a[216:219], v[20:23], v[72:75]\n"
"ds_read_b64_tr_b16 a[192:193], v200 offset:27712\n"
"v_mfma_f32_16x16x32_bf16 v[76:79], a[216:219], v[52:55], v[76:79]\n"
"ds_read_b64_tr_b16 a[194:195], v200 offset:32320\n"
"buffer_load_dwordx4 a[172:175], v246, s[28:31], s57 offen offset:16 sc1\n"
"v_mfma_f32_16x16x32_bf16 v[80:83], a[220:223], v[20:23], v[80:83]\n"
"ds_read_b64_tr_b16 a[196:197], v200 offset:27744\n"
"v_mfma_f32_16x16x32_bf16 v[84:87], a[220:223], v[52:55], v[84:87]\n"
"ds_read_b64_tr_b16 a[198:199], v200 offset:32352\n"
"v_mfma_f32_16x16x32_bf16 v[88:91], a[128:131], v[20:23], v[88:91]\n"
"ds_read_b64_tr_b16 a[200:201], v200 offset:27776\n"
"v_mfma_f32_16x16x32_bf16 v[92:95], a[128:131], v[52:55], v[92:95]\n"
"s_mov_b32 m0, s51\n"
"ds_read_b64_tr_b16 a[202:203], v200 offset:32384\n"
"v_mfma_f32_16x16x32_bf16 v[96:99], a[132:135], v[20:23], v[96:99]\n"
"ds_read_b64_tr_b16 a[204:205], v200 offset:27808\n"
"buffer_load_dwordx4 v213, s[24:27], s36 offen offset:3072 lds\n"
"v_mfma_f32_16x16x32_bf16 v[100:103], a[132:135], v[52:55], v[100:103]\n"
"s_waitcnt lgkmcnt(11)\n"
"ds_read_b64_tr_b16 a[206:207], v200 offset:32416\n"
"buffer_load_dwordx4 a[176:179], v246, s[28:31], s58 offen offset:0 sc1\n"
"v_mfma_f32_16x16x32_bf16 v[104:107], a[136:139], v[20:23], v[104:107]\n"
"ds_read_b64_tr_b16 a[208:209], v200 offset:27840\n"
"v_mfma_f32_16x16x32_bf16 v[108:111], a[136:139], v[52:55], v[108:111]\n"
"ds_read_b64_tr_b16 a[210:211], v200 offset:32448\n"
"v_mfma_f32_16x16x32_bf16 v[112:115], a[140:143], v[20:23], v[112:115]\n"
"ds_read_b64_tr_b16 a[212:213], v200 offset:27872\n"
"v_mfma_f32_16x16x32_bf16 v[116:119], a[140:143], v[52:55], v[116:119]\n"
"ds_read_b64_tr_b16 a[214:215], v200 offset:32480\n"
"v_mfma_f32_16x16x32_bf16 v[120:123], a[144:147], v[20:23], v[120:123]\n"
"s_add_u32 m0, s51, 4096\n"
"ds_read_b64_tr_b16 a[216:217], v203 offset:27648\n"
"v_mfma_f32_16x16x32_bf16 v[124:127], a[144:147], v[52:55], v[124:127]\n"
"ds_read_b64_tr_b16 a[218:219], v203 offset:32256\n"
"buffer_load_dwordx4 v214, s[24:27], s36 offen offset:0 lds\n"
"v_mfma_f32_16x16x32_bf16 v[226:229], a[148:151], v[20:23], v[226:229]\n"
"ds_read_b64_tr_b16 a[220:221], v200 offset:36864\n"
"buffer_load_dwordx4 a[180:183], v246, s[28:31], s58 offen offset:16 sc1\n"
"v_mfma_f32_16x16x32_bf16 v[230:233], a[148:151], v[52:55], v[230:233]\n"
"s_waitcnt lgkmcnt(11)\n"
"ds_read_b64_tr_b16 a[222:223], v200 offset:41472\n"
"v_mfma_f32_16x16x32_bf16 v[64:67], a[152:155], v[24:27], v[64:67]\n"
"ds_read_b64_tr_b16 a[128:129], v200 offset:36896\n"
"v_mfma_f32_16x16x32_bf16 v[68:71], a[152:155], v[56:59], v[68:71]\n"
"ds_read_b64_tr_b16 a[130:131], v200 offset:41504\n"
"v_mfma_f32_16x16x32_bf16 v[72:75], a[156:159], v[24:27], v[72:75]\n"
"ds_read_b64_tr_b16 a[132:133], v200 offset:36928\n"
"v_mfma_f32_16x16x32_bf16 v[76:79], a[156:159], v[56:59], v[76:79]\n"
"s_add_u32 m0, s51, 4096\n"
"ds_read_b64_tr_b16 a[134:135], v200 offset:41536\n"
"v_mfma_f32_16x16x32_bf16 v[80:83], a[192:195], v[24:27], v[80:83]\n"
"ds_read_b64_tr_b16 a[136:137], v200 offset:36960\n"
"buffer_load_dwordx4 v236, s[24:27], s36 offen offset:1024 lds\n"
"v_mfma_f32_16x16x32_bf16 v[84:87], a[192:195], v[56:59], v[84:87]\n"
"ds_read_b64_tr_b16 a[138:139], v200 offset:41568\n"
"buffer_load_dwordx4 a[184:187], v246, s[28:31], s59 offen offset:0 sc1\n"
"v_mfma_f32_16x16x32_bf16 v[88:91], a[196:199], v[24:27], v[88:91]\n"
"ds_read_b64_tr_b16 a[140:141], v200 offset:36992\n"
"v_mfma_f32_16x16x32_bf16 v[92:95], a[196:199], v[56:59], v[92:95]\n"
"s_waitcnt lgkmcnt(11)\n"
"ds_read_b64_tr_b16 a[142:143], v200 offset:41600\n"
"v_mfma_f32_16x16x32_bf16 v[96:99], a[200:203], v[24:27], v[96:99]\n"
"ds_read_b64_tr_b16 a[144:145], v200 offset:37024\n"
"v_mfma_f32_16x16x32_bf16 v[100:103], a[200:203], v[56:59], v[100:103]\n"
"ds_read_b64_tr_b16 a[146:147], v200 offset:41632\n"
"v_mfma_f32_16x16x32_bf16 v[104:107], a[204:207], v[24:27], v[104:107]\n"
"s_add_u32 m0, s51, 4096\n"
"ds_read_b64_tr_b16 a[148:149], v200 offset:37056\n"
"v_mfma_f32_16x16x32_bf16 v[108:111], a[204:207], v[56:59], v[108:111]\n"
"ds_read_b64_tr_b16 a[150:151], v200 offset:41664\n"
"buffer_load_dwordx4 v237, s[24:27], s36 offen offset:2048 lds\n"
"v_mfma_f32_16x16x32_bf16 v[112:115], a[208:211], v[24:27], v[112:115]\n"
"ds_read_b64_tr_b16 a[152:153], v200 offset:37088\n"
"buffer_load_dwordx4 a[188:191], v246, s[28:31], s59 offen offset:16 sc1\n"
"v_mfma_f32_16x16x32_bf16 v[116:119], a[208:211], v[56:59], v[116:119]\n"
"ds_read_b64_tr_b16 a[154:155], v200 offset:41696\n"
"v_mfma_f32_16x16x32_bf16 v[120:123], a[212:215], v[24:27], v[120:123]\n"
"ds_read_b64_tr_b16 a[156:157], v203 offset:36864\n"
"v_mfma_f32_16x16x32_bf16 v[124:127], a[212:215], v[56:59], v[124:127]\n"
"s_waitcnt lgkmcnt(11)\n"
"ds_read_b64_tr_b16 a[158:159], v203 offset:41472\n"
"v_mfma_f32_16x16x32_bf16 v[226:229], a[216:219], v[24:27], v[226:229]\n"
"ds_read_b64_tr_b16 a[192:193], v200 offset:46080\n"
"v_mfma_f32_16x16x32_bf16 v[230:233], a[216:219], v[56:59], v[230:233]\n"
"s_add_u32 m0, s51, 4096\n"
"ds_read_b64_tr_b16 a[194:195], v200 offset:50688\n"
"v_mfma_f32_16x16x32_bf16 v[64:67], a[220:223], v[28:31], v[64:67]\n"
"ds_read_b64_tr_b16 a[196:197], v200 offset:46112\n"
"buffer_load_dwordx4 v238, s[24:27], s36 offen offset:3072 lds\n"
"v_mfma_f32_16x16x32_bf16 v[68:71], a[220:223], v[60:63], v[68:71]\n"
"ds_read_b64_tr_b16 a[198:199], v200 offset:50720\n"
"buffer_load_dwordx2 a[224:225], v254, s[80:83], 0 offen\n"
"v_mfma_f32_16x16x32_bf16 v[72:75], a[128:131], v[28:31], v[72:75]\n"
"ds_read_b64_tr_b16 a[200:201], v200 offset:46144\n"
"v_mfma_f32_16x16x32_bf16 v[76:79], a[128:131], v[60:63], v[76:79]\n"
"ds_read_b64_tr_b16 a[202:203], v200 offset:50752\n"
"v_mfma_f32_16x16x32_bf16 v[80:83], a[132:135], v[28:31], v[80:83]\n"
"ds_read_b64_tr_b16 a[204:205], v200 offset:46176\n"
"v_mfma_f32_16x16x32_bf16 v[84:87], a[132:135], v[60:63], v[84:87]\n"
"s_waitcnt lgkmcnt(11)\n"
"ds_read_b64_tr_b16 a[206:207], v200 offset:50784\n"
"v_mfma_f32_16x16x32_bf16 v[88:91], a[136:139], v[28:31], v[88:91]\n"
"s_add_u32 m0, s51, 8192\n"
"ds_read_b64_tr_b16 a[208:209], v200 offset:46208\n"
"v_mfma_f32_16x16x32_bf16 v[92:95], a[136:139], v[60:63], v[92:95]\n"
"ds_read_b64_tr_b16 a[210:211], v200 offset:50816\n"
"buffer_load_dwordx4 v239, s[24:27], s36 offen offset:0 lds\n"
"v_mfma_f32_16x16x32_bf16 v[96:99], a[140:143], v[28:31], v[96:99]\n"
"ds_read_b64_tr_b16 a[212:213], v200 offset:46240\n"
"v_mfma_f32_16x16x32_bf16 v[100:103], a[140:143], v[60:63], v[100:103]\n"
"ds_read_b64_tr_b16 a[214:215], v200 offset:50848\n"
"v_mfma_f32_16x16x32_bf16 v[104:107], a[144:147], v[28:31], v[104:107]\n"
"ds_read_b64_tr_b16 a[216:217], v200 offset:46272\n"
"v_mfma_f32_16x16x32_bf16 v[108:111], a[144:147], v[60:63], v[108:111]\n"
"ds_read_b64_tr_b16 a[218:219], v200 offset:50880\n"
"v_mfma_f32_16x16x32_bf16 v[112:115], a[148:151], v[28:31], v[112:115]\n"
"ds_read_b64_tr_b16 a[220:221], v200 offset:46304\n"
"v_mfma_f32_16x16x32_bf16 v[116:119], a[148:151], v[60:63], v[116:119]\n"
"s_waitcnt lgkmcnt(11)\n"
"ds_read_b64_tr_b16 a[222:223], v200 offset:50912\n"
"v_mfma_f32_16x16x32_bf16 v[120:123], a[152:155], v[28:31], v[120:123]\n"
"ds_read_b64_tr_b16 a[128:129], v203 offset:46080\n"
"buffer_load_dword v249, v215, s[28:31], s23 offen sc1\n"
"v_mfma_f32_16x16x32_bf16 v[124:127], a[152:155], v[60:63], v[124:127]\n"
"ds_read_b64_tr_b16 a[130:131], v203 offset:50688\n"
"v_mfma_f32_16x16x32_bf16 v[226:229], a[156:159], v[28:31], v[226:229]\n"
"ds_read_b64_tr_b16 a[132:133], v200 offset:55296\n"
"v_mfma_f32_16x16x32_bf16 v[230:233], a[156:159], v[60:63], v[230:233]\n"
"ds_read_b64_tr_b16 a[134:135], v200 offset:59904\n"
"s_barrier\n"
"s_waitcnt vmcnt(2)\n"
"s_mov_b32 s87, 0x80000\n"
"v_accvgpr_read_b32 v26, a160\n"
"v_accvgpr_read_b32 v27, a161\n"
"v_accvgpr_read_b32 v28, a162\n"
"v_accvgpr_read_b32 v29, a163\n"
"v_accvgpr_read_b32 v30, a164\n"
"v_accvgpr_read_b32 v31, a165\n"
"v_accvgpr_read_b32 v32, a166\n"
"v_accvgpr_read_b32 v33, a167\n"
"v_accvgpr_read_b32 v34, a168\n"
"v_accvgpr_read_b32 v35, a169\n"
"v_accvgpr_read_b32 v36, a170\n"
"v_accvgpr_read_b32 v37, a171\n"
"v_accvgpr_read_b32 v38, a172\n"
"v_accvgpr_read_b32 v39, a173\n"
"v_accvgpr_read_b32 v40, a174\n"
"v_accvgpr_read_b32 v41, a175\n"
"v_accvgpr_read_b32 v42, a176\n"
"v_accvgpr_read_b32 v43, a177\n"
"v_accvgpr_read_b32 v44, a178\n"
"v_accvgpr_read_b32 v45, a179\n"
"v_accvgpr_read_b32 v46, a180\n"
"v_accvgpr_read_b32 v47, a181\n"
"v_accvgpr_read_b32 v48, a182\n"
"v_accvgpr_read_b32 v49, a183\n"
"v_accvgpr_read_b32 v50, a184\n"
"v_accvgpr_read_b32 v51, a185\n"
"v_accvgpr_read_b32 v52, a186\n"
"v_accvgpr_read_b32 v53, a187\n"
"v_accvgpr_read_b32 v54, a188\n"
"v_accvgpr_read_b32 v55, a189\n"
"v_accvgpr_read_b32 v56, a190\n"
"v_accvgpr_read_b32 v57, a191\n"
"v_accvgpr_read_b32 v58, a224\n"
"v_accvgpr_read_b32 v59, a225\n"
"AT_tchk:\n"
"v_xor_b32 v24, s85, v27\n"
"v_xor_b32 v25, s85, v29\n"
"v_bitop3_b32 v24, v31, s85, v24 bitop3:0xbe\n"
"v_bitop3_b32 v25, v33, s85, v25 bitop3:0xbe\n"
"v_bitop3_b32 v24, v35, s85, v24 bitop3:0xbe\n"
"v_bitop3_b32 v25, v37, s85, v25 bitop3:0xbe\n"
"v_bitop3_b32 v24, v39, s85, v24 bitop3:0xbe\n"
"v_bitop3_b32 v25, v41, s85, v25 bitop3:0xbe\n"
"v_bitop3_b32 v24, v43, s85, v24 bitop3:0xbe\n"
"v_bitop3_b32 v25, v45, s85, v25 bitop3:0xbe\n"
"v_bitop3_b32 v24, v47, s85, v24 bitop3:0xbe\n"
"v_bitop3_b32 v25, v49, s85, v25 bitop3:0xbe\n"
"v_bitop3_b32 v24, v51, s85, v24 bitop3:0xbe\n"
"v_bitop3_b32 v25, v53, s85, v25 bitop3:0xbe\n"
"v_bitop3_b32 v24, v55, s85, v24 bitop3:0xbe\n"
"v_bitop3_b32 v25, v57, s85, v25 bitop3:0xbe\n"
"v_or_b32 v24, v24, v25\n"
"v_cmp_ne_u32 s[62:63], 0, v24\n"
"v_cmp_gt_u32 vcc, 144, v253\n"
"s_nop 1\n"
"s_and_b64 s[62:63], vcc, s[62:63]\n"
"s_cmp_eq_u64 s[62:63], 0\n"
"s_cbranch_scc1 AT_tok\n"
"s_sub_u32 s87, s87, 1\n"
"s_cmp_eq_u32 s87, 0\n"
"s_cbranch_scc1 AT_tok\n"
"s_sleep 2\n"
"buffer_load_dwordx4 v[26:29], v246, s[28:31], s56 offen offset:0 sc1\n"
"buffer_load_dwordx4 v[30:33], v246, s[28:31], s56 offen offset:16 sc1\n"
"buffer_load_dwordx4 v[34:37], v246, s[28:31], s57 offen offset:0 sc1\n"
"buffer_load_dwordx4 v[38:41], v246, s[28:31], s57 offen offset:16 sc1\n"
"buffer_load_dwordx4 v[42:45], v246, s[28:31], s58 offen offset:0 sc1\n"
"buffer_load_dwordx4 v[46:49], v246, s[28:31], s58 offen offset:16 sc1\n"
"buffer_load_dwordx4 v[50:53], v246, s[28:31], s59 offen offset:0 sc1\n"
"buffer_load_dwordx4 v[54:57], v246, s[28:31], s59 offen offset:16 sc1\n"
"s_waitcnt vmcnt(0)\n"
"s_branch AT_tchk\n"
"AT_tok:\n"
"s_waitcnt vmcnt(0)\n"
"v_cmp_ne_u32 vcc, s85, v249\n"
"s_nop 0\n"
"s_cbranch_vccnz AT_x1\n"
"AT_x2:\n"
"buffer_load_dwordx4 a[0:3], v207, s[28:31], s20 offen offset:0 sc1\n"
"buffer_load_dwordx4 a[4:7], v207, s[28:31], s20 offen offset:1024 sc1\n"
"buffer_load_dwordx4 a[8:11], v207, s[28:31], s21 offen offset:0 sc1\n"
"buffer_load_dwordx4 a[12:15], v207, s[28:31], s21 offen offset:1024 sc1\n"
"buffer_load_dwordx4 a[16:19], v207, s[28:31], s22 offen offset:0 sc1\n"
"buffer_load_dwordx4 a[20:23], v207, s[28:31], s22 offen offset:1024 sc1\n"
"buffer_load_dwordx4 a[24:27], v207, s[28:31], s20 offen offset:2048 sc1\n"
"buffer_load_dwordx4 a[28:31], v207, s[28:31], s20 offen offset:3072 sc1\n"
"s_add_u32 s20, s20, 4096\n"
"v_cmp_gt_u32 vcc, 144, v253\n"
"v_lshlrev_b32 v0, 16, v58\n"
"v_and_b32 v1, 0xffff0000, v58\n"
"v_lshlrev_b32 v2, 16, v59\n"
"v_and_b32 v3, 0xffff0000, v59\n"
"v_mov_b32 v4, v26\n"
"v_add_f32 v12, 1.0, v42\n"
"v_mov_b32 v16, v34\n"
"v_mov_b32 v20, v50\n"
"v_mul_f32 v8, v12, v0\n"
"v_mov_b32 v5, v28\n"
"v_add_f32 v13, 1.0, v44\n"
"v_mov_b32 v17, v36\n"
"v_mov_b32 v21, v52\n"
"v_mul_f32 v9, v13, v1\n"
"v_mov_b32 v6, v30\n"
"v_add_f32 v14, 1.0, v46\n"
"v_mov_b32 v18, v38\n"
"v_mov_b32 v22, v54\n"
"v_mul_f32 v10, v14, v2\n"
"v_mov_b32 v7, v32\n"
"v_add_f32 v15, 1.0, v48\n"
"v_mov_b32 v19, v40\n"
"v_mov_b32 v23, v56\n"
"v_mul_f32 v11, v15, v3\n"
"s_mov_b64 exec, vcc\n"
"ds_write_b128 v247, v[4:7] offset:0\n"
"ds_write_b128 v247, v[8:11] offset:2304\n"
"ds_write_b128 v247, v[12:15] offset:4608\n"
"ds_write_b128 v247, v[16:19] offset:6912\n"
"ds_write_b128 v247, v[20:23] offset:38912\n"
"s_mov_b64 exec, -1\n"
"v_add_f32 v24, v8, v9\n"
"v_mul_f32 v25, v16, v0\n"
"v_add_f32 v24, v24, v10\n"
"v_fmac_f32 v25, v17, v1\n"
"v_add_f32 v24, v24, v11\n"
"v_fmac_f32 v25, v18, v2\n"
"s_nop 0\n"
"v_fmac_f32 v25, v19, v3\n"
"s_nop 1\n"
"v_add_f32_dpp v24, v24, v24 row_ror:1 row_mask:0xf bank_mask:0xf\n"
"v_add_f32_dpp v25, v25, v25 row_ror:1 row_mask:0xf bank_mask:0xf\n"
"s_nop 0\n"
"v_add_f32_dpp v24, v24, v24 row_ror:2 row_mask:0xf bank_mask:0xf\n"
"v_add_f32_dpp v25, v25, v25 row_ror:2 row_mask:0xf bank_mask:0xf\n"
"s_nop 0\n"
"v_add_f32_dpp v24, v24, v24 row_ror:4 row_mask:0xf bank_mask:0xf\n"
"v_add_f32_dpp v25, v25, v25 row_ror:4 row_mask:0xf bank_mask:0xf\n"
"s_nop 0\n"
"v_add_f32_dpp v24, v24, v24 row_ror:8 row_mask:0xf bank_mask:0xf\n"
"v_add_f32_dpp v25, v25, v25 row_ror:8 row_mask:0xf bank_mask:0xf\n"
"s_nop 0\n"
"v_mov_b32 v26, v24\n"
"v_mov_b32 v27, v25\n"
"s_nop 0\n"
"v_permlane16_swap_b32 v24, v26\n"
"v_permlane16_swap_b32 v25, v27\n"
"v_add_f32 v24, v24, v26\n"
"v_add_f32 v25, v25, v27\n"
"v_mov_b32 v26, v24\n"
"v_mov_b32 v27, v25\n"
"s_nop 0\n"
"v_permlane32_swap_b32 v24, v26\n"
"v_permlane32_swap_b32 v25, v27\n"
"v_add_f32 v24, v24, v26\n"
"v_add_f32 v25, v25, v27\n"
"s_lshl_b32 s62, s41, 3\n"
"s_add_u32 s62, s62, s40\n"
"v_mov_b32 v26, s62\n"
"v_add_u32 v26, 161536, v26\n"
"s_mov_b64 exec, 1\n"
"ds_write_b64 v26, v[24:25]\n"
"s_mov_b64 exec, -1\n"
"s_mov_b64 exec, 0\n"
"s_cmp_eq_u32 s41, 0\n"
"s_cbranch_scc1 AT_hw1_0\n"
"s_cmp_eq_u32 s41, 1\n"
"s_cbranch_scc1 AT_hw1_1\n"
"s_cmp_eq_u32 s41, 2\n"
"s_cbranch_scc1 AT_hw1_2\n"
"s_call_b64 %[kp], AT_pgA0\n"
"s_branch AT_hx1\n"
"AT_hw1_0:\n"
"s_call_b64 %[kp], AT_pg14\n"
"s_branch AT_hx1\n"
"AT_hw1_1:\n"
"s_call_b64 %[kp], AT_pg15\n"
"s_branch AT_hx1\n"
"AT_hw1_2:\n"
"s_call_b64 %[kp], AT_pg16\n"
"AT_hx1:\n"
"s_mov_b64 exec, -1\n"
"s_waitcnt vmcnt(6)\n"
"v_mfma_f32_16x16x32_bf16 v[64:67], a[192:195], a[0:3], v[64:67]\n"
"ds_read_b64_tr_b16 a[136:137], v200 offset:55328\n"
"buffer_load_dwordx4 a[32:35], v207, s[28:31], s21 offen offset:2048 sc1\n"
"v_mfma_f32_16x16x32_bf16 v[68:71], a[192:195], a[4:7], v[68:71]\n"
"s_add_u32 m0, s53, 36864\n"
"ds_read_b64_tr_b16 a[138:139], v200 offset:59936\n"
"v_mfma_f32_16x16x32_bf16 v[72:75], a[196:199], a[0:3], v[72:75]\n"
"ds_read_b64_tr_b16 a[140:141], v200 offset:55360\n"
"buffer_load_dwordx4 v210, s[24:27], s37 offen offset:0 lds\n"
"v_mfma_f32_16x16x32_bf16 v[76:79], a[196:199], a[4:7], v[76:79]\n"
"s_waitcnt lgkmcnt(14)\n"
"ds_read_b64_tr_b16 a[142:143], v200 offset:59968\n"
"v_mfma_f32_16x16x32_bf16 v[80:83], a[200:203], a[0:3], v[80:83]\n"
"ds_read_b64_tr_b16 a[144:145], v200 offset:55392\n"
"v_mfma_f32_16x16x32_bf16 v[84:87], a[200:203], a[4:7], v[84:87]\n"
"ds_read_b64_tr_b16 a[146:147], v200 offset:60000\n"
"buffer_load_dwordx4 a[36:39], v207, s[28:31], s21 offen offset:3072 sc1\n"
"v_mfma_f32_16x16x32_bf16 v[88:91], a[204:207], a[0:3], v[88:91]\n"
"s_add_u32 s21, s21, 4096\n"
"ds_read_b64_tr_b16 a[148:149], v200 offset:55424\n"
"v_mfma_f32_16x16x32_bf16 v[92:95], a[204:207], a[4:7], v[92:95]\n"
"ds_read_b64_tr_b16 a[150:151], v200 offset:60032\n"
"v_mfma_f32_16x16x32_bf16 v[96:99], a[208:211], a[0:3], v[96:99]\n"
"s_add_u32 m0, s53, 36864\n"
"ds_read_b64_tr_b16 a[152:153], v200 offset:55456\n"
"v_mfma_f32_16x16x32_bf16 v[100:103], a[208:211], a[4:7], v[100:103]\n"
"ds_read_b64_tr_b16 a[154:155], v200 offset:60064\n"
"buffer_load_dwordx4 v211, s[24:27], s37 offen offset:1024 lds\n"
"v_mfma_f32_16x16x32_bf16 v[104:107], a[212:215], a[0:3], v[104:107]\n"
"ds_read_b64_tr_b16 a[156:157], v200 offset:55488\n"
"buffer_load_dwordx4 a[40:43], v207, s[28:31], s22 offen offset:2048 sc1\n"
"v_mfma_f32_16x16x32_bf16 v[108:111], a[212:215], a[4:7], v[108:111]\n"
"ds_read_b64_tr_b16 a[158:159], v200 offset:60096\n"
"v_mfma_f32_16x16x32_bf16 v[112:115], a[216:219], a[0:3], v[112:115]\n"
"ds_read_b64_tr_b16 a[192:193], v200 offset:55520\n"
"v_mfma_f32_16x16x32_bf16 v[116:119], a[216:219], a[4:7], v[116:119]\n"
"s_waitcnt lgkmcnt(11)\n"
"ds_read_b64_tr_b16 a[194:195], v200 offset:60128\n"
"v_mfma_f32_16x16x32_bf16 v[120:123], a[220:223], a[0:3], v[120:123]\n"
"ds_read_b64_tr_b16 a[196:197], v203 offset:55296\n"
"v_mfma_f32_16x16x32_bf16 v[124:127], a[220:223], a[4:7], v[124:127]\n"
"s_add_u32 m0, s53, 36864\n"
"ds_read_b64_tr_b16 a[198:199], v203 offset:59904\n"
"buffer_load_dwordx4 a[44:47], v207, s[28:31], s22 offen offset:3072 sc1\n"
"v_mfma_f32_16x16x32_bf16 v[226:229], a[128:131], a[0:3], v[226:229]\n"
"s_add_u32 s22, s22, 4096\n"
"ds_read_b64_tr_b16 a[200:201], v200 offset:64512\n"
"buffer_load_dwordx4 v212, s[24:27], s37 offen offset:2048 lds\n"
"v_mfma_f32_16x16x32_bf16 v[230:233], a[128:131], a[4:7], v[230:233]\n"
"s_waitcnt vmcnt(11)\n"
"ds_read_b64_tr_b16 a[202:203], v201 offset:3584\n"
"v_mfma_f32_16x16x32_bf16 v[64:67], a[132:135], a[8:11], v[64:67]\n"
"ds_read_b64_tr_b16 a[204:205], v200 offset:64544\n"
"v_mfma_f32_16x16x32_bf16 v[68:71], a[132:135], a[12:15], v[68:71]\n"
"ds_read_b64_tr_b16 a[206:207], v201 offset:3616\n"
"v_mfma_f32_16x16x32_bf16 v[72:75], a[136:139], a[8:11], v[72:75]\n"
"ds_read_b64_tr_b16 a[208:209], v200 offset:64576\n"
"buffer_load_dwordx4 a[48:51], v207, s[28:31], s20 offen offset:0 sc1\n"
"v_mfma_f32_16x16x32_bf16 v[76:79], a[136:139], a[12:15], v[76:79]\n"
"s_waitcnt lgkmcnt(11)\n"
"ds_read_b64_tr_b16 a[210:211], v201 offset:3648\n"
"v_mfma_f32_16x16x32_bf16 v[80:83], a[140:143], a[8:11], v[80:83]\n"
"s_add_u32 m0, s53, 36864\n"
"ds_read_b64_tr_b16 a[212:213], v200 offset:64608\n"
"v_mfma_f32_16x16x32_bf16 v[84:87], a[140:143], a[12:15], v[84:87]\n"
"ds_read_b64_tr_b16 a[214:215], v201 offset:3680\n"
"buffer_load_dwordx4 v213, s[24:27], s37 offen offset:3072 lds\n"
"v_mfma_f32_16x16x32_bf16 v[88:91], a[144:147], a[8:11], v[88:91]\n"
"ds_read_b64_tr_b16 a[216:217], v200 offset:64640\n"
"v_mfma_f32_16x16x32_bf16 v[92:95], a[144:147], a[12:15], v[92:95]\n"
"ds_read_b64_tr_b16 a[218:219], v201 offset:3712\n"
"buffer_load_dwordx4 a[52:55], v207, s[28:31], s20 offen offset:1024 sc1\n"
"v_mfma_f32_16x16x32_bf16 v[96:99], a[148:151], a[8:11], v[96:99]\n"
"ds_read_b64_tr_b16 a[220:221], v200 offset:64672\n"
"v_mfma_f32_16x16x32_bf16 v[100:103], a[148:151], a[12:15], v[100:103]\n"
"ds_read_b64_tr_b16 a[222:223], v201 offset:3744\n"
"v_mfma_f32_16x16x32_bf16 v[104:107], a[152:155], a[8:11], v[104:107]\n"
"ds_read_b64_tr_b16 a[128:129], v200 offset:64704\n"
"v_mfma_f32_16x16x32_bf16 v[108:111], a[152:155], a[12:15], v[108:111]\n"
"s_waitcnt lgkmcnt(11)\n"
"ds_read_b64_tr_b16 a[130:131], v201 offset:3776\n"
"v_mfma_f32_16x16x32_bf16 v[112:115], a[156:159], a[8:11], v[112:115]\n"
"s_add_u32 m0, s53, 40960\n"
"ds_read_b64_tr_b16 a[132:133], v200 offset:64736\n"
"buffer_load_dwordx4 a[56:59], v207, s[28:31], s21 offen offset:0 sc1\n"
"v_mfma_f32_16x16x32_bf16 v[116:119], a[156:159], a[12:15], v[116:119]\n"
"ds_read_b64_tr_b16 a[134:135], v201 offset:3808\n"
"buffer_load_dwordx4 v214, s[24:27], s37 offen offset:0 lds\n"
"v_mfma_f32_16x16x32_bf16 v[120:123], a[192:195], a[8:11], v[120:123]\n"
"ds_read_b64_tr_b16 a[136:137], v203 offset:64512\n"
"v_mfma_f32_16x16x32_bf16 v[124:127], a[192:195], a[12:15], v[124:127]\n"
"ds_read_b64_tr_b16 a[138:139], v204 offset:3584\n"
"v_mfma_f32_16x16x32_bf16 v[226:229], a[196:199], a[8:11], v[226:229]\n"
"ds_read_b64_tr_b16 a[140:141], v201 offset:8192\n"
"v_mfma_f32_16x16x32_bf16 v[230:233], a[196:199], a[12:15], v[230:233]\n"
"s_waitcnt vmcnt(14)\n"
"ds_read_b64_tr_b16 a[142:143], v201 offset:12800\n"
"v_mfma_f32_16x16x32_bf16 v[64:67], a[200:203], a[16:19], v[64:67]\n"
"ds_read_b64_tr_b16 a[144:145], v201 offset:8224\n"
"v_mfma_f32_16x16x32_bf16 v[68:71], a[200:203], a[20:23], v[68:71]\n"
"s_waitcnt lgkmcnt(11)\n"
"s_add_u32 m0, s53, 40960\n"
"ds_read_b64_tr_b16 a[146:147], v201 offset:12832\n"
"buffer_load_dwordx4 a[60:63], v207, s[28:31], s21 offen offset:1024 sc1\n"
"v_mfma_f32_16x16x32_bf16 v[72:75], a[204:207], a[16:19], v[72:75]\n"
"ds_read_b64_tr_b16 a[148:149], v201 offset:8256\n"
"buffer_load_dwordx4 v236, s[24:27], s37 offen offset:1024 lds\n"
"v_mfma_f32_16x16x32_bf16 v[76:79], a[204:207], a[20:23], v[76:79]\n"
"ds_read_b64_tr_b16 a[150:151], v201 offset:12864\n"
"v_mfma_f32_16x16x32_bf16 v[80:83], a[208:211], a[16:19], v[80:83]\n"
"ds_read_b64_tr_b16 a[152:153], v201 offset:8288\n"
"v_mfma_f32_16x16x32_bf16 v[84:87], a[208:211], a[20:23], v[84:87]\n"
"ds_read_b64_tr_b16 a[154:155], v201 offset:12896\n"
"v_mfma_f32_16x16x32_bf16 v[88:91], a[212:215], a[16:19], v[88:91]\n"
"ds_read_b64_tr_b16 a[156:157], v201 offset:8320\n"
"v_mfma_f32_16x16x32_bf16 v[92:95], a[212:215], a[20:23], v[92:95]\n"
"ds_read_b64_tr_b16 a[158:159], v201 offset:12928\n"
"v_mfma_f32_16x16x32_bf16 v[96:99], a[216:219], a[16:19], v[96:99]\n"
"s_add_u32 m0, s53, 40960\n"
"ds_read_b64_tr_b16 a[192:193], v201 offset:8352\n"
"buffer_load_dwordx4 a[64:67], v207, s[28:31], s22 offen offset:0 sc1\n"
"v_mfma_f32_16x16x32_bf16 v[100:103], a[216:219], a[20:23], v[100:103]\n"
"s_waitcnt lgkmcnt(11)\n"
"ds_read_b64_tr_b16 a[194:195], v201 offset:12960\n"
"buffer_load_dwordx4 v237, s[24:27], s37 offen offset:2048 lds\n"
"v_mfma_f32_16x16x32_bf16 v[104:107], a[220:223], a[16:19], v[104:107]\n"
"ds_read_b64_tr_b16 a[196:197], v201 offset:8384\n"
"v_mfma_f32_16x16x32_bf16 v[108:111], a[220:223], a[20:23], v[108:111]\n"
"ds_read_b64_tr_b16 a[198:199], v201 offset:12992\n"
"v_mfma_f32_16x16x32_bf16 v[112:115], a[128:131], a[16:19], v[112:115]\n"
"ds_read_b64_tr_b16 a[200:201], v201 offset:8416\n"
"v_mfma_f32_16x16x32_bf16 v[116:119], a[128:131], a[20:23], v[116:119]\n"
"ds_read_b64_tr_b16 a[202:203], v201 offset:13024\n"
"v_mfma_f32_16x16x32_bf16 v[120:123], a[132:135], a[16:19], v[120:123]\n"
"ds_read_b64_tr_b16 a[204:205], v204 offset:8192\n"
"v_mfma_f32_16x16x32_bf16 v[124:127], a[132:135], a[20:23], v[124:127]\n"
"s_add_u32 m0, s53, 40960\n"
"ds_read_b64_tr_b16 a[206:207], v204 offset:12800\n"
"buffer_load_dwordx4 a[68:71], v207, s[28:31], s22 offen offset:1024 sc1\n"
"v_mfma_f32_16x16x32_bf16 v[226:229], a[136:139], a[16:19], v[226:229]\n"
"ds_read_b64_tr_b16 a[208:209], v201 offset:17408\n"
"buffer_load_dwordx4 v238, s[24:27], s37 offen offset:3072 lds\n"
"v_mfma_f32_16x16x32_bf16 v[230:233], a[136:139], a[20:23], v[230:233]\n"
"s_waitcnt lgkmcnt(11)\n"
"s_waitcnt vmcnt(18)\n"
"ds_read_b64_tr_b16 a[210:211], v201 offset:22016\n"
"v_mfma_f32_16x16x32_bf16 v[64:67], a[140:143], a[24:27], v[64:67]\n"
"ds_read_b64_tr_b16 a[212:213], v201 offset:17440\n"
"v_mfma_f32_16x16x32_bf16 v[68:71], a[140:143], a[28:31], v[68:71]\n"
"ds_read_b64_tr_b16 a[214:215], v201 offset:22048\n"
"v_mfma_f32_16x16x32_bf16 v[72:75], a[144:147], a[24:27], v[72:75]\n"
"ds_read_b64_tr_b16 a[216:217], v201 offset:17472\n"
"v_mfma_f32_16x16x32_bf16 v[76:79], a[144:147], a[28:31], v[76:79]\n"
"ds_read_b64_tr_b16 a[218:219], v201 offset:22080\n"
"v_mfma_f32_16x16x32_bf16 v[80:83], a[148:151], a[24:27], v[80:83]\n"
"s_add_u32 m0, s53, 45056\n"
"ds_read_b64_tr_b16 a[220:221], v201 offset:17504\n"
"buffer_load_dwordx4 a[72:75], v207, s[28:31], s20 offen offset:2048 sc1\n"
"v_mfma_f32_16x16x32_bf16 v[84:87], a[148:151], a[28:31], v[84:87]\n"
"AT_pg14:\n"
"s_cbranch_execz AT_hr\n"
"ds_read_b64_tr_b16 a[222:223], v201 offset:22112\n"
"buffer_load_dwordx4 v239, s[24:27], s37 offen offset:0 lds\n"
"v_mfma_f32_16x16x32_bf16 v[88:91], a[152:155], a[24:27], v[88:91]\n"
"ds_read_b64_tr_b16 a[128:129], v201 offset:17536\n"
"v_mfma_f32_16x16x32_bf16 v[92:95], a[152:155], a[28:31], v[92:95]\n"
"s_waitcnt lgkmcnt(11)\n"
"ds_read_b64_tr_b16 a[130:131], v201 offset:22144\n"
"v_mfma_f32_16x16x32_bf16 v[96:99], a[156:159], a[24:27], v[96:99]\n"
"ds_read_b64_tr_b16 a[132:133], v201 offset:17568\n"
"v_mfma_f32_16x16x32_bf16 v[100:103], a[156:159], a[28:31], v[100:103]\n"
"ds_read_b64_tr_b16 a[134:135], v201 offset:22176\n"
"v_mfma_f32_16x16x32_bf16 v[104:107], a[192:195], a[24:27], v[104:107]\n"
"ds_read_b64_tr_b16 a[136:137], v201 offset:17600\n"
"v_mfma_f32_16x16x32_bf16 v[108:111], a[192:195], a[28:31], v[108:111]\n"
"ds_read_b64_tr_b16 a[138:139], v201 offset:22208\n"
"buffer_load_dwordx4 a[76:79], v207, s[28:31], s20 offen offset:3072 sc1\n"
"v_mfma_f32_16x16x32_bf16 v[112:115], a[196:199], a[24:27], v[112:115]\n"
"s_add_u32 s20, s20, 4096\n"
"ds_read_b64_tr_b16 a[140:141], v201 offset:17632\n"
"v_mfma_f32_16x16x32_bf16 v[116:119], a[196:199], a[28:31], v[116:119]\n"
"ds_read_b64_tr_b16 a[142:143], v201 offset:22240\n"
"v_mfma_f32_16x16x32_bf16 v[120:123], a[200:203], a[24:27], v[120:123]\n"
"ds_read_b64_tr_b16 a[144:145], v204 offset:17408\n"
"v_mfma_f32_16x16x32_bf16 v[124:127], a[200:203], a[28:31], v[124:127]\n"
"s_waitcnt lgkmcnt(11)\n"
"ds_read_b64_tr_b16 a[146:147], v204 offset:22016\n"
"v_mfma_f32_16x16x32_bf16 v[226:229], a[204:207], a[24:27], v[226:229]\n"
"ds_read_b64_tr_b16 a[148:149], v201 offset:26624\n"
"v_mfma_f32_16x16x32_bf16 v[230:233], a[204:207], a[28:31], v[230:233]\n"
"s_waitcnt vmcnt(18)\n"
"ds_read_b64_tr_b16 a[150:151], v201 offset:31232\n"
"v_mfma_f32_16x16x32_bf16 v[64:67], a[208:211], a[32:35], v[64:67]\n"
"ds_read_b64_tr_b16 a[152:153], v201 offset:26656\n"
"buffer_load_dwordx4 a[80:83], v207, s[28:31], s21 offen offset:2048 sc1\n"
"v_mfma_f32_16x16x32_bf16 v[68:71], a[208:211], a[36:39], v[68:71]\n"
"s_waitcnt vmcnt(31)\n"
"ds_read_b64_tr_b16 a[154:155], v201 offset:31264\n"
"v_mfma_f32_16x16x32_bf16 v[72:75], a[212:215], a[32:35], v[72:75]\n"
"s_barrier\n"
"ds_read_b64_tr_b16 a[156:157], v201 offset:26688\n"
"v_mfma_f32_16x16x32_bf16 v[76:79], a[212:215], a[36:39], v[76:79]\n"
"ds_read_b64_tr_b16 a[158:159], v201 offset:31296\n"
"v_mfma_f32_16x16x32_bf16 v[80:83], a[216:219], a[32:35], v[80:83]\n"
"s_add_u32 m0, s53, 73728\n"
"ds_read_b64_tr_b16 a[192:193], v201 offset:26720\n"
"v_mfma_f32_16x16x32_bf16 v[84:87], a[216:219], a[36:39], v[84:87]\n"
"s_waitcnt lgkmcnt(11)\n"
"ds_read_b64_tr_b16 a[194:195], v201 offset:31328\n"
"buffer_load_dwordx4 v210, s[24:27], s38 offen offset:0 lds\n"
"v_mfma_f32_16x16x32_bf16 v[88:91], a[220:223], a[32:35], v[88:91]\n"
"ds_read_b64_tr_b16 a[196:197], v201 offset:26752\n"
"v_mfma_f32_16x16x32_bf16 v[92:95], a[220:223], a[36:39], v[92:95]\n"
"ds_read_b64_tr_b16 a[198:199], v201 offset:31360\n"
"buffer_load_dwordx4 a[84:87], v207, s[28:31], s21 offen offset:3072 sc1\n"
"v_mfma_f32_16x16x32_bf16 v[96:99], a[128:131], a[32:35], v[96:99]\n"
"s_add_u32 s21, s21, 4096\n"
"ds_read_b64_tr_b16 a[200:201], v201 offset:26784\n"
"v_mfma_f32_16x16x32_bf16 v[100:103], a[128:131], a[36:39], v[100:103]\n"
"ds_read_b64_tr_b16 a[202:203], v201 offset:31392\n"
"v_mfma_f32_16x16x32_bf16 v[104:107], a[132:135], a[32:35], v[104:107]\n"
"ds_read_b64_tr_b16 a[204:205], v201 offset:26816\n"
"v_mfma_f32_16x16x32_bf16 v[108:111], a[132:135], a[36:39], v[108:111]\n"
"s_add_u32 m0, s53, 73728\n"
"ds_read_b64_tr_b16 a[206:207], v201 offset:31424\n"
"v_mfma_f32_16x16x32_bf16 v[112:115], a[136:139], a[32:35], v[112:115]\n"
"ds_read_b64_tr_b16 a[208:209], v201 offset:26848\n"
"buffer_load_dwordx4 v211, s[24:27], s38 offen offset:1024 lds\n"
"v_mfma_f32_16x16x32_bf16 v[116:119], a[136:139], a[36:39], v[116:119]\n"
"s_waitcnt lgkmcnt(11)\n"
"ds_read_b64_tr_b16 a[210:211], v201 offset:31456\n"
"v_mfma_f32_16x16x32_bf16 v[120:123], a[140:143], a[32:35], v[120:123]\n"
"ds_read_b64_tr_b16 a[212:213], v204 offset:26624\n"
"buffer_load_dwordx4 a[88:91], v207, s[28:31], s22 offen offset:2048 sc1\n"
"v_mfma_f32_16x16x32_bf16 v[124:127], a[140:143], a[36:39], v[124:127]\n"
"ds_read_b64_tr_b16 a[214:215], v204 offset:31232\n"
"v_mfma_f32_16x16x32_bf16 v[226:229], a[144:147], a[32:35], v[226:229]\n"
"ds_read_b64_tr_b16 a[216:217], v201 offset:35840\n"
"v_mfma_f32_16x16x32_bf16 v[230:233], a[144:147], a[36:39], v[230:233]\n"
"s_waitcnt vmcnt(20)\n"
"ds_read_b64_tr_b16 a[218:219], v201 offset:40448\n"
"v_mfma_f32_16x16x32_bf16 v[64:67], a[148:151], a[40:43], v[64:67]\n"
"s_add_u32 m0, s53, 73728\n"
"ds_read_b64_tr_b16 a[220:221], v201 offset:35872\n"
"v_mfma_f32_16x16x32_bf16 v[68:71], a[148:151], a[44:47], v[68:71]\n"
"ds_read_b64_tr_b16 a[222:223], v201 offset:40480\n"
"buffer_load_dwordx4 v212, s[24:27], s38 offen offset:2048 lds\n"
"v_mfma_f32_16x16x32_bf16 v[72:75], a[152:155], a[40:43], v[72:75]\n"
"ds_read_b64_tr_b16 a[128:129], v201 offset:35904\n"
"v_mfma_f32_16x16x32_bf16 v[76:79], a[152:155], a[44:47], v[76:79]\n"
"s_waitcnt lgkmcnt(11)\n"
"ds_read_b64_tr_b16 a[130:131], v201 offset:40512\n"
"buffer_load_dwordx4 a[92:95], v207, s[28:31], s22 offen offset:3072 sc1\n"
"v_mfma_f32_16x16x32_bf16 v[80:83], a[156:159], a[40:43], v[80:83]\n"
"s_add_u32 s22, s22, 4096\n"
"ds_read_b64_tr_b16 a[132:133], v201 offset:35936\n"
"v_mfma_f32_16x16x32_bf16 v[84:87], a[156:159], a[44:47], v[84:87]\n"
"ds_read_b64_tr_b16 a[134:135], v201 offset:40544\n"
"v_mfma_f32_16x16x32_bf16 v[88:91], a[192:195], a[40:43], v[88:91]\n"
"ds_read_b64_tr_b16 a[136:137], v201 offset:35968\n"
"v_mfma_f32_16x16x32_bf16 v[92:95], a[192:195], a[44:47], v[92:95]\n"
"s_add_u32 m0, s53, 73728\n"
"ds_read_b64_tr_b16 a[138:139], v201 offset:40576\n"
"v_mfma_f32_16x16x32_bf16 v[96:99], a[196:199], a[40:43], v[96:99]\n"
"ds_read_b64_tr_b16 a[140:141], v201 offset:36000\n"
"buffer_load_dwordx4 v213, s[24:27], s38 offen offset:3072 lds\n"
"v_mfma_f32_16x16x32_bf16 v[100:103], a[196:199], a[44:47], v[100:103]\n"
"ds_read_b64_tr_b16 a[142:143], v201 offset:40608\n"
"v_mfma_f32_16x16x32_bf16 v[104:107], a[200:203], a[40:43], v[104:107]\n"
"ds_read_b64_tr_b16 a[144:145], v201 offset:36032\n"
"buffer_load_dwordx4 a[96:99], v207, s[28:31], s20 offen offset:0 sc1\n"
"v_mfma_f32_16x16x32_bf16 v[108:111], a[200:203], a[44:47], v[108:111]\n"
"s_waitcnt lgkmcnt(11)\n"
"ds_read_b64_tr_b16 a[146:147], v201 offset:40640\n"
"v_mfma_f32_16x16x32_bf16 v[112:115], a[204:207], a[40:43], v[112:115]\n"
"ds_read_b64_tr_b16 a[148:149], v201 offset:36064\n"
"v_mfma_f32_16x16x32_bf16 v[116:119], a[204:207], a[44:47], v[116:119]\n"
"ds_read_b64_tr_b16 a[150:151], v201 offset:40672\n"
"v_mfma_f32_16x16x32_bf16 v[120:123], a[208:211], a[40:43], v[120:123]\n"
"s_add_u32 m0, s53, 77824\n"
"ds_read_b64_tr_b16 a[152:153], v204 offset:35840\n"
"v_mfma_f32_16x16x32_bf16 v[124:127], a[208:211], a[44:47], v[124:127]\n"
"ds_read_b64_tr_b16 a[154:155], v204 offset:40448\n"
"buffer_load_dwordx4 v214, s[24:27], s38 offen offset:0 lds\n"
"v_mfma_f32_16x16x32_bf16 v[226:229], a[212:215], a[40:43], v[226:229]\n"
"ds_read_b64_tr_b16 a[156:157], v201 offset:45056\n"
"v_mfma_f32_16x16x32_bf16 v[230:233], a[212:215], a[44:47], v[230:233]\n"
"s_waitcnt vmcnt(21)\n"
"ds_read_b64_tr_b16 a[158:159], v201 offset:49664\n"
"buffer_load_dwordx4 a[100:103], v207, s[28:31], s20 offen offset:1024 sc1\n"
"v_mfma_f32_16x16x32_bf16 v[64:67], a[216:219], a[48:51], v[64:67]\n"
"ds_read_b64_tr_b16 a[192:193], v201 offset:45088\n"
"v_mfma_f32_16x16x32_bf16 v[68:71], a[216:219], a[52:55], v[68:71]\n"
"s_waitcnt lgkmcnt(11)\n"
"ds_read_b64_tr_b16 a[194:195], v201 offset:49696\n"
"v_mfma_f32_16x16x32_bf16 v[72:75], a[220:223], a[48:51], v[72:75]\n"
"ds_read_b64_tr_b16 a[196:197], v201 offset:45120\n"
"v_mfma_f32_16x16x32_bf16 v[76:79], a[220:223], a[52:55], v[76:79]\n"
"s_add_u32 m0, s53, 77824\n"
"ds_read_b64_tr_b16 a[198:199], v201 offset:49728\n"
"v_mfma_f32_16x16x32_bf16 v[80:83], a[128:131], a[48:51], v[80:83]\n"
"ds_read_b64_tr_b16 a[200:201], v201 offset:45152\n"
"buffer_load_dwordx4 v236, s[24:27], s38 offen offset:1024 lds\n"
"v_mfma_f32_16x16x32_bf16 v[84:87], a[128:131], a[52:55], v[84:87]\n"
"ds_read_b64_tr_b16 a[202:203], v201 offset:49760\n"
"v_mfma_f32_16x16x32_bf16 v[88:91], a[132:135], a[48:51], v[88:91]\n"
"ds_read_b64_tr_b16 a[204:205], v201 offset:45184\n"
"buffer_load_dwordx4 a[104:107], v207, s[28:31], s21 offen offset:0 sc1\n"
"v_mfma_f32_16x16x32_bf16 v[92:95], a[132:135], a[52:55], v[92:95]\n"
"ds_read_b64_tr_b16 a[206:207], v201 offset:49792\n"
"v_mfma_f32_16x16x32_bf16 v[96:99], a[136:139], a[48:51], v[96:99]\n"
"ds_read_b64_tr_b16 a[208:209], v201 offset:45216\n"
"v_mfma_f32_16x16x32_bf16 v[100:103], a[136:139], a[52:55], v[100:103]\n"
"s_waitcnt lgkmcnt(11)\n"
"ds_read_b64_tr_b16 a[210:211], v201 offset:49824\n"
"v_mfma_f32_16x16x32_bf16 v[104:107], a[140:143], a[48:51], v[104:107]\n"
"s_add_u32 m0, s53, 77824\n"
"ds_read_b64_tr_b16 a[212:213], v201 offset:45248\n"
"v_mfma_f32_16x16x32_bf16 v[108:111], a[140:143], a[52:55], v[108:111]\n"
"ds_read_b64_tr_b16 a[214:215], v201 offset:49856\n"
"buffer_load_dwordx4 v237, s[24:27], s38 offen offset:2048 lds\n"
"v_mfma_f32_16x16x32_bf16 v[112:115], a[144:147], a[48:51], v[112:115]\n"
"ds_read_b64_tr_b16 a[216:217], v201 offset:45280\n"
"v_mfma_f32_16x16x32_bf16 v[116:119], a[144:147], a[52:55], v[116:119]\n"
"ds_read_b64_tr_b16 a[218:219], v201 offset:49888\n"
"buffer_load_dwordx4 a[108:111], v207, s[28:31], s21 offen offset:1024 sc1\n"
"v_mfma_f32_16x16x32_bf16 v[120:123], a[148:151], a[48:51], v[120:123]\n"
"ds_read_b64_tr_b16 a[220:221], v204 offset:45056\n"
"v_mfma_f32_16x16x32_bf16 v[124:127], a[148:151], a[52:55], v[124:127]\n"
"ds_read_b64_tr_b16 a[222:223], v204 offset:49664\n"
"v_mfma_f32_16x16x32_bf16 v[226:229], a[152:155], a[48:51], v[226:229]\n"
"ds_read_b64_tr_b16 a[128:129], v202 offset:0\n"
"v_mfma_f32_16x16x32_bf16 v[230:233], a[152:155], a[52:55], v[230:233]\n"
"s_waitcnt lgkmcnt(11)\n"
"s_waitcnt vmcnt(23)\n"
"ds_read_b64_tr_b16 a[130:131], v202 offset:4608\n"
"v_mfma_f32_16x16x32_bf16 v[64:67], a[156:159], a[56:59], v[64:67]\n"
"s_add_u32 m0, s53, 77824\n"
"ds_read_b64_tr_b16 a[132:133], v202 offset:32\n"
"v_mfma_f32_16x16x32_bf16 v[68:71], a[156:159], a[60:63], v[68:71]\n"
"ds_read_b64_tr_b16 a[134:135], v202 offset:4640\n"
"buffer_load_dwordx4 v238, s[24:27], s38 offen offset:3072 lds\n"
"v_mfma_f32_16x16x32_bf16 v[72:75], a[192:195], a[56:59], v[72:75]\n"
"ds_read_b64_tr_b16 a[136:137], v202 offset:64\n"
"buffer_load_dwordx4 a[112:115], v207, s[28:31], s22 offen offset:0 sc1\n"
"v_mfma_f32_16x16x32_bf16 v[76:79], a[192:195], a[60:63], v[76:79]\n"
"ds_read_b64_tr_b16 a[138:139], v202 offset:4672\n"
"v_accvgpr_write_b32 a48, v128\n"
"v_mfma_f32_16x16x32_bf16 v[80:83], a[196:199], a[56:59], v[80:83]\n"
"ds_read_b64_tr_b16 a[140:141], v202 offset:96\n"
"v_accvgpr_write_b32 a49, v129\n"
"v_mfma_f32_16x16x32_bf16 v[84:87], a[196:199], a[60:63], v[84:87]\n"
"ds_read_b64_tr_b16 a[142:143], v202 offset:4704\n"
"v_accvgpr_write_b32 a50, v130\n"
"v_mfma_f32_16x16x32_bf16 v[88:91], a[200:203], a[56:59], v[88:91]\n"
"ds_read_b64_tr_b16 a[144:145], v202 offset:128\n"
"v_accvgpr_write_b32 a51, v131\n"
"v_mfma_f32_16x16x32_bf16 v[92:95], a[200:203], a[60:63], v[92:95]\n"
"s_waitcnt lgkmcnt(11)\n"
"s_add_u32 m0, s53, 81920\n"
"ds_read_b64_tr_b16 a[146:147], v202 offset:4736\n"
"v_accvgpr_write_b32 a52, v132\n"
"v_mfma_f32_16x16x32_bf16 v[96:99], a[204:207], a[56:59], v[96:99]\n"
"ds_read_b64_tr_b16 a[148:149], v202 offset:160\n"
"buffer_load_dwordx4 v239, s[24:27], s38 offen offset:0 lds\n"
"v_mfma_f32_16x16x32_bf16 v[100:103], a[204:207], a[60:63], v[100:103]\n"
"ds_read_b64_tr_b16 a[150:151], v202 offset:4768\n"
"buffer_load_dwordx4 a[116:119], v207, s[28:31], s22 offen offset:1024 sc1\n"
"v_mfma_f32_16x16x32_bf16 v[104:107], a[208:211], a[56:59], v[104:107]\n"
"ds_read_b64_tr_b16 a[152:153], v202 offset:192\n"
"v_accvgpr_write_b32 a53, v133\n"
"v_mfma_f32_16x16x32_bf16 v[108:111], a[208:211], a[60:63], v[108:111]\n"
"ds_read_b64_tr_b16 a[154:155], v202 offset:4800\n"
"v_accvgpr_write_b32 a54, v134\n"
"v_mfma_f32_16x16x32_bf16 v[112:115], a[212:215], a[56:59], v[112:115]\n"
"ds_read_b64_tr_b16 a[156:157], v202 offset:224\n"
"v_accvgpr_write_b32 a55, v135\n"
"v_mfma_f32_16x16x32_bf16 v[116:119], a[212:215], a[60:63], v[116:119]\n"
"ds_read_b64_tr_b16 a[158:159], v202 offset:4832\n"
"v_mfma_f32_16x16x32_bf16 v[120:123], a[216:219], a[56:59], v[120:123]\n"
"ds_read_b64_tr_b16 a[192:193], v205 offset:0\n"
"v_mfma_f32_16x16x32_bf16 v[124:127], a[216:219], a[60:63], v[124:127]\n"
"s_waitcnt lgkmcnt(11)\n"
"ds_read_b64_tr_b16 a[194:195], v205 offset:4608\n"
"v_mfma_f32_16x16x32_bf16 v[226:229], a[220:223], a[56:59], v[226:229]\n"
"ds_read_b64_tr_b16 a[196:197], v202 offset:9216\n"
"buffer_load_dwordx4 a[120:123], v207, s[28:31], s20 offen offset:2048 sc1\n"
"v_mfma_f32_16x16x32_bf16 v[230:233], a[220:223], a[60:63], v[230:233]\n"
"s_waitcnt vmcnt(24)\n"
"ds_read_b64_tr_b16 a[198:199], v202 offset:13824\n"
"v_mfma_f32_16x16x32_bf16 v[64:67], a[128:131], a[64:67], v[64:67]\n"
"ds_read_b64_tr_b16 a[200:201], v202 offset:9248\n"
"v_mfma_f32_16x16x32_bf16 v[68:71], a[128:131], a[68:71], v[68:71]\n"
"s_waitcnt vmcnt(21)\n"
"ds_read_b64_tr_b16 a[202:203], v202 offset:13856\n"
"v_accvgpr_write_b32 a56, v136\n"
"v_mfma_f32_16x16x32_bf16 v[72:75], a[132:135], a[64:67], v[72:75]\n"
"s_barrier\n"
"ds_read_b64_tr_b16 a[204:205], v202 offset:9280\n"
"v_accvgpr_write_b32 a57, v137\n"
"v_mfma_f32_16x16x32_bf16 v[76:79], a[132:135], a[68:71], v[76:79]\n"
"ds_read_b64_tr_b16 a[206:207], v202 offset:13888\n"
"v_accvgpr_write_b32 a58, v138\n"
"v_mfma_f32_16x16x32_bf16 v[80:83], a[136:139], a[64:67], v[80:83]\n"
"ds_read_b64_tr_b16 a[208:209], v202 offset:9312\n"
"v_accvgpr_write_b32 a59, v139\n"
"v_mfma_f32_16x16x32_bf16 v[84:87], a[136:139], a[68:71], v[84:87]\n"
"s_waitcnt lgkmcnt(11)\n"
"s_add_u32 m0, s53, 110592\n"
"ds_read_b64_tr_b16 a[210:211], v202 offset:13920\n"
"buffer_load_dwordx4 a[124:127], v207, s[28:31], s20 offen offset:3072 sc1\n"
"v_mfma_f32_16x16x32_bf16 v[88:91], a[140:143], a[64:67], v[88:91]\n"
"s_add_u32 s20, s20, 4096\n"
"ds_read_b64_tr_b16 a[212:213], v202 offset:9344\n"
"buffer_load_dwordx4 v210, s[24:27], s39 offen offset:0 lds\n"
"v_mfma_f32_16x16x32_bf16 v[92:95], a[140:143], a[68:71], v[92:95]\n"
"ds_read_b64_tr_b16 a[214:215], v202 offset:13952\n"
"v_accvgpr_write_b32 a60, v140\n"
"v_mfma_f32_16x16x32_bf16 v[96:99], a[144:147], a[64:67], v[96:99]\n"
"ds_read_b64_tr_b16 a[216:217], v202 offset:9376\n"
"v_accvgpr_write_b32 a61, v141\n"
"v_mfma_f32_16x16x32_bf16 v[100:103], a[144:147], a[68:71], v[100:103]\n"
"ds_read_b64_tr_b16 a[218:219], v202 offset:13984\n"
"v_accvgpr_write_b32 a62, v142\n"
"v_mfma_f32_16x16x32_bf16 v[104:107], a[148:151], a[64:67], v[104:107]\n"
"ds_read_b64_tr_b16 a[220:221], v202 offset:9408\n"
"v_accvgpr_write_b32 a63, v143\n"
"v_mfma_f32_16x16x32_bf16 v[108:111], a[148:151], a[68:71], v[108:111]\n"
"ds_read_b64_tr_b16 a[222:223], v202 offset:14016\n"
"v_mfma_f32_16x16x32_bf16 v[112:115], a[152:155], a[64:67], v[112:115]\n"
"s_add_u32 m0, s53, 110592\n"
"ds_read_b64_tr_b16 a[128:129], v202 offset:9440\n"
"buffer_load_dwordx4 v[0:3], v207, s[28:31], s21 offen offset:2048 sc1\n"
"v_mfma_f32_16x16x32_bf16 v[116:119], a[152:155], a[68:71], v[116:119]\n"
"s_waitcnt lgkmcnt(11)\n"
"ds_read_b64_tr_b16 a[130:131], v202 offset:14048\n"
"buffer_load_dwordx4 v211, s[24:27], s39 offen offset:1024 lds\n"
"v_mfma_f32_16x16x32_bf16 v[120:123], a[156:159], a[64:67], v[120:123]\n"
"ds_read_b64_tr_b16 a[132:133], v205 offset:9216\n"
"v_mfma_f32_16x16x32_bf16 v[124:127], a[156:159], a[68:71], v[124:127]\n"
"ds_read_b64_tr_b16 a[134:135], v205 offset:13824\n"
"v_mfma_f32_16x16x32_bf16 v[226:229], a[192:195], a[64:67], v[226:229]\n"
"ds_read_b64_tr_b16 a[136:137], v202 offset:18432\n"
"v_mfma_f32_16x16x32_bf16 v[230:233], a[192:195], a[68:71], v[230:233]\n"
"s_waitcnt vmcnt(24)\n"
"ds_read_b64_tr_b16 a[138:139], v202 offset:23040\n"
"v_mfma_f32_16x16x32_bf16 v[64:67], a[196:199], a[72:75], v[64:67]\n"
"ds_read_b64_tr_b16 a[140:141], v202 offset:18464\n"
"v_mfma_f32_16x16x32_bf16 v[68:71], a[196:199], a[76:79], v[68:71]\n"
"s_add_u32 m0, s53, 110592\n"
"ds_read_b64_tr_b16 a[142:143], v202 offset:23072\n"
"buffer_load_dwordx4 v[4:7], v207, s[28:31], s21 offen offset:3072 sc1\n"
"v_mfma_f32_16x16x32_bf16 v[72:75], a[200:203], a[72:75], v[72:75]\n"
"s_add_u32 s21, s21, 4096\n"
"ds_read_b64_tr_b16 a[144:145], v202 offset:18496\n"
"buffer_load_dwordx4 v212, s[24:27], s39 offen offset:2048 lds\n"
"v_mfma_f32_16x16x32_bf16 v[76:79], a[200:203], a[76:79], v[76:79]\n"
"s_waitcnt lgkmcnt(11)\n"
"ds_read_b64_tr_b16 a[146:147], v202 offset:23104\n"
"v_accvgpr_write_b32 a64, v144\n"
"v_mfma_f32_16x16x32_bf16 v[80:83], a[204:207], a[72:75], v[80:83]\n"
"ds_read_b64_tr_b16 a[148:149], v202 offset:18528\n"
"v_accvgpr_write_b32 a65, v145\n"
"v_mfma_f32_16x16x32_bf16 v[84:87], a[204:207], a[76:79], v[84:87]\n"
"ds_read_b64_tr_b16 a[150:151], v202 offset:23136\n"
"v_accvgpr_write_b32 a66, v146\n"
"v_mfma_f32_16x16x32_bf16 v[88:91], a[208:211], a[72:75], v[88:91]\n"
"ds_read_b64_tr_b16 a[152:153], v202 offset:18560\n"
"v_accvgpr_write_b32 a67, v147\n"
"v_mfma_f32_16x16x32_bf16 v[92:95], a[208:211], a[76:79], v[92:95]\n"
"ds_read_b64_tr_b16 a[154:155], v202 offset:23168\n"
"v_accvgpr_write_b32 a68, v148\n"
"v_mfma_f32_16x16x32_bf16 v[96:99], a[212:215], a[72:75], v[96:99]\n"
"s_add_u32 m0, s53, 110592\n"
"ds_read_b64_tr_b16 a[156:157], v202 offset:18592\n"
"buffer_load_dwordx4 v[8:11], v207, s[28:31], s22 offen offset:2048 sc1\n"
"v_mfma_f32_16x16x32_bf16 v[100:103], a[212:215], a[76:79], v[100:103]\n"
"ds_read_b64_tr_b16 a[158:159], v202 offset:23200\n"
"buffer_load_dwordx4 v213, s[24:27], s39 offen offset:3072 lds\n"
"v_mfma_f32_16x16x32_bf16 v[104:107], a[216:219], a[72:75], v[104:107]\n"
"ds_read_b64_tr_b16 a[192:193], v202 offset:18624\n"
"v_accvgpr_write_b32 a69, v149\n"
"v_mfma_f32_16x16x32_bf16 v[108:111], a[216:219], a[76:79], v[108:111]\n"
"s_waitcnt lgkmcnt(11)\n"
"ds_read_b64_tr_b16 a[194:195], v202 offset:23232\n"
"v_accvgpr_write_b32 a70, v150\n"
"v_mfma_f32_16x16x32_bf16 v[112:115], a[220:223], a[72:75], v[112:115]\n"
"ds_read_b64_tr_b16 a[196:197], v202 offset:18656\n"
"v_accvgpr_write_b32 a71, v151\n"
"v_mfma_f32_16x16x32_bf16 v[116:119], a[220:223], a[76:79], v[116:119]\n"
"ds_read_b64_tr_b16 a[198:199], v202 offset:23264\n"
"v_mfma_f32_16x16x32_bf16 v[120:123], a[128:131], a[72:75], v[120:123]\n"
"ds_read_b64_tr_b16 a[200:201], v205 offset:18432\n"
"v_mfma_f32_16x16x32_bf16 v[124:127], a[128:131], a[76:79], v[124:127]\n"
"s_add_u32 m0, s53, 114688\n"
"ds_read_b64_tr_b16 a[202:203], v205 offset:23040\n"
"buffer_load_dwordx4 v[12:15], v207, s[28:31], s22 offen offset:3072 sc1\n"
"v_mfma_f32_16x16x32_bf16 v[226:229], a[132:135], a[72:75], v[226:229]\n"
"s_add_u32 s22, s22, 4096\n"
"ds_read_b64_tr_b16 a[204:205], v200 offset:0\n"
"buffer_load_dwordx4 v214, s[24:27], s39 offen offset:0 lds\n"
"v_mfma_f32_16x16x32_bf16 v[230:233], a[132:135], a[76:79], v[230:233]\n"
"s_waitcnt vmcnt(27)\n"
"ds_read_b64_tr_b16 a[206:207], v200 offset:4608\n"
"v_mfma_f32_16x16x32_bf16 v[64:67], a[136:139], a[80:83], v[64:67]\n"
"ds_read_b64_tr_b16 a[208:209], v200 offset:32\n"
"v_mfma_f32_16x16x32_bf16 v[68:71], a[136:139], a[84:87], v[68:71]\n"
"s_waitcnt lgkmcnt(11)\n"
"ds_read_b64_tr_b16 a[210:211], v200 offset:4640\n"
"v_accvgpr_write_b32 a72, v152\n"
"v_mfma_f32_16x16x32_bf16 v[72:75], a[140:143], a[80:83], v[72:75]\n"
"ds_read_b64_tr_b16 a[212:213], v200 offset:64\n"
"v_accvgpr_write_b32 a73, v153\n"
"v_mfma_f32_16x16x32_bf16 v[76:79], a[140:143], a[84:87], v[76:79]\n"
"ds_read_b64_tr_b16 a[214:215], v200 offset:4672\n"
"v_accvgpr_write_b32 a74, v154\n"
"v_mfma_f32_16x16x32_bf16 v[80:83], a[144:147], a[80:83], v[80:83]\n"
"s_add_u32 m0, s53, 114688\n"
"ds_read_b64_tr_b16 a[216:217], v200 offset:96\n"
"buffer_load_dwordx4 v[16:19], v207, s[28:31], s20 offen offset:0 sc1\n"
"v_mfma_f32_16x16x32_bf16 v[84:87], a[144:147], a[84:87], v[84:87]\n"
"ds_read_b64_tr_b16 a[218:219], v200 offset:4704\n"
"buffer_load_dwordx4 v236, s[24:27], s39 offen offset:1024 lds\n"
"v_mfma_f32_16x16x32_bf16 v[88:91], a[148:151], a[80:83], v[88:91]\n"
"ds_read_b64_tr_b16 a[220:221], v200 offset:128\n"
"v_accvgpr_write_b32 a75, v155\n"
"v_mfma_f32_16x16x32_bf16 v[92:95], a[148:151], a[84:87], v[92:95]\n"
"ds_read_b64_tr_b16 a[222:223], v200 offset:4736\n"
"v_accvgpr_write_b32 a76, v156\n"
"v_mfma_f32_16x16x32_bf16 v[96:99], a[152:155], a[80:83], v[96:99]\n"
"ds_read_b64_tr_b16 a[128:129], v200 offset:160\n"
"v_accvgpr_write_b32 a77, v157\n"
"v_mfma_f32_16x16x32_bf16 v[100:103], a[152:155], a[84:87], v[100:103]\n"
"s_waitcnt lgkmcnt(11)\n"
"ds_read_b64_tr_b16 a[130:131], v200 offset:4768\n"
"v_accvgpr_write_b32 a78, v158\n"
"v_mfma_f32_16x16x32_bf16 v[104:107], a[156:159], a[80:83], v[104:107]\n"
"ds_read_b64_tr_b16 a[132:133], v200 offset:192\n"
"v_accvgpr_write_b32 a79, v159\n"
"v_mfma_f32_16x16x32_bf16 v[108:111], a[156:159], a[84:87], v[108:111]\n"
"s_add_u32 m0, s53, 114688\n"
"ds_read_b64_tr_b16 a[134:135], v200 offset:4800\n"
"buffer_load_dwordx4 v[20:23], v207, s[28:31], s20 offen offset:1024 sc1\n"
"v_mfma_f32_16x16x32_bf16 v[112:115], a[192:195], a[80:83], v[112:115]\n"
"ds_read_b64_tr_b16 a[136:137], v200 offset:224\n"
"buffer_load_dwordx4 v237, s[24:27], s39 offen offset:2048 lds\n"
"v_mfma_f32_16x16x32_bf16 v[116:119], a[192:195], a[84:87], v[116:119]\n"
"ds_read_b64_tr_b16 a[138:139], v200 offset:4832\n"
"v_mfma_f32_16x16x32_bf16 v[120:123], a[196:199], a[80:83], v[120:123]\n"
"ds_read_b64_tr_b16 a[140:141], v203 offset:0\n"
"v_mfma_f32_16x16x32_bf16 v[124:127], a[196:199], a[84:87], v[124:127]\n"
"ds_read_b64_tr_b16 a[142:143], v203 offset:4608\n"
"v_mfma_f32_16x16x32_bf16 v[226:229], a[200:203], a[80:83], v[226:229]\n"
"ds_read_b64_tr_b16 a[144:145], v200 offset:9216\n"
"v_mfma_f32_16x16x32_bf16 v[230:233], a[200:203], a[84:87], v[230:233]\n"
"s_waitcnt lgkmcnt(11)\n"
"s_waitcnt vmcnt(27)\n"
"ds_read_b64_tr_b16 a[146:147], v200 offset:13824\n"
"v_mfma_f32_16x16x32_bf16 v[64:67], a[204:207], a[88:91], v[64:67]\n"
"s_add_u32 m0, s53, 114688\n"
"ds_read_b64_tr_b16 a[148:149], v200 offset:9248\n"
"buffer_load_dwordx4 v[24:27], v207, s[28:31], s21 offen offset:0 sc1\n"
"v_mfma_f32_16x16x32_bf16 v[68:71], a[204:207], a[92:95], v[68:71]\n"
"ds_read_b64_tr_b16 a[150:151], v200 offset:13856\n"
"buffer_load_dwordx4 v238, s[24:27], s39 offen offset:3072 lds\n"
"v_mfma_f32_16x16x32_bf16 v[72:75], a[208:211], a[88:91], v[72:75]\n"
"ds_read_b64_tr_b16 a[152:153], v200 offset:9280\n"
"v_accvgpr_write_b32 a80, v160\n"
"v_mfma_f32_16x16x32_bf16 v[76:79], a[208:211], a[92:95], v[76:79]\n"
"ds_read_b64_tr_b16 a[154:155], v200 offset:13888\n"
"v_accvgpr_write_b32 a81, v161\n"
"v_mfma_f32_16x16x32_bf16 v[80:83], a[212:215], a[88:91], v[80:83]\n"
"ds_read_b64_tr_b16 a[156:157], v200 offset:9312\n"
"v_accvgpr_write_b32 a82, v162\n"
"v_mfma_f32_16x16x32_bf16 v[84:87], a[212:215], a[92:95], v[84:87]\n"
"ds_read_b64_tr_b16 a[158:159], v200 offset:13920\n"
"v_accvgpr_write_b32 a83, v163\n"
"v_mfma_f32_16x16x32_bf16 v[88:91], a[216:219], a[88:91], v[88:91]\n"
"ds_read_b64_tr_b16 a[192:193], v200 offset:9344\n"
"v_accvgpr_write_b32 a84, v164\n"
"v_mfma_f32_16x16x32_bf16 v[92:95], a[216:219], a[92:95], v[92:95]\n"
"s_waitcnt lgkmcnt(11)\n"
"s_add_u32 m0, s53, 118784\n"
"ds_read_b64_tr_b16 a[194:195], v200 offset:13952\n"
"buffer_load_dwordx4 v[28:31], v207, s[28:31], s21 offen offset:1024 sc1\n"
"v_mfma_f32_16x16x32_bf16 v[96:99], a[220:223], a[88:91], v[96:99]\n"
"ds_read_b64_tr_b16 a[196:197], v200 offset:9376\n"
"buffer_load_dwordx4 v239, s[24:27], s39 offen offset:0 lds\n"
"v_mfma_f32_16x16x32_bf16 v[100:103], a[220:223], a[92:95], v[100:103]\n"
"ds_read_b64_tr_b16 a[198:199], v200 offset:13984\n"
"v_accvgpr_write_b32 a85, v165\n"
"v_mfma_f32_16x16x32_bf16 v[104:107], a[128:131], a[88:91], v[104:107]\n"
"ds_read_b64_tr_b16 a[200:201], v200 offset:9408\n"
"v_accvgpr_write_b32 a86, v166\n"
"v_mfma_f32_16x16x32_bf16 v[108:111], a[128:131], a[92:95], v[108:111]\n"
"ds_read_b64_tr_b16 a[202:203], v200 offset:14016\n"
"v_accvgpr_write_b32 a87, v167\n"
"v_mfma_f32_16x16x32_bf16 v[112:115], a[132:135], a[88:91], v[112:115]\n"
"ds_read_b64_tr_b16 a[204:205], v200 offset:9440\n"
"v_mfma_f32_16x16x32_bf16 v[116:119], a[132:135], a[92:95], v[116:119]\n"
"ds_read_b64_tr_b16 a[206:207], v200 offset:14048\n"
"v_mfma_f32_16x16x32_bf16 v[120:123], a[136:139], a[88:91], v[120:123]\n"
"ds_read_b64_tr_b16 a[208:209], v203 offset:9216\n"
"buffer_load_dwordx4 v[32:35], v207, s[28:31], s22 offen offset:0 sc1\n"
"v_mfma_f32_16x16x32_bf16 v[124:127], a[136:139], a[92:95], v[124:127]\n"
"s_waitcnt lgkmcnt(11)\n"
"ds_read_b64_tr_b16 a[210:211], v203 offset:13824\n"
"v_mfma_f32_16x16x32_bf16 v[226:229], a[140:143], a[88:91], v[226:229]\n"
"ds_read_b64_tr_b16 a[212:213], v200 offset:18432\n"
"v_mfma_f32_16x16x32_bf16 v[230:233], a[140:143], a[92:95], v[230:233]\n"
"s_waitcnt vmcnt(28)\n"
"ds_read_b64_tr_b16 a[214:215], v200 offset:23040\n"
"v_mfma_f32_16x16x32_bf16 v[64:67], a[144:147], a[96:99], v[64:67]\n"
"ds_read_b64_tr_b16 a[216:217], v200 offset:18464\n"
"v_mfma_f32_16x16x32_bf16 v[68:71], a[144:147], a[100:103], v[68:71]\n"
"ds_read_b64_tr_b16 a[218:219], v200 offset:23072\n"
"v_accvgpr_write_b32 a88, v168\n"
"v_mfma_f32_16x16x32_bf16 v[72:75], a[148:151], a[96:99], v[72:75]\n"
"ds_read_b64_tr_b16 a[220:221], v200 offset:18496\n"
"v_accvgpr_write_b32 a89, v169\n"
"v_mfma_f32_16x16x32_bf16 v[76:79], a[148:151], a[100:103], v[76:79]\n"
"ds_read_b64_tr_b16 a[222:223], v200 offset:23104\n"
"buffer_load_dwordx4 v[36:39], v207, s[28:31], s22 offen offset:1024 sc1\n"
"v_mfma_f32_16x16x32_bf16 v[80:83], a[152:155], a[96:99], v[80:83]\n"
"ds_read_b64_tr_b16 a[128:129], v200 offset:18528\n"
"v_accvgpr_write_b32 a90, v170\n"
"v_mfma_f32_16x16x32_bf16 v[84:87], a[152:155], a[100:103], v[84:87]\n"
"s_waitcnt lgkmcnt(11)\n"
"ds_read_b64_tr_b16 a[130:131], v200 offset:23136\n"
"v_accvgpr_write_b32 a91, v171\n"
"v_mfma_f32_16x16x32_bf16 v[88:91], a[156:159], a[96:99], v[88:91]\n"
"ds_read_b64_tr_b16 a[132:133], v200 offset:18560\n"
"v_accvgpr_write_b32 a92, v172\n"
"v_mfma_f32_16x16x32_bf16 v[92:95], a[156:159], a[100:103], v[92:95]\n"
"ds_read_b64_tr_b16 a[134:135], v200 offset:23168\n"
"v_accvgpr_write_b32 a93, v173\n"
"v_mfma_f32_16x16x32_bf16 v[96:99], a[192:195], a[96:99], v[96:99]\n"
"ds_read_b64_tr_b16 a[136:137], v200 offset:18592\n"
"v_accvgpr_write_b32 a94, v174\n"
"v_mfma_f32_16x16x32_bf16 v[100:103], a[192:195], a[100:103], v[100:103]\n"
"ds_read_b64_tr_b16 a[138:139], v200 offset:23200\n"
"v_accvgpr_write_b32 a95, v175\n"
"v_mfma_f32_16x16x32_bf16 v[104:107], a[196:199], a[96:99], v[104:107]\n"
"ds_read_b64_tr_b16 a[140:141], v200 offset:18624\n"
"buffer_load_dwordx4 v[40:43], v207, s[28:31], s20 offen offset:2048 sc1\n"
"v_mfma_f32_16x16x32_bf16 v[108:111], a[196:199], a[100:103], v[108:111]\n"
"ds_read_b64_tr_b16 a[142:143], v200 offset:23232\n"
"v_mfma_f32_16x16x32_bf16 v[112:115], a[200:203], a[96:99], v[112:115]\n"
"ds_read_b64_tr_b16 a[144:145], v200 offset:18656\n"
"v_mfma_f32_16x16x32_bf16 v[116:119], a[200:203], a[100:103], v[116:119]\n"
"s_waitcnt lgkmcnt(11)\n"
"ds_read_b64_tr_b16 a[146:147], v200 offset:23264\n"
"v_mfma_f32_16x16x32_bf16 v[120:123], a[204:207], a[96:99], v[120:123]\n"
"ds_read_b64_tr_b16 a[148:149], v203 offset:18432\n"
"v_mfma_f32_16x16x32_bf16 v[124:127], a[204:207], a[100:103], v[124:127]\n"
"ds_read_b64_tr_b16 a[150:151], v203 offset:23040\n"
"v_mfma_f32_16x16x32_bf16 v[226:229], a[208:211], a[96:99], v[226:229]\n"
"ds_read_b64_tr_b16 a[152:153], v200 offset:27648\n"
"v_mfma_f32_16x16x32_bf16 v[230:233], a[208:211], a[100:103], v[230:233]\n"
"s_waitcnt vmcnt(26)\n"
"ds_read_b64_tr_b16 a[154:155], v200 offset:32256\n"
"buffer_load_dwordx4 v[44:47], v207, s[28:31], s20 offen offset:3072 sc1\n"
"v_mfma_f32_16x16x32_bf16 v[64:67], a[212:215], a[104:107], v[64:67]\n"
"s_add_u32 s20, s20, 4096\n"
"ds_read_b64_tr_b16 a[156:157], v200 offset:27680\n"
"v_mfma_f32_16x16x32_bf16 v[68:71], a[212:215], a[108:111], v[68:71]\n"
"ds_read_b64_tr_b16 a[158:159], v200 offset:32288\n"
"v_accvgpr_write_b32 a96, v176\n"
"v_mfma_f32_16x16x32_bf16 v[72:75], a[216:219], a[104:107], v[72:75]\n"
"ds_read_b64_tr_b16 a[192:193], v200 offset:27712\n"
"v_accvgpr_write_b32 a97, v177\n"
"v_mfma_f32_16x16x32_bf16 v[76:79], a[216:219], a[108:111], v[76:79]\n"
"s_waitcnt lgkmcnt(11)\n"
"ds_read_b64_tr_b16 a[194:195], v200 offset:32320\n"
"v_accvgpr_write_b32 a98, v178\n"
"v_mfma_f32_16x16x32_bf16 v[80:83], a[220:223], a[104:107], v[80:83]\n"
"ds_read_b64_tr_b16 a[196:197], v200 offset:27744\n"
"v_accvgpr_write_b32 a99, v179\n"
"v_mfma_f32_16x16x32_bf16 v[84:87], a[220:223], a[108:111], v[84:87]\n"
"ds_read_b64_tr_b16 a[198:199], v200 offset:32352\n"
"v_accvgpr_write_b32 a100, v180\n"
"v_mfma_f32_16x16x32_bf16 v[88:91], a[128:131], a[104:107], v[88:91]\n"
"ds_read_b64_tr_b16 a[200:201], v200 offset:27776\n"
"buffer_load_dwordx4 v[48:51], v207, s[28:31], s21 offen offset:2048 sc1\n"
"v_mfma_f32_16x16x32_bf16 v[92:95], a[128:131], a[108:111], v[92:95]\n"
"ds_read_b64_tr_b16 a[202:203], v200 offset:32384\n"
"v_accvgpr_write_b32 a101, v181\n"
"v_mfma_f32_16x16x32_bf16 v[96:99], a[132:135], a[104:107], v[96:99]\n"
"AT_pg15:\n"
"s_cbranch_execz AT_hr\n"
"ds_read_b64_tr_b16 a[204:205], v200 offset:27808\n"
"v_accvgpr_write_b32 a102, v182\n"
"v_mfma_f32_16x16x32_bf16 v[100:103], a[132:135], a[108:111], v[100:103]\n"
"ds_read_b64_tr_b16 a[206:207], v200 offset:32416\n"
"v_accvgpr_write_b32 a103, v183\n"
"v_mfma_f32_16x16x32_bf16 v[104:107], a[136:139], a[104:107], v[104:107]\n"
"ds_read_b64_tr_b16 a[208:209], v200 offset:27840\n"
"v_mfma_f32_16x16x32_bf16 v[108:111], a[136:139], a[108:111], v[108:111]\n"
"s_waitcnt lgkmcnt(11)\n"
"ds_read_b64_tr_b16 a[210:211], v200 offset:32448\n"
"v_mfma_f32_16x16x32_bf16 v[112:115], a[140:143], a[104:107], v[112:115]\n"
"ds_read_b64_tr_b16 a[212:213], v200 offset:27872\n"
"v_mfma_f32_16x16x32_bf16 v[116:119], a[140:143], a[108:111], v[116:119]\n"
"ds_read_b64_tr_b16 a[214:215], v200 offset:32480\n"
"buffer_load_dwordx4 v[52:55], v207, s[28:31], s21 offen offset:3072 sc1\n"
"v_mfma_f32_16x16x32_bf16 v[120:123], a[144:147], a[104:107], v[120:123]\n"
"s_add_u32 s21, s21, 4096\n"
"ds_read_b64_tr_b16 a[216:217], v203 offset:27648\n"
"v_mfma_f32_16x16x32_bf16 v[124:127], a[144:147], a[108:111], v[124:127]\n"
"ds_read_b64_tr_b16 a[218:219], v203 offset:32256\n"
"v_mfma_f32_16x16x32_bf16 v[226:229], a[148:151], a[104:107], v[226:229]\n"
"ds_read_b64_tr_b16 a[220:221], v200 offset:36864\n"
"v_mfma_f32_16x16x32_bf16 v[230:233], a[148:151], a[108:111], v[230:233]\n"
"s_waitcnt vmcnt(25)\n"
"ds_read_b64_tr_b16 a[222:223], v200 offset:41472\n"
"v_mfma_f32_16x16x32_bf16 v[64:67], a[152:155], a[112:115], v[64:67]\n"
"ds_read_b64_tr_b16 a[128:129], v200 offset:36896\n"
"v_mfma_f32_16x16x32_bf16 v[68:71], a[152:155], a[116:119], v[68:71]\n"
"s_waitcnt lgkmcnt(11)\n"
"ds_read_b64_tr_b16 a[130:131], v200 offset:41504\n"
"v_accvgpr_write_b32 a104, v184\n"
"v_mfma_f32_16x16x32_bf16 v[72:75], a[156:159], a[112:115], v[72:75]\n"
"ds_read_b64_tr_b16 a[132:133], v200 offset:36928\n"
"buffer_load_dwordx4 v[56:59], v207, s[28:31], s22 offen offset:2048 sc1\n"
"v_mfma_f32_16x16x32_bf16 v[76:79], a[156:159], a[116:119], v[76:79]\n"
"ds_read_b64_tr_b16 a[134:135], v200 offset:41536\n"
"v_accvgpr_write_b32 a105, v185\n"
"v_mfma_f32_16x16x32_bf16 v[80:83], a[192:195], a[112:115], v[80:83]\n"
"ds_read_b64_tr_b16 a[136:137], v200 offset:36960\n"
"v_accvgpr_write_b32 a106, v186\n"
"v_mfma_f32_16x16x32_bf16 v[84:87], a[192:195], a[116:119], v[84:87]\n"
"ds_read_b64_tr_b16 a[138:139], v200 offset:41568\n"
"v_accvgpr_write_b32 a107, v187\n"
"v_mfma_f32_16x16x32_bf16 v[88:91], a[196:199], a[112:115], v[88:91]\n"
"ds_read_b64_tr_b16 a[140:141], v200 offset:36992\n"
"v_accvgpr_write_b32 a108, v188\n"
"v_mfma_f32_16x16x32_bf16 v[92:95], a[196:199], a[116:119], v[92:95]\n"
"ds_read_b64_tr_b16 a[142:143], v200 offset:41600\n"
"v_accvgpr_write_b32 a109, v189\n"
"v_mfma_f32_16x16x32_bf16 v[96:99], a[200:203], a[112:115], v[96:99]\n"
"ds_read_b64_tr_b16 a[144:145], v200 offset:37024\n"
"v_accvgpr_write_b32 a110, v190\n"
"v_mfma_f32_16x16x32_bf16 v[100:103], a[200:203], a[116:119], v[100:103]\n"
"s_waitcnt lgkmcnt(11)\n"
"ds_read_b64_tr_b16 a[146:147], v200 offset:41632\n"
"buffer_load_dwordx4 v[60:63], v207, s[28:31], s22 offen offset:3072 sc1\n"
"v_mfma_f32_16x16x32_bf16 v[104:107], a[204:207], a[112:115], v[104:107]\n"
"s_add_u32 s22, s22, 4096\n"
"ds_read_b64_tr_b16 a[148:149], v200 offset:37056\n"
"v_accvgpr_write_b32 a111, v191\n"
"v_mfma_f32_16x16x32_bf16 v[108:111], a[204:207], a[116:119], v[108:111]\n"
"s_waitcnt vmcnt(8)\n"
"ds_read_b64_tr_b16 a[150:151], v200 offset:41664\n"
"v_mfma_f32_16x16x32_bf16 v[112:115], a[208:211], a[112:115], v[112:115]\n"
"s_barrier\n"
"ds_read_b64_tr_b16 a[152:153], v200 offset:37088\n"
"v_mfma_f32_16x16x32_bf16 v[116:119], a[208:211], a[116:119], v[116:119]\n"
"ds_read_b64_tr_b16 a[154:155], v200 offset:41696\n"
"v_mfma_f32_16x16x32_bf16 v[120:123], a[212:215], a[112:115], v[120:123]\n"
"ds_read_b64_tr_b16 a[156:157], v203 offset:36864\n"
"v_mfma_f32_16x16x32_bf16 v[124:127], a[212:215], a[116:119], v[124:127]\n"
"ds_read_b64_tr_b16 a[158:159], v203 offset:41472\n"
"v_mfma_f32_16x16x32_bf16 v[226:229], a[216:219], a[112:115], v[226:229]\n"
"ds_read_b64_tr_b16 a[192:193], v203 offset:46080\n"
"v_mfma_f32_16x16x32_bf16 v[230:233], a[216:219], a[116:119], v[230:233]\n"
"s_waitcnt lgkmcnt(11)\n"
"s_waitcnt vmcnt(25)\n"
"ds_read_b64_tr_b16 a[194:195], v203 offset:50688\n"
"v_mfma_f32_16x16x32_bf16 v[64:67], a[220:223], a[120:123], v[64:67]\n"
"ds_read_b64_tr_b16 a[196:197], v203 offset:55296\n"
"v_mfma_f32_16x16x32_bf16 v[68:71], a[220:223], a[124:127], v[68:71]\n"
"ds_read_b64_tr_b16 a[198:199], v203 offset:59904\n"
"v_accvgpr_write_b32 a112, v192\n"
"v_mfma_f32_16x16x32_bf16 v[72:75], a[128:131], a[120:123], v[72:75]\n"
"ds_read_b64_tr_b16 a[200:201], v203 offset:64512\n"
"v_accvgpr_write_b32 a113, v193\n"
"v_mfma_f32_16x16x32_bf16 v[76:79], a[128:131], a[124:127], v[76:79]\n"
"ds_read_b64_tr_b16 a[202:203], v204 offset:3584\n"
"v_accvgpr_write_b32 a114, v194\n"
"v_mfma_f32_16x16x32_bf16 v[80:83], a[132:135], a[120:123], v[80:83]\n"
"ds_read_b64_tr_b16 a[204:205], v204 offset:8192\n"
"v_accvgpr_write_b32 a115, v195\n"
"v_mfma_f32_16x16x32_bf16 v[84:87], a[132:135], a[124:127], v[84:87]\n"
"ds_read_b64_tr_b16 a[206:207], v204 offset:12800\n"
"v_accvgpr_write_b32 a116, v196\n"
"v_mfma_f32_16x16x32_bf16 v[88:91], a[136:139], a[120:123], v[88:91]\n"
"ds_read_b64_tr_b16 a[208:209], v204 offset:17408\n"
"v_accvgpr_write_b32 a117, v197\n"
"v_mfma_f32_16x16x32_bf16 v[92:95], a[136:139], a[124:127], v[92:95]\n"
"s_waitcnt lgkmcnt(11)\n"
"ds_read_b64_tr_b16 a[210:211], v204 offset:22016\n"
"v_accvgpr_write_b32 a118, v198\n"
"v_mfma_f32_16x16x32_bf16 v[96:99], a[140:143], a[120:123], v[96:99]\n"
"ds_read_b64_tr_b16 a[212:213], v204 offset:26624\n"
"v_accvgpr_write_b32 a119, v199\n"
"v_mfma_f32_16x16x32_bf16 v[100:103], a[140:143], a[124:127], v[100:103]\n"
"ds_read_b64_tr_b16 a[214:215], v204 offset:31232\n"
"v_mfma_f32_16x16x32_bf16 v[104:107], a[144:147], a[120:123], v[104:107]\n"
"ds_read_b64_tr_b16 a[216:217], v204 offset:35840\n"
"v_mfma_f32_16x16x32_bf16 v[108:111], a[144:147], a[124:127], v[108:111]\n"
"ds_read_b64_tr_b16 a[218:219], v204 offset:40448\n"
"v_mfma_f32_16x16x32_bf16 v[112:115], a[148:151], a[120:123], v[112:115]\n"
"ds_read_b64_tr_b16 a[220:221], v204 offset:45056\n"
"v_mfma_f32_16x16x32_bf16 v[116:119], a[148:151], a[124:127], v[116:119]\n"
"ds_read_b64_tr_b16 a[222:223], v204 offset:49664\n"
"v_mfma_f32_16x16x32_bf16 v[120:123], a[152:155], a[120:123], v[120:123]\n"
"ds_read_b64_tr_b16 a[128:129], v200 offset:46080\n"
"v_mfma_f32_16x16x32_bf16 v[124:127], a[152:155], a[124:127], v[124:127]\n"
"s_waitcnt lgkmcnt(11)\n"
"s_mov_b32 m0, s45\n"
"ds_read_b64_tr_b16 a[130:131], v200 offset:50688\n"
"v_mfma_f32_16x16x32_bf16 v[226:229], a[156:159], a[120:123], v[226:229]\n"
"ds_read_b64_tr_b16 a[132:133], v200 offset:55296\n"
"buffer_load_dwordx4 v207, s[16:19], s46 offen offset:0 lds\n"
"v_mfma_f32_16x16x32_bf16 v[230:233], a[156:159], a[124:127], v[230:233]\n"
"s_waitcnt vmcnt(22)\n"
"ds_read_b64_tr_b16 a[134:135], v200 offset:59904\n"
"v_mfma_f32_16x16x32_bf16 v[226:229], a[192:195], v[0:3], v[226:229]\n"
"ds_read_b64_tr_b16 a[136:137], v200 offset:64512\n"
"v_mfma_f32_16x16x32_bf16 v[230:233], a[192:195], v[4:7], v[230:233]\n"
"s_waitcnt vmcnt(18)\n"
"ds_read_b64_tr_b16 a[138:139], v201 offset:3584\n"
"v_mfma_f32_16x16x32_bf16 v[226:229], a[196:199], v[8:11], v[226:229]\n"
"s_mov_b32 m0, s45\n"
"ds_read_b64_tr_b16 a[140:141], v201 offset:8192\n"
"v_mfma_f32_16x16x32_bf16 v[230:233], a[196:199], v[12:15], v[230:233]\n"
"s_waitcnt vmcnt(14)\n"
"ds_read_b64_tr_b16 a[142:143], v201 offset:12800\n"
"buffer_load_dwordx4 v207, s[16:19], s46 offen offset:1024 lds\n"
"v_mfma_f32_16x16x32_bf16 v[226:229], a[200:203], v[16:19], v[226:229]\n"
"ds_read_b64_tr_b16 a[144:145], v201 offset:17408\n"
"v_mfma_f32_16x16x32_bf16 v[230:233], a[200:203], v[20:23], v[230:233]\n"
"s_waitcnt lgkmcnt(11)\n"
"s_waitcnt vmcnt(11)\n"
"ds_read_b64_tr_b16 a[146:147], v201 offset:22016\n"
"v_mfma_f32_16x16x32_bf16 v[226:229], a[204:207], v[24:27], v[226:229]\n"
"ds_read_b64_tr_b16 a[148:149], v201 offset:26624\n"
"v_mfma_f32_16x16x32_bf16 v[230:233], a[204:207], v[28:31], v[230:233]\n"
"s_waitcnt vmcnt(8)\n"
"ds_read_b64_tr_b16 a[150:151], v201 offset:31232\n"
"v_mfma_f32_16x16x32_bf16 v[226:229], a[208:211], v[32:35], v[226:229]\n"
"s_mov_b32 m0, s45\n"
"ds_read_b64_tr_b16 a[152:153], v201 offset:35840\n"
"v_mfma_f32_16x16x32_bf16 v[230:233], a[208:211], v[36:39], v[230:233]\n"
"s_waitcnt vmcnt(6)\n"
"ds_read_b64_tr_b16 a[154:155], v201 offset:40448\n"
"buffer_load_dwordx4 v207, s[16:19], s46 offen offset:2048 lds\n"
"v_mfma_f32_16x16x32_bf16 v[226:229], a[212:215], v[40:43], v[226:229]\n"
"ds_read_b64_tr_b16 a[156:157], v201 offset:45056\n"
"v_mfma_f32_16x16x32_bf16 v[230:233], a[212:215], v[44:47], v[230:233]\n"
"s_waitcnt vmcnt(5)\n"
"ds_read_b64_tr_b16 a[158:159], v201 offset:49664\n"
"v_mfma_f32_16x16x32_bf16 v[226:229], a[216:219], v[48:51], v[226:229]\n"
"ds_read_b64_tr_b16 a[192:193], v200 offset:46112\n"
"v_mfma_f32_16x16x32_bf16 v[230:233], a[216:219], v[52:55], v[230:233]\n"
"s_waitcnt lgkmcnt(11)\n"
"s_waitcnt vmcnt(3)\n"
"ds_read_b64_tr_b16 a[194:195], v200 offset:50720\n"
"v_mfma_f32_16x16x32_bf16 v[226:229], a[220:223], v[56:59], v[226:229]\n"
"ds_read_b64_tr_b16 a[196:197], v200 offset:55328\n"
"v_mfma_f32_16x16x32_bf16 v[230:233], a[220:223], v[60:63], v[230:233]\n"
"ds_read_b64_tr_b16 a[198:199], v200 offset:59936\n"
"v_mfma_f32_16x16x32_bf16 v[64:67], a[128:131], v[0:3], v[64:67]\n"
"ds_read_b64_tr_b16 a[200:201], v200 offset:64544\n"
"v_mfma_f32_16x16x32_bf16 v[68:71], a[128:131], v[4:7], v[68:71]\n"
"s_add_u32 m0, s84, 131072\n"
"ds_read_b64_tr_b16 a[202:203], v201 offset:3616\n"
"v_mfma_f32_16x16x32_bf16 v[64:67], a[132:135], v[8:11], v[64:67]\n"
"ds_read_b64_tr_b16 a[204:205], v201 offset:8224\n"
"buffer_load_dwordx4 v207, s[72:75], s50 offen offset:0 lds\n"
"v_mfma_f32_16x16x32_bf16 v[68:71], a[132:135], v[12:15], v[68:71]\n"
"ds_read_b64_tr_b16 a[206:207], v201 offset:12832\n"
"v_cvt_pk_bf16_f32 v218, v226, v227\n"
"v_mfma_f32_16x16x32_bf16 v[64:67], a[136:139], v[16:19], v[64:67]\n"
"ds_read_b64_tr_b16 a[208:209], v201 offset:17440\n"
"v_cvt_pk_bf16_f32 v219, v228, v229\n"
"v_mfma_f32_16x16x32_bf16 v[68:71], a[136:139], v[20:23], v[68:71]\n"
"s_waitcnt lgkmcnt(11)\n"
"ds_read_b64_tr_b16 a[210:211], v201 offset:22048\n"
"v_cvt_pk_bf16_f32 v222, v230, v231\n"
"v_mfma_f32_16x16x32_bf16 v[64:67], a[140:143], v[24:27], v[64:67]\n"
"ds_read_b64_tr_b16 a[212:213], v201 offset:26656\n"
"v_cvt_pk_bf16_f32 v223, v232, v233\n"
"v_mfma_f32_16x16x32_bf16 v[68:71], a[140:143], v[28:31], v[68:71]\n"
"ds_read_b64_tr_b16 a[214:215], v201 offset:31264\n"
"v_mfma_f32_16x16x32_bf16 v[64:67], a[144:147], v[32:35], v[64:67]\n"
"s_add_u32 m0, s84, 131072\n"
"ds_read_b64_tr_b16 a[216:217], v201 offset:35872\n"
"buffer_store_dwordx2 v[218:219], v207, s[28:31], s49 offen sc1\n"
"v_mfma_f32_16x16x32_bf16 v[68:71], a[144:147], v[36:39], v[68:71]\n"
"ds_read_b64_tr_b16 a[218:219], v201 offset:40480\n"
"buffer_load_dwordx4 v207, s[72:75], s50 offen offset:1024 lds\n"
"v_mfma_f32_16x16x32_bf16 v[64:67], a[148:151], v[40:43], v[64:67]\n"
"ds_read_b64_tr_b16 a[220:221], v201 offset:45088\n"
"buffer_store_dwordx2 v[222:223], v208, s[28:31], s49 offen sc1\n"
"v_mfma_f32_16x16x32_bf16 v[68:71], a[148:151], v[44:47], v[68:71]\n"
"ds_read_b64_tr_b16 a[222:223], v201 offset:49696\n"
"v_mfma_f32_16x16x32_bf16 v[64:67], a[152:155], v[48:51], v[64:67]\n"
"ds_read_b64_tr_b16 a[128:129], v200 offset:46144\n"
"v_mfma_f32_16x16x32_bf16 v[68:71], a[152:155], v[52:55], v[68:71]\n"
"s_waitcnt lgkmcnt(11)\n"
"ds_read_b64_tr_b16 a[130:131], v200 offset:50752\n"
"v_mfma_f32_16x16x32_bf16 v[64:67], a[156:159], v[56:59], v[64:67]\n"
"s_add_u32 m0, s84, 131072\n"
"ds_read_b64_tr_b16 a[132:133], v200 offset:55360\n"
"v_mfma_f32_16x16x32_bf16 v[68:71], a[156:159], v[60:63], v[68:71]\n"
"ds_read_b64_tr_b16 a[134:135], v200 offset:59968\n"
"buffer_load_dwordx4 v207, s[72:75], s50 offen offset:2048 lds\n"
"v_mfma_f32_16x16x32_bf16 v[72:75], a[192:195], v[0:3], v[72:75]\n"
"ds_read_b64_tr_b16 a[136:137], v200 offset:64576\n"
"v_mfma_f32_16x16x32_bf16 v[76:79], a[192:195], v[4:7], v[76:79]\n"
"ds_read_b64_tr_b16 a[138:139], v201 offset:3648\n"
"v_mfma_f32_16x16x32_bf16 v[72:75], a[196:199], v[8:11], v[72:75]\n"
"ds_read_b64_tr_b16 a[140:141], v201 offset:8256\n"
"v_mfma_f32_16x16x32_bf16 v[76:79], a[196:199], v[12:15], v[76:79]\n"
"ds_read_b64_tr_b16 a[142:143], v201 offset:12864\n"
"v_cvt_pk_bf16_f32 v218, v64, v65\n"
"v_mfma_f32_16x16x32_bf16 v[72:75], a[200:203], v[16:19], v[72:75]\n"
"ds_read_b64_tr_b16 a[144:145], v201 offset:17472\n"
"v_cvt_pk_bf16_f32 v219, v66, v67\n"
"v_mfma_f32_16x16x32_bf16 v[76:79], a[200:203], v[20:23], v[76:79]\n"
"s_waitcnt lgkmcnt(11)\n"
"ds_read_b64_tr_b16 a[146:147], v201 offset:22080\n"
"v_cvt_pk_bf16_f32 v222, v68, v69\n"
"v_mfma_f32_16x16x32_bf16 v[72:75], a[204:207], v[24:27], v[72:75]\n"
"s_add_u32 m0, s84, 131072\n"
"ds_read_b64_tr_b16 a[148:149], v201 offset:26688\n"
"v_cvt_pk_bf16_f32 v223, v70, v71\n"
"v_mfma_f32_16x16x32_bf16 v[76:79], a[204:207], v[28:31], v[76:79]\n"
"ds_read_b64_tr_b16 a[150:151], v201 offset:31296\n"
"buffer_load_dwordx4 v207, s[72:75], s50 offen offset:3072 lds\n"
"v_mfma_f32_16x16x32_bf16 v[72:75], a[208:211], v[32:35], v[72:75]\n"
"ds_read_b64_tr_b16 a[152:153], v201 offset:35904\n"
"v_mfma_f32_16x16x32_bf16 v[76:79], a[208:211], v[36:39], v[76:79]\n"
"ds_read_b64_tr_b16 a[154:155], v201 offset:40512\n"
"v_mfma_f32_16x16x32_bf16 v[72:75], a[212:215], v[40:43], v[72:75]\n"
"ds_read_b64_tr_b16 a[156:157], v201 offset:45120\n"
"v_mfma_f32_16x16x32_bf16 v[76:79], a[212:215], v[44:47], v[76:79]\n"
"ds_read_b64_tr_b16 a[158:159], v201 offset:49728\n"
"v_mfma_f32_16x16x32_bf16 v[72:75], a[216:219], v[48:51], v[72:75]\n"
"s_add_u32 m0, s84, 135168\n"
"s_add_u32 s50, s50, 4096\n"
"ds_read_b64_tr_b16 a[192:193], v200 offset:46176\n"
"v_mfma_f32_16x16x32_bf16 v[76:79], a[216:219], v[52:55], v[76:79]\n"
"s_waitcnt lgkmcnt(11)\n"
"ds_read_b64_tr_b16 a[194:195], v200 offset:50784\n"
"buffer_load_dwordx4 v207, s[72:75], s50 offen offset:0 lds\n"
"v_mfma_f32_16x16x32_bf16 v[72:75], a[220:223], v[56:59], v[72:75]\n"
"ds_read_b64_tr_b16 a[196:197], v200 offset:55392\n"
"v_mfma_f32_16x16x32_bf16 v[76:79], a[220:223], v[60:63], v[76:79]\n"
"ds_read_b64_tr_b16 a[198:199], v200 offset:60000\n"
"v_mfma_f32_16x16x32_bf16 v[80:83], a[128:131], v[0:3], v[80:83]\n"
"ds_read_b64_tr_b16 a[200:201], v200 offset:64608\n"
"v_mfma_f32_16x16x32_bf16 v[84:87], a[128:131], v[4:7], v[84:87]\n"
"ds_read_b64_tr_b16 a[202:203], v201 offset:3680\n"
"v_mfma_f32_16x16x32_bf16 v[80:83], a[132:135], v[8:11], v[80:83]\n"
"ds_read_b64_tr_b16 a[204:205], v201 offset:8288\n"
"v_mfma_f32_16x16x32_bf16 v[84:87], a[132:135], v[12:15], v[84:87]\n"
"ds_read_b64_tr_b16 a[206:207], v201 offset:12896\n"
"v_cvt_pk_bf16_f32 v220, v72, v73\n"
"v_mfma_f32_16x16x32_bf16 v[80:83], a[136:139], v[16:19], v[80:83]\n"
"ds_read_b64_tr_b16 a[208:209], v201 offset:17504\n"
"v_cvt_pk_bf16_f32 v221, v74, v75\n"
"v_mfma_f32_16x16x32_bf16 v[84:87], a[136:139], v[20:23], v[84:87]\n"
"s_waitcnt lgkmcnt(11)\n"
"ds_read_b64_tr_b16 a[210:211], v201 offset:22112\n"
"v_cvt_pk_bf16_f32 v224, v76, v77\n"
"v_mfma_f32_16x16x32_bf16 v[80:83], a[140:143], v[24:27], v[80:83]\n"
"s_add_u32 m0, s84, 135168\n"
"ds_read_b64_tr_b16 a[212:213], v201 offset:26720\n"
"v_cvt_pk_bf16_f32 v225, v78, v79\n"
"v_mfma_f32_16x16x32_bf16 v[84:87], a[140:143], v[28:31], v[84:87]\n"
"ds_read_b64_tr_b16 a[214:215], v201 offset:31328\n"
"buffer_load_dwordx4 v207, s[72:75], s50 offen offset:1024 lds\n"
"v_mfma_f32_16x16x32_bf16 v[80:83], a[144:147], v[32:35], v[80:83]\n"
"ds_read_b64_tr_b16 a[216:217], v201 offset:35936\n"
"buffer_store_dwordx4 v[218:221], v207, s[28:31], s54 offen offset:0 sc1\n"
"v_mfma_f32_16x16x32_bf16 v[84:87], a[144:147], v[36:39], v[84:87]\n"
"ds_read_b64_tr_b16 a[218:219], v201 offset:40544\n"
"v_mfma_f32_16x16x32_bf16 v[80:83], a[148:151], v[40:43], v[80:83]\n"
"ds_read_b64_tr_b16 a[220:221], v201 offset:45152\n"
"buffer_store_dwordx4 v[222:225], v208, s[28:31], s54 offen offset:0 sc1\n"
"v_mfma_f32_16x16x32_bf16 v[84:87], a[148:151], v[44:47], v[84:87]\n"
"ds_read_b64_tr_b16 a[222:223], v201 offset:49760\n"
"v_mfma_f32_16x16x32_bf16 v[80:83], a[152:155], v[48:51], v[80:83]\n"
"s_add_u32 m0, s84, 135168\n"
"ds_read_b64_tr_b16 a[128:129], v200 offset:46208\n"
"v_mfma_f32_16x16x32_bf16 v[84:87], a[152:155], v[52:55], v[84:87]\n"
"s_waitcnt lgkmcnt(11)\n"
"ds_read_b64_tr_b16 a[130:131], v200 offset:50816\n"
"buffer_load_dwordx4 v207, s[72:75], s50 offen offset:2048 lds\n"
"v_mfma_f32_16x16x32_bf16 v[80:83], a[156:159], v[56:59], v[80:83]\n"
"ds_read_b64_tr_b16 a[132:133], v200 offset:55424\n"
"v_mfma_f32_16x16x32_bf16 v[84:87], a[156:159], v[60:63], v[84:87]\n"
"ds_read_b64_tr_b16 a[134:135], v200 offset:60032\n"
"v_mfma_f32_16x16x32_bf16 v[88:91], a[192:195], v[0:3], v[88:91]\n"
"ds_read_b64_tr_b16 a[136:137], v200 offset:64640\n"
"v_mfma_f32_16x16x32_bf16 v[92:95], a[192:195], v[4:7], v[92:95]\n"
"ds_read_b64_tr_b16 a[138:139], v201 offset:3712\n"
"v_mfma_f32_16x16x32_bf16 v[88:91], a[196:199], v[8:11], v[88:91]\n"
"ds_read_b64_tr_b16 a[140:141], v201 offset:8320\n"
"v_mfma_f32_16x16x32_bf16 v[92:95], a[196:199], v[12:15], v[92:95]\n"
"ds_read_b64_tr_b16 a[142:143], v201 offset:12928\n"
"v_cvt_pk_bf16_f32 v218, v80, v81\n"
"v_mfma_f32_16x16x32_bf16 v[88:91], a[200:203], v[16:19], v[88:91]\n"
"ds_read_b64_tr_b16 a[144:145], v201 offset:17536\n"
"v_cvt_pk_bf16_f32 v219, v82, v83\n"
"v_mfma_f32_16x16x32_bf16 v[92:95], a[200:203], v[20:23], v[92:95]\n"
"s_waitcnt lgkmcnt(11)\n"
"ds_read_b64_tr_b16 a[146:147], v201 offset:22144\n"
"v_cvt_pk_bf16_f32 v222, v84, v85\n"
"v_mfma_f32_16x16x32_bf16 v[88:91], a[204:207], v[24:27], v[88:91]\n"
"ds_read_b64_tr_b16 a[148:149], v201 offset:26752\n"
"v_cvt_pk_bf16_f32 v223, v86, v87\n"
"v_mfma_f32_16x16x32_bf16 v[92:95], a[204:207], v[28:31], v[92:95]\n"
"ds_read_b64_tr_b16 a[150:151], v201 offset:31360\n"
"v_mfma_f32_16x16x32_bf16 v[88:91], a[208:211], v[32:35], v[88:91]\n"
"ds_read_b64_tr_b16 a[152:153], v201 offset:35968\n"
"v_mfma_f32_16x16x32_bf16 v[92:95], a[208:211], v[36:39], v[92:95]\n"
"ds_read_b64_tr_b16 a[154:155], v201 offset:40576\n"
"v_mfma_f32_16x16x32_bf16 v[88:91], a[212:215], v[40:43], v[88:91]\n"
"ds_read_b64_tr_b16 a[156:157], v201 offset:45184\n"
"v_mfma_f32_16x16x32_bf16 v[92:95], a[212:215], v[44:47], v[92:95]\n"
"ds_read_b64_tr_b16 a[158:159], v201 offset:49792\n"
"v_mfma_f32_16x16x32_bf16 v[88:91], a[216:219], v[48:51], v[88:91]\n"
"ds_read_b64_tr_b16 a[192:193], v200 offset:46240\n"
"v_mfma_f32_16x16x32_bf16 v[92:95], a[216:219], v[52:55], v[92:95]\n"
"s_waitcnt lgkmcnt(11)\n"
"ds_read_b64_tr_b16 a[194:195], v200 offset:50848\n"
"v_mfma_f32_16x16x32_bf16 v[88:91], a[220:223], v[56:59], v[88:91]\n"
"ds_read_b64_tr_b16 a[196:197], v200 offset:55456\n"
"v_mfma_f32_16x16x32_bf16 v[92:95], a[220:223], v[60:63], v[92:95]\n"
"ds_read_b64_tr_b16 a[198:199], v200 offset:60064\n"
"v_mfma_f32_16x16x32_bf16 v[96:99], a[128:131], v[0:3], v[96:99]\n"
"ds_read_b64_tr_b16 a[200:201], v200 offset:64672\n"
"v_mfma_f32_16x16x32_bf16 v[100:103], a[128:131], v[4:7], v[100:103]\n"
"ds_read_b64_tr_b16 a[202:203], v201 offset:3744\n"
"v_mfma_f32_16x16x32_bf16 v[96:99], a[132:135], v[8:11], v[96:99]\n"
"ds_read_b64_tr_b16 a[204:205], v201 offset:8352\n"
"v_mfma_f32_16x16x32_bf16 v[100:103], a[132:135], v[12:15], v[100:103]\n"
"ds_read_b64_tr_b16 a[206:207], v201 offset:12960\n"
"v_cvt_pk_bf16_f32 v220, v88, v89\n"
"v_mfma_f32_16x16x32_bf16 v[96:99], a[136:139], v[16:19], v[96:99]\n"
"ds_read_b64_tr_b16 a[208:209], v201 offset:17568\n"
"v_cvt_pk_bf16_f32 v221, v90, v91\n"
"v_mfma_f32_16x16x32_bf16 v[100:103], a[136:139], v[20:23], v[100:103]\n"
"s_waitcnt lgkmcnt(11)\n"
"ds_read_b64_tr_b16 a[210:211], v201 offset:22176\n"
"v_cvt_pk_bf16_f32 v224, v92, v93\n"
"v_mfma_f32_16x16x32_bf16 v[96:99], a[140:143], v[24:27], v[96:99]\n"
"ds_read_b64_tr_b16 a[212:213], v201 offset:26784\n"
"v_cvt_pk_bf16_f32 v225, v94, v95\n"
"v_mfma_f32_16x16x32_bf16 v[100:103], a[140:143], v[28:31], v[100:103]\n"
"ds_read_b64_tr_b16 a[214:215], v201 offset:31392\n"
"v_mfma_f32_16x16x32_bf16 v[96:99], a[144:147], v[32:35], v[96:99]\n"
"ds_read_b64_tr_b16 a[216:217], v201 offset:36000\n"
"buffer_store_dwordx4 v[218:221], v207, s[28:31], s54 offen offset:1024 sc1\n"
"v_mfma_f32_16x16x32_bf16 v[100:103], a[144:147], v[36:39], v[100:103]\n"
"ds_read_b64_tr_b16 a[218:219], v201 offset:40608\n"
"buffer_store_dword v252, v251, s[28:31], 0 offen sc1\n"
"v_mfma_f32_16x16x32_bf16 v[96:99], a[148:151], v[40:43], v[96:99]\n"
"ds_read_b64_tr_b16 a[220:221], v201 offset:45216\n"
"buffer_store_dwordx4 v[222:225], v208, s[28:31], s54 offen offset:1024 sc1\n"
"v_mfma_f32_16x16x32_bf16 v[100:103], a[148:151], v[44:47], v[100:103]\n"
"ds_read_b64_tr_b16 a[222:223], v201 offset:49824\n"
"v_mfma_f32_16x16x32_bf16 v[96:99], a[152:155], v[48:51], v[96:99]\n"
"ds_read_b64_tr_b16 a[128:129], v200 offset:46272\n"
"v_mfma_f32_16x16x32_bf16 v[100:103], a[152:155], v[52:55], v[100:103]\n"
"s_waitcnt lgkmcnt(11)\n"
"ds_read_b64_tr_b16 a[130:131], v200 offset:50880\n"
"v_mfma_f32_16x16x32_bf16 v[96:99], a[156:159], v[56:59], v[96:99]\n"
"ds_read_b64_tr_b16 a[132:133], v200 offset:55488\n"
"v_mfma_f32_16x16x32_bf16 v[100:103], a[156:159], v[60:63], v[100:103]\n"
"ds_read_b64_tr_b16 a[134:135], v200 offset:60096\n"
"v_mfma_f32_16x16x32_bf16 v[104:107], a[192:195], v[0:3], v[104:107]\n"
"ds_read_b64_tr_b16 a[136:137], v200 offset:64704\n"
"v_mfma_f32_16x16x32_bf16 v[108:111], a[192:195], v[4:7], v[108:111]\n"
"ds_read_b64_tr_b16 a[138:139], v201 offset:3776\n"
"v_mfma_f32_16x16x32_bf16 v[104:107], a[196:199], v[8:11], v[104:107]\n"
"ds_read_b64_tr_b16 a[140:141], v201 offset:8384\n"
"v_mfma_f32_16x16x32_bf16 v[108:111], a[196:199], v[12:15], v[108:111]\n"
"ds_read_b64_tr_b16 a[142:143], v201 offset:12992\n"
"v_cvt_pk_bf16_f32 v218, v96, v97\n"
"v_mfma_f32_16x16x32_bf16 v[104:107], a[200:203], v[16:19], v[104:107]\n"
"ds_read_b64_tr_b16 a[144:145], v201 offset:17600\n"
"v_cvt_pk_bf16_f32 v219, v98, v99\n"
"v_mfma_f32_16x16x32_bf16 v[108:111], a[200:203], v[20:23], v[108:111]\n"
"s_waitcnt lgkmcnt(11)\n"
"ds_read_b64_tr_b16 a[146:147], v201 offset:22208\n"
"v_cvt_pk_bf16_f32 v222, v100, v101\n"
"v_mfma_f32_16x16x32_bf16 v[104:107], a[204:207], v[24:27], v[104:107]\n"
"ds_read_b64_tr_b16 a[148:149], v201 offset:26816\n"
"v_cvt_pk_bf16_f32 v223, v102, v103\n"
"v_mfma_f32_16x16x32_bf16 v[108:111], a[204:207], v[28:31], v[108:111]\n"
"s_waitcnt vmcnt(14)\n"
"ds_read_b64_tr_b16 a[150:151], v201 offset:31424\n"
"v_mfma_f32_16x16x32_bf16 v[104:107], a[208:211], v[32:35], v[104:107]\n"
"s_barrier\n"
"ds_read_b64_tr_b16 a[152:153], v201 offset:36032\n"
"v_mfma_f32_16x16x32_bf16 v[108:111], a[208:211], v[36:39], v[108:111]\n"
"ds_read_b64_tr_b16 a[154:155], v201 offset:40640\n"
"v_mfma_f32_16x16x32_bf16 v[104:107], a[212:215], v[40:43], v[104:107]\n"
"ds_read_b64_tr_b16 a[156:157], v201 offset:45248\n"
"v_mfma_f32_16x16x32_bf16 v[108:111], a[212:215], v[44:47], v[108:111]\n"
"ds_read_b64_tr_b16 a[158:159], v201 offset:49856\n"
"v_mfma_f32_16x16x32_bf16 v[104:107], a[216:219], v[48:51], v[104:107]\n"
"ds_read_b64_tr_b16 a[192:193], v200 offset:46304\n"
"v_mfma_f32_16x16x32_bf16 v[108:111], a[216:219], v[52:55], v[108:111]\n"
"s_waitcnt lgkmcnt(11)\n"
"ds_read_b64_tr_b16 a[194:195], v200 offset:50912\n"
"v_mfma_f32_16x16x32_bf16 v[104:107], a[220:223], v[56:59], v[104:107]\n"
"ds_read_b64_tr_b16 a[196:197], v200 offset:55520\n"
"v_mfma_f32_16x16x32_bf16 v[108:111], a[220:223], v[60:63], v[108:111]\n"
"ds_read_b64_tr_b16 a[198:199], v200 offset:60128\n"
"v_mfma_f32_16x16x32_bf16 v[112:115], a[128:131], v[0:3], v[112:115]\n"
"ds_read_b64_tr_b16 a[200:201], v200 offset:64736\n"
"v_mfma_f32_16x16x32_bf16 v[116:119], a[128:131], v[4:7], v[116:119]\n"
"ds_read_b64_tr_b16 a[202:203], v201 offset:3808\n"
"v_mfma_f32_16x16x32_bf16 v[112:115], a[132:135], v[8:11], v[112:115]\n"
"ds_read_b64_tr_b16 a[204:205], v201 offset:8416\n"
"v_mfma_f32_16x16x32_bf16 v[116:119], a[132:135], v[12:15], v[116:119]\n"
"ds_read_b64_tr_b16 a[206:207], v201 offset:13024\n"
"v_cvt_pk_bf16_f32 v220, v104, v105\n"
"v_mfma_f32_16x16x32_bf16 v[112:115], a[136:139], v[16:19], v[112:115]\n"
"ds_read_b64_tr_b16 a[208:209], v201 offset:17632\n"
"v_cvt_pk_bf16_f32 v221, v106, v107\n"
"v_mfma_f32_16x16x32_bf16 v[116:119], a[136:139], v[20:23], v[116:119]\n"
"s_waitcnt lgkmcnt(11)\n"
"ds_read_b64_tr_b16 a[210:211], v201 offset:22240\n"
"v_cvt_pk_bf16_f32 v224, v108, v109\n"
"v_mfma_f32_16x16x32_bf16 v[112:115], a[140:143], v[24:27], v[112:115]\n"
"ds_read_b64_tr_b16 a[212:213], v201 offset:26848\n"
"v_cvt_pk_bf16_f32 v225, v110, v111\n"
"v_mfma_f32_16x16x32_bf16 v[116:119], a[140:143], v[28:31], v[116:119]\n"
"ds_read_b64_tr_b16 a[214:215], v201 offset:31456\n"
"v_mfma_f32_16x16x32_bf16 v[112:115], a[144:147], v[32:35], v[112:115]\n"
"ds_read_b64_tr_b16 a[216:217], v201 offset:36064\n"
"buffer_store_dwordx4 v[218:221], v207, s[28:31], s54 offen offset:2048 sc1\n"
"v_mfma_f32_16x16x32_bf16 v[116:119], a[144:147], v[36:39], v[116:119]\n"
"ds_read_b64_tr_b16 a[218:219], v201 offset:40672\n"
"v_mfma_f32_16x16x32_bf16 v[112:115], a[148:151], v[40:43], v[112:115]\n"
"ds_read_b64_tr_b16 a[220:221], v201 offset:45280\n"
"buffer_store_dwordx4 v[222:225], v208, s[28:31], s54 offen offset:2048 sc1\n"
"v_mfma_f32_16x16x32_bf16 v[116:119], a[148:151], v[44:47], v[116:119]\n"
"ds_read_b64_tr_b16 a[222:223], v201 offset:49888\n"
"v_mfma_f32_16x16x32_bf16 v[112:115], a[152:155], v[48:51], v[112:115]\n"
"s_nop 0\n"
"v_mfma_f32_16x16x32_bf16 v[116:119], a[152:155], v[52:55], v[116:119]\n"
"s_waitcnt lgkmcnt(10)\n"
"v_mfma_f32_16x16x32_bf16 v[112:115], a[156:159], v[56:59], v[112:115]\n"
"s_nop 0\n"
"v_mfma_f32_16x16x32_bf16 v[116:119], a[156:159], v[60:63], v[116:119]\n"
"s_nop 0\n"
"v_mfma_f32_16x16x32_bf16 v[120:123], a[192:195], v[0:3], v[120:123]\n"
"s_nop 0\n"
"v_mfma_f32_16x16x32_bf16 v[124:127], a[192:195], v[4:7], v[124:127]\n"
"s_nop 0\n"
"v_mfma_f32_16x16x32_bf16 v[120:123], a[196:199], v[8:11], v[120:123]\n"
"s_nop 0\n"
"v_mfma_f32_16x16x32_bf16 v[124:127], a[196:199], v[12:15], v[124:127]\n"
"v_cvt_pk_bf16_f32 v218, v112, v113\n"
"v_mfma_f32_16x16x32_bf16 v[120:123], a[200:203], v[16:19], v[120:123]\n"
"v_cvt_pk_bf16_f32 v219, v114, v115\n"
"v_mfma_f32_16x16x32_bf16 v[124:127], a[200:203], v[20:23], v[124:127]\n"
"s_waitcnt lgkmcnt(2)\n"
"v_cvt_pk_bf16_f32 v222, v116, v117\n"
"v_mfma_f32_16x16x32_bf16 v[120:123], a[204:207], v[24:27], v[120:123]\n"
"v_cvt_pk_bf16_f32 v223, v118, v119\n"
"v_mfma_f32_16x16x32_bf16 v[124:127], a[204:207], v[28:31], v[124:127]\n"
"s_nop 0\n"
"v_mfma_f32_16x16x32_bf16 v[120:123], a[208:211], v[32:35], v[120:123]\n"
"s_nop 0\n"
"v_mfma_f32_16x16x32_bf16 v[124:127], a[208:211], v[36:39], v[124:127]\n"
"s_nop 0\n"
"v_mfma_f32_16x16x32_bf16 v[120:123], a[212:215], v[40:43], v[120:123]\n"
"s_nop 0\n"
"v_mfma_f32_16x16x32_bf16 v[124:127], a[212:215], v[44:47], v[124:127]\n"
"s_nop 0\n"
"v_mfma_f32_16x16x32_bf16 v[120:123], a[216:219], v[48:51], v[120:123]\n"
"s_nop 0\n"
"v_mfma_f32_16x16x32_bf16 v[124:127], a[216:219], v[52:55], v[124:127]\n"
"s_waitcnt lgkmcnt(0)\n"
"v_mfma_f32_16x16x32_bf16 v[120:123], a[220:223], v[56:59], v[120:123]\n"
"s_nop 0\n"
"v_mfma_f32_16x16x32_bf16 v[124:127], a[220:223], v[60:63], v[124:127]\n"
"s_nop 0\n"
"s_nop 1\n"
"s_nop 1\n"
"s_nop 1\n"
"s_nop 1\n"
"v_cvt_pk_bf16_f32 v220, v120, v121\n"
"s_nop 1\n"
"v_cvt_pk_bf16_f32 v221, v122, v123\n"
"s_nop 1\n"
"v_cvt_pk_bf16_f32 v224, v124, v125\n"
"s_nop 1\n"
"v_cvt_pk_bf16_f32 v225, v126, v127\n"
"s_nop 1\n"
"s_nop 1\n"
"buffer_store_dwordx4 v[218:221], v207, s[28:31], s54 offen offset:3072 sc1\n"
"s_nop 1\n"
"s_nop 1\n"
"buffer_store_dwordx4 v[222:225], v208, s[28:31], s54 offen offset:3072 sc1\n"
"s_nop 1\n"
"s_add_u32 s56, s40, 161024\n"
"s_nop 1\n"
"v_mov_b32 v227, s56\n"
"v_mov_b32 v224, 0x3f80\n"
"v_mov_b32 v225, 0x3f800000\n"
"v_and_b32 v226, 7, v248\n"
"v_lshl_add_u32 v226, v226, 4, v227\n"
"s_mov_b32 s14, 0xfffff800\n"
"s_cmp_lg_u32 s41, 3\n"
"s_cbranch_scc1 AT_A0\n"
"s_mov_b64 s[8:9], 0\n"
"s_mov_b64 exec, 0\n"
"s_call_b64 %[kp], AT_pg17\n"
"s_mov_b64 exec, -1\n"
"s_mul_i32 s63, s41, 9216\n"
"s_add_u32 s63, s63, s40\n"
"s_add_u32 s63, s63, 9216\n"
"v_lshrrev_b32 v185, 2, v248\n"
"v_and_b32 v186, 3, v248\n"
"v_mul_u32_u24 v187, 96, v185\n"
"v_lshl_add_u32 v188, v186, 4, s63\n"
"v_lshl_add_u32 v189, v187, 1, v188\n"
"v_add_u32 v188, v187, v188\n"
"v_lshrrev_b32 v190, 4, v248\n"
"v_mul_u32_u24 v191, 384, v190\n"
"v_add_u32 v187, v187, v191\n"
"v_lshl_add_u32 v187, v186, 3, v187\n"
"v_add_u32 v187, s63, v187\n"
"s_mov_b32 exec_lo, 0x77777777\n"
"s_mov_b32 exec_hi, 0x77777777\n"
"ds_write_b128 v189, a[48:51] offset:0\n"
"ds_write_b128 v189, a[84:87] offset:48\n"
"ds_write_b128 v189, a[52:55] offset:96\n"
"ds_write_b128 v189, a[88:91] offset:144\n"
"ds_write_b128 v189, a[56:59] offset:3072\n"
"ds_write_b128 v189, a[92:95] offset:3120\n"
"ds_write_b128 v189, a[60:63] offset:3168\n"
"ds_write_b128 v189, a[96:99] offset:3216\n"
"ds_write_b128 v189, a[64:67] offset:6144\n"
"ds_write_b128 v189, a[100:103] offset:6192\n"
"ds_write_b128 v189, a[68:71] offset:6240\n"
"ds_write_b128 v189, a[104:107] offset:6288\n"
"s_mov_b64 exec, -1\n"
"AT_pg16:\n"
"s_cbranch_execz AT_hr\n"
"ds_read_b64_tr_b16 a[128:129], v187 offset:0\n"
"ds_read_b64_tr_b16 a[130:131], v187 offset:384\n"
"ds_read_b64_tr_b16 a[132:133], v187 offset:32\n"
"ds_read_b64_tr_b16 a[134:135], v187 offset:416\n"
"ds_read_b64_tr_b16 a[136:137], v187 offset:64\n"
"ds_read_b64_tr_b16 a[138:139], v187 offset:448\n"
"ds_read_b64_tr_b16 a[140:141], v187 offset:3072\n"
"ds_read_b64_tr_b16 a[142:143], v187 offset:3456\n"
"ds_read_b64_tr_b16 a[144:145], v187 offset:3104\n"
"ds_read_b64_tr_b16 a[146:147], v187 offset:3488\n"
"ds_read_b64_tr_b16 a[148:149], v187 offset:3136\n"
"ds_read_b64_tr_b16 a[150:151], v187 offset:3520\n"
"ds_read_b64_tr_b16 a[152:153], v187 offset:6144\n"
"ds_read_b64_tr_b16 a[154:155], v187 offset:6528\n"
"ds_read_b64_tr_b16 a[156:157], v187 offset:6176\n"
"ds_read_b64_tr_b16 a[158:159], v187 offset:6560\n"
"ds_read_b64_tr_b16 a[160:161], v187 offset:6208\n"
"ds_read_b64_tr_b16 a[162:163], v187 offset:6592\n"
"s_mov_b32 exec_lo, 0x77777777\n"
"s_mov_b32 exec_hi, 0x77777777\n"
"ds_write_b128 v189, a[72:75] offset:0\n"
"ds_write_b128 v189, a[108:111] offset:48\n"
"ds_write_b128 v189, a[76:79] offset:96\n"
"ds_write_b128 v189, a[112:115] offset:144\n"
"ds_write_b128 v188, a[80:83] offset:3072\n"
"ds_write_b128 v188, a[116:119] offset:3120\n"
"s_mov_b64 exec, -1\n"
"ds_read_b64_tr_b16 a[164:165], v187 offset:0\n"
"ds_read_b64_tr_b16 a[166:167], v187 offset:384\n"
"ds_read_b64_tr_b16 a[168:169], v187 offset:32\n"
"ds_read_b64_tr_b16 a[170:171], v187 offset:416\n"
"ds_read_b64_tr_b16 a[172:173], v187 offset:64\n"
"ds_read_b64_tr_b16 a[174:175], v187 offset:448\n"
"ds_read_b64_tr_b16 a[176:177], v187 offset:3072\n"
"ds_read_b64_tr_b16 a[178:179], v187 offset:3456\n"
"ds_read_b64_tr_b16 a[180:181], v187 offset:3104\n"
"ds_read_b64_tr_b16 a[182:183], v187 offset:3488\n"
"ds_read_b64_tr_b16 a[184:185], v187 offset:3136\n"
"ds_read_b64_tr_b16 a[186:187], v187 offset:3520\n"
"v_and_b32 v241, 1, v248\n"
"v_mul_u32_u24 v241, 1152, v241\n"
"v_lshl_add_u32 v241, v190, 4, v241\n"
"s_mul_i32 s63, s41, 288\n"
"v_add_u32 v241, s63, v241\n"
"v_and_b32 v242, 10, v248\n"
"v_lshl_add_u32 v241, v242, 26, v241\n"
"v_and_b32 v242, 32, v248\n"
"v_lshlrev_b32 v242, 25, v242\n"
"s_add_u32 s63, s63, s40\n"
"s_add_u32 s63, s63, 131072\n"
"v_cmp_eq_u32 vcc, 3, v190\n"
"v_and_b32 v185, 3, v185\n"
"v_lshlrev_b32 v191, 3, v190\n"
"v_cndmask_b32_e64 v191, v191, 0, vcc\n"
"v_add_u32 v191, v191, v185\n"
"v_mul_u32_u24 v191, 1152, v191\n"
"v_lshl_add_u32 v191, v186, 3, v191\n"
"v_add_u32 v255, s63, v191\n"
"v_lshrrev_b32 v243, 3, v248\n"
"v_mul_u32_u24 v243, 24576, v243\n"
"v_and_b32 v247, 7, v248\n"
"v_lshl_add_u32 v243, v247, 4, v243\n"
"v_mul_u32_u24 v224, 24576, v190\n"
"v_and_b32 v246, 15, v248\n"
"v_lshl_add_u32 v224, v246, 3, v224\n"
"v_or_b32 v224, v242, v224\n"
"v_subrev_u32 v252, 65536, v227\n"
"s_mul_i32 s63, s41, 384\n"
"v_mul_u32_u24 v244, 192, v190\n"
"v_lshl_add_u32 v244, v246, 2, v244\n"
"v_add3_u32 v244, v244, v252, s63\n"
"v_cmp_lt_u32 vcc, 23, v248\n"
"v_lshl_add_u32 v249, v248, 2, v252\n"
"v_add_u32 v185, 96, v249\n"
"v_cndmask_b32 v249, v249, v185, vcc\n"
"s_lshl_b32 s63, s41, 9\n"
"v_add_u32 v252, s63, v252\n"
"v_lshl_add_u32 v251, v248, 1, v252\n"
"v_add_u32 v185, 208, v251\n"
"v_cndmask_b32 v251, v251, v185, vcc\n"
"v_lshl_add_u32 v185, v248, 3, v252\n"
"v_lshl_add_u32 v252, v247, 6, v252\n"
"v_lshl_add_u32 v252, v190, 4, v252\n"
"s_min_u32 s63, s41, 2\n"
"s_mul_i32 s63, s63, 192\n"
"v_add_u32 v183, s63, v248\n"
"v_mul_u32_u24 v183, 911, v183\n"
"s_lshl_b32 s63, s55, 3\n"
"v_lshrrev_b32 v180, 16, v183\n"
"v_add_u32 v180, s63, v180\n"
"v_add_u32 v183, 58304, v183\n"
"v_lshrrev_b32 v181, 16, v183\n"
"v_add_u32 v181, s63, v181\n"
"v_add_u32 v183, 58304, v183\n"
"v_lshrrev_b32 v182, 16, v183\n"
"v_add_u32 v182, s63, v182\n"
"v_lshlrev_b32 v250, 4, v248\n"
"v_add_u32 v184, s45, v250\n"
"s_cmp_eq_u32 s41, 3\n"
"s_cselect_b32 s63, 0x10000000, 0\n"
"v_or_b32 v250, s63, v250\n"
"ds_read_b128 v[212:215], v227 offset:512\n"
"ds_read_b128 v[216:219], v227 offset:528\n"
"s_lshl_b32 s56, s55, 3\n"
"s_sub_u32 s56, 2047, s56\n"
"v_and_b32 v220, 7, v248\n"
"v_sub_u32 v220, s56, v220\n"
"s_waitcnt lgkmcnt(0)\n"
"v_add_f32 v212, v212, v214\n"
"v_add_f32 v213, v213, v215\n"
"v_add_f32 v212, v212, v216\n"
"v_add_f32 v213, v213, v217\n"
"v_mov_b32 v145, s85\n"
"v_mov_b32 v147, s85\n"
"s_mul_i32 s56, s55, 64\n"
"s_add_u32 s56, s56, 28745728\n"
"v_mov_b32 v148, 0\n"
"v_mul_u32_u24 v149, 64, v248\n"
"s_mov_b32 s57, 28745728\n"
"s_mov_b32 s58, 28749824\n"
"s_mov_b32 s59, 28753920\n"
"s_mov_b32 s60, 28758016\n"
"s_mov_b32 s87, 0x80000\n"
"s_waitcnt vmcnt(0)\n"
"s_barrier\n"
"ds_read_b128 v[200:203], v226 offset:0\n"
"ds_read_b128 v[204:207], v226 offset:128\n"
"ds_read_b128 v[208:211], v226 offset:256\n"
"s_waitcnt lgkmcnt(1)\n"
"v_add_f32 v200, v200, v204\n"
"v_add_f32 v201, v201, v205\n"
"v_add_f32 v202, v202, v206\n"
"s_waitcnt lgkmcnt(0)\n"
"v_add_f32 v200, v200, v208\n"
"v_add_f32 v201, v201, v209\n"
"v_add_f32 v202, v202, v210\n"
"v_mul_f32 v200, 0x3ae38e39, v200\n"
"v_mul_f32 v201, 0x3ae38e39, v201\n"
"v_fma_f32 v201, -v200, v200, v201\n"
"v_max_f32 v201, 0, v201\n"
"v_add_f32 v201, 0x3727c5ac, v201\n"
"v_rsq_f32 v192, v201\n"
"v_fma_f32 v202, -v200, v212, v202\n"
"v_fma_f32 v196, v192, v202, v213\n"
"v_ashrrev_i32 v203, 31, v196\n"
"v_or_b32 v203, 0x80000000, v203\n"
"v_xor_b32 v196, v196, v203\n"
"v_and_or_b32 v196, v196, s14, v220\n"
"v_mul_f32 v194, v200, v192\n"
"v_sub_f32 v194, 0, v194\n"
"v_mov_b32_dpp v203, v196 row_ror:1 row_mask:0xf bank_mask:0xf\n"
"v_min_u32 v198, v196, v203\n"
"v_max_u32 v197, v196, v203\n"
"s_nop 0\n"
"v_mov_b32_dpp v204, v198 row_ror:2 row_mask:0xf bank_mask:0xf\n"
"v_mov_b32_dpp v203, v197 row_ror:2 row_mask:0xf bank_mask:0xf\n"
"v_min_u32 v205, v197, v203\n"
"v_max_u32 v197, v197, v203\n"
"v_max3_u32 v198, v198, v204, v205\n"
"s_nop 0\n"
"v_mov_b32_dpp v203, v197 row_ror:4 row_mask:0xf bank_mask:0xf\n"
"v_mov_b32_dpp v204, v198 row_ror:4 row_mask:0xf bank_mask:0xf\n"
"v_min_u32 v205, v197, v203\n"
"v_max_u32 v197, v197, v203\n"
"v_max3_u32 v198, v198, v204, v205\n"
"v_mov_b32 v144, v197\n"
"v_mov_b32 v146, v198\n"
"s_mov_b64 exec, 1\n"
"buffer_store_dwordx4 v[144:147], v148, s[28:31], s56 offen sc1\n"
"s_mov_b64 exec, -1\n"
"AT_S2:\n"
"buffer_load_dwordx4 v[152:155], v149, s[28:31], s57 offen sc1\n"
"buffer_load_dwordx4 v[156:159], v149, s[28:31], s58 offen sc1\n"
"buffer_load_dwordx4 v[160:163], v149, s[28:31], s59 offen sc1\n"
"buffer_load_dwordx4 v[164:167], v149, s[28:31], s60 offen sc1\n"
"s_waitcnt vmcnt(0)\n"
"v_xor_b32 v150, s85, v153\n"
"v_xor_b32 v151, s85, v155\n"
"v_bitop3_b32 v150, v157, s85, v150 bitop3:0xbe\n"
"v_bitop3_b32 v151, v159, s85, v151 bitop3:0xbe\n"
"v_bitop3_b32 v150, v161, s85, v150 bitop3:0xbe\n"
"v_bitop3_b32 v151, v163, s85, v151 bitop3:0xbe\n"
"v_bitop3_b32 v150, v165, s85, v150 bitop3:0xbe\n"
"v_bitop3_b32 v151, v167, s85, v151 bitop3:0xbe\n"
"v_or_b32 v150, v150, v151\n"
"v_cmp_ne_u32 vcc, 0, v150\n"
"s_nop 0\n"
"s_cbranch_vccz AT_S3\n"
"s_sub_u32 s87, s87, 1\n"
"s_cmp_eq_u32 s87, 0\n"
"s_cbranch_scc1 AT_S3\n"
"s_sleep 1\n"
"s_branch AT_S2\n"
"AT_S3:\n"
"v_min_u32 v205, v152, v156\n"
"v_max_u32 v152, v152, v156\n"
"v_max3_u32 v154, v154, v158, v205\n"
"v_min_u32 v205, v160, v164\n"
"v_max_u32 v160, v160, v164\n"
"v_max3_u32 v162, v162, v166, v205\n"
"v_min_u32 v205, v152, v160\n"
"v_max_u32 v152, v152, v160\n"
"v_max3_u32 v154, v154, v162, v205\n"
"s_nop 0\n"
"v_mov_b32_dpp v156, v152 row_ror:1 row_mask:0xf bank_mask:0xf\n"
"v_mov_b32_dpp v158, v154 row_ror:1 row_mask:0xf bank_mask:0xf\n"
"v_min_u32 v205, v152, v156\n"
"v_max_u32 v152, v152, v156\n"
"v_max3_u32 v154, v154, v158, v205\n"
"s_nop 0\n"
"v_mov_b32_dpp v156, v152 row_ror:2 row_mask:0xf bank_mask:0xf\n"
"v_mov_b32_dpp v158, v154 row_ror:2 row_mask:0xf bank_mask:0xf\n"
"v_min_u32 v205, v152, v156\n"
"v_max_u32 v152, v152, v156\n"
"v_max3_u32 v154, v154, v158, v205\n"
"s_nop 0\n"
"v_mov_b32_dpp v156, v152 row_ror:4 row_mask:0xf bank_mask:0xf\n"
"v_mov_b32_dpp v158, v154 row_ror:4 row_mask:0xf bank_mask:0xf\n"
"v_min_u32 v205, v152, v156\n"
"v_max_u32 v152, v152, v156\n"
"v_max3_u32 v154, v154, v158, v205\n"
"s_nop 0\n"
"v_mov_b32_dpp v156, v152 row_ror:8 row_mask:0xf bank_mask:0xf\n"
"v_mov_b32_dpp v158, v154 row_ror:8 row_mask:0xf bank_mask:0xf\n"
"v_min_u32 v205, v152, v156\n"
"v_max_u32 v152, v152, v156\n"
"v_max3_u32 v154, v154, v158, v205\n"
"v_mov_b32 v156, v152\n"
"v_mov_b32 v158, v154\n"
"s_nop 0\n"
"v_permlane16_swap_b32 v152, v156\n"
"v_permlane16_swap_b32 v154, v158\n"
"v_min_u32 v205, v152, v156\n"
"v_max_u32 v152, v152, v156\n"
"v_max3_u32 v154, v154, v158, v205\n"
"v_mov_b32 v156, v152\n"
"v_mov_b32 v158, v154\n"
"s_nop 0\n"
"v_permlane32_swap_b32 v152, v156\n"
"v_permlane32_swap_b32 v154, v158\n"
"v_min_u32 v205, v152, v156\n"
"v_max_u32 v152, v152, v156\n"
"v_max3_u32 v154, v154, v158, v205\n"
"v_mov_b32 v153, v154\n"
"s_mov_b64 exec, 1\n"
"ds_write_b64 v227, v[152:153] offset:656\n"
"s_mov_b64 exec, -1\n"
"s_branch AT_B3\n"
"AT_A0:\n"
"AT_pgA0:\n"
"s_cbranch_execz AT_hr\n"
"v_and_b32 v192, 15, v248\n"
"v_lshlrev_b32 v192, 6, v192\n"
"s_lshl_b32 s57, s44, 10\n"
"buffer_load_dword v193, v192, s[28:31], s57 offen sc1\n"
"ds_read_b64 v[228:229], v240 offset:0\n"
"ds_read_b64 v[230:231], v240 offset:32\n"
"ds_read_b64 v[232:233], v240 offset:384\n"
"ds_read_b64 v[234:235], v240 offset:416\n"
"ds_read_b64 v[236:237], v240 offset:768\n"
"ds_read_b64 v[238:239], v240 offset:800\n"
"ds_read_b128 v[96:99], v244 offset:0\n"
"ds_read_b128 v[100:103], v244 offset:64\n"
"ds_read_b128 v[104:107], v244 offset:768\n"
"ds_read_b128 v[108:111], v244 offset:832\n"
"ds_read_b128 v[112:115], v244 offset:1536\n"
"ds_read_b128 v[116:119], v244 offset:1600\n"
"ds_read_b128 v[0:3], v244 offset:2304\n"
"ds_read_b128 v[4:7], v244 offset:2368\n"
"ds_read_b128 v[8:11], v244 offset:3072\n"
"ds_read_b128 v[12:15], v244 offset:3136\n"
"ds_read_b128 v[16:19], v244 offset:3840\n"
"ds_read_b128 v[20:23], v244 offset:3904\n"
"v_mov_b32 v48, 0\n"
"v_mov_b32 v49, 0\n"
"v_mov_b32 v50, 0\n"
"v_mov_b32 v51, 0\n"
"v_mov_b32 v52, 0\n"
"v_mov_b32 v53, 0\n"
"v_mov_b32 v54, 0\n"
"v_mov_b32 v55, 0\n"
"v_mov_b32 v56, 0\n"
"v_mov_b32 v57, 0\n"
"v_mov_b32 v58, 0\n"
"v_mov_b32 v59, 0\n"
"v_mov_b32 v60, 0\n"
"v_mov_b32 v61, 0\n"
"v_mov_b32 v62, 0\n"
"v_mov_b32 v63, 0\n"
"v_mov_b32 v64, 0\n"
"v_mov_b32 v65, 0\n"
"v_mov_b32 v66, 0\n"
"v_mov_b32 v67, 0\n"
"v_mov_b32 v68, 0\n"
"v_mov_b32 v69, 0\n"
"v_mov_b32 v70, 0\n"
"v_mov_b32 v71, 0\n"
"v_mov_b32 v72, 0\n"
"v_mov_b32 v73, 0\n"
"v_mov_b32 v74, 0\n"
"v_mov_b32 v75, 0\n"
"v_mov_b32 v76, 0\n"
"v_mov_b32 v77, 0\n"
"v_mov_b32 v78, 0\n"
"v_mov_b32 v79, 0\n"
"v_mov_b32 v80, 0\n"
"v_mov_b32 v81, 0\n"
"v_mov_b32 v82, 0\n"
"v_mov_b32 v83, 0\n"
"v_mov_b32 v84, 0\n"
"v_mov_b32 v85, 0\n"
"v_mov_b32 v86, 0\n"
"v_mov_b32 v87, 0\n"
"v_mov_b32 v88, 0\n"
"v_mov_b32 v89, 0\n"
"v_mov_b32 v90, 0\n"
"v_mov_b32 v91, 0\n"
"v_mov_b32 v92, 0\n"
"v_mov_b32 v93, 0\n"
"v_mov_b32 v94, 0\n"
"v_mov_b32 v95, 0\n"
"ds_read_b128 v[212:215], v227 offset:512\n"
"ds_read_b128 v[216:219], v227 offset:528\n"
"s_lshl_b32 s56, s55, 3\n"
"s_sub_u32 s56, 2047, s56\n"
"v_and_b32 v220, 7, v248\n"
"v_sub_u32 v220, s56, v220\n"
"s_waitcnt lgkmcnt(0)\n"
"v_add_f32 v212, v212, v214\n"
"v_add_f32 v213, v213, v215\n"
"v_add_f32 v212, v212, v216\n"
"v_add_f32 v213, v213, v217\n"
"s_mul_i32 s56, s41, 1536\n"
"s_add_u32 s56, s56, s40\n"
"s_add_u32 s56, s56, 49152\n"
"v_and_b32 v176, 32, v248\n"
"v_lshl_add_u32 v176, v176, 2, s56\n"
"v_bfe_u32 v177, v248, 3, 2\n"
"v_lshl_add_u32 v176, v177, 4, v176\n"
"v_lshl_add_u32 v177, v248, 5, s56\n"
"v_lshrrev_b32 v178, 3, v248\n"
"v_mul_u32_u24 v179, 43, v178\n"
"v_lshrrev_b32 v179, 7, v179\n"
"v_mul_u32_u24 v195, 3, v179\n"
"v_sub_u32 v178, v178, v195\n"
"v_mul_u32_u24 v178, 384, v178\n"
"v_and_b32 v195, 7, v248\n"
"v_lshl_add_u32 v178, v195, 4, v178\n"
"v_and_b32 v179, 2, v179\n"
"v_lshl_add_u32 v178, v179, 27, v178\n"
"s_lshl_b32 s56, s41, 7\n"
"v_add_u32 v178, s56, v178\n"
"s_mov_b32 s52, 0\n"
"s_mov_b32 s87, 0x80000\n"
"v_lshlrev_b32 v24, 16, v228\n"
"v_and_b32 v25, 0xffff0000, v228\n"
"v_lshlrev_b32 v26, 16, v229\n"
"v_and_b32 v27, 0xffff0000, v229\n"
"v_lshlrev_b32 v28, 16, v230\n"
"v_and_b32 v29, 0xffff0000, v230\n"
"v_lshlrev_b32 v30, 16, v231\n"
"v_and_b32 v31, 0xffff0000, v231\n"
"v_lshlrev_b32 v32, 16, v232\n"
"v_and_b32 v33, 0xffff0000, v232\n"
"v_lshlrev_b32 v34, 16, v233\n"
"v_and_b32 v35, 0xffff0000, v233\n"
"v_lshlrev_b32 v36, 16, v234\n"
"v_and_b32 v37, 0xffff0000, v234\n"
"v_lshlrev_b32 v38, 16, v235\n"
"v_and_b32 v39, 0xffff0000, v235\n"
"v_lshlrev_b32 v40, 16, v236\n"
"v_and_b32 v41, 0xffff0000, v236\n"
"v_lshlrev_b32 v42, 16, v237\n"
"v_and_b32 v43, 0xffff0000, v237\n"
"v_lshlrev_b32 v44, 16, v238\n"
"v_and_b32 v45, 0xffff0000, v238\n"
"v_lshlrev_b32 v46, 16, v239\n"
"v_and_b32 v47, 0xffff0000, v239\n"
"s_mul_i32 s63, s41, 9216\n"
"s_add_u32 s63, s63, s40\n"
"s_add_u32 s63, s63, 9216\n"
"v_lshrrev_b32 v185, 2, v248\n"
"v_and_b32 v186, 3, v248\n"
"v_mul_u32_u24 v187, 96, v185\n"
"v_lshl_add_u32 v188, v186, 4, s63\n"
"v_lshl_add_u32 v189, v187, 1, v188\n"
"v_add_u32 v188, v187, v188\n"
"v_lshrrev_b32 v190, 4, v248\n"
"v_mul_u32_u24 v191, 384, v190\n"
"v_add_u32 v187, v187, v191\n"
"v_lshl_add_u32 v187, v186, 3, v187\n"
"v_add_u32 v187, s63, v187\n"
"s_mov_b32 exec_lo, 0x77777777\n"
"s_mov_b32 exec_hi, 0x77777777\n"
"ds_write_b128 v189, a[48:51] offset:0\n"
"ds_write_b128 v189, a[84:87] offset:48\n"
"ds_write_b128 v189, a[52:55] offset:96\n"
"ds_write_b128 v189, a[88:91] offset:144\n"
"ds_write_b128 v189, a[56:59] offset:3072\n"
"ds_write_b128 v189, a[92:95] offset:3120\n"
"ds_write_b128 v189, a[60:63] offset:3168\n"
"ds_write_b128 v189, a[96:99] offset:3216\n"
"ds_write_b128 v189, a[64:67] offset:6144\n"
"ds_write_b128 v189, a[100:103] offset:6192\n"
"ds_write_b128 v189, a[68:71] offset:6240\n"
"ds_write_b128 v189, a[104:107] offset:6288\n"
"s_mov_b64 exec, -1\n"
"ds_read_b64_tr_b16 a[128:129], v187 offset:0\n"
"ds_read_b64_tr_b16 a[130:131], v187 offset:384\n"
"ds_read_b64_tr_b16 a[132:133], v187 offset:32\n"
"ds_read_b64_tr_b16 a[134:135], v187 offset:416\n"
"ds_read_b64_tr_b16 a[136:137], v187 offset:64\n"
"ds_read_b64_tr_b16 a[138:139], v187 offset:448\n"
"ds_read_b64_tr_b16 a[140:141], v187 offset:3072\n"
"ds_read_b64_tr_b16 a[142:143], v187 offset:3456\n"
"ds_read_b64_tr_b16 a[144:145], v187 offset:3104\n"
"ds_read_b64_tr_b16 a[146:147], v187 offset:3488\n"
"ds_read_b64_tr_b16 a[148:149], v187 offset:3136\n"
"ds_read_b64_tr_b16 a[150:151], v187 offset:3520\n"
"ds_read_b64_tr_b16 a[152:153], v187 offset:6144\n"
"ds_read_b64_tr_b16 a[154:155], v187 offset:6528\n"
"ds_read_b64_tr_b16 a[156:157], v187 offset:6176\n"
"ds_read_b64_tr_b16 a[158:159], v187 offset:6560\n"
"ds_read_b64_tr_b16 a[160:161], v187 offset:6208\n"
"ds_read_b64_tr_b16 a[162:163], v187 offset:6592\n"
"s_mov_b32 s10, 0\n"
"s_waitcnt vmcnt(0)\n"
"AT_A1:\n"
"v_cmp_eq_u32 vcc, s85, v193\n"
"s_sub_u32 s87, s87, 1\n"
"s_lshr_b32 s53, vcc_lo, 1\n"
"s_and_b32 s53, s53, vcc_lo\n"
"s_lshr_b32 s59, s53, 2\n"
"s_and_b32 s53, s53, s59\n"
"s_and_b32 s53, s53, 0x1111\n"
"s_cmp_lt_i32 s87, 0\n"
"s_cselect_b32 s53, 0x1111, s53\n"
"s_andn2_b32 s53, s53, s52\n"
"s_cmp_lg_u32 s53, 0\n"
"s_cbranch_scc1 AT_A2\n"
"s_sleep 2\n"
"AT_A1b:\n"
"buffer_load_dword v193, v192, s[28:31], s57 offen sc1\n"
"s_waitcnt vmcnt(0)\n"
"s_branch AT_A1\n"
"AT_A2:\n"
"s_bcnt1_i32_b32 s54, s53\n"
"s_bitcmp1_b32 s53, 0\n"
"s_cbranch_scc0 AT_Ai0\n"
"s_add_u32 s56, s47, 0\n"
"buffer_load_dwordx4 v[128:131], v241, s[28:31], s56 offen sc1\n"
"buffer_load_dwordx4 v[132:135], v242, s[28:31], s56 offen sc1\n"
"buffer_load_dwordx4 v[136:139], v243, s[28:31], s56 offen sc1\n"
"AT_Ai0:\n"
"s_bitcmp1_b32 s53, 4\n"
"s_cbranch_scc0 AT_Ai1\n"
"s_add_u32 s56, s47, 2359296\n"
"buffer_load_dwordx4 v[140:143], v241, s[28:31], s56 offen sc1\n"
"buffer_load_dwordx4 v[144:147], v242, s[28:31], s56 offen sc1\n"
"buffer_load_dwordx4 v[148:151], v243, s[28:31], s56 offen sc1\n"
"AT_Ai1:\n"
"s_bitcmp1_b32 s53, 8\n"
"s_cbranch_scc0 AT_Ai2\n"
"s_add_u32 s56, s47, 4718592\n"
"buffer_load_dwordx4 v[152:155], v241, s[28:31], s56 offen sc1\n"
"buffer_load_dwordx4 v[156:159], v242, s[28:31], s56 offen sc1\n"
"buffer_load_dwordx4 v[160:163], v243, s[28:31], s56 offen sc1\n"
"AT_Ai2:\n"
"s_bitcmp1_b32 s53, 12\n"
"s_cbranch_scc0 AT_Ai3\n"
"s_add_u32 s56, s47, 7077888\n"
"buffer_load_dwordx4 v[164:167], v241, s[28:31], s56 offen sc1\n"
"buffer_load_dwordx4 v[168:171], v242, s[28:31], s56 offen sc1\n"
"buffer_load_dwordx4 v[172:175], v243, s[28:31], s56 offen sc1\n"
"AT_Ai3:\n"
"s_cmp_lg_u32 s10, 0\n"
"s_cbranch_scc1 AT_Au\n"
"s_mov_b32 s10, 1\n"
"s_mov_b32 exec_lo, 0x77777777\n"
"s_mov_b32 exec_hi, 0x77777777\n"
"ds_write_b128 v189, a[72:75] offset:0\n"
"ds_write_b128 v189, a[108:111] offset:48\n"
"ds_write_b128 v189, a[76:79] offset:96\n"
"ds_write_b128 v189, a[112:115] offset:144\n"
"ds_write_b128 v188, a[80:83] offset:3072\n"
"ds_write_b128 v188, a[116:119] offset:3120\n"
"s_mov_b64 exec, -1\n"
"ds_read_b64_tr_b16 a[164:165], v187 offset:0\n"
"ds_read_b64_tr_b16 a[166:167], v187 offset:384\n"
"ds_read_b64_tr_b16 a[168:169], v187 offset:32\n"
"ds_read_b64_tr_b16 a[170:171], v187 offset:416\n"
"ds_read_b64_tr_b16 a[172:173], v187 offset:64\n"
"ds_read_b64_tr_b16 a[174:175], v187 offset:448\n"
"ds_read_b64_tr_b16 a[176:177], v187 offset:3072\n"
"ds_read_b64_tr_b16 a[178:179], v187 offset:3456\n"
"ds_read_b64_tr_b16 a[180:181], v187 offset:3104\n"
"ds_read_b64_tr_b16 a[182:183], v187 offset:3488\n"
"ds_read_b64_tr_b16 a[184:185], v187 offset:3136\n"
"ds_read_b64_tr_b16 a[186:187], v187 offset:3520\n"
"AT_Au:\n"
"s_bitcmp1_b32 s53, 0\n"
"s_cbranch_scc0 AT_Ar0\n"
"s_sub_u32 s54, s54, 1\n"
"s_cmp_eq_u32 s54, 0\n"
"s_cbranch_scc1 AT_Aw0_0\n"
"s_cmp_eq_u32 s54, 1\n"
"s_cbranch_scc1 AT_Aw0_1\n"
"s_cmp_eq_u32 s54, 2\n"
"s_cbranch_scc1 AT_Aw0_2\n"
"s_waitcnt vmcnt(9)\n"
"s_branch AT_Ax0\n"
"AT_Aw0_2:\n"
"s_waitcnt vmcnt(6)\n"
"s_branch AT_Ax0\n"
"AT_Aw0_1:\n"
"s_waitcnt vmcnt(3)\n"
"s_branch AT_Ax0\n"
"AT_Aw0_0:\n"
"s_waitcnt vmcnt(0)\n"
"AT_Ax0:\n"
"v_max3_u32 v194, v128, v129, v130\n"
"v_max3_u32 v195, v131, v132, v133\n"
"v_max3_u32 v194, v194, v134, v135\n"
"v_max3_u32 v195, v195, v136, v137\n"
"v_max3_u32 v194, v194, v138, v139\n"
"v_max_u32 v194, v194, v195\n"
"v_cmp_eq_u32 vcc, -1, v194\n"
"s_cmp_lt_i32 s87, 0\n"
"s_cselect_b64 vcc, 0, vcc\n"
"s_cbranch_vccnz AT_Ar0\n"
"s_bitset1_b32 s52, 0\n"
"v_dot2c_f32_bf16 v48, v128, v224\n"
"v_dot2c_f32_bf16 v49, v128, v225\n"
"v_dot2c_f32_bf16 v50, v129, v224\n"
"v_dot2c_f32_bf16 v51, v129, v225\n"
"v_dot2c_f32_bf16 v52, v130, v224\n"
"v_dot2c_f32_bf16 v53, v130, v225\n"
"v_dot2c_f32_bf16 v54, v131, v224\n"
"v_dot2c_f32_bf16 v55, v131, v225\n"
"v_dot2c_f32_bf16 v56, v132, v224\n"
"v_dot2c_f32_bf16 v57, v132, v225\n"
"v_dot2c_f32_bf16 v58, v133, v224\n"
"v_dot2c_f32_bf16 v59, v133, v225\n"
"v_dot2c_f32_bf16 v60, v134, v224\n"
"v_dot2c_f32_bf16 v61, v134, v225\n"
"v_dot2c_f32_bf16 v62, v135, v224\n"
"v_dot2c_f32_bf16 v63, v135, v225\n"
"v_dot2c_f32_bf16 v64, v136, v224\n"
"v_dot2c_f32_bf16 v65, v136, v225\n"
"v_dot2c_f32_bf16 v66, v137, v224\n"
"v_dot2c_f32_bf16 v67, v137, v225\n"
"v_dot2c_f32_bf16 v68, v138, v224\n"
"v_dot2c_f32_bf16 v69, v138, v225\n"
"v_dot2c_f32_bf16 v70, v139, v224\n"
"v_dot2c_f32_bf16 v71, v139, v225\n"
"AT_Ar0:\n"
"s_bitcmp1_b32 s53, 4\n"
"s_cbranch_scc0 AT_Ar1\n"
"s_sub_u32 s54, s54, 1\n"
"s_cmp_eq_u32 s54, 0\n"
"s_cbranch_scc1 AT_Aw1_0\n"
"s_cmp_eq_u32 s54, 1\n"
"s_cbranch_scc1 AT_Aw1_1\n"
"s_waitcnt vmcnt(6)\n"
"s_branch AT_Ax1\n"
"AT_Aw1_1:\n"
"s_waitcnt vmcnt(3)\n"
"s_branch AT_Ax1\n"
"AT_Aw1_0:\n"
"s_waitcnt vmcnt(0)\n"
"AT_Ax1:\n"
"v_max3_u32 v194, v140, v141, v142\n"
"v_max3_u32 v195, v143, v144, v145\n"
"v_max3_u32 v194, v194, v146, v147\n"
"v_max3_u32 v195, v195, v148, v149\n"
"v_max3_u32 v194, v194, v150, v151\n"
"v_max_u32 v194, v194, v195\n"
"v_cmp_eq_u32 vcc, -1, v194\n"
"s_cmp_lt_i32 s87, 0\n"
"s_cselect_b64 vcc, 0, vcc\n"
"s_cbranch_vccnz AT_Ar1\n"
"s_bitset1_b32 s52, 4\n"
"v_dot2c_f32_bf16 v48, v140, v224\n"
"v_dot2c_f32_bf16 v49, v140, v225\n"
"v_dot2c_f32_bf16 v50, v141, v224\n"
"v_dot2c_f32_bf16 v51, v141, v225\n"
"v_dot2c_f32_bf16 v52, v142, v224\n"
"v_dot2c_f32_bf16 v53, v142, v225\n"
"v_dot2c_f32_bf16 v54, v143, v224\n"
"v_dot2c_f32_bf16 v55, v143, v225\n"
"v_dot2c_f32_bf16 v56, v144, v224\n"
"v_dot2c_f32_bf16 v57, v144, v225\n"
"v_dot2c_f32_bf16 v58, v145, v224\n"
"v_dot2c_f32_bf16 v59, v145, v225\n"
"v_dot2c_f32_bf16 v60, v146, v224\n"
"v_dot2c_f32_bf16 v61, v146, v225\n"
"v_dot2c_f32_bf16 v62, v147, v224\n"
"v_dot2c_f32_bf16 v63, v147, v225\n"
"v_dot2c_f32_bf16 v64, v148, v224\n"
"v_dot2c_f32_bf16 v65, v148, v225\n"
"v_dot2c_f32_bf16 v66, v149, v224\n"
"v_dot2c_f32_bf16 v67, v149, v225\n"
"v_dot2c_f32_bf16 v68, v150, v224\n"
"v_dot2c_f32_bf16 v69, v150, v225\n"
"v_dot2c_f32_bf16 v70, v151, v224\n"
"v_dot2c_f32_bf16 v71, v151, v225\n"
"AT_Ar1:\n"
"s_bitcmp1_b32 s53, 8\n"
"s_cbranch_scc0 AT_Ar2\n"
"s_sub_u32 s54, s54, 1\n"
"s_cmp_eq_u32 s54, 0\n"
"s_cbranch_scc1 AT_Aw2_0\n"
"s_waitcnt vmcnt(3)\n"
"s_branch AT_Ax2\n"
"AT_Aw2_0:\n"
"s_waitcnt vmcnt(0)\n"
"AT_Ax2:\n"
"v_max3_u32 v194, v152, v153, v154\n"
"v_max3_u32 v195, v155, v156, v157\n"
"v_max3_u32 v194, v194, v158, v159\n"
"v_max3_u32 v195, v195, v160, v161\n"
"v_max3_u32 v194, v194, v162, v163\n"
"v_max_u32 v194, v194, v195\n"
"v_cmp_eq_u32 vcc, -1, v194\n"
"s_cmp_lt_i32 s87, 0\n"
"s_cselect_b64 vcc, 0, vcc\n"
"s_cbranch_vccnz AT_Ar2\n"
"s_bitset1_b32 s52, 8\n"
"v_dot2c_f32_bf16 v72, v152, v224\n"
"v_dot2c_f32_bf16 v73, v152, v225\n"
"v_dot2c_f32_bf16 v74, v153, v224\n"
"v_dot2c_f32_bf16 v75, v153, v225\n"
"v_dot2c_f32_bf16 v76, v154, v224\n"
"v_dot2c_f32_bf16 v77, v154, v225\n"
"v_dot2c_f32_bf16 v78, v155, v224\n"
"v_dot2c_f32_bf16 v79, v155, v225\n"
"v_dot2c_f32_bf16 v80, v156, v224\n"
"v_dot2c_f32_bf16 v81, v156, v225\n"
"v_dot2c_f32_bf16 v82, v157, v224\n"
"v_dot2c_f32_bf16 v83, v157, v225\n"
"v_dot2c_f32_bf16 v84, v158, v224\n"
"v_dot2c_f32_bf16 v85, v158, v225\n"
"v_dot2c_f32_bf16 v86, v159, v224\n"
"v_dot2c_f32_bf16 v87, v159, v225\n"
"v_dot2c_f32_bf16 v88, v160, v224\n"
"v_dot2c_f32_bf16 v89, v160, v225\n"
"v_dot2c_f32_bf16 v90, v161, v224\n"
"v_dot2c_f32_bf16 v91, v161, v225\n"
"v_dot2c_f32_bf16 v92, v162, v224\n"
"v_dot2c_f32_bf16 v93, v162, v225\n"
"v_dot2c_f32_bf16 v94, v163, v224\n"
"v_dot2c_f32_bf16 v95, v163, v225\n"
"AT_Ar2:\n"
"s_bitcmp1_b32 s53, 12\n"
"s_cbranch_scc0 AT_Ar3\n"
"s_sub_u32 s54, s54, 1\n"
"s_waitcnt vmcnt(0)\n"
"AT_Ax3:\n"
"v_max3_u32 v194, v164, v165, v166\n"
"v_max3_u32 v195, v167, v168, v169\n"
"v_max3_u32 v194, v194, v170, v171\n"
"v_max3_u32 v195, v195, v172, v173\n"
"v_max3_u32 v194, v194, v174, v175\n"
"v_max_u32 v194, v194, v195\n"
"v_cmp_eq_u32 vcc, -1, v194\n"
"s_cmp_lt_i32 s87, 0\n"
"s_cselect_b64 vcc, 0, vcc\n"
"s_cbranch_vccnz AT_Ar3\n"
"s_bitset1_b32 s52, 12\n"
"v_dot2c_f32_bf16 v72, v164, v224\n"
"v_dot2c_f32_bf16 v73, v164, v225\n"
"v_dot2c_f32_bf16 v74, v165, v224\n"
"v_dot2c_f32_bf16 v75, v165, v225\n"
"v_dot2c_f32_bf16 v76, v166, v224\n"
"v_dot2c_f32_bf16 v77, v166, v225\n"
"v_dot2c_f32_bf16 v78, v167, v224\n"
"v_dot2c_f32_bf16 v79, v167, v225\n"
"v_dot2c_f32_bf16 v80, v168, v224\n"
"v_dot2c_f32_bf16 v81, v168, v225\n"
"v_dot2c_f32_bf16 v82, v169, v224\n"
"v_dot2c_f32_bf16 v83, v169, v225\n"
"v_dot2c_f32_bf16 v84, v170, v224\n"
"v_dot2c_f32_bf16 v85, v170, v225\n"
"v_dot2c_f32_bf16 v86, v171, v224\n"
"v_dot2c_f32_bf16 v87, v171, v225\n"
"v_dot2c_f32_bf16 v88, v172, v224\n"
"v_dot2c_f32_bf16 v89, v172, v225\n"
"v_dot2c_f32_bf16 v90, v173, v224\n"
"v_dot2c_f32_bf16 v91, v173, v225\n"
"v_dot2c_f32_bf16 v92, v174, v224\n"
"v_dot2c_f32_bf16 v93, v174, v225\n"
"v_dot2c_f32_bf16 v94, v175, v224\n"
"v_dot2c_f32_bf16 v95, v175, v225\n"
"AT_Ar3:\n"
"s_cmp_lg_u32 s52, 0x1111\n"
"s_cbranch_scc1 AT_A1b\n"
"v_pk_add_f32 v[48:49], v[48:49], v[72:73]\n"
"v_pk_add_f32 v[50:51], v[50:51], v[74:75]\n"
"v_pk_add_f32 v[52:53], v[52:53], v[76:77]\n"
"v_pk_add_f32 v[54:55], v[54:55], v[78:79]\n"
"v_pk_add_f32 v[56:57], v[56:57], v[80:81]\n"
"v_pk_add_f32 v[58:59], v[58:59], v[82:83]\n"
"v_pk_add_f32 v[60:61], v[60:61], v[84:85]\n"
"v_pk_add_f32 v[62:63], v[62:63], v[86:87]\n"
"v_pk_add_f32 v[64:65], v[64:65], v[88:89]\n"
"v_pk_add_f32 v[66:67], v[66:67], v[90:91]\n"
"v_pk_add_f32 v[68:69], v[68:69], v[92:93]\n"
"v_pk_add_f32 v[70:71], v[70:71], v[94:95]\n"
"v_pk_fma_f32 v[48:49], v[48:49], v[96:97], v[24:25]\n"
"v_pk_fma_f32 v[50:51], v[50:51], v[98:99], v[26:27]\n"
"v_pk_fma_f32 v[52:53], v[52:53], v[100:101], v[28:29]\n"
"v_pk_fma_f32 v[54:55], v[54:55], v[102:103], v[30:31]\n"
"v_pk_fma_f32 v[56:57], v[56:57], v[104:105], v[32:33]\n"
"v_pk_fma_f32 v[58:59], v[58:59], v[106:107], v[34:35]\n"
"v_pk_fma_f32 v[60:61], v[60:61], v[108:109], v[36:37]\n"
"v_pk_fma_f32 v[62:63], v[62:63], v[110:111], v[38:39]\n"
"v_pk_fma_f32 v[64:65], v[64:65], v[112:113], v[40:41]\n"
"v_pk_fma_f32 v[66:67], v[66:67], v[114:115], v[42:43]\n"
"v_pk_fma_f32 v[68:69], v[68:69], v[116:117], v[44:45]\n"
"v_pk_fma_f32 v[70:71], v[70:71], v[118:119], v[46:47]\n"
"v_pk_mul_f32 v[194:195], v[48:49], v[48:49]\n"
"v_pk_mul_f32 v[196:197], v[48:49], v[0:1]\n"
"v_pk_add_f32 v[192:193], v[48:49], v[50:51]\n"
"v_pk_fma_f32 v[194:195], v[50:51], v[50:51], v[194:195]\n"
"v_pk_fma_f32 v[196:197], v[50:51], v[2:3], v[196:197]\n"
"v_pk_add_f32 v[192:193], v[192:193], v[52:53]\n"
"v_pk_fma_f32 v[194:195], v[52:53], v[52:53], v[194:195]\n"
"v_pk_fma_f32 v[196:197], v[52:53], v[4:5], v[196:197]\n"
"v_pk_add_f32 v[192:193], v[192:193], v[54:55]\n"
"v_pk_fma_f32 v[194:195], v[54:55], v[54:55], v[194:195]\n"
"v_pk_fma_f32 v[196:197], v[54:55], v[6:7], v[196:197]\n"
"v_pk_add_f32 v[192:193], v[192:193], v[56:57]\n"
"v_pk_fma_f32 v[194:195], v[56:57], v[56:57], v[194:195]\n"
"v_pk_fma_f32 v[196:197], v[56:57], v[8:9], v[196:197]\n"
"v_pk_add_f32 v[192:193], v[192:193], v[58:59]\n"
"v_pk_fma_f32 v[194:195], v[58:59], v[58:59], v[194:195]\n"
"v_pk_fma_f32 v[196:197], v[58:59], v[10:11], v[196:197]\n"
"AT_pg17:\n"
"s_cbranch_execz AT_hr\n"
"v_pk_add_f32 v[192:193], v[192:193], v[60:61]\n"
"v_pk_fma_f32 v[194:195], v[60:61], v[60:61], v[194:195]\n"
"v_pk_fma_f32 v[196:197], v[60:61], v[12:13], v[196:197]\n"
"v_pk_add_f32 v[192:193], v[192:193], v[62:63]\n"
"v_pk_fma_f32 v[194:195], v[62:63], v[62:63], v[194:195]\n"
"v_pk_fma_f32 v[196:197], v[62:63], v[14:15], v[196:197]\n"
"v_pk_add_f32 v[192:193], v[192:193], v[64:65]\n"
"v_pk_fma_f32 v[194:195], v[64:65], v[64:65], v[194:195]\n"
"v_pk_fma_f32 v[196:197], v[64:65], v[16:17], v[196:197]\n"
"v_pk_add_f32 v[192:193], v[192:193], v[66:67]\n"
"v_pk_fma_f32 v[194:195], v[66:67], v[66:67], v[194:195]\n"
"v_pk_fma_f32 v[196:197], v[66:67], v[18:19], v[196:197]\n"
"v_pk_add_f32 v[192:193], v[192:193], v[68:69]\n"
"v_pk_fma_f32 v[194:195], v[68:69], v[68:69], v[194:195]\n"
"v_pk_fma_f32 v[196:197], v[68:69], v[20:21], v[196:197]\n"
"v_pk_add_f32 v[192:193], v[192:193], v[70:71]\n"
"v_pk_fma_f32 v[194:195], v[70:71], v[70:71], v[194:195]\n"
"v_pk_fma_f32 v[196:197], v[70:71], v[22:23], v[196:197]\n"
"v_add_f32 v192, v192, v193\n"
"v_add_f32 v193, v194, v195\n"
"v_add_f32 v194, v196, v197\n"
"s_lshl_b32 s56, s41, 7\n"
"v_add_u32 v199, s56, v226\n"
"v_add_f32_dpp v192, v192, v192 row_ror:8 row_mask:0xf bank_mask:0xf\n"
"v_add_f32_dpp v193, v193, v193 row_ror:8 row_mask:0xf bank_mask:0xf\n"
"v_add_f32_dpp v194, v194, v194 row_ror:8 row_mask:0xf bank_mask:0xf\n"
"v_mov_b32 v196, v192\n"
"v_mov_b32 v197, v193\n"
"v_mov_b32 v198, v194\n"
"v_permlane16_swap_b32 v192, v196\n"
"v_permlane16_swap_b32 v193, v197\n"
"v_permlane16_swap_b32 v194, v198\n"
"v_add_f32 v192, v192, v196\n"
"v_add_f32 v193, v193, v197\n"
"v_add_f32 v194, v194, v198\n"
"v_mov_b32 v196, v192\n"
"v_mov_b32 v197, v193\n"
"v_mov_b32 v198, v194\n"
"v_permlane32_swap_b32 v192, v196\n"
"v_permlane32_swap_b32 v193, v197\n"
"v_permlane32_swap_b32 v194, v198\n"
"v_add_f32 v192, v192, v196\n"
"v_add_f32 v193, v193, v197\n"
"v_add_f32 v194, v194, v198\n"
"s_mov_b64 exec, 0xff\n"
"ds_write_b128 v199, v[192:195]\n"
"s_mov_b64 exec, -1\n"
"s_waitcnt lgkmcnt(0)\n"
"s_barrier\n"
"ds_read_b128 v[200:203], v226 offset:0\n"
"ds_read_b128 v[204:207], v226 offset:128\n"
"ds_read_b128 v[208:211], v226 offset:256\n"
"ds_read_b128 v[0:3], v244 offset:4608\n"
"ds_read_b128 v[4:7], v244 offset:4672\n"
"ds_read_b128 v[8:11], v244 offset:5376\n"
"ds_read_b128 v[12:15], v244 offset:5440\n"
"ds_read_b128 v[16:19], v244 offset:6144\n"
"ds_read_b128 v[20:23], v244 offset:6208\n"
"ds_read_b128 v[24:27], v244 offset:6912\n"
"ds_read_b128 v[28:31], v244 offset:6976\n"
"ds_read_b128 v[32:35], v244 offset:7680\n"
"ds_read_b128 v[36:39], v244 offset:7744\n"
"ds_read_b128 v[40:43], v244 offset:8448\n"
"ds_read_b128 v[44:47], v244 offset:8512\n"
"s_waitcnt lgkmcnt(13)\n"
"v_add_f32 v200, v200, v204\n"
"v_add_f32 v201, v201, v205\n"
"v_add_f32 v202, v202, v206\n"
"s_waitcnt lgkmcnt(12)\n"
"v_add_f32 v200, v200, v208\n"
"v_add_f32 v201, v201, v209\n"
"v_add_f32 v202, v202, v210\n"
"v_mul_f32 v200, 0x3ae38e39, v200\n"
"v_mul_f32 v201, 0x3ae38e39, v201\n"
"v_fma_f32 v201, -v200, v200, v201\n"
"v_max_f32 v201, 0, v201\n"
"v_add_f32 v201, 0x3727c5ac, v201\n"
"v_rsq_f32 v192, v201\n"
"v_fma_f32 v202, -v200, v212, v202\n"
"v_fma_f32 v196, v192, v202, v213\n"
"v_ashrrev_i32 v203, 31, v196\n"
"v_or_b32 v203, 0x80000000, v203\n"
"v_xor_b32 v196, v196, v203\n"
"v_and_or_b32 v196, v196, s14, v220\n"
"v_mul_f32 v194, v200, v192\n"
"v_sub_f32 v194, 0, v194\n"
"v_mov_b32_dpp v203, v196 row_ror:1 row_mask:0xf bank_mask:0xf\n"
"v_min_u32 v198, v196, v203\n"
"v_max_u32 v197, v196, v203\n"
"s_nop 0\n"
"v_mov_b32_dpp v204, v198 row_ror:2 row_mask:0xf bank_mask:0xf\n"
"v_mov_b32_dpp v203, v197 row_ror:2 row_mask:0xf bank_mask:0xf\n"
"v_min_u32 v205, v197, v203\n"
"v_max_u32 v197, v197, v203\n"
"v_max3_u32 v198, v198, v204, v205\n"
"s_nop 0\n"
"v_mov_b32_dpp v203, v197 row_ror:4 row_mask:0xf bank_mask:0xf\n"
"v_mov_b32_dpp v204, v198 row_ror:4 row_mask:0xf bank_mask:0xf\n"
"v_min_u32 v205, v197, v203\n"
"v_max_u32 v197, v197, v203\n"
"v_max3_u32 v198, v198, v204, v205\n"
"v_cmp_ge_u32 s[8:9], v196, v198\n"
"ds_read_b128 v[72:75], v244 offset:38912\n"
"ds_read_b128 v[76:79], v244 offset:38976\n"
"ds_read_b128 v[80:83], v244 offset:39680\n"
"ds_read_b128 v[84:87], v244 offset:39744\n"
"ds_read_b128 v[88:91], v244 offset:40448\n"
"ds_read_b128 v[92:95], v244 offset:40512\n"
"v_pk_fma_f32 v[144:145], v[48:49], v[192:193], v[194:195] op_sel_hi:[1,0,0]\n"
"v_pk_fma_f32 v[146:147], v[50:51], v[192:193], v[194:195] op_sel_hi:[1,0,0]\n"
"v_pk_fma_f32 v[148:149], v[52:53], v[192:193], v[194:195] op_sel_hi:[1,0,0]\n"
"v_pk_fma_f32 v[150:151], v[54:55], v[192:193], v[194:195] op_sel_hi:[1,0,0]\n"
"v_pk_fma_f32 v[152:153], v[56:57], v[192:193], v[194:195] op_sel_hi:[1,0,0]\n"
"v_pk_fma_f32 v[154:155], v[58:59], v[192:193], v[194:195] op_sel_hi:[1,0,0]\n"
"v_pk_fma_f32 v[156:157], v[60:61], v[192:193], v[194:195] op_sel_hi:[1,0,0]\n"
"v_pk_fma_f32 v[158:159], v[62:63], v[192:193], v[194:195] op_sel_hi:[1,0,0]\n"
"v_pk_fma_f32 v[160:161], v[64:65], v[192:193], v[194:195] op_sel_hi:[1,0,0]\n"
"v_pk_fma_f32 v[162:163], v[66:67], v[192:193], v[194:195] op_sel_hi:[1,0,0]\n"
"v_pk_fma_f32 v[164:165], v[68:69], v[192:193], v[194:195] op_sel_hi:[1,0,0]\n"
"v_pk_fma_f32 v[166:167], v[70:71], v[192:193], v[194:195] op_sel_hi:[1,0,0]\n"
"s_waitcnt lgkmcnt(6)\n"
"v_fma_f32 v96, v144, v0, v24\n"
"v_fma_f32 v97, v145, v1, v25\n"
"v_fma_f32 v98, v146, v2, v26\n"
"v_fma_f32 v99, v147, v3, v27\n"
"v_fma_f32 v100, v148, v4, v28\n"
"v_fma_f32 v101, v149, v5, v29\n"
"v_fma_f32 v102, v150, v6, v30\n"
"v_fma_f32 v103, v151, v7, v31\n"
"v_fma_f32 v104, v152, v8, v32\n"
"v_fma_f32 v105, v153, v9, v33\n"
"v_fma_f32 v106, v154, v10, v34\n"
"v_fma_f32 v107, v155, v11, v35\n"
"v_fma_f32 v108, v156, v12, v36\n"
"v_fma_f32 v109, v157, v13, v37\n"
"v_fma_f32 v110, v158, v14, v38\n"
"v_fma_f32 v111, v159, v15, v39\n"
"v_fma_f32 v112, v160, v16, v40\n"
"v_fma_f32 v113, v161, v17, v41\n"
"v_fma_f32 v114, v162, v18, v42\n"
"v_fma_f32 v115, v163, v19, v43\n"
"v_fma_f32 v116, v164, v20, v44\n"
"v_fma_f32 v117, v165, v21, v45\n"
"v_fma_f32 v118, v166, v22, v46\n"
"v_fma_f32 v119, v167, v23, v47\n"
"v_cmp_ne_u32 vcc, v196, v197\n"
"v_add_u32 v135, 768, v176\n"
"v_cmp_gt_u32 s[58:59], 24, v248\n"
"v_cndmask_b32 v134, v176, v135, vcc\n"
"s_mov_b64 exec, s[8:9]\n"
"ds_write_b128 v134, v[96:99] offset:0\n"
"ds_write_b128 v134, v[100:103] offset:64\n"
"ds_write_b128 v134, v[104:107] offset:256\n"
"ds_write_b128 v134, v[108:111] offset:320\n"
"ds_write_b128 v134, v[112:115] offset:512\n"
"ds_write_b128 v134, v[116:119] offset:576\n"
"s_mov_b64 exec, -1\n"
"ds_read_b128 v[136:139], v177 offset:0\n"
"ds_read_b128 v[140:143], v177 offset:16\n"
"v_cndmask_b32 v135, v198, v197, s[58:59]\n"
"v_not_b32 v135, v135\n"
"v_and_b32 v135, 7, v135\n"
"v_mul_u32_u24 v135, 2304, v135\n"
"v_add_u32 v135, v135, v178\n"
"s_mul_i32 s60, s55, 18432\n"
"s_add_u32 s60, s60, 30564352\n"
"s_waitcnt lgkmcnt(0)\n"
"v_cvt_pk_bf16_f32 v120, v136, v137\n"
"v_cvt_pk_bf16_f32 v121, v138, v139\n"
"v_cvt_pk_bf16_f32 v122, v140, v141\n"
"v_cvt_pk_bf16_f32 v123, v142, v143\n"
"buffer_store_dwordx4 v[120:123], v135, s[28:31], s60 offen sc1\n"
"v_lshlrev_b32 v128, 16, v120\n"
"v_and_b32 v129, 0xffff0000, v120\n"
"v_sub_f32 v136, v136, v128\n"
"v_sub_f32 v137, v137, v129\n"
"v_lshlrev_b32 v128, 16, v121\n"
"v_and_b32 v129, 0xffff0000, v121\n"
"v_sub_f32 v138, v138, v128\n"
"v_sub_f32 v139, v139, v129\n"
"v_lshlrev_b32 v128, 16, v122\n"
"v_and_b32 v129, 0xffff0000, v122\n"
"v_sub_f32 v140, v140, v128\n"
"v_sub_f32 v141, v141, v129\n"
"v_lshlrev_b32 v128, 16, v123\n"
"v_and_b32 v129, 0xffff0000, v123\n"
"v_sub_f32 v142, v142, v128\n"
"v_sub_f32 v143, v143, v129\n"
"v_cvt_pk_bf16_f32 v124, v136, v137\n"
"v_cvt_pk_bf16_f32 v125, v138, v139\n"
"v_cvt_pk_bf16_f32 v126, v140, v141\n"
"v_cvt_pk_bf16_f32 v127, v142, v143\n"
"buffer_store_dwordx4 v[124:127], v135, s[28:31], s60 offen offset:1152 sc1\n"
"v_fma_f32 v200, v72, v96, v48\n"
"v_fma_f32 v201, v73, v97, v49\n"
"v_fma_f32 v202, v74, v98, v50\n"
"v_fma_f32 v203, v75, v99, v51\n"
"v_fma_f32 v204, v76, v100, v52\n"
"v_fma_f32 v205, v77, v101, v53\n"
"v_fma_f32 v206, v78, v102, v54\n"
"v_fma_f32 v207, v79, v103, v55\n"
"v_fma_f32 v208, v80, v104, v56\n"
"v_fma_f32 v209, v81, v105, v57\n"
"v_fma_f32 v210, v82, v106, v58\n"
"v_fma_f32 v211, v83, v107, v59\n"
"v_fma_f32 v212, v84, v108, v60\n"
"v_fma_f32 v213, v85, v109, v61\n"
"v_fma_f32 v214, v86, v110, v62\n"
"v_fma_f32 v215, v87, v111, v63\n"
"v_fma_f32 v216, v88, v112, v64\n"
"v_fma_f32 v217, v89, v113, v65\n"
"v_fma_f32 v218, v90, v114, v66\n"
"v_fma_f32 v219, v91, v115, v67\n"
"v_fma_f32 v220, v92, v116, v68\n"
"v_fma_f32 v221, v93, v117, v69\n"
"v_fma_f32 v222, v94, v118, v70\n"
"v_fma_f32 v223, v95, v119, v71\n"
"v_cvt_pk_bf16_f32 v228, v200, v201\n"
"v_cvt_pk_bf16_f32 v229, v202, v203\n"
"v_cvt_pk_bf16_f32 v230, v204, v205\n"
"v_cvt_pk_bf16_f32 v231, v206, v207\n"
"v_cvt_pk_bf16_f32 v232, v208, v209\n"
"v_cvt_pk_bf16_f32 v233, v210, v211\n"
"v_cvt_pk_bf16_f32 v234, v212, v213\n"
"v_cvt_pk_bf16_f32 v235, v214, v215\n"
"v_cvt_pk_bf16_f32 v236, v216, v217\n"
"v_cvt_pk_bf16_f32 v237, v218, v219\n"
"v_cvt_pk_bf16_f32 v238, v220, v221\n"
"v_cvt_pk_bf16_f32 v239, v222, v223\n"
"ds_write_b64 v240, v[228:229] offset:0\n"
"ds_write_b64 v240, v[230:231] offset:32\n"
"ds_write_b64 v240, v[232:233] offset:384\n"
"ds_write_b64 v240, v[234:235] offset:416\n"
"ds_write_b64 v240, v[236:237] offset:768\n"
"ds_write_b64 v240, v[238:239] offset:800\n"
"s_mov_b64 s[8:9], -1\n"
"v_mov_b32 v225, v196\n"
"v_and_b32 v241, 1, v248\n"
"v_mul_u32_u24 v241, 1152, v241\n"
"v_lshl_add_u32 v241, v190, 4, v241\n"
"s_mul_i32 s63, s41, 288\n"
"v_add_u32 v241, s63, v241\n"
"v_and_b32 v242, 10, v248\n"
"v_lshl_add_u32 v241, v242, 26, v241\n"
"v_and_b32 v242, 32, v248\n"
"v_lshlrev_b32 v242, 25, v242\n"
"s_add_u32 s63, s63, s40\n"
"s_add_u32 s63, s63, 131072\n"
"v_cmp_eq_u32 vcc, 3, v190\n"
"v_and_b32 v185, 3, v185\n"
"v_lshlrev_b32 v191, 3, v190\n"
"v_cndmask_b32_e64 v191, v191, 0, vcc\n"
"v_add_u32 v191, v191, v185\n"
"v_mul_u32_u24 v191, 1152, v191\n"
"v_lshl_add_u32 v191, v186, 3, v191\n"
"v_add_u32 v255, s63, v191\n"
"v_lshrrev_b32 v243, 3, v248\n"
"v_mul_u32_u24 v243, 24576, v243\n"
"v_and_b32 v247, 7, v248\n"
"v_lshl_add_u32 v243, v247, 4, v243\n"
"v_mul_u32_u24 v224, 24576, v190\n"
"v_and_b32 v246, 15, v248\n"
"v_lshl_add_u32 v224, v246, 3, v224\n"
"v_or_b32 v224, v242, v224\n"
"v_subrev_u32 v252, 65536, v227\n"
"s_mul_i32 s63, s41, 384\n"
"v_mul_u32_u24 v244, 192, v190\n"
"v_lshl_add_u32 v244, v246, 2, v244\n"
"v_add3_u32 v244, v244, v252, s63\n"
"v_cmp_lt_u32 vcc, 23, v248\n"
"v_lshl_add_u32 v249, v248, 2, v252\n"
"v_add_u32 v185, 96, v249\n"
"v_cndmask_b32 v249, v249, v185, vcc\n"
"s_lshl_b32 s63, s41, 9\n"
"v_add_u32 v252, s63, v252\n"
"v_lshl_add_u32 v251, v248, 1, v252\n"
"v_add_u32 v185, 208, v251\n"
"v_cndmask_b32 v251, v251, v185, vcc\n"
"v_lshl_add_u32 v185, v248, 3, v252\n"
"v_lshl_add_u32 v252, v247, 6, v252\n"
"v_lshl_add_u32 v252, v190, 4, v252\n"
"s_min_u32 s63, s41, 2\n"
"s_mul_i32 s63, s63, 192\n"
"v_add_u32 v183, s63, v248\n"
"v_mul_u32_u24 v183, 911, v183\n"
"s_lshl_b32 s63, s55, 3\n"
"v_lshrrev_b32 v180, 16, v183\n"
"v_add_u32 v180, s63, v180\n"
"v_add_u32 v183, 58304, v183\n"
"v_lshrrev_b32 v181, 16, v183\n"
"v_add_u32 v181, s63, v181\n"
"v_add_u32 v183, 58304, v183\n"
"v_lshrrev_b32 v182, 16, v183\n"
"v_add_u32 v182, s63, v182\n"
"v_lshlrev_b32 v250, 4, v248\n"
"v_add_u32 v184, s45, v250\n"
"s_cmp_eq_u32 s41, 3\n"
"s_cselect_b32 s63, 0x10000000, 0\n"
"v_or_b32 v250, s63, v250\n"
"AT_B3:\n"
"s_waitcnt lgkmcnt(0)\n"
"s_barrier\n"
"ds_read_b64 v[246:247], v227 offset:656\n"
"v_and_b32 v186, 4, v248\n"
"v_cmp_eq_u32 vcc, 0, v186\n"
"s_mov_b32 s56, 30564352\n"
"s_waitcnt lgkmcnt(0)\n"
"v_cndmask_b32 v186, v247, v246, vcc\n"
"v_readfirstlane_b32 s10, v246\n"
"v_not_b32 v186, v186\n"
"v_readfirstlane_b32 s11, v247\n"
"v_and_b32 v186, 0x7ff, v186\n"
"v_mul_u32_u24 v186, 2304, v186\n"
"v_add_u32 v241, v241, v186\n"
"v_or_b32 v242, v242, v241\n"
"s_andn2_b32 s12, 0x7ff, s10\n"
"s_andn2_b32 s13, 0x7ff, s11\n"
"s_cmp_ge_u32 s42, 192\n"
"s_cbranch_scc1 AT_F0\n"
"buffer_load_dwordx4 v[0:3], v241, s[28:31], s56 offen offset:0 sc1\n"
"buffer_load_dwordx4 v[4:7], v241, s[28:31], s56 offen offset:64 sc1\n"
"buffer_load_dwordx4 v[8:11], v241, s[28:31], s56 offen offset:128 sc1\n"
"buffer_load_dwordx4 v[12:15], v241, s[28:31], s56 offen offset:192 sc1\n"
"buffer_load_dwordx4 v[16:19], v242, s[28:31], s56 offen offset:256 sc1\n"
"v_mov_b32 v20, 0\n"
"v_mov_b32 v21, 0\n"
"ds_write_b64 v185, v[20:21] offset:4096\n"
"ds_read_b64_tr_b16 a[192:193], v255 offset:0\n"
"ds_read_b64_tr_b16 a[194:195], v255 offset:4608\n"
"ds_read_b64_tr_b16 a[196:197], v255 offset:32\n"
"ds_read_b64_tr_b16 a[198:199], v255 offset:4640\n"
"ds_read_b64_tr_b16 a[200:201], v255 offset:64\n"
"ds_read_b64_tr_b16 a[202:203], v255 offset:4672\n"
"ds_read_b64_tr_b16 a[204:205], v255 offset:96\n"
"ds_read_b64_tr_b16 a[206:207], v255 offset:4704\n"
"ds_read_b64_tr_b16 a[208:209], v255 offset:128\n"
"ds_read_b64_tr_b16 a[210:211], v255 offset:4736\n"
"ds_read_b64_tr_b16 a[212:213], v255 offset:160\n"
"ds_read_b64_tr_b16 a[214:215], v255 offset:4768\n"
"ds_read_b64_tr_b16 a[216:217], v255 offset:192\n"
"ds_read_b64_tr_b16 a[218:219], v255 offset:4800\n"
"ds_read_b64_tr_b16 a[220:221], v255 offset:224\n"
"ds_read_b64_tr_b16 a[222:223], v255 offset:4832\n"
"ds_read_b64_tr_b16 a[224:225], v255 offset:256\n"
"ds_read_b64_tr_b16 a[226:227], v255 offset:4864\n"
"s_mul_i32 s57, s41, 442368\n"
"s_lshl_b32 s58, s42, 7\n"
"s_add_u32 s57, s57, s58\n"
"s_add_u32 s57, s57, 28794880\n"
"s_add_u32 s62, s57, 196608\n"
"s_add_u32 s63, s57, 393216\n"
"v_mov_b32 v85, s85\n"
"v_mov_b32 v87, s85\n"
"v_mov_b32 v89, s85\n"
"v_mov_b32 v91, s85\n"
"v_mov_b32 v93, s85\n"
"s_mov_b32 s87, 0x80000\n"
"s_waitcnt vmcnt(0)\n"
"AT_A6:\n"
"v_max3_u32 v22, v0, v1, v2\n"
"v_max3_u32 v23, v3, v4, v5\n"
"v_max3_u32 v22, v22, v6, v7\n"
"v_max3_u32 v23, v23, v8, v9\n"
"v_max3_u32 v22, v22, v10, v11\n"
"v_max3_u32 v23, v23, v12, v13\n"
"v_max3_u32 v22, v22, v14, v15\n"
"v_max3_u32 v23, v23, v16, v17\n"
"v_max3_u32 v22, v22, v18, v19\n"
"v_max_u32 v22, v22, v23\n"
"v_cmp_eq_u32 vcc, -1, v22\n"
"s_nop 0\n"
"s_cbranch_vccz AT_A7\n"
"s_sub_u32 s87, s87, 1\n"
"s_cmp_eq_u32 s87, 0\n"
"s_cbranch_scc1 AT_A7\n"
"s_sleep 1\n"
"buffer_load_dwordx4 v[0:3], v241, s[28:31], s56 offen offset:0 sc1\n"
"buffer_load_dwordx4 v[4:7], v241, s[28:31], s56 offen offset:64 sc1\n"
"buffer_load_dwordx4 v[8:11], v241, s[28:31], s56 offen offset:128 sc1\n"
"buffer_load_dwordx4 v[12:15], v241, s[28:31], s56 offen offset:192 sc1\n"
"buffer_load_dwordx4 v[16:19], v242, s[28:31], s56 offen offset:256 sc1\n"
"s_waitcnt vmcnt(0)\n"
"s_branch AT_A6\n"
"AT_A7:\n"
"v_mfma_f32_16x16x32_bf16 v[24:27], v[0:3], a[128:131], 0\n"
"v_mfma_f32_16x16x32_bf16 v[28:31], v[0:3], a[132:135], 0\n"
"v_mfma_f32_16x16x32_bf16 v[32:35], v[0:3], a[136:139], 0\n"
"v_mfma_f32_16x16x32_bf16 v[24:27], v[4:7], a[140:143], v[24:27]\n"
"v_mfma_f32_16x16x32_bf16 v[28:31], v[4:7], a[144:147], v[28:31]\n"
"v_mfma_f32_16x16x32_bf16 v[32:35], v[4:7], a[148:151], v[32:35]\n"
"v_mfma_f32_16x16x32_bf16 v[24:27], v[8:11], a[152:155], v[24:27]\n"
"v_mfma_f32_16x16x32_bf16 v[28:31], v[8:11], a[156:159], v[28:31]\n"
"v_mfma_f32_16x16x32_bf16 v[32:35], v[8:11], a[160:163], v[32:35]\n"
"v_mfma_f32_16x16x32_bf16 v[24:27], v[12:15], a[164:167], v[24:27]\n"
"v_mfma_f32_16x16x32_bf16 v[28:31], v[12:15], a[168:171], v[28:31]\n"
"v_mfma_f32_16x16x32_bf16 v[32:35], v[12:15], a[172:175], v[32:35]\n"
"v_mfma_f32_16x16x32_bf16 v[24:27], v[16:19], a[176:179], v[24:27]\n"
"v_mfma_f32_16x16x32_bf16 v[28:31], v[16:19], a[180:183], v[28:31]\n"
"v_mfma_f32_16x16x32_bf16 v[32:35], v[16:19], a[184:187], v[32:35]\n"
"s_nop 5\n"
"v_add_f32 v24, v24, v25\n"
"v_add_f32 v28, v28, v29\n"
"v_add_f32 v32, v32, v33\n"
"s_mov_b32 exec_hi, 0\n"
"ds_write_b32 v244, v24 offset:2048\n"
"ds_write_b32 v244, v28 offset:2112\n"
"ds_write_b32 v244, v32 offset:2176\n"
"s_mov_b64 exec, -1\n"
"v_cmp_eq_u32 vcc, s10, v225\n"
"v_cmp_eq_u32 s[58:59], s11, v225\n"
"s_nop 0\n"
"s_or_b64 s[58:59], s[58:59], vcc\n"
"s_and_b64 exec, s[8:9], s[58:59]\n"
"s_cbranch_execz AT_A5\n"
"s_mov_b32 s60, 28778496\n"
"v_add_u32 v116, 4608, v245\n"
"v_cndmask_b32 v116, v116, v245, vcc\n"
"v_mov_b32 v101, s85\n"
"v_mov_b32 v103, s85\n"
"v_mov_b32 v105, s85\n"
"v_mov_b32 v107, s85\n"
"v_mov_b32 v109, s85\n"
"v_mov_b32 v111, s85\n"
"v_mov_b32 v113, s85\n"
"v_mov_b32 v115, s85\n"
"v_mov_b32 v100, v200\n"
"v_mov_b32 v102, v201\n"
"buffer_store_dwordx4 v[100:103], v116, s[28:31], s60 offen offset:0 sc1\n"
"v_mov_b32 v104, v202\n"
"v_mov_b32 v106, v203\n"
"buffer_store_dwordx4 v[104:107], v116, s[28:31], s60 offen offset:16 sc1\n"
"v_mov_b32 v108, v204\n"
"v_mov_b32 v110, v205\n"
"buffer_store_dwordx4 v[108:111], v116, s[28:31], s60 offen offset:128 sc1\n"
"v_mov_b32 v112, v206\n"
"v_mov_b32 v114, v207\n"
"buffer_store_dwordx4 v[112:115], v116, s[28:31], s60 offen offset:144 sc1\n"
"v_mov_b32 v100, v208\n"
"v_mov_b32 v102, v209\n"
"buffer_store_dwordx4 v[100:103], v116, s[28:31], s60 offen offset:1536 sc1\n"
"v_mov_b32 v104, v210\n"
"v_mov_b32 v106, v211\n"
"buffer_store_dwordx4 v[104:107], v116, s[28:31], s60 offen offset:1552 sc1\n"
"v_mov_b32 v108, v212\n"
"v_mov_b32 v110, v213\n"
"buffer_store_dwordx4 v[108:111], v116, s[28:31], s60 offen offset:1664 sc1\n"
"v_mov_b32 v112, v214\n"
"v_mov_b32 v114, v215\n"
"buffer_store_dwordx4 v[112:115], v116, s[28:31], s60 offen offset:1680 sc1\n"
"v_mov_b32 v100, v216\n"
"v_mov_b32 v102, v217\n"
"buffer_store_dwordx4 v[100:103], v116, s[28:31], s60 offen offset:3072 sc1\n"
"v_mov_b32 v104, v218\n"
"v_mov_b32 v106, v219\n"
"buffer_store_dwordx4 v[104:107], v116, s[28:31], s60 offen offset:3088 sc1\n"
"v_mov_b32 v108, v220\n"
"v_mov_b32 v110, v221\n"
"buffer_store_dwordx4 v[108:111], v116, s[28:31], s60 offen offset:3200 sc1\n"
"v_mov_b32 v112, v222\n"
"v_mov_b32 v114, v223\n"
"buffer_store_dwordx4 v[112:115], v116, s[28:31], s60 offen offset:3216 sc1\n"
"AT_A5:\n"
"s_mov_b64 exec, -1\n"
"s_waitcnt lgkmcnt(0)\n"
"s_barrier\n"
"ds_read_b32 v0, v249 offset:2048\n"
"ds_read_b32 v1, v249 offset:2144\n"
"ds_read_b32 v2, v249 offset:2432\n"
"ds_read_b32 v3, v249 offset:2528\n"
"ds_read_b32 v4, v249 offset:2816\n"
"ds_read_b32 v5, v249 offset:2912\n"
"ds_read_b32 v6, v249 offset:3200\n"
"ds_read_b32 v7, v249 offset:3296\n"
"v_cmp_gt_u32 s[58:59], 32, v248\n"
"s_waitcnt lgkmcnt(5)\n"
"v_add_f32 v0, v0, v2\n"
"s_waitcnt lgkmcnt(1)\n"
"v_add_f32 v4, v4, v6\n"
"v_add_f32 v1, v1, v3\n"
"v_add_f32 v0, v0, v4\n"
"s_waitcnt lgkmcnt(0)\n"
"v_add_f32 v5, v5, v7\n"
"v_mul_f32 v2, 0xbfb8aa3b, v0\n"
"v_add_f32 v1, v1, v5\n"
"v_exp_f32 v2, v2\n"
"v_mul_f32 v0, v0, v1\n"
"v_add_f32 v2, 1.0, v2\n"
"v_rcp_f32 v2, v2\n"
"s_mov_b32 exec_hi, 0xffff\n"
"v_mul_f32 v0, v0, v2\n"
"v_cvt_pk_bf16_f32 v1, v0, v0\n"
"ds_write_b16 v251, v1 offset:4096\n"
"v_and_b32 v2, 0xffff0000, v1\n"
"v_sub_f32 v0, v0, v2\n"
"v_cvt_pk_bf16_f32 v0, v0, v0\n"
"ds_write_b16 v251, v0 offset:4160\n"
"s_mov_b64 exec, -1\n"
"ds_read_b128 v[8:11], v252 offset:4096\n"
"s_waitcnt lgkmcnt(0)\n"
"v_mfma_f32_16x16x32_bf16 v[40:43], v[8:11], a[192:195], 0\n"
"v_mfma_f32_16x16x32_bf16 v[44:47], v[8:11], a[196:199], 0\n"
"v_mfma_f32_16x16x32_bf16 v[48:51], v[8:11], a[200:203], 0\n"
"v_mfma_f32_16x16x32_bf16 v[52:55], v[8:11], a[204:207], 0\n"
"v_mfma_f32_16x16x32_bf16 v[56:59], v[8:11], a[208:211], 0\n"
"v_mfma_f32_16x16x32_bf16 v[60:63], v[8:11], a[212:215], 0\n"
"v_mfma_f32_16x16x32_bf16 v[64:67], v[8:11], a[216:219], 0\n"
"v_mfma_f32_16x16x32_bf16 v[68:71], v[8:11], a[220:223], 0\n"
"v_mfma_f32_16x16x32_bf16 v[72:75], v[8:11], a[224:227], 0\n"
"v_add_f32 v40, v40, v41\n"
"v_add_f32 v44, v44, v45\n"
"v_add_f32 v48, v48, v49\n"
"v_add_f32 v52, v52, v53\n"
"v_cndmask_b32 v84, v48, v40, s[58:59]\n"
"v_cndmask_b32 v86, v52, v44, s[58:59]\n"
"buffer_store_dwordx4 v[84:87], v243, s[28:31], s57 offen sc1\n"
"v_add_f32 v56, v56, v57\n"
"v_add_f32 v60, v60, v61\n"
"v_add_f32 v64, v64, v65\n"
"v_add_f32 v68, v68, v69\n"
"v_cndmask_b32 v88, v64, v56, s[58:59]\n"
"v_cndmask_b32 v90, v68, v60, s[58:59]\n"
"buffer_store_dwordx4 v[88:91], v243, s[28:31], s62 offen sc1\n"
"v_add_f32 v92, v72, v73\n"
"buffer_store_dwordx2 v[92:93], v224, s[28:31], s63 offen sc1\n"
"ds_read_b128 v[228:231], v184 offset:0\n"
"ds_read_b128 v[232:235], v184 offset:1024\n"
"ds_read_b128 v[236:239], v184 offset:2048\n"
"v_cmp_ne_u32 vcc, s12, v180\n"
"v_cmp_ne_u32 s[60:61], s13, v180\n"
"s_waitcnt lgkmcnt(2)\n"
"s_and_b64 exec, vcc, s[60:61]\n"
"buffer_store_dwordx4 v[228:231], v250, s[76:79], s46 offen offset:0 sc1\n"
"s_mov_b64 exec, -1\n"
"v_cmp_ne_u32 vcc, s12, v181\n"
"v_cmp_ne_u32 s[60:61], s13, v181\n"
"s_waitcnt lgkmcnt(1)\n"
"s_and_b64 exec, vcc, s[60:61]\n"
"buffer_store_dwordx4 v[232:235], v250, s[76:79], s46 offen offset:1024 sc1\n"
"s_mov_b64 exec, -1\n"
"v_cmp_ne_u32 vcc, s12, v182\n"
"v_cmp_ne_u32 s[60:61], s13, v182\n"
"s_waitcnt lgkmcnt(0)\n"
"s_and_b64 exec, vcc, s[60:61]\n"
"buffer_store_dwordx4 v[236:239], v250, s[76:79], s46 offen offset:2048 sc1\n"
"s_mov_b64 exec, -1\n"
"s_branch AT_F1\n"
"AT_F0:\n"
"v_cmp_eq_u32 vcc, s10, v225\n"
"v_cmp_eq_u32 s[58:59], s11, v225\n"
"s_nop 0\n"
"s_or_b64 s[58:59], s[58:59], vcc\n"
"s_and_b64 exec, s[8:9], s[58:59]\n"
"s_cbranch_execz AT_F0b\n"
"s_mov_b32 s60, 28778496\n"
"v_add_u32 v116, 4608, v245\n"
"v_cndmask_b32 v116, v116, v245, vcc\n"
"v_mov_b32 v101, s85\n"
"v_mov_b32 v103, s85\n"
"v_mov_b32 v105, s85\n"
"v_mov_b32 v107, s85\n"
"v_mov_b32 v109, s85\n"
"v_mov_b32 v111, s85\n"
"v_mov_b32 v113, s85\n"
"v_mov_b32 v115, s85\n"
"v_mov_b32 v100, v200\n"
"v_mov_b32 v102, v201\n"
"buffer_store_dwordx4 v[100:103], v116, s[28:31], s60 offen offset:0 sc1\n"
"v_mov_b32 v104, v202\n"
"v_mov_b32 v106, v203\n"
"buffer_store_dwordx4 v[104:107], v116, s[28:31], s60 offen offset:16 sc1\n"
"v_mov_b32 v108, v204\n"
"v_mov_b32 v110, v205\n"
"buffer_store_dwordx4 v[108:111], v116, s[28:31], s60 offen offset:128 sc1\n"
"v_mov_b32 v112, v206\n"
"v_mov_b32 v114, v207\n"
"buffer_store_dwordx4 v[112:115], v116, s[28:31], s60 offen offset:144 sc1\n"
"v_mov_b32 v100, v208\n"
"v_mov_b32 v102, v209\n"
"buffer_store_dwordx4 v[100:103], v116, s[28:31], s60 offen offset:1536 sc1\n"
"v_mov_b32 v104, v210\n"
"v_mov_b32 v106, v211\n"
"buffer_store_dwordx4 v[104:107], v116, s[28:31], s60 offen offset:1552 sc1\n"
"v_mov_b32 v108, v212\n"
"v_mov_b32 v110, v213\n"
"buffer_store_dwordx4 v[108:111], v116, s[28:31], s60 offen offset:1664 sc1\n"
"v_mov_b32 v112, v214\n"
"v_mov_b32 v114, v215\n"
"buffer_store_dwordx4 v[112:115], v116, s[28:31], s60 offen offset:1680 sc1\n"
"v_mov_b32 v100, v216\n"
"v_mov_b32 v102, v217\n"
"buffer_store_dwordx4 v[100:103], v116, s[28:31], s60 offen offset:3072 sc1\n"
"v_mov_b32 v104, v218\n"
"v_mov_b32 v106, v219\n"
"buffer_store_dwordx4 v[104:107], v116, s[28:31], s60 offen offset:3088 sc1\n"
"v_mov_b32 v108, v220\n"
"v_mov_b32 v110, v221\n"
"buffer_store_dwordx4 v[108:111], v116, s[28:31], s60 offen offset:3200 sc1\n"
"v_mov_b32 v112, v222\n"
"v_mov_b32 v114, v223\n"
"buffer_store_dwordx4 v[112:115], v116, s[28:31], s60 offen offset:3216 sc1\n"
"AT_F0b:\n"
"s_mov_b64 exec, -1\n"
"ds_read_b128 v[228:231], v184 offset:0\n"
"ds_read_b128 v[232:235], v184 offset:1024\n"
"ds_read_b128 v[236:239], v184 offset:2048\n"
"v_cmp_ne_u32 vcc, s12, v180\n"
"v_cmp_ne_u32 s[60:61], s13, v180\n"
"s_waitcnt lgkmcnt(2)\n"
"s_and_b64 exec, vcc, s[60:61]\n"
"buffer_store_dwordx4 v[228:231], v250, s[76:79], s46 offen offset:0 sc1\n"
"s_mov_b64 exec, -1\n"
"v_cmp_ne_u32 vcc, s12, v181\n"
"v_cmp_ne_u32 s[60:61], s13, v181\n"
"s_waitcnt lgkmcnt(1)\n"
"s_and_b64 exec, vcc, s[60:61]\n"
"buffer_store_dwordx4 v[232:235], v250, s[76:79], s46 offen offset:1024 sc1\n"
"s_mov_b64 exec, -1\n"
"v_cmp_ne_u32 vcc, s12, v182\n"
"v_cmp_ne_u32 s[60:61], s13, v182\n"
"s_waitcnt lgkmcnt(0)\n"
"s_and_b64 exec, vcc, s[60:61]\n"
"buffer_store_dwordx4 v[236:239], v250, s[76:79], s46 offen offset:2048 sc1\n"
"s_mov_b64 exec, -1\n"
"s_sleep 28\n"
"AT_F1:\n"
"s_cmp_lt_u32 s42, 184\n"
"s_cbranch_scc1 AT_end\n"
"s_sub_u32 s56, s42, 184\n"
"s_mul_i32 s57, s56, 57\n"
"s_lshr_b32 s57, s57, 10\n"
"s_mul_i32 s58, s57, 144\n"
"s_mul_i32 s57, s57, 18\n"
"s_sub_u32 s57, s56, s57\n"
"s_lshr_b32 s61, s57, 2\n"
"s_lshl_b32 s61, s61, 5\n"
"s_add_u32 s58, s58, s61\n"
"s_and_b32 s61, s57, 1\n"
"s_lshl_b32 s62, s61, 3\n"
"s_bfe_u32 s63, s57, 0x10001\n"
"s_cmp_ge_u32 s57, 16\n"
"s_cselect_b32 s62, 0, s62\n"
"s_cselect_b32 s63, s61, s63\n"
"s_cselect_b64 vcc, -1, 0\n"
"s_add_u32 s58, s58, s62\n"
"s_cmp_eq_u32 s63, 1\n"
"s_cselect_b32 s59, s13, s12\n"
"s_cselect_b32 s60, s11, s10\n"
"s_mul_i32 s56, s56, 24576\n"
"s_add_u32 s56, s56, 28794880\n"
"s_mul_i32 s61, s63, 576\n"
"s_add_u32 s61, s61, s58\n"
"s_lshl_b32 s61, s61, 3\n"
"s_add_u32 s61, s61, 28778496\n"
"v_lshlrev_b32 v30, 4, v253\n"
"v_add_u32 v31, 4096, v30\n"
"v_add_u32 v32, 8192, v30\n"
"v_add_u32 v33, 12288, v30\n"
"v_add_u32 v34, 16384, v30\n"
"v_add_u32 v35, 20480, v30\n"
"v_and_b32 v36, 15, v248\n"
"v_lshlrev_b32 v37, 3, v36\n"
"v_and_b32 v41, 1, v36\n"
"v_lshrrev_b32 v42, 1, v36\n"
"v_lshl_add_u32 v41, v41, 4, v42\n"
"v_cndmask_b32 v41, v41, v36, vcc\n"
"v_lshlrev_b32 v42, 3, v41\n"
"s_lshl_b32 s62, s58, 2\n"
"s_add_u32 s62, s62, s40\n"
"s_add_u32 s62, s62, 158720\n"
"v_lshl_add_u32 v38, v41, 2, s62\n"
"AT_pg18:\n"
"s_cbranch_execz AT_hr\n"
"ds_read_b32 v39, v38\n"
"s_mov_b32 s87, 0x80000\n"
"AT_F2:\n"
"buffer_load_dwordx4 v[0:3], v30, s[28:31], s56 offen sc1\n"
"buffer_load_dwordx4 v[4:7], v31, s[28:31], s56 offen sc1\n"
"buffer_load_dwordx4 v[8:11], v32, s[28:31], s56 offen sc1\n"
"buffer_load_dwordx4 v[12:15], v33, s[28:31], s56 offen sc1\n"
"buffer_load_dwordx4 v[16:19], v34, s[28:31], s56 offen sc1\n"
"buffer_load_dwordx4 v[20:23], v35, s[28:31], s56 offen sc1\n"
"buffer_load_dwordx2 v[24:25], v42, s[28:31], s61 offen sc1\n"
"s_and_b32 s62, s60, s14\n"
"s_ashr_i32 s63, s62, 31\n"
"s_not_b32 s63, s63\n"
"s_and_b32 s63, s63, s14\n"
"s_or_b32 s63, s63, 0x80000000\n"
"s_xor_b32 s62, s62, s63\n"
"v_mov_b32 v40, s62\n"
"v_mul_f32 v40, 0xbfb8aa3b, v40\n"
"v_exp_f32 v40, v40\n"
"s_nop 0\n"
"v_add_f32 v40, 1.0, v40\n"
"v_rcp_f32 v40, v40\n"
"s_waitcnt vmcnt(0) lgkmcnt(0)\n"
"v_xor_b32 v26, s85, v1\n"
"v_xor_b32 v27, s85, v3\n"
"v_bitop3_b32 v26, v5, s85, v26 bitop3:0xbe\n"
"v_bitop3_b32 v27, v7, s85, v27 bitop3:0xbe\n"
"v_bitop3_b32 v26, v9, s85, v26 bitop3:0xbe\n"
"v_bitop3_b32 v27, v11, s85, v27 bitop3:0xbe\n"
"v_bitop3_b32 v26, v13, s85, v26 bitop3:0xbe\n"
"v_bitop3_b32 v27, v15, s85, v27 bitop3:0xbe\n"
"v_bitop3_b32 v26, v17, s85, v26 bitop3:0xbe\n"
"v_bitop3_b32 v27, v19, s85, v27 bitop3:0xbe\n"
"v_bitop3_b32 v26, v21, s85, v26 bitop3:0xbe\n"
"v_bitop3_b32 v27, v23, s85, v27 bitop3:0xbe\n"
"v_bitop3_b32 v26, v25, s85, v26 bitop3:0xbe\n"
"v_or_b32 v26, v26, v27\n"
"v_cmp_ne_u32 vcc, 0, v26\n"
"s_nop 0\n"
"s_cbranch_vccz AT_F3\n"
"s_sub_u32 s87, s87, 1\n"
"s_cmp_eq_u32 s87, 0\n"
"s_cbranch_scc1 AT_F3\n"
"s_sleep 2\n"
"s_branch AT_F2\n"
"AT_F3:\n"
"v_add_f32 v0, v0, v4\n"
"v_add_f32 v2, v2, v6\n"
"v_add_f32 v8, v8, v12\n"
"v_add_f32 v10, v10, v14\n"
"v_add_f32 v16, v16, v20\n"
"v_add_f32 v18, v18, v22\n"
"v_add_f32 v0, v0, v8\n"
"v_add_f32 v2, v2, v10\n"
"v_add_f32 v0, v0, v16\n"
"v_add_f32 v1, v2, v18\n"
"v_mul_f32 v40, v40, v39\n"
"s_lshl_b32 s62, s41, 6\n"
"v_add3_u32 v28, v37, v227, s62\n"
"v_add_f32_dpp v0, v0, v0 row_ror:8 row_mask:0xf bank_mask:0xf\n"
"v_add_f32_dpp v1, v1, v1 row_ror:8 row_mask:0xf bank_mask:0xf\n"
"s_nop 0\n"
"v_mov_b32 v2, v0\n"
"v_mov_b32 v3, v1\n"
"s_nop 0\n"
"v_permlane16_swap_b32 v0, v2\n"
"v_permlane16_swap_b32 v1, v3\n"
"v_add_f32 v0, v0, v2\n"
"v_add_f32 v1, v1, v3\n"
"v_mov_b32 v2, v0\n"
"v_mov_b32 v3, v1\n"
"s_nop 0\n"
"v_permlane32_swap_b32 v0, v2\n"
"v_permlane32_swap_b32 v1, v3\n"
"v_add_f32 v0, v0, v2\n"
"v_add_f32 v1, v1, v3\n"
"s_mov_b64 exec, 0xff\n"
"ds_write_b64 v28, v[0:1] offset:1024\n"
"s_mov_b64 exec, -1\n"
"v_lshl_add_u32 v28, v36, 2, v227\n"
"s_waitcnt lgkmcnt(0)\n"
"s_barrier\n"
"s_cmp_lg_u32 s41, 0\n"
"s_cbranch_scc1 AT_end\n"
"ds_read_b32 v0, v28 offset:1024\n"
"ds_read_b32 v1, v28 offset:1088\n"
"ds_read_b32 v2, v28 offset:1152\n"
"ds_read_b32 v3, v28 offset:1216\n"
"s_mul_i32 s61, s59, 576\n"
"s_add_u32 s61, s61, s58\n"
"s_lshl_b32 s61, s61, 1\n"
"v_lshlrev_b32 v5, 1, v41\n"
"s_waitcnt lgkmcnt(0)\n"
"v_add_f32 v0, v0, v1\n"
"v_add_f32 v2, v2, v3\n"
"v_add_f32 v0, v0, v2\n"
"v_fma_f32 v0, v0, v40, v24\n"
"v_cvt_pk_bf16_f32 v0, v0, v0\n"
"s_mov_b64 exec, 0xffff\n"
"buffer_store_short v0, v5, s[76:79], s61 offen sc1\n"
"s_branch AT_end\n"
"AT_x1:\n"
"s_mov_b32 s87, 0x80000\n"
"AT_x3:\n"
"buffer_load_dword v249, v215, s[28:31], s23 offen sc1\n"
"s_waitcnt vmcnt(0)\n"
"v_cmp_ne_u32 vcc, s85, v249\n"
"s_sub_u32 s87, s87, 1\n"
"s_cbranch_vccz AT_x2\n"
"s_cmp_eq_u32 s87, 0\n"
"s_cbranch_scc1 AT_x2\n"
"s_sleep 1\n"
"s_branch AT_x3\n"
"AT_fb:\n"
"s_waitcnt vmcnt(0) lgkmcnt(0)\n"
"s_barrier\n"
"s_mov_b32 s45, 1\n"
"s_mov_b32 s87, 32\n"
"s_lshl_b32 s84, s41, 12\n"
"s_lshl_b32 s56, s43, 19\n"
"s_add_u32 s84, s84, s56\n"
"v_mov_b32 v160, 0xff800000\n"
"v_mov_b32 v161, 0xff800000\n"
"v_mov_b32 v162, 0xff800000\n"
"v_mov_b32 v163, 0xff800000\n"
"AT_fbl:\n"
"s_mov_b32 m0, s86\n"
"s_nop 0\n"
"buffer_load_dwordx4 v246, s[8:11], s84 offen offset:0 lds\n"
"buffer_load_dwordx4 v246, s[8:11], s84 offen offset:1024 lds\n"
"buffer_load_dwordx4 v247, s[8:11], s84 offen offset:2048 lds\n"
"buffer_load_dwordx4 v247, s[8:11], s84 offen offset:3072 lds\n"
"s_add_u32 s84, s84, 16384\n"
"s_waitcnt vmcnt(0)\n"
"s_barrier\n"
"ds_read_b128 a[128:131], v240 offset:0\n"
"ds_read_b128 a[132:135], v240 offset:256\n"
"ds_read_b128 a[136:139], v240 offset:512\n"
"ds_read_b128 a[140:143], v240 offset:768\n"
"ds_read_b128 a[144:147], v240 offset:4096\n"
"ds_read_b128 a[148:151], v240 offset:4352\n"
"ds_read_b128 a[152:155], v240 offset:4608\n"
"ds_read_b128 a[156:159], v240 offset:4864\n"
"s_waitcnt lgkmcnt(0)\n"
"v_mfma_f32_16x16x32_bf16 v[128:131], a[128:131], v[64:67], 0\n"
"v_mfma_f32_16x16x32_bf16 v[128:131], a[132:135], v[68:71], v[128:131]\n"
"v_mfma_f32_16x16x32_bf16 v[128:131], a[136:139], v[72:75], v[128:131]\n"
"v_mfma_f32_16x16x32_bf16 v[128:131], a[140:143], v[76:79], v[128:131]\n"
"v_mfma_f32_16x16x32_bf16 v[132:135], a[144:147], v[64:67], 0\n"
"v_mfma_f32_16x16x32_bf16 v[132:135], a[148:151], v[68:71], v[132:135]\n"
"v_mfma_f32_16x16x32_bf16 v[132:135], a[152:155], v[72:75], v[132:135]\n"
"v_mfma_f32_16x16x32_bf16 v[132:135], a[156:159], v[76:79], v[132:135]\n"
"v_mfma_f32_16x16x32_bf16 v[136:139], a[128:131], v[80:83], 0\n"
"v_mfma_f32_16x16x32_bf16 v[136:139], a[132:135], v[84:87], v[136:139]\n"
"v_mfma_f32_16x16x32_bf16 v[136:139], a[136:139], v[88:91], v[136:139]\n"
"v_mfma_f32_16x16x32_bf16 v[136:139], a[140:143], v[92:95], v[136:139]\n"
"v_mfma_f32_16x16x32_bf16 v[140:143], a[144:147], v[80:83], 0\n"
"v_mfma_f32_16x16x32_bf16 v[140:143], a[148:151], v[84:87], v[140:143]\n"
"v_mfma_f32_16x16x32_bf16 v[140:143], a[152:155], v[88:91], v[140:143]\n"
"v_mfma_f32_16x16x32_bf16 v[140:143], a[156:159], v[92:95], v[140:143]\n"
"v_mfma_f32_16x16x32_bf16 v[144:147], a[128:131], v[96:99], 0\n"
"v_mfma_f32_16x16x32_bf16 v[144:147], a[132:135], v[100:103], v[144:147]\n"
"v_mfma_f32_16x16x32_bf16 v[144:147], a[136:139], v[104:107], v[144:147]\n"
"v_mfma_f32_16x16x32_bf16 v[144:147], a[140:143], v[108:111], v[144:147]\n"
"v_mfma_f32_16x16x32_bf16 v[148:151], a[144:147], v[96:99], 0\n"
"v_mfma_f32_16x16x32_bf16 v[148:151], a[148:151], v[100:103], v[148:151]\n"
"v_mfma_f32_16x16x32_bf16 v[148:151], a[152:155], v[104:107], v[148:151]\n"
"v_mfma_f32_16x16x32_bf16 v[148:151], a[156:159], v[108:111], v[148:151]\n"
"v_mfma_f32_16x16x32_bf16 v[152:155], a[128:131], v[112:115], 0\n"
"v_mfma_f32_16x16x32_bf16 v[152:155], a[132:135], v[116:119], v[152:155]\n"
"v_mfma_f32_16x16x32_bf16 v[152:155], a[136:139], v[120:123], v[152:155]\n"
"v_mfma_f32_16x16x32_bf16 v[152:155], a[140:143], v[124:127], v[152:155]\n"
"v_mfma_f32_16x16x32_bf16 v[156:159], a[144:147], v[112:115], 0\n"
"v_mfma_f32_16x16x32_bf16 v[156:159], a[148:151], v[116:119], v[156:159]\n"
"v_mfma_f32_16x16x32_bf16 v[156:159], a[152:155], v[120:123], v[156:159]\n"
"v_mfma_f32_16x16x32_bf16 v[156:159], a[156:159], v[124:127], v[156:159]\n"
"s_nop 7\n"
"v_max3_f32 v160, v160, v128, v129\n"
"v_max3_f32 v160, v160, v130, v131\n"
"v_max3_f32 v160, v160, v132, v133\n"
"v_max3_f32 v160, v160, v134, v135\n"
"v_max3_f32 v161, v161, v136, v137\n"
"v_max3_f32 v161, v161, v138, v139\n"
"v_max3_f32 v161, v161, v140, v141\n"
"v_max3_f32 v161, v161, v142, v143\n"
"v_max3_f32 v162, v162, v144, v145\n"
"v_max3_f32 v162, v162, v146, v147\n"
"v_max3_f32 v162, v162, v148, v149\n"
"v_max3_f32 v162, v162, v150, v151\n"
"v_max3_f32 v163, v163, v152, v153\n"
"v_max3_f32 v163, v163, v154, v155\n"
"v_max3_f32 v163, v163, v156, v157\n"
"v_max3_f32 v163, v163, v158, v159\n"
"ds_read_b128 a[128:131], v240 offset:8192\n"
"ds_read_b128 a[132:135], v240 offset:8448\n"
"ds_read_b128 a[136:139], v240 offset:8704\n"
"ds_read_b128 a[140:143], v240 offset:8960\n"
"ds_read_b128 a[144:147], v240 offset:12288\n"
"ds_read_b128 a[148:151], v240 offset:12544\n"
"ds_read_b128 a[152:155], v240 offset:12800\n"
"ds_read_b128 a[156:159], v240 offset:13056\n"
"s_waitcnt lgkmcnt(0)\n"
"v_mfma_f32_16x16x32_bf16 v[128:131], a[128:131], v[64:67], 0\n"
"v_mfma_f32_16x16x32_bf16 v[128:131], a[132:135], v[68:71], v[128:131]\n"
"v_mfma_f32_16x16x32_bf16 v[128:131], a[136:139], v[72:75], v[128:131]\n"
"v_mfma_f32_16x16x32_bf16 v[128:131], a[140:143], v[76:79], v[128:131]\n"
"v_mfma_f32_16x16x32_bf16 v[132:135], a[144:147], v[64:67], 0\n"
"v_mfma_f32_16x16x32_bf16 v[132:135], a[148:151], v[68:71], v[132:135]\n"
"v_mfma_f32_16x16x32_bf16 v[132:135], a[152:155], v[72:75], v[132:135]\n"
"v_mfma_f32_16x16x32_bf16 v[132:135], a[156:159], v[76:79], v[132:135]\n"
"v_mfma_f32_16x16x32_bf16 v[136:139], a[128:131], v[80:83], 0\n"
"v_mfma_f32_16x16x32_bf16 v[136:139], a[132:135], v[84:87], v[136:139]\n"
"v_mfma_f32_16x16x32_bf16 v[136:139], a[136:139], v[88:91], v[136:139]\n"
"v_mfma_f32_16x16x32_bf16 v[136:139], a[140:143], v[92:95], v[136:139]\n"
"v_mfma_f32_16x16x32_bf16 v[140:143], a[144:147], v[80:83], 0\n"
"v_mfma_f32_16x16x32_bf16 v[140:143], a[148:151], v[84:87], v[140:143]\n"
"v_mfma_f32_16x16x32_bf16 v[140:143], a[152:155], v[88:91], v[140:143]\n"
"v_mfma_f32_16x16x32_bf16 v[140:143], a[156:159], v[92:95], v[140:143]\n"
"v_mfma_f32_16x16x32_bf16 v[144:147], a[128:131], v[96:99], 0\n"
"v_mfma_f32_16x16x32_bf16 v[144:147], a[132:135], v[100:103], v[144:147]\n"
"v_mfma_f32_16x16x32_bf16 v[144:147], a[136:139], v[104:107], v[144:147]\n"
"v_mfma_f32_16x16x32_bf16 v[144:147], a[140:143], v[108:111], v[144:147]\n"
"v_mfma_f32_16x16x32_bf16 v[148:151], a[144:147], v[96:99], 0\n"
"v_mfma_f32_16x16x32_bf16 v[148:151], a[148:151], v[100:103], v[148:151]\n"
"v_mfma_f32_16x16x32_bf16 v[148:151], a[152:155], v[104:107], v[148:151]\n"
"v_mfma_f32_16x16x32_bf16 v[148:151], a[156:159], v[108:111], v[148:151]\n"
"v_mfma_f32_16x16x32_bf16 v[152:155], a[128:131], v[112:115], 0\n"
"v_mfma_f32_16x16x32_bf16 v[152:155], a[132:135], v[116:119], v[152:155]\n"
"v_mfma_f32_16x16x32_bf16 v[152:155], a[136:139], v[120:123], v[152:155]\n"
"v_mfma_f32_16x16x32_bf16 v[152:155], a[140:143], v[124:127], v[152:155]\n"
"v_mfma_f32_16x16x32_bf16 v[156:159], a[144:147], v[112:115], 0\n"
"v_mfma_f32_16x16x32_bf16 v[156:159], a[148:151], v[116:119], v[156:159]\n"
"v_mfma_f32_16x16x32_bf16 v[156:159], a[152:155], v[120:123], v[156:159]\n"
"v_mfma_f32_16x16x32_bf16 v[156:159], a[156:159], v[124:127], v[156:159]\n"
"s_nop 7\n"
"v_max3_f32 v160, v160, v128, v129\n"
"v_max3_f32 v160, v160, v130, v131\n"
"v_max3_f32 v160, v160, v132, v133\n"
"v_max3_f32 v160, v160, v134, v135\n"
"v_max3_f32 v161, v161, v136, v137\n"
"v_max3_f32 v161, v161, v138, v139\n"
"v_max3_f32 v161, v161, v140, v141\n"
"v_max3_f32 v161, v161, v142, v143\n"
"v_max3_f32 v162, v162, v144, v145\n"
"v_max3_f32 v162, v162, v146, v147\n"
"v_max3_f32 v162, v162, v148, v149\n"
"v_max3_f32 v162, v162, v150, v151\n"
"v_max3_f32 v163, v163, v152, v153\n"
"v_max3_f32 v163, v163, v154, v155\n"
"v_max3_f32 v163, v163, v156, v157\n"
"v_max3_f32 v163, v163, v158, v159\n"
"s_barrier\n"
"s_sub_u32 s87, s87, 1\n"
"s_cmp_lg_u32 s87, 0\n"
"s_cbranch_scc1 AT_fbl\n"
"v_mov_b32 v164, v160\n"
"s_nop 1\n"
"v_permlane32_swap_b32 v160, v164\n"
"v_max_f32 v160, v160, v164\n"
"v_mov_b32 v164, v160\n"
"s_nop 1\n"
"v_permlane16_swap_b32 v160, v164\n"
"v_max_f32 v160, v160, v164\n"
"v_sub_f32 v24, 0, v160\n"
"v_sub_f32 v25, 0, v160\n"
"v_sub_f32 v26, 0, v160\n"
"v_sub_f32 v27, 0, v160\n"
"v_mov_b32 v164, v161\n"
"s_nop 1\n"
"v_permlane32_swap_b32 v161, v164\n"
"v_max_f32 v161, v161, v164\n"
"v_mov_b32 v164, v161\n"
"s_nop 1\n"
"v_permlane16_swap_b32 v161, v164\n"
"v_max_f32 v161, v161, v164\n"
"v_sub_f32 v28, 0, v161\n"
"v_sub_f32 v29, 0, v161\n"
"v_sub_f32 v30, 0, v161\n"
"v_sub_f32 v31, 0, v161\n"
"v_mov_b32 v164, v162\n"
"s_nop 1\n"
"v_permlane32_swap_b32 v162, v164\n"
"v_max_f32 v162, v162, v164\n"
"v_mov_b32 v164, v162\n"
"s_nop 1\n"
"v_permlane16_swap_b32 v162, v164\n"
"v_max_f32 v162, v162, v164\n"
"v_sub_f32 v32, 0, v162\n"
"v_sub_f32 v33, 0, v162\n"
"v_sub_f32 v34, 0, v162\n"
"v_sub_f32 v35, 0, v162\n"
"v_mov_b32 v164, v163\n"
"s_nop 1\n"
"v_permlane32_swap_b32 v163, v164\n"
"v_max_f32 v163, v163, v164\n"
"v_mov_b32 v164, v163\n"
"s_nop 1\n"
"v_permlane16_swap_b32 v163, v164\n"
"v_max_f32 v163, v163, v164\n"
"v_sub_f32 v36, 0, v163\n"
"v_sub_f32 v37, 0, v163\n"
"v_sub_f32 v38, 0, v163\n"
"v_sub_f32 v39, 0, v163\n"
"s_and_b32 s62, s42, 7\n"
"s_mul_i32 s46, s62, 663552\n"
"s_lshr_b32 s47, s43, 2\n"
"s_mul_i32 s47, s47, 1179648\n"
"s_getpc_b64 s[32:33]\n"
"AT_pc2:\n"
"s_sub_u32 s32, s32, AT_pc2-AT_start\n"
"s_subb_u32 s33, s33, 0\n"
"s_and_b32 s33, s33, 0xffff\n"
"s_mov_b32 s34, AT_kend-AT_start\n"
"s_mov_b32 s35, 0x20000\n"
"s_lshr_b32 s62, s42, 3\n"
"s_lshl_b32 s62, s62, 2\n"
"s_add_u32 s62, s62, s41\n"
"s_and_b32 s62, s62, 31\n"
"s_lshl_b32 s48, s62, 12\n"
"s_lshl_b32 s54, s44, 2\n"
"s_add_u32 s54, s54, s41\n"
"s_mul_i32 s54, s54, 36864\n"
"s_lshr_b32 s62, s43, 2\n"
"s_mul_i32 s62, s62, 2359296\n"
"s_add_u32 s54, s54, s62\n"
"s_add_u32 s54, s54, 139264\n"
"s_bfe_u32 s62, s43, 0x10001\n"
"s_mul_i32 s62, s62, 9216\n"
"s_add_u32 s54, s54, s62\n"
"s_and_b32 s62, s43, 1\n"
"s_lshl_b32 s49, s62, 3\n"
"s_add_u32 s49, s49, s54\n"
"s_add_u32 s49, s49, 4096\n"
"s_mul_i32 s62, s62, 5120\n"
"s_add_u32 s54, s54, s62\n"
"s_and_b32 s62, s43, 1\n"
"s_lshl_b32 s62, s62, 10\n"
"s_sub_u32 s54, s54, s62\n"
"s_add_u32 s49, s54, 4096\n"
"s_lshl_b32 s52, s44, 4\n"
"s_add_u32 s52, s52, s43\n"
"s_mul_i32 s52, s52, 18432\n"
"s_mul_i32 s62, s41, 5\n"
"s_min_u32 s62, s62, 13\n"
"s_lshl_b32 s62, s62, 10\n"
"s_add_u32 s52, s52, s62\n"
"s_add_u32 s52, s52, 30564352\n"
"s_add_u32 s53, s52, 4096\n"
"v_mov_b32 v40, 0x3f803f80\n"
"v_mov_b32 v41, 0x3f803f80\n"
"v_mov_b32 v42, 0x3f803f80\n"
"v_mov_b32 v43, 0x3f803f80\n"
"v_mov_b32 v20, 0\n"
"v_mov_b32 v21, 0\n"
"v_mov_b32 v22, 0\n"
"v_mov_b32 v23, 0\n"
"v_mov_b32 v44, 0\n"
"v_mov_b32 v52, 0\n"
"v_mov_b32 v216, -1\n"
"v_mov_b32 v217, -1\n"
"v_mov_b32 v218, -1\n"
"v_mov_b32 v219, -1\n"
"v_lshl_add_u32 v255, v248, 6, s48\n"
"v_lshlrev_b32 v207, 4, v248\n"
"v_add_u32 v208, 18432, v207\n"
"s_lshl_b32 s84, s41, 12\n"
"s_lshl_b32 s56, s43, 19\n"
"s_add_u32 s84, s84, s56\n"
"v_mov_b32 v251, 0x40000000\n"
"v_mov_b32 v252, 0x40000000\n"
"v_mov_b32 v253, 0x40000000\n"
"v_add_f32 v148, 0xc2480000, v161\n"
"v_add_f32 v149, 0xc2480000, v161\n"
"v_add_f32 v150, 0xc2480000, v161\n"
"v_add_f32 v151, 0xc2480000, v161\n"
"v_add_f32 v156, 0xc2480000, v163\n"
"v_add_f32 v157, 0xc2480000, v163\n"
"v_add_f32 v158, 0xc2480000, v163\n"
"v_add_f32 v159, 0xc2480000, v163\n"
"s_mov_b32 m0, s86\n"
"s_nop 0\n"
"buffer_load_dwordx4 v246, s[8:11], s84 offen offset:0 lds\n"
"buffer_load_dwordx4 v246, s[8:11], s84 offen offset:1024 lds\n"
"buffer_load_dwordx4 v247, s[8:11], s84 offen offset:2048 lds\n"
"buffer_load_dwordx4 v247, s[8:11], s84 offen offset:3072 lds\n"
"s_add_u32 m0, s86, 16384\n"
"s_nop 0\n"
"buffer_load_dwordx4 v246, s[12:15], s84 offen offset:0 lds\n"
"buffer_load_dwordx4 v247, s[12:15], s84 offen offset:1024 lds\n"
"buffer_load_dwordx4 v246, s[12:15], s84 offen offset:2048 lds\n"
"buffer_load_dwordx4 v247, s[12:15], s84 offen offset:3072 lds\n"
"s_add_u32 s84, s84, 16384\n"
"s_add_u32 m0, s86, 32768\n"
"s_nop 0\n"
"buffer_load_dwordx4 v246, s[8:11], s84 offen offset:0 lds\n"
"buffer_load_dwordx4 v246, s[8:11], s84 offen offset:1024 lds\n"
"buffer_load_dwordx4 v247, s[8:11], s84 offen offset:2048 lds\n"
"buffer_load_dwordx4 v247, s[8:11], s84 offen offset:3072 lds\n"
"s_add_u32 m0, s86, 49152\n"
"s_nop 0\n"
"buffer_load_dwordx4 v246, s[12:15], s84 offen offset:0 lds\n"
"buffer_load_dwordx4 v247, s[12:15], s84 offen offset:1024 lds\n"
"buffer_load_dwordx4 v246, s[12:15], s84 offen offset:2048 lds\n"
"buffer_load_dwordx4 v247, s[12:15], s84 offen offset:3072 lds\n"
"s_add_u32 s84, s84, 16384\n"
"s_add_u32 m0, s86, 65536\n"
"s_nop 0\n"
"buffer_load_dwordx4 v246, s[8:11], s84 offen offset:0 lds\n"
"buffer_load_dwordx4 v246, s[8:11], s84 offen offset:1024 lds\n"
"buffer_load_dwordx4 v247, s[8:11], s84 offen offset:2048 lds\n"
"buffer_load_dwordx4 v247, s[8:11], s84 offen offset:3072 lds\n"
"s_add_u32 m0, s86, 81920\n"
"s_nop 0\n"
"buffer_load_dwordx4 v246, s[12:15], s84 offen offset:0 lds\n"
"buffer_load_dwordx4 v247, s[12:15], s84 offen offset:1024 lds\n"
"buffer_load_dwordx4 v246, s[12:15], s84 offen offset:2048 lds\n"
"buffer_load_dwordx4 v247, s[12:15], s84 offen offset:3072 lds\n"
"s_add_u32 s84, s84, 16384\n"
"s_waitcnt vmcnt(16)\n"
"s_barrier\n"
"ds_read_b128 a[128:131], v240 offset:0\n"
"ds_read_b128 a[132:135], v240 offset:256\n"
"ds_read_b128 a[136:139], v240 offset:512\n"
"ds_read_b128 a[140:143], v240 offset:768\n"
"ds_read_b128 a[144:147], v240 offset:4096\n"
"ds_read_b128 a[148:151], v240 offset:4352\n"
"ds_read_b128 a[152:155], v240 offset:4608\n"
"ds_read_b128 a[156:159], v240 offset:4864\n"
"ds_read_b64_tr_b16 a[192:193], v242 offset:16384\n"
"ds_read_b64_tr_b16 a[194:195], v242 offset:20480\n"
"ds_read_b64_tr_b16 a[196:197], v243 offset:16384\n"
"ds_read_b64_tr_b16 a[198:199], v243 offset:20480\n"
"ds_read_b64_tr_b16 a[200:201], v242 offset:16640\n"
"ds_read_b64_tr_b16 a[202:203], v242 offset:20736\n"
"ds_read_b64_tr_b16 a[204:205], v243 offset:16640\n"
"ds_read_b64_tr_b16 a[206:207], v243 offset:20736\n"
"ds_read_b64_tr_b16 a[208:209], v242 offset:16896\n"
"ds_read_b64_tr_b16 a[210:211], v242 offset:20992\n"
"ds_read_b64_tr_b16 a[212:213], v243 offset:16896\n"
"ds_read_b64_tr_b16 a[214:215], v243 offset:20992\n"
"ds_read_b64_tr_b16 a[216:217], v242 offset:17152\n"
"ds_read_b64_tr_b16 a[218:219], v242 offset:21248\n"
"ds_read_b64_tr_b16 a[220:221], v243 offset:17152\n"
"ds_read_b64_tr_b16 a[222:223], v243 offset:21248\n"
"s_waitcnt lgkmcnt(0)\n"
"s_branch AT_main\n"
"AT_end:\n"
"AT_kend:\n"
  :: [kp] "s"(__builtin_amdgcn_kernarg_segment_ptr()), [blk] "s"(blockIdx.x), [wid] "s"(wid), [lds] "s"(ldsb) : "v0", "v1", "v2", "v3", "v4", "v5", "v6", "v7", "v8", "v9", "v10", "v11", "v12", "v13", "v14", "v15", "v16", "v17", "v18", "v19", "v20", "v21", "v22", "v23", "v24", "v25", "v26", "v27", "v28", "v29", "v30", "v31", "v32", "v33", "v34", "v35", "v36", "v37", "v38", "v39", "v40", "v41", "v42", "v43", "v44", "v45", "v46", "v47", "v48", "v49", "v50", "v51", "v52", "v53", "v54", "v55", "v56", "v57", "v58", "v59", "v60", "v61", "v62", "v63", "v64", "v65", "v66", "v67", "v68", "v69", "v70", "v71", "v72", "v73", "v74", "v75", "v76", "v77", "v78", "v79", "v80", "v81", "v82", "v83", "v84", "v85", "v86", "v87", "v88", "v89", "v90", "v91", "v92", "v93", "v94", "v95", "v96", "v97", "v98", "v99", "v100", "v101", "v102", "v103", "v104", "v105", "v106", "v107", "v108", "v109", "v110", "v111", "v112", "v113", "v114", "v115", "v116", "v117", "v118", "v119", "v120", "v121", "v122", "v123", "v124", "v125", "v126", "v127", "v128", "v129", "v130", "v131", "v132", "v133", "v134", "v135", "v136", "v137", "v138", "v139", "v140", "v141", "v142", "v143", "v144", "v145", "v146", "v147", "v148", "v149", "v150", "v151", "v152", "v153", "v154", "v155", "v156", "v157", "v158", "v159", "v160", "v161", "v162", "v163", "v164", "v165", "v166", "v167", "v168", "v169", "v170", "v171", "v172", "v173", "v174", "v175", "v176", "v177", "v178", "v179", "v180", "v181", "v182", "v183", "v184", "v185", "v186", "v187", "v188", "v189", "v190", "v191", "v192", "v193", "v194", "v195", "v196", "v197", "v198", "v199", "v200", "v201", "v202", "v203", "v204", "v205", "v206", "v207", "v208", "v209", "v210", "v211", "v212", "v213", "v214", "v215", "v216", "v217", "v218", "v219", "v220", "v221", "v222", "v223", "v224", "v225", "v226", "v227", "v228", "v229", "v230", "v231", "v232", "v233", "v234", "v235", "v236", "v237", "v238", "v239", "v240", "v241", "v242", "v243", "v244", "v245", "v246", "v247", "v248", "v249", "v250", "v251", "v252", "v253", "v254", "v255", "a0", "a1", "a2", "a3", "a4", "a5", "a6", "a7", "a8", "a9", "a10", "a11", "a12", "a13", "a14", "a15", "a16", "a17", "a18", "a19", "a20", "a21", "a22", "a23", "a24", "a25", "a26", "a27", "a28", "a29", "a30", "a31", "a32", "a33", "a34", "a35", "a36", "a37", "a38", "a39", "a40", "a41", "a42", "a43", "a44", "a45", "a46", "a47", "a48", "a49", "a50", "a51", "a52", "a53", "a54", "a55", "a56", "a57", "a58", "a59", "a60", "a61", "a62", "a63", "a64", "a65", "a66", "a67", "a68", "a69", "a70", "a71", "a72", "a73", "a74", "a75", "a76", "a77", "a78", "a79", "a80", "a81", "a82", "a83", "a84", "a85", "a86", "a87", "a88", "a89", "a90", "a91", "a92", "a93", "a94", "a95", "a96", "a97", "a98", "a99", "a100", "a101", "a102", "a103", "a104", "a105", "a106", "a107", "a108", "a109", "a110", "a111", "a112", "a113", "a114", "a115", "a116", "a117", "a118", "a119", "a120", "a121", "a122", "a123", "a124", "a125", "a126", "a127", "a128", "a129", "a130", "a131", "a132", "a133", "a134", "a135", "a136", "a137", "a138", "a139", "a140", "a141", "a142", "a143", "a144", "a145", "a146", "a147", "a148", "a149", "a150", "a151", "a152", "a153", "a154", "a155", "a156", "a157", "a158", "a159", "a160", "a161", "a162", "a163", "a164", "a165", "a166", "a167", "a168", "a169", "a170", "a171", "a172", "a173", "a174", "a175", "a176", "a177", "a178", "a179", "a180", "a181", "a182", "a183", "a184", "a185", "a186", "a187", "a188", "a189", "a190", "a191", "a192", "a193", "a194", "a195", "a196", "a197", "a198", "a199", "a200", "a201", "a202", "a203", "a204", "a205", "a206", "a207", "a208", "a209", "a210", "a211", "a212", "a213", "a214", "a215", "a216", "a217", "a218", "a219", "a220", "a221", "a222", "a223", "a224", "a225", "a226", "a227", "a228", "a229", "a230", "a231", "a232", "a233", "a234", "a235", "a236", "a237", "a238", "a239", "a240", "a241", "a242", "a243", "a244", "a245", "a246", "a247", "a248", "a249", "a250", "a251", "a252", "a253", "a254", "a255", "s8", "s9", "s10", "s11", "s12", "s13", "s14", "s15", "s16", "s17", "s18", "s19", "s20", "s21", "s22", "s23", "s24", "s25", "s26", "s27", "s28", "s29", "s30", "s31", "s32", "s33", "s34", "s35", "s36", "s37", "s38", "s39", "s40", "s41", "s42", "s43", "s44", "s45", "s46", "s47", "s48", "s49", "s50", "s51", "s52", "s53", "s54", "s55", "s56", "s57", "s58", "s59", "s60", "s61", "s62", "s63", "s64", "s65", "s66", "s67", "s68", "s69", "s70", "s71", "s72", "s73", "s74", "s75", "s76", "s77", "s78", "s79", "s80", "s81", "s82", "s83", "s84", "s85", "s86", "s87", "s88", "s89", "s90", "s91", "vcc", "scc", "memory");
}

extern "C" void kernel_launch(void* const* d_in, const int* in_sizes, int n_in, void* d_out, int out_size, void* d_ws, size_t ws_size,
                              hipStream_t stream) {
  const unsigned F = 0x20000u;
  KArgs a;
  a.k = {d_in[1], HKV * SQ * 256u, F}; a.v = {d_in[2], HKV * SQ * 256u, F}; a.x = {d_in[0], SQ * DM * 2u, F}; a.wq = {d_in[3], DM * 4096u * 2u, F};
  a.wo = {d_in[4], DM * 4096u * 2u, F}; a.ws = {d_ws, (unsigned)WS_END, F}; a.c = {d_in[10], DM * 2u, F}; a.wmod = {d_in[11], DM * 6u * DM * 2u, F};
  a.wg = {d_in[7], DM * FF * 2u, F}; a.wu = {d_in[8], DM * FF * 2u, F}; a.wd = {d_in[9], DM * FF * 2u, F}; a.out = {d_out, SQ * DM * 2u, F}; a.wr = {d_in[6], DM * 2u, F};
  (void)hipFuncSetAttribute((const void*)k_main, hipFuncAttributeMaxDynamicSharedMemorySize, 163840);
  k_main<<<256, 256, 163840, stream>>>(a);
}
```
